```python
import torch

def _softmax(x, axis=-1):
    return torch.softmax(x, dim=axis)
def _gelu(x):
    return torch.nn.functional.gelu(x, approximate="tanh")
def _silu(x):
    return torch.nn.functional.silu(x)
def _sigmoid(x):
    return torch.sigmoid(x)
def _layernorm(x, axis=-1, eps=1e-5):
    m = x.mean(dim=axis, keepdim=True)
    v = x.var(dim=axis, keepdim=True, unbiased=False)
    return (x - m) / torch.sqrt(v + eps)
def _scatter_add(tgt, idx, src, axis=0):
    return tgt.clone().index_add_(axis, idx, src)
def _segment_sum(src, seg, n):
    out = torch.zeros((n,) + src.shape[1:], dtype=src.dtype)
    return out.index_add_(0, seg, src)
def _pad(x, pw):
    flat = tuple(v for pair in reversed(pw) for v in pair)
    return torch.nn.functional.pad(x, flat)

kernel_name = 'concat_heads__ssm_conv_h3__sliding_local_dilated__prompt'

def setup_inputs():

    _w = (lambda a: (lambda t: t.to(torch.bfloat16) if t.is_floating_point() else t)(torch.as_tensor(a)))
    def _A_setup():
        B, S, D = 1, 2048, 1024
        ks = 4
        return {
            "q": _w((lambda *s: torch.randn(*s, dtype=torch.bfloat16) * 0.05)(B, S, D)),
            "k": _w((lambda *s: torch.randn(*s, dtype=torch.bfloat16) * 1.6)(B, S, D)),
            "v": _w((lambda *s: torch.randn(*s, dtype=torch.bfloat16) * 0.8)(B, S, D)),
            "shift_k": _w((lambda *s: torch.randn(*s, dtype=torch.bfloat16) * 1.6)(D, ks) * 0.5),
            "ssm_k": _w((lambda *s: torch.randn(*s, dtype=torch.bfloat16) * 1.6)(D, S) * (1.0 / S ** 0.5)),
            "ssm_D": _w((lambda *s: torch.randn(*s, dtype=torch.bfloat16) * 0.8)(D)),
        }
    _A_in = _A_setup()

    def _B_setup():
        B, S, D = 1, 2048, 1024
        return {
            "x": _w((lambda *s: torch.randn(*s, dtype=torch.bfloat16) * 0.2)(B, S, D)),
            "wqkv": _w((lambda *s: torch.randn(*s, dtype=torch.bfloat16) * 0.2)(D, 3 * D)),
            "wo": _w((lambda *s: torch.randn(*s, dtype=torch.bfloat16) * 0.05)(D, D)),
        }
    _B_in = _B_setup()

    return {
        "A_q": _A_in["q"],
        "A_k": _A_in["k"],
        "A_v": _A_in["v"],
        "A_shift_k": _A_in["shift_k"],
        "A_ssm_k": _A_in["ssm_k"],
        "A_ssm_D": _A_in["ssm_D"],
        "B_x": _B_in["x"],
        "B_wqkv": _B_in["wqkv"],
        "B_wo": _B_in["wo"],
    }

def reference(A_q, A_k, A_v, A_shift_k, A_ssm_k, A_ssm_D, B_x, B_wqkv, B_wo):
    def _A_ref(q, k, v, shift_k, ssm_k, ssm_D):
        B, S, D = q.shape
        ks = shift_k.shape[-1]
        k_pad = _pad(k, ((0, 0), (ks - 1, 0), (0, 0)))
        k_shift = torch.zeros_like(k)
        for t in range(ks):
            k_shift = k_shift + k_pad[:, t:t + S, :] * shift_k[:, ks - 1 - t][None, None, :]
        kv = k_shift * v
        L = 2 * S
        kv_pad = _pad(kv, ((0, 0), (0, S), (0, 0)))
        kf_pad = _pad(ssm_k, ((0, 0), (0, S)))
        kv_f = (lambda x, axes=(-2,-1): torch.fft.rfft2(x.float(), dim=axes))(kv_pad[:, None, :, :], axes=(1, 2))
        kf_f = (lambda x, axes=(-2,-1): torch.fft.rfft2(x.float(), dim=axes))(kf_pad[:, None, :], axes=(1, 2))
        kf_f = (lambda x, *d: x.permute(*d))(kf_f, 1, 2, 0)
        y_f = kv_f * kf_f[None, :, :, :]
        y = (lambda x, s, axes=(-2,-1): torch.fft.irfft2(x, s=s, dim=axes).to(torch.bfloat16))(y_f, (1, L), axes=(1, 2))[:, 0, :S, :]
        y = y + kv * ssm_D[None, None, :]
        return q * y

    def _B_ref(x, wqkv, wo):
        B, S, D = x.shape
        H = 32; Dh = D // H
        W, dil = 8, 2
        qkv = (lambda x, *d: x.permute(*d))((x @ wqkv).reshape(B, S, 3, H, Dh), 2, 0, 3, 1, 4)
        q, k, v = qkv[0], qkv[1], qkv[2]
        scale = Dh ** -0.5
        pos = torch.arange(S)
        off = torch.arange(W) * dil
        idx = pos[:, None] - off[None, :]
        mask = idx >= 0
        idx_c = torch.where(mask, idx, 0).reshape(-1)
        kg = k[:, :, idx_c, :].reshape(B, H, S, W, Dh)
        vg = v[:, :, idx_c, :].reshape(B, H, S, W, Dh)
        logits = torch.einsum("bhsd,bhswd->bhsw", q, kg) * scale
        logits = torch.where(mask[None, None, :, :], logits, -1e9)
        attn = _softmax(logits, axis=-1)
        o = torch.einsum("bhsw,bhswd->bhsd", attn, vg)
        return (lambda x, *d: x.permute(*d))(o, 0, 2, 1, 3).reshape(B, S, D) @ wo

    a = _A_ref(A_q, A_k, A_v, A_shift_k, A_ssm_k, A_ssm_D)
    b = _B_ref(B_x, B_wqkv, B_wo)
    return torch.concatenate([a, b], axis=-1)
```

```cpp
#include <hip/hip_runtime.h>
#include <hip/hip_bf16.h>
#include <stdint.h>

typedef unsigned short u16;
typedef short bf16x8 __attribute__((ext_vector_type(8)));
typedef short s16x4 __attribute__((ext_vector_type(4)));
typedef float f32x2 __attribute__((ext_vector_type(2)));
typedef float f32x4 __attribute__((ext_vector_type(4)));
typedef float f32x8 __attribute__((ext_vector_type(8)));
typedef float f32x16 __attribute__((ext_vector_type(16)));
typedef unsigned u32x4 __attribute__((ext_vector_type(4)));
typedef unsigned u32x2 __attribute__((ext_vector_type(2)));
typedef __bf16 bf16x2_t __attribute__((ext_vector_type(2)));
#define LDS_AS __attribute__((address_space(3)))

__device__ __forceinline__ float bflo(unsigned x){ return __uint_as_float(x<<16); }
__device__ __forceinline__ float bfhi(unsigned x){ return __uint_as_float(x&0xffff0000u); }
__device__ __forceinline__ unsigned pack2(float lo, float hi){ return __builtin_bit_cast(unsigned, __builtin_convertvector(f32x2{lo,hi}, bf16x2_t)); }
__device__ __forceinline__ float mul1(float a, float b){ float r; asm("v_mul_f32 %0, %1, %2" : "=v"(r) : "v"(a), "v"(b)); return r; }
__device__ __forceinline__ float fma1(float a, float b, float c){ float r; asm("v_fma_f32 %0, %1, %2, %3" : "=v"(r) : "v"(a), "v"(b), "v"(c)); return r; }

__device__ __forceinline__ void glds(const void* sbase, unsigned voff, unsigned ldsaddr){
  asm volatile("s_mov_b32 m0, %2\n\ts_nop 0\n\tglobal_load_lds_dwordx4 %0, %1" :: "v"(voff), "s"(sbase), "s"(ldsaddr) : "memory");
}
__device__ __forceinline__ void gldsc(const void* sbase, unsigned voff, unsigned ldsaddr){
  asm volatile("s_mov_b32 m0, %2\n\ts_nop 0\n\tglobal_load_lds_dwordx4 %0, %1 sc1" :: "v"(voff), "s"(sbase), "s"(ldsaddr) : "memory");
}
__device__ __forceinline__ void touch(const void* sbase, unsigned voff, unsigned ldsaddr){
  asm volatile("s_mov_b32 m0, %2\n\ts_nop 0\n\tglobal_load_lds_dword %0, %1" :: "v"(voff), "s"(sbase), "s"(ldsaddr) : "memory");
}
__device__ __forceinline__ void st16c(void* p, u32x4 v){ asm volatile("global_store_dwordx4 %0, %1, off sc1\n\ts_nop 1" :: "v"(p), "v"(v) : "memory"); }
__device__ __forceinline__ void st16cs(void* sbase, unsigned voff, u32x4 v){ asm volatile("global_store_dwordx4 %0, %1, %2 sc1\n\ts_nop 1" :: "v"(voff), "v"(v), "s"(sbase) : "memory"); }
__device__ __forceinline__ unsigned ldflag(const unsigned* p){ return __hip_atomic_load(p, __ATOMIC_RELAXED, __HIP_MEMORY_SCOPE_AGENT); }
__device__ __forceinline__ void stflag(unsigned* p, unsigned v){ __hip_atomic_store(p, v, __ATOMIC_RELAXED, __HIP_MEMORY_SCOPE_AGENT); }
template<int SLP> __device__ __forceinline__ void wait_flag(const unsigned* p, unsigned tag){
  int spins=0;
  while(true){ unsigned v=ldflag(p); if(__all(v==tag)) break; __builtin_amdgcn_s_sleep(SLP); if(++spins>(1<<21)) break; }
}
__device__ __forceinline__ unsigned lds_addr(const void* p){ return (unsigned)(size_t)(const LDS_AS void*)p; }
#define WAITV(n) asm volatile("s_waitcnt vmcnt(" #n ")":::"memory")
#define WAITL() asm volatile("s_waitcnt lgkmcnt(0)":::"memory")
#define SBAR() asm volatile("s_barrier":::"memory")
#define PUB(addr,val) asm volatile("ds_write_b32 %0, %1" :: "v"(addr), "v"(val) : "memory")
__device__ __forceinline__ s16x4 trread(const char* p){ return __builtin_amdgcn_ds_read_tr16_b64_v4i16((LDS_AS s16x4*)p); }
__device__ __forceinline__ bf16x8 cat44(s16x4 a, s16x4 b){ return __builtin_shufflevector(a,b,0,1,2,3,4,5,6,7); }
__device__ __forceinline__ int wswz(int row){ return ((row>>1)&1) | (((row>>3)&1)<<1); }
__device__ __forceinline__ u32x4 widen(u32x2 a, u32x2 b){
  auto r0=__builtin_amdgcn_permlane16_swap(a[0],b[0],false,false);
  auto r1=__builtin_amdgcn_permlane16_swap(a[1],b[1],false,false);
  return u32x4{r0[0],r1[0],r0[1],r1[1]};
}

constexpr int S=2048, DM=1024;
constexpr int WS=12288, XS=18432, X_OFF=6*WS;
constexpr int CREG=4608, C_OFF=131072;
constexpr int EX_OFF=C_OFF+4*CREG;
constexpr int SYN=EX_OFF+8192;
constexpr int SINK=SYN+256;
constexpr int ALL_LDS=SINK+768;
constexpr int F_PP=1024, F_O=4096, F_Y=8192;
#define KV0 1
#define KVW 3
#define KVS 7
#define LPR 3
#define CPR 2
#define GPR 1
#define OSLP 2
#define YSLP 8
#define TP0 0
#define TP1 16
#define TWO 1
#define OPULL 0
#define CVP 2
#define G2P 1
#define OUTC 1
#define ROT 1

#define N2_ASM \
  "v_mov_b32 v64, 0\n\t" \
  "v_mov_b32 v65, 0\n\t" \
  "v_mov_b32 v66, 0\n\t" \
  "v_mov_b32 v67, 0\n\t" \
  "v_mov_b32 v68, 0\n\t" \
  "v_mov_b32 v69, 0\n\t" \
  "v_mov_b32 v70, 0\n\t" \
  "v_mov_b32 v71, 0\n\t" \
  "v_mov_b32 v72, 0\n\t" \
  "v_mov_b32 v73, 0\n\t" \
  "v_mov_b32 v74, 0\n\t" \
  "v_mov_b32 v75, 0\n\t" \
  "v_mov_b32 v76, 0\n\t" \
  "v_mov_b32 v77, 0\n\t" \
  "v_mov_b32 v78, 0\n\t" \
  "v_mov_b32 v79, 0\n\t" \
  "v_mov_b32 v80, 0\n\t" \
  "v_mov_b32 v81, 0\n\t" \
  "v_mov_b32 v82, 0\n\t" \
  "v_mov_b32 v83, 0\n\t" \
  "v_mov_b32 v84, 0\n\t" \
  "v_mov_b32 v85, 0\n\t" \
  "v_mov_b32 v86, 0\n\t" \
  "v_mov_b32 v87, 0\n\t" \
  "v_mov_b32 v88, 0\n\t" \
  "v_mov_b32 v89, 0\n\t" \
  "v_mov_b32 v90, 0\n\t" \
  "v_mov_b32 v91, 0\n\t" \
  "v_mov_b32 v92, 0\n\t" \
  "v_mov_b32 v93, 0\n\t" \
  "v_mov_b32 v94, 0\n\t" \
  "v_mov_b32 v95, 0\n\t" \
  "v_mov_b32 v96, 0\n\t" \
  "v_mov_b32 v97, 0\n\t" \
  "v_mov_b32 v98, 0\n\t" \
  "v_mov_b32 v99, 0\n\t" \
  "v_mov_b32 v100, 0\n\t" \
  "v_mov_b32 v101, 0\n\t" \
  "v_mov_b32 v102, 0\n\t" \
  "v_mov_b32 v103, 0\n\t" \
  "v_mov_b32 v104, 0\n\t" \
  "v_mov_b32 v105, 0\n\t" \
  "v_mov_b32 v106, 0\n\t" \
  "v_mov_b32 v107, 0\n\t" \
  "v_mov_b32 v108, 0\n\t" \
  "v_mov_b32 v109, 0\n\t" \
  "v_mov_b32 v110, 0\n\t" \
  "v_mov_b32 v111, 0\n\t" \
  "v_mov_b32 v112, 0\n\t" \
  "v_mov_b32 v113, 0\n\t" \
  "v_mov_b32 v114, 0\n\t" \
  "v_mov_b32 v115, 0\n\t" \
  "v_mov_b32 v116, 0\n\t" \
  "v_mov_b32 v117, 0\n\t" \
  "v_mov_b32 v118, 0\n\t" \
  "v_mov_b32 v119, 0\n\t" \
  "v_mov_b32 v120, 0\n\t" \
  "v_mov_b32 v121, 0\n\t" \
  "v_mov_b32 v122, 0\n\t" \
  "v_mov_b32 v123, 0\n\t" \
  "v_mov_b32 v124, 0\n\t" \
  "v_mov_b32 v125, 0\n\t" \
  "v_mov_b32 v126, 0\n\t" \
  "v_mov_b32 v127, 0\n\t" \
  "v_mov_b32 v128, 0\n\t" \
  "v_mov_b32 v129, 0\n\t" \
  "v_mov_b32 v130, 0\n\t" \
  "v_mov_b32 v131, 0\n\t" \
  "v_mov_b32 v132, 0\n\t" \
  "v_mov_b32 v133, 0\n\t" \
  "v_mov_b32 v134, 0\n\t" \
  "v_mov_b32 v135, 0\n\t" \
  "v_mov_b32 v136, 0\n\t" \
  "v_mov_b32 v137, 0\n\t" \
  "v_mov_b32 v138, 0\n\t" \
  "v_mov_b32 v139, 0\n\t" \
  "v_mov_b32 v140, 0\n\t" \
  "v_mov_b32 v141, 0\n\t" \
  "v_mov_b32 v142, 0\n\t" \
  "v_mov_b32 v143, 0\n\t" \
  "v_mov_b32 v144, 0\n\t" \
  "v_mov_b32 v145, 0\n\t" \
  "v_mov_b32 v146, 0\n\t" \
  "v_mov_b32 v147, 0\n\t" \
  "v_mov_b32 v148, 0\n\t" \
  "v_mov_b32 v149, 0\n\t" \
  "v_mov_b32 v150, 0\n\t" \
  "v_mov_b32 v151, 0\n\t" \
  "v_mov_b32 v152, 0\n\t" \
  "v_mov_b32 v153, 0\n\t" \
  "v_mov_b32 v154, 0\n\t" \
  "v_mov_b32 v155, 0\n\t" \
  "v_mov_b32 v156, 0\n\t" \
  "v_mov_b32 v157, 0\n\t" \
  "v_mov_b32 v158, 0\n\t" \
  "v_mov_b32 v159, 0\n\t" \
  "v_mov_b32 v160, 0\n\t" \
  "v_mov_b32 v161, 0\n\t" \
  "v_mov_b32 v162, 0\n\t" \
  "v_mov_b32 v163, 0\n\t" \
  "v_mov_b32 v164, 0\n\t" \
  "v_mov_b32 v165, 0\n\t" \
  "v_mov_b32 v166, 0\n\t" \
  "v_mov_b32 v167, 0\n\t" \
  "s_mov_b32 s68, 0x50000\n\t" \
  "s_mov_b32 s69, 0x5550055\n\t" \
  "s_mov_b32 s70, 0xa0000\n\t" \
  "s_mov_b32 s71, 0xaaa00aa\n\t" \
  "s_mov_b32 s72, 0x150001\n\t" \
  "s_mov_b32 s73, 0x15550155\n\t" \
  "s_mov_b32 s74, 0x2a0002\n\t" \
  "s_mov_b32 s75, 0x2aaa02aa\n\t" \
  "s_mov_b32 s76, 0x55505555\n\t" \
  "s_mov_b32 s77, 0x50005500\n\t" \
  "s_mov_b32 s78, 0xaaa0aaaa\n\t" \
  "s_mov_b32 s79, 0xa000aa00\n\t" \
  "s_mov_b32 s80, 0x55405554\n\t" \
  "s_mov_b32 s81, 0x40005400\n\t" \
  "s_mov_b32 s82, 0xaa80aaa8\n\t" \
  "s_mov_b32 s83, 0x8000a800\n\t" \
  "s_mov_b32 s84, 0xaaaaaaaa\n\t" \
  "s_mov_b32 s85, 0xaaaaaaaa\n\t" \
  "v_add_u32 v60, 36864, v58\n\t" \
  "v_add_u32 v61, 36864, v59\n\t" \
  "s_mov_b32 %[it], 0\n\t" \
  "s_cmp_lg_u32 %[r], 0\n\t" \
  "s_barrier\n\t" \
  "s_cbranch_scc1 NB_%=\n\t" \
  "ds_read_b64_tr_b16 v[40:41], v60 offset:16384\n\t" \
  "ds_read_b64_tr_b16 v[42:43], v60 offset:16896\n\t" \
  "ds_read_b64_tr_b16 v[44:45], v60 offset:20480\n\t" \
  "ds_read_b64_tr_b16 v[46:47], v60 offset:20992\n\t" \
  "ds_read_b64_tr_b16 v[48:49], v61 offset:16384\n\t" \
  "ds_read_b64_tr_b16 v[50:51], v61 offset:16896\n\t" \
  "ds_read_b128 v[0:3], v56 offset:36864\n\t" \
  "ds_read_b128 v[4:7], v56 offset:38912\n\t" \
  "ds_read_b128 v[8:11], v56 offset:40960\n\t" \
  "ds_read_b128 v[12:15], v56 offset:43008\n\t" \
  "ds_read_b128 v[16:19], v56 offset:45056\n\t" \
  "s_waitcnt lgkmcnt(0)\n\t" \
  "s_branch N4a_%=\n\t" \
  "N0a_%=:\n\t" \
  "s_waitcnt lgkmcnt(4)\n\t" \
  "v_mfma_f32_16x16x32_bf16 v[160:163], v[40:43], v[0:3], v[160:163]\n\t" \
  "ds_read_b64_tr_b16 v[52:53], v59 offset:8192\n\t" \
  "v_mfma_f32_16x16x32_bf16 v[96:99], v[40:43], v[4:7], v[96:99]\n\t" \
  "ds_read_b64_tr_b16 v[54:55], v59 offset:8704\n\t" \
  "v_mfma_f32_16x16x32_bf16 v[104:107], v[40:43], v[8:11], v[104:107]\n\t" \
  "s_barrier\n\t" \
  "v_mfma_f32_16x16x32_bf16 v[112:115], v[40:43], v[12:15], v[112:115]\n\t" \
  "v_mfma_f32_16x16x32_bf16 v[120:123], v[40:43], v[16:19], v[120:123]\n\t" \
  "s_waitcnt lgkmcnt(4)\n\t" \
  "v_mfma_f32_16x16x32_bf16 v[128:131], v[4:7], v[44:47], v[128:131]\n\t" \
  "ds_read_b64_tr_b16 v[40:41], v58 offset:0\n\t" \
  "v_mfma_f32_16x16x32_bf16 v[136:139], v[8:11], v[44:47], v[136:139]\n\t" \
  "ds_read_b64_tr_b16 v[42:43], v58 offset:512\n\t" \
  "v_mfma_f32_16x16x32_bf16 v[144:147], v[12:15], v[44:47], v[144:147]\n\t" \
  "ds_read_b128 v[20:23], v57 offset:0\n\t" \
  "v_mfma_f32_16x16x32_bf16 v[152:155], v[16:19], v[44:47], v[152:155]\n\t" \
  "ds_read_b128 v[24:27], v57 offset:2048\n\t" \
  "s_waitcnt lgkmcnt(6)\n\t" \
  "v_mfma_f32_16x16x32_bf16 v[164:167], v[48:51], v[0:3], v[164:167]\n\t" \
  "ds_read_b64_tr_b16 v[44:45], v59 offset:0\n\t" \
  "v_mfma_f32_16x16x32_bf16 v[100:103], v[48:51], v[4:7], v[100:103]\n\t" \
  "ds_read_b64_tr_b16 v[46:47], v59 offset:512\n\t" \
  "v_mfma_f32_16x16x32_bf16 v[108:111], v[48:51], v[8:11], v[108:111]\n\t" \
  "ds_read_b128 v[28:31], v57 offset:4096\n\t" \
  "v_mfma_f32_16x16x32_bf16 v[116:119], v[48:51], v[12:15], v[116:119]\n\t" \
  "ds_read_b128 v[32:35], v57 offset:6144\n\t" \
  "v_mfma_f32_16x16x32_bf16 v[124:127], v[48:51], v[16:19], v[124:127]\n\t" \
  "s_waitcnt lgkmcnt(8)\n\t" \
  "v_mfma_f32_16x16x32_bf16 v[132:135], v[4:7], v[52:55], v[132:135]\n\t" \
  "ds_read_b64_tr_b16 v[48:49], v58 offset:16384\n\t" \
  "v_mfma_f32_16x16x32_bf16 v[140:143], v[8:11], v[52:55], v[140:143]\n\t" \
  "ds_read_b64_tr_b16 v[50:51], v58 offset:16896\n\t" \
  "v_mfma_f32_16x16x32_bf16 v[148:151], v[12:15], v[52:55], v[148:151]\n\t" \
  "ds_read_b128 v[36:39], v57 offset:8192\n\t" \
  "v_mfma_f32_16x16x32_bf16 v[156:159], v[16:19], v[52:55], v[156:159]\n\t" \
  "s_waitcnt lgkmcnt(9)\n\t" \
  "v_mfma_f32_16x16x32_bf16 v[64:67], v[40:43], v[4:7], v[64:67]\n\t" \
  "ds_read_b64_tr_b16 v[52:53], v58 offset:20480\n\t" \
  "v_mfma_f32_16x16x32_bf16 v[72:75], v[40:43], v[8:11], v[72:75]\n\t" \
  "ds_read_b64_tr_b16 v[54:55], v58 offset:20992\n\t" \
  "v_mfma_f32_16x16x32_bf16 v[80:83], v[40:43], v[12:15], v[80:83]\n\t" \
  "v_mfma_f32_16x16x32_bf16 v[88:91], v[40:43], v[16:19], v[88:91]\n\t" \
  "s_waitcnt lgkmcnt(7)\n\t" \
  "v_mfma_f32_16x16x32_bf16 v[68:71], v[44:47], v[4:7], v[68:71]\n\t" \
  "ds_read_b64_tr_b16 v[40:41], v59 offset:16384\n\t" \
  "v_mfma_f32_16x16x32_bf16 v[76:79], v[44:47], v[8:11], v[76:79]\n\t" \
  "ds_read_b64_tr_b16 v[42:43], v59 offset:16896\n\t" \
  "v_mfma_f32_16x16x32_bf16 v[84:87], v[44:47], v[12:15], v[84:87]\n\t" \
  "v_mfma_f32_16x16x32_bf16 v[92:95], v[44:47], v[16:19], v[92:95]\n\t" \
  "N1a_%=:\n\t" \
  "s_waitcnt lgkmcnt(4)\n\t" \
  "v_mfma_f32_16x16x32_bf16 v[160:163], v[48:51], v[20:23], v[160:163]\n\t" \
  "ds_read_b64_tr_b16 v[44:45], v59 offset:20480\n\t" \
  "v_mfma_f32_16x16x32_bf16 v[96:99], v[48:51], v[24:27], v[96:99]\n\t" \
  "ds_read_b64_tr_b16 v[46:47], v59 offset:20992\n\t" \
  "v_mfma_f32_16x16x32_bf16 v[104:107], v[48:51], v[28:31], v[104:107]\n\t" \
  "s_barrier\n\t" \
  "v_mfma_f32_16x16x32_bf16 v[112:115], v[48:51], v[32:35], v[112:115]\n\t" \
  "v_mfma_f32_16x16x32_bf16 v[120:123], v[48:51], v[36:39], v[120:123]\n\t" \
  "s_waitcnt lgkmcnt(4)\n\t" \
  "v_mfma_f32_16x16x32_bf16 v[128:131], v[24:27], v[52:55], v[128:131]\n\t" \
  "ds_read_b64_tr_b16 v[48:49], v58 offset:12288\n\t" \
  "v_mfma_f32_16x16x32_bf16 v[136:139], v[28:31], v[52:55], v[136:139]\n\t" \
  "ds_read_b64_tr_b16 v[50:51], v58 offset:12800\n\t" \
  "v_mfma_f32_16x16x32_bf16 v[144:147], v[32:35], v[52:55], v[144:147]\n\t" \
  "ds_read_b128 v[0:3], v56 offset:18432\n\t" \
  "v_mfma_f32_16x16x32_bf16 v[152:155], v[36:39], v[52:55], v[152:155]\n\t" \
  "ds_read_b128 v[4:7], v56 offset:20480\n\t" \
  "s_waitcnt lgkmcnt(6)\n\t" \
  "v_mfma_f32_16x16x32_bf16 v[164:167], v[40:43], v[20:23], v[164:167]\n\t" \
  "ds_read_b64_tr_b16 v[52:53], v59 offset:12288\n\t" \
  "v_mfma_f32_16x16x32_bf16 v[100:103], v[40:43], v[24:27], v[100:103]\n\t" \
  "ds_read_b64_tr_b16 v[54:55], v59 offset:12800\n\t" \
  "v_mfma_f32_16x16x32_bf16 v[108:111], v[40:43], v[28:31], v[108:111]\n\t" \
  "ds_read_b128 v[8:11], v56 offset:22528\n\t" \
  "v_mfma_f32_16x16x32_bf16 v[116:119], v[40:43], v[32:35], v[116:119]\n\t" \
  "ds_read_b128 v[12:15], v56 offset:24576\n\t" \
  "v_mfma_f32_16x16x32_bf16 v[124:127], v[40:43], v[36:39], v[124:127]\n\t" \
  "s_waitcnt lgkmcnt(8)\n\t" \
  "v_mfma_f32_16x16x32_bf16 v[132:135], v[24:27], v[44:47], v[132:135]\n\t" \
  "ds_read_b64_tr_b16 v[40:41], v58 offset:28672\n\t" \
  "v_mfma_f32_16x16x32_bf16 v[140:143], v[28:31], v[44:47], v[140:143]\n\t" \
  "ds_read_b64_tr_b16 v[42:43], v58 offset:29184\n\t" \
  "v_mfma_f32_16x16x32_bf16 v[148:151], v[32:35], v[44:47], v[148:151]\n\t" \
  "ds_read_b128 v[16:19], v56 offset:26624\n\t" \
  "v_mfma_f32_16x16x32_bf16 v[156:159], v[36:39], v[44:47], v[156:159]\n\t" \
  "s_waitcnt lgkmcnt(9)\n\t" \
  "v_mfma_f32_16x16x32_bf16 v[64:67], v[48:51], v[24:27], v[64:67]\n\t" \
  "ds_read_b64_tr_b16 v[44:45], v58 offset:32768\n\t" \
  "v_mfma_f32_16x16x32_bf16 v[72:75], v[48:51], v[28:31], v[72:75]\n\t" \
  "ds_read_b64_tr_b16 v[46:47], v58 offset:33280\n\t" \
  "v_mfma_f32_16x16x32_bf16 v[80:83], v[48:51], v[32:35], v[80:83]\n\t" \
  "v_mfma_f32_16x16x32_bf16 v[88:91], v[48:51], v[36:39], v[88:91]\n\t" \
  "s_waitcnt lgkmcnt(7)\n\t" \
  "v_mfma_f32_16x16x32_bf16 v[68:71], v[52:55], v[24:27], v[68:71]\n\t" \
  "ds_read_b64_tr_b16 v[48:49], v59 offset:28672\n\t" \
  "v_mfma_f32_16x16x32_bf16 v[76:79], v[52:55], v[28:31], v[76:79]\n\t" \
  "ds_read_b64_tr_b16 v[50:51], v59 offset:29184\n\t" \
  "v_mfma_f32_16x16x32_bf16 v[84:87], v[52:55], v[32:35], v[84:87]\n\t" \
  "v_mfma_f32_16x16x32_bf16 v[92:95], v[52:55], v[36:39], v[92:95]\n\t" \
  "N2a_%=:\n\t" \
  "s_waitcnt lgkmcnt(4)\n\t" \
  "v_mfma_f32_16x16x32_bf16 v[160:163], v[40:43], v[0:3], v[160:163]\n\t" \
  "ds_read_b64_tr_b16 v[52:53], v59 offset:32768\n\t" \
  "v_mfma_f32_16x16x32_bf16 v[96:99], v[40:43], v[4:7], v[96:99]\n\t" \
  "ds_read_b64_tr_b16 v[54:55], v59 offset:33280\n\t" \
  "v_mfma_f32_16x16x32_bf16 v[104:107], v[40:43], v[8:11], v[104:107]\n\t" \
  "s_barrier\n\t" \
  "v_mfma_f32_16x16x32_bf16 v[112:115], v[40:43], v[12:15], v[112:115]\n\t" \
  "v_mfma_f32_16x16x32_bf16 v[120:123], v[40:43], v[16:19], v[120:123]\n\t" \
  "s_waitcnt lgkmcnt(4)\n\t" \
  "v_mfma_f32_16x16x32_bf16 v[128:131], v[4:7], v[44:47], v[128:131]\n\t" \
  "ds_read_b64_tr_b16 v[40:41], v58 offset:24576\n\t" \
  "v_mfma_f32_16x16x32_bf16 v[136:139], v[8:11], v[44:47], v[136:139]\n\t" \
  "ds_read_b64_tr_b16 v[42:43], v58 offset:25088\n\t" \
  "v_mfma_f32_16x16x32_bf16 v[144:147], v[12:15], v[44:47], v[144:147]\n\t" \
  "ds_read_b128 v[20:23], v57 offset:18432\n\t" \
  "v_mfma_f32_16x16x32_bf16 v[152:155], v[16:19], v[44:47], v[152:155]\n\t" \
  "ds_read_b128 v[24:27], v57 offset:20480\n\t" \
  "s_waitcnt lgkmcnt(6)\n\t" \
  "v_mfma_f32_16x16x32_bf16 v[164:167], v[48:51], v[0:3], v[164:167]\n\t" \
  "ds_read_b64_tr_b16 v[44:45], v59 offset:24576\n\t" \
  "v_mfma_f32_16x16x32_bf16 v[100:103], v[48:51], v[4:7], v[100:103]\n\t" \
  "ds_read_b64_tr_b16 v[46:47], v59 offset:25088\n\t" \
  "v_mfma_f32_16x16x32_bf16 v[108:111], v[48:51], v[8:11], v[108:111]\n\t" \
  "ds_read_b128 v[28:31], v57 offset:22528\n\t" \
  "v_mfma_f32_16x16x32_bf16 v[116:119], v[48:51], v[12:15], v[116:119]\n\t" \
  "ds_read_b128 v[32:35], v57 offset:24576\n\t" \
  "v_mfma_f32_16x16x32_bf16 v[124:127], v[48:51], v[16:19], v[124:127]\n\t" \
  "s_waitcnt lgkmcnt(8)\n\t" \
  "v_mfma_f32_16x16x32_bf16 v[132:135], v[4:7], v[52:55], v[132:135]\n\t" \
  "ds_read_b64_tr_b16 v[48:49], v60 offset:4096\n\t" \
  "v_mfma_f32_16x16x32_bf16 v[140:143], v[8:11], v[52:55], v[140:143]\n\t" \
  "ds_read_b64_tr_b16 v[50:51], v60 offset:4608\n\t" \
  "v_mfma_f32_16x16x32_bf16 v[148:151], v[12:15], v[52:55], v[148:151]\n\t" \
  "ds_read_b128 v[36:39], v57 offset:26624\n\t" \
  "v_mfma_f32_16x16x32_bf16 v[156:159], v[16:19], v[52:55], v[156:159]\n\t" \
  "s_waitcnt lgkmcnt(9)\n\t" \
  "v_mfma_f32_16x16x32_bf16 v[64:67], v[40:43], v[4:7], v[64:67]\n\t" \
  "ds_read_b64_tr_b16 v[52:53], v60 offset:8192\n\t" \
  "v_mfma_f32_16x16x32_bf16 v[72:75], v[40:43], v[8:11], v[72:75]\n\t" \
  "ds_read_b64_tr_b16 v[54:55], v60 offset:8704\n\t" \
  "v_mfma_f32_16x16x32_bf16 v[80:83], v[40:43], v[12:15], v[80:83]\n\t" \
  "v_mfma_f32_16x16x32_bf16 v[88:91], v[40:43], v[16:19], v[88:91]\n\t" \
  "s_waitcnt lgkmcnt(7)\n\t" \
  "v_mfma_f32_16x16x32_bf16 v[68:71], v[44:47], v[4:7], v[68:71]\n\t" \
  "ds_read_b64_tr_b16 v[40:41], v61 offset:4096\n\t" \
  "v_mfma_f32_16x16x32_bf16 v[76:79], v[44:47], v[8:11], v[76:79]\n\t" \
  "ds_read_b64_tr_b16 v[42:43], v61 offset:4608\n\t" \
  "v_mfma_f32_16x16x32_bf16 v[84:87], v[44:47], v[12:15], v[84:87]\n\t" \
  "v_mfma_f32_16x16x32_bf16 v[92:95], v[44:47], v[16:19], v[92:95]\n\t" \
  "N3a_%=:\n\t" \
  "s_waitcnt lgkmcnt(4)\n\t" \
  "v_mfma_f32_16x16x32_bf16 v[160:163], v[48:51], v[20:23], v[160:163]\n\t" \
  "ds_read_b64_tr_b16 v[44:45], v61 offset:8192\n\t" \
  "v_mfma_f32_16x16x32_bf16 v[96:99], v[48:51], v[24:27], v[96:99]\n\t" \
  "ds_read_b64_tr_b16 v[46:47], v61 offset:8704\n\t" \
  "v_mfma_f32_16x16x32_bf16 v[104:107], v[48:51], v[28:31], v[104:107]\n\t" \
  "s_barrier\n\t" \
  "v_mfma_f32_16x16x32_bf16 v[112:115], v[48:51], v[32:35], v[112:115]\n\t" \
  "v_mfma_f32_16x16x32_bf16 v[120:123], v[48:51], v[36:39], v[120:123]\n\t" \
  "s_waitcnt lgkmcnt(4)\n\t" \
  "v_mfma_f32_16x16x32_bf16 v[128:131], v[24:27], v[52:55], v[128:131]\n\t" \
  "ds_read_b64_tr_b16 v[48:49], v60 offset:0\n\t" \
  "v_mfma_f32_16x16x32_bf16 v[136:139], v[28:31], v[52:55], v[136:139]\n\t" \
  "ds_read_b64_tr_b16 v[50:51], v60 offset:512\n\t" \
  "v_mfma_f32_16x16x32_bf16 v[144:147], v[32:35], v[52:55], v[144:147]\n\t" \
  "ds_read_b128 v[0:3], v56 offset:36864\n\t" \
  "v_mfma_f32_16x16x32_bf16 v[152:155], v[36:39], v[52:55], v[152:155]\n\t" \
  "ds_read_b128 v[4:7], v56 offset:38912\n\t" \
  "s_waitcnt lgkmcnt(6)\n\t" \
  "v_mfma_f32_16x16x32_bf16 v[164:167], v[40:43], v[20:23], v[164:167]\n\t" \
  "ds_read_b64_tr_b16 v[52:53], v61 offset:0\n\t" \
  "v_mfma_f32_16x16x32_bf16 v[100:103], v[40:43], v[24:27], v[100:103]\n\t" \
  "ds_read_b64_tr_b16 v[54:55], v61 offset:512\n\t" \
  "v_mfma_f32_16x16x32_bf16 v[108:111], v[40:43], v[28:31], v[108:111]\n\t" \
  "ds_read_b128 v[8:11], v56 offset:40960\n\t" \
  "v_mfma_f32_16x16x32_bf16 v[116:119], v[40:43], v[32:35], v[116:119]\n\t" \
  "ds_read_b128 v[12:15], v56 offset:43008\n\t" \
  "v_mfma_f32_16x16x32_bf16 v[124:127], v[40:43], v[36:39], v[124:127]\n\t" \
  "s_waitcnt lgkmcnt(8)\n\t" \
  "v_mfma_f32_16x16x32_bf16 v[132:135], v[24:27], v[44:47], v[132:135]\n\t" \
  "ds_read_b64_tr_b16 v[40:41], v60 offset:16384\n\t" \
  "v_mfma_f32_16x16x32_bf16 v[140:143], v[28:31], v[44:47], v[140:143]\n\t" \
  "ds_read_b64_tr_b16 v[42:43], v60 offset:16896\n\t" \
  "v_mfma_f32_16x16x32_bf16 v[148:151], v[32:35], v[44:47], v[148:151]\n\t" \
  "ds_read_b128 v[16:19], v56 offset:45056\n\t" \
  "v_mfma_f32_16x16x32_bf16 v[156:159], v[36:39], v[44:47], v[156:159]\n\t" \
  "s_waitcnt lgkmcnt(9)\n\t" \
  "v_mfma_f32_16x16x32_bf16 v[64:67], v[48:51], v[24:27], v[64:67]\n\t" \
  "ds_read_b64_tr_b16 v[44:45], v60 offset:20480\n\t" \
  "v_mfma_f32_16x16x32_bf16 v[72:75], v[48:51], v[28:31], v[72:75]\n\t" \
  "ds_read_b64_tr_b16 v[46:47], v60 offset:20992\n\t" \
  "v_mfma_f32_16x16x32_bf16 v[80:83], v[48:51], v[32:35], v[80:83]\n\t" \
  "v_mfma_f32_16x16x32_bf16 v[88:91], v[48:51], v[36:39], v[88:91]\n\t" \
  "s_waitcnt lgkmcnt(7)\n\t" \
  "v_mfma_f32_16x16x32_bf16 v[68:71], v[52:55], v[24:27], v[68:71]\n\t" \
  "ds_read_b64_tr_b16 v[48:49], v61 offset:16384\n\t" \
  "v_mfma_f32_16x16x32_bf16 v[76:79], v[52:55], v[28:31], v[76:79]\n\t" \
  "ds_read_b64_tr_b16 v[50:51], v61 offset:16896\n\t" \
  "v_mfma_f32_16x16x32_bf16 v[84:87], v[52:55], v[32:35], v[84:87]\n\t" \
  "v_mfma_f32_16x16x32_bf16 v[92:95], v[52:55], v[36:39], v[92:95]\n\t" \
  "N4a_%=:\n\t" \
  "s_waitcnt lgkmcnt(4)\n\t" \
  "v_mfma_f32_16x16x32_bf16 v[160:163], v[40:43], v[0:3], v[160:163]\n\t" \
  "ds_read_b64_tr_b16 v[52:53], v61 offset:20480\n\t" \
  "v_mfma_f32_16x16x32_bf16 v[96:99], v[40:43], v[4:7], v[96:99]\n\t" \
  "ds_read_b64_tr_b16 v[54:55], v61 offset:20992\n\t" \
  "v_mfma_f32_16x16x32_bf16 v[104:107], v[40:43], v[8:11], v[104:107]\n\t" \
  "s_barrier\n\t" \
  "v_mfma_f32_16x16x32_bf16 v[112:115], v[40:43], v[12:15], v[112:115]\n\t" \
  "v_mfma_f32_16x16x32_bf16 v[120:123], v[40:43], v[16:19], v[120:123]\n\t" \
  "s_waitcnt lgkmcnt(4)\n\t" \
  "v_mfma_f32_16x16x32_bf16 v[128:131], v[4:7], v[44:47], v[128:131]\n\t" \
  "ds_read_b64_tr_b16 v[40:41], v60 offset:12288\n\t" \
  "v_mfma_f32_16x16x32_bf16 v[136:139], v[8:11], v[44:47], v[136:139]\n\t" \
  "ds_read_b64_tr_b16 v[42:43], v60 offset:12800\n\t" \
  "v_mfma_f32_16x16x32_bf16 v[144:147], v[12:15], v[44:47], v[144:147]\n\t" \
  "ds_read_b128 v[20:23], v57 offset:36864\n\t" \
  "v_mfma_f32_16x16x32_bf16 v[152:155], v[16:19], v[44:47], v[152:155]\n\t" \
  "ds_read_b128 v[24:27], v57 offset:38912\n\t" \
  "s_waitcnt lgkmcnt(6)\n\t" \
  "v_mfma_f32_16x16x32_bf16 v[164:167], v[48:51], v[0:3], v[164:167]\n\t" \
  "ds_read_b64_tr_b16 v[44:45], v61 offset:12288\n\t" \
  "v_mfma_f32_16x16x32_bf16 v[100:103], v[48:51], v[4:7], v[100:103]\n\t" \
  "ds_read_b64_tr_b16 v[46:47], v61 offset:12800\n\t" \
  "v_mfma_f32_16x16x32_bf16 v[108:111], v[48:51], v[8:11], v[108:111]\n\t" \
  "ds_read_b128 v[28:31], v57 offset:40960\n\t" \
  "v_mfma_f32_16x16x32_bf16 v[116:119], v[48:51], v[12:15], v[116:119]\n\t" \
  "ds_read_b128 v[32:35], v57 offset:43008\n\t" \
  "v_mfma_f32_16x16x32_bf16 v[124:127], v[48:51], v[16:19], v[124:127]\n\t" \
  "s_waitcnt lgkmcnt(8)\n\t" \
  "v_mfma_f32_16x16x32_bf16 v[132:135], v[4:7], v[52:55], v[132:135]\n\t" \
  "ds_read_b64_tr_b16 v[48:49], v60 offset:28672\n\t" \
  "v_mfma_f32_16x16x32_bf16 v[140:143], v[8:11], v[52:55], v[140:143]\n\t" \
  "ds_read_b64_tr_b16 v[50:51], v60 offset:29184\n\t" \
  "v_mfma_f32_16x16x32_bf16 v[148:151], v[12:15], v[52:55], v[148:151]\n\t" \
  "ds_read_b128 v[36:39], v57 offset:45056\n\t" \
  "v_mfma_f32_16x16x32_bf16 v[156:159], v[16:19], v[52:55], v[156:159]\n\t" \
  "s_waitcnt lgkmcnt(9)\n\t" \
  "v_mfma_f32_16x16x32_bf16 v[64:67], v[40:43], v[4:7], v[64:67]\n\t" \
  "ds_read_b64_tr_b16 v[52:53], v60 offset:32768\n\t" \
  "v_mfma_f32_16x16x32_bf16 v[72:75], v[40:43], v[8:11], v[72:75]\n\t" \
  "ds_read_b64_tr_b16 v[54:55], v60 offset:33280\n\t" \
  "v_mfma_f32_16x16x32_bf16 v[80:83], v[40:43], v[12:15], v[80:83]\n\t" \
  "v_mfma_f32_16x16x32_bf16 v[88:91], v[40:43], v[16:19], v[88:91]\n\t" \
  "s_waitcnt lgkmcnt(7)\n\t" \
  "v_mfma_f32_16x16x32_bf16 v[68:71], v[44:47], v[4:7], v[68:71]\n\t" \
  "ds_read_b64_tr_b16 v[40:41], v61 offset:28672\n\t" \
  "v_mfma_f32_16x16x32_bf16 v[76:79], v[44:47], v[8:11], v[76:79]\n\t" \
  "ds_read_b64_tr_b16 v[42:43], v61 offset:29184\n\t" \
  "v_mfma_f32_16x16x32_bf16 v[84:87], v[44:47], v[12:15], v[84:87]\n\t" \
  "v_mfma_f32_16x16x32_bf16 v[92:95], v[44:47], v[16:19], v[92:95]\n\t" \
  "N5a_%=:\n\t" \
  "s_waitcnt lgkmcnt(4)\n\t" \
  "v_mfma_f32_16x16x32_bf16 v[160:163], v[48:51], v[20:23], v[160:163]\n\t" \
  "ds_read_b64_tr_b16 v[44:45], v61 offset:32768\n\t" \
  "v_mfma_f32_16x16x32_bf16 v[96:99], v[48:51], v[24:27], v[96:99]\n\t" \
  "ds_read_b64_tr_b16 v[46:47], v61 offset:33280\n\t" \
  "v_mfma_f32_16x16x32_bf16 v[104:107], v[48:51], v[28:31], v[104:107]\n\t" \
  "s_barrier\n\t" \
  "v_mfma_f32_16x16x32_bf16 v[112:115], v[48:51], v[32:35], v[112:115]\n\t" \
  "v_mfma_f32_16x16x32_bf16 v[120:123], v[48:51], v[36:39], v[120:123]\n\t" \
  "s_waitcnt lgkmcnt(4)\n\t" \
  "v_mfma_f32_16x16x32_bf16 v[128:131], v[24:27], v[52:55], v[128:131]\n\t" \
  "ds_read_b64_tr_b16 v[48:49], v60 offset:24576\n\t" \
  "v_mfma_f32_16x16x32_bf16 v[136:139], v[28:31], v[52:55], v[136:139]\n\t" \
  "ds_read_b64_tr_b16 v[50:51], v60 offset:25088\n\t" \
  "v_mfma_f32_16x16x32_bf16 v[144:147], v[32:35], v[52:55], v[144:147]\n\t" \
  "ds_read_b128 v[0:3], v56 offset:0\n\t" \
  "v_mfma_f32_16x16x32_bf16 v[152:155], v[36:39], v[52:55], v[152:155]\n\t" \
  "ds_read_b128 v[4:7], v56 offset:2048\n\t" \
  "s_waitcnt lgkmcnt(6)\n\t" \
  "v_mfma_f32_16x16x32_bf16 v[164:167], v[40:43], v[20:23], v[164:167]\n\t" \
  "ds_read_b64_tr_b16 v[52:53], v61 offset:24576\n\t" \
  "v_mfma_f32_16x16x32_bf16 v[100:103], v[40:43], v[24:27], v[100:103]\n\t" \
  "ds_read_b64_tr_b16 v[54:55], v61 offset:25088\n\t" \
  "v_mfma_f32_16x16x32_bf16 v[108:111], v[40:43], v[28:31], v[108:111]\n\t" \
  "ds_read_b128 v[8:11], v56 offset:4096\n\t" \
  "v_mfma_f32_16x16x32_bf16 v[116:119], v[40:43], v[32:35], v[116:119]\n\t" \
  "ds_read_b128 v[12:15], v56 offset:6144\n\t" \
  "v_mfma_f32_16x16x32_bf16 v[124:127], v[40:43], v[36:39], v[124:127]\n\t" \
  "s_waitcnt lgkmcnt(8)\n\t" \
  "v_mfma_f32_16x16x32_bf16 v[132:135], v[24:27], v[44:47], v[132:135]\n\t" \
  "ds_read_b64_tr_b16 v[40:41], v58 offset:4096\n\t" \
  "v_mfma_f32_16x16x32_bf16 v[140:143], v[28:31], v[44:47], v[140:143]\n\t" \
  "ds_read_b64_tr_b16 v[42:43], v58 offset:4608\n\t" \
  "v_mfma_f32_16x16x32_bf16 v[148:151], v[32:35], v[44:47], v[148:151]\n\t" \
  "ds_read_b128 v[16:19], v56 offset:8192\n\t" \
  "v_mfma_f32_16x16x32_bf16 v[156:159], v[36:39], v[44:47], v[156:159]\n\t" \
  "s_waitcnt lgkmcnt(9)\n\t" \
  "v_mfma_f32_16x16x32_bf16 v[64:67], v[48:51], v[24:27], v[64:67]\n\t" \
  "ds_read_b64_tr_b16 v[44:45], v58 offset:8192\n\t" \
  "v_mfma_f32_16x16x32_bf16 v[72:75], v[48:51], v[28:31], v[72:75]\n\t" \
  "ds_read_b64_tr_b16 v[46:47], v58 offset:8704\n\t" \
  "v_mfma_f32_16x16x32_bf16 v[80:83], v[48:51], v[32:35], v[80:83]\n\t" \
  "v_mfma_f32_16x16x32_bf16 v[88:91], v[48:51], v[36:39], v[88:91]\n\t" \
  "s_waitcnt lgkmcnt(7)\n\t" \
  "v_mfma_f32_16x16x32_bf16 v[68:71], v[52:55], v[24:27], v[68:71]\n\t" \
  "ds_read_b64_tr_b16 v[48:49], v59 offset:4096\n\t" \
  "v_mfma_f32_16x16x32_bf16 v[76:79], v[52:55], v[28:31], v[76:79]\n\t" \
  "ds_read_b64_tr_b16 v[50:51], v59 offset:4608\n\t" \
  "s_addk_i32 %[it], 1\n\t" \
  "v_mfma_f32_16x16x32_bf16 v[84:87], v[52:55], v[32:35], v[84:87]\n\t" \
  "s_cmp_lt_u32 %[it], 6\n\t" \
  "v_mfma_f32_16x16x32_bf16 v[92:95], v[52:55], v[36:39], v[92:95]\n\t" \
  "s_cbranch_scc1 N0a_%=\n\t" \
  "s_setprio 3\n\t" \
  "v_cvt_pk_bf16_f32 v120, v120, v121\n\t" \
  "v_cvt_pk_bf16_f32 v121, v122, v123\n\t" \
  "v_cvt_pk_bf16_f32 v122, v124, v125\n\t" \
  "v_cvt_pk_bf16_f32 v123, v126, v127\n\t" \
  "v_cvt_pk_bf16_f32 v28, v152, v153\n\t" \
  "v_cvt_pk_bf16_f32 v29, v154, v155\n\t" \
  "v_cvt_pk_bf16_f32 v38, v156, v157\n\t" \
  "v_cvt_pk_bf16_f32 v39, v158, v159\n\t" \
  "ds_write_b128 v63, v[120:123]\n\t" \
  "ds_write2_b64 v63, v[28:29], v[38:39] offset0:128 offset1:129\n\t" \
  "v_mov_b32 v57, %[fa]\n\t" \
  "v_mov_b32 v58, 1\n\t" \
  "s_mov_b64 exec, 1\n\t" \
  "ds_write_b32 v57, v58\n\t" \
  "s_mov_b64 exec, -1\n\t" \
  "v_cvt_pk_bf16_f32 v160, v160, v161\n\t" \
  "v_cvt_pk_bf16_f32 v161, v162, v163\n\t" \
  "v_cvt_pk_bf16_f32 v162, v164, v165\n\t" \
  "v_cvt_pk_bf16_f32 v163, v166, v167\n\t" \
  "s_branch NX_%=\n\t" \
  "NB_%=:\n\t" \
  "ds_read_b64_tr_b16 v[40:41], v60 offset:20480\n\t" \
  "ds_read_b64_tr_b16 v[42:43], v60 offset:20992\n\t" \
  "ds_read_b64_tr_b16 v[44:45], v60 offset:16384\n\t" \
  "ds_read_b64_tr_b16 v[46:47], v60 offset:16896\n\t" \
  "ds_read_b64_tr_b16 v[48:49], v61 offset:20480\n\t" \
  "ds_read_b64_tr_b16 v[50:51], v61 offset:20992\n\t" \
  "ds_read_b128 v[0:3], v56 offset:36864\n\t" \
  "ds_read_b128 v[4:7], v56 offset:47104\n\t" \
  "ds_read_b128 v[8:11], v56 offset:49152\n\t" \
  "ds_read_b128 v[12:15], v56 offset:51200\n\t" \
  "ds_read_b128 v[16:19], v56 offset:53248\n\t" \
  "s_waitcnt lgkmcnt(0)\n\t" \
  "s_branch N4b_%=\n\t" \
  "N0b_%=:\n\t" \
  "s_waitcnt lgkmcnt(4)\n\t" \
  "v_mfma_f32_16x16x32_bf16 v[160:163], v[0:3], v[40:43], v[160:163]\n\t" \
  "ds_read_b64_tr_b16 v[52:53], v59 offset:4096\n\t" \
  "v_mfma_f32_16x16x32_bf16 v[128:131], v[4:7], v[40:43], v[128:131]\n\t" \
  "ds_read_b64_tr_b16 v[54:55], v59 offset:4608\n\t" \
  "v_mfma_f32_16x16x32_bf16 v[136:139], v[8:11], v[40:43], v[136:139]\n\t" \
  "s_barrier\n\t" \
  "v_mfma_f32_16x16x32_bf16 v[144:147], v[12:15], v[40:43], v[144:147]\n\t" \
  "v_mfma_f32_16x16x32_bf16 v[152:155], v[16:19], v[40:43], v[152:155]\n\t" \
  "s_waitcnt lgkmcnt(4)\n\t" \
  "v_mfma_f32_16x16x32_bf16 v[96:99], v[44:47], v[4:7], v[96:99]\n\t" \
  "ds_read_b64_tr_b16 v[40:41], v58 offset:0\n\t" \
  "v_mfma_f32_16x16x32_bf16 v[104:107], v[44:47], v[8:11], v[104:107]\n\t" \
  "ds_read_b64_tr_b16 v[42:43], v58 offset:512\n\t" \
  "v_mfma_f32_16x16x32_bf16 v[112:115], v[44:47], v[12:15], v[112:115]\n\t" \
  "ds_read_b128 v[20:23], v57 offset:0\n\t" \
  "v_mfma_f32_16x16x32_bf16 v[120:123], v[44:47], v[16:19], v[120:123]\n\t" \
  "ds_read_b128 v[24:27], v57 offset:10240\n\t" \
  "s_waitcnt lgkmcnt(6)\n\t" \
  "v_mfma_f32_16x16x32_bf16 v[164:167], v[0:3], v[48:51], v[164:167]\n\t" \
  "ds_read_b64_tr_b16 v[44:45], v59 offset:0\n\t" \
  "v_mfma_f32_16x16x32_bf16 v[132:135], v[4:7], v[48:51], v[132:135]\n\t" \
  "ds_read_b64_tr_b16 v[46:47], v59 offset:512\n\t" \
  "v_mfma_f32_16x16x32_bf16 v[140:143], v[8:11], v[48:51], v[140:143]\n\t" \
  "ds_read_b128 v[28:31], v57 offset:12288\n\t" \
  "v_mfma_f32_16x16x32_bf16 v[148:151], v[12:15], v[48:51], v[148:151]\n\t" \
  "ds_read_b128 v[32:35], v57 offset:14336\n\t" \
  "v_mfma_f32_16x16x32_bf16 v[156:159], v[16:19], v[48:51], v[156:159]\n\t" \
  "s_waitcnt lgkmcnt(8)\n\t" \
  "v_mfma_f32_16x16x32_bf16 v[100:103], v[52:55], v[4:7], v[100:103]\n\t" \
  "ds_read_b64_tr_b16 v[48:49], v58 offset:20480\n\t" \
  "v_mfma_f32_16x16x32_bf16 v[108:111], v[52:55], v[8:11], v[108:111]\n\t" \
  "ds_read_b64_tr_b16 v[50:51], v58 offset:20992\n\t" \
  "v_mfma_f32_16x16x32_bf16 v[116:119], v[52:55], v[12:15], v[116:119]\n\t" \
  "ds_read_b128 v[36:39], v57 offset:16384\n\t" \
  "v_mfma_f32_16x16x32_bf16 v[124:127], v[52:55], v[16:19], v[124:127]\n\t" \
  "s_waitcnt lgkmcnt(9)\n\t" \
  "v_mfma_f32_16x16x32_bf16 v[64:67], v[40:43], v[4:7], v[64:67]\n\t" \
  "ds_read_b64_tr_b16 v[52:53], v58 offset:16384\n\t" \
  "v_mfma_f32_16x16x32_bf16 v[72:75], v[40:43], v[8:11], v[72:75]\n\t" \
  "ds_read_b64_tr_b16 v[54:55], v58 offset:16896\n\t" \
  "v_mfma_f32_16x16x32_bf16 v[80:83], v[40:43], v[12:15], v[80:83]\n\t" \
  "v_mfma_f32_16x16x32_bf16 v[88:91], v[40:43], v[16:19], v[88:91]\n\t" \
  "s_waitcnt lgkmcnt(7)\n\t" \
  "v_mfma_f32_16x16x32_bf16 v[68:71], v[44:47], v[4:7], v[68:71]\n\t" \
  "ds_read_b64_tr_b16 v[40:41], v59 offset:20480\n\t" \
  "v_mfma_f32_16x16x32_bf16 v[76:79], v[44:47], v[8:11], v[76:79]\n\t" \
  "ds_read_b64_tr_b16 v[42:43], v59 offset:20992\n\t" \
  "v_mfma_f32_16x16x32_bf16 v[84:87], v[44:47], v[12:15], v[84:87]\n\t" \
  "v_mfma_f32_16x16x32_bf16 v[92:95], v[44:47], v[16:19], v[92:95]\n\t" \
  "N1b_%=:\n\t" \
  "s_waitcnt lgkmcnt(4)\n\t" \
  "v_mfma_f32_16x16x32_bf16 v[160:163], v[20:23], v[48:51], v[160:163]\n\t" \
  "ds_read_b64_tr_b16 v[44:45], v59 offset:16384\n\t" \
  "v_mfma_f32_16x16x32_bf16 v[128:131], v[24:27], v[48:51], v[128:131]\n\t" \
  "ds_read_b64_tr_b16 v[46:47], v59 offset:16896\n\t" \
  "v_mfma_f32_16x16x32_bf16 v[136:139], v[28:31], v[48:51], v[136:139]\n\t" \
  "s_barrier\n\t" \
  "v_mfma_f32_16x16x32_bf16 v[144:147], v[32:35], v[48:51], v[144:147]\n\t" \
  "v_mfma_f32_16x16x32_bf16 v[152:155], v[36:39], v[48:51], v[152:155]\n\t" \
  "s_waitcnt lgkmcnt(4)\n\t" \
  "v_mfma_f32_16x16x32_bf16 v[96:99], v[52:55], v[24:27], v[96:99]\n\t" \
  "ds_read_b64_tr_b16 v[48:49], v58 offset:12288\n\t" \
  "v_mfma_f32_16x16x32_bf16 v[104:107], v[52:55], v[28:31], v[104:107]\n\t" \
  "ds_read_b64_tr_b16 v[50:51], v58 offset:12800\n\t" \
  "v_mfma_f32_16x16x32_bf16 v[112:115], v[52:55], v[32:35], v[112:115]\n\t" \
  "ds_read_b128 v[0:3], v56 offset:18432\n\t" \
  "v_mfma_f32_16x16x32_bf16 v[120:123], v[52:55], v[36:39], v[120:123]\n\t" \
  "ds_read_b128 v[4:7], v56 offset:28672\n\t" \
  "s_waitcnt lgkmcnt(6)\n\t" \
  "v_mfma_f32_16x16x32_bf16 v[164:167], v[20:23], v[40:43], v[164:167]\n\t" \
  "ds_read_b64_tr_b16 v[52:53], v59 offset:12288\n\t" \
  "v_mfma_f32_16x16x32_bf16 v[132:135], v[24:27], v[40:43], v[132:135]\n\t" \
  "ds_read_b64_tr_b16 v[54:55], v59 offset:12800\n\t" \
  "v_mfma_f32_16x16x32_bf16 v[140:143], v[28:31], v[40:43], v[140:143]\n\t" \
  "ds_read_b128 v[8:11], v56 offset:30720\n\t" \
  "v_mfma_f32_16x16x32_bf16 v[148:151], v[32:35], v[40:43], v[148:151]\n\t" \
  "ds_read_b128 v[12:15], v56 offset:32768\n\t" \
  "v_mfma_f32_16x16x32_bf16 v[156:159], v[36:39], v[40:43], v[156:159]\n\t" \
  "s_waitcnt lgkmcnt(8)\n\t" \
  "v_mfma_f32_16x16x32_bf16 v[100:103], v[44:47], v[24:27], v[100:103]\n\t" \
  "ds_read_b64_tr_b16 v[40:41], v58 offset:32768\n\t" \
  "v_mfma_f32_16x16x32_bf16 v[108:111], v[44:47], v[28:31], v[108:111]\n\t" \
  "ds_read_b64_tr_b16 v[42:43], v58 offset:33280\n\t" \
  "v_mfma_f32_16x16x32_bf16 v[116:119], v[44:47], v[32:35], v[116:119]\n\t" \
  "ds_read_b128 v[16:19], v56 offset:34816\n\t" \
  "v_mfma_f32_16x16x32_bf16 v[124:127], v[44:47], v[36:39], v[124:127]\n\t" \
  "s_waitcnt lgkmcnt(9)\n\t" \
  "v_mfma_f32_16x16x32_bf16 v[64:67], v[48:51], v[24:27], v[64:67]\n\t" \
  "ds_read_b64_tr_b16 v[44:45], v58 offset:28672\n\t" \
  "v_mfma_f32_16x16x32_bf16 v[72:75], v[48:51], v[28:31], v[72:75]\n\t" \
  "ds_read_b64_tr_b16 v[46:47], v58 offset:29184\n\t" \
  "v_mfma_f32_16x16x32_bf16 v[80:83], v[48:51], v[32:35], v[80:83]\n\t" \
  "v_mfma_f32_16x16x32_bf16 v[88:91], v[48:51], v[36:39], v[88:91]\n\t" \
  "s_waitcnt lgkmcnt(7)\n\t" \
  "v_mfma_f32_16x16x32_bf16 v[68:71], v[52:55], v[24:27], v[68:71]\n\t" \
  "ds_read_b64_tr_b16 v[48:49], v59 offset:32768\n\t" \
  "v_mfma_f32_16x16x32_bf16 v[76:79], v[52:55], v[28:31], v[76:79]\n\t" \
  "ds_read_b64_tr_b16 v[50:51], v59 offset:33280\n\t" \
  "v_mfma_f32_16x16x32_bf16 v[84:87], v[52:55], v[32:35], v[84:87]\n\t" \
  "v_mfma_f32_16x16x32_bf16 v[92:95], v[52:55], v[36:39], v[92:95]\n\t" \
  "N2b_%=:\n\t" \
  "s_waitcnt lgkmcnt(4)\n\t" \
  "v_mfma_f32_16x16x32_bf16 v[160:163], v[0:3], v[40:43], v[160:163]\n\t" \
  "ds_read_b64_tr_b16 v[52:53], v59 offset:28672\n\t" \
  "v_mfma_f32_16x16x32_bf16 v[128:131], v[4:7], v[40:43], v[128:131]\n\t" \
  "ds_read_b64_tr_b16 v[54:55], v59 offset:29184\n\t" \
  "v_mfma_f32_16x16x32_bf16 v[136:139], v[8:11], v[40:43], v[136:139]\n\t" \
  "s_barrier\n\t" \
  "v_mfma_f32_16x16x32_bf16 v[144:147], v[12:15], v[40:43], v[144:147]\n\t" \
  "v_mfma_f32_16x16x32_bf16 v[152:155], v[16:19], v[40:43], v[152:155]\n\t" \
  "s_waitcnt lgkmcnt(4)\n\t" \
  "v_mfma_f32_16x16x32_bf16 v[96:99], v[44:47], v[4:7], v[96:99]\n\t" \
  "ds_read_b64_tr_b16 v[40:41], v58 offset:24576\n\t" \
  "v_mfma_f32_16x16x32_bf16 v[104:107], v[44:47], v[8:11], v[104:107]\n\t" \
  "ds_read_b64_tr_b16 v[42:43], v58 offset:25088\n\t" \
  "v_mfma_f32_16x16x32_bf16 v[112:115], v[44:47], v[12:15], v[112:115]\n\t" \
  "ds_read_b128 v[20:23], v57 offset:18432\n\t" \
  "v_mfma_f32_16x16x32_bf16 v[120:123], v[44:47], v[16:19], v[120:123]\n\t" \
  "ds_read_b128 v[24:27], v57 offset:28672\n\t" \
  "s_waitcnt lgkmcnt(6)\n\t" \
  "v_mfma_f32_16x16x32_bf16 v[164:167], v[0:3], v[48:51], v[164:167]\n\t" \
  "ds_read_b64_tr_b16 v[44:45], v59 offset:24576\n\t" \
  "v_mfma_f32_16x16x32_bf16 v[132:135], v[4:7], v[48:51], v[132:135]\n\t" \
  "ds_read_b64_tr_b16 v[46:47], v59 offset:25088\n\t" \
  "v_mfma_f32_16x16x32_bf16 v[140:143], v[8:11], v[48:51], v[140:143]\n\t" \
  "ds_read_b128 v[28:31], v57 offset:30720\n\t" \
  "v_mfma_f32_16x16x32_bf16 v[148:151], v[12:15], v[48:51], v[148:151]\n\t" \
  "ds_read_b128 v[32:35], v57 offset:32768\n\t" \
  "v_mfma_f32_16x16x32_bf16 v[156:159], v[16:19], v[48:51], v[156:159]\n\t" \
  "s_waitcnt lgkmcnt(8)\n\t" \
  "v_mfma_f32_16x16x32_bf16 v[100:103], v[52:55], v[4:7], v[100:103]\n\t" \
  "ds_read_b64_tr_b16 v[48:49], v60 offset:8192\n\t" \
  "v_mfma_f32_16x16x32_bf16 v[108:111], v[52:55], v[8:11], v[108:111]\n\t" \
  "ds_read_b64_tr_b16 v[50:51], v60 offset:8704\n\t" \
  "v_mfma_f32_16x16x32_bf16 v[116:119], v[52:55], v[12:15], v[116:119]\n\t" \
  "ds_read_b128 v[36:39], v57 offset:34816\n\t" \
  "v_mfma_f32_16x16x32_bf16 v[124:127], v[52:55], v[16:19], v[124:127]\n\t" \
  "s_waitcnt lgkmcnt(9)\n\t" \
  "v_mfma_f32_16x16x32_bf16 v[64:67], v[40:43], v[4:7], v[64:67]\n\t" \
  "ds_read_b64_tr_b16 v[52:53], v60 offset:4096\n\t" \
  "v_mfma_f32_16x16x32_bf16 v[72:75], v[40:43], v[8:11], v[72:75]\n\t" \
  "ds_read_b64_tr_b16 v[54:55], v60 offset:4608\n\t" \
  "v_mfma_f32_16x16x32_bf16 v[80:83], v[40:43], v[12:15], v[80:83]\n\t" \
  "v_mfma_f32_16x16x32_bf16 v[88:91], v[40:43], v[16:19], v[88:91]\n\t" \
  "s_waitcnt lgkmcnt(7)\n\t" \
  "v_mfma_f32_16x16x32_bf16 v[68:71], v[44:47], v[4:7], v[68:71]\n\t" \
  "ds_read_b64_tr_b16 v[40:41], v61 offset:8192\n\t" \
  "v_mfma_f32_16x16x32_bf16 v[76:79], v[44:47], v[8:11], v[76:79]\n\t" \
  "ds_read_b64_tr_b16 v[42:43], v61 offset:8704\n\t" \
  "v_mfma_f32_16x16x32_bf16 v[84:87], v[44:47], v[12:15], v[84:87]\n\t" \
  "v_mfma_f32_16x16x32_bf16 v[92:95], v[44:47], v[16:19], v[92:95]\n\t" \
  "N3b_%=:\n\t" \
  "s_waitcnt lgkmcnt(4)\n\t" \
  "v_mfma_f32_16x16x32_bf16 v[160:163], v[20:23], v[48:51], v[160:163]\n\t" \
  "ds_read_b64_tr_b16 v[44:45], v61 offset:4096\n\t" \
  "v_mfma_f32_16x16x32_bf16 v[128:131], v[24:27], v[48:51], v[128:131]\n\t" \
  "ds_read_b64_tr_b16 v[46:47], v61 offset:4608\n\t" \
  "v_mfma_f32_16x16x32_bf16 v[136:139], v[28:31], v[48:51], v[136:139]\n\t" \
  "s_barrier\n\t" \
  "v_mfma_f32_16x16x32_bf16 v[144:147], v[32:35], v[48:51], v[144:147]\n\t" \
  "v_mfma_f32_16x16x32_bf16 v[152:155], v[36:39], v[48:51], v[152:155]\n\t" \
  "s_waitcnt lgkmcnt(4)\n\t" \
  "v_mfma_f32_16x16x32_bf16 v[96:99], v[52:55], v[24:27], v[96:99]\n\t" \
  "ds_read_b64_tr_b16 v[48:49], v60 offset:0\n\t" \
  "v_mfma_f32_16x16x32_bf16 v[104:107], v[52:55], v[28:31], v[104:107]\n\t" \
  "ds_read_b64_tr_b16 v[50:51], v60 offset:512\n\t" \
  "v_mfma_f32_16x16x32_bf16 v[112:115], v[52:55], v[32:35], v[112:115]\n\t" \
  "ds_read_b128 v[0:3], v56 offset:36864\n\t" \
  "v_mfma_f32_16x16x32_bf16 v[120:123], v[52:55], v[36:39], v[120:123]\n\t" \
  "ds_read_b128 v[4:7], v56 offset:47104\n\t" \
  "s_waitcnt lgkmcnt(6)\n\t" \
  "v_mfma_f32_16x16x32_bf16 v[164:167], v[20:23], v[40:43], v[164:167]\n\t" \
  "ds_read_b64_tr_b16 v[52:53], v61 offset:0\n\t" \
  "v_mfma_f32_16x16x32_bf16 v[132:135], v[24:27], v[40:43], v[132:135]\n\t" \
  "ds_read_b64_tr_b16 v[54:55], v61 offset:512\n\t" \
  "v_mfma_f32_16x16x32_bf16 v[140:143], v[28:31], v[40:43], v[140:143]\n\t" \
  "ds_read_b128 v[8:11], v56 offset:49152\n\t" \
  "v_mfma_f32_16x16x32_bf16 v[148:151], v[32:35], v[40:43], v[148:151]\n\t" \
  "ds_read_b128 v[12:15], v56 offset:51200\n\t" \
  "v_mfma_f32_16x16x32_bf16 v[156:159], v[36:39], v[40:43], v[156:159]\n\t" \
  "s_waitcnt lgkmcnt(8)\n\t" \
  "v_mfma_f32_16x16x32_bf16 v[100:103], v[44:47], v[24:27], v[100:103]\n\t" \
  "ds_read_b64_tr_b16 v[40:41], v60 offset:20480\n\t" \
  "v_mfma_f32_16x16x32_bf16 v[108:111], v[44:47], v[28:31], v[108:111]\n\t" \
  "ds_read_b64_tr_b16 v[42:43], v60 offset:20992\n\t" \
  "v_mfma_f32_16x16x32_bf16 v[116:119], v[44:47], v[32:35], v[116:119]\n\t" \
  "ds_read_b128 v[16:19], v56 offset:53248\n\t" \
  "v_mfma_f32_16x16x32_bf16 v[124:127], v[44:47], v[36:39], v[124:127]\n\t" \
  "s_waitcnt lgkmcnt(9)\n\t" \
  "v_mfma_f32_16x16x32_bf16 v[64:67], v[48:51], v[24:27], v[64:67]\n\t" \
  "ds_read_b64_tr_b16 v[44:45], v60 offset:16384\n\t" \
  "v_mfma_f32_16x16x32_bf16 v[72:75], v[48:51], v[28:31], v[72:75]\n\t" \
  "ds_read_b64_tr_b16 v[46:47], v60 offset:16896\n\t" \
  "v_mfma_f32_16x16x32_bf16 v[80:83], v[48:51], v[32:35], v[80:83]\n\t" \
  "v_mfma_f32_16x16x32_bf16 v[88:91], v[48:51], v[36:39], v[88:91]\n\t" \
  "s_waitcnt lgkmcnt(7)\n\t" \
  "v_mfma_f32_16x16x32_bf16 v[68:71], v[52:55], v[24:27], v[68:71]\n\t" \
  "ds_read_b64_tr_b16 v[48:49], v61 offset:20480\n\t" \
  "v_mfma_f32_16x16x32_bf16 v[76:79], v[52:55], v[28:31], v[76:79]\n\t" \
  "ds_read_b64_tr_b16 v[50:51], v61 offset:20992\n\t" \
  "v_mfma_f32_16x16x32_bf16 v[84:87], v[52:55], v[32:35], v[84:87]\n\t" \
  "v_mfma_f32_16x16x32_bf16 v[92:95], v[52:55], v[36:39], v[92:95]\n\t" \
  "N4b_%=:\n\t" \
  "s_waitcnt lgkmcnt(4)\n\t" \
  "v_mfma_f32_16x16x32_bf16 v[160:163], v[0:3], v[40:43], v[160:163]\n\t" \
  "ds_read_b64_tr_b16 v[52:53], v61 offset:16384\n\t" \
  "v_mfma_f32_16x16x32_bf16 v[128:131], v[4:7], v[40:43], v[128:131]\n\t" \
  "ds_read_b64_tr_b16 v[54:55], v61 offset:16896\n\t" \
  "v_mfma_f32_16x16x32_bf16 v[136:139], v[8:11], v[40:43], v[136:139]\n\t" \
  "s_barrier\n\t" \
  "v_mfma_f32_16x16x32_bf16 v[144:147], v[12:15], v[40:43], v[144:147]\n\t" \
  "v_mfma_f32_16x16x32_bf16 v[152:155], v[16:19], v[40:43], v[152:155]\n\t" \
  "s_waitcnt lgkmcnt(4)\n\t" \
  "v_mfma_f32_16x16x32_bf16 v[96:99], v[44:47], v[4:7], v[96:99]\n\t" \
  "ds_read_b64_tr_b16 v[40:41], v60 offset:12288\n\t" \
  "v_mfma_f32_16x16x32_bf16 v[104:107], v[44:47], v[8:11], v[104:107]\n\t" \
  "ds_read_b64_tr_b16 v[42:43], v60 offset:12800\n\t" \
  "v_mfma_f32_16x16x32_bf16 v[112:115], v[44:47], v[12:15], v[112:115]\n\t" \
  "ds_read_b128 v[20:23], v57 offset:36864\n\t" \
  "v_mfma_f32_16x16x32_bf16 v[120:123], v[44:47], v[16:19], v[120:123]\n\t" \
  "ds_read_b128 v[24:27], v57 offset:47104\n\t" \
  "s_waitcnt lgkmcnt(6)\n\t" \
  "v_mfma_f32_16x16x32_bf16 v[164:167], v[0:3], v[48:51], v[164:167]\n\t" \
  "ds_read_b64_tr_b16 v[44:45], v61 offset:12288\n\t" \
  "v_mfma_f32_16x16x32_bf16 v[132:135], v[4:7], v[48:51], v[132:135]\n\t" \
  "ds_read_b64_tr_b16 v[46:47], v61 offset:12800\n\t" \
  "v_mfma_f32_16x16x32_bf16 v[140:143], v[8:11], v[48:51], v[140:143]\n\t" \
  "ds_read_b128 v[28:31], v57 offset:49152\n\t" \
  "v_mfma_f32_16x16x32_bf16 v[148:151], v[12:15], v[48:51], v[148:151]\n\t" \
  "ds_read_b128 v[32:35], v57 offset:51200\n\t" \
  "v_mfma_f32_16x16x32_bf16 v[156:159], v[16:19], v[48:51], v[156:159]\n\t" \
  "s_waitcnt lgkmcnt(8)\n\t" \
  "v_mfma_f32_16x16x32_bf16 v[100:103], v[52:55], v[4:7], v[100:103]\n\t" \
  "ds_read_b64_tr_b16 v[48:49], v60 offset:32768\n\t" \
  "v_mfma_f32_16x16x32_bf16 v[108:111], v[52:55], v[8:11], v[108:111]\n\t" \
  "ds_read_b64_tr_b16 v[50:51], v60 offset:33280\n\t" \
  "v_mfma_f32_16x16x32_bf16 v[116:119], v[52:55], v[12:15], v[116:119]\n\t" \
  "ds_read_b128 v[36:39], v57 offset:53248\n\t" \
  "v_mfma_f32_16x16x32_bf16 v[124:127], v[52:55], v[16:19], v[124:127]\n\t" \
  "s_waitcnt lgkmcnt(9)\n\t" \
  "v_mfma_f32_16x16x32_bf16 v[64:67], v[40:43], v[4:7], v[64:67]\n\t" \
  "ds_read_b64_tr_b16 v[52:53], v60 offset:28672\n\t" \
  "v_mfma_f32_16x16x32_bf16 v[72:75], v[40:43], v[8:11], v[72:75]\n\t" \
  "ds_read_b64_tr_b16 v[54:55], v60 offset:29184\n\t" \
  "v_mfma_f32_16x16x32_bf16 v[80:83], v[40:43], v[12:15], v[80:83]\n\t" \
  "v_mfma_f32_16x16x32_bf16 v[88:91], v[40:43], v[16:19], v[88:91]\n\t" \
  "s_waitcnt lgkmcnt(7)\n\t" \
  "v_mfma_f32_16x16x32_bf16 v[68:71], v[44:47], v[4:7], v[68:71]\n\t" \
  "ds_read_b64_tr_b16 v[40:41], v61 offset:32768\n\t" \
  "v_mfma_f32_16x16x32_bf16 v[76:79], v[44:47], v[8:11], v[76:79]\n\t" \
  "ds_read_b64_tr_b16 v[42:43], v61 offset:33280\n\t" \
  "v_mfma_f32_16x16x32_bf16 v[84:87], v[44:47], v[12:15], v[84:87]\n\t" \
  "v_mfma_f32_16x16x32_bf16 v[92:95], v[44:47], v[16:19], v[92:95]\n\t" \
  "N5b_%=:\n\t" \
  "s_waitcnt lgkmcnt(4)\n\t" \
  "v_mfma_f32_16x16x32_bf16 v[160:163], v[20:23], v[48:51], v[160:163]\n\t" \
  "ds_read_b64_tr_b16 v[44:45], v61 offset:28672\n\t" \
  "v_mfma_f32_16x16x32_bf16 v[128:131], v[24:27], v[48:51], v[128:131]\n\t" \
  "ds_read_b64_tr_b16 v[46:47], v61 offset:29184\n\t" \
  "v_mfma_f32_16x16x32_bf16 v[136:139], v[28:31], v[48:51], v[136:139]\n\t" \
  "s_barrier\n\t" \
  "v_mfma_f32_16x16x32_bf16 v[144:147], v[32:35], v[48:51], v[144:147]\n\t" \
  "v_mfma_f32_16x16x32_bf16 v[152:155], v[36:39], v[48:51], v[152:155]\n\t" \
  "s_waitcnt lgkmcnt(4)\n\t" \
  "v_mfma_f32_16x16x32_bf16 v[96:99], v[52:55], v[24:27], v[96:99]\n\t" \
  "ds_read_b64_tr_b16 v[48:49], v60 offset:24576\n\t" \
  "v_mfma_f32_16x16x32_bf16 v[104:107], v[52:55], v[28:31], v[104:107]\n\t" \
  "ds_read_b64_tr_b16 v[50:51], v60 offset:25088\n\t" \
  "v_mfma_f32_16x16x32_bf16 v[112:115], v[52:55], v[32:35], v[112:115]\n\t" \
  "ds_read_b128 v[0:3], v56 offset:0\n\t" \
  "v_mfma_f32_16x16x32_bf16 v[120:123], v[52:55], v[36:39], v[120:123]\n\t" \
  "ds_read_b128 v[4:7], v56 offset:10240\n\t" \
  "s_waitcnt lgkmcnt(6)\n\t" \
  "v_mfma_f32_16x16x32_bf16 v[164:167], v[20:23], v[40:43], v[164:167]\n\t" \
  "ds_read_b64_tr_b16 v[52:53], v61 offset:24576\n\t" \
  "v_mfma_f32_16x16x32_bf16 v[132:135], v[24:27], v[40:43], v[132:135]\n\t" \
  "ds_read_b64_tr_b16 v[54:55], v61 offset:25088\n\t" \
  "v_mfma_f32_16x16x32_bf16 v[140:143], v[28:31], v[40:43], v[140:143]\n\t" \
  "ds_read_b128 v[8:11], v56 offset:12288\n\t" \
  "v_mfma_f32_16x16x32_bf16 v[148:151], v[32:35], v[40:43], v[148:151]\n\t" \
  "ds_read_b128 v[12:15], v56 offset:14336\n\t" \
  "v_mfma_f32_16x16x32_bf16 v[156:159], v[36:39], v[40:43], v[156:159]\n\t" \
  "s_waitcnt lgkmcnt(8)\n\t" \
  "v_mfma_f32_16x16x32_bf16 v[100:103], v[44:47], v[24:27], v[100:103]\n\t" \
  "ds_read_b64_tr_b16 v[40:41], v58 offset:8192\n\t" \
  "v_mfma_f32_16x16x32_bf16 v[108:111], v[44:47], v[28:31], v[108:111]\n\t" \
  "ds_read_b64_tr_b16 v[42:43], v58 offset:8704\n\t" \
  "v_mfma_f32_16x16x32_bf16 v[116:119], v[44:47], v[32:35], v[116:119]\n\t" \
  "ds_read_b128 v[16:19], v56 offset:16384\n\t" \
  "v_mfma_f32_16x16x32_bf16 v[124:127], v[44:47], v[36:39], v[124:127]\n\t" \
  "s_waitcnt lgkmcnt(9)\n\t" \
  "v_mfma_f32_16x16x32_bf16 v[64:67], v[48:51], v[24:27], v[64:67]\n\t" \
  "ds_read_b64_tr_b16 v[44:45], v58 offset:4096\n\t" \
  "v_mfma_f32_16x16x32_bf16 v[72:75], v[48:51], v[28:31], v[72:75]\n\t" \
  "ds_read_b64_tr_b16 v[46:47], v58 offset:4608\n\t" \
  "v_mfma_f32_16x16x32_bf16 v[80:83], v[48:51], v[32:35], v[80:83]\n\t" \
  "v_mfma_f32_16x16x32_bf16 v[88:91], v[48:51], v[36:39], v[88:91]\n\t" \
  "s_waitcnt lgkmcnt(7)\n\t" \
  "v_mfma_f32_16x16x32_bf16 v[68:71], v[52:55], v[24:27], v[68:71]\n\t" \
  "ds_read_b64_tr_b16 v[48:49], v59 offset:8192\n\t" \
  "v_mfma_f32_16x16x32_bf16 v[76:79], v[52:55], v[28:31], v[76:79]\n\t" \
  "ds_read_b64_tr_b16 v[50:51], v59 offset:8704\n\t" \
  "s_addk_i32 %[it], 1\n\t" \
  "v_mfma_f32_16x16x32_bf16 v[84:87], v[52:55], v[32:35], v[84:87]\n\t" \
  "s_cmp_lt_u32 %[it], 6\n\t" \
  "v_mfma_f32_16x16x32_bf16 v[92:95], v[52:55], v[36:39], v[92:95]\n\t" \
  "s_cbranch_scc1 N0b_%=\n\t" \
  "s_setprio 3\n\t" \
  "v_cvt_pk_bf16_f32 v160, v160, v161\n\t" \
  "v_cvt_pk_bf16_f32 v161, v162, v163\n\t" \
  "v_cvt_pk_bf16_f32 v162, v164, v165\n\t" \
  "v_cvt_pk_bf16_f32 v163, v166, v167\n\t" \
  "ds_write_b128 v63, v[160:163] offset:2048\n\t" \
  "v_mov_b32 v57, %[fa]\n\t" \
  "v_mov_b32 v58, 1\n\t" \
  "s_mov_b64 exec, 1\n\t" \
  "ds_write_b32 v57, v58\n\t" \
  "s_mov_b64 exec, -1\n\t" \
  "v_cvt_pk_bf16_f32 v120, v120, v121\n\t" \
  "v_cvt_pk_bf16_f32 v121, v122, v123\n\t" \
  "v_cvt_pk_bf16_f32 v122, v124, v125\n\t" \
  "v_cvt_pk_bf16_f32 v123, v126, v127\n\t" \
  "v_cvt_pk_bf16_f32 v28, v152, v153\n\t" \
  "v_cvt_pk_bf16_f32 v29, v154, v155\n\t" \
  "v_cvt_pk_bf16_f32 v38, v156, v157\n\t" \
  "v_cvt_pk_bf16_f32 v39, v158, v159\n\t" \
  "NX_%=:\n\t" \
  "v_cvt_pk_bf16_f32 v96, v96, v97\n\t" \
  "v_cvt_pk_bf16_f32 v97, v98, v99\n\t" \
  "v_cvt_pk_bf16_f32 v98, v100, v101\n\t" \
  "v_cvt_pk_bf16_f32 v99, v102, v103\n\t" \
  "v_cvt_pk_bf16_f32 v104, v104, v105\n\t" \
  "v_cvt_pk_bf16_f32 v105, v106, v107\n\t" \
  "v_cvt_pk_bf16_f32 v106, v108, v109\n\t" \
  "v_cvt_pk_bf16_f32 v107, v110, v111\n\t" \
  "v_mov_b32 v60, 0xff61b1e6\n\t" \
  "v_cndmask_b32_e64 v56, v60, 0, s[68:69]\n\t" \
  "v_cndmask_b32_e64 v57, v60, 0, s[70:71]\n\t" \
  "v_cndmask_b32_e64 v58, v60, 0, s[72:73]\n\t" \
  "v_cndmask_b32_e64 v59, v60, 0, s[74:75]\n\t" \
  "v_cvt_pk_bf16_f32 v72, v72, v73\n\t" \
  "v_cvt_pk_bf16_f32 v73, v74, v75\n\t" \
  "v_cvt_pk_bf16_f32 v74, v76, v77\n\t" \
  "v_cvt_pk_bf16_f32 v75, v78, v79\n\t" \
  "v_cndmask_b32_e64 v52, v60, 0, s[76:77]\n\t" \
  "v_cndmask_b32_e64 v53, v60, 0, s[78:79]\n\t" \
  "v_cndmask_b32_e64 v54, v60, 0, s[80:81]\n\t" \
  "v_cndmask_b32_e64 v55, v60, 0, s[82:83]\n\t" \
  "v_cvt_pk_bf16_f32 v24, v136, v137\n\t" \
  "v_cvt_pk_bf16_f32 v25, v138, v139\n\t" \
  "v_cvt_pk_bf16_f32 v34, v140, v141\n\t" \
  "v_cvt_pk_bf16_f32 v35, v142, v143\n\t" \
  "v_cvt_pk_bf16_f32 v26, v144, v145\n\t" \
  "v_cvt_pk_bf16_f32 v27, v146, v147\n\t" \
  "v_cvt_pk_bf16_f32 v36, v148, v149\n\t" \
  "v_cvt_pk_bf16_f32 v37, v150, v151\n\t" \
  "v_mfma_f32_16x16x32_bf16 v[136:139], v[96:99], v[72:75], v[56:59]\n\t" \
  "v_cvt_pk_bf16_f32 v112, v112, v113\n\t" \
  "v_cvt_pk_bf16_f32 v113, v114, v115\n\t" \
  "v_mfma_f32_16x16x32_bf16 v[140:143], v[104:107], v[72:75], v[52:55]\n\t" \
  "v_cvt_pk_bf16_f32 v114, v116, v117\n\t" \
  "v_cvt_pk_bf16_f32 v115, v118, v119\n\t" \
  "v_cvt_pk_bf16_f32 v80, v80, v81\n\t" \
  "v_cvt_pk_bf16_f32 v81, v82, v83\n\t" \
  "v_cvt_pk_bf16_f32 v82, v84, v85\n\t" \
  "v_cvt_pk_bf16_f32 v83, v86, v87\n\t" \
  "v_cvt_pk_bf16_f32 v22, v128, v129\n\t" \
  "v_cvt_pk_bf16_f32 v23, v130, v131\n\t" \
  "v_mfma_f32_16x16x32_bf16 v[144:147], v[104:107], v[80:83], v[56:59]\n\t" \
  "v_cvt_pk_bf16_f32 v32, v132, v133\n\t" \
  "v_cvt_pk_bf16_f32 v33, v134, v135\n\t" \
  "v_mfma_f32_16x16x32_bf16 v[148:151], v[112:115], v[80:83], v[52:55]\n\t" \
  "v_cvt_pk_bf16_f32 v88, v88, v89\n\t" \
  "v_cvt_pk_bf16_f32 v89, v90, v91\n\t" \
  "v_cvt_pk_bf16_f32 v90, v92, v93\n\t" \
  "v_cvt_pk_bf16_f32 v91, v94, v95\n\t" \
  "v_mov_b32 v61, %[fp]\n\t" \
  "v_add_u32 v60, %[vo], v63\n\t" \
  "v_mfma_f32_16x16x32_bf16 v[152:155], v[112:115], v[88:91], v[56:59]\n\t" \
  "ds_read_b32 v0, v61\n\t" \
  "ds_read_b64 v[20:21], v60\n\t" \
  "ds_read_b64 v[30:31], v60 offset:8\n\t" \
  "s_mov_b64 exec, %[rm]\n\t" \
  "ds_read_b128 v[160:163], v63\n\t" \
  "s_mov_b64 exec, -1\n\t" \
  "v_mfma_f32_16x16x32_bf16 v[156:159], v[120:123], v[88:91], v[52:55]\n\t" \
  "v_cvt_pk_bf16_f32 v64, v64, v65\n\t" \
  "v_cvt_pk_bf16_f32 v65, v66, v67\n\t" \
  "v_cvt_pk_bf16_f32 v66, v68, v69\n\t" \
  "v_cvt_pk_bf16_f32 v67, v70, v71\n\t" \
  "v_max_f32 v136, v136, v137\n\t" \
  "v_max_f32 v138, v138, v139\n\t" \
  "v_mfma_f32_16x16x32_bf16 v[132:135], v[96:99], v[64:67], v[52:55]\n\t" \
  "v_max_f32 v140, v140, v141\n\t" \
  "v_max_f32 v142, v142, v143\n\t" \
  "AW_%=:\n\t" \
  "s_waitcnt lgkmcnt(0)\n\t" \
  "v_cmp_eq_u32 vcc, 0, v0\n\t" \
  "v_max3_f32 v5, v136, v138, v140\n\t" \
  "v_max_f32 v5, v5, v142\n\t" \
  "s_cbranch_vccnz AR_%=\n\t" \
  "s_cmp_eq_u32 %[z], 0\n\t" \
  "s_cbranch_scc1 AZ_%=\n\t" \
  "AY_%=:\n\t" \
  "v_mfma_f32_16x16x32_bf16 v[128:131], v[160:163], v[64:67], v[56:59]\n\t" \
  "v_max_f32 v144, v144, v145\n\t" \
  "v_max_f32 v146, v146, v147\n\t" \
  "v_max_f32 v148, v148, v149\n\t" \
  "v_max_f32 v150, v150, v151\n\t" \
  "v_max3_f32 v6, v144, v146, v148\n\t" \
  "v_max_f32 v6, v6, v150\n\t" \
  "v_max_f32 v152, v152, v153\n\t" \
  "v_max_f32 v154, v154, v155\n\t" \
  "v_max_f32 v156, v156, v157\n\t" \
  "v_max_f32 v158, v158, v159\n\t" \
  "v_max3_f32 v7, v152, v154, v156\n\t" \
  "v_max_f32 v7, v7, v158\n\t" \
  "v_mov_b32 v8, 0xc0c0100\n\t" \
  "v_mov_b32 v9, 0x1000c0c\n\t" \
  "v_max_f32 v128, v128, v129\n\t" \
  "v_max_f32 v130, v130, v131\n\t" \
  "v_max_f32 v132, v132, v133\n\t" \
  "v_max_f32 v134, v134, v135\n\t" \
  "v_max3_f32 v4, v128, v130, v132\n\t" \
  "v_max_f32 v4, v4, v134\n\t" \
  "v_cndmask_b32_e64 v60, v8, v9, s[84:85]\n\t" \
  "v_mov_b32 v8, 0xc0c0302\n\t" \
  "v_permlane16_swap_b32 v6, v7\n\t" \
  "v_permlane16_swap_b32 v4, v5\n\t" \
  "v_max_f32 v6, v6, v7\n\t" \
  "v_max_f32 v4, v4, v5\n\t" \
  "v_mov_b32 v9, 0x3020c0c\n\t" \
  "v_cndmask_b32_e64 v61, v8, v9, s[84:85]\n\t" \
  "v_permlane32_swap_b32 v4, v6\n\t" \
  "v_max_f32 v4, v4, v6\n\t" \
  "v_mul_f32 v6, 0xbe8293ee, v4\n\t" \
  "v_mul_f32 v4, 0xbe8293ee, v4\n\t" \
  "s_nop 1\n\t" \
  "v_permlane32_swap_b32 v4, v6\n\t" \
  "v_mov_b32 v5, v4\n\t" \
  "v_mov_b32 v7, v6\n\t" \
  "s_nop 0\n\t" \
  "v_permlane16_swap_b32 v4, v5\n\t" \
  "v_permlane16_swap_b32 v6, v7\n\t" \
  "v_fmamk_f32 v128, v128, 0x3e8293ee, v4\n\t" \
  "v_fmamk_f32 v130, v130, 0x3e8293ee, v4\n\t" \
  "v_fmamk_f32 v132, v132, 0x3e8293ee, v4\n\t" \
  "v_fmamk_f32 v134, v134, 0x3e8293ee, v4\n\t" \
  "v_fmamk_f32 v136, v136, 0x3e8293ee, v5\n\t" \
  "v_fmamk_f32 v138, v138, 0x3e8293ee, v5\n\t" \
  "v_fmamk_f32 v140, v140, 0x3e8293ee, v5\n\t" \
  "v_fmamk_f32 v142, v142, 0x3e8293ee, v5\n\t" \
  "v_fmamk_f32 v144, v144, 0x3e8293ee, v6\n\t" \
  "v_fmamk_f32 v146, v146, 0x3e8293ee, v6\n\t" \
  "v_fmamk_f32 v148, v148, 0x3e8293ee, v6\n\t" \
  "v_fmamk_f32 v150, v150, 0x3e8293ee, v6\n\t" \
  "v_fmamk_f32 v152, v152, 0x3e8293ee, v7\n\t" \
  "v_fmamk_f32 v154, v154, 0x3e8293ee, v7\n\t" \
  "v_fmamk_f32 v156, v156, 0x3e8293ee, v7\n\t" \
  "v_fmamk_f32 v158, v158, 0x3e8293ee, v7\n\t" \
  "v_exp_f32 v128, v128\n\t" \
  "v_exp_f32 v130, v130\n\t" \
  "v_exp_f32 v132, v132\n\t" \
  "v_exp_f32 v134, v134\n\t" \
  "v_exp_f32 v136, v136\n\t" \
  "v_exp_f32 v138, v138\n\t" \
  "v_exp_f32 v140, v140\n\t" \
  "v_exp_f32 v142, v142\n\t" \
  "v_exp_f32 v144, v144\n\t" \
  "v_exp_f32 v146, v146\n\t" \
  "v_exp_f32 v148, v148\n\t" \
  "v_exp_f32 v150, v150\n\t" \
  "v_exp_f32 v152, v152\n\t" \
  "v_exp_f32 v154, v154\n\t" \
  "v_exp_f32 v156, v156\n\t" \
  "v_exp_f32 v158, v158\n\t" \
  "v_add_f32 v4, v128, v130\n\t" \
  "v_add_f32 v8, v132, v134\n\t" \
  "v_add_f32 v5, v136, v138\n\t" \
  "v_add_f32 v9, v140, v142\n\t" \
  "v_add_f32 v6, v144, v146\n\t" \
  "v_add_f32 v10, v148, v150\n\t" \
  "v_add_f32 v7, v152, v154\n\t" \
  "v_add_f32 v11, v156, v158\n\t" \
  "v_add_f32 v4, v4, v8\n\t" \
  "v_add_f32 v5, v5, v9\n\t" \
  "v_add_f32 v6, v6, v10\n\t" \
  "v_add_f32 v7, v7, v11\n\t" \
  "v_permlane16_swap_b32 v4, v5\n\t" \
  "s_nop 0\n\t" \
  "v_permlane16_swap_b32 v6, v7\n\t" \
  "v_add_f32 v4, v4, v5\n\t" \
  "v_add_f32 v6, v6, v7\n\t" \
  "s_nop 1\n\t" \
  "v_permlane32_swap_b32 v4, v6\n\t" \
  "v_add_f32 v4, v4, v6\n\t" \
  "v_rcp_f32 v4, v4\n\t" \
  "s_nop 0\n\t" \
  "v_mov_b32 v6, v4\n\t" \
  "s_nop 1\n\t" \
  "v_permlane32_swap_b32 v4, v6\n\t" \
  "v_mov_b32 v5, v4\n\t" \
  "v_mov_b32 v7, v6\n\t" \
  "s_nop 0\n\t" \
  "v_permlane16_swap_b32 v4, v5\n\t" \
  "v_permlane16_swap_b32 v6, v7\n\t" \
  "v_mul_f32 v128, v128, v4\n\t" \
  "v_mul_f32 v130, v130, v4\n\t" \
  "v_mul_f32 v132, v132, v4\n\t" \
  "v_mul_f32 v134, v134, v4\n\t" \
  "v_cvt_pk_bf16_f32 v128, v128, v130\n\t" \
  "v_cvt_pk_bf16_f32 v132, v132, v134\n\t" \
  "v_perm_b32 v68, 0, v128, v60\n\t" \
  "v_perm_b32 v69, 0, v128, v61\n\t" \
  "v_perm_b32 v70, 0, v132, v60\n\t" \
  "v_perm_b32 v71, 0, v132, v61\n\t" \
  "v_mul_f32 v136, v136, v5\n\t" \
  "v_mul_f32 v138, v138, v5\n\t" \
  "v_mfma_f32_16x16x32_bf16 v[128:131], v[20:23], v[68:71], 0\n\t" \
  "v_mul_f32 v140, v140, v5\n\t" \
  "v_mul_f32 v142, v142, v5\n\t" \
  "v_cvt_pk_bf16_f32 v136, v136, v138\n\t" \
  "v_mfma_f32_16x16x32_bf16 v[132:135], v[30:33], v[68:71], 0\n\t" \
  "v_cvt_pk_bf16_f32 v140, v140, v142\n\t" \
  "v_perm_b32 v76, 0, v136, v60\n\t" \
  "v_perm_b32 v77, 0, v136, v61\n\t" \
  "v_perm_b32 v78, 0, v140, v60\n\t" \
  "v_perm_b32 v79, 0, v140, v61\n\t" \
  "v_mul_f32 v144, v144, v6\n\t" \
  "v_mul_f32 v146, v146, v6\n\t" \
  "v_mfma_f32_16x16x32_bf16 v[136:139], v[22:25], v[76:79], 0\n\t" \
  "v_mul_f32 v148, v148, v6\n\t" \
  "v_mul_f32 v150, v150, v6\n\t" \
  "v_cvt_pk_bf16_f32 v144, v144, v146\n\t" \
  "v_mfma_f32_16x16x32_bf16 v[140:143], v[32:35], v[76:79], 0\n\t" \
  "v_cvt_pk_bf16_f32 v148, v148, v150\n\t" \
  "v_perm_b32 v84, 0, v144, v60\n\t" \
  "v_perm_b32 v85, 0, v144, v61\n\t" \
  "v_perm_b32 v86, 0, v148, v60\n\t" \
  "v_perm_b32 v87, 0, v148, v61\n\t" \
  "v_cvt_pk_bf16_f32 v100, v128, v129\n\t" \
  "v_cvt_pk_bf16_f32 v101, v130, v131\n\t" \
  "v_mfma_f32_16x16x32_bf16 v[144:147], v[24:27], v[84:87], 0\n\t" \
  "v_cvt_pk_bf16_f32 v102, v132, v133\n\t" \
  "v_cvt_pk_bf16_f32 v103, v134, v135\n\t" \
  "global_store_dwordx4 v62, v[100:103], %[ot] offset:0 sc1\n\t" \
  "v_mfma_f32_16x16x32_bf16 v[148:151], v[34:37], v[84:87], 0\n\t" \
  "v_mul_f32 v152, v152, v7\n\t" \
  "v_mul_f32 v154, v154, v7\n\t" \
  "v_mul_f32 v156, v156, v7\n\t" \
  "v_mul_f32 v158, v158, v7\n\t" \
  "v_cvt_pk_bf16_f32 v152, v152, v154\n\t" \
  "v_cvt_pk_bf16_f32 v156, v156, v158\n\t" \
  "v_perm_b32 v92, 0, v152, v60\n\t" \
  "v_perm_b32 v93, 0, v152, v61\n\t" \
  "v_perm_b32 v94, 0, v156, v60\n\t" \
  "v_perm_b32 v95, 0, v156, v61\n\t" \
  "v_cvt_pk_bf16_f32 v108, v136, v137\n\t" \
  "v_cvt_pk_bf16_f32 v109, v138, v139\n\t" \
  "v_mfma_f32_16x16x32_bf16 v[152:155], v[26:29], v[92:95], 0\n\t" \
  "v_cvt_pk_bf16_f32 v110, v140, v141\n\t" \
  "v_cvt_pk_bf16_f32 v111, v142, v143\n\t" \
  "global_store_dwordx4 v62, v[108:111], %[ot] offset:1024 sc1\n\t" \
  "v_mfma_f32_16x16x32_bf16 v[156:159], v[36:39], v[92:95], 0\n\t" \
  "v_cvt_pk_bf16_f32 v116, v144, v145\n\t" \
  "v_cvt_pk_bf16_f32 v117, v146, v147\n\t" \
  "v_cvt_pk_bf16_f32 v118, v148, v149\n\t" \
  "v_cvt_pk_bf16_f32 v119, v150, v151\n\t" \
  "global_store_dwordx4 v62, v[116:119], %[ou] offset:0 sc1\n\t" \
  "v_cvt_pk_bf16_f32 v124, v152, v153\n\t" \
  "v_cvt_pk_bf16_f32 v125, v154, v155\n\t" \
  "s_nop 0\n\t" \
  "v_cvt_pk_bf16_f32 v126, v156, v157\n\t" \
  "v_cvt_pk_bf16_f32 v127, v158, v159\n\t" \
  "global_store_dwordx4 v62, v[124:127], %[ou] offset:1024 sc1\n\t" \
  "s_branch AE_%=\n\t" \
  "AR_%=:\n\t" \
  "s_addk_i32 %[it], 1\n\t" \
  "s_cmp_gt_u32 %[it], 0x400000\n\t" \
  "s_cbranch_scc1 AY_%=\n\t" \
  "ds_read_b32 v0, v61\n\t" \
  "ds_read_b64 v[20:21], v60\n\t" \
  "ds_read_b64 v[30:31], v60 offset:8\n\t" \
  "s_mov_b64 exec, %[rm]\n\t" \
  "ds_read_b128 v[160:163], v63\n\t" \
  "s_mov_b64 exec, -1\n\t" \
  "s_branch AW_%=\n\t" \
  "AZ_%=:\n\t" \
  "v_mov_b32 v0, 0xff61b1e6\n\t" \
  "v_mov_b32 v1, 0xcfa8965f\n\t" \
  "s_nop 3\n\t" \
  "v_cndmask_b32_e64 v56, v0, v1, s[68:69]\n\t" \
  "v_cndmask_b32_e64 v57, v0, v1, s[70:71]\n\t" \
  "v_cndmask_b32_e64 v58, v0, v1, s[72:73]\n\t" \
  "v_cndmask_b32_e64 v59, v0, v1, s[74:75]\n\t" \
  "s_nop 1\n\t" \
  "s_branch AY_%=\n\t" \
  "AE_%=:\n\t" \
  "s_nop 1\n\t"
#define N2_CLOB "v0", "v1", "v2", "v3", "v4", "v5", "v6", "v7", "v8", "v9", "v10", "v11", "v12", "v13", "v14", "v15", "v16", "v17", "v18", "v19", "v20", "v21", "v22", "v23", "v24", "v25", "v26", "v27", "v28", "v29", "v30", "v31", "v32", "v33", "v34", "v35", "v36", "v37", "v38", "v39", "v40", "v41", "v42", "v43", "v44", "v45", "v46", "v47", "v48", "v49", "v50", "v51", "v52", "v53", "v54", "v55", "v60", "v61", "v64", "v65", "v66", "v67", "v68", "v69", "v70", "v71", "v72", "v73", "v74", "v75", "v76", "v77", "v78", "v79", "v80", "v81", "v82", "v83", "v84", "v85", "v86", "v87", "v88", "v89", "v90", "v91", "v92", "v93", "v94", "v95", "v96", "v97", "v98", "v99", "v100", "v101", "v102", "v103", "v104", "v105", "v106", "v107", "v108", "v109", "v110", "v111", "v112", "v113", "v114", "v115", "v116", "v117", "v118", "v119", "v120", "v121", "v122", "v123", "v124", "v125", "v126", "v127", "v128", "v129", "v130", "v131", "v132", "v133", "v134", "v135", "v136", "v137", "v138", "v139", "v140", "v141", "v142", "v143", "v144", "v145", "v146", "v147", "v148", "v149", "v150", "v151", "v152", "v153", "v154", "v155", "v156", "v157", "v158", "v159", "v160", "v161", "v162", "v163", "v164", "v165", "v166", "v167", "s68", "s69", "s70", "s71", "s72", "s73", "s74", "s75", "s76", "s77", "s78", "s79", "s80", "s81", "s82", "s83", "s84", "s85"

#define CV_ASM \
  "ds_read_b64 v[0:1], v28 offset:0\n\t" \
  "ds_read_b64 v[2:3], v29 offset:0\n\t" \
  "ds_read_b64 v[4:5], v30 offset:256\n\t" \
  "ds_read_b64 v[6:7], v31 offset:256\n\t" \
  "ds_read_b64 v[8:9], v24 offset:512\n\t" \
  "ds_read_b64 v[10:11], v25 offset:512\n\t" \
  "ds_read_b64 v[12:13], v26 offset:768\n\t" \
  "ds_read_b64 v[14:15], v27 offset:768\n\t" \
  "ds_read_b64 v[16:17], v28 offset:1024\n\t" \
  "ds_read_b64 v[18:19], v29 offset:1024\n\t" \
  "ds_read_b64 v[20:21], v30 offset:1280\n\t" \
  "ds_read_b64 v[22:23], v31 offset:1280\n\t" \
  "v_mov_b32 v128, 0\n\t" \
  "v_mov_b32 v129, 0\n\t" \
  "v_mov_b32 v130, 0\n\t" \
  "v_mov_b32 v131, 0\n\t" \
  "v_mov_b32 v132, 0\n\t" \
  "v_mov_b32 v133, 0\n\t" \
  "v_mov_b32 v134, 0\n\t" \
  "v_mov_b32 v135, 0\n\t" \
  "v_mov_b32 v136, 0\n\t" \
  "v_mov_b32 v137, 0\n\t" \
  "v_mov_b32 v138, 0\n\t" \
  "v_mov_b32 v139, 0\n\t" \
  "v_mov_b32 v140, 0\n\t" \
  "v_mov_b32 v141, 0\n\t" \
  "v_mov_b32 v142, 0\n\t" \
  "v_mov_b32 v143, 0\n\t" \
  "v_mov_b32 v144, 0\n\t" \
  "v_mov_b32 v145, 0\n\t" \
  "v_mov_b32 v146, 0\n\t" \
  "v_mov_b32 v147, 0\n\t" \
  "v_mov_b32 v148, 0\n\t" \
  "v_mov_b32 v149, 0\n\t" \
  "v_mov_b32 v150, 0\n\t" \
  "v_mov_b32 v151, 0\n\t" \
  "v_mov_b32 v152, 0\n\t" \
  "v_mov_b32 v153, 0\n\t" \
  "v_mov_b32 v154, 0\n\t" \
  "v_mov_b32 v155, 0\n\t" \
  "v_mov_b32 v156, 0\n\t" \
  "v_mov_b32 v157, 0\n\t" \
  "v_mov_b32 v158, 0\n\t" \
  "v_mov_b32 v159, 0\n\t" \
  "s_waitcnt lgkmcnt(10)\n\t" \
  "v_mfma_f32_16x16x32_bf16 v[128:131], v[68:71], v[0:3], v[128:131]\n\t" \
  "v_mfma_f32_16x16x32_bf16 v[132:135], v[76:79], v[0:3], v[132:135]\n\t" \
  "v_mfma_f32_16x16x32_bf16 v[136:139], v[84:87], v[0:3], v[136:139]\n\t" \
  "v_mfma_f32_16x16x32_bf16 v[140:143], v[92:95], v[0:3], v[140:143]\n\t" \
  "v_mfma_f32_16x16x32_bf16 v[144:147], v[100:103], v[0:3], v[144:147]\n\t" \
  "v_mfma_f32_16x16x32_bf16 v[148:151], v[108:111], v[0:3], v[148:151]\n\t" \
  "v_mfma_f32_16x16x32_bf16 v[152:155], v[116:119], v[0:3], v[152:155]\n\t" \
  "v_mfma_f32_16x16x32_bf16 v[156:159], v[124:127], v[0:3], v[156:159]\n\t" \
  "ds_read_b64 v[0:1], v24 offset:1536\n\t" \
  "s_waitcnt lgkmcnt(9)\n\t" \
  "v_mfma_f32_16x16x32_bf16 v[128:131], v[64:67], v[4:7], v[128:131]\n\t" \
  "ds_read_b64 v[2:3], v25 offset:1536\n\t" \
  "v_mfma_f32_16x16x32_bf16 v[132:135], v[72:75], v[4:7], v[132:135]\n\t" \
  "v_mfma_f32_16x16x32_bf16 v[136:139], v[80:83], v[4:7], v[136:139]\n\t" \
  "v_mfma_f32_16x16x32_bf16 v[140:143], v[88:91], v[4:7], v[140:143]\n\t" \
  "v_mfma_f32_16x16x32_bf16 v[144:147], v[96:99], v[4:7], v[144:147]\n\t" \
  "v_mfma_f32_16x16x32_bf16 v[148:151], v[104:107], v[4:7], v[148:151]\n\t" \
  "v_mfma_f32_16x16x32_bf16 v[152:155], v[112:115], v[4:7], v[152:155]\n\t" \
  "s_nop 0\n\t" \
  "ds_bpermute_b32 v34, v32, v131\n\t" \
  "v_readlane_b32 %[p0], v131, 63\n\t" \
  "v_mfma_f32_16x16x32_bf16 v[156:159], v[120:123], v[4:7], v[156:159]\n\t" \
  "ds_read_b64 v[4:5], v26 offset:1792\n\t" \
  "s_waitcnt lgkmcnt(10)\n\t" \
  "v_mfma_f32_16x16x32_bf16 v[132:135], v[68:71], v[8:11], v[132:135]\n\t" \
  "ds_read_b64 v[6:7], v27 offset:1792\n\t" \
  "v_mfma_f32_16x16x32_bf16 v[136:139], v[76:79], v[8:11], v[136:139]\n\t" \
  "v_mfma_f32_16x16x32_bf16 v[140:143], v[84:87], v[8:11], v[140:143]\n\t" \
  "v_mfma_f32_16x16x32_bf16 v[144:147], v[92:95], v[8:11], v[144:147]\n\t" \
  "v_mfma_f32_16x16x32_bf16 v[148:151], v[100:103], v[8:11], v[148:151]\n\t" \
  "s_waitcnt lgkmcnt(2)\n\t" \
  "v_writelane_b32 v34, 0, 0\n\t" \
  "v_pk_mov_b32 v[130:131], v[128:129], v[130:131] op_sel:[1,0]\n\t" \
  "v_pk_mov_b32 v[128:129], v[34:35], v[128:129] op_sel:[0,0]\n\t" \
  "v_mfma_f32_16x16x32_bf16 v[152:155], v[108:111], v[8:11], v[152:155]\n\t" \
  "v_mfma_f32_16x16x32_bf16 v[156:159], v[116:119], v[8:11], v[156:159]\n\t" \
  "ds_read_b64 v[8:9], v28 offset:2048\n\t" \
  "s_waitcnt lgkmcnt(10)\n\t" \
  "v_mfma_f32_16x16x32_bf16 v[132:135], v[64:67], v[12:15], v[132:135]\n\t" \
  "ds_read_b64 v[10:11], v29 offset:2048\n\t" \
  "v_mfma_f32_16x16x32_bf16 v[136:139], v[72:75], v[12:15], v[136:139]\n\t" \
  "v_mfma_f32_16x16x32_bf16 v[140:143], v[80:83], v[12:15], v[140:143]\n\t" \
  "v_mfma_f32_16x16x32_bf16 v[144:147], v[88:91], v[12:15], v[144:147]\n\t" \
  "v_mfma_f32_16x16x32_bf16 v[148:151], v[96:99], v[12:15], v[148:151]\n\t" \
  "v_mfma_f32_16x16x32_bf16 v[152:155], v[104:107], v[12:15], v[152:155]\n\t" \
  "v_mfma_f32_16x16x32_bf16 v[156:159], v[112:115], v[12:15], v[156:159]\n\t" \
  "ds_read_b64 v[12:13], v30 offset:2304\n\t" \
  "ds_bpermute_b32 v35, v32, v135\n\t" \
  "v_readlane_b32 %[p1], v135, 63\n\t" \
  "s_waitcnt lgkmcnt(11)\n\t" \
  "v_mfma_f32_16x16x32_bf16 v[136:139], v[68:71], v[16:19], v[136:139]\n\t" \
  "ds_read_b64 v[14:15], v31 offset:2304\n\t" \
  "v_mfma_f32_16x16x32_bf16 v[140:143], v[76:79], v[16:19], v[140:143]\n\t" \
  "v_mfma_f32_16x16x32_bf16 v[144:147], v[84:87], v[16:19], v[144:147]\n\t" \
  "v_mfma_f32_16x16x32_bf16 v[148:151], v[92:95], v[16:19], v[148:151]\n\t" \
  "v_mfma_f32_16x16x32_bf16 v[152:155], v[100:103], v[16:19], v[152:155]\n\t" \
  "v_mfma_f32_16x16x32_bf16 v[156:159], v[108:111], v[16:19], v[156:159]\n\t" \
  "ds_read_b64 v[16:17], v24 offset:2560\n\t" \
  "s_waitcnt lgkmcnt(2)\n\t" \
  "v_writelane_b32 v35, %[p0], 0\n\t" \
  "v_pk_mov_b32 v[134:135], v[132:133], v[134:135] op_sel:[1,0]\n\t" \
  "v_pk_mov_b32 v[132:133], v[34:35], v[132:133] op_sel:[1,0]\n\t" \
  "s_waitcnt lgkmcnt(11)\n\t" \
  "v_mfma_f32_16x16x32_bf16 v[136:139], v[64:67], v[20:23], v[136:139]\n\t" \
  "ds_read_b64 v[18:19], v25 offset:2560\n\t" \
  "v_mfma_f32_16x16x32_bf16 v[140:143], v[72:75], v[20:23], v[140:143]\n\t" \
  "v_mfma_f32_16x16x32_bf16 v[144:147], v[80:83], v[20:23], v[144:147]\n\t" \
  "v_mfma_f32_16x16x32_bf16 v[148:151], v[88:91], v[20:23], v[148:151]\n\t" \
  "v_mfma_f32_16x16x32_bf16 v[152:155], v[96:99], v[20:23], v[152:155]\n\t" \
  "v_mfma_f32_16x16x32_bf16 v[156:159], v[104:107], v[20:23], v[156:159]\n\t" \
  "ds_read_b64 v[20:21], v26 offset:2816\n\t" \
  "s_waitcnt lgkmcnt(11)\n\t" \
  "v_mfma_f32_16x16x32_bf16 v[140:143], v[68:71], v[0:3], v[140:143]\n\t" \
  "ds_read_b64 v[22:23], v27 offset:2816\n\t" \
  "ds_bpermute_b32 v36, v32, v139\n\t" \
  "v_readlane_b32 %[p2], v139, 63\n\t" \
  "v_mfma_f32_16x16x32_bf16 v[144:147], v[76:79], v[0:3], v[144:147]\n\t" \
  "v_mfma_f32_16x16x32_bf16 v[148:151], v[84:87], v[0:3], v[148:151]\n\t" \
  "v_mfma_f32_16x16x32_bf16 v[152:155], v[92:95], v[0:3], v[152:155]\n\t" \
  "v_mfma_f32_16x16x32_bf16 v[156:159], v[100:103], v[0:3], v[156:159]\n\t" \
  "ds_read_b64 v[0:1], v28 offset:3072\n\t" \
  "s_waitcnt lgkmcnt(11)\n\t" \
  "v_mfma_f32_16x16x32_bf16 v[140:143], v[64:67], v[4:7], v[140:143]\n\t" \
  "ds_read_b64 v[2:3], v29 offset:3072\n\t" \
  "v_mfma_f32_16x16x32_bf16 v[144:147], v[72:75], v[4:7], v[144:147]\n\t" \
  "s_waitcnt lgkmcnt(2)\n\t" \
  "v_writelane_b32 v36, %[p1], 0\n\t" \
  "v_pk_mov_b32 v[138:139], v[136:137], v[138:139] op_sel:[1,0]\n\t" \
  "v_pk_mov_b32 v[136:137], v[36:37], v[136:137] op_sel:[0,0]\n\t" \
  "v_mfma_f32_16x16x32_bf16 v[148:151], v[80:83], v[4:7], v[148:151]\n\t" \
  "v_mfma_f32_16x16x32_bf16 v[152:155], v[88:91], v[4:7], v[152:155]\n\t" \
  "v_mfma_f32_16x16x32_bf16 v[156:159], v[96:99], v[4:7], v[156:159]\n\t" \
  "ds_read_b64 v[4:5], v30 offset:3328\n\t" \
  "s_waitcnt lgkmcnt(11)\n\t" \
  "v_mfma_f32_16x16x32_bf16 v[144:147], v[68:71], v[8:11], v[144:147]\n\t" \
  "ds_read_b64 v[6:7], v31 offset:3328\n\t" \
  "v_mfma_f32_16x16x32_bf16 v[148:151], v[76:79], v[8:11], v[148:151]\n\t" \
  "ds_bpermute_b32 v37, v32, v143\n\t" \
  "v_readlane_b32 %[p3], v143, 63\n\t" \
  "v_mfma_f32_16x16x32_bf16 v[152:155], v[84:87], v[8:11], v[152:155]\n\t" \
  "v_mfma_f32_16x16x32_bf16 v[156:159], v[92:95], v[8:11], v[156:159]\n\t" \
  "ds_read_b64 v[8:9], v24 offset:3584\n\t" \
  "s_waitcnt lgkmcnt(11)\n\t" \
  "v_mfma_f32_16x16x32_bf16 v[144:147], v[64:67], v[12:15], v[144:147]\n\t" \
  "ds_read_b64 v[10:11], v25 offset:3584\n\t" \
  "v_mfma_f32_16x16x32_bf16 v[148:151], v[72:75], v[12:15], v[148:151]\n\t" \
  "v_mfma_f32_16x16x32_bf16 v[152:155], v[80:83], v[12:15], v[152:155]\n\t" \
  "v_mfma_f32_16x16x32_bf16 v[156:159], v[88:91], v[12:15], v[156:159]\n\t" \
  "ds_read_b64 v[12:13], v26 offset:3840\n\t" \
  "s_waitcnt lgkmcnt(3)\n\t" \
  "v_writelane_b32 v37, %[p2], 0\n\t" \
  "v_pk_mov_b32 v[142:143], v[140:141], v[142:143] op_sel:[1,0]\n\t" \
  "v_pk_mov_b32 v[140:141], v[36:37], v[140:141] op_sel:[1,0]\n\t" \
  "s_waitcnt lgkmcnt(11)\n\t" \
  "v_mfma_f32_16x16x32_bf16 v[148:151], v[68:71], v[16:19], v[148:151]\n\t" \
  "ds_read_b64 v[14:15], v27 offset:3840\n\t" \
  "v_mfma_f32_16x16x32_bf16 v[152:155], v[76:79], v[16:19], v[152:155]\n\t" \
  "v_mfma_f32_16x16x32_bf16 v[156:159], v[84:87], v[16:19], v[156:159]\n\t" \
  "ds_read_b64 v[16:17], v26 offset:0\n\t" \
  "ds_bpermute_b32 v38, v32, v147\n\t" \
  "v_readlane_b32 %[p4], v147, 63\n\t" \
  "s_waitcnt lgkmcnt(12)\n\t" \
  "v_mfma_f32_16x16x32_bf16 v[148:151], v[64:67], v[20:23], v[148:151]\n\t" \
  "ds_read_b64 v[18:19], v27 offset:0\n\t" \
  "v_mfma_f32_16x16x32_bf16 v[152:155], v[72:75], v[20:23], v[152:155]\n\t" \
  "v_mfma_f32_16x16x32_bf16 v[156:159], v[80:83], v[20:23], v[156:159]\n\t" \
  "ds_read_b64 v[20:21], v28 offset:256\n\t" \
  "s_waitcnt lgkmcnt(11)\n\t" \
  "v_mfma_f32_16x16x32_bf16 v[152:155], v[68:71], v[0:3], v[152:155]\n\t" \
  "ds_read_b64 v[22:23], v29 offset:256\n\t" \
  "v_mfma_f32_16x16x32_bf16 v[156:159], v[76:79], v[0:3], v[156:159]\n\t" \
  "ds_read_b64 v[0:1], v30 offset:512\n\t" \
  "s_waitcnt lgkmcnt(11)\n\t" \
  "v_mfma_f32_16x16x32_bf16 v[152:155], v[64:67], v[4:7], v[152:155]\n\t" \
  "ds_read_b64 v[2:3], v31 offset:512\n\t" \
  "s_waitcnt lgkmcnt(5)\n\t" \
  "v_writelane_b32 v38, %[p3], 0\n\t" \
  "v_pk_mov_b32 v[146:147], v[144:145], v[146:147] op_sel:[1,0]\n\t" \
  "v_pk_mov_b32 v[144:145], v[38:39], v[144:145] op_sel:[0,0]\n\t" \
  "v_mfma_f32_16x16x32_bf16 v[156:159], v[72:75], v[4:7], v[156:159]\n\t" \
  "ds_read_b64 v[4:5], v24 offset:768\n\t" \
  "ds_bpermute_b32 v39, v32, v151\n\t" \
  "v_readlane_b32 %[p5], v151, 63\n\t" \
  "s_waitcnt lgkmcnt(11)\n\t" \
  "v_mfma_f32_16x16x32_bf16 v[156:159], v[68:71], v[8:11], v[156:159]\n\t" \
  "ds_read_b64 v[6:7], v25 offset:768\n\t" \
  "ds_read_b64 v[8:9], v26 offset:1024\n\t" \
  "ds_bpermute_b32 v40, v32, v155\n\t" \
  "v_readlane_b32 %[p6], v155, 63\n\t" \
  "s_waitcnt lgkmcnt(12)\n\t" \
  "v_mfma_f32_16x16x32_bf16 v[156:159], v[64:67], v[12:15], v[156:159]\n\t" \
  "ds_read_b64 v[10:11], v27 offset:1024\n\t" \
  "ds_read_b64 v[12:13], v28 offset:1280\n\t" \
  "s_waitcnt lgkmcnt(11)\n\t" \
  "v_mfma_f32_16x16x32_bf16 v[128:131], v[68:71], v[16:19], v[128:131]\n\t" \
  "ds_read_b64 v[14:15], v29 offset:1280\n\t" \
  "v_mfma_f32_16x16x32_bf16 v[132:135], v[76:79], v[16:19], v[132:135]\n\t" \
  "v_mfma_f32_16x16x32_bf16 v[136:139], v[84:87], v[16:19], v[136:139]\n\t" \
  "v_mfma_f32_16x16x32_bf16 v[140:143], v[92:95], v[16:19], v[140:143]\n\t" \
  "s_waitcnt lgkmcnt(6)\n\t" \
  "v_writelane_b32 v39, %[p4], 0\n\t" \
  "v_pk_mov_b32 v[150:151], v[148:149], v[150:151] op_sel:[1,0]\n\t" \
  "v_pk_mov_b32 v[148:149], v[38:39], v[148:149] op_sel:[1,0]\n\t" \
  "v_mfma_f32_16x16x32_bf16 v[144:147], v[100:103], v[16:19], v[144:147]\n\t" \
  "s_waitcnt lgkmcnt(3)\n\t" \
  "v_writelane_b32 v40, %[p5], 0\n\t" \
  "v_pk_mov_b32 v[154:155], v[152:153], v[154:155] op_sel:[1,0]\n\t" \
  "v_pk_mov_b32 v[152:153], v[40:41], v[152:153] op_sel:[0,0]\n\t" \
  "v_mfma_f32_16x16x32_bf16 v[148:151], v[108:111], v[16:19], v[148:151]\n\t" \
  "ds_bpermute_b32 v41, v32, v159\n\t" \
  "v_mfma_f32_16x16x32_bf16 v[152:155], v[116:119], v[16:19], v[152:155]\n\t" \
  "s_waitcnt lgkmcnt(11)\n\t" \
  "v_mfma_f32_16x16x32_bf16 v[128:131], v[64:67], v[20:23], v[128:131]\n\t" \
  "v_mfma_f32_16x16x32_bf16 v[132:135], v[72:75], v[20:23], v[132:135]\n\t" \
  "v_mfma_f32_16x16x32_bf16 v[136:139], v[80:83], v[20:23], v[136:139]\n\t" \
  "v_mfma_f32_16x16x32_bf16 v[140:143], v[88:91], v[20:23], v[140:143]\n\t" \
  "v_mfma_f32_16x16x32_bf16 v[144:147], v[96:99], v[20:23], v[144:147]\n\t" \
  "s_waitcnt lgkmcnt(0)\n\t" \
  "v_writelane_b32 v41, %[p6], 0\n\t" \
  "v_pk_mov_b32 v[158:159], v[156:157], v[158:159] op_sel:[1,0]\n\t" \
  "v_pk_mov_b32 v[156:157], v[40:41], v[156:157] op_sel:[1,0]\n\t" \
  "v_mfma_f32_16x16x32_bf16 v[148:151], v[104:107], v[20:23], v[148:151]\n\t" \
  "v_mfma_f32_16x16x32_bf16 v[152:155], v[112:115], v[20:23], v[152:155]\n\t" \
  "ds_bpermute_b32 v34, v32, v131\n\t" \
  "v_readlane_b32 %[p0], v131, 63\n\t" \
  "v_mfma_f32_16x16x32_bf16 v[156:159], v[124:127], v[16:19], v[156:159]\n\t" \
  "ds_read_b64 v[16:17], v30 offset:1536\n\t" \
  "v_mfma_f32_16x16x32_bf16 v[156:159], v[120:123], v[20:23], v[156:159]\n\t" \
  "ds_read_b64 v[18:19], v31 offset:1536\n\t" \
  "ds_read_b64 v[20:21], v24 offset:1792\n\t" \
  "s_waitcnt lgkmcnt(13)\n\t" \
  "v_mfma_f32_16x16x32_bf16 v[132:135], v[68:71], v[0:3], v[132:135]\n\t" \
  "ds_read_b64 v[22:23], v25 offset:1792\n\t" \
  "v_mfma_f32_16x16x32_bf16 v[136:139], v[76:79], v[0:3], v[136:139]\n\t" \
  "v_mfma_f32_16x16x32_bf16 v[140:143], v[84:87], v[0:3], v[140:143]\n\t" \
  "v_mfma_f32_16x16x32_bf16 v[144:147], v[92:95], v[0:3], v[144:147]\n\t" \
  "s_waitcnt lgkmcnt(4)\n\t" \
  "v_writelane_b32 v34, 0, 0\n\t" \
  "v_pk_mov_b32 v[130:131], v[128:129], v[130:131] op_sel:[1,0]\n\t" \
  "v_pk_mov_b32 v[128:129], v[34:35], v[128:129] op_sel:[0,0]\n\t" \
  "v_mfma_f32_16x16x32_bf16 v[148:151], v[100:103], v[0:3], v[148:151]\n\t" \
  "v_mfma_f32_16x16x32_bf16 v[152:155], v[108:111], v[0:3], v[152:155]\n\t" \
  "v_mfma_f32_16x16x32_bf16 v[156:159], v[116:119], v[0:3], v[156:159]\n\t" \
  "ds_read_b64 v[0:1], v26 offset:2048\n\t" \
  "s_waitcnt lgkmcnt(12)\n\t" \
  "v_mfma_f32_16x16x32_bf16 v[132:135], v[64:67], v[4:7], v[132:135]\n\t" \
  "ds_read_b64 v[2:3], v27 offset:2048\n\t" \
  "v_mfma_f32_16x16x32_bf16 v[136:139], v[72:75], v[4:7], v[136:139]\n\t" \
  "v_mfma_f32_16x16x32_bf16 v[140:143], v[80:83], v[4:7], v[140:143]\n\t" \
  "v_mfma_f32_16x16x32_bf16 v[144:147], v[88:91], v[4:7], v[144:147]\n\t" \
  "v_mfma_f32_16x16x32_bf16 v[148:151], v[96:99], v[4:7], v[148:151]\n\t" \
  "v_mfma_f32_16x16x32_bf16 v[152:155], v[104:107], v[4:7], v[152:155]\n\t" \
  "v_mfma_f32_16x16x32_bf16 v[156:159], v[112:115], v[4:7], v[156:159]\n\t" \
  "ds_read_b64 v[4:5], v28 offset:2304\n\t" \
  "ds_bpermute_b32 v35, v32, v135\n\t" \
  "v_readlane_b32 %[p1], v135, 63\n\t" \
  "s_waitcnt lgkmcnt(12)\n\t" \
  "v_mfma_f32_16x16x32_bf16 v[136:139], v[68:71], v[8:11], v[136:139]\n\t" \
  "ds_read_b64 v[6:7], v29 offset:2304\n\t" \
  "v_mfma_f32_16x16x32_bf16 v[140:143], v[76:79], v[8:11], v[140:143]\n\t" \
  "v_mfma_f32_16x16x32_bf16 v[144:147], v[84:87], v[8:11], v[144:147]\n\t" \
  "v_mfma_f32_16x16x32_bf16 v[148:151], v[92:95], v[8:11], v[148:151]\n\t" \
  "v_mfma_f32_16x16x32_bf16 v[152:155], v[100:103], v[8:11], v[152:155]\n\t" \
  "v_mfma_f32_16x16x32_bf16 v[156:159], v[108:111], v[8:11], v[156:159]\n\t" \
  "ds_read_b64 v[8:9], v30 offset:2560\n\t" \
  "s_waitcnt lgkmcnt(2)\n\t" \
  "v_writelane_b32 v35, %[p0], 0\n\t" \
  "v_pk_mov_b32 v[134:135], v[132:133], v[134:135] op_sel:[1,0]\n\t" \
  "v_pk_mov_b32 v[132:133], v[34:35], v[132:133] op_sel:[1,0]\n\t" \
  "s_waitcnt lgkmcnt(12)\n\t" \
  "v_mfma_f32_16x16x32_bf16 v[136:139], v[64:67], v[12:15], v[136:139]\n\t" \
  "ds_read_b64 v[10:11], v31 offset:2560\n\t" \
  "v_mfma_f32_16x16x32_bf16 v[140:143], v[72:75], v[12:15], v[140:143]\n\t" \
  "v_mfma_f32_16x16x32_bf16 v[144:147], v[80:83], v[12:15], v[144:147]\n\t" \
  "v_mfma_f32_16x16x32_bf16 v[148:151], v[88:91], v[12:15], v[148:151]\n\t" \
  "v_mfma_f32_16x16x32_bf16 v[152:155], v[96:99], v[12:15], v[152:155]\n\t" \
  "v_mfma_f32_16x16x32_bf16 v[156:159], v[104:107], v[12:15], v[156:159]\n\t" \
  "ds_read_b64 v[12:13], v24 offset:2816\n\t" \
  "s_waitcnt lgkmcnt(10)\n\t" \
  "v_mfma_f32_16x16x32_bf16 v[140:143], v[68:71], v[16:19], v[140:143]\n\t" \
  "ds_read_b64 v[14:15], v25 offset:2816\n\t" \
  "ds_bpermute_b32 v36, v32, v139\n\t" \
  "v_readlane_b32 %[p2], v139, 63\n\t" \
  "v_mfma_f32_16x16x32_bf16 v[144:147], v[76:79], v[16:19], v[144:147]\n\t" \
  "v_mfma_f32_16x16x32_bf16 v[148:151], v[84:87], v[16:19], v[148:151]\n\t" \
  "v_mfma_f32_16x16x32_bf16 v[152:155], v[92:95], v[16:19], v[152:155]\n\t" \
  "v_mfma_f32_16x16x32_bf16 v[156:159], v[100:103], v[16:19], v[156:159]\n\t" \
  "ds_read_b64 v[16:17], v26 offset:3072\n\t" \
  "s_waitcnt lgkmcnt(11)\n\t" \
  "v_mfma_f32_16x16x32_bf16 v[140:143], v[64:67], v[20:23], v[140:143]\n\t" \
  "ds_read_b64 v[18:19], v27 offset:3072\n\t" \
  "v_mfma_f32_16x16x32_bf16 v[144:147], v[72:75], v[20:23], v[144:147]\n\t" \
  "s_waitcnt lgkmcnt(2)\n\t" \
  "v_writelane_b32 v36, %[p1], 0\n\t" \
  "v_pk_mov_b32 v[138:139], v[136:137], v[138:139] op_sel:[1,0]\n\t" \
  "v_pk_mov_b32 v[136:137], v[36:37], v[136:137] op_sel:[0,0]\n\t" \
  "v_mfma_f32_16x16x32_bf16 v[148:151], v[80:83], v[20:23], v[148:151]\n\t" \
  "v_mfma_f32_16x16x32_bf16 v[152:155], v[88:91], v[20:23], v[152:155]\n\t" \
  "v_mfma_f32_16x16x32_bf16 v[156:159], v[96:99], v[20:23], v[156:159]\n\t" \
  "ds_read_b64 v[20:21], v28 offset:3328\n\t" \
  "s_waitcnt lgkmcnt(11)\n\t" \
  "v_mfma_f32_16x16x32_bf16 v[144:147], v[68:71], v[0:3], v[144:147]\n\t" \
  "ds_read_b64 v[22:23], v29 offset:3328\n\t" \
  "v_mfma_f32_16x16x32_bf16 v[148:151], v[76:79], v[0:3], v[148:151]\n\t" \
  "ds_bpermute_b32 v37, v32, v143\n\t" \
  "v_readlane_b32 %[p3], v143, 63\n\t" \
  "v_mfma_f32_16x16x32_bf16 v[152:155], v[84:87], v[0:3], v[152:155]\n\t" \
  "v_mfma_f32_16x16x32_bf16 v[156:159], v[92:95], v[0:3], v[156:159]\n\t" \
  "ds_read_b64 v[0:1], v30 offset:3584\n\t" \
  "s_waitcnt lgkmcnt(11)\n\t" \
  "v_mfma_f32_16x16x32_bf16 v[144:147], v[64:67], v[4:7], v[144:147]\n\t" \
  "ds_read_b64 v[2:3], v31 offset:3584\n\t" \
  "v_mfma_f32_16x16x32_bf16 v[148:151], v[72:75], v[4:7], v[148:151]\n\t" \
  "v_mfma_f32_16x16x32_bf16 v[152:155], v[80:83], v[4:7], v[152:155]\n\t" \
  "v_mfma_f32_16x16x32_bf16 v[156:159], v[88:91], v[4:7], v[156:159]\n\t" \
  "ds_read_b64 v[4:5], v24 offset:3840\n\t" \
  "s_waitcnt lgkmcnt(3)\n\t" \
  "v_writelane_b32 v37, %[p2], 0\n\t" \
  "v_pk_mov_b32 v[142:143], v[140:141], v[142:143] op_sel:[1,0]\n\t" \
  "v_pk_mov_b32 v[140:141], v[36:37], v[140:141] op_sel:[1,0]\n\t" \
  "s_waitcnt lgkmcnt(11)\n\t" \
  "v_mfma_f32_16x16x32_bf16 v[148:151], v[68:71], v[8:11], v[148:151]\n\t" \
  "ds_read_b64 v[6:7], v25 offset:3840\n\t" \
  "v_mfma_f32_16x16x32_bf16 v[152:155], v[76:79], v[8:11], v[152:155]\n\t" \
  "v_mfma_f32_16x16x32_bf16 v[156:159], v[84:87], v[8:11], v[156:159]\n\t" \
  "ds_read_b64 v[8:9], v24 offset:0\n\t" \
  "ds_bpermute_b32 v38, v32, v147\n\t" \
  "v_readlane_b32 %[p4], v147, 63\n\t" \
  "s_waitcnt lgkmcnt(12)\n\t" \
  "v_mfma_f32_16x16x32_bf16 v[148:151], v[64:67], v[12:15], v[148:151]\n\t" \
  "ds_read_b64 v[10:11], v25 offset:0\n\t" \
  "v_mfma_f32_16x16x32_bf16 v[152:155], v[72:75], v[12:15], v[152:155]\n\t" \
  "v_mfma_f32_16x16x32_bf16 v[156:159], v[80:83], v[12:15], v[156:159]\n\t" \
  "ds_read_b64 v[12:13], v26 offset:256\n\t" \
  "s_waitcnt lgkmcnt(11)\n\t" \
  "v_mfma_f32_16x16x32_bf16 v[152:155], v[68:71], v[16:19], v[152:155]\n\t" \
  "ds_read_b64 v[14:15], v27 offset:256\n\t" \
  "v_mfma_f32_16x16x32_bf16 v[156:159], v[76:79], v[16:19], v[156:159]\n\t" \
  "ds_read_b64 v[16:17], v28 offset:512\n\t" \
  "s_waitcnt lgkmcnt(11)\n\t" \
  "v_mfma_f32_16x16x32_bf16 v[152:155], v[64:67], v[20:23], v[152:155]\n\t" \
  "ds_read_b64 v[18:19], v29 offset:512\n\t" \
  "s_waitcnt lgkmcnt(5)\n\t" \
  "v_writelane_b32 v38, %[p3], 0\n\t" \
  "v_pk_mov_b32 v[146:147], v[144:145], v[146:147] op_sel:[1,0]\n\t" \
  "v_pk_mov_b32 v[144:145], v[38:39], v[144:145] op_sel:[0,0]\n\t" \
  "v_mfma_f32_16x16x32_bf16 v[156:159], v[72:75], v[20:23], v[156:159]\n\t" \
  "ds_read_b64 v[20:21], v30 offset:768\n\t" \
  "ds_bpermute_b32 v39, v32, v151\n\t" \
  "v_readlane_b32 %[p5], v151, 63\n\t" \
  "s_waitcnt lgkmcnt(11)\n\t" \
  "v_mfma_f32_16x16x32_bf16 v[156:159], v[68:71], v[0:3], v[156:159]\n\t" \
  "ds_read_b64 v[22:23], v31 offset:768\n\t" \
  "ds_read_b64 v[0:1], v24 offset:1024\n\t" \
  "ds_bpermute_b32 v40, v32, v155\n\t" \
  "v_readlane_b32 %[p6], v155, 63\n\t" \
  "s_waitcnt lgkmcnt(12)\n\t" \
  "v_mfma_f32_16x16x32_bf16 v[156:159], v[64:67], v[4:7], v[156:159]\n\t" \
  "ds_read_b64 v[2:3], v25 offset:1024\n\t" \
  "ds_read_b64 v[4:5], v26 offset:1280\n\t" \
  "s_waitcnt lgkmcnt(11)\n\t" \
  "v_mfma_f32_16x16x32_bf16 v[128:131], v[68:71], v[8:11], v[128:131]\n\t" \
  "ds_read_b64 v[6:7], v27 offset:1280\n\t" \
  "v_mfma_f32_16x16x32_bf16 v[132:135], v[76:79], v[8:11], v[132:135]\n\t" \
  "v_mfma_f32_16x16x32_bf16 v[136:139], v[84:87], v[8:11], v[136:139]\n\t" \
  "v_mfma_f32_16x16x32_bf16 v[140:143], v[92:95], v[8:11], v[140:143]\n\t" \
  "s_waitcnt lgkmcnt(6)\n\t" \
  "v_writelane_b32 v39, %[p4], 0\n\t" \
  "v_pk_mov_b32 v[150:151], v[148:149], v[150:151] op_sel:[1,0]\n\t" \
  "v_pk_mov_b32 v[148:149], v[38:39], v[148:149] op_sel:[1,0]\n\t" \
  "v_mfma_f32_16x16x32_bf16 v[144:147], v[100:103], v[8:11], v[144:147]\n\t" \
  "s_waitcnt lgkmcnt(3)\n\t" \
  "v_writelane_b32 v40, %[p5], 0\n\t" \
  "v_pk_mov_b32 v[154:155], v[152:153], v[154:155] op_sel:[1,0]\n\t" \
  "v_pk_mov_b32 v[152:153], v[40:41], v[152:153] op_sel:[0,0]\n\t" \
  "v_mfma_f32_16x16x32_bf16 v[148:151], v[108:111], v[8:11], v[148:151]\n\t" \
  "ds_bpermute_b32 v41, v32, v159\n\t" \
  "v_mfma_f32_16x16x32_bf16 v[152:155], v[116:119], v[8:11], v[152:155]\n\t" \
  "s_waitcnt lgkmcnt(11)\n\t" \
  "v_mfma_f32_16x16x32_bf16 v[128:131], v[64:67], v[12:15], v[128:131]\n\t" \
  "v_mfma_f32_16x16x32_bf16 v[132:135], v[72:75], v[12:15], v[132:135]\n\t" \
  "v_mfma_f32_16x16x32_bf16 v[136:139], v[80:83], v[12:15], v[136:139]\n\t" \
  "v_mfma_f32_16x16x32_bf16 v[140:143], v[88:91], v[12:15], v[140:143]\n\t" \
  "v_mfma_f32_16x16x32_bf16 v[144:147], v[96:99], v[12:15], v[144:147]\n\t" \
  "s_waitcnt lgkmcnt(0)\n\t" \
  "v_writelane_b32 v41, %[p6], 0\n\t" \
  "v_pk_mov_b32 v[158:159], v[156:157], v[158:159] op_sel:[1,0]\n\t" \
  "v_pk_mov_b32 v[156:157], v[40:41], v[156:157] op_sel:[1,0]\n\t" \
  "v_mfma_f32_16x16x32_bf16 v[148:151], v[104:107], v[12:15], v[148:151]\n\t" \
  "v_mfma_f32_16x16x32_bf16 v[152:155], v[112:115], v[12:15], v[152:155]\n\t" \
  "ds_bpermute_b32 v34, v32, v131\n\t" \
  "v_readlane_b32 %[p0], v131, 63\n\t" \
  "v_mfma_f32_16x16x32_bf16 v[156:159], v[124:127], v[8:11], v[156:159]\n\t" \
  "ds_read_b64 v[8:9], v28 offset:1536\n\t" \
  "v_mfma_f32_16x16x32_bf16 v[156:159], v[120:123], v[12:15], v[156:159]\n\t" \
  "ds_read_b64 v[10:11], v29 offset:1536\n\t" \
  "ds_read_b64 v[12:13], v30 offset:1792\n\t" \
  "s_waitcnt lgkmcnt(13)\n\t" \
  "v_mfma_f32_16x16x32_bf16 v[132:135], v[68:71], v[16:19], v[132:135]\n\t" \
  "ds_read_b64 v[14:15], v31 offset:1792\n\t" \
  "v_mfma_f32_16x16x32_bf16 v[136:139], v[76:79], v[16:19], v[136:139]\n\t" \
  "v_mfma_f32_16x16x32_bf16 v[140:143], v[84:87], v[16:19], v[140:143]\n\t" \
  "v_mfma_f32_16x16x32_bf16 v[144:147], v[92:95], v[16:19], v[144:147]\n\t" \
  "s_waitcnt lgkmcnt(4)\n\t" \
  "v_writelane_b32 v34, 0, 0\n\t" \
  "v_pk_mov_b32 v[130:131], v[128:129], v[130:131] op_sel:[1,0]\n\t" \
  "v_pk_mov_b32 v[128:129], v[34:35], v[128:129] op_sel:[0,0]\n\t" \
  "v_mfma_f32_16x16x32_bf16 v[148:151], v[100:103], v[16:19], v[148:151]\n\t" \
  "v_mfma_f32_16x16x32_bf16 v[152:155], v[108:111], v[16:19], v[152:155]\n\t" \
  "v_mfma_f32_16x16x32_bf16 v[156:159], v[116:119], v[16:19], v[156:159]\n\t" \
  "ds_read_b64 v[16:17], v24 offset:2048\n\t" \
  "s_waitcnt lgkmcnt(12)\n\t" \
  "v_mfma_f32_16x16x32_bf16 v[132:135], v[64:67], v[20:23], v[132:135]\n\t" \
  "ds_read_b64 v[18:19], v25 offset:2048\n\t" \
  "v_mfma_f32_16x16x32_bf16 v[136:139], v[72:75], v[20:23], v[136:139]\n\t" \
  "v_mfma_f32_16x16x32_bf16 v[140:143], v[80:83], v[20:23], v[140:143]\n\t" \
  "v_mfma_f32_16x16x32_bf16 v[144:147], v[88:91], v[20:23], v[144:147]\n\t" \
  "v_mfma_f32_16x16x32_bf16 v[148:151], v[96:99], v[20:23], v[148:151]\n\t" \
  "v_mfma_f32_16x16x32_bf16 v[152:155], v[104:107], v[20:23], v[152:155]\n\t" \
  "v_mfma_f32_16x16x32_bf16 v[156:159], v[112:115], v[20:23], v[156:159]\n\t" \
  "ds_read_b64 v[20:21], v26 offset:2304\n\t" \
  "ds_bpermute_b32 v35, v32, v135\n\t" \
  "v_readlane_b32 %[p1], v135, 63\n\t" \
  "s_waitcnt lgkmcnt(12)\n\t" \
  "v_mfma_f32_16x16x32_bf16 v[136:139], v[68:71], v[0:3], v[136:139]\n\t" \
  "ds_read_b64 v[22:23], v27 offset:2304\n\t" \
  "v_mfma_f32_16x16x32_bf16 v[140:143], v[76:79], v[0:3], v[140:143]\n\t" \
  "v_mfma_f32_16x16x32_bf16 v[144:147], v[84:87], v[0:3], v[144:147]\n\t" \
  "v_mfma_f32_16x16x32_bf16 v[148:151], v[92:95], v[0:3], v[148:151]\n\t" \
  "v_mfma_f32_16x16x32_bf16 v[152:155], v[100:103], v[0:3], v[152:155]\n\t" \
  "v_mfma_f32_16x16x32_bf16 v[156:159], v[108:111], v[0:3], v[156:159]\n\t" \
  "ds_read_b64 v[0:1], v28 offset:2560\n\t" \
  "s_waitcnt lgkmcnt(2)\n\t" \
  "v_writelane_b32 v35, %[p0], 0\n\t" \
  "v_pk_mov_b32 v[134:135], v[132:133], v[134:135] op_sel:[1,0]\n\t" \
  "v_pk_mov_b32 v[132:133], v[34:35], v[132:133] op_sel:[1,0]\n\t" \
  "s_waitcnt lgkmcnt(12)\n\t" \
  "v_mfma_f32_16x16x32_bf16 v[136:139], v[64:67], v[4:7], v[136:139]\n\t" \
  "ds_read_b64 v[2:3], v29 offset:2560\n\t" \
  "v_mfma_f32_16x16x32_bf16 v[140:143], v[72:75], v[4:7], v[140:143]\n\t" \
  "v_mfma_f32_16x16x32_bf16 v[144:147], v[80:83], v[4:7], v[144:147]\n\t" \
  "v_mfma_f32_16x16x32_bf16 v[148:151], v[88:91], v[4:7], v[148:151]\n\t" \
  "v_mfma_f32_16x16x32_bf16 v[152:155], v[96:99], v[4:7], v[152:155]\n\t" \
  "v_mfma_f32_16x16x32_bf16 v[156:159], v[104:107], v[4:7], v[156:159]\n\t" \
  "ds_read_b64 v[4:5], v30 offset:2816\n\t" \
  "s_waitcnt lgkmcnt(10)\n\t" \
  "v_mfma_f32_16x16x32_bf16 v[140:143], v[68:71], v[8:11], v[140:143]\n\t" \
  "ds_read_b64 v[6:7], v31 offset:2816\n\t" \
  "ds_bpermute_b32 v36, v32, v139\n\t" \
  "v_readlane_b32 %[p2], v139, 63\n\t" \
  "v_mfma_f32_16x16x32_bf16 v[144:147], v[76:79], v[8:11], v[144:147]\n\t" \
  "v_mfma_f32_16x16x32_bf16 v[148:151], v[84:87], v[8:11], v[148:151]\n\t" \
  "v_mfma_f32_16x16x32_bf16 v[152:155], v[92:95], v[8:11], v[152:155]\n\t" \
  "v_mfma_f32_16x16x32_bf16 v[156:159], v[100:103], v[8:11], v[156:159]\n\t" \
  "ds_read_b64 v[8:9], v24 offset:3072\n\t" \
  "s_waitcnt lgkmcnt(11)\n\t" \
  "v_mfma_f32_16x16x32_bf16 v[140:143], v[64:67], v[12:15], v[140:143]\n\t" \
  "ds_read_b64 v[10:11], v25 offset:3072\n\t" \
  "v_mfma_f32_16x16x32_bf16 v[144:147], v[72:75], v[12:15], v[144:147]\n\t" \
  "s_waitcnt lgkmcnt(2)\n\t" \
  "v_writelane_b32 v36, %[p1], 0\n\t" \
  "v_pk_mov_b32 v[138:139], v[136:137], v[138:139] op_sel:[1,0]\n\t" \
  "v_pk_mov_b32 v[136:137], v[36:37], v[136:137] op_sel:[0,0]\n\t" \
  "v_mfma_f32_16x16x32_bf16 v[148:151], v[80:83], v[12:15], v[148:151]\n\t" \
  "v_mfma_f32_16x16x32_bf16 v[152:155], v[88:91], v[12:15], v[152:155]\n\t" \
  "v_mfma_f32_16x16x32_bf16 v[156:159], v[96:99], v[12:15], v[156:159]\n\t" \
  "ds_read_b64 v[12:13], v26 offset:3328\n\t" \
  "s_waitcnt lgkmcnt(11)\n\t" \
  "v_mfma_f32_16x16x32_bf16 v[144:147], v[68:71], v[16:19], v[144:147]\n\t" \
  "ds_read_b64 v[14:15], v27 offset:3328\n\t" \
  "v_mfma_f32_16x16x32_bf16 v[148:151], v[76:79], v[16:19], v[148:151]\n\t" \
  "ds_bpermute_b32 v37, v32, v143\n\t" \
  "v_readlane_b32 %[p3], v143, 63\n\t" \
  "v_mfma_f32_16x16x32_bf16 v[152:155], v[84:87], v[16:19], v[152:155]\n\t" \
  "v_mfma_f32_16x16x32_bf16 v[156:159], v[92:95], v[16:19], v[156:159]\n\t" \
  "ds_read_b64 v[16:17], v28 offset:3584\n\t" \
  "s_waitcnt lgkmcnt(11)\n\t" \
  "v_mfma_f32_16x16x32_bf16 v[144:147], v[64:67], v[20:23], v[144:147]\n\t" \
  "ds_read_b64 v[18:19], v29 offset:3584\n\t" \
  "v_mfma_f32_16x16x32_bf16 v[148:151], v[72:75], v[20:23], v[148:151]\n\t" \
  "v_mfma_f32_16x16x32_bf16 v[152:155], v[80:83], v[20:23], v[152:155]\n\t" \
  "v_mfma_f32_16x16x32_bf16 v[156:159], v[88:91], v[20:23], v[156:159]\n\t" \
  "ds_read_b64 v[20:21], v30 offset:3840\n\t" \
  "s_waitcnt lgkmcnt(3)\n\t" \
  "v_writelane_b32 v37, %[p2], 0\n\t" \
  "v_pk_mov_b32 v[142:143], v[140:141], v[142:143] op_sel:[1,0]\n\t" \
  "v_pk_mov_b32 v[140:141], v[36:37], v[140:141] op_sel:[1,0]\n\t" \
  "s_waitcnt lgkmcnt(11)\n\t" \
  "v_mfma_f32_16x16x32_bf16 v[148:151], v[68:71], v[0:3], v[148:151]\n\t" \
  "ds_read_b64 v[22:23], v31 offset:3840\n\t" \
  "v_mfma_f32_16x16x32_bf16 v[152:155], v[76:79], v[0:3], v[152:155]\n\t" \
  "v_mfma_f32_16x16x32_bf16 v[156:159], v[84:87], v[0:3], v[156:159]\n\t" \
  "ds_read_b64 v[0:1], v30 offset:0\n\t" \
  "ds_bpermute_b32 v38, v32, v147\n\t" \
  "v_readlane_b32 %[p4], v147, 63\n\t" \
  "s_waitcnt lgkmcnt(12)\n\t" \
  "v_mfma_f32_16x16x32_bf16 v[148:151], v[64:67], v[4:7], v[148:151]\n\t" \
  "ds_read_b64 v[2:3], v31 offset:0\n\t" \
  "v_mfma_f32_16x16x32_bf16 v[152:155], v[72:75], v[4:7], v[152:155]\n\t" \
  "v_mfma_f32_16x16x32_bf16 v[156:159], v[80:83], v[4:7], v[156:159]\n\t" \
  "ds_read_b64 v[4:5], v24 offset:256\n\t" \
  "s_waitcnt lgkmcnt(11)\n\t" \
  "v_mfma_f32_16x16x32_bf16 v[152:155], v[68:71], v[8:11], v[152:155]\n\t" \
  "ds_read_b64 v[6:7], v25 offset:256\n\t" \
  "v_mfma_f32_16x16x32_bf16 v[156:159], v[76:79], v[8:11], v[156:159]\n\t" \
  "ds_read_b64 v[8:9], v26 offset:512\n\t" \
  "s_waitcnt lgkmcnt(11)\n\t" \
  "v_mfma_f32_16x16x32_bf16 v[152:155], v[64:67], v[12:15], v[152:155]\n\t" \
  "ds_read_b64 v[10:11], v27 offset:512\n\t" \
  "s_waitcnt lgkmcnt(5)\n\t" \
  "v_writelane_b32 v38, %[p3], 0\n\t" \
  "v_pk_mov_b32 v[146:147], v[144:145], v[146:147] op_sel:[1,0]\n\t" \
  "v_pk_mov_b32 v[144:145], v[38:39], v[144:145] op_sel:[0,0]\n\t" \
  "v_mfma_f32_16x16x32_bf16 v[156:159], v[72:75], v[12:15], v[156:159]\n\t" \
  "ds_read_b64 v[12:13], v28 offset:768\n\t" \
  "ds_bpermute_b32 v39, v32, v151\n\t" \
  "v_readlane_b32 %[p5], v151, 63\n\t" \
  "s_waitcnt lgkmcnt(11)\n\t" \
  "v_mfma_f32_16x16x32_bf16 v[156:159], v[68:71], v[16:19], v[156:159]\n\t" \
  "ds_read_b64 v[14:15], v29 offset:768\n\t" \
  "ds_read_b64 v[16:17], v30 offset:1024\n\t" \
  "ds_bpermute_b32 v40, v32, v155\n\t" \
  "v_readlane_b32 %[p6], v155, 63\n\t" \
  "s_waitcnt lgkmcnt(12)\n\t" \
  "v_mfma_f32_16x16x32_bf16 v[156:159], v[64:67], v[20:23], v[156:159]\n\t" \
  "ds_read_b64 v[18:19], v31 offset:1024\n\t" \
  "ds_read_b64 v[20:21], v24 offset:1280\n\t" \
  "s_waitcnt lgkmcnt(11)\n\t" \
  "v_mfma_f32_16x16x32_bf16 v[128:131], v[68:71], v[0:3], v[128:131]\n\t" \
  "ds_read_b64 v[22:23], v25 offset:1280\n\t" \
  "v_mfma_f32_16x16x32_bf16 v[132:135], v[76:79], v[0:3], v[132:135]\n\t" \
  "v_mfma_f32_16x16x32_bf16 v[136:139], v[84:87], v[0:3], v[136:139]\n\t" \
  "v_mfma_f32_16x16x32_bf16 v[140:143], v[92:95], v[0:3], v[140:143]\n\t" \
  "s_waitcnt lgkmcnt(6)\n\t" \
  "v_writelane_b32 v39, %[p4], 0\n\t" \
  "v_pk_mov_b32 v[150:151], v[148:149], v[150:151] op_sel:[1,0]\n\t" \
  "v_pk_mov_b32 v[148:149], v[38:39], v[148:149] op_sel:[1,0]\n\t" \
  "v_mfma_f32_16x16x32_bf16 v[144:147], v[100:103], v[0:3], v[144:147]\n\t" \
  "s_waitcnt lgkmcnt(3)\n\t" \
  "v_writelane_b32 v40, %[p5], 0\n\t" \
  "v_pk_mov_b32 v[154:155], v[152:153], v[154:155] op_sel:[1,0]\n\t" \
  "v_pk_mov_b32 v[152:153], v[40:41], v[152:153] op_sel:[0,0]\n\t" \
  "v_mfma_f32_16x16x32_bf16 v[148:151], v[108:111], v[0:3], v[148:151]\n\t" \
  "ds_bpermute_b32 v41, v32, v159\n\t" \
  "v_mfma_f32_16x16x32_bf16 v[152:155], v[116:119], v[0:3], v[152:155]\n\t" \
  "s_waitcnt lgkmcnt(11)\n\t" \
  "v_mfma_f32_16x16x32_bf16 v[128:131], v[64:67], v[4:7], v[128:131]\n\t" \
  "v_mfma_f32_16x16x32_bf16 v[132:135], v[72:75], v[4:7], v[132:135]\n\t" \
  "v_mfma_f32_16x16x32_bf16 v[136:139], v[80:83], v[4:7], v[136:139]\n\t" \
  "v_mfma_f32_16x16x32_bf16 v[140:143], v[88:91], v[4:7], v[140:143]\n\t" \
  "v_mfma_f32_16x16x32_bf16 v[144:147], v[96:99], v[4:7], v[144:147]\n\t" \
  "s_waitcnt lgkmcnt(0)\n\t" \
  "v_writelane_b32 v41, %[p6], 0\n\t" \
  "v_pk_mov_b32 v[158:159], v[156:157], v[158:159] op_sel:[1,0]\n\t" \
  "v_pk_mov_b32 v[156:157], v[40:41], v[156:157] op_sel:[1,0]\n\t" \
  "v_mfma_f32_16x16x32_bf16 v[148:151], v[104:107], v[4:7], v[148:151]\n\t" \
  "v_mfma_f32_16x16x32_bf16 v[152:155], v[112:115], v[4:7], v[152:155]\n\t" \
  "v_mfma_f32_16x16x32_bf16 v[156:159], v[124:127], v[0:3], v[156:159]\n\t" \
  "ds_read_b64 v[0:1], v26 offset:1536\n\t" \
  "v_mfma_f32_16x16x32_bf16 v[156:159], v[120:123], v[4:7], v[156:159]\n\t" \
  "ds_read_b64 v[2:3], v27 offset:1536\n\t" \
  "ds_read_b64 v[4:5], v28 offset:1792\n\t" \
  "s_waitcnt lgkmcnt(12)\n\t" \
  "v_mfma_f32_16x16x32_bf16 v[132:135], v[68:71], v[8:11], v[132:135]\n\t" \
  "ds_read_b64 v[6:7], v29 offset:1792\n\t" \
  "v_mfma_f32_16x16x32_bf16 v[136:139], v[76:79], v[8:11], v[136:139]\n\t" \
  "v_mfma_f32_16x16x32_bf16 v[140:143], v[84:87], v[8:11], v[140:143]\n\t" \
  "v_mfma_f32_16x16x32_bf16 v[144:147], v[92:95], v[8:11], v[144:147]\n\t" \
  "v_mfma_f32_16x16x32_bf16 v[148:151], v[100:103], v[8:11], v[148:151]\n\t" \
  "v_mfma_f32_16x16x32_bf16 v[152:155], v[108:111], v[8:11], v[152:155]\n\t" \
  "v_mfma_f32_16x16x32_bf16 v[156:159], v[116:119], v[8:11], v[156:159]\n\t" \
  "ds_read_b64 v[8:9], v30 offset:2048\n\t" \
  "s_waitcnt lgkmcnt(11)\n\t" \
  "v_mfma_f32_16x16x32_bf16 v[132:135], v[64:67], v[12:15], v[132:135]\n\t" \
  "ds_read_b64 v[10:11], v31 offset:2048\n\t" \
  "v_mfma_f32_16x16x32_bf16 v[136:139], v[72:75], v[12:15], v[136:139]\n\t" \
  "v_mfma_f32_16x16x32_bf16 v[140:143], v[80:83], v[12:15], v[140:143]\n\t" \
  "v_mfma_f32_16x16x32_bf16 v[144:147], v[88:91], v[12:15], v[144:147]\n\t" \
  "v_mfma_f32_16x16x32_bf16 v[148:151], v[96:99], v[12:15], v[148:151]\n\t" \
  "v_mfma_f32_16x16x32_bf16 v[152:155], v[104:107], v[12:15], v[152:155]\n\t" \
  "v_mfma_f32_16x16x32_bf16 v[156:159], v[112:115], v[12:15], v[156:159]\n\t" \
  "ds_read_b64 v[12:13], v24 offset:2304\n\t" \
  "s_waitcnt lgkmcnt(10)\n\t" \
  "v_mfma_f32_16x16x32_bf16 v[136:139], v[68:71], v[16:19], v[136:139]\n\t" \
  "ds_read_b64 v[14:15], v25 offset:2304\n\t" \
  "v_mfma_f32_16x16x32_bf16 v[140:143], v[76:79], v[16:19], v[140:143]\n\t" \
  "v_mfma_f32_16x16x32_bf16 v[144:147], v[84:87], v[16:19], v[144:147]\n\t" \
  "v_mfma_f32_16x16x32_bf16 v[148:151], v[92:95], v[16:19], v[148:151]\n\t" \
  "v_mfma_f32_16x16x32_bf16 v[152:155], v[100:103], v[16:19], v[152:155]\n\t" \
  "v_mfma_f32_16x16x32_bf16 v[156:159], v[108:111], v[16:19], v[156:159]\n\t" \
  "ds_read_b64 v[16:17], v26 offset:2560\n\t" \
  "s_waitcnt lgkmcnt(10)\n\t" \
  "v_mfma_f32_16x16x32_bf16 v[136:139], v[64:67], v[20:23], v[136:139]\n\t" \
  "ds_read_b64 v[18:19], v27 offset:2560\n\t" \
  "v_mfma_f32_16x16x32_bf16 v[140:143], v[72:75], v[20:23], v[140:143]\n\t" \
  "v_mfma_f32_16x16x32_bf16 v[144:147], v[80:83], v[20:23], v[144:147]\n\t" \
  "v_mfma_f32_16x16x32_bf16 v[148:151], v[88:91], v[20:23], v[148:151]\n\t" \
  "v_mfma_f32_16x16x32_bf16 v[152:155], v[96:99], v[20:23], v[152:155]\n\t" \
  "v_mfma_f32_16x16x32_bf16 v[156:159], v[104:107], v[20:23], v[156:159]\n\t" \
  "ds_read_b64 v[20:21], v28 offset:2816\n\t" \
  "s_waitcnt lgkmcnt(9)\n\t" \
  "v_mfma_f32_16x16x32_bf16 v[140:143], v[68:71], v[0:3], v[140:143]\n\t" \
  "ds_read_b64 v[22:23], v29 offset:2816\n\t" \
  "v_mfma_f32_16x16x32_bf16 v[144:147], v[76:79], v[0:3], v[144:147]\n\t" \
  "v_mfma_f32_16x16x32_bf16 v[148:151], v[84:87], v[0:3], v[148:151]\n\t" \
  "v_mfma_f32_16x16x32_bf16 v[152:155], v[92:95], v[0:3], v[152:155]\n\t" \
  "v_mfma_f32_16x16x32_bf16 v[156:159], v[100:103], v[0:3], v[156:159]\n\t" \
  "ds_read_b64 v[0:1], v30 offset:3072\n\t" \
  "s_waitcnt lgkmcnt(9)\n\t" \
  "v_mfma_f32_16x16x32_bf16 v[140:143], v[64:67], v[4:7], v[140:143]\n\t" \
  "ds_read_b64 v[2:3], v31 offset:3072\n\t" \
  "v_mfma_f32_16x16x32_bf16 v[144:147], v[72:75], v[4:7], v[144:147]\n\t" \
  "v_mfma_f32_16x16x32_bf16 v[148:151], v[80:83], v[4:7], v[148:151]\n\t" \
  "v_mfma_f32_16x16x32_bf16 v[152:155], v[88:91], v[4:7], v[152:155]\n\t" \
  "v_mfma_f32_16x16x32_bf16 v[156:159], v[96:99], v[4:7], v[156:159]\n\t" \
  "ds_read_b64 v[4:5], v24 offset:3328\n\t" \
  "s_waitcnt lgkmcnt(9)\n\t" \
  "v_mfma_f32_16x16x32_bf16 v[144:147], v[68:71], v[8:11], v[144:147]\n\t" \
  "ds_read_b64 v[6:7], v25 offset:3328\n\t" \
  "v_mfma_f32_16x16x32_bf16 v[148:151], v[76:79], v[8:11], v[148:151]\n\t" \
  "v_mfma_f32_16x16x32_bf16 v[152:155], v[84:87], v[8:11], v[152:155]\n\t" \
  "v_mfma_f32_16x16x32_bf16 v[156:159], v[92:95], v[8:11], v[156:159]\n\t" \
  "ds_read_b64 v[8:9], v26 offset:3584\n\t" \
  "s_waitcnt lgkmcnt(9)\n\t" \
  "v_mfma_f32_16x16x32_bf16 v[144:147], v[64:67], v[12:15], v[144:147]\n\t" \
  "ds_read_b64 v[10:11], v27 offset:3584\n\t" \
  "v_mfma_f32_16x16x32_bf16 v[148:151], v[72:75], v[12:15], v[148:151]\n\t" \
  "v_mfma_f32_16x16x32_bf16 v[152:155], v[80:83], v[12:15], v[152:155]\n\t" \
  "v_mfma_f32_16x16x32_bf16 v[156:159], v[88:91], v[12:15], v[156:159]\n\t" \
  "ds_read_b64 v[12:13], v28 offset:3840\n\t" \
  "s_waitcnt lgkmcnt(9)\n\t" \
  "v_mfma_f32_16x16x32_bf16 v[148:151], v[68:71], v[16:19], v[148:151]\n\t" \
  "ds_read_b64 v[14:15], v29 offset:3840\n\t" \
  "v_mfma_f32_16x16x32_bf16 v[152:155], v[76:79], v[16:19], v[152:155]\n\t" \
  "v_mfma_f32_16x16x32_bf16 v[156:159], v[84:87], v[16:19], v[156:159]\n\t" \
  "s_waitcnt lgkmcnt(8)\n\t" \
  "v_mfma_f32_16x16x32_bf16 v[148:151], v[64:67], v[20:23], v[148:151]\n\t" \
  "v_mfma_f32_16x16x32_bf16 v[152:155], v[72:75], v[20:23], v[152:155]\n\t" \
  "v_mfma_f32_16x16x32_bf16 v[156:159], v[80:83], v[20:23], v[156:159]\n\t" \
  "s_waitcnt lgkmcnt(6)\n\t" \
  "v_mfma_f32_16x16x32_bf16 v[152:155], v[68:71], v[0:3], v[152:155]\n\t" \
  "v_mfma_f32_16x16x32_bf16 v[156:159], v[76:79], v[0:3], v[156:159]\n\t" \
  "s_waitcnt lgkmcnt(4)\n\t" \
  "v_mfma_f32_16x16x32_bf16 v[152:155], v[64:67], v[4:7], v[152:155]\n\t" \
  "v_mfma_f32_16x16x32_bf16 v[156:159], v[72:75], v[4:7], v[156:159]\n\t" \
  "s_waitcnt lgkmcnt(2)\n\t" \
  "v_mfma_f32_16x16x32_bf16 v[156:159], v[68:71], v[8:11], v[156:159]\n\t" \
  "s_waitcnt lgkmcnt(0)\n\t" \
  "v_mfma_f32_16x16x32_bf16 v[156:159], v[64:67], v[12:15], v[156:159]\n\t" \
  "s_waitcnt lgkmcnt(0)\n\t" \
  "s_nop 7\n\t" \
  "s_nop 1\n\t"
#define CV_CLOB "v0", "v1", "v2", "v3", "v4", "v5", "v6", "v7", "v8", "v9", "v10", "v11", "v12", "v13", "v14", "v15", "v16", "v17", "v18", "v19", "v20", "v21", "v22", "v23", "v34", "v35", "v36", "v37", "v38", "v39", "v40", "v41"

#define G3_ASM \
  "v_mov_b32 v128, 0\n\t" \
  "v_mov_b32 v129, 0\n\t" \
  "v_mov_b32 v130, 0\n\t" \
  "v_mov_b32 v131, 0\n\t" \
  "v_mov_b32 v132, 0\n\t" \
  "v_mov_b32 v133, 0\n\t" \
  "v_mov_b32 v134, 0\n\t" \
  "v_mov_b32 v135, 0\n\t" \
  "v_mov_b32 v136, 0\n\t" \
  "v_mov_b32 v137, 0\n\t" \
  "v_mov_b32 v138, 0\n\t" \
  "v_mov_b32 v139, 0\n\t" \
  "v_mov_b32 v140, 0\n\t" \
  "v_mov_b32 v141, 0\n\t" \
  "v_mov_b32 v142, 0\n\t" \
  "v_mov_b32 v143, 0\n\t" \
  "v_mov_b32 v144, 0\n\t" \
  "v_mov_b32 v145, 0\n\t" \
  "v_mov_b32 v146, 0\n\t" \
  "v_mov_b32 v147, 0\n\t" \
  "v_mov_b32 v148, 0\n\t" \
  "v_mov_b32 v149, 0\n\t" \
  "v_mov_b32 v150, 0\n\t" \
  "v_mov_b32 v151, 0\n\t" \
  "v_mov_b32 v152, 0\n\t" \
  "v_mov_b32 v153, 0\n\t" \
  "v_mov_b32 v154, 0\n\t" \
  "v_mov_b32 v155, 0\n\t" \
  "v_mov_b32 v156, 0\n\t" \
  "v_mov_b32 v157, 0\n\t" \
  "v_mov_b32 v158, 0\n\t" \
  "v_mov_b32 v159, 0\n\t" \
  "s_getpc_b64 s[92:93]\n\t" \
  "s_branch QC_%=\n\t" \
  "ds_read_b64_tr_b16 v[64:65], v96 offset:0\n\t" \
  "ds_read_b64_tr_b16 v[66:67], v96 offset:2048\n\t" \
  "ds_read_b64_tr_b16 v[68:69], v97 offset:0\n\t" \
  "ds_read_b64_tr_b16 v[70:71], v97 offset:2048\n\t" \
  "ds_read_b64_tr_b16 v[72:73], v98 offset:0\n\t" \
  "ds_read_b64_tr_b16 v[74:75], v98 offset:2048\n\t" \
  "ds_read_b64_tr_b16 v[76:77], v99 offset:0\n\t" \
  "ds_read_b64_tr_b16 v[78:79], v99 offset:2048\n\t" \
  "ds_read_b64_tr_b16 v[80:81], v96 offset:4096\n\t" \
  "ds_read_b64_tr_b16 v[82:83], v96 offset:6144\n\t" \
  "ds_read_b64_tr_b16 v[84:85], v97 offset:4096\n\t" \
  "ds_read_b64_tr_b16 v[86:87], v97 offset:6144\n\t" \
  "ds_read_b64_tr_b16 v[88:89], v98 offset:4096\n\t" \
  "ds_read_b64_tr_b16 v[90:91], v98 offset:6144\n\t" \
  "ds_read_b32 v102, v101\n\t" \
  "s_waitcnt lgkmcnt(0)\n\t" \
  "s_branch QGa0_%=\n\t" \
  "QBa0_%=:\n\t" \
  "s_cbranch_vccz QFa0_%=\n\t" \
  "s_waitcnt vmcnt(8)\n\t" \
  "QGa0_%=:\n\t" \
  "s_waitcnt lgkmcnt(11)\n\t" \
  "v_mfma_f32_16x16x32_bf16 v[128:131], v[64:67], v[0:3], v[128:131]\n\t" \
  "ds_read_b64_tr_b16 v[92:93], v99 offset:4096\n\t" \
  "v_mfma_f32_16x16x32_bf16 v[144:147], v[64:67], v[4:7], v[144:147]\n\t" \
  "ds_read_b64_tr_b16 v[94:95], v99 offset:6144\n\t" \
  "s_waitcnt lgkmcnt(7)\n\t" \
  "v_cmp_lt_u32 vcc, %[th], v102\n\t" \
  "v_mfma_f32_16x16x32_bf16 v[132:135], v[68:71], v[0:3], v[132:135]\n\t" \
  "s_mov_b64 exec, vcc\n\t" \
  "global_load_dwordx4 v[48:51], v100, %[ob] offset:0 sc1\n\t" \
  "s_mov_b64 exec, -1\n\t" \
  "v_mfma_f32_16x16x32_bf16 v[148:151], v[68:71], v[4:7], v[148:151]\n\t" \
  "ds_read_b64_tr_b16 v[64:65], v96 offset:8192\n\t" \
  "ds_read_b64_tr_b16 v[66:67], v96 offset:10240\n\t" \
  "v_mfma_f32_16x16x32_bf16 v[136:139], v[72:75], v[0:3], v[136:139]\n\t" \
  "s_mov_b64 exec, vcc\n\t" \
  "global_load_dwordx4 v[52:55], v100, %[ob] offset:1024 sc1\n\t" \
  "s_mov_b64 exec, -1\n\t" \
  "v_mfma_f32_16x16x32_bf16 v[152:155], v[72:75], v[4:7], v[152:155]\n\t" \
  "ds_read_b64_tr_b16 v[68:69], v97 offset:8192\n\t" \
  "ds_read_b64_tr_b16 v[70:71], v97 offset:10240\n\t" \
  "v_mfma_f32_16x16x32_bf16 v[140:143], v[76:79], v[0:3], v[140:143]\n\t" \
  "s_mov_b64 exec, vcc\n\t" \
  "global_load_dwordx4 v[56:59], v100, %[ob] offset:2048 sc1\n\t" \
  "s_mov_b64 exec, -1\n\t" \
  "v_mfma_f32_16x16x32_bf16 v[156:159], v[76:79], v[4:7], v[156:159]\n\t" \
  "ds_read_b64_tr_b16 v[72:73], v98 offset:8192\n\t" \
  "ds_read_b64_tr_b16 v[74:75], v98 offset:10240\n\t" \
  "s_waitcnt lgkmcnt(10)\n\t" \
  "v_mfma_f32_16x16x32_bf16 v[128:131], v[80:83], v[8:11], v[128:131]\n\t" \
  "s_mov_b64 exec, vcc\n\t" \
  "global_load_dwordx4 v[60:63], v100, %[ob] offset:3072 sc1\n\t" \
  "s_mov_b64 exec, -1\n\t" \
  "v_mfma_f32_16x16x32_bf16 v[144:147], v[80:83], v[12:15], v[144:147]\n\t" \
  "ds_read_b64_tr_b16 v[76:77], v99 offset:8192\n\t" \
  "ds_read_b64_tr_b16 v[78:79], v99 offset:10240\n\t" \
  "v_mfma_f32_16x16x32_bf16 v[132:135], v[84:87], v[8:11], v[132:135]\n\t" \
  "ds_read_b64_tr_b16 v[80:81], v96 offset:12288\n\t" \
  "s_and_b32 %[t0], vcc_lo, 0x4000\n\t" \
  "ds_read_b32 v102, v101\n\t" \
  "v_mfma_f32_16x16x32_bf16 v[148:151], v[84:87], v[12:15], v[148:151]\n\t" \
  "ds_read_b64_tr_b16 v[82:83], v96 offset:14336\n\t" \
  "v_add_u32 v100, %[t0], v100\n\t" \
  "s_waitcnt lgkmcnt(11)\n\t" \
  "v_mfma_f32_16x16x32_bf16 v[136:139], v[88:91], v[8:11], v[136:139]\n\t" \
  "ds_read_b64_tr_b16 v[84:85], v97 offset:12288\n\t" \
  "v_and_b32 v100, 0x3ffff, v100\n\t" \
  "v_mfma_f32_16x16x32_bf16 v[152:155], v[88:91], v[12:15], v[152:155]\n\t" \
  "ds_read_b64_tr_b16 v[86:87], v97 offset:14336\n\t" \
  "s_sub_u32 %[iss], %[iss], vcc_lo\n\t" \
  "v_mfma_f32_16x16x32_bf16 v[140:143], v[92:95], v[8:11], v[140:143]\n\t" \
  "ds_read_b64_tr_b16 v[88:89], v98 offset:12288\n\t" \
  "s_add_u32 %[c], %[c], 1\n\t" \
  "v_mfma_f32_16x16x32_bf16 v[156:159], v[92:95], v[12:15], v[156:159]\n\t" \
  "ds_read_b64_tr_b16 v[90:91], v98 offset:14336\n\t" \
  "s_add_u32 %[th], %[th], 1\n\t" \
  "QBa1_%=:\n\t" \
  "s_cbranch_vccz QFa1_%=\n\t" \
  "s_waitcnt vmcnt(8)\n\t" \
  "QGa1_%=:\n\t" \
  "s_waitcnt lgkmcnt(11)\n\t" \
  "v_mfma_f32_16x16x32_bf16 v[128:131], v[64:67], v[16:19], v[128:131]\n\t" \
  "ds_read_b64_tr_b16 v[92:93], v99 offset:12288\n\t" \
  "v_mfma_f32_16x16x32_bf16 v[144:147], v[64:67], v[20:23], v[144:147]\n\t" \
  "ds_read_b64_tr_b16 v[94:95], v99 offset:14336\n\t" \
  "s_waitcnt lgkmcnt(7)\n\t" \
  "v_cmp_lt_u32 vcc, %[th], v102\n\t" \
  "v_mfma_f32_16x16x32_bf16 v[132:135], v[68:71], v[16:19], v[132:135]\n\t" \
  "s_mov_b64 exec, vcc\n\t" \
  "global_load_dwordx4 v[0:3], v100, %[ob] offset:0 sc1\n\t" \
  "s_mov_b64 exec, -1\n\t" \
  "v_mfma_f32_16x16x32_bf16 v[148:151], v[68:71], v[20:23], v[148:151]\n\t" \
  "ds_read_b64_tr_b16 v[64:65], v96 offset:16384\n\t" \
  "ds_read_b64_tr_b16 v[66:67], v96 offset:18432\n\t" \
  "v_mfma_f32_16x16x32_bf16 v[136:139], v[72:75], v[16:19], v[136:139]\n\t" \
  "s_mov_b64 exec, vcc\n\t" \
  "global_load_dwordx4 v[4:7], v100, %[ob] offset:1024 sc1\n\t" \
  "s_mov_b64 exec, -1\n\t" \
  "v_mfma_f32_16x16x32_bf16 v[152:155], v[72:75], v[20:23], v[152:155]\n\t" \
  "ds_read_b64_tr_b16 v[68:69], v97 offset:16384\n\t" \
  "ds_read_b64_tr_b16 v[70:71], v97 offset:18432\n\t" \
  "v_mfma_f32_16x16x32_bf16 v[140:143], v[76:79], v[16:19], v[140:143]\n\t" \
  "s_mov_b64 exec, vcc\n\t" \
  "global_load_dwordx4 v[8:11], v100, %[ob] offset:2048 sc1\n\t" \
  "s_mov_b64 exec, -1\n\t" \
  "v_mfma_f32_16x16x32_bf16 v[156:159], v[76:79], v[20:23], v[156:159]\n\t" \
  "ds_read_b64_tr_b16 v[72:73], v98 offset:16384\n\t" \
  "ds_read_b64_tr_b16 v[74:75], v98 offset:18432\n\t" \
  "s_waitcnt lgkmcnt(10)\n\t" \
  "v_mfma_f32_16x16x32_bf16 v[128:131], v[80:83], v[24:27], v[128:131]\n\t" \
  "s_mov_b64 exec, vcc\n\t" \
  "global_load_dwordx4 v[12:15], v100, %[ob] offset:3072 sc1\n\t" \
  "s_mov_b64 exec, -1\n\t" \
  "v_mfma_f32_16x16x32_bf16 v[144:147], v[80:83], v[28:31], v[144:147]\n\t" \
  "ds_read_b64_tr_b16 v[76:77], v99 offset:16384\n\t" \
  "ds_read_b64_tr_b16 v[78:79], v99 offset:18432\n\t" \
  "v_mfma_f32_16x16x32_bf16 v[132:135], v[84:87], v[24:27], v[132:135]\n\t" \
  "ds_read_b64_tr_b16 v[80:81], v96 offset:20480\n\t" \
  "s_and_b32 %[t0], vcc_lo, 0x4000\n\t" \
  "ds_read_b32 v102, v101\n\t" \
  "v_mfma_f32_16x16x32_bf16 v[148:151], v[84:87], v[28:31], v[148:151]\n\t" \
  "ds_read_b64_tr_b16 v[82:83], v96 offset:22528\n\t" \
  "v_add_u32 v100, %[t0], v100\n\t" \
  "s_waitcnt lgkmcnt(11)\n\t" \
  "v_mfma_f32_16x16x32_bf16 v[136:139], v[88:91], v[24:27], v[136:139]\n\t" \
  "ds_read_b64_tr_b16 v[84:85], v97 offset:20480\n\t" \
  "v_and_b32 v100, 0x3ffff, v100\n\t" \
  "v_mfma_f32_16x16x32_bf16 v[152:155], v[88:91], v[28:31], v[152:155]\n\t" \
  "ds_read_b64_tr_b16 v[86:87], v97 offset:22528\n\t" \
  "s_sub_u32 %[iss], %[iss], vcc_lo\n\t" \
  "v_mfma_f32_16x16x32_bf16 v[140:143], v[92:95], v[24:27], v[140:143]\n\t" \
  "ds_read_b64_tr_b16 v[88:89], v98 offset:20480\n\t" \
  "s_add_u32 %[c], %[c], 1\n\t" \
  "v_mfma_f32_16x16x32_bf16 v[156:159], v[92:95], v[28:31], v[156:159]\n\t" \
  "ds_read_b64_tr_b16 v[90:91], v98 offset:22528\n\t" \
  "s_add_u32 %[th], %[th], 1\n\t" \
  "QBa2_%=:\n\t" \
  "s_cbranch_vccz QFa2_%=\n\t" \
  "s_waitcnt vmcnt(8)\n\t" \
  "QGa2_%=:\n\t" \
  "s_waitcnt lgkmcnt(11)\n\t" \
  "v_mfma_f32_16x16x32_bf16 v[128:131], v[64:67], v[32:35], v[128:131]\n\t" \
  "ds_read_b64_tr_b16 v[92:93], v99 offset:20480\n\t" \
  "v_mfma_f32_16x16x32_bf16 v[144:147], v[64:67], v[36:39], v[144:147]\n\t" \
  "ds_read_b64_tr_b16 v[94:95], v99 offset:22528\n\t" \
  "s_waitcnt lgkmcnt(7)\n\t" \
  "v_cmp_lt_u32 vcc, %[th], v102\n\t" \
  "v_mfma_f32_16x16x32_bf16 v[132:135], v[68:71], v[32:35], v[132:135]\n\t" \
  "s_mov_b64 exec, vcc\n\t" \
  "global_load_dwordx4 v[16:19], v100, %[ob] offset:0 sc1\n\t" \
  "s_mov_b64 exec, -1\n\t" \
  "v_mfma_f32_16x16x32_bf16 v[148:151], v[68:71], v[36:39], v[148:151]\n\t" \
  "ds_read_b64_tr_b16 v[64:65], v96 offset:24576\n\t" \
  "ds_read_b64_tr_b16 v[66:67], v96 offset:26624\n\t" \
  "v_mfma_f32_16x16x32_bf16 v[136:139], v[72:75], v[32:35], v[136:139]\n\t" \
  "s_mov_b64 exec, vcc\n\t" \
  "global_load_dwordx4 v[20:23], v100, %[ob] offset:1024 sc1\n\t" \
  "s_mov_b64 exec, -1\n\t" \
  "v_mfma_f32_16x16x32_bf16 v[152:155], v[72:75], v[36:39], v[152:155]\n\t" \
  "ds_read_b64_tr_b16 v[68:69], v97 offset:24576\n\t" \
  "ds_read_b64_tr_b16 v[70:71], v97 offset:26624\n\t" \
  "v_mfma_f32_16x16x32_bf16 v[140:143], v[76:79], v[32:35], v[140:143]\n\t" \
  "s_mov_b64 exec, vcc\n\t" \
  "global_load_dwordx4 v[24:27], v100, %[ob] offset:2048 sc1\n\t" \
  "s_mov_b64 exec, -1\n\t" \
  "v_mfma_f32_16x16x32_bf16 v[156:159], v[76:79], v[36:39], v[156:159]\n\t" \
  "ds_read_b64_tr_b16 v[72:73], v98 offset:24576\n\t" \
  "ds_read_b64_tr_b16 v[74:75], v98 offset:26624\n\t" \
  "s_waitcnt lgkmcnt(10)\n\t" \
  "v_mfma_f32_16x16x32_bf16 v[128:131], v[80:83], v[40:43], v[128:131]\n\t" \
  "s_mov_b64 exec, vcc\n\t" \
  "global_load_dwordx4 v[28:31], v100, %[ob] offset:3072 sc1\n\t" \
  "s_mov_b64 exec, -1\n\t" \
  "v_mfma_f32_16x16x32_bf16 v[144:147], v[80:83], v[44:47], v[144:147]\n\t" \
  "ds_read_b64_tr_b16 v[76:77], v99 offset:24576\n\t" \
  "ds_read_b64_tr_b16 v[78:79], v99 offset:26624\n\t" \
  "v_mfma_f32_16x16x32_bf16 v[132:135], v[84:87], v[40:43], v[132:135]\n\t" \
  "ds_read_b64_tr_b16 v[80:81], v96 offset:28672\n\t" \
  "s_and_b32 %[t0], vcc_lo, 0x4000\n\t" \
  "ds_read_b32 v102, v101\n\t" \
  "v_mfma_f32_16x16x32_bf16 v[148:151], v[84:87], v[44:47], v[148:151]\n\t" \
  "ds_read_b64_tr_b16 v[82:83], v96 offset:30720\n\t" \
  "v_add_u32 v100, %[t0], v100\n\t" \
  "s_waitcnt lgkmcnt(11)\n\t" \
  "v_mfma_f32_16x16x32_bf16 v[136:139], v[88:91], v[40:43], v[136:139]\n\t" \
  "ds_read_b64_tr_b16 v[84:85], v97 offset:28672\n\t" \
  "v_and_b32 v100, 0x3ffff, v100\n\t" \
  "v_mfma_f32_16x16x32_bf16 v[152:155], v[88:91], v[44:47], v[152:155]\n\t" \
  "ds_read_b64_tr_b16 v[86:87], v97 offset:30720\n\t" \
  "s_sub_u32 %[iss], %[iss], vcc_lo\n\t" \
  "v_mfma_f32_16x16x32_bf16 v[140:143], v[92:95], v[40:43], v[140:143]\n\t" \
  "ds_read_b64_tr_b16 v[88:89], v98 offset:28672\n\t" \
  "s_add_u32 %[c], %[c], 1\n\t" \
  "v_mfma_f32_16x16x32_bf16 v[156:159], v[92:95], v[44:47], v[156:159]\n\t" \
  "ds_read_b64_tr_b16 v[90:91], v98 offset:30720\n\t" \
  "s_add_u32 %[th], %[th], 1\n\t" \
  "QBa3_%=:\n\t" \
  "s_cbranch_vccz QFa3_%=\n\t" \
  "s_waitcnt vmcnt(8)\n\t" \
  "QGa3_%=:\n\t" \
  "s_waitcnt lgkmcnt(11)\n\t" \
  "v_mfma_f32_16x16x32_bf16 v[128:131], v[64:67], v[48:51], v[128:131]\n\t" \
  "ds_read_b64_tr_b16 v[92:93], v99 offset:28672\n\t" \
  "v_mfma_f32_16x16x32_bf16 v[144:147], v[64:67], v[52:55], v[144:147]\n\t" \
  "ds_read_b64_tr_b16 v[94:95], v99 offset:30720\n\t" \
  "s_waitcnt lgkmcnt(7)\n\t" \
  "v_cmp_lt_u32 vcc, %[th], v102\n\t" \
  "v_mfma_f32_16x16x32_bf16 v[132:135], v[68:71], v[48:51], v[132:135]\n\t" \
  "s_mov_b64 exec, vcc\n\t" \
  "global_load_dwordx4 v[32:35], v100, %[ob] offset:0 sc1\n\t" \
  "s_mov_b64 exec, -1\n\t" \
  "v_mfma_f32_16x16x32_bf16 v[148:151], v[68:71], v[52:55], v[148:151]\n\t" \
  "ds_read_b64_tr_b16 v[64:65], v96 offset:32768\n\t" \
  "ds_read_b64_tr_b16 v[66:67], v96 offset:34816\n\t" \
  "v_mfma_f32_16x16x32_bf16 v[136:139], v[72:75], v[48:51], v[136:139]\n\t" \
  "s_mov_b64 exec, vcc\n\t" \
  "global_load_dwordx4 v[36:39], v100, %[ob] offset:1024 sc1\n\t" \
  "s_mov_b64 exec, -1\n\t" \
  "v_mfma_f32_16x16x32_bf16 v[152:155], v[72:75], v[52:55], v[152:155]\n\t" \
  "ds_read_b64_tr_b16 v[68:69], v97 offset:32768\n\t" \
  "ds_read_b64_tr_b16 v[70:71], v97 offset:34816\n\t" \
  "v_mfma_f32_16x16x32_bf16 v[140:143], v[76:79], v[48:51], v[140:143]\n\t" \
  "s_mov_b64 exec, vcc\n\t" \
  "global_load_dwordx4 v[40:43], v100, %[ob] offset:2048 sc1\n\t" \
  "s_mov_b64 exec, -1\n\t" \
  "v_mfma_f32_16x16x32_bf16 v[156:159], v[76:79], v[52:55], v[156:159]\n\t" \
  "ds_read_b64_tr_b16 v[72:73], v98 offset:32768\n\t" \
  "ds_read_b64_tr_b16 v[74:75], v98 offset:34816\n\t" \
  "s_waitcnt lgkmcnt(10)\n\t" \
  "v_mfma_f32_16x16x32_bf16 v[128:131], v[80:83], v[56:59], v[128:131]\n\t" \
  "s_mov_b64 exec, vcc\n\t" \
  "global_load_dwordx4 v[44:47], v100, %[ob] offset:3072 sc1\n\t" \
  "s_mov_b64 exec, -1\n\t" \
  "v_mfma_f32_16x16x32_bf16 v[144:147], v[80:83], v[60:63], v[144:147]\n\t" \
  "ds_read_b64_tr_b16 v[76:77], v99 offset:32768\n\t" \
  "ds_read_b64_tr_b16 v[78:79], v99 offset:34816\n\t" \
  "v_mfma_f32_16x16x32_bf16 v[132:135], v[84:87], v[56:59], v[132:135]\n\t" \
  "ds_read_b64_tr_b16 v[80:81], v96 offset:36864\n\t" \
  "s_and_b32 %[t0], vcc_lo, 0x4000\n\t" \
  "ds_read_b32 v102, v101\n\t" \
  "v_mfma_f32_16x16x32_bf16 v[148:151], v[84:87], v[60:63], v[148:151]\n\t" \
  "ds_read_b64_tr_b16 v[82:83], v96 offset:38912\n\t" \
  "v_add_u32 v100, %[t0], v100\n\t" \
  "s_waitcnt lgkmcnt(11)\n\t" \
  "v_mfma_f32_16x16x32_bf16 v[136:139], v[88:91], v[56:59], v[136:139]\n\t" \
  "ds_read_b64_tr_b16 v[84:85], v97 offset:36864\n\t" \
  "v_and_b32 v100, 0x3ffff, v100\n\t" \
  "v_mfma_f32_16x16x32_bf16 v[152:155], v[88:91], v[60:63], v[152:155]\n\t" \
  "ds_read_b64_tr_b16 v[86:87], v97 offset:38912\n\t" \
  "s_sub_u32 %[iss], %[iss], vcc_lo\n\t" \
  "v_mfma_f32_16x16x32_bf16 v[140:143], v[92:95], v[56:59], v[140:143]\n\t" \
  "ds_read_b64_tr_b16 v[88:89], v98 offset:36864\n\t" \
  "s_add_u32 %[c], %[c], 1\n\t" \
  "v_mfma_f32_16x16x32_bf16 v[156:159], v[92:95], v[60:63], v[156:159]\n\t" \
  "ds_read_b64_tr_b16 v[90:91], v98 offset:38912\n\t" \
  "s_add_u32 %[th], %[th], 1\n\t" \
  "s_cmp_lt_u32 %[c], 12\n\t" \
  "v_add_u32 v96, 0x8000, v96\n\t" \
  "v_add_u32 v97, 0x8000, v97\n\t" \
  "v_add_u32 v98, 0x8000, v98\n\t" \
  "v_add_u32 v99, 0x8000, v99\n\t" \
  "s_cbranch_scc1 QBa0_%=\n\t" \
  "QBb0_%=:\n\t" \
  "s_cbranch_vccz QFb0_%=\n\t" \
  "s_waitcnt vmcnt(8)\n\t" \
  "QGb0_%=:\n\t" \
  "s_waitcnt lgkmcnt(11)\n\t" \
  "v_mfma_f32_16x16x32_bf16 v[128:131], v[64:67], v[0:3], v[128:131]\n\t" \
  "ds_read_b64_tr_b16 v[92:93], v99 offset:4096\n\t" \
  "v_mfma_f32_16x16x32_bf16 v[144:147], v[64:67], v[4:7], v[144:147]\n\t" \
  "ds_read_b64_tr_b16 v[94:95], v99 offset:6144\n\t" \
  "s_waitcnt lgkmcnt(7)\n\t" \
  "v_cmp_lt_u32 vcc, %[th], v102\n\t" \
  "v_mfma_f32_16x16x32_bf16 v[132:135], v[68:71], v[0:3], v[132:135]\n\t" \
  "s_mov_b64 exec, vcc\n\t" \
  "global_load_dwordx4 v[48:51], v100, %[ob] offset:0 sc1\n\t" \
  "s_mov_b64 exec, -1\n\t" \
  "v_mfma_f32_16x16x32_bf16 v[148:151], v[68:71], v[4:7], v[148:151]\n\t" \
  "ds_read_b64_tr_b16 v[64:65], v96 offset:8192\n\t" \
  "ds_read_b64_tr_b16 v[66:67], v96 offset:10240\n\t" \
  "v_mfma_f32_16x16x32_bf16 v[136:139], v[72:75], v[0:3], v[136:139]\n\t" \
  "s_mov_b64 exec, vcc\n\t" \
  "global_load_dwordx4 v[52:55], v100, %[ob] offset:1024 sc1\n\t" \
  "s_mov_b64 exec, -1\n\t" \
  "v_mfma_f32_16x16x32_bf16 v[152:155], v[72:75], v[4:7], v[152:155]\n\t" \
  "ds_read_b64_tr_b16 v[68:69], v97 offset:8192\n\t" \
  "ds_read_b64_tr_b16 v[70:71], v97 offset:10240\n\t" \
  "v_mfma_f32_16x16x32_bf16 v[140:143], v[76:79], v[0:3], v[140:143]\n\t" \
  "s_mov_b64 exec, vcc\n\t" \
  "global_load_dwordx4 v[56:59], v100, %[ob] offset:2048 sc1\n\t" \
  "s_mov_b64 exec, -1\n\t" \
  "v_mfma_f32_16x16x32_bf16 v[156:159], v[76:79], v[4:7], v[156:159]\n\t" \
  "ds_read_b64_tr_b16 v[72:73], v98 offset:8192\n\t" \
  "ds_read_b64_tr_b16 v[74:75], v98 offset:10240\n\t" \
  "s_waitcnt lgkmcnt(10)\n\t" \
  "v_mfma_f32_16x16x32_bf16 v[128:131], v[80:83], v[8:11], v[128:131]\n\t" \
  "s_mov_b64 exec, vcc\n\t" \
  "global_load_dwordx4 v[60:63], v100, %[ob] offset:3072 sc1\n\t" \
  "s_mov_b64 exec, -1\n\t" \
  "v_mfma_f32_16x16x32_bf16 v[144:147], v[80:83], v[12:15], v[144:147]\n\t" \
  "ds_read_b64_tr_b16 v[76:77], v99 offset:8192\n\t" \
  "ds_read_b64_tr_b16 v[78:79], v99 offset:10240\n\t" \
  "v_mfma_f32_16x16x32_bf16 v[132:135], v[84:87], v[8:11], v[132:135]\n\t" \
  "ds_read_b64_tr_b16 v[80:81], v96 offset:12288\n\t" \
  "s_and_b32 %[t0], vcc_lo, 0x4000\n\t" \
  "v_mfma_f32_16x16x32_bf16 v[148:151], v[84:87], v[12:15], v[148:151]\n\t" \
  "ds_read_b64_tr_b16 v[82:83], v96 offset:14336\n\t" \
  "v_add_u32 v100, %[t0], v100\n\t" \
  "s_waitcnt lgkmcnt(10)\n\t" \
  "v_mfma_f32_16x16x32_bf16 v[136:139], v[88:91], v[8:11], v[136:139]\n\t" \
  "ds_read_b64_tr_b16 v[84:85], v97 offset:12288\n\t" \
  "v_and_b32 v100, 0x3ffff, v100\n\t" \
  "v_mfma_f32_16x16x32_bf16 v[152:155], v[88:91], v[12:15], v[152:155]\n\t" \
  "ds_read_b64_tr_b16 v[86:87], v97 offset:14336\n\t" \
  "s_sub_u32 %[iss], %[iss], vcc_lo\n\t" \
  "v_mfma_f32_16x16x32_bf16 v[140:143], v[92:95], v[8:11], v[140:143]\n\t" \
  "ds_read_b64_tr_b16 v[88:89], v98 offset:12288\n\t" \
  "s_add_u32 %[c], %[c], 1\n\t" \
  "v_mfma_f32_16x16x32_bf16 v[156:159], v[92:95], v[12:15], v[156:159]\n\t" \
  "ds_read_b64_tr_b16 v[90:91], v98 offset:14336\n\t" \
  "s_add_u32 %[th], %[th], 1\n\t" \
  "QBb1_%=:\n\t" \
  "s_cbranch_vccz QFb1_%=\n\t" \
  "s_waitcnt vmcnt(8)\n\t" \
  "QGb1_%=:\n\t" \
  "s_waitcnt lgkmcnt(10)\n\t" \
  "v_mfma_f32_16x16x32_bf16 v[128:131], v[64:67], v[16:19], v[128:131]\n\t" \
  "ds_read_b64_tr_b16 v[92:93], v99 offset:12288\n\t" \
  "v_mfma_f32_16x16x32_bf16 v[144:147], v[64:67], v[20:23], v[144:147]\n\t" \
  "ds_read_b64_tr_b16 v[94:95], v99 offset:14336\n\t" \
  "v_mfma_f32_16x16x32_bf16 v[132:135], v[68:71], v[16:19], v[132:135]\n\t" \
  "ds_read_b64_tr_b16 v[64:65], v96 offset:16384\n\t" \
  "v_mfma_f32_16x16x32_bf16 v[148:151], v[68:71], v[20:23], v[148:151]\n\t" \
  "ds_read_b64_tr_b16 v[66:67], v96 offset:18432\n\t" \
  "s_waitcnt lgkmcnt(10)\n\t" \
  "v_mfma_f32_16x16x32_bf16 v[136:139], v[72:75], v[16:19], v[136:139]\n\t" \
  "ds_read_b64_tr_b16 v[68:69], v97 offset:16384\n\t" \
  "v_mfma_f32_16x16x32_bf16 v[152:155], v[72:75], v[20:23], v[152:155]\n\t" \
  "ds_read_b64_tr_b16 v[70:71], v97 offset:18432\n\t" \
  "v_mfma_f32_16x16x32_bf16 v[140:143], v[76:79], v[16:19], v[140:143]\n\t" \
  "ds_read_b64_tr_b16 v[72:73], v98 offset:16384\n\t" \
  "v_mfma_f32_16x16x32_bf16 v[156:159], v[76:79], v[20:23], v[156:159]\n\t" \
  "ds_read_b64_tr_b16 v[74:75], v98 offset:18432\n\t" \
  "s_waitcnt lgkmcnt(10)\n\t" \
  "v_mfma_f32_16x16x32_bf16 v[128:131], v[80:83], v[24:27], v[128:131]\n\t" \
  "ds_read_b64_tr_b16 v[76:77], v99 offset:16384\n\t" \
  "v_mfma_f32_16x16x32_bf16 v[144:147], v[80:83], v[28:31], v[144:147]\n\t" \
  "ds_read_b64_tr_b16 v[78:79], v99 offset:18432\n\t" \
  "v_mfma_f32_16x16x32_bf16 v[132:135], v[84:87], v[24:27], v[132:135]\n\t" \
  "ds_read_b64_tr_b16 v[80:81], v96 offset:20480\n\t" \
  "v_mfma_f32_16x16x32_bf16 v[148:151], v[84:87], v[28:31], v[148:151]\n\t" \
  "ds_read_b64_tr_b16 v[82:83], v96 offset:22528\n\t" \
  "s_waitcnt lgkmcnt(10)\n\t" \
  "v_mfma_f32_16x16x32_bf16 v[136:139], v[88:91], v[24:27], v[136:139]\n\t" \
  "ds_read_b64_tr_b16 v[84:85], v97 offset:20480\n\t" \
  "v_mfma_f32_16x16x32_bf16 v[152:155], v[88:91], v[28:31], v[152:155]\n\t" \
  "ds_read_b64_tr_b16 v[86:87], v97 offset:22528\n\t" \
  "v_mfma_f32_16x16x32_bf16 v[140:143], v[92:95], v[24:27], v[140:143]\n\t" \
  "ds_read_b64_tr_b16 v[88:89], v98 offset:20480\n\t" \
  "s_add_u32 %[c], %[c], 1\n\t" \
  "v_mfma_f32_16x16x32_bf16 v[156:159], v[92:95], v[28:31], v[156:159]\n\t" \
  "ds_read_b64_tr_b16 v[90:91], v98 offset:22528\n\t" \
  "QBb2_%=:\n\t" \
  "s_cbranch_vccz QFb2_%=\n\t" \
  "s_waitcnt vmcnt(4)\n\t" \
  "QGb2_%=:\n\t" \
  "s_waitcnt lgkmcnt(10)\n\t" \
  "v_mfma_f32_16x16x32_bf16 v[128:131], v[64:67], v[32:35], v[128:131]\n\t" \
  "ds_read_b64_tr_b16 v[92:93], v99 offset:20480\n\t" \
  "v_mfma_f32_16x16x32_bf16 v[144:147], v[64:67], v[36:39], v[144:147]\n\t" \
  "ds_read_b64_tr_b16 v[94:95], v99 offset:22528\n\t" \
  "v_mfma_f32_16x16x32_bf16 v[132:135], v[68:71], v[32:35], v[132:135]\n\t" \
  "ds_read_b64_tr_b16 v[64:65], v96 offset:24576\n\t" \
  "v_mfma_f32_16x16x32_bf16 v[148:151], v[68:71], v[36:39], v[148:151]\n\t" \
  "ds_read_b64_tr_b16 v[66:67], v96 offset:26624\n\t" \
  "s_waitcnt lgkmcnt(10)\n\t" \
  "v_mfma_f32_16x16x32_bf16 v[136:139], v[72:75], v[32:35], v[136:139]\n\t" \
  "ds_read_b64_tr_b16 v[68:69], v97 offset:24576\n\t" \
  "v_mfma_f32_16x16x32_bf16 v[152:155], v[72:75], v[36:39], v[152:155]\n\t" \
  "ds_read_b64_tr_b16 v[70:71], v97 offset:26624\n\t" \
  "v_mfma_f32_16x16x32_bf16 v[140:143], v[76:79], v[32:35], v[140:143]\n\t" \
  "ds_read_b64_tr_b16 v[72:73], v98 offset:24576\n\t" \
  "v_mfma_f32_16x16x32_bf16 v[156:159], v[76:79], v[36:39], v[156:159]\n\t" \
  "ds_read_b64_tr_b16 v[74:75], v98 offset:26624\n\t" \
  "s_waitcnt lgkmcnt(10)\n\t" \
  "v_mfma_f32_16x16x32_bf16 v[128:131], v[80:83], v[40:43], v[128:131]\n\t" \
  "ds_read_b64_tr_b16 v[76:77], v99 offset:24576\n\t" \
  "v_mfma_f32_16x16x32_bf16 v[144:147], v[80:83], v[44:47], v[144:147]\n\t" \
  "ds_read_b64_tr_b16 v[78:79], v99 offset:26624\n\t" \
  "v_mfma_f32_16x16x32_bf16 v[132:135], v[84:87], v[40:43], v[132:135]\n\t" \
  "ds_read_b64_tr_b16 v[80:81], v96 offset:28672\n\t" \
  "v_mfma_f32_16x16x32_bf16 v[148:151], v[84:87], v[44:47], v[148:151]\n\t" \
  "ds_read_b64_tr_b16 v[82:83], v96 offset:30720\n\t" \
  "s_waitcnt lgkmcnt(10)\n\t" \
  "v_mfma_f32_16x16x32_bf16 v[136:139], v[88:91], v[40:43], v[136:139]\n\t" \
  "ds_read_b64_tr_b16 v[84:85], v97 offset:28672\n\t" \
  "v_mfma_f32_16x16x32_bf16 v[152:155], v[88:91], v[44:47], v[152:155]\n\t" \
  "ds_read_b64_tr_b16 v[86:87], v97 offset:30720\n\t" \
  "v_mfma_f32_16x16x32_bf16 v[140:143], v[92:95], v[40:43], v[140:143]\n\t" \
  "ds_read_b64_tr_b16 v[88:89], v98 offset:28672\n\t" \
  "s_add_u32 %[c], %[c], 1\n\t" \
  "v_mfma_f32_16x16x32_bf16 v[156:159], v[92:95], v[44:47], v[156:159]\n\t" \
  "ds_read_b64_tr_b16 v[90:91], v98 offset:30720\n\t" \
  "QBb3_%=:\n\t" \
  "s_cbranch_vccz QFb3_%=\n\t" \
  "s_waitcnt vmcnt(0)\n\t" \
  "QGb3_%=:\n\t" \
  "s_waitcnt lgkmcnt(10)\n\t" \
  "v_mfma_f32_16x16x32_bf16 v[128:131], v[64:67], v[48:51], v[128:131]\n\t" \
  "ds_read_b64_tr_b16 v[92:93], v99 offset:28672\n\t" \
  "v_mfma_f32_16x16x32_bf16 v[144:147], v[64:67], v[52:55], v[144:147]\n\t" \
  "ds_read_b64_tr_b16 v[94:95], v99 offset:30720\n\t" \
  "v_mfma_f32_16x16x32_bf16 v[132:135], v[68:71], v[48:51], v[132:135]\n\t" \
  "v_mfma_f32_16x16x32_bf16 v[148:151], v[68:71], v[52:55], v[148:151]\n\t" \
  "s_waitcnt lgkmcnt(8)\n\t" \
  "v_mfma_f32_16x16x32_bf16 v[136:139], v[72:75], v[48:51], v[136:139]\n\t" \
  "v_mfma_f32_16x16x32_bf16 v[152:155], v[72:75], v[52:55], v[152:155]\n\t" \
  "v_mfma_f32_16x16x32_bf16 v[140:143], v[76:79], v[48:51], v[140:143]\n\t" \
  "v_mfma_f32_16x16x32_bf16 v[156:159], v[76:79], v[52:55], v[156:159]\n\t" \
  "s_waitcnt lgkmcnt(4)\n\t" \
  "v_mfma_f32_16x16x32_bf16 v[128:131], v[80:83], v[56:59], v[128:131]\n\t" \
  "v_mfma_f32_16x16x32_bf16 v[144:147], v[80:83], v[60:63], v[144:147]\n\t" \
  "v_mfma_f32_16x16x32_bf16 v[132:135], v[84:87], v[56:59], v[132:135]\n\t" \
  "v_mfma_f32_16x16x32_bf16 v[148:151], v[84:87], v[60:63], v[148:151]\n\t" \
  "s_waitcnt lgkmcnt(0)\n\t" \
  "v_mfma_f32_16x16x32_bf16 v[136:139], v[88:91], v[56:59], v[136:139]\n\t" \
  "v_mfma_f32_16x16x32_bf16 v[152:155], v[88:91], v[60:63], v[152:155]\n\t" \
  "v_mfma_f32_16x16x32_bf16 v[140:143], v[92:95], v[56:59], v[140:143]\n\t" \
  "s_add_u32 %[c], %[c], 1\n\t" \
  "v_mfma_f32_16x16x32_bf16 v[156:159], v[92:95], v[60:63], v[156:159]\n\t" \
  "s_branch QX_%=\n\t" \
  "QFa0_%=:\n\t" \
  "s_getpc_b64 s[92:93]\n\t" \
  "s_branch QC_%=\n\t" \
  "s_branch QGa0_%=\n\t" \
  "QFa1_%=:\n\t" \
  "s_getpc_b64 s[92:93]\n\t" \
  "s_branch QC_%=\n\t" \
  "s_branch QGa1_%=\n\t" \
  "QFa2_%=:\n\t" \
  "s_getpc_b64 s[92:93]\n\t" \
  "s_branch QC_%=\n\t" \
  "s_branch QGa2_%=\n\t" \
  "QFa3_%=:\n\t" \
  "s_getpc_b64 s[92:93]\n\t" \
  "s_branch QC_%=\n\t" \
  "s_branch QGa3_%=\n\t" \
  "QFb0_%=:\n\t" \
  "s_getpc_b64 s[92:93]\n\t" \
  "s_branch QC_%=\n\t" \
  "s_branch QGb0_%=\n\t" \
  "QFb1_%=:\n\t" \
  "s_getpc_b64 s[92:93]\n\t" \
  "s_branch QC_%=\n\t" \
  "s_branch QGb1_%=\n\t" \
  "QFb2_%=:\n\t" \
  "s_getpc_b64 s[92:93]\n\t" \
  "s_branch QC_%=\n\t" \
  "s_branch QGb2_%=\n\t" \
  "QFb3_%=:\n\t" \
  "s_getpc_b64 s[92:93]\n\t" \
  "s_branch QC_%=\n\t" \
  "s_branch QGb3_%=\n\t" \
  "QC_%=:\n\t" \
  "s_add_u32 s92, s92, 4\n\t" \
  "s_addc_u32 s93, s93, 0\n\t" \
  "QU_%=:\n\t" \
  "ds_read_b32 v102, v101\n\t" \
  "s_add_u32 %[t1], %[c], 3\n\t" \
  "s_waitcnt lgkmcnt(0)\n\t" \
  "v_readfirstlane_b32 %[t0], v102\n\t" \
  "s_nop 0\n\t" \
  "s_min_u32 %[t1], %[t1], %[t0]\n\t" \
  "QL_%=:\n\t" \
  "s_cmp_ge_u32 %[iss], %[t1]\n\t" \
  "s_cbranch_scc1 QD_%=\n\t" \
  "s_bitcmp1_b32 %[iss], 1\n\t" \
  "s_cbranch_scc1 QS2_%=\n\t" \
  "s_bitcmp1_b32 %[iss], 0\n\t" \
  "s_cbranch_scc1 QS1_%=\n\t" \
  "global_load_dwordx4 v[0:3], v100, %[ob] offset:0 sc1\n\t" \
  "global_load_dwordx4 v[4:7], v100, %[ob] offset:1024 sc1\n\t" \
  "global_load_dwordx4 v[8:11], v100, %[ob] offset:2048 sc1\n\t" \
  "global_load_dwordx4 v[12:15], v100, %[ob] offset:3072 sc1\n\t" \
  "v_add_u32 v100, 0x4000, v100\n\t" \
  "v_and_b32 v100, 0x3ffff, v100\n\t" \
  "s_add_u32 %[iss], %[iss], 1\n\t" \
  "s_cmp_ge_u32 %[iss], %[t1]\n\t" \
  "s_cbranch_scc1 QD_%=\n\t" \
  "QS1_%=:\n\t" \
  "global_load_dwordx4 v[16:19], v100, %[ob] offset:0 sc1\n\t" \
  "global_load_dwordx4 v[20:23], v100, %[ob] offset:1024 sc1\n\t" \
  "global_load_dwordx4 v[24:27], v100, %[ob] offset:2048 sc1\n\t" \
  "global_load_dwordx4 v[28:31], v100, %[ob] offset:3072 sc1\n\t" \
  "v_add_u32 v100, 0x4000, v100\n\t" \
  "v_and_b32 v100, 0x3ffff, v100\n\t" \
  "s_add_u32 %[iss], %[iss], 1\n\t" \
  "s_cmp_ge_u32 %[iss], %[t1]\n\t" \
  "s_cbranch_scc1 QD_%=\n\t" \
  "QS2_%=:\n\t" \
  "s_bitcmp1_b32 %[iss], 0\n\t" \
  "s_cbranch_scc1 QS3_%=\n\t" \
  "global_load_dwordx4 v[32:35], v100, %[ob] offset:0 sc1\n\t" \
  "global_load_dwordx4 v[36:39], v100, %[ob] offset:1024 sc1\n\t" \
  "global_load_dwordx4 v[40:43], v100, %[ob] offset:2048 sc1\n\t" \
  "global_load_dwordx4 v[44:47], v100, %[ob] offset:3072 sc1\n\t" \
  "v_add_u32 v100, 0x4000, v100\n\t" \
  "v_and_b32 v100, 0x3ffff, v100\n\t" \
  "s_add_u32 %[iss], %[iss], 1\n\t" \
  "s_cmp_ge_u32 %[iss], %[t1]\n\t" \
  "s_cbranch_scc1 QD_%=\n\t" \
  "QS3_%=:\n\t" \
  "global_load_dwordx4 v[48:51], v100, %[ob] offset:0 sc1\n\t" \
  "global_load_dwordx4 v[52:55], v100, %[ob] offset:1024 sc1\n\t" \
  "global_load_dwordx4 v[56:59], v100, %[ob] offset:2048 sc1\n\t" \
  "global_load_dwordx4 v[60:63], v100, %[ob] offset:3072 sc1\n\t" \
  "v_add_u32 v100, 0x4000, v100\n\t" \
  "v_and_b32 v100, 0x3ffff, v100\n\t" \
  "s_add_u32 %[iss], %[iss], 1\n\t" \
  "s_branch QL_%=\n\t" \
  "QD_%=:\n\t" \
  "s_cmp_gt_u32 %[iss], %[c]\n\t" \
  "s_cbranch_scc1 QW_%=\n\t" \
  "s_addk_i32 %[cn], 1\n\t" \
  "s_cmp_gt_u32 %[cn], 0x400000\n\t" \
  "s_cbranch_scc1 QT_%=\n\t" \
  "s_sleep 1\n\t" \
  "s_branch QU_%=\n\t" \
  "QT_%=:\n\t" \
  "s_add_u32 %[t1], %[c], 1\n\t" \
  "s_branch QL_%=\n\t" \
  "QW_%=:\n\t" \
  "s_cmp_eq_u32 %[iss], 16\n\t" \
  "s_cselect_b64 vcc, -1, 0\n\t" \
  "s_sub_u32 %[t0], %[iss], %[c]\n\t" \
  "s_cmp_eq_u32 %[t0], 3\n\t" \
  "s_cselect_b32 %[th], %[iss], 99\n\t" \
  "s_cbranch_scc1 QZ_%=\n\t" \
  "s_cmp_eq_u32 %[t0], 2\n\t" \
  "s_cbranch_scc1 QY_%=\n\t" \
  "s_waitcnt vmcnt(0)\n\t" \
  "s_setpc_b64 s[92:93]\n\t" \
  "QY_%=:\n\t" \
  "s_waitcnt vmcnt(4)\n\t" \
  "s_setpc_b64 s[92:93]\n\t" \
  "QZ_%=:\n\t" \
  "s_waitcnt vmcnt(8)\n\t" \
  "s_setpc_b64 s[92:93]\n\t" \
  "QX_%=:\n\t" \
  "s_nop 7\n\t"
#define G3_CLOB "v0", "v1", "v2", "v3", "v4", "v5", "v6", "v7", "v8", "v9", "v10", "v11", "v12", "v13", "v14", "v15", "v16", "v17", "v18", "v19", "v20", "v21", "v22", "v23", "v24", "v25", "v26", "v27", "v28", "v29", "v30", "v31", "v32", "v33", "v34", "v35", "v36", "v37", "v38", "v39", "v40", "v41", "v42", "v43", "v44", "v45", "v46", "v47", "v48", "v49", "v50", "v51", "v52", "v53", "v54", "v55", "v56", "v57", "v58", "v59", "v60", "v61", "v62", "v63", "v64", "v65", "v66", "v67", "v68", "v69", "v70", "v71", "v72", "v73", "v74", "v75", "v76", "v77", "v78", "v79", "v80", "v81", "v82", "v83", "v84", "v85", "v86", "v87", "v88", "v89", "v90", "v91", "v92", "v93", "v94", "v95", "v102", "s88", "s89", "s92", "s93"

#define TS(i)
#define TOUT(n)

#define SPIN(cond,slp) do{ int sp_=0; while(cond){ __builtin_amdgcn_s_sleep(slp); if(++sp_>(1<<22)) break; } }while(0)
__device__ __forceinline__ unsigned rd1(const char* p){ return __builtin_amdgcn_readfirstlane(*(volatile LDS_AS unsigned*)(LDS_AS char*)p); }
__device__ __forceinline__ unsigned lds_inc(const char* p, int l){ unsigned r=0; if(l==0) r=__hip_atomic_fetch_add((LDS_AS unsigned*)(LDS_AS char*)p, 1u, __ATOMIC_RELAXED, __HIP_MEMORY_SCOPE_WORKGROUP); return __builtin_amdgcn_readfirstlane(r); }
__device__ __forceinline__ void lds_add(unsigned adr){ asm volatile("s_mov_b64 exec, 1\n\tds_add_u32 %0, %1\n\ts_mov_b64 exec, -1" :: "v"(adr), "v"(1u) : "memory"); }
__device__ __forceinline__ void bars(int n){
  #pragma unroll 1
  for(int i=0;i<n;i++) SBAR();
}
__device__ __forceinline__ void stout(void* sbase, unsigned voff, u32x4 v){
  st16cs(sbase,voff,v);
}

__global__ __launch_bounds__(768,3) void fused_kernel(const u16* __restrict__ x, const u16* __restrict__ wqkv,
    const u16* __restrict__ kin, const u16* __restrict__ vin, const u16* __restrict__ shk,
    u16* o, u16* kvT, const u16* __restrict__ hk, const u16* __restrict__ wo, const u16* __restrict__ qin,
    const u16* __restrict__ sD, u16* yT, u16* __restrict__ out, unsigned* flags)
{
  __shared__ __attribute__((aligned(16))) char smem[ALL_LDS];
  TS(0);
  const int w8=__builtin_amdgcn_readfirstlane(threadIdx.x>>6);
  const int w=w8&3;
  const int bid=blockIdx.x, xcd=bid&7, idx=bid>>3;
  const int rb=(xcd>>1)*4+(idx&3), hp=(xcd&1)*8+(idx>>2);
  const int s0=rb*128;
  #define LANEVARS int l; asm volatile("v_mbcnt_lo_u32_b32 %0, -1, 0\n\tv_mbcnt_hi_u32_b32 %0, -1, %0" : "=v"(l)); const int g=l>>4, fr=l&15, tq=(l&15)>>2, tp=l&3; (void)g; (void)fr; (void)tq; (void)tp;
  const unsigned sb0=__builtin_amdgcn_readfirstlane(lds_addr(smem));
  const char* syn=smem+SYN;

  if(w8>=8){
    LANEVARS
    unsigned tagv; asm volatile("global_load_dword %0, %1, %2 sc1" : "=v"(tagv) : "v"(0u), "s"(flags) : "memory");
    __builtin_amdgcn_s_setprio(CPR);
    {
      constexpr int NP=TP1-TP0;
      const int q=w*64+l, q2=(3-w)*64+l;
      if(NP>0 && w*64<8*NP){ if(q<8*NP) touch((const char*)x+(size_t)bid*16384+TP0*128, (q/NP)*2048+(q%NP)*128, sb0+SINK+256); }
      if(NP>0 && (3-w)*64<12*NP){ if(q2<12*NP) touch((const char*)wqkv+(size_t)TP0*393216+bid*1536, (q2/12)*393216+(q2%12)*128, sb0+SINK+512); }
      if(TWO && w==0) touch((const char*)wo+(size_t)bid*8192, l*128, sb0+SINK+512);
    }
    char* cr=smem+C_OFF+w*CREG;
    const unsigned crs=sb0+C_OFF+w*CREG;
    const int sblk=bid>>3, td0=(bid&7)*128+w*32;
    const int d=bid*4+w;
    if(w==0 && l<16) ((unsigned*)(smem+SYN))[l]=0u;
    if(l<32) *(u32x4*)(cr+l*16)=u32x4{0,0,0,0};
    unsigned tag, hd, h0, dv;
    touch(shk+td0*4, l*4, crs+512);
    asm volatile("global_load_ushort %0, %1, %2" : "=v"(h0) : "v"(0u), "s"(hk+(size_t)d*S) : "memory");
    asm volatile("global_load_ushort %0, %1, %2" : "=v"(dv) : "v"(0u), "s"(sD+d) : "memory");
    const int kv0=KV0+(sblk&KVS);
    {
      const int dg=l&3, rg=l>>2;
      const int d0=td0+dg*8, r0=sblk*64+rg*4;
      WAITL();
      bars(kv0);
      u32x4 kk[7], vv[4], sh[4];
      {
        const unsigned ko=(unsigned)(r0*2048+d0*2);
        #pragma unroll
        for(int i=0;i<7;i++){ const unsigned of = (i<3 && r0==0)? ko+3*2048 : ko+i*2048; asm volatile("global_load_dwordx4 %0, %1, %2" : "=v"(kk[i]) : "v"(of), "s"((const char*)kin-3*2048) : "memory"); }
        #pragma unroll
        for(int i=0;i<4;i++) asm volatile("global_load_dwordx4 %0, %1, %2" : "=v"(vv[i]) : "v"(ko), "s"((const char*)vin+i*2048) : "memory");
      }
      bars(KVW);
      asm volatile("s_waitcnt vmcnt(0)" : "+v"(kk[0]), "+v"(kk[1]), "+v"(kk[2]), "+v"(kk[3]), "+v"(kk[4]), "+v"(kk[5]), "+v"(kk[6]), "+v"(vv[0]), "+v"(vv[1]), "+v"(vv[2]), "+v"(vv[3]),
                   "+v"(tagv), "+v"(h0), "+v"(dv) :: "memory");
      #pragma unroll
      for(int i=0;i<4;i++) sh[i]=*(const u32x4*)(cr+512+dg*64+i*16);
      TS(1);
      tag=__builtin_amdgcn_readfirstlane(tagv)+1u;
      if(w==0) PUB(sb0+(l==0? SYN : SINK+l*4), tag);
      { const float t=bflo(h0)+bflo(dv); hd=pack2(t,t); }
      if(sblk==0){
        #pragma unroll
        for(int i=0;i<3;i++) if(r0==0) kk[i]=u32x4{0,0,0,0};
      }
      u32x4 rs[4];
      #pragma unroll
      for(int e2=0;e2<4;e2++){
        float lo[4];
        #pragma unroll
        for(int hf=0;hf<2;hf++){
          const unsigned a01=sh[e2][2*hf], a23=sh[e2][2*hf+1];
          const float c0=bflo(a01), c1=bfhi(a01), c2=bflo(a23), c3=bfhi(a23);
          float kf[7], rr[4];
          #pragma unroll
          for(int i=0;i<7;i++) kf[i]= hf? bfhi(kk[i][e2]) : bflo(kk[i][e2]);
          #pragma unroll
          for(int i=0;i<4;i++) rr[i]=mul1(fma1(kf[i+3],c0,fma1(kf[i+2],c1,fma1(kf[i+1],c2,mul1(kf[i],c3)))), hf? bfhi(vv[i][e2]) : bflo(vv[i][e2]));
          if(hf==0){
            #pragma unroll
            for(int i=0;i<4;i++) lo[i]=rr[i];
            asm volatile("s_barrier" : "+v"(lo[0]), "+v"(lo[1]), "+v"(lo[2]), "+v"(lo[3]), "+v"(kk[0]), "+v"(kk[1]), "+v"(kk[2]), "+v"(kk[3]), "+v"(kk[4]), "+v"(kk[5]), "+v"(kk[6]), "+v"(vv[0]), "+v"(vv[1]), "+v"(vv[2]), "+v"(vv[3]), "+v"(sh[e2]) :: "memory");
          } else {
            unsigned pk[4];
            #pragma unroll
            for(int i=0;i<4;i++) pk[i]=pack2(lo[i],rr[i]);
            if(e2<3) asm volatile("s_barrier" : "+v"(pk[0]), "+v"(pk[1]), "+v"(pk[2]), "+v"(pk[3]), "+v"(kk[0]), "+v"(kk[1]), "+v"(kk[2]), "+v"(kk[3]), "+v"(kk[4]), "+v"(kk[5]), "+v"(kk[6]), "+v"(vv[0]), "+v"(vv[1]), "+v"(vv[2]), "+v"(vv[3]), "+v"(sh[e2<3?e2+1:3]) :: "memory");
            #pragma unroll
            for(int i=0;i<4;i++) rs[i][e2]=pk[i];
          }
        }
      }
      #pragma unroll
      for(int i=0;i<4;i++) *(u32x4*)(cr+512+(i*16+rg)*64+dg*16)=rs[i];
      SBAR(); SBAR();
      u32x4 tv[4];
      #pragma unroll
      for(int i=0;i<4;i++){
        const int dh=i>>1, c8=(i&1)*4+g; const int rho=((c8>>1)-(sblk>>1))&3; const int R=rho*16+(c8&1)*8+tq;
        tv[i]=__builtin_bit_cast(u32x4,cat44(trread(cr+512+R*64+dh*32+tp*8), trread(cr+512+(R+4)*64+dh*32+tp*8)));
      }
      char* kp=(char*)kvT+(size_t)td0*4096+sblk*256;
      const unsigned ko=(unsigned)((fr>>1)*8192+(fr&1)*64+g*16);
      st16cs(kp,ko,tv[0]); st16cs(kp+128,ko,tv[1]); st16cs(kp+65536,ko,tv[2]); st16cs(kp+65536+128,ko,tv[3]);
      SBAR(); SBAR(); SBAR();
      WAITV(0);
      if(l==0) stflag(flags+F_PP+(td0>>5)*32+sblk, tag);
    }
    TS(2);
    bars(28-(KVW+12)-kv0);
    const unsigned* pf=flags+F_PP+(d>>5)*32;
    unsigned pv; asm volatile("global_load_dword %0, %1, %2 sc1" : "=v"(pv) : "v"((unsigned)((l&31)*4)), "s"(pf) : "memory");
    SBAR();
    #pragma unroll
    for(int i=0;i<4;i++) glds(hk+(size_t)d*S+i*512, l*16, sb0+X_OFF+w*4096+i*1024);
    SBAR();
    asm volatile("s_waitcnt vmcnt(4)" : "+v"(pv) :: "memory");
    const bool early=__all(pv==tag);
    if(early){
      #pragma unroll
      for(int i=0;i<4;i++) gldsc((const char*)kvT+(size_t)(d>>1)*8192+(d&1)*64+i*2048, (l>>2)*128+(l&3)*16, crs+512+i*1024);
    }
    SBAR(); SBAR(); SBAR();
    if(!early){
      wait_flag<8>(pf+(l&31), tag);
      #pragma unroll
      for(int i=0;i<4;i++) gldsc((const char*)kvT+(size_t)(d>>1)*8192+(d&1)*64+i*2048, (l>>2)*128+(l&3)*16, crs+512+i*1024);
    }
    __builtin_amdgcn_s_setprio(CVP);
    const int fd0=(bid&15)*64, fs0=(bid>>4)*128+w*32;
    u32x4 qv[4];
    {
      const unsigned qo=(unsigned)((l>>3)*2048+(l&7)*16); const char* qp=(const char*)(qin+(size_t)fs0*DM+fd0);
      asm volatile("global_load_dwordx4 %0, %4, %5\n\tglobal_load_dwordx4 %1, %4, %6\n\tglobal_load_dwordx4 %2, %4, %7\n\tglobal_load_dwordx4 %3, %4, %8\n\ts_waitcnt vmcnt(8)"
        : "=&{v[44:47]}"(qv[0]), "=&{v[48:51]}"(qv[1]), "=&{v[52:55]}"(qv[2]), "=&{v[56:59]}"(qv[3]) : "v"(qo), "s"(qp), "s"(qp+8*2048), "s"(qp+16*2048), "s"(qp+24*2048) : "memory");
    }
    f32x16 A0,A1,A2,A3;
    {
      const unsigned ha=sb0+X_OFF+w*4096+(32*g-4*tq+4*tp)*2;
      #define HR(L) "ds_read_b64_tr_b16 v[" #L "*4+64:" #L "*4+65], %4 offset:" #L "*256+32\n\tds_read_b64_tr_b16 v[" #L "*4+66:" #L "*4+67], %4 offset:" #L "*256\n\t"
      asm volatile(HR(0) HR(1) HR(2) HR(3) HR(4) HR(5) HR(6) HR(7) HR(8) HR(9) HR(10) HR(11) HR(12) HR(13) HR(14) HR(15) "s_waitcnt lgkmcnt(0)"
        : "=&{v[64:79]}"(A0), "=&{v[80:95]}"(A1), "=&{v[96:111]}"(A2), "=&{v[112:127]}"(A3) : "v"(ha) : "memory");
      lds_add(sb0+SYN+8);
      const unsigned k2=(l==0? 0u:0xffffu)|(l<=4? 0u:0xffff0000u), s2=(l==0? 0xffffu:0u)|(l==4? 0xffff0000u:0u);
      const unsigned k3=(l<=8? 0u:0xffffu)|(l<=12? 0u:0xffff0000u), s3=(l==8? 0xffffu:0u)|(l==12? 0xffff0000u:0u);
      A0[2]=__uint_as_float((__float_as_uint(A0[2])&k2)|(hd&s2));
      A0[3]=__uint_as_float((__float_as_uint(A0[3])&k3)|(hd&s3));
    }
    WAITV(4);
    TS(3);
    f32x16 ac0, ac1;
    {
      unsigned Rb[4][2];
      #pragma unroll
      for(int r=0;r<4;r++)
        #pragma unroll
        for(int k=0;k<2;k++){ const int q=fr-1-2*g+k; Rb[r][k]=crs+256+(q>>2)*128+((r+(q>>3))&3)*32+(q&3)*8; }
      const int bidx=((l>=16)? l-16 : (l==0? 63 : l+47))*4;
      unsigned p0,p1,p2,p3,p4,p5,p6;
      asm volatile("s_nop 1\n\t" CV_ASM
        : "=&{v[128:143]}"(ac0), "=&{v[144:159]}"(ac1), [p0]"=&s"(p0), [p1]"=&s"(p1), [p2]"=&s"(p2), [p3]"=&s"(p3), [p4]"=&s"(p4), [p5]"=&s"(p5), [p6]"=&s"(p6),
          "+{v[44:47]}"(qv[0]), "+{v[48:51]}"(qv[1]), "+{v[52:55]}"(qv[2]), "+{v[56:59]}"(qv[3])
        : "{v[64:79]}"(A0), "{v[80:95]}"(A1), "{v[96:111]}"(A2), "{v[112:127]}"(A3),
          "{v24}"(Rb[0][0]), "{v25}"(Rb[0][1]), "{v26}"(Rb[1][0]), "{v27}"(Rb[1][1]), "{v28}"(Rb[2][0]), "{v29}"(Rb[2][1]), "{v30}"(Rb[3][0]), "{v31}"(Rb[3][1]), "{v32}"(bidx)
        : "memory", CV_CLOB);
    }
    TS(4);
    __builtin_amdgcn_s_setprio(3);
    {
      const unsigned yo=(unsigned)((256*(g&1)+16*fr+4*(g&2))*2);
      #pragma unroll
      for(int J=0;J<8;J+=2){
        #define YA(i) ((i)<16? ac0[(i)&15] : ac1[(i)&15])
        const u32x2 a={pack2(YA(4*J),YA(4*J+1)),pack2(YA(4*J+2),YA(4*J+3))}, b={pack2(YA(4*J+4),YA(4*J+5)),pack2(YA(4*J+6),YA(4*J+7))};
        st16cs(yT+(size_t)d*S+256*J, yo, widen(a,b));
      }
    }
    asm volatile("s_waitcnt vmcnt(0)" : "+{v[44:47]}"(qv[0]), "+{v[48:51]}"(qv[1]), "+{v[52:55]}"(qv[2]), "+{v[56:59]}"(qv[3]) :: "memory");
    if(lds_inc(syn+24,l)==3u){ if(l<16) stflag(flags+F_Y+(l*16+(bid>>4))*16+(bid&15), tag); }
    TS(5);
    if(w==0){ wait_flag<YSLP>(flags+F_Y+bid*16+(l&15), tag); PUB(sb0+(l==0? SYN+28 : SINK+l*4), 1u); }
    else SPIN(rd1(syn+28)==0u,4);
    #pragma unroll
    for(int i=0;i<4;i++) gldsc(yT+(size_t)(fd0+i*16)*S+fs0, (l>>2)*4096+(l&3)*16, crs+i*1024);
    WAITV(0);
    {
      u32x4 y2[4];
      #pragma unroll
      for(int i=0;i<4;i++){ const char* yp=cr+((i>>1)*32+g*8+tq)*64+(i&1)*32+tp*8; y2[i]=__builtin_bit_cast(u32x4,cat44(trread(yp),trread(yp+256))); }
      asm volatile("" : "+v"(y2[0]), "+v"(y2[1]), "+v"(y2[2]), "+v"(y2[3]) :: "memory");
      #pragma unroll
      for(int i=0;i<4;i++) *(u32x4*)(cr+((i&1)*16+fr)*128+((((i>>1)*4+g)^(fr&7))*16))=y2[i];
      asm volatile("" ::: "memory");
      char* op=(char*)(out+(size_t)fs0*2048+fd0); const unsigned oo=(unsigned)((l>>3)*4096+(l&7)*16);
      #pragma unroll
      for(int i=0;i<4;i++){
        const u32x4 y4=*(const u32x4*)(cr+(i*8+(l>>3))*128+(((l&7)^(l>>3))*16)), q4=qv[i]; u32x4 ov;
        #pragma unroll
        for(int e=0;e<4;e++) ov[e]=pack2(bflo(q4[e])*bflo(y4[e]), bfhi(q4[e])*bfhi(y4[e]));
        stout(op+i*32768, oo, ov);
      }
    }
    TS(6); TOUT(7);
    return;
  }

  if(w8>=4){
    LANEVARS
    #define LBAR() SBAR()
    __builtin_amdgcn_s_setprio(LPR);
    {
      const int xr=18*w+(l>>3);
      const unsigned ch=(unsigned)(((l&7)^(xr&7))*16);
      const unsigned xo=(unsigned)(xr*2048)+ch;
      const unsigned xoA=(s0==0 && xr<16)? 16*2048+ch : xo, xoB=(s0==0 && xr+8<16)? 8*2048+ch : xo;
      const int wr=8*w+(l>>3);
      const unsigned wof=(unsigned)(wr*6144+((((l&7)>>1)^wswz(wr))*32)+(l&1)*16);
      const char* xb=(const char*)x+(ptrdiff_t)(s0-16)*2048;
      const char* wb=(const char*)(wqkv+hp*64);
      const int hot0=(3-(int)((size_t)wqkv>>7))&7;
      const int jj=(ROT && (hp&7)==hot0)? (rb&3) : 0;
      auto kp=[&](int s)->int{ return (s&12)+((s+jj)&3); };
      auto issx=[&](int p, int hf){
        const unsigned dst=sb0+X_OFF+((p+2)%3)*XS+(18*w+72*hf)*128; const char* xs=xb+kp(p)*128+(size_t)hf*72*2048;
        glds(xs, hf? xo:xoA, dst); glds(xs+8*2048, hf? xo:xoB, dst+1024);
        asm volatile("s_mov_b32 m0, %2\n\ts_mov_b64 exec, 0xffff\n\tglobal_load_lds_dwordx4 %0, %1\n\ts_mov_b64 exec, -1" :: "v"(xo), "s"(xs+16*2048), "s"(dst+2048) : "memory");
      };
      auto issw=[&](int h){
        const unsigned dst=sb0+((h+4)%6)*WS+w*1024; const char* ws=wb+(size_t)(kp(h>>1)*2+(h&1))*32*6144;
        #pragma unroll
        for(int j=0;j<3;j++) glds(ws+j*2048, wof, dst+j*4096);
      };
      issw(0); issx(0,0); issx(0,1); issw(1); issx(1,0); issx(1,1); issw(2); issw(3);
      WAITV(15); LBAR();
      issx(2,0); issx(2,1);
      WAITV(18); LBAR();
      issw(4); issw(5);
      #pragma unroll 1
      for(int p=3;p<16;p++){
        WAITV(15); LBAR();
        issx(p,0); issw(2*p);
        WAITV(18); LBAR();
        issx(p,1); issw(2*p+1);
      }
    }
    TS(1);
    const unsigned wof2=(unsigned)((l>>3)*2048+((((l&7)>>1)^((l>>4)&3))*32)+(l&1)*16);
    const char* wob=(const char*)(wo+hp*64);
    auto isso=[&](int a, int n){
      #pragma unroll 1
      for(int P=a+w;P<a+n;P+=4) glds(wob+(size_t)((P+hp*8)&127)*16384, wof2, sb0+P*1024);
    };
    WAITV(15); LBAR();
    isso(0,12);
    WAITV(15); LBAR();
    isso(12,12);
    WAITV(9); LBAR();
    isso(24,12); isso(90,16);
    WAITV(13); LBAR();
    isso(36,12);
    LBAR();
    isso(48,12); isso(106,22); isso(88,2);
    SPIN(!__all(*(volatile LDS_AS unsigned*)(LDS_AS char*)(syn+32+(l&3)*4)!=0u),1);
    isso(60,12);
    SPIN(rd1(syn+8)<4u,1);
    isso(72,16);
    WAITV(0);
    lds_add(sb0+SYN+12);
    TS(2);
    if(w==3){
      const unsigned tag=rd1(syn);
      const int hs=(hp+l)&15;
      const unsigned* fp=flags+F_O+(rb*16+hp)*16+hs;
      const unsigned madr=sb0+(l==0? SYN+20 : SINK+l*4);
      const unsigned tof=(unsigned)((hp&7)*2048+(l&15)*128);
      SPIN(rd1(syn+12)<4u,1);
      unsigned rdy=0; int spins=0;
      while(rdy!=0xFFFFu){
        unsigned v=ldflag(fp); unsigned r=(unsigned)__ballot(v==tag)&0xFFFFu;
        if(++spins>(1<<21)) r=0xFFFFu;
        if(r!=rdy){
          PUB(madr,(unsigned)__builtin_ctz(~r));
          const unsigned nw=r&~rdy; rdy=r;
        } else __builtin_amdgcn_s_sleep(OSLP);
      }
      TS(3);
      WAITV(0);
      if(bid==0){
        int sp=0;
        while(true){
          u32x4 v; asm volatile("global_load_dwordx4 %0, %1, %2 sc1\n\ts_waitcnt vmcnt(0)" : "=&v"(v) : "v"(l*16), "s"(flags+F_Y) : "memory");
          if(__all(v[0]==tag && v[1]==tag && v[2]==tag && v[3]==tag) || ++sp>(1<<20)) break;
          __builtin_amdgcn_s_sleep(12);
        }
        if(l==0) stflag(flags, tag);
      }
    } else { TS(3); }
    TOUT(4);
    return;
  }

  __builtin_amdgcn_s_setprio(GPR);
  const int h=w&1, r=w>>1;
  {
    LANEVARS
    unsigned lx0=sb0+X_OFF+fr*128+((g^(fr&7))*16), lx1=sb0+X_OFF+fr*128+(((4+g)^(fr&7))*16);
    const int sw=wswz(8*g+tq);
    unsigned lw0=sb0+(8*g+tq)*128+(unsigned)((((2*h)^sw)*32)+8*tp), lw1=sb0+(8*g+tq)*128+(unsigned)((((2*h+1)^sw)*32)+8*tp);
    char* ot=(char*)o+(size_t)(rb*16+hp)*16384+r*8192+h*2048;
    unsigned it; unsigned long long rm; asm("s_cmp_lg_u32 %1, 0\n\ts_cselect_b64 %0, -1, 0" : "=s"(rm) : "s"(r) : "scc");
    asm volatile(N2_ASM
      : [it]"=&s"(it), "+{v56}"(lx0), "+{v57}"(lx1), "+{v58}"(lw0), "+{v59}"(lw1)
      : "{v62}"(l*16), "{v63}"(sb0+EX_OFF+h*3072+l*16), [r]"s"(r), [ot]"s"(ot), [ou]"s"(ot+4096), [fa]"s"(sb0+SYN+32+w*4), [fp]"s"(sb0+SYN+32+(w^2)*4),
        [vo]"s"(2048-1024*r), [rm]"s"(rm), [z]"s"(rb|r)
      : "memory", "scc", "vcc", N2_CLOB);
  }
  TS(3);
  LANEVARS
  {
    const unsigned tag=rd1(syn);
    WAITV(0);
    if(lds_inc(syn+16,l)==3u){ if(l<16) stflag(flags+F_O+(rb*16+l)*16+hp, tag); }
  }
  TS(4);
  __builtin_amdgcn_s_setprio(G2P);
  {
    f32x16 e0, e1; unsigned trc=0;
    {
      const unsigned ra=sb0+(4*g+tq)*128+8*tp; const int sw=(2*g+(tq>>1))&3;
      unsigned a0=ra+((0^sw)*32), a1=ra+((1^sw)*32), a2=ra+((2^sw)*32), a3=ra+((3^sw)*32), vo=(unsigned)(hp*16384+w*4096+l*16);
      unsigned c=0, iss=0, th, q0, q1, cn=0;
      asm volatile(G3_ASM
        : "={v[128:143]}"(e0), "={v[144:159]}"(e1), [c]"+s"(c), [iss]"+s"(iss), [th]"=&s"(th), [t0]"=&s"(q0), [t1]"=&s"(q1), [cn]"+s"(cn),
          "+{v96}"(a0), "+{v97}"(a1), "+{v98}"(a2), "+{v99}"(a3), "+{v100}"(vo)
        : "{v101}"(sb0+SYN+20), [ob]"s"((const char*)o+(size_t)rb*16*16384)
        : "memory", "scc", "vcc", G3_CLOB);
    }
    char* sg=smem+EX_OFF+w*2048;
    char* op=(char*)(out+(size_t)(s0+w*32)*2048+1024+hp*64); const unsigned oo=(unsigned)((l>>3)*4096+(l&7)*16);
    u32x4 ov[4];
    #pragma unroll
    for(int t=0;t<2;t++){
      #pragma unroll
      for(int j=0;j<4;j++){
        #define E2(i) (t? e1[(i)&15] : e0[(i)&15])
        *(volatile LDS_AS u32x2*)(LDS_AS char*)(sg+fr*128+(((2*j+(g>>1))^(fr&7))*16)+(g&1)*8)=u32x2{pack2(E2(4*j),E2(4*j+1)),pack2(E2(4*j+2),E2(4*j+3))};
      }
      #pragma unroll
      for(int i=0;i<2;i++) ov[2*t+i]=*(volatile LDS_AS u32x4*)(LDS_AS char*)(sg+(i*8+(l>>3))*128+(((l&7)^(l>>3))*16));
    }
    #pragma unroll
    for(int i=0;i<4;i++) stout(op+i*32768, oo, ov[i]);
    TS(6); TOUT(7);
  }
}

extern "C" void kernel_launch(void* const* d_in, const int* in_sizes, int n_in, void* d_out, int out_size, void* d_ws, size_t ws_size, hipStream_t stream){
  const u16* q=(const u16*)d_in[0]; const u16* k=(const u16*)d_in[1]; const u16* v=(const u16*)d_in[2];
  const u16* shk=(const u16*)d_in[3]; const u16* hk=(const u16*)d_in[4]; const u16* sD=(const u16*)d_in[5];
  const u16* x=(const u16*)d_in[6]; const u16* wqkv=(const u16*)d_in[7]; const u16* wo=(const u16*)d_in[8];
  u16* out=(u16*)d_out; u16* ws=(u16*)d_ws;
  u16* o=ws; u16* kvT=ws+(size_t)S*DM; u16* yT=kvT+(size_t)S*DM;
  unsigned* cnt=(unsigned*)(ws+(size_t)6*S*DM);
  fused_kernel<<<256,768,0,stream>>>(x,wqkv,k,v,shk,o,kvT,hk,wo,q,sD,yT,out,cnt);
}
```

```cpp
#include <hip/hip_runtime.h>
#include <hip/hip_bf16.h>
#include <stdint.h>

typedef unsigned short u16;
typedef short bf16x8 __attribute__((ext_vector_type(8)));
typedef short s16x4 __attribute__((ext_vector_type(4)));
typedef float f32x2 __attribute__((ext_vector_type(2)));
typedef float f32x4 __attribute__((ext_vector_type(4)));
typedef float f32x8 __attribute__((ext_vector_type(8)));
typedef float f32x16 __attribute__((ext_vector_type(16)));
typedef unsigned u32x4 __attribute__((ext_vector_type(4)));
typedef unsigned u32x2 __attribute__((ext_vector_type(2)));
typedef __bf16 bf16x2_t __attribute__((ext_vector_type(2)));
#define LDS_AS __attribute__((address_space(3)))

__device__ __forceinline__ float bflo(unsigned x){ return __uint_as_float(x<<16); }
__device__ __forceinline__ float bfhi(unsigned x){ return __uint_as_float(x&0xffff0000u); }
__device__ __forceinline__ unsigned pack2(float lo, float hi){ return __builtin_bit_cast(unsigned, __builtin_convertvector(f32x2{lo,hi}, bf16x2_t)); }
__device__ __forceinline__ float mul1(float a, float b){ float r; asm("v_mul_f32 %0, %1, %2" : "=v"(r) : "v"(a), "v"(b)); return r; }
__device__ __forceinline__ float fma1(float a, float b, float c){ float r; asm("v_fma_f32 %0, %1, %2, %3" : "=v"(r) : "v"(a), "v"(b), "v"(c)); return r; }

__device__ __forceinline__ void glds(const void* sbase, unsigned voff, unsigned ldsaddr){
  asm volatile("s_mov_b32 m0, %2\n\ts_nop 0\n\tglobal_load_lds_dwordx4 %0, %1" :: "v"(voff), "s"(sbase), "s"(ldsaddr) : "memory");
}
__device__ __forceinline__ void gldsc(const void* sbase, unsigned voff, unsigned ldsaddr){
  asm volatile("s_mov_b32 m0, %2\n\ts_nop 0\n\tglobal_load_lds_dwordx4 %0, %1 sc1" :: "v"(voff), "s"(sbase), "s"(ldsaddr) : "memory");
}
__device__ __forceinline__ void touch(const void* sbase, unsigned voff, unsigned ldsaddr){
  asm volatile("s_mov_b32 m0, %2\n\ts_nop 0\n\tglobal_load_lds_dword %0, %1" :: "v"(voff), "s"(sbase), "s"(ldsaddr) : "memory");
}
__device__ __forceinline__ void st16c(void* p, u32x4 v){ asm volatile("global_store_dwordx4 %0, %1, off sc1\n\ts_nop 1" :: "v"(p), "v"(v) : "memory"); }
__device__ __forceinline__ void st16cs(void* sbase, unsigned voff, u32x4 v){ asm volatile("global_store_dwordx4 %0, %1, %2 sc1\n\ts_nop 1" :: "v"(voff), "v"(v), "s"(sbase) : "memory"); }
__device__ __forceinline__ unsigned ldflag(const unsigned* p){ return __hip_atomic_load(p, __ATOMIC_RELAXED, __HIP_MEMORY_SCOPE_AGENT); }
__device__ __forceinline__ void stflag(unsigned* p, unsigned v){ __hip_atomic_store(p, v, __ATOMIC_RELAXED, __HIP_MEMORY_SCOPE_AGENT); }
template<int SLP> __device__ __forceinline__ void wait_flag(const unsigned* p, unsigned tag){
  int spins=0;
  while(true){ unsigned v=ldflag(p); if(__all(v==tag)) break; __builtin_amdgcn_s_sleep(SLP); if(++spins>(1<<21)) break; }
}
__device__ __forceinline__ unsigned lds_addr(const void* p){ return (unsigned)(size_t)(const LDS_AS void*)p; }
#define WAITV(n) asm volatile("s_waitcnt vmcnt(" #n ")":::"memory")
#define WAITL() asm volatile("s_waitcnt lgkmcnt(0)":::"memory")
#define SBAR() asm volatile("s_barrier":::"memory")
#define PUB(addr,val) asm volatile("ds_write_b32 %0, %1" :: "v"(addr), "v"(val) : "memory")
__device__ __forceinline__ s16x4 trread(const char* p){ return __builtin_amdgcn_ds_read_tr16_b64_v4i16((LDS_AS s16x4*)p); }
__device__ __forceinline__ bf16x8 cat44(s16x4 a, s16x4 b){ return __builtin_shufflevector(a,b,0,1,2,3,4,5,6,7); }
__device__ __forceinline__ int wswz(int row){ return ((row>>1)&1) | (((row>>3)&1)<<1); }
__device__ __forceinline__ u32x4 widen(u32x2 a, u32x2 b){
  auto r0=__builtin_amdgcn_permlane16_swap(a[0],b[0],false,false);
  auto r1=__builtin_amdgcn_permlane16_swap(a[1],b[1],false,false);
  return u32x4{r0[0],r1[0],r0[1],r1[1]};
}

constexpr int S=2048, DM=1024;
constexpr int WS=12288, XS=18432, X_OFF=6*WS;
constexpr int CREG=4608, C_OFF=131072;
constexpr int EX_OFF=C_OFF+4*CREG;
constexpr int SYN=EX_OFF+8192;
constexpr int SINK=SYN+256;
constexpr int ALL_LDS=SINK+768;
constexpr int F_PP=1024, F_O=4096, F_Y=8192;
#define KV0 1
#define KVW 3
#define KVS 3
#define LPR 3
#define CPR 2
#define GPR 1
#define WROT 4
#define LHOLD 0
#define HOTP 0
#define E2A 3
#define WLIM 4
#define HEARLY 1
#define QLATE 1
#define WTCH 0
#define CGATE 0
#define OSLP 2
#define YSLP 8
#define TP0 0
#define TP1 16
#define TWO 1
#define OPULL 0
#define CVP 2
#define G2P 1
#define OUTC 1
#define ROT 1

#define N2_ASM \
  "v_mov_b32 v64, 0\n\t" \
  "v_mov_b32 v65, 0\n\t" \
  "v_mov_b32 v66, 0\n\t" \
  "v_mov_b32 v67, 0\n\t" \
  "v_mov_b32 v68, 0\n\t" \
  "v_mov_b32 v69, 0\n\t" \
  "v_mov_b32 v70, 0\n\t" \
  "v_mov_b32 v71, 0\n\t" \
  "v_mov_b32 v72, 0\n\t" \
  "v_mov_b32 v73, 0\n\t" \
  "v_mov_b32 v74, 0\n\t" \
  "v_mov_b32 v75, 0\n\t" \
  "v_mov_b32 v76, 0\n\t" \
  "v_mov_b32 v77, 0\n\t" \
  "v_mov_b32 v78, 0\n\t" \
  "v_mov_b32 v79, 0\n\t" \
  "v_mov_b32 v80, 0\n\t" \
  "v_mov_b32 v81, 0\n\t" \
  "v_mov_b32 v82, 0\n\t" \
  "v_mov_b32 v83, 0\n\t" \
  "v_mov_b32 v84, 0\n\t" \
  "v_mov_b32 v85, 0\n\t" \
  "v_mov_b32 v86, 0\n\t" \
  "v_mov_b32 v87, 0\n\t" \
  "v_mov_b32 v88, 0\n\t" \
  "v_mov_b32 v89, 0\n\t" \
  "v_mov_b32 v90, 0\n\t" \
  "v_mov_b32 v91, 0\n\t" \
  "v_mov_b32 v92, 0\n\t" \
  "v_mov_b32 v93, 0\n\t" \
  "v_mov_b32 v94, 0\n\t" \
  "v_mov_b32 v95, 0\n\t" \
  "v_mov_b32 v96, 0\n\t" \
  "v_mov_b32 v97, 0\n\t" \
  "v_mov_b32 v98, 0\n\t" \
  "v_mov_b32 v99, 0\n\t" \
  "v_mov_b32 v100, 0\n\t" \
  "v_mov_b32 v101, 0\n\t" \
  "v_mov_b32 v102, 0\n\t" \
  "v_mov_b32 v103, 0\n\t" \
  "v_mov_b32 v104, 0\n\t" \
  "v_mov_b32 v105, 0\n\t" \
  "v_mov_b32 v106, 0\n\t" \
  "v_mov_b32 v107, 0\n\t" \
  "v_mov_b32 v108, 0\n\t" \
  "v_mov_b32 v109, 0\n\t" \
  "v_mov_b32 v110, 0\n\t" \
  "v_mov_b32 v111, 0\n\t" \
  "v_mov_b32 v112, 0\n\t" \
  "v_mov_b32 v113, 0\n\t" \
  "v_mov_b32 v114, 0\n\t" \
  "v_mov_b32 v115, 0\n\t" \
  "v_mov_b32 v116, 0\n\t" \
  "v_mov_b32 v117, 0\n\t" \
  "v_mov_b32 v118, 0\n\t" \
  "v_mov_b32 v119, 0\n\t" \
  "v_mov_b32 v120, 0\n\t" \
  "v_mov_b32 v121, 0\n\t" \
  "v_mov_b32 v122, 0\n\t" \
  "v_mov_b32 v123, 0\n\t" \
  "v_mov_b32 v124, 0\n\t" \
  "v_mov_b32 v125, 0\n\t" \
  "v_mov_b32 v126, 0\n\t" \
  "v_mov_b32 v127, 0\n\t" \
  "v_mov_b32 v128, 0\n\t" \
  "v_mov_b32 v129, 0\n\t" \
  "v_mov_b32 v130, 0\n\t" \
  "v_mov_b32 v131, 0\n\t" \
  "v_mov_b32 v132, 0\n\t" \
  "v_mov_b32 v133, 0\n\t" \
  "v_mov_b32 v134, 0\n\t" \
  "v_mov_b32 v135, 0\n\t" \
  "v_mov_b32 v136, 0\n\t" \
  "v_mov_b32 v137, 0\n\t" \
  "v_mov_b32 v138, 0\n\t" \
  "v_mov_b32 v139, 0\n\t" \
  "v_mov_b32 v140, 0\n\t" \
  "v_mov_b32 v141, 0\n\t" \
  "v_mov_b32 v142, 0\n\t" \
  "v_mov_b32 v143, 0\n\t" \
  "v_mov_b32 v144, 0\n\t" \
  "v_mov_b32 v145, 0\n\t" \
  "v_mov_b32 v146, 0\n\t" \
  "v_mov_b32 v147, 0\n\t" \
  "v_mov_b32 v148, 0\n\t" \
  "v_mov_b32 v149, 0\n\t" \
  "v_mov_b32 v150, 0\n\t" \
  "v_mov_b32 v151, 0\n\t" \
  "v_mov_b32 v152, 0\n\t" \
  "v_mov_b32 v153, 0\n\t" \
  "v_mov_b32 v154, 0\n\t" \
  "v_mov_b32 v155, 0\n\t" \
  "v_mov_b32 v156, 0\n\t" \
  "v_mov_b32 v157, 0\n\t" \
  "v_mov_b32 v158, 0\n\t" \
  "v_mov_b32 v159, 0\n\t" \
  "v_mov_b32 v160, 0\n\t" \
  "v_mov_b32 v161, 0\n\t" \
  "v_mov_b32 v162, 0\n\t" \
  "v_mov_b32 v163, 0\n\t" \
  "v_mov_b32 v164, 0\n\t" \
  "v_mov_b32 v165, 0\n\t" \
  "v_mov_b32 v166, 0\n\t" \
  "v_mov_b32 v167, 0\n\t" \
  "s_mov_b32 s68, 0x50000\n\t" \
  "s_mov_b32 s69, 0x5550055\n\t" \
  "s_mov_b32 s70, 0xa0000\n\t" \
  "s_mov_b32 s71, 0xaaa00aa\n\t" \
  "s_mov_b32 s72, 0x150001\n\t" \
  "s_mov_b32 s73, 0x15550155\n\t" \
  "s_mov_b32 s74, 0x2a0002\n\t" \
  "s_mov_b32 s75, 0x2aaa02aa\n\t" \
  "s_mov_b32 s76, 0x55505555\n\t" \
  "s_mov_b32 s77, 0x50005500\n\t" \
  "s_mov_b32 s78, 0xaaa0aaaa\n\t" \
  "s_mov_b32 s79, 0xa000aa00\n\t" \
  "s_mov_b32 s80, 0x55405554\n\t" \
  "s_mov_b32 s81, 0x40005400\n\t" \
  "s_mov_b32 s82, 0xaa80aaa8\n\t" \
  "s_mov_b32 s83, 0x8000a800\n\t" \
  "s_mov_b32 s84, 0xaaaaaaaa\n\t" \
  "s_mov_b32 s85, 0xaaaaaaaa\n\t" \
  "v_add_u32 v60, 36864, v58\n\t" \
  "v_add_u32 v61, 36864, v59\n\t" \
  "s_mov_b32 %[it], 0\n\t" \
  "s_cmp_lg_u32 %[r], 0\n\t" \
  "s_barrier\n\t" \
  "s_cbranch_scc1 NB_%=\n\t" \
  "ds_read_b64_tr_b16 v[40:41], v60 offset:16384\n\t" \
  "ds_read_b64_tr_b16 v[42:43], v60 offset:16896\n\t" \
  "ds_read_b64_tr_b16 v[44:45], v60 offset:20480\n\t" \
  "ds_read_b64_tr_b16 v[46:47], v60 offset:20992\n\t" \
  "ds_read_b64_tr_b16 v[48:49], v61 offset:16384\n\t" \
  "ds_read_b64_tr_b16 v[50:51], v61 offset:16896\n\t" \
  "ds_read_b128 v[0:3], v56 offset:36864\n\t" \
  "ds_read_b128 v[4:7], v56 offset:38912\n\t" \
  "ds_read_b128 v[8:11], v56 offset:40960\n\t" \
  "ds_read_b128 v[12:15], v56 offset:43008\n\t" \
  "ds_read_b128 v[16:19], v56 offset:45056\n\t" \
  "s_waitcnt lgkmcnt(0)\n\t" \
  "s_branch N4a_%=\n\t" \
  "N0a_%=:\n\t" \
  "s_waitcnt lgkmcnt(4)\n\t" \
  "v_mfma_f32_16x16x32_bf16 v[160:163], v[40:43], v[0:3], v[160:163]\n\t" \
  "ds_read_b64_tr_b16 v[52:53], v59 offset:8192\n\t" \
  "v_mfma_f32_16x16x32_bf16 v[96:99], v[40:43], v[4:7], v[96:99]\n\t" \
  "ds_read_b64_tr_b16 v[54:55], v59 offset:8704\n\t" \
  "v_mfma_f32_16x16x32_bf16 v[104:107], v[40:43], v[8:11], v[104:107]\n\t" \
  "s_barrier\n\t" \
  "v_mfma_f32_16x16x32_bf16 v[112:115], v[40:43], v[12:15], v[112:115]\n\t" \
  "v_mfma_f32_16x16x32_bf16 v[120:123], v[40:43], v[16:19], v[120:123]\n\t" \
  "s_waitcnt lgkmcnt(4)\n\t" \
  "v_mfma_f32_16x16x32_bf16 v[128:131], v[4:7], v[44:47], v[128:131]\n\t" \
  "ds_read_b64_tr_b16 v[40:41], v58 offset:0\n\t" \
  "v_mfma_f32_16x16x32_bf16 v[136:139], v[8:11], v[44:47], v[136:139]\n\t" \
  "ds_read_b64_tr_b16 v[42:43], v58 offset:512\n\t" \
  "v_mfma_f32_16x16x32_bf16 v[144:147], v[12:15], v[44:47], v[144:147]\n\t" \
  "ds_read_b128 v[20:23], v57 offset:0\n\t" \
  "v_mfma_f32_16x16x32_bf16 v[152:155], v[16:19], v[44:47], v[152:155]\n\t" \
  "ds_read_b128 v[24:27], v57 offset:2048\n\t" \
  "s_waitcnt lgkmcnt(6)\n\t" \
  "v_mfma_f32_16x16x32_bf16 v[164:167], v[48:51], v[0:3], v[164:167]\n\t" \
  "ds_read_b64_tr_b16 v[44:45], v59 offset:0\n\t" \
  "v_mfma_f32_16x16x32_bf16 v[100:103], v[48:51], v[4:7], v[100:103]\n\t" \
  "ds_read_b64_tr_b16 v[46:47], v59 offset:512\n\t" \
  "v_mfma_f32_16x16x32_bf16 v[108:111], v[48:51], v[8:11], v[108:111]\n\t" \
  "ds_read_b128 v[28:31], v57 offset:4096\n\t" \
  "v_mfma_f32_16x16x32_bf16 v[116:119], v[48:51], v[12:15], v[116:119]\n\t" \
  "ds_read_b128 v[32:35], v57 offset:6144\n\t" \
  "v_mfma_f32_16x16x32_bf16 v[124:127], v[48:51], v[16:19], v[124:127]\n\t" \
  "s_waitcnt lgkmcnt(8)\n\t" \
  "v_mfma_f32_16x16x32_bf16 v[132:135], v[4:7], v[52:55], v[132:135]\n\t" \
  "ds_read_b64_tr_b16 v[48:49], v58 offset:16384\n\t" \
  "v_mfma_f32_16x16x32_bf16 v[140:143], v[8:11], v[52:55], v[140:143]\n\t" \
  "ds_read_b64_tr_b16 v[50:51], v58 offset:16896\n\t" \
  "v_mfma_f32_16x16x32_bf16 v[148:151], v[12:15], v[52:55], v[148:151]\n\t" \
  "ds_read_b128 v[36:39], v57 offset:8192\n\t" \
  "v_mfma_f32_16x16x32_bf16 v[156:159], v[16:19], v[52:55], v[156:159]\n\t" \
  "s_waitcnt lgkmcnt(9)\n\t" \
  "v_mfma_f32_16x16x32_bf16 v[64:67], v[40:43], v[4:7], v[64:67]\n\t" \
  "ds_read_b64_tr_b16 v[52:53], v58 offset:20480\n\t" \
  "v_mfma_f32_16x16x32_bf16 v[72:75], v[40:43], v[8:11], v[72:75]\n\t" \
  "ds_read_b64_tr_b16 v[54:55], v58 offset:20992\n\t" \
  "v_mfma_f32_16x16x32_bf16 v[80:83], v[40:43], v[12:15], v[80:83]\n\t" \
  "v_mfma_f32_16x16x32_bf16 v[88:91], v[40:43], v[16:19], v[88:91]\n\t" \
  "s_waitcnt lgkmcnt(7)\n\t" \
  "v_mfma_f32_16x16x32_bf16 v[68:71], v[44:47], v[4:7], v[68:71]\n\t" \
  "ds_read_b64_tr_b16 v[40:41], v59 offset:16384\n\t" \
  "v_mfma_f32_16x16x32_bf16 v[76:79], v[44:47], v[8:11], v[76:79]\n\t" \
  "ds_read_b64_tr_b16 v[42:43], v59 offset:16896\n\t" \
  "v_mfma_f32_16x16x32_bf16 v[84:87], v[44:47], v[12:15], v[84:87]\n\t" \
  "v_mfma_f32_16x16x32_bf16 v[92:95], v[44:47], v[16:19], v[92:95]\n\t" \
  "N1a_%=:\n\t" \
  "s_waitcnt lgkmcnt(4)\n\t" \
  "v_mfma_f32_16x16x32_bf16 v[160:163], v[48:51], v[20:23], v[160:163]\n\t" \
  "ds_read_b64_tr_b16 v[44:45], v59 offset:20480\n\t" \
  "v_mfma_f32_16x16x32_bf16 v[96:99], v[48:51], v[24:27], v[96:99]\n\t" \
  "ds_read_b64_tr_b16 v[46:47], v59 offset:20992\n\t" \
  "v_mfma_f32_16x16x32_bf16 v[104:107], v[48:51], v[28:31], v[104:107]\n\t" \
  "s_barrier\n\t" \
  "v_mfma_f32_16x16x32_bf16 v[112:115], v[48:51], v[32:35], v[112:115]\n\t" \
  "v_mfma_f32_16x16x32_bf16 v[120:123], v[48:51], v[36:39], v[120:123]\n\t" \
  "s_waitcnt lgkmcnt(4)\n\t" \
  "v_mfma_f32_16x16x32_bf16 v[128:131], v[24:27], v[52:55], v[128:131]\n\t" \
  "ds_read_b64_tr_b16 v[48:49], v58 offset:12288\n\t" \
  "v_mfma_f32_16x16x32_bf16 v[136:139], v[28:31], v[52:55], v[136:139]\n\t" \
  "ds_read_b64_tr_b16 v[50:51], v58 offset:12800\n\t" \
  "v_mfma_f32_16x16x32_bf16 v[144:147], v[32:35], v[52:55], v[144:147]\n\t" \
  "ds_read_b128 v[0:3], v56 offset:18432\n\t" \
  "v_mfma_f32_16x16x32_bf16 v[152:155], v[36:39], v[52:55], v[152:155]\n\t" \
  "ds_read_b128 v[4:7], v56 offset:20480\n\t" \
  "s_waitcnt lgkmcnt(6)\n\t" \
  "v_mfma_f32_16x16x32_bf16 v[164:167], v[40:43], v[20:23], v[164:167]\n\t" \
  "ds_read_b64_tr_b16 v[52:53], v59 offset:12288\n\t" \
  "v_mfma_f32_16x16x32_bf16 v[100:103], v[40:43], v[24:27], v[100:103]\n\t" \
  "ds_read_b64_tr_b16 v[54:55], v59 offset:12800\n\t" \
  "v_mfma_f32_16x16x32_bf16 v[108:111], v[40:43], v[28:31], v[108:111]\n\t" \
  "ds_read_b128 v[8:11], v56 offset:22528\n\t" \
  "v_mfma_f32_16x16x32_bf16 v[116:119], v[40:43], v[32:35], v[116:119]\n\t" \
  "ds_read_b128 v[12:15], v56 offset:24576\n\t" \
  "v_mfma_f32_16x16x32_bf16 v[124:127], v[40:43], v[36:39], v[124:127]\n\t" \
  "s_waitcnt lgkmcnt(8)\n\t" \
  "v_mfma_f32_16x16x32_bf16 v[132:135], v[24:27], v[44:47], v[132:135]\n\t" \
  "ds_read_b64_tr_b16 v[40:41], v58 offset:28672\n\t" \
  "v_mfma_f32_16x16x32_bf16 v[140:143], v[28:31], v[44:47], v[140:143]\n\t" \
  "ds_read_b64_tr_b16 v[42:43], v58 offset:29184\n\t" \
  "v_mfma_f32_16x16x32_bf16 v[148:151], v[32:35], v[44:47], v[148:151]\n\t" \
  "ds_read_b128 v[16:19], v56 offset:26624\n\t" \
  "v_mfma_f32_16x16x32_bf16 v[156:159], v[36:39], v[44:47], v[156:159]\n\t" \
  "s_waitcnt lgkmcnt(9)\n\t" \
  "v_mfma_f32_16x16x32_bf16 v[64:67], v[48:51], v[24:27], v[64:67]\n\t" \
  "ds_read_b64_tr_b16 v[44:45], v58 offset:32768\n\t" \
  "v_mfma_f32_16x16x32_bf16 v[72:75], v[48:51], v[28:31], v[72:75]\n\t" \
  "ds_read_b64_tr_b16 v[46:47], v58 offset:33280\n\t" \
  "v_mfma_f32_16x16x32_bf16 v[80:83], v[48:51], v[32:35], v[80:83]\n\t" \
  "v_mfma_f32_16x16x32_bf16 v[88:91], v[48:51], v[36:39], v[88:91]\n\t" \
  "s_waitcnt lgkmcnt(7)\n\t" \
  "v_mfma_f32_16x16x32_bf16 v[68:71], v[52:55], v[24:27], v[68:71]\n\t" \
  "ds_read_b64_tr_b16 v[48:49], v59 offset:28672\n\t" \
  "v_mfma_f32_16x16x32_bf16 v[76:79], v[52:55], v[28:31], v[76:79]\n\t" \
  "ds_read_b64_tr_b16 v[50:51], v59 offset:29184\n\t" \
  "v_mfma_f32_16x16x32_bf16 v[84:87], v[52:55], v[32:35], v[84:87]\n\t" \
  "v_mfma_f32_16x16x32_bf16 v[92:95], v[52:55], v[36:39], v[92:95]\n\t" \
  "N2a_%=:\n\t" \
  "s_waitcnt lgkmcnt(4)\n\t" \
  "v_mfma_f32_16x16x32_bf16 v[160:163], v[40:43], v[0:3], v[160:163]\n\t" \
  "ds_read_b64_tr_b16 v[52:53], v59 offset:32768\n\t" \
  "v_mfma_f32_16x16x32_bf16 v[96:99], v[40:43], v[4:7], v[96:99]\n\t" \
  "ds_read_b64_tr_b16 v[54:55], v59 offset:33280\n\t" \
  "v_mfma_f32_16x16x32_bf16 v[104:107], v[40:43], v[8:11], v[104:107]\n\t" \
  "s_barrier\n\t" \
  "v_mfma_f32_16x16x32_bf16 v[112:115], v[40:43], v[12:15], v[112:115]\n\t" \
  "v_mfma_f32_16x16x32_bf16 v[120:123], v[40:43], v[16:19], v[120:123]\n\t" \
  "s_waitcnt lgkmcnt(4)\n\t" \
  "v_mfma_f32_16x16x32_bf16 v[128:131], v[4:7], v[44:47], v[128:131]\n\t" \
  "ds_read_b64_tr_b16 v[40:41], v58 offset:24576\n\t" \
  "v_mfma_f32_16x16x32_bf16 v[136:139], v[8:11], v[44:47], v[136:139]\n\t" \
  "ds_read_b64_tr_b16 v[42:43], v58 offset:25088\n\t" \
  "v_mfma_f32_16x16x32_bf16 v[144:147], v[12:15], v[44:47], v[144:147]\n\t" \
  "ds_read_b128 v[20:23], v57 offset:18432\n\t" \
  "v_mfma_f32_16x16x32_bf16 v[152:155], v[16:19], v[44:47], v[152:155]\n\t" \
  "ds_read_b128 v[24:27], v57 offset:20480\n\t" \
  "s_waitcnt lgkmcnt(6)\n\t" \
  "v_mfma_f32_16x16x32_bf16 v[164:167], v[48:51], v[0:3], v[164:167]\n\t" \
  "ds_read_b64_tr_b16 v[44:45], v59 offset:24576\n\t" \
  "v_mfma_f32_16x16x32_bf16 v[100:103], v[48:51], v[4:7], v[100:103]\n\t" \
  "ds_read_b64_tr_b16 v[46:47], v59 offset:25088\n\t" \
  "v_mfma_f32_16x16x32_bf16 v[108:111], v[48:51], v[8:11], v[108:111]\n\t" \
  "ds_read_b128 v[28:31], v57 offset:22528\n\t" \
  "v_mfma_f32_16x16x32_bf16 v[116:119], v[48:51], v[12:15], v[116:119]\n\t" \
  "ds_read_b128 v[32:35], v57 offset:24576\n\t" \
  "v_mfma_f32_16x16x32_bf16 v[124:127], v[48:51], v[16:19], v[124:127]\n\t" \
  "s_waitcnt lgkmcnt(8)\n\t" \
  "v_mfma_f32_16x16x32_bf16 v[132:135], v[4:7], v[52:55], v[132:135]\n\t" \
  "ds_read_b64_tr_b16 v[48:49], v60 offset:4096\n\t" \
  "v_mfma_f32_16x16x32_bf16 v[140:143], v[8:11], v[52:55], v[140:143]\n\t" \
  "ds_read_b64_tr_b16 v[50:51], v60 offset:4608\n\t" \
  "v_mfma_f32_16x16x32_bf16 v[148:151], v[12:15], v[52:55], v[148:151]\n\t" \
  "ds_read_b128 v[36:39], v57 offset:26624\n\t" \
  "v_mfma_f32_16x16x32_bf16 v[156:159], v[16:19], v[52:55], v[156:159]\n\t" \
  "s_waitcnt lgkmcnt(9)\n\t" \
  "v_mfma_f32_16x16x32_bf16 v[64:67], v[40:43], v[4:7], v[64:67]\n\t" \
  "ds_read_b64_tr_b16 v[52:53], v60 offset:8192\n\t" \
  "v_mfma_f32_16x16x32_bf16 v[72:75], v[40:43], v[8:11], v[72:75]\n\t" \
  "ds_read_b64_tr_b16 v[54:55], v60 offset:8704\n\t" \
  "v_mfma_f32_16x16x32_bf16 v[80:83], v[40:43], v[12:15], v[80:83]\n\t" \
  "v_mfma_f32_16x16x32_bf16 v[88:91], v[40:43], v[16:19], v[88:91]\n\t" \
  "s_waitcnt lgkmcnt(7)\n\t" \
  "v_mfma_f32_16x16x32_bf16 v[68:71], v[44:47], v[4:7], v[68:71]\n\t" \
  "ds_read_b64_tr_b16 v[40:41], v61 offset:4096\n\t" \
  "v_mfma_f32_16x16x32_bf16 v[76:79], v[44:47], v[8:11], v[76:79]\n\t" \
  "ds_read_b64_tr_b16 v[42:43], v61 offset:4608\n\t" \
  "v_mfma_f32_16x16x32_bf16 v[84:87], v[44:47], v[12:15], v[84:87]\n\t" \
  "v_mfma_f32_16x16x32_bf16 v[92:95], v[44:47], v[16:19], v[92:95]\n\t" \
  "N3a_%=:\n\t" \
  "s_waitcnt lgkmcnt(4)\n\t" \
  "v_mfma_f32_16x16x32_bf16 v[160:163], v[48:51], v[20:23], v[160:163]\n\t" \
  "ds_read_b64_tr_b16 v[44:45], v61 offset:8192\n\t" \
  "v_mfma_f32_16x16x32_bf16 v[96:99], v[48:51], v[24:27], v[96:99]\n\t" \
  "ds_read_b64_tr_b16 v[46:47], v61 offset:8704\n\t" \
  "s_cmp_eq_u32 %[it], 5\n\t" \
  "v_mfma_f32_16x16x32_bf16 v[104:107], v[48:51], v[28:31], v[104:107]\n\t" \
  "s_cbranch_scc1 NJ3a_%=\n\t" \
  "s_barrier\n\t" \
  "NJ3a_%=:\n\t" \
  "v_mfma_f32_16x16x32_bf16 v[112:115], v[48:51], v[32:35], v[112:115]\n\t" \
  "v_mfma_f32_16x16x32_bf16 v[120:123], v[48:51], v[36:39], v[120:123]\n\t" \
  "s_waitcnt lgkmcnt(4)\n\t" \
  "v_mfma_f32_16x16x32_bf16 v[128:131], v[24:27], v[52:55], v[128:131]\n\t" \
  "ds_read_b64_tr_b16 v[48:49], v60 offset:0\n\t" \
  "v_mfma_f32_16x16x32_bf16 v[136:139], v[28:31], v[52:55], v[136:139]\n\t" \
  "ds_read_b64_tr_b16 v[50:51], v60 offset:512\n\t" \
  "v_mfma_f32_16x16x32_bf16 v[144:147], v[32:35], v[52:55], v[144:147]\n\t" \
  "ds_read_b128 v[0:3], v56 offset:36864\n\t" \
  "v_mfma_f32_16x16x32_bf16 v[152:155], v[36:39], v[52:55], v[152:155]\n\t" \
  "ds_read_b128 v[4:7], v56 offset:38912\n\t" \
  "s_waitcnt lgkmcnt(6)\n\t" \
  "v_mfma_f32_16x16x32_bf16 v[164:167], v[40:43], v[20:23], v[164:167]\n\t" \
  "ds_read_b64_tr_b16 v[52:53], v61 offset:0\n\t" \
  "v_mfma_f32_16x16x32_bf16 v[100:103], v[40:43], v[24:27], v[100:103]\n\t" \
  "ds_read_b64_tr_b16 v[54:55], v61 offset:512\n\t" \
  "v_mfma_f32_16x16x32_bf16 v[108:111], v[40:43], v[28:31], v[108:111]\n\t" \
  "ds_read_b128 v[8:11], v56 offset:40960\n\t" \
  "v_mfma_f32_16x16x32_bf16 v[116:119], v[40:43], v[32:35], v[116:119]\n\t" \
  "ds_read_b128 v[12:15], v56 offset:43008\n\t" \
  "v_mfma_f32_16x16x32_bf16 v[124:127], v[40:43], v[36:39], v[124:127]\n\t" \
  "s_waitcnt lgkmcnt(8)\n\t" \
  "v_mfma_f32_16x16x32_bf16 v[132:135], v[24:27], v[44:47], v[132:135]\n\t" \
  "ds_read_b64_tr_b16 v[40:41], v60 offset:16384\n\t" \
  "v_mfma_f32_16x16x32_bf16 v[140:143], v[28:31], v[44:47], v[140:143]\n\t" \
  "ds_read_b64_tr_b16 v[42:43], v60 offset:16896\n\t" \
  "v_mfma_f32_16x16x32_bf16 v[148:151], v[32:35], v[44:47], v[148:151]\n\t" \
  "ds_read_b128 v[16:19], v56 offset:45056\n\t" \
  "v_mfma_f32_16x16x32_bf16 v[156:159], v[36:39], v[44:47], v[156:159]\n\t" \
  "s_waitcnt lgkmcnt(9)\n\t" \
  "v_mfma_f32_16x16x32_bf16 v[64:67], v[48:51], v[24:27], v[64:67]\n\t" \
  "ds_read_b64_tr_b16 v[44:45], v60 offset:20480\n\t" \
  "v_mfma_f32_16x16x32_bf16 v[72:75], v[48:51], v[28:31], v[72:75]\n\t" \
  "ds_read_b64_tr_b16 v[46:47], v60 offset:20992\n\t" \
  "v_mfma_f32_16x16x32_bf16 v[80:83], v[48:51], v[32:35], v[80:83]\n\t" \
  "v_mfma_f32_16x16x32_bf16 v[88:91], v[48:51], v[36:39], v[88:91]\n\t" \
  "s_waitcnt lgkmcnt(7)\n\t" \
  "v_mfma_f32_16x16x32_bf16 v[68:71], v[52:55], v[24:27], v[68:71]\n\t" \
  "ds_read_b64_tr_b16 v[48:49], v61 offset:16384\n\t" \
  "v_mfma_f32_16x16x32_bf16 v[76:79], v[52:55], v[28:31], v[76:79]\n\t" \
  "ds_read_b64_tr_b16 v[50:51], v61 offset:16896\n\t" \
  "v_mfma_f32_16x16x32_bf16 v[84:87], v[52:55], v[32:35], v[84:87]\n\t" \
  "v_mfma_f32_16x16x32_bf16 v[92:95], v[52:55], v[36:39], v[92:95]\n\t" \
  "N4a_%=:\n\t" \
  "s_waitcnt lgkmcnt(4)\n\t" \
  "v_mfma_f32_16x16x32_bf16 v[160:163], v[40:43], v[0:3], v[160:163]\n\t" \
  "ds_read_b64_tr_b16 v[52:53], v61 offset:20480\n\t" \
  "v_mfma_f32_16x16x32_bf16 v[96:99], v[40:43], v[4:7], v[96:99]\n\t" \
  "ds_read_b64_tr_b16 v[54:55], v61 offset:20992\n\t" \
  "s_cmp_eq_u32 %[it], 5\n\t" \
  "v_mfma_f32_16x16x32_bf16 v[104:107], v[40:43], v[8:11], v[104:107]\n\t" \
  "s_cbranch_scc1 NJ4a_%=\n\t" \
  "s_barrier\n\t" \
  "NJ4a_%=:\n\t" \
  "v_mfma_f32_16x16x32_bf16 v[112:115], v[40:43], v[12:15], v[112:115]\n\t" \
  "v_mfma_f32_16x16x32_bf16 v[120:123], v[40:43], v[16:19], v[120:123]\n\t" \
  "s_waitcnt lgkmcnt(4)\n\t" \
  "v_mfma_f32_16x16x32_bf16 v[128:131], v[4:7], v[44:47], v[128:131]\n\t" \
  "ds_read_b64_tr_b16 v[40:41], v60 offset:12288\n\t" \
  "v_mfma_f32_16x16x32_bf16 v[136:139], v[8:11], v[44:47], v[136:139]\n\t" \
  "ds_read_b64_tr_b16 v[42:43], v60 offset:12800\n\t" \
  "v_mfma_f32_16x16x32_bf16 v[144:147], v[12:15], v[44:47], v[144:147]\n\t" \
  "ds_read_b128 v[20:23], v57 offset:36864\n\t" \
  "v_mfma_f32_16x16x32_bf16 v[152:155], v[16:19], v[44:47], v[152:155]\n\t" \
  "ds_read_b128 v[24:27], v57 offset:38912\n\t" \
  "s_waitcnt lgkmcnt(6)\n\t" \
  "v_mfma_f32_16x16x32_bf16 v[164:167], v[48:51], v[0:3], v[164:167]\n\t" \
  "ds_read_b64_tr_b16 v[44:45], v61 offset:12288\n\t" \
  "v_mfma_f32_16x16x32_bf16 v[100:103], v[48:51], v[4:7], v[100:103]\n\t" \
  "ds_read_b64_tr_b16 v[46:47], v61 offset:12800\n\t" \
  "v_mfma_f32_16x16x32_bf16 v[108:111], v[48:51], v[8:11], v[108:111]\n\t" \
  "ds_read_b128 v[28:31], v57 offset:40960\n\t" \
  "v_mfma_f32_16x16x32_bf16 v[116:119], v[48:51], v[12:15], v[116:119]\n\t" \
  "ds_read_b128 v[32:35], v57 offset:43008\n\t" \
  "v_mfma_f32_16x16x32_bf16 v[124:127], v[48:51], v[16:19], v[124:127]\n\t" \
  "s_waitcnt lgkmcnt(8)\n\t" \
  "v_mfma_f32_16x16x32_bf16 v[132:135], v[4:7], v[52:55], v[132:135]\n\t" \
  "ds_read_b64_tr_b16 v[48:49], v60 offset:28672\n\t" \
  "v_mfma_f32_16x16x32_bf16 v[140:143], v[8:11], v[52:55], v[140:143]\n\t" \
  "ds_read_b64_tr_b16 v[50:51], v60 offset:29184\n\t" \
  "v_mfma_f32_16x16x32_bf16 v[148:151], v[12:15], v[52:55], v[148:151]\n\t" \
  "ds_read_b128 v[36:39], v57 offset:45056\n\t" \
  "v_mfma_f32_16x16x32_bf16 v[156:159], v[16:19], v[52:55], v[156:159]\n\t" \
  "s_waitcnt lgkmcnt(9)\n\t" \
  "v_mfma_f32_16x16x32_bf16 v[64:67], v[40:43], v[4:7], v[64:67]\n\t" \
  "ds_read_b64_tr_b16 v[52:53], v60 offset:32768\n\t" \
  "v_mfma_f32_16x16x32_bf16 v[72:75], v[40:43], v[8:11], v[72:75]\n\t" \
  "ds_read_b64_tr_b16 v[54:55], v60 offset:33280\n\t" \
  "v_mfma_f32_16x16x32_bf16 v[80:83], v[40:43], v[12:15], v[80:83]\n\t" \
  "v_mfma_f32_16x16x32_bf16 v[88:91], v[40:43], v[16:19], v[88:91]\n\t" \
  "s_waitcnt lgkmcnt(7)\n\t" \
  "v_mfma_f32_16x16x32_bf16 v[68:71], v[44:47], v[4:7], v[68:71]\n\t" \
  "ds_read_b64_tr_b16 v[40:41], v61 offset:28672\n\t" \
  "v_mfma_f32_16x16x32_bf16 v[76:79], v[44:47], v[8:11], v[76:79]\n\t" \
  "ds_read_b64_tr_b16 v[42:43], v61 offset:29184\n\t" \
  "v_mfma_f32_16x16x32_bf16 v[84:87], v[44:47], v[12:15], v[84:87]\n\t" \
  "v_mfma_f32_16x16x32_bf16 v[92:95], v[44:47], v[16:19], v[92:95]\n\t" \
  "N5a_%=:\n\t" \
  "s_waitcnt lgkmcnt(4)\n\t" \
  "v_mfma_f32_16x16x32_bf16 v[160:163], v[48:51], v[20:23], v[160:163]\n\t" \
  "ds_read_b64_tr_b16 v[44:45], v61 offset:32768\n\t" \
  "v_mfma_f32_16x16x32_bf16 v[96:99], v[48:51], v[24:27], v[96:99]\n\t" \
  "ds_read_b64_tr_b16 v[46:47], v61 offset:33280\n\t" \
  "s_cmp_eq_u32 %[it], 5\n\t" \
  "v_mfma_f32_16x16x32_bf16 v[104:107], v[48:51], v[28:31], v[104:107]\n\t" \
  "s_cbranch_scc1 NJ5a_%=\n\t" \
  "s_barrier\n\t" \
  "NJ5a_%=:\n\t" \
  "v_mfma_f32_16x16x32_bf16 v[112:115], v[48:51], v[32:35], v[112:115]\n\t" \
  "v_mfma_f32_16x16x32_bf16 v[120:123], v[48:51], v[36:39], v[120:123]\n\t" \
  "s_waitcnt lgkmcnt(4)\n\t" \
  "v_mfma_f32_16x16x32_bf16 v[128:131], v[24:27], v[52:55], v[128:131]\n\t" \
  "ds_read_b64_tr_b16 v[48:49], v60 offset:24576\n\t" \
  "v_mfma_f32_16x16x32_bf16 v[136:139], v[28:31], v[52:55], v[136:139]\n\t" \
  "ds_read_b64_tr_b16 v[50:51], v60 offset:25088\n\t" \
  "v_mfma_f32_16x16x32_bf16 v[144:147], v[32:35], v[52:55], v[144:147]\n\t" \
  "ds_read_b128 v[0:3], v56 offset:0\n\t" \
  "v_mfma_f32_16x16x32_bf16 v[152:155], v[36:39], v[52:55], v[152:155]\n\t" \
  "ds_read_b128 v[4:7], v56 offset:2048\n\t" \
  "s_waitcnt lgkmcnt(6)\n\t" \
  "v_mfma_f32_16x16x32_bf16 v[164:167], v[40:43], v[20:23], v[164:167]\n\t" \
  "ds_read_b64_tr_b16 v[52:53], v61 offset:24576\n\t" \
  "v_mfma_f32_16x16x32_bf16 v[100:103], v[40:43], v[24:27], v[100:103]\n\t" \
  "ds_read_b64_tr_b16 v[54:55], v61 offset:25088\n\t" \
  "v_mfma_f32_16x16x32_bf16 v[108:111], v[40:43], v[28:31], v[108:111]\n\t" \
  "ds_read_b128 v[8:11], v56 offset:4096\n\t" \
  "v_mfma_f32_16x16x32_bf16 v[116:119], v[40:43], v[32:35], v[116:119]\n\t" \
  "ds_read_b128 v[12:15], v56 offset:6144\n\t" \
  "v_mfma_f32_16x16x32_bf16 v[124:127], v[40:43], v[36:39], v[124:127]\n\t" \
  "s_waitcnt lgkmcnt(8)\n\t" \
  "v_mfma_f32_16x16x32_bf16 v[132:135], v[24:27], v[44:47], v[132:135]\n\t" \
  "ds_read_b64_tr_b16 v[40:41], v58 offset:4096\n\t" \
  "v_mfma_f32_16x16x32_bf16 v[140:143], v[28:31], v[44:47], v[140:143]\n\t" \
  "ds_read_b64_tr_b16 v[42:43], v58 offset:4608\n\t" \
  "v_mfma_f32_16x16x32_bf16 v[148:151], v[32:35], v[44:47], v[148:151]\n\t" \
  "ds_read_b128 v[16:19], v56 offset:8192\n\t" \
  "v_mfma_f32_16x16x32_bf16 v[156:159], v[36:39], v[44:47], v[156:159]\n\t" \
  "s_waitcnt lgkmcnt(9)\n\t" \
  "v_mfma_f32_16x16x32_bf16 v[64:67], v[48:51], v[24:27], v[64:67]\n\t" \
  "ds_read_b64_tr_b16 v[44:45], v58 offset:8192\n\t" \
  "v_mfma_f32_16x16x32_bf16 v[72:75], v[48:51], v[28:31], v[72:75]\n\t" \
  "ds_read_b64_tr_b16 v[46:47], v58 offset:8704\n\t" \
  "v_mfma_f32_16x16x32_bf16 v[80:83], v[48:51], v[32:35], v[80:83]\n\t" \
  "v_mfma_f32_16x16x32_bf16 v[88:91], v[48:51], v[36:39], v[88:91]\n\t" \
  "s_waitcnt lgkmcnt(7)\n\t" \
  "v_mfma_f32_16x16x32_bf16 v[68:71], v[52:55], v[24:27], v[68:71]\n\t" \
  "ds_read_b64_tr_b16 v[48:49], v59 offset:4096\n\t" \
  "v_mfma_f32_16x16x32_bf16 v[76:79], v[52:55], v[28:31], v[76:79]\n\t" \
  "ds_read_b64_tr_b16 v[50:51], v59 offset:4608\n\t" \
  "s_addk_i32 %[it], 1\n\t" \
  "v_mfma_f32_16x16x32_bf16 v[84:87], v[52:55], v[32:35], v[84:87]\n\t" \
  "s_cmp_lt_u32 %[it], 6\n\t" \
  "v_mfma_f32_16x16x32_bf16 v[92:95], v[52:55], v[36:39], v[92:95]\n\t" \
  "s_cbranch_scc1 N0a_%=\n\t" \
  "s_setprio 3\n\t" \
  "v_cvt_pk_bf16_f32 v120, v120, v121\n\t" \
  "v_cvt_pk_bf16_f32 v121, v122, v123\n\t" \
  "v_cvt_pk_bf16_f32 v122, v124, v125\n\t" \
  "v_cvt_pk_bf16_f32 v123, v126, v127\n\t" \
  "v_cvt_pk_bf16_f32 v28, v152, v153\n\t" \
  "v_cvt_pk_bf16_f32 v29, v154, v155\n\t" \
  "v_cvt_pk_bf16_f32 v38, v156, v157\n\t" \
  "v_cvt_pk_bf16_f32 v39, v158, v159\n\t" \
  "ds_write_b128 v63, v[120:123]\n\t" \
  "ds_write2_b64 v63, v[28:29], v[38:39] offset0:128 offset1:129\n\t" \
  "v_mov_b32 v57, %[fa]\n\t" \
  "v_mov_b32 v58, 1\n\t" \
  "s_mov_b64 exec, 1\n\t" \
  "ds_write_b32 v57, v58\n\t" \
  "s_mov_b64 exec, -1\n\t" \
  "v_cvt_pk_bf16_f32 v160, v160, v161\n\t" \
  "v_cvt_pk_bf16_f32 v161, v162, v163\n\t" \
  "v_cvt_pk_bf16_f32 v162, v164, v165\n\t" \
  "v_cvt_pk_bf16_f32 v163, v166, v167\n\t" \
  "s_branch NX_%=\n\t" \
  "NB_%=:\n\t" \
  "ds_read_b64_tr_b16 v[40:41], v60 offset:20480\n\t" \
  "ds_read_b64_tr_b16 v[42:43], v60 offset:20992\n\t" \
  "ds_read_b64_tr_b16 v[44:45], v60 offset:16384\n\t" \
  "ds_read_b64_tr_b16 v[46:47], v60 offset:16896\n\t" \
  "ds_read_b64_tr_b16 v[48:49], v61 offset:20480\n\t" \
  "ds_read_b64_tr_b16 v[50:51], v61 offset:20992\n\t" \
  "ds_read_b128 v[0:3], v56 offset:36864\n\t" \
  "ds_read_b128 v[4:7], v56 offset:47104\n\t" \
  "ds_read_b128 v[8:11], v56 offset:49152\n\t" \
  "ds_read_b128 v[12:15], v56 offset:51200\n\t" \
  "ds_read_b128 v[16:19], v56 offset:53248\n\t" \
  "s_waitcnt lgkmcnt(0)\n\t" \
  "s_branch N4b_%=\n\t" \
  "N0b_%=:\n\t" \
  "s_waitcnt lgkmcnt(4)\n\t" \
  "v_mfma_f32_16x16x32_bf16 v[160:163], v[0:3], v[40:43], v[160:163]\n\t" \
  "ds_read_b64_tr_b16 v[52:53], v59 offset:4096\n\t" \
  "v_mfma_f32_16x16x32_bf16 v[128:131], v[4:7], v[40:43], v[128:131]\n\t" \
  "ds_read_b64_tr_b16 v[54:55], v59 offset:4608\n\t" \
  "v_mfma_f32_16x16x32_bf16 v[136:139], v[8:11], v[40:43], v[136:139]\n\t" \
  "s_barrier\n\t" \
  "v_mfma_f32_16x16x32_bf16 v[144:147], v[12:15], v[40:43], v[144:147]\n\t" \
  "v_mfma_f32_16x16x32_bf16 v[152:155], v[16:19], v[40:43], v[152:155]\n\t" \
  "s_waitcnt lgkmcnt(4)\n\t" \
  "v_mfma_f32_16x16x32_bf16 v[96:99], v[44:47], v[4:7], v[96:99]\n\t" \
  "ds_read_b64_tr_b16 v[40:41], v58 offset:0\n\t" \
  "v_mfma_f32_16x16x32_bf16 v[104:107], v[44:47], v[8:11], v[104:107]\n\t" \
  "ds_read_b64_tr_b16 v[42:43], v58 offset:512\n\t" \
  "v_mfma_f32_16x16x32_bf16 v[112:115], v[44:47], v[12:15], v[112:115]\n\t" \
  "ds_read_b128 v[20:23], v57 offset:0\n\t" \
  "v_mfma_f32_16x16x32_bf16 v[120:123], v[44:47], v[16:19], v[120:123]\n\t" \
  "ds_read_b128 v[24:27], v57 offset:10240\n\t" \
  "s_waitcnt lgkmcnt(6)\n\t" \
  "v_mfma_f32_16x16x32_bf16 v[164:167], v[0:3], v[48:51], v[164:167]\n\t" \
  "ds_read_b64_tr_b16 v[44:45], v59 offset:0\n\t" \
  "v_mfma_f32_16x16x32_bf16 v[132:135], v[4:7], v[48:51], v[132:135]\n\t" \
  "ds_read_b64_tr_b16 v[46:47], v59 offset:512\n\t" \
  "v_mfma_f32_16x16x32_bf16 v[140:143], v[8:11], v[48:51], v[140:143]\n\t" \
  "ds_read_b128 v[28:31], v57 offset:12288\n\t" \
  "v_mfma_f32_16x16x32_bf16 v[148:151], v[12:15], v[48:51], v[148:151]\n\t" \
  "ds_read_b128 v[32:35], v57 offset:14336\n\t" \
  "v_mfma_f32_16x16x32_bf16 v[156:159], v[16:19], v[48:51], v[156:159]\n\t" \
  "s_waitcnt lgkmcnt(8)\n\t" \
  "v_mfma_f32_16x16x32_bf16 v[100:103], v[52:55], v[4:7], v[100:103]\n\t" \
  "ds_read_b64_tr_b16 v[48:49], v58 offset:20480\n\t" \
  "v_mfma_f32_16x16x32_bf16 v[108:111], v[52:55], v[8:11], v[108:111]\n\t" \
  "ds_read_b64_tr_b16 v[50:51], v58 offset:20992\n\t" \
  "v_mfma_f32_16x16x32_bf16 v[116:119], v[52:55], v[12:15], v[116:119]\n\t" \
  "ds_read_b128 v[36:39], v57 offset:16384\n\t" \
  "v_mfma_f32_16x16x32_bf16 v[124:127], v[52:55], v[16:19], v[124:127]\n\t" \
  "s_waitcnt lgkmcnt(9)\n\t" \
  "v_mfma_f32_16x16x32_bf16 v[64:67], v[40:43], v[4:7], v[64:67]\n\t" \
  "ds_read_b64_tr_b16 v[52:53], v58 offset:16384\n\t" \
  "v_mfma_f32_16x16x32_bf16 v[72:75], v[40:43], v[8:11], v[72:75]\n\t" \
  "ds_read_b64_tr_b16 v[54:55], v58 offset:16896\n\t" \
  "v_mfma_f32_16x16x32_bf16 v[80:83], v[40:43], v[12:15], v[80:83]\n\t" \
  "v_mfma_f32_16x16x32_bf16 v[88:91], v[40:43], v[16:19], v[88:91]\n\t" \
  "s_waitcnt lgkmcnt(7)\n\t" \
  "v_mfma_f32_16x16x32_bf16 v[68:71], v[44:47], v[4:7], v[68:71]\n\t" \
  "ds_read_b64_tr_b16 v[40:41], v59 offset:20480\n\t" \
  "v_mfma_f32_16x16x32_bf16 v[76:79], v[44:47], v[8:11], v[76:79]\n\t" \
  "ds_read_b64_tr_b16 v[42:43], v59 offset:20992\n\t" \
  "v_mfma_f32_16x16x32_bf16 v[84:87], v[44:47], v[12:15], v[84:87]\n\t" \
  "v_mfma_f32_16x16x32_bf16 v[92:95], v[44:47], v[16:19], v[92:95]\n\t" \
  "N1b_%=:\n\t" \
  "s_waitcnt lgkmcnt(4)\n\t" \
  "v_mfma_f32_16x16x32_bf16 v[160:163], v[20:23], v[48:51], v[160:163]\n\t" \
  "ds_read_b64_tr_b16 v[44:45], v59 offset:16384\n\t" \
  "v_mfma_f32_16x16x32_bf16 v[128:131], v[24:27], v[48:51], v[128:131]\n\t" \
  "ds_read_b64_tr_b16 v[46:47], v59 offset:16896\n\t" \
  "v_mfma_f32_16x16x32_bf16 v[136:139], v[28:31], v[48:51], v[136:139]\n\t" \
  "s_barrier\n\t" \
  "v_mfma_f32_16x16x32_bf16 v[144:147], v[32:35], v[48:51], v[144:147]\n\t" \
  "v_mfma_f32_16x16x32_bf16 v[152:155], v[36:39], v[48:51], v[152:155]\n\t" \
  "s_waitcnt lgkmcnt(4)\n\t" \
  "v_mfma_f32_16x16x32_bf16 v[96:99], v[52:55], v[24:27], v[96:99]\n\t" \
  "ds_read_b64_tr_b16 v[48:49], v58 offset:12288\n\t" \
  "v_mfma_f32_16x16x32_bf16 v[104:107], v[52:55], v[28:31], v[104:107]\n\t" \
  "ds_read_b64_tr_b16 v[50:51], v58 offset:12800\n\t" \
  "v_mfma_f32_16x16x32_bf16 v[112:115], v[52:55], v[32:35], v[112:115]\n\t" \
  "ds_read_b128 v[0:3], v56 offset:18432\n\t" \
  "v_mfma_f32_16x16x32_bf16 v[120:123], v[52:55], v[36:39], v[120:123]\n\t" \
  "ds_read_b128 v[4:7], v56 offset:28672\n\t" \
  "s_waitcnt lgkmcnt(6)\n\t" \
  "v_mfma_f32_16x16x32_bf16 v[164:167], v[20:23], v[40:43], v[164:167]\n\t" \
  "ds_read_b64_tr_b16 v[52:53], v59 offset:12288\n\t" \
  "v_mfma_f32_16x16x32_bf16 v[132:135], v[24:27], v[40:43], v[132:135]\n\t" \
  "ds_read_b64_tr_b16 v[54:55], v59 offset:12800\n\t" \
  "v_mfma_f32_16x16x32_bf16 v[140:143], v[28:31], v[40:43], v[140:143]\n\t" \
  "ds_read_b128 v[8:11], v56 offset:30720\n\t" \
  "v_mfma_f32_16x16x32_bf16 v[148:151], v[32:35], v[40:43], v[148:151]\n\t" \
  "ds_read_b128 v[12:15], v56 offset:32768\n\t" \
  "v_mfma_f32_16x16x32_bf16 v[156:159], v[36:39], v[40:43], v[156:159]\n\t" \
  "s_waitcnt lgkmcnt(8)\n\t" \
  "v_mfma_f32_16x16x32_bf16 v[100:103], v[44:47], v[24:27], v[100:103]\n\t" \
  "ds_read_b64_tr_b16 v[40:41], v58 offset:32768\n\t" \
  "v_mfma_f32_16x16x32_bf16 v[108:111], v[44:47], v[28:31], v[108:111]\n\t" \
  "ds_read_b64_tr_b16 v[42:43], v58 offset:33280\n\t" \
  "v_mfma_f32_16x16x32_bf16 v[116:119], v[44:47], v[32:35], v[116:119]\n\t" \
  "ds_read_b128 v[16:19], v56 offset:34816\n\t" \
  "v_mfma_f32_16x16x32_bf16 v[124:127], v[44:47], v[36:39], v[124:127]\n\t" \
  "s_waitcnt lgkmcnt(9)\n\t" \
  "v_mfma_f32_16x16x32_bf16 v[64:67], v[48:51], v[24:27], v[64:67]\n\t" \
  "ds_read_b64_tr_b16 v[44:45], v58 offset:28672\n\t" \
  "v_mfma_f32_16x16x32_bf16 v[72:75], v[48:51], v[28:31], v[72:75]\n\t" \
  "ds_read_b64_tr_b16 v[46:47], v58 offset:29184\n\t" \
  "v_mfma_f32_16x16x32_bf16 v[80:83], v[48:51], v[32:35], v[80:83]\n\t" \
  "v_mfma_f32_16x16x32_bf16 v[88:91], v[48:51], v[36:39], v[88:91]\n\t" \
  "s_waitcnt lgkmcnt(7)\n\t" \
  "v_mfma_f32_16x16x32_bf16 v[68:71], v[52:55], v[24:27], v[68:71]\n\t" \
  "ds_read_b64_tr_b16 v[48:49], v59 offset:32768\n\t" \
  "v_mfma_f32_16x16x32_bf16 v[76:79], v[52:55], v[28:31], v[76:79]\n\t" \
  "ds_read_b64_tr_b16 v[50:51], v59 offset:33280\n\t" \
  "v_mfma_f32_16x16x32_bf16 v[84:87], v[52:55], v[32:35], v[84:87]\n\t" \
  "v_mfma_f32_16x16x32_bf16 v[92:95], v[52:55], v[36:39], v[92:95]\n\t" \
  "N2b_%=:\n\t" \
  "s_waitcnt lgkmcnt(4)\n\t" \
  "v_mfma_f32_16x16x32_bf16 v[160:163], v[0:3], v[40:43], v[160:163]\n\t" \
  "ds_read_b64_tr_b16 v[52:53], v59 offset:28672\n\t" \
  "v_mfma_f32_16x16x32_bf16 v[128:131], v[4:7], v[40:43], v[128:131]\n\t" \
  "ds_read_b64_tr_b16 v[54:55], v59 offset:29184\n\t" \
  "v_mfma_f32_16x16x32_bf16 v[136:139], v[8:11], v[40:43], v[136:139]\n\t" \
  "s_barrier\n\t" \
  "v_mfma_f32_16x16x32_bf16 v[144:147], v[12:15], v[40:43], v[144:147]\n\t" \
  "v_mfma_f32_16x16x32_bf16 v[152:155], v[16:19], v[40:43], v[152:155]\n\t" \
  "s_waitcnt lgkmcnt(4)\n\t" \
  "v_mfma_f32_16x16x32_bf16 v[96:99], v[44:47], v[4:7], v[96:99]\n\t" \
  "ds_read_b64_tr_b16 v[40:41], v58 offset:24576\n\t" \
  "v_mfma_f32_16x16x32_bf16 v[104:107], v[44:47], v[8:11], v[104:107]\n\t" \
  "ds_read_b64_tr_b16 v[42:43], v58 offset:25088\n\t" \
  "v_mfma_f32_16x16x32_bf16 v[112:115], v[44:47], v[12:15], v[112:115]\n\t" \
  "ds_read_b128 v[20:23], v57 offset:18432\n\t" \
  "v_mfma_f32_16x16x32_bf16 v[120:123], v[44:47], v[16:19], v[120:123]\n\t" \
  "ds_read_b128 v[24:27], v57 offset:28672\n\t" \
  "s_waitcnt lgkmcnt(6)\n\t" \
  "v_mfma_f32_16x16x32_bf16 v[164:167], v[0:3], v[48:51], v[164:167]\n\t" \
  "ds_read_b64_tr_b16 v[44:45], v59 offset:24576\n\t" \
  "v_mfma_f32_16x16x32_bf16 v[132:135], v[4:7], v[48:51], v[132:135]\n\t" \
  "ds_read_b64_tr_b16 v[46:47], v59 offset:25088\n\t" \
  "v_mfma_f32_16x16x32_bf16 v[140:143], v[8:11], v[48:51], v[140:143]\n\t" \
  "ds_read_b128 v[28:31], v57 offset:30720\n\t" \
  "v_mfma_f32_16x16x32_bf16 v[148:151], v[12:15], v[48:51], v[148:151]\n\t" \
  "ds_read_b128 v[32:35], v57 offset:32768\n\t" \
  "v_mfma_f32_16x16x32_bf16 v[156:159], v[16:19], v[48:51], v[156:159]\n\t" \
  "s_waitcnt lgkmcnt(8)\n\t" \
  "v_mfma_f32_16x16x32_bf16 v[100:103], v[52:55], v[4:7], v[100:103]\n\t" \
  "ds_read_b64_tr_b16 v[48:49], v60 offset:8192\n\t" \
  "v_mfma_f32_16x16x32_bf16 v[108:111], v[52:55], v[8:11], v[108:111]\n\t" \
  "ds_read_b64_tr_b16 v[50:51], v60 offset:8704\n\t" \
  "v_mfma_f32_16x16x32_bf16 v[116:119], v[52:55], v[12:15], v[116:119]\n\t" \
  "ds_read_b128 v[36:39], v57 offset:34816\n\t" \
  "v_mfma_f32_16x16x32_bf16 v[124:127], v[52:55], v[16:19], v[124:127]\n\t" \
  "s_waitcnt lgkmcnt(9)\n\t" \
  "v_mfma_f32_16x16x32_bf16 v[64:67], v[40:43], v[4:7], v[64:67]\n\t" \
  "ds_read_b64_tr_b16 v[52:53], v60 offset:4096\n\t" \
  "v_mfma_f32_16x16x32_bf16 v[72:75], v[40:43], v[8:11], v[72:75]\n\t" \
  "ds_read_b64_tr_b16 v[54:55], v60 offset:4608\n\t" \
  "v_mfma_f32_16x16x32_bf16 v[80:83], v[40:43], v[12:15], v[80:83]\n\t" \
  "v_mfma_f32_16x16x32_bf16 v[88:91], v[40:43], v[16:19], v[88:91]\n\t" \
  "s_waitcnt lgkmcnt(7)\n\t" \
  "v_mfma_f32_16x16x32_bf16 v[68:71], v[44:47], v[4:7], v[68:71]\n\t" \
  "ds_read_b64_tr_b16 v[40:41], v61 offset:8192\n\t" \
  "v_mfma_f32_16x16x32_bf16 v[76:79], v[44:47], v[8:11], v[76:79]\n\t" \
  "ds_read_b64_tr_b16 v[42:43], v61 offset:8704\n\t" \
  "v_mfma_f32_16x16x32_bf16 v[84:87], v[44:47], v[12:15], v[84:87]\n\t" \
  "v_mfma_f32_16x16x32_bf16 v[92:95], v[44:47], v[16:19], v[92:95]\n\t" \
  "N3b_%=:\n\t" \
  "s_waitcnt lgkmcnt(4)\n\t" \
  "v_mfma_f32_16x16x32_bf16 v[160:163], v[20:23], v[48:51], v[160:163]\n\t" \
  "ds_read_b64_tr_b16 v[44:45], v61 offset:4096\n\t" \
  "v_mfma_f32_16x16x32_bf16 v[128:131], v[24:27], v[48:51], v[128:131]\n\t" \
  "ds_read_b64_tr_b16 v[46:47], v61 offset:4608\n\t" \
  "s_cmp_eq_u32 %[it], 5\n\t" \
  "v_mfma_f32_16x16x32_bf16 v[136:139], v[28:31], v[48:51], v[136:139]\n\t" \
  "s_cbranch_scc1 NJ3b_%=\n\t" \
  "s_barrier\n\t" \
  "NJ3b_%=:\n\t" \
  "v_mfma_f32_16x16x32_bf16 v[144:147], v[32:35], v[48:51], v[144:147]\n\t" \
  "v_mfma_f32_16x16x32_bf16 v[152:155], v[36:39], v[48:51], v[152:155]\n\t" \
  "s_waitcnt lgkmcnt(4)\n\t" \
  "v_mfma_f32_16x16x32_bf16 v[96:99], v[52:55], v[24:27], v[96:99]\n\t" \
  "ds_read_b64_tr_b16 v[48:49], v60 offset:0\n\t" \
  "v_mfma_f32_16x16x32_bf16 v[104:107], v[52:55], v[28:31], v[104:107]\n\t" \
  "ds_read_b64_tr_b16 v[50:51], v60 offset:512\n\t" \
  "v_mfma_f32_16x16x32_bf16 v[112:115], v[52:55], v[32:35], v[112:115]\n\t" \
  "ds_read_b128 v[0:3], v56 offset:36864\n\t" \
  "v_mfma_f32_16x16x32_bf16 v[120:123], v[52:55], v[36:39], v[120:123]\n\t" \
  "ds_read_b128 v[4:7], v56 offset:47104\n\t" \
  "s_waitcnt lgkmcnt(6)\n\t" \
  "v_mfma_f32_16x16x32_bf16 v[164:167], v[20:23], v[40:43], v[164:167]\n\t" \
  "ds_read_b64_tr_b16 v[52:53], v61 offset:0\n\t" \
  "v_mfma_f32_16x16x32_bf16 v[132:135], v[24:27], v[40:43], v[132:135]\n\t" \
  "ds_read_b64_tr_b16 v[54:55], v61 offset:512\n\t" \
  "v_mfma_f32_16x16x32_bf16 v[140:143], v[28:31], v[40:43], v[140:143]\n\t" \
  "ds_read_b128 v[8:11], v56 offset:49152\n\t" \
  "v_mfma_f32_16x16x32_bf16 v[148:151], v[32:35], v[40:43], v[148:151]\n\t" \
  "ds_read_b128 v[12:15], v56 offset:51200\n\t" \
  "v_mfma_f32_16x16x32_bf16 v[156:159], v[36:39], v[40:43], v[156:159]\n\t" \
  "s_waitcnt lgkmcnt(8)\n\t" \
  "v_mfma_f32_16x16x32_bf16 v[100:103], v[44:47], v[24:27], v[100:103]\n\t" \
  "ds_read_b64_tr_b16 v[40:41], v60 offset:20480\n\t" \
  "v_mfma_f32_16x16x32_bf16 v[108:111], v[44:47], v[28:31], v[108:111]\n\t" \
  "ds_read_b64_tr_b16 v[42:43], v60 offset:20992\n\t" \
  "v_mfma_f32_16x16x32_bf16 v[116:119], v[44:47], v[32:35], v[116:119]\n\t" \
  "ds_read_b128 v[16:19], v56 offset:53248\n\t" \
  "v_mfma_f32_16x16x32_bf16 v[124:127], v[44:47], v[36:39], v[124:127]\n\t" \
  "s_waitcnt lgkmcnt(9)\n\t" \
  "v_mfma_f32_16x16x32_bf16 v[64:67], v[48:51], v[24:27], v[64:67]\n\t" \
  "ds_read_b64_tr_b16 v[44:45], v60 offset:16384\n\t" \
  "v_mfma_f32_16x16x32_bf16 v[72:75], v[48:51], v[28:31], v[72:75]\n\t" \
  "ds_read_b64_tr_b16 v[46:47], v60 offset:16896\n\t" \
  "v_mfma_f32_16x16x32_bf16 v[80:83], v[48:51], v[32:35], v[80:83]\n\t" \
  "v_mfma_f32_16x16x32_bf16 v[88:91], v[48:51], v[36:39], v[88:91]\n\t" \
  "s_waitcnt lgkmcnt(7)\n\t" \
  "v_mfma_f32_16x16x32_bf16 v[68:71], v[52:55], v[24:27], v[68:71]\n\t" \
  "ds_read_b64_tr_b16 v[48:49], v61 offset:20480\n\t" \
  "v_mfma_f32_16x16x32_bf16 v[76:79], v[52:55], v[28:31], v[76:79]\n\t" \
  "ds_read_b64_tr_b16 v[50:51], v61 offset:20992\n\t" \
  "v_mfma_f32_16x16x32_bf16 v[84:87], v[52:55], v[32:35], v[84:87]\n\t" \
  "v_mfma_f32_16x16x32_bf16 v[92:95], v[52:55], v[36:39], v[92:95]\n\t" \
  "N4b_%=:\n\t" \
  "s_waitcnt lgkmcnt(4)\n\t" \
  "v_mfma_f32_16x16x32_bf16 v[160:163], v[0:3], v[40:43], v[160:163]\n\t" \
  "ds_read_b64_tr_b16 v[52:53], v61 offset:16384\n\t" \
  "v_mfma_f32_16x16x32_bf16 v[128:131], v[4:7], v[40:43], v[128:131]\n\t" \
  "ds_read_b64_tr_b16 v[54:55], v61 offset:16896\n\t" \
  "s_cmp_eq_u32 %[it], 5\n\t" \
  "v_mfma_f32_16x16x32_bf16 v[136:139], v[8:11], v[40:43], v[136:139]\n\t" \
  "s_cbranch_scc1 NJ4b_%=\n\t" \
  "s_barrier\n\t" \
  "NJ4b_%=:\n\t" \
  "v_mfma_f32_16x16x32_bf16 v[144:147], v[12:15], v[40:43], v[144:147]\n\t" \
  "v_mfma_f32_16x16x32_bf16 v[152:155], v[16:19], v[40:43], v[152:155]\n\t" \
  "s_waitcnt lgkmcnt(4)\n\t" \
  "v_mfma_f32_16x16x32_bf16 v[96:99], v[44:47], v[4:7], v[96:99]\n\t" \
  "ds_read_b64_tr_b16 v[40:41], v60 offset:12288\n\t" \
  "v_mfma_f32_16x16x32_bf16 v[104:107], v[44:47], v[8:11], v[104:107]\n\t" \
  "ds_read_b64_tr_b16 v[42:43], v60 offset:12800\n\t" \
  "v_mfma_f32_16x16x32_bf16 v[112:115], v[44:47], v[12:15], v[112:115]\n\t" \
  "ds_read_b128 v[20:23], v57 offset:36864\n\t" \
  "v_mfma_f32_16x16x32_bf16 v[120:123], v[44:47], v[16:19], v[120:123]\n\t" \
  "ds_read_b128 v[24:27], v57 offset:47104\n\t" \
  "s_waitcnt lgkmcnt(6)\n\t" \
  "v_mfma_f32_16x16x32_bf16 v[164:167], v[0:3], v[48:51], v[164:167]\n\t" \
  "ds_read_b64_tr_b16 v[44:45], v61 offset:12288\n\t" \
  "v_mfma_f32_16x16x32_bf16 v[132:135], v[4:7], v[48:51], v[132:135]\n\t" \
  "ds_read_b64_tr_b16 v[46:47], v61 offset:12800\n\t" \
  "v_mfma_f32_16x16x32_bf16 v[140:143], v[8:11], v[48:51], v[140:143]\n\t" \
  "ds_read_b128 v[28:31], v57 offset:49152\n\t" \
  "v_mfma_f32_16x16x32_bf16 v[148:151], v[12:15], v[48:51], v[148:151]\n\t" \
  "ds_read_b128 v[32:35], v57 offset:51200\n\t" \
  "v_mfma_f32_16x16x32_bf16 v[156:159], v[16:19], v[48:51], v[156:159]\n\t" \
  "s_waitcnt lgkmcnt(8)\n\t" \
  "v_mfma_f32_16x16x32_bf16 v[100:103], v[52:55], v[4:7], v[100:103]\n\t" \
  "ds_read_b64_tr_b16 v[48:49], v60 offset:32768\n\t" \
  "v_mfma_f32_16x16x32_bf16 v[108:111], v[52:55], v[8:11], v[108:111]\n\t" \
  "ds_read_b64_tr_b16 v[50:51], v60 offset:33280\n\t" \
  "v_mfma_f32_16x16x32_bf16 v[116:119], v[52:55], v[12:15], v[116:119]\n\t" \
  "ds_read_b128 v[36:39], v57 offset:53248\n\t" \
  "v_mfma_f32_16x16x32_bf16 v[124:127], v[52:55], v[16:19], v[124:127]\n\t" \
  "s_waitcnt lgkmcnt(9)\n\t" \
  "v_mfma_f32_16x16x32_bf16 v[64:67], v[40:43], v[4:7], v[64:67]\n\t" \
  "ds_read_b64_tr_b16 v[52:53], v60 offset:28672\n\t" \
  "v_mfma_f32_16x16x32_bf16 v[72:75], v[40:43], v[8:11], v[72:75]\n\t" \
  "ds_read_b64_tr_b16 v[54:55], v60 offset:29184\n\t" \
  "v_mfma_f32_16x16x32_bf16 v[80:83], v[40:43], v[12:15], v[80:83]\n\t" \
  "v_mfma_f32_16x16x32_bf16 v[88:91], v[40:43], v[16:19], v[88:91]\n\t" \
  "s_waitcnt lgkmcnt(7)\n\t" \
  "v_mfma_f32_16x16x32_bf16 v[68:71], v[44:47], v[4:7], v[68:71]\n\t" \
  "ds_read_b64_tr_b16 v[40:41], v61 offset:32768\n\t" \
  "v_mfma_f32_16x16x32_bf16 v[76:79], v[44:47], v[8:11], v[76:79]\n\t" \
  "ds_read_b64_tr_b16 v[42:43], v61 offset:33280\n\t" \
  "v_mfma_f32_16x16x32_bf16 v[84:87], v[44:47], v[12:15], v[84:87]\n\t" \
  "v_mfma_f32_16x16x32_bf16 v[92:95], v[44:47], v[16:19], v[92:95]\n\t" \
  "N5b_%=:\n\t" \
  "s_waitcnt lgkmcnt(4)\n\t" \
  "v_mfma_f32_16x16x32_bf16 v[160:163], v[20:23], v[48:51], v[160:163]\n\t" \
  "ds_read_b64_tr_b16 v[44:45], v61 offset:28672\n\t" \
  "v_mfma_f32_16x16x32_bf16 v[128:131], v[24:27], v[48:51], v[128:131]\n\t" \
  "ds_read_b64_tr_b16 v[46:47], v61 offset:29184\n\t" \
  "s_cmp_eq_u32 %[it], 5\n\t" \
  "v_mfma_f32_16x16x32_bf16 v[136:139], v[28:31], v[48:51], v[136:139]\n\t" \
  "s_cbranch_scc1 NJ5b_%=\n\t" \
  "s_barrier\n\t" \
  "NJ5b_%=:\n\t" \
  "v_mfma_f32_16x16x32_bf16 v[144:147], v[32:35], v[48:51], v[144:147]\n\t" \
  "v_mfma_f32_16x16x32_bf16 v[152:155], v[36:39], v[48:51], v[152:155]\n\t" \
  "s_waitcnt lgkmcnt(4)\n\t" \
  "v_mfma_f32_16x16x32_bf16 v[96:99], v[52:55], v[24:27], v[96:99]\n\t" \
  "ds_read_b64_tr_b16 v[48:49], v60 offset:24576\n\t" \
  "v_mfma_f32_16x16x32_bf16 v[104:107], v[52:55], v[28:31], v[104:107]\n\t" \
  "ds_read_b64_tr_b16 v[50:51], v60 offset:25088\n\t" \
  "v_mfma_f32_16x16x32_bf16 v[112:115], v[52:55], v[32:35], v[112:115]\n\t" \
  "ds_read_b128 v[0:3], v56 offset:0\n\t" \
  "v_mfma_f32_16x16x32_bf16 v[120:123], v[52:55], v[36:39], v[120:123]\n\t" \
  "ds_read_b128 v[4:7], v56 offset:10240\n\t" \
  "s_waitcnt lgkmcnt(6)\n\t" \
  "v_mfma_f32_16x16x32_bf16 v[164:167], v[20:23], v[40:43], v[164:167]\n\t" \
  "ds_read_b64_tr_b16 v[52:53], v61 offset:24576\n\t" \
  "v_mfma_f32_16x16x32_bf16 v[132:135], v[24:27], v[40:43], v[132:135]\n\t" \
  "ds_read_b64_tr_b16 v[54:55], v61 offset:25088\n\t" \
  "v_mfma_f32_16x16x32_bf16 v[140:143], v[28:31], v[40:43], v[140:143]\n\t" \
  "ds_read_b128 v[8:11], v56 offset:12288\n\t" \
  "v_mfma_f32_16x16x32_bf16 v[148:151], v[32:35], v[40:43], v[148:151]\n\t" \
  "ds_read_b128 v[12:15], v56 offset:14336\n\t" \
  "v_mfma_f32_16x16x32_bf16 v[156:159], v[36:39], v[40:43], v[156:159]\n\t" \
  "s_waitcnt lgkmcnt(8)\n\t" \
  "v_mfma_f32_16x16x32_bf16 v[100:103], v[44:47], v[24:27], v[100:103]\n\t" \
  "ds_read_b64_tr_b16 v[40:41], v58 offset:8192\n\t" \
  "v_mfma_f32_16x16x32_bf16 v[108:111], v[44:47], v[28:31], v[108:111]\n\t" \
  "ds_read_b64_tr_b16 v[42:43], v58 offset:8704\n\t" \
  "v_mfma_f32_16x16x32_bf16 v[116:119], v[44:47], v[32:35], v[116:119]\n\t" \
  "ds_read_b128 v[16:19], v56 offset:16384\n\t" \
  "v_mfma_f32_16x16x32_bf16 v[124:127], v[44:47], v[36:39], v[124:127]\n\t" \
  "s_waitcnt lgkmcnt(9)\n\t" \
  "v_mfma_f32_16x16x32_bf16 v[64:67], v[48:51], v[24:27], v[64:67]\n\t" \
  "ds_read_b64_tr_b16 v[44:45], v58 offset:4096\n\t" \
  "v_mfma_f32_16x16x32_bf16 v[72:75], v[48:51], v[28:31], v[72:75]\n\t" \
  "ds_read_b64_tr_b16 v[46:47], v58 offset:4608\n\t" \
  "v_mfma_f32_16x16x32_bf16 v[80:83], v[48:51], v[32:35], v[80:83]\n\t" \
  "v_mfma_f32_16x16x32_bf16 v[88:91], v[48:51], v[36:39], v[88:91]\n\t" \
  "s_waitcnt lgkmcnt(7)\n\t" \
  "v_mfma_f32_16x16x32_bf16 v[68:71], v[52:55], v[24:27], v[68:71]\n\t" \
  "ds_read_b64_tr_b16 v[48:49], v59 offset:8192\n\t" \
  "v_mfma_f32_16x16x32_bf16 v[76:79], v[52:55], v[28:31], v[76:79]\n\t" \
  "ds_read_b64_tr_b16 v[50:51], v59 offset:8704\n\t" \
  "s_addk_i32 %[it], 1\n\t" \
  "v_mfma_f32_16x16x32_bf16 v[84:87], v[52:55], v[32:35], v[84:87]\n\t" \
  "s_cmp_lt_u32 %[it], 6\n\t" \
  "v_mfma_f32_16x16x32_bf16 v[92:95], v[52:55], v[36:39], v[92:95]\n\t" \
  "s_cbranch_scc1 N0b_%=\n\t" \
  "s_setprio 3\n\t" \
  "v_cvt_pk_bf16_f32 v160, v160, v161\n\t" \
  "v_cvt_pk_bf16_f32 v161, v162, v163\n\t" \
  "v_cvt_pk_bf16_f32 v162, v164, v165\n\t" \
  "v_cvt_pk_bf16_f32 v163, v166, v167\n\t" \
  "ds_write_b128 v63, v[160:163] offset:2048\n\t" \
  "v_mov_b32 v57, %[fa]\n\t" \
  "v_mov_b32 v58, 1\n\t" \
  "s_mov_b64 exec, 1\n\t" \
  "ds_write_b32 v57, v58\n\t" \
  "s_mov_b64 exec, -1\n\t" \
  "v_cvt_pk_bf16_f32 v120, v120, v121\n\t" \
  "v_cvt_pk_bf16_f32 v121, v122, v123\n\t" \
  "v_cvt_pk_bf16_f32 v122, v124, v125\n\t" \
  "v_cvt_pk_bf16_f32 v123, v126, v127\n\t" \
  "v_cvt_pk_bf16_f32 v28, v152, v153\n\t" \
  "v_cvt_pk_bf16_f32 v29, v154, v155\n\t" \
  "v_cvt_pk_bf16_f32 v38, v156, v157\n\t" \
  "v_cvt_pk_bf16_f32 v39, v158, v159\n\t" \
  "NX_%=:\n\t" \
  "v_cvt_pk_bf16_f32 v96, v96, v97\n\t" \
  "v_cvt_pk_bf16_f32 v97, v98, v99\n\t" \
  "v_cvt_pk_bf16_f32 v98, v100, v101\n\t" \
  "v_cvt_pk_bf16_f32 v99, v102, v103\n\t" \
  "v_cvt_pk_bf16_f32 v104, v104, v105\n\t" \
  "v_cvt_pk_bf16_f32 v105, v106, v107\n\t" \
  "v_cvt_pk_bf16_f32 v106, v108, v109\n\t" \
  "v_cvt_pk_bf16_f32 v107, v110, v111\n\t" \
  "v_mov_b32 v60, 0xff61b1e6\n\t" \
  "v_cndmask_b32_e64 v56, v60, 0, s[68:69]\n\t" \
  "v_cndmask_b32_e64 v57, v60, 0, s[70:71]\n\t" \
  "v_cndmask_b32_e64 v58, v60, 0, s[72:73]\n\t" \
  "v_cndmask_b32_e64 v59, v60, 0, s[74:75]\n\t" \
  "v_cvt_pk_bf16_f32 v72, v72, v73\n\t" \
  "v_cvt_pk_bf16_f32 v73, v74, v75\n\t" \
  "v_cvt_pk_bf16_f32 v74, v76, v77\n\t" \
  "v_cvt_pk_bf16_f32 v75, v78, v79\n\t" \
  "v_cndmask_b32_e64 v52, v60, 0, s[76:77]\n\t" \
  "v_cndmask_b32_e64 v53, v60, 0, s[78:79]\n\t" \
  "v_cndmask_b32_e64 v54, v60, 0, s[80:81]\n\t" \
  "v_cndmask_b32_e64 v55, v60, 0, s[82:83]\n\t" \
  "v_cvt_pk_bf16_f32 v24, v136, v137\n\t" \
  "v_cvt_pk_bf16_f32 v25, v138, v139\n\t" \
  "v_cvt_pk_bf16_f32 v34, v140, v141\n\t" \
  "v_cvt_pk_bf16_f32 v35, v142, v143\n\t" \
  "v_cvt_pk_bf16_f32 v26, v144, v145\n\t" \
  "v_cvt_pk_bf16_f32 v27, v146, v147\n\t" \
  "v_cvt_pk_bf16_f32 v36, v148, v149\n\t" \
  "v_cvt_pk_bf16_f32 v37, v150, v151\n\t" \
  "v_mfma_f32_16x16x32_bf16 v[136:139], v[96:99], v[72:75], v[56:59]\n\t" \
  "v_cvt_pk_bf16_f32 v112, v112, v113\n\t" \
  "v_cvt_pk_bf16_f32 v113, v114, v115\n\t" \
  "v_mfma_f32_16x16x32_bf16 v[140:143], v[104:107], v[72:75], v[52:55]\n\t" \
  "v_cvt_pk_bf16_f32 v114, v116, v117\n\t" \
  "v_cvt_pk_bf16_f32 v115, v118, v119\n\t" \
  "v_cvt_pk_bf16_f32 v80, v80, v81\n\t" \
  "v_cvt_pk_bf16_f32 v81, v82, v83\n\t" \
  "v_cvt_pk_bf16_f32 v82, v84, v85\n\t" \
  "v_cvt_pk_bf16_f32 v83, v86, v87\n\t" \
  "v_cvt_pk_bf16_f32 v22, v128, v129\n\t" \
  "v_cvt_pk_bf16_f32 v23, v130, v131\n\t" \
  "v_mfma_f32_16x16x32_bf16 v[144:147], v[104:107], v[80:83], v[56:59]\n\t" \
  "v_cvt_pk_bf16_f32 v32, v132, v133\n\t" \
  "v_cvt_pk_bf16_f32 v33, v134, v135\n\t" \
  "v_mfma_f32_16x16x32_bf16 v[148:151], v[112:115], v[80:83], v[52:55]\n\t" \
  "v_cvt_pk_bf16_f32 v88, v88, v89\n\t" \
  "v_cvt_pk_bf16_f32 v89, v90, v91\n\t" \
  "v_cvt_pk_bf16_f32 v90, v92, v93\n\t" \
  "v_cvt_pk_bf16_f32 v91, v94, v95\n\t" \
  "v_mov_b32 v61, %[fp]\n\t" \
  "v_add_u32 v60, %[vo], v63\n\t" \
  "v_mfma_f32_16x16x32_bf16 v[152:155], v[112:115], v[88:91], v[56:59]\n\t" \
  "ds_read_b32 v0, v61\n\t" \
  "ds_read_b64 v[20:21], v60\n\t" \
  "ds_read_b64 v[30:31], v60 offset:8\n\t" \
  "s_mov_b64 exec, %[rm]\n\t" \
  "ds_read_b128 v[160:163], v63\n\t" \
  "s_mov_b64 exec, -1\n\t" \
  "v_mfma_f32_16x16x32_bf16 v[156:159], v[120:123], v[88:91], v[52:55]\n\t" \
  "v_cvt_pk_bf16_f32 v64, v64, v65\n\t" \
  "v_cvt_pk_bf16_f32 v65, v66, v67\n\t" \
  "v_cvt_pk_bf16_f32 v66, v68, v69\n\t" \
  "v_cvt_pk_bf16_f32 v67, v70, v71\n\t" \
  "v_max_f32 v136, v136, v137\n\t" \
  "v_max_f32 v138, v138, v139\n\t" \
  "v_mfma_f32_16x16x32_bf16 v[132:135], v[96:99], v[64:67], v[52:55]\n\t" \
  "v_max_f32 v140, v140, v141\n\t" \
  "v_max_f32 v142, v142, v143\n\t" \
  "AW_%=:\n\t" \
  "s_waitcnt lgkmcnt(0)\n\t" \
  "v_cmp_eq_u32 vcc, 0, v0\n\t" \
  "v_max3_f32 v5, v136, v138, v140\n\t" \
  "v_max_f32 v5, v5, v142\n\t" \
  "s_cbranch_vccnz AR_%=\n\t" \
  "s_cmp_eq_u32 %[z], 0\n\t" \
  "s_cbranch_scc1 AZ_%=\n\t" \
  "AY_%=:\n\t" \
  "v_mfma_f32_16x16x32_bf16 v[128:131], v[160:163], v[64:67], v[56:59]\n\t" \
  "v_max_f32 v144, v144, v145\n\t" \
  "v_max_f32 v146, v146, v147\n\t" \
  "v_max_f32 v148, v148, v149\n\t" \
  "v_max_f32 v150, v150, v151\n\t" \
  "v_max3_f32 v6, v144, v146, v148\n\t" \
  "v_max_f32 v6, v6, v150\n\t" \
  "v_max_f32 v152, v152, v153\n\t" \
  "v_max_f32 v154, v154, v155\n\t" \
  "v_max_f32 v156, v156, v157\n\t" \
  "v_max_f32 v158, v158, v159\n\t" \
  "v_max3_f32 v7, v152, v154, v156\n\t" \
  "v_max_f32 v7, v7, v158\n\t" \
  "v_mov_b32 v8, 0xc0c0100\n\t" \
  "v_mov_b32 v9, 0x1000c0c\n\t" \
  "v_max_f32 v128, v128, v129\n\t" \
  "v_max_f32 v130, v130, v131\n\t" \
  "v_max_f32 v132, v132, v133\n\t" \
  "v_max_f32 v134, v134, v135\n\t" \
  "v_max3_f32 v4, v128, v130, v132\n\t" \
  "v_max_f32 v4, v4, v134\n\t" \
  "v_cndmask_b32_e64 v60, v8, v9, s[84:85]\n\t" \
  "v_mov_b32 v8, 0xc0c0302\n\t" \
  "v_permlane16_swap_b32 v6, v7\n\t" \
  "v_permlane16_swap_b32 v4, v5\n\t" \
  "v_max_f32 v6, v6, v7\n\t" \
  "v_max_f32 v4, v4, v5\n\t" \
  "v_mov_b32 v9, 0x3020c0c\n\t" \
  "v_cndmask_b32_e64 v61, v8, v9, s[84:85]\n\t" \
  "v_permlane32_swap_b32 v4, v6\n\t" \
  "v_max_f32 v4, v4, v6\n\t" \
  "v_mul_f32 v6, 0xbe8293ee, v4\n\t" \
  "v_mul_f32 v4, 0xbe8293ee, v4\n\t" \
  "s_nop 1\n\t" \
  "v_permlane32_swap_b32 v4, v6\n\t" \
  "v_mov_b32 v5, v4\n\t" \
  "v_mov_b32 v7, v6\n\t" \
  "s_nop 0\n\t" \
  "v_permlane16_swap_b32 v4, v5\n\t" \
  "v_permlane16_swap_b32 v6, v7\n\t" \
  "v_fmamk_f32 v128, v128, 0x3e8293ee, v4\n\t" \
  "v_fmamk_f32 v130, v130, 0x3e8293ee, v4\n\t" \
  "v_fmamk_f32 v132, v132, 0x3e8293ee, v4\n\t" \
  "v_fmamk_f32 v134, v134, 0x3e8293ee, v4\n\t" \
  "v_fmamk_f32 v136, v136, 0x3e8293ee, v5\n\t" \
  "v_fmamk_f32 v138, v138, 0x3e8293ee, v5\n\t" \
  "v_fmamk_f32 v140, v140, 0x3e8293ee, v5\n\t" \
  "v_fmamk_f32 v142, v142, 0x3e8293ee, v5\n\t" \
  "v_fmamk_f32 v144, v144, 0x3e8293ee, v6\n\t" \
  "v_fmamk_f32 v146, v146, 0x3e8293ee, v6\n\t" \
  "v_fmamk_f32 v148, v148, 0x3e8293ee, v6\n\t" \
  "v_fmamk_f32 v150, v150, 0x3e8293ee, v6\n\t" \
  "v_fmamk_f32 v152, v152, 0x3e8293ee, v7\n\t" \
  "v_fmamk_f32 v154, v154, 0x3e8293ee, v7\n\t" \
  "v_fmamk_f32 v156, v156, 0x3e8293ee, v7\n\t" \
  "v_fmamk_f32 v158, v158, 0x3e8293ee, v7\n\t" \
  "v_exp_f32 v128, v128\n\t" \
  "v_exp_f32 v130, v130\n\t" \
  "v_exp_f32 v132, v132\n\t" \
  "v_exp_f32 v134, v134\n\t" \
  "v_exp_f32 v136, v136\n\t" \
  "v_exp_f32 v138, v138\n\t" \
  "v_exp_f32 v140, v140\n\t" \
  "v_exp_f32 v142, v142\n\t" \
  "v_exp_f32 v144, v144\n\t" \
  "v_exp_f32 v146, v146\n\t" \
  "v_exp_f32 v148, v148\n\t" \
  "v_exp_f32 v150, v150\n\t" \
  "v_exp_f32 v152, v152\n\t" \
  "v_exp_f32 v154, v154\n\t" \
  "v_exp_f32 v156, v156\n\t" \
  "v_exp_f32 v158, v158\n\t" \
  "v_add_f32 v4, v128, v130\n\t" \
  "v_add_f32 v8, v132, v134\n\t" \
  "v_add_f32 v5, v136, v138\n\t" \
  "v_add_f32 v9, v140, v142\n\t" \
  "v_add_f32 v6, v144, v146\n\t" \
  "v_add_f32 v10, v148, v150\n\t" \
  "v_add_f32 v7, v152, v154\n\t" \
  "v_add_f32 v11, v156, v158\n\t" \
  "v_add_f32 v4, v4, v8\n\t" \
  "v_add_f32 v5, v5, v9\n\t" \
  "v_add_f32 v6, v6, v10\n\t" \
  "v_add_f32 v7, v7, v11\n\t" \
  "v_permlane16_swap_b32 v4, v5\n\t" \
  "s_nop 0\n\t" \
  "v_permlane16_swap_b32 v6, v7\n\t" \
  "v_add_f32 v4, v4, v5\n\t" \
  "v_add_f32 v6, v6, v7\n\t" \
  "s_nop 1\n\t" \
  "v_permlane32_swap_b32 v4, v6\n\t" \
  "v_add_f32 v4, v4, v6\n\t" \
  "v_rcp_f32 v4, v4\n\t" \
  "s_nop 0\n\t" \
  "v_mov_b32 v6, v4\n\t" \
  "s_nop 1\n\t" \
  "v_permlane32_swap_b32 v4, v6\n\t" \
  "v_mov_b32 v5, v4\n\t" \
  "v_mov_b32 v7, v6\n\t" \
  "s_nop 0\n\t" \
  "v_permlane16_swap_b32 v4, v5\n\t" \
  "v_permlane16_swap_b32 v6, v7\n\t" \
  "v_mul_f32 v128, v128, v4\n\t" \
  "v_mul_f32 v130, v130, v4\n\t" \
  "v_mul_f32 v132, v132, v4\n\t" \
  "v_mul_f32 v134, v134, v4\n\t" \
  "v_cvt_pk_bf16_f32 v128, v128, v130\n\t" \
  "v_cvt_pk_bf16_f32 v132, v132, v134\n\t" \
  "v_perm_b32 v68, 0, v128, v60\n\t" \
  "v_perm_b32 v69, 0, v128, v61\n\t" \
  "v_perm_b32 v70, 0, v132, v60\n\t" \
  "v_perm_b32 v71, 0, v132, v61\n\t" \
  "v_mul_f32 v136, v136, v5\n\t" \
  "v_mul_f32 v138, v138, v5\n\t" \
  "v_mfma_f32_16x16x32_bf16 v[128:131], v[20:23], v[68:71], 0\n\t" \
  "v_mul_f32 v140, v140, v5\n\t" \
  "v_mul_f32 v142, v142, v5\n\t" \
  "v_cvt_pk_bf16_f32 v136, v136, v138\n\t" \
  "v_mfma_f32_16x16x32_bf16 v[132:135], v[30:33], v[68:71], 0\n\t" \
  "v_cvt_pk_bf16_f32 v140, v140, v142\n\t" \
  "v_perm_b32 v76, 0, v136, v60\n\t" \
  "v_perm_b32 v77, 0, v136, v61\n\t" \
  "v_perm_b32 v78, 0, v140, v60\n\t" \
  "v_perm_b32 v79, 0, v140, v61\n\t" \
  "v_mul_f32 v144, v144, v6\n\t" \
  "v_mul_f32 v146, v146, v6\n\t" \
  "v_mfma_f32_16x16x32_bf16 v[136:139], v[22:25], v[76:79], 0\n\t" \
  "v_mul_f32 v148, v148, v6\n\t" \
  "v_mul_f32 v150, v150, v6\n\t" \
  "v_cvt_pk_bf16_f32 v144, v144, v146\n\t" \
  "v_mfma_f32_16x16x32_bf16 v[140:143], v[32:35], v[76:79], 0\n\t" \
  "v_cvt_pk_bf16_f32 v148, v148, v150\n\t" \
  "v_perm_b32 v84, 0, v144, v60\n\t" \
  "v_perm_b32 v85, 0, v144, v61\n\t" \
  "v_perm_b32 v86, 0, v148, v60\n\t" \
  "v_perm_b32 v87, 0, v148, v61\n\t" \
  "v_cvt_pk_bf16_f32 v100, v128, v129\n\t" \
  "v_cvt_pk_bf16_f32 v101, v130, v131\n\t" \
  "v_mfma_f32_16x16x32_bf16 v[144:147], v[24:27], v[84:87], 0\n\t" \
  "v_cvt_pk_bf16_f32 v102, v132, v133\n\t" \
  "v_cvt_pk_bf16_f32 v103, v134, v135\n\t" \
  "global_store_dwordx4 v62, v[100:103], %[ot] offset:0 sc1\n\t" \
  "v_mfma_f32_16x16x32_bf16 v[148:151], v[34:37], v[84:87], 0\n\t" \
  "v_mul_f32 v152, v152, v7\n\t" \
  "v_mul_f32 v154, v154, v7\n\t" \
  "v_mul_f32 v156, v156, v7\n\t" \
  "v_mul_f32 v158, v158, v7\n\t" \
  "v_cvt_pk_bf16_f32 v152, v152, v154\n\t" \
  "v_cvt_pk_bf16_f32 v156, v156, v158\n\t" \
  "v_perm_b32 v92, 0, v152, v60\n\t" \
  "v_perm_b32 v93, 0, v152, v61\n\t" \
  "v_perm_b32 v94, 0, v156, v60\n\t" \
  "v_perm_b32 v95, 0, v156, v61\n\t" \
  "v_cvt_pk_bf16_f32 v108, v136, v137\n\t" \
  "v_cvt_pk_bf16_f32 v109, v138, v139\n\t" \
  "v_mfma_f32_16x16x32_bf16 v[152:155], v[26:29], v[92:95], 0\n\t" \
  "v_cvt_pk_bf16_f32 v110, v140, v141\n\t" \
  "v_cvt_pk_bf16_f32 v111, v142, v143\n\t" \
  "global_store_dwordx4 v62, v[108:111], %[ot] offset:1024 sc1\n\t" \
  "v_mfma_f32_16x16x32_bf16 v[156:159], v[36:39], v[92:95], 0\n\t" \
  "v_cvt_pk_bf16_f32 v116, v144, v145\n\t" \
  "v_cvt_pk_bf16_f32 v117, v146, v147\n\t" \
  "v_cvt_pk_bf16_f32 v118, v148, v149\n\t" \
  "v_cvt_pk_bf16_f32 v119, v150, v151\n\t" \
  "global_store_dwordx4 v62, v[116:119], %[ou] offset:0 sc1\n\t" \
  "v_cvt_pk_bf16_f32 v124, v152, v153\n\t" \
  "v_cvt_pk_bf16_f32 v125, v154, v155\n\t" \
  "s_nop 0\n\t" \
  "v_cvt_pk_bf16_f32 v126, v156, v157\n\t" \
  "v_cvt_pk_bf16_f32 v127, v158, v159\n\t" \
  "global_store_dwordx4 v62, v[124:127], %[ou] offset:1024 sc1\n\t" \
  "s_branch AE_%=\n\t" \
  "AR_%=:\n\t" \
  "s_addk_i32 %[it], 1\n\t" \
  "s_cmp_gt_u32 %[it], 0x400000\n\t" \
  "s_cbranch_scc1 AY_%=\n\t" \
  "ds_read_b32 v0, v61\n\t" \
  "ds_read_b64 v[20:21], v60\n\t" \
  "ds_read_b64 v[30:31], v60 offset:8\n\t" \
  "s_mov_b64 exec, %[rm]\n\t" \
  "ds_read_b128 v[160:163], v63\n\t" \
  "s_mov_b64 exec, -1\n\t" \
  "s_branch AW_%=\n\t" \
  "AZ_%=:\n\t" \
  "v_mov_b32 v0, 0xff61b1e6\n\t" \
  "v_mov_b32 v1, 0xcfa8965f\n\t" \
  "s_nop 3\n\t" \
  "v_cndmask_b32_e64 v56, v0, v1, s[68:69]\n\t" \
  "v_cndmask_b32_e64 v57, v0, v1, s[70:71]\n\t" \
  "v_cndmask_b32_e64 v58, v0, v1, s[72:73]\n\t" \
  "v_cndmask_b32_e64 v59, v0, v1, s[74:75]\n\t" \
  "s_nop 1\n\t" \
  "s_branch AY_%=\n\t" \
  "AE_%=:\n\t" \
  "s_nop 1\n\t"
#define N2_CLOB "v0", "v1", "v2", "v3", "v4", "v5", "v6", "v7", "v8", "v9", "v10", "v11", "v12", "v13", "v14", "v15", "v16", "v17", "v18", "v19", "v20", "v21", "v22", "v23", "v24", "v25", "v26", "v27", "v28", "v29", "v30", "v31", "v32", "v33", "v34", "v35", "v36", "v37", "v38", "v39", "v40", "v41", "v42", "v43", "v44", "v45", "v46", "v47", "v48", "v49", "v50", "v51", "v52", "v53", "v54", "v55", "v60", "v61", "v64", "v65", "v66", "v67", "v68", "v69", "v70", "v71", "v72", "v73", "v74", "v75", "v76", "v77", "v78", "v79", "v80", "v81", "v82", "v83", "v84", "v85", "v86", "v87", "v88", "v89", "v90", "v91", "v92", "v93", "v94", "v95", "v96", "v97", "v98", "v99", "v100", "v101", "v102", "v103", "v104", "v105", "v106", "v107", "v108", "v109", "v110", "v111", "v112", "v113", "v114", "v115", "v116", "v117", "v118", "v119", "v120", "v121", "v122", "v123", "v124", "v125", "v126", "v127", "v128", "v129", "v130", "v131", "v132", "v133", "v134", "v135", "v136", "v137", "v138", "v139", "v140", "v141", "v142", "v143", "v144", "v145", "v146", "v147", "v148", "v149", "v150", "v151", "v152", "v153", "v154", "v155", "v156", "v157", "v158", "v159", "v160", "v161", "v162", "v163", "v164", "v165", "v166", "v167", "s68", "s69", "s70", "s71", "s72", "s73", "s74", "s75", "s76", "s77", "s78", "s79", "s80", "s81", "s82", "s83", "s84", "s85"

#define CV_ASM \
  "ds_read_b64 v[0:1], v28 offset:0\n\t" \
  "ds_read_b64 v[2:3], v29 offset:0\n\t" \
  "ds_read_b64 v[4:5], v30 offset:256\n\t" \
  "ds_read_b64 v[6:7], v31 offset:256\n\t" \
  "ds_read_b64 v[8:9], v24 offset:512\n\t" \
  "ds_read_b64 v[10:11], v25 offset:512\n\t" \
  "ds_read_b64 v[12:13], v26 offset:768\n\t" \
  "ds_read_b64 v[14:15], v27 offset:768\n\t" \
  "ds_read_b64 v[16:17], v28 offset:1024\n\t" \
  "ds_read_b64 v[18:19], v29 offset:1024\n\t" \
  "ds_read_b64 v[20:21], v30 offset:1280\n\t" \
  "ds_read_b64 v[22:23], v31 offset:1280\n\t" \
  "s_waitcnt lgkmcnt(10)\n\t" \
  "v_mfma_f32_16x16x32_bf16 v[128:131], v[68:71], v[0:3], 0\n\t" \
  "v_mfma_f32_16x16x32_bf16 v[132:135], v[76:79], v[0:3], 0\n\t" \
  "v_mfma_f32_16x16x32_bf16 v[136:139], v[84:87], v[0:3], 0\n\t" \
  "v_mfma_f32_16x16x32_bf16 v[140:143], v[92:95], v[0:3], 0\n\t" \
  "v_mfma_f32_16x16x32_bf16 v[144:147], v[100:103], v[0:3], 0\n\t" \
  "v_mfma_f32_16x16x32_bf16 v[148:151], v[108:111], v[0:3], 0\n\t" \
  "v_mfma_f32_16x16x32_bf16 v[152:155], v[116:119], v[0:3], 0\n\t" \
  "v_mfma_f32_16x16x32_bf16 v[156:159], v[124:127], v[0:3], 0\n\t" \
  "ds_read_b64 v[0:1], v24 offset:1536\n\t" \
  "s_waitcnt lgkmcnt(9)\n\t" \
  "v_mfma_f32_16x16x32_bf16 v[128:131], v[64:67], v[4:7], v[128:131]\n\t" \
  "ds_read_b64 v[2:3], v25 offset:1536\n\t" \
  "v_mfma_f32_16x16x32_bf16 v[132:135], v[72:75], v[4:7], v[132:135]\n\t" \
  "v_mfma_f32_16x16x32_bf16 v[136:139], v[80:83], v[4:7], v[136:139]\n\t" \
  "v_mfma_f32_16x16x32_bf16 v[140:143], v[88:91], v[4:7], v[140:143]\n\t" \
  "v_mfma_f32_16x16x32_bf16 v[144:147], v[96:99], v[4:7], v[144:147]\n\t" \
  "v_mfma_f32_16x16x32_bf16 v[148:151], v[104:107], v[4:7], v[148:151]\n\t" \
  "v_mfma_f32_16x16x32_bf16 v[152:155], v[112:115], v[4:7], v[152:155]\n\t" \
  "v_mfma_f32_16x16x32_bf16 v[156:159], v[120:123], v[4:7], v[156:159]\n\t" \
  "ds_read_b64 v[4:5], v26 offset:1792\n\t" \
  "ds_bpermute_b32 v34, v32, v131\n\t" \
  "v_readlane_b32 %[p0], v131, 63\n\t" \
  "s_waitcnt lgkmcnt(10)\n\t" \
  "v_mfma_f32_16x16x32_bf16 v[132:135], v[68:71], v[8:11], v[132:135]\n\t" \
  "ds_read_b64 v[6:7], v27 offset:1792\n\t" \
  "v_mov_b32 v131, v130\n\t" \
  "v_mfma_f32_16x16x32_bf16 v[136:139], v[76:79], v[8:11], v[136:139]\n\t" \
  "v_mov_b32 v130, v129\n\t" \
  "v_mfma_f32_16x16x32_bf16 v[140:143], v[84:87], v[8:11], v[140:143]\n\t" \
  "v_mov_b32 v129, v128\n\t" \
  "v_mfma_f32_16x16x32_bf16 v[144:147], v[92:95], v[8:11], v[144:147]\n\t" \
  "v_mfma_f32_16x16x32_bf16 v[148:151], v[100:103], v[8:11], v[148:151]\n\t" \
  "v_mfma_f32_16x16x32_bf16 v[152:155], v[108:111], v[8:11], v[152:155]\n\t" \
  "v_mfma_f32_16x16x32_bf16 v[156:159], v[116:119], v[8:11], v[156:159]\n\t" \
  "ds_read_b64 v[8:9], v28 offset:2048\n\t" \
  "s_waitcnt lgkmcnt(10)\n\t" \
  "v_mfma_f32_16x16x32_bf16 v[132:135], v[64:67], v[12:15], v[132:135]\n\t" \
  "ds_read_b64 v[10:11], v29 offset:2048\n\t" \
  "v_mfma_f32_16x16x32_bf16 v[136:139], v[72:75], v[12:15], v[136:139]\n\t" \
  "v_mfma_f32_16x16x32_bf16 v[140:143], v[80:83], v[12:15], v[140:143]\n\t" \
  "v_mfma_f32_16x16x32_bf16 v[144:147], v[88:91], v[12:15], v[144:147]\n\t" \
  "v_mfma_f32_16x16x32_bf16 v[148:151], v[96:99], v[12:15], v[148:151]\n\t" \
  "s_waitcnt lgkmcnt(3)\n\t" \
  "v_mov_b32 v128, v34\n\t" \
  "v_writelane_b32 v128, 0, 0\n\t" \
  "v_mfma_f32_16x16x32_bf16 v[152:155], v[104:107], v[12:15], v[152:155]\n\t" \
  "v_mfma_f32_16x16x32_bf16 v[156:159], v[112:115], v[12:15], v[156:159]\n\t" \
  "ds_read_b64 v[12:13], v30 offset:2304\n\t" \
  "s_waitcnt lgkmcnt(10)\n\t" \
  "v_mfma_f32_16x16x32_bf16 v[136:139], v[68:71], v[16:19], v[136:139]\n\t" \
  "ds_read_b64 v[14:15], v31 offset:2304\n\t" \
  "ds_bpermute_b32 v35, v32, v135\n\t" \
  "v_readlane_b32 %[p1], v135, 63\n\t" \
  "v_mfma_f32_16x16x32_bf16 v[140:143], v[76:79], v[16:19], v[140:143]\n\t" \
  "v_mov_b32 v135, v134\n\t" \
  "v_mfma_f32_16x16x32_bf16 v[144:147], v[84:87], v[16:19], v[144:147]\n\t" \
  "v_mov_b32 v134, v133\n\t" \
  "v_mfma_f32_16x16x32_bf16 v[148:151], v[92:95], v[16:19], v[148:151]\n\t" \
  "v_mov_b32 v133, v132\n\t" \
  "v_mfma_f32_16x16x32_bf16 v[152:155], v[100:103], v[16:19], v[152:155]\n\t" \
  "v_mfma_f32_16x16x32_bf16 v[156:159], v[108:111], v[16:19], v[156:159]\n\t" \
  "ds_read_b64 v[16:17], v24 offset:2560\n\t" \
  "s_waitcnt lgkmcnt(11)\n\t" \
  "v_mfma_f32_16x16x32_bf16 v[136:139], v[64:67], v[20:23], v[136:139]\n\t" \
  "ds_read_b64 v[18:19], v25 offset:2560\n\t" \
  "v_mfma_f32_16x16x32_bf16 v[140:143], v[72:75], v[20:23], v[140:143]\n\t" \
  "v_mfma_f32_16x16x32_bf16 v[144:147], v[80:83], v[20:23], v[144:147]\n\t" \
  "v_mfma_f32_16x16x32_bf16 v[148:151], v[88:91], v[20:23], v[148:151]\n\t" \
  "v_mfma_f32_16x16x32_bf16 v[152:155], v[96:99], v[20:23], v[152:155]\n\t" \
  "v_mfma_f32_16x16x32_bf16 v[156:159], v[104:107], v[20:23], v[156:159]\n\t" \
  "ds_read_b64 v[20:21], v26 offset:2816\n\t" \
  "s_waitcnt lgkmcnt(11)\n\t" \
  "v_mfma_f32_16x16x32_bf16 v[140:143], v[68:71], v[0:3], v[140:143]\n\t" \
  "ds_read_b64 v[22:23], v27 offset:2816\n\t" \
  "s_waitcnt lgkmcnt(4)\n\t" \
  "v_mov_b32 v132, v35\n\t" \
  "v_writelane_b32 v132, %[p0], 0\n\t" \
  "v_mfma_f32_16x16x32_bf16 v[144:147], v[76:79], v[0:3], v[144:147]\n\t" \
  "ds_bpermute_b32 v36, v32, v139\n\t" \
  "v_readlane_b32 %[p2], v139, 63\n\t" \
  "v_mfma_f32_16x16x32_bf16 v[148:151], v[84:87], v[0:3], v[148:151]\n\t" \
  "v_mov_b32 v139, v138\n\t" \
  "v_mfma_f32_16x16x32_bf16 v[152:155], v[92:95], v[0:3], v[152:155]\n\t" \
  "v_mov_b32 v138, v137\n\t" \
  "v_mfma_f32_16x16x32_bf16 v[156:159], v[100:103], v[0:3], v[156:159]\n\t" \
  "ds_read_b64 v[0:1], v28 offset:3072\n\t" \
  "v_mov_b32 v137, v136\n\t" \
  "s_waitcnt lgkmcnt(11)\n\t" \
  "v_mfma_f32_16x16x32_bf16 v[140:143], v[64:67], v[4:7], v[140:143]\n\t" \
  "ds_read_b64 v[2:3], v29 offset:3072\n\t" \
  "v_mfma_f32_16x16x32_bf16 v[144:147], v[72:75], v[4:7], v[144:147]\n\t" \
  "v_mfma_f32_16x16x32_bf16 v[148:151], v[80:83], v[4:7], v[148:151]\n\t" \
  "v_mfma_f32_16x16x32_bf16 v[152:155], v[88:91], v[4:7], v[152:155]\n\t" \
  "v_mfma_f32_16x16x32_bf16 v[156:159], v[96:99], v[4:7], v[156:159]\n\t" \
  "ds_read_b64 v[4:5], v30 offset:3328\n\t" \
  "s_waitcnt lgkmcnt(11)\n\t" \
  "v_mfma_f32_16x16x32_bf16 v[144:147], v[68:71], v[8:11], v[144:147]\n\t" \
  "ds_read_b64 v[6:7], v31 offset:3328\n\t" \
  "v_mfma_f32_16x16x32_bf16 v[148:151], v[76:79], v[8:11], v[148:151]\n\t" \
  "v_mfma_f32_16x16x32_bf16 v[152:155], v[84:87], v[8:11], v[152:155]\n\t" \
  "ds_bpermute_b32 v37, v32, v143\n\t" \
  "v_readlane_b32 %[p3], v143, 63\n\t" \
  "v_mfma_f32_16x16x32_bf16 v[156:159], v[92:95], v[8:11], v[156:159]\n\t" \
  "ds_read_b64 v[8:9], v24 offset:3584\n\t" \
  "s_waitcnt lgkmcnt(6)\n\t" \
  "v_mov_b32 v136, v36\n\t" \
  "v_writelane_b32 v136, %[p1], 0\n\t" \
  "v_mov_b32 v143, v142\n\t" \
  "s_waitcnt lgkmcnt(12)\n\t" \
  "v_mfma_f32_16x16x32_bf16 v[144:147], v[64:67], v[12:15], v[144:147]\n\t" \
  "ds_read_b64 v[10:11], v25 offset:3584\n\t" \
  "v_mov_b32 v142, v141\n\t" \
  "v_mfma_f32_16x16x32_bf16 v[148:151], v[72:75], v[12:15], v[148:151]\n\t" \
  "v_mov_b32 v141, v140\n\t" \
  "v_mfma_f32_16x16x32_bf16 v[152:155], v[80:83], v[12:15], v[152:155]\n\t" \
  "v_mfma_f32_16x16x32_bf16 v[156:159], v[88:91], v[12:15], v[156:159]\n\t" \
  "ds_read_b64 v[12:13], v26 offset:3840\n\t" \
  "s_waitcnt lgkmcnt(11)\n\t" \
  "v_mfma_f32_16x16x32_bf16 v[148:151], v[68:71], v[16:19], v[148:151]\n\t" \
  "ds_read_b64 v[14:15], v27 offset:3840\n\t" \
  "v_mfma_f32_16x16x32_bf16 v[152:155], v[76:79], v[16:19], v[152:155]\n\t" \
  "v_mfma_f32_16x16x32_bf16 v[156:159], v[84:87], v[16:19], v[156:159]\n\t" \
  "ds_read_b64 v[16:17], v26 offset:0\n\t" \
  "s_waitcnt lgkmcnt(11)\n\t" \
  "v_mfma_f32_16x16x32_bf16 v[148:151], v[64:67], v[20:23], v[148:151]\n\t" \
  "ds_read_b64 v[18:19], v27 offset:0\n\t" \
  "ds_bpermute_b32 v38, v32, v147\n\t" \
  "v_readlane_b32 %[p4], v147, 63\n\t" \
  "v_mfma_f32_16x16x32_bf16 v[152:155], v[72:75], v[20:23], v[152:155]\n\t" \
  "v_mov_b32 v147, v146\n\t" \
  "v_mfma_f32_16x16x32_bf16 v[156:159], v[80:83], v[20:23], v[156:159]\n\t" \
  "ds_read_b64 v[20:21], v28 offset:256\n\t" \
  "v_mov_b32 v146, v145\n\t" \
  "s_waitcnt lgkmcnt(11)\n\t" \
  "v_mfma_f32_16x16x32_bf16 v[152:155], v[68:71], v[0:3], v[152:155]\n\t" \
  "ds_read_b64 v[22:23], v29 offset:256\n\t" \
  "s_waitcnt lgkmcnt(9)\n\t" \
  "v_mov_b32 v140, v37\n\t" \
  "v_writelane_b32 v140, %[p2], 0\n\t" \
  "v_mov_b32 v145, v144\n\t" \
  "v_mfma_f32_16x16x32_bf16 v[156:159], v[76:79], v[0:3], v[156:159]\n\t" \
  "ds_read_b64 v[0:1], v30 offset:512\n\t" \
  "ds_bpermute_b32 v39, v32, v151\n\t" \
  "v_readlane_b32 %[p5], v151, 63\n\t" \
  "s_waitcnt lgkmcnt(12)\n\t" \
  "v_mfma_f32_16x16x32_bf16 v[152:155], v[64:67], v[4:7], v[152:155]\n\t" \
  "ds_read_b64 v[2:3], v31 offset:512\n\t" \
  "v_mov_b32 v151, v150\n\t" \
  "v_mfma_f32_16x16x32_bf16 v[156:159], v[72:75], v[4:7], v[156:159]\n\t" \
  "ds_read_b64 v[4:5], v24 offset:768\n\t" \
  "v_mov_b32 v150, v149\n\t" \
  "s_waitcnt lgkmcnt(11)\n\t" \
  "v_mfma_f32_16x16x32_bf16 v[156:159], v[68:71], v[8:11], v[156:159]\n\t" \
  "ds_read_b64 v[6:7], v25 offset:768\n\t" \
  "ds_read_b64 v[8:9], v26 offset:1024\n\t" \
  "v_mov_b32 v149, v148\n\t" \
  "ds_bpermute_b32 v40, v32, v155\n\t" \
  "v_readlane_b32 %[p6], v155, 63\n\t" \
  "s_waitcnt lgkmcnt(12)\n\t" \
  "v_mfma_f32_16x16x32_bf16 v[156:159], v[64:67], v[12:15], v[156:159]\n\t" \
  "ds_read_b64 v[10:11], v27 offset:1024\n\t" \
  "ds_read_b64 v[12:13], v28 offset:1280\n\t" \
  "v_mov_b32 v155, v154\n\t" \
  "s_waitcnt lgkmcnt(12)\n\t" \
  "v_mfma_f32_16x16x32_bf16 v[128:131], v[68:71], v[16:19], v[128:131]\n\t" \
  "ds_read_b64 v[14:15], v29 offset:1280\n\t" \
  "v_mov_b32 v154, v153\n\t" \
  "v_mfma_f32_16x16x32_bf16 v[132:135], v[76:79], v[16:19], v[132:135]\n\t" \
  "s_waitcnt lgkmcnt(12)\n\t" \
  "v_mov_b32 v144, v38\n\t" \
  "v_writelane_b32 v144, %[p3], 0\n\t" \
  "v_mov_b32 v153, v152\n\t" \
  "v_mfma_f32_16x16x32_bf16 v[136:139], v[84:87], v[16:19], v[136:139]\n\t" \
  "s_waitcnt lgkmcnt(8)\n\t" \
  "v_mov_b32 v148, v39\n\t" \
  "v_writelane_b32 v148, %[p4], 0\n\t" \
  "v_mfma_f32_16x16x32_bf16 v[140:143], v[92:95], v[16:19], v[140:143]\n\t" \
  "s_waitcnt lgkmcnt(3)\n\t" \
  "v_mov_b32 v152, v40\n\t" \
  "v_writelane_b32 v152, %[p5], 0\n\t" \
  "v_mfma_f32_16x16x32_bf16 v[144:147], v[100:103], v[16:19], v[144:147]\n\t" \
  "ds_bpermute_b32 v41, v32, v159\n\t" \
  "v_mfma_f32_16x16x32_bf16 v[148:151], v[108:111], v[16:19], v[148:151]\n\t" \
  "v_mov_b32 v159, v158\n\t" \
  "v_mfma_f32_16x16x32_bf16 v[152:155], v[116:119], v[16:19], v[152:155]\n\t" \
  "v_mov_b32 v158, v157\n\t" \
  "s_waitcnt lgkmcnt(11)\n\t" \
  "v_mfma_f32_16x16x32_bf16 v[128:131], v[64:67], v[20:23], v[128:131]\n\t" \
  "v_mov_b32 v157, v156\n\t" \
  "v_mfma_f32_16x16x32_bf16 v[132:135], v[72:75], v[20:23], v[132:135]\n\t" \
  "v_mfma_f32_16x16x32_bf16 v[136:139], v[80:83], v[20:23], v[136:139]\n\t" \
  "v_mfma_f32_16x16x32_bf16 v[140:143], v[88:91], v[20:23], v[140:143]\n\t" \
  "v_mfma_f32_16x16x32_bf16 v[144:147], v[96:99], v[20:23], v[144:147]\n\t" \
  "s_waitcnt lgkmcnt(0)\n\t" \
  "v_mov_b32 v156, v41\n\t" \
  "v_writelane_b32 v156, %[p6], 0\n\t" \
  "v_mfma_f32_16x16x32_bf16 v[148:151], v[104:107], v[20:23], v[148:151]\n\t" \
  "v_mfma_f32_16x16x32_bf16 v[152:155], v[112:115], v[20:23], v[152:155]\n\t" \
  "v_mfma_f32_16x16x32_bf16 v[156:159], v[124:127], v[16:19], v[156:159]\n\t" \
  "ds_read_b64 v[16:17], v30 offset:1536\n\t" \
  "ds_bpermute_b32 v34, v32, v131\n\t" \
  "v_readlane_b32 %[p0], v131, 63\n\t" \
  "v_mfma_f32_16x16x32_bf16 v[156:159], v[120:123], v[20:23], v[156:159]\n\t" \
  "ds_read_b64 v[18:19], v31 offset:1536\n\t" \
  "ds_read_b64 v[20:21], v24 offset:1792\n\t" \
  "v_mov_b32 v131, v130\n\t" \
  "s_waitcnt lgkmcnt(12)\n\t" \
  "v_mfma_f32_16x16x32_bf16 v[132:135], v[68:71], v[0:3], v[132:135]\n\t" \
  "ds_read_b64 v[22:23], v25 offset:1792\n\t" \
  "v_mov_b32 v130, v129\n\t" \
  "v_mfma_f32_16x16x32_bf16 v[136:139], v[76:79], v[0:3], v[136:139]\n\t" \
  "v_mov_b32 v129, v128\n\t" \
  "v_mfma_f32_16x16x32_bf16 v[140:143], v[84:87], v[0:3], v[140:143]\n\t" \
  "v_mfma_f32_16x16x32_bf16 v[144:147], v[92:95], v[0:3], v[144:147]\n\t" \
  "v_mfma_f32_16x16x32_bf16 v[148:151], v[100:103], v[0:3], v[148:151]\n\t" \
  "v_mfma_f32_16x16x32_bf16 v[152:155], v[108:111], v[0:3], v[152:155]\n\t" \
  "v_mfma_f32_16x16x32_bf16 v[156:159], v[116:119], v[0:3], v[156:159]\n\t" \
  "ds_read_b64 v[0:1], v26 offset:2048\n\t" \
  "s_waitcnt lgkmcnt(12)\n\t" \
  "v_mfma_f32_16x16x32_bf16 v[132:135], v[64:67], v[4:7], v[132:135]\n\t" \
  "ds_read_b64 v[2:3], v27 offset:2048\n\t" \
  "v_mfma_f32_16x16x32_bf16 v[136:139], v[72:75], v[4:7], v[136:139]\n\t" \
  "v_mfma_f32_16x16x32_bf16 v[140:143], v[80:83], v[4:7], v[140:143]\n\t" \
  "v_mfma_f32_16x16x32_bf16 v[144:147], v[88:91], v[4:7], v[144:147]\n\t" \
  "s_waitcnt lgkmcnt(5)\n\t" \
  "v_mov_b32 v128, v34\n\t" \
  "v_writelane_b32 v128, 0, 0\n\t" \
  "v_mfma_f32_16x16x32_bf16 v[148:151], v[96:99], v[4:7], v[148:151]\n\t" \
  "v_mfma_f32_16x16x32_bf16 v[152:155], v[104:107], v[4:7], v[152:155]\n\t" \
  "v_mfma_f32_16x16x32_bf16 v[156:159], v[112:115], v[4:7], v[156:159]\n\t" \
  "ds_read_b64 v[4:5], v28 offset:2304\n\t" \
  "s_waitcnt lgkmcnt(11)\n\t" \
  "v_mfma_f32_16x16x32_bf16 v[136:139], v[68:71], v[8:11], v[136:139]\n\t" \
  "ds_read_b64 v[6:7], v29 offset:2304\n\t" \
  "ds_bpermute_b32 v35, v32, v135\n\t" \
  "v_readlane_b32 %[p1], v135, 63\n\t" \
  "v_mfma_f32_16x16x32_bf16 v[140:143], v[76:79], v[8:11], v[140:143]\n\t" \
  "v_mov_b32 v135, v134\n\t" \
  "v_mfma_f32_16x16x32_bf16 v[144:147], v[84:87], v[8:11], v[144:147]\n\t" \
  "v_mov_b32 v134, v133\n\t" \
  "v_mfma_f32_16x16x32_bf16 v[148:151], v[92:95], v[8:11], v[148:151]\n\t" \
  "v_mov_b32 v133, v132\n\t" \
  "v_mfma_f32_16x16x32_bf16 v[152:155], v[100:103], v[8:11], v[152:155]\n\t" \
  "v_mfma_f32_16x16x32_bf16 v[156:159], v[108:111], v[8:11], v[156:159]\n\t" \
  "ds_read_b64 v[8:9], v30 offset:2560\n\t" \
  "s_waitcnt lgkmcnt(12)\n\t" \
  "v_mfma_f32_16x16x32_bf16 v[136:139], v[64:67], v[12:15], v[136:139]\n\t" \
  "ds_read_b64 v[10:11], v31 offset:2560\n\t" \
  "v_mfma_f32_16x16x32_bf16 v[140:143], v[72:75], v[12:15], v[140:143]\n\t" \
  "v_mfma_f32_16x16x32_bf16 v[144:147], v[80:83], v[12:15], v[144:147]\n\t" \
  "v_mfma_f32_16x16x32_bf16 v[148:151], v[88:91], v[12:15], v[148:151]\n\t" \
  "v_mfma_f32_16x16x32_bf16 v[152:155], v[96:99], v[12:15], v[152:155]\n\t" \
  "v_mfma_f32_16x16x32_bf16 v[156:159], v[104:107], v[12:15], v[156:159]\n\t" \
  "ds_read_b64 v[12:13], v24 offset:2816\n\t" \
  "s_waitcnt lgkmcnt(10)\n\t" \
  "v_mfma_f32_16x16x32_bf16 v[140:143], v[68:71], v[16:19], v[140:143]\n\t" \
  "ds_read_b64 v[14:15], v25 offset:2816\n\t" \
  "s_waitcnt lgkmcnt(4)\n\t" \
  "v_mov_b32 v132, v35\n\t" \
  "v_writelane_b32 v132, %[p0], 0\n\t" \
  "v_mfma_f32_16x16x32_bf16 v[144:147], v[76:79], v[16:19], v[144:147]\n\t" \
  "ds_bpermute_b32 v36, v32, v139\n\t" \
  "v_readlane_b32 %[p2], v139, 63\n\t" \
  "v_mfma_f32_16x16x32_bf16 v[148:151], v[84:87], v[16:19], v[148:151]\n\t" \
  "v_mov_b32 v139, v138\n\t" \
  "v_mfma_f32_16x16x32_bf16 v[152:155], v[92:95], v[16:19], v[152:155]\n\t" \
  "v_mov_b32 v138, v137\n\t" \
  "v_mfma_f32_16x16x32_bf16 v[156:159], v[100:103], v[16:19], v[156:159]\n\t" \
  "ds_read_b64 v[16:17], v26 offset:3072\n\t" \
  "v_mov_b32 v137, v136\n\t" \
  "s_waitcnt lgkmcnt(11)\n\t" \
  "v_mfma_f32_16x16x32_bf16 v[140:143], v[64:67], v[20:23], v[140:143]\n\t" \
  "ds_read_b64 v[18:19], v27 offset:3072\n\t" \
  "v_mfma_f32_16x16x32_bf16 v[144:147], v[72:75], v[20:23], v[144:147]\n\t" \
  "v_mfma_f32_16x16x32_bf16 v[148:151], v[80:83], v[20:23], v[148:151]\n\t" \
  "v_mfma_f32_16x16x32_bf16 v[152:155], v[88:91], v[20:23], v[152:155]\n\t" \
  "v_mfma_f32_16x16x32_bf16 v[156:159], v[96:99], v[20:23], v[156:159]\n\t" \
  "ds_read_b64 v[20:21], v28 offset:3328\n\t" \
  "s_waitcnt lgkmcnt(11)\n\t" \
  "v_mfma_f32_16x16x32_bf16 v[144:147], v[68:71], v[0:3], v[144:147]\n\t" \
  "ds_read_b64 v[22:23], v29 offset:3328\n\t" \
  "v_mfma_f32_16x16x32_bf16 v[148:151], v[76:79], v[0:3], v[148:151]\n\t" \
  "v_mfma_f32_16x16x32_bf16 v[152:155], v[84:87], v[0:3], v[152:155]\n\t" \
  "ds_bpermute_b32 v37, v32, v143\n\t" \
  "v_readlane_b32 %[p3], v143, 63\n\t" \
  "v_mfma_f32_16x16x32_bf16 v[156:159], v[92:95], v[0:3], v[156:159]\n\t" \
  "ds_read_b64 v[0:1], v30 offset:3584\n\t" \
  "s_waitcnt lgkmcnt(6)\n\t" \
  "v_mov_b32 v136, v36\n\t" \
  "v_writelane_b32 v136, %[p1], 0\n\t" \
  "v_mov_b32 v143, v142\n\t" \
  "s_waitcnt lgkmcnt(12)\n\t" \
  "v_mfma_f32_16x16x32_bf16 v[144:147], v[64:67], v[4:7], v[144:147]\n\t" \
  "ds_read_b64 v[2:3], v31 offset:3584\n\t" \
  "v_mov_b32 v142, v141\n\t" \
  "v_mfma_f32_16x16x32_bf16 v[148:151], v[72:75], v[4:7], v[148:151]\n\t" \
  "v_mov_b32 v141, v140\n\t" \
  "v_mfma_f32_16x16x32_bf16 v[152:155], v[80:83], v[4:7], v[152:155]\n\t" \
  "v_mfma_f32_16x16x32_bf16 v[156:159], v[88:91], v[4:7], v[156:159]\n\t" \
  "ds_read_b64 v[4:5], v24 offset:3840\n\t" \
  "s_waitcnt lgkmcnt(11)\n\t" \
  "v_mfma_f32_16x16x32_bf16 v[148:151], v[68:71], v[8:11], v[148:151]\n\t" \
  "ds_read_b64 v[6:7], v25 offset:3840\n\t" \
  "v_mfma_f32_16x16x32_bf16 v[152:155], v[76:79], v[8:11], v[152:155]\n\t" \
  "v_mfma_f32_16x16x32_bf16 v[156:159], v[84:87], v[8:11], v[156:159]\n\t" \
  "ds_read_b64 v[8:9], v24 offset:0\n\t" \
  "s_waitcnt lgkmcnt(11)\n\t" \
  "v_mfma_f32_16x16x32_bf16 v[148:151], v[64:67], v[12:15], v[148:151]\n\t" \
  "ds_read_b64 v[10:11], v25 offset:0\n\t" \
  "ds_bpermute_b32 v38, v32, v147\n\t" \
  "v_readlane_b32 %[p4], v147, 63\n\t" \
  "v_mfma_f32_16x16x32_bf16 v[152:155], v[72:75], v[12:15], v[152:155]\n\t" \
  "v_mov_b32 v147, v146\n\t" \
  "v_mfma_f32_16x16x32_bf16 v[156:159], v[80:83], v[12:15], v[156:159]\n\t" \
  "ds_read_b64 v[12:13], v26 offset:256\n\t" \
  "v_mov_b32 v146, v145\n\t" \
  "s_waitcnt lgkmcnt(11)\n\t" \
  "v_mfma_f32_16x16x32_bf16 v[152:155], v[68:71], v[16:19], v[152:155]\n\t" \
  "ds_read_b64 v[14:15], v27 offset:256\n\t" \
  "s_waitcnt lgkmcnt(9)\n\t" \
  "v_mov_b32 v140, v37\n\t" \
  "v_writelane_b32 v140, %[p2], 0\n\t" \
  "v_mov_b32 v145, v144\n\t" \
  "v_mfma_f32_16x16x32_bf16 v[156:159], v[76:79], v[16:19], v[156:159]\n\t" \
  "ds_read_b64 v[16:17], v28 offset:512\n\t" \
  "ds_bpermute_b32 v39, v32, v151\n\t" \
  "v_readlane_b32 %[p5], v151, 63\n\t" \
  "s_waitcnt lgkmcnt(12)\n\t" \
  "v_mfma_f32_16x16x32_bf16 v[152:155], v[64:67], v[20:23], v[152:155]\n\t" \
  "ds_read_b64 v[18:19], v29 offset:512\n\t" \
  "v_mov_b32 v151, v150\n\t" \
  "v_mfma_f32_16x16x32_bf16 v[156:159], v[72:75], v[20:23], v[156:159]\n\t" \
  "ds_read_b64 v[20:21], v30 offset:768\n\t" \
  "v_mov_b32 v150, v149\n\t" \
  "s_waitcnt lgkmcnt(11)\n\t" \
  "v_mfma_f32_16x16x32_bf16 v[156:159], v[68:71], v[0:3], v[156:159]\n\t" \
  "ds_read_b64 v[22:23], v31 offset:768\n\t" \
  "ds_read_b64 v[0:1], v24 offset:1024\n\t" \
  "v_mov_b32 v149, v148\n\t" \
  "ds_bpermute_b32 v40, v32, v155\n\t" \
  "v_readlane_b32 %[p6], v155, 63\n\t" \
  "s_waitcnt lgkmcnt(12)\n\t" \
  "v_mfma_f32_16x16x32_bf16 v[156:159], v[64:67], v[4:7], v[156:159]\n\t" \
  "ds_read_b64 v[2:3], v25 offset:1024\n\t" \
  "ds_read_b64 v[4:5], v26 offset:1280\n\t" \
  "v_mov_b32 v155, v154\n\t" \
  "s_waitcnt lgkmcnt(12)\n\t" \
  "v_mfma_f32_16x16x32_bf16 v[128:131], v[68:71], v[8:11], v[128:131]\n\t" \
  "ds_read_b64 v[6:7], v27 offset:1280\n\t" \
  "v_mov_b32 v154, v153\n\t" \
  "v_mfma_f32_16x16x32_bf16 v[132:135], v[76:79], v[8:11], v[132:135]\n\t" \
  "s_waitcnt lgkmcnt(12)\n\t" \
  "v_mov_b32 v144, v38\n\t" \
  "v_writelane_b32 v144, %[p3], 0\n\t" \
  "v_mov_b32 v153, v152\n\t" \
  "v_mfma_f32_16x16x32_bf16 v[136:139], v[84:87], v[8:11], v[136:139]\n\t" \
  "s_waitcnt lgkmcnt(8)\n\t" \
  "v_mov_b32 v148, v39\n\t" \
  "v_writelane_b32 v148, %[p4], 0\n\t" \
  "v_mfma_f32_16x16x32_bf16 v[140:143], v[92:95], v[8:11], v[140:143]\n\t" \
  "s_waitcnt lgkmcnt(3)\n\t" \
  "v_mov_b32 v152, v40\n\t" \
  "v_writelane_b32 v152, %[p5], 0\n\t" \
  "v_mfma_f32_16x16x32_bf16 v[144:147], v[100:103], v[8:11], v[144:147]\n\t" \
  "ds_bpermute_b32 v41, v32, v159\n\t" \
  "v_mfma_f32_16x16x32_bf16 v[148:151], v[108:111], v[8:11], v[148:151]\n\t" \
  "v_mov_b32 v159, v158\n\t" \
  "v_mfma_f32_16x16x32_bf16 v[152:155], v[116:119], v[8:11], v[152:155]\n\t" \
  "v_mov_b32 v158, v157\n\t" \
  "s_waitcnt lgkmcnt(11)\n\t" \
  "v_mfma_f32_16x16x32_bf16 v[128:131], v[64:67], v[12:15], v[128:131]\n\t" \
  "v_mov_b32 v157, v156\n\t" \
  "v_mfma_f32_16x16x32_bf16 v[132:135], v[72:75], v[12:15], v[132:135]\n\t" \
  "v_mfma_f32_16x16x32_bf16 v[136:139], v[80:83], v[12:15], v[136:139]\n\t" \
  "v_mfma_f32_16x16x32_bf16 v[140:143], v[88:91], v[12:15], v[140:143]\n\t" \
  "v_mfma_f32_16x16x32_bf16 v[144:147], v[96:99], v[12:15], v[144:147]\n\t" \
  "s_waitcnt lgkmcnt(0)\n\t" \
  "v_mov_b32 v156, v41\n\t" \
  "v_writelane_b32 v156, %[p6], 0\n\t" \
  "v_mfma_f32_16x16x32_bf16 v[148:151], v[104:107], v[12:15], v[148:151]\n\t" \
  "v_mfma_f32_16x16x32_bf16 v[152:155], v[112:115], v[12:15], v[152:155]\n\t" \
  "v_mfma_f32_16x16x32_bf16 v[156:159], v[124:127], v[8:11], v[156:159]\n\t" \
  "ds_read_b64 v[8:9], v28 offset:1536\n\t" \
  "ds_bpermute_b32 v34, v32, v131\n\t" \
  "v_readlane_b32 %[p0], v131, 63\n\t" \
  "v_mfma_f32_16x16x32_bf16 v[156:159], v[120:123], v[12:15], v[156:159]\n\t" \
  "ds_read_b64 v[10:11], v29 offset:1536\n\t" \
  "ds_read_b64 v[12:13], v30 offset:1792\n\t" \
  "v_mov_b32 v131, v130\n\t" \
  "s_waitcnt lgkmcnt(12)\n\t" \
  "v_mfma_f32_16x16x32_bf16 v[132:135], v[68:71], v[16:19], v[132:135]\n\t" \
  "ds_read_b64 v[14:15], v31 offset:1792\n\t" \
  "v_mov_b32 v130, v129\n\t" \
  "v_mfma_f32_16x16x32_bf16 v[136:139], v[76:79], v[16:19], v[136:139]\n\t" \
  "v_mov_b32 v129, v128\n\t" \
  "v_mfma_f32_16x16x32_bf16 v[140:143], v[84:87], v[16:19], v[140:143]\n\t" \
  "v_mfma_f32_16x16x32_bf16 v[144:147], v[92:95], v[16:19], v[144:147]\n\t" \
  "v_mfma_f32_16x16x32_bf16 v[148:151], v[100:103], v[16:19], v[148:151]\n\t" \
  "v_mfma_f32_16x16x32_bf16 v[152:155], v[108:111], v[16:19], v[152:155]\n\t" \
  "v_mfma_f32_16x16x32_bf16 v[156:159], v[116:119], v[16:19], v[156:159]\n\t" \
  "ds_read_b64 v[16:17], v24 offset:2048\n\t" \
  "s_waitcnt lgkmcnt(12)\n\t" \
  "v_mfma_f32_16x16x32_bf16 v[132:135], v[64:67], v[20:23], v[132:135]\n\t" \
  "ds_read_b64 v[18:19], v25 offset:2048\n\t" \
  "v_mfma_f32_16x16x32_bf16 v[136:139], v[72:75], v[20:23], v[136:139]\n\t" \
  "v_mfma_f32_16x16x32_bf16 v[140:143], v[80:83], v[20:23], v[140:143]\n\t" \
  "v_mfma_f32_16x16x32_bf16 v[144:147], v[88:91], v[20:23], v[144:147]\n\t" \
  "s_waitcnt lgkmcnt(5)\n\t" \
  "v_mov_b32 v128, v34\n\t" \
  "v_writelane_b32 v128, 0, 0\n\t" \
  "v_mfma_f32_16x16x32_bf16 v[148:151], v[96:99], v[20:23], v[148:151]\n\t" \
  "v_mfma_f32_16x16x32_bf16 v[152:155], v[104:107], v[20:23], v[152:155]\n\t" \
  "v_mfma_f32_16x16x32_bf16 v[156:159], v[112:115], v[20:23], v[156:159]\n\t" \
  "ds_read_b64 v[20:21], v26 offset:2304\n\t" \
  "s_waitcnt lgkmcnt(11)\n\t" \
  "v_mfma_f32_16x16x32_bf16 v[136:139], v[68:71], v[0:3], v[136:139]\n\t" \
  "ds_read_b64 v[22:23], v27 offset:2304\n\t" \
  "ds_bpermute_b32 v35, v32, v135\n\t" \
  "v_readlane_b32 %[p1], v135, 63\n\t" \
  "v_mfma_f32_16x16x32_bf16 v[140:143], v[76:79], v[0:3], v[140:143]\n\t" \
  "v_mov_b32 v135, v134\n\t" \
  "v_mfma_f32_16x16x32_bf16 v[144:147], v[84:87], v[0:3], v[144:147]\n\t" \
  "v_mov_b32 v134, v133\n\t" \
  "v_mfma_f32_16x16x32_bf16 v[148:151], v[92:95], v[0:3], v[148:151]\n\t" \
  "v_mov_b32 v133, v132\n\t" \
  "v_mfma_f32_16x16x32_bf16 v[152:155], v[100:103], v[0:3], v[152:155]\n\t" \
  "v_mfma_f32_16x16x32_bf16 v[156:159], v[108:111], v[0:3], v[156:159]\n\t" \
  "ds_read_b64 v[0:1], v28 offset:2560\n\t" \
  "s_waitcnt lgkmcnt(12)\n\t" \
  "v_mfma_f32_16x16x32_bf16 v[136:139], v[64:67], v[4:7], v[136:139]\n\t" \
  "ds_read_b64 v[2:3], v29 offset:2560\n\t" \
  "v_mfma_f32_16x16x32_bf16 v[140:143], v[72:75], v[4:7], v[140:143]\n\t" \
  "v_mfma_f32_16x16x32_bf16 v[144:147], v[80:83], v[4:7], v[144:147]\n\t" \
  "v_mfma_f32_16x16x32_bf16 v[148:151], v[88:91], v[4:7], v[148:151]\n\t" \
  "v_mfma_f32_16x16x32_bf16 v[152:155], v[96:99], v[4:7], v[152:155]\n\t" \
  "v_mfma_f32_16x16x32_bf16 v[156:159], v[104:107], v[4:7], v[156:159]\n\t" \
  "ds_read_b64 v[4:5], v30 offset:2816\n\t" \
  "s_waitcnt lgkmcnt(10)\n\t" \
  "v_mfma_f32_16x16x32_bf16 v[140:143], v[68:71], v[8:11], v[140:143]\n\t" \
  "ds_read_b64 v[6:7], v31 offset:2816\n\t" \
  "s_waitcnt lgkmcnt(4)\n\t" \
  "v_mov_b32 v132, v35\n\t" \
  "v_writelane_b32 v132, %[p0], 0\n\t" \
  "v_mfma_f32_16x16x32_bf16 v[144:147], v[76:79], v[8:11], v[144:147]\n\t" \
  "ds_bpermute_b32 v36, v32, v139\n\t" \
  "v_readlane_b32 %[p2], v139, 63\n\t" \
  "v_mfma_f32_16x16x32_bf16 v[148:151], v[84:87], v[8:11], v[148:151]\n\t" \
  "v_mov_b32 v139, v138\n\t" \
  "v_mfma_f32_16x16x32_bf16 v[152:155], v[92:95], v[8:11], v[152:155]\n\t" \
  "v_mov_b32 v138, v137\n\t" \
  "v_mfma_f32_16x16x32_bf16 v[156:159], v[100:103], v[8:11], v[156:159]\n\t" \
  "ds_read_b64 v[8:9], v24 offset:3072\n\t" \
  "v_mov_b32 v137, v136\n\t" \
  "s_waitcnt lgkmcnt(11)\n\t" \
  "v_mfma_f32_16x16x32_bf16 v[140:143], v[64:67], v[12:15], v[140:143]\n\t" \
  "ds_read_b64 v[10:11], v25 offset:3072\n\t" \
  "v_mfma_f32_16x16x32_bf16 v[144:147], v[72:75], v[12:15], v[144:147]\n\t" \
  "v_mfma_f32_16x16x32_bf16 v[148:151], v[80:83], v[12:15], v[148:151]\n\t" \
  "v_mfma_f32_16x16x32_bf16 v[152:155], v[88:91], v[12:15], v[152:155]\n\t" \
  "v_mfma_f32_16x16x32_bf16 v[156:159], v[96:99], v[12:15], v[156:159]\n\t" \
  "ds_read_b64 v[12:13], v26 offset:3328\n\t" \
  "s_waitcnt lgkmcnt(11)\n\t" \
  "v_mfma_f32_16x16x32_bf16 v[144:147], v[68:71], v[16:19], v[144:147]\n\t" \
  "ds_read_b64 v[14:15], v27 offset:3328\n\t" \
  "v_mfma_f32_16x16x32_bf16 v[148:151], v[76:79], v[16:19], v[148:151]\n\t" \
  "v_mfma_f32_16x16x32_bf16 v[152:155], v[84:87], v[16:19], v[152:155]\n\t" \
  "ds_bpermute_b32 v37, v32, v143\n\t" \
  "v_readlane_b32 %[p3], v143, 63\n\t" \
  "v_mfma_f32_16x16x32_bf16 v[156:159], v[92:95], v[16:19], v[156:159]\n\t" \
  "ds_read_b64 v[16:17], v28 offset:3584\n\t" \
  "s_waitcnt lgkmcnt(6)\n\t" \
  "v_mov_b32 v136, v36\n\t" \
  "v_writelane_b32 v136, %[p1], 0\n\t" \
  "v_mov_b32 v143, v142\n\t" \
  "s_waitcnt lgkmcnt(12)\n\t" \
  "v_mfma_f32_16x16x32_bf16 v[144:147], v[64:67], v[20:23], v[144:147]\n\t" \
  "ds_read_b64 v[18:19], v29 offset:3584\n\t" \
  "v_mov_b32 v142, v141\n\t" \
  "v_mfma_f32_16x16x32_bf16 v[148:151], v[72:75], v[20:23], v[148:151]\n\t" \
  "v_mov_b32 v141, v140\n\t" \
  "v_mfma_f32_16x16x32_bf16 v[152:155], v[80:83], v[20:23], v[152:155]\n\t" \
  "v_mfma_f32_16x16x32_bf16 v[156:159], v[88:91], v[20:23], v[156:159]\n\t" \
  "ds_read_b64 v[20:21], v30 offset:3840\n\t" \
  "s_waitcnt lgkmcnt(11)\n\t" \
  "v_mfma_f32_16x16x32_bf16 v[148:151], v[68:71], v[0:3], v[148:151]\n\t" \
  "ds_read_b64 v[22:23], v31 offset:3840\n\t" \
  "v_mfma_f32_16x16x32_bf16 v[152:155], v[76:79], v[0:3], v[152:155]\n\t" \
  "v_mfma_f32_16x16x32_bf16 v[156:159], v[84:87], v[0:3], v[156:159]\n\t" \
  "ds_read_b64 v[0:1], v30 offset:0\n\t" \
  "s_waitcnt lgkmcnt(11)\n\t" \
  "v_mfma_f32_16x16x32_bf16 v[148:151], v[64:67], v[4:7], v[148:151]\n\t" \
  "ds_read_b64 v[2:3], v31 offset:0\n\t" \
  "ds_bpermute_b32 v38, v32, v147\n\t" \
  "v_readlane_b32 %[p4], v147, 63\n\t" \
  "v_mfma_f32_16x16x32_bf16 v[152:155], v[72:75], v[4:7], v[152:155]\n\t" \
  "v_mov_b32 v147, v146\n\t" \
  "v_mfma_f32_16x16x32_bf16 v[156:159], v[80:83], v[4:7], v[156:159]\n\t" \
  "ds_read_b64 v[4:5], v24 offset:256\n\t" \
  "v_mov_b32 v146, v145\n\t" \
  "s_waitcnt lgkmcnt(11)\n\t" \
  "v_mfma_f32_16x16x32_bf16 v[152:155], v[68:71], v[8:11], v[152:155]\n\t" \
  "ds_read_b64 v[6:7], v25 offset:256\n\t" \
  "s_waitcnt lgkmcnt(9)\n\t" \
  "v_mov_b32 v140, v37\n\t" \
  "v_writelane_b32 v140, %[p2], 0\n\t" \
  "v_mov_b32 v145, v144\n\t" \
  "v_mfma_f32_16x16x32_bf16 v[156:159], v[76:79], v[8:11], v[156:159]\n\t" \
  "ds_read_b64 v[8:9], v26 offset:512\n\t" \
  "ds_bpermute_b32 v39, v32, v151\n\t" \
  "v_readlane_b32 %[p5], v151, 63\n\t" \
  "s_waitcnt lgkmcnt(12)\n\t" \
  "v_mfma_f32_16x16x32_bf16 v[152:155], v[64:67], v[12:15], v[152:155]\n\t" \
  "ds_read_b64 v[10:11], v27 offset:512\n\t" \
  "v_mov_b32 v151, v150\n\t" \
  "v_mfma_f32_16x16x32_bf16 v[156:159], v[72:75], v[12:15], v[156:159]\n\t" \
  "ds_read_b64 v[12:13], v28 offset:768\n\t" \
  "v_mov_b32 v150, v149\n\t" \
  "s_waitcnt lgkmcnt(11)\n\t" \
  "v_mfma_f32_16x16x32_bf16 v[156:159], v[68:71], v[16:19], v[156:159]\n\t" \
  "ds_read_b64 v[14:15], v29 offset:768\n\t" \
  "ds_read_b64 v[16:17], v30 offset:1024\n\t" \
  "v_mov_b32 v149, v148\n\t" \
  "ds_bpermute_b32 v40, v32, v155\n\t" \
  "v_readlane_b32 %[p6], v155, 63\n\t" \
  "s_waitcnt lgkmcnt(12)\n\t" \
  "v_mfma_f32_16x16x32_bf16 v[156:159], v[64:67], v[20:23], v[156:159]\n\t" \
  "ds_read_b64 v[18:19], v31 offset:1024\n\t" \
  "ds_read_b64 v[20:21], v24 offset:1280\n\t" \
  "v_mov_b32 v155, v154\n\t" \
  "s_waitcnt lgkmcnt(12)\n\t" \
  "v_mfma_f32_16x16x32_bf16 v[128:131], v[68:71], v[0:3], v[128:131]\n\t" \
  "ds_read_b64 v[22:23], v25 offset:1280\n\t" \
  "v_mov_b32 v154, v153\n\t" \
  "v_mfma_f32_16x16x32_bf16 v[132:135], v[76:79], v[0:3], v[132:135]\n\t" \
  "s_waitcnt lgkmcnt(12)\n\t" \
  "v_mov_b32 v144, v38\n\t" \
  "v_writelane_b32 v144, %[p3], 0\n\t" \
  "v_mov_b32 v153, v152\n\t" \
  "v_mfma_f32_16x16x32_bf16 v[136:139], v[84:87], v[0:3], v[136:139]\n\t" \
  "s_waitcnt lgkmcnt(8)\n\t" \
  "v_mov_b32 v148, v39\n\t" \
  "v_writelane_b32 v148, %[p4], 0\n\t" \
  "v_mfma_f32_16x16x32_bf16 v[140:143], v[92:95], v[0:3], v[140:143]\n\t" \
  "s_waitcnt lgkmcnt(3)\n\t" \
  "v_mov_b32 v152, v40\n\t" \
  "v_writelane_b32 v152, %[p5], 0\n\t" \
  "v_mfma_f32_16x16x32_bf16 v[144:147], v[100:103], v[0:3], v[144:147]\n\t" \
  "ds_bpermute_b32 v41, v32, v159\n\t" \
  "v_mfma_f32_16x16x32_bf16 v[148:151], v[108:111], v[0:3], v[148:151]\n\t" \
  "v_mov_b32 v159, v158\n\t" \
  "v_mfma_f32_16x16x32_bf16 v[152:155], v[116:119], v[0:3], v[152:155]\n\t" \
  "v_mov_b32 v158, v157\n\t" \
  "s_waitcnt lgkmcnt(11)\n\t" \
  "v_mfma_f32_16x16x32_bf16 v[128:131], v[64:67], v[4:7], v[128:131]\n\t" \
  "v_mov_b32 v157, v156\n\t" \
  "v_mfma_f32_16x16x32_bf16 v[132:135], v[72:75], v[4:7], v[132:135]\n\t" \
  "v_mfma_f32_16x16x32_bf16 v[136:139], v[80:83], v[4:7], v[136:139]\n\t" \
  "v_mfma_f32_16x16x32_bf16 v[140:143], v[88:91], v[4:7], v[140:143]\n\t" \
  "v_mfma_f32_16x16x32_bf16 v[144:147], v[96:99], v[4:7], v[144:147]\n\t" \
  "s_waitcnt lgkmcnt(0)\n\t" \
  "v_mov_b32 v156, v41\n\t" \
  "v_writelane_b32 v156, %[p6], 0\n\t" \
  "v_mfma_f32_16x16x32_bf16 v[148:151], v[104:107], v[4:7], v[148:151]\n\t" \
  "v_mfma_f32_16x16x32_bf16 v[152:155], v[112:115], v[4:7], v[152:155]\n\t" \
  "s_mov_b64 exec, 1\n\t" \
  "ds_write_b32 v33, v33\n\t" \
  "s_mov_b64 exec, -1\n\t" \
  "v_mfma_f32_16x16x32_bf16 v[156:159], v[124:127], v[0:3], v[156:159]\n\t" \
  "ds_read_b64 v[0:1], v26 offset:1536\n\t" \
  "v_mfma_f32_16x16x32_bf16 v[156:159], v[120:123], v[4:7], v[156:159]\n\t" \
  "ds_read_b64 v[2:3], v27 offset:1536\n\t" \
  "ds_read_b64 v[4:5], v28 offset:1792\n\t" \
  "s_waitcnt lgkmcnt(12)\n\t" \
  "v_mfma_f32_16x16x32_bf16 v[132:135], v[68:71], v[8:11], v[132:135]\n\t" \
  "ds_read_b64 v[6:7], v29 offset:1792\n\t" \
  "v_mfma_f32_16x16x32_bf16 v[136:139], v[76:79], v[8:11], v[136:139]\n\t" \
  "v_mfma_f32_16x16x32_bf16 v[140:143], v[84:87], v[8:11], v[140:143]\n\t" \
  "v_mfma_f32_16x16x32_bf16 v[144:147], v[92:95], v[8:11], v[144:147]\n\t" \
  "v_mfma_f32_16x16x32_bf16 v[148:151], v[100:103], v[8:11], v[148:151]\n\t" \
  "v_mfma_f32_16x16x32_bf16 v[152:155], v[108:111], v[8:11], v[152:155]\n\t" \
  "v_mfma_f32_16x16x32_bf16 v[156:159], v[116:119], v[8:11], v[156:159]\n\t" \
  "ds_read_b64 v[8:9], v30 offset:2048\n\t" \
  "s_waitcnt lgkmcnt(12)\n\t" \
  "v_mfma_f32_16x16x32_bf16 v[132:135], v[64:67], v[12:15], v[132:135]\n\t" \
  "ds_read_b64 v[10:11], v31 offset:2048\n\t" \
  "v_mfma_f32_16x16x32_bf16 v[136:139], v[72:75], v[12:15], v[136:139]\n\t" \
  "v_mfma_f32_16x16x32_bf16 v[140:143], v[80:83], v[12:15], v[140:143]\n\t" \
  "v_mfma_f32_16x16x32_bf16 v[144:147], v[88:91], v[12:15], v[144:147]\n\t" \
  "v_mfma_f32_16x16x32_bf16 v[148:151], v[96:99], v[12:15], v[148:151]\n\t" \
  "v_mfma_f32_16x16x32_bf16 v[152:155], v[104:107], v[12:15], v[152:155]\n\t" \
  "v_mfma_f32_16x16x32_bf16 v[156:159], v[112:115], v[12:15], v[156:159]\n\t" \
  "ds_read_b64 v[12:13], v24 offset:2304\n\t" \
  "s_waitcnt lgkmcnt(11)\n\t" \
  "v_mfma_f32_16x16x32_bf16 v[136:139], v[68:71], v[16:19], v[136:139]\n\t" \
  "ds_read_b64 v[14:15], v25 offset:2304\n\t" \
  "v_mfma_f32_16x16x32_bf16 v[140:143], v[76:79], v[16:19], v[140:143]\n\t" \
  "v_mfma_f32_16x16x32_bf16 v[144:147], v[84:87], v[16:19], v[144:147]\n\t" \
  "v_mfma_f32_16x16x32_bf16 v[148:151], v[92:95], v[16:19], v[148:151]\n\t" \
  "v_mfma_f32_16x16x32_bf16 v[152:155], v[100:103], v[16:19], v[152:155]\n\t" \
  "v_mfma_f32_16x16x32_bf16 v[156:159], v[108:111], v[16:19], v[156:159]\n\t" \
  "ds_read_b64 v[16:17], v26 offset:2560\n\t" \
  "s_waitcnt lgkmcnt(11)\n\t" \
  "v_mfma_f32_16x16x32_bf16 v[136:139], v[64:67], v[20:23], v[136:139]\n\t" \
  "ds_read_b64 v[18:19], v27 offset:2560\n\t" \
  "v_mfma_f32_16x16x32_bf16 v[140:143], v[72:75], v[20:23], v[140:143]\n\t" \
  "v_mfma_f32_16x16x32_bf16 v[144:147], v[80:83], v[20:23], v[144:147]\n\t" \
  "v_mfma_f32_16x16x32_bf16 v[148:151], v[88:91], v[20:23], v[148:151]\n\t" \
  "v_mfma_f32_16x16x32_bf16 v[152:155], v[96:99], v[20:23], v[152:155]\n\t" \
  "v_mfma_f32_16x16x32_bf16 v[156:159], v[104:107], v[20:23], v[156:159]\n\t" \
  "ds_read_b64 v[20:21], v28 offset:2816\n\t" \
  "s_waitcnt lgkmcnt(9)\n\t" \
  "v_mfma_f32_16x16x32_bf16 v[140:143], v[68:71], v[0:3], v[140:143]\n\t" \
  "ds_read_b64 v[22:23], v29 offset:2816\n\t" \
  "v_mfma_f32_16x16x32_bf16 v[144:147], v[76:79], v[0:3], v[144:147]\n\t" \
  "v_mfma_f32_16x16x32_bf16 v[148:151], v[84:87], v[0:3], v[148:151]\n\t" \
  "v_mfma_f32_16x16x32_bf16 v[152:155], v[92:95], v[0:3], v[152:155]\n\t" \
  "v_mfma_f32_16x16x32_bf16 v[156:159], v[100:103], v[0:3], v[156:159]\n\t" \
  "ds_read_b64 v[0:1], v30 offset:3072\n\t" \
  "s_waitcnt lgkmcnt(9)\n\t" \
  "v_mfma_f32_16x16x32_bf16 v[140:143], v[64:67], v[4:7], v[140:143]\n\t" \
  "ds_read_b64 v[2:3], v31 offset:3072\n\t" \
  "v_mfma_f32_16x16x32_bf16 v[144:147], v[72:75], v[4:7], v[144:147]\n\t" \
  "v_mfma_f32_16x16x32_bf16 v[148:151], v[80:83], v[4:7], v[148:151]\n\t" \
  "v_mfma_f32_16x16x32_bf16 v[152:155], v[88:91], v[4:7], v[152:155]\n\t" \
  "v_mfma_f32_16x16x32_bf16 v[156:159], v[96:99], v[4:7], v[156:159]\n\t" \
  "ds_read_b64 v[4:5], v24 offset:3328\n\t" \
  "s_waitcnt lgkmcnt(9)\n\t" \
  "v_mfma_f32_16x16x32_bf16 v[144:147], v[68:71], v[8:11], v[144:147]\n\t" \
  "ds_read_b64 v[6:7], v25 offset:3328\n\t" \
  "v_mfma_f32_16x16x32_bf16 v[148:151], v[76:79], v[8:11], v[148:151]\n\t" \
  "v_mfma_f32_16x16x32_bf16 v[152:155], v[84:87], v[8:11], v[152:155]\n\t" \
  "v_mfma_f32_16x16x32_bf16 v[156:159], v[92:95], v[8:11], v[156:159]\n\t" \
  "ds_read_b64 v[8:9], v26 offset:3584\n\t" \
  "s_waitcnt lgkmcnt(9)\n\t" \
  "v_mfma_f32_16x16x32_bf16 v[144:147], v[64:67], v[12:15], v[144:147]\n\t" \
  "ds_read_b64 v[10:11], v27 offset:3584\n\t" \
  "v_mfma_f32_16x16x32_bf16 v[148:151], v[72:75], v[12:15], v[148:151]\n\t" \
  "v_mfma_f32_16x16x32_bf16 v[152:155], v[80:83], v[12:15], v[152:155]\n\t" \
  "v_mfma_f32_16x16x32_bf16 v[156:159], v[88:91], v[12:15], v[156:159]\n\t" \
  "ds_read_b64 v[12:13], v28 offset:3840\n\t" \
  "s_waitcnt lgkmcnt(9)\n\t" \
  "v_mfma_f32_16x16x32_bf16 v[148:151], v[68:71], v[16:19], v[148:151]\n\t" \
  "ds_read_b64 v[14:15], v29 offset:3840\n\t" \
  "v_mfma_f32_16x16x32_bf16 v[152:155], v[76:79], v[16:19], v[152:155]\n\t" \
  "v_mfma_f32_16x16x32_bf16 v[156:159], v[84:87], v[16:19], v[156:159]\n\t" \
  "s_waitcnt lgkmcnt(8)\n\t" \
  "v_mfma_f32_16x16x32_bf16 v[148:151], v[64:67], v[20:23], v[148:151]\n\t" \
  "v_mfma_f32_16x16x32_bf16 v[152:155], v[72:75], v[20:23], v[152:155]\n\t" \
  "v_mfma_f32_16x16x32_bf16 v[156:159], v[80:83], v[20:23], v[156:159]\n\t" \
  "s_waitcnt lgkmcnt(6)\n\t" \
  "v_mfma_f32_16x16x32_bf16 v[152:155], v[68:71], v[0:3], v[152:155]\n\t" \
  "v_mfma_f32_16x16x32_bf16 v[156:159], v[76:79], v[0:3], v[156:159]\n\t" \
  "s_waitcnt lgkmcnt(4)\n\t" \
  "v_mfma_f32_16x16x32_bf16 v[152:155], v[64:67], v[4:7], v[152:155]\n\t" \
  "v_mfma_f32_16x16x32_bf16 v[156:159], v[72:75], v[4:7], v[156:159]\n\t" \
  "s_waitcnt lgkmcnt(2)\n\t" \
  "v_mfma_f32_16x16x32_bf16 v[156:159], v[68:71], v[8:11], v[156:159]\n\t" \
  "s_waitcnt lgkmcnt(0)\n\t" \
  "v_mfma_f32_16x16x32_bf16 v[156:159], v[64:67], v[12:15], v[156:159]\n\t" \
  "s_waitcnt lgkmcnt(0)\n\t" \
  "s_nop 7\n\t"
#define CV_CLOB "v0", "v1", "v2", "v3", "v4", "v5", "v6", "v7", "v8", "v9", "v10", "v11", "v12", "v13", "v14", "v15", "v16", "v17", "v18", "v19", "v20", "v21", "v22", "v23", "v34", "v35", "v36", "v37", "v38", "v39", "v40", "v41"

#define G3_ASM \
  "v_mov_b32 v128, 0\n\t" \
  "v_mov_b32 v129, 0\n\t" \
  "v_mov_b32 v130, 0\n\t" \
  "v_mov_b32 v131, 0\n\t" \
  "v_mov_b32 v132, 0\n\t" \
  "v_mov_b32 v133, 0\n\t" \
  "v_mov_b32 v134, 0\n\t" \
  "v_mov_b32 v135, 0\n\t" \
  "v_mov_b32 v136, 0\n\t" \
  "v_mov_b32 v137, 0\n\t" \
  "v_mov_b32 v138, 0\n\t" \
  "v_mov_b32 v139, 0\n\t" \
  "v_mov_b32 v140, 0\n\t" \
  "v_mov_b32 v141, 0\n\t" \
  "v_mov_b32 v142, 0\n\t" \
  "v_mov_b32 v143, 0\n\t" \
  "v_mov_b32 v144, 0\n\t" \
  "v_mov_b32 v145, 0\n\t" \
  "v_mov_b32 v146, 0\n\t" \
  "v_mov_b32 v147, 0\n\t" \
  "v_mov_b32 v148, 0\n\t" \
  "v_mov_b32 v149, 0\n\t" \
  "v_mov_b32 v150, 0\n\t" \
  "v_mov_b32 v151, 0\n\t" \
  "v_mov_b32 v152, 0\n\t" \
  "v_mov_b32 v153, 0\n\t" \
  "v_mov_b32 v154, 0\n\t" \
  "v_mov_b32 v155, 0\n\t" \
  "v_mov_b32 v156, 0\n\t" \
  "v_mov_b32 v157, 0\n\t" \
  "v_mov_b32 v158, 0\n\t" \
  "v_mov_b32 v159, 0\n\t" \
  "s_getpc_b64 s[92:93]\n\t" \
  "s_branch QC_%=\n\t" \
  "ds_read_b64_tr_b16 v[64:65], v96 offset:0\n\t" \
  "ds_read_b64_tr_b16 v[66:67], v96 offset:2048\n\t" \
  "ds_read_b64_tr_b16 v[68:69], v97 offset:0\n\t" \
  "ds_read_b64_tr_b16 v[70:71], v97 offset:2048\n\t" \
  "ds_read_b64_tr_b16 v[72:73], v98 offset:0\n\t" \
  "ds_read_b64_tr_b16 v[74:75], v98 offset:2048\n\t" \
  "ds_read_b64_tr_b16 v[76:77], v99 offset:0\n\t" \
  "ds_read_b64_tr_b16 v[78:79], v99 offset:2048\n\t" \
  "ds_read_b64_tr_b16 v[80:81], v96 offset:4096\n\t" \
  "ds_read_b64_tr_b16 v[82:83], v96 offset:6144\n\t" \
  "ds_read_b64_tr_b16 v[84:85], v97 offset:4096\n\t" \
  "ds_read_b64_tr_b16 v[86:87], v97 offset:6144\n\t" \
  "ds_read_b64_tr_b16 v[88:89], v98 offset:4096\n\t" \
  "ds_read_b64_tr_b16 v[90:91], v98 offset:6144\n\t" \
  "QH_%=:\n\t" \
  "ds_read_b32 v102, v104\n\t" \
  "s_addk_i32 %[cn], 1\n\t" \
  "s_cmp_gt_u32 %[cn], 0x400000\n\t" \
  "s_cbranch_scc1 QI_%=\n\t" \
  "s_waitcnt lgkmcnt(0)\n\t" \
  "v_cmp_eq_u32 vcc, 0, v102\n\t" \
  "s_cbranch_vccz QI_%=\n\t" \
  "s_sleep 2\n\t" \
  "s_getpc_b64 s[92:93]\n\t" \
  "s_branch QC_%=\n\t" \
  "s_branch QH_%=\n\t" \
  "QI_%=:\n\t" \
  "ds_read_b32 v102, v101\n\t" \
  "s_waitcnt lgkmcnt(0)\n\t" \
  "s_branch QGa0_%=\n\t" \
  "QBa0_%=:\n\t" \
  "s_cbranch_vccz QFa0_%=\n\t" \
  "s_waitcnt vmcnt(8)\n\t" \
  "QGa0_%=:\n\t" \
  "s_waitcnt lgkmcnt(11)\n\t" \
  "v_mfma_f32_16x16x32_bf16 v[128:131], v[64:67], v[0:3], v[128:131]\n\t" \
  "ds_read_b64_tr_b16 v[92:93], v99 offset:4096\n\t" \
  "v_mfma_f32_16x16x32_bf16 v[144:147], v[64:67], v[4:7], v[144:147]\n\t" \
  "ds_read_b64_tr_b16 v[94:95], v99 offset:6144\n\t" \
  "s_waitcnt lgkmcnt(7)\n\t" \
  "v_cmp_lt_u32 vcc, %[th], v102\n\t" \
  "v_mfma_f32_16x16x32_bf16 v[132:135], v[68:71], v[0:3], v[132:135]\n\t" \
  "s_mov_b64 exec, vcc\n\t" \
  "global_load_dwordx4 v[48:51], v100, %[ob] offset:0 sc1\n\t" \
  "s_mov_b64 exec, -1\n\t" \
  "v_mfma_f32_16x16x32_bf16 v[148:151], v[68:71], v[4:7], v[148:151]\n\t" \
  "ds_read_b64_tr_b16 v[64:65], v96 offset:8192\n\t" \
  "ds_read_b64_tr_b16 v[66:67], v96 offset:10240\n\t" \
  "v_mfma_f32_16x16x32_bf16 v[136:139], v[72:75], v[0:3], v[136:139]\n\t" \
  "s_mov_b64 exec, vcc\n\t" \
  "global_load_dwordx4 v[52:55], v100, %[ob] offset:1024 sc1\n\t" \
  "s_mov_b64 exec, -1\n\t" \
  "v_mfma_f32_16x16x32_bf16 v[152:155], v[72:75], v[4:7], v[152:155]\n\t" \
  "ds_read_b64_tr_b16 v[68:69], v97 offset:8192\n\t" \
  "ds_read_b64_tr_b16 v[70:71], v97 offset:10240\n\t" \
  "v_mfma_f32_16x16x32_bf16 v[140:143], v[76:79], v[0:3], v[140:143]\n\t" \
  "s_mov_b64 exec, vcc\n\t" \
  "global_load_dwordx4 v[56:59], v100, %[ob] offset:2048 sc1\n\t" \
  "s_mov_b64 exec, -1\n\t" \
  "v_mfma_f32_16x16x32_bf16 v[156:159], v[76:79], v[4:7], v[156:159]\n\t" \
  "ds_read_b64_tr_b16 v[72:73], v98 offset:8192\n\t" \
  "ds_read_b64_tr_b16 v[74:75], v98 offset:10240\n\t" \
  "s_waitcnt lgkmcnt(10)\n\t" \
  "v_mfma_f32_16x16x32_bf16 v[128:131], v[80:83], v[8:11], v[128:131]\n\t" \
  "s_mov_b64 exec, vcc\n\t" \
  "global_load_dwordx4 v[60:63], v100, %[ob] offset:3072 sc1\n\t" \
  "s_mov_b64 exec, -1\n\t" \
  "v_mfma_f32_16x16x32_bf16 v[144:147], v[80:83], v[12:15], v[144:147]\n\t" \
  "ds_read_b64_tr_b16 v[76:77], v99 offset:8192\n\t" \
  "ds_read_b64_tr_b16 v[78:79], v99 offset:10240\n\t" \
  "v_mfma_f32_16x16x32_bf16 v[132:135], v[84:87], v[8:11], v[132:135]\n\t" \
  "ds_read_b64_tr_b16 v[80:81], v96 offset:12288\n\t" \
  "s_and_b32 %[t0], vcc_lo, 0x4000\n\t" \
  "ds_read_b32 v102, v101\n\t" \
  "v_mfma_f32_16x16x32_bf16 v[148:151], v[84:87], v[12:15], v[148:151]\n\t" \
  "ds_read_b64_tr_b16 v[82:83], v96 offset:14336\n\t" \
  "v_add_u32 v100, %[t0], v100\n\t" \
  "s_waitcnt lgkmcnt(11)\n\t" \
  "v_mfma_f32_16x16x32_bf16 v[136:139], v[88:91], v[8:11], v[136:139]\n\t" \
  "ds_read_b64_tr_b16 v[84:85], v97 offset:12288\n\t" \
  "v_and_b32 v100, 0x3ffff, v100\n\t" \
  "v_mfma_f32_16x16x32_bf16 v[152:155], v[88:91], v[12:15], v[152:155]\n\t" \
  "ds_read_b64_tr_b16 v[86:87], v97 offset:14336\n\t" \
  "s_sub_u32 %[iss], %[iss], vcc_lo\n\t" \
  "v_mfma_f32_16x16x32_bf16 v[140:143], v[92:95], v[8:11], v[140:143]\n\t" \
  "ds_read_b64_tr_b16 v[88:89], v98 offset:12288\n\t" \
  "s_add_u32 %[c], %[c], 1\n\t" \
  "v_mfma_f32_16x16x32_bf16 v[156:159], v[92:95], v[12:15], v[156:159]\n\t" \
  "ds_read_b64_tr_b16 v[90:91], v98 offset:14336\n\t" \
  "s_add_u32 %[th], %[th], 1\n\t" \
  "QBa1_%=:\n\t" \
  "s_cbranch_vccz QFa1_%=\n\t" \
  "s_waitcnt vmcnt(8)\n\t" \
  "QGa1_%=:\n\t" \
  "s_waitcnt lgkmcnt(11)\n\t" \
  "v_mfma_f32_16x16x32_bf16 v[128:131], v[64:67], v[16:19], v[128:131]\n\t" \
  "ds_read_b64_tr_b16 v[92:93], v99 offset:12288\n\t" \
  "v_mfma_f32_16x16x32_bf16 v[144:147], v[64:67], v[20:23], v[144:147]\n\t" \
  "ds_read_b64_tr_b16 v[94:95], v99 offset:14336\n\t" \
  "s_waitcnt lgkmcnt(7)\n\t" \
  "v_cmp_lt_u32 vcc, %[th], v102\n\t" \
  "v_mfma_f32_16x16x32_bf16 v[132:135], v[68:71], v[16:19], v[132:135]\n\t" \
  "s_mov_b64 exec, vcc\n\t" \
  "global_load_dwordx4 v[0:3], v100, %[ob] offset:0 sc1\n\t" \
  "s_mov_b64 exec, -1\n\t" \
  "v_mfma_f32_16x16x32_bf16 v[148:151], v[68:71], v[20:23], v[148:151]\n\t" \
  "ds_read_b64_tr_b16 v[64:65], v96 offset:16384\n\t" \
  "ds_read_b64_tr_b16 v[66:67], v96 offset:18432\n\t" \
  "v_mfma_f32_16x16x32_bf16 v[136:139], v[72:75], v[16:19], v[136:139]\n\t" \
  "s_mov_b64 exec, vcc\n\t" \
  "global_load_dwordx4 v[4:7], v100, %[ob] offset:1024 sc1\n\t" \
  "s_mov_b64 exec, -1\n\t" \
  "v_mfma_f32_16x16x32_bf16 v[152:155], v[72:75], v[20:23], v[152:155]\n\t" \
  "ds_read_b64_tr_b16 v[68:69], v97 offset:16384\n\t" \
  "ds_read_b64_tr_b16 v[70:71], v97 offset:18432\n\t" \
  "v_mfma_f32_16x16x32_bf16 v[140:143], v[76:79], v[16:19], v[140:143]\n\t" \
  "s_mov_b64 exec, vcc\n\t" \
  "global_load_dwordx4 v[8:11], v100, %[ob] offset:2048 sc1\n\t" \
  "s_mov_b64 exec, -1\n\t" \
  "v_mfma_f32_16x16x32_bf16 v[156:159], v[76:79], v[20:23], v[156:159]\n\t" \
  "ds_read_b64_tr_b16 v[72:73], v98 offset:16384\n\t" \
  "ds_read_b64_tr_b16 v[74:75], v98 offset:18432\n\t" \
  "s_waitcnt lgkmcnt(10)\n\t" \
  "v_mfma_f32_16x16x32_bf16 v[128:131], v[80:83], v[24:27], v[128:131]\n\t" \
  "s_mov_b64 exec, vcc\n\t" \
  "global_load_dwordx4 v[12:15], v100, %[ob] offset:3072 sc1\n\t" \
  "s_mov_b64 exec, -1\n\t" \
  "v_mfma_f32_16x16x32_bf16 v[144:147], v[80:83], v[28:31], v[144:147]\n\t" \
  "ds_read_b64_tr_b16 v[76:77], v99 offset:16384\n\t" \
  "ds_read_b64_tr_b16 v[78:79], v99 offset:18432\n\t" \
  "v_mfma_f32_16x16x32_bf16 v[132:135], v[84:87], v[24:27], v[132:135]\n\t" \
  "ds_read_b64_tr_b16 v[80:81], v96 offset:20480\n\t" \
  "s_and_b32 %[t0], vcc_lo, 0x4000\n\t" \
  "ds_read_b32 v102, v101\n\t" \
  "v_mfma_f32_16x16x32_bf16 v[148:151], v[84:87], v[28:31], v[148:151]\n\t" \
  "ds_read_b64_tr_b16 v[82:83], v96 offset:22528\n\t" \
  "v_add_u32 v100, %[t0], v100\n\t" \
  "s_waitcnt lgkmcnt(11)\n\t" \
  "v_mfma_f32_16x16x32_bf16 v[136:139], v[88:91], v[24:27], v[136:139]\n\t" \
  "ds_read_b64_tr_b16 v[84:85], v97 offset:20480\n\t" \
  "v_and_b32 v100, 0x3ffff, v100\n\t" \
  "v_mfma_f32_16x16x32_bf16 v[152:155], v[88:91], v[28:31], v[152:155]\n\t" \
  "ds_read_b64_tr_b16 v[86:87], v97 offset:22528\n\t" \
  "s_sub_u32 %[iss], %[iss], vcc_lo\n\t" \
  "v_mfma_f32_16x16x32_bf16 v[140:143], v[92:95], v[24:27], v[140:143]\n\t" \
  "ds_read_b64_tr_b16 v[88:89], v98 offset:20480\n\t" \
  "s_add_u32 %[c], %[c], 1\n\t" \
  "v_mfma_f32_16x16x32_bf16 v[156:159], v[92:95], v[28:31], v[156:159]\n\t" \
  "ds_read_b64_tr_b16 v[90:91], v98 offset:22528\n\t" \
  "s_add_u32 %[th], %[th], 1\n\t" \
  "QBa2_%=:\n\t" \
  "s_cbranch_vccz QFa2_%=\n\t" \
  "s_waitcnt vmcnt(8)\n\t" \
  "QGa2_%=:\n\t" \
  "s_waitcnt lgkmcnt(11)\n\t" \
  "v_mfma_f32_16x16x32_bf16 v[128:131], v[64:67], v[32:35], v[128:131]\n\t" \
  "ds_read_b64_tr_b16 v[92:93], v99 offset:20480\n\t" \
  "v_mfma_f32_16x16x32_bf16 v[144:147], v[64:67], v[36:39], v[144:147]\n\t" \
  "ds_read_b64_tr_b16 v[94:95], v99 offset:22528\n\t" \
  "s_waitcnt lgkmcnt(7)\n\t" \
  "v_cmp_lt_u32 vcc, %[th], v102\n\t" \
  "v_mfma_f32_16x16x32_bf16 v[132:135], v[68:71], v[32:35], v[132:135]\n\t" \
  "s_mov_b64 exec, vcc\n\t" \
  "global_load_dwordx4 v[16:19], v100, %[ob] offset:0 sc1\n\t" \
  "s_mov_b64 exec, -1\n\t" \
  "v_mfma_f32_16x16x32_bf16 v[148:151], v[68:71], v[36:39], v[148:151]\n\t" \
  "ds_read_b64_tr_b16 v[64:65], v96 offset:24576\n\t" \
  "ds_read_b64_tr_b16 v[66:67], v96 offset:26624\n\t" \
  "v_mfma_f32_16x16x32_bf16 v[136:139], v[72:75], v[32:35], v[136:139]\n\t" \
  "s_mov_b64 exec, vcc\n\t" \
  "global_load_dwordx4 v[20:23], v100, %[ob] offset:1024 sc1\n\t" \
  "s_mov_b64 exec, -1\n\t" \
  "v_mfma_f32_16x16x32_bf16 v[152:155], v[72:75], v[36:39], v[152:155]\n\t" \
  "ds_read_b64_tr_b16 v[68:69], v97 offset:24576\n\t" \
  "ds_read_b64_tr_b16 v[70:71], v97 offset:26624\n\t" \
  "v_mfma_f32_16x16x32_bf16 v[140:143], v[76:79], v[32:35], v[140:143]\n\t" \
  "s_mov_b64 exec, vcc\n\t" \
  "global_load_dwordx4 v[24:27], v100, %[ob] offset:2048 sc1\n\t" \
  "s_mov_b64 exec, -1\n\t" \
  "v_mfma_f32_16x16x32_bf16 v[156:159], v[76:79], v[36:39], v[156:159]\n\t" \
  "ds_read_b64_tr_b16 v[72:73], v98 offset:24576\n\t" \
  "ds_read_b64_tr_b16 v[74:75], v98 offset:26624\n\t" \
  "s_waitcnt lgkmcnt(10)\n\t" \
  "v_mfma_f32_16x16x32_bf16 v[128:131], v[80:83], v[40:43], v[128:131]\n\t" \
  "s_mov_b64 exec, vcc\n\t" \
  "global_load_dwordx4 v[28:31], v100, %[ob] offset:3072 sc1\n\t" \
  "s_mov_b64 exec, -1\n\t" \
  "v_mfma_f32_16x16x32_bf16 v[144:147], v[80:83], v[44:47], v[144:147]\n\t" \
  "ds_read_b64_tr_b16 v[76:77], v99 offset:24576\n\t" \
  "ds_read_b64_tr_b16 v[78:79], v99 offset:26624\n\t" \
  "v_mfma_f32_16x16x32_bf16 v[132:135], v[84:87], v[40:43], v[132:135]\n\t" \
  "ds_read_b64_tr_b16 v[80:81], v96 offset:28672\n\t" \
  "s_and_b32 %[t0], vcc_lo, 0x4000\n\t" \
  "ds_read_b32 v102, v101\n\t" \
  "v_mfma_f32_16x16x32_bf16 v[148:151], v[84:87], v[44:47], v[148:151]\n\t" \
  "ds_read_b64_tr_b16 v[82:83], v96 offset:30720\n\t" \
  "v_add_u32 v100, %[t0], v100\n\t" \
  "s_waitcnt lgkmcnt(11)\n\t" \
  "v_mfma_f32_16x16x32_bf16 v[136:139], v[88:91], v[40:43], v[136:139]\n\t" \
  "ds_read_b64_tr_b16 v[84:85], v97 offset:28672\n\t" \
  "v_and_b32 v100, 0x3ffff, v100\n\t" \
  "v_mfma_f32_16x16x32_bf16 v[152:155], v[88:91], v[44:47], v[152:155]\n\t" \
  "ds_read_b64_tr_b16 v[86:87], v97 offset:30720\n\t" \
  "s_sub_u32 %[iss], %[iss], vcc_lo\n\t" \
  "v_mfma_f32_16x16x32_bf16 v[140:143], v[92:95], v[40:43], v[140:143]\n\t" \
  "ds_read_b64_tr_b16 v[88:89], v98 offset:28672\n\t" \
  "s_add_u32 %[c], %[c], 1\n\t" \
  "v_mfma_f32_16x16x32_bf16 v[156:159], v[92:95], v[44:47], v[156:159]\n\t" \
  "ds_read_b64_tr_b16 v[90:91], v98 offset:30720\n\t" \
  "s_add_u32 %[th], %[th], 1\n\t" \
  "QBa3_%=:\n\t" \
  "s_cbranch_vccz QFa3_%=\n\t" \
  "s_waitcnt vmcnt(8)\n\t" \
  "QGa3_%=:\n\t" \
  "s_waitcnt lgkmcnt(11)\n\t" \
  "v_mfma_f32_16x16x32_bf16 v[128:131], v[64:67], v[48:51], v[128:131]\n\t" \
  "ds_read_b64_tr_b16 v[92:93], v99 offset:28672\n\t" \
  "v_mfma_f32_16x16x32_bf16 v[144:147], v[64:67], v[52:55], v[144:147]\n\t" \
  "ds_read_b64_tr_b16 v[94:95], v99 offset:30720\n\t" \
  "s_waitcnt lgkmcnt(7)\n\t" \
  "v_cmp_lt_u32 vcc, %[th], v102\n\t" \
  "v_mfma_f32_16x16x32_bf16 v[132:135], v[68:71], v[48:51], v[132:135]\n\t" \
  "s_mov_b64 exec, vcc\n\t" \
  "global_load_dwordx4 v[32:35], v100, %[ob] offset:0 sc1\n\t" \
  "s_mov_b64 exec, -1\n\t" \
  "v_mfma_f32_16x16x32_bf16 v[148:151], v[68:71], v[52:55], v[148:151]\n\t" \
  "ds_read_b64_tr_b16 v[64:65], v96 offset:32768\n\t" \
  "ds_read_b64_tr_b16 v[66:67], v96 offset:34816\n\t" \
  "v_mfma_f32_16x16x32_bf16 v[136:139], v[72:75], v[48:51], v[136:139]\n\t" \
  "s_mov_b64 exec, vcc\n\t" \
  "global_load_dwordx4 v[36:39], v100, %[ob] offset:1024 sc1\n\t" \
  "s_mov_b64 exec, -1\n\t" \
  "v_mfma_f32_16x16x32_bf16 v[152:155], v[72:75], v[52:55], v[152:155]\n\t" \
  "ds_read_b64_tr_b16 v[68:69], v97 offset:32768\n\t" \
  "ds_read_b64_tr_b16 v[70:71], v97 offset:34816\n\t" \
  "v_mfma_f32_16x16x32_bf16 v[140:143], v[76:79], v[48:51], v[140:143]\n\t" \
  "s_mov_b64 exec, vcc\n\t" \
  "global_load_dwordx4 v[40:43], v100, %[ob] offset:2048 sc1\n\t" \
  "s_mov_b64 exec, -1\n\t" \
  "v_mfma_f32_16x16x32_bf16 v[156:159], v[76:79], v[52:55], v[156:159]\n\t" \
  "ds_read_b64_tr_b16 v[72:73], v98 offset:32768\n\t" \
  "ds_read_b64_tr_b16 v[74:75], v98 offset:34816\n\t" \
  "s_waitcnt lgkmcnt(10)\n\t" \
  "v_mfma_f32_16x16x32_bf16 v[128:131], v[80:83], v[56:59], v[128:131]\n\t" \
  "s_mov_b64 exec, vcc\n\t" \
  "global_load_dwordx4 v[44:47], v100, %[ob] offset:3072 sc1\n\t" \
  "s_mov_b64 exec, -1\n\t" \
  "v_mfma_f32_16x16x32_bf16 v[144:147], v[80:83], v[60:63], v[144:147]\n\t" \
  "ds_read_b64_tr_b16 v[76:77], v99 offset:32768\n\t" \
  "ds_read_b64_tr_b16 v[78:79], v99 offset:34816\n\t" \
  "v_mfma_f32_16x16x32_bf16 v[132:135], v[84:87], v[56:59], v[132:135]\n\t" \
  "ds_read_b64_tr_b16 v[80:81], v96 offset:36864\n\t" \
  "s_and_b32 %[t0], vcc_lo, 0x4000\n\t" \
  "ds_read_b32 v102, v101\n\t" \
  "v_mfma_f32_16x16x32_bf16 v[148:151], v[84:87], v[60:63], v[148:151]\n\t" \
  "ds_read_b64_tr_b16 v[82:83], v96 offset:38912\n\t" \
  "v_add_u32 v100, %[t0], v100\n\t" \
  "s_waitcnt lgkmcnt(11)\n\t" \
  "v_mfma_f32_16x16x32_bf16 v[136:139], v[88:91], v[56:59], v[136:139]\n\t" \
  "ds_read_b64_tr_b16 v[84:85], v97 offset:36864\n\t" \
  "v_and_b32 v100, 0x3ffff, v100\n\t" \
  "v_mfma_f32_16x16x32_bf16 v[152:155], v[88:91], v[60:63], v[152:155]\n\t" \
  "ds_read_b64_tr_b16 v[86:87], v97 offset:38912\n\t" \
  "s_sub_u32 %[iss], %[iss], vcc_lo\n\t" \
  "v_mfma_f32_16x16x32_bf16 v[140:143], v[92:95], v[56:59], v[140:143]\n\t" \
  "ds_read_b64_tr_b16 v[88:89], v98 offset:36864\n\t" \
  "s_add_u32 %[c], %[c], 1\n\t" \
  "v_mfma_f32_16x16x32_bf16 v[156:159], v[92:95], v[60:63], v[156:159]\n\t" \
  "ds_read_b64_tr_b16 v[90:91], v98 offset:38912\n\t" \
  "s_add_u32 %[th], %[th], 1\n\t" \
  "s_cmp_lt_u32 %[c], 12\n\t" \
  "v_add_u32 v96, 0x8000, v96\n\t" \
  "v_add_u32 v97, 0x8000, v97\n\t" \
  "v_add_u32 v98, 0x8000, v98\n\t" \
  "v_add_u32 v99, 0x8000, v99\n\t" \
  "s_cbranch_scc1 QBa0_%=\n\t" \
  "QBb0_%=:\n\t" \
  "s_cbranch_vccz QFb0_%=\n\t" \
  "s_waitcnt vmcnt(8)\n\t" \
  "QGb0_%=:\n\t" \
  "s_waitcnt lgkmcnt(11)\n\t" \
  "v_mfma_f32_16x16x32_bf16 v[128:131], v[64:67], v[0:3], v[128:131]\n\t" \
  "ds_read_b64_tr_b16 v[92:93], v99 offset:4096\n\t" \
  "v_mfma_f32_16x16x32_bf16 v[144:147], v[64:67], v[4:7], v[144:147]\n\t" \
  "ds_read_b64_tr_b16 v[94:95], v99 offset:6144\n\t" \
  "s_waitcnt lgkmcnt(7)\n\t" \
  "v_cmp_lt_u32 vcc, %[th], v102\n\t" \
  "v_mfma_f32_16x16x32_bf16 v[132:135], v[68:71], v[0:3], v[132:135]\n\t" \
  "s_mov_b64 exec, vcc\n\t" \
  "global_load_dwordx4 v[48:51], v100, %[ob] offset:0 sc1\n\t" \
  "s_mov_b64 exec, -1\n\t" \
  "v_mfma_f32_16x16x32_bf16 v[148:151], v[68:71], v[4:7], v[148:151]\n\t" \
  "ds_read_b64_tr_b16 v[64:65], v96 offset:8192\n\t" \
  "ds_read_b64_tr_b16 v[66:67], v96 offset:10240\n\t" \
  "v_mfma_f32_16x16x32_bf16 v[136:139], v[72:75], v[0:3], v[136:139]\n\t" \
  "s_mov_b64 exec, vcc\n\t" \
  "global_load_dwordx4 v[52:55], v100, %[ob] offset:1024 sc1\n\t" \
  "s_mov_b64 exec, -1\n\t" \
  "v_mfma_f32_16x16x32_bf16 v[152:155], v[72:75], v[4:7], v[152:155]\n\t" \
  "ds_read_b64_tr_b16 v[68:69], v97 offset:8192\n\t" \
  "ds_read_b64_tr_b16 v[70:71], v97 offset:10240\n\t" \
  "v_mfma_f32_16x16x32_bf16 v[140:143], v[76:79], v[0:3], v[140:143]\n\t" \
  "s_mov_b64 exec, vcc\n\t" \
  "global_load_dwordx4 v[56:59], v100, %[ob] offset:2048 sc1\n\t" \
  "s_mov_b64 exec, -1\n\t" \
  "v_mfma_f32_16x16x32_bf16 v[156:159], v[76:79], v[4:7], v[156:159]\n\t" \
  "ds_read_b64_tr_b16 v[72:73], v98 offset:8192\n\t" \
  "ds_read_b64_tr_b16 v[74:75], v98 offset:10240\n\t" \
  "s_waitcnt lgkmcnt(10)\n\t" \
  "v_mfma_f32_16x16x32_bf16 v[128:131], v[80:83], v[8:11], v[128:131]\n\t" \
  "s_mov_b64 exec, vcc\n\t" \
  "global_load_dwordx4 v[60:63], v100, %[ob] offset:3072 sc1\n\t" \
  "s_mov_b64 exec, -1\n\t" \
  "v_mfma_f32_16x16x32_bf16 v[144:147], v[80:83], v[12:15], v[144:147]\n\t" \
  "ds_read_b64_tr_b16 v[76:77], v99 offset:8192\n\t" \
  "ds_read_b64_tr_b16 v[78:79], v99 offset:10240\n\t" \
  "v_mfma_f32_16x16x32_bf16 v[132:135], v[84:87], v[8:11], v[132:135]\n\t" \
  "ds_read_b64_tr_b16 v[80:81], v96 offset:12288\n\t" \
  "s_and_b32 %[t0], vcc_lo, 0x4000\n\t" \
  "v_mfma_f32_16x16x32_bf16 v[148:151], v[84:87], v[12:15], v[148:151]\n\t" \
  "ds_read_b64_tr_b16 v[82:83], v96 offset:14336\n\t" \
  "v_add_u32 v100, %[t0], v100\n\t" \
  "s_waitcnt lgkmcnt(10)\n\t" \
  "v_mfma_f32_16x16x32_bf16 v[136:139], v[88:91], v[8:11], v[136:139]\n\t" \
  "ds_read_b64_tr_b16 v[84:85], v97 offset:12288\n\t" \
  "v_and_b32 v100, 0x3ffff, v100\n\t" \
  "v_mfma_f32_16x16x32_bf16 v[152:155], v[88:91], v[12:15], v[152:155]\n\t" \
  "ds_read_b64_tr_b16 v[86:87], v97 offset:14336\n\t" \
  "s_sub_u32 %[iss], %[iss], vcc_lo\n\t" \
  "v_mfma_f32_16x16x32_bf16 v[140:143], v[92:95], v[8:11], v[140:143]\n\t" \
  "ds_read_b64_tr_b16 v[88:89], v98 offset:12288\n\t" \
  "s_add_u32 %[c], %[c], 1\n\t" \
  "v_mfma_f32_16x16x32_bf16 v[156:159], v[92:95], v[12:15], v[156:159]\n\t" \
  "ds_read_b64_tr_b16 v[90:91], v98 offset:14336\n\t" \
  "s_add_u32 %[th], %[th], 1\n\t" \
  "QBb1_%=:\n\t" \
  "s_cbranch_vccz QFb1_%=\n\t" \
  "s_waitcnt vmcnt(8)\n\t" \
  "QGb1_%=:\n\t" \
  "s_waitcnt lgkmcnt(10)\n\t" \
  "v_mfma_f32_16x16x32_bf16 v[128:131], v[64:67], v[16:19], v[128:131]\n\t" \
  "ds_read_b64_tr_b16 v[92:93], v99 offset:12288\n\t" \
  "v_mfma_f32_16x16x32_bf16 v[144:147], v[64:67], v[20:23], v[144:147]\n\t" \
  "ds_read_b64_tr_b16 v[94:95], v99 offset:14336\n\t" \
  "v_mfma_f32_16x16x32_bf16 v[132:135], v[68:71], v[16:19], v[132:135]\n\t" \
  "ds_read_b64_tr_b16 v[64:65], v96 offset:16384\n\t" \
  "v_mfma_f32_16x16x32_bf16 v[148:151], v[68:71], v[20:23], v[148:151]\n\t" \
  "ds_read_b64_tr_b16 v[66:67], v96 offset:18432\n\t" \
  "s_waitcnt lgkmcnt(10)\n\t" \
  "v_mfma_f32_16x16x32_bf16 v[136:139], v[72:75], v[16:19], v[136:139]\n\t" \
  "ds_read_b64_tr_b16 v[68:69], v97 offset:16384\n\t" \
  "v_mfma_f32_16x16x32_bf16 v[152:155], v[72:75], v[20:23], v[152:155]\n\t" \
  "ds_read_b64_tr_b16 v[70:71], v97 offset:18432\n\t" \
  "v_mfma_f32_16x16x32_bf16 v[140:143], v[76:79], v[16:19], v[140:143]\n\t" \
  "ds_read_b64_tr_b16 v[72:73], v98 offset:16384\n\t" \
  "v_mfma_f32_16x16x32_bf16 v[156:159], v[76:79], v[20:23], v[156:159]\n\t" \
  "ds_read_b64_tr_b16 v[74:75], v98 offset:18432\n\t" \
  "s_waitcnt lgkmcnt(10)\n\t" \
  "v_mfma_f32_16x16x32_bf16 v[128:131], v[80:83], v[24:27], v[128:131]\n\t" \
  "ds_read_b64_tr_b16 v[76:77], v99 offset:16384\n\t" \
  "v_mfma_f32_16x16x32_bf16 v[144:147], v[80:83], v[28:31], v[144:147]\n\t" \
  "ds_read_b64_tr_b16 v[78:79], v99 offset:18432\n\t" \
  "v_mfma_f32_16x16x32_bf16 v[132:135], v[84:87], v[24:27], v[132:135]\n\t" \
  "ds_read_b64_tr_b16 v[80:81], v96 offset:20480\n\t" \
  "v_mfma_f32_16x16x32_bf16 v[148:151], v[84:87], v[28:31], v[148:151]\n\t" \
  "ds_read_b64_tr_b16 v[82:83], v96 offset:22528\n\t" \
  "s_waitcnt lgkmcnt(10)\n\t" \
  "v_mfma_f32_16x16x32_bf16 v[136:139], v[88:91], v[24:27], v[136:139]\n\t" \
  "ds_read_b64_tr_b16 v[84:85], v97 offset:20480\n\t" \
  "v_mfma_f32_16x16x32_bf16 v[152:155], v[88:91], v[28:31], v[152:155]\n\t" \
  "ds_read_b64_tr_b16 v[86:87], v97 offset:22528\n\t" \
  "v_mfma_f32_16x16x32_bf16 v[140:143], v[92:95], v[24:27], v[140:143]\n\t" \
  "ds_read_b64_tr_b16 v[88:89], v98 offset:20480\n\t" \
  "s_add_u32 %[c], %[c], 1\n\t" \
  "v_mfma_f32_16x16x32_bf16 v[156:159], v[92:95], v[28:31], v[156:159]\n\t" \
  "ds_read_b64_tr_b16 v[90:91], v98 offset:22528\n\t" \
  "QBb2_%=:\n\t" \
  "s_cbranch_vccz QFb2_%=\n\t" \
  "s_waitcnt vmcnt(4)\n\t" \
  "QGb2_%=:\n\t" \
  "s_waitcnt lgkmcnt(10)\n\t" \
  "v_mfma_f32_16x16x32_bf16 v[128:131], v[64:67], v[32:35], v[128:131]\n\t" \
  "ds_read_b64_tr_b16 v[92:93], v99 offset:20480\n\t" \
  "v_mfma_f32_16x16x32_bf16 v[144:147], v[64:67], v[36:39], v[144:147]\n\t" \
  "ds_read_b64_tr_b16 v[94:95], v99 offset:22528\n\t" \
  "v_mfma_f32_16x16x32_bf16 v[132:135], v[68:71], v[32:35], v[132:135]\n\t" \
  "ds_read_b64_tr_b16 v[64:65], v96 offset:24576\n\t" \
  "v_mfma_f32_16x16x32_bf16 v[148:151], v[68:71], v[36:39], v[148:151]\n\t" \
  "ds_read_b64_tr_b16 v[66:67], v96 offset:26624\n\t" \
  "s_waitcnt lgkmcnt(10)\n\t" \
  "v_mfma_f32_16x16x32_bf16 v[136:139], v[72:75], v[32:35], v[136:139]\n\t" \
  "ds_read_b64_tr_b16 v[68:69], v97 offset:24576\n\t" \
  "v_mfma_f32_16x16x32_bf16 v[152:155], v[72:75], v[36:39], v[152:155]\n\t" \
  "ds_read_b64_tr_b16 v[70:71], v97 offset:26624\n\t" \
  "v_mfma_f32_16x16x32_bf16 v[140:143], v[76:79], v[32:35], v[140:143]\n\t" \
  "ds_read_b64_tr_b16 v[72:73], v98 offset:24576\n\t" \
  "v_mfma_f32_16x16x32_bf16 v[156:159], v[76:79], v[36:39], v[156:159]\n\t" \
  "ds_read_b64_tr_b16 v[74:75], v98 offset:26624\n\t" \
  "s_waitcnt lgkmcnt(10)\n\t" \
  "v_mfma_f32_16x16x32_bf16 v[128:131], v[80:83], v[40:43], v[128:131]\n\t" \
  "ds_read_b64_tr_b16 v[76:77], v99 offset:24576\n\t" \
  "v_mfma_f32_16x16x32_bf16 v[144:147], v[80:83], v[44:47], v[144:147]\n\t" \
  "ds_read_b64_tr_b16 v[78:79], v99 offset:26624\n\t" \
  "v_mfma_f32_16x16x32_bf16 v[132:135], v[84:87], v[40:43], v[132:135]\n\t" \
  "ds_read_b64_tr_b16 v[80:81], v96 offset:28672\n\t" \
  "v_mfma_f32_16x16x32_bf16 v[148:151], v[84:87], v[44:47], v[148:151]\n\t" \
  "ds_read_b64_tr_b16 v[82:83], v96 offset:30720\n\t" \
  "s_waitcnt lgkmcnt(10)\n\t" \
  "v_mfma_f32_16x16x32_bf16 v[136:139], v[88:91], v[40:43], v[136:139]\n\t" \
  "ds_read_b64_tr_b16 v[84:85], v97 offset:28672\n\t" \
  "v_mfma_f32_16x16x32_bf16 v[152:155], v[88:91], v[44:47], v[152:155]\n\t" \
  "ds_read_b64_tr_b16 v[86:87], v97 offset:30720\n\t" \
  "v_mfma_f32_16x16x32_bf16 v[140:143], v[92:95], v[40:43], v[140:143]\n\t" \
  "ds_read_b64_tr_b16 v[88:89], v98 offset:28672\n\t" \
  "s_add_u32 %[c], %[c], 1\n\t" \
  "v_mfma_f32_16x16x32_bf16 v[156:159], v[92:95], v[44:47], v[156:159]\n\t" \
  "ds_read_b64_tr_b16 v[90:91], v98 offset:30720\n\t" \
  "QBb3_%=:\n\t" \
  "s_cbranch_vccz QFb3_%=\n\t" \
  "s_waitcnt vmcnt(0)\n\t" \
  "QGb3_%=:\n\t" \
  "s_waitcnt lgkmcnt(10)\n\t" \
  "v_mfma_f32_16x16x32_bf16 v[128:131], v[64:67], v[48:51], v[128:131]\n\t" \
  "ds_read_b64_tr_b16 v[92:93], v99 offset:28672\n\t" \
  "v_mfma_f32_16x16x32_bf16 v[144:147], v[64:67], v[52:55], v[144:147]\n\t" \
  "ds_read_b64_tr_b16 v[94:95], v99 offset:30720\n\t" \
  "v_mfma_f32_16x16x32_bf16 v[132:135], v[68:71], v[48:51], v[132:135]\n\t" \
  "v_mfma_f32_16x16x32_bf16 v[148:151], v[68:71], v[52:55], v[148:151]\n\t" \
  "s_waitcnt lgkmcnt(8)\n\t" \
  "v_mfma_f32_16x16x32_bf16 v[136:139], v[72:75], v[48:51], v[136:139]\n\t" \
  "v_mfma_f32_16x16x32_bf16 v[152:155], v[72:75], v[52:55], v[152:155]\n\t" \
  "v_mfma_f32_16x16x32_bf16 v[140:143], v[76:79], v[48:51], v[140:143]\n\t" \
  "v_mfma_f32_16x16x32_bf16 v[156:159], v[76:79], v[52:55], v[156:159]\n\t" \
  "s_waitcnt lgkmcnt(4)\n\t" \
  "v_mfma_f32_16x16x32_bf16 v[128:131], v[80:83], v[56:59], v[128:131]\n\t" \
  "v_mfma_f32_16x16x32_bf16 v[144:147], v[80:83], v[60:63], v[144:147]\n\t" \
  "v_mfma_f32_16x16x32_bf16 v[132:135], v[84:87], v[56:59], v[132:135]\n\t" \
  "v_mfma_f32_16x16x32_bf16 v[148:151], v[84:87], v[60:63], v[148:151]\n\t" \
  "s_waitcnt lgkmcnt(0)\n\t" \
  "v_mfma_f32_16x16x32_bf16 v[136:139], v[88:91], v[56:59], v[136:139]\n\t" \
  "v_mfma_f32_16x16x32_bf16 v[152:155], v[88:91], v[60:63], v[152:155]\n\t" \
  "v_mfma_f32_16x16x32_bf16 v[140:143], v[92:95], v[56:59], v[140:143]\n\t" \
  "s_add_u32 %[c], %[c], 1\n\t" \
  "v_mfma_f32_16x16x32_bf16 v[156:159], v[92:95], v[60:63], v[156:159]\n\t" \
  "s_branch QX_%=\n\t" \
  "QFa0_%=:\n\t" \
  "s_getpc_b64 s[92:93]\n\t" \
  "s_branch QC_%=\n\t" \
  "s_branch QGa0_%=\n\t" \
  "QFa1_%=:\n\t" \
  "s_getpc_b64 s[92:93]\n\t" \
  "s_branch QC_%=\n\t" \
  "s_branch QGa1_%=\n\t" \
  "QFa2_%=:\n\t" \
  "s_getpc_b64 s[92:93]\n\t" \
  "s_branch QC_%=\n\t" \
  "s_branch QGa2_%=\n\t" \
  "QFa3_%=:\n\t" \
  "s_getpc_b64 s[92:93]\n\t" \
  "s_branch QC_%=\n\t" \
  "s_branch QGa3_%=\n\t" \
  "QFb0_%=:\n\t" \
  "s_getpc_b64 s[92:93]\n\t" \
  "s_branch QC_%=\n\t" \
  "s_branch QGb0_%=\n\t" \
  "QFb1_%=:\n\t" \
  "s_getpc_b64 s[92:93]\n\t" \
  "s_branch QC_%=\n\t" \
  "s_branch QGb1_%=\n\t" \
  "QFb2_%=:\n\t" \
  "s_getpc_b64 s[92:93]\n\t" \
  "s_branch QC_%=\n\t" \
  "s_branch QGb2_%=\n\t" \
  "QFb3_%=:\n\t" \
  "s_getpc_b64 s[92:93]\n\t" \
  "s_branch QC_%=\n\t" \
  "s_branch QGb3_%=\n\t" \
  "QC_%=:\n\t" \
  "s_add_u32 s92, s92, 4\n\t" \
  "s_addc_u32 s93, s93, 0\n\t" \
  "QU_%=:\n\t" \
  "ds_read_b32 v102, v101\n\t" \
  "s_add_u32 %[t1], %[c], 3\n\t" \
  "s_waitcnt lgkmcnt(0)\n\t" \
  "v_readfirstlane_b32 %[t0], v102\n\t" \
  "s_nop 0\n\t" \
  "s_min_u32 %[t1], %[t1], %[t0]\n\t" \
  "QL_%=:\n\t" \
  "s_cmp_ge_u32 %[iss], %[t1]\n\t" \
  "s_cbranch_scc1 QD_%=\n\t" \
  "s_bitcmp1_b32 %[iss], 1\n\t" \
  "s_cbranch_scc1 QS2_%=\n\t" \
  "s_bitcmp1_b32 %[iss], 0\n\t" \
  "s_cbranch_scc1 QS1_%=\n\t" \
  "global_load_dwordx4 v[0:3], v100, %[ob] offset:0 sc1\n\t" \
  "global_load_dwordx4 v[4:7], v100, %[ob] offset:1024 sc1\n\t" \
  "global_load_dwordx4 v[8:11], v100, %[ob] offset:2048 sc1\n\t" \
  "global_load_dwordx4 v[12:15], v100, %[ob] offset:3072 sc1\n\t" \
  "v_add_u32 v100, 0x4000, v100\n\t" \
  "v_and_b32 v100, 0x3ffff, v100\n\t" \
  "s_add_u32 %[iss], %[iss], 1\n\t" \
  "s_cmp_ge_u32 %[iss], %[t1]\n\t" \
  "s_cbranch_scc1 QD_%=\n\t" \
  "QS1_%=:\n\t" \
  "global_load_dwordx4 v[16:19], v100, %[ob] offset:0 sc1\n\t" \
  "global_load_dwordx4 v[20:23], v100, %[ob] offset:1024 sc1\n\t" \
  "global_load_dwordx4 v[24:27], v100, %[ob] offset:2048 sc1\n\t" \
  "global_load_dwordx4 v[28:31], v100, %[ob] offset:3072 sc1\n\t" \
  "v_add_u32 v100, 0x4000, v100\n\t" \
  "v_and_b32 v100, 0x3ffff, v100\n\t" \
  "s_add_u32 %[iss], %[iss], 1\n\t" \
  "s_cmp_ge_u32 %[iss], %[t1]\n\t" \
  "s_cbranch_scc1 QD_%=\n\t" \
  "QS2_%=:\n\t" \
  "s_bitcmp1_b32 %[iss], 0\n\t" \
  "s_cbranch_scc1 QS3_%=\n\t" \
  "global_load_dwordx4 v[32:35], v100, %[ob] offset:0 sc1\n\t" \
  "global_load_dwordx4 v[36:39], v100, %[ob] offset:1024 sc1\n\t" \
  "global_load_dwordx4 v[40:43], v100, %[ob] offset:2048 sc1\n\t" \
  "global_load_dwordx4 v[44:47], v100, %[ob] offset:3072 sc1\n\t" \
  "v_add_u32 v100, 0x4000, v100\n\t" \
  "v_and_b32 v100, 0x3ffff, v100\n\t" \
  "s_add_u32 %[iss], %[iss], 1\n\t" \
  "s_cmp_ge_u32 %[iss], %[t1]\n\t" \
  "s_cbranch_scc1 QD_%=\n\t" \
  "QS3_%=:\n\t" \
  "global_load_dwordx4 v[48:51], v100, %[ob] offset:0 sc1\n\t" \
  "global_load_dwordx4 v[52:55], v100, %[ob] offset:1024 sc1\n\t" \
  "global_load_dwordx4 v[56:59], v100, %[ob] offset:2048 sc1\n\t" \
  "global_load_dwordx4 v[60:63], v100, %[ob] offset:3072 sc1\n\t" \
  "v_add_u32 v100, 0x4000, v100\n\t" \
  "v_and_b32 v100, 0x3ffff, v100\n\t" \
  "s_add_u32 %[iss], %[iss], 1\n\t" \
  "s_branch QL_%=\n\t" \
  "QD_%=:\n\t" \
  "s_cmp_gt_u32 %[iss], %[c]\n\t" \
  "s_cbranch_scc1 QW_%=\n\t" \
  "s_addk_i32 %[cn], 1\n\t" \
  "s_cmp_gt_u32 %[cn], 0x400000\n\t" \
  "s_cbranch_scc1 QT_%=\n\t" \
  "s_sleep 1\n\t" \
  "s_branch QU_%=\n\t" \
  "QT_%=:\n\t" \
  "s_add_u32 %[t1], %[c], 1\n\t" \
  "s_branch QL_%=\n\t" \
  "QW_%=:\n\t" \
  "s_cmp_eq_u32 %[iss], 16\n\t" \
  "s_cselect_b64 vcc, -1, 0\n\t" \
  "s_sub_u32 %[t0], %[iss], %[c]\n\t" \
  "s_cmp_eq_u32 %[t0], 3\n\t" \
  "s_cselect_b32 %[th], %[iss], 99\n\t" \
  "s_cbranch_scc1 QZ_%=\n\t" \
  "s_cmp_eq_u32 %[t0], 2\n\t" \
  "s_cbranch_scc1 QY_%=\n\t" \
  "s_waitcnt vmcnt(0)\n\t" \
  "s_setpc_b64 s[92:93]\n\t" \
  "QY_%=:\n\t" \
  "s_waitcnt vmcnt(4)\n\t" \
  "s_setpc_b64 s[92:93]\n\t" \
  "QZ_%=:\n\t" \
  "s_waitcnt vmcnt(8)\n\t" \
  "s_setpc_b64 s[92:93]\n\t" \
  "QX_%=:\n\t" \
  "s_nop 7\n\t"
#define G3_CLOB "v0", "v1", "v2", "v3", "v4", "v5", "v6", "v7", "v8", "v9", "v10", "v11", "v12", "v13", "v14", "v15", "v16", "v17", "v18", "v19", "v20", "v21", "v22", "v23", "v24", "v25", "v26", "v27", "v28", "v29", "v30", "v31", "v32", "v33", "v34", "v35", "v36", "v37", "v38", "v39", "v40", "v41", "v42", "v43", "v44", "v45", "v46", "v47", "v48", "v49", "v50", "v51", "v52", "v53", "v54", "v55", "v56", "v57", "v58", "v59", "v60", "v61", "v62", "v63", "v64", "v65", "v66", "v67", "v68", "v69", "v70", "v71", "v72", "v73", "v74", "v75", "v76", "v77", "v78", "v79", "v80", "v81", "v82", "v83", "v84", "v85", "v86", "v87", "v88", "v89", "v90", "v91", "v92", "v93", "v94", "v95", "v102", "s88", "s89", "s92", "s93"

#define PP_ASM \
  "s_mov_b32 %[t0], %[n0]\n\t" \
  "s_cmp_eq_u32 %[t0], 0\n\t" \
  "s_cbranch_scc1 PAx_%=\n\t" \
  "PA_%=:\n\t" \
  "s_barrier\n\t" \
  "s_sub_u32 %[t0], %[t0], 1\n\t" \
  "s_cmp_lg_u32 %[t0], 0\n\t" \
  "s_cbranch_scc1 PA_%=\n\t" \
  "PAx_%=:\n\t" \
  "s_mov_b32 m0, %[tsk]\n\t" \
  "s_mov_b64 exec, %[tm]\n\t" \
  "global_load_lds_dword v164, %[tp]\n\t" \
  "s_mov_b64 exec, -1\n\t" \
  "v_mov_b32 v60, 0\n\t" \
  "s_mov_b32 m0, %[shd]\n\t" \
  "global_load_dword v160, v60, %[fl] sc1\n\t" \
  "global_load_lds_dwordx4 v163, %[shp]\n\t" \
  "s_mov_b32 m0, %[tgd]\n\t" \
  "s_mov_b64 exec, 1\n\t" \
  "global_load_lds_dword v60, %[fl] sc1\n\t" \
  "s_mov_b64 exec, -1\n\t" \
  "global_load_ushort v161, v60, %[hp]\n\t" \
  "global_load_ushort v162, v60, %[dp]\n\t" \
  "global_load_dwordx4 v[0:3], v152, %[kb0]\n\t" \
  "global_load_dwordx4 v[4:7], v153, %[kb0]\n\t" \
  "global_load_dwordx4 v[8:11], v154, %[kb0]\n\t" \
  "global_load_dwordx4 v[12:15], v155, %[kb3] offset:0\n\t" \
  "global_load_dwordx4 v[16:19], v155, %[kb3] offset:2048\n\t" \
  "global_load_dwordx4 v[20:23], v155, %[kb5] offset:0\n\t" \
  "global_load_dwordx4 v[24:27], v155, %[kb5] offset:2048\n\t" \
  "global_load_dwordx4 v[28:31], v155, %[vb0] offset:0\n\t" \
  "global_load_dwordx4 v[32:35], v155, %[vb0] offset:2048\n\t" \
  "global_load_dwordx4 v[36:39], v155, %[vb2] offset:0\n\t" \
  "global_load_dwordx4 v[40:43], v155, %[vb2] offset:2048\n\t" \
  "s_mov_b32 %[t0], %[n1]\n\t" \
  "s_cmp_eq_u32 %[t0], 0\n\t" \
  "s_cbranch_scc1 PBx_%=\n\t" \
  "PB_%=:\n\t" \
  "s_barrier\n\t" \
  "s_sub_u32 %[t0], %[t0], 1\n\t" \
  "s_cmp_lg_u32 %[t0], 0\n\t" \
  "s_cbranch_scc1 PB_%=\n\t" \
  "PBx_%=:\n\t" \
  "s_waitcnt vmcnt(11)\n\t" \
  "ds_read_b128 v[44:47], v156 offset:0\n\t" \
  "ds_read_b128 v[48:51], v156 offset:16\n\t" \
  "ds_read_b128 v[52:55], v156 offset:32\n\t" \
  "ds_read_b128 v[56:59], v156 offset:48\n\t" \
  "s_waitcnt lgkmcnt(0)\n\t" \
  "v_lshlrev_b32 v64, 16, v44\n\t" \
  "v_and_b32 v65, 0xffff0000, v44\n\t" \
  "v_lshlrev_b32 v66, 16, v45\n\t" \
  "v_and_b32 v67, 0xffff0000, v45\n\t" \
  "v_lshlrev_b32 v68, 16, v46\n\t" \
  "v_and_b32 v69, 0xffff0000, v46\n\t" \
  "v_lshlrev_b32 v70, 16, v47\n\t" \
  "v_and_b32 v71, 0xffff0000, v47\n\t" \
  "v_lshlrev_b32 v72, 16, v48\n\t" \
  "v_and_b32 v73, 0xffff0000, v48\n\t" \
  "v_lshlrev_b32 v74, 16, v49\n\t" \
  "v_and_b32 v75, 0xffff0000, v49\n\t" \
  "v_lshlrev_b32 v76, 16, v50\n\t" \
  "v_and_b32 v77, 0xffff0000, v50\n\t" \
  "v_lshlrev_b32 v78, 16, v51\n\t" \
  "v_and_b32 v79, 0xffff0000, v51\n\t" \
  "v_lshlrev_b32 v80, 16, v52\n\t" \
  "v_and_b32 v81, 0xffff0000, v52\n\t" \
  "v_lshlrev_b32 v82, 16, v53\n\t" \
  "v_and_b32 v83, 0xffff0000, v53\n\t" \
  "v_lshlrev_b32 v84, 16, v54\n\t" \
  "v_and_b32 v85, 0xffff0000, v54\n\t" \
  "v_lshlrev_b32 v86, 16, v55\n\t" \
  "v_and_b32 v87, 0xffff0000, v55\n\t" \
  "v_lshlrev_b32 v88, 16, v56\n\t" \
  "v_and_b32 v89, 0xffff0000, v56\n\t" \
  "v_lshlrev_b32 v90, 16, v57\n\t" \
  "v_and_b32 v91, 0xffff0000, v57\n\t" \
  "v_lshlrev_b32 v92, 16, v58\n\t" \
  "v_and_b32 v93, 0xffff0000, v58\n\t" \
  "v_lshlrev_b32 v94, 16, v59\n\t" \
  "v_and_b32 v95, 0xffff0000, v59\n\t" \
  "s_barrier\n\t" \
  "s_waitcnt vmcnt(8)\n\t" \
  "s_cmp_eq_u64 %[zm], 0\n\t" \
  "s_cbranch_scc1 PZ_%=\n\t" \
  "s_mov_b64 exec, %[zm]\n\t" \
  "v_mov_b32 v0, 0\n\t" \
  "v_mov_b32 v1, 0\n\t" \
  "v_mov_b32 v2, 0\n\t" \
  "v_mov_b32 v3, 0\n\t" \
  "v_mov_b32 v4, 0\n\t" \
  "v_mov_b32 v5, 0\n\t" \
  "v_mov_b32 v6, 0\n\t" \
  "v_mov_b32 v7, 0\n\t" \
  "v_mov_b32 v8, 0\n\t" \
  "v_mov_b32 v9, 0\n\t" \
  "v_mov_b32 v10, 0\n\t" \
  "v_mov_b32 v11, 0\n\t" \
  "s_mov_b64 exec, -1\n\t" \
  "PZ_%=:\n\t" \
  "v_lshlrev_b32 v128, 16, v0\n\t" \
  "v_and_b32 v129, 0xffff0000, v0\n\t" \
  "v_lshlrev_b32 v130, 16, v1\n\t" \
  "v_and_b32 v131, 0xffff0000, v1\n\t" \
  "v_lshlrev_b32 v132, 16, v2\n\t" \
  "v_and_b32 v133, 0xffff0000, v2\n\t" \
  "v_lshlrev_b32 v134, 16, v3\n\t" \
  "v_and_b32 v135, 0xffff0000, v3\n\t" \
  "v_mul_f32 v96, v128, v67\n\t" \
  "v_mul_f32 v97, v129, v71\n\t" \
  "v_mul_f32 v98, v130, v75\n\t" \
  "v_mul_f32 v99, v131, v79\n\t" \
  "v_mul_f32 v100, v132, v83\n\t" \
  "v_mul_f32 v101, v133, v87\n\t" \
  "v_mul_f32 v102, v134, v91\n\t" \
  "v_mul_f32 v103, v135, v95\n\t" \
  "v_lshlrev_b32 v128, 16, v4\n\t" \
  "v_and_b32 v129, 0xffff0000, v4\n\t" \
  "v_lshlrev_b32 v130, 16, v5\n\t" \
  "v_and_b32 v131, 0xffff0000, v5\n\t" \
  "v_lshlrev_b32 v132, 16, v6\n\t" \
  "v_and_b32 v133, 0xffff0000, v6\n\t" \
  "v_lshlrev_b32 v134, 16, v7\n\t" \
  "v_and_b32 v135, 0xffff0000, v7\n\t" \
  "v_fmac_f32 v96, v128, v66\n\t" \
  "v_fmac_f32 v97, v129, v70\n\t" \
  "v_fmac_f32 v98, v130, v74\n\t" \
  "v_fmac_f32 v99, v131, v78\n\t" \
  "v_fmac_f32 v100, v132, v82\n\t" \
  "v_fmac_f32 v101, v133, v86\n\t" \
  "v_fmac_f32 v102, v134, v90\n\t" \
  "v_fmac_f32 v103, v135, v94\n\t" \
  "v_mul_f32 v104, v128, v67\n\t" \
  "v_mul_f32 v105, v129, v71\n\t" \
  "v_mul_f32 v106, v130, v75\n\t" \
  "v_mul_f32 v107, v131, v79\n\t" \
  "v_mul_f32 v108, v132, v83\n\t" \
  "v_mul_f32 v109, v133, v87\n\t" \
  "v_mul_f32 v110, v134, v91\n\t" \
  "v_mul_f32 v111, v135, v95\n\t" \
  "s_barrier\n\t" \
  "v_lshlrev_b32 v128, 16, v8\n\t" \
  "v_and_b32 v129, 0xffff0000, v8\n\t" \
  "v_lshlrev_b32 v130, 16, v9\n\t" \
  "v_and_b32 v131, 0xffff0000, v9\n\t" \
  "v_lshlrev_b32 v132, 16, v10\n\t" \
  "v_and_b32 v133, 0xffff0000, v10\n\t" \
  "v_lshlrev_b32 v134, 16, v11\n\t" \
  "v_and_b32 v135, 0xffff0000, v11\n\t" \
  "v_fmac_f32 v96, v128, v65\n\t" \
  "v_fmac_f32 v97, v129, v69\n\t" \
  "v_fmac_f32 v98, v130, v73\n\t" \
  "v_fmac_f32 v99, v131, v77\n\t" \
  "v_fmac_f32 v100, v132, v81\n\t" \
  "v_fmac_f32 v101, v133, v85\n\t" \
  "v_fmac_f32 v102, v134, v89\n\t" \
  "v_fmac_f32 v103, v135, v93\n\t" \
  "v_fmac_f32 v104, v128, v66\n\t" \
  "v_fmac_f32 v105, v129, v70\n\t" \
  "v_fmac_f32 v106, v130, v74\n\t" \
  "v_fmac_f32 v107, v131, v78\n\t" \
  "v_fmac_f32 v108, v132, v82\n\t" \
  "v_fmac_f32 v109, v133, v86\n\t" \
  "v_fmac_f32 v110, v134, v90\n\t" \
  "v_fmac_f32 v111, v135, v94\n\t" \
  "v_mul_f32 v112, v128, v67\n\t" \
  "v_mul_f32 v113, v129, v71\n\t" \
  "v_mul_f32 v114, v130, v75\n\t" \
  "v_mul_f32 v115, v131, v79\n\t" \
  "v_mul_f32 v116, v132, v83\n\t" \
  "v_mul_f32 v117, v133, v87\n\t" \
  "v_mul_f32 v118, v134, v91\n\t" \
  "v_mul_f32 v119, v135, v95\n\t" \
  "s_barrier\n\t" \
  "s_waitcnt vmcnt(0)\n\t" \
  "v_lshlrev_b32 v128, 16, v12\n\t" \
  "v_and_b32 v129, 0xffff0000, v12\n\t" \
  "v_lshlrev_b32 v130, 16, v13\n\t" \
  "v_and_b32 v131, 0xffff0000, v13\n\t" \
  "v_lshlrev_b32 v132, 16, v14\n\t" \
  "v_and_b32 v133, 0xffff0000, v14\n\t" \
  "v_lshlrev_b32 v134, 16, v15\n\t" \
  "v_and_b32 v135, 0xffff0000, v15\n\t" \
  "v_fmac_f32 v96, v128, v64\n\t" \
  "v_fmac_f32 v97, v129, v68\n\t" \
  "v_fmac_f32 v98, v130, v72\n\t" \
  "v_fmac_f32 v99, v131, v76\n\t" \
  "v_fmac_f32 v100, v132, v80\n\t" \
  "v_fmac_f32 v101, v133, v84\n\t" \
  "v_fmac_f32 v102, v134, v88\n\t" \
  "v_fmac_f32 v103, v135, v92\n\t" \
  "v_fmac_f32 v104, v128, v65\n\t" \
  "v_fmac_f32 v105, v129, v69\n\t" \
  "v_fmac_f32 v106, v130, v73\n\t" \
  "v_fmac_f32 v107, v131, v77\n\t" \
  "v_fmac_f32 v108, v132, v81\n\t" \
  "v_fmac_f32 v109, v133, v85\n\t" \
  "v_fmac_f32 v110, v134, v89\n\t" \
  "v_fmac_f32 v111, v135, v93\n\t" \
  "v_fmac_f32 v112, v128, v66\n\t" \
  "v_fmac_f32 v113, v129, v70\n\t" \
  "v_fmac_f32 v114, v130, v74\n\t" \
  "v_fmac_f32 v115, v131, v78\n\t" \
  "v_fmac_f32 v116, v132, v82\n\t" \
  "v_fmac_f32 v117, v133, v86\n\t" \
  "v_fmac_f32 v118, v134, v90\n\t" \
  "v_fmac_f32 v119, v135, v94\n\t" \
  "v_mul_f32 v120, v128, v67\n\t" \
  "v_mul_f32 v121, v129, v71\n\t" \
  "v_mul_f32 v122, v130, v75\n\t" \
  "v_mul_f32 v123, v131, v79\n\t" \
  "v_mul_f32 v124, v132, v83\n\t" \
  "v_mul_f32 v125, v133, v87\n\t" \
  "v_mul_f32 v126, v134, v91\n\t" \
  "v_mul_f32 v127, v135, v95\n\t" \
  "s_barrier\n\t" \
  "v_lshlrev_b32 v128, 16, v16\n\t" \
  "v_and_b32 v129, 0xffff0000, v16\n\t" \
  "v_lshlrev_b32 v130, 16, v17\n\t" \
  "v_and_b32 v131, 0xffff0000, v17\n\t" \
  "v_fmac_f32 v104, v128, v64\n\t" \
  "v_fmac_f32 v105, v129, v68\n\t" \
  "v_fmac_f32 v106, v130, v72\n\t" \
  "v_fmac_f32 v107, v131, v76\n\t" \
  "v_fmac_f32 v112, v128, v65\n\t" \
  "v_fmac_f32 v113, v129, v69\n\t" \
  "v_fmac_f32 v114, v130, v73\n\t" \
  "v_fmac_f32 v115, v131, v77\n\t" \
  "v_fmac_f32 v120, v128, v66\n\t" \
  "v_fmac_f32 v121, v129, v70\n\t" \
  "v_fmac_f32 v122, v130, v74\n\t" \
  "v_fmac_f32 v123, v131, v78\n\t" \
  "v_lshlrev_b32 v128, 16, v28\n\t" \
  "v_and_b32 v129, 0xffff0000, v28\n\t" \
  "v_lshlrev_b32 v130, 16, v29\n\t" \
  "v_and_b32 v131, 0xffff0000, v29\n\t" \
  "v_lshlrev_b32 v132, 16, v30\n\t" \
  "v_and_b32 v133, 0xffff0000, v30\n\t" \
  "v_lshlrev_b32 v134, 16, v31\n\t" \
  "v_and_b32 v135, 0xffff0000, v31\n\t" \
  "v_mul_f32 v96, v96, v128\n\t" \
  "v_mul_f32 v97, v97, v129\n\t" \
  "v_mul_f32 v98, v98, v130\n\t" \
  "v_mul_f32 v99, v99, v131\n\t" \
  "v_mul_f32 v100, v100, v132\n\t" \
  "v_mul_f32 v101, v101, v133\n\t" \
  "v_mul_f32 v102, v102, v134\n\t" \
  "v_mul_f32 v103, v103, v135\n\t" \
  "v_cvt_pk_bf16_f32 v136, v96, v97\n\t" \
  "v_cvt_pk_bf16_f32 v137, v98, v99\n\t" \
  "v_cvt_pk_bf16_f32 v138, v100, v101\n\t" \
  "v_cvt_pk_bf16_f32 v139, v102, v103\n\t" \
  "s_barrier\n\t" \
  "v_lshlrev_b32 v132, 16, v18\n\t" \
  "v_and_b32 v133, 0xffff0000, v18\n\t" \
  "v_lshlrev_b32 v134, 16, v19\n\t" \
  "v_and_b32 v135, 0xffff0000, v19\n\t" \
  "v_fmac_f32 v108, v132, v80\n\t" \
  "v_fmac_f32 v109, v133, v84\n\t" \
  "v_fmac_f32 v110, v134, v88\n\t" \
  "v_fmac_f32 v111, v135, v92\n\t" \
  "v_fmac_f32 v116, v132, v81\n\t" \
  "v_fmac_f32 v117, v133, v85\n\t" \
  "v_fmac_f32 v118, v134, v89\n\t" \
  "v_fmac_f32 v119, v135, v93\n\t" \
  "v_fmac_f32 v124, v132, v82\n\t" \
  "v_fmac_f32 v125, v133, v86\n\t" \
  "v_fmac_f32 v126, v134, v90\n\t" \
  "v_fmac_f32 v127, v135, v94\n\t" \
  "v_lshlrev_b32 v128, 16, v32\n\t" \
  "v_and_b32 v129, 0xffff0000, v32\n\t" \
  "v_lshlrev_b32 v130, 16, v33\n\t" \
  "v_and_b32 v131, 0xffff0000, v33\n\t" \
  "v_lshlrev_b32 v132, 16, v34\n\t" \
  "v_and_b32 v133, 0xffff0000, v34\n\t" \
  "v_lshlrev_b32 v134, 16, v35\n\t" \
  "v_and_b32 v135, 0xffff0000, v35\n\t" \
  "v_mul_f32 v104, v104, v128\n\t" \
  "v_mul_f32 v105, v105, v129\n\t" \
  "v_mul_f32 v106, v106, v130\n\t" \
  "v_mul_f32 v107, v107, v131\n\t" \
  "v_mul_f32 v108, v108, v132\n\t" \
  "v_mul_f32 v109, v109, v133\n\t" \
  "v_mul_f32 v110, v110, v134\n\t" \
  "v_mul_f32 v111, v111, v135\n\t" \
  "v_cvt_pk_bf16_f32 v140, v104, v105\n\t" \
  "v_cvt_pk_bf16_f32 v141, v106, v107\n\t" \
  "v_cvt_pk_bf16_f32 v142, v108, v109\n\t" \
  "v_cvt_pk_bf16_f32 v143, v110, v111\n\t" \
  "s_barrier\n\t" \
  "v_lshlrev_b32 v128, 16, v20\n\t" \
  "v_and_b32 v129, 0xffff0000, v20\n\t" \
  "v_lshlrev_b32 v130, 16, v21\n\t" \
  "v_and_b32 v131, 0xffff0000, v21\n\t" \
  "v_lshlrev_b32 v132, 16, v22\n\t" \
  "v_and_b32 v133, 0xffff0000, v22\n\t" \
  "v_lshlrev_b32 v134, 16, v23\n\t" \
  "v_and_b32 v135, 0xffff0000, v23\n\t" \
  "v_fmac_f32 v112, v128, v64\n\t" \
  "v_fmac_f32 v113, v129, v68\n\t" \
  "v_fmac_f32 v114, v130, v72\n\t" \
  "v_fmac_f32 v115, v131, v76\n\t" \
  "v_fmac_f32 v116, v132, v80\n\t" \
  "v_fmac_f32 v117, v133, v84\n\t" \
  "v_fmac_f32 v118, v134, v88\n\t" \
  "v_fmac_f32 v119, v135, v92\n\t" \
  "v_fmac_f32 v120, v128, v65\n\t" \
  "v_fmac_f32 v121, v129, v69\n\t" \
  "v_fmac_f32 v122, v130, v73\n\t" \
  "v_fmac_f32 v123, v131, v77\n\t" \
  "v_fmac_f32 v124, v132, v81\n\t" \
  "v_fmac_f32 v125, v133, v85\n\t" \
  "v_fmac_f32 v126, v134, v89\n\t" \
  "v_fmac_f32 v127, v135, v93\n\t" \
  "v_lshlrev_b32 v128, 16, v36\n\t" \
  "v_and_b32 v129, 0xffff0000, v36\n\t" \
  "v_lshlrev_b32 v130, 16, v37\n\t" \
  "v_and_b32 v131, 0xffff0000, v37\n\t" \
  "v_lshlrev_b32 v132, 16, v38\n\t" \
  "v_and_b32 v133, 0xffff0000, v38\n\t" \
  "v_lshlrev_b32 v134, 16, v39\n\t" \
  "v_and_b32 v135, 0xffff0000, v39\n\t" \
  "v_mul_f32 v112, v112, v128\n\t" \
  "v_mul_f32 v113, v113, v129\n\t" \
  "v_mul_f32 v114, v114, v130\n\t" \
  "v_mul_f32 v115, v115, v131\n\t" \
  "s_barrier\n\t" \
  "v_mul_f32 v116, v116, v132\n\t" \
  "v_mul_f32 v117, v117, v133\n\t" \
  "v_mul_f32 v118, v118, v134\n\t" \
  "v_mul_f32 v119, v119, v135\n\t" \
  "v_cvt_pk_bf16_f32 v144, v112, v113\n\t" \
  "v_cvt_pk_bf16_f32 v145, v114, v115\n\t" \
  "v_cvt_pk_bf16_f32 v146, v116, v117\n\t" \
  "v_cvt_pk_bf16_f32 v147, v118, v119\n\t" \
  "v_lshlrev_b32 v128, 16, v24\n\t" \
  "v_and_b32 v129, 0xffff0000, v24\n\t" \
  "v_lshlrev_b32 v130, 16, v25\n\t" \
  "v_and_b32 v131, 0xffff0000, v25\n\t" \
  "v_lshlrev_b32 v132, 16, v26\n\t" \
  "v_and_b32 v133, 0xffff0000, v26\n\t" \
  "v_lshlrev_b32 v134, 16, v27\n\t" \
  "v_and_b32 v135, 0xffff0000, v27\n\t" \
  "v_fmac_f32 v120, v128, v64\n\t" \
  "v_fmac_f32 v121, v129, v68\n\t" \
  "v_fmac_f32 v122, v130, v72\n\t" \
  "v_fmac_f32 v123, v131, v76\n\t" \
  "v_fmac_f32 v124, v132, v80\n\t" \
  "v_fmac_f32 v125, v133, v84\n\t" \
  "v_fmac_f32 v126, v134, v88\n\t" \
  "v_fmac_f32 v127, v135, v92\n\t" \
  "s_barrier\n\t" \
  "v_lshlrev_b32 v128, 16, v40\n\t" \
  "v_and_b32 v129, 0xffff0000, v40\n\t" \
  "v_lshlrev_b32 v130, 16, v41\n\t" \
  "v_and_b32 v131, 0xffff0000, v41\n\t" \
  "v_lshlrev_b32 v132, 16, v42\n\t" \
  "v_and_b32 v133, 0xffff0000, v42\n\t" \
  "v_lshlrev_b32 v134, 16, v43\n\t" \
  "v_and_b32 v135, 0xffff0000, v43\n\t" \
  "v_mul_f32 v120, v120, v128\n\t" \
  "v_mul_f32 v121, v121, v129\n\t" \
  "v_mul_f32 v122, v122, v130\n\t" \
  "v_mul_f32 v123, v123, v131\n\t" \
  "v_mul_f32 v124, v124, v132\n\t" \
  "v_mul_f32 v125, v125, v133\n\t" \
  "v_mul_f32 v126, v126, v134\n\t" \
  "v_mul_f32 v127, v127, v135\n\t" \
  "v_cvt_pk_bf16_f32 v148, v120, v121\n\t" \
  "v_cvt_pk_bf16_f32 v149, v122, v123\n\t" \
  "v_cvt_pk_bf16_f32 v150, v124, v125\n\t" \
  "v_cvt_pk_bf16_f32 v151, v126, v127\n\t" \
  "ds_write_b128 v157, v[136:139] offset:0\n\t" \
  "ds_write_b128 v157, v[140:143] offset:1024\n\t" \
  "ds_write_b128 v157, v[144:147] offset:2048\n\t" \
  "ds_write_b128 v157, v[148:151] offset:3072\n\t" \
  "s_barrier\n\t" \
  "s_barrier\n\t" \
  "ds_read_b64_tr_b16 v[0:1], v158 offset:0\n\t" \
  "ds_read_b64_tr_b16 v[2:3], v158 offset:512\n\t" \
  "ds_read_b64_tr_b16 v[4:5], v158 offset:32\n\t" \
  "ds_read_b64_tr_b16 v[6:7], v158 offset:544\n\t" \
  "ds_read_b64_tr_b16 v[8:9], v158 offset:64\n\t" \
  "ds_read_b64_tr_b16 v[10:11], v158 offset:576\n\t" \
  "ds_read_b64_tr_b16 v[12:13], v158 offset:96\n\t" \
  "ds_read_b64_tr_b16 v[14:15], v158 offset:608\n\t" \
  "s_barrier\n\t" \
  "s_waitcnt lgkmcnt(0)\n\t" \
  "global_store_dwordx4 v159, v[0:3], %[sb] sc1\n\t" \
  "v_add_u32 v159, 0x10000, v159\n\t" \
  "global_store_dwordx4 v159, v[4:7], %[sb] sc1\n\t" \
  "v_add_u32 v159, 0x10000, v159\n\t" \
  "global_store_dwordx4 v159, v[8:11], %[sb] sc1\n\t" \
  "v_add_u32 v159, 0x10000, v159\n\t" \
  "global_store_dwordx4 v159, v[12:15], %[sb] sc1\n\t" \
  "s_nop 1\n\t"
#define PP_NB 11
#define PP_CLOB "v0", "v1", "v2", "v3", "v4", "v5", "v6", "v7", "v8", "v9", "v10", "v11", "v12", "v13", "v14", "v15", "v16", "v17", "v18", "v19", "v20", "v21", "v22", "v23", "v24", "v25", "v26", "v27", "v28", "v29", "v30", "v31", "v32", "v33", "v34", "v35", "v36", "v37", "v38", "v39", "v40", "v41", "v42", "v43", "v44", "v45", "v46", "v47", "v48", "v49", "v50", "v51", "v52", "v53", "v54", "v55", "v56", "v57", "v58", "v59", "v60", "v61", "v62", "v63", "v64", "v65", "v66", "v67", "v68", "v69", "v70", "v71", "v72", "v73", "v74", "v75", "v76", "v77", "v78", "v79", "v80", "v81", "v82", "v83", "v84", "v85", "v86", "v87", "v88", "v89", "v90", "v91", "v92", "v93", "v94", "v95", "v96", "v97", "v98", "v99", "v100", "v101", "v102", "v103", "v104", "v105", "v106", "v107", "v108", "v109", "v110", "v111", "v112", "v113", "v114", "v115", "v116", "v117", "v118", "v119", "v120", "v121", "v122", "v123", "v124", "v125", "v126", "v127", "v128", "v129", "v130", "v131", "v132", "v133", "v134", "v135", "v136", "v137", "v138", "v139", "v140", "v141", "v142", "v143", "v144", "v145", "v146", "v147", "v148", "v149", "v150", "v151"

#define WO_ASM \
  "v_add_u32 v4, 0x10000, v1\n\t" \
  "global_load_dwordx4 v[8:11], v0, %[wb]\n\t" \
  "v_add_u32 v0, 0x10000, v0\n\t" \
  "v_and_b32 v0, 0x1fffff, v0\n\t" \
  "global_load_dwordx4 v[12:15], v0, %[wb]\n\t" \
  "v_add_u32 v0, 0x10000, v0\n\t" \
  "v_and_b32 v0, 0x1fffff, v0\n\t" \
  "global_load_dwordx4 v[16:19], v0, %[wb]\n\t" \
  "v_add_u32 v0, 0x10000, v0\n\t" \
  "v_and_b32 v0, 0x1fffff, v0\n\t" \
  "s_waitcnt vmcnt(3)\n\t" \
  "s_barrier\n\t" \
  "s_sleep 4\n\t" \
  "global_load_dwordx4 v[20:23], v0, %[wb]\n\t" \
  "v_add_u32 v0, 0x10000, v0\n\t" \
  "v_and_b32 v0, 0x1fffff, v0\n\t" \
  "global_load_dwordx4 v[24:27], v0, %[wb]\n\t" \
  "v_add_u32 v0, 0x10000, v0\n\t" \
  "v_and_b32 v0, 0x1fffff, v0\n\t" \
  "global_load_dwordx4 v[28:31], v0, %[wb]\n\t" \
  "v_add_u32 v0, 0x10000, v0\n\t" \
  "v_and_b32 v0, 0x1fffff, v0\n\t" \
  "global_load_dwordx4 v[32:35], v0, %[wb]\n\t" \
  "v_add_u32 v0, 0x10000, v0\n\t" \
  "v_and_b32 v0, 0x1fffff, v0\n\t" \
  "global_load_dwordx4 v[36:39], v0, %[wb]\n\t" \
  "v_add_u32 v0, 0x10000, v0\n\t" \
  "v_and_b32 v0, 0x1fffff, v0\n\t" \
  "global_load_dwordx4 v[40:43], v0, %[wb]\n\t" \
  "v_add_u32 v0, 0x10000, v0\n\t" \
  "v_and_b32 v0, 0x1fffff, v0\n\t" \
  "global_load_dwordx4 v[44:47], v0, %[wb]\n\t" \
  "v_add_u32 v0, 0x10000, v0\n\t" \
  "v_and_b32 v0, 0x1fffff, v0\n\t" \
  "global_load_dwordx4 v[48:51], v0, %[wb]\n\t" \
  "v_add_u32 v0, 0x10000, v0\n\t" \
  "v_and_b32 v0, 0x1fffff, v0\n\t" \
  "global_load_dwordx4 v[52:55], v0, %[wb]\n\t" \
  "v_add_u32 v0, 0x10000, v0\n\t" \
  "v_and_b32 v0, 0x1fffff, v0\n\t" \
  "global_load_dwordx4 v[56:59], v0, %[wb]\n\t" \
  "v_add_u32 v0, 0x10000, v0\n\t" \
  "v_and_b32 v0, 0x1fffff, v0\n\t" \
  "global_load_dwordx4 v[60:63], v0, %[wb]\n\t" \
  "v_add_u32 v0, 0x10000, v0\n\t" \
  "v_and_b32 v0, 0x1fffff, v0\n\t" \
  "global_load_dwordx4 v[64:67], v0, %[wb]\n\t" \
  "v_add_u32 v0, 0x10000, v0\n\t" \
  "v_and_b32 v0, 0x1fffff, v0\n\t" \
  "global_load_dwordx4 v[68:71], v0, %[wb]\n\t" \
  "v_add_u32 v0, 0x10000, v0\n\t" \
  "v_and_b32 v0, 0x1fffff, v0\n\t" \
  "global_load_dwordx4 v[72:75], v0, %[wb]\n\t" \
  "v_add_u32 v0, 0x10000, v0\n\t" \
  "v_and_b32 v0, 0x1fffff, v0\n\t" \
  "global_load_dwordx4 v[76:79], v0, %[wb]\n\t" \
  "v_add_u32 v0, 0x10000, v0\n\t" \
  "v_and_b32 v0, 0x1fffff, v0\n\t" \
  "global_load_dwordx4 v[80:83], v0, %[wb]\n\t" \
  "v_add_u32 v0, 0x10000, v0\n\t" \
  "v_and_b32 v0, 0x1fffff, v0\n\t" \
  "global_load_dwordx4 v[84:87], v0, %[wb]\n\t" \
  "v_add_u32 v0, 0x10000, v0\n\t" \
  "v_and_b32 v0, 0x1fffff, v0\n\t" \
  "global_load_dwordx4 v[88:91], v0, %[wb]\n\t" \
  "v_add_u32 v0, 0x10000, v0\n\t" \
  "v_and_b32 v0, 0x1fffff, v0\n\t" \
  "global_load_dwordx4 v[92:95], v0, %[wb]\n\t" \
  "v_add_u32 v0, 0x10000, v0\n\t" \
  "v_and_b32 v0, 0x1fffff, v0\n\t" \
  "global_load_dwordx4 v[96:99], v0, %[wb]\n\t" \
  "v_add_u32 v0, 0x10000, v0\n\t" \
  "v_and_b32 v0, 0x1fffff, v0\n\t" \
  "global_load_dwordx4 v[100:103], v0, %[wb]\n\t" \
  "v_add_u32 v0, 0x10000, v0\n\t" \
  "v_and_b32 v0, 0x1fffff, v0\n\t" \
  "global_load_dwordx4 v[104:107], v0, %[wb]\n\t" \
  "v_add_u32 v0, 0x10000, v0\n\t" \
  "v_and_b32 v0, 0x1fffff, v0\n\t" \
  "global_load_dwordx4 v[108:111], v0, %[wb]\n\t" \
  "v_add_u32 v0, 0x10000, v0\n\t" \
  "v_and_b32 v0, 0x1fffff, v0\n\t" \
  "global_load_dwordx4 v[112:115], v0, %[wb]\n\t" \
  "v_add_u32 v0, 0x10000, v0\n\t" \
  "v_and_b32 v0, 0x1fffff, v0\n\t" \
  "global_load_dwordx4 v[116:119], v0, %[wb]\n\t" \
  "v_add_u32 v0, 0x10000, v0\n\t" \
  "v_and_b32 v0, 0x1fffff, v0\n\t" \
  "global_load_dwordx4 v[120:123], v0, %[wb]\n\t" \
  "v_add_u32 v0, 0x10000, v0\n\t" \
  "v_and_b32 v0, 0x1fffff, v0\n\t" \
  "global_load_dwordx4 v[124:127], v0, %[wb]\n\t" \
  "v_add_u32 v0, 0x10000, v0\n\t" \
  "v_and_b32 v0, 0x1fffff, v0\n\t" \
  "global_load_dwordx4 v[128:131], v0, %[wb]\n\t" \
  "v_add_u32 v0, 0x10000, v0\n\t" \
  "v_and_b32 v0, 0x1fffff, v0\n\t" \
  "global_load_dwordx4 v[132:135], v0, %[wb]\n\t" \
  "v_add_u32 v0, 0x10000, v0\n\t" \
  "v_and_b32 v0, 0x1fffff, v0\n\t" \
  "s_waitcnt vmcnt(31)\n\t" \
  "ds_write_b128 v1, v[8:11] offset:0\n\t" \
  "s_waitcnt vmcnt(30)\n\t" \
  "ds_write_b128 v1, v[12:15] offset:4096\n\t" \
  "s_waitcnt vmcnt(29)\n\t" \
  "ds_write_b128 v1, v[16:19] offset:8192\n\t" \
  "s_waitcnt vmcnt(28)\n\t" \
  "ds_write_b128 v1, v[20:23] offset:12288\n\t" \
  "s_waitcnt vmcnt(27)\n\t" \
  "ds_write_b128 v1, v[24:27] offset:16384\n\t" \
  "s_waitcnt vmcnt(26)\n\t" \
  "ds_write_b128 v1, v[28:31] offset:20480\n\t" \
  "WA_%=:\n\t" \
  "ds_read_b32 v3, v2 offset:0\n\t" \
  "s_addk_i32 %[cn], 1\n\t" \
  "s_cmp_gt_u32 %[cn], 0x400000\n\t" \
  "s_cbranch_scc1 WAx_%=\n\t" \
  "s_waitcnt lgkmcnt(0)\n\t" \
  "v_cmp_eq_u32 vcc, 0, v3\n\t" \
  "s_cbranch_vccz WAx_%=\n\t" \
  "s_sleep 1\n\t" \
  "s_branch WA_%=\n\t" \
  "WAx_%=:\n\t" \
  "s_waitcnt vmcnt(25)\n\t" \
  "ds_write_b128 v1, v[32:35] offset:24576\n\t" \
  "s_sleep 1\n\t" \
  "s_waitcnt vmcnt(24)\n\t" \
  "ds_write_b128 v1, v[36:39] offset:28672\n\t" \
  "s_sleep 1\n\t" \
  "s_waitcnt vmcnt(23)\n\t" \
  "ds_write_b128 v1, v[40:43] offset:32768\n\t" \
  "s_sleep 1\n\t" \
  "s_waitcnt vmcnt(22)\n\t" \
  "ds_write_b128 v1, v[44:47] offset:36864\n\t" \
  "s_sleep 1\n\t" \
  "s_waitcnt vmcnt(21)\n\t" \
  "ds_write_b128 v1, v[48:51] offset:40960\n\t" \
  "s_sleep 1\n\t" \
  "s_waitcnt vmcnt(20)\n\t" \
  "ds_write_b128 v1, v[52:55] offset:45056\n\t" \
  "s_sleep 1\n\t" \
  "s_waitcnt vmcnt(19)\n\t" \
  "ds_write_b128 v1, v[56:59] offset:49152\n\t" \
  "s_sleep 1\n\t" \
  "s_waitcnt vmcnt(18)\n\t" \
  "ds_write_b128 v1, v[60:63] offset:53248\n\t" \
  "s_sleep 1\n\t" \
  "s_waitcnt vmcnt(17)\n\t" \
  "ds_write_b128 v1, v[64:67] offset:57344\n\t" \
  "s_sleep 1\n\t" \
  "s_waitcnt vmcnt(16)\n\t" \
  "ds_write_b128 v1, v[68:71] offset:61440\n\t" \
  "s_sleep 1\n\t" \
  "s_waitcnt vmcnt(15)\n\t" \
  "ds_write_b128 v4, v[72:75] offset:0\n\t" \
  "s_sleep 1\n\t" \
  "s_waitcnt vmcnt(14)\n\t" \
  "ds_write_b128 v4, v[76:79] offset:4096\n\t" \
  "s_sleep 1\n\t" \
  "s_waitcnt vmcnt(9)\n\t" \
  "ds_write_b128 v4, v[96:99] offset:24576\n\t" \
  "s_sleep 1\n\t" \
  "s_waitcnt vmcnt(8)\n\t" \
  "ds_write_b128 v4, v[100:103] offset:28672\n\t" \
  "s_sleep 1\n\t" \
  "s_waitcnt vmcnt(7)\n\t" \
  "ds_write_b128 v4, v[104:107] offset:32768\n\t" \
  "s_sleep 1\n\t" \
  "s_waitcnt vmcnt(6)\n\t" \
  "ds_write_b128 v4, v[108:111] offset:36864\n\t" \
  "s_sleep 1\n\t" \
  "s_waitcnt vmcnt(5)\n\t" \
  "ds_write_b128 v4, v[112:115] offset:40960\n\t" \
  "s_sleep 1\n\t" \
  "s_waitcnt vmcnt(4)\n\t" \
  "ds_write_b128 v4, v[116:119] offset:45056\n\t" \
  "s_sleep 1\n\t" \
  "s_waitcnt vmcnt(3)\n\t" \
  "ds_write_b128 v4, v[120:123] offset:49152\n\t" \
  "s_sleep 1\n\t" \
  "s_waitcnt vmcnt(2)\n\t" \
  "ds_write_b128 v4, v[124:127] offset:53248\n\t" \
  "s_sleep 1\n\t" \
  "s_waitcnt vmcnt(1)\n\t" \
  "ds_write_b128 v4, v[128:131] offset:57344\n\t" \
  "s_sleep 1\n\t" \
  "s_waitcnt vmcnt(0)\n\t" \
  "ds_write_b128 v4, v[132:135] offset:61440\n\t" \
  "s_sleep 1\n\t" \
  "WB_%=:\n\t" \
  "ds_read_b32 v3, v2 offset:16\n\t" \
  "s_addk_i32 %[cn], 1\n\t" \
  "s_cmp_gt_u32 %[cn], 0x400000\n\t" \
  "s_cbranch_scc1 WBx_%=\n\t" \
  "s_waitcnt lgkmcnt(0)\n\t" \
  "v_cmp_eq_u32 vcc, 0, v3\n\t" \
  "s_cbranch_vccz WBx_%=\n\t" \
  "s_sleep 1\n\t" \
  "s_branch WB_%=\n\t" \
  "WBx_%=:\n\t" \
  "ds_write_b128 v4, v[80:83] offset:8192\n\t" \
  "s_sleep 1\n\t" \
  "ds_write_b128 v4, v[84:87] offset:12288\n\t" \
  "s_sleep 1\n\t" \
  "ds_write_b128 v4, v[88:91] offset:16384\n\t" \
  "s_sleep 1\n\t" \
  "ds_write_b128 v4, v[92:95] offset:20480\n\t" \
  "s_sleep 1\n\t" \
  "s_waitcnt lgkmcnt(0)\n\t"
#define WO_CLOB "v3", "v4", "v8", "v9", "v10", "v11", "v12", "v13", "v14", "v15", "v16", "v17", "v18", "v19", "v20", "v21", "v22", "v23", "v24", "v25", "v26", "v27", "v28", "v29", "v30", "v31", "v32", "v33", "v34", "v35", "v36", "v37", "v38", "v39", "v40", "v41", "v42", "v43", "v44", "v45", "v46", "v47", "v48", "v49", "v50", "v51", "v52", "v53", "v54", "v55", "v56", "v57", "v58", "v59", "v60", "v61", "v62", "v63", "v64", "v65", "v66", "v67", "v68", "v69", "v70", "v71", "v72", "v73", "v74", "v75", "v76", "v77", "v78", "v79", "v80", "v81", "v82", "v83", "v84", "v85", "v86", "v87", "v88", "v89", "v90", "v91", "v92", "v93", "v94", "v95", "v96", "v97", "v98", "v99", "v100", "v101", "v102", "v103", "v104", "v105", "v106", "v107", "v108", "v109", "v110", "v111", "v112", "v113", "v114", "v115", "v116", "v117", "v118", "v119", "v120", "v121", "v122", "v123", "v124", "v125", "v126", "v127", "v128", "v129", "v130", "v131", "v132", "v133", "v134", "v135"

#define TS(i)
#define TOUT(n)

#define SPIN(cond,slp) do{ int sp_=0; while(cond){ __builtin_amdgcn_s_sleep(slp); if(++sp_>(1<<22)) break; } }while(0)
__device__ __forceinline__ unsigned rd1(const char* p){ return __builtin_amdgcn_readfirstlane(*(volatile LDS_AS unsigned*)(LDS_AS char*)p); }
__device__ __forceinline__ unsigned lds_inc(const char* p, int l){ unsigned r=0; if(l==0) r=__hip_atomic_fetch_add((LDS_AS unsigned*)(LDS_AS char*)p, 1u, __ATOMIC_RELAXED, __HIP_MEMORY_SCOPE_WORKGROUP); return __builtin_amdgcn_readfirstlane(r); }
__device__ __forceinline__ void lds_add(unsigned adr){ asm volatile("s_mov_b64 exec, 1\n\tds_add_u32 %0, %1\n\ts_mov_b64 exec, -1" :: "v"(adr), "v"(1u) : "memory"); }
__device__ __forceinline__ void bars(int n){
  asm volatile("BL_%=:\n\ts_barrier\n\ts_sub_u32 %0, %0, 1\n\ts_cmp_lg_u32 %0, 0\n\ts_cbranch_scc1 BL_%=" : "+s"(n) :: "memory", "scc");
}
__device__ __forceinline__ void stout(void* sbase, unsigned voff, u32x4 v){
  st16cs(sbase,voff,v);
}

__global__ __launch_bounds__(768,3) void fused_kernel(const u16* __restrict__ x, const u16* __restrict__ wqkv,
    const u16* __restrict__ kin, const u16* __restrict__ vin, const u16* __restrict__ shk,
    u16* o, u16* kvT, const u16* __restrict__ hk, const u16* __restrict__ wo, const u16* __restrict__ qin,
    const u16* __restrict__ sD, u16* yT, u16* __restrict__ out, unsigned* flags)
{
  __shared__ __attribute__((aligned(16))) char smem[ALL_LDS];
  TS(0);
  const int w8=__builtin_amdgcn_readfirstlane(threadIdx.x>>6);
  const int w=w8&3;
  const int bid=blockIdx.x, xcd=bid&7, idx=bid>>3;
  const int rb=(xcd>>1)*4+(idx&3), hp=(xcd&1)*8+(idx>>2);
  const int s0=rb*128;
  const int k2=(hp+WROT*(rb&3))&15;
  #define LANEVARS int l; asm volatile("v_mbcnt_lo_u32_b32 %0, -1, 0\n\tv_mbcnt_hi_u32_b32 %0, -1, %0" : "=v"(l)); const int g=l>>4, fr=l&15, tq=(l&15)>>2, tp=l&3; (void)g; (void)fr; (void)tq; (void)tp;
  const unsigned sb0=__builtin_amdgcn_readfirstlane(lds_addr(smem));
  const char* syn=smem+SYN;
  const int hot0=(3-(int)((size_t)wqkv>>7))&7;

  if(w8>=8){
    char* cr=smem+C_OFF+w*CREG;
    const unsigned crs=sb0+C_OFF+w*CREG;
    const int sblk=bid>>3, td0=(bid&7)*128+(w&1)*64;
    const int d=bid*4+w;
    const int kv0=KV0+(sblk&KVS);
    static_assert(26-(KVW+2+PP_NB)-KV0-KVS>=1, "barrier budget");
    unsigned tagv, h0, dv;
    {
    LANEVARS
    if(w==0 && l<32 && l!=16) ((unsigned*)(smem+SYN))[l]= (HEARLY && (l>>2)==3)? 1u : 0u;
    if(l<32) *(u32x4*)(cr+l*16)=u32x4{0,0,0,0};
    asm volatile("" ::: "memory");
    __builtin_amdgcn_s_setprio(CPR);
    const char* hcol=(const char*)wqkv+((xcd&1)*8+hot0)*128+(size_t)idx*2*6144;
    const unsigned hto=(unsigned)((l/6)*393216+(l%6)*2048);
    {
      const int q=w*64+l, q2=(3-w)*64+l;
      if(w*64<8*TP1){ if(q<8*TP1) touch((const char*)x+(size_t)(xcd*256+idx*8)*2048, (q/TP1)*2048+(q%TP1)*128, sb0+SINK+256); }
      if((3-w)*64<12*TP1){ if(q2<12*TP1){ const int e=q2%12+(idx&1)*12; touch((const char*)wqkv+(size_t)((xcd>>1)*16+(idx>>1))*6144+(xcd&1)*1024, (q2/12)*393216+(e>>3)*2048+(e&7)*128, sb0+SINK+512); } }
      if(HOTP && w==(TP1>8? 0:1)){ if(l<48) touch(hcol+393216, hto, sb0+SINK+512); }
      if(TWO==1 && w==(TP1>8? 0:1)) touch((const char*)wo+(size_t)bid*8192, l*128, sb0+SINK+512);
    }
    {
      const int dg=l&7, rg=l>>3;
      const int rw=sblk*64+(w>>1)*32;
      const unsigned ko=(unsigned)(rg*8192+dg*16);
      const bool top=(rw==0 && rg==0);
      const char* kb=(const char*)(kin+(size_t)rw*DM+td0); const char* vb=(const char*)(vin+(size_t)rw*DM+td0);
      unsigned long long zm; asm("s_cmp_eq_u32 %1, 0\n\ts_cselect_b64 %0, 0xff, 0" : "=s"(zm) : "s"(rw) : "scc");
      unsigned long long tm; asm("s_cmp_eq_u32 %1, 0\n\ts_cselect_b64 %0, %2, 0\n\ts_cmp_eq_u32 %1, 1\n\ts_cselect_b64 %0, %3, %0" : "=&s"(tm) : "s"(w), "s"(HOTP? (1ull<<42)-1 : 0ull), "s"(TWO==3? ~0ull : 0ull) : "scc");
      unsigned so=(unsigned)((fr>>1)*8192+(fr&1)*64+g*16), t0;
      asm volatile(PP_ASM
        : [t0]"=&s"(t0), "+{v159}"(so), "=&{v160}"(tagv), "=&{v161}"(h0), "=&{v162}"(dv)
        : "{v152}"(top? ko+6144:ko), "{v153}"(top? ko+6144:ko+2048), "{v154}"(top? ko+6144:ko+4096), "{v155}"(ko), "{v156}"(crs+512+(w&1)*512+dg*64),
          "{v157}"(crs+512+l*16), "{v158}"(crs+512+(((g-(sblk>>1))&3)*8+tq)*128+tp*8),
          [kb0]"s"(kb-6144), [kb3]"s"(kb), [kb5]"s"(kb+4096), [vb0]"s"(vb), [vb2]"s"(vb+4096), [sb]"s"((char*)kvT+(size_t)td0*4096+sblk*256+(w>>1)*128),
          [n0]"s"(kv0), [n1]"n"(KVW-1), [zm]"s"(zm), "{v163}"(l*16), [shd]"s"(crs+512), [tgd]"s"(sb0+SYN+64), [shp]"s"(shk+(bid&7)*512), [fl]"s"(flags), [hp]"s"(hk+(size_t)d*S), [dp]"s"(sD+d),
          "{v164}"(w? (unsigned)(l*128) : (l<42? hto:0u)), [tsk]"s"(sb0+SINK+512), [tm]"s"(tm), [tp]"s"(w? (const char*)wo+(size_t)bid*8192 : hcol+9*393216)
        : "memory", "scc", PP_CLOB);
    }
    }
    LANEVARS
    const unsigned tag=__builtin_amdgcn_readfirstlane(tagv)+1u;
    unsigned hd; { const float t=bflo(h0)+bflo(dv); hd=pack2(t,t); }
    const unsigned hreg= HEARLY? crs+512 : sb0+X_OFF+w*4096;
    f32x16 A0,A1,A2,A3;
    auto hload=[&](){
      #pragma unroll
      for(int i=0;i<4;i++) glds(hk+(size_t)d*S+i*512, l*16, hreg+i*1024);
    };
    auto hfrag=[&](){
      const unsigned ha=hreg+(32*g-4*tq+4*tp)*2;
      #define HR(L) "ds_read_b64_tr_b16 v[" #L "*4+64:" #L "*4+65], %4 offset:" #L "*256+32\n\tds_read_b64_tr_b16 v[" #L "*4+66:" #L "*4+67], %4 offset:" #L "*256\n\t"
      asm volatile(HR(0) HR(1) HR(2) HR(3) HR(4) HR(5) HR(6) HR(7) HR(8) HR(9) HR(10) HR(11) HR(12) HR(13) HR(14) HR(15) "s_waitcnt lgkmcnt(0)"
        : "=&{v[64:79]}"(A0), "=&{v[80:95]}"(A1), "=&{v[96:111]}"(A2), "=&{v[112:127]}"(A3) : "v"(ha) : "memory");
      if(!HEARLY) lds_add(sb0+SYN+48+w*4);
      const unsigned k2=(l==0? 0u:0xffffu)|(l<=4? 0u:0xffff0000u), s2=(l==0? 0xffffu:0u)|(l==4? 0xffff0000u:0u);
      const unsigned k3=(l<=8? 0u:0xffffu)|(l<=12? 0u:0xffff0000u), s3=(l==8? 0xffffu:0u)|(l==12? 0xffff0000u:0u);
      A0[2]=__uint_as_float((__float_as_uint(A0[2])&k2)|(hd&s2));
      A0[3]=__uint_as_float((__float_as_uint(A0[3])&k3)|(hd&s3));
      asm volatile("" : "+{v[64:79]}"(A0));
    };
    auto wtch=[&](){ touch((const char*)wo+(size_t)(idx*32+w*8)*2048+(xcd&1)*1024, (l>>3)*2048+(l&7)*128, sb0+SINK+512); };
    if(HEARLY) hload();
    if(WTCH==1) wtch();
    SBAR(); SBAR(); SBAR();
    WAITV(0);
    if(l==0) stflag(flags+F_PP+(td0>>6)*64+sblk*2+(w>>1), tag);
    if(TWO==2 && w==0) touch((const char*)wo+(size_t)bid*8192, l*128, sb0+SINK+512);
    if(WTCH==2) wtch();
    if(HEARLY) hfrag();
    TS(2);
    bars(26-(KVW+2+PP_NB)-kv0);
    const unsigned* pf=flags+F_PP+(d>>6)*64;
    const char* kvp=(const char*)kvT+(size_t)(d>>1)*8192+(d&1)*64; const unsigned kvo=(unsigned)((l>>2)*128+(l&3)*16);
    unsigned pv; asm volatile("global_load_dword %0, %1, %2 sc1" : "=v"(pv) : "v"((unsigned)(l*4)), "s"(pf) : "memory");
      unsigned Rb[4][2];
      #pragma unroll
      for(int r=0;r<4;r++)
        #pragma unroll
        for(int k=0;k<2;k++){ const int q=fr-1-2*g+k; Rb[r][k]=crs+256+(q>>1)*64+((r+(q>>3))&3)*16+(q&1)*8; }
      int bidx=((l>=16)? l-16 : (l==0? 63 : l+47))*4;
      asm volatile("" : "+{v24}"(Rb[0][0]), "+{v25}"(Rb[0][1]), "+{v26}"(Rb[1][0]), "+{v27}"(Rb[1][1]), "+{v28}"(Rb[2][0]), "+{v29}"(Rb[2][1]), "+{v30}"(Rb[3][0]), "+{v31}"(Rb[3][1]), "+{v32}"(bidx));
    SBAR(); SBAR(); SBAR();
    asm volatile("s_waitcnt vmcnt(0)" : "+v"(pv) :: "memory");
    const bool have=__all(pv==tag);
    if(have){
      #pragma unroll
      for(int i=0;i<4;i++) gldsc(kvp+i*2048, kvo, crs+512+i*1024);
    }
    SBAR();
    if(!HEARLY) hload();
    const int fd0=(bid&15)*64, fs0=(bid>>4)*128+w*32;
    u32x4 qv[4];
    const unsigned qo=(unsigned)((l>>3)*2048+(l&7)*16); const char* qp=(const char*)(qin+(size_t)fs0*DM+fd0);
    #define QLOAD asm volatile("global_load_dwordx4 %0, %4, %5\n\tglobal_load_dwordx4 %1, %4, %6\n\tglobal_load_dwordx4 %2, %4, %7\n\tglobal_load_dwordx4 %3, %4, %8" \
        : "=&{v[44:47]}"(qv[0]), "=&{v[48:51]}"(qv[1]), "=&{v[52:55]}"(qv[2]), "=&{v[56:59]}"(qv[3]) : "v"(qo), "s"(qp), "s"(qp+8*2048), "s"(qp+16*2048), "s"(qp+24*2048) : "memory");
    __builtin_amdgcn_s_setprio(CVP);
    if(!HEARLY){ if(QLATE) WAITV(0); else WAITV(4); hfrag(); }
    if(!have){
      wait_flag<8>(pf+l, tag);
      #pragma unroll
      for(int i=0;i<4;i++) gldsc(kvp+i*2048, kvo, crs+512+i*1024);
    }
    WAITV(0);
    TS(3);
    f32x16 ac0, ac1;
    {
      unsigned p0,p1,p2,p3,p4,p5,p6;
      asm volatile("s_nop 1\n\t" CV_ASM
        : "=&{v[128:143]}"(ac0), "=&{v[144:159]}"(ac1), [p0]"=&s"(p0), [p1]"=&s"(p1), [p2]"=&s"(p2), [p3]"=&s"(p3), [p4]"=&s"(p4), [p5]"=&s"(p5), [p6]"=&s"(p6)
        : "{v[64:79]}"(A0), "{v[80:95]}"(A1), "{v[96:111]}"(A2), "{v[112:127]}"(A3),
          "{v24}"(Rb[0][0]), "{v25}"(Rb[0][1]), "{v26}"(Rb[1][0]), "{v27}"(Rb[1][1]), "{v28}"(Rb[2][0]), "{v29}"(Rb[2][1]), "{v30}"(Rb[3][0]), "{v31}"(Rb[3][1]), "{v32}"(bidx), "{v33}"(sb0+SYN+80+w*4)
        : "memory", CV_CLOB);
    }
    TS(4);
    __builtin_amdgcn_s_setprio(3);
    {
      const unsigned yo=(unsigned)((256*(g&1)+16*fr+4*(g&2))*2);
      #pragma unroll
      for(int J=0;J<8;J+=2){
        #define YA(i) ((i)<16? ac0[(i)&15] : ac1[(i)&15])
        const u32x2 a={pack2(YA(4*J),YA(4*J+1)),pack2(YA(4*J+2),YA(4*J+3))}, b={pack2(YA(4*J+4),YA(4*J+5)),pack2(YA(4*J+6),YA(4*J+7))};
        st16cs(yT+(size_t)d*S+256*J, yo, widen(a,b));
      }
    }
    QLOAD
    WAITV(4);
    if(lds_inc(syn+24,l)==3u){ if(l<16) stflag(flags+F_Y+(l*16+(bid>>4))*16+(bid&15), tag); }
    TS(5);
    if(w==0){ wait_flag<YSLP>(flags+F_Y+bid*16+(l&15), tag); PUB(sb0+(l==0? SYN+28 : SINK+l*4), 1u); }
    else SPIN(rd1(syn+28)==0u,4);
    #pragma unroll
    for(int i=0;i<4;i++) gldsc(yT+(size_t)(fd0+i*16)*S+fs0, (l>>2)*4096+(l&3)*16, crs+i*1024);
    asm volatile("s_waitcnt vmcnt(0)" : "+{v[44:47]}"(qv[0]), "+{v[48:51]}"(qv[1]), "+{v[52:55]}"(qv[2]), "+{v[56:59]}"(qv[3]) :: "memory");
    {
      u32x4 y2[4];
      #pragma unroll
      for(int i=0;i<4;i++){ const char* yp=cr+((i>>1)*32+g*8+tq)*64+(i&1)*32+tp*8; y2[i]=__builtin_bit_cast(u32x4,cat44(trread(yp),trread(yp+256))); }
      asm volatile("" : "+v"(y2[0]), "+v"(y2[1]), "+v"(y2[2]), "+v"(y2[3]) :: "memory");
      #pragma unroll
      for(int i=0;i<4;i++) *(u32x4*)(cr+((i&1)*16+fr)*128+((((i>>1)*4+g)^(fr&7))*16))=y2[i];
      asm volatile("" ::: "memory");
      char* op=(char*)(out+(size_t)fs0*2048+fd0); const unsigned oo=(unsigned)((l>>3)*4096+(l&7)*16);
      #pragma unroll
      for(int i=0;i<4;i++){
        const u32x4 y4=*(const u32x4*)(cr+(i*8+(l>>3))*128+(((l&7)^(l>>3))*16)), q4=qv[i]; u32x4 ov;
        #pragma unroll
        for(int e=0;e<4;e++) ov[e]=pack2(bflo(q4[e])*bflo(y4[e]), bfhi(q4[e])*bfhi(y4[e]));
        stout(op+i*32768, oo, ov);
      }
    }
    TS(6); TOUT(7);
    return;
  }

  if(w8>=4){
    LANEVARS
    #define LBAR() SBAR()
    __builtin_amdgcn_s_setprio(LPR);
    {
      const int xr=18*w+(l>>3);
      const unsigned ch=(unsigned)(((l&7)^(xr&7))*16);
      const unsigned xo=(unsigned)(xr*2048)+ch;
      const unsigned xoA=(s0==0 && xr<16)? 16*2048+ch : xo, xoB=(s0==0 && xr+8<16)? 8*2048+ch : xo;
      const int wr=8*w+(l>>3);
      const unsigned wof=(unsigned)(wr*6144+((((l&7)>>1)^wswz(wr))*32)+(l&1)*16);
      const char* xb=(const char*)x+(ptrdiff_t)(s0-16)*2048;
      const char* wb=(const char*)(wqkv+hp*64);
      const int jj=(ROT && (hp&7)==hot0)? (rb&3) : 0;
      auto kp=[&](int s)->int{ return (s&12)+((s+jj)&3); };
      auto issx=[&](int p, int hf){
        const unsigned dst=sb0+X_OFF+((p+2)%3)*XS+(18*w+72*hf)*128; const char* xs=xb+kp(p)*128+(size_t)hf*72*2048;
        glds(xs, hf? xo:xoA, dst); glds(xs+8*2048, hf? xo:xoB, dst+1024);
        asm volatile("s_mov_b32 m0, %2\n\ts_mov_b64 exec, 0xffff\n\tglobal_load_lds_dwordx4 %0, %1\n\ts_mov_b64 exec, -1" :: "v"(xo), "s"(xs+16*2048), "s"(dst+2048) : "memory");
      };
      auto issw=[&](int h){
        const unsigned dst=sb0+((h+4)%6)*WS+w*1024; const char* ws=wb+(size_t)(kp(h>>1)*2+(h&1))*32*6144;
        #pragma unroll
        for(int j=0;j<3;j++) glds(ws+j*2048, wof, dst+j*4096);
      };
      issw(0); issx(0,0); issx(0,1); issw(1); issx(1,0); issx(1,1); issw(2); issw(3);
      WAITV(15); LBAR();
      issx(2,0); issx(2,1);
      WAITV(18); LBAR();
      issw(4); issw(5);
      #pragma unroll 1
      for(int p=3;p<16;p++){
        WAITV(15); LBAR();
        issx(p,0); issw(2*p);
        WAITV(18); LBAR();
        issx(p,1); issw(2*p+1);
        if(TP1==8 && p==E2A+(idx&7)){
          const int pr=8+(idx&7), sh=idx>>3;
          if(w==0) touch((const char*)x+(size_t)(xcd*256+sh*64)*2048+pr*128, l*2048, sb0+SINK+256);
          else if(l<32) touch((const char*)wqkv+(size_t)(pr*64+(xcd>>1)*16+sh*4)*6144+(w-1)*2048+(xcd&1)*1024, (l>>3)*6144+(l&7)*128, sb0+SINK+256);
        }
      }
    }
    TS(1);
    const unsigned wof2=(unsigned)((l>>3)*2048+((((l&7)>>1)^((l>>4)&3))*32)+(l&1)*16);
    WAITV(15); LBAR();
    {
      unsigned so=wof2+(unsigned)(((w+k2*8)&127)*16384), cn=0;
      asm volatile(WO_ASM : "+{v0}"(so), [cn]"+s"(cn) : "{v1}"(sb0+w*1024+l*16), "{v2}"(sb0+SYN+32+(l&3)*4), [wb]"s"(wo+hp*64) : "memory", "scc", "vcc", WO_CLOB);
    }
    lds_add(sb0+SYN+12);
    TS(2);
    if(w==3){
      const unsigned tag=rd1(syn+64)+1u;
      const int hs=(k2+l)&15;
      const unsigned* fp=flags+F_O+(rb*16+hp)*16+hs;
      const unsigned madr=sb0+(l==0? SYN+20 : SINK+l*4);
      const unsigned tof=(unsigned)((hp&7)*2048+(l&15)*128);
      SPIN(rd1(syn+12)<4u,1);
      unsigned rdy=0; int spins=0;
      while(rdy!=0xFFFFu){
        unsigned v=ldflag(fp); unsigned r=(unsigned)__ballot(v==tag)&0xFFFFu;
        if(++spins>(1<<21)) r=0xFFFFu;
        if(r!=rdy){
          PUB(madr,(unsigned)__builtin_ctz(~r));
          const unsigned nw=r&~rdy; rdy=r;
        } else __builtin_amdgcn_s_sleep(OSLP);
      }
      TS(3);
      WAITV(0);
      if(bid==0){
        int sp=0;
        while(true){
          u32x4 v; asm volatile("global_load_dwordx4 %0, %1, %2 sc1\n\ts_waitcnt vmcnt(0)" : "=&v"(v) : "v"(l*16), "s"(flags+F_Y) : "memory");
          if(__all(v[0]==tag && v[1]==tag && v[2]==tag && v[3]==tag) || ++sp>(1<<20)) break;
          __builtin_amdgcn_s_sleep(12);
        }
        if(l==0) stflag(flags, tag);
      }
    } else { TS(3); }
    TOUT(4);
    return;
  }

  __builtin_amdgcn_s_setprio(GPR);
  const int h=w&1, r=w>>1;
  {
    LANEVARS
    unsigned lx0=sb0+X_OFF+fr*128+((g^(fr&7))*16), lx1=sb0+X_OFF+fr*128+(((4+g)^(fr&7))*16);
    const int sw=wswz(8*g+tq);
    unsigned lw0=sb0+(8*g+tq)*128+(unsigned)((((2*h)^sw)*32)+8*tp), lw1=sb0+(8*g+tq)*128+(unsigned)((((2*h+1)^sw)*32)+8*tp);
    char* ot=(char*)o+(size_t)(rb*16+hp)*16384+r*8192+h*2048;
    unsigned it; unsigned long long rm; asm("s_cmp_lg_u32 %1, 0\n\ts_cselect_b64 %0, -1, 0" : "=s"(rm) : "s"(r) : "scc");
    asm volatile(N2_ASM
      : [it]"=&s"(it), "+{v56}"(lx0), "+{v57}"(lx1), "+{v58}"(lw0), "+{v59}"(lw1)
      : "{v62}"(l*16), "{v63}"(sb0+EX_OFF+h*3072+l*16), [r]"s"(r), [ot]"s"(ot), [ou]"s"(ot+4096), [fa]"s"(sb0+SYN+32+w*4), [fp]"s"(sb0+SYN+32+(w^2)*4),
        [vo]"s"(2048-1024*r), [rm]"s"(rm), [z]"s"(rb|r)
      : "memory", "scc", "vcc", N2_CLOB);
  }
  TS(3);
  LANEVARS
  {
    const unsigned tag=rd1(syn+64)+1u;
    WAITV(0);
    if(lds_inc(syn+16,l)==3u){ if(l<16) stflag(flags+F_O+(rb*16+l)*16+hp, tag); }
  }
  TS(4);
  __builtin_amdgcn_s_setprio(G2P);
  {
    f32x16 e0, e1; unsigned trc=0;
    {
      const unsigned ra=sb0+(4*g+tq)*128+8*tp; const int sw=(2*g+(tq>>1))&3;
      unsigned a0=ra+((0^sw)*32), a1=ra+((1^sw)*32), a2=ra+((2^sw)*32), a3=ra+((3^sw)*32), vo=(unsigned)(k2*16384+w*4096+l*16);
      unsigned c=0, iss=0, th, q0, q1, cn=0;
      asm volatile(G3_ASM
        : "={v[128:143]}"(e0), "={v[144:159]}"(e1), [c]"+s"(c), [iss]"+s"(iss), [th]"=&s"(th), [t0]"=&s"(q0), [t1]"=&s"(q1), [cn]"+s"(cn),
          "+{v96}"(a0), "+{v97}"(a1), "+{v98}"(a2), "+{v99}"(a3), "+{v100}"(vo)
        : "{v101}"(sb0+SYN+20), "{v104}"(sb0+SYN+80+w*4), [ob]"s"((const char*)o+(size_t)rb*16*16384)
        : "memory", "scc", "vcc", G3_CLOB);
    }
    char* sg=smem+EX_OFF+w*2048;
    char* op=(char*)(out+(size_t)(s0+w*32)*2048+1024+hp*64); const unsigned oo=(unsigned)((l>>3)*4096+(l&7)*16);
    u32x4 ov[4];
    #pragma unroll
    for(int t=0;t<2;t++){
      #pragma unroll
      for(int j=0;j<4;j++){
        #define E2(i) (t? e1[(i)&15] : e0[(i)&15])
        *(volatile LDS_AS u32x2*)(LDS_AS char*)(sg+fr*128+(((2*j+(g>>1))^(fr&7))*16)+(g&1)*8)=u32x2{pack2(E2(4*j),E2(4*j+1)),pack2(E2(4*j+2),E2(4*j+3))};
      }
      #pragma unroll
      for(int i=0;i<2;i++) ov[2*t+i]=*(volatile LDS_AS u32x4*)(LDS_AS char*)(sg+(i*8+(l>>3))*128+(((l&7)^(l>>3))*16));
    }
    #pragma unroll
    for(int i=0;i<4;i++) stout(op+i*32768, oo, ov[i]);
    TS(6); TOUT(7);
  }
}

extern "C" void kernel_launch(void* const* d_in, const int* in_sizes, int n_in, void* d_out, int out_size, void* d_ws, size_t ws_size, hipStream_t stream){
  const u16* q=(const u16*)d_in[0]; const u16* k=(const u16*)d_in[1]; const u16* v=(const u16*)d_in[2];
  const u16* shk=(const u16*)d_in[3]; const u16* hk=(const u16*)d_in[4]; const u16* sD=(const u16*)d_in[5];
  const u16* x=(const u16*)d_in[6]; const u16* wqkv=(const u16*)d_in[7]; const u16* wo=(const u16*)d_in[8];
  u16* out=(u16*)d_out; u16* ws=(u16*)d_ws;
  u16* o=ws; u16* kvT=ws+(size_t)S*DM; u16* yT=kvT+(size_t)S*DM;
  unsigned* cnt=(unsigned*)(ws+(size_t)6*S*DM);
  fused_kernel<<<256,768,0,stream>>>(x,wqkv,k,v,shk,o,kvT,hk,wo,q,sD,yT,out,cnt);
}
```

```cpp
#include <hip/hip_runtime.h>
#include <hip/hip_bf16.h>
#include <stdint.h>

typedef unsigned short u16;
typedef short bf16x8 __attribute__((ext_vector_type(8)));
typedef short s16x4 __attribute__((ext_vector_type(4)));
typedef float f32x2 __attribute__((ext_vector_type(2)));
typedef float f32x4 __attribute__((ext_vector_type(4)));
typedef float f32x8 __attribute__((ext_vector_type(8)));
typedef float f32x16 __attribute__((ext_vector_type(16)));
typedef unsigned u32x4 __attribute__((ext_vector_type(4)));
typedef unsigned u32x2 __attribute__((ext_vector_type(2)));
typedef __bf16 bf16x2_t __attribute__((ext_vector_type(2)));
#define LDS_AS __attribute__((address_space(3)))

__device__ __forceinline__ float bflo(unsigned x){ return __uint_as_float(x<<16); }
__device__ __forceinline__ float bfhi(unsigned x){ return __uint_as_float(x&0xffff0000u); }
__device__ __forceinline__ unsigned pack2(float lo, float hi){ return __builtin_bit_cast(unsigned, __builtin_convertvector(f32x2{lo,hi}, bf16x2_t)); }
__device__ __forceinline__ float mul1(float a, float b){ float r; asm("v_mul_f32 %0, %1, %2" : "=v"(r) : "v"(a), "v"(b)); return r; }
__device__ __forceinline__ float fma1(float a, float b, float c){ float r; asm("v_fma_f32 %0, %1, %2, %3" : "=v"(r) : "v"(a), "v"(b), "v"(c)); return r; }

__device__ __forceinline__ void glds(const void* sbase, unsigned voff, unsigned ldsaddr){
  asm volatile("s_mov_b32 m0, %2\n\ts_nop 0\n\tglobal_load_lds_dwordx4 %0, %1" :: "v"(voff), "s"(sbase), "s"(ldsaddr) : "memory");
}
__device__ __forceinline__ void gldsc(const void* sbase, unsigned voff, unsigned ldsaddr){
  asm volatile("s_mov_b32 m0, %2\n\ts_nop 0\n\tglobal_load_lds_dwordx4 %0, %1 sc1" :: "v"(voff), "s"(sbase), "s"(ldsaddr) : "memory");
}
__device__ __forceinline__ void touch(const void* sbase, unsigned voff, unsigned ldsaddr){
  asm volatile("s_mov_b32 m0, %2\n\ts_nop 0\n\tglobal_load_lds_dword %0, %1" :: "v"(voff), "s"(sbase), "s"(ldsaddr) : "memory");
}
__device__ __forceinline__ void st16c(void* p, u32x4 v){ asm volatile("global_store_dwordx4 %0, %1, off sc1\n\ts_nop 1" :: "v"(p), "v"(v) : "memory"); }
__device__ __forceinline__ void st16cs(void* sbase, unsigned voff, u32x4 v){ asm volatile("global_store_dwordx4 %0, %1, %2 sc1\n\ts_nop 1" :: "v"(voff), "v"(v), "s"(sbase) : "memory"); }
__device__ __forceinline__ unsigned ldflag(const unsigned* p){ return __hip_atomic_load(p, __ATOMIC_RELAXED, __HIP_MEMORY_SCOPE_AGENT); }
__device__ __forceinline__ void stflag(unsigned* p, unsigned v){ __hip_atomic_store(p, v, __ATOMIC_RELAXED, __HIP_MEMORY_SCOPE_AGENT); }
template<int SLP> __device__ __forceinline__ void wait_flag(const unsigned* p, unsigned tag){
  int spins=0;
  while(true){ unsigned v=ldflag(p); if(__all(v==tag)) break; __builtin_amdgcn_s_sleep(SLP); if(++spins>(1<<21)) break; }
}
__device__ __forceinline__ unsigned lds_addr(const void* p){ return (unsigned)(size_t)(const LDS_AS void*)p; }
#define WAITV(n) asm volatile("s_waitcnt vmcnt(" #n ")":::"memory")
#define WAITL() asm volatile("s_waitcnt lgkmcnt(0)":::"memory")
#define SBAR() asm volatile("s_barrier":::"memory")
#define PUB(addr,val) asm volatile("ds_write_b32 %0, %1" :: "v"(addr), "v"(val) : "memory")
__device__ __forceinline__ s16x4 trread(const char* p){ return __builtin_amdgcn_ds_read_tr16_b64_v4i16((LDS_AS s16x4*)p); }
__device__ __forceinline__ bf16x8 cat44(s16x4 a, s16x4 b){ return __builtin_shufflevector(a,b,0,1,2,3,4,5,6,7); }
__device__ __forceinline__ int wswz(int row){ return ((row>>1)&1) | (((row>>3)&1)<<1); }
__device__ __forceinline__ u32x4 widen(u32x2 a, u32x2 b){
  auto r0=__builtin_amdgcn_permlane16_swap(a[0],b[0],false,false);
  auto r1=__builtin_amdgcn_permlane16_swap(a[1],b[1],false,false);
  return u32x4{r0[0],r1[0],r0[1],r1[1]};
}

constexpr int S=2048, DM=1024;
constexpr int WS=12288, XS=18432, X_OFF=6*WS;
constexpr int CREG=4608, C_OFF=131072;
constexpr int EX_OFF=C_OFF+4*CREG;
constexpr int SYN=EX_OFF+8192;
constexpr int SINK=SYN+256;
constexpr int ALL_LDS=SINK+768;
constexpr int F_PP=1024, F_O=4096, F_Y=8192;
#define KV0 1
#define KVW 3
#define KVS 3
#define LPR 2
#define CPR 2
#define GPR 1
#define WROT 4
#define LHOLD 0
#define HOTP 0
#define E2A 3
#define WLIM 4
#define HEARLY 1
#define QLATE 1
#define WTCH 0
#define CGATE 1
#define OSLP 2
#define YSLP 8
#define TP0 0
#define TP1 16
#define TWO 0
#define OPULL 0
#define CVP 2
#define G2P 1
#define OUTC 1
#define CREM 0
#define OPOL 1
#define PREM 9
#define KTH 1
#define KT0 0
#define YDEF 1
#define TPW 0
#define PB2 1
#define DRB 3
#define KLB 2
#define CDL 8
#define WTL 2
#define WTP 10
#define WTM 3
#define WTW 1
#define ROT 1

#define N2_ASM \
  "v_mov_b32 v64, 0\n\t" \
  "v_mov_b32 v65, 0\n\t" \
  "v_mov_b32 v66, 0\n\t" \
  "v_mov_b32 v67, 0\n\t" \
  "v_mov_b32 v68, 0\n\t" \
  "v_mov_b32 v69, 0\n\t" \
  "v_mov_b32 v70, 0\n\t" \
  "v_mov_b32 v71, 0\n\t" \
  "v_mov_b32 v72, 0\n\t" \
  "v_mov_b32 v73, 0\n\t" \
  "v_mov_b32 v74, 0\n\t" \
  "v_mov_b32 v75, 0\n\t" \
  "v_mov_b32 v76, 0\n\t" \
  "v_mov_b32 v77, 0\n\t" \
  "v_mov_b32 v78, 0\n\t" \
  "v_mov_b32 v79, 0\n\t" \
  "v_mov_b32 v80, 0\n\t" \
  "v_mov_b32 v81, 0\n\t" \
  "v_mov_b32 v82, 0\n\t" \
  "v_mov_b32 v83, 0\n\t" \
  "v_mov_b32 v84, 0\n\t" \
  "v_mov_b32 v85, 0\n\t" \
  "v_mov_b32 v86, 0\n\t" \
  "v_mov_b32 v87, 0\n\t" \
  "v_mov_b32 v88, 0\n\t" \
  "v_mov_b32 v89, 0\n\t" \
  "v_mov_b32 v90, 0\n\t" \
  "v_mov_b32 v91, 0\n\t" \
  "v_mov_b32 v92, 0\n\t" \
  "v_mov_b32 v93, 0\n\t" \
  "v_mov_b32 v94, 0\n\t" \
  "v_mov_b32 v95, 0\n\t" \
  "v_mov_b32 v96, 0\n\t" \
  "v_mov_b32 v97, 0\n\t" \
  "v_mov_b32 v98, 0\n\t" \
  "v_mov_b32 v99, 0\n\t" \
  "v_mov_b32 v100, 0\n\t" \
  "v_mov_b32 v101, 0\n\t" \
  "v_mov_b32 v102, 0\n\t" \
  "v_mov_b32 v103, 0\n\t" \
  "v_mov_b32 v104, 0\n\t" \
  "v_mov_b32 v105, 0\n\t" \
  "v_mov_b32 v106, 0\n\t" \
  "v_mov_b32 v107, 0\n\t" \
  "v_mov_b32 v108, 0\n\t" \
  "v_mov_b32 v109, 0\n\t" \
  "v_mov_b32 v110, 0\n\t" \
  "v_mov_b32 v111, 0\n\t" \
  "v_mov_b32 v112, 0\n\t" \
  "v_mov_b32 v113, 0\n\t" \
  "v_mov_b32 v114, 0\n\t" \
  "v_mov_b32 v115, 0\n\t" \
  "v_mov_b32 v116, 0\n\t" \
  "v_mov_b32 v117, 0\n\t" \
  "v_mov_b32 v118, 0\n\t" \
  "v_mov_b32 v119, 0\n\t" \
  "v_mov_b32 v120, 0\n\t" \
  "v_mov_b32 v121, 0\n\t" \
  "v_mov_b32 v122, 0\n\t" \
  "v_mov_b32 v123, 0\n\t" \
  "v_mov_b32 v124, 0\n\t" \
  "v_mov_b32 v125, 0\n\t" \
  "v_mov_b32 v126, 0\n\t" \
  "v_mov_b32 v127, 0\n\t" \
  "v_mov_b32 v128, 0\n\t" \
  "v_mov_b32 v129, 0\n\t" \
  "v_mov_b32 v130, 0\n\t" \
  "v_mov_b32 v131, 0\n\t" \
  "v_mov_b32 v132, 0\n\t" \
  "v_mov_b32 v133, 0\n\t" \
  "v_mov_b32 v134, 0\n\t" \
  "v_mov_b32 v135, 0\n\t" \
  "v_mov_b32 v136, 0\n\t" \
  "v_mov_b32 v137, 0\n\t" \
  "v_mov_b32 v138, 0\n\t" \
  "v_mov_b32 v139, 0\n\t" \
  "v_mov_b32 v140, 0\n\t" \
  "v_mov_b32 v141, 0\n\t" \
  "v_mov_b32 v142, 0\n\t" \
  "v_mov_b32 v143, 0\n\t" \
  "v_mov_b32 v144, 0\n\t" \
  "v_mov_b32 v145, 0\n\t" \
  "v_mov_b32 v146, 0\n\t" \
  "v_mov_b32 v147, 0\n\t" \
  "v_mov_b32 v148, 0\n\t" \
  "v_mov_b32 v149, 0\n\t" \
  "v_mov_b32 v150, 0\n\t" \
  "v_mov_b32 v151, 0\n\t" \
  "v_mov_b32 v152, 0\n\t" \
  "v_mov_b32 v153, 0\n\t" \
  "v_mov_b32 v154, 0\n\t" \
  "v_mov_b32 v155, 0\n\t" \
  "v_mov_b32 v156, 0\n\t" \
  "v_mov_b32 v157, 0\n\t" \
  "v_mov_b32 v158, 0\n\t" \
  "v_mov_b32 v159, 0\n\t" \
  "v_mov_b32 v160, 0\n\t" \
  "v_mov_b32 v161, 0\n\t" \
  "v_mov_b32 v162, 0\n\t" \
  "v_mov_b32 v163, 0\n\t" \
  "v_mov_b32 v164, 0\n\t" \
  "v_mov_b32 v165, 0\n\t" \
  "v_mov_b32 v166, 0\n\t" \
  "v_mov_b32 v167, 0\n\t" \
  "s_mov_b32 s68, 0x50000\n\t" \
  "s_mov_b32 s69, 0x5550055\n\t" \
  "s_mov_b32 s70, 0xa0000\n\t" \
  "s_mov_b32 s71, 0xaaa00aa\n\t" \
  "s_mov_b32 s72, 0x150001\n\t" \
  "s_mov_b32 s73, 0x15550155\n\t" \
  "s_mov_b32 s74, 0x2a0002\n\t" \
  "s_mov_b32 s75, 0x2aaa02aa\n\t" \
  "s_mov_b32 s76, 0x55505555\n\t" \
  "s_mov_b32 s77, 0x50005500\n\t" \
  "s_mov_b32 s78, 0xaaa0aaaa\n\t" \
  "s_mov_b32 s79, 0xa000aa00\n\t" \
  "s_mov_b32 s80, 0x55405554\n\t" \
  "s_mov_b32 s81, 0x40005400\n\t" \
  "s_mov_b32 s82, 0xaa80aaa8\n\t" \
  "s_mov_b32 s83, 0x8000a800\n\t" \
  "s_mov_b32 s84, 0xaaaaaaaa\n\t" \
  "s_mov_b32 s85, 0xaaaaaaaa\n\t" \
  "v_add_u32 v60, 36864, v58\n\t" \
  "v_add_u32 v61, 36864, v59\n\t" \
  "s_mov_b32 %[it], 0\n\t" \
  "s_cmp_lg_u32 %[r], 0\n\t" \
  "s_barrier\n\t" \
  "s_cbranch_scc1 NB_%=\n\t" \
  "ds_read_b64_tr_b16 v[40:41], v60 offset:16384\n\t" \
  "ds_read_b64_tr_b16 v[42:43], v60 offset:16896\n\t" \
  "ds_read_b64_tr_b16 v[44:45], v60 offset:20480\n\t" \
  "ds_read_b64_tr_b16 v[46:47], v60 offset:20992\n\t" \
  "ds_read_b64_tr_b16 v[48:49], v61 offset:16384\n\t" \
  "ds_read_b64_tr_b16 v[50:51], v61 offset:16896\n\t" \
  "ds_read_b128 v[0:3], v56 offset:36864\n\t" \
  "ds_read_b128 v[4:7], v56 offset:38912\n\t" \
  "ds_read_b128 v[8:11], v56 offset:40960\n\t" \
  "ds_read_b128 v[12:15], v56 offset:43008\n\t" \
  "ds_read_b128 v[16:19], v56 offset:45056\n\t" \
  "s_waitcnt lgkmcnt(0)\n\t" \
  "s_branch N4a_%=\n\t" \
  "N0a_%=:\n\t" \
  "s_waitcnt lgkmcnt(4)\n\t" \
  "v_mfma_f32_16x16x32_bf16 v[160:163], v[40:43], v[0:3], v[160:163]\n\t" \
  "ds_read_b64_tr_b16 v[52:53], v59 offset:8192\n\t" \
  "v_mfma_f32_16x16x32_bf16 v[96:99], v[40:43], v[4:7], v[96:99]\n\t" \
  "ds_read_b64_tr_b16 v[54:55], v59 offset:8704\n\t" \
  "v_mfma_f32_16x16x32_bf16 v[104:107], v[40:43], v[8:11], v[104:107]\n\t" \
  "s_barrier\n\t" \
  "v_mfma_f32_16x16x32_bf16 v[112:115], v[40:43], v[12:15], v[112:115]\n\t" \
  "v_mfma_f32_16x16x32_bf16 v[120:123], v[40:43], v[16:19], v[120:123]\n\t" \
  "s_waitcnt lgkmcnt(4)\n\t" \
  "v_mfma_f32_16x16x32_bf16 v[128:131], v[4:7], v[44:47], v[128:131]\n\t" \
  "ds_read_b64_tr_b16 v[40:41], v58 offset:0\n\t" \
  "v_mfma_f32_16x16x32_bf16 v[136:139], v[8:11], v[44:47], v[136:139]\n\t" \
  "ds_read_b64_tr_b16 v[42:43], v58 offset:512\n\t" \
  "v_mfma_f32_16x16x32_bf16 v[144:147], v[12:15], v[44:47], v[144:147]\n\t" \
  "ds_read_b128 v[20:23], v57 offset:0\n\t" \
  "v_mfma_f32_16x16x32_bf16 v[152:155], v[16:19], v[44:47], v[152:155]\n\t" \
  "ds_read_b128 v[24:27], v57 offset:2048\n\t" \
  "s_waitcnt lgkmcnt(6)\n\t" \
  "v_mfma_f32_16x16x32_bf16 v[164:167], v[48:51], v[0:3], v[164:167]\n\t" \
  "ds_read_b64_tr_b16 v[44:45], v59 offset:0\n\t" \
  "v_mfma_f32_16x16x32_bf16 v[100:103], v[48:51], v[4:7], v[100:103]\n\t" \
  "ds_read_b64_tr_b16 v[46:47], v59 offset:512\n\t" \
  "v_mfma_f32_16x16x32_bf16 v[108:111], v[48:51], v[8:11], v[108:111]\n\t" \
  "ds_read_b128 v[28:31], v57 offset:4096\n\t" \
  "v_mfma_f32_16x16x32_bf16 v[116:119], v[48:51], v[12:15], v[116:119]\n\t" \
  "ds_read_b128 v[32:35], v57 offset:6144\n\t" \
  "v_mfma_f32_16x16x32_bf16 v[124:127], v[48:51], v[16:19], v[124:127]\n\t" \
  "s_waitcnt lgkmcnt(8)\n\t" \
  "v_mfma_f32_16x16x32_bf16 v[132:135], v[4:7], v[52:55], v[132:135]\n\t" \
  "ds_read_b64_tr_b16 v[48:49], v58 offset:16384\n\t" \
  "v_mfma_f32_16x16x32_bf16 v[140:143], v[8:11], v[52:55], v[140:143]\n\t" \
  "ds_read_b64_tr_b16 v[50:51], v58 offset:16896\n\t" \
  "v_mfma_f32_16x16x32_bf16 v[148:151], v[12:15], v[52:55], v[148:151]\n\t" \
  "ds_read_b128 v[36:39], v57 offset:8192\n\t" \
  "v_mfma_f32_16x16x32_bf16 v[156:159], v[16:19], v[52:55], v[156:159]\n\t" \
  "s_waitcnt lgkmcnt(9)\n\t" \
  "v_mfma_f32_16x16x32_bf16 v[64:67], v[40:43], v[4:7], v[64:67]\n\t" \
  "ds_read_b64_tr_b16 v[52:53], v58 offset:20480\n\t" \
  "v_mfma_f32_16x16x32_bf16 v[72:75], v[40:43], v[8:11], v[72:75]\n\t" \
  "ds_read_b64_tr_b16 v[54:55], v58 offset:20992\n\t" \
  "v_mfma_f32_16x16x32_bf16 v[80:83], v[40:43], v[12:15], v[80:83]\n\t" \
  "v_mfma_f32_16x16x32_bf16 v[88:91], v[40:43], v[16:19], v[88:91]\n\t" \
  "s_waitcnt lgkmcnt(7)\n\t" \
  "v_mfma_f32_16x16x32_bf16 v[68:71], v[44:47], v[4:7], v[68:71]\n\t" \
  "ds_read_b64_tr_b16 v[40:41], v59 offset:16384\n\t" \
  "v_mfma_f32_16x16x32_bf16 v[76:79], v[44:47], v[8:11], v[76:79]\n\t" \
  "ds_read_b64_tr_b16 v[42:43], v59 offset:16896\n\t" \
  "v_mfma_f32_16x16x32_bf16 v[84:87], v[44:47], v[12:15], v[84:87]\n\t" \
  "v_mfma_f32_16x16x32_bf16 v[92:95], v[44:47], v[16:19], v[92:95]\n\t" \
  "N1a_%=:\n\t" \
  "s_waitcnt lgkmcnt(4)\n\t" \
  "v_mfma_f32_16x16x32_bf16 v[160:163], v[48:51], v[20:23], v[160:163]\n\t" \
  "ds_read_b64_tr_b16 v[44:45], v59 offset:20480\n\t" \
  "v_mfma_f32_16x16x32_bf16 v[96:99], v[48:51], v[24:27], v[96:99]\n\t" \
  "ds_read_b64_tr_b16 v[46:47], v59 offset:20992\n\t" \
  "v_mfma_f32_16x16x32_bf16 v[104:107], v[48:51], v[28:31], v[104:107]\n\t" \
  "s_barrier\n\t" \
  "v_mfma_f32_16x16x32_bf16 v[112:115], v[48:51], v[32:35], v[112:115]\n\t" \
  "v_mfma_f32_16x16x32_bf16 v[120:123], v[48:51], v[36:39], v[120:123]\n\t" \
  "s_waitcnt lgkmcnt(4)\n\t" \
  "v_mfma_f32_16x16x32_bf16 v[128:131], v[24:27], v[52:55], v[128:131]\n\t" \
  "ds_read_b64_tr_b16 v[48:49], v58 offset:12288\n\t" \
  "v_mfma_f32_16x16x32_bf16 v[136:139], v[28:31], v[52:55], v[136:139]\n\t" \
  "ds_read_b64_tr_b16 v[50:51], v58 offset:12800\n\t" \
  "v_mfma_f32_16x16x32_bf16 v[144:147], v[32:35], v[52:55], v[144:147]\n\t" \
  "ds_read_b128 v[0:3], v56 offset:18432\n\t" \
  "v_mfma_f32_16x16x32_bf16 v[152:155], v[36:39], v[52:55], v[152:155]\n\t" \
  "ds_read_b128 v[4:7], v56 offset:20480\n\t" \
  "s_waitcnt lgkmcnt(6)\n\t" \
  "v_mfma_f32_16x16x32_bf16 v[164:167], v[40:43], v[20:23], v[164:167]\n\t" \
  "ds_read_b64_tr_b16 v[52:53], v59 offset:12288\n\t" \
  "v_mfma_f32_16x16x32_bf16 v[100:103], v[40:43], v[24:27], v[100:103]\n\t" \
  "ds_read_b64_tr_b16 v[54:55], v59 offset:12800\n\t" \
  "v_mfma_f32_16x16x32_bf16 v[108:111], v[40:43], v[28:31], v[108:111]\n\t" \
  "ds_read_b128 v[8:11], v56 offset:22528\n\t" \
  "v_mfma_f32_16x16x32_bf16 v[116:119], v[40:43], v[32:35], v[116:119]\n\t" \
  "ds_read_b128 v[12:15], v56 offset:24576\n\t" \
  "v_mfma_f32_16x16x32_bf16 v[124:127], v[40:43], v[36:39], v[124:127]\n\t" \
  "s_waitcnt lgkmcnt(8)\n\t" \
  "v_mfma_f32_16x16x32_bf16 v[132:135], v[24:27], v[44:47], v[132:135]\n\t" \
  "ds_read_b64_tr_b16 v[40:41], v58 offset:28672\n\t" \
  "v_mfma_f32_16x16x32_bf16 v[140:143], v[28:31], v[44:47], v[140:143]\n\t" \
  "ds_read_b64_tr_b16 v[42:43], v58 offset:29184\n\t" \
  "v_mfma_f32_16x16x32_bf16 v[148:151], v[32:35], v[44:47], v[148:151]\n\t" \
  "ds_read_b128 v[16:19], v56 offset:26624\n\t" \
  "v_mfma_f32_16x16x32_bf16 v[156:159], v[36:39], v[44:47], v[156:159]\n\t" \
  "s_waitcnt lgkmcnt(9)\n\t" \
  "v_mfma_f32_16x16x32_bf16 v[64:67], v[48:51], v[24:27], v[64:67]\n\t" \
  "ds_read_b64_tr_b16 v[44:45], v58 offset:32768\n\t" \
  "v_mfma_f32_16x16x32_bf16 v[72:75], v[48:51], v[28:31], v[72:75]\n\t" \
  "ds_read_b64_tr_b16 v[46:47], v58 offset:33280\n\t" \
  "v_mfma_f32_16x16x32_bf16 v[80:83], v[48:51], v[32:35], v[80:83]\n\t" \
  "v_mfma_f32_16x16x32_bf16 v[88:91], v[48:51], v[36:39], v[88:91]\n\t" \
  "s_waitcnt lgkmcnt(7)\n\t" \
  "v_mfma_f32_16x16x32_bf16 v[68:71], v[52:55], v[24:27], v[68:71]\n\t" \
  "ds_read_b64_tr_b16 v[48:49], v59 offset:28672\n\t" \
  "v_mfma_f32_16x16x32_bf16 v[76:79], v[52:55], v[28:31], v[76:79]\n\t" \
  "ds_read_b64_tr_b16 v[50:51], v59 offset:29184\n\t" \
  "v_mfma_f32_16x16x32_bf16 v[84:87], v[52:55], v[32:35], v[84:87]\n\t" \
  "v_mfma_f32_16x16x32_bf16 v[92:95], v[52:55], v[36:39], v[92:95]\n\t" \
  "N2a_%=:\n\t" \
  "s_waitcnt lgkmcnt(4)\n\t" \
  "v_mfma_f32_16x16x32_bf16 v[160:163], v[40:43], v[0:3], v[160:163]\n\t" \
  "ds_read_b64_tr_b16 v[52:53], v59 offset:32768\n\t" \
  "v_mfma_f32_16x16x32_bf16 v[96:99], v[40:43], v[4:7], v[96:99]\n\t" \
  "ds_read_b64_tr_b16 v[54:55], v59 offset:33280\n\t" \
  "v_mfma_f32_16x16x32_bf16 v[104:107], v[40:43], v[8:11], v[104:107]\n\t" \
  "s_barrier\n\t" \
  "v_mfma_f32_16x16x32_bf16 v[112:115], v[40:43], v[12:15], v[112:115]\n\t" \
  "v_mfma_f32_16x16x32_bf16 v[120:123], v[40:43], v[16:19], v[120:123]\n\t" \
  "s_waitcnt lgkmcnt(4)\n\t" \
  "v_mfma_f32_16x16x32_bf16 v[128:131], v[4:7], v[44:47], v[128:131]\n\t" \
  "ds_read_b64_tr_b16 v[40:41], v58 offset:24576\n\t" \
  "v_mfma_f32_16x16x32_bf16 v[136:139], v[8:11], v[44:47], v[136:139]\n\t" \
  "ds_read_b64_tr_b16 v[42:43], v58 offset:25088\n\t" \
  "v_mfma_f32_16x16x32_bf16 v[144:147], v[12:15], v[44:47], v[144:147]\n\t" \
  "ds_read_b128 v[20:23], v57 offset:18432\n\t" \
  "v_mfma_f32_16x16x32_bf16 v[152:155], v[16:19], v[44:47], v[152:155]\n\t" \
  "ds_read_b128 v[24:27], v57 offset:20480\n\t" \
  "s_waitcnt lgkmcnt(6)\n\t" \
  "v_mfma_f32_16x16x32_bf16 v[164:167], v[48:51], v[0:3], v[164:167]\n\t" \
  "ds_read_b64_tr_b16 v[44:45], v59 offset:24576\n\t" \
  "v_mfma_f32_16x16x32_bf16 v[100:103], v[48:51], v[4:7], v[100:103]\n\t" \
  "ds_read_b64_tr_b16 v[46:47], v59 offset:25088\n\t" \
  "v_mfma_f32_16x16x32_bf16 v[108:111], v[48:51], v[8:11], v[108:111]\n\t" \
  "ds_read_b128 v[28:31], v57 offset:22528\n\t" \
  "v_mfma_f32_16x16x32_bf16 v[116:119], v[48:51], v[12:15], v[116:119]\n\t" \
  "ds_read_b128 v[32:35], v57 offset:24576\n\t" \
  "v_mfma_f32_16x16x32_bf16 v[124:127], v[48:51], v[16:19], v[124:127]\n\t" \
  "s_waitcnt lgkmcnt(8)\n\t" \
  "v_mfma_f32_16x16x32_bf16 v[132:135], v[4:7], v[52:55], v[132:135]\n\t" \
  "ds_read_b64_tr_b16 v[48:49], v60 offset:4096\n\t" \
  "v_mfma_f32_16x16x32_bf16 v[140:143], v[8:11], v[52:55], v[140:143]\n\t" \
  "ds_read_b64_tr_b16 v[50:51], v60 offset:4608\n\t" \
  "v_mfma_f32_16x16x32_bf16 v[148:151], v[12:15], v[52:55], v[148:151]\n\t" \
  "ds_read_b128 v[36:39], v57 offset:26624\n\t" \
  "v_mfma_f32_16x16x32_bf16 v[156:159], v[16:19], v[52:55], v[156:159]\n\t" \
  "s_waitcnt lgkmcnt(9)\n\t" \
  "v_mfma_f32_16x16x32_bf16 v[64:67], v[40:43], v[4:7], v[64:67]\n\t" \
  "ds_read_b64_tr_b16 v[52:53], v60 offset:8192\n\t" \
  "v_mfma_f32_16x16x32_bf16 v[72:75], v[40:43], v[8:11], v[72:75]\n\t" \
  "ds_read_b64_tr_b16 v[54:55], v60 offset:8704\n\t" \
  "v_mfma_f32_16x16x32_bf16 v[80:83], v[40:43], v[12:15], v[80:83]\n\t" \
  "v_mfma_f32_16x16x32_bf16 v[88:91], v[40:43], v[16:19], v[88:91]\n\t" \
  "s_waitcnt lgkmcnt(7)\n\t" \
  "v_mfma_f32_16x16x32_bf16 v[68:71], v[44:47], v[4:7], v[68:71]\n\t" \
  "ds_read_b64_tr_b16 v[40:41], v61 offset:4096\n\t" \
  "v_mfma_f32_16x16x32_bf16 v[76:79], v[44:47], v[8:11], v[76:79]\n\t" \
  "ds_read_b64_tr_b16 v[42:43], v61 offset:4608\n\t" \
  "v_mfma_f32_16x16x32_bf16 v[84:87], v[44:47], v[12:15], v[84:87]\n\t" \
  "v_mfma_f32_16x16x32_bf16 v[92:95], v[44:47], v[16:19], v[92:95]\n\t" \
  "N3a_%=:\n\t" \
  "s_waitcnt lgkmcnt(4)\n\t" \
  "v_mfma_f32_16x16x32_bf16 v[160:163], v[48:51], v[20:23], v[160:163]\n\t" \
  "ds_read_b64_tr_b16 v[44:45], v61 offset:8192\n\t" \
  "v_mfma_f32_16x16x32_bf16 v[96:99], v[48:51], v[24:27], v[96:99]\n\t" \
  "ds_read_b64_tr_b16 v[46:47], v61 offset:8704\n\t" \
  "v_mfma_f32_16x16x32_bf16 v[104:107], v[48:51], v[28:31], v[104:107]\n\t" \
  "s_barrier\n\t" \
  "v_mfma_f32_16x16x32_bf16 v[112:115], v[48:51], v[32:35], v[112:115]\n\t" \
  "v_mfma_f32_16x16x32_bf16 v[120:123], v[48:51], v[36:39], v[120:123]\n\t" \
  "s_waitcnt lgkmcnt(4)\n\t" \
  "v_mfma_f32_16x16x32_bf16 v[128:131], v[24:27], v[52:55], v[128:131]\n\t" \
  "ds_read_b64_tr_b16 v[48:49], v60 offset:0\n\t" \
  "v_mfma_f32_16x16x32_bf16 v[136:139], v[28:31], v[52:55], v[136:139]\n\t" \
  "ds_read_b64_tr_b16 v[50:51], v60 offset:512\n\t" \
  "v_mfma_f32_16x16x32_bf16 v[144:147], v[32:35], v[52:55], v[144:147]\n\t" \
  "ds_read_b128 v[0:3], v56 offset:36864\n\t" \
  "v_mfma_f32_16x16x32_bf16 v[152:155], v[36:39], v[52:55], v[152:155]\n\t" \
  "ds_read_b128 v[4:7], v56 offset:38912\n\t" \
  "s_waitcnt lgkmcnt(6)\n\t" \
  "v_mfma_f32_16x16x32_bf16 v[164:167], v[40:43], v[20:23], v[164:167]\n\t" \
  "ds_read_b64_tr_b16 v[52:53], v61 offset:0\n\t" \
  "v_mfma_f32_16x16x32_bf16 v[100:103], v[40:43], v[24:27], v[100:103]\n\t" \
  "ds_read_b64_tr_b16 v[54:55], v61 offset:512\n\t" \
  "v_mfma_f32_16x16x32_bf16 v[108:111], v[40:43], v[28:31], v[108:111]\n\t" \
  "ds_read_b128 v[8:11], v56 offset:40960\n\t" \
  "v_mfma_f32_16x16x32_bf16 v[116:119], v[40:43], v[32:35], v[116:119]\n\t" \
  "ds_read_b128 v[12:15], v56 offset:43008\n\t" \
  "v_mfma_f32_16x16x32_bf16 v[124:127], v[40:43], v[36:39], v[124:127]\n\t" \
  "s_waitcnt lgkmcnt(8)\n\t" \
  "v_mfma_f32_16x16x32_bf16 v[132:135], v[24:27], v[44:47], v[132:135]\n\t" \
  "ds_read_b64_tr_b16 v[40:41], v60 offset:16384\n\t" \
  "v_mfma_f32_16x16x32_bf16 v[140:143], v[28:31], v[44:47], v[140:143]\n\t" \
  "ds_read_b64_tr_b16 v[42:43], v60 offset:16896\n\t" \
  "v_mfma_f32_16x16x32_bf16 v[148:151], v[32:35], v[44:47], v[148:151]\n\t" \
  "ds_read_b128 v[16:19], v56 offset:45056\n\t" \
  "v_mfma_f32_16x16x32_bf16 v[156:159], v[36:39], v[44:47], v[156:159]\n\t" \
  "s_waitcnt lgkmcnt(9)\n\t" \
  "v_mfma_f32_16x16x32_bf16 v[64:67], v[48:51], v[24:27], v[64:67]\n\t" \
  "ds_read_b64_tr_b16 v[44:45], v60 offset:20480\n\t" \
  "v_mfma_f32_16x16x32_bf16 v[72:75], v[48:51], v[28:31], v[72:75]\n\t" \
  "ds_read_b64_tr_b16 v[46:47], v60 offset:20992\n\t" \
  "v_mfma_f32_16x16x32_bf16 v[80:83], v[48:51], v[32:35], v[80:83]\n\t" \
  "v_mfma_f32_16x16x32_bf16 v[88:91], v[48:51], v[36:39], v[88:91]\n\t" \
  "s_waitcnt lgkmcnt(7)\n\t" \
  "v_mfma_f32_16x16x32_bf16 v[68:71], v[52:55], v[24:27], v[68:71]\n\t" \
  "ds_read_b64_tr_b16 v[48:49], v61 offset:16384\n\t" \
  "v_mfma_f32_16x16x32_bf16 v[76:79], v[52:55], v[28:31], v[76:79]\n\t" \
  "ds_read_b64_tr_b16 v[50:51], v61 offset:16896\n\t" \
  "v_mfma_f32_16x16x32_bf16 v[84:87], v[52:55], v[32:35], v[84:87]\n\t" \
  "v_mfma_f32_16x16x32_bf16 v[92:95], v[52:55], v[36:39], v[92:95]\n\t" \
  "N4a_%=:\n\t" \
  "s_waitcnt lgkmcnt(4)\n\t" \
  "v_mfma_f32_16x16x32_bf16 v[160:163], v[40:43], v[0:3], v[160:163]\n\t" \
  "ds_read_b64_tr_b16 v[52:53], v61 offset:20480\n\t" \
  "v_mfma_f32_16x16x32_bf16 v[96:99], v[40:43], v[4:7], v[96:99]\n\t" \
  "ds_read_b64_tr_b16 v[54:55], v61 offset:20992\n\t" \
  "v_mfma_f32_16x16x32_bf16 v[104:107], v[40:43], v[8:11], v[104:107]\n\t" \
  "s_barrier\n\t" \
  "v_mfma_f32_16x16x32_bf16 v[112:115], v[40:43], v[12:15], v[112:115]\n\t" \
  "v_mfma_f32_16x16x32_bf16 v[120:123], v[40:43], v[16:19], v[120:123]\n\t" \
  "s_waitcnt lgkmcnt(4)\n\t" \
  "v_mfma_f32_16x16x32_bf16 v[128:131], v[4:7], v[44:47], v[128:131]\n\t" \
  "ds_read_b64_tr_b16 v[40:41], v60 offset:12288\n\t" \
  "v_mfma_f32_16x16x32_bf16 v[136:139], v[8:11], v[44:47], v[136:139]\n\t" \
  "ds_read_b64_tr_b16 v[42:43], v60 offset:12800\n\t" \
  "v_mfma_f32_16x16x32_bf16 v[144:147], v[12:15], v[44:47], v[144:147]\n\t" \
  "ds_read_b128 v[20:23], v57 offset:36864\n\t" \
  "v_mfma_f32_16x16x32_bf16 v[152:155], v[16:19], v[44:47], v[152:155]\n\t" \
  "ds_read_b128 v[24:27], v57 offset:38912\n\t" \
  "s_waitcnt lgkmcnt(6)\n\t" \
  "v_mfma_f32_16x16x32_bf16 v[164:167], v[48:51], v[0:3], v[164:167]\n\t" \
  "ds_read_b64_tr_b16 v[44:45], v61 offset:12288\n\t" \
  "v_mfma_f32_16x16x32_bf16 v[100:103], v[48:51], v[4:7], v[100:103]\n\t" \
  "ds_read_b64_tr_b16 v[46:47], v61 offset:12800\n\t" \
  "v_mfma_f32_16x16x32_bf16 v[108:111], v[48:51], v[8:11], v[108:111]\n\t" \
  "ds_read_b128 v[28:31], v57 offset:40960\n\t" \
  "v_mfma_f32_16x16x32_bf16 v[116:119], v[48:51], v[12:15], v[116:119]\n\t" \
  "ds_read_b128 v[32:35], v57 offset:43008\n\t" \
  "v_mfma_f32_16x16x32_bf16 v[124:127], v[48:51], v[16:19], v[124:127]\n\t" \
  "s_waitcnt lgkmcnt(8)\n\t" \
  "v_mfma_f32_16x16x32_bf16 v[132:135], v[4:7], v[52:55], v[132:135]\n\t" \
  "ds_read_b64_tr_b16 v[48:49], v60 offset:28672\n\t" \
  "v_mfma_f32_16x16x32_bf16 v[140:143], v[8:11], v[52:55], v[140:143]\n\t" \
  "ds_read_b64_tr_b16 v[50:51], v60 offset:29184\n\t" \
  "v_mfma_f32_16x16x32_bf16 v[148:151], v[12:15], v[52:55], v[148:151]\n\t" \
  "ds_read_b128 v[36:39], v57 offset:45056\n\t" \
  "v_mfma_f32_16x16x32_bf16 v[156:159], v[16:19], v[52:55], v[156:159]\n\t" \
  "s_waitcnt lgkmcnt(9)\n\t" \
  "v_mfma_f32_16x16x32_bf16 v[64:67], v[40:43], v[4:7], v[64:67]\n\t" \
  "ds_read_b64_tr_b16 v[52:53], v60 offset:32768\n\t" \
  "v_mfma_f32_16x16x32_bf16 v[72:75], v[40:43], v[8:11], v[72:75]\n\t" \
  "ds_read_b64_tr_b16 v[54:55], v60 offset:33280\n\t" \
  "v_mfma_f32_16x16x32_bf16 v[80:83], v[40:43], v[12:15], v[80:83]\n\t" \
  "v_mfma_f32_16x16x32_bf16 v[88:91], v[40:43], v[16:19], v[88:91]\n\t" \
  "s_waitcnt lgkmcnt(7)\n\t" \
  "v_mfma_f32_16x16x32_bf16 v[68:71], v[44:47], v[4:7], v[68:71]\n\t" \
  "ds_read_b64_tr_b16 v[40:41], v61 offset:28672\n\t" \
  "v_mfma_f32_16x16x32_bf16 v[76:79], v[44:47], v[8:11], v[76:79]\n\t" \
  "ds_read_b64_tr_b16 v[42:43], v61 offset:29184\n\t" \
  "v_mfma_f32_16x16x32_bf16 v[84:87], v[44:47], v[12:15], v[84:87]\n\t" \
  "v_mfma_f32_16x16x32_bf16 v[92:95], v[44:47], v[16:19], v[92:95]\n\t" \
  "N5a_%=:\n\t" \
  "s_waitcnt lgkmcnt(4)\n\t" \
  "v_mfma_f32_16x16x32_bf16 v[160:163], v[48:51], v[20:23], v[160:163]\n\t" \
  "ds_read_b64_tr_b16 v[44:45], v61 offset:32768\n\t" \
  "v_mfma_f32_16x16x32_bf16 v[96:99], v[48:51], v[24:27], v[96:99]\n\t" \
  "ds_read_b64_tr_b16 v[46:47], v61 offset:33280\n\t" \
  "v_mfma_f32_16x16x32_bf16 v[104:107], v[48:51], v[28:31], v[104:107]\n\t" \
  "s_barrier\n\t" \
  "v_mfma_f32_16x16x32_bf16 v[112:115], v[48:51], v[32:35], v[112:115]\n\t" \
  "v_mfma_f32_16x16x32_bf16 v[120:123], v[48:51], v[36:39], v[120:123]\n\t" \
  "s_waitcnt lgkmcnt(4)\n\t" \
  "v_mfma_f32_16x16x32_bf16 v[128:131], v[24:27], v[52:55], v[128:131]\n\t" \
  "ds_read_b64_tr_b16 v[48:49], v60 offset:24576\n\t" \
  "v_mfma_f32_16x16x32_bf16 v[136:139], v[28:31], v[52:55], v[136:139]\n\t" \
  "ds_read_b64_tr_b16 v[50:51], v60 offset:25088\n\t" \
  "v_mfma_f32_16x16x32_bf16 v[144:147], v[32:35], v[52:55], v[144:147]\n\t" \
  "ds_read_b128 v[0:3], v56 offset:0\n\t" \
  "v_mfma_f32_16x16x32_bf16 v[152:155], v[36:39], v[52:55], v[152:155]\n\t" \
  "ds_read_b128 v[4:7], v56 offset:2048\n\t" \
  "s_waitcnt lgkmcnt(6)\n\t" \
  "v_mfma_f32_16x16x32_bf16 v[164:167], v[40:43], v[20:23], v[164:167]\n\t" \
  "ds_read_b64_tr_b16 v[52:53], v61 offset:24576\n\t" \
  "v_mfma_f32_16x16x32_bf16 v[100:103], v[40:43], v[24:27], v[100:103]\n\t" \
  "ds_read_b64_tr_b16 v[54:55], v61 offset:25088\n\t" \
  "v_mfma_f32_16x16x32_bf16 v[108:111], v[40:43], v[28:31], v[108:111]\n\t" \
  "ds_read_b128 v[8:11], v56 offset:4096\n\t" \
  "v_mfma_f32_16x16x32_bf16 v[116:119], v[40:43], v[32:35], v[116:119]\n\t" \
  "ds_read_b128 v[12:15], v56 offset:6144\n\t" \
  "v_mfma_f32_16x16x32_bf16 v[124:127], v[40:43], v[36:39], v[124:127]\n\t" \
  "s_waitcnt lgkmcnt(8)\n\t" \
  "v_mfma_f32_16x16x32_bf16 v[132:135], v[24:27], v[44:47], v[132:135]\n\t" \
  "ds_read_b64_tr_b16 v[40:41], v58 offset:4096\n\t" \
  "v_mfma_f32_16x16x32_bf16 v[140:143], v[28:31], v[44:47], v[140:143]\n\t" \
  "ds_read_b64_tr_b16 v[42:43], v58 offset:4608\n\t" \
  "v_mfma_f32_16x16x32_bf16 v[148:151], v[32:35], v[44:47], v[148:151]\n\t" \
  "ds_read_b128 v[16:19], v56 offset:8192\n\t" \
  "v_mfma_f32_16x16x32_bf16 v[156:159], v[36:39], v[44:47], v[156:159]\n\t" \
  "s_waitcnt lgkmcnt(9)\n\t" \
  "v_mfma_f32_16x16x32_bf16 v[64:67], v[48:51], v[24:27], v[64:67]\n\t" \
  "ds_read_b64_tr_b16 v[44:45], v58 offset:8192\n\t" \
  "v_mfma_f32_16x16x32_bf16 v[72:75], v[48:51], v[28:31], v[72:75]\n\t" \
  "ds_read_b64_tr_b16 v[46:47], v58 offset:8704\n\t" \
  "v_mfma_f32_16x16x32_bf16 v[80:83], v[48:51], v[32:35], v[80:83]\n\t" \
  "v_mfma_f32_16x16x32_bf16 v[88:91], v[48:51], v[36:39], v[88:91]\n\t" \
  "s_waitcnt lgkmcnt(7)\n\t" \
  "v_mfma_f32_16x16x32_bf16 v[68:71], v[52:55], v[24:27], v[68:71]\n\t" \
  "ds_read_b64_tr_b16 v[48:49], v59 offset:4096\n\t" \
  "v_mfma_f32_16x16x32_bf16 v[76:79], v[52:55], v[28:31], v[76:79]\n\t" \
  "ds_read_b64_tr_b16 v[50:51], v59 offset:4608\n\t" \
  "s_addk_i32 %[it], 1\n\t" \
  "v_mfma_f32_16x16x32_bf16 v[84:87], v[52:55], v[32:35], v[84:87]\n\t" \
  "s_cmp_lt_u32 %[it], 6\n\t" \
  "v_mfma_f32_16x16x32_bf16 v[92:95], v[52:55], v[36:39], v[92:95]\n\t" \
  "s_cbranch_scc1 N0a_%=\n\t" \
  "s_setprio 3\n\t" \
  "v_cvt_pk_bf16_f32 v120, v120, v121\n\t" \
  "v_cvt_pk_bf16_f32 v121, v122, v123\n\t" \
  "v_cvt_pk_bf16_f32 v122, v124, v125\n\t" \
  "v_cvt_pk_bf16_f32 v123, v126, v127\n\t" \
  "v_cvt_pk_bf16_f32 v28, v152, v153\n\t" \
  "v_cvt_pk_bf16_f32 v29, v154, v155\n\t" \
  "v_cvt_pk_bf16_f32 v38, v156, v157\n\t" \
  "v_cvt_pk_bf16_f32 v39, v158, v159\n\t" \
  "ds_write_b128 v63, v[120:123]\n\t" \
  "ds_write2_b64 v63, v[28:29], v[38:39] offset0:128 offset1:129\n\t" \
  "v_mov_b32 v57, %[fa]\n\t" \
  "v_mov_b32 v58, 1\n\t" \
  "s_mov_b64 exec, 1\n\t" \
  "ds_write_b32 v57, v58\n\t" \
  "s_mov_b64 exec, -1\n\t" \
  "v_cvt_pk_bf16_f32 v160, v160, v161\n\t" \
  "v_cvt_pk_bf16_f32 v161, v162, v163\n\t" \
  "v_cvt_pk_bf16_f32 v162, v164, v165\n\t" \
  "v_cvt_pk_bf16_f32 v163, v166, v167\n\t" \
  "s_branch NX_%=\n\t" \
  "NB_%=:\n\t" \
  "ds_read_b64_tr_b16 v[40:41], v60 offset:20480\n\t" \
  "ds_read_b64_tr_b16 v[42:43], v60 offset:20992\n\t" \
  "ds_read_b64_tr_b16 v[44:45], v60 offset:16384\n\t" \
  "ds_read_b64_tr_b16 v[46:47], v60 offset:16896\n\t" \
  "ds_read_b64_tr_b16 v[48:49], v61 offset:20480\n\t" \
  "ds_read_b64_tr_b16 v[50:51], v61 offset:20992\n\t" \
  "ds_read_b128 v[0:3], v56 offset:36864\n\t" \
  "ds_read_b128 v[4:7], v56 offset:47104\n\t" \
  "ds_read_b128 v[8:11], v56 offset:49152\n\t" \
  "ds_read_b128 v[12:15], v56 offset:51200\n\t" \
  "ds_read_b128 v[16:19], v56 offset:53248\n\t" \
  "s_waitcnt lgkmcnt(0)\n\t" \
  "s_branch N4b_%=\n\t" \
  "N0b_%=:\n\t" \
  "s_waitcnt lgkmcnt(4)\n\t" \
  "v_mfma_f32_16x16x32_bf16 v[160:163], v[0:3], v[40:43], v[160:163]\n\t" \
  "ds_read_b64_tr_b16 v[52:53], v59 offset:4096\n\t" \
  "v_mfma_f32_16x16x32_bf16 v[128:131], v[4:7], v[40:43], v[128:131]\n\t" \
  "ds_read_b64_tr_b16 v[54:55], v59 offset:4608\n\t" \
  "v_mfma_f32_16x16x32_bf16 v[136:139], v[8:11], v[40:43], v[136:139]\n\t" \
  "s_barrier\n\t" \
  "v_mfma_f32_16x16x32_bf16 v[144:147], v[12:15], v[40:43], v[144:147]\n\t" \
  "v_mfma_f32_16x16x32_bf16 v[152:155], v[16:19], v[40:43], v[152:155]\n\t" \
  "s_waitcnt lgkmcnt(4)\n\t" \
  "v_mfma_f32_16x16x32_bf16 v[96:99], v[44:47], v[4:7], v[96:99]\n\t" \
  "ds_read_b64_tr_b16 v[40:41], v58 offset:0\n\t" \
  "v_mfma_f32_16x16x32_bf16 v[104:107], v[44:47], v[8:11], v[104:107]\n\t" \
  "ds_read_b64_tr_b16 v[42:43], v58 offset:512\n\t" \
  "v_mfma_f32_16x16x32_bf16 v[112:115], v[44:47], v[12:15], v[112:115]\n\t" \
  "ds_read_b128 v[20:23], v57 offset:0\n\t" \
  "v_mfma_f32_16x16x32_bf16 v[120:123], v[44:47], v[16:19], v[120:123]\n\t" \
  "ds_read_b128 v[24:27], v57 offset:10240\n\t" \
  "s_waitcnt lgkmcnt(6)\n\t" \
  "v_mfma_f32_16x16x32_bf16 v[164:167], v[0:3], v[48:51], v[164:167]\n\t" \
  "ds_read_b64_tr_b16 v[44:45], v59 offset:0\n\t" \
  "v_mfma_f32_16x16x32_bf16 v[132:135], v[4:7], v[48:51], v[132:135]\n\t" \
  "ds_read_b64_tr_b16 v[46:47], v59 offset:512\n\t" \
  "v_mfma_f32_16x16x32_bf16 v[140:143], v[8:11], v[48:51], v[140:143]\n\t" \
  "ds_read_b128 v[28:31], v57 offset:12288\n\t" \
  "v_mfma_f32_16x16x32_bf16 v[148:151], v[12:15], v[48:51], v[148:151]\n\t" \
  "ds_read_b128 v[32:35], v57 offset:14336\n\t" \
  "v_mfma_f32_16x16x32_bf16 v[156:159], v[16:19], v[48:51], v[156:159]\n\t" \
  "s_waitcnt lgkmcnt(8)\n\t" \
  "v_mfma_f32_16x16x32_bf16 v[100:103], v[52:55], v[4:7], v[100:103]\n\t" \
  "ds_read_b64_tr_b16 v[48:49], v58 offset:20480\n\t" \
  "v_mfma_f32_16x16x32_bf16 v[108:111], v[52:55], v[8:11], v[108:111]\n\t" \
  "ds_read_b64_tr_b16 v[50:51], v58 offset:20992\n\t" \
  "v_mfma_f32_16x16x32_bf16 v[116:119], v[52:55], v[12:15], v[116:119]\n\t" \
  "ds_read_b128 v[36:39], v57 offset:16384\n\t" \
  "v_mfma_f32_16x16x32_bf16 v[124:127], v[52:55], v[16:19], v[124:127]\n\t" \
  "s_waitcnt lgkmcnt(9)\n\t" \
  "v_mfma_f32_16x16x32_bf16 v[64:67], v[40:43], v[4:7], v[64:67]\n\t" \
  "ds_read_b64_tr_b16 v[52:53], v58 offset:16384\n\t" \
  "v_mfma_f32_16x16x32_bf16 v[72:75], v[40:43], v[8:11], v[72:75]\n\t" \
  "ds_read_b64_tr_b16 v[54:55], v58 offset:16896\n\t" \
  "v_mfma_f32_16x16x32_bf16 v[80:83], v[40:43], v[12:15], v[80:83]\n\t" \
  "v_mfma_f32_16x16x32_bf16 v[88:91], v[40:43], v[16:19], v[88:91]\n\t" \
  "s_waitcnt lgkmcnt(7)\n\t" \
  "v_mfma_f32_16x16x32_bf16 v[68:71], v[44:47], v[4:7], v[68:71]\n\t" \
  "ds_read_b64_tr_b16 v[40:41], v59 offset:20480\n\t" \
  "v_mfma_f32_16x16x32_bf16 v[76:79], v[44:47], v[8:11], v[76:79]\n\t" \
  "ds_read_b64_tr_b16 v[42:43], v59 offset:20992\n\t" \
  "v_mfma_f32_16x16x32_bf16 v[84:87], v[44:47], v[12:15], v[84:87]\n\t" \
  "v_mfma_f32_16x16x32_bf16 v[92:95], v[44:47], v[16:19], v[92:95]\n\t" \
  "N1b_%=:\n\t" \
  "s_waitcnt lgkmcnt(4)\n\t" \
  "v_mfma_f32_16x16x32_bf16 v[160:163], v[20:23], v[48:51], v[160:163]\n\t" \
  "ds_read_b64_tr_b16 v[44:45], v59 offset:16384\n\t" \
  "v_mfma_f32_16x16x32_bf16 v[128:131], v[24:27], v[48:51], v[128:131]\n\t" \
  "ds_read_b64_tr_b16 v[46:47], v59 offset:16896\n\t" \
  "v_mfma_f32_16x16x32_bf16 v[136:139], v[28:31], v[48:51], v[136:139]\n\t" \
  "s_barrier\n\t" \
  "v_mfma_f32_16x16x32_bf16 v[144:147], v[32:35], v[48:51], v[144:147]\n\t" \
  "v_mfma_f32_16x16x32_bf16 v[152:155], v[36:39], v[48:51], v[152:155]\n\t" \
  "s_waitcnt lgkmcnt(4)\n\t" \
  "v_mfma_f32_16x16x32_bf16 v[96:99], v[52:55], v[24:27], v[96:99]\n\t" \
  "ds_read_b64_tr_b16 v[48:49], v58 offset:12288\n\t" \
  "v_mfma_f32_16x16x32_bf16 v[104:107], v[52:55], v[28:31], v[104:107]\n\t" \
  "ds_read_b64_tr_b16 v[50:51], v58 offset:12800\n\t" \
  "v_mfma_f32_16x16x32_bf16 v[112:115], v[52:55], v[32:35], v[112:115]\n\t" \
  "ds_read_b128 v[0:3], v56 offset:18432\n\t" \
  "v_mfma_f32_16x16x32_bf16 v[120:123], v[52:55], v[36:39], v[120:123]\n\t" \
  "ds_read_b128 v[4:7], v56 offset:28672\n\t" \
  "s_waitcnt lgkmcnt(6)\n\t" \
  "v_mfma_f32_16x16x32_bf16 v[164:167], v[20:23], v[40:43], v[164:167]\n\t" \
  "ds_read_b64_tr_b16 v[52:53], v59 offset:12288\n\t" \
  "v_mfma_f32_16x16x32_bf16 v[132:135], v[24:27], v[40:43], v[132:135]\n\t" \
  "ds_read_b64_tr_b16 v[54:55], v59 offset:12800\n\t" \
  "v_mfma_f32_16x16x32_bf16 v[140:143], v[28:31], v[40:43], v[140:143]\n\t" \
  "ds_read_b128 v[8:11], v56 offset:30720\n\t" \
  "v_mfma_f32_16x16x32_bf16 v[148:151], v[32:35], v[40:43], v[148:151]\n\t" \
  "ds_read_b128 v[12:15], v56 offset:32768\n\t" \
  "v_mfma_f32_16x16x32_bf16 v[156:159], v[36:39], v[40:43], v[156:159]\n\t" \
  "s_waitcnt lgkmcnt(8)\n\t" \
  "v_mfma_f32_16x16x32_bf16 v[100:103], v[44:47], v[24:27], v[100:103]\n\t" \
  "ds_read_b64_tr_b16 v[40:41], v58 offset:32768\n\t" \
  "v_mfma_f32_16x16x32_bf16 v[108:111], v[44:47], v[28:31], v[108:111]\n\t" \
  "ds_read_b64_tr_b16 v[42:43], v58 offset:33280\n\t" \
  "v_mfma_f32_16x16x32_bf16 v[116:119], v[44:47], v[32:35], v[116:119]\n\t" \
  "ds_read_b128 v[16:19], v56 offset:34816\n\t" \
  "v_mfma_f32_16x16x32_bf16 v[124:127], v[44:47], v[36:39], v[124:127]\n\t" \
  "s_waitcnt lgkmcnt(9)\n\t" \
  "v_mfma_f32_16x16x32_bf16 v[64:67], v[48:51], v[24:27], v[64:67]\n\t" \
  "ds_read_b64_tr_b16 v[44:45], v58 offset:28672\n\t" \
  "v_mfma_f32_16x16x32_bf16 v[72:75], v[48:51], v[28:31], v[72:75]\n\t" \
  "ds_read_b64_tr_b16 v[46:47], v58 offset:29184\n\t" \
  "v_mfma_f32_16x16x32_bf16 v[80:83], v[48:51], v[32:35], v[80:83]\n\t" \
  "v_mfma_f32_16x16x32_bf16 v[88:91], v[48:51], v[36:39], v[88:91]\n\t" \
  "s_waitcnt lgkmcnt(7)\n\t" \
  "v_mfma_f32_16x16x32_bf16 v[68:71], v[52:55], v[24:27], v[68:71]\n\t" \
  "ds_read_b64_tr_b16 v[48:49], v59 offset:32768\n\t" \
  "v_mfma_f32_16x16x32_bf16 v[76:79], v[52:55], v[28:31], v[76:79]\n\t" \
  "ds_read_b64_tr_b16 v[50:51], v59 offset:33280\n\t" \
  "v_mfma_f32_16x16x32_bf16 v[84:87], v[52:55], v[32:35], v[84:87]\n\t" \
  "v_mfma_f32_16x16x32_bf16 v[92:95], v[52:55], v[36:39], v[92:95]\n\t" \
  "N2b_%=:\n\t" \
  "s_waitcnt lgkmcnt(4)\n\t" \
  "v_mfma_f32_16x16x32_bf16 v[160:163], v[0:3], v[40:43], v[160:163]\n\t" \
  "ds_read_b64_tr_b16 v[52:53], v59 offset:28672\n\t" \
  "v_mfma_f32_16x16x32_bf16 v[128:131], v[4:7], v[40:43], v[128:131]\n\t" \
  "ds_read_b64_tr_b16 v[54:55], v59 offset:29184\n\t" \
  "v_mfma_f32_16x16x32_bf16 v[136:139], v[8:11], v[40:43], v[136:139]\n\t" \
  "s_barrier\n\t" \
  "v_mfma_f32_16x16x32_bf16 v[144:147], v[12:15], v[40:43], v[144:147]\n\t" \
  "v_mfma_f32_16x16x32_bf16 v[152:155], v[16:19], v[40:43], v[152:155]\n\t" \
  "s_waitcnt lgkmcnt(4)\n\t" \
  "v_mfma_f32_16x16x32_bf16 v[96:99], v[44:47], v[4:7], v[96:99]\n\t" \
  "ds_read_b64_tr_b16 v[40:41], v58 offset:24576\n\t" \
  "v_mfma_f32_16x16x32_bf16 v[104:107], v[44:47], v[8:11], v[104:107]\n\t" \
  "ds_read_b64_tr_b16 v[42:43], v58 offset:25088\n\t" \
  "v_mfma_f32_16x16x32_bf16 v[112:115], v[44:47], v[12:15], v[112:115]\n\t" \
  "ds_read_b128 v[20:23], v57 offset:18432\n\t" \
  "v_mfma_f32_16x16x32_bf16 v[120:123], v[44:47], v[16:19], v[120:123]\n\t" \
  "ds_read_b128 v[24:27], v57 offset:28672\n\t" \
  "s_waitcnt lgkmcnt(6)\n\t" \
  "v_mfma_f32_16x16x32_bf16 v[164:167], v[0:3], v[48:51], v[164:167]\n\t" \
  "ds_read_b64_tr_b16 v[44:45], v59 offset:24576\n\t" \
  "v_mfma_f32_16x16x32_bf16 v[132:135], v[4:7], v[48:51], v[132:135]\n\t" \
  "ds_read_b64_tr_b16 v[46:47], v59 offset:25088\n\t" \
  "v_mfma_f32_16x16x32_bf16 v[140:143], v[8:11], v[48:51], v[140:143]\n\t" \
  "ds_read_b128 v[28:31], v57 offset:30720\n\t" \
  "v_mfma_f32_16x16x32_bf16 v[148:151], v[12:15], v[48:51], v[148:151]\n\t" \
  "ds_read_b128 v[32:35], v57 offset:32768\n\t" \
  "v_mfma_f32_16x16x32_bf16 v[156:159], v[16:19], v[48:51], v[156:159]\n\t" \
  "s_waitcnt lgkmcnt(8)\n\t" \
  "v_mfma_f32_16x16x32_bf16 v[100:103], v[52:55], v[4:7], v[100:103]\n\t" \
  "ds_read_b64_tr_b16 v[48:49], v60 offset:8192\n\t" \
  "v_mfma_f32_16x16x32_bf16 v[108:111], v[52:55], v[8:11], v[108:111]\n\t" \
  "ds_read_b64_tr_b16 v[50:51], v60 offset:8704\n\t" \
  "v_mfma_f32_16x16x32_bf16 v[116:119], v[52:55], v[12:15], v[116:119]\n\t" \
  "ds_read_b128 v[36:39], v57 offset:34816\n\t" \
  "v_mfma_f32_16x16x32_bf16 v[124:127], v[52:55], v[16:19], v[124:127]\n\t" \
  "s_waitcnt lgkmcnt(9)\n\t" \
  "v_mfma_f32_16x16x32_bf16 v[64:67], v[40:43], v[4:7], v[64:67]\n\t" \
  "ds_read_b64_tr_b16 v[52:53], v60 offset:4096\n\t" \
  "v_mfma_f32_16x16x32_bf16 v[72:75], v[40:43], v[8:11], v[72:75]\n\t" \
  "ds_read_b64_tr_b16 v[54:55], v60 offset:4608\n\t" \
  "v_mfma_f32_16x16x32_bf16 v[80:83], v[40:43], v[12:15], v[80:83]\n\t" \
  "v_mfma_f32_16x16x32_bf16 v[88:91], v[40:43], v[16:19], v[88:91]\n\t" \
  "s_waitcnt lgkmcnt(7)\n\t" \
  "v_mfma_f32_16x16x32_bf16 v[68:71], v[44:47], v[4:7], v[68:71]\n\t" \
  "ds_read_b64_tr_b16 v[40:41], v61 offset:8192\n\t" \
  "v_mfma_f32_16x16x32_bf16 v[76:79], v[44:47], v[8:11], v[76:79]\n\t" \
  "ds_read_b64_tr_b16 v[42:43], v61 offset:8704\n\t" \
  "v_mfma_f32_16x16x32_bf16 v[84:87], v[44:47], v[12:15], v[84:87]\n\t" \
  "v_mfma_f32_16x16x32_bf16 v[92:95], v[44:47], v[16:19], v[92:95]\n\t" \
  "N3b_%=:\n\t" \
  "s_waitcnt lgkmcnt(4)\n\t" \
  "v_mfma_f32_16x16x32_bf16 v[160:163], v[20:23], v[48:51], v[160:163]\n\t" \
  "ds_read_b64_tr_b16 v[44:45], v61 offset:4096\n\t" \
  "v_mfma_f32_16x16x32_bf16 v[128:131], v[24:27], v[48:51], v[128:131]\n\t" \
  "ds_read_b64_tr_b16 v[46:47], v61 offset:4608\n\t" \
  "v_mfma_f32_16x16x32_bf16 v[136:139], v[28:31], v[48:51], v[136:139]\n\t" \
  "s_barrier\n\t" \
  "v_mfma_f32_16x16x32_bf16 v[144:147], v[32:35], v[48:51], v[144:147]\n\t" \
  "v_mfma_f32_16x16x32_bf16 v[152:155], v[36:39], v[48:51], v[152:155]\n\t" \
  "s_waitcnt lgkmcnt(4)\n\t" \
  "v_mfma_f32_16x16x32_bf16 v[96:99], v[52:55], v[24:27], v[96:99]\n\t" \
  "ds_read_b64_tr_b16 v[48:49], v60 offset:0\n\t" \
  "v_mfma_f32_16x16x32_bf16 v[104:107], v[52:55], v[28:31], v[104:107]\n\t" \
  "ds_read_b64_tr_b16 v[50:51], v60 offset:512\n\t" \
  "v_mfma_f32_16x16x32_bf16 v[112:115], v[52:55], v[32:35], v[112:115]\n\t" \
  "ds_read_b128 v[0:3], v56 offset:36864\n\t" \
  "v_mfma_f32_16x16x32_bf16 v[120:123], v[52:55], v[36:39], v[120:123]\n\t" \
  "ds_read_b128 v[4:7], v56 offset:47104\n\t" \
  "s_waitcnt lgkmcnt(6)\n\t" \
  "v_mfma_f32_16x16x32_bf16 v[164:167], v[20:23], v[40:43], v[164:167]\n\t" \
  "ds_read_b64_tr_b16 v[52:53], v61 offset:0\n\t" \
  "v_mfma_f32_16x16x32_bf16 v[132:135], v[24:27], v[40:43], v[132:135]\n\t" \
  "ds_read_b64_tr_b16 v[54:55], v61 offset:512\n\t" \
  "v_mfma_f32_16x16x32_bf16 v[140:143], v[28:31], v[40:43], v[140:143]\n\t" \
  "ds_read_b128 v[8:11], v56 offset:49152\n\t" \
  "v_mfma_f32_16x16x32_bf16 v[148:151], v[32:35], v[40:43], v[148:151]\n\t" \
  "ds_read_b128 v[12:15], v56 offset:51200\n\t" \
  "v_mfma_f32_16x16x32_bf16 v[156:159], v[36:39], v[40:43], v[156:159]\n\t" \
  "s_waitcnt lgkmcnt(8)\n\t" \
  "v_mfma_f32_16x16x32_bf16 v[100:103], v[44:47], v[24:27], v[100:103]\n\t" \
  "ds_read_b64_tr_b16 v[40:41], v60 offset:20480\n\t" \
  "v_mfma_f32_16x16x32_bf16 v[108:111], v[44:47], v[28:31], v[108:111]\n\t" \
  "ds_read_b64_tr_b16 v[42:43], v60 offset:20992\n\t" \
  "v_mfma_f32_16x16x32_bf16 v[116:119], v[44:47], v[32:35], v[116:119]\n\t" \
  "ds_read_b128 v[16:19], v56 offset:53248\n\t" \
  "v_mfma_f32_16x16x32_bf16 v[124:127], v[44:47], v[36:39], v[124:127]\n\t" \
  "s_waitcnt lgkmcnt(9)\n\t" \
  "v_mfma_f32_16x16x32_bf16 v[64:67], v[48:51], v[24:27], v[64:67]\n\t" \
  "ds_read_b64_tr_b16 v[44:45], v60 offset:16384\n\t" \
  "v_mfma_f32_16x16x32_bf16 v[72:75], v[48:51], v[28:31], v[72:75]\n\t" \
  "ds_read_b64_tr_b16 v[46:47], v60 offset:16896\n\t" \
  "v_mfma_f32_16x16x32_bf16 v[80:83], v[48:51], v[32:35], v[80:83]\n\t" \
  "v_mfma_f32_16x16x32_bf16 v[88:91], v[48:51], v[36:39], v[88:91]\n\t" \
  "s_waitcnt lgkmcnt(7)\n\t" \
  "v_mfma_f32_16x16x32_bf16 v[68:71], v[52:55], v[24:27], v[68:71]\n\t" \
  "ds_read_b64_tr_b16 v[48:49], v61 offset:20480\n\t" \
  "v_mfma_f32_16x16x32_bf16 v[76:79], v[52:55], v[28:31], v[76:79]\n\t" \
  "ds_read_b64_tr_b16 v[50:51], v61 offset:20992\n\t" \
  "v_mfma_f32_16x16x32_bf16 v[84:87], v[52:55], v[32:35], v[84:87]\n\t" \
  "v_mfma_f32_16x16x32_bf16 v[92:95], v[52:55], v[36:39], v[92:95]\n\t" \
  "N4b_%=:\n\t" \
  "s_waitcnt lgkmcnt(4)\n\t" \
  "v_mfma_f32_16x16x32_bf16 v[160:163], v[0:3], v[40:43], v[160:163]\n\t" \
  "ds_read_b64_tr_b16 v[52:53], v61 offset:16384\n\t" \
  "v_mfma_f32_16x16x32_bf16 v[128:131], v[4:7], v[40:43], v[128:131]\n\t" \
  "ds_read_b64_tr_b16 v[54:55], v61 offset:16896\n\t" \
  "v_mfma_f32_16x16x32_bf16 v[136:139], v[8:11], v[40:43], v[136:139]\n\t" \
  "s_barrier\n\t" \
  "v_mfma_f32_16x16x32_bf16 v[144:147], v[12:15], v[40:43], v[144:147]\n\t" \
  "v_mfma_f32_16x16x32_bf16 v[152:155], v[16:19], v[40:43], v[152:155]\n\t" \
  "s_waitcnt lgkmcnt(4)\n\t" \
  "v_mfma_f32_16x16x32_bf16 v[96:99], v[44:47], v[4:7], v[96:99]\n\t" \
  "ds_read_b64_tr_b16 v[40:41], v60 offset:12288\n\t" \
  "v_mfma_f32_16x16x32_bf16 v[104:107], v[44:47], v[8:11], v[104:107]\n\t" \
  "ds_read_b64_tr_b16 v[42:43], v60 offset:12800\n\t" \
  "v_mfma_f32_16x16x32_bf16 v[112:115], v[44:47], v[12:15], v[112:115]\n\t" \
  "ds_read_b128 v[20:23], v57 offset:36864\n\t" \
  "v_mfma_f32_16x16x32_bf16 v[120:123], v[44:47], v[16:19], v[120:123]\n\t" \
  "ds_read_b128 v[24:27], v57 offset:47104\n\t" \
  "s_waitcnt lgkmcnt(6)\n\t" \
  "v_mfma_f32_16x16x32_bf16 v[164:167], v[0:3], v[48:51], v[164:167]\n\t" \
  "ds_read_b64_tr_b16 v[44:45], v61 offset:12288\n\t" \
  "v_mfma_f32_16x16x32_bf16 v[132:135], v[4:7], v[48:51], v[132:135]\n\t" \
  "ds_read_b64_tr_b16 v[46:47], v61 offset:12800\n\t" \
  "v_mfma_f32_16x16x32_bf16 v[140:143], v[8:11], v[48:51], v[140:143]\n\t" \
  "ds_read_b128 v[28:31], v57 offset:49152\n\t" \
  "v_mfma_f32_16x16x32_bf16 v[148:151], v[12:15], v[48:51], v[148:151]\n\t" \
  "ds_read_b128 v[32:35], v57 offset:51200\n\t" \
  "v_mfma_f32_16x16x32_bf16 v[156:159], v[16:19], v[48:51], v[156:159]\n\t" \
  "s_waitcnt lgkmcnt(8)\n\t" \
  "v_mfma_f32_16x16x32_bf16 v[100:103], v[52:55], v[4:7], v[100:103]\n\t" \
  "ds_read_b64_tr_b16 v[48:49], v60 offset:32768\n\t" \
  "v_mfma_f32_16x16x32_bf16 v[108:111], v[52:55], v[8:11], v[108:111]\n\t" \
  "ds_read_b64_tr_b16 v[50:51], v60 offset:33280\n\t" \
  "v_mfma_f32_16x16x32_bf16 v[116:119], v[52:55], v[12:15], v[116:119]\n\t" \
  "ds_read_b128 v[36:39], v57 offset:53248\n\t" \
  "v_mfma_f32_16x16x32_bf16 v[124:127], v[52:55], v[16:19], v[124:127]\n\t" \
  "s_waitcnt lgkmcnt(9)\n\t" \
  "v_mfma_f32_16x16x32_bf16 v[64:67], v[40:43], v[4:7], v[64:67]\n\t" \
  "ds_read_b64_tr_b16 v[52:53], v60 offset:28672\n\t" \
  "v_mfma_f32_16x16x32_bf16 v[72:75], v[40:43], v[8:11], v[72:75]\n\t" \
  "ds_read_b64_tr_b16 v[54:55], v60 offset:29184\n\t" \
  "v_mfma_f32_16x16x32_bf16 v[80:83], v[40:43], v[12:15], v[80:83]\n\t" \
  "v_mfma_f32_16x16x32_bf16 v[88:91], v[40:43], v[16:19], v[88:91]\n\t" \
  "s_waitcnt lgkmcnt(7)\n\t" \
  "v_mfma_f32_16x16x32_bf16 v[68:71], v[44:47], v[4:7], v[68:71]\n\t" \
  "ds_read_b64_tr_b16 v[40:41], v61 offset:32768\n\t" \
  "v_mfma_f32_16x16x32_bf16 v[76:79], v[44:47], v[8:11], v[76:79]\n\t" \
  "ds_read_b64_tr_b16 v[42:43], v61 offset:33280\n\t" \
  "v_mfma_f32_16x16x32_bf16 v[84:87], v[44:47], v[12:15], v[84:87]\n\t" \
  "v_mfma_f32_16x16x32_bf16 v[92:95], v[44:47], v[16:19], v[92:95]\n\t" \
  "N5b_%=:\n\t" \
  "s_waitcnt lgkmcnt(4)\n\t" \
  "v_mfma_f32_16x16x32_bf16 v[160:163], v[20:23], v[48:51], v[160:163]\n\t" \
  "ds_read_b64_tr_b16 v[44:45], v61 offset:28672\n\t" \
  "v_mfma_f32_16x16x32_bf16 v[128:131], v[24:27], v[48:51], v[128:131]\n\t" \
  "ds_read_b64_tr_b16 v[46:47], v61 offset:29184\n\t" \
  "v_mfma_f32_16x16x32_bf16 v[136:139], v[28:31], v[48:51], v[136:139]\n\t" \
  "s_barrier\n\t" \
  "v_mfma_f32_16x16x32_bf16 v[144:147], v[32:35], v[48:51], v[144:147]\n\t" \
  "v_mfma_f32_16x16x32_bf16 v[152:155], v[36:39], v[48:51], v[152:155]\n\t" \
  "s_waitcnt lgkmcnt(4)\n\t" \
  "v_mfma_f32_16x16x32_bf16 v[96:99], v[52:55], v[24:27], v[96:99]\n\t" \
  "ds_read_b64_tr_b16 v[48:49], v60 offset:24576\n\t" \
  "v_mfma_f32_16x16x32_bf16 v[104:107], v[52:55], v[28:31], v[104:107]\n\t" \
  "ds_read_b64_tr_b16 v[50:51], v60 offset:25088\n\t" \
  "v_mfma_f32_16x16x32_bf16 v[112:115], v[52:55], v[32:35], v[112:115]\n\t" \
  "ds_read_b128 v[0:3], v56 offset:0\n\t" \
  "v_mfma_f32_16x16x32_bf16 v[120:123], v[52:55], v[36:39], v[120:123]\n\t" \
  "ds_read_b128 v[4:7], v56 offset:10240\n\t" \
  "s_waitcnt lgkmcnt(6)\n\t" \
  "v_mfma_f32_16x16x32_bf16 v[164:167], v[20:23], v[40:43], v[164:167]\n\t" \
  "ds_read_b64_tr_b16 v[52:53], v61 offset:24576\n\t" \
  "v_mfma_f32_16x16x32_bf16 v[132:135], v[24:27], v[40:43], v[132:135]\n\t" \
  "ds_read_b64_tr_b16 v[54:55], v61 offset:25088\n\t" \
  "v_mfma_f32_16x16x32_bf16 v[140:143], v[28:31], v[40:43], v[140:143]\n\t" \
  "ds_read_b128 v[8:11], v56 offset:12288\n\t" \
  "v_mfma_f32_16x16x32_bf16 v[148:151], v[32:35], v[40:43], v[148:151]\n\t" \
  "ds_read_b128 v[12:15], v56 offset:14336\n\t" \
  "v_mfma_f32_16x16x32_bf16 v[156:159], v[36:39], v[40:43], v[156:159]\n\t" \
  "s_waitcnt lgkmcnt(8)\n\t" \
  "v_mfma_f32_16x16x32_bf16 v[100:103], v[44:47], v[24:27], v[100:103]\n\t" \
  "ds_read_b64_tr_b16 v[40:41], v58 offset:8192\n\t" \
  "v_mfma_f32_16x16x32_bf16 v[108:111], v[44:47], v[28:31], v[108:111]\n\t" \
  "ds_read_b64_tr_b16 v[42:43], v58 offset:8704\n\t" \
  "v_mfma_f32_16x16x32_bf16 v[116:119], v[44:47], v[32:35], v[116:119]\n\t" \
  "ds_read_b128 v[16:19], v56 offset:16384\n\t" \
  "v_mfma_f32_16x16x32_bf16 v[124:127], v[44:47], v[36:39], v[124:127]\n\t" \
  "s_waitcnt lgkmcnt(9)\n\t" \
  "v_mfma_f32_16x16x32_bf16 v[64:67], v[48:51], v[24:27], v[64:67]\n\t" \
  "ds_read_b64_tr_b16 v[44:45], v58 offset:4096\n\t" \
  "v_mfma_f32_16x16x32_bf16 v[72:75], v[48:51], v[28:31], v[72:75]\n\t" \
  "ds_read_b64_tr_b16 v[46:47], v58 offset:4608\n\t" \
  "v_mfma_f32_16x16x32_bf16 v[80:83], v[48:51], v[32:35], v[80:83]\n\t" \
  "v_mfma_f32_16x16x32_bf16 v[88:91], v[48:51], v[36:39], v[88:91]\n\t" \
  "s_waitcnt lgkmcnt(7)\n\t" \
  "v_mfma_f32_16x16x32_bf16 v[68:71], v[52:55], v[24:27], v[68:71]\n\t" \
  "ds_read_b64_tr_b16 v[48:49], v59 offset:8192\n\t" \
  "v_mfma_f32_16x16x32_bf16 v[76:79], v[52:55], v[28:31], v[76:79]\n\t" \
  "ds_read_b64_tr_b16 v[50:51], v59 offset:8704\n\t" \
  "s_addk_i32 %[it], 1\n\t" \
  "v_mfma_f32_16x16x32_bf16 v[84:87], v[52:55], v[32:35], v[84:87]\n\t" \
  "s_cmp_lt_u32 %[it], 6\n\t" \
  "v_mfma_f32_16x16x32_bf16 v[92:95], v[52:55], v[36:39], v[92:95]\n\t" \
  "s_cbranch_scc1 N0b_%=\n\t" \
  "s_setprio 3\n\t" \
  "v_cvt_pk_bf16_f32 v160, v160, v161\n\t" \
  "v_cvt_pk_bf16_f32 v161, v162, v163\n\t" \
  "v_cvt_pk_bf16_f32 v162, v164, v165\n\t" \
  "v_cvt_pk_bf16_f32 v163, v166, v167\n\t" \
  "ds_write_b128 v63, v[160:163] offset:2048\n\t" \
  "v_mov_b32 v57, %[fa]\n\t" \
  "v_mov_b32 v58, 1\n\t" \
  "s_mov_b64 exec, 1\n\t" \
  "ds_write_b32 v57, v58\n\t" \
  "s_mov_b64 exec, -1\n\t" \
  "v_cvt_pk_bf16_f32 v120, v120, v121\n\t" \
  "v_cvt_pk_bf16_f32 v121, v122, v123\n\t" \
  "v_cvt_pk_bf16_f32 v122, v124, v125\n\t" \
  "v_cvt_pk_bf16_f32 v123, v126, v127\n\t" \
  "v_cvt_pk_bf16_f32 v28, v152, v153\n\t" \
  "v_cvt_pk_bf16_f32 v29, v154, v155\n\t" \
  "v_cvt_pk_bf16_f32 v38, v156, v157\n\t" \
  "v_cvt_pk_bf16_f32 v39, v158, v159\n\t" \
  "NX_%=:\n\t" \
  "v_cvt_pk_bf16_f32 v96, v96, v97\n\t" \
  "v_cvt_pk_bf16_f32 v97, v98, v99\n\t" \
  "v_cvt_pk_bf16_f32 v98, v100, v101\n\t" \
  "v_cvt_pk_bf16_f32 v99, v102, v103\n\t" \
  "v_cvt_pk_bf16_f32 v104, v104, v105\n\t" \
  "v_cvt_pk_bf16_f32 v105, v106, v107\n\t" \
  "v_cvt_pk_bf16_f32 v106, v108, v109\n\t" \
  "v_cvt_pk_bf16_f32 v107, v110, v111\n\t" \
  "v_mov_b32 v60, 0xff61b1e6\n\t" \
  "v_cndmask_b32_e64 v56, v60, 0, s[68:69]\n\t" \
  "v_cndmask_b32_e64 v57, v60, 0, s[70:71]\n\t" \
  "v_cndmask_b32_e64 v58, v60, 0, s[72:73]\n\t" \
  "v_cndmask_b32_e64 v59, v60, 0, s[74:75]\n\t" \
  "v_cvt_pk_bf16_f32 v72, v72, v73\n\t" \
  "v_cvt_pk_bf16_f32 v73, v74, v75\n\t" \
  "v_cvt_pk_bf16_f32 v74, v76, v77\n\t" \
  "v_cvt_pk_bf16_f32 v75, v78, v79\n\t" \
  "v_cndmask_b32_e64 v52, v60, 0, s[76:77]\n\t" \
  "v_cndmask_b32_e64 v53, v60, 0, s[78:79]\n\t" \
  "v_cndmask_b32_e64 v54, v60, 0, s[80:81]\n\t" \
  "v_cndmask_b32_e64 v55, v60, 0, s[82:83]\n\t" \
  "v_cvt_pk_bf16_f32 v24, v136, v137\n\t" \
  "v_cvt_pk_bf16_f32 v25, v138, v139\n\t" \
  "v_cvt_pk_bf16_f32 v34, v140, v141\n\t" \
  "v_cvt_pk_bf16_f32 v35, v142, v143\n\t" \
  "v_cvt_pk_bf16_f32 v26, v144, v145\n\t" \
  "v_cvt_pk_bf16_f32 v27, v146, v147\n\t" \
  "v_cvt_pk_bf16_f32 v36, v148, v149\n\t" \
  "v_cvt_pk_bf16_f32 v37, v150, v151\n\t" \
  "v_mfma_f32_16x16x32_bf16 v[136:139], v[96:99], v[72:75], v[56:59]\n\t" \
  "v_cvt_pk_bf16_f32 v112, v112, v113\n\t" \
  "v_cvt_pk_bf16_f32 v113, v114, v115\n\t" \
  "v_mfma_f32_16x16x32_bf16 v[140:143], v[104:107], v[72:75], v[52:55]\n\t" \
  "v_cvt_pk_bf16_f32 v114, v116, v117\n\t" \
  "v_cvt_pk_bf16_f32 v115, v118, v119\n\t" \
  "v_cvt_pk_bf16_f32 v80, v80, v81\n\t" \
  "v_cvt_pk_bf16_f32 v81, v82, v83\n\t" \
  "v_cvt_pk_bf16_f32 v82, v84, v85\n\t" \
  "v_cvt_pk_bf16_f32 v83, v86, v87\n\t" \
  "v_cvt_pk_bf16_f32 v22, v128, v129\n\t" \
  "v_cvt_pk_bf16_f32 v23, v130, v131\n\t" \
  "v_mfma_f32_16x16x32_bf16 v[144:147], v[104:107], v[80:83], v[56:59]\n\t" \
  "v_cvt_pk_bf16_f32 v32, v132, v133\n\t" \
  "v_cvt_pk_bf16_f32 v33, v134, v135\n\t" \
  "v_mfma_f32_16x16x32_bf16 v[148:151], v[112:115], v[80:83], v[52:55]\n\t" \
  "v_cvt_pk_bf16_f32 v88, v88, v89\n\t" \
  "v_cvt_pk_bf16_f32 v89, v90, v91\n\t" \
  "v_cvt_pk_bf16_f32 v90, v92, v93\n\t" \
  "v_cvt_pk_bf16_f32 v91, v94, v95\n\t" \
  "v_mov_b32 v61, %[fp]\n\t" \
  "v_add_u32 v60, %[vo], v63\n\t" \
  "v_mfma_f32_16x16x32_bf16 v[152:155], v[112:115], v[88:91], v[56:59]\n\t" \
  "ds_read_b32 v0, v61\n\t" \
  "ds_read_b64 v[20:21], v60\n\t" \
  "ds_read_b64 v[30:31], v60 offset:8\n\t" \
  "s_mov_b64 exec, %[rm]\n\t" \
  "ds_read_b128 v[160:163], v63\n\t" \
  "s_mov_b64 exec, -1\n\t" \
  "v_mfma_f32_16x16x32_bf16 v[156:159], v[120:123], v[88:91], v[52:55]\n\t" \
  "v_cvt_pk_bf16_f32 v64, v64, v65\n\t" \
  "v_cvt_pk_bf16_f32 v65, v66, v67\n\t" \
  "v_cvt_pk_bf16_f32 v66, v68, v69\n\t" \
  "v_cvt_pk_bf16_f32 v67, v70, v71\n\t" \
  "v_max_f32 v136, v136, v137\n\t" \
  "v_max_f32 v138, v138, v139\n\t" \
  "v_mfma_f32_16x16x32_bf16 v[132:135], v[96:99], v[64:67], v[52:55]\n\t" \
  "v_max_f32 v140, v140, v141\n\t" \
  "v_max_f32 v142, v142, v143\n\t" \
  "AW_%=:\n\t" \
  "s_waitcnt lgkmcnt(0)\n\t" \
  "v_cmp_eq_u32 vcc, 0, v0\n\t" \
  "v_max3_f32 v5, v136, v138, v140\n\t" \
  "v_max_f32 v5, v5, v142\n\t" \
  "s_cbranch_vccnz AR_%=\n\t" \
  "s_cmp_eq_u32 %[z], 0\n\t" \
  "s_cbranch_scc1 AZ_%=\n\t" \
  "AY_%=:\n\t" \
  "v_mfma_f32_16x16x32_bf16 v[128:131], v[160:163], v[64:67], v[56:59]\n\t" \
  "v_max_f32 v144, v144, v145\n\t" \
  "v_max_f32 v146, v146, v147\n\t" \
  "v_max_f32 v148, v148, v149\n\t" \
  "v_max_f32 v150, v150, v151\n\t" \
  "v_max3_f32 v6, v144, v146, v148\n\t" \
  "v_max_f32 v6, v6, v150\n\t" \
  "v_max_f32 v152, v152, v153\n\t" \
  "v_max_f32 v154, v154, v155\n\t" \
  "v_max_f32 v156, v156, v157\n\t" \
  "v_max_f32 v158, v158, v159\n\t" \
  "v_max3_f32 v7, v152, v154, v156\n\t" \
  "v_max_f32 v7, v7, v158\n\t" \
  "v_mov_b32 v8, 0xc0c0100\n\t" \
  "v_mov_b32 v9, 0x1000c0c\n\t" \
  "v_max_f32 v128, v128, v129\n\t" \
  "v_max_f32 v130, v130, v131\n\t" \
  "v_max_f32 v132, v132, v133\n\t" \
  "v_max_f32 v134, v134, v135\n\t" \
  "v_max3_f32 v4, v128, v130, v132\n\t" \
  "v_max_f32 v4, v4, v134\n\t" \
  "v_cndmask_b32_e64 v60, v8, v9, s[84:85]\n\t" \
  "v_mov_b32 v8, 0xc0c0302\n\t" \
  "v_permlane16_swap_b32 v6, v7\n\t" \
  "v_permlane16_swap_b32 v4, v5\n\t" \
  "v_max_f32 v6, v6, v7\n\t" \
  "v_max_f32 v4, v4, v5\n\t" \
  "v_mov_b32 v9, 0x3020c0c\n\t" \
  "v_cndmask_b32_e64 v61, v8, v9, s[84:85]\n\t" \
  "v_permlane32_swap_b32 v4, v6\n\t" \
  "v_max_f32 v4, v4, v6\n\t" \
  "v_mul_f32 v6, 0xbe8293ee, v4\n\t" \
  "v_mul_f32 v4, 0xbe8293ee, v4\n\t" \
  "s_nop 1\n\t" \
  "v_permlane32_swap_b32 v4, v6\n\t" \
  "v_mov_b32 v5, v4\n\t" \
  "v_mov_b32 v7, v6\n\t" \
  "s_nop 0\n\t" \
  "v_permlane16_swap_b32 v4, v5\n\t" \
  "v_permlane16_swap_b32 v6, v7\n\t" \
  "v_fmamk_f32 v128, v128, 0x3e8293ee, v4\n\t" \
  "v_fmamk_f32 v130, v130, 0x3e8293ee, v4\n\t" \
  "v_fmamk_f32 v132, v132, 0x3e8293ee, v4\n\t" \
  "v_fmamk_f32 v134, v134, 0x3e8293ee, v4\n\t" \
  "v_fmamk_f32 v136, v136, 0x3e8293ee, v5\n\t" \
  "v_fmamk_f32 v138, v138, 0x3e8293ee, v5\n\t" \
  "v_fmamk_f32 v140, v140, 0x3e8293ee, v5\n\t" \
  "v_fmamk_f32 v142, v142, 0x3e8293ee, v5\n\t" \
  "v_fmamk_f32 v144, v144, 0x3e8293ee, v6\n\t" \
  "v_fmamk_f32 v146, v146, 0x3e8293ee, v6\n\t" \
  "v_fmamk_f32 v148, v148, 0x3e8293ee, v6\n\t" \
  "v_fmamk_f32 v150, v150, 0x3e8293ee, v6\n\t" \
  "v_fmamk_f32 v152, v152, 0x3e8293ee, v7\n\t" \
  "v_fmamk_f32 v154, v154, 0x3e8293ee, v7\n\t" \
  "v_fmamk_f32 v156, v156, 0x3e8293ee, v7\n\t" \
  "v_fmamk_f32 v158, v158, 0x3e8293ee, v7\n\t" \
  "v_exp_f32 v128, v128\n\t" \
  "v_exp_f32 v130, v130\n\t" \
  "v_exp_f32 v132, v132\n\t" \
  "v_exp_f32 v134, v134\n\t" \
  "v_exp_f32 v136, v136\n\t" \
  "v_exp_f32 v138, v138\n\t" \
  "v_exp_f32 v140, v140\n\t" \
  "v_exp_f32 v142, v142\n\t" \
  "v_exp_f32 v144, v144\n\t" \
  "v_exp_f32 v146, v146\n\t" \
  "v_exp_f32 v148, v148\n\t" \
  "v_exp_f32 v150, v150\n\t" \
  "v_exp_f32 v152, v152\n\t" \
  "v_exp_f32 v154, v154\n\t" \
  "v_exp_f32 v156, v156\n\t" \
  "v_exp_f32 v158, v158\n\t" \
  "v_add_f32 v4, v128, v130\n\t" \
  "v_add_f32 v8, v132, v134\n\t" \
  "v_add_f32 v5, v136, v138\n\t" \
  "v_add_f32 v9, v140, v142\n\t" \
  "v_add_f32 v6, v144, v146\n\t" \
  "v_add_f32 v10, v148, v150\n\t" \
  "v_add_f32 v7, v152, v154\n\t" \
  "v_add_f32 v11, v156, v158\n\t" \
  "v_add_f32 v4, v4, v8\n\t" \
  "v_add_f32 v5, v5, v9\n\t" \
  "v_add_f32 v6, v6, v10\n\t" \
  "v_add_f32 v7, v7, v11\n\t" \
  "v_permlane16_swap_b32 v4, v5\n\t" \
  "s_nop 0\n\t" \
  "v_permlane16_swap_b32 v6, v7\n\t" \
  "v_add_f32 v4, v4, v5\n\t" \
  "v_add_f32 v6, v6, v7\n\t" \
  "s_nop 1\n\t" \
  "v_permlane32_swap_b32 v4, v6\n\t" \
  "v_add_f32 v4, v4, v6\n\t" \
  "v_rcp_f32 v4, v4\n\t" \
  "s_nop 0\n\t" \
  "v_mov_b32 v6, v4\n\t" \
  "s_nop 1\n\t" \
  "v_permlane32_swap_b32 v4, v6\n\t" \
  "v_mov_b32 v5, v4\n\t" \
  "v_mov_b32 v7, v6\n\t" \
  "s_nop 0\n\t" \
  "v_permlane16_swap_b32 v4, v5\n\t" \
  "v_permlane16_swap_b32 v6, v7\n\t" \
  "v_mul_f32 v128, v128, v4\n\t" \
  "v_mul_f32 v130, v130, v4\n\t" \
  "v_mul_f32 v132, v132, v4\n\t" \
  "v_mul_f32 v134, v134, v4\n\t" \
  "v_cvt_pk_bf16_f32 v128, v128, v130\n\t" \
  "v_cvt_pk_bf16_f32 v132, v132, v134\n\t" \
  "v_perm_b32 v68, 0, v128, v60\n\t" \
  "v_perm_b32 v69, 0, v128, v61\n\t" \
  "v_perm_b32 v70, 0, v132, v60\n\t" \
  "v_perm_b32 v71, 0, v132, v61\n\t" \
  "v_mul_f32 v136, v136, v5\n\t" \
  "v_mul_f32 v138, v138, v5\n\t" \
  "v_mfma_f32_16x16x32_bf16 v[128:131], v[20:23], v[68:71], 0\n\t" \
  "v_mul_f32 v140, v140, v5\n\t" \
  "v_mul_f32 v142, v142, v5\n\t" \
  "v_cvt_pk_bf16_f32 v136, v136, v138\n\t" \
  "v_mfma_f32_16x16x32_bf16 v[132:135], v[30:33], v[68:71], 0\n\t" \
  "v_cvt_pk_bf16_f32 v140, v140, v142\n\t" \
  "v_perm_b32 v76, 0, v136, v60\n\t" \
  "v_perm_b32 v77, 0, v136, v61\n\t" \
  "v_perm_b32 v78, 0, v140, v60\n\t" \
  "v_perm_b32 v79, 0, v140, v61\n\t" \
  "v_mul_f32 v144, v144, v6\n\t" \
  "v_mul_f32 v146, v146, v6\n\t" \
  "v_mfma_f32_16x16x32_bf16 v[136:139], v[22:25], v[76:79], 0\n\t" \
  "v_mul_f32 v148, v148, v6\n\t" \
  "v_mul_f32 v150, v150, v6\n\t" \
  "v_cvt_pk_bf16_f32 v144, v144, v146\n\t" \
  "v_mfma_f32_16x16x32_bf16 v[140:143], v[32:35], v[76:79], 0\n\t" \
  "v_cvt_pk_bf16_f32 v148, v148, v150\n\t" \
  "v_perm_b32 v84, 0, v144, v60\n\t" \
  "v_perm_b32 v85, 0, v144, v61\n\t" \
  "v_perm_b32 v86, 0, v148, v60\n\t" \
  "v_perm_b32 v87, 0, v148, v61\n\t" \
  "v_cvt_pk_bf16_f32 v100, v128, v129\n\t" \
  "v_cvt_pk_bf16_f32 v101, v130, v131\n\t" \
  "v_mfma_f32_16x16x32_bf16 v[144:147], v[24:27], v[84:87], 0\n\t" \
  "v_cvt_pk_bf16_f32 v102, v132, v133\n\t" \
  "v_cvt_pk_bf16_f32 v103, v134, v135\n\t" \
  "global_store_dwordx4 v62, v[100:103], %[ot] offset:0 sc1\n\t" \
  "v_mfma_f32_16x16x32_bf16 v[148:151], v[34:37], v[84:87], 0\n\t" \
  "v_mul_f32 v152, v152, v7\n\t" \
  "v_mul_f32 v154, v154, v7\n\t" \
  "v_mul_f32 v156, v156, v7\n\t" \
  "v_mul_f32 v158, v158, v7\n\t" \
  "v_cvt_pk_bf16_f32 v152, v152, v154\n\t" \
  "v_cvt_pk_bf16_f32 v156, v156, v158\n\t" \
  "v_perm_b32 v92, 0, v152, v60\n\t" \
  "v_perm_b32 v93, 0, v152, v61\n\t" \
  "v_perm_b32 v94, 0, v156, v60\n\t" \
  "v_perm_b32 v95, 0, v156, v61\n\t" \
  "v_cvt_pk_bf16_f32 v108, v136, v137\n\t" \
  "v_cvt_pk_bf16_f32 v109, v138, v139\n\t" \
  "v_mfma_f32_16x16x32_bf16 v[152:155], v[26:29], v[92:95], 0\n\t" \
  "v_cvt_pk_bf16_f32 v110, v140, v141\n\t" \
  "v_cvt_pk_bf16_f32 v111, v142, v143\n\t" \
  "global_store_dwordx4 v62, v[108:111], %[ot] offset:1024 sc1\n\t" \
  "v_mfma_f32_16x16x32_bf16 v[156:159], v[36:39], v[92:95], 0\n\t" \
  "v_cvt_pk_bf16_f32 v116, v144, v145\n\t" \
  "v_cvt_pk_bf16_f32 v117, v146, v147\n\t" \
  "v_cvt_pk_bf16_f32 v118, v148, v149\n\t" \
  "v_cvt_pk_bf16_f32 v119, v150, v151\n\t" \
  "global_store_dwordx4 v62, v[116:119], %[ou] offset:0 sc1\n\t" \
  "v_cvt_pk_bf16_f32 v124, v152, v153\n\t" \
  "v_cvt_pk_bf16_f32 v125, v154, v155\n\t" \
  "s_nop 0\n\t" \
  "v_cvt_pk_bf16_f32 v126, v156, v157\n\t" \
  "v_cvt_pk_bf16_f32 v127, v158, v159\n\t" \
  "global_store_dwordx4 v62, v[124:127], %[ou] offset:1024 sc1\n\t" \
  "s_branch AE_%=\n\t" \
  "AR_%=:\n\t" \
  "s_addk_i32 %[it], 1\n\t" \
  "s_cmp_gt_u32 %[it], 0x400000\n\t" \
  "s_cbranch_scc1 AY_%=\n\t" \
  "ds_read_b32 v0, v61\n\t" \
  "ds_read_b64 v[20:21], v60\n\t" \
  "ds_read_b64 v[30:31], v60 offset:8\n\t" \
  "s_mov_b64 exec, %[rm]\n\t" \
  "ds_read_b128 v[160:163], v63\n\t" \
  "s_mov_b64 exec, -1\n\t" \
  "s_branch AW_%=\n\t" \
  "AZ_%=:\n\t" \
  "v_mov_b32 v0, 0xff61b1e6\n\t" \
  "v_mov_b32 v1, 0xcfa8965f\n\t" \
  "s_nop 3\n\t" \
  "v_cndmask_b32_e64 v56, v0, v1, s[68:69]\n\t" \
  "v_cndmask_b32_e64 v57, v0, v1, s[70:71]\n\t" \
  "v_cndmask_b32_e64 v58, v0, v1, s[72:73]\n\t" \
  "v_cndmask_b32_e64 v59, v0, v1, s[74:75]\n\t" \
  "s_nop 1\n\t" \
  "s_branch AY_%=\n\t" \
  "AE_%=:\n\t" \
  "s_nop 1\n\t"
#define N2_CLOB "v0", "v1", "v2", "v3", "v4", "v5", "v6", "v7", "v8", "v9", "v10", "v11", "v12", "v13", "v14", "v15", "v16", "v17", "v18", "v19", "v20", "v21", "v22", "v23", "v24", "v25", "v26", "v27", "v28", "v29", "v30", "v31", "v32", "v33", "v34", "v35", "v36", "v37", "v38", "v39", "v40", "v41", "v42", "v43", "v44", "v45", "v46", "v47", "v48", "v49", "v50", "v51", "v52", "v53", "v54", "v55", "v60", "v61", "v64", "v65", "v66", "v67", "v68", "v69", "v70", "v71", "v72", "v73", "v74", "v75", "v76", "v77", "v78", "v79", "v80", "v81", "v82", "v83", "v84", "v85", "v86", "v87", "v88", "v89", "v90", "v91", "v92", "v93", "v94", "v95", "v96", "v97", "v98", "v99", "v100", "v101", "v102", "v103", "v104", "v105", "v106", "v107", "v108", "v109", "v110", "v111", "v112", "v113", "v114", "v115", "v116", "v117", "v118", "v119", "v120", "v121", "v122", "v123", "v124", "v125", "v126", "v127", "v128", "v129", "v130", "v131", "v132", "v133", "v134", "v135", "v136", "v137", "v138", "v139", "v140", "v141", "v142", "v143", "v144", "v145", "v146", "v147", "v148", "v149", "v150", "v151", "v152", "v153", "v154", "v155", "v156", "v157", "v158", "v159", "v160", "v161", "v162", "v163", "v164", "v165", "v166", "v167", "s68", "s69", "s70", "s71", "s72", "s73", "s74", "s75", "s76", "s77", "s78", "s79", "s80", "s81", "s82", "s83", "s84", "s85"

#define CV_ASM \
  "ds_read_b64 v[0:1], v28 offset:0\n\t" \
  "ds_read_b64 v[2:3], v29 offset:0\n\t" \
  "ds_read_b64 v[4:5], v30 offset:1024\n\t" \
  "ds_read_b64 v[6:7], v31 offset:1024\n\t" \
  "ds_read_b64 v[8:9], v24 offset:2048\n\t" \
  "ds_read_b64 v[10:11], v25 offset:2048\n\t" \
  "ds_read_b64 v[12:13], v26 offset:3072\n\t" \
  "ds_read_b64 v[14:15], v27 offset:3072\n\t" \
  "ds_read_b64 v[16:17], v28 offset:4096\n\t" \
  "ds_read_b64 v[18:19], v29 offset:4096\n\t" \
  "ds_read_b64 v[20:21], v30 offset:5120\n\t" \
  "ds_read_b64 v[22:23], v31 offset:5120\n\t" \
  "s_waitcnt lgkmcnt(10)\n\t" \
  "v_mfma_f32_16x16x32_bf16 v[128:131], v[68:71], v[0:3], 0\n\t" \
  "v_mfma_f32_16x16x32_bf16 v[132:135], v[76:79], v[0:3], 0\n\t" \
  "v_mfma_f32_16x16x32_bf16 v[136:139], v[84:87], v[0:3], 0\n\t" \
  "v_mfma_f32_16x16x32_bf16 v[140:143], v[92:95], v[0:3], 0\n\t" \
  "v_mfma_f32_16x16x32_bf16 v[144:147], v[100:103], v[0:3], 0\n\t" \
  "v_mfma_f32_16x16x32_bf16 v[148:151], v[108:111], v[0:3], 0\n\t" \
  "v_mfma_f32_16x16x32_bf16 v[152:155], v[116:119], v[0:3], 0\n\t" \
  "v_mfma_f32_16x16x32_bf16 v[156:159], v[124:127], v[0:3], 0\n\t" \
  "ds_read_b64 v[0:1], v24 offset:6144\n\t" \
  "s_waitcnt lgkmcnt(9)\n\t" \
  "v_mfma_f32_16x16x32_bf16 v[128:131], v[64:67], v[4:7], v[128:131]\n\t" \
  "ds_read_b64 v[2:3], v25 offset:6144\n\t" \
  "v_mfma_f32_16x16x32_bf16 v[132:135], v[72:75], v[4:7], v[132:135]\n\t" \
  "v_mfma_f32_16x16x32_bf16 v[136:139], v[80:83], v[4:7], v[136:139]\n\t" \
  "v_mfma_f32_16x16x32_bf16 v[140:143], v[88:91], v[4:7], v[140:143]\n\t" \
  "v_mfma_f32_16x16x32_bf16 v[144:147], v[96:99], v[4:7], v[144:147]\n\t" \
  "v_mfma_f32_16x16x32_bf16 v[148:151], v[104:107], v[4:7], v[148:151]\n\t" \
  "v_mfma_f32_16x16x32_bf16 v[152:155], v[112:115], v[4:7], v[152:155]\n\t" \
  "v_mfma_f32_16x16x32_bf16 v[156:159], v[120:123], v[4:7], v[156:159]\n\t" \
  "s_cmp_eq_u32 %[rem], 0\n\t" \
  "s_cbranch_scc1 CB16_%=\n\t" \
  "s_barrier\n\t" \
  "s_sub_u32 %[rem], %[rem], 1\n\t" \
  "CB16_%=:\n\t" \
  "ds_read_b64 v[4:5], v26 offset:7168\n\t" \
  "ds_bpermute_b32 v34, v32, v131\n\t" \
  "v_readlane_b32 %[p0], v131, 63\n\t" \
  "s_waitcnt lgkmcnt(10)\n\t" \
  "v_mfma_f32_16x16x32_bf16 v[132:135], v[68:71], v[8:11], v[132:135]\n\t" \
  "ds_read_b64 v[6:7], v27 offset:7168\n\t" \
  "v_mov_b32 v131, v130\n\t" \
  "v_mfma_f32_16x16x32_bf16 v[136:139], v[76:79], v[8:11], v[136:139]\n\t" \
  "v_mov_b32 v130, v129\n\t" \
  "v_mfma_f32_16x16x32_bf16 v[140:143], v[84:87], v[8:11], v[140:143]\n\t" \
  "v_mov_b32 v129, v128\n\t" \
  "v_mfma_f32_16x16x32_bf16 v[144:147], v[92:95], v[8:11], v[144:147]\n\t" \
  "v_mfma_f32_16x16x32_bf16 v[148:151], v[100:103], v[8:11], v[148:151]\n\t" \
  "v_mfma_f32_16x16x32_bf16 v[152:155], v[108:111], v[8:11], v[152:155]\n\t" \
  "v_mfma_f32_16x16x32_bf16 v[156:159], v[116:119], v[8:11], v[156:159]\n\t" \
  "ds_read_b64 v[8:9], v28 offset:8192\n\t" \
  "s_waitcnt lgkmcnt(10)\n\t" \
  "v_mfma_f32_16x16x32_bf16 v[132:135], v[64:67], v[12:15], v[132:135]\n\t" \
  "ds_read_b64 v[10:11], v29 offset:8192\n\t" \
  "v_mfma_f32_16x16x32_bf16 v[136:139], v[72:75], v[12:15], v[136:139]\n\t" \
  "v_mfma_f32_16x16x32_bf16 v[140:143], v[80:83], v[12:15], v[140:143]\n\t" \
  "v_mfma_f32_16x16x32_bf16 v[144:147], v[88:91], v[12:15], v[144:147]\n\t" \
  "v_mfma_f32_16x16x32_bf16 v[148:151], v[96:99], v[12:15], v[148:151]\n\t" \
  "s_waitcnt lgkmcnt(3)\n\t" \
  "v_mov_b32 v128, v34\n\t" \
  "v_writelane_b32 v128, 0, 0\n\t" \
  "v_mfma_f32_16x16x32_bf16 v[152:155], v[104:107], v[12:15], v[152:155]\n\t" \
  "v_mfma_f32_16x16x32_bf16 v[156:159], v[112:115], v[12:15], v[156:159]\n\t" \
  "ds_read_b64 v[12:13], v30 offset:9216\n\t" \
  "s_waitcnt lgkmcnt(10)\n\t" \
  "v_mfma_f32_16x16x32_bf16 v[136:139], v[68:71], v[16:19], v[136:139]\n\t" \
  "ds_read_b64 v[14:15], v31 offset:9216\n\t" \
  "ds_bpermute_b32 v35, v32, v135\n\t" \
  "v_readlane_b32 %[p1], v135, 63\n\t" \
  "v_mfma_f32_16x16x32_bf16 v[140:143], v[76:79], v[16:19], v[140:143]\n\t" \
  "s_cmp_eq_u32 %[rem], 0\n\t" \
  "s_cbranch_scc1 CB32_%=\n\t" \
  "s_barrier\n\t" \
  "s_sub_u32 %[rem], %[rem], 1\n\t" \
  "CB32_%=:\n\t" \
  "v_mov_b32 v135, v134\n\t" \
  "v_mfma_f32_16x16x32_bf16 v[144:147], v[84:87], v[16:19], v[144:147]\n\t" \
  "v_mov_b32 v134, v133\n\t" \
  "v_mfma_f32_16x16x32_bf16 v[148:151], v[92:95], v[16:19], v[148:151]\n\t" \
  "v_mov_b32 v133, v132\n\t" \
  "v_mfma_f32_16x16x32_bf16 v[152:155], v[100:103], v[16:19], v[152:155]\n\t" \
  "v_mfma_f32_16x16x32_bf16 v[156:159], v[108:111], v[16:19], v[156:159]\n\t" \
  "ds_read_b64 v[16:17], v24 offset:10240\n\t" \
  "s_waitcnt lgkmcnt(11)\n\t" \
  "v_mfma_f32_16x16x32_bf16 v[136:139], v[64:67], v[20:23], v[136:139]\n\t" \
  "ds_read_b64 v[18:19], v25 offset:10240\n\t" \
  "v_mfma_f32_16x16x32_bf16 v[140:143], v[72:75], v[20:23], v[140:143]\n\t" \
  "v_mfma_f32_16x16x32_bf16 v[144:147], v[80:83], v[20:23], v[144:147]\n\t" \
  "v_mfma_f32_16x16x32_bf16 v[148:151], v[88:91], v[20:23], v[148:151]\n\t" \
  "v_mfma_f32_16x16x32_bf16 v[152:155], v[96:99], v[20:23], v[152:155]\n\t" \
  "v_mfma_f32_16x16x32_bf16 v[156:159], v[104:107], v[20:23], v[156:159]\n\t" \
  "ds_read_b64 v[20:21], v26 offset:11264\n\t" \
  "s_waitcnt lgkmcnt(11)\n\t" \
  "v_mfma_f32_16x16x32_bf16 v[140:143], v[68:71], v[0:3], v[140:143]\n\t" \
  "ds_read_b64 v[22:23], v27 offset:11264\n\t" \
  "s_waitcnt lgkmcnt(4)\n\t" \
  "v_mov_b32 v132, v35\n\t" \
  "v_writelane_b32 v132, %[p0], 0\n\t" \
  "v_mfma_f32_16x16x32_bf16 v[144:147], v[76:79], v[0:3], v[144:147]\n\t" \
  "ds_bpermute_b32 v36, v32, v139\n\t" \
  "v_readlane_b32 %[p2], v139, 63\n\t" \
  "v_mfma_f32_16x16x32_bf16 v[148:151], v[84:87], v[0:3], v[148:151]\n\t" \
  "v_mov_b32 v139, v138\n\t" \
  "v_mfma_f32_16x16x32_bf16 v[152:155], v[92:95], v[0:3], v[152:155]\n\t" \
  "v_mov_b32 v138, v137\n\t" \
  "v_mfma_f32_16x16x32_bf16 v[156:159], v[100:103], v[0:3], v[156:159]\n\t" \
  "ds_read_b64 v[0:1], v28 offset:12288\n\t" \
  "v_mov_b32 v137, v136\n\t" \
  "s_waitcnt lgkmcnt(11)\n\t" \
  "v_mfma_f32_16x16x32_bf16 v[140:143], v[64:67], v[4:7], v[140:143]\n\t" \
  "s_cmp_eq_u32 %[rem], 0\n\t" \
  "s_cbranch_scc1 CB48_%=\n\t" \
  "s_barrier\n\t" \
  "s_sub_u32 %[rem], %[rem], 1\n\t" \
  "CB48_%=:\n\t" \
  "ds_read_b64 v[2:3], v29 offset:12288\n\t" \
  "v_mfma_f32_16x16x32_bf16 v[144:147], v[72:75], v[4:7], v[144:147]\n\t" \
  "v_mfma_f32_16x16x32_bf16 v[148:151], v[80:83], v[4:7], v[148:151]\n\t" \
  "v_mfma_f32_16x16x32_bf16 v[152:155], v[88:91], v[4:7], v[152:155]\n\t" \
  "v_mfma_f32_16x16x32_bf16 v[156:159], v[96:99], v[4:7], v[156:159]\n\t" \
  "ds_read_b64 v[4:5], v30 offset:13312\n\t" \
  "s_waitcnt lgkmcnt(11)\n\t" \
  "v_mfma_f32_16x16x32_bf16 v[144:147], v[68:71], v[8:11], v[144:147]\n\t" \
  "ds_read_b64 v[6:7], v31 offset:13312\n\t" \
  "v_mfma_f32_16x16x32_bf16 v[148:151], v[76:79], v[8:11], v[148:151]\n\t" \
  "v_mfma_f32_16x16x32_bf16 v[152:155], v[84:87], v[8:11], v[152:155]\n\t" \
  "ds_bpermute_b32 v37, v32, v143\n\t" \
  "v_readlane_b32 %[p3], v143, 63\n\t" \
  "v_mfma_f32_16x16x32_bf16 v[156:159], v[92:95], v[8:11], v[156:159]\n\t" \
  "ds_read_b64 v[8:9], v24 offset:14336\n\t" \
  "s_waitcnt lgkmcnt(6)\n\t" \
  "v_mov_b32 v136, v36\n\t" \
  "v_writelane_b32 v136, %[p1], 0\n\t" \
  "v_mov_b32 v143, v142\n\t" \
  "s_waitcnt lgkmcnt(12)\n\t" \
  "v_mfma_f32_16x16x32_bf16 v[144:147], v[64:67], v[12:15], v[144:147]\n\t" \
  "ds_read_b64 v[10:11], v25 offset:14336\n\t" \
  "v_mov_b32 v142, v141\n\t" \
  "v_mfma_f32_16x16x32_bf16 v[148:151], v[72:75], v[12:15], v[148:151]\n\t" \
  "v_mov_b32 v141, v140\n\t" \
  "v_mfma_f32_16x16x32_bf16 v[152:155], v[80:83], v[12:15], v[152:155]\n\t" \
  "v_mfma_f32_16x16x32_bf16 v[156:159], v[88:91], v[12:15], v[156:159]\n\t" \
  "ds_read_b64 v[12:13], v26 offset:15360\n\t" \
  "s_waitcnt lgkmcnt(11)\n\t" \
  "v_mfma_f32_16x16x32_bf16 v[148:151], v[68:71], v[16:19], v[148:151]\n\t" \
  "ds_read_b64 v[14:15], v27 offset:15360\n\t" \
  "v_mfma_f32_16x16x32_bf16 v[152:155], v[76:79], v[16:19], v[152:155]\n\t" \
  "v_mfma_f32_16x16x32_bf16 v[156:159], v[84:87], v[16:19], v[156:159]\n\t" \
  "ds_read_b64 v[16:17], v26 offset:0\n\t" \
  "s_waitcnt lgkmcnt(11)\n\t" \
  "v_mfma_f32_16x16x32_bf16 v[148:151], v[64:67], v[20:23], v[148:151]\n\t" \
  "s_cmp_eq_u32 %[rem], 0\n\t" \
  "s_cbranch_scc1 CB64_%=\n\t" \
  "s_barrier\n\t" \
  "s_sub_u32 %[rem], %[rem], 1\n\t" \
  "CB64_%=:\n\t" \
  "ds_read_b64 v[18:19], v27 offset:0\n\t" \
  "ds_bpermute_b32 v38, v32, v147\n\t" \
  "v_readlane_b32 %[p4], v147, 63\n\t" \
  "v_mfma_f32_16x16x32_bf16 v[152:155], v[72:75], v[20:23], v[152:155]\n\t" \
  "v_mov_b32 v147, v146\n\t" \
  "v_mfma_f32_16x16x32_bf16 v[156:159], v[80:83], v[20:23], v[156:159]\n\t" \
  "ds_read_b64 v[20:21], v28 offset:1024\n\t" \
  "v_mov_b32 v146, v145\n\t" \
  "s_waitcnt lgkmcnt(11)\n\t" \
  "v_mfma_f32_16x16x32_bf16 v[152:155], v[68:71], v[0:3], v[152:155]\n\t" \
  "ds_read_b64 v[22:23], v29 offset:1024\n\t" \
  "s_waitcnt lgkmcnt(9)\n\t" \
  "v_mov_b32 v140, v37\n\t" \
  "v_writelane_b32 v140, %[p2], 0\n\t" \
  "v_mov_b32 v145, v144\n\t" \
  "v_mfma_f32_16x16x32_bf16 v[156:159], v[76:79], v[0:3], v[156:159]\n\t" \
  "ds_read_b64 v[0:1], v30 offset:2048\n\t" \
  "ds_bpermute_b32 v39, v32, v151\n\t" \
  "v_readlane_b32 %[p5], v151, 63\n\t" \
  "s_waitcnt lgkmcnt(12)\n\t" \
  "v_mfma_f32_16x16x32_bf16 v[152:155], v[64:67], v[4:7], v[152:155]\n\t" \
  "ds_read_b64 v[2:3], v31 offset:2048\n\t" \
  "v_mov_b32 v151, v150\n\t" \
  "v_mfma_f32_16x16x32_bf16 v[156:159], v[72:75], v[4:7], v[156:159]\n\t" \
  "ds_read_b64 v[4:5], v24 offset:3072\n\t" \
  "v_mov_b32 v150, v149\n\t" \
  "s_waitcnt lgkmcnt(11)\n\t" \
  "v_mfma_f32_16x16x32_bf16 v[156:159], v[68:71], v[8:11], v[156:159]\n\t" \
  "ds_read_b64 v[6:7], v25 offset:3072\n\t" \
  "ds_read_b64 v[8:9], v26 offset:4096\n\t" \
  "v_mov_b32 v149, v148\n\t" \
  "ds_bpermute_b32 v40, v32, v155\n\t" \
  "v_readlane_b32 %[p6], v155, 63\n\t" \
  "s_waitcnt lgkmcnt(12)\n\t" \
  "v_mfma_f32_16x16x32_bf16 v[156:159], v[64:67], v[12:15], v[156:159]\n\t" \
  "ds_read_b64 v[10:11], v27 offset:4096\n\t" \
  "ds_read_b64 v[12:13], v28 offset:5120\n\t" \
  "v_mov_b32 v155, v154\n\t" \
  "s_waitcnt lgkmcnt(12)\n\t" \
  "v_mfma_f32_16x16x32_bf16 v[128:131], v[68:71], v[16:19], v[128:131]\n\t" \
  "ds_read_b64 v[14:15], v29 offset:5120\n\t" \
  "v_mov_b32 v154, v153\n\t" \
  "v_mfma_f32_16x16x32_bf16 v[132:135], v[76:79], v[16:19], v[132:135]\n\t" \
  "s_waitcnt lgkmcnt(12)\n\t" \
  "v_mov_b32 v144, v38\n\t" \
  "v_writelane_b32 v144, %[p3], 0\n\t" \
  "v_mov_b32 v153, v152\n\t" \
  "v_mfma_f32_16x16x32_bf16 v[136:139], v[84:87], v[16:19], v[136:139]\n\t" \
  "s_waitcnt lgkmcnt(8)\n\t" \
  "v_mov_b32 v148, v39\n\t" \
  "v_writelane_b32 v148, %[p4], 0\n\t" \
  "v_mfma_f32_16x16x32_bf16 v[140:143], v[92:95], v[16:19], v[140:143]\n\t" \
  "s_waitcnt lgkmcnt(3)\n\t" \
  "v_mov_b32 v152, v40\n\t" \
  "v_writelane_b32 v152, %[p5], 0\n\t" \
  "v_mfma_f32_16x16x32_bf16 v[144:147], v[100:103], v[16:19], v[144:147]\n\t" \
  "ds_bpermute_b32 v41, v32, v159\n\t" \
  "v_mfma_f32_16x16x32_bf16 v[148:151], v[108:111], v[16:19], v[148:151]\n\t" \
  "v_mov_b32 v159, v158\n\t" \
  "v_mfma_f32_16x16x32_bf16 v[152:155], v[116:119], v[16:19], v[152:155]\n\t" \
  "v_mov_b32 v158, v157\n\t" \
  "s_waitcnt lgkmcnt(11)\n\t" \
  "v_mfma_f32_16x16x32_bf16 v[128:131], v[64:67], v[20:23], v[128:131]\n\t" \
  "s_cmp_eq_u32 %[rem], 0\n\t" \
  "s_cbranch_scc1 CB80_%=\n\t" \
  "s_barrier\n\t" \
  "s_sub_u32 %[rem], %[rem], 1\n\t" \
  "CB80_%=:\n\t" \
  "v_mov_b32 v157, v156\n\t" \
  "v_mfma_f32_16x16x32_bf16 v[132:135], v[72:75], v[20:23], v[132:135]\n\t" \
  "v_mfma_f32_16x16x32_bf16 v[136:139], v[80:83], v[20:23], v[136:139]\n\t" \
  "v_mfma_f32_16x16x32_bf16 v[140:143], v[88:91], v[20:23], v[140:143]\n\t" \
  "v_mfma_f32_16x16x32_bf16 v[144:147], v[96:99], v[20:23], v[144:147]\n\t" \
  "s_waitcnt lgkmcnt(0)\n\t" \
  "v_mov_b32 v156, v41\n\t" \
  "v_writelane_b32 v156, %[p6], 0\n\t" \
  "v_mfma_f32_16x16x32_bf16 v[148:151], v[104:107], v[20:23], v[148:151]\n\t" \
  "v_mfma_f32_16x16x32_bf16 v[152:155], v[112:115], v[20:23], v[152:155]\n\t" \
  "v_mfma_f32_16x16x32_bf16 v[156:159], v[124:127], v[16:19], v[156:159]\n\t" \
  "ds_read_b64 v[16:17], v30 offset:6144\n\t" \
  "ds_bpermute_b32 v34, v32, v131\n\t" \
  "v_readlane_b32 %[p0], v131, 63\n\t" \
  "v_mfma_f32_16x16x32_bf16 v[156:159], v[120:123], v[20:23], v[156:159]\n\t" \
  "ds_read_b64 v[18:19], v31 offset:6144\n\t" \
  "ds_read_b64 v[20:21], v24 offset:7168\n\t" \
  "v_mov_b32 v131, v130\n\t" \
  "s_waitcnt lgkmcnt(12)\n\t" \
  "v_mfma_f32_16x16x32_bf16 v[132:135], v[68:71], v[0:3], v[132:135]\n\t" \
  "ds_read_b64 v[22:23], v25 offset:7168\n\t" \
  "v_mov_b32 v130, v129\n\t" \
  "v_mfma_f32_16x16x32_bf16 v[136:139], v[76:79], v[0:3], v[136:139]\n\t" \
  "v_mov_b32 v129, v128\n\t" \
  "v_mfma_f32_16x16x32_bf16 v[140:143], v[84:87], v[0:3], v[140:143]\n\t" \
  "v_mfma_f32_16x16x32_bf16 v[144:147], v[92:95], v[0:3], v[144:147]\n\t" \
  "v_mfma_f32_16x16x32_bf16 v[148:151], v[100:103], v[0:3], v[148:151]\n\t" \
  "v_mfma_f32_16x16x32_bf16 v[152:155], v[108:111], v[0:3], v[152:155]\n\t" \
  "v_mfma_f32_16x16x32_bf16 v[156:159], v[116:119], v[0:3], v[156:159]\n\t" \
  "ds_read_b64 v[0:1], v26 offset:8192\n\t" \
  "s_waitcnt lgkmcnt(12)\n\t" \
  "v_mfma_f32_16x16x32_bf16 v[132:135], v[64:67], v[4:7], v[132:135]\n\t" \
  "s_cmp_eq_u32 %[rem], 0\n\t" \
  "s_cbranch_scc1 CB96_%=\n\t" \
  "s_barrier\n\t" \
  "s_sub_u32 %[rem], %[rem], 1\n\t" \
  "CB96_%=:\n\t" \
  "ds_read_b64 v[2:3], v27 offset:8192\n\t" \
  "v_mfma_f32_16x16x32_bf16 v[136:139], v[72:75], v[4:7], v[136:139]\n\t" \
  "v_mfma_f32_16x16x32_bf16 v[140:143], v[80:83], v[4:7], v[140:143]\n\t" \
  "v_mfma_f32_16x16x32_bf16 v[144:147], v[88:91], v[4:7], v[144:147]\n\t" \
  "s_waitcnt lgkmcnt(5)\n\t" \
  "v_mov_b32 v128, v34\n\t" \
  "v_writelane_b32 v128, 0, 0\n\t" \
  "v_mfma_f32_16x16x32_bf16 v[148:151], v[96:99], v[4:7], v[148:151]\n\t" \
  "v_mfma_f32_16x16x32_bf16 v[152:155], v[104:107], v[4:7], v[152:155]\n\t" \
  "v_mfma_f32_16x16x32_bf16 v[156:159], v[112:115], v[4:7], v[156:159]\n\t" \
  "ds_read_b64 v[4:5], v28 offset:9216\n\t" \
  "s_waitcnt lgkmcnt(11)\n\t" \
  "v_mfma_f32_16x16x32_bf16 v[136:139], v[68:71], v[8:11], v[136:139]\n\t" \
  "ds_read_b64 v[6:7], v29 offset:9216\n\t" \
  "ds_bpermute_b32 v35, v32, v135\n\t" \
  "v_readlane_b32 %[p1], v135, 63\n\t" \
  "v_mfma_f32_16x16x32_bf16 v[140:143], v[76:79], v[8:11], v[140:143]\n\t" \
  "v_mov_b32 v135, v134\n\t" \
  "v_mfma_f32_16x16x32_bf16 v[144:147], v[84:87], v[8:11], v[144:147]\n\t" \
  "v_mov_b32 v134, v133\n\t" \
  "v_mfma_f32_16x16x32_bf16 v[148:151], v[92:95], v[8:11], v[148:151]\n\t" \
  "v_mov_b32 v133, v132\n\t" \
  "v_mfma_f32_16x16x32_bf16 v[152:155], v[100:103], v[8:11], v[152:155]\n\t" \
  "v_mfma_f32_16x16x32_bf16 v[156:159], v[108:111], v[8:11], v[156:159]\n\t" \
  "ds_read_b64 v[8:9], v30 offset:10240\n\t" \
  "s_waitcnt lgkmcnt(12)\n\t" \
  "v_mfma_f32_16x16x32_bf16 v[136:139], v[64:67], v[12:15], v[136:139]\n\t" \
  "ds_read_b64 v[10:11], v31 offset:10240\n\t" \
  "v_mfma_f32_16x16x32_bf16 v[140:143], v[72:75], v[12:15], v[140:143]\n\t" \
  "v_mfma_f32_16x16x32_bf16 v[144:147], v[80:83], v[12:15], v[144:147]\n\t" \
  "v_mfma_f32_16x16x32_bf16 v[148:151], v[88:91], v[12:15], v[148:151]\n\t" \
  "s_cmp_eq_u32 %[rem], 0\n\t" \
  "s_cbranch_scc1 CB112_%=\n\t" \
  "s_barrier\n\t" \
  "s_sub_u32 %[rem], %[rem], 1\n\t" \
  "CB112_%=:\n\t" \
  "v_mfma_f32_16x16x32_bf16 v[152:155], v[96:99], v[12:15], v[152:155]\n\t" \
  "v_mfma_f32_16x16x32_bf16 v[156:159], v[104:107], v[12:15], v[156:159]\n\t" \
  "ds_read_b64 v[12:13], v24 offset:11264\n\t" \
  "s_waitcnt lgkmcnt(10)\n\t" \
  "v_mfma_f32_16x16x32_bf16 v[140:143], v[68:71], v[16:19], v[140:143]\n\t" \
  "ds_read_b64 v[14:15], v25 offset:11264\n\t" \
  "s_waitcnt lgkmcnt(4)\n\t" \
  "v_mov_b32 v132, v35\n\t" \
  "v_writelane_b32 v132, %[p0], 0\n\t" \
  "v_mfma_f32_16x16x32_bf16 v[144:147], v[76:79], v[16:19], v[144:147]\n\t" \
  "ds_bpermute_b32 v36, v32, v139\n\t" \
  "v_readlane_b32 %[p2], v139, 63\n\t" \
  "v_mfma_f32_16x16x32_bf16 v[148:151], v[84:87], v[16:19], v[148:151]\n\t" \
  "v_mov_b32 v139, v138\n\t" \
  "v_mfma_f32_16x16x32_bf16 v[152:155], v[92:95], v[16:19], v[152:155]\n\t" \
  "v_mov_b32 v138, v137\n\t" \
  "v_mfma_f32_16x16x32_bf16 v[156:159], v[100:103], v[16:19], v[156:159]\n\t" \
  "ds_read_b64 v[16:17], v26 offset:12288\n\t" \
  "v_mov_b32 v137, v136\n\t" \
  "s_waitcnt lgkmcnt(11)\n\t" \
  "v_mfma_f32_16x16x32_bf16 v[140:143], v[64:67], v[20:23], v[140:143]\n\t" \
  "ds_read_b64 v[18:19], v27 offset:12288\n\t" \
  "v_mfma_f32_16x16x32_bf16 v[144:147], v[72:75], v[20:23], v[144:147]\n\t" \
  "v_mfma_f32_16x16x32_bf16 v[148:151], v[80:83], v[20:23], v[148:151]\n\t" \
  "v_mfma_f32_16x16x32_bf16 v[152:155], v[88:91], v[20:23], v[152:155]\n\t" \
  "v_mfma_f32_16x16x32_bf16 v[156:159], v[96:99], v[20:23], v[156:159]\n\t" \
  "ds_read_b64 v[20:21], v28 offset:13312\n\t" \
  "s_waitcnt lgkmcnt(11)\n\t" \
  "v_mfma_f32_16x16x32_bf16 v[144:147], v[68:71], v[0:3], v[144:147]\n\t" \
  "ds_read_b64 v[22:23], v29 offset:13312\n\t" \
  "v_mfma_f32_16x16x32_bf16 v[148:151], v[76:79], v[0:3], v[148:151]\n\t" \
  "v_mfma_f32_16x16x32_bf16 v[152:155], v[84:87], v[0:3], v[152:155]\n\t" \
  "ds_bpermute_b32 v37, v32, v143\n\t" \
  "v_readlane_b32 %[p3], v143, 63\n\t" \
  "v_mfma_f32_16x16x32_bf16 v[156:159], v[92:95], v[0:3], v[156:159]\n\t" \
  "s_cmp_eq_u32 %[rem], 0\n\t" \
  "s_cbranch_scc1 CB128_%=\n\t" \
  "s_barrier\n\t" \
  "s_sub_u32 %[rem], %[rem], 1\n\t" \
  "CB128_%=:\n\t" \
  "ds_read_b64 v[0:1], v30 offset:14336\n\t" \
  "s_waitcnt lgkmcnt(6)\n\t" \
  "v_mov_b32 v136, v36\n\t" \
  "v_writelane_b32 v136, %[p1], 0\n\t" \
  "v_mov_b32 v143, v142\n\t" \
  "s_waitcnt lgkmcnt(12)\n\t" \
  "v_mfma_f32_16x16x32_bf16 v[144:147], v[64:67], v[4:7], v[144:147]\n\t" \
  "ds_read_b64 v[2:3], v31 offset:14336\n\t" \
  "v_mov_b32 v142, v141\n\t" \
  "v_mfma_f32_16x16x32_bf16 v[148:151], v[72:75], v[4:7], v[148:151]\n\t" \
  "v_mov_b32 v141, v140\n\t" \
  "v_mfma_f32_16x16x32_bf16 v[152:155], v[80:83], v[4:7], v[152:155]\n\t" \
  "v_mfma_f32_16x16x32_bf16 v[156:159], v[88:91], v[4:7], v[156:159]\n\t" \
  "ds_read_b64 v[4:5], v24 offset:15360\n\t" \
  "s_waitcnt lgkmcnt(11)\n\t" \
  "v_mfma_f32_16x16x32_bf16 v[148:151], v[68:71], v[8:11], v[148:151]\n\t" \
  "ds_read_b64 v[6:7], v25 offset:15360\n\t" \
  "v_mfma_f32_16x16x32_bf16 v[152:155], v[76:79], v[8:11], v[152:155]\n\t" \
  "v_mfma_f32_16x16x32_bf16 v[156:159], v[84:87], v[8:11], v[156:159]\n\t" \
  "ds_read_b64 v[8:9], v24 offset:0\n\t" \
  "s_waitcnt lgkmcnt(11)\n\t" \
  "v_mfma_f32_16x16x32_bf16 v[148:151], v[64:67], v[12:15], v[148:151]\n\t" \
  "ds_read_b64 v[10:11], v25 offset:0\n\t" \
  "ds_bpermute_b32 v38, v32, v147\n\t" \
  "v_readlane_b32 %[p4], v147, 63\n\t" \
  "v_mfma_f32_16x16x32_bf16 v[152:155], v[72:75], v[12:15], v[152:155]\n\t" \
  "v_mov_b32 v147, v146\n\t" \
  "v_mfma_f32_16x16x32_bf16 v[156:159], v[80:83], v[12:15], v[156:159]\n\t" \
  "ds_read_b64 v[12:13], v26 offset:1024\n\t" \
  "v_mov_b32 v146, v145\n\t" \
  "s_waitcnt lgkmcnt(11)\n\t" \
  "v_mfma_f32_16x16x32_bf16 v[152:155], v[68:71], v[16:19], v[152:155]\n\t" \
  "ds_read_b64 v[14:15], v27 offset:1024\n\t" \
  "s_waitcnt lgkmcnt(9)\n\t" \
  "v_mov_b32 v140, v37\n\t" \
  "v_writelane_b32 v140, %[p2], 0\n\t" \
  "v_mov_b32 v145, v144\n\t" \
  "v_mfma_f32_16x16x32_bf16 v[156:159], v[76:79], v[16:19], v[156:159]\n\t" \
  "ds_read_b64 v[16:17], v28 offset:2048\n\t" \
  "ds_bpermute_b32 v39, v32, v151\n\t" \
  "v_readlane_b32 %[p5], v151, 63\n\t" \
  "s_waitcnt lgkmcnt(12)\n\t" \
  "v_mfma_f32_16x16x32_bf16 v[152:155], v[64:67], v[20:23], v[152:155]\n\t" \
  "ds_read_b64 v[18:19], v29 offset:2048\n\t" \
  "v_mov_b32 v151, v150\n\t" \
  "v_mfma_f32_16x16x32_bf16 v[156:159], v[72:75], v[20:23], v[156:159]\n\t" \
  "ds_read_b64 v[20:21], v30 offset:3072\n\t" \
  "v_mov_b32 v150, v149\n\t" \
  "s_waitcnt lgkmcnt(11)\n\t" \
  "v_mfma_f32_16x16x32_bf16 v[156:159], v[68:71], v[0:3], v[156:159]\n\t" \
  "ds_read_b64 v[22:23], v31 offset:3072\n\t" \
  "ds_read_b64 v[0:1], v24 offset:4096\n\t" \
  "v_mov_b32 v149, v148\n\t" \
  "ds_bpermute_b32 v40, v32, v155\n\t" \
  "v_readlane_b32 %[p6], v155, 63\n\t" \
  "s_waitcnt lgkmcnt(12)\n\t" \
  "v_mfma_f32_16x16x32_bf16 v[156:159], v[64:67], v[4:7], v[156:159]\n\t" \
  "s_cmp_eq_u32 %[rem], 0\n\t" \
  "s_cbranch_scc1 CB144_%=\n\t" \
  "s_barrier\n\t" \
  "s_sub_u32 %[rem], %[rem], 1\n\t" \
  "CB144_%=:\n\t" \
  "ds_read_b64 v[2:3], v25 offset:4096\n\t" \
  "ds_read_b64 v[4:5], v26 offset:5120\n\t" \
  "v_mov_b32 v155, v154\n\t" \
  "s_waitcnt lgkmcnt(12)\n\t" \
  "v_mfma_f32_16x16x32_bf16 v[128:131], v[68:71], v[8:11], v[128:131]\n\t" \
  "ds_read_b64 v[6:7], v27 offset:5120\n\t" \
  "v_mov_b32 v154, v153\n\t" \
  "v_mfma_f32_16x16x32_bf16 v[132:135], v[76:79], v[8:11], v[132:135]\n\t" \
  "s_waitcnt lgkmcnt(12)\n\t" \
  "v_mov_b32 v144, v38\n\t" \
  "v_writelane_b32 v144, %[p3], 0\n\t" \
  "v_mov_b32 v153, v152\n\t" \
  "v_mfma_f32_16x16x32_bf16 v[136:139], v[84:87], v[8:11], v[136:139]\n\t" \
  "s_waitcnt lgkmcnt(8)\n\t" \
  "v_mov_b32 v148, v39\n\t" \
  "v_writelane_b32 v148, %[p4], 0\n\t" \
  "v_mfma_f32_16x16x32_bf16 v[140:143], v[92:95], v[8:11], v[140:143]\n\t" \
  "s_waitcnt lgkmcnt(3)\n\t" \
  "v_mov_b32 v152, v40\n\t" \
  "v_writelane_b32 v152, %[p5], 0\n\t" \
  "v_mfma_f32_16x16x32_bf16 v[144:147], v[100:103], v[8:11], v[144:147]\n\t" \
  "ds_bpermute_b32 v41, v32, v159\n\t" \
  "v_mfma_f32_16x16x32_bf16 v[148:151], v[108:111], v[8:11], v[148:151]\n\t" \
  "v_mov_b32 v159, v158\n\t" \
  "v_mfma_f32_16x16x32_bf16 v[152:155], v[116:119], v[8:11], v[152:155]\n\t" \
  "v_mov_b32 v158, v157\n\t" \
  "s_waitcnt lgkmcnt(11)\n\t" \
  "v_mfma_f32_16x16x32_bf16 v[128:131], v[64:67], v[12:15], v[128:131]\n\t" \
  "v_mov_b32 v157, v156\n\t" \
  "v_mfma_f32_16x16x32_bf16 v[132:135], v[72:75], v[12:15], v[132:135]\n\t" \
  "v_mfma_f32_16x16x32_bf16 v[136:139], v[80:83], v[12:15], v[136:139]\n\t" \
  "v_mfma_f32_16x16x32_bf16 v[140:143], v[88:91], v[12:15], v[140:143]\n\t" \
  "v_mfma_f32_16x16x32_bf16 v[144:147], v[96:99], v[12:15], v[144:147]\n\t" \
  "s_waitcnt lgkmcnt(0)\n\t" \
  "v_mov_b32 v156, v41\n\t" \
  "v_writelane_b32 v156, %[p6], 0\n\t" \
  "v_mfma_f32_16x16x32_bf16 v[148:151], v[104:107], v[12:15], v[148:151]\n\t" \
  "v_mfma_f32_16x16x32_bf16 v[152:155], v[112:115], v[12:15], v[152:155]\n\t" \
  "v_mfma_f32_16x16x32_bf16 v[156:159], v[124:127], v[8:11], v[156:159]\n\t" \
  "ds_read_b64 v[8:9], v28 offset:6144\n\t" \
  "ds_bpermute_b32 v34, v32, v131\n\t" \
  "v_readlane_b32 %[p0], v131, 63\n\t" \
  "v_mfma_f32_16x16x32_bf16 v[156:159], v[120:123], v[12:15], v[156:159]\n\t" \
  "s_cmp_eq_u32 %[rem], 0\n\t" \
  "s_cbranch_scc1 CB160_%=\n\t" \
  "s_barrier\n\t" \
  "s_sub_u32 %[rem], %[rem], 1\n\t" \
  "CB160_%=:\n\t" \
  "ds_read_b64 v[10:11], v29 offset:6144\n\t" \
  "ds_read_b64 v[12:13], v30 offset:7168\n\t" \
  "v_mov_b32 v131, v130\n\t" \
  "s_waitcnt lgkmcnt(12)\n\t" \
  "v_mfma_f32_16x16x32_bf16 v[132:135], v[68:71], v[16:19], v[132:135]\n\t" \
  "ds_read_b64 v[14:15], v31 offset:7168\n\t" \
  "v_mov_b32 v130, v129\n\t" \
  "v_mfma_f32_16x16x32_bf16 v[136:139], v[76:79], v[16:19], v[136:139]\n\t" \
  "v_mov_b32 v129, v128\n\t" \
  "v_mfma_f32_16x16x32_bf16 v[140:143], v[84:87], v[16:19], v[140:143]\n\t" \
  "v_mfma_f32_16x16x32_bf16 v[144:147], v[92:95], v[16:19], v[144:147]\n\t" \
  "v_mfma_f32_16x16x32_bf16 v[148:151], v[100:103], v[16:19], v[148:151]\n\t" \
  "v_mfma_f32_16x16x32_bf16 v[152:155], v[108:111], v[16:19], v[152:155]\n\t" \
  "v_mfma_f32_16x16x32_bf16 v[156:159], v[116:119], v[16:19], v[156:159]\n\t" \
  "ds_read_b64 v[16:17], v24 offset:8192\n\t" \
  "s_waitcnt lgkmcnt(12)\n\t" \
  "v_mfma_f32_16x16x32_bf16 v[132:135], v[64:67], v[20:23], v[132:135]\n\t" \
  "ds_read_b64 v[18:19], v25 offset:8192\n\t" \
  "v_mfma_f32_16x16x32_bf16 v[136:139], v[72:75], v[20:23], v[136:139]\n\t" \
  "v_mfma_f32_16x16x32_bf16 v[140:143], v[80:83], v[20:23], v[140:143]\n\t" \
  "v_mfma_f32_16x16x32_bf16 v[144:147], v[88:91], v[20:23], v[144:147]\n\t" \
  "s_waitcnt lgkmcnt(5)\n\t" \
  "v_mov_b32 v128, v34\n\t" \
  "v_writelane_b32 v128, 0, 0\n\t" \
  "v_mfma_f32_16x16x32_bf16 v[148:151], v[96:99], v[20:23], v[148:151]\n\t" \
  "v_mfma_f32_16x16x32_bf16 v[152:155], v[104:107], v[20:23], v[152:155]\n\t" \
  "v_mfma_f32_16x16x32_bf16 v[156:159], v[112:115], v[20:23], v[156:159]\n\t" \
  "ds_read_b64 v[20:21], v26 offset:9216\n\t" \
  "s_waitcnt lgkmcnt(11)\n\t" \
  "v_mfma_f32_16x16x32_bf16 v[136:139], v[68:71], v[0:3], v[136:139]\n\t" \
  "ds_read_b64 v[22:23], v27 offset:9216\n\t" \
  "ds_bpermute_b32 v35, v32, v135\n\t" \
  "v_readlane_b32 %[p1], v135, 63\n\t" \
  "v_mfma_f32_16x16x32_bf16 v[140:143], v[76:79], v[0:3], v[140:143]\n\t" \
  "s_cmp_eq_u32 %[rem], 0\n\t" \
  "s_cbranch_scc1 CB176_%=\n\t" \
  "s_barrier\n\t" \
  "s_sub_u32 %[rem], %[rem], 1\n\t" \
  "CB176_%=:\n\t" \
  "v_mov_b32 v135, v134\n\t" \
  "v_mfma_f32_16x16x32_bf16 v[144:147], v[84:87], v[0:3], v[144:147]\n\t" \
  "v_mov_b32 v134, v133\n\t" \
  "v_mfma_f32_16x16x32_bf16 v[148:151], v[92:95], v[0:3], v[148:151]\n\t" \
  "v_mov_b32 v133, v132\n\t" \
  "v_mfma_f32_16x16x32_bf16 v[152:155], v[100:103], v[0:3], v[152:155]\n\t" \
  "v_mfma_f32_16x16x32_bf16 v[156:159], v[108:111], v[0:3], v[156:159]\n\t" \
  "ds_read_b64 v[0:1], v28 offset:10240\n\t" \
  "s_waitcnt lgkmcnt(12)\n\t" \
  "v_mfma_f32_16x16x32_bf16 v[136:139], v[64:67], v[4:7], v[136:139]\n\t" \
  "ds_read_b64 v[2:3], v29 offset:10240\n\t" \
  "v_mfma_f32_16x16x32_bf16 v[140:143], v[72:75], v[4:7], v[140:143]\n\t" \
  "v_mfma_f32_16x16x32_bf16 v[144:147], v[80:83], v[4:7], v[144:147]\n\t" \
  "v_mfma_f32_16x16x32_bf16 v[148:151], v[88:91], v[4:7], v[148:151]\n\t" \
  "v_mfma_f32_16x16x32_bf16 v[152:155], v[96:99], v[4:7], v[152:155]\n\t" \
  "v_mfma_f32_16x16x32_bf16 v[156:159], v[104:107], v[4:7], v[156:159]\n\t" \
  "ds_read_b64 v[4:5], v30 offset:11264\n\t" \
  "s_waitcnt lgkmcnt(10)\n\t" \
  "v_mfma_f32_16x16x32_bf16 v[140:143], v[68:71], v[8:11], v[140:143]\n\t" \
  "ds_read_b64 v[6:7], v31 offset:11264\n\t" \
  "s_waitcnt lgkmcnt(4)\n\t" \
  "v_mov_b32 v132, v35\n\t" \
  "v_writelane_b32 v132, %[p0], 0\n\t" \
  "v_mfma_f32_16x16x32_bf16 v[144:147], v[76:79], v[8:11], v[144:147]\n\t" \
  "ds_bpermute_b32 v36, v32, v139\n\t" \
  "v_readlane_b32 %[p2], v139, 63\n\t" \
  "v_mfma_f32_16x16x32_bf16 v[148:151], v[84:87], v[8:11], v[148:151]\n\t" \
  "v_mov_b32 v139, v138\n\t" \
  "v_mfma_f32_16x16x32_bf16 v[152:155], v[92:95], v[8:11], v[152:155]\n\t" \
  "v_mov_b32 v138, v137\n\t" \
  "v_mfma_f32_16x16x32_bf16 v[156:159], v[100:103], v[8:11], v[156:159]\n\t" \
  "ds_read_b64 v[8:9], v24 offset:12288\n\t" \
  "v_mov_b32 v137, v136\n\t" \
  "s_waitcnt lgkmcnt(11)\n\t" \
  "v_mfma_f32_16x16x32_bf16 v[140:143], v[64:67], v[12:15], v[140:143]\n\t" \
  "s_cmp_eq_u32 %[rem], 0\n\t" \
  "s_cbranch_scc1 CB192_%=\n\t" \
  "s_barrier\n\t" \
  "s_sub_u32 %[rem], %[rem], 1\n\t" \
  "CB192_%=:\n\t" \
  "ds_read_b64 v[10:11], v25 offset:12288\n\t" \
  "v_mfma_f32_16x16x32_bf16 v[144:147], v[72:75], v[12:15], v[144:147]\n\t" \
  "v_mfma_f32_16x16x32_bf16 v[148:151], v[80:83], v[12:15], v[148:151]\n\t" \
  "v_mfma_f32_16x16x32_bf16 v[152:155], v[88:91], v[12:15], v[152:155]\n\t" \
  "v_mfma_f32_16x16x32_bf16 v[156:159], v[96:99], v[12:15], v[156:159]\n\t" \
  "ds_read_b64 v[12:13], v26 offset:13312\n\t" \
  "s_waitcnt lgkmcnt(11)\n\t" \
  "v_mfma_f32_16x16x32_bf16 v[144:147], v[68:71], v[16:19], v[144:147]\n\t" \
  "ds_read_b64 v[14:15], v27 offset:13312\n\t" \
  "v_mfma_f32_16x16x32_bf16 v[148:151], v[76:79], v[16:19], v[148:151]\n\t" \
  "v_mfma_f32_16x16x32_bf16 v[152:155], v[84:87], v[16:19], v[152:155]\n\t" \
  "ds_bpermute_b32 v37, v32, v143\n\t" \
  "v_readlane_b32 %[p3], v143, 63\n\t" \
  "v_mfma_f32_16x16x32_bf16 v[156:159], v[92:95], v[16:19], v[156:159]\n\t" \
  "ds_read_b64 v[16:17], v28 offset:14336\n\t" \
  "s_waitcnt lgkmcnt(6)\n\t" \
  "v_mov_b32 v136, v36\n\t" \
  "v_writelane_b32 v136, %[p1], 0\n\t" \
  "v_mov_b32 v143, v142\n\t" \
  "s_waitcnt lgkmcnt(12)\n\t" \
  "v_mfma_f32_16x16x32_bf16 v[144:147], v[64:67], v[20:23], v[144:147]\n\t" \
  "ds_read_b64 v[18:19], v29 offset:14336\n\t" \
  "v_mov_b32 v142, v141\n\t" \
  "v_mfma_f32_16x16x32_bf16 v[148:151], v[72:75], v[20:23], v[148:151]\n\t" \
  "v_mov_b32 v141, v140\n\t" \
  "v_mfma_f32_16x16x32_bf16 v[152:155], v[80:83], v[20:23], v[152:155]\n\t" \
  "v_mfma_f32_16x16x32_bf16 v[156:159], v[88:91], v[20:23], v[156:159]\n\t" \
  "ds_read_b64 v[20:21], v30 offset:15360\n\t" \
  "s_waitcnt lgkmcnt(11)\n\t" \
  "v_mfma_f32_16x16x32_bf16 v[148:151], v[68:71], v[0:3], v[148:151]\n\t" \
  "ds_read_b64 v[22:23], v31 offset:15360\n\t" \
  "v_mfma_f32_16x16x32_bf16 v[152:155], v[76:79], v[0:3], v[152:155]\n\t" \
  "v_mfma_f32_16x16x32_bf16 v[156:159], v[84:87], v[0:3], v[156:159]\n\t" \
  "ds_read_b64 v[0:1], v30 offset:0\n\t" \
  "s_waitcnt lgkmcnt(11)\n\t" \
  "v_mfma_f32_16x16x32_bf16 v[148:151], v[64:67], v[4:7], v[148:151]\n\t" \
  "s_cmp_eq_u32 %[rem], 0\n\t" \
  "s_cbranch_scc1 CB208_%=\n\t" \
  "s_barrier\n\t" \
  "s_sub_u32 %[rem], %[rem], 1\n\t" \
  "CB208_%=:\n\t" \
  "ds_read_b64 v[2:3], v31 offset:0\n\t" \
  "ds_bpermute_b32 v38, v32, v147\n\t" \
  "v_readlane_b32 %[p4], v147, 63\n\t" \
  "v_mfma_f32_16x16x32_bf16 v[152:155], v[72:75], v[4:7], v[152:155]\n\t" \
  "v_mov_b32 v147, v146\n\t" \
  "v_mfma_f32_16x16x32_bf16 v[156:159], v[80:83], v[4:7], v[156:159]\n\t" \
  "ds_read_b64 v[4:5], v24 offset:1024\n\t" \
  "v_mov_b32 v146, v145\n\t" \
  "s_waitcnt lgkmcnt(11)\n\t" \
  "v_mfma_f32_16x16x32_bf16 v[152:155], v[68:71], v[8:11], v[152:155]\n\t" \
  "ds_read_b64 v[6:7], v25 offset:1024\n\t" \
  "s_waitcnt lgkmcnt(9)\n\t" \
  "v_mov_b32 v140, v37\n\t" \
  "v_writelane_b32 v140, %[p2], 0\n\t" \
  "v_mov_b32 v145, v144\n\t" \
  "v_mfma_f32_16x16x32_bf16 v[156:159], v[76:79], v[8:11], v[156:159]\n\t" \
  "ds_read_b64 v[8:9], v26 offset:2048\n\t" \
  "ds_bpermute_b32 v39, v32, v151\n\t" \
  "v_readlane_b32 %[p5], v151, 63\n\t" \
  "s_waitcnt lgkmcnt(12)\n\t" \
  "v_mfma_f32_16x16x32_bf16 v[152:155], v[64:67], v[12:15], v[152:155]\n\t" \
  "ds_read_b64 v[10:11], v27 offset:2048\n\t" \
  "v_mov_b32 v151, v150\n\t" \
  "v_mfma_f32_16x16x32_bf16 v[156:159], v[72:75], v[12:15], v[156:159]\n\t" \
  "ds_read_b64 v[12:13], v28 offset:3072\n\t" \
  "v_mov_b32 v150, v149\n\t" \
  "s_waitcnt lgkmcnt(11)\n\t" \
  "v_mfma_f32_16x16x32_bf16 v[156:159], v[68:71], v[16:19], v[156:159]\n\t" \
  "ds_read_b64 v[14:15], v29 offset:3072\n\t" \
  "ds_read_b64 v[16:17], v30 offset:4096\n\t" \
  "v_mov_b32 v149, v148\n\t" \
  "ds_bpermute_b32 v40, v32, v155\n\t" \
  "v_readlane_b32 %[p6], v155, 63\n\t" \
  "s_waitcnt lgkmcnt(12)\n\t" \
  "v_mfma_f32_16x16x32_bf16 v[156:159], v[64:67], v[20:23], v[156:159]\n\t" \
  "ds_read_b64 v[18:19], v31 offset:4096\n\t" \
  "ds_read_b64 v[20:21], v24 offset:5120\n\t" \
  "v_mov_b32 v155, v154\n\t" \
  "s_waitcnt lgkmcnt(12)\n\t" \
  "v_mfma_f32_16x16x32_bf16 v[128:131], v[68:71], v[0:3], v[128:131]\n\t" \
  "ds_read_b64 v[22:23], v25 offset:5120\n\t" \
  "v_mov_b32 v154, v153\n\t" \
  "v_mfma_f32_16x16x32_bf16 v[132:135], v[76:79], v[0:3], v[132:135]\n\t" \
  "s_waitcnt lgkmcnt(12)\n\t" \
  "v_mov_b32 v144, v38\n\t" \
  "v_writelane_b32 v144, %[p3], 0\n\t" \
  "v_mov_b32 v153, v152\n\t" \
  "v_mfma_f32_16x16x32_bf16 v[136:139], v[84:87], v[0:3], v[136:139]\n\t" \
  "s_waitcnt lgkmcnt(8)\n\t" \
  "v_mov_b32 v148, v39\n\t" \
  "v_writelane_b32 v148, %[p4], 0\n\t" \
  "v_mfma_f32_16x16x32_bf16 v[140:143], v[92:95], v[0:3], v[140:143]\n\t" \
  "s_waitcnt lgkmcnt(3)\n\t" \
  "v_mov_b32 v152, v40\n\t" \
  "v_writelane_b32 v152, %[p5], 0\n\t" \
  "v_mfma_f32_16x16x32_bf16 v[144:147], v[100:103], v[0:3], v[144:147]\n\t" \
  "ds_bpermute_b32 v41, v32, v159\n\t" \
  "v_mfma_f32_16x16x32_bf16 v[148:151], v[108:111], v[0:3], v[148:151]\n\t" \
  "v_mov_b32 v159, v158\n\t" \
  "v_mfma_f32_16x16x32_bf16 v[152:155], v[116:119], v[0:3], v[152:155]\n\t" \
  "v_mov_b32 v158, v157\n\t" \
  "s_waitcnt lgkmcnt(11)\n\t" \
  "v_mfma_f32_16x16x32_bf16 v[128:131], v[64:67], v[4:7], v[128:131]\n\t" \
  "s_cmp_eq_u32 %[rem], 0\n\t" \
  "s_cbranch_scc1 CB224_%=\n\t" \
  "s_barrier\n\t" \
  "s_sub_u32 %[rem], %[rem], 1\n\t" \
  "CB224_%=:\n\t" \
  "v_mov_b32 v157, v156\n\t" \
  "v_mfma_f32_16x16x32_bf16 v[132:135], v[72:75], v[4:7], v[132:135]\n\t" \
  "v_mfma_f32_16x16x32_bf16 v[136:139], v[80:83], v[4:7], v[136:139]\n\t" \
  "v_mfma_f32_16x16x32_bf16 v[140:143], v[88:91], v[4:7], v[140:143]\n\t" \
  "v_mfma_f32_16x16x32_bf16 v[144:147], v[96:99], v[4:7], v[144:147]\n\t" \
  "s_waitcnt lgkmcnt(0)\n\t" \
  "v_mov_b32 v156, v41\n\t" \
  "v_writelane_b32 v156, %[p6], 0\n\t" \
  "v_mfma_f32_16x16x32_bf16 v[148:151], v[104:107], v[4:7], v[148:151]\n\t" \
  "v_mfma_f32_16x16x32_bf16 v[152:155], v[112:115], v[4:7], v[152:155]\n\t" \
  "s_mov_b64 exec, 1\n\t" \
  "ds_write_b32 v33, v33\n\t" \
  "s_mov_b64 exec, -1\n\t" \
  "v_mfma_f32_16x16x32_bf16 v[156:159], v[124:127], v[0:3], v[156:159]\n\t" \
  "ds_read_b64 v[0:1], v26 offset:6144\n\t" \
  "v_mfma_f32_16x16x32_bf16 v[156:159], v[120:123], v[4:7], v[156:159]\n\t" \
  "ds_read_b64 v[2:3], v27 offset:6144\n\t" \
  "ds_read_b64 v[4:5], v28 offset:7168\n\t" \
  "s_waitcnt lgkmcnt(12)\n\t" \
  "v_mfma_f32_16x16x32_bf16 v[132:135], v[68:71], v[8:11], v[132:135]\n\t" \
  "ds_read_b64 v[6:7], v29 offset:7168\n\t" \
  "v_mfma_f32_16x16x32_bf16 v[136:139], v[76:79], v[8:11], v[136:139]\n\t" \
  "v_mfma_f32_16x16x32_bf16 v[140:143], v[84:87], v[8:11], v[140:143]\n\t" \
  "v_mfma_f32_16x16x32_bf16 v[144:147], v[92:95], v[8:11], v[144:147]\n\t" \
  "v_mfma_f32_16x16x32_bf16 v[148:151], v[100:103], v[8:11], v[148:151]\n\t" \
  "v_mfma_f32_16x16x32_bf16 v[152:155], v[108:111], v[8:11], v[152:155]\n\t" \
  "v_mfma_f32_16x16x32_bf16 v[156:159], v[116:119], v[8:11], v[156:159]\n\t" \
  "ds_read_b64 v[8:9], v30 offset:8192\n\t" \
  "s_waitcnt lgkmcnt(12)\n\t" \
  "v_mfma_f32_16x16x32_bf16 v[132:135], v[64:67], v[12:15], v[132:135]\n\t" \
  "s_cmp_eq_u32 %[rem], 0\n\t" \
  "s_cbranch_scc1 CB240_%=\n\t" \
  "s_barrier\n\t" \
  "s_sub_u32 %[rem], %[rem], 1\n\t" \
  "CB240_%=:\n\t" \
  "ds_read_b64 v[10:11], v31 offset:8192\n\t" \
  "v_mfma_f32_16x16x32_bf16 v[136:139], v[72:75], v[12:15], v[136:139]\n\t" \
  "v_mfma_f32_16x16x32_bf16 v[140:143], v[80:83], v[12:15], v[140:143]\n\t" \
  "v_mfma_f32_16x16x32_bf16 v[144:147], v[88:91], v[12:15], v[144:147]\n\t" \
  "v_mfma_f32_16x16x32_bf16 v[148:151], v[96:99], v[12:15], v[148:151]\n\t" \
  "v_mfma_f32_16x16x32_bf16 v[152:155], v[104:107], v[12:15], v[152:155]\n\t" \
  "v_mfma_f32_16x16x32_bf16 v[156:159], v[112:115], v[12:15], v[156:159]\n\t" \
  "ds_read_b64 v[12:13], v24 offset:9216\n\t" \
  "s_waitcnt lgkmcnt(11)\n\t" \
  "v_mfma_f32_16x16x32_bf16 v[136:139], v[68:71], v[16:19], v[136:139]\n\t" \
  "ds_read_b64 v[14:15], v25 offset:9216\n\t" \
  "v_mfma_f32_16x16x32_bf16 v[140:143], v[76:79], v[16:19], v[140:143]\n\t" \
  "v_mfma_f32_16x16x32_bf16 v[144:147], v[84:87], v[16:19], v[144:147]\n\t" \
  "v_mfma_f32_16x16x32_bf16 v[148:151], v[92:95], v[16:19], v[148:151]\n\t" \
  "v_mfma_f32_16x16x32_bf16 v[152:155], v[100:103], v[16:19], v[152:155]\n\t" \
  "v_mfma_f32_16x16x32_bf16 v[156:159], v[108:111], v[16:19], v[156:159]\n\t" \
  "ds_read_b64 v[16:17], v26 offset:10240\n\t" \
  "s_waitcnt lgkmcnt(11)\n\t" \
  "v_mfma_f32_16x16x32_bf16 v[136:139], v[64:67], v[20:23], v[136:139]\n\t" \
  "ds_read_b64 v[18:19], v27 offset:10240\n\t" \
  "v_mfma_f32_16x16x32_bf16 v[140:143], v[72:75], v[20:23], v[140:143]\n\t" \
  "v_mfma_f32_16x16x32_bf16 v[144:147], v[80:83], v[20:23], v[144:147]\n\t" \
  "v_mfma_f32_16x16x32_bf16 v[148:151], v[88:91], v[20:23], v[148:151]\n\t" \
  "s_cmp_eq_u32 %[rem], 0\n\t" \
  "s_cbranch_scc1 CB256_%=\n\t" \
  "s_barrier\n\t" \
  "s_sub_u32 %[rem], %[rem], 1\n\t" \
  "CB256_%=:\n\t" \
  "v_mfma_f32_16x16x32_bf16 v[152:155], v[96:99], v[20:23], v[152:155]\n\t" \
  "v_mfma_f32_16x16x32_bf16 v[156:159], v[104:107], v[20:23], v[156:159]\n\t" \
  "ds_read_b64 v[20:21], v28 offset:11264\n\t" \
  "s_waitcnt lgkmcnt(9)\n\t" \
  "v_mfma_f32_16x16x32_bf16 v[140:143], v[68:71], v[0:3], v[140:143]\n\t" \
  "ds_read_b64 v[22:23], v29 offset:11264\n\t" \
  "v_mfma_f32_16x16x32_bf16 v[144:147], v[76:79], v[0:3], v[144:147]\n\t" \
  "v_mfma_f32_16x16x32_bf16 v[148:151], v[84:87], v[0:3], v[148:151]\n\t" \
  "v_mfma_f32_16x16x32_bf16 v[152:155], v[92:95], v[0:3], v[152:155]\n\t" \
  "v_mfma_f32_16x16x32_bf16 v[156:159], v[100:103], v[0:3], v[156:159]\n\t" \
  "ds_read_b64 v[0:1], v30 offset:12288\n\t" \
  "s_waitcnt lgkmcnt(9)\n\t" \
  "v_mfma_f32_16x16x32_bf16 v[140:143], v[64:67], v[4:7], v[140:143]\n\t" \
  "ds_read_b64 v[2:3], v31 offset:12288\n\t" \
  "v_mfma_f32_16x16x32_bf16 v[144:147], v[72:75], v[4:7], v[144:147]\n\t" \
  "v_mfma_f32_16x16x32_bf16 v[148:151], v[80:83], v[4:7], v[148:151]\n\t" \
  "v_mfma_f32_16x16x32_bf16 v[152:155], v[88:91], v[4:7], v[152:155]\n\t" \
  "v_mfma_f32_16x16x32_bf16 v[156:159], v[96:99], v[4:7], v[156:159]\n\t" \
  "ds_read_b64 v[4:5], v24 offset:13312\n\t" \
  "s_waitcnt lgkmcnt(9)\n\t" \
  "v_mfma_f32_16x16x32_bf16 v[144:147], v[68:71], v[8:11], v[144:147]\n\t" \
  "ds_read_b64 v[6:7], v25 offset:13312\n\t" \
  "v_mfma_f32_16x16x32_bf16 v[148:151], v[76:79], v[8:11], v[148:151]\n\t" \
  "v_mfma_f32_16x16x32_bf16 v[152:155], v[84:87], v[8:11], v[152:155]\n\t" \
  "v_mfma_f32_16x16x32_bf16 v[156:159], v[92:95], v[8:11], v[156:159]\n\t" \
  "s_cmp_eq_u32 %[rem], 0\n\t" \
  "s_cbranch_scc1 CB272_%=\n\t" \
  "s_barrier\n\t" \
  "s_sub_u32 %[rem], %[rem], 1\n\t" \
  "CB272_%=:\n\t" \
  "ds_read_b64 v[8:9], v26 offset:14336\n\t" \
  "s_waitcnt lgkmcnt(9)\n\t" \
  "v_mfma_f32_16x16x32_bf16 v[144:147], v[64:67], v[12:15], v[144:147]\n\t" \
  "ds_read_b64 v[10:11], v27 offset:14336\n\t" \
  "v_mfma_f32_16x16x32_bf16 v[148:151], v[72:75], v[12:15], v[148:151]\n\t" \
  "v_mfma_f32_16x16x32_bf16 v[152:155], v[80:83], v[12:15], v[152:155]\n\t" \
  "v_mfma_f32_16x16x32_bf16 v[156:159], v[88:91], v[12:15], v[156:159]\n\t" \
  "ds_read_b64 v[12:13], v28 offset:15360\n\t" \
  "s_waitcnt lgkmcnt(9)\n\t" \
  "v_mfma_f32_16x16x32_bf16 v[148:151], v[68:71], v[16:19], v[148:151]\n\t" \
  "ds_read_b64 v[14:15], v29 offset:15360\n\t" \
  "v_mfma_f32_16x16x32_bf16 v[152:155], v[76:79], v[16:19], v[152:155]\n\t" \
  "v_mfma_f32_16x16x32_bf16 v[156:159], v[84:87], v[16:19], v[156:159]\n\t" \
  "s_waitcnt lgkmcnt(8)\n\t" \
  "v_mfma_f32_16x16x32_bf16 v[148:151], v[64:67], v[20:23], v[148:151]\n\t" \
  "v_mfma_f32_16x16x32_bf16 v[152:155], v[72:75], v[20:23], v[152:155]\n\t" \
  "v_mfma_f32_16x16x32_bf16 v[156:159], v[80:83], v[20:23], v[156:159]\n\t" \
  "s_waitcnt lgkmcnt(6)\n\t" \
  "v_mfma_f32_16x16x32_bf16 v[152:155], v[68:71], v[0:3], v[152:155]\n\t" \
  "v_mfma_f32_16x16x32_bf16 v[156:159], v[76:79], v[0:3], v[156:159]\n\t" \
  "s_waitcnt lgkmcnt(4)\n\t" \
  "v_mfma_f32_16x16x32_bf16 v[152:155], v[64:67], v[4:7], v[152:155]\n\t" \
  "v_mfma_f32_16x16x32_bf16 v[156:159], v[72:75], v[4:7], v[156:159]\n\t" \
  "s_waitcnt lgkmcnt(2)\n\t" \
  "v_mfma_f32_16x16x32_bf16 v[156:159], v[68:71], v[8:11], v[156:159]\n\t" \
  "s_waitcnt lgkmcnt(0)\n\t" \
  "v_mfma_f32_16x16x32_bf16 v[156:159], v[64:67], v[12:15], v[156:159]\n\t" \
  "s_waitcnt lgkmcnt(0)\n\t" \
  "s_nop 7\n\t"
#define CV_CLOB "v0", "v1", "v2", "v3", "v4", "v5", "v6", "v7", "v8", "v9", "v10", "v11", "v12", "v13", "v14", "v15", "v16", "v17", "v18", "v19", "v20", "v21", "v22", "v23", "v34", "v35", "v36", "v37", "v38", "v39", "v40", "v41"

#define G3_ASM \
  "s_getpc_b64 s[92:93]\n\t" \
  "s_branch QC_%=\n\t" \
  "ds_read_b64_tr_b16 v[96:97], v160 offset:0\n\t" \
  "ds_read_b64_tr_b16 v[98:99], v160 offset:2048\n\t" \
  "ds_read_b64_tr_b16 v[100:101], v161 offset:0\n\t" \
  "ds_read_b64_tr_b16 v[102:103], v161 offset:2048\n\t" \
  "ds_read_b64_tr_b16 v[104:105], v162 offset:0\n\t" \
  "ds_read_b64_tr_b16 v[106:107], v162 offset:2048\n\t" \
  "ds_read_b64_tr_b16 v[108:109], v163 offset:0\n\t" \
  "ds_read_b64_tr_b16 v[110:111], v163 offset:2048\n\t" \
  "ds_read_b64_tr_b16 v[112:113], v160 offset:4096\n\t" \
  "ds_read_b64_tr_b16 v[114:115], v160 offset:6144\n\t" \
  "ds_read_b64_tr_b16 v[116:117], v161 offset:4096\n\t" \
  "ds_read_b64_tr_b16 v[118:119], v161 offset:6144\n\t" \
  "ds_read_b64_tr_b16 v[120:121], v162 offset:4096\n\t" \
  "ds_read_b64_tr_b16 v[122:123], v162 offset:6144\n\t" \
  "QH_%=:\n\t" \
  "v_add_u32 v166, %[go], v165\n\t" \
  "ds_read_b32 v166, v166\n\t" \
  "s_addk_i32 %[cn], 1\n\t" \
  "s_cmp_gt_u32 %[cn], 0x400000\n\t" \
  "s_cbranch_scc1 QI_%=\n\t" \
  "s_waitcnt lgkmcnt(0)\n\t" \
  "v_cmp_eq_u32 vcc, 0, v166\n\t" \
  "s_cbranch_vccz QI_%=\n\t" \
  "s_sleep 2\n\t" \
  "s_getpc_b64 s[92:93]\n\t" \
  "s_branch QC_%=\n\t" \
  "s_branch QH_%=\n\t" \
  "QI_%=:\n\t" \
  "ds_read_b32 v166, v165\n\t" \
  "s_waitcnt lgkmcnt(11)\n\t" \
  "v_mfma_f32_16x16x32_bf16 v[128:131], v[96:99], v[0:3], 0\n\t" \
  "ds_read_b64_tr_b16 v[124:125], v163 offset:4096\n\t" \
  "v_mfma_f32_16x16x32_bf16 v[144:147], v[96:99], v[4:7], 0\n\t" \
  "ds_read_b64_tr_b16 v[126:127], v163 offset:6144\n\t" \
  "s_waitcnt lgkmcnt(2)\n\t" \
  "v_cmp_lt_u32 vcc, %[th], v166\n\t" \
  "v_mfma_f32_16x16x32_bf16 v[132:135], v[100:103], v[0:3], 0\n\t" \
  "s_mov_b64 exec, vcc\n\t" \
  "global_load_dwordx4 v[80:83], v164, %[ob] offset:0 sc1\n\t" \
  "s_mov_b64 exec, -1\n\t" \
  "v_mfma_f32_16x16x32_bf16 v[148:151], v[100:103], v[4:7], 0\n\t" \
  "ds_read_b64_tr_b16 v[96:97], v160 offset:8192\n\t" \
  "ds_read_b64_tr_b16 v[98:99], v160 offset:10240\n\t" \
  "v_mfma_f32_16x16x32_bf16 v[136:139], v[104:107], v[0:3], 0\n\t" \
  "s_mov_b64 exec, vcc\n\t" \
  "global_load_dwordx4 v[84:87], v164, %[ob] offset:1024 sc1\n\t" \
  "s_mov_b64 exec, -1\n\t" \
  "v_mfma_f32_16x16x32_bf16 v[152:155], v[104:107], v[4:7], 0\n\t" \
  "ds_read_b64_tr_b16 v[100:101], v161 offset:8192\n\t" \
  "ds_read_b64_tr_b16 v[102:103], v161 offset:10240\n\t" \
  "v_mfma_f32_16x16x32_bf16 v[140:143], v[108:111], v[0:3], 0\n\t" \
  "s_mov_b64 exec, vcc\n\t" \
  "global_load_dwordx4 v[88:91], v164, %[ob] offset:2048 sc1\n\t" \
  "s_mov_b64 exec, -1\n\t" \
  "v_mfma_f32_16x16x32_bf16 v[156:159], v[108:111], v[4:7], 0\n\t" \
  "ds_read_b64_tr_b16 v[104:105], v162 offset:8192\n\t" \
  "ds_read_b64_tr_b16 v[106:107], v162 offset:10240\n\t" \
  "v_mfma_f32_16x16x32_bf16 v[128:131], v[112:115], v[8:11], v[128:131]\n\t" \
  "s_mov_b64 exec, vcc\n\t" \
  "global_load_dwordx4 v[92:95], v164, %[ob] offset:3072 sc1\n\t" \
  "s_mov_b64 exec, -1\n\t" \
  "v_mfma_f32_16x16x32_bf16 v[144:147], v[112:115], v[12:15], v[144:147]\n\t" \
  "ds_read_b64_tr_b16 v[108:109], v163 offset:8192\n\t" \
  "ds_read_b64_tr_b16 v[110:111], v163 offset:10240\n\t" \
  "v_mfma_f32_16x16x32_bf16 v[132:135], v[116:119], v[8:11], v[132:135]\n\t" \
  "ds_read_b64_tr_b16 v[112:113], v160 offset:12288\n\t" \
  "s_and_b32 %[t0], vcc_lo, 0x4000\n\t" \
  "ds_read_b32 v166, v165\n\t" \
  "v_mfma_f32_16x16x32_bf16 v[148:151], v[116:119], v[12:15], v[148:151]\n\t" \
  "ds_read_b64_tr_b16 v[114:115], v160 offset:14336\n\t" \
  "v_add_u32 v164, %[t0], v164\n\t" \
  "s_waitcnt lgkmcnt(11)\n\t" \
  "v_mfma_f32_16x16x32_bf16 v[136:139], v[120:123], v[8:11], v[136:139]\n\t" \
  "ds_read_b64_tr_b16 v[116:117], v161 offset:12288\n\t" \
  "v_and_b32 v164, 0x3ffff, v164\n\t" \
  "v_mfma_f32_16x16x32_bf16 v[152:155], v[120:123], v[12:15], v[152:155]\n\t" \
  "ds_read_b64_tr_b16 v[118:119], v161 offset:14336\n\t" \
  "s_sub_u32 %[iss], %[iss], vcc_lo\n\t" \
  "v_mfma_f32_16x16x32_bf16 v[140:143], v[124:127], v[8:11], v[140:143]\n\t" \
  "ds_read_b64_tr_b16 v[120:121], v162 offset:12288\n\t" \
  "v_mfma_f32_16x16x32_bf16 v[156:159], v[124:127], v[12:15], v[156:159]\n\t" \
  "ds_read_b64_tr_b16 v[122:123], v162 offset:14336\n\t" \
  "s_add_u32 %[th], %[th], 1\n\t" \
  "s_cbranch_vccz QF1_%=\n\t" \
  "s_waitcnt vmcnt(16)\n\t" \
  "QG1_%=:\n\t" \
  "s_waitcnt lgkmcnt(11)\n\t" \
  "v_mfma_f32_16x16x32_bf16 v[128:131], v[96:99], v[16:19], v[128:131]\n\t" \
  "ds_read_b64_tr_b16 v[124:125], v163 offset:12288\n\t" \
  "v_mfma_f32_16x16x32_bf16 v[144:147], v[96:99], v[20:23], v[144:147]\n\t" \
  "ds_read_b64_tr_b16 v[126:127], v163 offset:14336\n\t" \
  "s_waitcnt lgkmcnt(7)\n\t" \
  "v_cmp_lt_u32 vcc, %[th], v166\n\t" \
  "v_mfma_f32_16x16x32_bf16 v[132:135], v[100:103], v[16:19], v[132:135]\n\t" \
  "s_mov_b64 exec, vcc\n\t" \
  "global_load_dwordx4 v[0:3], v164, %[ob] offset:0 sc1\n\t" \
  "s_mov_b64 exec, -1\n\t" \
  "v_mfma_f32_16x16x32_bf16 v[148:151], v[100:103], v[20:23], v[148:151]\n\t" \
  "ds_read_b64_tr_b16 v[96:97], v160 offset:16384\n\t" \
  "ds_read_b64_tr_b16 v[98:99], v160 offset:18432\n\t" \
  "v_mfma_f32_16x16x32_bf16 v[136:139], v[104:107], v[16:19], v[136:139]\n\t" \
  "s_mov_b64 exec, vcc\n\t" \
  "global_load_dwordx4 v[4:7], v164, %[ob] offset:1024 sc1\n\t" \
  "s_mov_b64 exec, -1\n\t" \
  "v_mfma_f32_16x16x32_bf16 v[152:155], v[104:107], v[20:23], v[152:155]\n\t" \
  "ds_read_b64_tr_b16 v[100:101], v161 offset:16384\n\t" \
  "ds_read_b64_tr_b16 v[102:103], v161 offset:18432\n\t" \
  "v_mfma_f32_16x16x32_bf16 v[140:143], v[108:111], v[16:19], v[140:143]\n\t" \
  "s_mov_b64 exec, vcc\n\t" \
  "global_load_dwordx4 v[8:11], v164, %[ob] offset:2048 sc1\n\t" \
  "s_mov_b64 exec, -1\n\t" \
  "v_mfma_f32_16x16x32_bf16 v[156:159], v[108:111], v[20:23], v[156:159]\n\t" \
  "ds_read_b64_tr_b16 v[104:105], v162 offset:16384\n\t" \
  "ds_read_b64_tr_b16 v[106:107], v162 offset:18432\n\t" \
  "s_waitcnt lgkmcnt(10)\n\t" \
  "v_mfma_f32_16x16x32_bf16 v[128:131], v[112:115], v[24:27], v[128:131]\n\t" \
  "s_mov_b64 exec, vcc\n\t" \
  "global_load_dwordx4 v[12:15], v164, %[ob] offset:3072 sc1\n\t" \
  "s_mov_b64 exec, -1\n\t" \
  "v_mfma_f32_16x16x32_bf16 v[144:147], v[112:115], v[28:31], v[144:147]\n\t" \
  "ds_read_b64_tr_b16 v[108:109], v163 offset:16384\n\t" \
  "ds_read_b64_tr_b16 v[110:111], v163 offset:18432\n\t" \
  "v_mfma_f32_16x16x32_bf16 v[132:135], v[116:119], v[24:27], v[132:135]\n\t" \
  "ds_read_b64_tr_b16 v[112:113], v160 offset:20480\n\t" \
  "s_and_b32 %[t0], vcc_lo, 0x4000\n\t" \
  "ds_read_b32 v166, v165\n\t" \
  "v_mfma_f32_16x16x32_bf16 v[148:151], v[116:119], v[28:31], v[148:151]\n\t" \
  "ds_read_b64_tr_b16 v[114:115], v160 offset:22528\n\t" \
  "v_add_u32 v164, %[t0], v164\n\t" \
  "s_waitcnt lgkmcnt(11)\n\t" \
  "v_mfma_f32_16x16x32_bf16 v[136:139], v[120:123], v[24:27], v[136:139]\n\t" \
  "ds_read_b64_tr_b16 v[116:117], v161 offset:20480\n\t" \
  "v_and_b32 v164, 0x3ffff, v164\n\t" \
  "v_mfma_f32_16x16x32_bf16 v[152:155], v[120:123], v[28:31], v[152:155]\n\t" \
  "ds_read_b64_tr_b16 v[118:119], v161 offset:22528\n\t" \
  "s_sub_u32 %[iss], %[iss], vcc_lo\n\t" \
  "v_mfma_f32_16x16x32_bf16 v[140:143], v[124:127], v[24:27], v[140:143]\n\t" \
  "ds_read_b64_tr_b16 v[120:121], v162 offset:20480\n\t" \
  "v_mfma_f32_16x16x32_bf16 v[156:159], v[124:127], v[28:31], v[156:159]\n\t" \
  "ds_read_b64_tr_b16 v[122:123], v162 offset:22528\n\t" \
  "s_add_u32 %[th], %[th], 1\n\t" \
  "s_cbranch_vccz QF2_%=\n\t" \
  "s_waitcnt vmcnt(16)\n\t" \
  "QG2_%=:\n\t" \
  "s_waitcnt lgkmcnt(11)\n\t" \
  "v_mfma_f32_16x16x32_bf16 v[128:131], v[96:99], v[32:35], v[128:131]\n\t" \
  "ds_read_b64_tr_b16 v[124:125], v163 offset:20480\n\t" \
  "v_mfma_f32_16x16x32_bf16 v[144:147], v[96:99], v[36:39], v[144:147]\n\t" \
  "ds_read_b64_tr_b16 v[126:127], v163 offset:22528\n\t" \
  "s_waitcnt lgkmcnt(7)\n\t" \
  "v_cmp_lt_u32 vcc, %[th], v166\n\t" \
  "v_mfma_f32_16x16x32_bf16 v[132:135], v[100:103], v[32:35], v[132:135]\n\t" \
  "s_mov_b64 exec, vcc\n\t" \
  "global_load_dwordx4 v[16:19], v164, %[ob] offset:0 sc1\n\t" \
  "s_mov_b64 exec, -1\n\t" \
  "v_mfma_f32_16x16x32_bf16 v[148:151], v[100:103], v[36:39], v[148:151]\n\t" \
  "ds_read_b64_tr_b16 v[96:97], v160 offset:24576\n\t" \
  "ds_read_b64_tr_b16 v[98:99], v160 offset:26624\n\t" \
  "v_mfma_f32_16x16x32_bf16 v[136:139], v[104:107], v[32:35], v[136:139]\n\t" \
  "s_mov_b64 exec, vcc\n\t" \
  "global_load_dwordx4 v[20:23], v164, %[ob] offset:1024 sc1\n\t" \
  "s_mov_b64 exec, -1\n\t" \
  "v_mfma_f32_16x16x32_bf16 v[152:155], v[104:107], v[36:39], v[152:155]\n\t" \
  "ds_read_b64_tr_b16 v[100:101], v161 offset:24576\n\t" \
  "ds_read_b64_tr_b16 v[102:103], v161 offset:26624\n\t" \
  "v_mfma_f32_16x16x32_bf16 v[140:143], v[108:111], v[32:35], v[140:143]\n\t" \
  "s_mov_b64 exec, vcc\n\t" \
  "global_load_dwordx4 v[24:27], v164, %[ob] offset:2048 sc1\n\t" \
  "s_mov_b64 exec, -1\n\t" \
  "v_mfma_f32_16x16x32_bf16 v[156:159], v[108:111], v[36:39], v[156:159]\n\t" \
  "ds_read_b64_tr_b16 v[104:105], v162 offset:24576\n\t" \
  "ds_read_b64_tr_b16 v[106:107], v162 offset:26624\n\t" \
  "s_waitcnt lgkmcnt(10)\n\t" \
  "v_mfma_f32_16x16x32_bf16 v[128:131], v[112:115], v[40:43], v[128:131]\n\t" \
  "s_mov_b64 exec, vcc\n\t" \
  "global_load_dwordx4 v[28:31], v164, %[ob] offset:3072 sc1\n\t" \
  "s_mov_b64 exec, -1\n\t" \
  "v_mfma_f32_16x16x32_bf16 v[144:147], v[112:115], v[44:47], v[144:147]\n\t" \
  "ds_read_b64_tr_b16 v[108:109], v163 offset:24576\n\t" \
  "ds_read_b64_tr_b16 v[110:111], v163 offset:26624\n\t" \
  "v_mfma_f32_16x16x32_bf16 v[132:135], v[116:119], v[40:43], v[132:135]\n\t" \
  "ds_read_b64_tr_b16 v[112:113], v160 offset:28672\n\t" \
  "s_and_b32 %[t0], vcc_lo, 0x4000\n\t" \
  "ds_read_b32 v166, v165\n\t" \
  "v_mfma_f32_16x16x32_bf16 v[148:151], v[116:119], v[44:47], v[148:151]\n\t" \
  "ds_read_b64_tr_b16 v[114:115], v160 offset:30720\n\t" \
  "v_add_u32 v164, %[t0], v164\n\t" \
  "s_waitcnt lgkmcnt(11)\n\t" \
  "v_mfma_f32_16x16x32_bf16 v[136:139], v[120:123], v[40:43], v[136:139]\n\t" \
  "ds_read_b64_tr_b16 v[116:117], v161 offset:28672\n\t" \
  "v_and_b32 v164, 0x3ffff, v164\n\t" \
  "v_mfma_f32_16x16x32_bf16 v[152:155], v[120:123], v[44:47], v[152:155]\n\t" \
  "ds_read_b64_tr_b16 v[118:119], v161 offset:30720\n\t" \
  "s_sub_u32 %[iss], %[iss], vcc_lo\n\t" \
  "v_mfma_f32_16x16x32_bf16 v[140:143], v[124:127], v[40:43], v[140:143]\n\t" \
  "ds_read_b64_tr_b16 v[120:121], v162 offset:28672\n\t" \
  "v_mfma_f32_16x16x32_bf16 v[156:159], v[124:127], v[44:47], v[156:159]\n\t" \
  "ds_read_b64_tr_b16 v[122:123], v162 offset:30720\n\t" \
  "s_add_u32 %[th], %[th], 1\n\t" \
  "s_cbranch_vccz QF3_%=\n\t" \
  "s_waitcnt vmcnt(16)\n\t" \
  "QG3_%=:\n\t" \
  "s_waitcnt lgkmcnt(11)\n\t" \
  "v_mfma_f32_16x16x32_bf16 v[128:131], v[96:99], v[48:51], v[128:131]\n\t" \
  "ds_read_b64_tr_b16 v[124:125], v163 offset:28672\n\t" \
  "v_mfma_f32_16x16x32_bf16 v[144:147], v[96:99], v[52:55], v[144:147]\n\t" \
  "ds_read_b64_tr_b16 v[126:127], v163 offset:30720\n\t" \
  "s_waitcnt lgkmcnt(7)\n\t" \
  "v_cmp_lt_u32 vcc, %[th], v166\n\t" \
  "v_mfma_f32_16x16x32_bf16 v[132:135], v[100:103], v[48:51], v[132:135]\n\t" \
  "s_mov_b64 exec, vcc\n\t" \
  "global_load_dwordx4 v[32:35], v164, %[ob] offset:0 sc1\n\t" \
  "s_mov_b64 exec, -1\n\t" \
  "v_mfma_f32_16x16x32_bf16 v[148:151], v[100:103], v[52:55], v[148:151]\n\t" \
  "ds_read_b64_tr_b16 v[96:97], v160 offset:32768\n\t" \
  "ds_read_b64_tr_b16 v[98:99], v160 offset:34816\n\t" \
  "v_mfma_f32_16x16x32_bf16 v[136:139], v[104:107], v[48:51], v[136:139]\n\t" \
  "s_mov_b64 exec, vcc\n\t" \
  "global_load_dwordx4 v[36:39], v164, %[ob] offset:1024 sc1\n\t" \
  "s_mov_b64 exec, -1\n\t" \
  "v_mfma_f32_16x16x32_bf16 v[152:155], v[104:107], v[52:55], v[152:155]\n\t" \
  "ds_read_b64_tr_b16 v[100:101], v161 offset:32768\n\t" \
  "ds_read_b64_tr_b16 v[102:103], v161 offset:34816\n\t" \
  "v_mfma_f32_16x16x32_bf16 v[140:143], v[108:111], v[48:51], v[140:143]\n\t" \
  "s_mov_b64 exec, vcc\n\t" \
  "global_load_dwordx4 v[40:43], v164, %[ob] offset:2048 sc1\n\t" \
  "s_mov_b64 exec, -1\n\t" \
  "v_mfma_f32_16x16x32_bf16 v[156:159], v[108:111], v[52:55], v[156:159]\n\t" \
  "ds_read_b64_tr_b16 v[104:105], v162 offset:32768\n\t" \
  "ds_read_b64_tr_b16 v[106:107], v162 offset:34816\n\t" \
  "s_waitcnt lgkmcnt(10)\n\t" \
  "v_mfma_f32_16x16x32_bf16 v[128:131], v[112:115], v[56:59], v[128:131]\n\t" \
  "s_mov_b64 exec, vcc\n\t" \
  "global_load_dwordx4 v[44:47], v164, %[ob] offset:3072 sc1\n\t" \
  "s_mov_b64 exec, -1\n\t" \
  "v_mfma_f32_16x16x32_bf16 v[144:147], v[112:115], v[60:63], v[144:147]\n\t" \
  "ds_read_b64_tr_b16 v[108:109], v163 offset:32768\n\t" \
  "ds_read_b64_tr_b16 v[110:111], v163 offset:34816\n\t" \
  "v_mfma_f32_16x16x32_bf16 v[132:135], v[116:119], v[56:59], v[132:135]\n\t" \
  "ds_read_b64_tr_b16 v[112:113], v160 offset:36864\n\t" \
  "s_and_b32 %[t0], vcc_lo, 0x4000\n\t" \
  "ds_read_b32 v166, v165\n\t" \
  "v_mfma_f32_16x16x32_bf16 v[148:151], v[116:119], v[60:63], v[148:151]\n\t" \
  "ds_read_b64_tr_b16 v[114:115], v160 offset:38912\n\t" \
  "v_add_u32 v164, %[t0], v164\n\t" \
  "s_waitcnt lgkmcnt(11)\n\t" \
  "v_mfma_f32_16x16x32_bf16 v[136:139], v[120:123], v[56:59], v[136:139]\n\t" \
  "ds_read_b64_tr_b16 v[116:117], v161 offset:36864\n\t" \
  "v_and_b32 v164, 0x3ffff, v164\n\t" \
  "v_mfma_f32_16x16x32_bf16 v[152:155], v[120:123], v[60:63], v[152:155]\n\t" \
  "ds_read_b64_tr_b16 v[118:119], v161 offset:38912\n\t" \
  "s_sub_u32 %[iss], %[iss], vcc_lo\n\t" \
  "v_mfma_f32_16x16x32_bf16 v[140:143], v[124:127], v[56:59], v[140:143]\n\t" \
  "ds_read_b64_tr_b16 v[120:121], v162 offset:36864\n\t" \
  "v_mfma_f32_16x16x32_bf16 v[156:159], v[124:127], v[60:63], v[156:159]\n\t" \
  "ds_read_b64_tr_b16 v[122:123], v162 offset:38912\n\t" \
  "s_add_u32 %[th], %[th], 1\n\t" \
  "s_cbranch_vccz QF4_%=\n\t" \
  "s_waitcnt vmcnt(16)\n\t" \
  "QG4_%=:\n\t" \
  "s_waitcnt lgkmcnt(11)\n\t" \
  "v_mfma_f32_16x16x32_bf16 v[128:131], v[96:99], v[64:67], v[128:131]\n\t" \
  "ds_read_b64_tr_b16 v[124:125], v163 offset:36864\n\t" \
  "v_mfma_f32_16x16x32_bf16 v[144:147], v[96:99], v[68:71], v[144:147]\n\t" \
  "ds_read_b64_tr_b16 v[126:127], v163 offset:38912\n\t" \
  "s_waitcnt lgkmcnt(7)\n\t" \
  "v_cmp_lt_u32 vcc, %[th], v166\n\t" \
  "v_mfma_f32_16x16x32_bf16 v[132:135], v[100:103], v[64:67], v[132:135]\n\t" \
  "s_mov_b64 exec, vcc\n\t" \
  "global_load_dwordx4 v[48:51], v164, %[ob] offset:0 sc1\n\t" \
  "s_mov_b64 exec, -1\n\t" \
  "v_mfma_f32_16x16x32_bf16 v[148:151], v[100:103], v[68:71], v[148:151]\n\t" \
  "ds_read_b64_tr_b16 v[96:97], v160 offset:40960\n\t" \
  "ds_read_b64_tr_b16 v[98:99], v160 offset:43008\n\t" \
  "v_mfma_f32_16x16x32_bf16 v[136:139], v[104:107], v[64:67], v[136:139]\n\t" \
  "s_mov_b64 exec, vcc\n\t" \
  "global_load_dwordx4 v[52:55], v164, %[ob] offset:1024 sc1\n\t" \
  "s_mov_b64 exec, -1\n\t" \
  "v_mfma_f32_16x16x32_bf16 v[152:155], v[104:107], v[68:71], v[152:155]\n\t" \
  "ds_read_b64_tr_b16 v[100:101], v161 offset:40960\n\t" \
  "ds_read_b64_tr_b16 v[102:103], v161 offset:43008\n\t" \
  "v_mfma_f32_16x16x32_bf16 v[140:143], v[108:111], v[64:67], v[140:143]\n\t" \
  "s_mov_b64 exec, vcc\n\t" \
  "global_load_dwordx4 v[56:59], v164, %[ob] offset:2048 sc1\n\t" \
  "s_mov_b64 exec, -1\n\t" \
  "v_mfma_f32_16x16x32_bf16 v[156:159], v[108:111], v[68:71], v[156:159]\n\t" \
  "ds_read_b64_tr_b16 v[104:105], v162 offset:40960\n\t" \
  "ds_read_b64_tr_b16 v[106:107], v162 offset:43008\n\t" \
  "s_waitcnt lgkmcnt(10)\n\t" \
  "v_mfma_f32_16x16x32_bf16 v[128:131], v[112:115], v[72:75], v[128:131]\n\t" \
  "s_mov_b64 exec, vcc\n\t" \
  "global_load_dwordx4 v[60:63], v164, %[ob] offset:3072 sc1\n\t" \
  "s_mov_b64 exec, -1\n\t" \
  "v_mfma_f32_16x16x32_bf16 v[144:147], v[112:115], v[76:79], v[144:147]\n\t" \
  "ds_read_b64_tr_b16 v[108:109], v163 offset:40960\n\t" \
  "ds_read_b64_tr_b16 v[110:111], v163 offset:43008\n\t" \
  "v_mfma_f32_16x16x32_bf16 v[132:135], v[116:119], v[72:75], v[132:135]\n\t" \
  "ds_read_b64_tr_b16 v[112:113], v160 offset:45056\n\t" \
  "s_and_b32 %[t0], vcc_lo, 0x4000\n\t" \
  "ds_read_b32 v166, v165\n\t" \
  "v_mfma_f32_16x16x32_bf16 v[148:151], v[116:119], v[76:79], v[148:151]\n\t" \
  "ds_read_b64_tr_b16 v[114:115], v160 offset:47104\n\t" \
  "v_add_u32 v164, %[t0], v164\n\t" \
  "s_waitcnt lgkmcnt(11)\n\t" \
  "v_mfma_f32_16x16x32_bf16 v[136:139], v[120:123], v[72:75], v[136:139]\n\t" \
  "ds_read_b64_tr_b16 v[116:117], v161 offset:45056\n\t" \
  "v_and_b32 v164, 0x3ffff, v164\n\t" \
  "v_mfma_f32_16x16x32_bf16 v[152:155], v[120:123], v[76:79], v[152:155]\n\t" \
  "ds_read_b64_tr_b16 v[118:119], v161 offset:47104\n\t" \
  "s_sub_u32 %[iss], %[iss], vcc_lo\n\t" \
  "v_mfma_f32_16x16x32_bf16 v[140:143], v[124:127], v[72:75], v[140:143]\n\t" \
  "ds_read_b64_tr_b16 v[120:121], v162 offset:45056\n\t" \
  "v_mfma_f32_16x16x32_bf16 v[156:159], v[124:127], v[76:79], v[156:159]\n\t" \
  "ds_read_b64_tr_b16 v[122:123], v162 offset:47104\n\t" \
  "s_add_u32 %[th], %[th], 1\n\t" \
  "s_cbranch_vccz QF5_%=\n\t" \
  "s_waitcnt vmcnt(16)\n\t" \
  "QG5_%=:\n\t" \
  "s_waitcnt lgkmcnt(11)\n\t" \
  "v_mfma_f32_16x16x32_bf16 v[128:131], v[96:99], v[80:83], v[128:131]\n\t" \
  "ds_read_b64_tr_b16 v[124:125], v163 offset:45056\n\t" \
  "v_mfma_f32_16x16x32_bf16 v[144:147], v[96:99], v[84:87], v[144:147]\n\t" \
  "ds_read_b64_tr_b16 v[126:127], v163 offset:47104\n\t" \
  "s_waitcnt lgkmcnt(7)\n\t" \
  "v_cmp_lt_u32 vcc, %[th], v166\n\t" \
  "v_mfma_f32_16x16x32_bf16 v[132:135], v[100:103], v[80:83], v[132:135]\n\t" \
  "s_mov_b64 exec, vcc\n\t" \
  "global_load_dwordx4 v[64:67], v164, %[ob] offset:0 sc1\n\t" \
  "s_mov_b64 exec, -1\n\t" \
  "v_mfma_f32_16x16x32_bf16 v[148:151], v[100:103], v[84:87], v[148:151]\n\t" \
  "ds_read_b64_tr_b16 v[96:97], v160 offset:49152\n\t" \
  "ds_read_b64_tr_b16 v[98:99], v160 offset:51200\n\t" \
  "v_mfma_f32_16x16x32_bf16 v[136:139], v[104:107], v[80:83], v[136:139]\n\t" \
  "s_mov_b64 exec, vcc\n\t" \
  "global_load_dwordx4 v[68:71], v164, %[ob] offset:1024 sc1\n\t" \
  "s_mov_b64 exec, -1\n\t" \
  "v_mfma_f32_16x16x32_bf16 v[152:155], v[104:107], v[84:87], v[152:155]\n\t" \
  "ds_read_b64_tr_b16 v[100:101], v161 offset:49152\n\t" \
  "ds_read_b64_tr_b16 v[102:103], v161 offset:51200\n\t" \
  "v_mfma_f32_16x16x32_bf16 v[140:143], v[108:111], v[80:83], v[140:143]\n\t" \
  "s_mov_b64 exec, vcc\n\t" \
  "global_load_dwordx4 v[72:75], v164, %[ob] offset:2048 sc1\n\t" \
  "s_mov_b64 exec, -1\n\t" \
  "v_mfma_f32_16x16x32_bf16 v[156:159], v[108:111], v[84:87], v[156:159]\n\t" \
  "ds_read_b64_tr_b16 v[104:105], v162 offset:49152\n\t" \
  "ds_read_b64_tr_b16 v[106:107], v162 offset:51200\n\t" \
  "s_waitcnt lgkmcnt(10)\n\t" \
  "v_mfma_f32_16x16x32_bf16 v[128:131], v[112:115], v[88:91], v[128:131]\n\t" \
  "s_mov_b64 exec, vcc\n\t" \
  "global_load_dwordx4 v[76:79], v164, %[ob] offset:3072 sc1\n\t" \
  "s_mov_b64 exec, -1\n\t" \
  "v_mfma_f32_16x16x32_bf16 v[144:147], v[112:115], v[92:95], v[144:147]\n\t" \
  "ds_read_b64_tr_b16 v[108:109], v163 offset:49152\n\t" \
  "ds_read_b64_tr_b16 v[110:111], v163 offset:51200\n\t" \
  "v_mfma_f32_16x16x32_bf16 v[132:135], v[116:119], v[88:91], v[132:135]\n\t" \
  "ds_read_b64_tr_b16 v[112:113], v160 offset:53248\n\t" \
  "s_and_b32 %[t0], vcc_lo, 0x4000\n\t" \
  "ds_read_b32 v166, v165\n\t" \
  "v_mfma_f32_16x16x32_bf16 v[148:151], v[116:119], v[92:95], v[148:151]\n\t" \
  "ds_read_b64_tr_b16 v[114:115], v160 offset:55296\n\t" \
  "v_add_u32 v164, %[t0], v164\n\t" \
  "s_waitcnt lgkmcnt(11)\n\t" \
  "v_mfma_f32_16x16x32_bf16 v[136:139], v[120:123], v[88:91], v[136:139]\n\t" \
  "ds_read_b64_tr_b16 v[116:117], v161 offset:53248\n\t" \
  "v_and_b32 v164, 0x3ffff, v164\n\t" \
  "v_mfma_f32_16x16x32_bf16 v[152:155], v[120:123], v[92:95], v[152:155]\n\t" \
  "ds_read_b64_tr_b16 v[118:119], v161 offset:55296\n\t" \
  "s_sub_u32 %[iss], %[iss], vcc_lo\n\t" \
  "v_mfma_f32_16x16x32_bf16 v[140:143], v[124:127], v[88:91], v[140:143]\n\t" \
  "ds_read_b64_tr_b16 v[120:121], v162 offset:53248\n\t" \
  "v_mfma_f32_16x16x32_bf16 v[156:159], v[124:127], v[92:95], v[156:159]\n\t" \
  "ds_read_b64_tr_b16 v[122:123], v162 offset:55296\n\t" \
  "s_add_u32 %[th], %[th], 1\n\t" \
  "s_cbranch_vccz QF6_%=\n\t" \
  "s_waitcnt vmcnt(16)\n\t" \
  "QG6_%=:\n\t" \
  "s_waitcnt lgkmcnt(11)\n\t" \
  "v_mfma_f32_16x16x32_bf16 v[128:131], v[96:99], v[0:3], v[128:131]\n\t" \
  "ds_read_b64_tr_b16 v[124:125], v163 offset:53248\n\t" \
  "v_mfma_f32_16x16x32_bf16 v[144:147], v[96:99], v[4:7], v[144:147]\n\t" \
  "ds_read_b64_tr_b16 v[126:127], v163 offset:55296\n\t" \
  "s_waitcnt lgkmcnt(7)\n\t" \
  "v_cmp_lt_u32 vcc, %[th], v166\n\t" \
  "v_mfma_f32_16x16x32_bf16 v[132:135], v[100:103], v[0:3], v[132:135]\n\t" \
  "s_mov_b64 exec, vcc\n\t" \
  "global_load_dwordx4 v[80:83], v164, %[ob] offset:0 sc1\n\t" \
  "s_mov_b64 exec, -1\n\t" \
  "v_mfma_f32_16x16x32_bf16 v[148:151], v[100:103], v[4:7], v[148:151]\n\t" \
  "ds_read_b64_tr_b16 v[96:97], v160 offset:57344\n\t" \
  "ds_read_b64_tr_b16 v[98:99], v160 offset:59392\n\t" \
  "v_mfma_f32_16x16x32_bf16 v[136:139], v[104:107], v[0:3], v[136:139]\n\t" \
  "s_mov_b64 exec, vcc\n\t" \
  "global_load_dwordx4 v[84:87], v164, %[ob] offset:1024 sc1\n\t" \
  "s_mov_b64 exec, -1\n\t" \
  "v_mfma_f32_16x16x32_bf16 v[152:155], v[104:107], v[4:7], v[152:155]\n\t" \
  "ds_read_b64_tr_b16 v[100:101], v161 offset:57344\n\t" \
  "ds_read_b64_tr_b16 v[102:103], v161 offset:59392\n\t" \
  "v_mfma_f32_16x16x32_bf16 v[140:143], v[108:111], v[0:3], v[140:143]\n\t" \
  "s_mov_b64 exec, vcc\n\t" \
  "global_load_dwordx4 v[88:91], v164, %[ob] offset:2048 sc1\n\t" \
  "s_mov_b64 exec, -1\n\t" \
  "v_mfma_f32_16x16x32_bf16 v[156:159], v[108:111], v[4:7], v[156:159]\n\t" \
  "ds_read_b64_tr_b16 v[104:105], v162 offset:57344\n\t" \
  "ds_read_b64_tr_b16 v[106:107], v162 offset:59392\n\t" \
  "s_waitcnt lgkmcnt(10)\n\t" \
  "v_mfma_f32_16x16x32_bf16 v[128:131], v[112:115], v[8:11], v[128:131]\n\t" \
  "s_mov_b64 exec, vcc\n\t" \
  "global_load_dwordx4 v[92:95], v164, %[ob] offset:3072 sc1\n\t" \
  "s_mov_b64 exec, -1\n\t" \
  "v_mfma_f32_16x16x32_bf16 v[144:147], v[112:115], v[12:15], v[144:147]\n\t" \
  "ds_read_b64_tr_b16 v[108:109], v163 offset:57344\n\t" \
  "ds_read_b64_tr_b16 v[110:111], v163 offset:59392\n\t" \
  "v_mfma_f32_16x16x32_bf16 v[132:135], v[116:119], v[8:11], v[132:135]\n\t" \
  "ds_read_b64_tr_b16 v[112:113], v160 offset:61440\n\t" \
  "s_and_b32 %[t0], vcc_lo, 0x4000\n\t" \
  "ds_read_b32 v166, v165\n\t" \
  "v_mfma_f32_16x16x32_bf16 v[148:151], v[116:119], v[12:15], v[148:151]\n\t" \
  "ds_read_b64_tr_b16 v[114:115], v160 offset:63488\n\t" \
  "v_add_u32 v164, %[t0], v164\n\t" \
  "s_waitcnt lgkmcnt(11)\n\t" \
  "v_mfma_f32_16x16x32_bf16 v[136:139], v[120:123], v[8:11], v[136:139]\n\t" \
  "ds_read_b64_tr_b16 v[116:117], v161 offset:61440\n\t" \
  "v_and_b32 v164, 0x3ffff, v164\n\t" \
  "v_mfma_f32_16x16x32_bf16 v[152:155], v[120:123], v[12:15], v[152:155]\n\t" \
  "ds_read_b64_tr_b16 v[118:119], v161 offset:63488\n\t" \
  "s_sub_u32 %[iss], %[iss], vcc_lo\n\t" \
  "v_mfma_f32_16x16x32_bf16 v[140:143], v[124:127], v[8:11], v[140:143]\n\t" \
  "ds_read_b64_tr_b16 v[120:121], v162 offset:61440\n\t" \
  "v_mfma_f32_16x16x32_bf16 v[156:159], v[124:127], v[12:15], v[156:159]\n\t" \
  "ds_read_b64_tr_b16 v[122:123], v162 offset:63488\n\t" \
  "s_add_u32 %[th], %[th], 1\n\t" \
  "s_cbranch_vccz QF7_%=\n\t" \
  "s_waitcnt vmcnt(16)\n\t" \
  "QG7_%=:\n\t" \
  "s_waitcnt lgkmcnt(11)\n\t" \
  "v_mfma_f32_16x16x32_bf16 v[128:131], v[96:99], v[16:19], v[128:131]\n\t" \
  "ds_read_b64_tr_b16 v[124:125], v163 offset:61440\n\t" \
  "v_mfma_f32_16x16x32_bf16 v[144:147], v[96:99], v[20:23], v[144:147]\n\t" \
  "ds_read_b64_tr_b16 v[126:127], v163 offset:63488\n\t" \
  "s_waitcnt lgkmcnt(7)\n\t" \
  "v_cmp_lt_u32 vcc, %[th], v166\n\t" \
  "v_mfma_f32_16x16x32_bf16 v[132:135], v[100:103], v[16:19], v[132:135]\n\t" \
  "v_add_u32 v160, 0x10000, v160\n\t" \
  "s_mov_b64 exec, vcc\n\t" \
  "global_load_dwordx4 v[0:3], v164, %[ob] offset:0 sc1\n\t" \
  "s_mov_b64 exec, -1\n\t" \
  "v_mfma_f32_16x16x32_bf16 v[148:151], v[100:103], v[20:23], v[148:151]\n\t" \
  "ds_read_b64_tr_b16 v[96:97], v160 offset:0\n\t" \
  "ds_read_b64_tr_b16 v[98:99], v160 offset:2048\n\t" \
  "v_mfma_f32_16x16x32_bf16 v[136:139], v[104:107], v[16:19], v[136:139]\n\t" \
  "v_add_u32 v161, 0x10000, v161\n\t" \
  "s_mov_b64 exec, vcc\n\t" \
  "global_load_dwordx4 v[4:7], v164, %[ob] offset:1024 sc1\n\t" \
  "s_mov_b64 exec, -1\n\t" \
  "v_mfma_f32_16x16x32_bf16 v[152:155], v[104:107], v[20:23], v[152:155]\n\t" \
  "ds_read_b64_tr_b16 v[100:101], v161 offset:0\n\t" \
  "ds_read_b64_tr_b16 v[102:103], v161 offset:2048\n\t" \
  "v_mfma_f32_16x16x32_bf16 v[140:143], v[108:111], v[16:19], v[140:143]\n\t" \
  "v_add_u32 v162, 0x10000, v162\n\t" \
  "s_mov_b64 exec, vcc\n\t" \
  "global_load_dwordx4 v[8:11], v164, %[ob] offset:2048 sc1\n\t" \
  "s_mov_b64 exec, -1\n\t" \
  "v_mfma_f32_16x16x32_bf16 v[156:159], v[108:111], v[20:23], v[156:159]\n\t" \
  "ds_read_b64_tr_b16 v[104:105], v162 offset:0\n\t" \
  "ds_read_b64_tr_b16 v[106:107], v162 offset:2048\n\t" \
  "s_waitcnt lgkmcnt(10)\n\t" \
  "v_mfma_f32_16x16x32_bf16 v[128:131], v[112:115], v[24:27], v[128:131]\n\t" \
  "v_add_u32 v163, 0x10000, v163\n\t" \
  "s_mov_b64 exec, vcc\n\t" \
  "global_load_dwordx4 v[12:15], v164, %[ob] offset:3072 sc1\n\t" \
  "s_mov_b64 exec, -1\n\t" \
  "v_mfma_f32_16x16x32_bf16 v[144:147], v[112:115], v[28:31], v[144:147]\n\t" \
  "ds_read_b64_tr_b16 v[108:109], v163 offset:0\n\t" \
  "ds_read_b64_tr_b16 v[110:111], v163 offset:2048\n\t" \
  "v_mfma_f32_16x16x32_bf16 v[132:135], v[116:119], v[24:27], v[132:135]\n\t" \
  "ds_read_b64_tr_b16 v[112:113], v160 offset:4096\n\t" \
  "s_and_b32 %[t0], vcc_lo, 0x4000\n\t" \
  "ds_read_b32 v166, v165\n\t" \
  "v_mfma_f32_16x16x32_bf16 v[148:151], v[116:119], v[28:31], v[148:151]\n\t" \
  "ds_read_b64_tr_b16 v[114:115], v160 offset:6144\n\t" \
  "v_add_u32 v164, %[t0], v164\n\t" \
  "s_waitcnt lgkmcnt(11)\n\t" \
  "v_mfma_f32_16x16x32_bf16 v[136:139], v[120:123], v[24:27], v[136:139]\n\t" \
  "ds_read_b64_tr_b16 v[116:117], v161 offset:4096\n\t" \
  "v_and_b32 v164, 0x3ffff, v164\n\t" \
  "v_mfma_f32_16x16x32_bf16 v[152:155], v[120:123], v[28:31], v[152:155]\n\t" \
  "ds_read_b64_tr_b16 v[118:119], v161 offset:6144\n\t" \
  "s_sub_u32 %[iss], %[iss], vcc_lo\n\t" \
  "v_mfma_f32_16x16x32_bf16 v[140:143], v[124:127], v[24:27], v[140:143]\n\t" \
  "ds_read_b64_tr_b16 v[120:121], v162 offset:4096\n\t" \
  "v_mfma_f32_16x16x32_bf16 v[156:159], v[124:127], v[28:31], v[156:159]\n\t" \
  "ds_read_b64_tr_b16 v[122:123], v162 offset:6144\n\t" \
  "s_add_u32 %[th], %[th], 1\n\t" \
  "s_cbranch_vccz QF8_%=\n\t" \
  "s_waitcnt vmcnt(16)\n\t" \
  "QG8_%=:\n\t" \
  "s_waitcnt lgkmcnt(11)\n\t" \
  "v_mfma_f32_16x16x32_bf16 v[128:131], v[96:99], v[32:35], v[128:131]\n\t" \
  "ds_read_b64_tr_b16 v[124:125], v163 offset:4096\n\t" \
  "v_mfma_f32_16x16x32_bf16 v[144:147], v[96:99], v[36:39], v[144:147]\n\t" \
  "ds_read_b64_tr_b16 v[126:127], v163 offset:6144\n\t" \
  "s_waitcnt lgkmcnt(7)\n\t" \
  "v_cmp_lt_u32 vcc, %[th], v166\n\t" \
  "v_mfma_f32_16x16x32_bf16 v[132:135], v[100:103], v[32:35], v[132:135]\n\t" \
  "s_mov_b64 exec, vcc\n\t" \
  "global_load_dwordx4 v[16:19], v164, %[ob] offset:0 sc1\n\t" \
  "s_mov_b64 exec, -1\n\t" \
  "v_mfma_f32_16x16x32_bf16 v[148:151], v[100:103], v[36:39], v[148:151]\n\t" \
  "ds_read_b64_tr_b16 v[96:97], v160 offset:8192\n\t" \
  "ds_read_b64_tr_b16 v[98:99], v160 offset:10240\n\t" \
  "v_mfma_f32_16x16x32_bf16 v[136:139], v[104:107], v[32:35], v[136:139]\n\t" \
  "s_mov_b64 exec, vcc\n\t" \
  "global_load_dwordx4 v[20:23], v164, %[ob] offset:1024 sc1\n\t" \
  "s_mov_b64 exec, -1\n\t" \
  "v_mfma_f32_16x16x32_bf16 v[152:155], v[104:107], v[36:39], v[152:155]\n\t" \
  "ds_read_b64_tr_b16 v[100:101], v161 offset:8192\n\t" \
  "ds_read_b64_tr_b16 v[102:103], v161 offset:10240\n\t" \
  "v_mfma_f32_16x16x32_bf16 v[140:143], v[108:111], v[32:35], v[140:143]\n\t" \
  "s_mov_b64 exec, vcc\n\t" \
  "global_load_dwordx4 v[24:27], v164, %[ob] offset:2048 sc1\n\t" \
  "s_mov_b64 exec, -1\n\t" \
  "v_mfma_f32_16x16x32_bf16 v[156:159], v[108:111], v[36:39], v[156:159]\n\t" \
  "ds_read_b64_tr_b16 v[104:105], v162 offset:8192\n\t" \
  "ds_read_b64_tr_b16 v[106:107], v162 offset:10240\n\t" \
  "s_waitcnt lgkmcnt(10)\n\t" \
  "v_mfma_f32_16x16x32_bf16 v[128:131], v[112:115], v[40:43], v[128:131]\n\t" \
  "s_mov_b64 exec, vcc\n\t" \
  "global_load_dwordx4 v[28:31], v164, %[ob] offset:3072 sc1\n\t" \
  "s_mov_b64 exec, -1\n\t" \
  "v_mfma_f32_16x16x32_bf16 v[144:147], v[112:115], v[44:47], v[144:147]\n\t" \
  "ds_read_b64_tr_b16 v[108:109], v163 offset:8192\n\t" \
  "ds_read_b64_tr_b16 v[110:111], v163 offset:10240\n\t" \
  "v_mfma_f32_16x16x32_bf16 v[132:135], v[116:119], v[40:43], v[132:135]\n\t" \
  "ds_read_b64_tr_b16 v[112:113], v160 offset:12288\n\t" \
  "s_and_b32 %[t0], vcc_lo, 0x4000\n\t" \
  "ds_read_b32 v166, v165\n\t" \
  "v_mfma_f32_16x16x32_bf16 v[148:151], v[116:119], v[44:47], v[148:151]\n\t" \
  "ds_read_b64_tr_b16 v[114:115], v160 offset:14336\n\t" \
  "v_add_u32 v164, %[t0], v164\n\t" \
  "s_waitcnt lgkmcnt(11)\n\t" \
  "v_mfma_f32_16x16x32_bf16 v[136:139], v[120:123], v[40:43], v[136:139]\n\t" \
  "ds_read_b64_tr_b16 v[116:117], v161 offset:12288\n\t" \
  "v_and_b32 v164, 0x3ffff, v164\n\t" \
  "v_mfma_f32_16x16x32_bf16 v[152:155], v[120:123], v[44:47], v[152:155]\n\t" \
  "ds_read_b64_tr_b16 v[118:119], v161 offset:14336\n\t" \
  "s_sub_u32 %[iss], %[iss], vcc_lo\n\t" \
  "v_mfma_f32_16x16x32_bf16 v[140:143], v[124:127], v[40:43], v[140:143]\n\t" \
  "ds_read_b64_tr_b16 v[120:121], v162 offset:12288\n\t" \
  "v_mfma_f32_16x16x32_bf16 v[156:159], v[124:127], v[44:47], v[156:159]\n\t" \
  "ds_read_b64_tr_b16 v[122:123], v162 offset:14336\n\t" \
  "s_add_u32 %[th], %[th], 1\n\t" \
  "s_cbranch_vccz QF9_%=\n\t" \
  "s_waitcnt vmcnt(16)\n\t" \
  "QG9_%=:\n\t" \
  "s_waitcnt lgkmcnt(11)\n\t" \
  "v_mfma_f32_16x16x32_bf16 v[128:131], v[96:99], v[48:51], v[128:131]\n\t" \
  "ds_read_b64_tr_b16 v[124:125], v163 offset:12288\n\t" \
  "v_mfma_f32_16x16x32_bf16 v[144:147], v[96:99], v[52:55], v[144:147]\n\t" \
  "ds_read_b64_tr_b16 v[126:127], v163 offset:14336\n\t" \
  "s_waitcnt lgkmcnt(7)\n\t" \
  "v_cmp_lt_u32 vcc, %[th], v166\n\t" \
  "v_mfma_f32_16x16x32_bf16 v[132:135], v[100:103], v[48:51], v[132:135]\n\t" \
  "s_mov_b64 exec, vcc\n\t" \
  "global_load_dwordx4 v[32:35], v164, %[ob] offset:0 sc1\n\t" \
  "s_mov_b64 exec, -1\n\t" \
  "v_mfma_f32_16x16x32_bf16 v[148:151], v[100:103], v[52:55], v[148:151]\n\t" \
  "ds_read_b64_tr_b16 v[96:97], v160 offset:16384\n\t" \
  "ds_read_b64_tr_b16 v[98:99], v160 offset:18432\n\t" \
  "v_mfma_f32_16x16x32_bf16 v[136:139], v[104:107], v[48:51], v[136:139]\n\t" \
  "s_mov_b64 exec, vcc\n\t" \
  "global_load_dwordx4 v[36:39], v164, %[ob] offset:1024 sc1\n\t" \
  "s_mov_b64 exec, -1\n\t" \
  "v_mfma_f32_16x16x32_bf16 v[152:155], v[104:107], v[52:55], v[152:155]\n\t" \
  "ds_read_b64_tr_b16 v[100:101], v161 offset:16384\n\t" \
  "ds_read_b64_tr_b16 v[102:103], v161 offset:18432\n\t" \
  "v_mfma_f32_16x16x32_bf16 v[140:143], v[108:111], v[48:51], v[140:143]\n\t" \
  "s_mov_b64 exec, vcc\n\t" \
  "global_load_dwordx4 v[40:43], v164, %[ob] offset:2048 sc1\n\t" \
  "s_mov_b64 exec, -1\n\t" \
  "v_mfma_f32_16x16x32_bf16 v[156:159], v[108:111], v[52:55], v[156:159]\n\t" \
  "ds_read_b64_tr_b16 v[104:105], v162 offset:16384\n\t" \
  "ds_read_b64_tr_b16 v[106:107], v162 offset:18432\n\t" \
  "s_waitcnt lgkmcnt(10)\n\t" \
  "v_mfma_f32_16x16x32_bf16 v[128:131], v[112:115], v[56:59], v[128:131]\n\t" \
  "s_mov_b64 exec, vcc\n\t" \
  "global_load_dwordx4 v[44:47], v164, %[ob] offset:3072 sc1\n\t" \
  "s_mov_b64 exec, -1\n\t" \
  "v_mfma_f32_16x16x32_bf16 v[144:147], v[112:115], v[60:63], v[144:147]\n\t" \
  "ds_read_b64_tr_b16 v[108:109], v163 offset:16384\n\t" \
  "ds_read_b64_tr_b16 v[110:111], v163 offset:18432\n\t" \
  "v_mfma_f32_16x16x32_bf16 v[132:135], v[116:119], v[56:59], v[132:135]\n\t" \
  "ds_read_b64_tr_b16 v[112:113], v160 offset:20480\n\t" \
  "s_and_b32 %[t0], vcc_lo, 0x4000\n\t" \
  "ds_read_b32 v166, v165\n\t" \
  "v_mfma_f32_16x16x32_bf16 v[148:151], v[116:119], v[60:63], v[148:151]\n\t" \
  "ds_read_b64_tr_b16 v[114:115], v160 offset:22528\n\t" \
  "v_add_u32 v164, %[t0], v164\n\t" \
  "s_waitcnt lgkmcnt(11)\n\t" \
  "v_mfma_f32_16x16x32_bf16 v[136:139], v[120:123], v[56:59], v[136:139]\n\t" \
  "ds_read_b64_tr_b16 v[116:117], v161 offset:20480\n\t" \
  "v_and_b32 v164, 0x3ffff, v164\n\t" \
  "v_mfma_f32_16x16x32_bf16 v[152:155], v[120:123], v[60:63], v[152:155]\n\t" \
  "ds_read_b64_tr_b16 v[118:119], v161 offset:22528\n\t" \
  "s_sub_u32 %[iss], %[iss], vcc_lo\n\t" \
  "v_mfma_f32_16x16x32_bf16 v[140:143], v[124:127], v[56:59], v[140:143]\n\t" \
  "ds_read_b64_tr_b16 v[120:121], v162 offset:20480\n\t" \
  "s_mov_b64 exec, 1\n\t" \
  "ds_write_b32 v165, v165 offset:76\n\t" \
  "s_mov_b64 exec, -1\n\t" \
  "v_mfma_f32_16x16x32_bf16 v[156:159], v[124:127], v[60:63], v[156:159]\n\t" \
  "ds_read_b64_tr_b16 v[122:123], v162 offset:22528\n\t" \
  "s_add_u32 %[th], %[th], 1\n\t" \
  "s_cbranch_vccz QF10_%=\n\t" \
  "s_waitcnt vmcnt(16)\n\t" \
  "QG10_%=:\n\t" \
  "s_waitcnt lgkmcnt(12)\n\t" \
  "v_mfma_f32_16x16x32_bf16 v[128:131], v[96:99], v[64:67], v[128:131]\n\t" \
  "ds_read_b64_tr_b16 v[124:125], v163 offset:20480\n\t" \
  "v_mfma_f32_16x16x32_bf16 v[144:147], v[96:99], v[68:71], v[144:147]\n\t" \
  "ds_read_b64_tr_b16 v[126:127], v163 offset:22528\n\t" \
  "s_waitcnt lgkmcnt(8)\n\t" \
  "v_cmp_lt_u32 vcc, %[th], v166\n\t" \
  "v_mfma_f32_16x16x32_bf16 v[132:135], v[100:103], v[64:67], v[132:135]\n\t" \
  "s_mov_b64 exec, vcc\n\t" \
  "global_load_dwordx4 v[48:51], v164, %[ob] offset:0 sc1\n\t" \
  "s_mov_b64 exec, -1\n\t" \
  "v_mfma_f32_16x16x32_bf16 v[148:151], v[100:103], v[68:71], v[148:151]\n\t" \
  "ds_read_b64_tr_b16 v[96:97], v160 offset:24576\n\t" \
  "ds_read_b64_tr_b16 v[98:99], v160 offset:26624\n\t" \
  "v_mfma_f32_16x16x32_bf16 v[136:139], v[104:107], v[64:67], v[136:139]\n\t" \
  "s_mov_b64 exec, vcc\n\t" \
  "global_load_dwordx4 v[52:55], v164, %[ob] offset:1024 sc1\n\t" \
  "s_mov_b64 exec, -1\n\t" \
  "v_mfma_f32_16x16x32_bf16 v[152:155], v[104:107], v[68:71], v[152:155]\n\t" \
  "ds_read_b64_tr_b16 v[100:101], v161 offset:24576\n\t" \
  "ds_read_b64_tr_b16 v[102:103], v161 offset:26624\n\t" \
  "v_mfma_f32_16x16x32_bf16 v[140:143], v[108:111], v[64:67], v[140:143]\n\t" \
  "s_mov_b64 exec, vcc\n\t" \
  "global_load_dwordx4 v[56:59], v164, %[ob] offset:2048 sc1\n\t" \
  "s_mov_b64 exec, -1\n\t" \
  "v_mfma_f32_16x16x32_bf16 v[156:159], v[108:111], v[68:71], v[156:159]\n\t" \
  "ds_read_b64_tr_b16 v[104:105], v162 offset:24576\n\t" \
  "ds_read_b64_tr_b16 v[106:107], v162 offset:26624\n\t" \
  "s_waitcnt lgkmcnt(11)\n\t" \
  "v_mfma_f32_16x16x32_bf16 v[128:131], v[112:115], v[72:75], v[128:131]\n\t" \
  "s_mov_b64 exec, vcc\n\t" \
  "global_load_dwordx4 v[60:63], v164, %[ob] offset:3072 sc1\n\t" \
  "s_mov_b64 exec, -1\n\t" \
  "v_mfma_f32_16x16x32_bf16 v[144:147], v[112:115], v[76:79], v[144:147]\n\t" \
  "ds_read_b64_tr_b16 v[108:109], v163 offset:24576\n\t" \
  "ds_read_b64_tr_b16 v[110:111], v163 offset:26624\n\t" \
  "v_mfma_f32_16x16x32_bf16 v[132:135], v[116:119], v[72:75], v[132:135]\n\t" \
  "ds_read_b64_tr_b16 v[112:113], v160 offset:28672\n\t" \
  "s_and_b32 %[t0], vcc_lo, 0x4000\n\t" \
  "v_mfma_f32_16x16x32_bf16 v[148:151], v[116:119], v[76:79], v[148:151]\n\t" \
  "ds_read_b64_tr_b16 v[114:115], v160 offset:30720\n\t" \
  "v_add_u32 v164, %[t0], v164\n\t" \
  "s_waitcnt lgkmcnt(10)\n\t" \
  "v_mfma_f32_16x16x32_bf16 v[136:139], v[120:123], v[72:75], v[136:139]\n\t" \
  "ds_read_b64_tr_b16 v[116:117], v161 offset:28672\n\t" \
  "v_and_b32 v164, 0x3ffff, v164\n\t" \
  "v_mfma_f32_16x16x32_bf16 v[152:155], v[120:123], v[76:79], v[152:155]\n\t" \
  "ds_read_b64_tr_b16 v[118:119], v161 offset:30720\n\t" \
  "s_sub_u32 %[iss], %[iss], vcc_lo\n\t" \
  "v_mfma_f32_16x16x32_bf16 v[140:143], v[124:127], v[72:75], v[140:143]\n\t" \
  "ds_read_b64_tr_b16 v[120:121], v162 offset:28672\n\t" \
  "v_mfma_f32_16x16x32_bf16 v[156:159], v[124:127], v[76:79], v[156:159]\n\t" \
  "ds_read_b64_tr_b16 v[122:123], v162 offset:30720\n\t" \
  "s_add_u32 %[th], %[th], 1\n\t" \
  "s_cbranch_vccz QF11_%=\n\t" \
  "s_waitcnt vmcnt(16)\n\t" \
  "QG11_%=:\n\t" \
  "s_waitcnt lgkmcnt(10)\n\t" \
  "v_mfma_f32_16x16x32_bf16 v[128:131], v[96:99], v[80:83], v[128:131]\n\t" \
  "ds_read_b64_tr_b16 v[124:125], v163 offset:28672\n\t" \
  "v_mfma_f32_16x16x32_bf16 v[144:147], v[96:99], v[84:87], v[144:147]\n\t" \
  "ds_read_b64_tr_b16 v[126:127], v163 offset:30720\n\t" \
  "v_mfma_f32_16x16x32_bf16 v[132:135], v[100:103], v[80:83], v[132:135]\n\t" \
  "ds_read_b64_tr_b16 v[96:97], v160 offset:32768\n\t" \
  "v_mfma_f32_16x16x32_bf16 v[148:151], v[100:103], v[84:87], v[148:151]\n\t" \
  "ds_read_b64_tr_b16 v[98:99], v160 offset:34816\n\t" \
  "s_waitcnt lgkmcnt(10)\n\t" \
  "v_mfma_f32_16x16x32_bf16 v[136:139], v[104:107], v[80:83], v[136:139]\n\t" \
  "ds_read_b64_tr_b16 v[100:101], v161 offset:32768\n\t" \
  "v_mfma_f32_16x16x32_bf16 v[152:155], v[104:107], v[84:87], v[152:155]\n\t" \
  "ds_read_b64_tr_b16 v[102:103], v161 offset:34816\n\t" \
  "v_mfma_f32_16x16x32_bf16 v[140:143], v[108:111], v[80:83], v[140:143]\n\t" \
  "ds_read_b64_tr_b16 v[104:105], v162 offset:32768\n\t" \
  "v_mfma_f32_16x16x32_bf16 v[156:159], v[108:111], v[84:87], v[156:159]\n\t" \
  "ds_read_b64_tr_b16 v[106:107], v162 offset:34816\n\t" \
  "s_waitcnt lgkmcnt(10)\n\t" \
  "v_mfma_f32_16x16x32_bf16 v[128:131], v[112:115], v[88:91], v[128:131]\n\t" \
  "ds_read_b64_tr_b16 v[108:109], v163 offset:32768\n\t" \
  "v_mfma_f32_16x16x32_bf16 v[144:147], v[112:115], v[92:95], v[144:147]\n\t" \
  "ds_read_b64_tr_b16 v[110:111], v163 offset:34816\n\t" \
  "v_mfma_f32_16x16x32_bf16 v[132:135], v[116:119], v[88:91], v[132:135]\n\t" \
  "ds_read_b64_tr_b16 v[112:113], v160 offset:36864\n\t" \
  "v_mfma_f32_16x16x32_bf16 v[148:151], v[116:119], v[92:95], v[148:151]\n\t" \
  "ds_read_b64_tr_b16 v[114:115], v160 offset:38912\n\t" \
  "s_waitcnt lgkmcnt(10)\n\t" \
  "v_mfma_f32_16x16x32_bf16 v[136:139], v[120:123], v[88:91], v[136:139]\n\t" \
  "ds_read_b64_tr_b16 v[116:117], v161 offset:36864\n\t" \
  "v_mfma_f32_16x16x32_bf16 v[152:155], v[120:123], v[92:95], v[152:155]\n\t" \
  "ds_read_b64_tr_b16 v[118:119], v161 offset:38912\n\t" \
  "v_mfma_f32_16x16x32_bf16 v[140:143], v[124:127], v[88:91], v[140:143]\n\t" \
  "ds_read_b64_tr_b16 v[120:121], v162 offset:36864\n\t" \
  "v_mfma_f32_16x16x32_bf16 v[156:159], v[124:127], v[92:95], v[156:159]\n\t" \
  "ds_read_b64_tr_b16 v[122:123], v162 offset:38912\n\t" \
  "s_cbranch_vccz QF12_%=\n\t" \
  "s_waitcnt vmcnt(12)\n\t" \
  "QG12_%=:\n\t" \
  "s_waitcnt lgkmcnt(10)\n\t" \
  "v_mfma_f32_16x16x32_bf16 v[128:131], v[96:99], v[0:3], v[128:131]\n\t" \
  "ds_read_b64_tr_b16 v[124:125], v163 offset:36864\n\t" \
  "v_mfma_f32_16x16x32_bf16 v[144:147], v[96:99], v[4:7], v[144:147]\n\t" \
  "ds_read_b64_tr_b16 v[126:127], v163 offset:38912\n\t" \
  "v_mfma_f32_16x16x32_bf16 v[132:135], v[100:103], v[0:3], v[132:135]\n\t" \
  "ds_read_b64_tr_b16 v[96:97], v160 offset:40960\n\t" \
  "v_mfma_f32_16x16x32_bf16 v[148:151], v[100:103], v[4:7], v[148:151]\n\t" \
  "ds_read_b64_tr_b16 v[98:99], v160 offset:43008\n\t" \
  "s_waitcnt lgkmcnt(10)\n\t" \
  "v_mfma_f32_16x16x32_bf16 v[136:139], v[104:107], v[0:3], v[136:139]\n\t" \
  "ds_read_b64_tr_b16 v[100:101], v161 offset:40960\n\t" \
  "v_mfma_f32_16x16x32_bf16 v[152:155], v[104:107], v[4:7], v[152:155]\n\t" \
  "ds_read_b64_tr_b16 v[102:103], v161 offset:43008\n\t" \
  "v_mfma_f32_16x16x32_bf16 v[140:143], v[108:111], v[0:3], v[140:143]\n\t" \
  "ds_read_b64_tr_b16 v[104:105], v162 offset:40960\n\t" \
  "v_mfma_f32_16x16x32_bf16 v[156:159], v[108:111], v[4:7], v[156:159]\n\t" \
  "ds_read_b64_tr_b16 v[106:107], v162 offset:43008\n\t" \
  "s_waitcnt lgkmcnt(10)\n\t" \
  "v_mfma_f32_16x16x32_bf16 v[128:131], v[112:115], v[8:11], v[128:131]\n\t" \
  "ds_read_b64_tr_b16 v[108:109], v163 offset:40960\n\t" \
  "v_mfma_f32_16x16x32_bf16 v[144:147], v[112:115], v[12:15], v[144:147]\n\t" \
  "ds_read_b64_tr_b16 v[110:111], v163 offset:43008\n\t" \
  "v_mfma_f32_16x16x32_bf16 v[132:135], v[116:119], v[8:11], v[132:135]\n\t" \
  "ds_read_b64_tr_b16 v[112:113], v160 offset:45056\n\t" \
  "v_mfma_f32_16x16x32_bf16 v[148:151], v[116:119], v[12:15], v[148:151]\n\t" \
  "ds_read_b64_tr_b16 v[114:115], v160 offset:47104\n\t" \
  "s_waitcnt lgkmcnt(10)\n\t" \
  "v_mfma_f32_16x16x32_bf16 v[136:139], v[120:123], v[8:11], v[136:139]\n\t" \
  "ds_read_b64_tr_b16 v[116:117], v161 offset:45056\n\t" \
  "v_mfma_f32_16x16x32_bf16 v[152:155], v[120:123], v[12:15], v[152:155]\n\t" \
  "ds_read_b64_tr_b16 v[118:119], v161 offset:47104\n\t" \
  "v_mfma_f32_16x16x32_bf16 v[140:143], v[124:127], v[8:11], v[140:143]\n\t" \
  "ds_read_b64_tr_b16 v[120:121], v162 offset:45056\n\t" \
  "v_mfma_f32_16x16x32_bf16 v[156:159], v[124:127], v[12:15], v[156:159]\n\t" \
  "ds_read_b64_tr_b16 v[122:123], v162 offset:47104\n\t" \
  "s_cbranch_vccz QF13_%=\n\t" \
  "s_waitcnt vmcnt(8)\n\t" \
  "QG13_%=:\n\t" \
  "s_waitcnt lgkmcnt(10)\n\t" \
  "v_mfma_f32_16x16x32_bf16 v[128:131], v[96:99], v[16:19], v[128:131]\n\t" \
  "ds_read_b64_tr_b16 v[124:125], v163 offset:45056\n\t" \
  "v_mfma_f32_16x16x32_bf16 v[144:147], v[96:99], v[20:23], v[144:147]\n\t" \
  "ds_read_b64_tr_b16 v[126:127], v163 offset:47104\n\t" \
  "v_mfma_f32_16x16x32_bf16 v[132:135], v[100:103], v[16:19], v[132:135]\n\t" \
  "ds_read_b64_tr_b16 v[96:97], v160 offset:49152\n\t" \
  "v_mfma_f32_16x16x32_bf16 v[148:151], v[100:103], v[20:23], v[148:151]\n\t" \
  "ds_read_b64_tr_b16 v[98:99], v160 offset:51200\n\t" \
  "s_waitcnt lgkmcnt(10)\n\t" \
  "v_mfma_f32_16x16x32_bf16 v[136:139], v[104:107], v[16:19], v[136:139]\n\t" \
  "ds_read_b64_tr_b16 v[100:101], v161 offset:49152\n\t" \
  "v_mfma_f32_16x16x32_bf16 v[152:155], v[104:107], v[20:23], v[152:155]\n\t" \
  "ds_read_b64_tr_b16 v[102:103], v161 offset:51200\n\t" \
  "v_mfma_f32_16x16x32_bf16 v[140:143], v[108:111], v[16:19], v[140:143]\n\t" \
  "ds_read_b64_tr_b16 v[104:105], v162 offset:49152\n\t" \
  "v_mfma_f32_16x16x32_bf16 v[156:159], v[108:111], v[20:23], v[156:159]\n\t" \
  "ds_read_b64_tr_b16 v[106:107], v162 offset:51200\n\t" \
  "s_waitcnt lgkmcnt(10)\n\t" \
  "v_mfma_f32_16x16x32_bf16 v[128:131], v[112:115], v[24:27], v[128:131]\n\t" \
  "ds_read_b64_tr_b16 v[108:109], v163 offset:49152\n\t" \
  "v_mfma_f32_16x16x32_bf16 v[144:147], v[112:115], v[28:31], v[144:147]\n\t" \
  "ds_read_b64_tr_b16 v[110:111], v163 offset:51200\n\t" \
  "v_mfma_f32_16x16x32_bf16 v[132:135], v[116:119], v[24:27], v[132:135]\n\t" \
  "ds_read_b64_tr_b16 v[112:113], v160 offset:53248\n\t" \
  "v_mfma_f32_16x16x32_bf16 v[148:151], v[116:119], v[28:31], v[148:151]\n\t" \
  "ds_read_b64_tr_b16 v[114:115], v160 offset:55296\n\t" \
  "s_waitcnt lgkmcnt(10)\n\t" \
  "v_mfma_f32_16x16x32_bf16 v[136:139], v[120:123], v[24:27], v[136:139]\n\t" \
  "ds_read_b64_tr_b16 v[116:117], v161 offset:53248\n\t" \
  "v_mfma_f32_16x16x32_bf16 v[152:155], v[120:123], v[28:31], v[152:155]\n\t" \
  "ds_read_b64_tr_b16 v[118:119], v161 offset:55296\n\t" \
  "v_mfma_f32_16x16x32_bf16 v[140:143], v[124:127], v[24:27], v[140:143]\n\t" \
  "ds_read_b64_tr_b16 v[120:121], v162 offset:53248\n\t" \
  "v_mfma_f32_16x16x32_bf16 v[156:159], v[124:127], v[28:31], v[156:159]\n\t" \
  "ds_read_b64_tr_b16 v[122:123], v162 offset:55296\n\t" \
  "s_cbranch_vccz QF14_%=\n\t" \
  "s_waitcnt vmcnt(4)\n\t" \
  "QG14_%=:\n\t" \
  "s_waitcnt lgkmcnt(10)\n\t" \
  "v_mfma_f32_16x16x32_bf16 v[128:131], v[96:99], v[32:35], v[128:131]\n\t" \
  "ds_read_b64_tr_b16 v[124:125], v163 offset:53248\n\t" \
  "v_mfma_f32_16x16x32_bf16 v[144:147], v[96:99], v[36:39], v[144:147]\n\t" \
  "ds_read_b64_tr_b16 v[126:127], v163 offset:55296\n\t" \
  "v_mfma_f32_16x16x32_bf16 v[132:135], v[100:103], v[32:35], v[132:135]\n\t" \
  "ds_read_b64_tr_b16 v[96:97], v160 offset:57344\n\t" \
  "v_mfma_f32_16x16x32_bf16 v[148:151], v[100:103], v[36:39], v[148:151]\n\t" \
  "ds_read_b64_tr_b16 v[98:99], v160 offset:59392\n\t" \
  "s_waitcnt lgkmcnt(10)\n\t" \
  "v_mfma_f32_16x16x32_bf16 v[136:139], v[104:107], v[32:35], v[136:139]\n\t" \
  "ds_read_b64_tr_b16 v[100:101], v161 offset:57344\n\t" \
  "v_mfma_f32_16x16x32_bf16 v[152:155], v[104:107], v[36:39], v[152:155]\n\t" \
  "ds_read_b64_tr_b16 v[102:103], v161 offset:59392\n\t" \
  "v_mfma_f32_16x16x32_bf16 v[140:143], v[108:111], v[32:35], v[140:143]\n\t" \
  "ds_read_b64_tr_b16 v[104:105], v162 offset:57344\n\t" \
  "v_mfma_f32_16x16x32_bf16 v[156:159], v[108:111], v[36:39], v[156:159]\n\t" \
  "ds_read_b64_tr_b16 v[106:107], v162 offset:59392\n\t" \
  "s_waitcnt lgkmcnt(10)\n\t" \
  "v_mfma_f32_16x16x32_bf16 v[128:131], v[112:115], v[40:43], v[128:131]\n\t" \
  "ds_read_b64_tr_b16 v[108:109], v163 offset:57344\n\t" \
  "v_mfma_f32_16x16x32_bf16 v[144:147], v[112:115], v[44:47], v[144:147]\n\t" \
  "ds_read_b64_tr_b16 v[110:111], v163 offset:59392\n\t" \
  "v_mfma_f32_16x16x32_bf16 v[132:135], v[116:119], v[40:43], v[132:135]\n\t" \
  "ds_read_b64_tr_b16 v[112:113], v160 offset:61440\n\t" \
  "v_mfma_f32_16x16x32_bf16 v[148:151], v[116:119], v[44:47], v[148:151]\n\t" \
  "ds_read_b64_tr_b16 v[114:115], v160 offset:63488\n\t" \
  "s_waitcnt lgkmcnt(10)\n\t" \
  "v_mfma_f32_16x16x32_bf16 v[136:139], v[120:123], v[40:43], v[136:139]\n\t" \
  "ds_read_b64_tr_b16 v[116:117], v161 offset:61440\n\t" \
  "v_mfma_f32_16x16x32_bf16 v[152:155], v[120:123], v[44:47], v[152:155]\n\t" \
  "ds_read_b64_tr_b16 v[118:119], v161 offset:63488\n\t" \
  "v_mfma_f32_16x16x32_bf16 v[140:143], v[124:127], v[40:43], v[140:143]\n\t" \
  "ds_read_b64_tr_b16 v[120:121], v162 offset:61440\n\t" \
  "v_mfma_f32_16x16x32_bf16 v[156:159], v[124:127], v[44:47], v[156:159]\n\t" \
  "ds_read_b64_tr_b16 v[122:123], v162 offset:63488\n\t" \
  "s_cbranch_vccz QF15_%=\n\t" \
  "s_waitcnt vmcnt(0)\n\t" \
  "QG15_%=:\n\t" \
  "s_waitcnt lgkmcnt(10)\n\t" \
  "v_mfma_f32_16x16x32_bf16 v[128:131], v[96:99], v[48:51], v[128:131]\n\t" \
  "ds_read_b64_tr_b16 v[124:125], v163 offset:61440\n\t" \
  "v_mfma_f32_16x16x32_bf16 v[144:147], v[96:99], v[52:55], v[144:147]\n\t" \
  "ds_read_b64_tr_b16 v[126:127], v163 offset:63488\n\t" \
  "v_mfma_f32_16x16x32_bf16 v[132:135], v[100:103], v[48:51], v[132:135]\n\t" \
  "v_mfma_f32_16x16x32_bf16 v[148:151], v[100:103], v[52:55], v[148:151]\n\t" \
  "s_waitcnt lgkmcnt(8)\n\t" \
  "v_mfma_f32_16x16x32_bf16 v[136:139], v[104:107], v[48:51], v[136:139]\n\t" \
  "v_mfma_f32_16x16x32_bf16 v[152:155], v[104:107], v[52:55], v[152:155]\n\t" \
  "v_mfma_f32_16x16x32_bf16 v[140:143], v[108:111], v[48:51], v[140:143]\n\t" \
  "v_mfma_f32_16x16x32_bf16 v[156:159], v[108:111], v[52:55], v[156:159]\n\t" \
  "s_waitcnt lgkmcnt(4)\n\t" \
  "v_mfma_f32_16x16x32_bf16 v[128:131], v[112:115], v[56:59], v[128:131]\n\t" \
  "v_mfma_f32_16x16x32_bf16 v[144:147], v[112:115], v[60:63], v[144:147]\n\t" \
  "v_mfma_f32_16x16x32_bf16 v[132:135], v[116:119], v[56:59], v[132:135]\n\t" \
  "v_mfma_f32_16x16x32_bf16 v[148:151], v[116:119], v[60:63], v[148:151]\n\t" \
  "s_waitcnt lgkmcnt(0)\n\t" \
  "v_mfma_f32_16x16x32_bf16 v[136:139], v[120:123], v[56:59], v[136:139]\n\t" \
  "v_mfma_f32_16x16x32_bf16 v[152:155], v[120:123], v[60:63], v[152:155]\n\t" \
  "v_mfma_f32_16x16x32_bf16 v[140:143], v[124:127], v[56:59], v[140:143]\n\t" \
  "v_mfma_f32_16x16x32_bf16 v[156:159], v[124:127], v[60:63], v[156:159]\n\t" \
  "s_branch QX_%=\n\t" \
  "QF1_%=:\n\t" \
  "s_mov_b32 %[c], 1\n\t" \
  "s_getpc_b64 s[92:93]\n\t" \
  "s_branch QC_%=\n\t" \
  "s_branch QG1_%=\n\t" \
  "QF2_%=:\n\t" \
  "s_mov_b32 %[c], 2\n\t" \
  "s_getpc_b64 s[92:93]\n\t" \
  "s_branch QC_%=\n\t" \
  "s_branch QG2_%=\n\t" \
  "QF3_%=:\n\t" \
  "s_mov_b32 %[c], 3\n\t" \
  "s_getpc_b64 s[92:93]\n\t" \
  "s_branch QC_%=\n\t" \
  "s_branch QG3_%=\n\t" \
  "QF4_%=:\n\t" \
  "s_mov_b32 %[c], 4\n\t" \
  "s_getpc_b64 s[92:93]\n\t" \
  "s_branch QC_%=\n\t" \
  "s_branch QG4_%=\n\t" \
  "QF5_%=:\n\t" \
  "s_mov_b32 %[c], 5\n\t" \
  "s_getpc_b64 s[92:93]\n\t" \
  "s_branch QC_%=\n\t" \
  "s_branch QG5_%=\n\t" \
  "QF6_%=:\n\t" \
  "s_mov_b32 %[c], 6\n\t" \
  "s_getpc_b64 s[92:93]\n\t" \
  "s_branch QC_%=\n\t" \
  "s_branch QG6_%=\n\t" \
  "QF7_%=:\n\t" \
  "s_mov_b32 %[c], 7\n\t" \
  "s_getpc_b64 s[92:93]\n\t" \
  "s_branch QC_%=\n\t" \
  "s_branch QG7_%=\n\t" \
  "QF8_%=:\n\t" \
  "s_mov_b32 %[c], 8\n\t" \
  "s_getpc_b64 s[92:93]\n\t" \
  "s_branch QC_%=\n\t" \
  "s_branch QG8_%=\n\t" \
  "QF9_%=:\n\t" \
  "s_mov_b32 %[c], 9\n\t" \
  "s_getpc_b64 s[92:93]\n\t" \
  "s_branch QC_%=\n\t" \
  "s_branch QG9_%=\n\t" \
  "QF10_%=:\n\t" \
  "s_mov_b32 %[c], 10\n\t" \
  "s_getpc_b64 s[92:93]\n\t" \
  "s_branch QC_%=\n\t" \
  "s_branch QG10_%=\n\t" \
  "QF11_%=:\n\t" \
  "s_mov_b32 %[c], 11\n\t" \
  "s_getpc_b64 s[92:93]\n\t" \
  "s_branch QC_%=\n\t" \
  "s_branch QG11_%=\n\t" \
  "QF12_%=:\n\t" \
  "s_mov_b32 %[c], 12\n\t" \
  "s_getpc_b64 s[92:93]\n\t" \
  "s_branch QC_%=\n\t" \
  "s_branch QG12_%=\n\t" \
  "QF13_%=:\n\t" \
  "s_mov_b32 %[c], 13\n\t" \
  "s_getpc_b64 s[92:93]\n\t" \
  "s_branch QC_%=\n\t" \
  "s_branch QG13_%=\n\t" \
  "QF14_%=:\n\t" \
  "s_mov_b32 %[c], 14\n\t" \
  "s_getpc_b64 s[92:93]\n\t" \
  "s_branch QC_%=\n\t" \
  "s_branch QG14_%=\n\t" \
  "QF15_%=:\n\t" \
  "s_mov_b32 %[c], 15\n\t" \
  "s_getpc_b64 s[92:93]\n\t" \
  "s_branch QC_%=\n\t" \
  "s_branch QG15_%=\n\t" \
  "QC_%=:\n\t" \
  "s_add_u32 s92, s92, 4\n\t" \
  "s_addc_u32 s93, s93, 0\n\t" \
  "QU_%=:\n\t" \
  "ds_read_b32 v166, v165\n\t" \
  "s_add_u32 %[t1], %[c], 5\n\t" \
  "s_waitcnt lgkmcnt(0)\n\t" \
  "v_readfirstlane_b32 %[t0], v166\n\t" \
  "s_nop 0\n\t" \
  "s_min_u32 %[t1], %[t1], %[t0]\n\t" \
  "QL_%=:\n\t" \
  "s_cmp_ge_u32 %[iss], %[t1]\n\t" \
  "s_cbranch_scc1 QD_%=\n\t" \
  "s_mov_b32 %[t0], %[iss]\n\t" \
  "s_sub_u32 %[th], %[t0], 6\n\t" \
  "s_cselect_b32 %[t0], %[t0], %[th]\n\t" \
  "s_sub_u32 %[th], %[t0], 6\n\t" \
  "s_cselect_b32 %[t0], %[t0], %[th]\n\t" \
  "s_cmp_eq_u32 %[t0], 1\n\t" \
  "s_cbranch_scc1 QS1_%=\n\t" \
  "s_cmp_eq_u32 %[t0], 2\n\t" \
  "s_cbranch_scc1 QS2_%=\n\t" \
  "s_cmp_eq_u32 %[t0], 3\n\t" \
  "s_cbranch_scc1 QS3_%=\n\t" \
  "s_cmp_eq_u32 %[t0], 4\n\t" \
  "s_cbranch_scc1 QS4_%=\n\t" \
  "s_cmp_eq_u32 %[t0], 5\n\t" \
  "s_cbranch_scc1 QS5_%=\n\t" \
  "QS0_%=:\n\t" \
  "global_load_dwordx4 v[0:3], v164, %[ob] offset:0 sc1\n\t" \
  "global_load_dwordx4 v[4:7], v164, %[ob] offset:1024 sc1\n\t" \
  "global_load_dwordx4 v[8:11], v164, %[ob] offset:2048 sc1\n\t" \
  "global_load_dwordx4 v[12:15], v164, %[ob] offset:3072 sc1\n\t" \
  "v_add_u32 v164, 0x4000, v164\n\t" \
  "v_and_b32 v164, 0x3ffff, v164\n\t" \
  "s_add_u32 %[iss], %[iss], 1\n\t" \
  "s_cmp_ge_u32 %[iss], %[t1]\n\t" \
  "s_cbranch_scc1 QD_%=\n\t" \
  "QS1_%=:\n\t" \
  "global_load_dwordx4 v[16:19], v164, %[ob] offset:0 sc1\n\t" \
  "global_load_dwordx4 v[20:23], v164, %[ob] offset:1024 sc1\n\t" \
  "global_load_dwordx4 v[24:27], v164, %[ob] offset:2048 sc1\n\t" \
  "global_load_dwordx4 v[28:31], v164, %[ob] offset:3072 sc1\n\t" \
  "v_add_u32 v164, 0x4000, v164\n\t" \
  "v_and_b32 v164, 0x3ffff, v164\n\t" \
  "s_add_u32 %[iss], %[iss], 1\n\t" \
  "s_cmp_ge_u32 %[iss], %[t1]\n\t" \
  "s_cbranch_scc1 QD_%=\n\t" \
  "QS2_%=:\n\t" \
  "global_load_dwordx4 v[32:35], v164, %[ob] offset:0 sc1\n\t" \
  "global_load_dwordx4 v[36:39], v164, %[ob] offset:1024 sc1\n\t" \
  "global_load_dwordx4 v[40:43], v164, %[ob] offset:2048 sc1\n\t" \
  "global_load_dwordx4 v[44:47], v164, %[ob] offset:3072 sc1\n\t" \
  "v_add_u32 v164, 0x4000, v164\n\t" \
  "v_and_b32 v164, 0x3ffff, v164\n\t" \
  "s_add_u32 %[iss], %[iss], 1\n\t" \
  "s_cmp_ge_u32 %[iss], %[t1]\n\t" \
  "s_cbranch_scc1 QD_%=\n\t" \
  "QS3_%=:\n\t" \
  "global_load_dwordx4 v[48:51], v164, %[ob] offset:0 sc1\n\t" \
  "global_load_dwordx4 v[52:55], v164, %[ob] offset:1024 sc1\n\t" \
  "global_load_dwordx4 v[56:59], v164, %[ob] offset:2048 sc1\n\t" \
  "global_load_dwordx4 v[60:63], v164, %[ob] offset:3072 sc1\n\t" \
  "v_add_u32 v164, 0x4000, v164\n\t" \
  "v_and_b32 v164, 0x3ffff, v164\n\t" \
  "s_add_u32 %[iss], %[iss], 1\n\t" \
  "s_cmp_ge_u32 %[iss], %[t1]\n\t" \
  "s_cbranch_scc1 QD_%=\n\t" \
  "QS4_%=:\n\t" \
  "global_load_dwordx4 v[64:67], v164, %[ob] offset:0 sc1\n\t" \
  "global_load_dwordx4 v[68:71], v164, %[ob] offset:1024 sc1\n\t" \
  "global_load_dwordx4 v[72:75], v164, %[ob] offset:2048 sc1\n\t" \
  "global_load_dwordx4 v[76:79], v164, %[ob] offset:3072 sc1\n\t" \
  "v_add_u32 v164, 0x4000, v164\n\t" \
  "v_and_b32 v164, 0x3ffff, v164\n\t" \
  "s_add_u32 %[iss], %[iss], 1\n\t" \
  "s_cmp_ge_u32 %[iss], %[t1]\n\t" \
  "s_cbranch_scc1 QD_%=\n\t" \
  "QS5_%=:\n\t" \
  "global_load_dwordx4 v[80:83], v164, %[ob] offset:0 sc1\n\t" \
  "global_load_dwordx4 v[84:87], v164, %[ob] offset:1024 sc1\n\t" \
  "global_load_dwordx4 v[88:91], v164, %[ob] offset:2048 sc1\n\t" \
  "global_load_dwordx4 v[92:95], v164, %[ob] offset:3072 sc1\n\t" \
  "v_add_u32 v164, 0x4000, v164\n\t" \
  "v_and_b32 v164, 0x3ffff, v164\n\t" \
  "s_add_u32 %[iss], %[iss], 1\n\t" \
  "s_cmp_ge_u32 %[iss], %[t1]\n\t" \
  "s_cbranch_scc0 QS0_%=\n\t" \
  "QD_%=:\n\t" \
  "s_cmp_gt_u32 %[iss], %[c]\n\t" \
  "s_cbranch_scc1 QW_%=\n\t" \
  "s_addk_i32 %[cn], 1\n\t" \
  "s_cmp_gt_u32 %[cn], 0x400000\n\t" \
  "s_cbranch_scc1 QT_%=\n\t" \
  "s_sleep 1\n\t" \
  "s_branch QU_%=\n\t" \
  "QT_%=:\n\t" \
  "s_add_u32 %[t1], %[c], 1\n\t" \
  "s_branch QL_%=\n\t" \
  "QW_%=:\n\t" \
  "s_cmp_eq_u32 %[iss], 16\n\t" \
  "s_cselect_b64 vcc, -1, 0\n\t" \
  "s_sub_u32 %[t0], %[iss], %[c]\n\t" \
  "s_cmp_eq_u32 %[t0], 5\n\t" \
  "s_cselect_b32 %[th], %[iss], 99\n\t" \
  "s_waitcnt vmcnt(16)\n\t" \
  "s_cmp_ge_u32 %[t0], 5\n\t" \
  "s_cbranch_scc1 QR_%=\n\t" \
  "s_waitcnt vmcnt(12)\n\t" \
  "s_cmp_ge_u32 %[t0], 4\n\t" \
  "s_cbranch_scc1 QR_%=\n\t" \
  "s_waitcnt vmcnt(8)\n\t" \
  "s_cmp_ge_u32 %[t0], 3\n\t" \
  "s_cbranch_scc1 QR_%=\n\t" \
  "s_waitcnt vmcnt(4)\n\t" \
  "s_cmp_ge_u32 %[t0], 2\n\t" \
  "s_cbranch_scc1 QR_%=\n\t" \
  "s_waitcnt vmcnt(0)\n\t" \
  "QR_%=:\n\t" \
  "s_setpc_b64 s[92:93]\n\t" \
  "QX_%=:\n\t" \
  "s_nop 7\n\t"
#define G3_CLOB "v0", "v1", "v2", "v3", "v4", "v5", "v6", "v7", "v8", "v9", "v10", "v11", "v12", "v13", "v14", "v15", "v16", "v17", "v18", "v19", "v20", "v21", "v22", "v23", "v24", "v25", "v26", "v27", "v28", "v29", "v30", "v31", "v32", "v33", "v34", "v35", "v36", "v37", "v38", "v39", "v40", "v41", "v42", "v43", "v44", "v45", "v46", "v47", "v48", "v49", "v50", "v51", "v52", "v53", "v54", "v55", "v56", "v57", "v58", "v59", "v60", "v61", "v62", "v63", "v64", "v65", "v66", "v67", "v68", "v69", "v70", "v71", "v72", "v73", "v74", "v75", "v76", "v77", "v78", "v79", "v80", "v81", "v82", "v83", "v84", "v85", "v86", "v87", "v88", "v89", "v90", "v91", "v92", "v93", "v94", "v95", "v96", "v97", "v98", "v99", "v100", "v101", "v102", "v103", "v104", "v105", "v106", "v107", "v108", "v109", "v110", "v111", "v112", "v113", "v114", "v115", "v116", "v117", "v118", "v119", "v120", "v121", "v122", "v123", "v124", "v125", "v126", "v127", "v166", "s88", "s89", "s92", "s93"

#define PP_ASM \
  "v_mov_b32 v60, 0\n\t" \
  "s_mov_b32 %[t0], %[n0]\n\t" \
  "s_cmp_eq_u32 %[t0], 0\n\t" \
  "s_cbranch_scc1 PAx_%=\n\t" \
  "PA_%=:\n\t" \
  "s_barrier\n\t" \
  "s_sub_u32 %[t0], %[t0], 1\n\t" \
  "s_cmp_lg_u32 %[t0], 0\n\t" \
  "s_cbranch_scc1 PA_%=\n\t" \
  "PAx_%=:\n\t" \
  "s_mov_b32 m0, %[tsk]\n\t" \
  "s_mov_b64 exec, %[tm]\n\t" \
  "global_load_lds_dword v164, %[tp]\n\t" \
  "s_mov_b64 exec, -1\n\t" \
  "s_mov_b32 m0, %[shd]\n\t" \
  "global_load_dword v160, v60, %[fl] sc1\n\t" \
  "global_load_lds_dwordx4 v163, %[shp]\n\t" \
  "s_mov_b32 m0, %[tgd]\n\t" \
  "s_mov_b64 exec, %[em]\n\t" \
  "global_load_lds_dword v60, %[fl] sc1\n\t" \
  "s_mov_b64 exec, -1\n\t" \
  "global_load_ushort v161, v60, %[hp]\n\t" \
  "global_load_ushort v162, v60, %[dp]\n\t" \
  "global_load_dwordx4 v[0:3], v152, %[kb0]\n\t" \
  "global_load_dwordx4 v[4:7], v153, %[kb0]\n\t" \
  "global_load_dwordx4 v[8:11], v154, %[kb0]\n\t" \
  "global_load_dwordx4 v[12:15], v155, %[kb3] offset:0\n\t" \
  "global_load_dwordx4 v[28:31], v155, %[vb0] offset:0\n\t" \
  "global_load_dwordx4 v[16:19], v155, %[kb3] offset:2048\n\t" \
  "global_load_dwordx4 v[32:35], v155, %[vb0] offset:2048\n\t" \
  "global_load_dwordx4 v[20:23], v155, %[kb5] offset:0\n\t" \
  "global_load_dwordx4 v[36:39], v155, %[vb2] offset:0\n\t" \
  "global_load_dwordx4 v[24:27], v155, %[kb5] offset:2048\n\t" \
  "global_load_dwordx4 v[40:43], v155, %[vb2] offset:2048\n\t" \
  "s_setprio 2\n\t" \
  "s_barrier\n\t" \
  "s_barrier\n\t" \
  "s_waitcnt vmcnt(11)\n\t" \
  "ds_read_b128 v[128:131], v156 offset:0\n\t" \
  "ds_read_b128 v[132:135], v156 offset:16\n\t" \
  "ds_read_b128 v[136:139], v156 offset:32\n\t" \
  "ds_read_b128 v[140:143], v156 offset:48\n\t" \
  "s_waitcnt lgkmcnt(0)\n\t" \
  "v_lshlrev_b32 v64, 16, v128\n\t" \
  "v_and_b32 v65, 0xffff0000, v128\n\t" \
  "v_lshlrev_b32 v66, 16, v129\n\t" \
  "v_and_b32 v67, 0xffff0000, v129\n\t" \
  "v_lshlrev_b32 v68, 16, v130\n\t" \
  "v_and_b32 v69, 0xffff0000, v130\n\t" \
  "v_lshlrev_b32 v70, 16, v131\n\t" \
  "v_and_b32 v71, 0xffff0000, v131\n\t" \
  "v_lshlrev_b32 v72, 16, v132\n\t" \
  "v_and_b32 v73, 0xffff0000, v132\n\t" \
  "v_lshlrev_b32 v74, 16, v133\n\t" \
  "v_and_b32 v75, 0xffff0000, v133\n\t" \
  "v_lshlrev_b32 v76, 16, v134\n\t" \
  "v_and_b32 v77, 0xffff0000, v134\n\t" \
  "v_lshlrev_b32 v78, 16, v135\n\t" \
  "v_and_b32 v79, 0xffff0000, v135\n\t" \
  "v_lshlrev_b32 v80, 16, v136\n\t" \
  "v_and_b32 v81, 0xffff0000, v136\n\t" \
  "v_lshlrev_b32 v82, 16, v137\n\t" \
  "v_and_b32 v83, 0xffff0000, v137\n\t" \
  "v_lshlrev_b32 v84, 16, v138\n\t" \
  "v_and_b32 v85, 0xffff0000, v138\n\t" \
  "v_lshlrev_b32 v86, 16, v139\n\t" \
  "v_and_b32 v87, 0xffff0000, v139\n\t" \
  "v_lshlrev_b32 v88, 16, v140\n\t" \
  "v_and_b32 v89, 0xffff0000, v140\n\t" \
  "v_lshlrev_b32 v90, 16, v141\n\t" \
  "v_and_b32 v91, 0xffff0000, v141\n\t" \
  "v_lshlrev_b32 v92, 16, v142\n\t" \
  "v_and_b32 v93, 0xffff0000, v142\n\t" \
  "v_lshlrev_b32 v94, 16, v143\n\t" \
  "v_and_b32 v95, 0xffff0000, v143\n\t" \
  "s_waitcnt vmcnt(10)\n\t" \
  "s_mov_b64 exec, %[zm]\n\t" \
  "v_mov_b32 v0, 0\n\t" \
  "v_mov_b32 v1, 0\n\t" \
  "v_mov_b32 v2, 0\n\t" \
  "v_mov_b32 v3, 0\n\t" \
  "s_mov_b64 exec, -1\n\t" \
  "v_lshlrev_b32 v128, 16, v0\n\t" \
  "v_and_b32 v129, 0xffff0000, v0\n\t" \
  "s_barrier\n\t" \
  "v_lshlrev_b32 v130, 16, v1\n\t" \
  "v_and_b32 v131, 0xffff0000, v1\n\t" \
  "v_lshlrev_b32 v132, 16, v2\n\t" \
  "v_and_b32 v133, 0xffff0000, v2\n\t" \
  "v_lshlrev_b32 v134, 16, v3\n\t" \
  "v_and_b32 v135, 0xffff0000, v3\n\t" \
  "v_mul_f32 v96, v128, v67\n\t" \
  "v_mul_f32 v97, v129, v71\n\t" \
  "v_mul_f32 v98, v130, v75\n\t" \
  "v_mul_f32 v99, v131, v79\n\t" \
  "v_mul_f32 v100, v132, v83\n\t" \
  "v_mul_f32 v101, v133, v87\n\t" \
  "v_mul_f32 v102, v134, v91\n\t" \
  "v_mul_f32 v103, v135, v95\n\t" \
  "s_waitcnt vmcnt(9)\n\t" \
  "s_mov_b64 exec, %[zm]\n\t" \
  "v_mov_b32 v4, 0\n\t" \
  "v_mov_b32 v5, 0\n\t" \
  "v_mov_b32 v6, 0\n\t" \
  "v_mov_b32 v7, 0\n\t" \
  "s_mov_b64 exec, -1\n\t" \
  "v_lshlrev_b32 v128, 16, v4\n\t" \
  "v_and_b32 v129, 0xffff0000, v4\n\t" \
  "v_lshlrev_b32 v130, 16, v5\n\t" \
  "v_and_b32 v131, 0xffff0000, v5\n\t" \
  "v_lshlrev_b32 v132, 16, v6\n\t" \
  "v_and_b32 v133, 0xffff0000, v6\n\t" \
  "v_lshlrev_b32 v134, 16, v7\n\t" \
  "v_and_b32 v135, 0xffff0000, v7\n\t" \
  "v_fmac_f32 v96, v128, v66\n\t" \
  "v_fmac_f32 v97, v129, v70\n\t" \
  "v_fmac_f32 v98, v130, v74\n\t" \
  "v_fmac_f32 v99, v131, v78\n\t" \
  "v_fmac_f32 v100, v132, v82\n\t" \
  "v_fmac_f32 v101, v133, v86\n\t" \
  "v_fmac_f32 v102, v134, v90\n\t" \
  "v_fmac_f32 v103, v135, v94\n\t" \
  "v_mul_f32 v104, v128, v67\n\t" \
  "v_mul_f32 v105, v129, v71\n\t" \
  "v_mul_f32 v106, v130, v75\n\t" \
  "v_mul_f32 v107, v131, v79\n\t" \
  "v_mul_f32 v108, v132, v83\n\t" \
  "v_mul_f32 v109, v133, v87\n\t" \
  "v_mul_f32 v110, v134, v91\n\t" \
  "v_mul_f32 v111, v135, v95\n\t" \
  "s_waitcnt vmcnt(8)\n\t" \
  "s_mov_b64 exec, %[zm]\n\t" \
  "v_mov_b32 v8, 0\n\t" \
  "v_mov_b32 v9, 0\n\t" \
  "v_mov_b32 v10, 0\n\t" \
  "v_mov_b32 v11, 0\n\t" \
  "s_mov_b64 exec, -1\n\t" \
  "s_setprio 3\n\t" \
  "s_barrier\n\t" \
  "s_mov_b32 m0, %[hd]\n\t" \
  "s_nop 0\n\t" \
  "global_load_lds_dwordx4 v163, %[hp] offset:0\n\t" \
  "global_load_lds_dwordx4 v163, %[hp] offset:1024\n\t" \
  "global_load_lds_dwordx4 v163, %[hp] offset:2048\n\t" \
  "global_load_lds_dwordx4 v163, %[hp] offset:3072\n\t" \
  "global_load_dwordx4 v[44:47], v155, %[qb0] offset:0\n\t" \
  "global_load_dwordx4 v[48:51], v155, %[qb0] offset:2048\n\t" \
  "global_load_dwordx4 v[52:55], v155, %[qb2] offset:0\n\t" \
  "global_load_dwordx4 v[56:59], v155, %[qb2] offset:2048\n\t" \
  "s_setprio 2\n\t" \
  "v_lshlrev_b32 v128, 16, v8\n\t" \
  "v_and_b32 v129, 0xffff0000, v8\n\t" \
  "v_lshlrev_b32 v130, 16, v9\n\t" \
  "v_and_b32 v131, 0xffff0000, v9\n\t" \
  "v_lshlrev_b32 v132, 16, v10\n\t" \
  "v_and_b32 v133, 0xffff0000, v10\n\t" \
  "v_lshlrev_b32 v134, 16, v11\n\t" \
  "v_and_b32 v135, 0xffff0000, v11\n\t" \
  "v_fmac_f32 v96, v128, v65\n\t" \
  "v_fmac_f32 v97, v129, v69\n\t" \
  "v_fmac_f32 v98, v130, v73\n\t" \
  "v_fmac_f32 v99, v131, v77\n\t" \
  "v_fmac_f32 v100, v132, v81\n\t" \
  "v_fmac_f32 v101, v133, v85\n\t" \
  "v_fmac_f32 v102, v134, v89\n\t" \
  "v_fmac_f32 v103, v135, v93\n\t" \
  "v_fmac_f32 v104, v128, v66\n\t" \
  "v_fmac_f32 v105, v129, v70\n\t" \
  "v_fmac_f32 v106, v130, v74\n\t" \
  "v_fmac_f32 v107, v131, v78\n\t" \
  "v_fmac_f32 v108, v132, v82\n\t" \
  "v_fmac_f32 v109, v133, v86\n\t" \
  "v_fmac_f32 v110, v134, v90\n\t" \
  "v_fmac_f32 v111, v135, v94\n\t" \
  "v_mul_f32 v112, v128, v67\n\t" \
  "v_mul_f32 v113, v129, v71\n\t" \
  "v_mul_f32 v114, v130, v75\n\t" \
  "v_mul_f32 v115, v131, v79\n\t" \
  "v_mul_f32 v116, v132, v83\n\t" \
  "v_mul_f32 v117, v133, v87\n\t" \
  "v_mul_f32 v118, v134, v91\n\t" \
  "v_mul_f32 v119, v135, v95\n\t" \
  "s_waitcnt vmcnt(15)\n\t" \
  "v_lshlrev_b32 v128, 16, v12\n\t" \
  "v_and_b32 v129, 0xffff0000, v12\n\t" \
  "v_lshlrev_b32 v130, 16, v13\n\t" \
  "v_and_b32 v131, 0xffff0000, v13\n\t" \
  "v_lshlrev_b32 v132, 16, v14\n\t" \
  "v_and_b32 v133, 0xffff0000, v14\n\t" \
  "v_lshlrev_b32 v134, 16, v15\n\t" \
  "v_and_b32 v135, 0xffff0000, v15\n\t" \
  "s_barrier\n\t" \
  "v_fmac_f32 v96, v128, v64\n\t" \
  "v_fmac_f32 v97, v129, v68\n\t" \
  "v_fmac_f32 v98, v130, v72\n\t" \
  "v_fmac_f32 v99, v131, v76\n\t" \
  "v_fmac_f32 v100, v132, v80\n\t" \
  "v_fmac_f32 v101, v133, v84\n\t" \
  "v_fmac_f32 v102, v134, v88\n\t" \
  "v_fmac_f32 v103, v135, v92\n\t" \
  "v_fmac_f32 v104, v128, v65\n\t" \
  "v_fmac_f32 v105, v129, v69\n\t" \
  "v_fmac_f32 v106, v130, v73\n\t" \
  "v_fmac_f32 v107, v131, v77\n\t" \
  "v_fmac_f32 v108, v132, v81\n\t" \
  "v_fmac_f32 v109, v133, v85\n\t" \
  "v_fmac_f32 v110, v134, v89\n\t" \
  "v_fmac_f32 v111, v135, v93\n\t" \
  "v_fmac_f32 v112, v128, v66\n\t" \
  "v_fmac_f32 v113, v129, v70\n\t" \
  "v_fmac_f32 v114, v130, v74\n\t" \
  "v_fmac_f32 v115, v131, v78\n\t" \
  "v_fmac_f32 v116, v132, v82\n\t" \
  "v_fmac_f32 v117, v133, v86\n\t" \
  "v_fmac_f32 v118, v134, v90\n\t" \
  "v_fmac_f32 v119, v135, v94\n\t" \
  "v_mul_f32 v120, v128, v67\n\t" \
  "v_mul_f32 v121, v129, v71\n\t" \
  "v_mul_f32 v122, v130, v75\n\t" \
  "v_mul_f32 v123, v131, v79\n\t" \
  "v_mul_f32 v124, v132, v83\n\t" \
  "v_mul_f32 v125, v133, v87\n\t" \
  "v_mul_f32 v126, v134, v91\n\t" \
  "v_mul_f32 v127, v135, v95\n\t" \
  "s_waitcnt vmcnt(14)\n\t" \
  "v_lshlrev_b32 v128, 16, v28\n\t" \
  "v_and_b32 v129, 0xffff0000, v28\n\t" \
  "v_lshlrev_b32 v130, 16, v29\n\t" \
  "v_and_b32 v131, 0xffff0000, v29\n\t" \
  "v_lshlrev_b32 v132, 16, v30\n\t" \
  "v_and_b32 v133, 0xffff0000, v30\n\t" \
  "v_lshlrev_b32 v134, 16, v31\n\t" \
  "v_and_b32 v135, 0xffff0000, v31\n\t" \
  "s_barrier\n\t" \
  "v_mul_f32 v96, v96, v128\n\t" \
  "v_mul_f32 v97, v97, v129\n\t" \
  "v_mul_f32 v98, v98, v130\n\t" \
  "v_mul_f32 v99, v99, v131\n\t" \
  "v_mul_f32 v100, v100, v132\n\t" \
  "v_mul_f32 v101, v101, v133\n\t" \
  "v_mul_f32 v102, v102, v134\n\t" \
  "v_mul_f32 v103, v103, v135\n\t" \
  "v_cvt_pk_bf16_f32 v136, v96, v97\n\t" \
  "v_cvt_pk_bf16_f32 v137, v98, v99\n\t" \
  "v_cvt_pk_bf16_f32 v138, v100, v101\n\t" \
  "v_cvt_pk_bf16_f32 v139, v102, v103\n\t" \
  "v_mov_b32_dpp v128, v136 row_ror:8 row_mask:0xf bank_mask:0xf\n\t" \
  "v_mov_b32_dpp v129, v137 row_ror:8 row_mask:0xf bank_mask:0xf\n\t" \
  "v_mov_b32_dpp v130, v138 row_ror:8 row_mask:0xf bank_mask:0xf\n\t" \
  "v_mov_b32_dpp v131, v139 row_ror:8 row_mask:0xf bank_mask:0xf\n\t" \
  "v_perm_b32 v136, v128, v136, v157\n\t" \
  "v_perm_b32 v137, v129, v137, v157\n\t" \
  "v_perm_b32 v138, v130, v138, v157\n\t" \
  "v_perm_b32 v139, v131, v139, v157\n\t" \
  "v_permlane16_swap_b32 v136, v137\n\t" \
  "s_nop 0\n\t" \
  "v_permlane16_swap_b32 v138, v139\n\t" \
  "s_nop 1\n\t" \
  "v_permlane32_swap_b32 v136, v138\n\t" \
  "v_permlane32_swap_b32 v137, v139\n\t" \
  "s_waitcnt vmcnt(13)\n\t" \
  "v_lshlrev_b32 v128, 16, v16\n\t" \
  "v_and_b32 v129, 0xffff0000, v16\n\t" \
  "v_lshlrev_b32 v130, 16, v17\n\t" \
  "v_and_b32 v131, 0xffff0000, v17\n\t" \
  "v_lshlrev_b32 v132, 16, v18\n\t" \
  "v_and_b32 v133, 0xffff0000, v18\n\t" \
  "v_lshlrev_b32 v134, 16, v19\n\t" \
  "v_and_b32 v135, 0xffff0000, v19\n\t" \
  "v_fmac_f32 v104, v128, v64\n\t" \
  "v_fmac_f32 v105, v129, v68\n\t" \
  "v_fmac_f32 v106, v130, v72\n\t" \
  "v_fmac_f32 v107, v131, v76\n\t" \
  "v_fmac_f32 v108, v132, v80\n\t" \
  "v_fmac_f32 v109, v133, v84\n\t" \
  "s_setprio 3\n\t" \
  "s_barrier\n\t" \
  "global_store_dwordx4 v159, v[136:139], %[sb] sc1\n\t" \
  "v_add_u32 v158, 0x80, v159\n\t" \
  "v_bfi_b32 v159, %[mk], v158, v159\n\t" \
  "s_setprio 2\n\t" \
  "v_fmac_f32 v110, v134, v88\n\t" \
  "v_fmac_f32 v111, v135, v92\n\t" \
  "v_fmac_f32 v112, v128, v65\n\t" \
  "v_fmac_f32 v113, v129, v69\n\t" \
  "v_fmac_f32 v114, v130, v73\n\t" \
  "v_fmac_f32 v115, v131, v77\n\t" \
  "v_fmac_f32 v116, v132, v81\n\t" \
  "v_fmac_f32 v117, v133, v85\n\t" \
  "v_fmac_f32 v118, v134, v89\n\t" \
  "v_fmac_f32 v119, v135, v93\n\t" \
  "v_fmac_f32 v120, v128, v66\n\t" \
  "v_fmac_f32 v121, v129, v70\n\t" \
  "v_fmac_f32 v122, v130, v74\n\t" \
  "v_fmac_f32 v123, v131, v78\n\t" \
  "v_fmac_f32 v124, v132, v82\n\t" \
  "v_fmac_f32 v125, v133, v86\n\t" \
  "v_fmac_f32 v126, v134, v90\n\t" \
  "v_fmac_f32 v127, v135, v94\n\t" \
  "s_waitcnt vmcnt(13)\n\t" \
  "v_lshlrev_b32 v128, 16, v32\n\t" \
  "v_and_b32 v129, 0xffff0000, v32\n\t" \
  "v_lshlrev_b32 v130, 16, v33\n\t" \
  "v_and_b32 v131, 0xffff0000, v33\n\t" \
  "v_lshlrev_b32 v132, 16, v34\n\t" \
  "v_and_b32 v133, 0xffff0000, v34\n\t" \
  "v_lshlrev_b32 v134, 16, v35\n\t" \
  "v_and_b32 v135, 0xffff0000, v35\n\t" \
  "v_mul_f32 v104, v104, v128\n\t" \
  "v_mul_f32 v105, v105, v129\n\t" \
  "v_mul_f32 v106, v106, v130\n\t" \
  "v_mul_f32 v107, v107, v131\n\t" \
  "v_mul_f32 v108, v108, v132\n\t" \
  "v_mul_f32 v109, v109, v133\n\t" \
  "v_mul_f32 v110, v110, v134\n\t" \
  "v_mul_f32 v111, v111, v135\n\t" \
  "v_cvt_pk_bf16_f32 v140, v104, v105\n\t" \
  "v_cvt_pk_bf16_f32 v141, v106, v107\n\t" \
  "v_cvt_pk_bf16_f32 v142, v108, v109\n\t" \
  "v_cvt_pk_bf16_f32 v143, v110, v111\n\t" \
  "v_mov_b32_dpp v128, v140 row_ror:8 row_mask:0xf bank_mask:0xf\n\t" \
  "v_mov_b32_dpp v129, v141 row_ror:8 row_mask:0xf bank_mask:0xf\n\t" \
  "s_barrier\n\t" \
  "v_mov_b32_dpp v130, v142 row_ror:8 row_mask:0xf bank_mask:0xf\n\t" \
  "v_mov_b32_dpp v131, v143 row_ror:8 row_mask:0xf bank_mask:0xf\n\t" \
  "v_perm_b32 v140, v128, v140, v157\n\t" \
  "v_perm_b32 v141, v129, v141, v157\n\t" \
  "v_perm_b32 v142, v130, v142, v157\n\t" \
  "v_perm_b32 v143, v131, v143, v157\n\t" \
  "v_permlane16_swap_b32 v140, v141\n\t" \
  "s_nop 0\n\t" \
  "v_permlane16_swap_b32 v142, v143\n\t" \
  "s_nop 1\n\t" \
  "v_permlane32_swap_b32 v140, v142\n\t" \
  "v_permlane32_swap_b32 v141, v143\n\t" \
  "s_waitcnt vmcnt(12)\n\t" \
  "v_lshlrev_b32 v128, 16, v20\n\t" \
  "v_and_b32 v129, 0xffff0000, v20\n\t" \
  "v_lshlrev_b32 v130, 16, v21\n\t" \
  "v_and_b32 v131, 0xffff0000, v21\n\t" \
  "v_lshlrev_b32 v132, 16, v22\n\t" \
  "v_and_b32 v133, 0xffff0000, v22\n\t" \
  "v_lshlrev_b32 v134, 16, v23\n\t" \
  "v_and_b32 v135, 0xffff0000, v23\n\t" \
  "v_fmac_f32 v112, v128, v64\n\t" \
  "v_fmac_f32 v113, v129, v68\n\t" \
  "v_fmac_f32 v114, v130, v72\n\t" \
  "v_fmac_f32 v115, v131, v76\n\t" \
  "v_fmac_f32 v116, v132, v80\n\t" \
  "v_fmac_f32 v117, v133, v84\n\t" \
  "v_fmac_f32 v118, v134, v88\n\t" \
  "v_fmac_f32 v119, v135, v92\n\t" \
  "v_fmac_f32 v120, v128, v65\n\t" \
  "v_fmac_f32 v121, v129, v69\n\t" \
  "v_fmac_f32 v122, v130, v73\n\t" \
  "v_fmac_f32 v123, v131, v77\n\t" \
  "v_fmac_f32 v124, v132, v81\n\t" \
  "v_fmac_f32 v125, v133, v85\n\t" \
  "v_fmac_f32 v126, v134, v89\n\t" \
  "v_fmac_f32 v127, v135, v93\n\t" \
  "s_waitcnt vmcnt(11)\n\t" \
  "v_lshlrev_b32 v128, 16, v36\n\t" \
  "v_and_b32 v129, 0xffff0000, v36\n\t" \
  "v_lshlrev_b32 v130, 16, v37\n\t" \
  "v_and_b32 v131, 0xffff0000, v37\n\t" \
  "s_setprio 3\n\t" \
  "s_barrier\n\t" \
  "global_store_dwordx4 v159, v[140:143], %[sb] sc1\n\t" \
  "v_add_u32 v158, 0x80, v159\n\t" \
  "v_bfi_b32 v159, %[mk], v158, v159\n\t" \
  "s_setprio 2\n\t" \
  "v_lshlrev_b32 v132, 16, v38\n\t" \
  "v_and_b32 v133, 0xffff0000, v38\n\t" \
  "v_lshlrev_b32 v134, 16, v39\n\t" \
  "v_and_b32 v135, 0xffff0000, v39\n\t" \
  "v_mul_f32 v112, v112, v128\n\t" \
  "v_mul_f32 v113, v113, v129\n\t" \
  "v_mul_f32 v114, v114, v130\n\t" \
  "v_mul_f32 v115, v115, v131\n\t" \
  "v_mul_f32 v116, v116, v132\n\t" \
  "v_mul_f32 v117, v117, v133\n\t" \
  "v_mul_f32 v118, v118, v134\n\t" \
  "v_mul_f32 v119, v119, v135\n\t" \
  "v_cvt_pk_bf16_f32 v144, v112, v113\n\t" \
  "v_cvt_pk_bf16_f32 v145, v114, v115\n\t" \
  "v_cvt_pk_bf16_f32 v146, v116, v117\n\t" \
  "v_cvt_pk_bf16_f32 v147, v118, v119\n\t" \
  "v_mov_b32_dpp v128, v144 row_ror:8 row_mask:0xf bank_mask:0xf\n\t" \
  "v_mov_b32_dpp v129, v145 row_ror:8 row_mask:0xf bank_mask:0xf\n\t" \
  "v_mov_b32_dpp v130, v146 row_ror:8 row_mask:0xf bank_mask:0xf\n\t" \
  "v_mov_b32_dpp v131, v147 row_ror:8 row_mask:0xf bank_mask:0xf\n\t" \
  "v_perm_b32 v144, v128, v144, v157\n\t" \
  "v_perm_b32 v145, v129, v145, v157\n\t" \
  "v_perm_b32 v146, v130, v146, v157\n\t" \
  "v_perm_b32 v147, v131, v147, v157\n\t" \
  "v_permlane16_swap_b32 v144, v145\n\t" \
  "s_nop 0\n\t" \
  "v_permlane16_swap_b32 v146, v147\n\t" \
  "s_nop 1\n\t" \
  "v_permlane32_swap_b32 v144, v146\n\t" \
  "v_permlane32_swap_b32 v145, v147\n\t" \
  "s_waitcnt vmcnt(11)\n\t" \
  "v_lshlrev_b32 v128, 16, v24\n\t" \
  "v_and_b32 v129, 0xffff0000, v24\n\t" \
  "v_lshlrev_b32 v130, 16, v25\n\t" \
  "v_and_b32 v131, 0xffff0000, v25\n\t" \
  "v_lshlrev_b32 v132, 16, v26\n\t" \
  "v_and_b32 v133, 0xffff0000, v26\n\t" \
  "v_lshlrev_b32 v134, 16, v27\n\t" \
  "v_and_b32 v135, 0xffff0000, v27\n\t" \
  "v_fmac_f32 v120, v128, v64\n\t" \
  "v_fmac_f32 v121, v129, v68\n\t" \
  "s_setprio 3\n\t" \
  "s_barrier\n\t" \
  "global_store_dwordx4 v159, v[144:147], %[sb] sc1\n\t" \
  "v_add_u32 v158, 0x80, v159\n\t" \
  "v_bfi_b32 v159, %[mk], v158, v159\n\t" \
  "s_setprio 2\n\t" \
  "v_fmac_f32 v122, v130, v72\n\t" \
  "v_fmac_f32 v123, v131, v76\n\t" \
  "v_fmac_f32 v124, v132, v80\n\t" \
  "v_fmac_f32 v125, v133, v84\n\t" \
  "v_fmac_f32 v126, v134, v88\n\t" \
  "v_fmac_f32 v127, v135, v92\n\t" \
  "s_waitcnt vmcnt(11)\n\t" \
  "v_lshlrev_b32 v128, 16, v40\n\t" \
  "v_and_b32 v129, 0xffff0000, v40\n\t" \
  "v_lshlrev_b32 v130, 16, v41\n\t" \
  "v_and_b32 v131, 0xffff0000, v41\n\t" \
  "v_lshlrev_b32 v132, 16, v42\n\t" \
  "v_and_b32 v133, 0xffff0000, v42\n\t" \
  "v_lshlrev_b32 v134, 16, v43\n\t" \
  "v_and_b32 v135, 0xffff0000, v43\n\t" \
  "v_mul_f32 v120, v120, v128\n\t" \
  "v_mul_f32 v121, v121, v129\n\t" \
  "v_mul_f32 v122, v122, v130\n\t" \
  "v_mul_f32 v123, v123, v131\n\t" \
  "v_mul_f32 v124, v124, v132\n\t" \
  "v_mul_f32 v125, v125, v133\n\t" \
  "v_mul_f32 v126, v126, v134\n\t" \
  "v_mul_f32 v127, v127, v135\n\t" \
  "v_cvt_pk_bf16_f32 v148, v120, v121\n\t" \
  "v_cvt_pk_bf16_f32 v149, v122, v123\n\t" \
  "v_cvt_pk_bf16_f32 v150, v124, v125\n\t" \
  "v_cvt_pk_bf16_f32 v151, v126, v127\n\t" \
  "v_mov_b32_dpp v128, v148 row_ror:8 row_mask:0xf bank_mask:0xf\n\t" \
  "v_mov_b32_dpp v129, v149 row_ror:8 row_mask:0xf bank_mask:0xf\n\t" \
  "v_mov_b32_dpp v130, v150 row_ror:8 row_mask:0xf bank_mask:0xf\n\t" \
  "v_mov_b32_dpp v131, v151 row_ror:8 row_mask:0xf bank_mask:0xf\n\t" \
  "v_perm_b32 v148, v128, v148, v157\n\t" \
  "v_perm_b32 v149, v129, v149, v157\n\t" \
  "v_perm_b32 v150, v130, v150, v157\n\t" \
  "v_perm_b32 v151, v131, v151, v157\n\t" \
  "v_permlane16_swap_b32 v148, v149\n\t" \
  "s_nop 0\n\t" \
  "v_permlane16_swap_b32 v150, v151\n\t" \
  "s_nop 1\n\t" \
  "v_permlane32_swap_b32 v148, v150\n\t" \
  "v_permlane32_swap_b32 v149, v151\n\t" \
  "s_setprio 3\n\t" \
  "s_barrier\n\t" \
  "global_store_dwordx4 v159, v[148:151], %[sb] sc1\n\t" \
  "s_setprio 2\n\t" \
  "s_waitcnt vmcnt(8)\n\t" \
  "ds_read_b64_tr_b16 v[64:65], v165 offset:32\n\t" \
  "ds_read_b64_tr_b16 v[66:67], v165 offset:0\n\t" \
  "ds_read_b64_tr_b16 v[68:69], v165 offset:288\n\t" \
  "ds_read_b64_tr_b16 v[70:71], v165 offset:256\n\t" \
  "ds_read_b64_tr_b16 v[72:73], v165 offset:544\n\t" \
  "ds_read_b64_tr_b16 v[74:75], v165 offset:512\n\t" \
  "ds_read_b64_tr_b16 v[76:77], v165 offset:800\n\t" \
  "ds_read_b64_tr_b16 v[78:79], v165 offset:768\n\t" \
  "v_mov_b32 v24, v166\n\t" \
  "v_mov_b32 v25, v167\n\t" \
  "v_mov_b32 v63, 0xc0\n\t" \
  "v_add_u32 v158, 64, v24\n\t" \
  "v_bfi_b32 v26, v63, v158, v24\n\t" \
  "v_add_u32 v158, 64, v25\n\t" \
  "v_bfi_b32 v27, v63, v158, v25\n\t" \
  "v_add_u32 v158, 64, v26\n\t" \
  "v_bfi_b32 v28, v63, v158, v26\n\t" \
  "v_add_u32 v158, 64, v27\n\t" \
  "v_bfi_b32 v29, v63, v158, v27\n\t" \
  "v_add_u32 v158, 64, v28\n\t" \
  "v_bfi_b32 v30, v63, v158, v28\n\t" \
  "v_add_u32 v158, 64, v29\n\t" \
  "v_bfi_b32 v31, v63, v158, v29\n\t" \
  "s_barrier\n\t" \
  "ds_read_b64_tr_b16 v[80:81], v165 offset:1056\n\t" \
  "ds_read_b64_tr_b16 v[82:83], v165 offset:1024\n\t" \
  "ds_read_b64_tr_b16 v[84:85], v165 offset:1312\n\t" \
  "ds_read_b64_tr_b16 v[86:87], v165 offset:1280\n\t" \
  "ds_read_b64_tr_b16 v[88:89], v165 offset:1568\n\t" \
  "ds_read_b64_tr_b16 v[90:91], v165 offset:1536\n\t" \
  "ds_read_b64_tr_b16 v[92:93], v165 offset:1824\n\t" \
  "ds_read_b64_tr_b16 v[94:95], v165 offset:1792\n\t" \
  "s_setprio 3\n\t" \
  "s_barrier\n\t" \
  "s_waitcnt vmcnt(0)\n\t" \
  "v_add_u32 v61, 1, v160\n\t" \
  "s_mov_b64 exec, 1\n\t" \
  "global_store_dword v60, v61, %[ff] sc1\n\t" \
  "s_mov_b64 exec, -1\n\t" \
  "s_setprio 2\n\t" \
  "ds_read_b64_tr_b16 v[96:97], v165 offset:2080\n\t" \
  "ds_read_b64_tr_b16 v[98:99], v165 offset:2048\n\t" \
  "ds_read_b64_tr_b16 v[100:101], v165 offset:2336\n\t" \
  "ds_read_b64_tr_b16 v[102:103], v165 offset:2304\n\t" \
  "ds_read_b64_tr_b16 v[104:105], v165 offset:2592\n\t" \
  "ds_read_b64_tr_b16 v[106:107], v165 offset:2560\n\t" \
  "ds_read_b64_tr_b16 v[108:109], v165 offset:2848\n\t" \
  "ds_read_b64_tr_b16 v[110:111], v165 offset:2816\n\t" \
  "s_barrier\n\t" \
  "ds_read_b64_tr_b16 v[112:113], v165 offset:3104\n\t" \
  "ds_read_b64_tr_b16 v[114:115], v165 offset:3072\n\t" \
  "ds_read_b64_tr_b16 v[116:117], v165 offset:3360\n\t" \
  "ds_read_b64_tr_b16 v[118:119], v165 offset:3328\n\t" \
  "ds_read_b64_tr_b16 v[120:121], v165 offset:3616\n\t" \
  "ds_read_b64_tr_b16 v[122:123], v165 offset:3584\n\t" \
  "ds_read_b64_tr_b16 v[124:125], v165 offset:3872\n\t" \
  "ds_read_b64_tr_b16 v[126:127], v165 offset:3840\n\t" \
  "s_barrier\n\t" \
  "s_waitcnt lgkmcnt(0)\n\t" \
  "v_lshlrev_b32 v61, 16, v161\n\t" \
  "v_lshlrev_b32 v62, 16, v162\n\t" \
  "v_add_f32 v61, v61, v62\n\t" \
  "v_mov_b32 v62, 0xffff\n\t" \
  "v_cvt_pk_bf16_f32 v61, v61, v61\n\t" \
  "s_mov_b64 exec, 1\n\t" \
  "v_bfi_b32 v66, v62, v61, v66\n\t" \
  "s_mov_b64 exec, 15\n\t" \
  "v_and_b32 v66, v62, v66\n\t" \
  "s_mov_b64 exec, 16\n\t" \
  "v_bfi_b32 v66, v62, v66, v61\n\t" \
  "s_mov_b64 exec, 0xff\n\t" \
  "v_mov_b32 v67, 0\n\t" \
  "s_mov_b64 exec, 0x100\n\t" \
  "v_mov_b32 v67, v61\n\t" \
  "s_mov_b64 exec, 0xf00\n\t" \
  "v_and_b32 v67, v62, v67\n\t" \
  "s_mov_b64 exec, 0x1000\n\t" \
  "v_bfi_b32 v67, v62, v67, v61\n\t" \
  "s_mov_b64 exec, -1\n\t" \
  "s_waitcnt lgkmcnt(0)\n\t"
#define PP_NB 15
#define PP_Q 1
#define PP_CLOB "v0", "v1", "v2", "v3", "v4", "v5", "v6", "v7", "v8", "v9", "v10", "v11", "v12", "v13", "v14", "v15", "v16", "v17", "v18", "v19", "v20", "v21", "v22", "v23", "v32", "v33", "v34", "v35", "v36", "v37", "v38", "v39", "v40", "v41", "v42", "v43", "v60", "v61", "v62", "v63", "v128", "v129", "v130", "v131", "v132", "v133", "v134", "v135", "v136", "v137", "v138", "v139", "v140", "v141", "v142", "v143", "v144", "v145", "v146", "v147", "v148", "v149", "v150", "v151", "v158", "v161", "v162"

#define LD_ASM \
  "s_add_u32 s68, %[rw0], 0\n\t" \
  "s_add_u32 s69, %[rx0], 0\n\t" \
  "s_add_u32 s70, %[rx0], 147456\n\t" \
  "s_add_u32 s71, %[rw0], 196608\n\t" \
  "s_add_u32 s72, %[rx1], 0\n\t" \
  "s_add_u32 s73, %[rx1], 147456\n\t" \
  "s_add_u32 s74, %[rw1], 0\n\t" \
  "s_add_u32 s75, %[rw1], 196608\n\t" \
  "s_add_u32 m0, %[wb], 49152\n\t" \
  "s_nop 0\n\t" \
  "buffer_load_dwordx4 v5, %[sw], s68 offen offset:0 lds\n\t" \
  "buffer_load_dwordx4 v6, %[sw], s68 offen offset:1024 lds\n\t" \
  "buffer_load_dwordx4 v7, %[sw], s68 offen offset:2048 lds\n\t" \
  "s_add_u32 m0, %[xb], 36864\n\t" \
  "s_nop 0\n\t" \
  "buffer_load_dwordx4 v0, %[sx], s69 offen offset:0 lds\n\t" \
  "buffer_load_dwordx4 v1, %[sx], s69 offen offset:1024 lds\n\t" \
  "s_mov_b64 exec, 0xffff\n\t" \
  "buffer_load_dwordx4 v4, %[sx], s69 offen offset:2048 lds\n\t" \
  "s_add_u32 m0, %[xb], 46080\n\t" \
  "s_mov_b64 exec, -1\n\t" \
  "buffer_load_dwordx4 v2, %[sx], s70 offen offset:0 lds\n\t" \
  "buffer_load_dwordx4 v3, %[sx], s70 offen offset:1024 lds\n\t" \
  "s_mov_b64 exec, 0xffff\n\t" \
  "buffer_load_dwordx4 v4, %[sx], s70 offen offset:2048 lds\n\t" \
  "s_add_u32 m0, %[wb], 61440\n\t" \
  "s_mov_b64 exec, -1\n\t" \
  "buffer_load_dwordx4 v5, %[sw], s71 offen offset:0 lds\n\t" \
  "s_mov_b32 s85, 0x120000\n\t" \
  "s_load_dword s84, %[pw], s85\n\t" \
  "buffer_load_dwordx4 v6, %[sw], s71 offen offset:1024 lds\n\t" \
  "s_mov_b32 s85, 0x120800\n\t" \
  "s_load_dword s84, %[pw], s85\n\t" \
  "buffer_load_dwordx4 v7, %[sw], s71 offen offset:2048 lds\n\t" \
  "s_mov_b32 s85, 0x121000\n\t" \
  "s_load_dword s84, %[pw], s85\n\t" \
  "s_add_u32 m0, %[xb], 0\n\t" \
  "s_nop 0\n\t" \
  "buffer_load_dwordx4 v0, %[sx], s72 offen offset:0 lds\n\t" \
  "s_mov_b32 s85, 0x150000\n\t" \
  "s_load_dword s84, %[pw], s85\n\t" \
  "buffer_load_dwordx4 v1, %[sx], s72 offen offset:1024 lds\n\t" \
  "s_mov_b32 s85, 0x150800\n\t" \
  "s_load_dword s84, %[pw], s85\n\t" \
  "s_mov_b64 exec, 0xffff\n\t" \
  "buffer_load_dwordx4 v4, %[sx], s72 offen offset:2048 lds\n\t" \
  "s_add_u32 m0, %[xb], 9216\n\t" \
  "s_mov_b64 exec, -1\n\t" \
  "buffer_load_dwordx4 v2, %[sx], s73 offen offset:0 lds\n\t" \
  "s_mov_b32 s85, 0x151000\n\t" \
  "s_load_dword s84, %[pw], s85\n\t" \
  "buffer_load_dwordx4 v3, %[sx], s73 offen offset:1024 lds\n\t" \
  "s_mov_b32 s85, 0x180000\n\t" \
  "s_load_dword s84, %[pw], s85\n\t" \
  "s_mov_b64 exec, 0xffff\n\t" \
  "buffer_load_dwordx4 v4, %[sx], s73 offen offset:2048 lds\n\t" \
  "s_add_u32 m0, %[wb], 0\n\t" \
  "s_mov_b64 exec, -1\n\t" \
  "buffer_load_dwordx4 v5, %[sw], s74 offen offset:0 lds\n\t" \
  "s_mov_b32 s85, 0x180800\n\t" \
  "s_load_dword s84, %[pw], s85\n\t" \
  "buffer_load_dwordx4 v6, %[sw], s74 offen offset:1024 lds\n\t" \
  "buffer_load_dwordx4 v7, %[sw], s74 offen offset:2048 lds\n\t" \
  "s_add_u32 m0, %[wb], 12288\n\t" \
  "s_nop 0\n\t" \
  "buffer_load_dwordx4 v5, %[sw], s75 offen offset:0 lds\n\t" \
  "buffer_load_dwordx4 v6, %[sw], s75 offen offset:1024 lds\n\t" \
  "buffer_load_dwordx4 v7, %[sw], s75 offen offset:2048 lds\n\t" \
  "s_add_u32 s68, %[rx2], 0\n\t" \
  "s_add_u32 s69, %[rx2], 147456\n\t" \
  "s_add_u32 m0, %[xb], 18432\n\t" \
  "s_waitcnt vmcnt(15)\n\t" \
  "s_barrier\n\t" \
  "buffer_load_dwordx4 v0, %[sx], s68 offen offset:0 lds\n\t" \
  "s_mov_b32 s85, 0x181000\n\t" \
  "s_load_dword s84, %[pw], s85\n\t" \
  "buffer_load_dwordx4 v1, %[sx], s68 offen offset:1024 lds\n\t" \
  "s_mov_b32 s85, 0x1b0000\n\t" \
  "s_load_dword s84, %[pw], s85\n\t" \
  "s_mov_b64 exec, 0xffff\n\t" \
  "buffer_load_dwordx4 v4, %[sx], s68 offen offset:2048 lds\n\t" \
  "s_add_u32 m0, %[xb], 27648\n\t" \
  "s_mov_b64 exec, -1\n\t" \
  "buffer_load_dwordx4 v2, %[sx], s69 offen offset:0 lds\n\t" \
  "s_mov_b32 s85, 0x1b0800\n\t" \
  "s_load_dword s84, %[pw], s85\n\t" \
  "buffer_load_dwordx4 v3, %[sx], s69 offen offset:1024 lds\n\t" \
  "s_mov_b32 s85, 0x1b1000\n\t" \
  "s_load_dword s84, %[pw], s85\n\t" \
  "s_mov_b64 exec, 0xffff\n\t" \
  "buffer_load_dwordx4 v4, %[sx], s69 offen offset:2048 lds\n\t" \
  "s_mov_b64 exec, -1\n\t" \
  "s_add_u32 s68, %[rw2], 0\n\t" \
  "s_add_u32 s69, %[rw2], 196608\n\t" \
  "s_add_u32 m0, %[wb], 24576\n\t" \
  "s_waitcnt vmcnt(18)\n\t" \
  "s_barrier\n\t" \
  "buffer_load_dwordx4 v5, %[sw], s68 offen offset:0 lds\n\t" \
  "s_mov_b32 s85, 0x1e0000\n\t" \
  "s_load_dword s84, %[pw], s85\n\t" \
  "buffer_load_dwordx4 v6, %[sw], s68 offen offset:1024 lds\n\t" \
  "s_mov_b32 s85, 0x1e0800\n\t" \
  "s_load_dword s84, %[pw], s85\n\t" \
  "buffer_load_dwordx4 v7, %[sw], s68 offen offset:2048 lds\n\t" \
  "s_mov_b32 s85, 0x1e1000\n\t" \
  "s_load_dword s84, %[pw], s85\n\t" \
  "s_add_u32 m0, %[wb], 36864\n\t" \
  "s_nop 0\n\t" \
  "buffer_load_dwordx4 v5, %[sw], s69 offen offset:0 lds\n\t" \
  "s_mov_b32 s85, 0x210000\n\t" \
  "s_load_dword s84, %[pw], s85\n\t" \
  "buffer_load_dwordx4 v6, %[sw], s69 offen offset:1024 lds\n\t" \
  "buffer_load_dwordx4 v7, %[sw], s69 offen offset:2048 lds\n\t" \
  "s_add_u32 s68, %[rx3], 0\n\t" \
  "s_add_u32 s69, %[rw3], 0\n\t" \
  "s_add_u32 m0, %[xb], 36864\n\t" \
  "s_waitcnt vmcnt(15)\n\t" \
  "s_barrier\n\t" \
  "buffer_load_dwordx4 v0, %[sx], s68 offen offset:0 lds\n\t" \
  "s_mov_b32 s85, 0x210800\n\t" \
  "s_load_dword s84, %[pw], s85\n\t" \
  "buffer_load_dwordx4 v1, %[sx], s68 offen offset:1024 lds\n\t" \
  "s_mov_b32 s85, 0x211000\n\t" \
  "s_load_dword s84, %[pw], s85\n\t" \
  "s_mov_b64 exec, 0xffff\n\t" \
  "buffer_load_dwordx4 v4, %[sx], s68 offen offset:2048 lds\n\t" \
  "s_add_u32 m0, %[wb], 49152\n\t" \
  "s_mov_b64 exec, -1\n\t" \
  "buffer_load_dwordx4 v5, %[sw], s69 offen offset:0 lds\n\t" \
  "s_mov_b32 s85, 0x240000\n\t" \
  "s_load_dword s84, %[pw], s85\n\t" \
  "buffer_load_dwordx4 v6, %[sw], s69 offen offset:1024 lds\n\t" \
  "s_mov_b32 s85, 0x240800\n\t" \
  "s_load_dword s84, %[pw], s85\n\t" \
  "buffer_load_dwordx4 v7, %[sw], s69 offen offset:2048 lds\n\t" \
  "s_add_u32 s68, %[rx3], 147456\n\t" \
  "s_add_u32 s69, %[rw3], 196608\n\t" \
  "s_add_u32 m0, %[xb], 46080\n\t" \
  "s_waitcnt vmcnt(18)\n\t" \
  "s_barrier\n\t" \
  "buffer_load_dwordx4 v2, %[sx], s68 offen offset:0 lds\n\t" \
  "s_mov_b32 s85, 0x241000\n\t" \
  "s_load_dword s84, %[pw], s85\n\t" \
  "buffer_load_dwordx4 v3, %[sx], s68 offen offset:1024 lds\n\t" \
  "s_mov_b32 s85, 0x270000\n\t" \
  "s_load_dword s84, %[pw], s85\n\t" \
  "s_mov_b64 exec, 0xffff\n\t" \
  "buffer_load_dwordx4 v4, %[sx], s68 offen offset:2048 lds\n\t" \
  "s_add_u32 m0, %[wb], 61440\n\t" \
  "s_mov_b64 exec, -1\n\t" \
  "buffer_load_dwordx4 v5, %[sw], s69 offen offset:0 lds\n\t" \
  "s_mov_b32 s85, 0x270800\n\t" \
  "s_load_dword s84, %[pw], s85\n\t" \
  "buffer_load_dwordx4 v6, %[sw], s69 offen offset:1024 lds\n\t" \
  "s_mov_b32 s85, 0x271000\n\t" \
  "s_load_dword s84, %[pw], s85\n\t" \
  "buffer_load_dwordx4 v7, %[sw], s69 offen offset:2048 lds\n\t" \
  "s_add_u32 s68, %[rx0], 512\n\t" \
  "s_add_u32 s69, %[rw0], 1572864\n\t" \
  "s_add_u32 m0, %[xb], 0\n\t" \
  "s_waitcnt vmcnt(15)\n\t" \
  "s_barrier\n\t" \
  "buffer_load_dwordx4 v0, %[sx], s68 offen offset:0 lds\n\t" \
  "s_mov_b32 s85, 0x2a0000\n\t" \
  "s_load_dword s84, %[pw], s85\n\t" \
  "buffer_load_dwordx4 v1, %[sx], s68 offen offset:1024 lds\n\t" \
  "s_mov_b32 s85, 0x2a0800\n\t" \
  "s_load_dword s84, %[pw], s85\n\t" \
  "s_mov_b64 exec, 0xffff\n\t" \
  "buffer_load_dwordx4 v4, %[sx], s68 offen offset:2048 lds\n\t" \
  "s_add_u32 m0, %[wb], 0\n\t" \
  "s_mov_b64 exec, -1\n\t" \
  "buffer_load_dwordx4 v5, %[sw], s69 offen offset:0 lds\n\t" \
  "s_mov_b32 s85, 0x2a1000\n\t" \
  "s_load_dword s84, %[pw], s85\n\t" \
  "buffer_load_dwordx4 v6, %[sw], s69 offen offset:1024 lds\n\t" \
  "s_mov_b32 s85, 0x2d0000\n\t" \
  "s_load_dword s84, %[pw], s85\n\t" \
  "buffer_load_dwordx4 v7, %[sw], s69 offen offset:2048 lds\n\t" \
  "s_add_u32 s68, %[rx0], 147968\n\t" \
  "s_add_u32 s69, %[rw0], 1769472\n\t" \
  "s_add_u32 m0, %[xb], 9216\n\t" \
  "s_waitcnt vmcnt(18)\n\t" \
  "s_barrier\n\t" \
  "buffer_load_dwordx4 v2, %[sx], s68 offen offset:0 lds\n\t" \
  "s_mov_b32 s85, 0x2d0800\n\t" \
  "s_load_dword s84, %[pw], s85\n\t" \
  "buffer_load_dwordx4 v3, %[sx], s68 offen offset:1024 lds\n\t" \
  "s_mov_b32 s85, 0x2d1000\n\t" \
  "s_load_dword s84, %[pw], s85\n\t" \
  "s_mov_b64 exec, 0xffff\n\t" \
  "buffer_load_dwordx4 v4, %[sx], s68 offen offset:2048 lds\n\t" \
  "s_add_u32 m0, %[wb], 12288\n\t" \
  "s_mov_b64 exec, -1\n\t" \
  "buffer_load_dwordx4 v5, %[sw], s69 offen offset:0 lds\n\t" \
  "s_mov_b32 s85, 0x300000\n\t" \
  "s_load_dword s84, %[pw], s85\n\t" \
  "buffer_load_dwordx4 v6, %[sw], s69 offen offset:1024 lds\n\t" \
  "s_mov_b32 s85, 0x300800\n\t" \
  "s_load_dword s84, %[pw], s85\n\t" \
  "buffer_load_dwordx4 v7, %[sw], s69 offen offset:2048 lds\n\t" \
  "s_add_u32 s68, %[rx1], 512\n\t" \
  "s_add_u32 s69, %[rw1], 1572864\n\t" \
  "s_add_u32 m0, %[xb], 18432\n\t" \
  "s_waitcnt vmcnt(15)\n\t" \
  "s_barrier\n\t" \
  "buffer_load_dwordx4 v0, %[sx], s68 offen offset:0 lds\n\t" \
  "s_mov_b32 s85, 0x301000\n\t" \
  "s_load_dword s84, %[pw], s85\n\t" \
  "buffer_load_dwordx4 v1, %[sx], s68 offen offset:1024 lds\n\t" \
  "s_mov_b32 s85, 0x330000\n\t" \
  "s_load_dword s84, %[pw], s85\n\t" \
  "s_mov_b64 exec, 0xffff\n\t" \
  "buffer_load_dwordx4 v4, %[sx], s68 offen offset:2048 lds\n\t" \
  "s_add_u32 m0, %[wb], 24576\n\t" \
  "s_mov_b64 exec, -1\n\t" \
  "buffer_load_dwordx4 v5, %[sw], s69 offen offset:0 lds\n\t" \
  "s_mov_b32 s85, 0x330800\n\t" \
  "s_load_dword s84, %[pw], s85\n\t" \
  "buffer_load_dwordx4 v6, %[sw], s69 offen offset:1024 lds\n\t" \
  "s_mov_b32 s85, 0x331000\n\t" \
  "s_load_dword s84, %[pw], s85\n\t" \
  "buffer_load_dwordx4 v7, %[sw], s69 offen offset:2048 lds\n\t" \
  "s_add_u32 s68, %[rx1], 147968\n\t" \
  "s_add_u32 s69, %[rw1], 1769472\n\t" \
  "s_add_u32 m0, %[xb], 27648\n\t" \
  "s_waitcnt vmcnt(18)\n\t" \
  "s_barrier\n\t" \
  "buffer_load_dwordx4 v2, %[sx], s68 offen offset:0 lds\n\t" \
  "s_mov_b32 s85, 0x360000\n\t" \
  "s_load_dword s84, %[pw], s85\n\t" \
  "buffer_load_dwordx4 v3, %[sx], s68 offen offset:1024 lds\n\t" \
  "s_mov_b32 s85, 0x360800\n\t" \
  "s_load_dword s84, %[pw], s85\n\t" \
  "s_mov_b64 exec, 0xffff\n\t" \
  "buffer_load_dwordx4 v4, %[sx], s68 offen offset:2048 lds\n\t" \
  "s_add_u32 m0, %[wb], 36864\n\t" \
  "s_mov_b64 exec, -1\n\t" \
  "buffer_load_dwordx4 v5, %[sw], s69 offen offset:0 lds\n\t" \
  "s_mov_b32 s85, 0x361000\n\t" \
  "s_load_dword s84, %[pw], s85\n\t" \
  "buffer_load_dwordx4 v6, %[sw], s69 offen offset:1024 lds\n\t" \
  "s_mov_b32 s85, 0x390000\n\t" \
  "s_load_dword s84, %[pw], s85\n\t" \
  "buffer_load_dwordx4 v7, %[sw], s69 offen offset:2048 lds\n\t" \
  "s_add_u32 s68, %[rx2], 512\n\t" \
  "s_add_u32 s69, %[rw2], 1572864\n\t" \
  "s_add_u32 m0, %[xb], 36864\n\t" \
  "s_waitcnt vmcnt(15)\n\t" \
  "s_barrier\n\t" \
  "buffer_load_dwordx4 v0, %[sx], s68 offen offset:0 lds\n\t" \
  "s_mov_b32 s85, 0x390800\n\t" \
  "s_load_dword s84, %[pw], s85\n\t" \
  "buffer_load_dwordx4 v1, %[sx], s68 offen offset:1024 lds\n\t" \
  "s_mov_b32 s85, 0x391000\n\t" \
  "s_load_dword s84, %[pw], s85\n\t" \
  "s_mov_b64 exec, 0xffff\n\t" \
  "buffer_load_dwordx4 v4, %[sx], s68 offen offset:2048 lds\n\t" \
  "s_add_u32 m0, %[wb], 49152\n\t" \
  "s_mov_b64 exec, -1\n\t" \
  "buffer_load_dwordx4 v5, %[sw], s69 offen offset:0 lds\n\t" \
  "s_mov_b32 s85, 0x3c0000\n\t" \
  "s_load_dword s84, %[pw], s85\n\t" \
  "buffer_load_dwordx4 v6, %[sw], s69 offen offset:1024 lds\n\t" \
  "s_mov_b32 s85, 0x3c0800\n\t" \
  "s_load_dword s84, %[pw], s85\n\t" \
  "buffer_load_dwordx4 v7, %[sw], s69 offen offset:2048 lds\n\t" \
  "s_add_u32 s68, %[rx2], 147968\n\t" \
  "s_add_u32 s69, %[rw2], 1769472\n\t" \
  "s_add_u32 m0, %[xb], 46080\n\t" \
  "s_waitcnt vmcnt(18)\n\t" \
  "s_barrier\n\t" \
  "buffer_load_dwordx4 v2, %[sx], s68 offen offset:0 lds\n\t" \
  "s_mov_b32 s85, 0x3c1000\n\t" \
  "s_load_dword s84, %[pw], s85\n\t" \
  "buffer_load_dwordx4 v3, %[sx], s68 offen offset:1024 lds\n\t" \
  "s_mov_b32 s85, 0x3f0000\n\t" \
  "s_load_dword s84, %[pw], s85\n\t" \
  "s_mov_b64 exec, 0xffff\n\t" \
  "buffer_load_dwordx4 v4, %[sx], s68 offen offset:2048 lds\n\t" \
  "s_add_u32 m0, %[wb], 61440\n\t" \
  "s_mov_b64 exec, -1\n\t" \
  "buffer_load_dwordx4 v5, %[sw], s69 offen offset:0 lds\n\t" \
  "s_mov_b32 s85, 0x3f0800\n\t" \
  "s_load_dword s84, %[pw], s85\n\t" \
  "buffer_load_dwordx4 v6, %[sw], s69 offen offset:1024 lds\n\t" \
  "s_mov_b32 s85, 0x3f1000\n\t" \
  "s_load_dword s84, %[pw], s85\n\t" \
  "buffer_load_dwordx4 v7, %[sw], s69 offen offset:2048 lds\n\t" \
  "s_add_u32 s68, %[rx3], 512\n\t" \
  "s_add_u32 s69, %[rw3], 1572864\n\t" \
  "s_add_u32 m0, %[xb], 0\n\t" \
  "s_waitcnt vmcnt(15)\n\t" \
  "s_barrier\n\t" \
  "buffer_load_dwordx4 v0, %[sx], s68 offen offset:0 lds\n\t" \
  "s_mov_b32 s85, 0x420000\n\t" \
  "s_load_dword s84, %[pw], s85\n\t" \
  "buffer_load_dwordx4 v1, %[sx], s68 offen offset:1024 lds\n\t" \
  "s_mov_b32 s85, 0x420800\n\t" \
  "s_load_dword s84, %[pw], s85\n\t" \
  "s_mov_b64 exec, 0xffff\n\t" \
  "buffer_load_dwordx4 v4, %[sx], s68 offen offset:2048 lds\n\t" \
  "s_add_u32 m0, %[wb], 0\n\t" \
  "s_mov_b64 exec, -1\n\t" \
  "buffer_load_dwordx4 v5, %[sw], s69 offen offset:0 lds\n\t" \
  "s_mov_b32 s85, 0x421000\n\t" \
  "s_load_dword s84, %[pw], s85\n\t" \
  "buffer_load_dwordx4 v6, %[sw], s69 offen offset:1024 lds\n\t" \
  "s_mov_b32 s85, 0x450000\n\t" \
  "s_load_dword s84, %[pw], s85\n\t" \
  "buffer_load_dwordx4 v7, %[sw], s69 offen offset:2048 lds\n\t" \
  "s_add_u32 s68, %[rx3], 147968\n\t" \
  "s_add_u32 s69, %[rw3], 1769472\n\t" \
  "s_add_u32 m0, %[xb], 9216\n\t" \
  "s_waitcnt vmcnt(18)\n\t" \
  "s_barrier\n\t" \
  "buffer_load_dwordx4 v2, %[sx], s68 offen offset:0 lds\n\t" \
  "s_mov_b32 s85, 0x450800\n\t" \
  "s_load_dword s84, %[pw], s85\n\t" \
  "buffer_load_dwordx4 v3, %[sx], s68 offen offset:1024 lds\n\t" \
  "s_mov_b32 s85, 0x451000\n\t" \
  "s_load_dword s84, %[pw], s85\n\t" \
  "s_mov_b64 exec, 0xffff\n\t" \
  "buffer_load_dwordx4 v4, %[sx], s68 offen offset:2048 lds\n\t" \
  "s_add_u32 m0, %[wb], 12288\n\t" \
  "s_mov_b64 exec, -1\n\t" \
  "buffer_load_dwordx4 v5, %[sw], s69 offen offset:0 lds\n\t" \
  "s_mov_b32 s85, 0x480000\n\t" \
  "s_load_dword s84, %[pw], s85\n\t" \
  "buffer_load_dwordx4 v6, %[sw], s69 offen offset:1024 lds\n\t" \
  "s_mov_b32 s85, 0x480800\n\t" \
  "s_load_dword s84, %[pw], s85\n\t" \
  "buffer_load_dwordx4 v7, %[sw], s69 offen offset:2048 lds\n\t" \
  "s_add_u32 s68, %[rx0], 1024\n\t" \
  "s_add_u32 s69, %[rw0], 3145728\n\t" \
  "s_add_u32 m0, %[xb], 18432\n\t" \
  "s_waitcnt vmcnt(15)\n\t" \
  "s_barrier\n\t" \
  "buffer_load_dwordx4 v0, %[sx], s68 offen offset:0 lds\n\t" \
  "s_mov_b32 s85, 0x481000\n\t" \
  "s_load_dword s84, %[pw], s85\n\t" \
  "buffer_load_dwordx4 v1, %[sx], s68 offen offset:1024 lds\n\t" \
  "s_mov_b32 s85, 0x4b0000\n\t" \
  "s_load_dword s84, %[pw], s85\n\t" \
  "s_mov_b64 exec, 0xffff\n\t" \
  "buffer_load_dwordx4 v4, %[sx], s68 offen offset:2048 lds\n\t" \
  "s_add_u32 m0, %[wb], 24576\n\t" \
  "s_mov_b64 exec, -1\n\t" \
  "buffer_load_dwordx4 v5, %[sw], s69 offen offset:0 lds\n\t" \
  "s_mov_b32 s85, 0x4b0800\n\t" \
  "s_load_dword s84, %[pw], s85\n\t" \
  "buffer_load_dwordx4 v6, %[sw], s69 offen offset:1024 lds\n\t" \
  "s_mov_b32 s85, 0x4b1000\n\t" \
  "s_load_dword s84, %[pw], s85\n\t" \
  "buffer_load_dwordx4 v7, %[sw], s69 offen offset:2048 lds\n\t" \
  "s_add_u32 s68, %[rx0], 148480\n\t" \
  "s_add_u32 s69, %[rw0], 3342336\n\t" \
  "s_add_u32 m0, %[xb], 27648\n\t" \
  "s_waitcnt vmcnt(18)\n\t" \
  "s_barrier\n\t" \
  "buffer_load_dwordx4 v2, %[sx], s68 offen offset:0 lds\n\t" \
  "s_mov_b32 s85, 0x4e0000\n\t" \
  "s_load_dword s84, %[pw], s85\n\t" \
  "buffer_load_dwordx4 v3, %[sx], s68 offen offset:1024 lds\n\t" \
  "s_mov_b32 s85, 0x4e0800\n\t" \
  "s_load_dword s84, %[pw], s85\n\t" \
  "s_mov_b64 exec, 0xffff\n\t" \
  "buffer_load_dwordx4 v4, %[sx], s68 offen offset:2048 lds\n\t" \
  "s_add_u32 m0, %[wb], 36864\n\t" \
  "s_mov_b64 exec, -1\n\t" \
  "buffer_load_dwordx4 v5, %[sw], s69 offen offset:0 lds\n\t" \
  "s_mov_b32 s85, 0x4e1000\n\t" \
  "s_load_dword s84, %[pw], s85\n\t" \
  "buffer_load_dwordx4 v6, %[sw], s69 offen offset:1024 lds\n\t" \
  "s_mov_b32 s85, 0x510000\n\t" \
  "s_load_dword s84, %[pw], s85\n\t" \
  "buffer_load_dwordx4 v7, %[sw], s69 offen offset:2048 lds\n\t" \
  "s_add_u32 s68, %[rx1], 1024\n\t" \
  "s_add_u32 s69, %[rw1], 3145728\n\t" \
  "s_add_u32 m0, %[xb], 36864\n\t" \
  "s_waitcnt vmcnt(15)\n\t" \
  "s_barrier\n\t" \
  "buffer_load_dwordx4 v0, %[sx], s68 offen offset:0 lds\n\t" \
  "s_mov_b32 s85, 0x510800\n\t" \
  "s_load_dword s84, %[pw], s85\n\t" \
  "buffer_load_dwordx4 v1, %[sx], s68 offen offset:1024 lds\n\t" \
  "s_mov_b32 s85, 0x511000\n\t" \
  "s_load_dword s84, %[pw], s85\n\t" \
  "s_mov_b64 exec, 0xffff\n\t" \
  "buffer_load_dwordx4 v4, %[sx], s68 offen offset:2048 lds\n\t" \
  "s_add_u32 m0, %[wb], 49152\n\t" \
  "s_mov_b64 exec, -1\n\t" \
  "buffer_load_dwordx4 v5, %[sw], s69 offen offset:0 lds\n\t" \
  "s_mov_b32 s85, 0x540000\n\t" \
  "s_load_dword s84, %[pw], s85\n\t" \
  "buffer_load_dwordx4 v6, %[sw], s69 offen offset:1024 lds\n\t" \
  "s_mov_b32 s85, 0x540800\n\t" \
  "s_load_dword s84, %[pw], s85\n\t" \
  "buffer_load_dwordx4 v7, %[sw], s69 offen offset:2048 lds\n\t" \
  "s_add_u32 s68, %[rx1], 148480\n\t" \
  "s_add_u32 s69, %[rw1], 3342336\n\t" \
  "s_add_u32 m0, %[xb], 46080\n\t" \
  "s_waitcnt vmcnt(18)\n\t" \
  "s_barrier\n\t" \
  "buffer_load_dwordx4 v2, %[sx], s68 offen offset:0 lds\n\t" \
  "s_mov_b32 s85, 0x541000\n\t" \
  "s_load_dword s84, %[pw], s85\n\t" \
  "buffer_load_dwordx4 v3, %[sx], s68 offen offset:1024 lds\n\t" \
  "s_mov_b32 s85, 0x570000\n\t" \
  "s_load_dword s84, %[pw], s85\n\t" \
  "s_mov_b64 exec, 0xffff\n\t" \
  "buffer_load_dwordx4 v4, %[sx], s68 offen offset:2048 lds\n\t" \
  "s_add_u32 m0, %[wb], 61440\n\t" \
  "s_mov_b64 exec, -1\n\t" \
  "buffer_load_dwordx4 v5, %[sw], s69 offen offset:0 lds\n\t" \
  "s_mov_b32 s85, 0x570800\n\t" \
  "s_load_dword s84, %[pw], s85\n\t" \
  "buffer_load_dwordx4 v6, %[sw], s69 offen offset:1024 lds\n\t" \
  "s_mov_b32 s85, 0x571000\n\t" \
  "s_load_dword s84, %[pw], s85\n\t" \
  "buffer_load_dwordx4 v7, %[sw], s69 offen offset:2048 lds\n\t" \
  "s_add_u32 s68, %[rx2], 1024\n\t" \
  "s_add_u32 s69, %[rw2], 3145728\n\t" \
  "s_add_u32 m0, %[xb], 0\n\t" \
  "s_waitcnt vmcnt(15)\n\t" \
  "s_barrier\n\t" \
  "buffer_load_dwordx4 v0, %[sx], s68 offen offset:0 lds\n\t" \
  "s_mov_b32 s85, 0x5a0000\n\t" \
  "s_load_dword s84, %[pw], s85\n\t" \
  "buffer_load_dwordx4 v1, %[sx], s68 offen offset:1024 lds\n\t" \
  "s_mov_b32 s85, 0x5a0800\n\t" \
  "s_load_dword s84, %[pw], s85\n\t" \
  "s_mov_b64 exec, 0xffff\n\t" \
  "buffer_load_dwordx4 v4, %[sx], s68 offen offset:2048 lds\n\t" \
  "s_add_u32 m0, %[wb], 0\n\t" \
  "s_mov_b64 exec, -1\n\t" \
  "buffer_load_dwordx4 v5, %[sw], s69 offen offset:0 lds\n\t" \
  "s_mov_b32 s85, 0x5a1000\n\t" \
  "s_load_dword s84, %[pw], s85\n\t" \
  "buffer_load_dwordx4 v6, %[sw], s69 offen offset:1024 lds\n\t" \
  "s_mov_b32 s85, 0x5d0000\n\t" \
  "s_load_dword s84, %[pw], s85\n\t" \
  "buffer_load_dwordx4 v7, %[sw], s69 offen offset:2048 lds\n\t" \
  "s_add_u32 s68, %[rx2], 148480\n\t" \
  "s_add_u32 s69, %[rw2], 3342336\n\t" \
  "s_add_u32 m0, %[xb], 9216\n\t" \
  "s_waitcnt vmcnt(18)\n\t" \
  "s_barrier\n\t" \
  "buffer_load_dwordx4 v2, %[sx], s68 offen offset:0 lds\n\t" \
  "s_mov_b32 s85, 0x5d0800\n\t" \
  "s_load_dword s84, %[pw], s85\n\t" \
  "buffer_load_dwordx4 v3, %[sx], s68 offen offset:1024 lds\n\t" \
  "s_mov_b32 s85, 0x5d1000\n\t" \
  "s_load_dword s84, %[pw], s85\n\t" \
  "s_mov_b64 exec, 0xffff\n\t" \
  "buffer_load_dwordx4 v4, %[sx], s68 offen offset:2048 lds\n\t" \
  "s_add_u32 m0, %[wb], 12288\n\t" \
  "s_mov_b64 exec, -1\n\t" \
  "buffer_load_dwordx4 v5, %[sw], s69 offen offset:0 lds\n\t" \
  "s_load_dword s84, %[po], 0x0\n\t" \
  "buffer_load_dwordx4 v6, %[sw], s69 offen offset:1024 lds\n\t" \
  "s_load_dword s84, %[po], 0x40\n\t" \
  "buffer_load_dwordx4 v7, %[sw], s69 offen offset:2048 lds\n\t" \
  "s_mov_b32 m0, %[tsk]\n\t" \
  "s_mov_b64 exec, %[tm]\n\t" \
  "global_load_lds_dword v12, %[tp]\n\t" \
  "s_mov_b64 exec, -1\n\t" \
  "s_add_u32 s68, %[rx3], 1024\n\t" \
  "s_add_u32 s69, %[rw3], 3145728\n\t" \
  "s_add_u32 m0, %[xb], 18432\n\t" \
  "s_waitcnt vmcnt(15)\n\t" \
  "s_barrier\n\t" \
  "buffer_load_dwordx4 v0, %[sx], s68 offen offset:0 lds\n\t" \
  "s_load_dword s84, %[po], 0x80\n\t" \
  "buffer_load_dwordx4 v1, %[sx], s68 offen offset:1024 lds\n\t" \
  "s_load_dword s84, %[po], 0xc0\n\t" \
  "s_mov_b64 exec, 0xffff\n\t" \
  "buffer_load_dwordx4 v4, %[sx], s68 offen offset:2048 lds\n\t" \
  "s_add_u32 m0, %[wb], 24576\n\t" \
  "s_mov_b64 exec, -1\n\t" \
  "buffer_load_dwordx4 v5, %[sw], s69 offen offset:0 lds\n\t" \
  "s_load_dword s84, %[po], 0x100\n\t" \
  "buffer_load_dwordx4 v6, %[sw], s69 offen offset:1024 lds\n\t" \
  "s_load_dword s84, %[po], 0x140\n\t" \
  "buffer_load_dwordx4 v7, %[sw], s69 offen offset:2048 lds\n\t" \
  "s_add_u32 s68, %[rx3], 148480\n\t" \
  "s_add_u32 s69, %[rw3], 3342336\n\t" \
  "s_add_u32 m0, %[xb], 27648\n\t" \
  "s_waitcnt vmcnt(18)\n\t" \
  "s_barrier\n\t" \
  "buffer_load_dwordx4 v2, %[sx], s68 offen offset:0 lds\n\t" \
  "s_load_dword s84, %[po], 0x180\n\t" \
  "buffer_load_dwordx4 v3, %[sx], s68 offen offset:1024 lds\n\t" \
  "s_load_dword s84, %[po], 0x1c0\n\t" \
  "s_mov_b64 exec, 0xffff\n\t" \
  "buffer_load_dwordx4 v4, %[sx], s68 offen offset:2048 lds\n\t" \
  "s_add_u32 m0, %[wb], 36864\n\t" \
  "s_mov_b64 exec, -1\n\t" \
  "buffer_load_dwordx4 v5, %[sw], s69 offen offset:0 lds\n\t" \
  "s_load_dword s84, %[po], 0x200\n\t" \
  "buffer_load_dwordx4 v6, %[sw], s69 offen offset:1024 lds\n\t" \
  "s_load_dword s84, %[po], 0x240\n\t" \
  "buffer_load_dwordx4 v7, %[sw], s69 offen offset:2048 lds\n\t" \
  "s_add_u32 s68, %[rx0], 1536\n\t" \
  "s_add_u32 s69, %[rw0], 4718592\n\t" \
  "s_add_u32 m0, %[xb], 36864\n\t" \
  "s_waitcnt vmcnt(15)\n\t" \
  "s_barrier\n\t" \
  "buffer_load_dwordx4 v0, %[sx], s68 offen offset:0 lds\n\t" \
  "s_load_dword s84, %[po], 0x280\n\t" \
  "buffer_load_dwordx4 v1, %[sx], s68 offen offset:1024 lds\n\t" \
  "s_load_dword s84, %[po], 0x2c0\n\t" \
  "s_mov_b64 exec, 0xffff\n\t" \
  "buffer_load_dwordx4 v4, %[sx], s68 offen offset:2048 lds\n\t" \
  "s_add_u32 m0, %[wb], 49152\n\t" \
  "s_mov_b64 exec, -1\n\t" \
  "buffer_load_dwordx4 v5, %[sw], s69 offen offset:0 lds\n\t" \
  "s_load_dword s84, %[po], 0x300\n\t" \
  "buffer_load_dwordx4 v6, %[sw], s69 offen offset:1024 lds\n\t" \
  "s_load_dword s84, %[po], 0x340\n\t" \
  "buffer_load_dwordx4 v7, %[sw], s69 offen offset:2048 lds\n\t" \
  "s_add_u32 s68, %[rx0], 148992\n\t" \
  "s_add_u32 s69, %[rw0], 4915200\n\t" \
  "s_add_u32 m0, %[xb], 46080\n\t" \
  "s_waitcnt vmcnt(18)\n\t" \
  "s_barrier\n\t" \
  "buffer_load_dwordx4 v2, %[sx], s68 offen offset:0 lds\n\t" \
  "s_load_dword s84, %[po], 0x380\n\t" \
  "buffer_load_dwordx4 v3, %[sx], s68 offen offset:1024 lds\n\t" \
  "s_load_dword s84, %[po], 0x3c0\n\t" \
  "s_mov_b64 exec, 0xffff\n\t" \
  "buffer_load_dwordx4 v4, %[sx], s68 offen offset:2048 lds\n\t" \
  "s_add_u32 m0, %[wb], 61440\n\t" \
  "s_mov_b64 exec, -1\n\t" \
  "buffer_load_dwordx4 v5, %[sw], s69 offen offset:0 lds\n\t" \
  "s_load_dword s84, %[po], 0x400\n\t" \
  "buffer_load_dwordx4 v6, %[sw], s69 offen offset:1024 lds\n\t" \
  "s_load_dword s84, %[po], 0x440\n\t" \
  "buffer_load_dwordx4 v7, %[sw], s69 offen offset:2048 lds\n\t" \
  "s_add_u32 s68, %[rx1], 1536\n\t" \
  "s_add_u32 s69, %[rw1], 4718592\n\t" \
  "s_add_u32 m0, %[xb], 0\n\t" \
  "s_waitcnt vmcnt(15)\n\t" \
  "s_barrier\n\t" \
  "buffer_load_dwordx4 v0, %[sx], s68 offen offset:0 lds\n\t" \
  "s_load_dword s84, %[po], 0x480\n\t" \
  "buffer_load_dwordx4 v1, %[sx], s68 offen offset:1024 lds\n\t" \
  "s_load_dword s84, %[po], 0x4c0\n\t" \
  "s_mov_b64 exec, 0xffff\n\t" \
  "buffer_load_dwordx4 v4, %[sx], s68 offen offset:2048 lds\n\t" \
  "s_add_u32 m0, %[wb], 0\n\t" \
  "s_mov_b64 exec, -1\n\t" \
  "buffer_load_dwordx4 v5, %[sw], s69 offen offset:0 lds\n\t" \
  "s_load_dword s84, %[po], 0x500\n\t" \
  "buffer_load_dwordx4 v6, %[sw], s69 offen offset:1024 lds\n\t" \
  "s_load_dword s84, %[po], 0x540\n\t" \
  "buffer_load_dwordx4 v7, %[sw], s69 offen offset:2048 lds\n\t" \
  "s_add_u32 s68, %[rx1], 148992\n\t" \
  "s_add_u32 s69, %[rw1], 4915200\n\t" \
  "s_add_u32 m0, %[xb], 9216\n\t" \
  "s_waitcnt vmcnt(18)\n\t" \
  "s_barrier\n\t" \
  "buffer_load_dwordx4 v2, %[sx], s68 offen offset:0 lds\n\t" \
  "s_load_dword s84, %[po], 0x580\n\t" \
  "buffer_load_dwordx4 v3, %[sx], s68 offen offset:1024 lds\n\t" \
  "s_load_dword s84, %[po], 0x5c0\n\t" \
  "s_mov_b64 exec, 0xffff\n\t" \
  "buffer_load_dwordx4 v4, %[sx], s68 offen offset:2048 lds\n\t" \
  "s_add_u32 m0, %[wb], 12288\n\t" \
  "s_mov_b64 exec, -1\n\t" \
  "buffer_load_dwordx4 v5, %[sw], s69 offen offset:0 lds\n\t" \
  "s_load_dword s84, %[po], 0x600\n\t" \
  "buffer_load_dwordx4 v6, %[sw], s69 offen offset:1024 lds\n\t" \
  "s_load_dword s84, %[po], 0x640\n\t" \
  "buffer_load_dwordx4 v7, %[sw], s69 offen offset:2048 lds\n\t" \
  "s_add_u32 s68, %[rx2], 1536\n\t" \
  "s_add_u32 s69, %[rw2], 4718592\n\t" \
  "s_add_u32 m0, %[xb], 18432\n\t" \
  "s_waitcnt vmcnt(15)\n\t" \
  "s_barrier\n\t" \
  "buffer_load_dwordx4 v0, %[sx], s68 offen offset:0 lds\n\t" \
  "s_load_dword s84, %[po], 0x680\n\t" \
  "buffer_load_dwordx4 v1, %[sx], s68 offen offset:1024 lds\n\t" \
  "s_load_dword s84, %[po], 0x6c0\n\t" \
  "s_mov_b64 exec, 0xffff\n\t" \
  "buffer_load_dwordx4 v4, %[sx], s68 offen offset:2048 lds\n\t" \
  "s_add_u32 m0, %[wb], 24576\n\t" \
  "s_mov_b64 exec, -1\n\t" \
  "buffer_load_dwordx4 v5, %[sw], s69 offen offset:0 lds\n\t" \
  "s_load_dword s84, %[po], 0x700\n\t" \
  "buffer_load_dwordx4 v6, %[sw], s69 offen offset:1024 lds\n\t" \
  "s_load_dword s84, %[po], 0x740\n\t" \
  "buffer_load_dwordx4 v7, %[sw], s69 offen offset:2048 lds\n\t" \
  "s_add_u32 s68, %[rx2], 148992\n\t" \
  "s_add_u32 s69, %[rw2], 4915200\n\t" \
  "s_add_u32 m0, %[xb], 27648\n\t" \
  "s_waitcnt vmcnt(18)\n\t" \
  "s_barrier\n\t" \
  "buffer_load_dwordx4 v2, %[sx], s68 offen offset:0 lds\n\t" \
  "s_load_dword s84, %[po], 0x780\n\t" \
  "buffer_load_dwordx4 v3, %[sx], s68 offen offset:1024 lds\n\t" \
  "s_load_dword s84, %[po], 0x7c0\n\t" \
  "s_mov_b64 exec, 0xffff\n\t" \
  "buffer_load_dwordx4 v4, %[sx], s68 offen offset:2048 lds\n\t" \
  "s_add_u32 m0, %[wb], 36864\n\t" \
  "s_mov_b64 exec, -1\n\t" \
  "buffer_load_dwordx4 v5, %[sw], s69 offen offset:0 lds\n\t" \
  "buffer_load_dwordx4 v6, %[sw], s69 offen offset:1024 lds\n\t" \
  "buffer_load_dwordx4 v7, %[sw], s69 offen offset:2048 lds\n\t" \
  "s_add_u32 s68, %[rx3], 1536\n\t" \
  "s_add_u32 s69, %[rw3], 4718592\n\t" \
  "s_add_u32 m0, %[xb], 36864\n\t" \
  "s_waitcnt vmcnt(15)\n\t" \
  "s_barrier\n\t" \
  "buffer_load_dwordx4 v0, %[sx], s68 offen offset:0 lds\n\t" \
  "buffer_load_dwordx4 v1, %[sx], s68 offen offset:1024 lds\n\t" \
  "s_mov_b64 exec, 0xffff\n\t" \
  "buffer_load_dwordx4 v4, %[sx], s68 offen offset:2048 lds\n\t" \
  "s_add_u32 m0, %[wb], 49152\n\t" \
  "s_mov_b64 exec, -1\n\t" \
  "buffer_load_dwordx4 v5, %[sw], s69 offen offset:0 lds\n\t" \
  "buffer_load_dwordx4 v6, %[sw], s69 offen offset:1024 lds\n\t" \
  "buffer_load_dwordx4 v7, %[sw], s69 offen offset:2048 lds\n\t" \
  "s_add_u32 s68, %[rx3], 148992\n\t" \
  "s_add_u32 s69, %[rw3], 4915200\n\t" \
  "s_add_u32 m0, %[xb], 46080\n\t" \
  "s_waitcnt vmcnt(18)\n\t" \
  "s_barrier\n\t" \
  "buffer_load_dwordx4 v2, %[sx], s68 offen offset:0 lds\n\t" \
  "buffer_load_dwordx4 v3, %[sx], s68 offen offset:1024 lds\n\t" \
  "s_mov_b64 exec, 0xffff\n\t" \
  "buffer_load_dwordx4 v4, %[sx], s68 offen offset:2048 lds\n\t" \
  "s_add_u32 m0, %[wb], 61440\n\t" \
  "s_mov_b64 exec, -1\n\t" \
  "buffer_load_dwordx4 v5, %[sw], s69 offen offset:0 lds\n\t" \
  "buffer_load_dwordx4 v6, %[sw], s69 offen offset:1024 lds\n\t" \
  "buffer_load_dwordx4 v7, %[sw], s69 offen offset:2048 lds\n\t" \
  "v_readlane_b32 s68, v9, 0\n\t" \
  "v_readlane_b32 s69, v9, 1\n\t" \
  "v_readlane_b32 s70, v9, 2\n\t" \
  "v_readlane_b32 s71, v9, 18\n\t" \
  "v_readlane_b32 s72, v9, 19\n\t" \
  "s_add_u32 m0, %[ob], 0\n\t" \
  "s_waitcnt vmcnt(15)\n\t" \
  "s_barrier\n\t" \
  "s_nop 2\n\t" \
  "buffer_load_dwordx4 v8, %[so], s68 offen lds\n\t" \
  "s_add_u32 m0, %[ob], 4096\n\t" \
  "s_nop 0\n\t" \
  "buffer_load_dwordx4 v8, %[so], s69 offen lds\n\t" \
  "s_add_u32 m0, %[ob], 8192\n\t" \
  "s_nop 0\n\t" \
  "buffer_load_dwordx4 v8, %[so], s70 offen lds\n\t" \
  "s_add_u32 m0, %[ob], 73728\n\t" \
  "s_nop 0\n\t" \
  "buffer_load_dwordx4 v8, %[so], s71 offen lds\n\t" \
  "s_add_u32 m0, %[ob], 77824\n\t" \
  "s_nop 0\n\t" \
  "buffer_load_dwordx4 v8, %[so], s72 offen lds\n\t" \
  "v_readlane_b32 s68, v9, 3\n\t" \
  "v_readlane_b32 s69, v9, 4\n\t" \
  "v_readlane_b32 s70, v9, 5\n\t" \
  "v_readlane_b32 s71, v9, 20\n\t" \
  "v_readlane_b32 s72, v9, 21\n\t" \
  "s_add_u32 m0, %[ob], 12288\n\t" \
  "s_waitcnt vmcnt(17)\n\t" \
  "s_barrier\n\t" \
  "s_nop 2\n\t" \
  "buffer_load_dwordx4 v8, %[so], s68 offen lds\n\t" \
  "s_add_u32 m0, %[ob], 16384\n\t" \
  "s_nop 0\n\t" \
  "buffer_load_dwordx4 v8, %[so], s69 offen lds\n\t" \
  "s_add_u32 m0, %[ob], 20480\n\t" \
  "s_nop 0\n\t" \
  "buffer_load_dwordx4 v8, %[so], s70 offen lds\n\t" \
  "s_add_u32 m0, %[ob], 81920\n\t" \
  "s_nop 0\n\t" \
  "buffer_load_dwordx4 v8, %[so], s71 offen lds\n\t" \
  "s_add_u32 m0, %[ob], 86016\n\t" \
  "s_nop 0\n\t" \
  "buffer_load_dwordx4 v8, %[so], s72 offen lds\n\t" \
  "v_readlane_b32 s68, v9, 6\n\t" \
  "v_readlane_b32 s69, v9, 7\n\t" \
  "v_readlane_b32 s70, v9, 8\n\t" \
  "v_readlane_b32 s71, v9, 22\n\t" \
  "v_readlane_b32 s72, v9, 23\n\t" \
  "v_readlane_b32 s73, v9, 24\n\t" \
  "s_add_u32 m0, %[ob], 24576\n\t" \
  "s_waitcnt vmcnt(13)\n\t" \
  "s_barrier\n\t" \
  "s_nop 2\n\t" \
  "buffer_load_dwordx4 v8, %[so], s68 offen lds\n\t" \
  "s_add_u32 m0, %[ob], 28672\n\t" \
  "s_nop 0\n\t" \
  "buffer_load_dwordx4 v8, %[so], s69 offen lds\n\t" \
  "s_add_u32 m0, %[ob], 32768\n\t" \
  "s_nop 0\n\t" \
  "buffer_load_dwordx4 v8, %[so], s70 offen lds\n\t" \
  "s_add_u32 m0, %[ob], 90112\n\t" \
  "s_nop 0\n\t" \
  "buffer_load_dwordx4 v8, %[so], s71 offen lds\n\t" \
  "s_add_u32 m0, %[ob], 94208\n\t" \
  "s_nop 0\n\t" \
  "buffer_load_dwordx4 v8, %[so], s72 offen lds\n\t" \
  "s_add_u32 m0, %[ob], 98304\n\t" \
  "s_nop 0\n\t" \
  "buffer_load_dwordx4 v8, %[so], s73 offen lds\n\t" \
  "v_readlane_b32 s68, v9, 9\n\t" \
  "v_readlane_b32 s69, v9, 10\n\t" \
  "v_readlane_b32 s70, v9, 11\n\t" \
  "v_readlane_b32 s71, v9, 25\n\t" \
  "v_readlane_b32 s72, v9, 26\n\t" \
  "s_add_u32 m0, %[ob], 36864\n\t" \
  "s_waitcnt vmcnt(16)\n\t" \
  "s_barrier\n\t" \
  "s_nop 2\n\t" \
  "buffer_load_dwordx4 v8, %[so], s68 offen lds\n\t" \
  "s_add_u32 m0, %[ob], 40960\n\t" \
  "s_nop 0\n\t" \
  "buffer_load_dwordx4 v8, %[so], s69 offen lds\n\t" \
  "s_add_u32 m0, %[ob], 45056\n\t" \
  "s_nop 0\n\t" \
  "buffer_load_dwordx4 v8, %[so], s70 offen lds\n\t" \
  "s_add_u32 m0, %[ob], 102400\n\t" \
  "s_nop 0\n\t" \
  "buffer_load_dwordx4 v8, %[so], s71 offen lds\n\t" \
  "s_add_u32 m0, %[ob], 106496\n\t" \
  "s_nop 0\n\t" \
  "buffer_load_dwordx4 v8, %[so], s72 offen lds\n\t" \
  "v_readlane_b32 s68, v9, 12\n\t" \
  "v_readlane_b32 s69, v9, 13\n\t" \
  "v_readlane_b32 s70, v9, 14\n\t" \
  "v_readlane_b32 s71, v9, 27\n\t" \
  "v_readlane_b32 s72, v9, 28\n\t" \
  "v_readlane_b32 s73, v9, 29\n\t" \
  "s_add_u32 m0, %[ob], 49152\n\t" \
  "s_nop 3\n\t" \
  "s_barrier\n\t" \
  "s_nop 2\n\t" \
  "buffer_load_dwordx4 v8, %[so], s68 offen lds\n\t" \
  "s_add_u32 m0, %[ob], 53248\n\t" \
  "s_nop 0\n\t" \
  "buffer_load_dwordx4 v8, %[so], s69 offen lds\n\t" \
  "s_add_u32 m0, %[ob], 57344\n\t" \
  "s_nop 0\n\t" \
  "buffer_load_dwordx4 v8, %[so], s70 offen lds\n\t" \
  "s_add_u32 m0, %[ob], 110592\n\t" \
  "s_nop 0\n\t" \
  "buffer_load_dwordx4 v8, %[so], s71 offen lds\n\t" \
  "s_add_u32 m0, %[ob], 114688\n\t" \
  "s_nop 0\n\t" \
  "buffer_load_dwordx4 v8, %[so], s72 offen lds\n\t" \
  "s_add_u32 m0, %[ob], 118784\n\t" \
  "s_nop 0\n\t" \
  "buffer_load_dwordx4 v8, %[so], s73 offen lds\n\t" \
  "v_readlane_b32 s68, v9, 15\n\t" \
  "v_readlane_b32 s69, v9, 16\n\t" \
  "v_readlane_b32 s70, v9, 17\n\t" \
  "v_readlane_b32 s71, v9, 30\n\t" \
  "v_readlane_b32 s72, v9, 31\n\t" \
  "s_add_u32 m0, %[ob], 61440\n\t" \
  "LS_%=:\n\t" \
  "ds_read_b32 v11, v10\n\t" \
  "s_addk_i32 %[cn], 1\n\t" \
  "s_cmp_gt_u32 %[cn], 0x400000\n\t" \
  "s_cbranch_scc1 LX_%=\n\t" \
  "s_waitcnt lgkmcnt(0)\n\t" \
  "v_cmp_eq_u32 vcc, 0, v11\n\t" \
  "s_cbranch_vccz LX_%=\n\t" \
  "s_sleep 1\n\t" \
  "s_branch LS_%=\n\t" \
  "LX_%=:\n\t" \
  "s_nop 2\n\t" \
  "buffer_load_dwordx4 v8, %[so], s68 offen lds\n\t" \
  "s_add_u32 m0, %[ob], 65536\n\t" \
  "s_nop 0\n\t" \
  "buffer_load_dwordx4 v8, %[so], s69 offen lds\n\t" \
  "s_add_u32 m0, %[ob], 69632\n\t" \
  "s_nop 0\n\t" \
  "buffer_load_dwordx4 v8, %[so], s70 offen lds\n\t" \
  "s_add_u32 m0, %[ob], 122880\n\t" \
  "s_nop 0\n\t" \
  "buffer_load_dwordx4 v8, %[so], s71 offen lds\n\t" \
  "s_add_u32 m0, %[ob], 126976\n\t" \
  "s_nop 0\n\t" \
  "buffer_load_dwordx4 v8, %[so], s72 offen lds\n\t" \
  "s_waitcnt vmcnt(0) lgkmcnt(0)\n\t"
#define LD_CLOB "v11", "s68", "s69", "s70", "s71", "s72", "s73", "s74", "s75", "s76", "s77", "s78", "s79", "s80", "s81", "s82", "s83", "s84", "s85"

#define ENDW
#define TS(i)
#define TOUT(n)

#define SPIN(cond,slp) do{ int sp_=0; while(cond){ __builtin_amdgcn_s_sleep(slp); if(++sp_>(1<<22)) break; } }while(0)
__device__ __forceinline__ unsigned rd1(const char* p){ return __builtin_amdgcn_readfirstlane(*(volatile LDS_AS unsigned*)(LDS_AS char*)p); }
__device__ __forceinline__ unsigned lds_inc(const char* p, int l){ unsigned r=0; if(l==0) r=__hip_atomic_fetch_add((LDS_AS unsigned*)(LDS_AS char*)p, 1u, __ATOMIC_RELAXED, __HIP_MEMORY_SCOPE_WORKGROUP); return __builtin_amdgcn_readfirstlane(r); }
__device__ __forceinline__ void lds_add(unsigned adr){ asm volatile("s_mov_b64 exec, 1\n\tds_add_u32 %0, %1\n\ts_mov_b64 exec, -1" :: "v"(adr), "v"(1u) : "memory"); }
__device__ __forceinline__ void bars(int n){
  asm volatile("BL_%=:\n\ts_barrier\n\ts_sub_u32 %0, %0, 1\n\ts_cmp_lg_u32 %0, 0\n\ts_cbranch_scc1 BL_%=" : "+s"(n) :: "memory", "scc");
}
__device__ __forceinline__ void barc(unsigned& rem){
  asm volatile("s_cmp_eq_u32 %0, 0\n\ts_cbranch_scc1 BC_%=\n\ts_barrier\n\ts_sub_u32 %0, %0, 1\n\tBC_%=:" : "+s"(rem) :: "memory", "scc");
}
template<int SLP> __device__ __forceinline__ void wait_flag_b(const unsigned* p, unsigned voff, unsigned tag, unsigned& rem){
  int spins=0;
  while(true){
    unsigned v; asm volatile("global_load_dword %0, %1, %2 sc1" : "=v"(v) : "v"(voff), "s"(p) : "memory");
    barc(rem); if(PB2) barc(rem);
    asm volatile("s_waitcnt vmcnt(0)" : "+v"(v) :: "memory");
    if(__all(v==tag) || ++spins>(1<<21)) break;
    if(rem==0) __builtin_amdgcn_s_sleep(SLP);
  }
}
__device__ __forceinline__ void stout(void* sbase, unsigned voff, u32x4 v){
  st16cs(sbase,voff,v);
}

__global__ __launch_bounds__(768,3) void fused_kernel(const u16* __restrict__ x, const u16* __restrict__ wqkv,
    const u16* __restrict__ kin, const u16* __restrict__ vin, const u16* __restrict__ shk,
    u16* o, u16* kvT, const u16* __restrict__ hk, const u16* __restrict__ wo, const u16* __restrict__ qin,
    const u16* __restrict__ sD, u16* yT, u16* __restrict__ out, unsigned* flags)
{
  __shared__ __attribute__((aligned(128))) char smem[ALL_LDS];
  TS(0);
  const int w8=__builtin_amdgcn_readfirstlane(threadIdx.x>>6);
  const int w=w8&3;
  const int bid=blockIdx.x, xcd=bid&7, idx=bid>>3;
  const int rb=(xcd>>1)*4+(idx&3), hp=(xcd&1)*8+(idx>>2);
  const int s0=rb*128;
  const int ps=2*(hp&7)+(hp>>3);
  #define LANEVARS int l; asm volatile("v_mbcnt_lo_u32_b32 %0, -1, 0\n\tv_mbcnt_hi_u32_b32 %0, -1, %0" : "=v"(l)); const int g=l>>4, fr=l&15, tq=(l&15)>>2, tp=l&3; (void)g; (void)fr; (void)tq; (void)tp;
  const unsigned sb0=__builtin_amdgcn_readfirstlane(lds_addr(smem));
  const char* syn=smem+SYN;
  const int hot0=(3-(int)((size_t)wqkv>>7))&7;

  if(w8>=8){
    char* cr=smem+C_OFF+2048+w*4096;
    const unsigned crs=sb0+C_OFF+2048+w*4096;
    const int sblk=bid>>3, td0=(bid&7)*128+(w&1)*64;
    const int d=(bid>>4)*64+(((bid>>3)&1)*4+w)*8+(bid&7);
    const int kv0=KV0+(sblk&KVS);
    static_assert(KV0+KVS+PP_NB<=33, "barrier budget");
    unsigned tagv; f32x8 RB;
    const int fd0=(bid&15)*64, fs0=(bid>>4)*128+w*32;
    u32x4 qv[4]; const char* qp=(const char*)(qin+(size_t)fs0*DM+fd0);
    f32x16 A0,A1,A2,A3;
    {
    LANEVARS
    if(w==0 && l<25 && l!=16) ((unsigned*)(smem+SYN))[l]=0u;
    if(l<32) *(u32x4*)(smem+C_OFF+w*512+l*16)=u32x4{0,0,0,0};
    asm volatile("" ::: "memory");
    __builtin_amdgcn_s_setprio(CPR);
    const char* hcol=(const char*)wqkv+((xcd&1)*8+hot0)*128+(size_t)idx*2*6144;
    const unsigned hto=(unsigned)((l/6)*393216+(l%6)*2048);
    {
      const int q=w*64+l, q2=(3-w)*64+l;
      if(w*64<8*TP1){ if(q<8*TP1) touch((const char*)x+(size_t)(xcd*256+idx*8)*2048, (q/TP1)*2048+(q%TP1)*128, sb0+SINK+256); }
      if((3-w)*64<12*TPW){ if(q2<12*TPW){ const int e=q2%12+(idx&1)*12; touch((const char*)wqkv+(size_t)((xcd>>1)*16+(idx>>1))*6144+(xcd&1)*1024, (q2/12)*393216+(e>>3)*2048+(e&7)*128, sb0+SINK+512); } }
      if(HOTP && w==(TP1>8? 0:1)){ if(l<48) touch(hcol+393216, hto, sb0+SINK+512); }
      if(TWO==1 && w==(TP1>8? 0:1)) touch((const char*)wo+(size_t)bid*8192, l*128, sb0+SINK+512);
    }
    {
      const int dg=l&7, rg=l>>3;
      const int rw=sblk*64+(w>>1)*32;
      const unsigned ko=(unsigned)(rg*8192+dg*16);
      const bool top=(rw==0 && rg==0);
      const char* kb=(const char*)(kin+(size_t)rw*DM+td0); const char* vb=(const char*)(vin+(size_t)rw*DM+td0);
      unsigned long long zm; asm("s_cmp_eq_u32 %1, 0\n\ts_cselect_b64 %0, 0xff, 0" : "=s"(zm) : "s"(rw) : "scc");
      unsigned long long tm; asm("s_cmp_eq_u32 %1, 0\n\ts_cselect_b64 %0, %2, 0\n\ts_cmp_eq_u32 %1, 1\n\ts_cselect_b64 %0, %3, %0" : "=&s"(tm) : "s"(w), "s"(HOTP? (1ull<<42)-1 : 0ull), "s"(TWO==3? ~0ull : 0ull) : "scc");
      const int j=rw>>5;
      unsigned so=(unsigned)(rg*32768+((j>>2)&3)*128+((dg&4)|((dg+j)&3))*16), t0;
      unsigned rb0[2];
      #pragma unroll
      for(int k=0;k<2;k++){ const int q=fr-1-2*g+k; rb0[k]=sb0+C_OFF+(4+(q>>1))*256+((q>>3)&3)*64+((w+(q>>1))&3)*16+(q&1)*8; }
      asm volatile(PP_ASM
        : [t0]"=&s"(t0), "+{v159}"(so), "=&{v160}"(tagv), "=&{v[24:31]}"(RB), "=&{v[44:47]}"(qv[0]), "=&{v[48:51]}"(qv[1]), "=&{v[52:55]}"(qv[2]), "=&{v[56:59]}"(qv[3]),
          "=&{v[64:79]}"(A0), "=&{v[80:95]}"(A1), "=&{v[96:111]}"(A2), "=&{v[112:127]}"(A3)
        : "{v152}"(top? ko+6144:ko), "{v153}"(top? ko+6144:ko+2048), "{v154}"(top? ko+6144:ko+4096), "{v155}"(ko), "{v156}"(sb0+EX_OFF+w*1024+(w&1)*512+dg*64),
          "{v157}"((l&8)? 0x03020706u : 0x05040100u), "{v166}"(rb0[0]), "{v167}"(rb0[1]), [qb0]"s"(qp), [qb2]"s"(qp+4096), [mk]"s"(0x180u), [em]"s"(1ull|(512ull<<w)), [hd]"s"(crs),
          [kb0]"s"(kb-6144), [kb3]"s"(kb), [kb5]"s"(kb+4096), [vb0]"s"(vb), [vb2]"s"(vb+4096), [sb]"s"((char*)kvT+(size_t)td0*4096+j*512),
          [n0]"s"(kv0), "{v165}"(crs+(32*g-4*tq+4*tp)*2), [ff]"s"(flags+F_PP+(td0>>6)*64+sblk*2+(w>>1)), [zm]"s"(zm), "{v163}"(l*16), [shd]"s"(sb0+EX_OFF+w*1024), [tgd]"s"(sb0+SYN+64), [shp]"s"(shk+(bid&7)*512), [fl]"s"(flags), [hp]"s"(hk+(size_t)d*S), [dp]"s"(sD+d),
          "{v164}"(w? (unsigned)(l*128) : (l<42? hto:0u)), [tsk]"s"(sb0+SINK+512), [tm]"s"(tm), [tp]"s"(w? (const char*)wo+(size_t)bid*8192 : hcol+9*393216)
        : "memory", "scc", PP_CLOB);
    }
    }
    LANEVARS
    const unsigned tag=__builtin_amdgcn_readfirstlane(tagv)+1u;
    unsigned rem=33-PP_NB-kv0;
    barc(rem);
    const unsigned* pf=flags+F_PP+(d>>6)*64;

    const char* kvp=(const char*)kvT+(size_t)((bid>>4)*8+(bid&7))*32768+((bid>>3)&1)*64+w*8192;
 const unsigned kvo=(unsigned)((l>>2)*128+(l&3)*16);
    int bidx=((l>=16)? l-16 : (l==0? 63 : l+47))*4;
    asm volatile("" : "+{v32}"(bidx));
    unsigned qo=(unsigned)((l>>3)*8192+(l&7)*16);
    asm volatile("" : "+v"(qo));
    __builtin_amdgcn_s_setprio(3);
    while(rem>PREM) barc(rem);
    wait_flag_b<8>(pf, l*4, tag, rem);
    #pragma unroll
    for(int i=0;i<4;i++){ if(i%KTH==0 && (i || KT0)) barc(rem);
      gldsc(kvp+i*2048, kvo, crs+i*1024);
    }
    #define QLOAD asm volatile("global_load_dwordx4 %0, %4, %5\n\tglobal_load_dwordx4 %1, %4, %6\n\tglobal_load_dwordx4 %2, %4, %7\n\tglobal_load_dwordx4 %3, %4, %8" \
        : "=&{v[44:47]}"(qv[0]), "=&{v[48:51]}"(qv[1]), "=&{v[52:55]}"(qv[2]), "=&{v[56:59]}"(qv[3]) : "v"(qo), "s"(qp), "s"(qp+2048), "s"(qp+4096), "s"(qp+6144) : "memory");
    barc(rem); barc(rem);
    WAITV(0);
    asm volatile("s_mov_b32 m0, %2\n\ts_mov_b64 exec, 1\n\tglobal_load_lds_dword %0, %1 sc1\n\ts_mov_b64 exec, -1" :: "v"(0u), "s"(pf), "s"(sb0+SYN+100+w*4) : "memory");
    while(rem>CREM) barc(rem);
    { int sp=0; while(!__all(*(volatile LDS_AS unsigned*)(LDS_AS char*)(syn+100+(l&3)*4)==tag) && ++sp<(1<<22)){ if(rem) barc(rem); else __builtin_amdgcn_s_sleep(1); } }
    SPIN(rd1(syn+32+w*4)==0u,1);
    __builtin_amdgcn_s_setprio(CVP);
    TS(3);
    f32x16 ac0, ac1;
    {
      unsigned p0,p1,p2,p3,p4,p5,p6;
      asm volatile("s_nop 1\n\t" CV_ASM
        : "=&{v[128:143]}"(ac0), "=&{v[144:159]}"(ac1), [p0]"=&s"(p0), [p1]"=&s"(p1), [p2]"=&s"(p2), [p3]"=&s"(p3), [p4]"=&s"(p4), [p5]"=&s"(p5), [p6]"=&s"(p6)
          , "+{v[44:47]}"(qv[0]), "+{v[48:51]}"(qv[1]), "+{v[52:55]}"(qv[2]), "+{v[56:59]}"(qv[3]), [rem]"+s"(rem)
        : "{v[64:79]}"(A0), "{v[80:95]}"(A1), "{v[96:111]}"(A2), "{v[112:127]}"(A3),
          "{v[24:31]}"(RB), "{v32}"(bidx), "{v33}"(sb0+SYN+80+w*4)
        : "memory", "scc", CV_CLOB);
    }
    TS(4);
    __builtin_amdgcn_s_setprio(3);
    {
      const unsigned yo=(unsigned)((256*(g&1)+16*fr+4*(g&2))*2);
      #pragma unroll
      for(int J=0;J<8;J+=2){
        #define YA(i) ((i)<16? ac0[(i)&15] : ac1[(i)&15])
        const u32x2 a={pack2(YA(4*J),YA(4*J+1)),pack2(YA(4*J+2),YA(4*J+3))}, b={pack2(YA(4*J+4),YA(4*J+5)),pack2(YA(4*J+6),YA(4*J+7))};
        st16cs(yT+(size_t)d*S+256*J, yo, widen(a,b));
      }
    }
    barc(rem); barc(rem);
    WAITV(0);
    if(lds_inc(syn+24,l)==3u){ if(l<16) stflag(flags+F_Y+(l*16+(bid>>4))*16+(bid&15), tag); }
    TS(5);
    if(w==0){ wait_flag_b<YSLP>(flags+F_Y+bid*16, (l&15)*4, tag, rem);
      { int sp=0; while(rd1(syn+24)<4u && ++sp<(1<<22)){ if(rem) barc(rem); else __builtin_amdgcn_s_sleep(1); } }
      PUB(sb0+(l==0? SYN+28 : SINK+l*4), 1u); }
    else { int sp=0; while(rd1(syn+28)==0u && ++sp<(1<<22)){ if(rem) barc(rem); else __builtin_amdgcn_s_sleep(4); } }
    SPIN(rd1(syn+96)==0u,2);
    #pragma unroll
    for(int i=0;i<4;i++) gldsc(yT+(size_t)(fd0+i*16)*S+fs0, (l>>2)*4096+(l&3)*16, crs+i*1024);
    barc(rem); barc(rem);
    asm volatile("s_waitcnt vmcnt(0)" : "+{v[44:47]}"(qv[0]), "+{v[48:51]}"(qv[1]), "+{v[52:55]}"(qv[2]), "+{v[56:59]}"(qv[3]) :: "memory");
    {
      u32x4 y2[4];
      #pragma unroll
      for(int i=0;i<4;i++){ const char* yp=cr+((i>>1)*32+g*8+tq)*64+(i&1)*32+tp*8; y2[i]=__builtin_bit_cast(u32x4,cat44(trread(yp),trread(yp+256))); }
      asm volatile("" : "+v"(y2[0]), "+v"(y2[1]), "+v"(y2[2]), "+v"(y2[3]) :: "memory");
      #pragma unroll
      for(int i=0;i<4;i++) *(u32x4*)(cr+((i&1)*16+fr)*128+((((i>>1)*4+g)^(fr&7))*16))=y2[i];
      asm volatile("" ::: "memory");
      char* op=(char*)(out+(size_t)fs0*2048+fd0); const unsigned oo=(unsigned)((l>>3)*16384+(l&7)*16);
      #pragma unroll
      for(int i=0;i<4;i++){
        const int row=4*(l>>3)+i;
        const u32x4 y4=*(const u32x4*)(cr+row*128+(((l&7)^(row&7))*16)), q4=qv[i]; u32x4 ov;
        #pragma unroll
        for(int e=0;e<4;e++) ov[e]=pack2(bflo(q4[e])*bflo(y4[e]), bfhi(q4[e])*bfhi(y4[e]));
        stout(op+i*4096, oo, ov);
      }
    }
    while(rem) barc(rem);
    ENDW TS(6); TOUT(7);
    return;
  }

  if(w8>=4){
    LANEVARS
    __builtin_amdgcn_s_setprio(LPR);
    unsigned ltr=0, ltr2=0, ltr3=0; (void)ltr; (void)ltr2; (void)ltr3;
    {
      const int xr=18*w+(l>>3);
      const unsigned ch=(unsigned)(((l&7)^(xr&7))*16);
      const unsigned xo=(unsigned)(xr*2048)+ch;
      const unsigned xoA=(s0==0 && xr<16)? 16*2048+ch : xo, xoB=(s0==0 && xr+8<16)? 8*2048+ch : xo;
      unsigned wof[3];
      #pragma unroll
      for(int i=0;i<3;i++){ const int P=3*w+i, wr=8*(P&3)+(l>>3); wof[i]=(unsigned)(wr*6144+(P>>2)*2048-1024*i+((((l&7)>>1)^wswz(wr))*32)+(l&1)*16); }
      const unsigned wof2=(unsigned)(w*16384+(l>>3)*2048+((((l&7)>>1)^((l>>4)&3))*32)+(l&1)*16);
      const int pos=(ps+(l>>1))&15;
      const unsigned tab=(unsigned)(((pos>>1)+8*(pos&1))*131072+(l&1)*65536);
      const size_t xb=(size_t)x+(ptrdiff_t)(s0-16)*2048, wb=(size_t)(wqkv+hp*64), ob=(size_t)(wo+hp*64);
      const u32x4 sx={(unsigned)xb,(unsigned)(xb>>32)&0xffffu,144u*2048u,0x20000u}, sw={(unsigned)wb,(unsigned)(wb>>32)&0xffffu,6291456u-hp*128u,0x20000u}, so={(unsigned)ob,(unsigned)(ob>>32)&0xffffu,2097152u-hp*128u,0x20000u};
      const int jj=(ROT && (hp&7)==hot0)? (rb&3) : 0;
      unsigned long long tm; asm("s_cmp_eq_u32 %1, 0\n\ts_cselect_b64 %0, %2, 0" : "=s"(tm) : "s"(w), "s"(WTL==2? 0xffffffffull : 0ull) : "scc");
      unsigned cn=0;
      asm volatile(LD_ASM
        : [cn]"+s"(cn)
        : "{v0}"(xoA), "{v1}"(xoB+15360), "{v2}"(xo), "{v3}"(xo+15360), "{v4}"(xo+30720), "{v5}"(wof[0]), "{v6}"(wof[1]), "{v7}"(wof[2]), "{v8}"(wof2), "{v9}"(tab),
          "{v10}"(sb0+SYN+32+(l&3)*4), "{v12}"((l&31)*2048),
          [sx]"s"(sx), [sw]"s"(sw), [so]"s"(so), [xb]"s"(sb0+X_OFF+w*2304), [wb]"s"(sb0+w*3072), [ob]"s"(sb0+w*1024),
          [rx0]"s"(jj*128), [rx1]"s"(((jj+1)&3)*128), [rx2]"s"(((jj+2)&3)*128), [rx3]"s"(((jj+3)&3)*128),
          [rw0]"s"(jj*393216), [rw1]"s"(((jj+1)&3)*393216), [rw2]"s"(((jj+2)&3)*393216), [rw3]"s"(((jj+3)&3)*393216),
          [pw]"s"((const char*)wqkv+(size_t)idx*6144+(xcd&1)*1024+(xcd>>1)*256+w*64), [px]"s"((const char*)x+(size_t)(xcd*256+idx*8+w*2)*2048), [po]"s"((const char*)wo+(size_t)(bid*4+w)*2048),
          [tsk]"s"(sb0+SINK+256), [tm]"s"(tm), [tp]"s"((const char*)wo+(size_t)(idx*32)*2048+(xcd&1)*1024+((3-(int)((size_t)wo>>7))&7)*128)
        : "memory", "scc", "vcc", LD_CLOB);
    }
    const unsigned tag=rd1(syn+64)+1u;
    lds_add(sb0+SYN+12);
    TS(2);
    if(w==3){
      const unsigned* fp=flags+F_O+(rb*16+hp)*16; const unsigned fo=(unsigned)(((ps+l)&15)*4);
      const unsigned madr=sb0+(l==0? SYN+20 : SINK+l*4);
      SPIN(rd1(syn+12)<4u || rd1(syn+16)<4u,1);
      PUB(madr,1u);
      unsigned rdy=1; int spins=0;
      while(rdy!=0xFFFFu){
        unsigned v;
        asm volatile("s_mov_b64 exec, 0xffff\n\tglobal_atomic_or %0, %1, %3, %2 sc0 sc1\n\ts_mov_b64 exec, -1\n\ts_waitcnt vmcnt(0)" : "=&v"(v) : "v"(fo), "s"(fp), "v"(0u) : "memory");
        unsigned r=((unsigned)__ballot(v==tag)&0xFFFFu)|1u;
        if(++spins>(1<<21)) r=0xFFFFu;
        if(r!=rdy){ PUB(madr,(unsigned)__builtin_ctz(~r)); rdy=r; }
        else __builtin_amdgcn_s_sleep(OSLP);
      }
      TS(3);
      WAITV(0);
      if(bid==0){
        int sp=0;
        while(true){
          u32x4 v; asm volatile("global_load_dwordx4 %0, %1, %2 sc1\n\ts_waitcnt vmcnt(0)" : "=&v"(v) : "v"(l*16), "s"(flags+F_Y) : "memory");
          if(__all(v[0]==tag && v[1]==tag && v[2]==tag && v[3]==tag) || ++sp>(1<<20)) break;
          __builtin_amdgcn_s_sleep(12);
        }
        if(l==0) stflag(flags, tag);
      }
    } else { TS(3); }
    TOUT(4);
    return;
  }

  __builtin_amdgcn_s_setprio(GPR);
  const int h=w&1, r=w>>1;
  f32x16 e0, e1; unsigned trc=0; (void)trc;
  {
    LANEVARS
    unsigned lx0=sb0+X_OFF+fr*128+((g^(fr&7))*16), lx1=sb0+X_OFF+fr*128+(((4+g)^(fr&7))*16);
    const int sw=wswz(8*g+tq);
    unsigned lw0=sb0+(8*g+tq)*128+(unsigned)((((2*h)^sw)*32)+8*tp), lw1=sb0+(8*g+tq)*128+(unsigned)((((2*h+1)^sw)*32)+8*tp);
    char* ot=(char*)o+(size_t)(rb*16+ps)*16384+r*8192+h*2048;
    unsigned it; unsigned long long rm; asm("s_cmp_lg_u32 %1, 0\n\ts_cselect_b64 %0, -1, 0" : "=s"(rm) : "s"(r) : "scc");
    asm volatile(N2_ASM
      : [it]"=&s"(it), "+{v56}"(lx0), "+{v57}"(lx1), "+{v58}"(lw0), "+{v59}"(lw1)
      : "{v62}"(l*16), "{v63}"(sb0+EX_OFF+h*3072+l*16), [r]"s"(r), [ot]"s"(ot), [ou]"s"(ot+4096), [fa]"s"(sb0+SYN+32+w*4), [fp]"s"(sb0+SYN+32+(w^2)*4),
        [vo]"s"(2048-1024*r), [rm]"s"(rm), [z]"s"(rb|r)
      : "memory", "scc", "vcc", N2_CLOB);
  }
  TS(3);
  {
    LANEVARS
    const unsigned tag=rd1(syn+64)+1u;
    const unsigned ra=sb0+(4*g+tq)*128+8*tp; const int sw=(2*g+(tq>>1))&3;
    unsigned a0=ra+((0^sw)*32), a1=ra+((1^sw)*32), a2=ra+((2^sw)*32), a3=ra+((3^sw)*32), vo=(unsigned)(ps*16384+w*4096+l*16);
    asm volatile("" : "+{v160}"(a0), "+{v161}"(a1), "+{v162}"(a2), "+{v163}"(a3), "+{v164}"(vo));
    WAITV(0);
    if(lds_inc(syn+16,l)==3u){ if(l<16) stflag(flags+F_O+(rb*16+l)*16+ps, tag); }
    TS(4);
    __builtin_amdgcn_s_setprio(G2P);
    unsigned c=0, iss=0, th, q0, q1, cn=0;
    asm volatile(G3_ASM
      : "={v[128:143]}"(e0), "={v[144:159]}"(e1), [c]"+s"(c), [iss]"+s"(iss), [th]"=&s"(th), [t0]"=&s"(q0), [t1]"=&s"(q1), [cn]"+s"(cn),
        "+{v160}"(a0), "+{v161}"(a1), "+{v162}"(a2), "+{v163}"(a3), "+{v164}"(vo)
      : "{v165}"(sb0+SYN+20), [go]"s"(60+w*4), [ob]"s"((const char*)o+(size_t)rb*16*16384)
      : "memory", "scc", "vcc", G3_CLOB);
  }
  LANEVARS
  {
    char* sg=smem+EX_OFF+w*2048;
    char* op=(char*)(out+(size_t)(s0+w*32)*2048+1024+hp*64); const unsigned oo=(unsigned)((l>>3)*4096+(l&7)*16);
    u32x4 ov[4];
    #pragma unroll
    for(int t=0;t<2;t++){
      #pragma unroll
      for(int j=0;j<4;j++){
        #define E2(i) (t? e1[(i)&15] : e0[(i)&15])
        *(volatile LDS_AS u32x2*)(LDS_AS char*)(sg+fr*128+(((2*j+(g>>1))^(fr&7))*16)+(g&1)*8)=u32x2{pack2(E2(4*j),E2(4*j+1)),pack2(E2(4*j+2),E2(4*j+3))};
      }
      #pragma unroll
      for(int i=0;i<2;i++) ov[2*t+i]=*(volatile LDS_AS u32x4*)(LDS_AS char*)(sg+(i*8+(l>>3))*128+(((l&7)^(l>>3))*16));
    }
    #pragma unroll
    for(int i=0;i<4;i++) stout(op+i*32768, oo, ov[i]);
    ENDW TS(6); TOUT(7);
  }
}

extern "C" void kernel_launch(void* const* d_in, const int* in_sizes, int n_in, void* d_out, int out_size, void* d_ws, size_t ws_size, hipStream_t stream){
  const u16* q=(const u16*)d_in[0]; const u16* k=(const u16*)d_in[1]; const u16* v=(const u16*)d_in[2];
  const u16* shk=(const u16*)d_in[3]; const u16* hk=(const u16*)d_in[4]; const u16* sD=(const u16*)d_in[5];
  const u16* x=(const u16*)d_in[6]; const u16* wqkv=(const u16*)d_in[7]; const u16* wo=(const u16*)d_in[8];
  u16* out=(u16*)d_out; u16* ws=(u16*)d_ws;
  u16* o=ws; u16* kvT=ws+(size_t)S*DM; u16* yT=kvT+(size_t)S*DM;
  unsigned* cnt=(unsigned*)(ws+(size_t)6*S*DM);
  fused_kernel<<<256,768,0,stream>>>(x,wqkv,k,v,shk,o,kvT,hk,wo,q,sD,yT,out,cnt);
}
```

```cpp
#define STAMP
#include <hip/hip_runtime.h>
#include <hip/hip_bf16.h>
#include <stdint.h>

typedef unsigned short u16;
typedef short bf16x8 __attribute__((ext_vector_type(8)));
typedef short s16x4 __attribute__((ext_vector_type(4)));
typedef float f32x2 __attribute__((ext_vector_type(2)));
typedef float f32x4 __attribute__((ext_vector_type(4)));
typedef float f32x8 __attribute__((ext_vector_type(8)));
typedef float f32x16 __attribute__((ext_vector_type(16)));
typedef unsigned u32x4 __attribute__((ext_vector_type(4)));
typedef unsigned u32x2 __attribute__((ext_vector_type(2)));
typedef __bf16 bf16x2_t __attribute__((ext_vector_type(2)));
#define LDS_AS __attribute__((address_space(3)))

__device__ __forceinline__ float bflo(unsigned x){ return __uint_as_float(x<<16); }
__device__ __forceinline__ float bfhi(unsigned x){ return __uint_as_float(x&0xffff0000u); }
__device__ __forceinline__ unsigned pack2(float lo, float hi){ return __builtin_bit_cast(unsigned, __builtin_convertvector(f32x2{lo,hi}, bf16x2_t)); }
__device__ __forceinline__ float mul1(float a, float b){ float r; asm("v_mul_f32 %0, %1, %2" : "=v"(r) : "v"(a), "v"(b)); return r; }
__device__ __forceinline__ float fma1(float a, float b, float c){ float r; asm("v_fma_f32 %0, %1, %2, %3" : "=v"(r) : "v"(a), "v"(b), "v"(c)); return r; }

__device__ __forceinline__ void glds(const void* sbase, unsigned voff, unsigned ldsaddr){
  asm volatile("s_mov_b32 m0, %2\n\ts_nop 0\n\tglobal_load_lds_dwordx4 %0, %1" :: "v"(voff), "s"(sbase), "s"(ldsaddr) : "memory");
}
__device__ __forceinline__ void gldsc(const void* sbase, unsigned voff, unsigned ldsaddr){
  asm volatile("s_mov_b32 m0, %2\n\ts_nop 0\n\tglobal_load_lds_dwordx4 %0, %1 sc1" :: "v"(voff), "s"(sbase), "s"(ldsaddr) : "memory");
}
__device__ __forceinline__ void touch(const void* sbase, unsigned voff, unsigned ldsaddr){
  asm volatile("s_mov_b32 m0, %2\n\ts_nop 0\n\tglobal_load_lds_dword %0, %1" :: "v"(voff), "s"(sbase), "s"(ldsaddr) : "memory");
}
__device__ __forceinline__ void st16c(void* p, u32x4 v){ asm volatile("global_store_dwordx4 %0, %1, off sc1\n\ts_nop 1" :: "v"(p), "v"(v) : "memory"); }
__device__ __forceinline__ void st16cs(void* sbase, unsigned voff, u32x4 v){ asm volatile("global_store_dwordx4 %0, %1, %2 sc1\n\ts_nop 1" :: "v"(voff), "v"(v), "s"(sbase) : "memory"); }
__device__ __forceinline__ unsigned ldflag(const unsigned* p){ return __hip_atomic_load(p, __ATOMIC_RELAXED, __HIP_MEMORY_SCOPE_AGENT); }
__device__ __forceinline__ void stflag(unsigned* p, unsigned v){ __hip_atomic_store(p, v, __ATOMIC_RELAXED, __HIP_MEMORY_SCOPE_AGENT); }
template<int SLP> __device__ __forceinline__ void wait_flag(const unsigned* p, unsigned tag){
  int spins=0;
  while(true){ unsigned v=ldflag(p); if(__all(v==tag)) break; __builtin_amdgcn_s_sleep(SLP); if(++spins>(1<<21)) break; }
}
__device__ __forceinline__ unsigned lds_addr(const void* p){ return (unsigned)(size_t)(const LDS_AS void*)p; }
#define WAITV(n) asm volatile("s_waitcnt vmcnt(" #n ")":::"memory")
#define WAITL() asm volatile("s_waitcnt lgkmcnt(0)":::"memory")
#define SBAR() asm volatile("s_barrier":::"memory")
#define PUB(addr,val) asm volatile("ds_write_b32 %0, %1" :: "v"(addr), "v"(val) : "memory")
__device__ __forceinline__ s16x4 trread(const char* p){ return __builtin_amdgcn_ds_read_tr16_b64_v4i16((LDS_AS s16x4*)p); }
__device__ __forceinline__ bf16x8 cat44(s16x4 a, s16x4 b){ return __builtin_shufflevector(a,b,0,1,2,3,4,5,6,7); }
__device__ __forceinline__ int wswz(int row){ return ((row>>1)&1) | (((row>>3)&1)<<1); }
__device__ __forceinline__ u32x4 widen(u32x2 a, u32x2 b){
  auto r0=__builtin_amdgcn_permlane16_swap(a[0],b[0],false,false);
  auto r1=__builtin_amdgcn_permlane16_swap(a[1],b[1],false,false);
  return u32x4{r0[0],r1[0],r0[1],r1[1]};
}

constexpr int S=2048, DM=1024;
constexpr int WS=12288, XS=18432, X_OFF=6*WS;
constexpr int CREG=4608, C_OFF=131072;
constexpr int EX_OFF=C_OFF+4*CREG;
constexpr int SYN=EX_OFF+8192;
constexpr int SINK=SYN+256;
constexpr int ALL_LDS=SINK+768;
constexpr int F_PP=1024, F_O=4096, F_Y=8192;
#define KV0 1
#define KVW 3
#define KVS 3
#define LPR 2
#define CPR 2
#define GPR 1
#define WROT 4
#define LHOLD 0
#define HOTP 0
#define E2A 3
#define WLIM 4
#define HEARLY 1
#define QLATE 1
#define WTCH 0
#define CGATE 1
#define OSLP 2
#define YSLP 8
#define TP0 0
#define TP1 16
#define TWO 0
#define OPULL 0
#define CVP 2
#define G2P 1
#define OUTC 1
#define CREM 0
#define OPOL 1
#define PREM 9
#define KTH 1
#define KT0 0
#define YDEF 1
#define TPW 0
#define PB2 1
#define DRB 3
#define KLB 2
#define CDL 8
#define WTL 2
#define WTP 10
#define WTM 3
#define WTW 1
#define ROT 1

#define N2_ASM \
  "v_mov_b32 v64, 0\n\t" \
  "v_mov_b32 v65, 0\n\t" \
  "v_mov_b32 v66, 0\n\t" \
  "v_mov_b32 v67, 0\n\t" \
  "v_mov_b32 v68, 0\n\t" \
  "v_mov_b32 v69, 0\n\t" \
  "v_mov_b32 v70, 0\n\t" \
  "v_mov_b32 v71, 0\n\t" \
  "v_mov_b32 v72, 0\n\t" \
  "v_mov_b32 v73, 0\n\t" \
  "v_mov_b32 v74, 0\n\t" \
  "v_mov_b32 v75, 0\n\t" \
  "v_mov_b32 v76, 0\n\t" \
  "v_mov_b32 v77, 0\n\t" \
  "v_mov_b32 v78, 0\n\t" \
  "v_mov_b32 v79, 0\n\t" \
  "v_mov_b32 v80, 0\n\t" \
  "v_mov_b32 v81, 0\n\t" \
  "v_mov_b32 v82, 0\n\t" \
  "v_mov_b32 v83, 0\n\t" \
  "v_mov_b32 v84, 0\n\t" \
  "v_mov_b32 v85, 0\n\t" \
  "v_mov_b32 v86, 0\n\t" \
  "v_mov_b32 v87, 0\n\t" \
  "v_mov_b32 v88, 0\n\t" \
  "v_mov_b32 v89, 0\n\t" \
  "v_mov_b32 v90, 0\n\t" \
  "v_mov_b32 v91, 0\n\t" \
  "v_mov_b32 v92, 0\n\t" \
  "v_mov_b32 v93, 0\n\t" \
  "v_mov_b32 v94, 0\n\t" \
  "v_mov_b32 v95, 0\n\t" \
  "v_mov_b32 v96, 0\n\t" \
  "v_mov_b32 v97, 0\n\t" \
  "v_mov_b32 v98, 0\n\t" \
  "v_mov_b32 v99, 0\n\t" \
  "v_mov_b32 v100, 0\n\t" \
  "v_mov_b32 v101, 0\n\t" \
  "v_mov_b32 v102, 0\n\t" \
  "v_mov_b32 v103, 0\n\t" \
  "v_mov_b32 v104, 0\n\t" \
  "v_mov_b32 v105, 0\n\t" \
  "v_mov_b32 v106, 0\n\t" \
  "v_mov_b32 v107, 0\n\t" \
  "v_mov_b32 v108, 0\n\t" \
  "v_mov_b32 v109, 0\n\t" \
  "v_mov_b32 v110, 0\n\t" \
  "v_mov_b32 v111, 0\n\t" \
  "v_mov_b32 v112, 0\n\t" \
  "v_mov_b32 v113, 0\n\t" \
  "v_mov_b32 v114, 0\n\t" \
  "v_mov_b32 v115, 0\n\t" \
  "v_mov_b32 v116, 0\n\t" \
  "v_mov_b32 v117, 0\n\t" \
  "v_mov_b32 v118, 0\n\t" \
  "v_mov_b32 v119, 0\n\t" \
  "v_mov_b32 v120, 0\n\t" \
  "v_mov_b32 v121, 0\n\t" \
  "v_mov_b32 v122, 0\n\t" \
  "v_mov_b32 v123, 0\n\t" \
  "v_mov_b32 v124, 0\n\t" \
  "v_mov_b32 v125, 0\n\t" \
  "v_mov_b32 v126, 0\n\t" \
  "v_mov_b32 v127, 0\n\t" \
  "v_mov_b32 v128, 0\n\t" \
  "v_mov_b32 v129, 0\n\t" \
  "v_mov_b32 v130, 0\n\t" \
  "v_mov_b32 v131, 0\n\t" \
  "v_mov_b32 v132, 0\n\t" \
  "v_mov_b32 v133, 0\n\t" \
  "v_mov_b32 v134, 0\n\t" \
  "v_mov_b32 v135, 0\n\t" \
  "v_mov_b32 v136, 0\n\t" \
  "v_mov_b32 v137, 0\n\t" \
  "v_mov_b32 v138, 0\n\t" \
  "v_mov_b32 v139, 0\n\t" \
  "v_mov_b32 v140, 0\n\t" \
  "v_mov_b32 v141, 0\n\t" \
  "v_mov_b32 v142, 0\n\t" \
  "v_mov_b32 v143, 0\n\t" \
  "v_mov_b32 v144, 0\n\t" \
  "v_mov_b32 v145, 0\n\t" \
  "v_mov_b32 v146, 0\n\t" \
  "v_mov_b32 v147, 0\n\t" \
  "v_mov_b32 v148, 0\n\t" \
  "v_mov_b32 v149, 0\n\t" \
  "v_mov_b32 v150, 0\n\t" \
  "v_mov_b32 v151, 0\n\t" \
  "v_mov_b32 v152, 0\n\t" \
  "v_mov_b32 v153, 0\n\t" \
  "v_mov_b32 v154, 0\n\t" \
  "v_mov_b32 v155, 0\n\t" \
  "v_mov_b32 v156, 0\n\t" \
  "v_mov_b32 v157, 0\n\t" \
  "v_mov_b32 v158, 0\n\t" \
  "v_mov_b32 v159, 0\n\t" \
  "v_mov_b32 v160, 0\n\t" \
  "v_mov_b32 v161, 0\n\t" \
  "v_mov_b32 v162, 0\n\t" \
  "v_mov_b32 v163, 0\n\t" \
  "v_mov_b32 v164, 0\n\t" \
  "v_mov_b32 v165, 0\n\t" \
  "v_mov_b32 v166, 0\n\t" \
  "v_mov_b32 v167, 0\n\t" \
  "s_mov_b32 s68, 0x50000\n\t" \
  "s_mov_b32 s69, 0x5550055\n\t" \
  "s_mov_b32 s70, 0xa0000\n\t" \
  "s_mov_b32 s71, 0xaaa00aa\n\t" \
  "s_mov_b32 s72, 0x150001\n\t" \
  "s_mov_b32 s73, 0x15550155\n\t" \
  "s_mov_b32 s74, 0x2a0002\n\t" \
  "s_mov_b32 s75, 0x2aaa02aa\n\t" \
  "s_mov_b32 s76, 0x55505555\n\t" \
  "s_mov_b32 s77, 0x50005500\n\t" \
  "s_mov_b32 s78, 0xaaa0aaaa\n\t" \
  "s_mov_b32 s79, 0xa000aa00\n\t" \
  "s_mov_b32 s80, 0x55405554\n\t" \
  "s_mov_b32 s81, 0x40005400\n\t" \
  "s_mov_b32 s82, 0xaa80aaa8\n\t" \
  "s_mov_b32 s83, 0x8000a800\n\t" \
  "s_mov_b32 s84, 0xaaaaaaaa\n\t" \
  "s_mov_b32 s85, 0xaaaaaaaa\n\t" \
  "v_add_u32 v60, 36864, v58\n\t" \
  "v_add_u32 v61, 36864, v59\n\t" \
  "s_mov_b32 %[it], 0\n\t" \
  "s_cmp_lg_u32 %[r], 0\n\t" \
  "s_barrier\n\t" \
  "s_memrealtime %[st1]\n\t" \
  "s_waitcnt lgkmcnt(0)\n\t" \
  "s_cbranch_scc1 NB_%=\n\t" \
  "ds_read_b64_tr_b16 v[40:41], v60 offset:16384\n\t" \
  "ds_read_b64_tr_b16 v[42:43], v60 offset:16896\n\t" \
  "ds_read_b64_tr_b16 v[44:45], v60 offset:20480\n\t" \
  "ds_read_b64_tr_b16 v[46:47], v60 offset:20992\n\t" \
  "ds_read_b64_tr_b16 v[48:49], v61 offset:16384\n\t" \
  "ds_read_b64_tr_b16 v[50:51], v61 offset:16896\n\t" \
  "ds_read_b128 v[0:3], v56 offset:36864\n\t" \
  "ds_read_b128 v[4:7], v56 offset:38912\n\t" \
  "ds_read_b128 v[8:11], v56 offset:40960\n\t" \
  "ds_read_b128 v[12:15], v56 offset:43008\n\t" \
  "ds_read_b128 v[16:19], v56 offset:45056\n\t" \
  "s_waitcnt lgkmcnt(0)\n\t" \
  "s_branch N4a_%=\n\t" \
  "N0a_%=:\n\t" \
  "s_waitcnt lgkmcnt(4)\n\t" \
  "v_mfma_f32_16x16x32_bf16 v[160:163], v[40:43], v[0:3], v[160:163]\n\t" \
  "ds_read_b64_tr_b16 v[52:53], v59 offset:8192\n\t" \
  "v_mfma_f32_16x16x32_bf16 v[96:99], v[40:43], v[4:7], v[96:99]\n\t" \
  "ds_read_b64_tr_b16 v[54:55], v59 offset:8704\n\t" \
  "v_mfma_f32_16x16x32_bf16 v[104:107], v[40:43], v[8:11], v[104:107]\n\t" \
  "s_barrier\n\t" \
  "v_mfma_f32_16x16x32_bf16 v[112:115], v[40:43], v[12:15], v[112:115]\n\t" \
  "v_mfma_f32_16x16x32_bf16 v[120:123], v[40:43], v[16:19], v[120:123]\n\t" \
  "s_waitcnt lgkmcnt(4)\n\t" \
  "v_mfma_f32_16x16x32_bf16 v[128:131], v[4:7], v[44:47], v[128:131]\n\t" \
  "ds_read_b64_tr_b16 v[40:41], v58 offset:0\n\t" \
  "v_mfma_f32_16x16x32_bf16 v[136:139], v[8:11], v[44:47], v[136:139]\n\t" \
  "ds_read_b64_tr_b16 v[42:43], v58 offset:512\n\t" \
  "v_mfma_f32_16x16x32_bf16 v[144:147], v[12:15], v[44:47], v[144:147]\n\t" \
  "ds_read_b128 v[20:23], v57 offset:0\n\t" \
  "v_mfma_f32_16x16x32_bf16 v[152:155], v[16:19], v[44:47], v[152:155]\n\t" \
  "ds_read_b128 v[24:27], v57 offset:2048\n\t" \
  "s_waitcnt lgkmcnt(6)\n\t" \
  "v_mfma_f32_16x16x32_bf16 v[164:167], v[48:51], v[0:3], v[164:167]\n\t" \
  "ds_read_b64_tr_b16 v[44:45], v59 offset:0\n\t" \
  "v_mfma_f32_16x16x32_bf16 v[100:103], v[48:51], v[4:7], v[100:103]\n\t" \
  "ds_read_b64_tr_b16 v[46:47], v59 offset:512\n\t" \
  "v_mfma_f32_16x16x32_bf16 v[108:111], v[48:51], v[8:11], v[108:111]\n\t" \
  "ds_read_b128 v[28:31], v57 offset:4096\n\t" \
  "v_mfma_f32_16x16x32_bf16 v[116:119], v[48:51], v[12:15], v[116:119]\n\t" \
  "ds_read_b128 v[32:35], v57 offset:6144\n\t" \
  "v_mfma_f32_16x16x32_bf16 v[124:127], v[48:51], v[16:19], v[124:127]\n\t" \
  "s_waitcnt lgkmcnt(8)\n\t" \
  "v_mfma_f32_16x16x32_bf16 v[132:135], v[4:7], v[52:55], v[132:135]\n\t" \
  "ds_read_b64_tr_b16 v[48:49], v58 offset:16384\n\t" \
  "v_mfma_f32_16x16x32_bf16 v[140:143], v[8:11], v[52:55], v[140:143]\n\t" \
  "ds_read_b64_tr_b16 v[50:51], v58 offset:16896\n\t" \
  "v_mfma_f32_16x16x32_bf16 v[148:151], v[12:15], v[52:55], v[148:151]\n\t" \
  "ds_read_b128 v[36:39], v57 offset:8192\n\t" \
  "v_mfma_f32_16x16x32_bf16 v[156:159], v[16:19], v[52:55], v[156:159]\n\t" \
  "s_waitcnt lgkmcnt(9)\n\t" \
  "v_mfma_f32_16x16x32_bf16 v[64:67], v[40:43], v[4:7], v[64:67]\n\t" \
  "ds_read_b64_tr_b16 v[52:53], v58 offset:20480\n\t" \
  "v_mfma_f32_16x16x32_bf16 v[72:75], v[40:43], v[8:11], v[72:75]\n\t" \
  "ds_read_b64_tr_b16 v[54:55], v58 offset:20992\n\t" \
  "v_mfma_f32_16x16x32_bf16 v[80:83], v[40:43], v[12:15], v[80:83]\n\t" \
  "v_mfma_f32_16x16x32_bf16 v[88:91], v[40:43], v[16:19], v[88:91]\n\t" \
  "s_waitcnt lgkmcnt(7)\n\t" \
  "v_mfma_f32_16x16x32_bf16 v[68:71], v[44:47], v[4:7], v[68:71]\n\t" \
  "ds_read_b64_tr_b16 v[40:41], v59 offset:16384\n\t" \
  "v_mfma_f32_16x16x32_bf16 v[76:79], v[44:47], v[8:11], v[76:79]\n\t" \
  "ds_read_b64_tr_b16 v[42:43], v59 offset:16896\n\t" \
  "v_mfma_f32_16x16x32_bf16 v[84:87], v[44:47], v[12:15], v[84:87]\n\t" \
  "v_mfma_f32_16x16x32_bf16 v[92:95], v[44:47], v[16:19], v[92:95]\n\t" \
  "N1a_%=:\n\t" \
  "s_waitcnt lgkmcnt(4)\n\t" \
  "v_mfma_f32_16x16x32_bf16 v[160:163], v[48:51], v[20:23], v[160:163]\n\t" \
  "ds_read_b64_tr_b16 v[44:45], v59 offset:20480\n\t" \
  "v_mfma_f32_16x16x32_bf16 v[96:99], v[48:51], v[24:27], v[96:99]\n\t" \
  "ds_read_b64_tr_b16 v[46:47], v59 offset:20992\n\t" \
  "v_mfma_f32_16x16x32_bf16 v[104:107], v[48:51], v[28:31], v[104:107]\n\t" \
  "s_barrier\n\t" \
  "v_mfma_f32_16x16x32_bf16 v[112:115], v[48:51], v[32:35], v[112:115]\n\t" \
  "v_mfma_f32_16x16x32_bf16 v[120:123], v[48:51], v[36:39], v[120:123]\n\t" \
  "s_waitcnt lgkmcnt(4)\n\t" \
  "v_mfma_f32_16x16x32_bf16 v[128:131], v[24:27], v[52:55], v[128:131]\n\t" \
  "ds_read_b64_tr_b16 v[48:49], v58 offset:12288\n\t" \
  "v_mfma_f32_16x16x32_bf16 v[136:139], v[28:31], v[52:55], v[136:139]\n\t" \
  "ds_read_b64_tr_b16 v[50:51], v58 offset:12800\n\t" \
  "v_mfma_f32_16x16x32_bf16 v[144:147], v[32:35], v[52:55], v[144:147]\n\t" \
  "ds_read_b128 v[0:3], v56 offset:18432\n\t" \
  "v_mfma_f32_16x16x32_bf16 v[152:155], v[36:39], v[52:55], v[152:155]\n\t" \
  "ds_read_b128 v[4:7], v56 offset:20480\n\t" \
  "s_waitcnt lgkmcnt(6)\n\t" \
  "v_mfma_f32_16x16x32_bf16 v[164:167], v[40:43], v[20:23], v[164:167]\n\t" \
  "ds_read_b64_tr_b16 v[52:53], v59 offset:12288\n\t" \
  "v_mfma_f32_16x16x32_bf16 v[100:103], v[40:43], v[24:27], v[100:103]\n\t" \
  "ds_read_b64_tr_b16 v[54:55], v59 offset:12800\n\t" \
  "v_mfma_f32_16x16x32_bf16 v[108:111], v[40:43], v[28:31], v[108:111]\n\t" \
  "ds_read_b128 v[8:11], v56 offset:22528\n\t" \
  "v_mfma_f32_16x16x32_bf16 v[116:119], v[40:43], v[32:35], v[116:119]\n\t" \
  "ds_read_b128 v[12:15], v56 offset:24576\n\t" \
  "v_mfma_f32_16x16x32_bf16 v[124:127], v[40:43], v[36:39], v[124:127]\n\t" \
  "s_waitcnt lgkmcnt(8)\n\t" \
  "v_mfma_f32_16x16x32_bf16 v[132:135], v[24:27], v[44:47], v[132:135]\n\t" \
  "ds_read_b64_tr_b16 v[40:41], v58 offset:28672\n\t" \
  "v_mfma_f32_16x16x32_bf16 v[140:143], v[28:31], v[44:47], v[140:143]\n\t" \
  "ds_read_b64_tr_b16 v[42:43], v58 offset:29184\n\t" \
  "v_mfma_f32_16x16x32_bf16 v[148:151], v[32:35], v[44:47], v[148:151]\n\t" \
  "ds_read_b128 v[16:19], v56 offset:26624\n\t" \
  "v_mfma_f32_16x16x32_bf16 v[156:159], v[36:39], v[44:47], v[156:159]\n\t" \
  "s_waitcnt lgkmcnt(9)\n\t" \
  "v_mfma_f32_16x16x32_bf16 v[64:67], v[48:51], v[24:27], v[64:67]\n\t" \
  "ds_read_b64_tr_b16 v[44:45], v58 offset:32768\n\t" \
  "v_mfma_f32_16x16x32_bf16 v[72:75], v[48:51], v[28:31], v[72:75]\n\t" \
  "ds_read_b64_tr_b16 v[46:47], v58 offset:33280\n\t" \
  "v_mfma_f32_16x16x32_bf16 v[80:83], v[48:51], v[32:35], v[80:83]\n\t" \
  "v_mfma_f32_16x16x32_bf16 v[88:91], v[48:51], v[36:39], v[88:91]\n\t" \
  "s_waitcnt lgkmcnt(7)\n\t" \
  "v_mfma_f32_16x16x32_bf16 v[68:71], v[52:55], v[24:27], v[68:71]\n\t" \
  "ds_read_b64_tr_b16 v[48:49], v59 offset:28672\n\t" \
  "v_mfma_f32_16x16x32_bf16 v[76:79], v[52:55], v[28:31], v[76:79]\n\t" \
  "ds_read_b64_tr_b16 v[50:51], v59 offset:29184\n\t" \
  "v_mfma_f32_16x16x32_bf16 v[84:87], v[52:55], v[32:35], v[84:87]\n\t" \
  "v_mfma_f32_16x16x32_bf16 v[92:95], v[52:55], v[36:39], v[92:95]\n\t" \
  "N2a_%=:\n\t" \
  "s_waitcnt lgkmcnt(4)\n\t" \
  "v_mfma_f32_16x16x32_bf16 v[160:163], v[40:43], v[0:3], v[160:163]\n\t" \
  "ds_read_b64_tr_b16 v[52:53], v59 offset:32768\n\t" \
  "v_mfma_f32_16x16x32_bf16 v[96:99], v[40:43], v[4:7], v[96:99]\n\t" \
  "ds_read_b64_tr_b16 v[54:55], v59 offset:33280\n\t" \
  "v_mfma_f32_16x16x32_bf16 v[104:107], v[40:43], v[8:11], v[104:107]\n\t" \
  "s_barrier\n\t" \
  "v_mfma_f32_16x16x32_bf16 v[112:115], v[40:43], v[12:15], v[112:115]\n\t" \
  "v_mfma_f32_16x16x32_bf16 v[120:123], v[40:43], v[16:19], v[120:123]\n\t" \
  "s_waitcnt lgkmcnt(4)\n\t" \
  "v_mfma_f32_16x16x32_bf16 v[128:131], v[4:7], v[44:47], v[128:131]\n\t" \
  "ds_read_b64_tr_b16 v[40:41], v58 offset:24576\n\t" \
  "v_mfma_f32_16x16x32_bf16 v[136:139], v[8:11], v[44:47], v[136:139]\n\t" \
  "ds_read_b64_tr_b16 v[42:43], v58 offset:25088\n\t" \
  "v_mfma_f32_16x16x32_bf16 v[144:147], v[12:15], v[44:47], v[144:147]\n\t" \
  "ds_read_b128 v[20:23], v57 offset:18432\n\t" \
  "v_mfma_f32_16x16x32_bf16 v[152:155], v[16:19], v[44:47], v[152:155]\n\t" \
  "ds_read_b128 v[24:27], v57 offset:20480\n\t" \
  "s_waitcnt lgkmcnt(6)\n\t" \
  "v_mfma_f32_16x16x32_bf16 v[164:167], v[48:51], v[0:3], v[164:167]\n\t" \
  "ds_read_b64_tr_b16 v[44:45], v59 offset:24576\n\t" \
  "v_mfma_f32_16x16x32_bf16 v[100:103], v[48:51], v[4:7], v[100:103]\n\t" \
  "ds_read_b64_tr_b16 v[46:47], v59 offset:25088\n\t" \
  "v_mfma_f32_16x16x32_bf16 v[108:111], v[48:51], v[8:11], v[108:111]\n\t" \
  "ds_read_b128 v[28:31], v57 offset:22528\n\t" \
  "v_mfma_f32_16x16x32_bf16 v[116:119], v[48:51], v[12:15], v[116:119]\n\t" \
  "ds_read_b128 v[32:35], v57 offset:24576\n\t" \
  "v_mfma_f32_16x16x32_bf16 v[124:127], v[48:51], v[16:19], v[124:127]\n\t" \
  "s_waitcnt lgkmcnt(8)\n\t" \
  "v_mfma_f32_16x16x32_bf16 v[132:135], v[4:7], v[52:55], v[132:135]\n\t" \
  "ds_read_b64_tr_b16 v[48:49], v60 offset:4096\n\t" \
  "v_mfma_f32_16x16x32_bf16 v[140:143], v[8:11], v[52:55], v[140:143]\n\t" \
  "ds_read_b64_tr_b16 v[50:51], v60 offset:4608\n\t" \
  "v_mfma_f32_16x16x32_bf16 v[148:151], v[12:15], v[52:55], v[148:151]\n\t" \
  "ds_read_b128 v[36:39], v57 offset:26624\n\t" \
  "v_mfma_f32_16x16x32_bf16 v[156:159], v[16:19], v[52:55], v[156:159]\n\t" \
  "s_waitcnt lgkmcnt(9)\n\t" \
  "v_mfma_f32_16x16x32_bf16 v[64:67], v[40:43], v[4:7], v[64:67]\n\t" \
  "ds_read_b64_tr_b16 v[52:53], v60 offset:8192\n\t" \
  "v_mfma_f32_16x16x32_bf16 v[72:75], v[40:43], v[8:11], v[72:75]\n\t" \
  "ds_read_b64_tr_b16 v[54:55], v60 offset:8704\n\t" \
  "v_mfma_f32_16x16x32_bf16 v[80:83], v[40:43], v[12:15], v[80:83]\n\t" \
  "v_mfma_f32_16x16x32_bf16 v[88:91], v[40:43], v[16:19], v[88:91]\n\t" \
  "s_waitcnt lgkmcnt(7)\n\t" \
  "v_mfma_f32_16x16x32_bf16 v[68:71], v[44:47], v[4:7], v[68:71]\n\t" \
  "ds_read_b64_tr_b16 v[40:41], v61 offset:4096\n\t" \
  "v_mfma_f32_16x16x32_bf16 v[76:79], v[44:47], v[8:11], v[76:79]\n\t" \
  "ds_read_b64_tr_b16 v[42:43], v61 offset:4608\n\t" \
  "v_mfma_f32_16x16x32_bf16 v[84:87], v[44:47], v[12:15], v[84:87]\n\t" \
  "v_mfma_f32_16x16x32_bf16 v[92:95], v[44:47], v[16:19], v[92:95]\n\t" \
  "N3a_%=:\n\t" \
  "s_waitcnt lgkmcnt(4)\n\t" \
  "v_mfma_f32_16x16x32_bf16 v[160:163], v[48:51], v[20:23], v[160:163]\n\t" \
  "ds_read_b64_tr_b16 v[44:45], v61 offset:8192\n\t" \
  "v_mfma_f32_16x16x32_bf16 v[96:99], v[48:51], v[24:27], v[96:99]\n\t" \
  "ds_read_b64_tr_b16 v[46:47], v61 offset:8704\n\t" \
  "v_mfma_f32_16x16x32_bf16 v[104:107], v[48:51], v[28:31], v[104:107]\n\t" \
  "s_barrier\n\t" \
  "v_mfma_f32_16x16x32_bf16 v[112:115], v[48:51], v[32:35], v[112:115]\n\t" \
  "v_mfma_f32_16x16x32_bf16 v[120:123], v[48:51], v[36:39], v[120:123]\n\t" \
  "s_waitcnt lgkmcnt(4)\n\t" \
  "v_mfma_f32_16x16x32_bf16 v[128:131], v[24:27], v[52:55], v[128:131]\n\t" \
  "ds_read_b64_tr_b16 v[48:49], v60 offset:0\n\t" \
  "v_mfma_f32_16x16x32_bf16 v[136:139], v[28:31], v[52:55], v[136:139]\n\t" \
  "ds_read_b64_tr_b16 v[50:51], v60 offset:512\n\t" \
  "v_mfma_f32_16x16x32_bf16 v[144:147], v[32:35], v[52:55], v[144:147]\n\t" \
  "ds_read_b128 v[0:3], v56 offset:36864\n\t" \
  "v_mfma_f32_16x16x32_bf16 v[152:155], v[36:39], v[52:55], v[152:155]\n\t" \
  "ds_read_b128 v[4:7], v56 offset:38912\n\t" \
  "s_waitcnt lgkmcnt(6)\n\t" \
  "v_mfma_f32_16x16x32_bf16 v[164:167], v[40:43], v[20:23], v[164:167]\n\t" \
  "ds_read_b64_tr_b16 v[52:53], v61 offset:0\n\t" \
  "v_mfma_f32_16x16x32_bf16 v[100:103], v[40:43], v[24:27], v[100:103]\n\t" \
  "ds_read_b64_tr_b16 v[54:55], v61 offset:512\n\t" \
  "v_mfma_f32_16x16x32_bf16 v[108:111], v[40:43], v[28:31], v[108:111]\n\t" \
  "ds_read_b128 v[8:11], v56 offset:40960\n\t" \
  "v_mfma_f32_16x16x32_bf16 v[116:119], v[40:43], v[32:35], v[116:119]\n\t" \
  "ds_read_b128 v[12:15], v56 offset:43008\n\t" \
  "v_mfma_f32_16x16x32_bf16 v[124:127], v[40:43], v[36:39], v[124:127]\n\t" \
  "s_waitcnt lgkmcnt(8)\n\t" \
  "v_mfma_f32_16x16x32_bf16 v[132:135], v[24:27], v[44:47], v[132:135]\n\t" \
  "ds_read_b64_tr_b16 v[40:41], v60 offset:16384\n\t" \
  "v_mfma_f32_16x16x32_bf16 v[140:143], v[28:31], v[44:47], v[140:143]\n\t" \
  "ds_read_b64_tr_b16 v[42:43], v60 offset:16896\n\t" \
  "v_mfma_f32_16x16x32_bf16 v[148:151], v[32:35], v[44:47], v[148:151]\n\t" \
  "ds_read_b128 v[16:19], v56 offset:45056\n\t" \
  "v_mfma_f32_16x16x32_bf16 v[156:159], v[36:39], v[44:47], v[156:159]\n\t" \
  "s_waitcnt lgkmcnt(9)\n\t" \
  "v_mfma_f32_16x16x32_bf16 v[64:67], v[48:51], v[24:27], v[64:67]\n\t" \
  "ds_read_b64_tr_b16 v[44:45], v60 offset:20480\n\t" \
  "v_mfma_f32_16x16x32_bf16 v[72:75], v[48:51], v[28:31], v[72:75]\n\t" \
  "ds_read_b64_tr_b16 v[46:47], v60 offset:20992\n\t" \
  "v_mfma_f32_16x16x32_bf16 v[80:83], v[48:51], v[32:35], v[80:83]\n\t" \
  "v_mfma_f32_16x16x32_bf16 v[88:91], v[48:51], v[36:39], v[88:91]\n\t" \
  "s_waitcnt lgkmcnt(7)\n\t" \
  "v_mfma_f32_16x16x32_bf16 v[68:71], v[52:55], v[24:27], v[68:71]\n\t" \
  "ds_read_b64_tr_b16 v[48:49], v61 offset:16384\n\t" \
  "v_mfma_f32_16x16x32_bf16 v[76:79], v[52:55], v[28:31], v[76:79]\n\t" \
  "ds_read_b64_tr_b16 v[50:51], v61 offset:16896\n\t" \
  "v_mfma_f32_16x16x32_bf16 v[84:87], v[52:55], v[32:35], v[84:87]\n\t" \
  "v_mfma_f32_16x16x32_bf16 v[92:95], v[52:55], v[36:39], v[92:95]\n\t" \
  "N4a_%=:\n\t" \
  "s_waitcnt lgkmcnt(4)\n\t" \
  "v_mfma_f32_16x16x32_bf16 v[160:163], v[40:43], v[0:3], v[160:163]\n\t" \
  "ds_read_b64_tr_b16 v[52:53], v61 offset:20480\n\t" \
  "v_mfma_f32_16x16x32_bf16 v[96:99], v[40:43], v[4:7], v[96:99]\n\t" \
  "ds_read_b64_tr_b16 v[54:55], v61 offset:20992\n\t" \
  "v_mfma_f32_16x16x32_bf16 v[104:107], v[40:43], v[8:11], v[104:107]\n\t" \
  "s_barrier\n\t" \
  "v_mfma_f32_16x16x32_bf16 v[112:115], v[40:43], v[12:15], v[112:115]\n\t" \
  "v_mfma_f32_16x16x32_bf16 v[120:123], v[40:43], v[16:19], v[120:123]\n\t" \
  "s_waitcnt lgkmcnt(4)\n\t" \
  "v_mfma_f32_16x16x32_bf16 v[128:131], v[4:7], v[44:47], v[128:131]\n\t" \
  "ds_read_b64_tr_b16 v[40:41], v60 offset:12288\n\t" \
  "v_mfma_f32_16x16x32_bf16 v[136:139], v[8:11], v[44:47], v[136:139]\n\t" \
  "ds_read_b64_tr_b16 v[42:43], v60 offset:12800\n\t" \
  "v_mfma_f32_16x16x32_bf16 v[144:147], v[12:15], v[44:47], v[144:147]\n\t" \
  "ds_read_b128 v[20:23], v57 offset:36864\n\t" \
  "v_mfma_f32_16x16x32_bf16 v[152:155], v[16:19], v[44:47], v[152:155]\n\t" \
  "ds_read_b128 v[24:27], v57 offset:38912\n\t" \
  "s_waitcnt lgkmcnt(6)\n\t" \
  "v_mfma_f32_16x16x32_bf16 v[164:167], v[48:51], v[0:3], v[164:167]\n\t" \
  "ds_read_b64_tr_b16 v[44:45], v61 offset:12288\n\t" \
  "v_mfma_f32_16x16x32_bf16 v[100:103], v[48:51], v[4:7], v[100:103]\n\t" \
  "ds_read_b64_tr_b16 v[46:47], v61 offset:12800\n\t" \
  "v_mfma_f32_16x16x32_bf16 v[108:111], v[48:51], v[8:11], v[108:111]\n\t" \
  "ds_read_b128 v[28:31], v57 offset:40960\n\t" \
  "v_mfma_f32_16x16x32_bf16 v[116:119], v[48:51], v[12:15], v[116:119]\n\t" \
  "ds_read_b128 v[32:35], v57 offset:43008\n\t" \
  "v_mfma_f32_16x16x32_bf16 v[124:127], v[48:51], v[16:19], v[124:127]\n\t" \
  "s_waitcnt lgkmcnt(8)\n\t" \
  "v_mfma_f32_16x16x32_bf16 v[132:135], v[4:7], v[52:55], v[132:135]\n\t" \
  "ds_read_b64_tr_b16 v[48:49], v60 offset:28672\n\t" \
  "v_mfma_f32_16x16x32_bf16 v[140:143], v[8:11], v[52:55], v[140:143]\n\t" \
  "ds_read_b64_tr_b16 v[50:51], v60 offset:29184\n\t" \
  "v_mfma_f32_16x16x32_bf16 v[148:151], v[12:15], v[52:55], v[148:151]\n\t" \
  "ds_read_b128 v[36:39], v57 offset:45056\n\t" \
  "v_mfma_f32_16x16x32_bf16 v[156:159], v[16:19], v[52:55], v[156:159]\n\t" \
  "s_waitcnt lgkmcnt(9)\n\t" \
  "v_mfma_f32_16x16x32_bf16 v[64:67], v[40:43], v[4:7], v[64:67]\n\t" \
  "ds_read_b64_tr_b16 v[52:53], v60 offset:32768\n\t" \
  "v_mfma_f32_16x16x32_bf16 v[72:75], v[40:43], v[8:11], v[72:75]\n\t" \
  "ds_read_b64_tr_b16 v[54:55], v60 offset:33280\n\t" \
  "v_mfma_f32_16x16x32_bf16 v[80:83], v[40:43], v[12:15], v[80:83]\n\t" \
  "v_mfma_f32_16x16x32_bf16 v[88:91], v[40:43], v[16:19], v[88:91]\n\t" \
  "s_waitcnt lgkmcnt(7)\n\t" \
  "v_mfma_f32_16x16x32_bf16 v[68:71], v[44:47], v[4:7], v[68:71]\n\t" \
  "ds_read_b64_tr_b16 v[40:41], v61 offset:28672\n\t" \
  "v_mfma_f32_16x16x32_bf16 v[76:79], v[44:47], v[8:11], v[76:79]\n\t" \
  "ds_read_b64_tr_b16 v[42:43], v61 offset:29184\n\t" \
  "v_mfma_f32_16x16x32_bf16 v[84:87], v[44:47], v[12:15], v[84:87]\n\t" \
  "v_mfma_f32_16x16x32_bf16 v[92:95], v[44:47], v[16:19], v[92:95]\n\t" \
  "N5a_%=:\n\t" \
  "s_waitcnt lgkmcnt(4)\n\t" \
  "v_mfma_f32_16x16x32_bf16 v[160:163], v[48:51], v[20:23], v[160:163]\n\t" \
  "ds_read_b64_tr_b16 v[44:45], v61 offset:32768\n\t" \
  "v_mfma_f32_16x16x32_bf16 v[96:99], v[48:51], v[24:27], v[96:99]\n\t" \
  "ds_read_b64_tr_b16 v[46:47], v61 offset:33280\n\t" \
  "v_mfma_f32_16x16x32_bf16 v[104:107], v[48:51], v[28:31], v[104:107]\n\t" \
  "s_barrier\n\t" \
  "v_mfma_f32_16x16x32_bf16 v[112:115], v[48:51], v[32:35], v[112:115]\n\t" \
  "v_mfma_f32_16x16x32_bf16 v[120:123], v[48:51], v[36:39], v[120:123]\n\t" \
  "s_waitcnt lgkmcnt(4)\n\t" \
  "v_mfma_f32_16x16x32_bf16 v[128:131], v[24:27], v[52:55], v[128:131]\n\t" \
  "ds_read_b64_tr_b16 v[48:49], v60 offset:24576\n\t" \
  "v_mfma_f32_16x16x32_bf16 v[136:139], v[28:31], v[52:55], v[136:139]\n\t" \
  "ds_read_b64_tr_b16 v[50:51], v60 offset:25088\n\t" \
  "v_mfma_f32_16x16x32_bf16 v[144:147], v[32:35], v[52:55], v[144:147]\n\t" \
  "ds_read_b128 v[0:3], v56 offset:0\n\t" \
  "v_mfma_f32_16x16x32_bf16 v[152:155], v[36:39], v[52:55], v[152:155]\n\t" \
  "ds_read_b128 v[4:7], v56 offset:2048\n\t" \
  "s_waitcnt lgkmcnt(6)\n\t" \
  "v_mfma_f32_16x16x32_bf16 v[164:167], v[40:43], v[20:23], v[164:167]\n\t" \
  "ds_read_b64_tr_b16 v[52:53], v61 offset:24576\n\t" \
  "v_mfma_f32_16x16x32_bf16 v[100:103], v[40:43], v[24:27], v[100:103]\n\t" \
  "ds_read_b64_tr_b16 v[54:55], v61 offset:25088\n\t" \
  "v_mfma_f32_16x16x32_bf16 v[108:111], v[40:43], v[28:31], v[108:111]\n\t" \
  "ds_read_b128 v[8:11], v56 offset:4096\n\t" \
  "v_mfma_f32_16x16x32_bf16 v[116:119], v[40:43], v[32:35], v[116:119]\n\t" \
  "ds_read_b128 v[12:15], v56 offset:6144\n\t" \
  "v_mfma_f32_16x16x32_bf16 v[124:127], v[40:43], v[36:39], v[124:127]\n\t" \
  "s_waitcnt lgkmcnt(8)\n\t" \
  "v_mfma_f32_16x16x32_bf16 v[132:135], v[24:27], v[44:47], v[132:135]\n\t" \
  "ds_read_b64_tr_b16 v[40:41], v58 offset:4096\n\t" \
  "v_mfma_f32_16x16x32_bf16 v[140:143], v[28:31], v[44:47], v[140:143]\n\t" \
  "ds_read_b64_tr_b16 v[42:43], v58 offset:4608\n\t" \
  "v_mfma_f32_16x16x32_bf16 v[148:151], v[32:35], v[44:47], v[148:151]\n\t" \
  "ds_read_b128 v[16:19], v56 offset:8192\n\t" \
  "v_mfma_f32_16x16x32_bf16 v[156:159], v[36:39], v[44:47], v[156:159]\n\t" \
  "s_waitcnt lgkmcnt(9)\n\t" \
  "v_mfma_f32_16x16x32_bf16 v[64:67], v[48:51], v[24:27], v[64:67]\n\t" \
  "ds_read_b64_tr_b16 v[44:45], v58 offset:8192\n\t" \
  "v_mfma_f32_16x16x32_bf16 v[72:75], v[48:51], v[28:31], v[72:75]\n\t" \
  "ds_read_b64_tr_b16 v[46:47], v58 offset:8704\n\t" \
  "v_mfma_f32_16x16x32_bf16 v[80:83], v[48:51], v[32:35], v[80:83]\n\t" \
  "v_mfma_f32_16x16x32_bf16 v[88:91], v[48:51], v[36:39], v[88:91]\n\t" \
  "s_waitcnt lgkmcnt(7)\n\t" \
  "v_mfma_f32_16x16x32_bf16 v[68:71], v[52:55], v[24:27], v[68:71]\n\t" \
  "ds_read_b64_tr_b16 v[48:49], v59 offset:4096\n\t" \
  "v_mfma_f32_16x16x32_bf16 v[76:79], v[52:55], v[28:31], v[76:79]\n\t" \
  "ds_read_b64_tr_b16 v[50:51], v59 offset:4608\n\t" \
  "s_addk_i32 %[it], 1\n\t" \
  "v_mfma_f32_16x16x32_bf16 v[84:87], v[52:55], v[32:35], v[84:87]\n\t" \
  "s_cmp_lt_u32 %[it], 6\n\t" \
  "v_mfma_f32_16x16x32_bf16 v[92:95], v[52:55], v[36:39], v[92:95]\n\t" \
  "s_cbranch_scc1 N0a_%=\n\t" \
  "s_memrealtime %[st2]\n\t" \
  "s_setprio 3\n\t" \
  "v_cvt_pk_bf16_f32 v120, v120, v121\n\t" \
  "v_cvt_pk_bf16_f32 v121, v122, v123\n\t" \
  "v_cvt_pk_bf16_f32 v122, v124, v125\n\t" \
  "v_cvt_pk_bf16_f32 v123, v126, v127\n\t" \
  "v_cvt_pk_bf16_f32 v28, v152, v153\n\t" \
  "v_cvt_pk_bf16_f32 v29, v154, v155\n\t" \
  "v_cvt_pk_bf16_f32 v38, v156, v157\n\t" \
  "v_cvt_pk_bf16_f32 v39, v158, v159\n\t" \
  "ds_write_b128 v63, v[120:123]\n\t" \
  "ds_write2_b64 v63, v[28:29], v[38:39] offset0:128 offset1:129\n\t" \
  "v_mov_b32 v57, %[fa]\n\t" \
  "v_mov_b32 v58, 1\n\t" \
  "s_mov_b64 exec, 1\n\t" \
  "ds_write_b32 v57, v58\n\t" \
  "s_mov_b64 exec, -1\n\t" \
  "v_cvt_pk_bf16_f32 v160, v160, v161\n\t" \
  "v_cvt_pk_bf16_f32 v161, v162, v163\n\t" \
  "v_cvt_pk_bf16_f32 v162, v164, v165\n\t" \
  "v_cvt_pk_bf16_f32 v163, v166, v167\n\t" \
  "s_branch NX_%=\n\t" \
  "NB_%=:\n\t" \
  "ds_read_b64_tr_b16 v[40:41], v60 offset:20480\n\t" \
  "ds_read_b64_tr_b16 v[42:43], v60 offset:20992\n\t" \
  "ds_read_b64_tr_b16 v[44:45], v60 offset:16384\n\t" \
  "ds_read_b64_tr_b16 v[46:47], v60 offset:16896\n\t" \
  "ds_read_b64_tr_b16 v[48:49], v61 offset:20480\n\t" \
  "ds_read_b64_tr_b16 v[50:51], v61 offset:20992\n\t" \
  "ds_read_b128 v[0:3], v56 offset:36864\n\t" \
  "ds_read_b128 v[4:7], v56 offset:47104\n\t" \
  "ds_read_b128 v[8:11], v56 offset:49152\n\t" \
  "ds_read_b128 v[12:15], v56 offset:51200\n\t" \
  "ds_read_b128 v[16:19], v56 offset:53248\n\t" \
  "s_waitcnt lgkmcnt(0)\n\t" \
  "s_branch N4b_%=\n\t" \
  "N0b_%=:\n\t" \
  "s_waitcnt lgkmcnt(4)\n\t" \
  "v_mfma_f32_16x16x32_bf16 v[160:163], v[0:3], v[40:43], v[160:163]\n\t" \
  "ds_read_b64_tr_b16 v[52:53], v59 offset:4096\n\t" \
  "v_mfma_f32_16x16x32_bf16 v[128:131], v[4:7], v[40:43], v[128:131]\n\t" \
  "ds_read_b64_tr_b16 v[54:55], v59 offset:4608\n\t" \
  "v_mfma_f32_16x16x32_bf16 v[136:139], v[8:11], v[40:43], v[136:139]\n\t" \
  "s_barrier\n\t" \
  "v_mfma_f32_16x16x32_bf16 v[144:147], v[12:15], v[40:43], v[144:147]\n\t" \
  "v_mfma_f32_16x16x32_bf16 v[152:155], v[16:19], v[40:43], v[152:155]\n\t" \
  "s_waitcnt lgkmcnt(4)\n\t" \
  "v_mfma_f32_16x16x32_bf16 v[96:99], v[44:47], v[4:7], v[96:99]\n\t" \
  "ds_read_b64_tr_b16 v[40:41], v58 offset:0\n\t" \
  "v_mfma_f32_16x16x32_bf16 v[104:107], v[44:47], v[8:11], v[104:107]\n\t" \
  "ds_read_b64_tr_b16 v[42:43], v58 offset:512\n\t" \
  "v_mfma_f32_16x16x32_bf16 v[112:115], v[44:47], v[12:15], v[112:115]\n\t" \
  "ds_read_b128 v[20:23], v57 offset:0\n\t" \
  "v_mfma_f32_16x16x32_bf16 v[120:123], v[44:47], v[16:19], v[120:123]\n\t" \
  "ds_read_b128 v[24:27], v57 offset:10240\n\t" \
  "s_waitcnt lgkmcnt(6)\n\t" \
  "v_mfma_f32_16x16x32_bf16 v[164:167], v[0:3], v[48:51], v[164:167]\n\t" \
  "ds_read_b64_tr_b16 v[44:45], v59 offset:0\n\t" \
  "v_mfma_f32_16x16x32_bf16 v[132:135], v[4:7], v[48:51], v[132:135]\n\t" \
  "ds_read_b64_tr_b16 v[46:47], v59 offset:512\n\t" \
  "v_mfma_f32_16x16x32_bf16 v[140:143], v[8:11], v[48:51], v[140:143]\n\t" \
  "ds_read_b128 v[28:31], v57 offset:12288\n\t" \
  "v_mfma_f32_16x16x32_bf16 v[148:151], v[12:15], v[48:51], v[148:151]\n\t" \
  "ds_read_b128 v[32:35], v57 offset:14336\n\t" \
  "v_mfma_f32_16x16x32_bf16 v[156:159], v[16:19], v[48:51], v[156:159]\n\t" \
  "s_waitcnt lgkmcnt(8)\n\t" \
  "v_mfma_f32_16x16x32_bf16 v[100:103], v[52:55], v[4:7], v[100:103]\n\t" \
  "ds_read_b64_tr_b16 v[48:49], v58 offset:20480\n\t" \
  "v_mfma_f32_16x16x32_bf16 v[108:111], v[52:55], v[8:11], v[108:111]\n\t" \
  "ds_read_b64_tr_b16 v[50:51], v58 offset:20992\n\t" \
  "v_mfma_f32_16x16x32_bf16 v[116:119], v[52:55], v[12:15], v[116:119]\n\t" \
  "ds_read_b128 v[36:39], v57 offset:16384\n\t" \
  "v_mfma_f32_16x16x32_bf16 v[124:127], v[52:55], v[16:19], v[124:127]\n\t" \
  "s_waitcnt lgkmcnt(9)\n\t" \
  "v_mfma_f32_16x16x32_bf16 v[64:67], v[40:43], v[4:7], v[64:67]\n\t" \
  "ds_read_b64_tr_b16 v[52:53], v58 offset:16384\n\t" \
  "v_mfma_f32_16x16x32_bf16 v[72:75], v[40:43], v[8:11], v[72:75]\n\t" \
  "ds_read_b64_tr_b16 v[54:55], v58 offset:16896\n\t" \
  "v_mfma_f32_16x16x32_bf16 v[80:83], v[40:43], v[12:15], v[80:83]\n\t" \
  "v_mfma_f32_16x16x32_bf16 v[88:91], v[40:43], v[16:19], v[88:91]\n\t" \
  "s_waitcnt lgkmcnt(7)\n\t" \
  "v_mfma_f32_16x16x32_bf16 v[68:71], v[44:47], v[4:7], v[68:71]\n\t" \
  "ds_read_b64_tr_b16 v[40:41], v59 offset:20480\n\t" \
  "v_mfma_f32_16x16x32_bf16 v[76:79], v[44:47], v[8:11], v[76:79]\n\t" \
  "ds_read_b64_tr_b16 v[42:43], v59 offset:20992\n\t" \
  "v_mfma_f32_16x16x32_bf16 v[84:87], v[44:47], v[12:15], v[84:87]\n\t" \
  "v_mfma_f32_16x16x32_bf16 v[92:95], v[44:47], v[16:19], v[92:95]\n\t" \
  "N1b_%=:\n\t" \
  "s_waitcnt lgkmcnt(4)\n\t" \
  "v_mfma_f32_16x16x32_bf16 v[160:163], v[20:23], v[48:51], v[160:163]\n\t" \
  "ds_read_b64_tr_b16 v[44:45], v59 offset:16384\n\t" \
  "v_mfma_f32_16x16x32_bf16 v[128:131], v[24:27], v[48:51], v[128:131]\n\t" \
  "ds_read_b64_tr_b16 v[46:47], v59 offset:16896\n\t" \
  "v_mfma_f32_16x16x32_bf16 v[136:139], v[28:31], v[48:51], v[136:139]\n\t" \
  "s_barrier\n\t" \
  "v_mfma_f32_16x16x32_bf16 v[144:147], v[32:35], v[48:51], v[144:147]\n\t" \
  "v_mfma_f32_16x16x32_bf16 v[152:155], v[36:39], v[48:51], v[152:155]\n\t" \
  "s_waitcnt lgkmcnt(4)\n\t" \
  "v_mfma_f32_16x16x32_bf16 v[96:99], v[52:55], v[24:27], v[96:99]\n\t" \
  "ds_read_b64_tr_b16 v[48:49], v58 offset:12288\n\t" \
  "v_mfma_f32_16x16x32_bf16 v[104:107], v[52:55], v[28:31], v[104:107]\n\t" \
  "ds_read_b64_tr_b16 v[50:51], v58 offset:12800\n\t" \
  "v_mfma_f32_16x16x32_bf16 v[112:115], v[52:55], v[32:35], v[112:115]\n\t" \
  "ds_read_b128 v[0:3], v56 offset:18432\n\t" \
  "v_mfma_f32_16x16x32_bf16 v[120:123], v[52:55], v[36:39], v[120:123]\n\t" \
  "ds_read_b128 v[4:7], v56 offset:28672\n\t" \
  "s_waitcnt lgkmcnt(6)\n\t" \
  "v_mfma_f32_16x16x32_bf16 v[164:167], v[20:23], v[40:43], v[164:167]\n\t" \
  "ds_read_b64_tr_b16 v[52:53], v59 offset:12288\n\t" \
  "v_mfma_f32_16x16x32_bf16 v[132:135], v[24:27], v[40:43], v[132:135]\n\t" \
  "ds_read_b64_tr_b16 v[54:55], v59 offset:12800\n\t" \
  "v_mfma_f32_16x16x32_bf16 v[140:143], v[28:31], v[40:43], v[140:143]\n\t" \
  "ds_read_b128 v[8:11], v56 offset:30720\n\t" \
  "v_mfma_f32_16x16x32_bf16 v[148:151], v[32:35], v[40:43], v[148:151]\n\t" \
  "ds_read_b128 v[12:15], v56 offset:32768\n\t" \
  "v_mfma_f32_16x16x32_bf16 v[156:159], v[36:39], v[40:43], v[156:159]\n\t" \
  "s_waitcnt lgkmcnt(8)\n\t" \
  "v_mfma_f32_16x16x32_bf16 v[100:103], v[44:47], v[24:27], v[100:103]\n\t" \
  "ds_read_b64_tr_b16 v[40:41], v58 offset:32768\n\t" \
  "v_mfma_f32_16x16x32_bf16 v[108:111], v[44:47], v[28:31], v[108:111]\n\t" \
  "ds_read_b64_tr_b16 v[42:43], v58 offset:33280\n\t" \
  "v_mfma_f32_16x16x32_bf16 v[116:119], v[44:47], v[32:35], v[116:119]\n\t" \
  "ds_read_b128 v[16:19], v56 offset:34816\n\t" \
  "v_mfma_f32_16x16x32_bf16 v[124:127], v[44:47], v[36:39], v[124:127]\n\t" \
  "s_waitcnt lgkmcnt(9)\n\t" \
  "v_mfma_f32_16x16x32_bf16 v[64:67], v[48:51], v[24:27], v[64:67]\n\t" \
  "ds_read_b64_tr_b16 v[44:45], v58 offset:28672\n\t" \
  "v_mfma_f32_16x16x32_bf16 v[72:75], v[48:51], v[28:31], v[72:75]\n\t" \
  "ds_read_b64_tr_b16 v[46:47], v58 offset:29184\n\t" \
  "v_mfma_f32_16x16x32_bf16 v[80:83], v[48:51], v[32:35], v[80:83]\n\t" \
  "v_mfma_f32_16x16x32_bf16 v[88:91], v[48:51], v[36:39], v[88:91]\n\t" \
  "s_waitcnt lgkmcnt(7)\n\t" \
  "v_mfma_f32_16x16x32_bf16 v[68:71], v[52:55], v[24:27], v[68:71]\n\t" \
  "ds_read_b64_tr_b16 v[48:49], v59 offset:32768\n\t" \
  "v_mfma_f32_16x16x32_bf16 v[76:79], v[52:55], v[28:31], v[76:79]\n\t" \
  "ds_read_b64_tr_b16 v[50:51], v59 offset:33280\n\t" \
  "v_mfma_f32_16x16x32_bf16 v[84:87], v[52:55], v[32:35], v[84:87]\n\t" \
  "v_mfma_f32_16x16x32_bf16 v[92:95], v[52:55], v[36:39], v[92:95]\n\t" \
  "N2b_%=:\n\t" \
  "s_waitcnt lgkmcnt(4)\n\t" \
  "v_mfma_f32_16x16x32_bf16 v[160:163], v[0:3], v[40:43], v[160:163]\n\t" \
  "ds_read_b64_tr_b16 v[52:53], v59 offset:28672\n\t" \
  "v_mfma_f32_16x16x32_bf16 v[128:131], v[4:7], v[40:43], v[128:131]\n\t" \
  "ds_read_b64_tr_b16 v[54:55], v59 offset:29184\n\t" \
  "v_mfma_f32_16x16x32_bf16 v[136:139], v[8:11], v[40:43], v[136:139]\n\t" \
  "s_barrier\n\t" \
  "v_mfma_f32_16x16x32_bf16 v[144:147], v[12:15], v[40:43], v[144:147]\n\t" \
  "v_mfma_f32_16x16x32_bf16 v[152:155], v[16:19], v[40:43], v[152:155]\n\t" \
  "s_waitcnt lgkmcnt(4)\n\t" \
  "v_mfma_f32_16x16x32_bf16 v[96:99], v[44:47], v[4:7], v[96:99]\n\t" \
  "ds_read_b64_tr_b16 v[40:41], v58 offset:24576\n\t" \
  "v_mfma_f32_16x16x32_bf16 v[104:107], v[44:47], v[8:11], v[104:107]\n\t" \
  "ds_read_b64_tr_b16 v[42:43], v58 offset:25088\n\t" \
  "v_mfma_f32_16x16x32_bf16 v[112:115], v[44:47], v[12:15], v[112:115]\n\t" \
  "ds_read_b128 v[20:23], v57 offset:18432\n\t" \
  "v_mfma_f32_16x16x32_bf16 v[120:123], v[44:47], v[16:19], v[120:123]\n\t" \
  "ds_read_b128 v[24:27], v57 offset:28672\n\t" \
  "s_waitcnt lgkmcnt(6)\n\t" \
  "v_mfma_f32_16x16x32_bf16 v[164:167], v[0:3], v[48:51], v[164:167]\n\t" \
  "ds_read_b64_tr_b16 v[44:45], v59 offset:24576\n\t" \
  "v_mfma_f32_16x16x32_bf16 v[132:135], v[4:7], v[48:51], v[132:135]\n\t" \
  "ds_read_b64_tr_b16 v[46:47], v59 offset:25088\n\t" \
  "v_mfma_f32_16x16x32_bf16 v[140:143], v[8:11], v[48:51], v[140:143]\n\t" \
  "ds_read_b128 v[28:31], v57 offset:30720\n\t" \
  "v_mfma_f32_16x16x32_bf16 v[148:151], v[12:15], v[48:51], v[148:151]\n\t" \
  "ds_read_b128 v[32:35], v57 offset:32768\n\t" \
  "v_mfma_f32_16x16x32_bf16 v[156:159], v[16:19], v[48:51], v[156:159]\n\t" \
  "s_waitcnt lgkmcnt(8)\n\t" \
  "v_mfma_f32_16x16x32_bf16 v[100:103], v[52:55], v[4:7], v[100:103]\n\t" \
  "ds_read_b64_tr_b16 v[48:49], v60 offset:8192\n\t" \
  "v_mfma_f32_16x16x32_bf16 v[108:111], v[52:55], v[8:11], v[108:111]\n\t" \
  "ds_read_b64_tr_b16 v[50:51], v60 offset:8704\n\t" \
  "v_mfma_f32_16x16x32_bf16 v[116:119], v[52:55], v[12:15], v[116:119]\n\t" \
  "ds_read_b128 v[36:39], v57 offset:34816\n\t" \
  "v_mfma_f32_16x16x32_bf16 v[124:127], v[52:55], v[16:19], v[124:127]\n\t" \
  "s_waitcnt lgkmcnt(9)\n\t" \
  "v_mfma_f32_16x16x32_bf16 v[64:67], v[40:43], v[4:7], v[64:67]\n\t" \
  "ds_read_b64_tr_b16 v[52:53], v60 offset:4096\n\t" \
  "v_mfma_f32_16x16x32_bf16 v[72:75], v[40:43], v[8:11], v[72:75]\n\t" \
  "ds_read_b64_tr_b16 v[54:55], v60 offset:4608\n\t" \
  "v_mfma_f32_16x16x32_bf16 v[80:83], v[40:43], v[12:15], v[80:83]\n\t" \
  "v_mfma_f32_16x16x32_bf16 v[88:91], v[40:43], v[16:19], v[88:91]\n\t" \
  "s_waitcnt lgkmcnt(7)\n\t" \
  "v_mfma_f32_16x16x32_bf16 v[68:71], v[44:47], v[4:7], v[68:71]\n\t" \
  "ds_read_b64_tr_b16 v[40:41], v61 offset:8192\n\t" \
  "v_mfma_f32_16x16x32_bf16 v[76:79], v[44:47], v[8:11], v[76:79]\n\t" \
  "ds_read_b64_tr_b16 v[42:43], v61 offset:8704\n\t" \
  "v_mfma_f32_16x16x32_bf16 v[84:87], v[44:47], v[12:15], v[84:87]\n\t" \
  "v_mfma_f32_16x16x32_bf16 v[92:95], v[44:47], v[16:19], v[92:95]\n\t" \
  "N3b_%=:\n\t" \
  "s_waitcnt lgkmcnt(4)\n\t" \
  "v_mfma_f32_16x16x32_bf16 v[160:163], v[20:23], v[48:51], v[160:163]\n\t" \
  "ds_read_b64_tr_b16 v[44:45], v61 offset:4096\n\t" \
  "v_mfma_f32_16x16x32_bf16 v[128:131], v[24:27], v[48:51], v[128:131]\n\t" \
  "ds_read_b64_tr_b16 v[46:47], v61 offset:4608\n\t" \
  "v_mfma_f32_16x16x32_bf16 v[136:139], v[28:31], v[48:51], v[136:139]\n\t" \
  "s_barrier\n\t" \
  "v_mfma_f32_16x16x32_bf16 v[144:147], v[32:35], v[48:51], v[144:147]\n\t" \
  "v_mfma_f32_16x16x32_bf16 v[152:155], v[36:39], v[48:51], v[152:155]\n\t" \
  "s_waitcnt lgkmcnt(4)\n\t" \
  "v_mfma_f32_16x16x32_bf16 v[96:99], v[52:55], v[24:27], v[96:99]\n\t" \
  "ds_read_b64_tr_b16 v[48:49], v60 offset:0\n\t" \
  "v_mfma_f32_16x16x32_bf16 v[104:107], v[52:55], v[28:31], v[104:107]\n\t" \
  "ds_read_b64_tr_b16 v[50:51], v60 offset:512\n\t" \
  "v_mfma_f32_16x16x32_bf16 v[112:115], v[52:55], v[32:35], v[112:115]\n\t" \
  "ds_read_b128 v[0:3], v56 offset:36864\n\t" \
  "v_mfma_f32_16x16x32_bf16 v[120:123], v[52:55], v[36:39], v[120:123]\n\t" \
  "ds_read_b128 v[4:7], v56 offset:47104\n\t" \
  "s_waitcnt lgkmcnt(6)\n\t" \
  "v_mfma_f32_16x16x32_bf16 v[164:167], v[20:23], v[40:43], v[164:167]\n\t" \
  "ds_read_b64_tr_b16 v[52:53], v61 offset:0\n\t" \
  "v_mfma_f32_16x16x32_bf16 v[132:135], v[24:27], v[40:43], v[132:135]\n\t" \
  "ds_read_b64_tr_b16 v[54:55], v61 offset:512\n\t" \
  "v_mfma_f32_16x16x32_bf16 v[140:143], v[28:31], v[40:43], v[140:143]\n\t" \
  "ds_read_b128 v[8:11], v56 offset:49152\n\t" \
  "v_mfma_f32_16x16x32_bf16 v[148:151], v[32:35], v[40:43], v[148:151]\n\t" \
  "ds_read_b128 v[12:15], v56 offset:51200\n\t" \
  "v_mfma_f32_16x16x32_bf16 v[156:159], v[36:39], v[40:43], v[156:159]\n\t" \
  "s_waitcnt lgkmcnt(8)\n\t" \
  "v_mfma_f32_16x16x32_bf16 v[100:103], v[44:47], v[24:27], v[100:103]\n\t" \
  "ds_read_b64_tr_b16 v[40:41], v60 offset:20480\n\t" \
  "v_mfma_f32_16x16x32_bf16 v[108:111], v[44:47], v[28:31], v[108:111]\n\t" \
  "ds_read_b64_tr_b16 v[42:43], v60 offset:20992\n\t" \
  "v_mfma_f32_16x16x32_bf16 v[116:119], v[44:47], v[32:35], v[116:119]\n\t" \
  "ds_read_b128 v[16:19], v56 offset:53248\n\t" \
  "v_mfma_f32_16x16x32_bf16 v[124:127], v[44:47], v[36:39], v[124:127]\n\t" \
  "s_waitcnt lgkmcnt(9)\n\t" \
  "v_mfma_f32_16x16x32_bf16 v[64:67], v[48:51], v[24:27], v[64:67]\n\t" \
  "ds_read_b64_tr_b16 v[44:45], v60 offset:16384\n\t" \
  "v_mfma_f32_16x16x32_bf16 v[72:75], v[48:51], v[28:31], v[72:75]\n\t" \
  "ds_read_b64_tr_b16 v[46:47], v60 offset:16896\n\t" \
  "v_mfma_f32_16x16x32_bf16 v[80:83], v[48:51], v[32:35], v[80:83]\n\t" \
  "v_mfma_f32_16x16x32_bf16 v[88:91], v[48:51], v[36:39], v[88:91]\n\t" \
  "s_waitcnt lgkmcnt(7)\n\t" \
  "v_mfma_f32_16x16x32_bf16 v[68:71], v[52:55], v[24:27], v[68:71]\n\t" \
  "ds_read_b64_tr_b16 v[48:49], v61 offset:20480\n\t" \
  "v_mfma_f32_16x16x32_bf16 v[76:79], v[52:55], v[28:31], v[76:79]\n\t" \
  "ds_read_b64_tr_b16 v[50:51], v61 offset:20992\n\t" \
  "v_mfma_f32_16x16x32_bf16 v[84:87], v[52:55], v[32:35], v[84:87]\n\t" \
  "v_mfma_f32_16x16x32_bf16 v[92:95], v[52:55], v[36:39], v[92:95]\n\t" \
  "N4b_%=:\n\t" \
  "s_waitcnt lgkmcnt(4)\n\t" \
  "v_mfma_f32_16x16x32_bf16 v[160:163], v[0:3], v[40:43], v[160:163]\n\t" \
  "ds_read_b64_tr_b16 v[52:53], v61 offset:16384\n\t" \
  "v_mfma_f32_16x16x32_bf16 v[128:131], v[4:7], v[40:43], v[128:131]\n\t" \
  "ds_read_b64_tr_b16 v[54:55], v61 offset:16896\n\t" \
  "v_mfma_f32_16x16x32_bf16 v[136:139], v[8:11], v[40:43], v[136:139]\n\t" \
  "s_barrier\n\t" \
  "v_mfma_f32_16x16x32_bf16 v[144:147], v[12:15], v[40:43], v[144:147]\n\t" \
  "v_mfma_f32_16x16x32_bf16 v[152:155], v[16:19], v[40:43], v[152:155]\n\t" \
  "s_waitcnt lgkmcnt(4)\n\t" \
  "v_mfma_f32_16x16x32_bf16 v[96:99], v[44:47], v[4:7], v[96:99]\n\t" \
  "ds_read_b64_tr_b16 v[40:41], v60 offset:12288\n\t" \
  "v_mfma_f32_16x16x32_bf16 v[104:107], v[44:47], v[8:11], v[104:107]\n\t" \
  "ds_read_b64_tr_b16 v[42:43], v60 offset:12800\n\t" \
  "v_mfma_f32_16x16x32_bf16 v[112:115], v[44:47], v[12:15], v[112:115]\n\t" \
  "ds_read_b128 v[20:23], v57 offset:36864\n\t" \
  "v_mfma_f32_16x16x32_bf16 v[120:123], v[44:47], v[16:19], v[120:123]\n\t" \
  "ds_read_b128 v[24:27], v57 offset:47104\n\t" \
  "s_waitcnt lgkmcnt(6)\n\t" \
  "v_mfma_f32_16x16x32_bf16 v[164:167], v[0:3], v[48:51], v[164:167]\n\t" \
  "ds_read_b64_tr_b16 v[44:45], v61 offset:12288\n\t" \
  "v_mfma_f32_16x16x32_bf16 v[132:135], v[4:7], v[48:51], v[132:135]\n\t" \
  "ds_read_b64_tr_b16 v[46:47], v61 offset:12800\n\t" \
  "v_mfma_f32_16x16x32_bf16 v[140:143], v[8:11], v[48:51], v[140:143]\n\t" \
  "ds_read_b128 v[28:31], v57 offset:49152\n\t" \
  "v_mfma_f32_16x16x32_bf16 v[148:151], v[12:15], v[48:51], v[148:151]\n\t" \
  "ds_read_b128 v[32:35], v57 offset:51200\n\t" \
  "v_mfma_f32_16x16x32_bf16 v[156:159], v[16:19], v[48:51], v[156:159]\n\t" \
  "s_waitcnt lgkmcnt(8)\n\t" \
  "v_mfma_f32_16x16x32_bf16 v[100:103], v[52:55], v[4:7], v[100:103]\n\t" \
  "ds_read_b64_tr_b16 v[48:49], v60 offset:32768\n\t" \
  "v_mfma_f32_16x16x32_bf16 v[108:111], v[52:55], v[8:11], v[108:111]\n\t" \
  "ds_read_b64_tr_b16 v[50:51], v60 offset:33280\n\t" \
  "v_mfma_f32_16x16x32_bf16 v[116:119], v[52:55], v[12:15], v[116:119]\n\t" \
  "ds_read_b128 v[36:39], v57 offset:53248\n\t" \
  "v_mfma_f32_16x16x32_bf16 v[124:127], v[52:55], v[16:19], v[124:127]\n\t" \
  "s_waitcnt lgkmcnt(9)\n\t" \
  "v_mfma_f32_16x16x32_bf16 v[64:67], v[40:43], v[4:7], v[64:67]\n\t" \
  "ds_read_b64_tr_b16 v[52:53], v60 offset:28672\n\t" \
  "v_mfma_f32_16x16x32_bf16 v[72:75], v[40:43], v[8:11], v[72:75]\n\t" \
  "ds_read_b64_tr_b16 v[54:55], v60 offset:29184\n\t" \
  "v_mfma_f32_16x16x32_bf16 v[80:83], v[40:43], v[12:15], v[80:83]\n\t" \
  "v_mfma_f32_16x16x32_bf16 v[88:91], v[40:43], v[16:19], v[88:91]\n\t" \
  "s_waitcnt lgkmcnt(7)\n\t" \
  "v_mfma_f32_16x16x32_bf16 v[68:71], v[44:47], v[4:7], v[68:71]\n\t" \
  "ds_read_b64_tr_b16 v[40:41], v61 offset:32768\n\t" \
  "v_mfma_f32_16x16x32_bf16 v[76:79], v[44:47], v[8:11], v[76:79]\n\t" \
  "ds_read_b64_tr_b16 v[42:43], v61 offset:33280\n\t" \
  "v_mfma_f32_16x16x32_bf16 v[84:87], v[44:47], v[12:15], v[84:87]\n\t" \
  "v_mfma_f32_16x16x32_bf16 v[92:95], v[44:47], v[16:19], v[92:95]\n\t" \
  "N5b_%=:\n\t" \
  "s_waitcnt lgkmcnt(4)\n\t" \
  "v_mfma_f32_16x16x32_bf16 v[160:163], v[20:23], v[48:51], v[160:163]\n\t" \
  "ds_read_b64_tr_b16 v[44:45], v61 offset:28672\n\t" \
  "v_mfma_f32_16x16x32_bf16 v[128:131], v[24:27], v[48:51], v[128:131]\n\t" \
  "ds_read_b64_tr_b16 v[46:47], v61 offset:29184\n\t" \
  "v_mfma_f32_16x16x32_bf16 v[136:139], v[28:31], v[48:51], v[136:139]\n\t" \
  "s_barrier\n\t" \
  "v_mfma_f32_16x16x32_bf16 v[144:147], v[32:35], v[48:51], v[144:147]\n\t" \
  "v_mfma_f32_16x16x32_bf16 v[152:155], v[36:39], v[48:51], v[152:155]\n\t" \
  "s_waitcnt lgkmcnt(4)\n\t" \
  "v_mfma_f32_16x16x32_bf16 v[96:99], v[52:55], v[24:27], v[96:99]\n\t" \
  "ds_read_b64_tr_b16 v[48:49], v60 offset:24576\n\t" \
  "v_mfma_f32_16x16x32_bf16 v[104:107], v[52:55], v[28:31], v[104:107]\n\t" \
  "ds_read_b64_tr_b16 v[50:51], v60 offset:25088\n\t" \
  "v_mfma_f32_16x16x32_bf16 v[112:115], v[52:55], v[32:35], v[112:115]\n\t" \
  "ds_read_b128 v[0:3], v56 offset:0\n\t" \
  "v_mfma_f32_16x16x32_bf16 v[120:123], v[52:55], v[36:39], v[120:123]\n\t" \
  "ds_read_b128 v[4:7], v56 offset:10240\n\t" \
  "s_waitcnt lgkmcnt(6)\n\t" \
  "v_mfma_f32_16x16x32_bf16 v[164:167], v[20:23], v[40:43], v[164:167]\n\t" \
  "ds_read_b64_tr_b16 v[52:53], v61 offset:24576\n\t" \
  "v_mfma_f32_16x16x32_bf16 v[132:135], v[24:27], v[40:43], v[132:135]\n\t" \
  "ds_read_b64_tr_b16 v[54:55], v61 offset:25088\n\t" \
  "v_mfma_f32_16x16x32_bf16 v[140:143], v[28:31], v[40:43], v[140:143]\n\t" \
  "ds_read_b128 v[8:11], v56 offset:12288\n\t" \
  "v_mfma_f32_16x16x32_bf16 v[148:151], v[32:35], v[40:43], v[148:151]\n\t" \
  "ds_read_b128 v[12:15], v56 offset:14336\n\t" \
  "v_mfma_f32_16x16x32_bf16 v[156:159], v[36:39], v[40:43], v[156:159]\n\t" \
  "s_waitcnt lgkmcnt(8)\n\t" \
  "v_mfma_f32_16x16x32_bf16 v[100:103], v[44:47], v[24:27], v[100:103]\n\t" \
  "ds_read_b64_tr_b16 v[40:41], v58 offset:8192\n\t" \
  "v_mfma_f32_16x16x32_bf16 v[108:111], v[44:47], v[28:31], v[108:111]\n\t" \
  "ds_read_b64_tr_b16 v[42:43], v58 offset:8704\n\t" \
  "v_mfma_f32_16x16x32_bf16 v[116:119], v[44:47], v[32:35], v[116:119]\n\t" \
  "ds_read_b128 v[16:19], v56 offset:16384\n\t" \
  "v_mfma_f32_16x16x32_bf16 v[124:127], v[44:47], v[36:39], v[124:127]\n\t" \
  "s_waitcnt lgkmcnt(9)\n\t" \
  "v_mfma_f32_16x16x32_bf16 v[64:67], v[48:51], v[24:27], v[64:67]\n\t" \
  "ds_read_b64_tr_b16 v[44:45], v58 offset:4096\n\t" \
  "v_mfma_f32_16x16x32_bf16 v[72:75], v[48:51], v[28:31], v[72:75]\n\t" \
  "ds_read_b64_tr_b16 v[46:47], v58 offset:4608\n\t" \
  "v_mfma_f32_16x16x32_bf16 v[80:83], v[48:51], v[32:35], v[80:83]\n\t" \
  "v_mfma_f32_16x16x32_bf16 v[88:91], v[48:51], v[36:39], v[88:91]\n\t" \
  "s_waitcnt lgkmcnt(7)\n\t" \
  "v_mfma_f32_16x16x32_bf16 v[68:71], v[52:55], v[24:27], v[68:71]\n\t" \
  "ds_read_b64_tr_b16 v[48:49], v59 offset:8192\n\t" \
  "v_mfma_f32_16x16x32_bf16 v[76:79], v[52:55], v[28:31], v[76:79]\n\t" \
  "ds_read_b64_tr_b16 v[50:51], v59 offset:8704\n\t" \
  "s_addk_i32 %[it], 1\n\t" \
  "v_mfma_f32_16x16x32_bf16 v[84:87], v[52:55], v[32:35], v[84:87]\n\t" \
  "s_cmp_lt_u32 %[it], 6\n\t" \
  "v_mfma_f32_16x16x32_bf16 v[92:95], v[52:55], v[36:39], v[92:95]\n\t" \
  "s_cbranch_scc1 N0b_%=\n\t" \
  "s_memrealtime %[st2]\n\t" \
  "s_setprio 3\n\t" \
  "v_cvt_pk_bf16_f32 v160, v160, v161\n\t" \
  "v_cvt_pk_bf16_f32 v161, v162, v163\n\t" \
  "v_cvt_pk_bf16_f32 v162, v164, v165\n\t" \
  "v_cvt_pk_bf16_f32 v163, v166, v167\n\t" \
  "ds_write_b128 v63, v[160:163] offset:2048\n\t" \
  "v_mov_b32 v57, %[fa]\n\t" \
  "v_mov_b32 v58, 1\n\t" \
  "s_mov_b64 exec, 1\n\t" \
  "ds_write_b32 v57, v58\n\t" \
  "s_mov_b64 exec, -1\n\t" \
  "v_cvt_pk_bf16_f32 v120, v120, v121\n\t" \
  "v_cvt_pk_bf16_f32 v121, v122, v123\n\t" \
  "v_cvt_pk_bf16_f32 v122, v124, v125\n\t" \
  "v_cvt_pk_bf16_f32 v123, v126, v127\n\t" \
  "v_cvt_pk_bf16_f32 v28, v152, v153\n\t" \
  "v_cvt_pk_bf16_f32 v29, v154, v155\n\t" \
  "v_cvt_pk_bf16_f32 v38, v156, v157\n\t" \
  "v_cvt_pk_bf16_f32 v39, v158, v159\n\t" \
  "NX_%=:\n\t" \
  "v_cvt_pk_bf16_f32 v96, v96, v97\n\t" \
  "v_cvt_pk_bf16_f32 v97, v98, v99\n\t" \
  "v_cvt_pk_bf16_f32 v98, v100, v101\n\t" \
  "v_cvt_pk_bf16_f32 v99, v102, v103\n\t" \
  "v_cvt_pk_bf16_f32 v104, v104, v105\n\t" \
  "v_cvt_pk_bf16_f32 v105, v106, v107\n\t" \
  "v_cvt_pk_bf16_f32 v106, v108, v109\n\t" \
  "v_cvt_pk_bf16_f32 v107, v110, v111\n\t" \
  "v_mov_b32 v60, 0xff61b1e6\n\t" \
  "v_cndmask_b32_e64 v56, v60, 0, s[68:69]\n\t" \
  "v_cndmask_b32_e64 v57, v60, 0, s[70:71]\n\t" \
  "v_cndmask_b32_e64 v58, v60, 0, s[72:73]\n\t" \
  "v_cndmask_b32_e64 v59, v60, 0, s[74:75]\n\t" \
  "v_cvt_pk_bf16_f32 v72, v72, v73\n\t" \
  "v_cvt_pk_bf16_f32 v73, v74, v75\n\t" \
  "v_cvt_pk_bf16_f32 v74, v76, v77\n\t" \
  "v_cvt_pk_bf16_f32 v75, v78, v79\n\t" \
  "v_cndmask_b32_e64 v52, v60, 0, s[76:77]\n\t" \
  "v_cndmask_b32_e64 v53, v60, 0, s[78:79]\n\t" \
  "v_cndmask_b32_e64 v54, v60, 0, s[80:81]\n\t" \
  "v_cndmask_b32_e64 v55, v60, 0, s[82:83]\n\t" \
  "v_cvt_pk_bf16_f32 v24, v136, v137\n\t" \
  "v_cvt_pk_bf16_f32 v25, v138, v139\n\t" \
  "v_cvt_pk_bf16_f32 v34, v140, v141\n\t" \
  "v_cvt_pk_bf16_f32 v35, v142, v143\n\t" \
  "v_cvt_pk_bf16_f32 v26, v144, v145\n\t" \
  "v_cvt_pk_bf16_f32 v27, v146, v147\n\t" \
  "v_cvt_pk_bf16_f32 v36, v148, v149\n\t" \
  "v_cvt_pk_bf16_f32 v37, v150, v151\n\t" \
  "v_mfma_f32_16x16x32_bf16 v[136:139], v[96:99], v[72:75], v[56:59]\n\t" \
  "v_cvt_pk_bf16_f32 v112, v112, v113\n\t" \
  "v_cvt_pk_bf16_f32 v113, v114, v115\n\t" \
  "v_mfma_f32_16x16x32_bf16 v[140:143], v[104:107], v[72:75], v[52:55]\n\t" \
  "v_cvt_pk_bf16_f32 v114, v116, v117\n\t" \
  "v_cvt_pk_bf16_f32 v115, v118, v119\n\t" \
  "v_cvt_pk_bf16_f32 v80, v80, v81\n\t" \
  "v_cvt_pk_bf16_f32 v81, v82, v83\n\t" \
  "v_cvt_pk_bf16_f32 v82, v84, v85\n\t" \
  "v_cvt_pk_bf16_f32 v83, v86, v87\n\t" \
  "v_cvt_pk_bf16_f32 v22, v128, v129\n\t" \
  "v_cvt_pk_bf16_f32 v23, v130, v131\n\t" \
  "v_mfma_f32_16x16x32_bf16 v[144:147], v[104:107], v[80:83], v[56:59]\n\t" \
  "v_cvt_pk_bf16_f32 v32, v132, v133\n\t" \
  "v_cvt_pk_bf16_f32 v33, v134, v135\n\t" \
  "v_mfma_f32_16x16x32_bf16 v[148:151], v[112:115], v[80:83], v[52:55]\n\t" \
  "v_cvt_pk_bf16_f32 v88, v88, v89\n\t" \
  "v_cvt_pk_bf16_f32 v89, v90, v91\n\t" \
  "v_cvt_pk_bf16_f32 v90, v92, v93\n\t" \
  "v_cvt_pk_bf16_f32 v91, v94, v95\n\t" \
  "v_mov_b32 v61, %[fp]\n\t" \
  "v_add_u32 v60, %[vo], v63\n\t" \
  "v_mfma_f32_16x16x32_bf16 v[152:155], v[112:115], v[88:91], v[56:59]\n\t" \
  "ds_read_b32 v0, v61\n\t" \
  "ds_read_b64 v[20:21], v60\n\t" \
  "ds_read_b64 v[30:31], v60 offset:8\n\t" \
  "s_mov_b64 exec, %[rm]\n\t" \
  "ds_read_b128 v[160:163], v63\n\t" \
  "s_mov_b64 exec, -1\n\t" \
  "v_mfma_f32_16x16x32_bf16 v[156:159], v[120:123], v[88:91], v[52:55]\n\t" \
  "v_cvt_pk_bf16_f32 v64, v64, v65\n\t" \
  "v_cvt_pk_bf16_f32 v65, v66, v67\n\t" \
  "v_cvt_pk_bf16_f32 v66, v68, v69\n\t" \
  "v_cvt_pk_bf16_f32 v67, v70, v71\n\t" \
  "v_max_f32 v136, v136, v137\n\t" \
  "v_max_f32 v138, v138, v139\n\t" \
  "v_mfma_f32_16x16x32_bf16 v[132:135], v[96:99], v[64:67], v[52:55]\n\t" \
  "v_max_f32 v140, v140, v141\n\t" \
  "v_max_f32 v142, v142, v143\n\t" \
  "AW_%=:\n\t" \
  "s_waitcnt lgkmcnt(0)\n\t" \
  "v_cmp_eq_u32 vcc, 0, v0\n\t" \
  "v_max3_f32 v5, v136, v138, v140\n\t" \
  "v_max_f32 v5, v5, v142\n\t" \
  "s_cbranch_vccnz AR_%=\n\t" \
  "s_cmp_eq_u32 %[z], 0\n\t" \
  "s_cbranch_scc1 AZ_%=\n\t" \
  "AY_%=:\n\t" \
  "v_mfma_f32_16x16x32_bf16 v[128:131], v[160:163], v[64:67], v[56:59]\n\t" \
  "v_max_f32 v144, v144, v145\n\t" \
  "v_max_f32 v146, v146, v147\n\t" \
  "v_max_f32 v148, v148, v149\n\t" \
  "v_max_f32 v150, v150, v151\n\t" \
  "v_max3_f32 v6, v144, v146, v148\n\t" \
  "v_max_f32 v6, v6, v150\n\t" \
  "v_max_f32 v152, v152, v153\n\t" \
  "v_max_f32 v154, v154, v155\n\t" \
  "v_max_f32 v156, v156, v157\n\t" \
  "v_max_f32 v158, v158, v159\n\t" \
  "v_max3_f32 v7, v152, v154, v156\n\t" \
  "v_max_f32 v7, v7, v158\n\t" \
  "v_mov_b32 v8, 0xc0c0100\n\t" \
  "v_mov_b32 v9, 0x1000c0c\n\t" \
  "v_max_f32 v128, v128, v129\n\t" \
  "v_max_f32 v130, v130, v131\n\t" \
  "v_max_f32 v132, v132, v133\n\t" \
  "v_max_f32 v134, v134, v135\n\t" \
  "v_max3_f32 v4, v128, v130, v132\n\t" \
  "v_max_f32 v4, v4, v134\n\t" \
  "v_cndmask_b32_e64 v60, v8, v9, s[84:85]\n\t" \
  "v_mov_b32 v8, 0xc0c0302\n\t" \
  "v_permlane16_swap_b32 v6, v7\n\t" \
  "v_permlane16_swap_b32 v4, v5\n\t" \
  "v_max_f32 v6, v6, v7\n\t" \
  "v_max_f32 v4, v4, v5\n\t" \
  "v_mov_b32 v9, 0x3020c0c\n\t" \
  "v_cndmask_b32_e64 v61, v8, v9, s[84:85]\n\t" \
  "v_permlane32_swap_b32 v4, v6\n\t" \
  "v_max_f32 v4, v4, v6\n\t" \
  "v_mul_f32 v6, 0xbe8293ee, v4\n\t" \
  "v_mul_f32 v4, 0xbe8293ee, v4\n\t" \
  "s_nop 1\n\t" \
  "v_permlane32_swap_b32 v4, v6\n\t" \
  "v_mov_b32 v5, v4\n\t" \
  "v_mov_b32 v7, v6\n\t" \
  "s_nop 0\n\t" \
  "v_permlane16_swap_b32 v4, v5\n\t" \
  "v_permlane16_swap_b32 v6, v7\n\t" \
  "v_fmamk_f32 v128, v128, 0x3e8293ee, v4\n\t" \
  "v_fmamk_f32 v130, v130, 0x3e8293ee, v4\n\t" \
  "v_fmamk_f32 v132, v132, 0x3e8293ee, v4\n\t" \
  "v_fmamk_f32 v134, v134, 0x3e8293ee, v4\n\t" \
  "v_fmamk_f32 v136, v136, 0x3e8293ee, v5\n\t" \
  "v_fmamk_f32 v138, v138, 0x3e8293ee, v5\n\t" \
  "v_fmamk_f32 v140, v140, 0x3e8293ee, v5\n\t" \
  "v_fmamk_f32 v142, v142, 0x3e8293ee, v5\n\t" \
  "v_fmamk_f32 v144, v144, 0x3e8293ee, v6\n\t" \
  "v_fmamk_f32 v146, v146, 0x3e8293ee, v6\n\t" \
  "v_fmamk_f32 v148, v148, 0x3e8293ee, v6\n\t" \
  "v_fmamk_f32 v150, v150, 0x3e8293ee, v6\n\t" \
  "v_fmamk_f32 v152, v152, 0x3e8293ee, v7\n\t" \
  "v_fmamk_f32 v154, v154, 0x3e8293ee, v7\n\t" \
  "v_fmamk_f32 v156, v156, 0x3e8293ee, v7\n\t" \
  "v_fmamk_f32 v158, v158, 0x3e8293ee, v7\n\t" \
  "v_exp_f32 v128, v128\n\t" \
  "v_exp_f32 v130, v130\n\t" \
  "v_exp_f32 v132, v132\n\t" \
  "v_exp_f32 v134, v134\n\t" \
  "v_exp_f32 v136, v136\n\t" \
  "v_exp_f32 v138, v138\n\t" \
  "v_exp_f32 v140, v140\n\t" \
  "v_exp_f32 v142, v142\n\t" \
  "v_exp_f32 v144, v144\n\t" \
  "v_exp_f32 v146, v146\n\t" \
  "v_exp_f32 v148, v148\n\t" \
  "v_exp_f32 v150, v150\n\t" \
  "v_exp_f32 v152, v152\n\t" \
  "v_exp_f32 v154, v154\n\t" \
  "v_exp_f32 v156, v156\n\t" \
  "v_exp_f32 v158, v158\n\t" \
  "v_add_f32 v4, v128, v130\n\t" \
  "v_add_f32 v8, v132, v134\n\t" \
  "v_add_f32 v5, v136, v138\n\t" \
  "v_add_f32 v9, v140, v142\n\t" \
  "v_add_f32 v6, v144, v146\n\t" \
  "v_add_f32 v10, v148, v150\n\t" \
  "v_add_f32 v7, v152, v154\n\t" \
  "v_add_f32 v11, v156, v158\n\t" \
  "v_add_f32 v4, v4, v8\n\t" \
  "v_add_f32 v5, v5, v9\n\t" \
  "v_add_f32 v6, v6, v10\n\t" \
  "v_add_f32 v7, v7, v11\n\t" \
  "v_permlane16_swap_b32 v4, v5\n\t" \
  "s_nop 0\n\t" \
  "v_permlane16_swap_b32 v6, v7\n\t" \
  "v_add_f32 v4, v4, v5\n\t" \
  "v_add_f32 v6, v6, v7\n\t" \
  "s_nop 1\n\t" \
  "v_permlane32_swap_b32 v4, v6\n\t" \
  "v_add_f32 v4, v4, v6\n\t" \
  "v_rcp_f32 v4, v4\n\t" \
  "s_nop 0\n\t" \
  "v_mov_b32 v6, v4\n\t" \
  "s_nop 1\n\t" \
  "v_permlane32_swap_b32 v4, v6\n\t" \
  "v_mov_b32 v5, v4\n\t" \
  "v_mov_b32 v7, v6\n\t" \
  "s_nop 0\n\t" \
  "v_permlane16_swap_b32 v4, v5\n\t" \
  "v_permlane16_swap_b32 v6, v7\n\t" \
  "v_mul_f32 v128, v128, v4\n\t" \
  "v_mul_f32 v130, v130, v4\n\t" \
  "v_mul_f32 v132, v132, v4\n\t" \
  "v_mul_f32 v134, v134, v4\n\t" \
  "v_cvt_pk_bf16_f32 v128, v128, v130\n\t" \
  "v_cvt_pk_bf16_f32 v132, v132, v134\n\t" \
  "v_perm_b32 v68, 0, v128, v60\n\t" \
  "v_perm_b32 v69, 0, v128, v61\n\t" \
  "v_perm_b32 v70, 0, v132, v60\n\t" \
  "v_perm_b32 v71, 0, v132, v61\n\t" \
  "v_mul_f32 v136, v136, v5\n\t" \
  "v_mul_f32 v138, v138, v5\n\t" \
  "v_mfma_f32_16x16x32_bf16 v[128:131], v[20:23], v[68:71], 0\n\t" \
  "v_mul_f32 v140, v140, v5\n\t" \
  "v_mul_f32 v142, v142, v5\n\t" \
  "v_cvt_pk_bf16_f32 v136, v136, v138\n\t" \
  "v_mfma_f32_16x16x32_bf16 v[132:135], v[30:33], v[68:71], 0\n\t" \
  "v_cvt_pk_bf16_f32 v140, v140, v142\n\t" \
  "v_perm_b32 v76, 0, v136, v60\n\t" \
  "v_perm_b32 v77, 0, v136, v61\n\t" \
  "v_perm_b32 v78, 0, v140, v60\n\t" \
  "v_perm_b32 v79, 0, v140, v61\n\t" \
  "v_mul_f32 v144, v144, v6\n\t" \
  "v_mul_f32 v146, v146, v6\n\t" \
  "v_mfma_f32_16x16x32_bf16 v[136:139], v[22:25], v[76:79], 0\n\t" \
  "v_mul_f32 v148, v148, v6\n\t" \
  "v_mul_f32 v150, v150, v6\n\t" \
  "v_cvt_pk_bf16_f32 v144, v144, v146\n\t" \
  "v_mfma_f32_16x16x32_bf16 v[140:143], v[32:35], v[76:79], 0\n\t" \
  "v_cvt_pk_bf16_f32 v148, v148, v150\n\t" \
  "v_perm_b32 v84, 0, v144, v60\n\t" \
  "v_perm_b32 v85, 0, v144, v61\n\t" \
  "v_perm_b32 v86, 0, v148, v60\n\t" \
  "v_perm_b32 v87, 0, v148, v61\n\t" \
  "v_cvt_pk_bf16_f32 v100, v128, v129\n\t" \
  "v_cvt_pk_bf16_f32 v101, v130, v131\n\t" \
  "v_mfma_f32_16x16x32_bf16 v[144:147], v[24:27], v[84:87], 0\n\t" \
  "v_cvt_pk_bf16_f32 v102, v132, v133\n\t" \
  "v_cvt_pk_bf16_f32 v103, v134, v135\n\t" \
  "global_store_dwordx4 v62, v[100:103], %[ot] offset:0 sc1\n\t" \
  "v_mfma_f32_16x16x32_bf16 v[148:151], v[34:37], v[84:87], 0\n\t" \
  "v_mul_f32 v152, v152, v7\n\t" \
  "v_mul_f32 v154, v154, v7\n\t" \
  "v_mul_f32 v156, v156, v7\n\t" \
  "v_mul_f32 v158, v158, v7\n\t" \
  "v_cvt_pk_bf16_f32 v152, v152, v154\n\t" \
  "v_cvt_pk_bf16_f32 v156, v156, v158\n\t" \
  "v_perm_b32 v92, 0, v152, v60\n\t" \
  "v_perm_b32 v93, 0, v152, v61\n\t" \
  "v_perm_b32 v94, 0, v156, v60\n\t" \
  "v_perm_b32 v95, 0, v156, v61\n\t" \
  "v_cvt_pk_bf16_f32 v108, v136, v137\n\t" \
  "v_cvt_pk_bf16_f32 v109, v138, v139\n\t" \
  "v_mfma_f32_16x16x32_bf16 v[152:155], v[26:29], v[92:95], 0\n\t" \
  "v_cvt_pk_bf16_f32 v110, v140, v141\n\t" \
  "v_cvt_pk_bf16_f32 v111, v142, v143\n\t" \
  "global_store_dwordx4 v62, v[108:111], %[ot] offset:1024 sc1\n\t" \
  "v_mfma_f32_16x16x32_bf16 v[156:159], v[36:39], v[92:95], 0\n\t" \
  "v_cvt_pk_bf16_f32 v116, v144, v145\n\t" \
  "v_cvt_pk_bf16_f32 v117, v146, v147\n\t" \
  "v_cvt_pk_bf16_f32 v118, v148, v149\n\t" \
  "v_cvt_pk_bf16_f32 v119, v150, v151\n\t" \
  "global_store_dwordx4 v62, v[116:119], %[ou] offset:0 sc1\n\t" \
  "v_cvt_pk_bf16_f32 v124, v152, v153\n\t" \
  "v_cvt_pk_bf16_f32 v125, v154, v155\n\t" \
  "s_nop 0\n\t" \
  "v_cvt_pk_bf16_f32 v126, v156, v157\n\t" \
  "v_cvt_pk_bf16_f32 v127, v158, v159\n\t" \
  "global_store_dwordx4 v62, v[124:127], %[ou] offset:1024 sc1\n\t" \
  "s_branch AE_%=\n\t" \
  "AR_%=:\n\t" \
  "s_addk_i32 %[it], 1\n\t" \
  "s_cmp_gt_u32 %[it], 0x400000\n\t" \
  "s_cbranch_scc1 AY_%=\n\t" \
  "ds_read_b32 v0, v61\n\t" \
  "ds_read_b64 v[20:21], v60\n\t" \
  "ds_read_b64 v[30:31], v60 offset:8\n\t" \
  "s_mov_b64 exec, %[rm]\n\t" \
  "ds_read_b128 v[160:163], v63\n\t" \
  "s_mov_b64 exec, -1\n\t" \
  "s_branch AW_%=\n\t" \
  "AZ_%=:\n\t" \
  "v_mov_b32 v0, 0xff61b1e6\n\t" \
  "v_mov_b32 v1, 0xcfa8965f\n\t" \
  "s_nop 3\n\t" \
  "v_cndmask_b32_e64 v56, v0, v1, s[68:69]\n\t" \
  "v_cndmask_b32_e64 v57, v0, v1, s[70:71]\n\t" \
  "v_cndmask_b32_e64 v58, v0, v1, s[72:73]\n\t" \
  "v_cndmask_b32_e64 v59, v0, v1, s[74:75]\n\t" \
  "s_nop 1\n\t" \
  "s_branch AY_%=\n\t" \
  "AE_%=:\n\t" \
  "s_nop 1\n\t"
#define N2_CLOB "v0", "v1", "v2", "v3", "v4", "v5", "v6", "v7", "v8", "v9", "v10", "v11", "v12", "v13", "v14", "v15", "v16", "v17", "v18", "v19", "v20", "v21", "v22", "v23", "v24", "v25", "v26", "v27", "v28", "v29", "v30", "v31", "v32", "v33", "v34", "v35", "v36", "v37", "v38", "v39", "v40", "v41", "v42", "v43", "v44", "v45", "v46", "v47", "v48", "v49", "v50", "v51", "v52", "v53", "v54", "v55", "v60", "v61", "v64", "v65", "v66", "v67", "v68", "v69", "v70", "v71", "v72", "v73", "v74", "v75", "v76", "v77", "v78", "v79", "v80", "v81", "v82", "v83", "v84", "v85", "v86", "v87", "v88", "v89", "v90", "v91", "v92", "v93", "v94", "v95", "v96", "v97", "v98", "v99", "v100", "v101", "v102", "v103", "v104", "v105", "v106", "v107", "v108", "v109", "v110", "v111", "v112", "v113", "v114", "v115", "v116", "v117", "v118", "v119", "v120", "v121", "v122", "v123", "v124", "v125", "v126", "v127", "v128", "v129", "v130", "v131", "v132", "v133", "v134", "v135", "v136", "v137", "v138", "v139", "v140", "v141", "v142", "v143", "v144", "v145", "v146", "v147", "v148", "v149", "v150", "v151", "v152", "v153", "v154", "v155", "v156", "v157", "v158", "v159", "v160", "v161", "v162", "v163", "v164", "v165", "v166", "v167", "s68", "s69", "s70", "s71", "s72", "s73", "s74", "s75", "s76", "s77", "s78", "s79", "s80", "s81", "s82", "s83", "s84", "s85"

#define CV_ASM \
  "ds_read_b64 v[0:1], v28 offset:0\n\t" \
  "ds_read_b64 v[2:3], v29 offset:0\n\t" \
  "ds_read_b64 v[4:5], v30 offset:1024\n\t" \
  "ds_read_b64 v[6:7], v31 offset:1024\n\t" \
  "ds_read_b64 v[8:9], v24 offset:2048\n\t" \
  "ds_read_b64 v[10:11], v25 offset:2048\n\t" \
  "ds_read_b64 v[12:13], v26 offset:3072\n\t" \
  "ds_read_b64 v[14:15], v27 offset:3072\n\t" \
  "ds_read_b64 v[16:17], v28 offset:4096\n\t" \
  "ds_read_b64 v[18:19], v29 offset:4096\n\t" \
  "ds_read_b64 v[20:21], v30 offset:5120\n\t" \
  "ds_read_b64 v[22:23], v31 offset:5120\n\t" \
  "s_waitcnt lgkmcnt(10)\n\t" \
  "v_mfma_f32_16x16x32_bf16 v[128:131], v[68:71], v[0:3], 0\n\t" \
  "v_mfma_f32_16x16x32_bf16 v[132:135], v[76:79], v[0:3], 0\n\t" \
  "v_mfma_f32_16x16x32_bf16 v[136:139], v[84:87], v[0:3], 0\n\t" \
  "v_mfma_f32_16x16x32_bf16 v[140:143], v[92:95], v[0:3], 0\n\t" \
  "v_mfma_f32_16x16x32_bf16 v[144:147], v[100:103], v[0:3], 0\n\t" \
  "v_mfma_f32_16x16x32_bf16 v[148:151], v[108:111], v[0:3], 0\n\t" \
  "v_mfma_f32_16x16x32_bf16 v[152:155], v[116:119], v[0:3], 0\n\t" \
  "v_mfma_f32_16x16x32_bf16 v[156:159], v[124:127], v[0:3], 0\n\t" \
  "ds_read_b64 v[0:1], v24 offset:6144\n\t" \
  "s_waitcnt lgkmcnt(9)\n\t" \
  "v_mfma_f32_16x16x32_bf16 v[128:131], v[64:67], v[4:7], v[128:131]\n\t" \
  "ds_read_b64 v[2:3], v25 offset:6144\n\t" \
  "v_mfma_f32_16x16x32_bf16 v[132:135], v[72:75], v[4:7], v[132:135]\n\t" \
  "v_mfma_f32_16x16x32_bf16 v[136:139], v[80:83], v[4:7], v[136:139]\n\t" \
  "v_mfma_f32_16x16x32_bf16 v[140:143], v[88:91], v[4:7], v[140:143]\n\t" \
  "v_mfma_f32_16x16x32_bf16 v[144:147], v[96:99], v[4:7], v[144:147]\n\t" \
  "v_mfma_f32_16x16x32_bf16 v[148:151], v[104:107], v[4:7], v[148:151]\n\t" \
  "v_mfma_f32_16x16x32_bf16 v[152:155], v[112:115], v[4:7], v[152:155]\n\t" \
  "v_mfma_f32_16x16x32_bf16 v[156:159], v[120:123], v[4:7], v[156:159]\n\t" \
  "s_cmp_eq_u32 %[rem], 0\n\t" \
  "s_cbranch_scc1 CB16_%=\n\t" \
  "s_barrier\n\t" \
  "s_sub_u32 %[rem], %[rem], 1\n\t" \
  "CB16_%=:\n\t" \
  "ds_read_b64 v[4:5], v26 offset:7168\n\t" \
  "ds_bpermute_b32 v34, v32, v131\n\t" \
  "v_readlane_b32 %[p0], v131, 63\n\t" \
  "s_waitcnt lgkmcnt(10)\n\t" \
  "v_mfma_f32_16x16x32_bf16 v[132:135], v[68:71], v[8:11], v[132:135]\n\t" \
  "ds_read_b64 v[6:7], v27 offset:7168\n\t" \
  "v_mov_b32 v131, v130\n\t" \
  "v_mfma_f32_16x16x32_bf16 v[136:139], v[76:79], v[8:11], v[136:139]\n\t" \
  "v_mov_b32 v130, v129\n\t" \
  "v_mfma_f32_16x16x32_bf16 v[140:143], v[84:87], v[8:11], v[140:143]\n\t" \
  "v_mov_b32 v129, v128\n\t" \
  "v_mfma_f32_16x16x32_bf16 v[144:147], v[92:95], v[8:11], v[144:147]\n\t" \
  "v_mfma_f32_16x16x32_bf16 v[148:151], v[100:103], v[8:11], v[148:151]\n\t" \
  "v_mfma_f32_16x16x32_bf16 v[152:155], v[108:111], v[8:11], v[152:155]\n\t" \
  "v_mfma_f32_16x16x32_bf16 v[156:159], v[116:119], v[8:11], v[156:159]\n\t" \
  "ds_read_b64 v[8:9], v28 offset:8192\n\t" \
  "s_waitcnt lgkmcnt(10)\n\t" \
  "v_mfma_f32_16x16x32_bf16 v[132:135], v[64:67], v[12:15], v[132:135]\n\t" \
  "ds_read_b64 v[10:11], v29 offset:8192\n\t" \
  "v_mfma_f32_16x16x32_bf16 v[136:139], v[72:75], v[12:15], v[136:139]\n\t" \
  "v_mfma_f32_16x16x32_bf16 v[140:143], v[80:83], v[12:15], v[140:143]\n\t" \
  "v_mfma_f32_16x16x32_bf16 v[144:147], v[88:91], v[12:15], v[144:147]\n\t" \
  "v_mfma_f32_16x16x32_bf16 v[148:151], v[96:99], v[12:15], v[148:151]\n\t" \
  "s_waitcnt lgkmcnt(3)\n\t" \
  "v_mov_b32 v128, v34\n\t" \
  "v_writelane_b32 v128, 0, 0\n\t" \
  "v_mfma_f32_16x16x32_bf16 v[152:155], v[104:107], v[12:15], v[152:155]\n\t" \
  "v_mfma_f32_16x16x32_bf16 v[156:159], v[112:115], v[12:15], v[156:159]\n\t" \
  "ds_read_b64 v[12:13], v30 offset:9216\n\t" \
  "s_waitcnt lgkmcnt(10)\n\t" \
  "v_mfma_f32_16x16x32_bf16 v[136:139], v[68:71], v[16:19], v[136:139]\n\t" \
  "ds_read_b64 v[14:15], v31 offset:9216\n\t" \
  "ds_bpermute_b32 v35, v32, v135\n\t" \
  "v_readlane_b32 %[p1], v135, 63\n\t" \
  "v_mfma_f32_16x16x32_bf16 v[140:143], v[76:79], v[16:19], v[140:143]\n\t" \
  "s_cmp_eq_u32 %[rem], 0\n\t" \
  "s_cbranch_scc1 CB32_%=\n\t" \
  "s_barrier\n\t" \
  "s_sub_u32 %[rem], %[rem], 1\n\t" \
  "CB32_%=:\n\t" \
  "v_mov_b32 v135, v134\n\t" \
  "v_mfma_f32_16x16x32_bf16 v[144:147], v[84:87], v[16:19], v[144:147]\n\t" \
  "v_mov_b32 v134, v133\n\t" \
  "v_mfma_f32_16x16x32_bf16 v[148:151], v[92:95], v[16:19], v[148:151]\n\t" \
  "v_mov_b32 v133, v132\n\t" \
  "v_mfma_f32_16x16x32_bf16 v[152:155], v[100:103], v[16:19], v[152:155]\n\t" \
  "v_mfma_f32_16x16x32_bf16 v[156:159], v[108:111], v[16:19], v[156:159]\n\t" \
  "ds_read_b64 v[16:17], v24 offset:10240\n\t" \
  "s_waitcnt lgkmcnt(11)\n\t" \
  "v_mfma_f32_16x16x32_bf16 v[136:139], v[64:67], v[20:23], v[136:139]\n\t" \
  "ds_read_b64 v[18:19], v25 offset:10240\n\t" \
  "v_mfma_f32_16x16x32_bf16 v[140:143], v[72:75], v[20:23], v[140:143]\n\t" \
  "v_mfma_f32_16x16x32_bf16 v[144:147], v[80:83], v[20:23], v[144:147]\n\t" \
  "v_mfma_f32_16x16x32_bf16 v[148:151], v[88:91], v[20:23], v[148:151]\n\t" \
  "v_mfma_f32_16x16x32_bf16 v[152:155], v[96:99], v[20:23], v[152:155]\n\t" \
  "v_mfma_f32_16x16x32_bf16 v[156:159], v[104:107], v[20:23], v[156:159]\n\t" \
  "ds_read_b64 v[20:21], v26 offset:11264\n\t" \
  "s_waitcnt lgkmcnt(11)\n\t" \
  "v_mfma_f32_16x16x32_bf16 v[140:143], v[68:71], v[0:3], v[140:143]\n\t" \
  "ds_read_b64 v[22:23], v27 offset:11264\n\t" \
  "s_waitcnt lgkmcnt(4)\n\t" \
  "v_mov_b32 v132, v35\n\t" \
  "v_writelane_b32 v132, %[p0], 0\n\t" \
  "v_mfma_f32_16x16x32_bf16 v[144:147], v[76:79], v[0:3], v[144:147]\n\t" \
  "ds_bpermute_b32 v36, v32, v139\n\t" \
  "v_readlane_b32 %[p2], v139, 63\n\t" \
  "v_mfma_f32_16x16x32_bf16 v[148:151], v[84:87], v[0:3], v[148:151]\n\t" \
  "v_mov_b32 v139, v138\n\t" \
  "v_mfma_f32_16x16x32_bf16 v[152:155], v[92:95], v[0:3], v[152:155]\n\t" \
  "v_mov_b32 v138, v137\n\t" \
  "v_mfma_f32_16x16x32_bf16 v[156:159], v[100:103], v[0:3], v[156:159]\n\t" \
  "ds_read_b64 v[0:1], v28 offset:12288\n\t" \
  "v_mov_b32 v137, v136\n\t" \
  "s_waitcnt lgkmcnt(11)\n\t" \
  "v_mfma_f32_16x16x32_bf16 v[140:143], v[64:67], v[4:7], v[140:143]\n\t" \
  "s_cmp_eq_u32 %[rem], 0\n\t" \
  "s_cbranch_scc1 CB48_%=\n\t" \
  "s_barrier\n\t" \
  "s_sub_u32 %[rem], %[rem], 1\n\t" \
  "CB48_%=:\n\t" \
  "ds_read_b64 v[2:3], v29 offset:12288\n\t" \
  "v_mfma_f32_16x16x32_bf16 v[144:147], v[72:75], v[4:7], v[144:147]\n\t" \
  "v_mfma_f32_16x16x32_bf16 v[148:151], v[80:83], v[4:7], v[148:151]\n\t" \
  "v_mfma_f32_16x16x32_bf16 v[152:155], v[88:91], v[4:7], v[152:155]\n\t" \
  "v_mfma_f32_16x16x32_bf16 v[156:159], v[96:99], v[4:7], v[156:159]\n\t" \
  "ds_read_b64 v[4:5], v30 offset:13312\n\t" \
  "s_waitcnt lgkmcnt(11)\n\t" \
  "v_mfma_f32_16x16x32_bf16 v[144:147], v[68:71], v[8:11], v[144:147]\n\t" \
  "ds_read_b64 v[6:7], v31 offset:13312\n\t" \
  "v_mfma_f32_16x16x32_bf16 v[148:151], v[76:79], v[8:11], v[148:151]\n\t" \
  "v_mfma_f32_16x16x32_bf16 v[152:155], v[84:87], v[8:11], v[152:155]\n\t" \
  "ds_bpermute_b32 v37, v32, v143\n\t" \
  "v_readlane_b32 %[p3], v143, 63\n\t" \
  "v_mfma_f32_16x16x32_bf16 v[156:159], v[92:95], v[8:11], v[156:159]\n\t" \
  "ds_read_b64 v[8:9], v24 offset:14336\n\t" \
  "s_waitcnt lgkmcnt(6)\n\t" \
  "v_mov_b32 v136, v36\n\t" \
  "v_writelane_b32 v136, %[p1], 0\n\t" \
  "v_mov_b32 v143, v142\n\t" \
  "s_waitcnt lgkmcnt(12)\n\t" \
  "v_mfma_f32_16x16x32_bf16 v[144:147], v[64:67], v[12:15], v[144:147]\n\t" \
  "ds_read_b64 v[10:11], v25 offset:14336\n\t" \
  "v_mov_b32 v142, v141\n\t" \
  "v_mfma_f32_16x16x32_bf16 v[148:151], v[72:75], v[12:15], v[148:151]\n\t" \
  "v_mov_b32 v141, v140\n\t" \
  "v_mfma_f32_16x16x32_bf16 v[152:155], v[80:83], v[12:15], v[152:155]\n\t" \
  "v_mfma_f32_16x16x32_bf16 v[156:159], v[88:91], v[12:15], v[156:159]\n\t" \
  "ds_read_b64 v[12:13], v26 offset:15360\n\t" \
  "s_waitcnt lgkmcnt(11)\n\t" \
  "v_mfma_f32_16x16x32_bf16 v[148:151], v[68:71], v[16:19], v[148:151]\n\t" \
  "ds_read_b64 v[14:15], v27 offset:15360\n\t" \
  "v_mfma_f32_16x16x32_bf16 v[152:155], v[76:79], v[16:19], v[152:155]\n\t" \
  "v_mfma_f32_16x16x32_bf16 v[156:159], v[84:87], v[16:19], v[156:159]\n\t" \
  "ds_read_b64 v[16:17], v26 offset:0\n\t" \
  "s_waitcnt lgkmcnt(11)\n\t" \
  "v_mfma_f32_16x16x32_bf16 v[148:151], v[64:67], v[20:23], v[148:151]\n\t" \
  "s_cmp_eq_u32 %[rem], 0\n\t" \
  "s_cbranch_scc1 CB64_%=\n\t" \
  "s_barrier\n\t" \
  "s_sub_u32 %[rem], %[rem], 1\n\t" \
  "CB64_%=:\n\t" \
  "ds_read_b64 v[18:19], v27 offset:0\n\t" \
  "ds_bpermute_b32 v38, v32, v147\n\t" \
  "v_readlane_b32 %[p4], v147, 63\n\t" \
  "v_mfma_f32_16x16x32_bf16 v[152:155], v[72:75], v[20:23], v[152:155]\n\t" \
  "v_mov_b32 v147, v146\n\t" \
  "v_mfma_f32_16x16x32_bf16 v[156:159], v[80:83], v[20:23], v[156:159]\n\t" \
  "ds_read_b64 v[20:21], v28 offset:1024\n\t" \
  "v_mov_b32 v146, v145\n\t" \
  "s_waitcnt lgkmcnt(11)\n\t" \
  "v_mfma_f32_16x16x32_bf16 v[152:155], v[68:71], v[0:3], v[152:155]\n\t" \
  "ds_read_b64 v[22:23], v29 offset:1024\n\t" \
  "s_waitcnt lgkmcnt(9)\n\t" \
  "v_mov_b32 v140, v37\n\t" \
  "v_writelane_b32 v140, %[p2], 0\n\t" \
  "v_mov_b32 v145, v144\n\t" \
  "v_mfma_f32_16x16x32_bf16 v[156:159], v[76:79], v[0:3], v[156:159]\n\t" \
  "ds_read_b64 v[0:1], v30 offset:2048\n\t" \
  "ds_bpermute_b32 v39, v32, v151\n\t" \
  "v_readlane_b32 %[p5], v151, 63\n\t" \
  "s_waitcnt lgkmcnt(12)\n\t" \
  "v_mfma_f32_16x16x32_bf16 v[152:155], v[64:67], v[4:7], v[152:155]\n\t" \
  "ds_read_b64 v[2:3], v31 offset:2048\n\t" \
  "v_mov_b32 v151, v150\n\t" \
  "v_mfma_f32_16x16x32_bf16 v[156:159], v[72:75], v[4:7], v[156:159]\n\t" \
  "ds_read_b64 v[4:5], v24 offset:3072\n\t" \
  "v_mov_b32 v150, v149\n\t" \
  "s_waitcnt lgkmcnt(11)\n\t" \
  "v_mfma_f32_16x16x32_bf16 v[156:159], v[68:71], v[8:11], v[156:159]\n\t" \
  "ds_read_b64 v[6:7], v25 offset:3072\n\t" \
  "ds_read_b64 v[8:9], v26 offset:4096\n\t" \
  "v_mov_b32 v149, v148\n\t" \
  "ds_bpermute_b32 v40, v32, v155\n\t" \
  "v_readlane_b32 %[p6], v155, 63\n\t" \
  "s_waitcnt lgkmcnt(12)\n\t" \
  "v_mfma_f32_16x16x32_bf16 v[156:159], v[64:67], v[12:15], v[156:159]\n\t" \
  "ds_read_b64 v[10:11], v27 offset:4096\n\t" \
  "ds_read_b64 v[12:13], v28 offset:5120\n\t" \
  "v_mov_b32 v155, v154\n\t" \
  "s_waitcnt lgkmcnt(12)\n\t" \
  "v_mfma_f32_16x16x32_bf16 v[128:131], v[68:71], v[16:19], v[128:131]\n\t" \
  "ds_read_b64 v[14:15], v29 offset:5120\n\t" \
  "v_mov_b32 v154, v153\n\t" \
  "v_mfma_f32_16x16x32_bf16 v[132:135], v[76:79], v[16:19], v[132:135]\n\t" \
  "s_waitcnt lgkmcnt(12)\n\t" \
  "v_mov_b32 v144, v38\n\t" \
  "v_writelane_b32 v144, %[p3], 0\n\t" \
  "v_mov_b32 v153, v152\n\t" \
  "v_mfma_f32_16x16x32_bf16 v[136:139], v[84:87], v[16:19], v[136:139]\n\t" \
  "s_waitcnt lgkmcnt(8)\n\t" \
  "v_mov_b32 v148, v39\n\t" \
  "v_writelane_b32 v148, %[p4], 0\n\t" \
  "v_mfma_f32_16x16x32_bf16 v[140:143], v[92:95], v[16:19], v[140:143]\n\t" \
  "s_waitcnt lgkmcnt(3)\n\t" \
  "v_mov_b32 v152, v40\n\t" \
  "v_writelane_b32 v152, %[p5], 0\n\t" \
  "v_mfma_f32_16x16x32_bf16 v[144:147], v[100:103], v[16:19], v[144:147]\n\t" \
  "ds_bpermute_b32 v41, v32, v159\n\t" \
  "v_mfma_f32_16x16x32_bf16 v[148:151], v[108:111], v[16:19], v[148:151]\n\t" \
  "v_mov_b32 v159, v158\n\t" \
  "v_mfma_f32_16x16x32_bf16 v[152:155], v[116:119], v[16:19], v[152:155]\n\t" \
  "v_mov_b32 v158, v157\n\t" \
  "s_waitcnt lgkmcnt(11)\n\t" \
  "v_mfma_f32_16x16x32_bf16 v[128:131], v[64:67], v[20:23], v[128:131]\n\t" \
  "s_cmp_eq_u32 %[rem], 0\n\t" \
  "s_cbranch_scc1 CB80_%=\n\t" \
  "s_barrier\n\t" \
  "s_sub_u32 %[rem], %[rem], 1\n\t" \
  "CB80_%=:\n\t" \
  "v_mov_b32 v157, v156\n\t" \
  "v_mfma_f32_16x16x32_bf16 v[132:135], v[72:75], v[20:23], v[132:135]\n\t" \
  "v_mfma_f32_16x16x32_bf16 v[136:139], v[80:83], v[20:23], v[136:139]\n\t" \
  "v_mfma_f32_16x16x32_bf16 v[140:143], v[88:91], v[20:23], v[140:143]\n\t" \
  "v_mfma_f32_16x16x32_bf16 v[144:147], v[96:99], v[20:23], v[144:147]\n\t" \
  "s_waitcnt lgkmcnt(0)\n\t" \
  "v_mov_b32 v156, v41\n\t" \
  "v_writelane_b32 v156, %[p6], 0\n\t" \
  "v_mfma_f32_16x16x32_bf16 v[148:151], v[104:107], v[20:23], v[148:151]\n\t" \
  "v_mfma_f32_16x16x32_bf16 v[152:155], v[112:115], v[20:23], v[152:155]\n\t" \
  "v_mfma_f32_16x16x32_bf16 v[156:159], v[124:127], v[16:19], v[156:159]\n\t" \
  "ds_read_b64 v[16:17], v30 offset:6144\n\t" \
  "ds_bpermute_b32 v34, v32, v131\n\t" \
  "v_readlane_b32 %[p0], v131, 63\n\t" \
  "v_mfma_f32_16x16x32_bf16 v[156:159], v[120:123], v[20:23], v[156:159]\n\t" \
  "ds_read_b64 v[18:19], v31 offset:6144\n\t" \
  "ds_read_b64 v[20:21], v24 offset:7168\n\t" \
  "v_mov_b32 v131, v130\n\t" \
  "s_waitcnt lgkmcnt(12)\n\t" \
  "v_mfma_f32_16x16x32_bf16 v[132:135], v[68:71], v[0:3], v[132:135]\n\t" \
  "ds_read_b64 v[22:23], v25 offset:7168\n\t" \
  "v_mov_b32 v130, v129\n\t" \
  "v_mfma_f32_16x16x32_bf16 v[136:139], v[76:79], v[0:3], v[136:139]\n\t" \
  "v_mov_b32 v129, v128\n\t" \
  "v_mfma_f32_16x16x32_bf16 v[140:143], v[84:87], v[0:3], v[140:143]\n\t" \
  "v_mfma_f32_16x16x32_bf16 v[144:147], v[92:95], v[0:3], v[144:147]\n\t" \
  "v_mfma_f32_16x16x32_bf16 v[148:151], v[100:103], v[0:3], v[148:151]\n\t" \
  "v_mfma_f32_16x16x32_bf16 v[152:155], v[108:111], v[0:3], v[152:155]\n\t" \
  "v_mfma_f32_16x16x32_bf16 v[156:159], v[116:119], v[0:3], v[156:159]\n\t" \
  "ds_read_b64 v[0:1], v26 offset:8192\n\t" \
  "s_waitcnt lgkmcnt(12)\n\t" \
  "v_mfma_f32_16x16x32_bf16 v[132:135], v[64:67], v[4:7], v[132:135]\n\t" \
  "s_cmp_eq_u32 %[rem], 0\n\t" \
  "s_cbranch_scc1 CB96_%=\n\t" \
  "s_barrier\n\t" \
  "s_sub_u32 %[rem], %[rem], 1\n\t" \
  "CB96_%=:\n\t" \
  "ds_read_b64 v[2:3], v27 offset:8192\n\t" \
  "v_mfma_f32_16x16x32_bf16 v[136:139], v[72:75], v[4:7], v[136:139]\n\t" \
  "v_mfma_f32_16x16x32_bf16 v[140:143], v[80:83], v[4:7], v[140:143]\n\t" \
  "v_mfma_f32_16x16x32_bf16 v[144:147], v[88:91], v[4:7], v[144:147]\n\t" \
  "s_waitcnt lgkmcnt(5)\n\t" \
  "v_mov_b32 v128, v34\n\t" \
  "v_writelane_b32 v128, 0, 0\n\t" \
  "v_mfma_f32_16x16x32_bf16 v[148:151], v[96:99], v[4:7], v[148:151]\n\t" \
  "v_mfma_f32_16x16x32_bf16 v[152:155], v[104:107], v[4:7], v[152:155]\n\t" \
  "v_mfma_f32_16x16x32_bf16 v[156:159], v[112:115], v[4:7], v[156:159]\n\t" \
  "ds_read_b64 v[4:5], v28 offset:9216\n\t" \
  "s_waitcnt lgkmcnt(11)\n\t" \
  "v_mfma_f32_16x16x32_bf16 v[136:139], v[68:71], v[8:11], v[136:139]\n\t" \
  "ds_read_b64 v[6:7], v29 offset:9216\n\t" \
  "ds_bpermute_b32 v35, v32, v135\n\t" \
  "v_readlane_b32 %[p1], v135, 63\n\t" \
  "v_mfma_f32_16x16x32_bf16 v[140:143], v[76:79], v[8:11], v[140:143]\n\t" \
  "v_mov_b32 v135, v134\n\t" \
  "v_mfma_f32_16x16x32_bf16 v[144:147], v[84:87], v[8:11], v[144:147]\n\t" \
  "v_mov_b32 v134, v133\n\t" \
  "v_mfma_f32_16x16x32_bf16 v[148:151], v[92:95], v[8:11], v[148:151]\n\t" \
  "v_mov_b32 v133, v132\n\t" \
  "v_mfma_f32_16x16x32_bf16 v[152:155], v[100:103], v[8:11], v[152:155]\n\t" \
  "v_mfma_f32_16x16x32_bf16 v[156:159], v[108:111], v[8:11], v[156:159]\n\t" \
  "ds_read_b64 v[8:9], v30 offset:10240\n\t" \
  "s_waitcnt lgkmcnt(12)\n\t" \
  "v_mfma_f32_16x16x32_bf16 v[136:139], v[64:67], v[12:15], v[136:139]\n\t" \
  "ds_read_b64 v[10:11], v31 offset:10240\n\t" \
  "v_mfma_f32_16x16x32_bf16 v[140:143], v[72:75], v[12:15], v[140:143]\n\t" \
  "v_mfma_f32_16x16x32_bf16 v[144:147], v[80:83], v[12:15], v[144:147]\n\t" \
  "v_mfma_f32_16x16x32_bf16 v[148:151], v[88:91], v[12:15], v[148:151]\n\t" \
  "s_cmp_eq_u32 %[rem], 0\n\t" \
  "s_cbranch_scc1 CB112_%=\n\t" \
  "s_barrier\n\t" \
  "s_sub_u32 %[rem], %[rem], 1\n\t" \
  "CB112_%=:\n\t" \
  "v_mfma_f32_16x16x32_bf16 v[152:155], v[96:99], v[12:15], v[152:155]\n\t" \
  "v_mfma_f32_16x16x32_bf16 v[156:159], v[104:107], v[12:15], v[156:159]\n\t" \
  "ds_read_b64 v[12:13], v24 offset:11264\n\t" \
  "s_waitcnt lgkmcnt(10)\n\t" \
  "v_mfma_f32_16x16x32_bf16 v[140:143], v[68:71], v[16:19], v[140:143]\n\t" \
  "ds_read_b64 v[14:15], v25 offset:11264\n\t" \
  "s_waitcnt lgkmcnt(4)\n\t" \
  "v_mov_b32 v132, v35\n\t" \
  "v_writelane_b32 v132, %[p0], 0\n\t" \
  "v_mfma_f32_16x16x32_bf16 v[144:147], v[76:79], v[16:19], v[144:147]\n\t" \
  "ds_bpermute_b32 v36, v32, v139\n\t" \
  "v_readlane_b32 %[p2], v139, 63\n\t" \
  "v_mfma_f32_16x16x32_bf16 v[148:151], v[84:87], v[16:19], v[148:151]\n\t" \
  "v_mov_b32 v139, v138\n\t" \
  "v_mfma_f32_16x16x32_bf16 v[152:155], v[92:95], v[16:19], v[152:155]\n\t" \
  "v_mov_b32 v138, v137\n\t" \
  "v_mfma_f32_16x16x32_bf16 v[156:159], v[100:103], v[16:19], v[156:159]\n\t" \
  "ds_read_b64 v[16:17], v26 offset:12288\n\t" \
  "v_mov_b32 v137, v136\n\t" \
  "s_waitcnt lgkmcnt(11)\n\t" \
  "v_mfma_f32_16x16x32_bf16 v[140:143], v[64:67], v[20:23], v[140:143]\n\t" \
  "ds_read_b64 v[18:19], v27 offset:12288\n\t" \
  "v_mfma_f32_16x16x32_bf16 v[144:147], v[72:75], v[20:23], v[144:147]\n\t" \
  "v_mfma_f32_16x16x32_bf16 v[148:151], v[80:83], v[20:23], v[148:151]\n\t" \
  "v_mfma_f32_16x16x32_bf16 v[152:155], v[88:91], v[20:23], v[152:155]\n\t" \
  "v_mfma_f32_16x16x32_bf16 v[156:159], v[96:99], v[20:23], v[156:159]\n\t" \
  "ds_read_b64 v[20:21], v28 offset:13312\n\t" \
  "s_waitcnt lgkmcnt(11)\n\t" \
  "v_mfma_f32_16x16x32_bf16 v[144:147], v[68:71], v[0:3], v[144:147]\n\t" \
  "ds_read_b64 v[22:23], v29 offset:13312\n\t" \
  "v_mfma_f32_16x16x32_bf16 v[148:151], v[76:79], v[0:3], v[148:151]\n\t" \
  "v_mfma_f32_16x16x32_bf16 v[152:155], v[84:87], v[0:3], v[152:155]\n\t" \
  "ds_bpermute_b32 v37, v32, v143\n\t" \
  "v_readlane_b32 %[p3], v143, 63\n\t" \
  "v_mfma_f32_16x16x32_bf16 v[156:159], v[92:95], v[0:3], v[156:159]\n\t" \
  "s_cmp_eq_u32 %[rem], 0\n\t" \
  "s_cbranch_scc1 CB128_%=\n\t" \
  "s_barrier\n\t" \
  "s_sub_u32 %[rem], %[rem], 1\n\t" \
  "CB128_%=:\n\t" \
  "ds_read_b64 v[0:1], v30 offset:14336\n\t" \
  "s_waitcnt lgkmcnt(6)\n\t" \
  "v_mov_b32 v136, v36\n\t" \
  "v_writelane_b32 v136, %[p1], 0\n\t" \
  "v_mov_b32 v143, v142\n\t" \
  "s_waitcnt lgkmcnt(12)\n\t" \
  "v_mfma_f32_16x16x32_bf16 v[144:147], v[64:67], v[4:7], v[144:147]\n\t" \
  "ds_read_b64 v[2:3], v31 offset:14336\n\t" \
  "v_mov_b32 v142, v141\n\t" \
  "v_mfma_f32_16x16x32_bf16 v[148:151], v[72:75], v[4:7], v[148:151]\n\t" \
  "v_mov_b32 v141, v140\n\t" \
  "v_mfma_f32_16x16x32_bf16 v[152:155], v[80:83], v[4:7], v[152:155]\n\t" \
  "v_mfma_f32_16x16x32_bf16 v[156:159], v[88:91], v[4:7], v[156:159]\n\t" \
  "ds_read_b64 v[4:5], v24 offset:15360\n\t" \
  "s_waitcnt lgkmcnt(11)\n\t" \
  "v_mfma_f32_16x16x32_bf16 v[148:151], v[68:71], v[8:11], v[148:151]\n\t" \
  "ds_read_b64 v[6:7], v25 offset:15360\n\t" \
  "v_mfma_f32_16x16x32_bf16 v[152:155], v[76:79], v[8:11], v[152:155]\n\t" \
  "v_mfma_f32_16x16x32_bf16 v[156:159], v[84:87], v[8:11], v[156:159]\n\t" \
  "ds_read_b64 v[8:9], v24 offset:0\n\t" \
  "s_waitcnt lgkmcnt(11)\n\t" \
  "v_mfma_f32_16x16x32_bf16 v[148:151], v[64:67], v[12:15], v[148:151]\n\t" \
  "ds_read_b64 v[10:11], v25 offset:0\n\t" \
  "ds_bpermute_b32 v38, v32, v147\n\t" \
  "v_readlane_b32 %[p4], v147, 63\n\t" \
  "v_mfma_f32_16x16x32_bf16 v[152:155], v[72:75], v[12:15], v[152:155]\n\t" \
  "v_mov_b32 v147, v146\n\t" \
  "v_mfma_f32_16x16x32_bf16 v[156:159], v[80:83], v[12:15], v[156:159]\n\t" \
  "ds_read_b64 v[12:13], v26 offset:1024\n\t" \
  "v_mov_b32 v146, v145\n\t" \
  "s_waitcnt lgkmcnt(11)\n\t" \
  "v_mfma_f32_16x16x32_bf16 v[152:155], v[68:71], v[16:19], v[152:155]\n\t" \
  "ds_read_b64 v[14:15], v27 offset:1024\n\t" \
  "s_waitcnt lgkmcnt(9)\n\t" \
  "v_mov_b32 v140, v37\n\t" \
  "v_writelane_b32 v140, %[p2], 0\n\t" \
  "v_mov_b32 v145, v144\n\t" \
  "v_mfma_f32_16x16x32_bf16 v[156:159], v[76:79], v[16:19], v[156:159]\n\t" \
  "ds_read_b64 v[16:17], v28 offset:2048\n\t" \
  "ds_bpermute_b32 v39, v32, v151\n\t" \
  "v_readlane_b32 %[p5], v151, 63\n\t" \
  "s_waitcnt lgkmcnt(12)\n\t" \
  "v_mfma_f32_16x16x32_bf16 v[152:155], v[64:67], v[20:23], v[152:155]\n\t" \
  "ds_read_b64 v[18:19], v29 offset:2048\n\t" \
  "v_mov_b32 v151, v150\n\t" \
  "v_mfma_f32_16x16x32_bf16 v[156:159], v[72:75], v[20:23], v[156:159]\n\t" \
  "ds_read_b64 v[20:21], v30 offset:3072\n\t" \
  "v_mov_b32 v150, v149\n\t" \
  "s_waitcnt lgkmcnt(11)\n\t" \
  "v_mfma_f32_16x16x32_bf16 v[156:159], v[68:71], v[0:3], v[156:159]\n\t" \
  "ds_read_b64 v[22:23], v31 offset:3072\n\t" \
  "ds_read_b64 v[0:1], v24 offset:4096\n\t" \
  "v_mov_b32 v149, v148\n\t" \
  "ds_bpermute_b32 v40, v32, v155\n\t" \
  "v_readlane_b32 %[p6], v155, 63\n\t" \
  "s_waitcnt lgkmcnt(12)\n\t" \
  "v_mfma_f32_16x16x32_bf16 v[156:159], v[64:67], v[4:7], v[156:159]\n\t" \
  "s_cmp_eq_u32 %[rem], 0\n\t" \
  "s_cbranch_scc1 CB144_%=\n\t" \
  "s_barrier\n\t" \
  "s_sub_u32 %[rem], %[rem], 1\n\t" \
  "CB144_%=:\n\t" \
  "ds_read_b64 v[2:3], v25 offset:4096\n\t" \
  "ds_read_b64 v[4:5], v26 offset:5120\n\t" \
  "v_mov_b32 v155, v154\n\t" \
  "s_waitcnt lgkmcnt(12)\n\t" \
  "v_mfma_f32_16x16x32_bf16 v[128:131], v[68:71], v[8:11], v[128:131]\n\t" \
  "ds_read_b64 v[6:7], v27 offset:5120\n\t" \
  "v_mov_b32 v154, v153\n\t" \
  "v_mfma_f32_16x16x32_bf16 v[132:135], v[76:79], v[8:11], v[132:135]\n\t" \
  "s_waitcnt lgkmcnt(12)\n\t" \
  "v_mov_b32 v144, v38\n\t" \
  "v_writelane_b32 v144, %[p3], 0\n\t" \
  "v_mov_b32 v153, v152\n\t" \
  "v_mfma_f32_16x16x32_bf16 v[136:139], v[84:87], v[8:11], v[136:139]\n\t" \
  "s_waitcnt lgkmcnt(8)\n\t" \
  "v_mov_b32 v148, v39\n\t" \
  "v_writelane_b32 v148, %[p4], 0\n\t" \
  "v_mfma_f32_16x16x32_bf16 v[140:143], v[92:95], v[8:11], v[140:143]\n\t" \
  "s_waitcnt lgkmcnt(3)\n\t" \
  "v_mov_b32 v152, v40\n\t" \
  "v_writelane_b32 v152, %[p5], 0\n\t" \
  "v_mfma_f32_16x16x32_bf16 v[144:147], v[100:103], v[8:11], v[144:147]\n\t" \
  "ds_bpermute_b32 v41, v32, v159\n\t" \
  "v_mfma_f32_16x16x32_bf16 v[148:151], v[108:111], v[8:11], v[148:151]\n\t" \
  "v_mov_b32 v159, v158\n\t" \
  "v_mfma_f32_16x16x32_bf16 v[152:155], v[116:119], v[8:11], v[152:155]\n\t" \
  "v_mov_b32 v158, v157\n\t" \
  "s_waitcnt lgkmcnt(11)\n\t" \
  "v_mfma_f32_16x16x32_bf16 v[128:131], v[64:67], v[12:15], v[128:131]\n\t" \
  "v_mov_b32 v157, v156\n\t" \
  "v_mfma_f32_16x16x32_bf16 v[132:135], v[72:75], v[12:15], v[132:135]\n\t" \
  "v_mfma_f32_16x16x32_bf16 v[136:139], v[80:83], v[12:15], v[136:139]\n\t" \
  "v_mfma_f32_16x16x32_bf16 v[140:143], v[88:91], v[12:15], v[140:143]\n\t" \
  "v_mfma_f32_16x16x32_bf16 v[144:147], v[96:99], v[12:15], v[144:147]\n\t" \
  "s_waitcnt lgkmcnt(0)\n\t" \
  "v_mov_b32 v156, v41\n\t" \
  "v_writelane_b32 v156, %[p6], 0\n\t" \
  "v_mfma_f32_16x16x32_bf16 v[148:151], v[104:107], v[12:15], v[148:151]\n\t" \
  "v_mfma_f32_16x16x32_bf16 v[152:155], v[112:115], v[12:15], v[152:155]\n\t" \
  "v_mfma_f32_16x16x32_bf16 v[156:159], v[124:127], v[8:11], v[156:159]\n\t" \
  "ds_read_b64 v[8:9], v28 offset:6144\n\t" \
  "ds_bpermute_b32 v34, v32, v131\n\t" \
  "v_readlane_b32 %[p0], v131, 63\n\t" \
  "v_mfma_f32_16x16x32_bf16 v[156:159], v[120:123], v[12:15], v[156:159]\n\t" \
  "s_cmp_eq_u32 %[rem], 0\n\t" \
  "s_cbranch_scc1 CB160_%=\n\t" \
  "s_barrier\n\t" \
  "s_sub_u32 %[rem], %[rem], 1\n\t" \
  "CB160_%=:\n\t" \
  "ds_read_b64 v[10:11], v29 offset:6144\n\t" \
  "ds_read_b64 v[12:13], v30 offset:7168\n\t" \
  "v_mov_b32 v131, v130\n\t" \
  "s_waitcnt lgkmcnt(12)\n\t" \
  "v_mfma_f32_16x16x32_bf16 v[132:135], v[68:71], v[16:19], v[132:135]\n\t" \
  "ds_read_b64 v[14:15], v31 offset:7168\n\t" \
  "v_mov_b32 v130, v129\n\t" \
  "v_mfma_f32_16x16x32_bf16 v[136:139], v[76:79], v[16:19], v[136:139]\n\t" \
  "v_mov_b32 v129, v128\n\t" \
  "v_mfma_f32_16x16x32_bf16 v[140:143], v[84:87], v[16:19], v[140:143]\n\t" \
  "v_mfma_f32_16x16x32_bf16 v[144:147], v[92:95], v[16:19], v[144:147]\n\t" \
  "v_mfma_f32_16x16x32_bf16 v[148:151], v[100:103], v[16:19], v[148:151]\n\t" \
  "v_mfma_f32_16x16x32_bf16 v[152:155], v[108:111], v[16:19], v[152:155]\n\t" \
  "v_mfma_f32_16x16x32_bf16 v[156:159], v[116:119], v[16:19], v[156:159]\n\t" \
  "ds_read_b64 v[16:17], v24 offset:8192\n\t" \
  "s_waitcnt lgkmcnt(12)\n\t" \
  "v_mfma_f32_16x16x32_bf16 v[132:135], v[64:67], v[20:23], v[132:135]\n\t" \
  "ds_read_b64 v[18:19], v25 offset:8192\n\t" \
  "v_mfma_f32_16x16x32_bf16 v[136:139], v[72:75], v[20:23], v[136:139]\n\t" \
  "v_mfma_f32_16x16x32_bf16 v[140:143], v[80:83], v[20:23], v[140:143]\n\t" \
  "v_mfma_f32_16x16x32_bf16 v[144:147], v[88:91], v[20:23], v[144:147]\n\t" \
  "s_waitcnt lgkmcnt(5)\n\t" \
  "v_mov_b32 v128, v34\n\t" \
  "v_writelane_b32 v128, 0, 0\n\t" \
  "v_mfma_f32_16x16x32_bf16 v[148:151], v[96:99], v[20:23], v[148:151]\n\t" \
  "v_mfma_f32_16x16x32_bf16 v[152:155], v[104:107], v[20:23], v[152:155]\n\t" \
  "v_mfma_f32_16x16x32_bf16 v[156:159], v[112:115], v[20:23], v[156:159]\n\t" \
  "ds_read_b64 v[20:21], v26 offset:9216\n\t" \
  "s_waitcnt lgkmcnt(11)\n\t" \
  "v_mfma_f32_16x16x32_bf16 v[136:139], v[68:71], v[0:3], v[136:139]\n\t" \
  "ds_read_b64 v[22:23], v27 offset:9216\n\t" \
  "ds_bpermute_b32 v35, v32, v135\n\t" \
  "v_readlane_b32 %[p1], v135, 63\n\t" \
  "v_mfma_f32_16x16x32_bf16 v[140:143], v[76:79], v[0:3], v[140:143]\n\t" \
  "s_cmp_eq_u32 %[rem], 0\n\t" \
  "s_cbranch_scc1 CB176_%=\n\t" \
  "s_barrier\n\t" \
  "s_sub_u32 %[rem], %[rem], 1\n\t" \
  "CB176_%=:\n\t" \
  "v_mov_b32 v135, v134\n\t" \
  "v_mfma_f32_16x16x32_bf16 v[144:147], v[84:87], v[0:3], v[144:147]\n\t" \
  "v_mov_b32 v134, v133\n\t" \
  "v_mfma_f32_16x16x32_bf16 v[148:151], v[92:95], v[0:3], v[148:151]\n\t" \
  "v_mov_b32 v133, v132\n\t" \
  "v_mfma_f32_16x16x32_bf16 v[152:155], v[100:103], v[0:3], v[152:155]\n\t" \
  "v_mfma_f32_16x16x32_bf16 v[156:159], v[108:111], v[0:3], v[156:159]\n\t" \
  "ds_read_b64 v[0:1], v28 offset:10240\n\t" \
  "s_waitcnt lgkmcnt(12)\n\t" \
  "v_mfma_f32_16x16x32_bf16 v[136:139], v[64:67], v[4:7], v[136:139]\n\t" \
  "ds_read_b64 v[2:3], v29 offset:10240\n\t" \
  "v_mfma_f32_16x16x32_bf16 v[140:143], v[72:75], v[4:7], v[140:143]\n\t" \
  "v_mfma_f32_16x16x32_bf16 v[144:147], v[80:83], v[4:7], v[144:147]\n\t" \
  "v_mfma_f32_16x16x32_bf16 v[148:151], v[88:91], v[4:7], v[148:151]\n\t" \
  "v_mfma_f32_16x16x32_bf16 v[152:155], v[96:99], v[4:7], v[152:155]\n\t" \
  "v_mfma_f32_16x16x32_bf16 v[156:159], v[104:107], v[4:7], v[156:159]\n\t" \
  "ds_read_b64 v[4:5], v30 offset:11264\n\t" \
  "s_waitcnt lgkmcnt(10)\n\t" \
  "v_mfma_f32_16x16x32_bf16 v[140:143], v[68:71], v[8:11], v[140:143]\n\t" \
  "ds_read_b64 v[6:7], v31 offset:11264\n\t" \
  "s_waitcnt lgkmcnt(4)\n\t" \
  "v_mov_b32 v132, v35\n\t" \
  "v_writelane_b32 v132, %[p0], 0\n\t" \
  "v_mfma_f32_16x16x32_bf16 v[144:147], v[76:79], v[8:11], v[144:147]\n\t" \
  "ds_bpermute_b32 v36, v32, v139\n\t" \
  "v_readlane_b32 %[p2], v139, 63\n\t" \
  "v_mfma_f32_16x16x32_bf16 v[148:151], v[84:87], v[8:11], v[148:151]\n\t" \
  "v_mov_b32 v139, v138\n\t" \
  "v_mfma_f32_16x16x32_bf16 v[152:155], v[92:95], v[8:11], v[152:155]\n\t" \
  "v_mov_b32 v138, v137\n\t" \
  "v_mfma_f32_16x16x32_bf16 v[156:159], v[100:103], v[8:11], v[156:159]\n\t" \
  "ds_read_b64 v[8:9], v24 offset:12288\n\t" \
  "v_mov_b32 v137, v136\n\t" \
  "s_waitcnt lgkmcnt(11)\n\t" \
  "v_mfma_f32_16x16x32_bf16 v[140:143], v[64:67], v[12:15], v[140:143]\n\t" \
  "s_cmp_eq_u32 %[rem], 0\n\t" \
  "s_cbranch_scc1 CB192_%=\n\t" \
  "s_barrier\n\t" \
  "s_sub_u32 %[rem], %[rem], 1\n\t" \
  "CB192_%=:\n\t" \
  "ds_read_b64 v[10:11], v25 offset:12288\n\t" \
  "v_mfma_f32_16x16x32_bf16 v[144:147], v[72:75], v[12:15], v[144:147]\n\t" \
  "v_mfma_f32_16x16x32_bf16 v[148:151], v[80:83], v[12:15], v[148:151]\n\t" \
  "v_mfma_f32_16x16x32_bf16 v[152:155], v[88:91], v[12:15], v[152:155]\n\t" \
  "v_mfma_f32_16x16x32_bf16 v[156:159], v[96:99], v[12:15], v[156:159]\n\t" \
  "ds_read_b64 v[12:13], v26 offset:13312\n\t" \
  "s_waitcnt lgkmcnt(11)\n\t" \
  "v_mfma_f32_16x16x32_bf16 v[144:147], v[68:71], v[16:19], v[144:147]\n\t" \
  "ds_read_b64 v[14:15], v27 offset:13312\n\t" \
  "v_mfma_f32_16x16x32_bf16 v[148:151], v[76:79], v[16:19], v[148:151]\n\t" \
  "v_mfma_f32_16x16x32_bf16 v[152:155], v[84:87], v[16:19], v[152:155]\n\t" \
  "ds_bpermute_b32 v37, v32, v143\n\t" \
  "v_readlane_b32 %[p3], v143, 63\n\t" \
  "v_mfma_f32_16x16x32_bf16 v[156:159], v[92:95], v[16:19], v[156:159]\n\t" \
  "ds_read_b64 v[16:17], v28 offset:14336\n\t" \
  "s_waitcnt lgkmcnt(6)\n\t" \
  "v_mov_b32 v136, v36\n\t" \
  "v_writelane_b32 v136, %[p1], 0\n\t" \
  "v_mov_b32 v143, v142\n\t" \
  "s_waitcnt lgkmcnt(12)\n\t" \
  "v_mfma_f32_16x16x32_bf16 v[144:147], v[64:67], v[20:23], v[144:147]\n\t" \
  "ds_read_b64 v[18:19], v29 offset:14336\n\t" \
  "v_mov_b32 v142, v141\n\t" \
  "v_mfma_f32_16x16x32_bf16 v[148:151], v[72:75], v[20:23], v[148:151]\n\t" \
  "v_mov_b32 v141, v140\n\t" \
  "v_mfma_f32_16x16x32_bf16 v[152:155], v[80:83], v[20:23], v[152:155]\n\t" \
  "v_mfma_f32_16x16x32_bf16 v[156:159], v[88:91], v[20:23], v[156:159]\n\t" \
  "ds_read_b64 v[20:21], v30 offset:15360\n\t" \
  "s_waitcnt lgkmcnt(11)\n\t" \
  "v_mfma_f32_16x16x32_bf16 v[148:151], v[68:71], v[0:3], v[148:151]\n\t" \
  "ds_read_b64 v[22:23], v31 offset:15360\n\t" \
  "v_mfma_f32_16x16x32_bf16 v[152:155], v[76:79], v[0:3], v[152:155]\n\t" \
  "v_mfma_f32_16x16x32_bf16 v[156:159], v[84:87], v[0:3], v[156:159]\n\t" \
  "ds_read_b64 v[0:1], v30 offset:0\n\t" \
  "s_waitcnt lgkmcnt(11)\n\t" \
  "v_mfma_f32_16x16x32_bf16 v[148:151], v[64:67], v[4:7], v[148:151]\n\t" \
  "s_cmp_eq_u32 %[rem], 0\n\t" \
  "s_cbranch_scc1 CB208_%=\n\t" \
  "s_barrier\n\t" \
  "s_sub_u32 %[rem], %[rem], 1\n\t" \
  "CB208_%=:\n\t" \
  "ds_read_b64 v[2:3], v31 offset:0\n\t" \
  "ds_bpermute_b32 v38, v32, v147\n\t" \
  "v_readlane_b32 %[p4], v147, 63\n\t" \
  "v_mfma_f32_16x16x32_bf16 v[152:155], v[72:75], v[4:7], v[152:155]\n\t" \
  "v_mov_b32 v147, v146\n\t" \
  "v_mfma_f32_16x16x32_bf16 v[156:159], v[80:83], v[4:7], v[156:159]\n\t" \
  "ds_read_b64 v[4:5], v24 offset:1024\n\t" \
  "v_mov_b32 v146, v145\n\t" \
  "s_waitcnt lgkmcnt(11)\n\t" \
  "v_mfma_f32_16x16x32_bf16 v[152:155], v[68:71], v[8:11], v[152:155]\n\t" \
  "ds_read_b64 v[6:7], v25 offset:1024\n\t" \
  "s_waitcnt lgkmcnt(9)\n\t" \
  "v_mov_b32 v140, v37\n\t" \
  "v_writelane_b32 v140, %[p2], 0\n\t" \
  "v_mov_b32 v145, v144\n\t" \
  "v_mfma_f32_16x16x32_bf16 v[156:159], v[76:79], v[8:11], v[156:159]\n\t" \
  "ds_read_b64 v[8:9], v26 offset:2048\n\t" \
  "ds_bpermute_b32 v39, v32, v151\n\t" \
  "v_readlane_b32 %[p5], v151, 63\n\t" \
  "s_waitcnt lgkmcnt(12)\n\t" \
  "v_mfma_f32_16x16x32_bf16 v[152:155], v[64:67], v[12:15], v[152:155]\n\t" \
  "ds_read_b64 v[10:11], v27 offset:2048\n\t" \
  "v_mov_b32 v151, v150\n\t" \
  "v_mfma_f32_16x16x32_bf16 v[156:159], v[72:75], v[12:15], v[156:159]\n\t" \
  "ds_read_b64 v[12:13], v28 offset:3072\n\t" \
  "v_mov_b32 v150, v149\n\t" \
  "s_waitcnt lgkmcnt(11)\n\t" \
  "v_mfma_f32_16x16x32_bf16 v[156:159], v[68:71], v[16:19], v[156:159]\n\t" \
  "ds_read_b64 v[14:15], v29 offset:3072\n\t" \
  "ds_read_b64 v[16:17], v30 offset:4096\n\t" \
  "v_mov_b32 v149, v148\n\t" \
  "ds_bpermute_b32 v40, v32, v155\n\t" \
  "v_readlane_b32 %[p6], v155, 63\n\t" \
  "s_waitcnt lgkmcnt(12)\n\t" \
  "v_mfma_f32_16x16x32_bf16 v[156:159], v[64:67], v[20:23], v[156:159]\n\t" \
  "ds_read_b64 v[18:19], v31 offset:4096\n\t" \
  "ds_read_b64 v[20:21], v24 offset:5120\n\t" \
  "v_mov_b32 v155, v154\n\t" \
  "s_waitcnt lgkmcnt(12)\n\t" \
  "v_mfma_f32_16x16x32_bf16 v[128:131], v[68:71], v[0:3], v[128:131]\n\t" \
  "ds_read_b64 v[22:23], v25 offset:5120\n\t" \
  "v_mov_b32 v154, v153\n\t" \
  "v_mfma_f32_16x16x32_bf16 v[132:135], v[76:79], v[0:3], v[132:135]\n\t" \
  "s_waitcnt lgkmcnt(12)\n\t" \
  "v_mov_b32 v144, v38\n\t" \
  "v_writelane_b32 v144, %[p3], 0\n\t" \
  "v_mov_b32 v153, v152\n\t" \
  "v_mfma_f32_16x16x32_bf16 v[136:139], v[84:87], v[0:3], v[136:139]\n\t" \
  "s_waitcnt lgkmcnt(8)\n\t" \
  "v_mov_b32 v148, v39\n\t" \
  "v_writelane_b32 v148, %[p4], 0\n\t" \
  "v_mfma_f32_16x16x32_bf16 v[140:143], v[92:95], v[0:3], v[140:143]\n\t" \
  "s_waitcnt lgkmcnt(3)\n\t" \
  "v_mov_b32 v152, v40\n\t" \
  "v_writelane_b32 v152, %[p5], 0\n\t" \
  "v_mfma_f32_16x16x32_bf16 v[144:147], v[100:103], v[0:3], v[144:147]\n\t" \
  "ds_bpermute_b32 v41, v32, v159\n\t" \
  "v_mfma_f32_16x16x32_bf16 v[148:151], v[108:111], v[0:3], v[148:151]\n\t" \
  "v_mov_b32 v159, v158\n\t" \
  "v_mfma_f32_16x16x32_bf16 v[152:155], v[116:119], v[0:3], v[152:155]\n\t" \
  "v_mov_b32 v158, v157\n\t" \
  "s_waitcnt lgkmcnt(11)\n\t" \
  "v_mfma_f32_16x16x32_bf16 v[128:131], v[64:67], v[4:7], v[128:131]\n\t" \
  "s_cmp_eq_u32 %[rem], 0\n\t" \
  "s_cbranch_scc1 CB224_%=\n\t" \
  "s_barrier\n\t" \
  "s_sub_u32 %[rem], %[rem], 1\n\t" \
  "CB224_%=:\n\t" \
  "v_mov_b32 v157, v156\n\t" \
  "v_mfma_f32_16x16x32_bf16 v[132:135], v[72:75], v[4:7], v[132:135]\n\t" \
  "v_mfma_f32_16x16x32_bf16 v[136:139], v[80:83], v[4:7], v[136:139]\n\t" \
  "v_mfma_f32_16x16x32_bf16 v[140:143], v[88:91], v[4:7], v[140:143]\n\t" \
  "v_mfma_f32_16x16x32_bf16 v[144:147], v[96:99], v[4:7], v[144:147]\n\t" \
  "s_waitcnt lgkmcnt(0)\n\t" \
  "v_mov_b32 v156, v41\n\t" \
  "v_writelane_b32 v156, %[p6], 0\n\t" \
  "v_mfma_f32_16x16x32_bf16 v[148:151], v[104:107], v[4:7], v[148:151]\n\t" \
  "v_mfma_f32_16x16x32_bf16 v[152:155], v[112:115], v[4:7], v[152:155]\n\t" \
  "s_mov_b64 exec, 1\n\t" \
  "ds_write_b32 v33, v33\n\t" \
  "s_mov_b64 exec, -1\n\t" \
  "v_mfma_f32_16x16x32_bf16 v[156:159], v[124:127], v[0:3], v[156:159]\n\t" \
  "ds_read_b64 v[0:1], v26 offset:6144\n\t" \
  "v_mfma_f32_16x16x32_bf16 v[156:159], v[120:123], v[4:7], v[156:159]\n\t" \
  "ds_read_b64 v[2:3], v27 offset:6144\n\t" \
  "ds_read_b64 v[4:5], v28 offset:7168\n\t" \
  "s_waitcnt lgkmcnt(12)\n\t" \
  "v_mfma_f32_16x16x32_bf16 v[132:135], v[68:71], v[8:11], v[132:135]\n\t" \
  "ds_read_b64 v[6:7], v29 offset:7168\n\t" \
  "v_mfma_f32_16x16x32_bf16 v[136:139], v[76:79], v[8:11], v[136:139]\n\t" \
  "v_mfma_f32_16x16x32_bf16 v[140:143], v[84:87], v[8:11], v[140:143]\n\t" \
  "v_mfma_f32_16x16x32_bf16 v[144:147], v[92:95], v[8:11], v[144:147]\n\t" \
  "v_mfma_f32_16x16x32_bf16 v[148:151], v[100:103], v[8:11], v[148:151]\n\t" \
  "v_mfma_f32_16x16x32_bf16 v[152:155], v[108:111], v[8:11], v[152:155]\n\t" \
  "v_mfma_f32_16x16x32_bf16 v[156:159], v[116:119], v[8:11], v[156:159]\n\t" \
  "ds_read_b64 v[8:9], v30 offset:8192\n\t" \
  "s_waitcnt lgkmcnt(12)\n\t" \
  "v_mfma_f32_16x16x32_bf16 v[132:135], v[64:67], v[12:15], v[132:135]\n\t" \
  "s_cmp_eq_u32 %[rem], 0\n\t" \
  "s_cbranch_scc1 CB240_%=\n\t" \
  "s_barrier\n\t" \
  "s_sub_u32 %[rem], %[rem], 1\n\t" \
  "CB240_%=:\n\t" \
  "ds_read_b64 v[10:11], v31 offset:8192\n\t" \
  "v_mfma_f32_16x16x32_bf16 v[136:139], v[72:75], v[12:15], v[136:139]\n\t" \
  "v_mfma_f32_16x16x32_bf16 v[140:143], v[80:83], v[12:15], v[140:143]\n\t" \
  "v_mfma_f32_16x16x32_bf16 v[144:147], v[88:91], v[12:15], v[144:147]\n\t" \
  "v_mfma_f32_16x16x32_bf16 v[148:151], v[96:99], v[12:15], v[148:151]\n\t" \
  "v_mfma_f32_16x16x32_bf16 v[152:155], v[104:107], v[12:15], v[152:155]\n\t" \
  "v_mfma_f32_16x16x32_bf16 v[156:159], v[112:115], v[12:15], v[156:159]\n\t" \
  "ds_read_b64 v[12:13], v24 offset:9216\n\t" \
  "s_waitcnt lgkmcnt(11)\n\t" \
  "v_mfma_f32_16x16x32_bf16 v[136:139], v[68:71], v[16:19], v[136:139]\n\t" \
  "ds_read_b64 v[14:15], v25 offset:9216\n\t" \
  "v_mfma_f32_16x16x32_bf16 v[140:143], v[76:79], v[16:19], v[140:143]\n\t" \
  "v_mfma_f32_16x16x32_bf16 v[144:147], v[84:87], v[16:19], v[144:147]\n\t" \
  "v_mfma_f32_16x16x32_bf16 v[148:151], v[92:95], v[16:19], v[148:151]\n\t" \
  "v_mfma_f32_16x16x32_bf16 v[152:155], v[100:103], v[16:19], v[152:155]\n\t" \
  "v_mfma_f32_16x16x32_bf16 v[156:159], v[108:111], v[16:19], v[156:159]\n\t" \
  "ds_read_b64 v[16:17], v26 offset:10240\n\t" \
  "s_waitcnt lgkmcnt(11)\n\t" \
  "v_mfma_f32_16x16x32_bf16 v[136:139], v[64:67], v[20:23], v[136:139]\n\t" \
  "ds_read_b64 v[18:19], v27 offset:10240\n\t" \
  "v_mfma_f32_16x16x32_bf16 v[140:143], v[72:75], v[20:23], v[140:143]\n\t" \
  "v_mfma_f32_16x16x32_bf16 v[144:147], v[80:83], v[20:23], v[144:147]\n\t" \
  "v_mfma_f32_16x16x32_bf16 v[148:151], v[88:91], v[20:23], v[148:151]\n\t" \
  "s_cmp_eq_u32 %[rem], 0\n\t" \
  "s_cbranch_scc1 CB256_%=\n\t" \
  "s_barrier\n\t" \
  "s_sub_u32 %[rem], %[rem], 1\n\t" \
  "CB256_%=:\n\t" \
  "v_mfma_f32_16x16x32_bf16 v[152:155], v[96:99], v[20:23], v[152:155]\n\t" \
  "v_mfma_f32_16x16x32_bf16 v[156:159], v[104:107], v[20:23], v[156:159]\n\t" \
  "ds_read_b64 v[20:21], v28 offset:11264\n\t" \
  "s_waitcnt lgkmcnt(9)\n\t" \
  "v_mfma_f32_16x16x32_bf16 v[140:143], v[68:71], v[0:3], v[140:143]\n\t" \
  "ds_read_b64 v[22:23], v29 offset:11264\n\t" \
  "v_mfma_f32_16x16x32_bf16 v[144:147], v[76:79], v[0:3], v[144:147]\n\t" \
  "v_mfma_f32_16x16x32_bf16 v[148:151], v[84:87], v[0:3], v[148:151]\n\t" \
  "v_mfma_f32_16x16x32_bf16 v[152:155], v[92:95], v[0:3], v[152:155]\n\t" \
  "v_mfma_f32_16x16x32_bf16 v[156:159], v[100:103], v[0:3], v[156:159]\n\t" \
  "ds_read_b64 v[0:1], v30 offset:12288\n\t" \
  "s_waitcnt lgkmcnt(9)\n\t" \
  "v_mfma_f32_16x16x32_bf16 v[140:143], v[64:67], v[4:7], v[140:143]\n\t" \
  "ds_read_b64 v[2:3], v31 offset:12288\n\t" \
  "v_mfma_f32_16x16x32_bf16 v[144:147], v[72:75], v[4:7], v[144:147]\n\t" \
  "v_mfma_f32_16x16x32_bf16 v[148:151], v[80:83], v[4:7], v[148:151]\n\t" \
  "v_mfma_f32_16x16x32_bf16 v[152:155], v[88:91], v[4:7], v[152:155]\n\t" \
  "v_mfma_f32_16x16x32_bf16 v[156:159], v[96:99], v[4:7], v[156:159]\n\t" \
  "ds_read_b64 v[4:5], v24 offset:13312\n\t" \
  "s_waitcnt lgkmcnt(9)\n\t" \
  "v_mfma_f32_16x16x32_bf16 v[144:147], v[68:71], v[8:11], v[144:147]\n\t" \
  "ds_read_b64 v[6:7], v25 offset:13312\n\t" \
  "v_mfma_f32_16x16x32_bf16 v[148:151], v[76:79], v[8:11], v[148:151]\n\t" \
  "v_mfma_f32_16x16x32_bf16 v[152:155], v[84:87], v[8:11], v[152:155]\n\t" \
  "v_mfma_f32_16x16x32_bf16 v[156:159], v[92:95], v[8:11], v[156:159]\n\t" \
  "s_cmp_eq_u32 %[rem], 0\n\t" \
  "s_cbranch_scc1 CB272_%=\n\t" \
  "s_barrier\n\t" \
  "s_sub_u32 %[rem], %[rem], 1\n\t" \
  "CB272_%=:\n\t" \
  "ds_read_b64 v[8:9], v26 offset:14336\n\t" \
  "s_waitcnt lgkmcnt(9)\n\t" \
  "v_mfma_f32_16x16x32_bf16 v[144:147], v[64:67], v[12:15], v[144:147]\n\t" \
  "ds_read_b64 v[10:11], v27 offset:14336\n\t" \
  "v_mfma_f32_16x16x32_bf16 v[148:151], v[72:75], v[12:15], v[148:151]\n\t" \
  "v_mfma_f32_16x16x32_bf16 v[152:155], v[80:83], v[12:15], v[152:155]\n\t" \
  "v_mfma_f32_16x16x32_bf16 v[156:159], v[88:91], v[12:15], v[156:159]\n\t" \
  "ds_read_b64 v[12:13], v28 offset:15360\n\t" \
  "s_waitcnt lgkmcnt(9)\n\t" \
  "v_mfma_f32_16x16x32_bf16 v[148:151], v[68:71], v[16:19], v[148:151]\n\t" \
  "ds_read_b64 v[14:15], v29 offset:15360\n\t" \
  "v_mfma_f32_16x16x32_bf16 v[152:155], v[76:79], v[16:19], v[152:155]\n\t" \
  "v_mfma_f32_16x16x32_bf16 v[156:159], v[84:87], v[16:19], v[156:159]\n\t" \
  "s_waitcnt lgkmcnt(8)\n\t" \
  "v_mfma_f32_16x16x32_bf16 v[148:151], v[64:67], v[20:23], v[148:151]\n\t" \
  "v_mfma_f32_16x16x32_bf16 v[152:155], v[72:75], v[20:23], v[152:155]\n\t" \
  "v_mfma_f32_16x16x32_bf16 v[156:159], v[80:83], v[20:23], v[156:159]\n\t" \
  "s_waitcnt lgkmcnt(6)\n\t" \
  "v_mfma_f32_16x16x32_bf16 v[152:155], v[68:71], v[0:3], v[152:155]\n\t" \
  "v_mfma_f32_16x16x32_bf16 v[156:159], v[76:79], v[0:3], v[156:159]\n\t" \
  "s_waitcnt lgkmcnt(4)\n\t" \
  "v_mfma_f32_16x16x32_bf16 v[152:155], v[64:67], v[4:7], v[152:155]\n\t" \
  "v_mfma_f32_16x16x32_bf16 v[156:159], v[72:75], v[4:7], v[156:159]\n\t" \
  "s_waitcnt lgkmcnt(2)\n\t" \
  "v_mfma_f32_16x16x32_bf16 v[156:159], v[68:71], v[8:11], v[156:159]\n\t" \
  "s_waitcnt lgkmcnt(0)\n\t" \
  "v_mfma_f32_16x16x32_bf16 v[156:159], v[64:67], v[12:15], v[156:159]\n\t" \
  "s_waitcnt lgkmcnt(0)\n\t" \
  "s_nop 7\n\t"
#define CV_CLOB "v0", "v1", "v2", "v3", "v4", "v5", "v6", "v7", "v8", "v9", "v10", "v11", "v12", "v13", "v14", "v15", "v16", "v17", "v18", "v19", "v20", "v21", "v22", "v23", "v34", "v35", "v36", "v37", "v38", "v39", "v40", "v41"

#define G3_ASM \
  "s_getpc_b64 s[92:93]\n\t" \
  "s_branch QC_%=\n\t" \
  "s_memrealtime %[st5]\n\t" \
  "s_waitcnt lgkmcnt(0)\n\t" \
  "ds_read_b64_tr_b16 v[96:97], v160 offset:0\n\t" \
  "ds_read_b64_tr_b16 v[98:99], v160 offset:2048\n\t" \
  "ds_read_b64_tr_b16 v[100:101], v161 offset:0\n\t" \
  "ds_read_b64_tr_b16 v[102:103], v161 offset:2048\n\t" \
  "ds_read_b64_tr_b16 v[104:105], v162 offset:0\n\t" \
  "ds_read_b64_tr_b16 v[106:107], v162 offset:2048\n\t" \
  "ds_read_b64_tr_b16 v[108:109], v163 offset:0\n\t" \
  "ds_read_b64_tr_b16 v[110:111], v163 offset:2048\n\t" \
  "ds_read_b64_tr_b16 v[112:113], v160 offset:4096\n\t" \
  "ds_read_b64_tr_b16 v[114:115], v160 offset:6144\n\t" \
  "ds_read_b64_tr_b16 v[116:117], v161 offset:4096\n\t" \
  "ds_read_b64_tr_b16 v[118:119], v161 offset:6144\n\t" \
  "ds_read_b64_tr_b16 v[120:121], v162 offset:4096\n\t" \
  "ds_read_b64_tr_b16 v[122:123], v162 offset:6144\n\t" \
  "QH_%=:\n\t" \
  "v_add_u32 v166, %[go], v165\n\t" \
  "ds_read_b32 v166, v166\n\t" \
  "s_addk_i32 %[cn], 1\n\t" \
  "s_cmp_gt_u32 %[cn], 0x400000\n\t" \
  "s_cbranch_scc1 QI_%=\n\t" \
  "s_waitcnt lgkmcnt(0)\n\t" \
  "v_cmp_eq_u32 vcc, 0, v166\n\t" \
  "s_cbranch_vccz QI_%=\n\t" \
  "s_sleep 2\n\t" \
  "s_getpc_b64 s[92:93]\n\t" \
  "s_branch QC_%=\n\t" \
  "s_branch QH_%=\n\t" \
  "QI_%=:\n\t" \
  "ds_read_b32 v166, v165\n\t" \
  "s_waitcnt lgkmcnt(11)\n\t" \
  "v_mfma_f32_16x16x32_bf16 v[128:131], v[96:99], v[0:3], 0\n\t" \
  "ds_read_b64_tr_b16 v[124:125], v163 offset:4096\n\t" \
  "v_mfma_f32_16x16x32_bf16 v[144:147], v[96:99], v[4:7], 0\n\t" \
  "ds_read_b64_tr_b16 v[126:127], v163 offset:6144\n\t" \
  "s_waitcnt lgkmcnt(2)\n\t" \
  "v_cmp_lt_u32 vcc, %[th], v166\n\t" \
  "v_mfma_f32_16x16x32_bf16 v[132:135], v[100:103], v[0:3], 0\n\t" \
  "s_mov_b64 exec, vcc\n\t" \
  "global_load_dwordx4 v[80:83], v164, %[ob] offset:0 sc1\n\t" \
  "s_mov_b64 exec, -1\n\t" \
  "v_mfma_f32_16x16x32_bf16 v[148:151], v[100:103], v[4:7], 0\n\t" \
  "ds_read_b64_tr_b16 v[96:97], v160 offset:8192\n\t" \
  "ds_read_b64_tr_b16 v[98:99], v160 offset:10240\n\t" \
  "v_mfma_f32_16x16x32_bf16 v[136:139], v[104:107], v[0:3], 0\n\t" \
  "s_mov_b64 exec, vcc\n\t" \
  "global_load_dwordx4 v[84:87], v164, %[ob] offset:1024 sc1\n\t" \
  "s_mov_b64 exec, -1\n\t" \
  "v_mfma_f32_16x16x32_bf16 v[152:155], v[104:107], v[4:7], 0\n\t" \
  "ds_read_b64_tr_b16 v[100:101], v161 offset:8192\n\t" \
  "ds_read_b64_tr_b16 v[102:103], v161 offset:10240\n\t" \
  "v_mfma_f32_16x16x32_bf16 v[140:143], v[108:111], v[0:3], 0\n\t" \
  "s_mov_b64 exec, vcc\n\t" \
  "global_load_dwordx4 v[88:91], v164, %[ob] offset:2048 sc1\n\t" \
  "s_mov_b64 exec, -1\n\t" \
  "v_mfma_f32_16x16x32_bf16 v[156:159], v[108:111], v[4:7], 0\n\t" \
  "ds_read_b64_tr_b16 v[104:105], v162 offset:8192\n\t" \
  "ds_read_b64_tr_b16 v[106:107], v162 offset:10240\n\t" \
  "v_mfma_f32_16x16x32_bf16 v[128:131], v[112:115], v[8:11], v[128:131]\n\t" \
  "s_mov_b64 exec, vcc\n\t" \
  "global_load_dwordx4 v[92:95], v164, %[ob] offset:3072 sc1\n\t" \
  "s_mov_b64 exec, -1\n\t" \
  "v_mfma_f32_16x16x32_bf16 v[144:147], v[112:115], v[12:15], v[144:147]\n\t" \
  "ds_read_b64_tr_b16 v[108:109], v163 offset:8192\n\t" \
  "ds_read_b64_tr_b16 v[110:111], v163 offset:10240\n\t" \
  "v_mfma_f32_16x16x32_bf16 v[132:135], v[116:119], v[8:11], v[132:135]\n\t" \
  "ds_read_b64_tr_b16 v[112:113], v160 offset:12288\n\t" \
  "s_and_b32 %[t0], vcc_lo, 0x4000\n\t" \
  "ds_read_b32 v166, v165\n\t" \
  "v_mfma_f32_16x16x32_bf16 v[148:151], v[116:119], v[12:15], v[148:151]\n\t" \
  "ds_read_b64_tr_b16 v[114:115], v160 offset:14336\n\t" \
  "v_add_u32 v164, %[t0], v164\n\t" \
  "s_waitcnt lgkmcnt(11)\n\t" \
  "v_mfma_f32_16x16x32_bf16 v[136:139], v[120:123], v[8:11], v[136:139]\n\t" \
  "ds_read_b64_tr_b16 v[116:117], v161 offset:12288\n\t" \
  "v_and_b32 v164, 0x3ffff, v164\n\t" \
  "v_mfma_f32_16x16x32_bf16 v[152:155], v[120:123], v[12:15], v[152:155]\n\t" \
  "ds_read_b64_tr_b16 v[118:119], v161 offset:14336\n\t" \
  "s_sub_u32 %[iss], %[iss], vcc_lo\n\t" \
  "v_mfma_f32_16x16x32_bf16 v[140:143], v[124:127], v[8:11], v[140:143]\n\t" \
  "ds_read_b64_tr_b16 v[120:121], v162 offset:12288\n\t" \
  "v_mfma_f32_16x16x32_bf16 v[156:159], v[124:127], v[12:15], v[156:159]\n\t" \
  "ds_read_b64_tr_b16 v[122:123], v162 offset:14336\n\t" \
  "s_add_u32 %[th], %[th], 1\n\t" \
  "s_cbranch_vccz QF1_%=\n\t" \
  "s_waitcnt vmcnt(16)\n\t" \
  "QG1_%=:\n\t" \
  "s_waitcnt lgkmcnt(11)\n\t" \
  "v_mfma_f32_16x16x32_bf16 v[128:131], v[96:99], v[16:19], v[128:131]\n\t" \
  "ds_read_b64_tr_b16 v[124:125], v163 offset:12288\n\t" \
  "v_mfma_f32_16x16x32_bf16 v[144:147], v[96:99], v[20:23], v[144:147]\n\t" \
  "ds_read_b64_tr_b16 v[126:127], v163 offset:14336\n\t" \
  "s_waitcnt lgkmcnt(7)\n\t" \
  "v_cmp_lt_u32 vcc, %[th], v166\n\t" \
  "v_mfma_f32_16x16x32_bf16 v[132:135], v[100:103], v[16:19], v[132:135]\n\t" \
  "s_mov_b64 exec, vcc\n\t" \
  "global_load_dwordx4 v[0:3], v164, %[ob] offset:0 sc1\n\t" \
  "s_mov_b64 exec, -1\n\t" \
  "v_mfma_f32_16x16x32_bf16 v[148:151], v[100:103], v[20:23], v[148:151]\n\t" \
  "ds_read_b64_tr_b16 v[96:97], v160 offset:16384\n\t" \
  "ds_read_b64_tr_b16 v[98:99], v160 offset:18432\n\t" \
  "v_mfma_f32_16x16x32_bf16 v[136:139], v[104:107], v[16:19], v[136:139]\n\t" \
  "s_mov_b64 exec, vcc\n\t" \
  "global_load_dwordx4 v[4:7], v164, %[ob] offset:1024 sc1\n\t" \
  "s_mov_b64 exec, -1\n\t" \
  "v_mfma_f32_16x16x32_bf16 v[152:155], v[104:107], v[20:23], v[152:155]\n\t" \
  "ds_read_b64_tr_b16 v[100:101], v161 offset:16384\n\t" \
  "ds_read_b64_tr_b16 v[102:103], v161 offset:18432\n\t" \
  "v_mfma_f32_16x16x32_bf16 v[140:143], v[108:111], v[16:19], v[140:143]\n\t" \
  "s_mov_b64 exec, vcc\n\t" \
  "global_load_dwordx4 v[8:11], v164, %[ob] offset:2048 sc1\n\t" \
  "s_mov_b64 exec, -1\n\t" \
  "v_mfma_f32_16x16x32_bf16 v[156:159], v[108:111], v[20:23], v[156:159]\n\t" \
  "ds_read_b64_tr_b16 v[104:105], v162 offset:16384\n\t" \
  "ds_read_b64_tr_b16 v[106:107], v162 offset:18432\n\t" \
  "s_waitcnt lgkmcnt(10)\n\t" \
  "v_mfma_f32_16x16x32_bf16 v[128:131], v[112:115], v[24:27], v[128:131]\n\t" \
  "s_mov_b64 exec, vcc\n\t" \
  "global_load_dwordx4 v[12:15], v164, %[ob] offset:3072 sc1\n\t" \
  "s_mov_b64 exec, -1\n\t" \
  "v_mfma_f32_16x16x32_bf16 v[144:147], v[112:115], v[28:31], v[144:147]\n\t" \
  "ds_read_b64_tr_b16 v[108:109], v163 offset:16384\n\t" \
  "ds_read_b64_tr_b16 v[110:111], v163 offset:18432\n\t" \
  "v_mfma_f32_16x16x32_bf16 v[132:135], v[116:119], v[24:27], v[132:135]\n\t" \
  "ds_read_b64_tr_b16 v[112:113], v160 offset:20480\n\t" \
  "s_and_b32 %[t0], vcc_lo, 0x4000\n\t" \
  "ds_read_b32 v166, v165\n\t" \
  "v_mfma_f32_16x16x32_bf16 v[148:151], v[116:119], v[28:31], v[148:151]\n\t" \
  "ds_read_b64_tr_b16 v[114:115], v160 offset:22528\n\t" \
  "v_add_u32 v164, %[t0], v164\n\t" \
  "s_waitcnt lgkmcnt(11)\n\t" \
  "v_mfma_f32_16x16x32_bf16 v[136:139], v[120:123], v[24:27], v[136:139]\n\t" \
  "ds_read_b64_tr_b16 v[116:117], v161 offset:20480\n\t" \
  "v_and_b32 v164, 0x3ffff, v164\n\t" \
  "v_mfma_f32_16x16x32_bf16 v[152:155], v[120:123], v[28:31], v[152:155]\n\t" \
  "ds_read_b64_tr_b16 v[118:119], v161 offset:22528\n\t" \
  "s_sub_u32 %[iss], %[iss], vcc_lo\n\t" \
  "v_mfma_f32_16x16x32_bf16 v[140:143], v[124:127], v[24:27], v[140:143]\n\t" \
  "ds_read_b64_tr_b16 v[120:121], v162 offset:20480\n\t" \
  "v_mfma_f32_16x16x32_bf16 v[156:159], v[124:127], v[28:31], v[156:159]\n\t" \
  "ds_read_b64_tr_b16 v[122:123], v162 offset:22528\n\t" \
  "s_add_u32 %[th], %[th], 1\n\t" \
  "s_cbranch_vccz QF2_%=\n\t" \
  "s_waitcnt vmcnt(16)\n\t" \
  "QG2_%=:\n\t" \
  "s_waitcnt lgkmcnt(11)\n\t" \
  "v_mfma_f32_16x16x32_bf16 v[128:131], v[96:99], v[32:35], v[128:131]\n\t" \
  "ds_read_b64_tr_b16 v[124:125], v163 offset:20480\n\t" \
  "v_mfma_f32_16x16x32_bf16 v[144:147], v[96:99], v[36:39], v[144:147]\n\t" \
  "ds_read_b64_tr_b16 v[126:127], v163 offset:22528\n\t" \
  "s_waitcnt lgkmcnt(7)\n\t" \
  "v_cmp_lt_u32 vcc, %[th], v166\n\t" \
  "v_mfma_f32_16x16x32_bf16 v[132:135], v[100:103], v[32:35], v[132:135]\n\t" \
  "s_mov_b64 exec, vcc\n\t" \
  "global_load_dwordx4 v[16:19], v164, %[ob] offset:0 sc1\n\t" \
  "s_mov_b64 exec, -1\n\t" \
  "v_mfma_f32_16x16x32_bf16 v[148:151], v[100:103], v[36:39], v[148:151]\n\t" \
  "ds_read_b64_tr_b16 v[96:97], v160 offset:24576\n\t" \
  "ds_read_b64_tr_b16 v[98:99], v160 offset:26624\n\t" \
  "v_mfma_f32_16x16x32_bf16 v[136:139], v[104:107], v[32:35], v[136:139]\n\t" \
  "s_mov_b64 exec, vcc\n\t" \
  "global_load_dwordx4 v[20:23], v164, %[ob] offset:1024 sc1\n\t" \
  "s_mov_b64 exec, -1\n\t" \
  "v_mfma_f32_16x16x32_bf16 v[152:155], v[104:107], v[36:39], v[152:155]\n\t" \
  "ds_read_b64_tr_b16 v[100:101], v161 offset:24576\n\t" \
  "ds_read_b64_tr_b16 v[102:103], v161 offset:26624\n\t" \
  "v_mfma_f32_16x16x32_bf16 v[140:143], v[108:111], v[32:35], v[140:143]\n\t" \
  "s_mov_b64 exec, vcc\n\t" \
  "global_load_dwordx4 v[24:27], v164, %[ob] offset:2048 sc1\n\t" \
  "s_mov_b64 exec, -1\n\t" \
  "v_mfma_f32_16x16x32_bf16 v[156:159], v[108:111], v[36:39], v[156:159]\n\t" \
  "ds_read_b64_tr_b16 v[104:105], v162 offset:24576\n\t" \
  "ds_read_b64_tr_b16 v[106:107], v162 offset:26624\n\t" \
  "s_waitcnt lgkmcnt(10)\n\t" \
  "v_mfma_f32_16x16x32_bf16 v[128:131], v[112:115], v[40:43], v[128:131]\n\t" \
  "s_mov_b64 exec, vcc\n\t" \
  "global_load_dwordx4 v[28:31], v164, %[ob] offset:3072 sc1\n\t" \
  "s_mov_b64 exec, -1\n\t" \
  "v_mfma_f32_16x16x32_bf16 v[144:147], v[112:115], v[44:47], v[144:147]\n\t" \
  "ds_read_b64_tr_b16 v[108:109], v163 offset:24576\n\t" \
  "ds_read_b64_tr_b16 v[110:111], v163 offset:26624\n\t" \
  "v_mfma_f32_16x16x32_bf16 v[132:135], v[116:119], v[40:43], v[132:135]\n\t" \
  "ds_read_b64_tr_b16 v[112:113], v160 offset:28672\n\t" \
  "s_and_b32 %[t0], vcc_lo, 0x4000\n\t" \
  "ds_read_b32 v166, v165\n\t" \
  "v_mfma_f32_16x16x32_bf16 v[148:151], v[116:119], v[44:47], v[148:151]\n\t" \
  "ds_read_b64_tr_b16 v[114:115], v160 offset:30720\n\t" \
  "v_add_u32 v164, %[t0], v164\n\t" \
  "s_waitcnt lgkmcnt(11)\n\t" \
  "v_mfma_f32_16x16x32_bf16 v[136:139], v[120:123], v[40:43], v[136:139]\n\t" \
  "ds_read_b64_tr_b16 v[116:117], v161 offset:28672\n\t" \
  "v_and_b32 v164, 0x3ffff, v164\n\t" \
  "v_mfma_f32_16x16x32_bf16 v[152:155], v[120:123], v[44:47], v[152:155]\n\t" \
  "ds_read_b64_tr_b16 v[118:119], v161 offset:30720\n\t" \
  "s_sub_u32 %[iss], %[iss], vcc_lo\n\t" \
  "v_mfma_f32_16x16x32_bf16 v[140:143], v[124:127], v[40:43], v[140:143]\n\t" \
  "ds_read_b64_tr_b16 v[120:121], v162 offset:28672\n\t" \
  "v_mfma_f32_16x16x32_bf16 v[156:159], v[124:127], v[44:47], v[156:159]\n\t" \
  "ds_read_b64_tr_b16 v[122:123], v162 offset:30720\n\t" \
  "s_add_u32 %[th], %[th], 1\n\t" \
  "s_cbranch_vccz QF3_%=\n\t" \
  "s_waitcnt vmcnt(16)\n\t" \
  "QG3_%=:\n\t" \
  "s_waitcnt lgkmcnt(11)\n\t" \
  "v_mfma_f32_16x16x32_bf16 v[128:131], v[96:99], v[48:51], v[128:131]\n\t" \
  "ds_read_b64_tr_b16 v[124:125], v163 offset:28672\n\t" \
  "v_mfma_f32_16x16x32_bf16 v[144:147], v[96:99], v[52:55], v[144:147]\n\t" \
  "ds_read_b64_tr_b16 v[126:127], v163 offset:30720\n\t" \
  "s_waitcnt lgkmcnt(7)\n\t" \
  "v_cmp_lt_u32 vcc, %[th], v166\n\t" \
  "v_mfma_f32_16x16x32_bf16 v[132:135], v[100:103], v[48:51], v[132:135]\n\t" \
  "s_mov_b64 exec, vcc\n\t" \
  "global_load_dwordx4 v[32:35], v164, %[ob] offset:0 sc1\n\t" \
  "s_mov_b64 exec, -1\n\t" \
  "v_mfma_f32_16x16x32_bf16 v[148:151], v[100:103], v[52:55], v[148:151]\n\t" \
  "ds_read_b64_tr_b16 v[96:97], v160 offset:32768\n\t" \
  "ds_read_b64_tr_b16 v[98:99], v160 offset:34816\n\t" \
  "v_mfma_f32_16x16x32_bf16 v[136:139], v[104:107], v[48:51], v[136:139]\n\t" \
  "s_mov_b64 exec, vcc\n\t" \
  "global_load_dwordx4 v[36:39], v164, %[ob] offset:1024 sc1\n\t" \
  "s_mov_b64 exec, -1\n\t" \
  "v_mfma_f32_16x16x32_bf16 v[152:155], v[104:107], v[52:55], v[152:155]\n\t" \
  "ds_read_b64_tr_b16 v[100:101], v161 offset:32768\n\t" \
  "ds_read_b64_tr_b16 v[102:103], v161 offset:34816\n\t" \
  "v_mfma_f32_16x16x32_bf16 v[140:143], v[108:111], v[48:51], v[140:143]\n\t" \
  "s_mov_b64 exec, vcc\n\t" \
  "global_load_dwordx4 v[40:43], v164, %[ob] offset:2048 sc1\n\t" \
  "s_mov_b64 exec, -1\n\t" \
  "v_mfma_f32_16x16x32_bf16 v[156:159], v[108:111], v[52:55], v[156:159]\n\t" \
  "ds_read_b64_tr_b16 v[104:105], v162 offset:32768\n\t" \
  "ds_read_b64_tr_b16 v[106:107], v162 offset:34816\n\t" \
  "s_waitcnt lgkmcnt(10)\n\t" \
  "v_mfma_f32_16x16x32_bf16 v[128:131], v[112:115], v[56:59], v[128:131]\n\t" \
  "s_mov_b64 exec, vcc\n\t" \
  "global_load_dwordx4 v[44:47], v164, %[ob] offset:3072 sc1\n\t" \
  "s_mov_b64 exec, -1\n\t" \
  "v_mfma_f32_16x16x32_bf16 v[144:147], v[112:115], v[60:63], v[144:147]\n\t" \
  "ds_read_b64_tr_b16 v[108:109], v163 offset:32768\n\t" \
  "ds_read_b64_tr_b16 v[110:111], v163 offset:34816\n\t" \
  "v_mfma_f32_16x16x32_bf16 v[132:135], v[116:119], v[56:59], v[132:135]\n\t" \
  "ds_read_b64_tr_b16 v[112:113], v160 offset:36864\n\t" \
  "s_and_b32 %[t0], vcc_lo, 0x4000\n\t" \
  "ds_read_b32 v166, v165\n\t" \
  "v_mfma_f32_16x16x32_bf16 v[148:151], v[116:119], v[60:63], v[148:151]\n\t" \
  "ds_read_b64_tr_b16 v[114:115], v160 offset:38912\n\t" \
  "v_add_u32 v164, %[t0], v164\n\t" \
  "s_waitcnt lgkmcnt(11)\n\t" \
  "v_mfma_f32_16x16x32_bf16 v[136:139], v[120:123], v[56:59], v[136:139]\n\t" \
  "ds_read_b64_tr_b16 v[116:117], v161 offset:36864\n\t" \
  "v_and_b32 v164, 0x3ffff, v164\n\t" \
  "v_mfma_f32_16x16x32_bf16 v[152:155], v[120:123], v[60:63], v[152:155]\n\t" \
  "ds_read_b64_tr_b16 v[118:119], v161 offset:38912\n\t" \
  "s_sub_u32 %[iss], %[iss], vcc_lo\n\t" \
  "v_mfma_f32_16x16x32_bf16 v[140:143], v[124:127], v[56:59], v[140:143]\n\t" \
  "ds_read_b64_tr_b16 v[120:121], v162 offset:36864\n\t" \
  "v_mfma_f32_16x16x32_bf16 v[156:159], v[124:127], v[60:63], v[156:159]\n\t" \
  "ds_read_b64_tr_b16 v[122:123], v162 offset:38912\n\t" \
  "s_add_u32 %[th], %[th], 1\n\t" \
  "s_cbranch_vccz QF4_%=\n\t" \
  "s_waitcnt vmcnt(16)\n\t" \
  "QG4_%=:\n\t" \
  "s_waitcnt lgkmcnt(11)\n\t" \
  "v_mfma_f32_16x16x32_bf16 v[128:131], v[96:99], v[64:67], v[128:131]\n\t" \
  "ds_read_b64_tr_b16 v[124:125], v163 offset:36864\n\t" \
  "v_mfma_f32_16x16x32_bf16 v[144:147], v[96:99], v[68:71], v[144:147]\n\t" \
  "ds_read_b64_tr_b16 v[126:127], v163 offset:38912\n\t" \
  "s_waitcnt lgkmcnt(7)\n\t" \
  "v_cmp_lt_u32 vcc, %[th], v166\n\t" \
  "v_mfma_f32_16x16x32_bf16 v[132:135], v[100:103], v[64:67], v[132:135]\n\t" \
  "s_mov_b64 exec, vcc\n\t" \
  "global_load_dwordx4 v[48:51], v164, %[ob] offset:0 sc1\n\t" \
  "s_mov_b64 exec, -1\n\t" \
  "v_mfma_f32_16x16x32_bf16 v[148:151], v[100:103], v[68:71], v[148:151]\n\t" \
  "ds_read_b64_tr_b16 v[96:97], v160 offset:40960\n\t" \
  "ds_read_b64_tr_b16 v[98:99], v160 offset:43008\n\t" \
  "v_mfma_f32_16x16x32_bf16 v[136:139], v[104:107], v[64:67], v[136:139]\n\t" \
  "s_mov_b64 exec, vcc\n\t" \
  "global_load_dwordx4 v[52:55], v164, %[ob] offset:1024 sc1\n\t" \
  "s_mov_b64 exec, -1\n\t" \
  "v_mfma_f32_16x16x32_bf16 v[152:155], v[104:107], v[68:71], v[152:155]\n\t" \
  "ds_read_b64_tr_b16 v[100:101], v161 offset:40960\n\t" \
  "ds_read_b64_tr_b16 v[102:103], v161 offset:43008\n\t" \
  "v_mfma_f32_16x16x32_bf16 v[140:143], v[108:111], v[64:67], v[140:143]\n\t" \
  "s_mov_b64 exec, vcc\n\t" \
  "global_load_dwordx4 v[56:59], v164, %[ob] offset:2048 sc1\n\t" \
  "s_mov_b64 exec, -1\n\t" \
  "v_mfma_f32_16x16x32_bf16 v[156:159], v[108:111], v[68:71], v[156:159]\n\t" \
  "ds_read_b64_tr_b16 v[104:105], v162 offset:40960\n\t" \
  "ds_read_b64_tr_b16 v[106:107], v162 offset:43008\n\t" \
  "s_waitcnt lgkmcnt(10)\n\t" \
  "v_mfma_f32_16x16x32_bf16 v[128:131], v[112:115], v[72:75], v[128:131]\n\t" \
  "s_mov_b64 exec, vcc\n\t" \
  "global_load_dwordx4 v[60:63], v164, %[ob] offset:3072 sc1\n\t" \
  "s_mov_b64 exec, -1\n\t" \
  "v_mfma_f32_16x16x32_bf16 v[144:147], v[112:115], v[76:79], v[144:147]\n\t" \
  "ds_read_b64_tr_b16 v[108:109], v163 offset:40960\n\t" \
  "ds_read_b64_tr_b16 v[110:111], v163 offset:43008\n\t" \
  "v_mfma_f32_16x16x32_bf16 v[132:135], v[116:119], v[72:75], v[132:135]\n\t" \
  "ds_read_b64_tr_b16 v[112:113], v160 offset:45056\n\t" \
  "s_and_b32 %[t0], vcc_lo, 0x4000\n\t" \
  "ds_read_b32 v166, v165\n\t" \
  "v_mfma_f32_16x16x32_bf16 v[148:151], v[116:119], v[76:79], v[148:151]\n\t" \
  "ds_read_b64_tr_b16 v[114:115], v160 offset:47104\n\t" \
  "v_add_u32 v164, %[t0], v164\n\t" \
  "s_waitcnt lgkmcnt(11)\n\t" \
  "v_mfma_f32_16x16x32_bf16 v[136:139], v[120:123], v[72:75], v[136:139]\n\t" \
  "ds_read_b64_tr_b16 v[116:117], v161 offset:45056\n\t" \
  "v_and_b32 v164, 0x3ffff, v164\n\t" \
  "v_mfma_f32_16x16x32_bf16 v[152:155], v[120:123], v[76:79], v[152:155]\n\t" \
  "ds_read_b64_tr_b16 v[118:119], v161 offset:47104\n\t" \
  "s_sub_u32 %[iss], %[iss], vcc_lo\n\t" \
  "v_mfma_f32_16x16x32_bf16 v[140:143], v[124:127], v[72:75], v[140:143]\n\t" \
  "ds_read_b64_tr_b16 v[120:121], v162 offset:45056\n\t" \
  "v_mfma_f32_16x16x32_bf16 v[156:159], v[124:127], v[76:79], v[156:159]\n\t" \
  "ds_read_b64_tr_b16 v[122:123], v162 offset:47104\n\t" \
  "s_add_u32 %[th], %[th], 1\n\t" \
  "s_cbranch_vccz QF5_%=\n\t" \
  "s_waitcnt vmcnt(16)\n\t" \
  "QG5_%=:\n\t" \
  "s_waitcnt lgkmcnt(11)\n\t" \
  "v_mfma_f32_16x16x32_bf16 v[128:131], v[96:99], v[80:83], v[128:131]\n\t" \
  "ds_read_b64_tr_b16 v[124:125], v163 offset:45056\n\t" \
  "v_mfma_f32_16x16x32_bf16 v[144:147], v[96:99], v[84:87], v[144:147]\n\t" \
  "ds_read_b64_tr_b16 v[126:127], v163 offset:47104\n\t" \
  "s_waitcnt lgkmcnt(7)\n\t" \
  "v_cmp_lt_u32 vcc, %[th], v166\n\t" \
  "v_mfma_f32_16x16x32_bf16 v[132:135], v[100:103], v[80:83], v[132:135]\n\t" \
  "s_mov_b64 exec, vcc\n\t" \
  "global_load_dwordx4 v[64:67], v164, %[ob] offset:0 sc1\n\t" \
  "s_mov_b64 exec, -1\n\t" \
  "v_mfma_f32_16x16x32_bf16 v[148:151], v[100:103], v[84:87], v[148:151]\n\t" \
  "ds_read_b64_tr_b16 v[96:97], v160 offset:49152\n\t" \
  "ds_read_b64_tr_b16 v[98:99], v160 offset:51200\n\t" \
  "v_mfma_f32_16x16x32_bf16 v[136:139], v[104:107], v[80:83], v[136:139]\n\t" \
  "s_mov_b64 exec, vcc\n\t" \
  "global_load_dwordx4 v[68:71], v164, %[ob] offset:1024 sc1\n\t" \
  "s_mov_b64 exec, -1\n\t" \
  "v_mfma_f32_16x16x32_bf16 v[152:155], v[104:107], v[84:87], v[152:155]\n\t" \
  "ds_read_b64_tr_b16 v[100:101], v161 offset:49152\n\t" \
  "ds_read_b64_tr_b16 v[102:103], v161 offset:51200\n\t" \
  "v_mfma_f32_16x16x32_bf16 v[140:143], v[108:111], v[80:83], v[140:143]\n\t" \
  "s_mov_b64 exec, vcc\n\t" \
  "global_load_dwordx4 v[72:75], v164, %[ob] offset:2048 sc1\n\t" \
  "s_mov_b64 exec, -1\n\t" \
  "v_mfma_f32_16x16x32_bf16 v[156:159], v[108:111], v[84:87], v[156:159]\n\t" \
  "ds_read_b64_tr_b16 v[104:105], v162 offset:49152\n\t" \
  "ds_read_b64_tr_b16 v[106:107], v162 offset:51200\n\t" \
  "s_waitcnt lgkmcnt(10)\n\t" \
  "v_mfma_f32_16x16x32_bf16 v[128:131], v[112:115], v[88:91], v[128:131]\n\t" \
  "s_mov_b64 exec, vcc\n\t" \
  "global_load_dwordx4 v[76:79], v164, %[ob] offset:3072 sc1\n\t" \
  "s_mov_b64 exec, -1\n\t" \
  "v_mfma_f32_16x16x32_bf16 v[144:147], v[112:115], v[92:95], v[144:147]\n\t" \
  "ds_read_b64_tr_b16 v[108:109], v163 offset:49152\n\t" \
  "ds_read_b64_tr_b16 v[110:111], v163 offset:51200\n\t" \
  "v_mfma_f32_16x16x32_bf16 v[132:135], v[116:119], v[88:91], v[132:135]\n\t" \
  "ds_read_b64_tr_b16 v[112:113], v160 offset:53248\n\t" \
  "s_and_b32 %[t0], vcc_lo, 0x4000\n\t" \
  "ds_read_b32 v166, v165\n\t" \
  "v_mfma_f32_16x16x32_bf16 v[148:151], v[116:119], v[92:95], v[148:151]\n\t" \
  "ds_read_b64_tr_b16 v[114:115], v160 offset:55296\n\t" \
  "v_add_u32 v164, %[t0], v164\n\t" \
  "s_waitcnt lgkmcnt(11)\n\t" \
  "v_mfma_f32_16x16x32_bf16 v[136:139], v[120:123], v[88:91], v[136:139]\n\t" \
  "ds_read_b64_tr_b16 v[116:117], v161 offset:53248\n\t" \
  "v_and_b32 v164, 0x3ffff, v164\n\t" \
  "v_mfma_f32_16x16x32_bf16 v[152:155], v[120:123], v[92:95], v[152:155]\n\t" \
  "ds_read_b64_tr_b16 v[118:119], v161 offset:55296\n\t" \
  "s_sub_u32 %[iss], %[iss], vcc_lo\n\t" \
  "v_mfma_f32_16x16x32_bf16 v[140:143], v[124:127], v[88:91], v[140:143]\n\t" \
  "ds_read_b64_tr_b16 v[120:121], v162 offset:53248\n\t" \
  "v_mfma_f32_16x16x32_bf16 v[156:159], v[124:127], v[92:95], v[156:159]\n\t" \
  "ds_read_b64_tr_b16 v[122:123], v162 offset:55296\n\t" \
  "s_add_u32 %[th], %[th], 1\n\t" \
  "s_cbranch_vccz QF6_%=\n\t" \
  "s_waitcnt vmcnt(16)\n\t" \
  "QG6_%=:\n\t" \
  "s_waitcnt lgkmcnt(11)\n\t" \
  "v_mfma_f32_16x16x32_bf16 v[128:131], v[96:99], v[0:3], v[128:131]\n\t" \
  "ds_read_b64_tr_b16 v[124:125], v163 offset:53248\n\t" \
  "v_mfma_f32_16x16x32_bf16 v[144:147], v[96:99], v[4:7], v[144:147]\n\t" \
  "ds_read_b64_tr_b16 v[126:127], v163 offset:55296\n\t" \
  "s_waitcnt lgkmcnt(7)\n\t" \
  "v_cmp_lt_u32 vcc, %[th], v166\n\t" \
  "v_mfma_f32_16x16x32_bf16 v[132:135], v[100:103], v[0:3], v[132:135]\n\t" \
  "s_mov_b64 exec, vcc\n\t" \
  "global_load_dwordx4 v[80:83], v164, %[ob] offset:0 sc1\n\t" \
  "s_mov_b64 exec, -1\n\t" \
  "v_mfma_f32_16x16x32_bf16 v[148:151], v[100:103], v[4:7], v[148:151]\n\t" \
  "ds_read_b64_tr_b16 v[96:97], v160 offset:57344\n\t" \
  "ds_read_b64_tr_b16 v[98:99], v160 offset:59392\n\t" \
  "v_mfma_f32_16x16x32_bf16 v[136:139], v[104:107], v[0:3], v[136:139]\n\t" \
  "s_mov_b64 exec, vcc\n\t" \
  "global_load_dwordx4 v[84:87], v164, %[ob] offset:1024 sc1\n\t" \
  "s_mov_b64 exec, -1\n\t" \
  "v_mfma_f32_16x16x32_bf16 v[152:155], v[104:107], v[4:7], v[152:155]\n\t" \
  "ds_read_b64_tr_b16 v[100:101], v161 offset:57344\n\t" \
  "ds_read_b64_tr_b16 v[102:103], v161 offset:59392\n\t" \
  "v_mfma_f32_16x16x32_bf16 v[140:143], v[108:111], v[0:3], v[140:143]\n\t" \
  "s_mov_b64 exec, vcc\n\t" \
  "global_load_dwordx4 v[88:91], v164, %[ob] offset:2048 sc1\n\t" \
  "s_mov_b64 exec, -1\n\t" \
  "v_mfma_f32_16x16x32_bf16 v[156:159], v[108:111], v[4:7], v[156:159]\n\t" \
  "ds_read_b64_tr_b16 v[104:105], v162 offset:57344\n\t" \
  "ds_read_b64_tr_b16 v[106:107], v162 offset:59392\n\t" \
  "s_waitcnt lgkmcnt(10)\n\t" \
  "v_mfma_f32_16x16x32_bf16 v[128:131], v[112:115], v[8:11], v[128:131]\n\t" \
  "s_mov_b64 exec, vcc\n\t" \
  "global_load_dwordx4 v[92:95], v164, %[ob] offset:3072 sc1\n\t" \
  "s_mov_b64 exec, -1\n\t" \
  "v_mfma_f32_16x16x32_bf16 v[144:147], v[112:115], v[12:15], v[144:147]\n\t" \
  "ds_read_b64_tr_b16 v[108:109], v163 offset:57344\n\t" \
  "ds_read_b64_tr_b16 v[110:111], v163 offset:59392\n\t" \
  "v_mfma_f32_16x16x32_bf16 v[132:135], v[116:119], v[8:11], v[132:135]\n\t" \
  "ds_read_b64_tr_b16 v[112:113], v160 offset:61440\n\t" \
  "s_and_b32 %[t0], vcc_lo, 0x4000\n\t" \
  "ds_read_b32 v166, v165\n\t" \
  "v_mfma_f32_16x16x32_bf16 v[148:151], v[116:119], v[12:15], v[148:151]\n\t" \
  "ds_read_b64_tr_b16 v[114:115], v160 offset:63488\n\t" \
  "v_add_u32 v164, %[t0], v164\n\t" \
  "s_waitcnt lgkmcnt(11)\n\t" \
  "v_mfma_f32_16x16x32_bf16 v[136:139], v[120:123], v[8:11], v[136:139]\n\t" \
  "ds_read_b64_tr_b16 v[116:117], v161 offset:61440\n\t" \
  "v_and_b32 v164, 0x3ffff, v164\n\t" \
  "v_mfma_f32_16x16x32_bf16 v[152:155], v[120:123], v[12:15], v[152:155]\n\t" \
  "ds_read_b64_tr_b16 v[118:119], v161 offset:63488\n\t" \
  "s_sub_u32 %[iss], %[iss], vcc_lo\n\t" \
  "v_mfma_f32_16x16x32_bf16 v[140:143], v[124:127], v[8:11], v[140:143]\n\t" \
  "ds_read_b64_tr_b16 v[120:121], v162 offset:61440\n\t" \
  "v_mfma_f32_16x16x32_bf16 v[156:159], v[124:127], v[12:15], v[156:159]\n\t" \
  "ds_read_b64_tr_b16 v[122:123], v162 offset:63488\n\t" \
  "s_add_u32 %[th], %[th], 1\n\t" \
  "s_cbranch_vccz QF7_%=\n\t" \
  "s_waitcnt vmcnt(16)\n\t" \
  "QG7_%=:\n\t" \
  "s_waitcnt lgkmcnt(11)\n\t" \
  "v_mfma_f32_16x16x32_bf16 v[128:131], v[96:99], v[16:19], v[128:131]\n\t" \
  "ds_read_b64_tr_b16 v[124:125], v163 offset:61440\n\t" \
  "v_mfma_f32_16x16x32_bf16 v[144:147], v[96:99], v[20:23], v[144:147]\n\t" \
  "ds_read_b64_tr_b16 v[126:127], v163 offset:63488\n\t" \
  "s_waitcnt lgkmcnt(7)\n\t" \
  "v_cmp_lt_u32 vcc, %[th], v166\n\t" \
  "v_mfma_f32_16x16x32_bf16 v[132:135], v[100:103], v[16:19], v[132:135]\n\t" \
  "v_add_u32 v160, 0x10000, v160\n\t" \
  "s_mov_b64 exec, vcc\n\t" \
  "global_load_dwordx4 v[0:3], v164, %[ob] offset:0 sc1\n\t" \
  "s_mov_b64 exec, -1\n\t" \
  "v_mfma_f32_16x16x32_bf16 v[148:151], v[100:103], v[20:23], v[148:151]\n\t" \
  "ds_read_b64_tr_b16 v[96:97], v160 offset:0\n\t" \
  "ds_read_b64_tr_b16 v[98:99], v160 offset:2048\n\t" \
  "v_mfma_f32_16x16x32_bf16 v[136:139], v[104:107], v[16:19], v[136:139]\n\t" \
  "v_add_u32 v161, 0x10000, v161\n\t" \
  "s_mov_b64 exec, vcc\n\t" \
  "global_load_dwordx4 v[4:7], v164, %[ob] offset:1024 sc1\n\t" \
  "s_mov_b64 exec, -1\n\t" \
  "v_mfma_f32_16x16x32_bf16 v[152:155], v[104:107], v[20:23], v[152:155]\n\t" \
  "ds_read_b64_tr_b16 v[100:101], v161 offset:0\n\t" \
  "ds_read_b64_tr_b16 v[102:103], v161 offset:2048\n\t" \
  "v_mfma_f32_16x16x32_bf16 v[140:143], v[108:111], v[16:19], v[140:143]\n\t" \
  "v_add_u32 v162, 0x10000, v162\n\t" \
  "s_mov_b64 exec, vcc\n\t" \
  "global_load_dwordx4 v[8:11], v164, %[ob] offset:2048 sc1\n\t" \
  "s_mov_b64 exec, -1\n\t" \
  "v_mfma_f32_16x16x32_bf16 v[156:159], v[108:111], v[20:23], v[156:159]\n\t" \
  "ds_read_b64_tr_b16 v[104:105], v162 offset:0\n\t" \
  "ds_read_b64_tr_b16 v[106:107], v162 offset:2048\n\t" \
  "s_waitcnt lgkmcnt(10)\n\t" \
  "v_mfma_f32_16x16x32_bf16 v[128:131], v[112:115], v[24:27], v[128:131]\n\t" \
  "v_add_u32 v163, 0x10000, v163\n\t" \
  "s_mov_b64 exec, vcc\n\t" \
  "global_load_dwordx4 v[12:15], v164, %[ob] offset:3072 sc1\n\t" \
  "s_mov_b64 exec, -1\n\t" \
  "v_mfma_f32_16x16x32_bf16 v[144:147], v[112:115], v[28:31], v[144:147]\n\t" \
  "ds_read_b64_tr_b16 v[108:109], v163 offset:0\n\t" \
  "ds_read_b64_tr_b16 v[110:111], v163 offset:2048\n\t" \
  "v_mfma_f32_16x16x32_bf16 v[132:135], v[116:119], v[24:27], v[132:135]\n\t" \
  "ds_read_b64_tr_b16 v[112:113], v160 offset:4096\n\t" \
  "s_and_b32 %[t0], vcc_lo, 0x4000\n\t" \
  "ds_read_b32 v166, v165\n\t" \
  "v_mfma_f32_16x16x32_bf16 v[148:151], v[116:119], v[28:31], v[148:151]\n\t" \
  "ds_read_b64_tr_b16 v[114:115], v160 offset:6144\n\t" \
  "v_add_u32 v164, %[t0], v164\n\t" \
  "s_waitcnt lgkmcnt(11)\n\t" \
  "v_mfma_f32_16x16x32_bf16 v[136:139], v[120:123], v[24:27], v[136:139]\n\t" \
  "ds_read_b64_tr_b16 v[116:117], v161 offset:4096\n\t" \
  "v_and_b32 v164, 0x3ffff, v164\n\t" \
  "v_mfma_f32_16x16x32_bf16 v[152:155], v[120:123], v[28:31], v[152:155]\n\t" \
  "ds_read_b64_tr_b16 v[118:119], v161 offset:6144\n\t" \
  "s_sub_u32 %[iss], %[iss], vcc_lo\n\t" \
  "v_mfma_f32_16x16x32_bf16 v[140:143], v[124:127], v[24:27], v[140:143]\n\t" \
  "ds_read_b64_tr_b16 v[120:121], v162 offset:4096\n\t" \
  "v_mfma_f32_16x16x32_bf16 v[156:159], v[124:127], v[28:31], v[156:159]\n\t" \
  "ds_read_b64_tr_b16 v[122:123], v162 offset:6144\n\t" \
  "s_add_u32 %[th], %[th], 1\n\t" \
  "s_cbranch_vccz QF8_%=\n\t" \
  "s_waitcnt vmcnt(16)\n\t" \
  "QG8_%=:\n\t" \
  "s_waitcnt lgkmcnt(11)\n\t" \
  "v_mfma_f32_16x16x32_bf16 v[128:131], v[96:99], v[32:35], v[128:131]\n\t" \
  "ds_read_b64_tr_b16 v[124:125], v163 offset:4096\n\t" \
  "v_mfma_f32_16x16x32_bf16 v[144:147], v[96:99], v[36:39], v[144:147]\n\t" \
  "ds_read_b64_tr_b16 v[126:127], v163 offset:6144\n\t" \
  "s_waitcnt lgkmcnt(7)\n\t" \
  "v_cmp_lt_u32 vcc, %[th], v166\n\t" \
  "v_mfma_f32_16x16x32_bf16 v[132:135], v[100:103], v[32:35], v[132:135]\n\t" \
  "s_mov_b64 exec, vcc\n\t" \
  "global_load_dwordx4 v[16:19], v164, %[ob] offset:0 sc1\n\t" \
  "s_mov_b64 exec, -1\n\t" \
  "v_mfma_f32_16x16x32_bf16 v[148:151], v[100:103], v[36:39], v[148:151]\n\t" \
  "ds_read_b64_tr_b16 v[96:97], v160 offset:8192\n\t" \
  "ds_read_b64_tr_b16 v[98:99], v160 offset:10240\n\t" \
  "v_mfma_f32_16x16x32_bf16 v[136:139], v[104:107], v[32:35], v[136:139]\n\t" \
  "s_mov_b64 exec, vcc\n\t" \
  "global_load_dwordx4 v[20:23], v164, %[ob] offset:1024 sc1\n\t" \
  "s_mov_b64 exec, -1\n\t" \
  "v_mfma_f32_16x16x32_bf16 v[152:155], v[104:107], v[36:39], v[152:155]\n\t" \
  "ds_read_b64_tr_b16 v[100:101], v161 offset:8192\n\t" \
  "ds_read_b64_tr_b16 v[102:103], v161 offset:10240\n\t" \
  "v_mfma_f32_16x16x32_bf16 v[140:143], v[108:111], v[32:35], v[140:143]\n\t" \
  "s_mov_b64 exec, vcc\n\t" \
  "global_load_dwordx4 v[24:27], v164, %[ob] offset:2048 sc1\n\t" \
  "s_mov_b64 exec, -1\n\t" \
  "v_mfma_f32_16x16x32_bf16 v[156:159], v[108:111], v[36:39], v[156:159]\n\t" \
  "ds_read_b64_tr_b16 v[104:105], v162 offset:8192\n\t" \
  "ds_read_b64_tr_b16 v[106:107], v162 offset:10240\n\t" \
  "s_waitcnt lgkmcnt(10)\n\t" \
  "v_mfma_f32_16x16x32_bf16 v[128:131], v[112:115], v[40:43], v[128:131]\n\t" \
  "s_mov_b64 exec, vcc\n\t" \
  "global_load_dwordx4 v[28:31], v164, %[ob] offset:3072 sc1\n\t" \
  "s_mov_b64 exec, -1\n\t" \
  "v_mfma_f32_16x16x32_bf16 v[144:147], v[112:115], v[44:47], v[144:147]\n\t" \
  "ds_read_b64_tr_b16 v[108:109], v163 offset:8192\n\t" \
  "ds_read_b64_tr_b16 v[110:111], v163 offset:10240\n\t" \
  "v_mfma_f32_16x16x32_bf16 v[132:135], v[116:119], v[40:43], v[132:135]\n\t" \
  "ds_read_b64_tr_b16 v[112:113], v160 offset:12288\n\t" \
  "s_and_b32 %[t0], vcc_lo, 0x4000\n\t" \
  "ds_read_b32 v166, v165\n\t" \
  "v_mfma_f32_16x16x32_bf16 v[148:151], v[116:119], v[44:47], v[148:151]\n\t" \
  "ds_read_b64_tr_b16 v[114:115], v160 offset:14336\n\t" \
  "v_add_u32 v164, %[t0], v164\n\t" \
  "s_waitcnt lgkmcnt(11)\n\t" \
  "v_mfma_f32_16x16x32_bf16 v[136:139], v[120:123], v[40:43], v[136:139]\n\t" \
  "ds_read_b64_tr_b16 v[116:117], v161 offset:12288\n\t" \
  "v_and_b32 v164, 0x3ffff, v164\n\t" \
  "v_mfma_f32_16x16x32_bf16 v[152:155], v[120:123], v[44:47], v[152:155]\n\t" \
  "ds_read_b64_tr_b16 v[118:119], v161 offset:14336\n\t" \
  "s_sub_u32 %[iss], %[iss], vcc_lo\n\t" \
  "v_mfma_f32_16x16x32_bf16 v[140:143], v[124:127], v[40:43], v[140:143]\n\t" \
  "ds_read_b64_tr_b16 v[120:121], v162 offset:12288\n\t" \
  "v_mfma_f32_16x16x32_bf16 v[156:159], v[124:127], v[44:47], v[156:159]\n\t" \
  "ds_read_b64_tr_b16 v[122:123], v162 offset:14336\n\t" \
  "s_add_u32 %[th], %[th], 1\n\t" \
  "s_cbranch_vccz QF9_%=\n\t" \
  "s_waitcnt vmcnt(16)\n\t" \
  "QG9_%=:\n\t" \
  "s_waitcnt lgkmcnt(11)\n\t" \
  "v_mfma_f32_16x16x32_bf16 v[128:131], v[96:99], v[48:51], v[128:131]\n\t" \
  "ds_read_b64_tr_b16 v[124:125], v163 offset:12288\n\t" \
  "v_mfma_f32_16x16x32_bf16 v[144:147], v[96:99], v[52:55], v[144:147]\n\t" \
  "ds_read_b64_tr_b16 v[126:127], v163 offset:14336\n\t" \
  "s_waitcnt lgkmcnt(7)\n\t" \
  "v_cmp_lt_u32 vcc, %[th], v166\n\t" \
  "v_mfma_f32_16x16x32_bf16 v[132:135], v[100:103], v[48:51], v[132:135]\n\t" \
  "s_mov_b64 exec, vcc\n\t" \
  "global_load_dwordx4 v[32:35], v164, %[ob] offset:0 sc1\n\t" \
  "s_mov_b64 exec, -1\n\t" \
  "v_mfma_f32_16x16x32_bf16 v[148:151], v[100:103], v[52:55], v[148:151]\n\t" \
  "ds_read_b64_tr_b16 v[96:97], v160 offset:16384\n\t" \
  "ds_read_b64_tr_b16 v[98:99], v160 offset:18432\n\t" \
  "v_mfma_f32_16x16x32_bf16 v[136:139], v[104:107], v[48:51], v[136:139]\n\t" \
  "s_mov_b64 exec, vcc\n\t" \
  "global_load_dwordx4 v[36:39], v164, %[ob] offset:1024 sc1\n\t" \
  "s_mov_b64 exec, -1\n\t" \
  "v_mfma_f32_16x16x32_bf16 v[152:155], v[104:107], v[52:55], v[152:155]\n\t" \
  "ds_read_b64_tr_b16 v[100:101], v161 offset:16384\n\t" \
  "ds_read_b64_tr_b16 v[102:103], v161 offset:18432\n\t" \
  "v_mfma_f32_16x16x32_bf16 v[140:143], v[108:111], v[48:51], v[140:143]\n\t" \
  "s_mov_b64 exec, vcc\n\t" \
  "global_load_dwordx4 v[40:43], v164, %[ob] offset:2048 sc1\n\t" \
  "s_mov_b64 exec, -1\n\t" \
  "v_mfma_f32_16x16x32_bf16 v[156:159], v[108:111], v[52:55], v[156:159]\n\t" \
  "ds_read_b64_tr_b16 v[104:105], v162 offset:16384\n\t" \
  "ds_read_b64_tr_b16 v[106:107], v162 offset:18432\n\t" \
  "s_waitcnt lgkmcnt(10)\n\t" \
  "v_mfma_f32_16x16x32_bf16 v[128:131], v[112:115], v[56:59], v[128:131]\n\t" \
  "s_mov_b64 exec, vcc\n\t" \
  "global_load_dwordx4 v[44:47], v164, %[ob] offset:3072 sc1\n\t" \
  "s_mov_b64 exec, -1\n\t" \
  "v_mfma_f32_16x16x32_bf16 v[144:147], v[112:115], v[60:63], v[144:147]\n\t" \
  "ds_read_b64_tr_b16 v[108:109], v163 offset:16384\n\t" \
  "ds_read_b64_tr_b16 v[110:111], v163 offset:18432\n\t" \
  "v_mfma_f32_16x16x32_bf16 v[132:135], v[116:119], v[56:59], v[132:135]\n\t" \
  "ds_read_b64_tr_b16 v[112:113], v160 offset:20480\n\t" \
  "s_and_b32 %[t0], vcc_lo, 0x4000\n\t" \
  "ds_read_b32 v166, v165\n\t" \
  "v_mfma_f32_16x16x32_bf16 v[148:151], v[116:119], v[60:63], v[148:151]\n\t" \
  "ds_read_b64_tr_b16 v[114:115], v160 offset:22528\n\t" \
  "v_add_u32 v164, %[t0], v164\n\t" \
  "s_waitcnt lgkmcnt(11)\n\t" \
  "v_mfma_f32_16x16x32_bf16 v[136:139], v[120:123], v[56:59], v[136:139]\n\t" \
  "ds_read_b64_tr_b16 v[116:117], v161 offset:20480\n\t" \
  "v_and_b32 v164, 0x3ffff, v164\n\t" \
  "v_mfma_f32_16x16x32_bf16 v[152:155], v[120:123], v[60:63], v[152:155]\n\t" \
  "ds_read_b64_tr_b16 v[118:119], v161 offset:22528\n\t" \
  "s_sub_u32 %[iss], %[iss], vcc_lo\n\t" \
  "v_mfma_f32_16x16x32_bf16 v[140:143], v[124:127], v[56:59], v[140:143]\n\t" \
  "ds_read_b64_tr_b16 v[120:121], v162 offset:20480\n\t" \
  "s_mov_b64 exec, 1\n\t" \
  "ds_write_b32 v165, v165 offset:76\n\t" \
  "s_mov_b64 exec, -1\n\t" \
  "v_mfma_f32_16x16x32_bf16 v[156:159], v[124:127], v[60:63], v[156:159]\n\t" \
  "ds_read_b64_tr_b16 v[122:123], v162 offset:22528\n\t" \
  "s_add_u32 %[th], %[th], 1\n\t" \
  "s_cbranch_vccz QF10_%=\n\t" \
  "s_waitcnt vmcnt(16)\n\t" \
  "QG10_%=:\n\t" \
  "s_waitcnt lgkmcnt(12)\n\t" \
  "v_mfma_f32_16x16x32_bf16 v[128:131], v[96:99], v[64:67], v[128:131]\n\t" \
  "ds_read_b64_tr_b16 v[124:125], v163 offset:20480\n\t" \
  "v_mfma_f32_16x16x32_bf16 v[144:147], v[96:99], v[68:71], v[144:147]\n\t" \
  "ds_read_b64_tr_b16 v[126:127], v163 offset:22528\n\t" \
  "s_waitcnt lgkmcnt(8)\n\t" \
  "v_cmp_lt_u32 vcc, %[th], v166\n\t" \
  "v_mfma_f32_16x16x32_bf16 v[132:135], v[100:103], v[64:67], v[132:135]\n\t" \
  "s_mov_b64 exec, vcc\n\t" \
  "global_load_dwordx4 v[48:51], v164, %[ob] offset:0 sc1\n\t" \
  "s_mov_b64 exec, -1\n\t" \
  "v_mfma_f32_16x16x32_bf16 v[148:151], v[100:103], v[68:71], v[148:151]\n\t" \
  "ds_read_b64_tr_b16 v[96:97], v160 offset:24576\n\t" \
  "ds_read_b64_tr_b16 v[98:99], v160 offset:26624\n\t" \
  "v_mfma_f32_16x16x32_bf16 v[136:139], v[104:107], v[64:67], v[136:139]\n\t" \
  "s_mov_b64 exec, vcc\n\t" \
  "global_load_dwordx4 v[52:55], v164, %[ob] offset:1024 sc1\n\t" \
  "s_mov_b64 exec, -1\n\t" \
  "v_mfma_f32_16x16x32_bf16 v[152:155], v[104:107], v[68:71], v[152:155]\n\t" \
  "ds_read_b64_tr_b16 v[100:101], v161 offset:24576\n\t" \
  "ds_read_b64_tr_b16 v[102:103], v161 offset:26624\n\t" \
  "v_mfma_f32_16x16x32_bf16 v[140:143], v[108:111], v[64:67], v[140:143]\n\t" \
  "s_mov_b64 exec, vcc\n\t" \
  "global_load_dwordx4 v[56:59], v164, %[ob] offset:2048 sc1\n\t" \
  "s_mov_b64 exec, -1\n\t" \
  "v_mfma_f32_16x16x32_bf16 v[156:159], v[108:111], v[68:71], v[156:159]\n\t" \
  "ds_read_b64_tr_b16 v[104:105], v162 offset:24576\n\t" \
  "ds_read_b64_tr_b16 v[106:107], v162 offset:26624\n\t" \
  "s_waitcnt lgkmcnt(11)\n\t" \
  "v_mfma_f32_16x16x32_bf16 v[128:131], v[112:115], v[72:75], v[128:131]\n\t" \
  "s_mov_b64 exec, vcc\n\t" \
  "global_load_dwordx4 v[60:63], v164, %[ob] offset:3072 sc1\n\t" \
  "s_mov_b64 exec, -1\n\t" \
  "v_mfma_f32_16x16x32_bf16 v[144:147], v[112:115], v[76:79], v[144:147]\n\t" \
  "ds_read_b64_tr_b16 v[108:109], v163 offset:24576\n\t" \
  "ds_read_b64_tr_b16 v[110:111], v163 offset:26624\n\t" \
  "v_mfma_f32_16x16x32_bf16 v[132:135], v[116:119], v[72:75], v[132:135]\n\t" \
  "ds_read_b64_tr_b16 v[112:113], v160 offset:28672\n\t" \
  "s_and_b32 %[t0], vcc_lo, 0x4000\n\t" \
  "v_mfma_f32_16x16x32_bf16 v[148:151], v[116:119], v[76:79], v[148:151]\n\t" \
  "ds_read_b64_tr_b16 v[114:115], v160 offset:30720\n\t" \
  "v_add_u32 v164, %[t0], v164\n\t" \
  "s_waitcnt lgkmcnt(10)\n\t" \
  "v_mfma_f32_16x16x32_bf16 v[136:139], v[120:123], v[72:75], v[136:139]\n\t" \
  "ds_read_b64_tr_b16 v[116:117], v161 offset:28672\n\t" \
  "v_and_b32 v164, 0x3ffff, v164\n\t" \
  "v_mfma_f32_16x16x32_bf16 v[152:155], v[120:123], v[76:79], v[152:155]\n\t" \
  "ds_read_b64_tr_b16 v[118:119], v161 offset:30720\n\t" \
  "s_sub_u32 %[iss], %[iss], vcc_lo\n\t" \
  "v_mfma_f32_16x16x32_bf16 v[140:143], v[124:127], v[72:75], v[140:143]\n\t" \
  "ds_read_b64_tr_b16 v[120:121], v162 offset:28672\n\t" \
  "v_mfma_f32_16x16x32_bf16 v[156:159], v[124:127], v[76:79], v[156:159]\n\t" \
  "ds_read_b64_tr_b16 v[122:123], v162 offset:30720\n\t" \
  "s_add_u32 %[th], %[th], 1\n\t" \
  "s_cbranch_vccz QF11_%=\n\t" \
  "s_waitcnt vmcnt(16)\n\t" \
  "QG11_%=:\n\t" \
  "s_waitcnt lgkmcnt(10)\n\t" \
  "v_mfma_f32_16x16x32_bf16 v[128:131], v[96:99], v[80:83], v[128:131]\n\t" \
  "ds_read_b64_tr_b16 v[124:125], v163 offset:28672\n\t" \
  "v_mfma_f32_16x16x32_bf16 v[144:147], v[96:99], v[84:87], v[144:147]\n\t" \
  "ds_read_b64_tr_b16 v[126:127], v163 offset:30720\n\t" \
  "v_mfma_f32_16x16x32_bf16 v[132:135], v[100:103], v[80:83], v[132:135]\n\t" \
  "ds_read_b64_tr_b16 v[96:97], v160 offset:32768\n\t" \
  "v_mfma_f32_16x16x32_bf16 v[148:151], v[100:103], v[84:87], v[148:151]\n\t" \
  "ds_read_b64_tr_b16 v[98:99], v160 offset:34816\n\t" \
  "s_waitcnt lgkmcnt(10)\n\t" \
  "v_mfma_f32_16x16x32_bf16 v[136:139], v[104:107], v[80:83], v[136:139]\n\t" \
  "ds_read_b64_tr_b16 v[100:101], v161 offset:32768\n\t" \
  "v_mfma_f32_16x16x32_bf16 v[152:155], v[104:107], v[84:87], v[152:155]\n\t" \
  "ds_read_b64_tr_b16 v[102:103], v161 offset:34816\n\t" \
  "v_mfma_f32_16x16x32_bf16 v[140:143], v[108:111], v[80:83], v[140:143]\n\t" \
  "ds_read_b64_tr_b16 v[104:105], v162 offset:32768\n\t" \
  "v_mfma_f32_16x16x32_bf16 v[156:159], v[108:111], v[84:87], v[156:159]\n\t" \
  "ds_read_b64_tr_b16 v[106:107], v162 offset:34816\n\t" \
  "s_waitcnt lgkmcnt(10)\n\t" \
  "v_mfma_f32_16x16x32_bf16 v[128:131], v[112:115], v[88:91], v[128:131]\n\t" \
  "ds_read_b64_tr_b16 v[108:109], v163 offset:32768\n\t" \
  "v_mfma_f32_16x16x32_bf16 v[144:147], v[112:115], v[92:95], v[144:147]\n\t" \
  "ds_read_b64_tr_b16 v[110:111], v163 offset:34816\n\t" \
  "v_mfma_f32_16x16x32_bf16 v[132:135], v[116:119], v[88:91], v[132:135]\n\t" \
  "ds_read_b64_tr_b16 v[112:113], v160 offset:36864\n\t" \
  "v_mfma_f32_16x16x32_bf16 v[148:151], v[116:119], v[92:95], v[148:151]\n\t" \
  "ds_read_b64_tr_b16 v[114:115], v160 offset:38912\n\t" \
  "s_waitcnt lgkmcnt(10)\n\t" \
  "v_mfma_f32_16x16x32_bf16 v[136:139], v[120:123], v[88:91], v[136:139]\n\t" \
  "ds_read_b64_tr_b16 v[116:117], v161 offset:36864\n\t" \
  "v_mfma_f32_16x16x32_bf16 v[152:155], v[120:123], v[92:95], v[152:155]\n\t" \
  "ds_read_b64_tr_b16 v[118:119], v161 offset:38912\n\t" \
  "v_mfma_f32_16x16x32_bf16 v[140:143], v[124:127], v[88:91], v[140:143]\n\t" \
  "ds_read_b64_tr_b16 v[120:121], v162 offset:36864\n\t" \
  "v_mfma_f32_16x16x32_bf16 v[156:159], v[124:127], v[92:95], v[156:159]\n\t" \
  "ds_read_b64_tr_b16 v[122:123], v162 offset:38912\n\t" \
  "s_cbranch_vccz QF12_%=\n\t" \
  "s_waitcnt vmcnt(12)\n\t" \
  "QG12_%=:\n\t" \
  "s_waitcnt lgkmcnt(10)\n\t" \
  "v_mfma_f32_16x16x32_bf16 v[128:131], v[96:99], v[0:3], v[128:131]\n\t" \
  "ds_read_b64_tr_b16 v[124:125], v163 offset:36864\n\t" \
  "v_mfma_f32_16x16x32_bf16 v[144:147], v[96:99], v[4:7], v[144:147]\n\t" \
  "ds_read_b64_tr_b16 v[126:127], v163 offset:38912\n\t" \
  "v_mfma_f32_16x16x32_bf16 v[132:135], v[100:103], v[0:3], v[132:135]\n\t" \
  "ds_read_b64_tr_b16 v[96:97], v160 offset:40960\n\t" \
  "v_mfma_f32_16x16x32_bf16 v[148:151], v[100:103], v[4:7], v[148:151]\n\t" \
  "ds_read_b64_tr_b16 v[98:99], v160 offset:43008\n\t" \
  "s_waitcnt lgkmcnt(10)\n\t" \
  "v_mfma_f32_16x16x32_bf16 v[136:139], v[104:107], v[0:3], v[136:139]\n\t" \
  "ds_read_b64_tr_b16 v[100:101], v161 offset:40960\n\t" \
  "v_mfma_f32_16x16x32_bf16 v[152:155], v[104:107], v[4:7], v[152:155]\n\t" \
  "ds_read_b64_tr_b16 v[102:103], v161 offset:43008\n\t" \
  "v_mfma_f32_16x16x32_bf16 v[140:143], v[108:111], v[0:3], v[140:143]\n\t" \
  "ds_read_b64_tr_b16 v[104:105], v162 offset:40960\n\t" \
  "v_mfma_f32_16x16x32_bf16 v[156:159], v[108:111], v[4:7], v[156:159]\n\t" \
  "ds_read_b64_tr_b16 v[106:107], v162 offset:43008\n\t" \
  "s_waitcnt lgkmcnt(10)\n\t" \
  "v_mfma_f32_16x16x32_bf16 v[128:131], v[112:115], v[8:11], v[128:131]\n\t" \
  "ds_read_b64_tr_b16 v[108:109], v163 offset:40960\n\t" \
  "v_mfma_f32_16x16x32_bf16 v[144:147], v[112:115], v[12:15], v[144:147]\n\t" \
  "ds_read_b64_tr_b16 v[110:111], v163 offset:43008\n\t" \
  "v_mfma_f32_16x16x32_bf16 v[132:135], v[116:119], v[8:11], v[132:135]\n\t" \
  "ds_read_b64_tr_b16 v[112:113], v160 offset:45056\n\t" \
  "v_mfma_f32_16x16x32_bf16 v[148:151], v[116:119], v[12:15], v[148:151]\n\t" \
  "ds_read_b64_tr_b16 v[114:115], v160 offset:47104\n\t" \
  "s_waitcnt lgkmcnt(10)\n\t" \
  "v_mfma_f32_16x16x32_bf16 v[136:139], v[120:123], v[8:11], v[136:139]\n\t" \
  "ds_read_b64_tr_b16 v[116:117], v161 offset:45056\n\t" \
  "v_mfma_f32_16x16x32_bf16 v[152:155], v[120:123], v[12:15], v[152:155]\n\t" \
  "ds_read_b64_tr_b16 v[118:119], v161 offset:47104\n\t" \
  "v_mfma_f32_16x16x32_bf16 v[140:143], v[124:127], v[8:11], v[140:143]\n\t" \
  "ds_read_b64_tr_b16 v[120:121], v162 offset:45056\n\t" \
  "v_mfma_f32_16x16x32_bf16 v[156:159], v[124:127], v[12:15], v[156:159]\n\t" \
  "ds_read_b64_tr_b16 v[122:123], v162 offset:47104\n\t" \
  "s_cbranch_vccz QF13_%=\n\t" \
  "s_waitcnt vmcnt(8)\n\t" \
  "QG13_%=:\n\t" \
  "s_waitcnt lgkmcnt(10)\n\t" \
  "v_mfma_f32_16x16x32_bf16 v[128:131], v[96:99], v[16:19], v[128:131]\n\t" \
  "ds_read_b64_tr_b16 v[124:125], v163 offset:45056\n\t" \
  "v_mfma_f32_16x16x32_bf16 v[144:147], v[96:99], v[20:23], v[144:147]\n\t" \
  "ds_read_b64_tr_b16 v[126:127], v163 offset:47104\n\t" \
  "v_mfma_f32_16x16x32_bf16 v[132:135], v[100:103], v[16:19], v[132:135]\n\t" \
  "ds_read_b64_tr_b16 v[96:97], v160 offset:49152\n\t" \
  "v_mfma_f32_16x16x32_bf16 v[148:151], v[100:103], v[20:23], v[148:151]\n\t" \
  "ds_read_b64_tr_b16 v[98:99], v160 offset:51200\n\t" \
  "s_waitcnt lgkmcnt(10)\n\t" \
  "v_mfma_f32_16x16x32_bf16 v[136:139], v[104:107], v[16:19], v[136:139]\n\t" \
  "ds_read_b64_tr_b16 v[100:101], v161 offset:49152\n\t" \
  "v_mfma_f32_16x16x32_bf16 v[152:155], v[104:107], v[20:23], v[152:155]\n\t" \
  "ds_read_b64_tr_b16 v[102:103], v161 offset:51200\n\t" \
  "v_mfma_f32_16x16x32_bf16 v[140:143], v[108:111], v[16:19], v[140:143]\n\t" \
  "ds_read_b64_tr_b16 v[104:105], v162 offset:49152\n\t" \
  "v_mfma_f32_16x16x32_bf16 v[156:159], v[108:111], v[20:23], v[156:159]\n\t" \
  "ds_read_b64_tr_b16 v[106:107], v162 offset:51200\n\t" \
  "s_waitcnt lgkmcnt(10)\n\t" \
  "v_mfma_f32_16x16x32_bf16 v[128:131], v[112:115], v[24:27], v[128:131]\n\t" \
  "ds_read_b64_tr_b16 v[108:109], v163 offset:49152\n\t" \
  "v_mfma_f32_16x16x32_bf16 v[144:147], v[112:115], v[28:31], v[144:147]\n\t" \
  "ds_read_b64_tr_b16 v[110:111], v163 offset:51200\n\t" \
  "v_mfma_f32_16x16x32_bf16 v[132:135], v[116:119], v[24:27], v[132:135]\n\t" \
  "ds_read_b64_tr_b16 v[112:113], v160 offset:53248\n\t" \
  "v_mfma_f32_16x16x32_bf16 v[148:151], v[116:119], v[28:31], v[148:151]\n\t" \
  "ds_read_b64_tr_b16 v[114:115], v160 offset:55296\n\t" \
  "s_waitcnt lgkmcnt(10)\n\t" \
  "v_mfma_f32_16x16x32_bf16 v[136:139], v[120:123], v[24:27], v[136:139]\n\t" \
  "ds_read_b64_tr_b16 v[116:117], v161 offset:53248\n\t" \
  "v_mfma_f32_16x16x32_bf16 v[152:155], v[120:123], v[28:31], v[152:155]\n\t" \
  "ds_read_b64_tr_b16 v[118:119], v161 offset:55296\n\t" \
  "v_mfma_f32_16x16x32_bf16 v[140:143], v[124:127], v[24:27], v[140:143]\n\t" \
  "ds_read_b64_tr_b16 v[120:121], v162 offset:53248\n\t" \
  "v_mfma_f32_16x16x32_bf16 v[156:159], v[124:127], v[28:31], v[156:159]\n\t" \
  "ds_read_b64_tr_b16 v[122:123], v162 offset:55296\n\t" \
  "s_cbranch_vccz QF14_%=\n\t" \
  "s_waitcnt vmcnt(4)\n\t" \
  "QG14_%=:\n\t" \
  "s_waitcnt lgkmcnt(10)\n\t" \
  "v_mfma_f32_16x16x32_bf16 v[128:131], v[96:99], v[32:35], v[128:131]\n\t" \
  "ds_read_b64_tr_b16 v[124:125], v163 offset:53248\n\t" \
  "v_mfma_f32_16x16x32_bf16 v[144:147], v[96:99], v[36:39], v[144:147]\n\t" \
  "ds_read_b64_tr_b16 v[126:127], v163 offset:55296\n\t" \
  "v_mfma_f32_16x16x32_bf16 v[132:135], v[100:103], v[32:35], v[132:135]\n\t" \
  "ds_read_b64_tr_b16 v[96:97], v160 offset:57344\n\t" \
  "v_mfma_f32_16x16x32_bf16 v[148:151], v[100:103], v[36:39], v[148:151]\n\t" \
  "ds_read_b64_tr_b16 v[98:99], v160 offset:59392\n\t" \
  "s_waitcnt lgkmcnt(10)\n\t" \
  "v_mfma_f32_16x16x32_bf16 v[136:139], v[104:107], v[32:35], v[136:139]\n\t" \
  "ds_read_b64_tr_b16 v[100:101], v161 offset:57344\n\t" \
  "v_mfma_f32_16x16x32_bf16 v[152:155], v[104:107], v[36:39], v[152:155]\n\t" \
  "ds_read_b64_tr_b16 v[102:103], v161 offset:59392\n\t" \
  "v_mfma_f32_16x16x32_bf16 v[140:143], v[108:111], v[32:35], v[140:143]\n\t" \
  "ds_read_b64_tr_b16 v[104:105], v162 offset:57344\n\t" \
  "v_mfma_f32_16x16x32_bf16 v[156:159], v[108:111], v[36:39], v[156:159]\n\t" \
  "ds_read_b64_tr_b16 v[106:107], v162 offset:59392\n\t" \
  "s_waitcnt lgkmcnt(10)\n\t" \
  "v_mfma_f32_16x16x32_bf16 v[128:131], v[112:115], v[40:43], v[128:131]\n\t" \
  "ds_read_b64_tr_b16 v[108:109], v163 offset:57344\n\t" \
  "v_mfma_f32_16x16x32_bf16 v[144:147], v[112:115], v[44:47], v[144:147]\n\t" \
  "ds_read_b64_tr_b16 v[110:111], v163 offset:59392\n\t" \
  "v_mfma_f32_16x16x32_bf16 v[132:135], v[116:119], v[40:43], v[132:135]\n\t" \
  "ds_read_b64_tr_b16 v[112:113], v160 offset:61440\n\t" \
  "v_mfma_f32_16x16x32_bf16 v[148:151], v[116:119], v[44:47], v[148:151]\n\t" \
  "ds_read_b64_tr_b16 v[114:115], v160 offset:63488\n\t" \
  "s_waitcnt lgkmcnt(10)\n\t" \
  "v_mfma_f32_16x16x32_bf16 v[136:139], v[120:123], v[40:43], v[136:139]\n\t" \
  "ds_read_b64_tr_b16 v[116:117], v161 offset:61440\n\t" \
  "v_mfma_f32_16x16x32_bf16 v[152:155], v[120:123], v[44:47], v[152:155]\n\t" \
  "ds_read_b64_tr_b16 v[118:119], v161 offset:63488\n\t" \
  "v_mfma_f32_16x16x32_bf16 v[140:143], v[124:127], v[40:43], v[140:143]\n\t" \
  "ds_read_b64_tr_b16 v[120:121], v162 offset:61440\n\t" \
  "v_mfma_f32_16x16x32_bf16 v[156:159], v[124:127], v[44:47], v[156:159]\n\t" \
  "ds_read_b64_tr_b16 v[122:123], v162 offset:63488\n\t" \
  "s_cbranch_vccz QF15_%=\n\t" \
  "s_waitcnt vmcnt(0)\n\t" \
  "QG15_%=:\n\t" \
  "s_waitcnt lgkmcnt(10)\n\t" \
  "v_mfma_f32_16x16x32_bf16 v[128:131], v[96:99], v[48:51], v[128:131]\n\t" \
  "ds_read_b64_tr_b16 v[124:125], v163 offset:61440\n\t" \
  "v_mfma_f32_16x16x32_bf16 v[144:147], v[96:99], v[52:55], v[144:147]\n\t" \
  "ds_read_b64_tr_b16 v[126:127], v163 offset:63488\n\t" \
  "v_mfma_f32_16x16x32_bf16 v[132:135], v[100:103], v[48:51], v[132:135]\n\t" \
  "v_mfma_f32_16x16x32_bf16 v[148:151], v[100:103], v[52:55], v[148:151]\n\t" \
  "s_waitcnt lgkmcnt(8)\n\t" \
  "v_mfma_f32_16x16x32_bf16 v[136:139], v[104:107], v[48:51], v[136:139]\n\t" \
  "v_mfma_f32_16x16x32_bf16 v[152:155], v[104:107], v[52:55], v[152:155]\n\t" \
  "v_mfma_f32_16x16x32_bf16 v[140:143], v[108:111], v[48:51], v[140:143]\n\t" \
  "v_mfma_f32_16x16x32_bf16 v[156:159], v[108:111], v[52:55], v[156:159]\n\t" \
  "s_waitcnt lgkmcnt(4)\n\t" \
  "v_mfma_f32_16x16x32_bf16 v[128:131], v[112:115], v[56:59], v[128:131]\n\t" \
  "v_mfma_f32_16x16x32_bf16 v[144:147], v[112:115], v[60:63], v[144:147]\n\t" \
  "v_mfma_f32_16x16x32_bf16 v[132:135], v[116:119], v[56:59], v[132:135]\n\t" \
  "v_mfma_f32_16x16x32_bf16 v[148:151], v[116:119], v[60:63], v[148:151]\n\t" \
  "s_waitcnt lgkmcnt(0)\n\t" \
  "v_mfma_f32_16x16x32_bf16 v[136:139], v[120:123], v[56:59], v[136:139]\n\t" \
  "v_mfma_f32_16x16x32_bf16 v[152:155], v[120:123], v[60:63], v[152:155]\n\t" \
  "v_mfma_f32_16x16x32_bf16 v[140:143], v[124:127], v[56:59], v[140:143]\n\t" \
  "v_mfma_f32_16x16x32_bf16 v[156:159], v[124:127], v[60:63], v[156:159]\n\t" \
  "s_branch QX_%=\n\t" \
  "QF1_%=:\n\t" \
  "s_mov_b32 %[c], 1\n\t" \
  "s_getpc_b64 s[92:93]\n\t" \
  "s_branch QC_%=\n\t" \
  "s_branch QG1_%=\n\t" \
  "QF2_%=:\n\t" \
  "s_mov_b32 %[c], 2\n\t" \
  "s_getpc_b64 s[92:93]\n\t" \
  "s_branch QC_%=\n\t" \
  "s_branch QG2_%=\n\t" \
  "QF3_%=:\n\t" \
  "s_mov_b32 %[c], 3\n\t" \
  "s_getpc_b64 s[92:93]\n\t" \
  "s_branch QC_%=\n\t" \
  "s_branch QG3_%=\n\t" \
  "QF4_%=:\n\t" \
  "s_mov_b32 %[c], 4\n\t" \
  "s_getpc_b64 s[92:93]\n\t" \
  "s_branch QC_%=\n\t" \
  "s_branch QG4_%=\n\t" \
  "QF5_%=:\n\t" \
  "s_mov_b32 %[c], 5\n\t" \
  "s_getpc_b64 s[92:93]\n\t" \
  "s_branch QC_%=\n\t" \
  "s_branch QG5_%=\n\t" \
  "QF6_%=:\n\t" \
  "s_mov_b32 %[c], 6\n\t" \
  "s_getpc_b64 s[92:93]\n\t" \
  "s_branch QC_%=\n\t" \
  "s_branch QG6_%=\n\t" \
  "QF7_%=:\n\t" \
  "s_mov_b32 %[c], 7\n\t" \
  "s_getpc_b64 s[92:93]\n\t" \
  "s_branch QC_%=\n\t" \
  "s_branch QG7_%=\n\t" \
  "QF8_%=:\n\t" \
  "s_mov_b32 %[c], 8\n\t" \
  "s_getpc_b64 s[92:93]\n\t" \
  "s_branch QC_%=\n\t" \
  "s_branch QG8_%=\n\t" \
  "QF9_%=:\n\t" \
  "s_mov_b32 %[c], 9\n\t" \
  "s_getpc_b64 s[92:93]\n\t" \
  "s_branch QC_%=\n\t" \
  "s_branch QG9_%=\n\t" \
  "QF10_%=:\n\t" \
  "s_mov_b32 %[c], 10\n\t" \
  "s_getpc_b64 s[92:93]\n\t" \
  "s_branch QC_%=\n\t" \
  "s_branch QG10_%=\n\t" \
  "QF11_%=:\n\t" \
  "s_mov_b32 %[c], 11\n\t" \
  "s_getpc_b64 s[92:93]\n\t" \
  "s_branch QC_%=\n\t" \
  "s_branch QG11_%=\n\t" \
  "QF12_%=:\n\t" \
  "s_mov_b32 %[c], 12\n\t" \
  "s_getpc_b64 s[92:93]\n\t" \
  "s_branch QC_%=\n\t" \
  "s_branch QG12_%=\n\t" \
  "QF13_%=:\n\t" \
  "s_mov_b32 %[c], 13\n\t" \
  "s_getpc_b64 s[92:93]\n\t" \
  "s_branch QC_%=\n\t" \
  "s_branch QG13_%=\n\t" \
  "QF14_%=:\n\t" \
  "s_mov_b32 %[c], 14\n\t" \
  "s_getpc_b64 s[92:93]\n\t" \
  "s_branch QC_%=\n\t" \
  "s_branch QG14_%=\n\t" \
  "QF15_%=:\n\t" \
  "s_mov_b32 %[c], 15\n\t" \
  "s_getpc_b64 s[92:93]\n\t" \
  "s_branch QC_%=\n\t" \
  "s_branch QG15_%=\n\t" \
  "QC_%=:\n\t" \
  "s_add_u32 s92, s92, 4\n\t" \
  "s_addc_u32 s93, s93, 0\n\t" \
  "QU_%=:\n\t" \
  "ds_read_b32 v166, v165\n\t" \
  "s_add_u32 %[t1], %[c], 5\n\t" \
  "s_waitcnt lgkmcnt(0)\n\t" \
  "v_readfirstlane_b32 %[t0], v166\n\t" \
  "s_nop 0\n\t" \
  "s_min_u32 %[t1], %[t1], %[t0]\n\t" \
  "QL_%=:\n\t" \
  "s_cmp_ge_u32 %[iss], %[t1]\n\t" \
  "s_cbranch_scc1 QD_%=\n\t" \
  "s_mov_b32 %[t0], %[iss]\n\t" \
  "s_sub_u32 %[th], %[t0], 6\n\t" \
  "s_cselect_b32 %[t0], %[t0], %[th]\n\t" \
  "s_sub_u32 %[th], %[t0], 6\n\t" \
  "s_cselect_b32 %[t0], %[t0], %[th]\n\t" \
  "s_cmp_eq_u32 %[t0], 1\n\t" \
  "s_cbranch_scc1 QS1_%=\n\t" \
  "s_cmp_eq_u32 %[t0], 2\n\t" \
  "s_cbranch_scc1 QS2_%=\n\t" \
  "s_cmp_eq_u32 %[t0], 3\n\t" \
  "s_cbranch_scc1 QS3_%=\n\t" \
  "s_cmp_eq_u32 %[t0], 4\n\t" \
  "s_cbranch_scc1 QS4_%=\n\t" \
  "s_cmp_eq_u32 %[t0], 5\n\t" \
  "s_cbranch_scc1 QS5_%=\n\t" \
  "QS0_%=:\n\t" \
  "global_load_dwordx4 v[0:3], v164, %[ob] offset:0 sc1\n\t" \
  "global_load_dwordx4 v[4:7], v164, %[ob] offset:1024 sc1\n\t" \
  "global_load_dwordx4 v[8:11], v164, %[ob] offset:2048 sc1\n\t" \
  "global_load_dwordx4 v[12:15], v164, %[ob] offset:3072 sc1\n\t" \
  "v_add_u32 v164, 0x4000, v164\n\t" \
  "v_and_b32 v164, 0x3ffff, v164\n\t" \
  "s_add_u32 %[iss], %[iss], 1\n\t" \
  "s_cmp_ge_u32 %[iss], %[t1]\n\t" \
  "s_cbranch_scc1 QD_%=\n\t" \
  "QS1_%=:\n\t" \
  "global_load_dwordx4 v[16:19], v164, %[ob] offset:0 sc1\n\t" \
  "global_load_dwordx4 v[20:23], v164, %[ob] offset:1024 sc1\n\t" \
  "global_load_dwordx4 v[24:27], v164, %[ob] offset:2048 sc1\n\t" \
  "global_load_dwordx4 v[28:31], v164, %[ob] offset:3072 sc1\n\t" \
  "v_add_u32 v164, 0x4000, v164\n\t" \
  "v_and_b32 v164, 0x3ffff, v164\n\t" \
  "s_add_u32 %[iss], %[iss], 1\n\t" \
  "s_cmp_ge_u32 %[iss], %[t1]\n\t" \
  "s_cbranch_scc1 QD_%=\n\t" \
  "QS2_%=:\n\t" \
  "global_load_dwordx4 v[32:35], v164, %[ob] offset:0 sc1\n\t" \
  "global_load_dwordx4 v[36:39], v164, %[ob] offset:1024 sc1\n\t" \
  "global_load_dwordx4 v[40:43], v164, %[ob] offset:2048 sc1\n\t" \
  "global_load_dwordx4 v[44:47], v164, %[ob] offset:3072 sc1\n\t" \
  "v_add_u32 v164, 0x4000, v164\n\t" \
  "v_and_b32 v164, 0x3ffff, v164\n\t" \
  "s_add_u32 %[iss], %[iss], 1\n\t" \
  "s_cmp_ge_u32 %[iss], %[t1]\n\t" \
  "s_cbranch_scc1 QD_%=\n\t" \
  "QS3_%=:\n\t" \
  "global_load_dwordx4 v[48:51], v164, %[ob] offset:0 sc1\n\t" \
  "global_load_dwordx4 v[52:55], v164, %[ob] offset:1024 sc1\n\t" \
  "global_load_dwordx4 v[56:59], v164, %[ob] offset:2048 sc1\n\t" \
  "global_load_dwordx4 v[60:63], v164, %[ob] offset:3072 sc1\n\t" \
  "v_add_u32 v164, 0x4000, v164\n\t" \
  "v_and_b32 v164, 0x3ffff, v164\n\t" \
  "s_add_u32 %[iss], %[iss], 1\n\t" \
  "s_cmp_ge_u32 %[iss], %[t1]\n\t" \
  "s_cbranch_scc1 QD_%=\n\t" \
  "QS4_%=:\n\t" \
  "global_load_dwordx4 v[64:67], v164, %[ob] offset:0 sc1\n\t" \
  "global_load_dwordx4 v[68:71], v164, %[ob] offset:1024 sc1\n\t" \
  "global_load_dwordx4 v[72:75], v164, %[ob] offset:2048 sc1\n\t" \
  "global_load_dwordx4 v[76:79], v164, %[ob] offset:3072 sc1\n\t" \
  "v_add_u32 v164, 0x4000, v164\n\t" \
  "v_and_b32 v164, 0x3ffff, v164\n\t" \
  "s_add_u32 %[iss], %[iss], 1\n\t" \
  "s_cmp_ge_u32 %[iss], %[t1]\n\t" \
  "s_cbranch_scc1 QD_%=\n\t" \
  "QS5_%=:\n\t" \
  "global_load_dwordx4 v[80:83], v164, %[ob] offset:0 sc1\n\t" \
  "global_load_dwordx4 v[84:87], v164, %[ob] offset:1024 sc1\n\t" \
  "global_load_dwordx4 v[88:91], v164, %[ob] offset:2048 sc1\n\t" \
  "global_load_dwordx4 v[92:95], v164, %[ob] offset:3072 sc1\n\t" \
  "v_add_u32 v164, 0x4000, v164\n\t" \
  "v_and_b32 v164, 0x3ffff, v164\n\t" \
  "s_add_u32 %[iss], %[iss], 1\n\t" \
  "s_cmp_ge_u32 %[iss], %[t1]\n\t" \
  "s_cbranch_scc0 QS0_%=\n\t" \
  "QD_%=:\n\t" \
  "s_cmp_gt_u32 %[iss], %[c]\n\t" \
  "s_cbranch_scc1 QW_%=\n\t" \
  "s_addk_i32 %[cn], 1\n\t" \
  "s_cmp_gt_u32 %[cn], 0x400000\n\t" \
  "s_cbranch_scc1 QT_%=\n\t" \
  "s_sleep 1\n\t" \
  "s_branch QU_%=\n\t" \
  "QT_%=:\n\t" \
  "s_add_u32 %[t1], %[c], 1\n\t" \
  "s_branch QL_%=\n\t" \
  "QW_%=:\n\t" \
  "s_cmp_eq_u32 %[iss], 16\n\t" \
  "s_cselect_b64 vcc, -1, 0\n\t" \
  "s_sub_u32 %[t0], %[iss], %[c]\n\t" \
  "s_cmp_eq_u32 %[t0], 5\n\t" \
  "s_cselect_b32 %[th], %[iss], 99\n\t" \
  "s_waitcnt vmcnt(16)\n\t" \
  "s_cmp_ge_u32 %[t0], 5\n\t" \
  "s_cbranch_scc1 QR_%=\n\t" \
  "s_waitcnt vmcnt(12)\n\t" \
  "s_cmp_ge_u32 %[t0], 4\n\t" \
  "s_cbranch_scc1 QR_%=\n\t" \
  "s_waitcnt vmcnt(8)\n\t" \
  "s_cmp_ge_u32 %[t0], 3\n\t" \
  "s_cbranch_scc1 QR_%=\n\t" \
  "s_waitcnt vmcnt(4)\n\t" \
  "s_cmp_ge_u32 %[t0], 2\n\t" \
  "s_cbranch_scc1 QR_%=\n\t" \
  "s_waitcnt vmcnt(0)\n\t" \
  "QR_%=:\n\t" \
  "s_setpc_b64 s[92:93]\n\t" \
  "QX_%=:\n\t" \
  "s_nop 7\n\t"
#define G3_CLOB "v0", "v1", "v2", "v3", "v4", "v5", "v6", "v7", "v8", "v9", "v10", "v11", "v12", "v13", "v14", "v15", "v16", "v17", "v18", "v19", "v20", "v21", "v22", "v23", "v24", "v25", "v26", "v27", "v28", "v29", "v30", "v31", "v32", "v33", "v34", "v35", "v36", "v37", "v38", "v39", "v40", "v41", "v42", "v43", "v44", "v45", "v46", "v47", "v48", "v49", "v50", "v51", "v52", "v53", "v54", "v55", "v56", "v57", "v58", "v59", "v60", "v61", "v62", "v63", "v64", "v65", "v66", "v67", "v68", "v69", "v70", "v71", "v72", "v73", "v74", "v75", "v76", "v77", "v78", "v79", "v80", "v81", "v82", "v83", "v84", "v85", "v86", "v87", "v88", "v89", "v90", "v91", "v92", "v93", "v94", "v95", "v96", "v97", "v98", "v99", "v100", "v101", "v102", "v103", "v104", "v105", "v106", "v107", "v108", "v109", "v110", "v111", "v112", "v113", "v114", "v115", "v116", "v117", "v118", "v119", "v120", "v121", "v122", "v123", "v124", "v125", "v126", "v127", "v166", "s88", "s89", "s92", "s93"

#define PP_ASM \
  "v_mov_b32 v60, 0\n\t" \
  "s_mov_b32 %[t0], %[n0]\n\t" \
  "s_cmp_eq_u32 %[t0], 0\n\t" \
  "s_cbranch_scc1 PAx_%=\n\t" \
  "PA_%=:\n\t" \
  "s_barrier\n\t" \
  "s_sub_u32 %[t0], %[t0], 1\n\t" \
  "s_cmp_lg_u32 %[t0], 0\n\t" \
  "s_cbranch_scc1 PA_%=\n\t" \
  "PAx_%=:\n\t" \
  "s_mov_b32 m0, %[tsk]\n\t" \
  "s_mov_b64 exec, %[tm]\n\t" \
  "global_load_lds_dword v164, %[tp]\n\t" \
  "s_mov_b64 exec, -1\n\t" \
  "s_mov_b32 m0, %[shd]\n\t" \
  "global_load_dword v160, v60, %[fl] sc1\n\t" \
  "global_load_lds_dwordx4 v163, %[shp]\n\t" \
  "s_mov_b32 m0, %[tgd]\n\t" \
  "s_mov_b64 exec, %[em]\n\t" \
  "global_load_lds_dword v60, %[fl] sc1\n\t" \
  "s_mov_b64 exec, -1\n\t" \
  "global_load_ushort v161, v60, %[hp]\n\t" \
  "global_load_ushort v162, v60, %[dp]\n\t" \
  "global_load_dwordx4 v[0:3], v152, %[kb0]\n\t" \
  "global_load_dwordx4 v[4:7], v153, %[kb0]\n\t" \
  "global_load_dwordx4 v[8:11], v154, %[kb0]\n\t" \
  "global_load_dwordx4 v[12:15], v155, %[kb3] offset:0\n\t" \
  "global_load_dwordx4 v[28:31], v155, %[vb0] offset:0\n\t" \
  "global_load_dwordx4 v[16:19], v155, %[kb3] offset:2048\n\t" \
  "global_load_dwordx4 v[32:35], v155, %[vb0] offset:2048\n\t" \
  "global_load_dwordx4 v[20:23], v155, %[kb5] offset:0\n\t" \
  "global_load_dwordx4 v[36:39], v155, %[vb2] offset:0\n\t" \
  "global_load_dwordx4 v[24:27], v155, %[kb5] offset:2048\n\t" \
  "global_load_dwordx4 v[40:43], v155, %[vb2] offset:2048\n\t" \
  "s_setprio 2\n\t" \
  "s_barrier\n\t" \
  "s_barrier\n\t" \
  "s_waitcnt vmcnt(11)\n\t" \
  "ds_read_b128 v[128:131], v156 offset:0\n\t" \
  "ds_read_b128 v[132:135], v156 offset:16\n\t" \
  "ds_read_b128 v[136:139], v156 offset:32\n\t" \
  "ds_read_b128 v[140:143], v156 offset:48\n\t" \
  "s_waitcnt lgkmcnt(0)\n\t" \
  "v_lshlrev_b32 v64, 16, v128\n\t" \
  "v_and_b32 v65, 0xffff0000, v128\n\t" \
  "v_lshlrev_b32 v66, 16, v129\n\t" \
  "v_and_b32 v67, 0xffff0000, v129\n\t" \
  "v_lshlrev_b32 v68, 16, v130\n\t" \
  "v_and_b32 v69, 0xffff0000, v130\n\t" \
  "v_lshlrev_b32 v70, 16, v131\n\t" \
  "v_and_b32 v71, 0xffff0000, v131\n\t" \
  "v_lshlrev_b32 v72, 16, v132\n\t" \
  "v_and_b32 v73, 0xffff0000, v132\n\t" \
  "v_lshlrev_b32 v74, 16, v133\n\t" \
  "v_and_b32 v75, 0xffff0000, v133\n\t" \
  "v_lshlrev_b32 v76, 16, v134\n\t" \
  "v_and_b32 v77, 0xffff0000, v134\n\t" \
  "v_lshlrev_b32 v78, 16, v135\n\t" \
  "v_and_b32 v79, 0xffff0000, v135\n\t" \
  "v_lshlrev_b32 v80, 16, v136\n\t" \
  "v_and_b32 v81, 0xffff0000, v136\n\t" \
  "v_lshlrev_b32 v82, 16, v137\n\t" \
  "v_and_b32 v83, 0xffff0000, v137\n\t" \
  "v_lshlrev_b32 v84, 16, v138\n\t" \
  "v_and_b32 v85, 0xffff0000, v138\n\t" \
  "v_lshlrev_b32 v86, 16, v139\n\t" \
  "v_and_b32 v87, 0xffff0000, v139\n\t" \
  "v_lshlrev_b32 v88, 16, v140\n\t" \
  "v_and_b32 v89, 0xffff0000, v140\n\t" \
  "v_lshlrev_b32 v90, 16, v141\n\t" \
  "v_and_b32 v91, 0xffff0000, v141\n\t" \
  "v_lshlrev_b32 v92, 16, v142\n\t" \
  "v_and_b32 v93, 0xffff0000, v142\n\t" \
  "v_lshlrev_b32 v94, 16, v143\n\t" \
  "v_and_b32 v95, 0xffff0000, v143\n\t" \
  "s_waitcnt vmcnt(10)\n\t" \
  "s_memrealtime %[st1]\n\t" \
  "s_mov_b64 exec, %[zm]\n\t" \
  "v_mov_b32 v0, 0\n\t" \
  "v_mov_b32 v1, 0\n\t" \
  "v_mov_b32 v2, 0\n\t" \
  "v_mov_b32 v3, 0\n\t" \
  "s_mov_b64 exec, -1\n\t" \
  "v_lshlrev_b32 v128, 16, v0\n\t" \
  "v_and_b32 v129, 0xffff0000, v0\n\t" \
  "s_barrier\n\t" \
  "v_lshlrev_b32 v130, 16, v1\n\t" \
  "v_and_b32 v131, 0xffff0000, v1\n\t" \
  "v_lshlrev_b32 v132, 16, v2\n\t" \
  "v_and_b32 v133, 0xffff0000, v2\n\t" \
  "v_lshlrev_b32 v134, 16, v3\n\t" \
  "v_and_b32 v135, 0xffff0000, v3\n\t" \
  "v_mul_f32 v96, v128, v67\n\t" \
  "v_mul_f32 v97, v129, v71\n\t" \
  "v_mul_f32 v98, v130, v75\n\t" \
  "v_mul_f32 v99, v131, v79\n\t" \
  "v_mul_f32 v100, v132, v83\n\t" \
  "v_mul_f32 v101, v133, v87\n\t" \
  "v_mul_f32 v102, v134, v91\n\t" \
  "v_mul_f32 v103, v135, v95\n\t" \
  "s_waitcnt vmcnt(9)\n\t" \
  "s_mov_b64 exec, %[zm]\n\t" \
  "v_mov_b32 v4, 0\n\t" \
  "v_mov_b32 v5, 0\n\t" \
  "v_mov_b32 v6, 0\n\t" \
  "v_mov_b32 v7, 0\n\t" \
  "s_mov_b64 exec, -1\n\t" \
  "v_lshlrev_b32 v128, 16, v4\n\t" \
  "v_and_b32 v129, 0xffff0000, v4\n\t" \
  "v_lshlrev_b32 v130, 16, v5\n\t" \
  "v_and_b32 v131, 0xffff0000, v5\n\t" \
  "v_lshlrev_b32 v132, 16, v6\n\t" \
  "v_and_b32 v133, 0xffff0000, v6\n\t" \
  "v_lshlrev_b32 v134, 16, v7\n\t" \
  "v_and_b32 v135, 0xffff0000, v7\n\t" \
  "v_fmac_f32 v96, v128, v66\n\t" \
  "v_fmac_f32 v97, v129, v70\n\t" \
  "v_fmac_f32 v98, v130, v74\n\t" \
  "v_fmac_f32 v99, v131, v78\n\t" \
  "v_fmac_f32 v100, v132, v82\n\t" \
  "v_fmac_f32 v101, v133, v86\n\t" \
  "v_fmac_f32 v102, v134, v90\n\t" \
  "v_fmac_f32 v103, v135, v94\n\t" \
  "v_mul_f32 v104, v128, v67\n\t" \
  "v_mul_f32 v105, v129, v71\n\t" \
  "v_mul_f32 v106, v130, v75\n\t" \
  "v_mul_f32 v107, v131, v79\n\t" \
  "v_mul_f32 v108, v132, v83\n\t" \
  "v_mul_f32 v109, v133, v87\n\t" \
  "v_mul_f32 v110, v134, v91\n\t" \
  "v_mul_f32 v111, v135, v95\n\t" \
  "s_waitcnt vmcnt(8)\n\t" \
  "s_mov_b64 exec, %[zm]\n\t" \
  "v_mov_b32 v8, 0\n\t" \
  "v_mov_b32 v9, 0\n\t" \
  "v_mov_b32 v10, 0\n\t" \
  "v_mov_b32 v11, 0\n\t" \
  "s_mov_b64 exec, -1\n\t" \
  "s_setprio 3\n\t" \
  "s_barrier\n\t" \
  "s_mov_b32 m0, %[hd]\n\t" \
  "s_nop 0\n\t" \
  "global_load_lds_dwordx4 v163, %[hp] offset:0\n\t" \
  "global_load_lds_dwordx4 v163, %[hp] offset:1024\n\t" \
  "global_load_lds_dwordx4 v163, %[hp] offset:2048\n\t" \
  "global_load_lds_dwordx4 v163, %[hp] offset:3072\n\t" \
  "global_load_dwordx4 v[44:47], v155, %[qb0] offset:0\n\t" \
  "global_load_dwordx4 v[48:51], v155, %[qb0] offset:2048\n\t" \
  "global_load_dwordx4 v[52:55], v155, %[qb2] offset:0\n\t" \
  "global_load_dwordx4 v[56:59], v155, %[qb2] offset:2048\n\t" \
  "s_setprio 2\n\t" \
  "v_lshlrev_b32 v128, 16, v8\n\t" \
  "v_and_b32 v129, 0xffff0000, v8\n\t" \
  "v_lshlrev_b32 v130, 16, v9\n\t" \
  "v_and_b32 v131, 0xffff0000, v9\n\t" \
  "v_lshlrev_b32 v132, 16, v10\n\t" \
  "v_and_b32 v133, 0xffff0000, v10\n\t" \
  "v_lshlrev_b32 v134, 16, v11\n\t" \
  "v_and_b32 v135, 0xffff0000, v11\n\t" \
  "v_fmac_f32 v96, v128, v65\n\t" \
  "v_fmac_f32 v97, v129, v69\n\t" \
  "v_fmac_f32 v98, v130, v73\n\t" \
  "v_fmac_f32 v99, v131, v77\n\t" \
  "v_fmac_f32 v100, v132, v81\n\t" \
  "v_fmac_f32 v101, v133, v85\n\t" \
  "v_fmac_f32 v102, v134, v89\n\t" \
  "v_fmac_f32 v103, v135, v93\n\t" \
  "v_fmac_f32 v104, v128, v66\n\t" \
  "v_fmac_f32 v105, v129, v70\n\t" \
  "v_fmac_f32 v106, v130, v74\n\t" \
  "v_fmac_f32 v107, v131, v78\n\t" \
  "v_fmac_f32 v108, v132, v82\n\t" \
  "v_fmac_f32 v109, v133, v86\n\t" \
  "v_fmac_f32 v110, v134, v90\n\t" \
  "v_fmac_f32 v111, v135, v94\n\t" \
  "v_mul_f32 v112, v128, v67\n\t" \
  "v_mul_f32 v113, v129, v71\n\t" \
  "v_mul_f32 v114, v130, v75\n\t" \
  "v_mul_f32 v115, v131, v79\n\t" \
  "v_mul_f32 v116, v132, v83\n\t" \
  "v_mul_f32 v117, v133, v87\n\t" \
  "v_mul_f32 v118, v134, v91\n\t" \
  "v_mul_f32 v119, v135, v95\n\t" \
  "s_waitcnt vmcnt(15)\n\t" \
  "v_lshlrev_b32 v128, 16, v12\n\t" \
  "v_and_b32 v129, 0xffff0000, v12\n\t" \
  "v_lshlrev_b32 v130, 16, v13\n\t" \
  "v_and_b32 v131, 0xffff0000, v13\n\t" \
  "v_lshlrev_b32 v132, 16, v14\n\t" \
  "v_and_b32 v133, 0xffff0000, v14\n\t" \
  "v_lshlrev_b32 v134, 16, v15\n\t" \
  "v_and_b32 v135, 0xffff0000, v15\n\t" \
  "s_barrier\n\t" \
  "v_fmac_f32 v96, v128, v64\n\t" \
  "v_fmac_f32 v97, v129, v68\n\t" \
  "v_fmac_f32 v98, v130, v72\n\t" \
  "v_fmac_f32 v99, v131, v76\n\t" \
  "v_fmac_f32 v100, v132, v80\n\t" \
  "v_fmac_f32 v101, v133, v84\n\t" \
  "v_fmac_f32 v102, v134, v88\n\t" \
  "v_fmac_f32 v103, v135, v92\n\t" \
  "v_fmac_f32 v104, v128, v65\n\t" \
  "v_fmac_f32 v105, v129, v69\n\t" \
  "v_fmac_f32 v106, v130, v73\n\t" \
  "v_fmac_f32 v107, v131, v77\n\t" \
  "v_fmac_f32 v108, v132, v81\n\t" \
  "v_fmac_f32 v109, v133, v85\n\t" \
  "v_fmac_f32 v110, v134, v89\n\t" \
  "v_fmac_f32 v111, v135, v93\n\t" \
  "v_fmac_f32 v112, v128, v66\n\t" \
  "v_fmac_f32 v113, v129, v70\n\t" \
  "v_fmac_f32 v114, v130, v74\n\t" \
  "v_fmac_f32 v115, v131, v78\n\t" \
  "v_fmac_f32 v116, v132, v82\n\t" \
  "v_fmac_f32 v117, v133, v86\n\t" \
  "v_fmac_f32 v118, v134, v90\n\t" \
  "v_fmac_f32 v119, v135, v94\n\t" \
  "v_mul_f32 v120, v128, v67\n\t" \
  "v_mul_f32 v121, v129, v71\n\t" \
  "v_mul_f32 v122, v130, v75\n\t" \
  "v_mul_f32 v123, v131, v79\n\t" \
  "v_mul_f32 v124, v132, v83\n\t" \
  "v_mul_f32 v125, v133, v87\n\t" \
  "v_mul_f32 v126, v134, v91\n\t" \
  "v_mul_f32 v127, v135, v95\n\t" \
  "s_waitcnt vmcnt(14)\n\t" \
  "v_lshlrev_b32 v128, 16, v28\n\t" \
  "v_and_b32 v129, 0xffff0000, v28\n\t" \
  "v_lshlrev_b32 v130, 16, v29\n\t" \
  "v_and_b32 v131, 0xffff0000, v29\n\t" \
  "v_lshlrev_b32 v132, 16, v30\n\t" \
  "v_and_b32 v133, 0xffff0000, v30\n\t" \
  "v_lshlrev_b32 v134, 16, v31\n\t" \
  "v_and_b32 v135, 0xffff0000, v31\n\t" \
  "s_barrier\n\t" \
  "v_mul_f32 v96, v96, v128\n\t" \
  "v_mul_f32 v97, v97, v129\n\t" \
  "v_mul_f32 v98, v98, v130\n\t" \
  "v_mul_f32 v99, v99, v131\n\t" \
  "v_mul_f32 v100, v100, v132\n\t" \
  "v_mul_f32 v101, v101, v133\n\t" \
  "v_mul_f32 v102, v102, v134\n\t" \
  "v_mul_f32 v103, v103, v135\n\t" \
  "v_cvt_pk_bf16_f32 v136, v96, v97\n\t" \
  "v_cvt_pk_bf16_f32 v137, v98, v99\n\t" \
  "v_cvt_pk_bf16_f32 v138, v100, v101\n\t" \
  "v_cvt_pk_bf16_f32 v139, v102, v103\n\t" \
  "v_mov_b32_dpp v128, v136 row_ror:8 row_mask:0xf bank_mask:0xf\n\t" \
  "v_mov_b32_dpp v129, v137 row_ror:8 row_mask:0xf bank_mask:0xf\n\t" \
  "v_mov_b32_dpp v130, v138 row_ror:8 row_mask:0xf bank_mask:0xf\n\t" \
  "v_mov_b32_dpp v131, v139 row_ror:8 row_mask:0xf bank_mask:0xf\n\t" \
  "v_perm_b32 v136, v128, v136, v157\n\t" \
  "v_perm_b32 v137, v129, v137, v157\n\t" \
  "v_perm_b32 v138, v130, v138, v157\n\t" \
  "v_perm_b32 v139, v131, v139, v157\n\t" \
  "v_permlane16_swap_b32 v136, v137\n\t" \
  "s_nop 0\n\t" \
  "v_permlane16_swap_b32 v138, v139\n\t" \
  "s_nop 1\n\t" \
  "v_permlane32_swap_b32 v136, v138\n\t" \
  "v_permlane32_swap_b32 v137, v139\n\t" \
  "s_waitcnt vmcnt(13)\n\t" \
  "v_lshlrev_b32 v128, 16, v16\n\t" \
  "v_and_b32 v129, 0xffff0000, v16\n\t" \
  "v_lshlrev_b32 v130, 16, v17\n\t" \
  "v_and_b32 v131, 0xffff0000, v17\n\t" \
  "v_lshlrev_b32 v132, 16, v18\n\t" \
  "v_and_b32 v133, 0xffff0000, v18\n\t" \
  "v_lshlrev_b32 v134, 16, v19\n\t" \
  "v_and_b32 v135, 0xffff0000, v19\n\t" \
  "v_fmac_f32 v104, v128, v64\n\t" \
  "v_fmac_f32 v105, v129, v68\n\t" \
  "v_fmac_f32 v106, v130, v72\n\t" \
  "v_fmac_f32 v107, v131, v76\n\t" \
  "v_fmac_f32 v108, v132, v80\n\t" \
  "v_fmac_f32 v109, v133, v84\n\t" \
  "s_setprio 3\n\t" \
  "s_barrier\n\t" \
  "global_store_dwordx4 v159, v[136:139], %[sb] sc1\n\t" \
  "v_add_u32 v158, 0x80, v159\n\t" \
  "v_bfi_b32 v159, %[mk], v158, v159\n\t" \
  "s_setprio 2\n\t" \
  "v_fmac_f32 v110, v134, v88\n\t" \
  "v_fmac_f32 v111, v135, v92\n\t" \
  "v_fmac_f32 v112, v128, v65\n\t" \
  "v_fmac_f32 v113, v129, v69\n\t" \
  "v_fmac_f32 v114, v130, v73\n\t" \
  "v_fmac_f32 v115, v131, v77\n\t" \
  "v_fmac_f32 v116, v132, v81\n\t" \
  "v_fmac_f32 v117, v133, v85\n\t" \
  "v_fmac_f32 v118, v134, v89\n\t" \
  "v_fmac_f32 v119, v135, v93\n\t" \
  "v_fmac_f32 v120, v128, v66\n\t" \
  "v_fmac_f32 v121, v129, v70\n\t" \
  "v_fmac_f32 v122, v130, v74\n\t" \
  "v_fmac_f32 v123, v131, v78\n\t" \
  "v_fmac_f32 v124, v132, v82\n\t" \
  "v_fmac_f32 v125, v133, v86\n\t" \
  "v_fmac_f32 v126, v134, v90\n\t" \
  "v_fmac_f32 v127, v135, v94\n\t" \
  "s_waitcnt vmcnt(13)\n\t" \
  "v_lshlrev_b32 v128, 16, v32\n\t" \
  "v_and_b32 v129, 0xffff0000, v32\n\t" \
  "v_lshlrev_b32 v130, 16, v33\n\t" \
  "v_and_b32 v131, 0xffff0000, v33\n\t" \
  "v_lshlrev_b32 v132, 16, v34\n\t" \
  "v_and_b32 v133, 0xffff0000, v34\n\t" \
  "v_lshlrev_b32 v134, 16, v35\n\t" \
  "v_and_b32 v135, 0xffff0000, v35\n\t" \
  "v_mul_f32 v104, v104, v128\n\t" \
  "v_mul_f32 v105, v105, v129\n\t" \
  "v_mul_f32 v106, v106, v130\n\t" \
  "v_mul_f32 v107, v107, v131\n\t" \
  "v_mul_f32 v108, v108, v132\n\t" \
  "v_mul_f32 v109, v109, v133\n\t" \
  "v_mul_f32 v110, v110, v134\n\t" \
  "v_mul_f32 v111, v111, v135\n\t" \
  "v_cvt_pk_bf16_f32 v140, v104, v105\n\t" \
  "v_cvt_pk_bf16_f32 v141, v106, v107\n\t" \
  "v_cvt_pk_bf16_f32 v142, v108, v109\n\t" \
  "v_cvt_pk_bf16_f32 v143, v110, v111\n\t" \
  "v_mov_b32_dpp v128, v140 row_ror:8 row_mask:0xf bank_mask:0xf\n\t" \
  "v_mov_b32_dpp v129, v141 row_ror:8 row_mask:0xf bank_mask:0xf\n\t" \
  "s_barrier\n\t" \
  "v_mov_b32_dpp v130, v142 row_ror:8 row_mask:0xf bank_mask:0xf\n\t" \
  "v_mov_b32_dpp v131, v143 row_ror:8 row_mask:0xf bank_mask:0xf\n\t" \
  "v_perm_b32 v140, v128, v140, v157\n\t" \
  "v_perm_b32 v141, v129, v141, v157\n\t" \
  "v_perm_b32 v142, v130, v142, v157\n\t" \
  "v_perm_b32 v143, v131, v143, v157\n\t" \
  "v_permlane16_swap_b32 v140, v141\n\t" \
  "s_nop 0\n\t" \
  "v_permlane16_swap_b32 v142, v143\n\t" \
  "s_nop 1\n\t" \
  "v_permlane32_swap_b32 v140, v142\n\t" \
  "v_permlane32_swap_b32 v141, v143\n\t" \
  "s_waitcnt vmcnt(12)\n\t" \
  "v_lshlrev_b32 v128, 16, v20\n\t" \
  "v_and_b32 v129, 0xffff0000, v20\n\t" \
  "v_lshlrev_b32 v130, 16, v21\n\t" \
  "v_and_b32 v131, 0xffff0000, v21\n\t" \
  "v_lshlrev_b32 v132, 16, v22\n\t" \
  "v_and_b32 v133, 0xffff0000, v22\n\t" \
  "v_lshlrev_b32 v134, 16, v23\n\t" \
  "v_and_b32 v135, 0xffff0000, v23\n\t" \
  "v_fmac_f32 v112, v128, v64\n\t" \
  "v_fmac_f32 v113, v129, v68\n\t" \
  "v_fmac_f32 v114, v130, v72\n\t" \
  "v_fmac_f32 v115, v131, v76\n\t" \
  "v_fmac_f32 v116, v132, v80\n\t" \
  "v_fmac_f32 v117, v133, v84\n\t" \
  "v_fmac_f32 v118, v134, v88\n\t" \
  "v_fmac_f32 v119, v135, v92\n\t" \
  "v_fmac_f32 v120, v128, v65\n\t" \
  "v_fmac_f32 v121, v129, v69\n\t" \
  "v_fmac_f32 v122, v130, v73\n\t" \
  "v_fmac_f32 v123, v131, v77\n\t" \
  "v_fmac_f32 v124, v132, v81\n\t" \
  "v_fmac_f32 v125, v133, v85\n\t" \
  "v_fmac_f32 v126, v134, v89\n\t" \
  "v_fmac_f32 v127, v135, v93\n\t" \
  "s_waitcnt vmcnt(11)\n\t" \
  "v_lshlrev_b32 v128, 16, v36\n\t" \
  "v_and_b32 v129, 0xffff0000, v36\n\t" \
  "v_lshlrev_b32 v130, 16, v37\n\t" \
  "v_and_b32 v131, 0xffff0000, v37\n\t" \
  "s_setprio 3\n\t" \
  "s_barrier\n\t" \
  "global_store_dwordx4 v159, v[140:143], %[sb] sc1\n\t" \
  "v_add_u32 v158, 0x80, v159\n\t" \
  "v_bfi_b32 v159, %[mk], v158, v159\n\t" \
  "s_setprio 2\n\t" \
  "v_lshlrev_b32 v132, 16, v38\n\t" \
  "v_and_b32 v133, 0xffff0000, v38\n\t" \
  "v_lshlrev_b32 v134, 16, v39\n\t" \
  "v_and_b32 v135, 0xffff0000, v39\n\t" \
  "v_mul_f32 v112, v112, v128\n\t" \
  "v_mul_f32 v113, v113, v129\n\t" \
  "v_mul_f32 v114, v114, v130\n\t" \
  "v_mul_f32 v115, v115, v131\n\t" \
  "v_mul_f32 v116, v116, v132\n\t" \
  "v_mul_f32 v117, v117, v133\n\t" \
  "v_mul_f32 v118, v118, v134\n\t" \
  "v_mul_f32 v119, v119, v135\n\t" \
  "v_cvt_pk_bf16_f32 v144, v112, v113\n\t" \
  "v_cvt_pk_bf16_f32 v145, v114, v115\n\t" \
  "v_cvt_pk_bf16_f32 v146, v116, v117\n\t" \
  "v_cvt_pk_bf16_f32 v147, v118, v119\n\t" \
  "v_mov_b32_dpp v128, v144 row_ror:8 row_mask:0xf bank_mask:0xf\n\t" \
  "v_mov_b32_dpp v129, v145 row_ror:8 row_mask:0xf bank_mask:0xf\n\t" \
  "v_mov_b32_dpp v130, v146 row_ror:8 row_mask:0xf bank_mask:0xf\n\t" \
  "v_mov_b32_dpp v131, v147 row_ror:8 row_mask:0xf bank_mask:0xf\n\t" \
  "v_perm_b32 v144, v128, v144, v157\n\t" \
  "v_perm_b32 v145, v129, v145, v157\n\t" \
  "v_perm_b32 v146, v130, v146, v157\n\t" \
  "v_perm_b32 v147, v131, v147, v157\n\t" \
  "v_permlane16_swap_b32 v144, v145\n\t" \
  "s_nop 0\n\t" \
  "v_permlane16_swap_b32 v146, v147\n\t" \
  "s_nop 1\n\t" \
  "v_permlane32_swap_b32 v144, v146\n\t" \
  "v_permlane32_swap_b32 v145, v147\n\t" \
  "s_waitcnt vmcnt(11)\n\t" \
  "v_lshlrev_b32 v128, 16, v24\n\t" \
  "v_and_b32 v129, 0xffff0000, v24\n\t" \
  "v_lshlrev_b32 v130, 16, v25\n\t" \
  "v_and_b32 v131, 0xffff0000, v25\n\t" \
  "v_lshlrev_b32 v132, 16, v26\n\t" \
  "v_and_b32 v133, 0xffff0000, v26\n\t" \
  "v_lshlrev_b32 v134, 16, v27\n\t" \
  "v_and_b32 v135, 0xffff0000, v27\n\t" \
  "v_fmac_f32 v120, v128, v64\n\t" \
  "v_fmac_f32 v121, v129, v68\n\t" \
  "s_setprio 3\n\t" \
  "s_barrier\n\t" \
  "global_store_dwordx4 v159, v[144:147], %[sb] sc1\n\t" \
  "v_add_u32 v158, 0x80, v159\n\t" \
  "v_bfi_b32 v159, %[mk], v158, v159\n\t" \
  "s_setprio 2\n\t" \
  "v_fmac_f32 v122, v130, v72\n\t" \
  "v_fmac_f32 v123, v131, v76\n\t" \
  "v_fmac_f32 v124, v132, v80\n\t" \
  "v_fmac_f32 v125, v133, v84\n\t" \
  "v_fmac_f32 v126, v134, v88\n\t" \
  "v_fmac_f32 v127, v135, v92\n\t" \
  "s_waitcnt vmcnt(11)\n\t" \
  "v_lshlrev_b32 v128, 16, v40\n\t" \
  "v_and_b32 v129, 0xffff0000, v40\n\t" \
  "v_lshlrev_b32 v130, 16, v41\n\t" \
  "v_and_b32 v131, 0xffff0000, v41\n\t" \
  "v_lshlrev_b32 v132, 16, v42\n\t" \
  "v_and_b32 v133, 0xffff0000, v42\n\t" \
  "v_lshlrev_b32 v134, 16, v43\n\t" \
  "v_and_b32 v135, 0xffff0000, v43\n\t" \
  "v_mul_f32 v120, v120, v128\n\t" \
  "v_mul_f32 v121, v121, v129\n\t" \
  "v_mul_f32 v122, v122, v130\n\t" \
  "v_mul_f32 v123, v123, v131\n\t" \
  "v_mul_f32 v124, v124, v132\n\t" \
  "v_mul_f32 v125, v125, v133\n\t" \
  "v_mul_f32 v126, v126, v134\n\t" \
  "v_mul_f32 v127, v127, v135\n\t" \
  "v_cvt_pk_bf16_f32 v148, v120, v121\n\t" \
  "v_cvt_pk_bf16_f32 v149, v122, v123\n\t" \
  "v_cvt_pk_bf16_f32 v150, v124, v125\n\t" \
  "v_cvt_pk_bf16_f32 v151, v126, v127\n\t" \
  "v_mov_b32_dpp v128, v148 row_ror:8 row_mask:0xf bank_mask:0xf\n\t" \
  "v_mov_b32_dpp v129, v149 row_ror:8 row_mask:0xf bank_mask:0xf\n\t" \
  "v_mov_b32_dpp v130, v150 row_ror:8 row_mask:0xf bank_mask:0xf\n\t" \
  "v_mov_b32_dpp v131, v151 row_ror:8 row_mask:0xf bank_mask:0xf\n\t" \
  "v_perm_b32 v148, v128, v148, v157\n\t" \
  "v_perm_b32 v149, v129, v149, v157\n\t" \
  "v_perm_b32 v150, v130, v150, v157\n\t" \
  "v_perm_b32 v151, v131, v151, v157\n\t" \
  "v_permlane16_swap_b32 v148, v149\n\t" \
  "s_nop 0\n\t" \
  "v_permlane16_swap_b32 v150, v151\n\t" \
  "s_nop 1\n\t" \
  "v_permlane32_swap_b32 v148, v150\n\t" \
  "v_permlane32_swap_b32 v149, v151\n\t" \
  "s_setprio 3\n\t" \
  "s_barrier\n\t" \
  "global_store_dwordx4 v159, v[148:151], %[sb] sc1\n\t" \
  "s_setprio 2\n\t" \
  "s_waitcnt vmcnt(8)\n\t" \
  "ds_read_b64_tr_b16 v[64:65], v165 offset:32\n\t" \
  "ds_read_b64_tr_b16 v[66:67], v165 offset:0\n\t" \
  "ds_read_b64_tr_b16 v[68:69], v165 offset:288\n\t" \
  "ds_read_b64_tr_b16 v[70:71], v165 offset:256\n\t" \
  "ds_read_b64_tr_b16 v[72:73], v165 offset:544\n\t" \
  "ds_read_b64_tr_b16 v[74:75], v165 offset:512\n\t" \
  "ds_read_b64_tr_b16 v[76:77], v165 offset:800\n\t" \
  "ds_read_b64_tr_b16 v[78:79], v165 offset:768\n\t" \
  "v_mov_b32 v24, v166\n\t" \
  "v_mov_b32 v25, v167\n\t" \
  "v_mov_b32 v63, 0xc0\n\t" \
  "v_add_u32 v158, 64, v24\n\t" \
  "v_bfi_b32 v26, v63, v158, v24\n\t" \
  "v_add_u32 v158, 64, v25\n\t" \
  "v_bfi_b32 v27, v63, v158, v25\n\t" \
  "v_add_u32 v158, 64, v26\n\t" \
  "v_bfi_b32 v28, v63, v158, v26\n\t" \
  "v_add_u32 v158, 64, v27\n\t" \
  "v_bfi_b32 v29, v63, v158, v27\n\t" \
  "v_add_u32 v158, 64, v28\n\t" \
  "v_bfi_b32 v30, v63, v158, v28\n\t" \
  "v_add_u32 v158, 64, v29\n\t" \
  "v_bfi_b32 v31, v63, v158, v29\n\t" \
  "s_barrier\n\t" \
  "ds_read_b64_tr_b16 v[80:81], v165 offset:1056\n\t" \
  "ds_read_b64_tr_b16 v[82:83], v165 offset:1024\n\t" \
  "ds_read_b64_tr_b16 v[84:85], v165 offset:1312\n\t" \
  "ds_read_b64_tr_b16 v[86:87], v165 offset:1280\n\t" \
  "ds_read_b64_tr_b16 v[88:89], v165 offset:1568\n\t" \
  "ds_read_b64_tr_b16 v[90:91], v165 offset:1536\n\t" \
  "ds_read_b64_tr_b16 v[92:93], v165 offset:1824\n\t" \
  "ds_read_b64_tr_b16 v[94:95], v165 offset:1792\n\t" \
  "s_setprio 3\n\t" \
  "s_barrier\n\t" \
  "s_waitcnt vmcnt(0)\n\t" \
  "v_add_u32 v61, 1, v160\n\t" \
  "s_mov_b64 exec, 1\n\t" \
  "global_store_dword v60, v61, %[ff] sc1\n\t" \
  "s_mov_b64 exec, -1\n\t" \
  "s_memrealtime %[st2]\n\t" \
  "s_setprio 2\n\t" \
  "ds_read_b64_tr_b16 v[96:97], v165 offset:2080\n\t" \
  "ds_read_b64_tr_b16 v[98:99], v165 offset:2048\n\t" \
  "ds_read_b64_tr_b16 v[100:101], v165 offset:2336\n\t" \
  "ds_read_b64_tr_b16 v[102:103], v165 offset:2304\n\t" \
  "ds_read_b64_tr_b16 v[104:105], v165 offset:2592\n\t" \
  "ds_read_b64_tr_b16 v[106:107], v165 offset:2560\n\t" \
  "ds_read_b64_tr_b16 v[108:109], v165 offset:2848\n\t" \
  "ds_read_b64_tr_b16 v[110:111], v165 offset:2816\n\t" \
  "s_barrier\n\t" \
  "ds_read_b64_tr_b16 v[112:113], v165 offset:3104\n\t" \
  "ds_read_b64_tr_b16 v[114:115], v165 offset:3072\n\t" \
  "ds_read_b64_tr_b16 v[116:117], v165 offset:3360\n\t" \
  "ds_read_b64_tr_b16 v[118:119], v165 offset:3328\n\t" \
  "ds_read_b64_tr_b16 v[120:121], v165 offset:3616\n\t" \
  "ds_read_b64_tr_b16 v[122:123], v165 offset:3584\n\t" \
  "ds_read_b64_tr_b16 v[124:125], v165 offset:3872\n\t" \
  "ds_read_b64_tr_b16 v[126:127], v165 offset:3840\n\t" \
  "s_barrier\n\t" \
  "s_waitcnt lgkmcnt(0)\n\t" \
  "v_lshlrev_b32 v61, 16, v161\n\t" \
  "v_lshlrev_b32 v62, 16, v162\n\t" \
  "v_add_f32 v61, v61, v62\n\t" \
  "v_mov_b32 v62, 0xffff\n\t" \
  "v_cvt_pk_bf16_f32 v61, v61, v61\n\t" \
  "s_mov_b64 exec, 1\n\t" \
  "v_bfi_b32 v66, v62, v61, v66\n\t" \
  "s_mov_b64 exec, 15\n\t" \
  "v_and_b32 v66, v62, v66\n\t" \
  "s_mov_b64 exec, 16\n\t" \
  "v_bfi_b32 v66, v62, v66, v61\n\t" \
  "s_mov_b64 exec, 0xff\n\t" \
  "v_mov_b32 v67, 0\n\t" \
  "s_mov_b64 exec, 0x100\n\t" \
  "v_mov_b32 v67, v61\n\t" \
  "s_mov_b64 exec, 0xf00\n\t" \
  "v_and_b32 v67, v62, v67\n\t" \
  "s_mov_b64 exec, 0x1000\n\t" \
  "v_bfi_b32 v67, v62, v67, v61\n\t" \
  "s_mov_b64 exec, -1\n\t" \
  "s_waitcnt lgkmcnt(0)\n\t"
#define PP_NB 15
#define PP_Q 1
#define PP_CLOB "v0", "v1", "v2", "v3", "v4", "v5", "v6", "v7", "v8", "v9", "v10", "v11", "v12", "v13", "v14", "v15", "v16", "v17", "v18", "v19", "v20", "v21", "v22", "v23", "v32", "v33", "v34", "v35", "v36", "v37", "v38", "v39", "v40", "v41", "v42", "v43", "v60", "v61", "v62", "v63", "v128", "v129", "v130", "v131", "v132", "v133", "v134", "v135", "v136", "v137", "v138", "v139", "v140", "v141", "v142", "v143", "v144", "v145", "v146", "v147", "v148", "v149", "v150", "v151", "v158", "v161", "v162"

#define LD_ASM \
  "s_add_u32 s68, %[rw0], 0\n\t" \
  "s_add_u32 s69, %[rx0], 0\n\t" \
  "s_add_u32 s70, %[rx0], 147456\n\t" \
  "s_add_u32 s71, %[rw0], 196608\n\t" \
  "s_add_u32 s72, %[rx1], 0\n\t" \
  "s_add_u32 s73, %[rx1], 147456\n\t" \
  "s_add_u32 s74, %[rw1], 0\n\t" \
  "s_add_u32 s75, %[rw1], 196608\n\t" \
  "s_add_u32 m0, %[wb], 49152\n\t" \
  "s_nop 0\n\t" \
  "buffer_load_dwordx4 v5, %[sw], s68 offen offset:0 lds\n\t" \
  "buffer_load_dwordx4 v6, %[sw], s68 offen offset:1024 lds\n\t" \
  "buffer_load_dwordx4 v7, %[sw], s68 offen offset:2048 lds\n\t" \
  "s_add_u32 m0, %[xb], 36864\n\t" \
  "s_nop 0\n\t" \
  "buffer_load_dwordx4 v0, %[sx], s69 offen offset:0 lds\n\t" \
  "buffer_load_dwordx4 v1, %[sx], s69 offen offset:1024 lds\n\t" \
  "s_mov_b64 exec, 0xffff\n\t" \
  "buffer_load_dwordx4 v4, %[sx], s69 offen offset:2048 lds\n\t" \
  "s_add_u32 m0, %[xb], 46080\n\t" \
  "s_mov_b64 exec, -1\n\t" \
  "buffer_load_dwordx4 v2, %[sx], s70 offen offset:0 lds\n\t" \
  "buffer_load_dwordx4 v3, %[sx], s70 offen offset:1024 lds\n\t" \
  "s_mov_b64 exec, 0xffff\n\t" \
  "buffer_load_dwordx4 v4, %[sx], s70 offen offset:2048 lds\n\t" \
  "s_add_u32 m0, %[wb], 61440\n\t" \
  "s_mov_b64 exec, -1\n\t" \
  "buffer_load_dwordx4 v5, %[sw], s71 offen offset:0 lds\n\t" \
  "s_mov_b32 s85, 0x120000\n\t" \
  "s_load_dword s84, %[pw], s85\n\t" \
  "buffer_load_dwordx4 v6, %[sw], s71 offen offset:1024 lds\n\t" \
  "s_mov_b32 s85, 0x120800\n\t" \
  "s_load_dword s84, %[pw], s85\n\t" \
  "buffer_load_dwordx4 v7, %[sw], s71 offen offset:2048 lds\n\t" \
  "s_mov_b32 s85, 0x121000\n\t" \
  "s_load_dword s84, %[pw], s85\n\t" \
  "s_add_u32 m0, %[xb], 0\n\t" \
  "s_nop 0\n\t" \
  "buffer_load_dwordx4 v0, %[sx], s72 offen offset:0 lds\n\t" \
  "s_mov_b32 s85, 0x150000\n\t" \
  "s_load_dword s84, %[pw], s85\n\t" \
  "buffer_load_dwordx4 v1, %[sx], s72 offen offset:1024 lds\n\t" \
  "s_mov_b32 s85, 0x150800\n\t" \
  "s_load_dword s84, %[pw], s85\n\t" \
  "s_mov_b64 exec, 0xffff\n\t" \
  "buffer_load_dwordx4 v4, %[sx], s72 offen offset:2048 lds\n\t" \
  "s_add_u32 m0, %[xb], 9216\n\t" \
  "s_mov_b64 exec, -1\n\t" \
  "buffer_load_dwordx4 v2, %[sx], s73 offen offset:0 lds\n\t" \
  "s_mov_b32 s85, 0x151000\n\t" \
  "s_load_dword s84, %[pw], s85\n\t" \
  "buffer_load_dwordx4 v3, %[sx], s73 offen offset:1024 lds\n\t" \
  "s_mov_b32 s85, 0x180000\n\t" \
  "s_load_dword s84, %[pw], s85\n\t" \
  "s_mov_b64 exec, 0xffff\n\t" \
  "buffer_load_dwordx4 v4, %[sx], s73 offen offset:2048 lds\n\t" \
  "s_add_u32 m0, %[wb], 0\n\t" \
  "s_mov_b64 exec, -1\n\t" \
  "buffer_load_dwordx4 v5, %[sw], s74 offen offset:0 lds\n\t" \
  "s_mov_b32 s85, 0x180800\n\t" \
  "s_load_dword s84, %[pw], s85\n\t" \
  "buffer_load_dwordx4 v6, %[sw], s74 offen offset:1024 lds\n\t" \
  "buffer_load_dwordx4 v7, %[sw], s74 offen offset:2048 lds\n\t" \
  "s_add_u32 m0, %[wb], 12288\n\t" \
  "s_nop 0\n\t" \
  "buffer_load_dwordx4 v5, %[sw], s75 offen offset:0 lds\n\t" \
  "buffer_load_dwordx4 v6, %[sw], s75 offen offset:1024 lds\n\t" \
  "buffer_load_dwordx4 v7, %[sw], s75 offen offset:2048 lds\n\t" \
  "s_add_u32 s68, %[rx2], 0\n\t" \
  "s_add_u32 s69, %[rx2], 147456\n\t" \
  "s_add_u32 m0, %[xb], 18432\n\t" \
  "s_waitcnt vmcnt(15)\n\t" \
  "s_barrier\n\t" \
  "buffer_load_dwordx4 v0, %[sx], s68 offen offset:0 lds\n\t" \
  "s_mov_b32 s85, 0x181000\n\t" \
  "s_load_dword s84, %[pw], s85\n\t" \
  "buffer_load_dwordx4 v1, %[sx], s68 offen offset:1024 lds\n\t" \
  "s_mov_b32 s85, 0x1b0000\n\t" \
  "s_load_dword s84, %[pw], s85\n\t" \
  "s_mov_b64 exec, 0xffff\n\t" \
  "buffer_load_dwordx4 v4, %[sx], s68 offen offset:2048 lds\n\t" \
  "s_add_u32 m0, %[xb], 27648\n\t" \
  "s_mov_b64 exec, -1\n\t" \
  "buffer_load_dwordx4 v2, %[sx], s69 offen offset:0 lds\n\t" \
  "s_mov_b32 s85, 0x1b0800\n\t" \
  "s_load_dword s84, %[pw], s85\n\t" \
  "buffer_load_dwordx4 v3, %[sx], s69 offen offset:1024 lds\n\t" \
  "s_mov_b32 s85, 0x1b1000\n\t" \
  "s_load_dword s84, %[pw], s85\n\t" \
  "s_mov_b64 exec, 0xffff\n\t" \
  "buffer_load_dwordx4 v4, %[sx], s69 offen offset:2048 lds\n\t" \
  "s_mov_b64 exec, -1\n\t" \
  "s_add_u32 s68, %[rw2], 0\n\t" \
  "s_add_u32 s69, %[rw2], 196608\n\t" \
  "s_add_u32 m0, %[wb], 24576\n\t" \
  "s_waitcnt vmcnt(18)\n\t" \
  "s_barrier\n\t" \
  "buffer_load_dwordx4 v5, %[sw], s68 offen offset:0 lds\n\t" \
  "s_mov_b32 s85, 0x1e0000\n\t" \
  "s_load_dword s84, %[pw], s85\n\t" \
  "buffer_load_dwordx4 v6, %[sw], s68 offen offset:1024 lds\n\t" \
  "s_mov_b32 s85, 0x1e0800\n\t" \
  "s_load_dword s84, %[pw], s85\n\t" \
  "buffer_load_dwordx4 v7, %[sw], s68 offen offset:2048 lds\n\t" \
  "s_mov_b32 s85, 0x1e1000\n\t" \
  "s_load_dword s84, %[pw], s85\n\t" \
  "s_add_u32 m0, %[wb], 36864\n\t" \
  "s_nop 0\n\t" \
  "buffer_load_dwordx4 v5, %[sw], s69 offen offset:0 lds\n\t" \
  "s_mov_b32 s85, 0x210000\n\t" \
  "s_load_dword s84, %[pw], s85\n\t" \
  "buffer_load_dwordx4 v6, %[sw], s69 offen offset:1024 lds\n\t" \
  "buffer_load_dwordx4 v7, %[sw], s69 offen offset:2048 lds\n\t" \
  "s_add_u32 s68, %[rx3], 0\n\t" \
  "s_add_u32 s69, %[rw3], 0\n\t" \
  "s_add_u32 m0, %[xb], 36864\n\t" \
  "s_waitcnt vmcnt(15)\n\t" \
  "s_barrier\n\t" \
  "buffer_load_dwordx4 v0, %[sx], s68 offen offset:0 lds\n\t" \
  "s_mov_b32 s85, 0x210800\n\t" \
  "s_load_dword s84, %[pw], s85\n\t" \
  "buffer_load_dwordx4 v1, %[sx], s68 offen offset:1024 lds\n\t" \
  "s_mov_b32 s85, 0x211000\n\t" \
  "s_load_dword s84, %[pw], s85\n\t" \
  "s_mov_b64 exec, 0xffff\n\t" \
  "buffer_load_dwordx4 v4, %[sx], s68 offen offset:2048 lds\n\t" \
  "s_add_u32 m0, %[wb], 49152\n\t" \
  "s_mov_b64 exec, -1\n\t" \
  "buffer_load_dwordx4 v5, %[sw], s69 offen offset:0 lds\n\t" \
  "s_mov_b32 s85, 0x240000\n\t" \
  "s_load_dword s84, %[pw], s85\n\t" \
  "buffer_load_dwordx4 v6, %[sw], s69 offen offset:1024 lds\n\t" \
  "s_mov_b32 s85, 0x240800\n\t" \
  "s_load_dword s84, %[pw], s85\n\t" \
  "buffer_load_dwordx4 v7, %[sw], s69 offen offset:2048 lds\n\t" \
  "s_add_u32 s68, %[rx3], 147456\n\t" \
  "s_add_u32 s69, %[rw3], 196608\n\t" \
  "s_add_u32 m0, %[xb], 46080\n\t" \
  "s_waitcnt vmcnt(18)\n\t" \
  "s_barrier\n\t" \
  "buffer_load_dwordx4 v2, %[sx], s68 offen offset:0 lds\n\t" \
  "s_mov_b32 s85, 0x241000\n\t" \
  "s_load_dword s84, %[pw], s85\n\t" \
  "buffer_load_dwordx4 v3, %[sx], s68 offen offset:1024 lds\n\t" \
  "s_mov_b32 s85, 0x270000\n\t" \
  "s_load_dword s84, %[pw], s85\n\t" \
  "s_mov_b64 exec, 0xffff\n\t" \
  "buffer_load_dwordx4 v4, %[sx], s68 offen offset:2048 lds\n\t" \
  "s_add_u32 m0, %[wb], 61440\n\t" \
  "s_mov_b64 exec, -1\n\t" \
  "buffer_load_dwordx4 v5, %[sw], s69 offen offset:0 lds\n\t" \
  "s_mov_b32 s85, 0x270800\n\t" \
  "s_load_dword s84, %[pw], s85\n\t" \
  "buffer_load_dwordx4 v6, %[sw], s69 offen offset:1024 lds\n\t" \
  "s_mov_b32 s85, 0x271000\n\t" \
  "s_load_dword s84, %[pw], s85\n\t" \
  "buffer_load_dwordx4 v7, %[sw], s69 offen offset:2048 lds\n\t" \
  "s_add_u32 s68, %[rx0], 512\n\t" \
  "s_add_u32 s69, %[rw0], 1572864\n\t" \
  "s_add_u32 m0, %[xb], 0\n\t" \
  "s_waitcnt vmcnt(15)\n\t" \
  "s_barrier\n\t" \
  "buffer_load_dwordx4 v0, %[sx], s68 offen offset:0 lds\n\t" \
  "s_mov_b32 s85, 0x2a0000\n\t" \
  "s_load_dword s84, %[pw], s85\n\t" \
  "buffer_load_dwordx4 v1, %[sx], s68 offen offset:1024 lds\n\t" \
  "s_mov_b32 s85, 0x2a0800\n\t" \
  "s_load_dword s84, %[pw], s85\n\t" \
  "s_mov_b64 exec, 0xffff\n\t" \
  "buffer_load_dwordx4 v4, %[sx], s68 offen offset:2048 lds\n\t" \
  "s_add_u32 m0, %[wb], 0\n\t" \
  "s_mov_b64 exec, -1\n\t" \
  "buffer_load_dwordx4 v5, %[sw], s69 offen offset:0 lds\n\t" \
  "s_mov_b32 s85, 0x2a1000\n\t" \
  "s_load_dword s84, %[pw], s85\n\t" \
  "buffer_load_dwordx4 v6, %[sw], s69 offen offset:1024 lds\n\t" \
  "s_mov_b32 s85, 0x2d0000\n\t" \
  "s_load_dword s84, %[pw], s85\n\t" \
  "buffer_load_dwordx4 v7, %[sw], s69 offen offset:2048 lds\n\t" \
  "s_add_u32 s68, %[rx0], 147968\n\t" \
  "s_add_u32 s69, %[rw0], 1769472\n\t" \
  "s_add_u32 m0, %[xb], 9216\n\t" \
  "s_waitcnt vmcnt(18)\n\t" \
  "s_barrier\n\t" \
  "buffer_load_dwordx4 v2, %[sx], s68 offen offset:0 lds\n\t" \
  "s_mov_b32 s85, 0x2d0800\n\t" \
  "s_load_dword s84, %[pw], s85\n\t" \
  "buffer_load_dwordx4 v3, %[sx], s68 offen offset:1024 lds\n\t" \
  "s_mov_b32 s85, 0x2d1000\n\t" \
  "s_load_dword s84, %[pw], s85\n\t" \
  "s_mov_b64 exec, 0xffff\n\t" \
  "buffer_load_dwordx4 v4, %[sx], s68 offen offset:2048 lds\n\t" \
  "s_add_u32 m0, %[wb], 12288\n\t" \
  "s_mov_b64 exec, -1\n\t" \
  "buffer_load_dwordx4 v5, %[sw], s69 offen offset:0 lds\n\t" \
  "s_mov_b32 s85, 0x300000\n\t" \
  "s_load_dword s84, %[pw], s85\n\t" \
  "buffer_load_dwordx4 v6, %[sw], s69 offen offset:1024 lds\n\t" \
  "s_mov_b32 s85, 0x300800\n\t" \
  "s_load_dword s84, %[pw], s85\n\t" \
  "buffer_load_dwordx4 v7, %[sw], s69 offen offset:2048 lds\n\t" \
  "s_add_u32 s68, %[rx1], 512\n\t" \
  "s_add_u32 s69, %[rw1], 1572864\n\t" \
  "s_add_u32 m0, %[xb], 18432\n\t" \
  "s_waitcnt vmcnt(15)\n\t" \
  "s_barrier\n\t" \
  "buffer_load_dwordx4 v0, %[sx], s68 offen offset:0 lds\n\t" \
  "s_mov_b32 s85, 0x301000\n\t" \
  "s_load_dword s84, %[pw], s85\n\t" \
  "buffer_load_dwordx4 v1, %[sx], s68 offen offset:1024 lds\n\t" \
  "s_mov_b32 s85, 0x330000\n\t" \
  "s_load_dword s84, %[pw], s85\n\t" \
  "s_mov_b64 exec, 0xffff\n\t" \
  "buffer_load_dwordx4 v4, %[sx], s68 offen offset:2048 lds\n\t" \
  "s_add_u32 m0, %[wb], 24576\n\t" \
  "s_mov_b64 exec, -1\n\t" \
  "buffer_load_dwordx4 v5, %[sw], s69 offen offset:0 lds\n\t" \
  "s_mov_b32 s85, 0x330800\n\t" \
  "s_load_dword s84, %[pw], s85\n\t" \
  "buffer_load_dwordx4 v6, %[sw], s69 offen offset:1024 lds\n\t" \
  "s_mov_b32 s85, 0x331000\n\t" \
  "s_load_dword s84, %[pw], s85\n\t" \
  "buffer_load_dwordx4 v7, %[sw], s69 offen offset:2048 lds\n\t" \
  "s_add_u32 s68, %[rx1], 147968\n\t" \
  "s_add_u32 s69, %[rw1], 1769472\n\t" \
  "s_add_u32 m0, %[xb], 27648\n\t" \
  "s_waitcnt vmcnt(18)\n\t" \
  "s_barrier\n\t" \
  "buffer_load_dwordx4 v2, %[sx], s68 offen offset:0 lds\n\t" \
  "s_mov_b32 s85, 0x360000\n\t" \
  "s_load_dword s84, %[pw], s85\n\t" \
  "buffer_load_dwordx4 v3, %[sx], s68 offen offset:1024 lds\n\t" \
  "s_mov_b32 s85, 0x360800\n\t" \
  "s_load_dword s84, %[pw], s85\n\t" \
  "s_mov_b64 exec, 0xffff\n\t" \
  "buffer_load_dwordx4 v4, %[sx], s68 offen offset:2048 lds\n\t" \
  "s_add_u32 m0, %[wb], 36864\n\t" \
  "s_mov_b64 exec, -1\n\t" \
  "buffer_load_dwordx4 v5, %[sw], s69 offen offset:0 lds\n\t" \
  "s_mov_b32 s85, 0x361000\n\t" \
  "s_load_dword s84, %[pw], s85\n\t" \
  "buffer_load_dwordx4 v6, %[sw], s69 offen offset:1024 lds\n\t" \
  "s_mov_b32 s85, 0x390000\n\t" \
  "s_load_dword s84, %[pw], s85\n\t" \
  "buffer_load_dwordx4 v7, %[sw], s69 offen offset:2048 lds\n\t" \
  "s_add_u32 s68, %[rx2], 512\n\t" \
  "s_add_u32 s69, %[rw2], 1572864\n\t" \
  "s_add_u32 m0, %[xb], 36864\n\t" \
  "s_waitcnt vmcnt(15)\n\t" \
  "s_barrier\n\t" \
  "buffer_load_dwordx4 v0, %[sx], s68 offen offset:0 lds\n\t" \
  "s_mov_b32 s85, 0x390800\n\t" \
  "s_load_dword s84, %[pw], s85\n\t" \
  "buffer_load_dwordx4 v1, %[sx], s68 offen offset:1024 lds\n\t" \
  "s_mov_b32 s85, 0x391000\n\t" \
  "s_load_dword s84, %[pw], s85\n\t" \
  "s_mov_b64 exec, 0xffff\n\t" \
  "buffer_load_dwordx4 v4, %[sx], s68 offen offset:2048 lds\n\t" \
  "s_add_u32 m0, %[wb], 49152\n\t" \
  "s_mov_b64 exec, -1\n\t" \
  "buffer_load_dwordx4 v5, %[sw], s69 offen offset:0 lds\n\t" \
  "s_mov_b32 s85, 0x3c0000\n\t" \
  "s_load_dword s84, %[pw], s85\n\t" \
  "buffer_load_dwordx4 v6, %[sw], s69 offen offset:1024 lds\n\t" \
  "s_mov_b32 s85, 0x3c0800\n\t" \
  "s_load_dword s84, %[pw], s85\n\t" \
  "buffer_load_dwordx4 v7, %[sw], s69 offen offset:2048 lds\n\t" \
  "s_add_u32 s68, %[rx2], 147968\n\t" \
  "s_add_u32 s69, %[rw2], 1769472\n\t" \
  "s_add_u32 m0, %[xb], 46080\n\t" \
  "s_waitcnt vmcnt(18)\n\t" \
  "s_barrier\n\t" \
  "buffer_load_dwordx4 v2, %[sx], s68 offen offset:0 lds\n\t" \
  "s_mov_b32 s85, 0x3c1000\n\t" \
  "s_load_dword s84, %[pw], s85\n\t" \
  "buffer_load_dwordx4 v3, %[sx], s68 offen offset:1024 lds\n\t" \
  "s_mov_b32 s85, 0x3f0000\n\t" \
  "s_load_dword s84, %[pw], s85\n\t" \
  "s_mov_b64 exec, 0xffff\n\t" \
  "buffer_load_dwordx4 v4, %[sx], s68 offen offset:2048 lds\n\t" \
  "s_add_u32 m0, %[wb], 61440\n\t" \
  "s_mov_b64 exec, -1\n\t" \
  "buffer_load_dwordx4 v5, %[sw], s69 offen offset:0 lds\n\t" \
  "s_mov_b32 s85, 0x3f0800\n\t" \
  "s_load_dword s84, %[pw], s85\n\t" \
  "buffer_load_dwordx4 v6, %[sw], s69 offen offset:1024 lds\n\t" \
  "s_mov_b32 s85, 0x3f1000\n\t" \
  "s_load_dword s84, %[pw], s85\n\t" \
  "buffer_load_dwordx4 v7, %[sw], s69 offen offset:2048 lds\n\t" \
  "s_add_u32 s68, %[rx3], 512\n\t" \
  "s_add_u32 s69, %[rw3], 1572864\n\t" \
  "s_add_u32 m0, %[xb], 0\n\t" \
  "s_waitcnt vmcnt(15)\n\t" \
  "s_barrier\n\t" \
  "buffer_load_dwordx4 v0, %[sx], s68 offen offset:0 lds\n\t" \
  "s_mov_b32 s85, 0x420000\n\t" \
  "s_load_dword s84, %[pw], s85\n\t" \
  "buffer_load_dwordx4 v1, %[sx], s68 offen offset:1024 lds\n\t" \
  "s_mov_b32 s85, 0x420800\n\t" \
  "s_load_dword s84, %[pw], s85\n\t" \
  "s_mov_b64 exec, 0xffff\n\t" \
  "buffer_load_dwordx4 v4, %[sx], s68 offen offset:2048 lds\n\t" \
  "s_add_u32 m0, %[wb], 0\n\t" \
  "s_mov_b64 exec, -1\n\t" \
  "buffer_load_dwordx4 v5, %[sw], s69 offen offset:0 lds\n\t" \
  "s_mov_b32 s85, 0x421000\n\t" \
  "s_load_dword s84, %[pw], s85\n\t" \
  "buffer_load_dwordx4 v6, %[sw], s69 offen offset:1024 lds\n\t" \
  "s_mov_b32 s85, 0x450000\n\t" \
  "s_load_dword s84, %[pw], s85\n\t" \
  "buffer_load_dwordx4 v7, %[sw], s69 offen offset:2048 lds\n\t" \
  "s_add_u32 s68, %[rx3], 147968\n\t" \
  "s_add_u32 s69, %[rw3], 1769472\n\t" \
  "s_add_u32 m0, %[xb], 9216\n\t" \
  "s_waitcnt vmcnt(18)\n\t" \
  "s_barrier\n\t" \
  "buffer_load_dwordx4 v2, %[sx], s68 offen offset:0 lds\n\t" \
  "s_mov_b32 s85, 0x450800\n\t" \
  "s_load_dword s84, %[pw], s85\n\t" \
  "buffer_load_dwordx4 v3, %[sx], s68 offen offset:1024 lds\n\t" \
  "s_mov_b32 s85, 0x451000\n\t" \
  "s_load_dword s84, %[pw], s85\n\t" \
  "s_mov_b64 exec, 0xffff\n\t" \
  "buffer_load_dwordx4 v4, %[sx], s68 offen offset:2048 lds\n\t" \
  "s_add_u32 m0, %[wb], 12288\n\t" \
  "s_mov_b64 exec, -1\n\t" \
  "buffer_load_dwordx4 v5, %[sw], s69 offen offset:0 lds\n\t" \
  "s_mov_b32 s85, 0x480000\n\t" \
  "s_load_dword s84, %[pw], s85\n\t" \
  "buffer_load_dwordx4 v6, %[sw], s69 offen offset:1024 lds\n\t" \
  "s_mov_b32 s85, 0x480800\n\t" \
  "s_load_dword s84, %[pw], s85\n\t" \
  "buffer_load_dwordx4 v7, %[sw], s69 offen offset:2048 lds\n\t" \
  "s_add_u32 s68, %[rx0], 1024\n\t" \
  "s_add_u32 s69, %[rw0], 3145728\n\t" \
  "s_add_u32 m0, %[xb], 18432\n\t" \
  "s_waitcnt vmcnt(15)\n\t" \
  "s_barrier\n\t" \
  "buffer_load_dwordx4 v0, %[sx], s68 offen offset:0 lds\n\t" \
  "s_mov_b32 s85, 0x481000\n\t" \
  "s_load_dword s84, %[pw], s85\n\t" \
  "buffer_load_dwordx4 v1, %[sx], s68 offen offset:1024 lds\n\t" \
  "s_mov_b32 s85, 0x4b0000\n\t" \
  "s_load_dword s84, %[pw], s85\n\t" \
  "s_mov_b64 exec, 0xffff\n\t" \
  "buffer_load_dwordx4 v4, %[sx], s68 offen offset:2048 lds\n\t" \
  "s_add_u32 m0, %[wb], 24576\n\t" \
  "s_mov_b64 exec, -1\n\t" \
  "buffer_load_dwordx4 v5, %[sw], s69 offen offset:0 lds\n\t" \
  "s_mov_b32 s85, 0x4b0800\n\t" \
  "s_load_dword s84, %[pw], s85\n\t" \
  "buffer_load_dwordx4 v6, %[sw], s69 offen offset:1024 lds\n\t" \
  "s_mov_b32 s85, 0x4b1000\n\t" \
  "s_load_dword s84, %[pw], s85\n\t" \
  "buffer_load_dwordx4 v7, %[sw], s69 offen offset:2048 lds\n\t" \
  "s_add_u32 s68, %[rx0], 148480\n\t" \
  "s_add_u32 s69, %[rw0], 3342336\n\t" \
  "s_add_u32 m0, %[xb], 27648\n\t" \
  "s_waitcnt vmcnt(18)\n\t" \
  "s_barrier\n\t" \
  "buffer_load_dwordx4 v2, %[sx], s68 offen offset:0 lds\n\t" \
  "s_mov_b32 s85, 0x4e0000\n\t" \
  "s_load_dword s84, %[pw], s85\n\t" \
  "buffer_load_dwordx4 v3, %[sx], s68 offen offset:1024 lds\n\t" \
  "s_mov_b32 s85, 0x4e0800\n\t" \
  "s_load_dword s84, %[pw], s85\n\t" \
  "s_mov_b64 exec, 0xffff\n\t" \
  "buffer_load_dwordx4 v4, %[sx], s68 offen offset:2048 lds\n\t" \
  "s_add_u32 m0, %[wb], 36864\n\t" \
  "s_mov_b64 exec, -1\n\t" \
  "buffer_load_dwordx4 v5, %[sw], s69 offen offset:0 lds\n\t" \
  "s_mov_b32 s85, 0x4e1000\n\t" \
  "s_load_dword s84, %[pw], s85\n\t" \
  "buffer_load_dwordx4 v6, %[sw], s69 offen offset:1024 lds\n\t" \
  "s_mov_b32 s85, 0x510000\n\t" \
  "s_load_dword s84, %[pw], s85\n\t" \
  "buffer_load_dwordx4 v7, %[sw], s69 offen offset:2048 lds\n\t" \
  "s_add_u32 s68, %[rx1], 1024\n\t" \
  "s_add_u32 s69, %[rw1], 3145728\n\t" \
  "s_add_u32 m0, %[xb], 36864\n\t" \
  "s_waitcnt vmcnt(15)\n\t" \
  "s_barrier\n\t" \
  "buffer_load_dwordx4 v0, %[sx], s68 offen offset:0 lds\n\t" \
  "s_mov_b32 s85, 0x510800\n\t" \
  "s_load_dword s84, %[pw], s85\n\t" \
  "buffer_load_dwordx4 v1, %[sx], s68 offen offset:1024 lds\n\t" \
  "s_mov_b32 s85, 0x511000\n\t" \
  "s_load_dword s84, %[pw], s85\n\t" \
  "s_mov_b64 exec, 0xffff\n\t" \
  "buffer_load_dwordx4 v4, %[sx], s68 offen offset:2048 lds\n\t" \
  "s_add_u32 m0, %[wb], 49152\n\t" \
  "s_mov_b64 exec, -1\n\t" \
  "buffer_load_dwordx4 v5, %[sw], s69 offen offset:0 lds\n\t" \
  "s_mov_b32 s85, 0x540000\n\t" \
  "s_load_dword s84, %[pw], s85\n\t" \
  "buffer_load_dwordx4 v6, %[sw], s69 offen offset:1024 lds\n\t" \
  "s_mov_b32 s85, 0x540800\n\t" \
  "s_load_dword s84, %[pw], s85\n\t" \
  "buffer_load_dwordx4 v7, %[sw], s69 offen offset:2048 lds\n\t" \
  "s_add_u32 s68, %[rx1], 148480\n\t" \
  "s_add_u32 s69, %[rw1], 3342336\n\t" \
  "s_add_u32 m0, %[xb], 46080\n\t" \
  "s_waitcnt vmcnt(18)\n\t" \
  "s_barrier\n\t" \
  "buffer_load_dwordx4 v2, %[sx], s68 offen offset:0 lds\n\t" \
  "s_mov_b32 s85, 0x541000\n\t" \
  "s_load_dword s84, %[pw], s85\n\t" \
  "buffer_load_dwordx4 v3, %[sx], s68 offen offset:1024 lds\n\t" \
  "s_mov_b32 s85, 0x570000\n\t" \
  "s_load_dword s84, %[pw], s85\n\t" \
  "s_mov_b64 exec, 0xffff\n\t" \
  "buffer_load_dwordx4 v4, %[sx], s68 offen offset:2048 lds\n\t" \
  "s_add_u32 m0, %[wb], 61440\n\t" \
  "s_mov_b64 exec, -1\n\t" \
  "buffer_load_dwordx4 v5, %[sw], s69 offen offset:0 lds\n\t" \
  "s_mov_b32 s85, 0x570800\n\t" \
  "s_load_dword s84, %[pw], s85\n\t" \
  "buffer_load_dwordx4 v6, %[sw], s69 offen offset:1024 lds\n\t" \
  "s_mov_b32 s85, 0x571000\n\t" \
  "s_load_dword s84, %[pw], s85\n\t" \
  "buffer_load_dwordx4 v7, %[sw], s69 offen offset:2048 lds\n\t" \
  "s_add_u32 s68, %[rx2], 1024\n\t" \
  "s_add_u32 s69, %[rw2], 3145728\n\t" \
  "s_add_u32 m0, %[xb], 0\n\t" \
  "s_waitcnt vmcnt(15)\n\t" \
  "s_barrier\n\t" \
  "buffer_load_dwordx4 v0, %[sx], s68 offen offset:0 lds\n\t" \
  "s_mov_b32 s85, 0x5a0000\n\t" \
  "s_load_dword s84, %[pw], s85\n\t" \
  "buffer_load_dwordx4 v1, %[sx], s68 offen offset:1024 lds\n\t" \
  "s_mov_b32 s85, 0x5a0800\n\t" \
  "s_load_dword s84, %[pw], s85\n\t" \
  "s_mov_b64 exec, 0xffff\n\t" \
  "buffer_load_dwordx4 v4, %[sx], s68 offen offset:2048 lds\n\t" \
  "s_add_u32 m0, %[wb], 0\n\t" \
  "s_mov_b64 exec, -1\n\t" \
  "buffer_load_dwordx4 v5, %[sw], s69 offen offset:0 lds\n\t" \
  "s_mov_b32 s85, 0x5a1000\n\t" \
  "s_load_dword s84, %[pw], s85\n\t" \
  "buffer_load_dwordx4 v6, %[sw], s69 offen offset:1024 lds\n\t" \
  "s_mov_b32 s85, 0x5d0000\n\t" \
  "s_load_dword s84, %[pw], s85\n\t" \
  "buffer_load_dwordx4 v7, %[sw], s69 offen offset:2048 lds\n\t" \
  "s_add_u32 s68, %[rx2], 148480\n\t" \
  "s_add_u32 s69, %[rw2], 3342336\n\t" \
  "s_add_u32 m0, %[xb], 9216\n\t" \
  "s_waitcnt vmcnt(18)\n\t" \
  "s_barrier\n\t" \
  "buffer_load_dwordx4 v2, %[sx], s68 offen offset:0 lds\n\t" \
  "s_mov_b32 s85, 0x5d0800\n\t" \
  "s_load_dword s84, %[pw], s85\n\t" \
  "buffer_load_dwordx4 v3, %[sx], s68 offen offset:1024 lds\n\t" \
  "s_mov_b32 s85, 0x5d1000\n\t" \
  "s_load_dword s84, %[pw], s85\n\t" \
  "s_mov_b64 exec, 0xffff\n\t" \
  "buffer_load_dwordx4 v4, %[sx], s68 offen offset:2048 lds\n\t" \
  "s_add_u32 m0, %[wb], 12288\n\t" \
  "s_mov_b64 exec, -1\n\t" \
  "buffer_load_dwordx4 v5, %[sw], s69 offen offset:0 lds\n\t" \
  "s_load_dword s84, %[po], 0x0\n\t" \
  "buffer_load_dwordx4 v6, %[sw], s69 offen offset:1024 lds\n\t" \
  "s_load_dword s84, %[po], 0x40\n\t" \
  "buffer_load_dwordx4 v7, %[sw], s69 offen offset:2048 lds\n\t" \
  "s_mov_b32 m0, %[tsk]\n\t" \
  "s_mov_b64 exec, %[tm]\n\t" \
  "global_load_lds_dword v12, %[tp]\n\t" \
  "s_mov_b64 exec, -1\n\t" \
  "s_add_u32 s68, %[rx3], 1024\n\t" \
  "s_add_u32 s69, %[rw3], 3145728\n\t" \
  "s_add_u32 m0, %[xb], 18432\n\t" \
  "s_waitcnt vmcnt(15)\n\t" \
  "s_barrier\n\t" \
  "buffer_load_dwordx4 v0, %[sx], s68 offen offset:0 lds\n\t" \
  "s_load_dword s84, %[po], 0x80\n\t" \
  "buffer_load_dwordx4 v1, %[sx], s68 offen offset:1024 lds\n\t" \
  "s_load_dword s84, %[po], 0xc0\n\t" \
  "s_mov_b64 exec, 0xffff\n\t" \
  "buffer_load_dwordx4 v4, %[sx], s68 offen offset:2048 lds\n\t" \
  "s_add_u32 m0, %[wb], 24576\n\t" \
  "s_mov_b64 exec, -1\n\t" \
  "buffer_load_dwordx4 v5, %[sw], s69 offen offset:0 lds\n\t" \
  "s_load_dword s84, %[po], 0x100\n\t" \
  "buffer_load_dwordx4 v6, %[sw], s69 offen offset:1024 lds\n\t" \
  "s_load_dword s84, %[po], 0x140\n\t" \
  "buffer_load_dwordx4 v7, %[sw], s69 offen offset:2048 lds\n\t" \
  "s_add_u32 s68, %[rx3], 148480\n\t" \
  "s_add_u32 s69, %[rw3], 3342336\n\t" \
  "s_add_u32 m0, %[xb], 27648\n\t" \
  "s_waitcnt vmcnt(18)\n\t" \
  "s_barrier\n\t" \
  "buffer_load_dwordx4 v2, %[sx], s68 offen offset:0 lds\n\t" \
  "s_load_dword s84, %[po], 0x180\n\t" \
  "buffer_load_dwordx4 v3, %[sx], s68 offen offset:1024 lds\n\t" \
  "s_load_dword s84, %[po], 0x1c0\n\t" \
  "s_mov_b64 exec, 0xffff\n\t" \
  "buffer_load_dwordx4 v4, %[sx], s68 offen offset:2048 lds\n\t" \
  "s_add_u32 m0, %[wb], 36864\n\t" \
  "s_mov_b64 exec, -1\n\t" \
  "buffer_load_dwordx4 v5, %[sw], s69 offen offset:0 lds\n\t" \
  "s_load_dword s84, %[po], 0x200\n\t" \
  "buffer_load_dwordx4 v6, %[sw], s69 offen offset:1024 lds\n\t" \
  "s_load_dword s84, %[po], 0x240\n\t" \
  "buffer_load_dwordx4 v7, %[sw], s69 offen offset:2048 lds\n\t" \
  "s_add_u32 s68, %[rx0], 1536\n\t" \
  "s_add_u32 s69, %[rw0], 4718592\n\t" \
  "s_add_u32 m0, %[xb], 36864\n\t" \
  "s_waitcnt vmcnt(15)\n\t" \
  "s_barrier\n\t" \
  "buffer_load_dwordx4 v0, %[sx], s68 offen offset:0 lds\n\t" \
  "s_load_dword s84, %[po], 0x280\n\t" \
  "buffer_load_dwordx4 v1, %[sx], s68 offen offset:1024 lds\n\t" \
  "s_load_dword s84, %[po], 0x2c0\n\t" \
  "s_mov_b64 exec, 0xffff\n\t" \
  "buffer_load_dwordx4 v4, %[sx], s68 offen offset:2048 lds\n\t" \
  "s_add_u32 m0, %[wb], 49152\n\t" \
  "s_mov_b64 exec, -1\n\t" \
  "buffer_load_dwordx4 v5, %[sw], s69 offen offset:0 lds\n\t" \
  "s_load_dword s84, %[po], 0x300\n\t" \
  "buffer_load_dwordx4 v6, %[sw], s69 offen offset:1024 lds\n\t" \
  "s_load_dword s84, %[po], 0x340\n\t" \
  "buffer_load_dwordx4 v7, %[sw], s69 offen offset:2048 lds\n\t" \
  "s_add_u32 s68, %[rx0], 148992\n\t" \
  "s_add_u32 s69, %[rw0], 4915200\n\t" \
  "s_add_u32 m0, %[xb], 46080\n\t" \
  "s_waitcnt vmcnt(18)\n\t" \
  "s_barrier\n\t" \
  "buffer_load_dwordx4 v2, %[sx], s68 offen offset:0 lds\n\t" \
  "s_load_dword s84, %[po], 0x380\n\t" \
  "buffer_load_dwordx4 v3, %[sx], s68 offen offset:1024 lds\n\t" \
  "s_load_dword s84, %[po], 0x3c0\n\t" \
  "s_mov_b64 exec, 0xffff\n\t" \
  "buffer_load_dwordx4 v4, %[sx], s68 offen offset:2048 lds\n\t" \
  "s_add_u32 m0, %[wb], 61440\n\t" \
  "s_mov_b64 exec, -1\n\t" \
  "buffer_load_dwordx4 v5, %[sw], s69 offen offset:0 lds\n\t" \
  "s_load_dword s84, %[po], 0x400\n\t" \
  "buffer_load_dwordx4 v6, %[sw], s69 offen offset:1024 lds\n\t" \
  "s_load_dword s84, %[po], 0x440\n\t" \
  "buffer_load_dwordx4 v7, %[sw], s69 offen offset:2048 lds\n\t" \
  "s_add_u32 s68, %[rx1], 1536\n\t" \
  "s_add_u32 s69, %[rw1], 4718592\n\t" \
  "s_add_u32 m0, %[xb], 0\n\t" \
  "s_waitcnt vmcnt(15)\n\t" \
  "s_barrier\n\t" \
  "buffer_load_dwordx4 v0, %[sx], s68 offen offset:0 lds\n\t" \
  "s_load_dword s84, %[po], 0x480\n\t" \
  "buffer_load_dwordx4 v1, %[sx], s68 offen offset:1024 lds\n\t" \
  "s_load_dword s84, %[po], 0x4c0\n\t" \
  "s_mov_b64 exec, 0xffff\n\t" \
  "buffer_load_dwordx4 v4, %[sx], s68 offen offset:2048 lds\n\t" \
  "s_add_u32 m0, %[wb], 0\n\t" \
  "s_mov_b64 exec, -1\n\t" \
  "buffer_load_dwordx4 v5, %[sw], s69 offen offset:0 lds\n\t" \
  "s_load_dword s84, %[po], 0x500\n\t" \
  "buffer_load_dwordx4 v6, %[sw], s69 offen offset:1024 lds\n\t" \
  "s_load_dword s84, %[po], 0x540\n\t" \
  "buffer_load_dwordx4 v7, %[sw], s69 offen offset:2048 lds\n\t" \
  "s_add_u32 s68, %[rx1], 148992\n\t" \
  "s_add_u32 s69, %[rw1], 4915200\n\t" \
  "s_add_u32 m0, %[xb], 9216\n\t" \
  "s_waitcnt vmcnt(18)\n\t" \
  "s_barrier\n\t" \
  "buffer_load_dwordx4 v2, %[sx], s68 offen offset:0 lds\n\t" \
  "s_load_dword s84, %[po], 0x580\n\t" \
  "buffer_load_dwordx4 v3, %[sx], s68 offen offset:1024 lds\n\t" \
  "s_load_dword s84, %[po], 0x5c0\n\t" \
  "s_mov_b64 exec, 0xffff\n\t" \
  "buffer_load_dwordx4 v4, %[sx], s68 offen offset:2048 lds\n\t" \
  "s_add_u32 m0, %[wb], 12288\n\t" \
  "s_mov_b64 exec, -1\n\t" \
  "buffer_load_dwordx4 v5, %[sw], s69 offen offset:0 lds\n\t" \
  "s_load_dword s84, %[po], 0x600\n\t" \
  "buffer_load_dwordx4 v6, %[sw], s69 offen offset:1024 lds\n\t" \
  "s_load_dword s84, %[po], 0x640\n\t" \
  "buffer_load_dwordx4 v7, %[sw], s69 offen offset:2048 lds\n\t" \
  "s_add_u32 s68, %[rx2], 1536\n\t" \
  "s_add_u32 s69, %[rw2], 4718592\n\t" \
  "s_add_u32 m0, %[xb], 18432\n\t" \
  "s_waitcnt vmcnt(15)\n\t" \
  "s_barrier\n\t" \
  "buffer_load_dwordx4 v0, %[sx], s68 offen offset:0 lds\n\t" \
  "s_load_dword s84, %[po], 0x680\n\t" \
  "buffer_load_dwordx4 v1, %[sx], s68 offen offset:1024 lds\n\t" \
  "s_load_dword s84, %[po], 0x6c0\n\t" \
  "s_mov_b64 exec, 0xffff\n\t" \
  "buffer_load_dwordx4 v4, %[sx], s68 offen offset:2048 lds\n\t" \
  "s_add_u32 m0, %[wb], 24576\n\t" \
  "s_mov_b64 exec, -1\n\t" \
  "buffer_load_dwordx4 v5, %[sw], s69 offen offset:0 lds\n\t" \
  "s_load_dword s84, %[po], 0x700\n\t" \
  "buffer_load_dwordx4 v6, %[sw], s69 offen offset:1024 lds\n\t" \
  "s_load_dword s84, %[po], 0x740\n\t" \
  "buffer_load_dwordx4 v7, %[sw], s69 offen offset:2048 lds\n\t" \
  "s_add_u32 s68, %[rx2], 148992\n\t" \
  "s_add_u32 s69, %[rw2], 4915200\n\t" \
  "s_add_u32 m0, %[xb], 27648\n\t" \
  "s_waitcnt vmcnt(18)\n\t" \
  "s_barrier\n\t" \
  "buffer_load_dwordx4 v2, %[sx], s68 offen offset:0 lds\n\t" \
  "s_load_dword s84, %[po], 0x780\n\t" \
  "buffer_load_dwordx4 v3, %[sx], s68 offen offset:1024 lds\n\t" \
  "s_load_dword s84, %[po], 0x7c0\n\t" \
  "s_mov_b64 exec, 0xffff\n\t" \
  "buffer_load_dwordx4 v4, %[sx], s68 offen offset:2048 lds\n\t" \
  "s_add_u32 m0, %[wb], 36864\n\t" \
  "s_mov_b64 exec, -1\n\t" \
  "buffer_load_dwordx4 v5, %[sw], s69 offen offset:0 lds\n\t" \
  "buffer_load_dwordx4 v6, %[sw], s69 offen offset:1024 lds\n\t" \
  "buffer_load_dwordx4 v7, %[sw], s69 offen offset:2048 lds\n\t" \
  "s_add_u32 s68, %[rx3], 1536\n\t" \
  "s_add_u32 s69, %[rw3], 4718592\n\t" \
  "s_add_u32 m0, %[xb], 36864\n\t" \
  "s_waitcnt vmcnt(15)\n\t" \
  "s_barrier\n\t" \
  "buffer_load_dwordx4 v0, %[sx], s68 offen offset:0 lds\n\t" \
  "buffer_load_dwordx4 v1, %[sx], s68 offen offset:1024 lds\n\t" \
  "s_mov_b64 exec, 0xffff\n\t" \
  "buffer_load_dwordx4 v4, %[sx], s68 offen offset:2048 lds\n\t" \
  "s_add_u32 m0, %[wb], 49152\n\t" \
  "s_mov_b64 exec, -1\n\t" \
  "buffer_load_dwordx4 v5, %[sw], s69 offen offset:0 lds\n\t" \
  "buffer_load_dwordx4 v6, %[sw], s69 offen offset:1024 lds\n\t" \
  "buffer_load_dwordx4 v7, %[sw], s69 offen offset:2048 lds\n\t" \
  "s_add_u32 s68, %[rx3], 148992\n\t" \
  "s_add_u32 s69, %[rw3], 4915200\n\t" \
  "s_add_u32 m0, %[xb], 46080\n\t" \
  "s_waitcnt vmcnt(18)\n\t" \
  "s_barrier\n\t" \
  "buffer_load_dwordx4 v2, %[sx], s68 offen offset:0 lds\n\t" \
  "buffer_load_dwordx4 v3, %[sx], s68 offen offset:1024 lds\n\t" \
  "s_mov_b64 exec, 0xffff\n\t" \
  "buffer_load_dwordx4 v4, %[sx], s68 offen offset:2048 lds\n\t" \
  "s_add_u32 m0, %[wb], 61440\n\t" \
  "s_mov_b64 exec, -1\n\t" \
  "buffer_load_dwordx4 v5, %[sw], s69 offen offset:0 lds\n\t" \
  "buffer_load_dwordx4 v6, %[sw], s69 offen offset:1024 lds\n\t" \
  "buffer_load_dwordx4 v7, %[sw], s69 offen offset:2048 lds\n\t" \
  "s_memrealtime %[st1]\n\t" \
  "v_readlane_b32 s68, v9, 0\n\t" \
  "v_readlane_b32 s69, v9, 1\n\t" \
  "v_readlane_b32 s70, v9, 2\n\t" \
  "v_readlane_b32 s71, v9, 18\n\t" \
  "v_readlane_b32 s72, v9, 19\n\t" \
  "s_add_u32 m0, %[ob], 0\n\t" \
  "s_waitcnt vmcnt(15)\n\t" \
  "s_barrier\n\t" \
  "s_nop 2\n\t" \
  "buffer_load_dwordx4 v8, %[so], s68 offen lds\n\t" \
  "s_add_u32 m0, %[ob], 4096\n\t" \
  "s_nop 0\n\t" \
  "buffer_load_dwordx4 v8, %[so], s69 offen lds\n\t" \
  "s_add_u32 m0, %[ob], 8192\n\t" \
  "s_nop 0\n\t" \
  "buffer_load_dwordx4 v8, %[so], s70 offen lds\n\t" \
  "s_add_u32 m0, %[ob], 73728\n\t" \
  "s_nop 0\n\t" \
  "buffer_load_dwordx4 v8, %[so], s71 offen lds\n\t" \
  "s_add_u32 m0, %[ob], 77824\n\t" \
  "s_nop 0\n\t" \
  "buffer_load_dwordx4 v8, %[so], s72 offen lds\n\t" \
  "v_readlane_b32 s68, v9, 3\n\t" \
  "v_readlane_b32 s69, v9, 4\n\t" \
  "v_readlane_b32 s70, v9, 5\n\t" \
  "v_readlane_b32 s71, v9, 20\n\t" \
  "v_readlane_b32 s72, v9, 21\n\t" \
  "s_add_u32 m0, %[ob], 12288\n\t" \
  "s_waitcnt vmcnt(17)\n\t" \
  "s_barrier\n\t" \
  "s_nop 2\n\t" \
  "buffer_load_dwordx4 v8, %[so], s68 offen lds\n\t" \
  "s_add_u32 m0, %[ob], 16384\n\t" \
  "s_nop 0\n\t" \
  "buffer_load_dwordx4 v8, %[so], s69 offen lds\n\t" \
  "s_add_u32 m0, %[ob], 20480\n\t" \
  "s_nop 0\n\t" \
  "buffer_load_dwordx4 v8, %[so], s70 offen lds\n\t" \
  "s_add_u32 m0, %[ob], 81920\n\t" \
  "s_nop 0\n\t" \
  "buffer_load_dwordx4 v8, %[so], s71 offen lds\n\t" \
  "s_add_u32 m0, %[ob], 86016\n\t" \
  "s_nop 0\n\t" \
  "buffer_load_dwordx4 v8, %[so], s72 offen lds\n\t" \
  "v_readlane_b32 s68, v9, 6\n\t" \
  "v_readlane_b32 s69, v9, 7\n\t" \
  "v_readlane_b32 s70, v9, 8\n\t" \
  "v_readlane_b32 s71, v9, 22\n\t" \
  "v_readlane_b32 s72, v9, 23\n\t" \
  "v_readlane_b32 s73, v9, 24\n\t" \
  "s_add_u32 m0, %[ob], 24576\n\t" \
  "s_waitcnt vmcnt(13)\n\t" \
  "s_barrier\n\t" \
  "s_nop 2\n\t" \
  "buffer_load_dwordx4 v8, %[so], s68 offen lds\n\t" \
  "s_add_u32 m0, %[ob], 28672\n\t" \
  "s_nop 0\n\t" \
  "buffer_load_dwordx4 v8, %[so], s69 offen lds\n\t" \
  "s_add_u32 m0, %[ob], 32768\n\t" \
  "s_nop 0\n\t" \
  "buffer_load_dwordx4 v8, %[so], s70 offen lds\n\t" \
  "s_add_u32 m0, %[ob], 90112\n\t" \
  "s_nop 0\n\t" \
  "buffer_load_dwordx4 v8, %[so], s71 offen lds\n\t" \
  "s_add_u32 m0, %[ob], 94208\n\t" \
  "s_nop 0\n\t" \
  "buffer_load_dwordx4 v8, %[so], s72 offen lds\n\t" \
  "s_add_u32 m0, %[ob], 98304\n\t" \
  "s_nop 0\n\t" \
  "buffer_load_dwordx4 v8, %[so], s73 offen lds\n\t" \
  "v_readlane_b32 s68, v9, 9\n\t" \
  "v_readlane_b32 s69, v9, 10\n\t" \
  "v_readlane_b32 s70, v9, 11\n\t" \
  "v_readlane_b32 s71, v9, 25\n\t" \
  "v_readlane_b32 s72, v9, 26\n\t" \
  "s_add_u32 m0, %[ob], 36864\n\t" \
  "s_waitcnt vmcnt(16)\n\t" \
  "s_barrier\n\t" \
  "s_nop 2\n\t" \
  "buffer_load_dwordx4 v8, %[so], s68 offen lds\n\t" \
  "s_add_u32 m0, %[ob], 40960\n\t" \
  "s_nop 0\n\t" \
  "buffer_load_dwordx4 v8, %[so], s69 offen lds\n\t" \
  "s_add_u32 m0, %[ob], 45056\n\t" \
  "s_nop 0\n\t" \
  "buffer_load_dwordx4 v8, %[so], s70 offen lds\n\t" \
  "s_add_u32 m0, %[ob], 102400\n\t" \
  "s_nop 0\n\t" \
  "buffer_load_dwordx4 v8, %[so], s71 offen lds\n\t" \
  "s_add_u32 m0, %[ob], 106496\n\t" \
  "s_nop 0\n\t" \
  "buffer_load_dwordx4 v8, %[so], s72 offen lds\n\t" \
  "v_readlane_b32 s68, v9, 12\n\t" \
  "v_readlane_b32 s69, v9, 13\n\t" \
  "v_readlane_b32 s70, v9, 14\n\t" \
  "v_readlane_b32 s71, v9, 27\n\t" \
  "v_readlane_b32 s72, v9, 28\n\t" \
  "v_readlane_b32 s73, v9, 29\n\t" \
  "s_add_u32 m0, %[ob], 49152\n\t" \
  "s_nop 3\n\t" \
  "s_barrier\n\t" \
  "s_nop 2\n\t" \
  "buffer_load_dwordx4 v8, %[so], s68 offen lds\n\t" \
  "s_add_u32 m0, %[ob], 53248\n\t" \
  "s_nop 0\n\t" \
  "buffer_load_dwordx4 v8, %[so], s69 offen lds\n\t" \
  "s_add_u32 m0, %[ob], 57344\n\t" \
  "s_nop 0\n\t" \
  "buffer_load_dwordx4 v8, %[so], s70 offen lds\n\t" \
  "s_add_u32 m0, %[ob], 110592\n\t" \
  "s_nop 0\n\t" \
  "buffer_load_dwordx4 v8, %[so], s71 offen lds\n\t" \
  "s_add_u32 m0, %[ob], 114688\n\t" \
  "s_nop 0\n\t" \
  "buffer_load_dwordx4 v8, %[so], s72 offen lds\n\t" \
  "s_add_u32 m0, %[ob], 118784\n\t" \
  "s_nop 0\n\t" \
  "buffer_load_dwordx4 v8, %[so], s73 offen lds\n\t" \
  "v_readlane_b32 s68, v9, 15\n\t" \
  "v_readlane_b32 s69, v9, 16\n\t" \
  "v_readlane_b32 s70, v9, 17\n\t" \
  "v_readlane_b32 s71, v9, 30\n\t" \
  "v_readlane_b32 s72, v9, 31\n\t" \
  "s_add_u32 m0, %[ob], 61440\n\t" \
  "LS_%=:\n\t" \
  "ds_read_b32 v11, v10\n\t" \
  "s_addk_i32 %[cn], 1\n\t" \
  "s_cmp_gt_u32 %[cn], 0x400000\n\t" \
  "s_cbranch_scc1 LX_%=\n\t" \
  "s_waitcnt lgkmcnt(0)\n\t" \
  "v_cmp_eq_u32 vcc, 0, v11\n\t" \
  "s_cbranch_vccz LX_%=\n\t" \
  "s_sleep 1\n\t" \
  "s_branch LS_%=\n\t" \
  "LX_%=:\n\t" \
  "s_nop 2\n\t" \
  "buffer_load_dwordx4 v8, %[so], s68 offen lds\n\t" \
  "s_add_u32 m0, %[ob], 65536\n\t" \
  "s_nop 0\n\t" \
  "buffer_load_dwordx4 v8, %[so], s69 offen lds\n\t" \
  "s_add_u32 m0, %[ob], 69632\n\t" \
  "s_nop 0\n\t" \
  "buffer_load_dwordx4 v8, %[so], s70 offen lds\n\t" \
  "s_add_u32 m0, %[ob], 122880\n\t" \
  "s_nop 0\n\t" \
  "buffer_load_dwordx4 v8, %[so], s71 offen lds\n\t" \
  "s_add_u32 m0, %[ob], 126976\n\t" \
  "s_nop 0\n\t" \
  "buffer_load_dwordx4 v8, %[so], s72 offen lds\n\t" \
  "s_waitcnt vmcnt(0) lgkmcnt(0)\n\t"
#define LD_CLOB "v11", "s68", "s69", "s70", "s71", "s72", "s73", "s74", "s75", "s76", "s77", "s78", "s79", "s80", "s81", "s82", "s83", "s84", "s85"

#define ENDW WAITV(0);
#define TS(i) do{ T[i]=__builtin_amdgcn_s_memrealtime(); }while(0)
#define TOUT(n) do{ if(l==0){ unsigned long long* dbg=(unsigned long long*)(flags+65536)+(bid*12+w8)*8; for(int i_=1;i_<(n);i_++) dbg[i_-1]=T[i_]-T[0]; dbg[6]=R0; dbg[7]=__builtin_amdgcn_s_memrealtime(); } }while(0)

#define SPIN(cond,slp) do{ int sp_=0; while(cond){ __builtin_amdgcn_s_sleep(slp); if(++sp_>(1<<22)) break; } }while(0)
__device__ __forceinline__ unsigned rd1(const char* p){ return __builtin_amdgcn_readfirstlane(*(volatile LDS_AS unsigned*)(LDS_AS char*)p); }
__device__ __forceinline__ unsigned lds_inc(const char* p, int l){ unsigned r=0; if(l==0) r=__hip_atomic_fetch_add((LDS_AS unsigned*)(LDS_AS char*)p, 1u, __ATOMIC_RELAXED, __HIP_MEMORY_SCOPE_WORKGROUP); return __builtin_amdgcn_readfirstlane(r); }
__device__ __forceinline__ void lds_add(unsigned adr){ asm volatile("s_mov_b64 exec, 1\n\tds_add_u32 %0, %1\n\ts_mov_b64 exec, -1" :: "v"(adr), "v"(1u) : "memory"); }
__device__ __forceinline__ void bars(int n){
  asm volatile("BL_%=:\n\ts_barrier\n\ts_sub_u32 %0, %0, 1\n\ts_cmp_lg_u32 %0, 0\n\ts_cbranch_scc1 BL_%=" : "+s"(n) :: "memory", "scc");
}
__device__ __forceinline__ void barc(unsigned& rem){
  asm volatile("s_cmp_eq_u32 %0, 0\n\ts_cbranch_scc1 BC_%=\n\ts_barrier\n\ts_sub_u32 %0, %0, 1\n\tBC_%=:" : "+s"(rem) :: "memory", "scc");
}
template<int SLP> __device__ __forceinline__ void wait_flag_b(const unsigned* p, unsigned voff, unsigned tag, unsigned& rem){
  int spins=0;
  while(true){
    unsigned v; asm volatile("global_load_dword %0, %1, %2 sc1" : "=v"(v) : "v"(voff), "s"(p) : "memory");
    barc(rem); if(PB2) barc(rem);
    asm volatile("s_waitcnt vmcnt(0)" : "+v"(v) :: "memory");
    if(__all(v==tag) || ++spins>(1<<21)) break;
    if(rem==0) __builtin_amdgcn_s_sleep(SLP);
  }
}
__device__ __forceinline__ void stout(void* sbase, unsigned voff, u32x4 v){
  st16cs(sbase,voff,v);
}

__global__ __launch_bounds__(768,3) void fused_kernel(const u16* __restrict__ x, const u16* __restrict__ wqkv,
    const u16* __restrict__ kin, const u16* __restrict__ vin, const u16* __restrict__ shk,
    u16* o, u16* kvT, const u16* __restrict__ hk, const u16* __restrict__ wo, const u16* __restrict__ qin,
    const u16* __restrict__ sD, u16* yT, u16* __restrict__ out, unsigned* flags)
{
  __shared__ __attribute__((aligned(128))) char smem[ALL_LDS];
  unsigned long long T[8]; unsigned long long R0=__builtin_amdgcn_s_memrealtime();
  TS(0);
  const int w8=__builtin_amdgcn_readfirstlane(threadIdx.x>>6);
  const int w=w8&3;
  const int bid=blockIdx.x, xcd=bid&7, idx=bid>>3;
  const int rb=(xcd>>1)*4+(idx&3), hp=(xcd&1)*8+(idx>>2);
  const int s0=rb*128;
  const int ps=2*(hp&7)+(hp>>3);
  #define LANEVARS int l; asm volatile("v_mbcnt_lo_u32_b32 %0, -1, 0\n\tv_mbcnt_hi_u32_b32 %0, -1, %0" : "=v"(l)); const int g=l>>4, fr=l&15, tq=(l&15)>>2, tp=l&3; (void)g; (void)fr; (void)tq; (void)tp;
  const unsigned sb0=__builtin_amdgcn_readfirstlane(lds_addr(smem));
  const char* syn=smem+SYN;
  const int hot0=(3-(int)((size_t)wqkv>>7))&7;

  if(w8>=8){
    char* cr=smem+C_OFF+2048+w*4096;
    const unsigned crs=sb0+C_OFF+2048+w*4096;
    const int sblk=bid>>3, td0=(bid&7)*128+(w&1)*64;
    const int d=(bid>>4)*64+(((bid>>3)&1)*4+w)*8+(bid&7);
    const int kv0=KV0+(sblk&KVS);
    static_assert(KV0+KVS+PP_NB<=33, "barrier budget");
    unsigned tagv; f32x8 RB;
    const int fd0=(bid&15)*64, fs0=(bid>>4)*128+w*32;
    u32x4 qv[4]; const char* qp=(const char*)(qin+(size_t)fs0*DM+fd0);
    f32x16 A0,A1,A2,A3;
    {
    LANEVARS
    if(w==0 && l<25 && l!=16) ((unsigned*)(smem+SYN))[l]=0u;
    if(l<32) *(u32x4*)(smem+C_OFF+w*512+l*16)=u32x4{0,0,0,0};
    asm volatile("" ::: "memory");
    __builtin_amdgcn_s_setprio(CPR);
    const char* hcol=(const char*)wqkv+((xcd&1)*8+hot0)*128+(size_t)idx*2*6144;
    const unsigned hto=(unsigned)((l/6)*393216+(l%6)*2048);
    {
      const int q=w*64+l, q2=(3-w)*64+l;
      if(w*64<8*TP1){ if(q<8*TP1) touch((const char*)x+(size_t)(xcd*256+idx*8)*2048, (q/TP1)*2048+(q%TP1)*128, sb0+SINK+256); }
      if((3-w)*64<12*TPW){ if(q2<12*TPW){ const int e=q2%12+(idx&1)*12; touch((const char*)wqkv+(size_t)((xcd>>1)*16+(idx>>1))*6144+(xcd&1)*1024, (q2/12)*393216+(e>>3)*2048+(e&7)*128, sb0+SINK+512); } }
      if(HOTP && w==(TP1>8? 0:1)){ if(l<48) touch(hcol+393216, hto, sb0+SINK+512); }
      if(TWO==1 && w==(TP1>8? 0:1)) touch((const char*)wo+(size_t)bid*8192, l*128, sb0+SINK+512);
    }
    {
      const int dg=l&7, rg=l>>3;
      const int rw=sblk*64+(w>>1)*32;
      const unsigned ko=(unsigned)(rg*8192+dg*16);
      const bool top=(rw==0 && rg==0);
      const char* kb=(const char*)(kin+(size_t)rw*DM+td0); const char* vb=(const char*)(vin+(size_t)rw*DM+td0);
      unsigned long long zm; asm("s_cmp_eq_u32 %1, 0\n\ts_cselect_b64 %0, 0xff, 0" : "=s"(zm) : "s"(rw) : "scc");
      unsigned long long tm; asm("s_cmp_eq_u32 %1, 0\n\ts_cselect_b64 %0, %2, 0\n\ts_cmp_eq_u32 %1, 1\n\ts_cselect_b64 %0, %3, %0" : "=&s"(tm) : "s"(w), "s"(HOTP? (1ull<<42)-1 : 0ull), "s"(TWO==3? ~0ull : 0ull) : "scc");
      const int j=rw>>5;
      unsigned so=(unsigned)(rg*32768+((j>>2)&3)*128+((dg&4)|((dg+j)&3))*16), t0;
      unsigned rb0[2];
      #pragma unroll
      for(int k=0;k<2;k++){ const int q=fr-1-2*g+k; rb0[k]=sb0+C_OFF+(4+(q>>1))*256+((q>>3)&3)*64+((w+(q>>1))&3)*16+(q&1)*8; }
      asm volatile(PP_ASM
        : [t0]"=&s"(t0), "+{v159}"(so), "=&{v160}"(tagv), "=&{v[24:31]}"(RB), "=&{v[44:47]}"(qv[0]), "=&{v[48:51]}"(qv[1]), "=&{v[52:55]}"(qv[2]), "=&{v[56:59]}"(qv[3]),
          "=&{v[64:79]}"(A0), "=&{v[80:95]}"(A1), "=&{v[96:111]}"(A2), "=&{v[112:127]}"(A3)
          , [st1]"=&s"(T[1]), [st2]"=&s"(T[2])
        : "{v152}"(top? ko+6144:ko), "{v153}"(top? ko+6144:ko+2048), "{v154}"(top? ko+6144:ko+4096), "{v155}"(ko), "{v156}"(sb0+EX_OFF+w*1024+(w&1)*512+dg*64),
          "{v157}"((l&8)? 0x03020706u : 0x05040100u), "{v166}"(rb0[0]), "{v167}"(rb0[1]), [qb0]"s"(qp), [qb2]"s"(qp+4096), [mk]"s"(0x180u), [em]"s"(1ull|(512ull<<w)), [hd]"s"(crs),
          [kb0]"s"(kb-6144), [kb3]"s"(kb), [kb5]"s"(kb+4096), [vb0]"s"(vb), [vb2]"s"(vb+4096), [sb]"s"((char*)kvT+(size_t)td0*4096+j*512),
          [n0]"s"(kv0), "{v165}"(crs+(32*g-4*tq+4*tp)*2), [ff]"s"(flags+F_PP+(td0>>6)*64+sblk*2+(w>>1)), [zm]"s"(zm), "{v163}"(l*16), [shd]"s"(sb0+EX_OFF+w*1024), [tgd]"s"(sb0+SYN+64), [shp]"s"(shk+(bid&7)*512), [fl]"s"(flags), [hp]"s"(hk+(size_t)d*S), [dp]"s"(sD+d),
          "{v164}"(w? (unsigned)(l*128) : (l<42? hto:0u)), [tsk]"s"(sb0+SINK+512), [tm]"s"(tm), [tp]"s"(w? (const char*)wo+(size_t)bid*8192 : hcol+9*393216)
        : "memory", "scc", PP_CLOB);
    }
    }
    LANEVARS
    const unsigned tag=__builtin_amdgcn_readfirstlane(tagv)+1u;
    unsigned rem=33-PP_NB-kv0;
    barc(rem);
    const unsigned* pf=flags+F_PP+(d>>6)*64;

    const char* kvp=(const char*)kvT+(size_t)((bid>>4)*8+(bid&7))*32768+((bid>>3)&1)*64+w*8192;
 const unsigned kvo=(unsigned)((l>>2)*128+(l&3)*16);
    int bidx=((l>=16)? l-16 : (l==0? 63 : l+47))*4;
    asm volatile("" : "+{v32}"(bidx));
    unsigned qo=(unsigned)((l>>3)*8192+(l&7)*16);
    asm volatile("" : "+v"(qo));
    __builtin_amdgcn_s_setprio(3);
    while(rem>PREM) barc(rem);
    wait_flag_b<8>(pf, l*4, tag, rem);
    #pragma unroll
    for(int i=0;i<4;i++){ if(i%KTH==0 && (i || KT0)) barc(rem);
      gldsc(kvp+i*2048, kvo, crs+i*1024);
    }
    #define QLOAD asm volatile("global_load_dwordx4 %0, %4, %5\n\tglobal_load_dwordx4 %1, %4, %6\n\tglobal_load_dwordx4 %2, %4, %7\n\tglobal_load_dwordx4 %3, %4, %8" \
        : "=&{v[44:47]}"(qv[0]), "=&{v[48:51]}"(qv[1]), "=&{v[52:55]}"(qv[2]), "=&{v[56:59]}"(qv[3]) : "v"(qo), "s"(qp), "s"(qp+2048), "s"(qp+4096), "s"(qp+6144) : "memory");
    barc(rem); barc(rem);
    WAITV(0);
    asm volatile("s_mov_b32 m0, %2\n\ts_mov_b64 exec, 1\n\tglobal_load_lds_dword %0, %1 sc1\n\ts_mov_b64 exec, -1" :: "v"(0u), "s"(pf), "s"(sb0+SYN+100+w*4) : "memory");
    while(rem>CREM) barc(rem);
    { int sp=0; while(!__all(*(volatile LDS_AS unsigned*)(LDS_AS char*)(syn+100+(l&3)*4)==tag) && ++sp<(1<<22)){ if(rem) barc(rem); else __builtin_amdgcn_s_sleep(1); } }
    SPIN(rd1(syn+32+w*4)==0u,1);
    __builtin_amdgcn_s_setprio(CVP);
    TS(3);
    f32x16 ac0, ac1;
    {
      unsigned p0,p1,p2,p3,p4,p5,p6;
      asm volatile("s_nop 1\n\t" CV_ASM
        : "=&{v[128:143]}"(ac0), "=&{v[144:159]}"(ac1), [p0]"=&s"(p0), [p1]"=&s"(p1), [p2]"=&s"(p2), [p3]"=&s"(p3), [p4]"=&s"(p4), [p5]"=&s"(p5), [p6]"=&s"(p6)
          , "+{v[44:47]}"(qv[0]), "+{v[48:51]}"(qv[1]), "+{v[52:55]}"(qv[2]), "+{v[56:59]}"(qv[3]), [rem]"+s"(rem)
        : "{v[64:79]}"(A0), "{v[80:95]}"(A1), "{v[96:111]}"(A2), "{v[112:127]}"(A3),
          "{v[24:31]}"(RB), "{v32}"(bidx), "{v33}"(sb0+SYN+80+w*4)
        : "memory", "scc", CV_CLOB);
    }
    TS(4);
    __builtin_amdgcn_s_setprio(3);
    {
      const unsigned yo=(unsigned)((256*(g&1)+16*fr+4*(g&2))*2);
      #pragma unroll
      for(int J=0;J<8;J+=2){
        #define YA(i) ((i)<16? ac0[(i)&15] : ac1[(i)&15])
        const u32x2 a={pack2(YA(4*J),YA(4*J+1)),pack2(YA(4*J+2),YA(4*J+3))}, b={pack2(YA(4*J+4),YA(4*J+5)),pack2(YA(4*J+6),YA(4*J+7))};
        st16cs(yT+(size_t)d*S+256*J, yo, widen(a,b));
      }
    }
    barc(rem); barc(rem);
    WAITV(0);
    if(lds_inc(syn+24,l)==3u){ if(l<16) stflag(flags+F_Y+(l*16+(bid>>4))*16+(bid&15), tag); }
    TS(5);
    if(w==0){ wait_flag_b<YSLP>(flags+F_Y+bid*16, (l&15)*4, tag, rem);
      { int sp=0; while(rd1(syn+24)<4u && ++sp<(1<<22)){ if(rem) barc(rem); else __builtin_amdgcn_s_sleep(1); } }
      PUB(sb0+(l==0? SYN+28 : SINK+l*4), 1u); }
    else { int sp=0; while(rd1(syn+28)==0u && ++sp<(1<<22)){ if(rem) barc(rem); else __builtin_amdgcn_s_sleep(4); } }
    SPIN(rd1(syn+96)==0u,2);
    #pragma unroll
    for(int i=0;i<4;i++) gldsc(yT+(size_t)(fd0+i*16)*S+fs0, (l>>2)*4096+(l&3)*16, crs+i*1024);
    barc(rem); barc(rem);
    asm volatile("s_waitcnt vmcnt(0)" : "+{v[44:47]}"(qv[0]), "+{v[48:51]}"(qv[1]), "+{v[52:55]}"(qv[2]), "+{v[56:59]}"(qv[3]) :: "memory");
    {
      u32x4 y2[4];
      #pragma unroll
      for(int i=0;i<4;i++){ const char* yp=cr+((i>>1)*32+g*8+tq)*64+(i&1)*32+tp*8; y2[i]=__builtin_bit_cast(u32x4,cat44(trread(yp),trread(yp+256))); }
      asm volatile("" : "+v"(y2[0]), "+v"(y2[1]), "+v"(y2[2]), "+v"(y2[3]) :: "memory");
      #pragma unroll
      for(int i=0;i<4;i++) *(u32x4*)(cr+((i&1)*16+fr)*128+((((i>>1)*4+g)^(fr&7))*16))=y2[i];
      asm volatile("" ::: "memory");
      char* op=(char*)(out+(size_t)fs0*2048+fd0); const unsigned oo=(unsigned)((l>>3)*16384+(l&7)*16);
      #pragma unroll
      for(int i=0;i<4;i++){
        const int row=4*(l>>3)+i;
        const u32x4 y4=*(const u32x4*)(cr+row*128+(((l&7)^(row&7))*16)), q4=qv[i]; u32x4 ov;
        #pragma unroll
        for(int e=0;e<4;e++) ov[e]=pack2(bflo(q4[e])*bflo(y4[e]), bfhi(q4[e])*bfhi(y4[e]));
        stout(op+i*4096, oo, ov);
      }
    }
    while(rem) barc(rem);
    ENDW TS(6); TOUT(7);
    return;
  }

  if(w8>=4){
    LANEVARS
    __builtin_amdgcn_s_setprio(LPR);
    unsigned ltr=0, ltr2=0, ltr3=0; (void)ltr; (void)ltr2; (void)ltr3;
    {
      const int xr=18*w+(l>>3);
      const unsigned ch=(unsigned)(((l&7)^(xr&7))*16);
      const unsigned xo=(unsigned)(xr*2048)+ch;
      const unsigned xoA=(s0==0 && xr<16)? 16*2048+ch : xo, xoB=(s0==0 && xr+8<16)? 8*2048+ch : xo;
      unsigned wof[3];
      #pragma unroll
      for(int i=0;i<3;i++){ const int P=3*w+i, wr=8*(P&3)+(l>>3); wof[i]=(unsigned)(wr*6144+(P>>2)*2048-1024*i+((((l&7)>>1)^wswz(wr))*32)+(l&1)*16); }
      const unsigned wof2=(unsigned)(w*16384+(l>>3)*2048+((((l&7)>>1)^((l>>4)&3))*32)+(l&1)*16);
      const int pos=(ps+(l>>1))&15;
      const unsigned tab=(unsigned)(((pos>>1)+8*(pos&1))*131072+(l&1)*65536);
      const size_t xb=(size_t)x+(ptrdiff_t)(s0-16)*2048, wb=(size_t)(wqkv+hp*64), ob=(size_t)(wo+hp*64);
      const u32x4 sx={(unsigned)xb,(unsigned)(xb>>32)&0xffffu,144u*2048u,0x20000u}, sw={(unsigned)wb,(unsigned)(wb>>32)&0xffffu,6291456u-hp*128u,0x20000u}, so={(unsigned)ob,(unsigned)(ob>>32)&0xffffu,2097152u-hp*128u,0x20000u};
      const int jj=(ROT && (hp&7)==hot0)? (rb&3) : 0;
      unsigned long long tm; asm("s_cmp_eq_u32 %1, 0\n\ts_cselect_b64 %0, %2, 0" : "=s"(tm) : "s"(w), "s"(WTL==2? 0xffffffffull : 0ull) : "scc");
      unsigned cn=0;
      asm volatile(LD_ASM
        : [cn]"+s"(cn)
          , [st1]"=&s"(T[1])
        : "{v0}"(xoA), "{v1}"(xoB+15360), "{v2}"(xo), "{v3}"(xo+15360), "{v4}"(xo+30720), "{v5}"(wof[0]), "{v6}"(wof[1]), "{v7}"(wof[2]), "{v8}"(wof2), "{v9}"(tab),
          "{v10}"(sb0+SYN+32+(l&3)*4), "{v12}"((l&31)*2048),
          [sx]"s"(sx), [sw]"s"(sw), [so]"s"(so), [xb]"s"(sb0+X_OFF+w*2304), [wb]"s"(sb0+w*3072), [ob]"s"(sb0+w*1024),
          [rx0]"s"(jj*128), [rx1]"s"(((jj+1)&3)*128), [rx2]"s"(((jj+2)&3)*128), [rx3]"s"(((jj+3)&3)*128),
          [rw0]"s"(jj*393216), [rw1]"s"(((jj+1)&3)*393216), [rw2]"s"(((jj+2)&3)*393216), [rw3]"s"(((jj+3)&3)*393216),
          [pw]"s"((const char*)wqkv+(size_t)idx*6144+(xcd&1)*1024+(xcd>>1)*256+w*64), [px]"s"((const char*)x+(size_t)(xcd*256+idx*8+w*2)*2048), [po]"s"((const char*)wo+(size_t)(bid*4+w)*2048),
          [tsk]"s"(sb0+SINK+256), [tm]"s"(tm), [tp]"s"((const char*)wo+(size_t)(idx*32)*2048+(xcd&1)*1024+((3-(int)((size_t)wo>>7))&7)*128)
        : "memory", "scc", "vcc", LD_CLOB);
    }
    const unsigned tag=rd1(syn+64)+1u;
    lds_add(sb0+SYN+12);
    TS(2);
    if(w==3){
      const unsigned* fp=flags+F_O+(rb*16+hp)*16; const unsigned fo=(unsigned)(((ps+l)&15)*4);
      const unsigned madr=sb0+(l==0? SYN+20 : SINK+l*4);
      SPIN(rd1(syn+12)<4u || rd1(syn+16)<4u,1);
      PUB(madr,1u);
      unsigned rdy=1; int spins=0;
      while(rdy!=0xFFFFu){
        unsigned v;
        asm volatile("s_mov_b64 exec, 0xffff\n\tglobal_atomic_or %0, %1, %3, %2 sc0 sc1\n\ts_mov_b64 exec, -1\n\ts_waitcnt vmcnt(0)" : "=&v"(v) : "v"(fo), "s"(fp), "v"(0u) : "memory");
        unsigned r=((unsigned)__ballot(v==tag)&0xFFFFu)|1u;
        if(++spins>(1<<21)) r=0xFFFFu;
        if(r!=rdy){ PUB(madr,(unsigned)__builtin_ctz(~r)); rdy=r; }
        else __builtin_amdgcn_s_sleep(OSLP);
      }
      TS(3);
      WAITV(0);
      if(bid==0){
        int sp=0;
        while(true){
          u32x4 v; asm volatile("global_load_dwordx4 %0, %1, %2 sc1\n\ts_waitcnt vmcnt(0)" : "=&v"(v) : "v"(l*16), "s"(flags+F_Y) : "memory");
          if(__all(v[0]==tag && v[1]==tag && v[2]==tag && v[3]==tag) || ++sp>(1<<20)) break;
          __builtin_amdgcn_s_sleep(12);
        }
        if(l==0) stflag(flags, tag);
      }
    } else { TS(3); }
    TOUT(4);
    return;
  }

  __builtin_amdgcn_s_setprio(GPR);
  const int h=w&1, r=w>>1;
  f32x16 e0, e1; unsigned trc=0; (void)trc;
  {
    LANEVARS
    unsigned lx0=sb0+X_OFF+fr*128+((g^(fr&7))*16), lx1=sb0+X_OFF+fr*128+(((4+g)^(fr&7))*16);
    const int sw=wswz(8*g+tq);
    unsigned lw0=sb0+(8*g+tq)*128+(unsigned)((((2*h)^sw)*32)+8*tp), lw1=sb0+(8*g+tq)*128+(unsigned)((((2*h+1)^sw)*32)+8*tp);
    char* ot=(char*)o+(size_t)(rb*16+ps)*16384+r*8192+h*2048;
    unsigned it; unsigned long long rm; asm("s_cmp_lg_u32 %1, 0\n\ts_cselect_b64 %0, -1, 0" : "=s"(rm) : "s"(r) : "scc");
    asm volatile(N2_ASM
      : [it]"=&s"(it), "+{v56}"(lx0), "+{v57}"(lx1), "+{v58}"(lw0), "+{v59}"(lw1)
        , [st1]"=&s"(T[1]), [st2]"=&s"(T[2])
      : "{v62}"(l*16), "{v63}"(sb0+EX_OFF+h*3072+l*16), [r]"s"(r), [ot]"s"(ot), [ou]"s"(ot+4096), [fa]"s"(sb0+SYN+32+w*4), [fp]"s"(sb0+SYN+32+(w^2)*4),
        [vo]"s"(2048-1024*r), [rm]"s"(rm), [z]"s"(rb|r)
      : "memory", "scc", "vcc", N2_CLOB);
  }
  TS(3);
  {
    LANEVARS
    const unsigned tag=rd1(syn+64)+1u;
    const unsigned ra=sb0+(4*g+tq)*128+8*tp; const int sw=(2*g+(tq>>1))&3;
    unsigned a0=ra+((0^sw)*32), a1=ra+((1^sw)*32), a2=ra+((2^sw)*32), a3=ra+((3^sw)*32), vo=(unsigned)(ps*16384+w*4096+l*16);
    asm volatile("" : "+{v160}"(a0), "+{v161}"(a1), "+{v162}"(a2), "+{v163}"(a3), "+{v164}"(vo));
    WAITV(0);
    if(lds_inc(syn+16,l)==3u){ if(l<16) stflag(flags+F_O+(rb*16+l)*16+ps, tag); }
    TS(4);
    __builtin_amdgcn_s_setprio(G2P);
    unsigned c=0, iss=0, th, q0, q1, cn=0;
    asm volatile(G3_ASM
      : "={v[128:143]}"(e0), "={v[144:159]}"(e1), [c]"+s"(c), [iss]"+s"(iss), [th]"=&s"(th), [t0]"=&s"(q0), [t1]"=&s"(q1), [cn]"+s"(cn),
        "+{v160}"(a0), "+{v161}"(a1), "+{v162}"(a2), "+{v163}"(a3), "+{v164}"(vo)
        , [st5]"=&s"(T[5])
      : "{v165}"(sb0+SYN+20), [go]"s"(60+w*4), [ob]"s"((const char*)o+(size_t)rb*16*16384)
      : "memory", "scc", "vcc", G3_CLOB);
  }
  LANEVARS
  {
    char* sg=smem+EX_OFF+w*2048;
    char* op=(char*)(out+(size_t)(s0+w*32)*2048+1024+hp*64); const unsigned oo=(unsigned)((l>>3)*4096+(l&7)*16);
    u32x4 ov[4];
    #pragma unroll
    for(int t=0;t<2;t++){
      #pragma unroll
      for(int j=0;j<4;j++){
        #define E2(i) (t? e1[(i)&15] : e0[(i)&15])
        *(volatile LDS_AS u32x2*)(LDS_AS char*)(sg+fr*128+(((2*j+(g>>1))^(fr&7))*16)+(g&1)*8)=u32x2{pack2(E2(4*j),E2(4*j+1)),pack2(E2(4*j+2),E2(4*j+3))};
      }
      #pragma unroll
      for(int i=0;i<2;i++) ov[2*t+i]=*(volatile LDS_AS u32x4*)(LDS_AS char*)(sg+(i*8+(l>>3))*128+(((l&7)^(l>>3))*16));
    }
    #pragma unroll
    for(int i=0;i<4;i++) stout(op+i*32768, oo, ov[i]);
    ENDW TS(6); TOUT(7);
  }
}

__global__ void amp_kernel(unsigned* flags){
  const int l=threadIdx.x;
  const unsigned long long* tb=(const unsigned long long*)(flags+65536);
  const unsigned long long ref=tb[6]-1000;
  unsigned sum=0, mx=0, mns=0xffffffffu, mxe=0, mxs=0;
  for(int q=0;q<4;q++){ const int b=q*64+l;
    unsigned v=(unsigned)tb[(b*12+0)*8+0]; sum+=v; mx=max(mx,v);
    for(int r=0;r<12;r++){ unsigned st=(unsigned)(tb[(b*12+r)*8+6]-ref), en=(unsigned)(tb[(b*12+r)*8+7]-ref); mns=min(mns,st); mxs=max(mxs,st); mxe=max(mxe,en); } }
  for(int o=32;o;o>>=1){ sum+=__shfl_xor(sum,o); mx=max(mx,(unsigned)__shfl_xor(mx,o)); mns=min(mns,(unsigned)__shfl_xor(mns,o)); mxs=max(mxs,(unsigned)__shfl_xor(mxs,o)); mxe=max(mxe,(unsigned)__shfl_xor(mxe,o)); }
  unsigned X4= 0u;
  if(X4>20000u) X4=20000u;
  const unsigned long long end=ref+mns+5000u+X4;
  int sp=0; while(__builtin_amdgcn_s_memrealtime()<end && ++sp<(1<<20)) __builtin_amdgcn_s_sleep(1);
}
extern "C" void kernel_launch(void* const* d_in, const int* in_sizes, int n_in, void* d_out, int out_size, void* d_ws, size_t ws_size, hipStream_t stream){
  const u16* q=(const u16*)d_in[0]; const u16* k=(const u16*)d_in[1]; const u16* v=(const u16*)d_in[2];
  const u16* shk=(const u16*)d_in[3]; const u16* hk=(const u16*)d_in[4]; const u16* sD=(const u16*)d_in[5];
  const u16* x=(const u16*)d_in[6]; const u16* wqkv=(const u16*)d_in[7]; const u16* wo=(const u16*)d_in[8];
  u16* out=(u16*)d_out; u16* ws=(u16*)d_ws;
  u16* o=ws; u16* kvT=ws+(size_t)S*DM; u16* yT=kvT+(size_t)S*DM;
  unsigned* cnt=(unsigned*)(ws+(size_t)6*S*DM);
  fused_kernel<<<256,768,0,stream>>>(x,wqkv,k,v,shk,o,kvT,hk,wo,q,sD,yT,out,cnt);
  amp_kernel<<<1,64,0,stream>>>(cnt);
}
```

```cpp
#include <hip/hip_runtime.h>
#include <hip/hip_bf16.h>
#include <stdint.h>

typedef unsigned short u16;
typedef short bf16x8 __attribute__((ext_vector_type(8)));
typedef short s16x4 __attribute__((ext_vector_type(4)));
typedef float f32x2 __attribute__((ext_vector_type(2)));
typedef float f32x4 __attribute__((ext_vector_type(4)));
typedef float f32x8 __attribute__((ext_vector_type(8)));
typedef float f32x16 __attribute__((ext_vector_type(16)));
typedef unsigned u32x4 __attribute__((ext_vector_type(4)));
typedef unsigned u32x2 __attribute__((ext_vector_type(2)));
typedef __bf16 bf16x2_t __attribute__((ext_vector_type(2)));
#define LDS_AS __attribute__((address_space(3)))

__device__ __forceinline__ float bflo(unsigned x){ return __uint_as_float(x<<16); }
__device__ __forceinline__ float bfhi(unsigned x){ return __uint_as_float(x&0xffff0000u); }
__device__ __forceinline__ unsigned pack2(float lo, float hi){ return __builtin_bit_cast(unsigned, __builtin_convertvector(f32x2{lo,hi}, bf16x2_t)); }
__device__ __forceinline__ float mul1(float a, float b){ float r; asm("v_mul_f32 %0, %1, %2" : "=v"(r) : "v"(a), "v"(b)); return r; }
__device__ __forceinline__ float fma1(float a, float b, float c){ float r; asm("v_fma_f32 %0, %1, %2, %3" : "=v"(r) : "v"(a), "v"(b), "v"(c)); return r; }

__device__ __forceinline__ void glds(const void* sbase, unsigned voff, unsigned ldsaddr){
  asm volatile("s_mov_b32 m0, %2\n\ts_nop 0\n\tglobal_load_lds_dwordx4 %0, %1" :: "v"(voff), "s"(sbase), "s"(ldsaddr) : "memory");
}
__device__ __forceinline__ void gldsc(const void* sbase, unsigned voff, unsigned ldsaddr){
  asm volatile("s_mov_b32 m0, %2\n\ts_nop 0\n\tglobal_load_lds_dwordx4 %0, %1 sc1" :: "v"(voff), "s"(sbase), "s"(ldsaddr) : "memory");
}
__device__ __forceinline__ void touch(const void* sbase, unsigned voff, unsigned ldsaddr){
  asm volatile("s_mov_b32 m0, %2\n\ts_nop 0\n\tglobal_load_lds_dword %0, %1" :: "v"(voff), "s"(sbase), "s"(ldsaddr) : "memory");
}
__device__ __forceinline__ void st16c(void* p, u32x4 v){ asm volatile("global_store_dwordx4 %0, %1, off sc1\n\ts_nop 1" :: "v"(p), "v"(v) : "memory"); }
__device__ __forceinline__ void st16cs(void* sbase, unsigned voff, u32x4 v){ asm volatile("global_store_dwordx4 %0, %1, %2 sc1\n\ts_nop 1" :: "v"(voff), "v"(v), "s"(sbase) : "memory"); }
#define GLB_AS __attribute__((address_space(1)))
__device__ __forceinline__ unsigned ldflag(const unsigned* p){ return __hip_atomic_load((const GLB_AS unsigned*)p, __ATOMIC_RELAXED, __HIP_MEMORY_SCOPE_AGENT); }
__device__ __forceinline__ void stflag(unsigned* p, unsigned v){ __hip_atomic_store((GLB_AS unsigned*)p, v, __ATOMIC_RELAXED, __HIP_MEMORY_SCOPE_AGENT); }
template<int SLP> __device__ __forceinline__ void wait_flag(const unsigned* p, unsigned tag){
  int spins=0;
  while(true){ unsigned v=ldflag(p); if(__all(v==tag)) break; __builtin_amdgcn_s_sleep(SLP); if(++spins>(1<<21)) break; }
}
__device__ __forceinline__ unsigned lds_addr(const void* p){ return (unsigned)(size_t)(const LDS_AS void*)p; }
#define WAITV(n) asm volatile("s_waitcnt vmcnt(" #n ")":::"memory")
#define WAITL() asm volatile("s_waitcnt lgkmcnt(0)":::"memory")
#define SBAR() asm volatile("s_barrier":::"memory")
#define PUB(addr,val) asm volatile("ds_write_b32 %0, %1" :: "v"(addr), "v"(val) : "memory")
__device__ __forceinline__ s16x4 trread(const char* p){ return __builtin_amdgcn_ds_read_tr16_b64_v4i16((LDS_AS s16x4*)p); }
__device__ __forceinline__ bf16x8 cat44(s16x4 a, s16x4 b){ return __builtin_shufflevector(a,b,0,1,2,3,4,5,6,7); }
__device__ __forceinline__ int wswz(int row){ return ((row>>1)&1) | (((row>>3)&1)<<1); }
__device__ __forceinline__ u32x4 widen(u32x2 a, u32x2 b){
  auto r0=__builtin_amdgcn_permlane16_swap(a[0],b[0],false,false);
  auto r1=__builtin_amdgcn_permlane16_swap(a[1],b[1],false,false);
  return u32x4{r0[0],r1[0],r0[1],r1[1]};
}

constexpr int S=2048, DM=1024;
constexpr int WS=12288, XS=18432, X_OFF=6*WS;
constexpr int CREG=4608, C_OFF=131072;
constexpr int EX_OFF=C_OFF+4*CREG;
constexpr int SYN=EX_OFF+8192;
constexpr int SINK=SYN+256;
constexpr int ALL_LDS=SINK+768;
constexpr int F_PP=1024, F_O=4096, F_Y=8192;
#define KV0 1
#define KVS 3
#define LPR 2
#define CPR 2
#define GPR 1
#define CGATE 0
#define OSLP 2
#define YSLP 8
#define TPX 16
#define TPW 2
#define TWO 0
#define CVP 1
#define G2P 0
#define OUTC 1
#define ROT 1
#define WTL 2
#define PB2 1
#define PREM 9
#define KTH 1
#define YDEF 3
#define OPOL 1
#define CODEL 280
#define HLAST 1

#define N2_ASM \
  "v_mov_b32 v64, 0\n\t" \
  "v_mov_b32 v65, 0\n\t" \
  "v_mov_b32 v66, 0\n\t" \
  "v_mov_b32 v67, 0\n\t" \
  "v_mov_b32 v68, 0\n\t" \
  "v_mov_b32 v69, 0\n\t" \
  "v_mov_b32 v70, 0\n\t" \
  "v_mov_b32 v71, 0\n\t" \
  "v_mov_b32 v72, 0\n\t" \
  "v_mov_b32 v73, 0\n\t" \
  "v_mov_b32 v74, 0\n\t" \
  "v_mov_b32 v75, 0\n\t" \
  "v_mov_b32 v76, 0\n\t" \
  "v_mov_b32 v77, 0\n\t" \
  "v_mov_b32 v78, 0\n\t" \
  "v_mov_b32 v79, 0\n\t" \
  "v_mov_b32 v80, 0\n\t" \
  "v_mov_b32 v81, 0\n\t" \
  "v_mov_b32 v82, 0\n\t" \
  "v_mov_b32 v83, 0\n\t" \
  "v_mov_b32 v84, 0\n\t" \
  "v_mov_b32 v85, 0\n\t" \
  "v_mov_b32 v86, 0\n\t" \
  "v_mov_b32 v87, 0\n\t" \
  "v_mov_b32 v88, 0\n\t" \
  "v_mov_b32 v89, 0\n\t" \
  "v_mov_b32 v90, 0\n\t" \
  "v_mov_b32 v91, 0\n\t" \
  "v_mov_b32 v92, 0\n\t" \
  "v_mov_b32 v93, 0\n\t" \
  "v_mov_b32 v94, 0\n\t" \
  "v_mov_b32 v95, 0\n\t" \
  "v_mov_b32 v96, 0\n\t" \
  "v_mov_b32 v97, 0\n\t" \
  "v_mov_b32 v98, 0\n\t" \
  "v_mov_b32 v99, 0\n\t" \
  "v_mov_b32 v100, 0\n\t" \
  "v_mov_b32 v101, 0\n\t" \
  "v_mov_b32 v102, 0\n\t" \
  "v_mov_b32 v103, 0\n\t" \
  "v_mov_b32 v104, 0\n\t" \
  "v_mov_b32 v105, 0\n\t" \
  "v_mov_b32 v106, 0\n\t" \
  "v_mov_b32 v107, 0\n\t" \
  "v_mov_b32 v108, 0\n\t" \
  "v_mov_b32 v109, 0\n\t" \
  "v_mov_b32 v110, 0\n\t" \
  "v_mov_b32 v111, 0\n\t" \
  "v_mov_b32 v112, 0\n\t" \
  "v_mov_b32 v113, 0\n\t" \
  "v_mov_b32 v114, 0\n\t" \
  "v_mov_b32 v115, 0\n\t" \
  "v_mov_b32 v116, 0\n\t" \
  "v_mov_b32 v117, 0\n\t" \
  "v_mov_b32 v118, 0\n\t" \
  "v_mov_b32 v119, 0\n\t" \
  "v_mov_b32 v120, 0\n\t" \
  "v_mov_b32 v121, 0\n\t" \
  "v_mov_b32 v122, 0\n\t" \
  "v_mov_b32 v123, 0\n\t" \
  "v_mov_b32 v124, 0\n\t" \
  "v_mov_b32 v125, 0\n\t" \
  "v_mov_b32 v126, 0\n\t" \
  "v_mov_b32 v127, 0\n\t" \
  "v_mov_b32 v128, 0\n\t" \
  "v_mov_b32 v129, 0\n\t" \
  "v_mov_b32 v130, 0\n\t" \
  "v_mov_b32 v131, 0\n\t" \
  "v_mov_b32 v132, 0\n\t" \
  "v_mov_b32 v133, 0\n\t" \
  "v_mov_b32 v134, 0\n\t" \
  "v_mov_b32 v135, 0\n\t" \
  "v_mov_b32 v136, 0\n\t" \
  "v_mov_b32 v137, 0\n\t" \
  "v_mov_b32 v138, 0\n\t" \
  "v_mov_b32 v139, 0\n\t" \
  "v_mov_b32 v140, 0\n\t" \
  "v_mov_b32 v141, 0\n\t" \
  "v_mov_b32 v142, 0\n\t" \
  "v_mov_b32 v143, 0\n\t" \
  "v_mov_b32 v144, 0\n\t" \
  "v_mov_b32 v145, 0\n\t" \
  "v_mov_b32 v146, 0\n\t" \
  "v_mov_b32 v147, 0\n\t" \
  "v_mov_b32 v148, 0\n\t" \
  "v_mov_b32 v149, 0\n\t" \
  "v_mov_b32 v150, 0\n\t" \
  "v_mov_b32 v151, 0\n\t" \
  "v_mov_b32 v152, 0\n\t" \
  "v_mov_b32 v153, 0\n\t" \
  "v_mov_b32 v154, 0\n\t" \
  "v_mov_b32 v155, 0\n\t" \
  "v_mov_b32 v156, 0\n\t" \
  "v_mov_b32 v157, 0\n\t" \
  "v_mov_b32 v158, 0\n\t" \
  "v_mov_b32 v159, 0\n\t" \
  "v_mov_b32 v160, 0\n\t" \
  "v_mov_b32 v161, 0\n\t" \
  "v_mov_b32 v162, 0\n\t" \
  "v_mov_b32 v163, 0\n\t" \
  "v_mov_b32 v164, 0\n\t" \
  "v_mov_b32 v165, 0\n\t" \
  "v_mov_b32 v166, 0\n\t" \
  "v_mov_b32 v167, 0\n\t" \
  "s_mov_b32 s68, 0x50000\n\t" \
  "s_mov_b32 s69, 0x5550055\n\t" \
  "s_mov_b32 s70, 0xa0000\n\t" \
  "s_mov_b32 s71, 0xaaa00aa\n\t" \
  "s_mov_b32 s72, 0x150001\n\t" \
  "s_mov_b32 s73, 0x15550155\n\t" \
  "s_mov_b32 s74, 0x2a0002\n\t" \
  "s_mov_b32 s75, 0x2aaa02aa\n\t" \
  "s_mov_b32 s76, 0x55505555\n\t" \
  "s_mov_b32 s77, 0x50005500\n\t" \
  "s_mov_b32 s78, 0xaaa0aaaa\n\t" \
  "s_mov_b32 s79, 0xa000aa00\n\t" \
  "s_mov_b32 s80, 0x55405554\n\t" \
  "s_mov_b32 s81, 0x40005400\n\t" \
  "s_mov_b32 s82, 0xaa80aaa8\n\t" \
  "s_mov_b32 s83, 0x8000a800\n\t" \
  "s_mov_b32 s84, 0xaaaaaaaa\n\t" \
  "s_mov_b32 s85, 0xaaaaaaaa\n\t" \
  "v_add_u32 v60, 36864, v58\n\t" \
  "v_add_u32 v61, 36864, v59\n\t" \
  "s_mov_b32 %[it], 0\n\t" \
  "s_cmp_lg_u32 %[r], 0\n\t" \
  "s_barrier\n\t" \
  "s_cbranch_scc1 NB_%=\n\t" \
  "ds_read_b64_tr_b16 v[40:41], v60 offset:16384\n\t" \
  "ds_read_b64_tr_b16 v[42:43], v60 offset:16896\n\t" \
  "ds_read_b64_tr_b16 v[44:45], v60 offset:20480\n\t" \
  "ds_read_b64_tr_b16 v[46:47], v60 offset:20992\n\t" \
  "ds_read_b64_tr_b16 v[48:49], v61 offset:16384\n\t" \
  "ds_read_b64_tr_b16 v[50:51], v61 offset:16896\n\t" \
  "ds_read_b128 v[0:3], v56 offset:36864\n\t" \
  "ds_read_b128 v[4:7], v56 offset:38912\n\t" \
  "ds_read_b128 v[8:11], v56 offset:40960\n\t" \
  "ds_read_b128 v[12:15], v56 offset:43008\n\t" \
  "ds_read_b128 v[16:19], v56 offset:45056\n\t" \
  "s_waitcnt lgkmcnt(0)\n\t" \
  "s_branch N4a_%=\n\t" \
  "N0a_%=:\n\t" \
  "s_waitcnt lgkmcnt(4)\n\t" \
  "v_mfma_f32_16x16x32_bf16 v[160:163], v[40:43], v[0:3], v[160:163]\n\t" \
  "ds_read_b64_tr_b16 v[52:53], v59 offset:8192\n\t" \
  "v_mfma_f32_16x16x32_bf16 v[96:99], v[40:43], v[4:7], v[96:99]\n\t" \
  "ds_read_b64_tr_b16 v[54:55], v59 offset:8704\n\t" \
  "v_mfma_f32_16x16x32_bf16 v[104:107], v[40:43], v[8:11], v[104:107]\n\t" \
  "s_barrier\n\t" \
  "v_mfma_f32_16x16x32_bf16 v[112:115], v[40:43], v[12:15], v[112:115]\n\t" \
  "v_mfma_f32_16x16x32_bf16 v[120:123], v[40:43], v[16:19], v[120:123]\n\t" \
  "s_waitcnt lgkmcnt(4)\n\t" \
  "v_mfma_f32_16x16x32_bf16 v[128:131], v[4:7], v[44:47], v[128:131]\n\t" \
  "ds_read_b64_tr_b16 v[40:41], v58 offset:0\n\t" \
  "v_mfma_f32_16x16x32_bf16 v[136:139], v[8:11], v[44:47], v[136:139]\n\t" \
  "ds_read_b64_tr_b16 v[42:43], v58 offset:512\n\t" \
  "v_mfma_f32_16x16x32_bf16 v[144:147], v[12:15], v[44:47], v[144:147]\n\t" \
  "ds_read_b128 v[20:23], v57 offset:0\n\t" \
  "v_mfma_f32_16x16x32_bf16 v[152:155], v[16:19], v[44:47], v[152:155]\n\t" \
  "ds_read_b128 v[24:27], v57 offset:2048\n\t" \
  "s_waitcnt lgkmcnt(6)\n\t" \
  "v_mfma_f32_16x16x32_bf16 v[164:167], v[48:51], v[0:3], v[164:167]\n\t" \
  "ds_read_b64_tr_b16 v[44:45], v59 offset:0\n\t" \
  "v_mfma_f32_16x16x32_bf16 v[100:103], v[48:51], v[4:7], v[100:103]\n\t" \
  "ds_read_b64_tr_b16 v[46:47], v59 offset:512\n\t" \
  "v_mfma_f32_16x16x32_bf16 v[108:111], v[48:51], v[8:11], v[108:111]\n\t" \
  "ds_read_b128 v[28:31], v57 offset:4096\n\t" \
  "v_mfma_f32_16x16x32_bf16 v[116:119], v[48:51], v[12:15], v[116:119]\n\t" \
  "ds_read_b128 v[32:35], v57 offset:6144\n\t" \
  "v_mfma_f32_16x16x32_bf16 v[124:127], v[48:51], v[16:19], v[124:127]\n\t" \
  "s_waitcnt lgkmcnt(8)\n\t" \
  "v_mfma_f32_16x16x32_bf16 v[132:135], v[4:7], v[52:55], v[132:135]\n\t" \
  "ds_read_b64_tr_b16 v[48:49], v58 offset:16384\n\t" \
  "v_mfma_f32_16x16x32_bf16 v[140:143], v[8:11], v[52:55], v[140:143]\n\t" \
  "ds_read_b64_tr_b16 v[50:51], v58 offset:16896\n\t" \
  "v_mfma_f32_16x16x32_bf16 v[148:151], v[12:15], v[52:55], v[148:151]\n\t" \
  "ds_read_b128 v[36:39], v57 offset:8192\n\t" \
  "v_mfma_f32_16x16x32_bf16 v[156:159], v[16:19], v[52:55], v[156:159]\n\t" \
  "s_waitcnt lgkmcnt(9)\n\t" \
  "v_mfma_f32_16x16x32_bf16 v[64:67], v[40:43], v[4:7], v[64:67]\n\t" \
  "ds_read_b64_tr_b16 v[52:53], v58 offset:20480\n\t" \
  "v_mfma_f32_16x16x32_bf16 v[72:75], v[40:43], v[8:11], v[72:75]\n\t" \
  "ds_read_b64_tr_b16 v[54:55], v58 offset:20992\n\t" \
  "v_mfma_f32_16x16x32_bf16 v[80:83], v[40:43], v[12:15], v[80:83]\n\t" \
  "v_mfma_f32_16x16x32_bf16 v[88:91], v[40:43], v[16:19], v[88:91]\n\t" \
  "s_waitcnt lgkmcnt(7)\n\t" \
  "v_mfma_f32_16x16x32_bf16 v[68:71], v[44:47], v[4:7], v[68:71]\n\t" \
  "ds_read_b64_tr_b16 v[40:41], v59 offset:16384\n\t" \
  "v_mfma_f32_16x16x32_bf16 v[76:79], v[44:47], v[8:11], v[76:79]\n\t" \
  "ds_read_b64_tr_b16 v[42:43], v59 offset:16896\n\t" \
  "v_mfma_f32_16x16x32_bf16 v[84:87], v[44:47], v[12:15], v[84:87]\n\t" \
  "v_mfma_f32_16x16x32_bf16 v[92:95], v[44:47], v[16:19], v[92:95]\n\t" \
  "N1a_%=:\n\t" \
  "s_waitcnt lgkmcnt(4)\n\t" \
  "v_mfma_f32_16x16x32_bf16 v[160:163], v[48:51], v[20:23], v[160:163]\n\t" \
  "ds_read_b64_tr_b16 v[44:45], v59 offset:20480\n\t" \
  "v_mfma_f32_16x16x32_bf16 v[96:99], v[48:51], v[24:27], v[96:99]\n\t" \
  "ds_read_b64_tr_b16 v[46:47], v59 offset:20992\n\t" \
  "v_mfma_f32_16x16x32_bf16 v[104:107], v[48:51], v[28:31], v[104:107]\n\t" \
  "s_barrier\n\t" \
  "v_mfma_f32_16x16x32_bf16 v[112:115], v[48:51], v[32:35], v[112:115]\n\t" \
  "v_mfma_f32_16x16x32_bf16 v[120:123], v[48:51], v[36:39], v[120:123]\n\t" \
  "s_waitcnt lgkmcnt(4)\n\t" \
  "v_mfma_f32_16x16x32_bf16 v[128:131], v[24:27], v[52:55], v[128:131]\n\t" \
  "ds_read_b64_tr_b16 v[48:49], v58 offset:12288\n\t" \
  "v_mfma_f32_16x16x32_bf16 v[136:139], v[28:31], v[52:55], v[136:139]\n\t" \
  "ds_read_b64_tr_b16 v[50:51], v58 offset:12800\n\t" \
  "v_mfma_f32_16x16x32_bf16 v[144:147], v[32:35], v[52:55], v[144:147]\n\t" \
  "ds_read_b128 v[0:3], v56 offset:18432\n\t" \
  "v_mfma_f32_16x16x32_bf16 v[152:155], v[36:39], v[52:55], v[152:155]\n\t" \
  "ds_read_b128 v[4:7], v56 offset:20480\n\t" \
  "s_waitcnt lgkmcnt(6)\n\t" \
  "v_mfma_f32_16x16x32_bf16 v[164:167], v[40:43], v[20:23], v[164:167]\n\t" \
  "ds_read_b64_tr_b16 v[52:53], v59 offset:12288\n\t" \
  "v_mfma_f32_16x16x32_bf16 v[100:103], v[40:43], v[24:27], v[100:103]\n\t" \
  "ds_read_b64_tr_b16 v[54:55], v59 offset:12800\n\t" \
  "v_mfma_f32_16x16x32_bf16 v[108:111], v[40:43], v[28:31], v[108:111]\n\t" \
  "ds_read_b128 v[8:11], v56 offset:22528\n\t" \
  "v_mfma_f32_16x16x32_bf16 v[116:119], v[40:43], v[32:35], v[116:119]\n\t" \
  "ds_read_b128 v[12:15], v56 offset:24576\n\t" \
  "v_mfma_f32_16x16x32_bf16 v[124:127], v[40:43], v[36:39], v[124:127]\n\t" \
  "s_waitcnt lgkmcnt(8)\n\t" \
  "v_mfma_f32_16x16x32_bf16 v[132:135], v[24:27], v[44:47], v[132:135]\n\t" \
  "ds_read_b64_tr_b16 v[40:41], v58 offset:28672\n\t" \
  "v_mfma_f32_16x16x32_bf16 v[140:143], v[28:31], v[44:47], v[140:143]\n\t" \
  "ds_read_b64_tr_b16 v[42:43], v58 offset:29184\n\t" \
  "v_mfma_f32_16x16x32_bf16 v[148:151], v[32:35], v[44:47], v[148:151]\n\t" \
  "ds_read_b128 v[16:19], v56 offset:26624\n\t" \
  "v_mfma_f32_16x16x32_bf16 v[156:159], v[36:39], v[44:47], v[156:159]\n\t" \
  "s_waitcnt lgkmcnt(9)\n\t" \
  "v_mfma_f32_16x16x32_bf16 v[64:67], v[48:51], v[24:27], v[64:67]\n\t" \
  "ds_read_b64_tr_b16 v[44:45], v58 offset:32768\n\t" \
  "v_mfma_f32_16x16x32_bf16 v[72:75], v[48:51], v[28:31], v[72:75]\n\t" \
  "ds_read_b64_tr_b16 v[46:47], v58 offset:33280\n\t" \
  "v_mfma_f32_16x16x32_bf16 v[80:83], v[48:51], v[32:35], v[80:83]\n\t" \
  "v_mfma_f32_16x16x32_bf16 v[88:91], v[48:51], v[36:39], v[88:91]\n\t" \
  "s_waitcnt lgkmcnt(7)\n\t" \
  "v_mfma_f32_16x16x32_bf16 v[68:71], v[52:55], v[24:27], v[68:71]\n\t" \
  "ds_read_b64_tr_b16 v[48:49], v59 offset:28672\n\t" \
  "v_mfma_f32_16x16x32_bf16 v[76:79], v[52:55], v[28:31], v[76:79]\n\t" \
  "ds_read_b64_tr_b16 v[50:51], v59 offset:29184\n\t" \
  "v_mfma_f32_16x16x32_bf16 v[84:87], v[52:55], v[32:35], v[84:87]\n\t" \
  "v_mfma_f32_16x16x32_bf16 v[92:95], v[52:55], v[36:39], v[92:95]\n\t" \
  "N2a_%=:\n\t" \
  "s_waitcnt lgkmcnt(4)\n\t" \
  "v_mfma_f32_16x16x32_bf16 v[160:163], v[40:43], v[0:3], v[160:163]\n\t" \
  "ds_read_b64_tr_b16 v[52:53], v59 offset:32768\n\t" \
  "v_mfma_f32_16x16x32_bf16 v[96:99], v[40:43], v[4:7], v[96:99]\n\t" \
  "ds_read_b64_tr_b16 v[54:55], v59 offset:33280\n\t" \
  "v_mfma_f32_16x16x32_bf16 v[104:107], v[40:43], v[8:11], v[104:107]\n\t" \
  "s_barrier\n\t" \
  "v_mfma_f32_16x16x32_bf16 v[112:115], v[40:43], v[12:15], v[112:115]\n\t" \
  "v_mfma_f32_16x16x32_bf16 v[120:123], v[40:43], v[16:19], v[120:123]\n\t" \
  "s_waitcnt lgkmcnt(4)\n\t" \
  "v_mfma_f32_16x16x32_bf16 v[128:131], v[4:7], v[44:47], v[128:131]\n\t" \
  "ds_read_b64_tr_b16 v[40:41], v58 offset:24576\n\t" \
  "v_mfma_f32_16x16x32_bf16 v[136:139], v[8:11], v[44:47], v[136:139]\n\t" \
  "ds_read_b64_tr_b16 v[42:43], v58 offset:25088\n\t" \
  "v_mfma_f32_16x16x32_bf16 v[144:147], v[12:15], v[44:47], v[144:147]\n\t" \
  "ds_read_b128 v[20:23], v57 offset:18432\n\t" \
  "v_mfma_f32_16x16x32_bf16 v[152:155], v[16:19], v[44:47], v[152:155]\n\t" \
  "ds_read_b128 v[24:27], v57 offset:20480\n\t" \
  "s_waitcnt lgkmcnt(6)\n\t" \
  "v_mfma_f32_16x16x32_bf16 v[164:167], v[48:51], v[0:3], v[164:167]\n\t" \
  "ds_read_b64_tr_b16 v[44:45], v59 offset:24576\n\t" \
  "v_mfma_f32_16x16x32_bf16 v[100:103], v[48:51], v[4:7], v[100:103]\n\t" \
  "ds_read_b64_tr_b16 v[46:47], v59 offset:25088\n\t" \
  "v_mfma_f32_16x16x32_bf16 v[108:111], v[48:51], v[8:11], v[108:111]\n\t" \
  "ds_read_b128 v[28:31], v57 offset:22528\n\t" \
  "v_mfma_f32_16x16x32_bf16 v[116:119], v[48:51], v[12:15], v[116:119]\n\t" \
  "ds_read_b128 v[32:35], v57 offset:24576\n\t" \
  "v_mfma_f32_16x16x32_bf16 v[124:127], v[48:51], v[16:19], v[124:127]\n\t" \
  "s_waitcnt lgkmcnt(8)\n\t" \
  "v_mfma_f32_16x16x32_bf16 v[132:135], v[4:7], v[52:55], v[132:135]\n\t" \
  "ds_read_b64_tr_b16 v[48:49], v60 offset:4096\n\t" \
  "v_mfma_f32_16x16x32_bf16 v[140:143], v[8:11], v[52:55], v[140:143]\n\t" \
  "ds_read_b64_tr_b16 v[50:51], v60 offset:4608\n\t" \
  "v_mfma_f32_16x16x32_bf16 v[148:151], v[12:15], v[52:55], v[148:151]\n\t" \
  "ds_read_b128 v[36:39], v57 offset:26624\n\t" \
  "v_mfma_f32_16x16x32_bf16 v[156:159], v[16:19], v[52:55], v[156:159]\n\t" \
  "s_waitcnt lgkmcnt(9)\n\t" \
  "v_mfma_f32_16x16x32_bf16 v[64:67], v[40:43], v[4:7], v[64:67]\n\t" \
  "ds_read_b64_tr_b16 v[52:53], v60 offset:8192\n\t" \
  "v_mfma_f32_16x16x32_bf16 v[72:75], v[40:43], v[8:11], v[72:75]\n\t" \
  "ds_read_b64_tr_b16 v[54:55], v60 offset:8704\n\t" \
  "v_mfma_f32_16x16x32_bf16 v[80:83], v[40:43], v[12:15], v[80:83]\n\t" \
  "v_mfma_f32_16x16x32_bf16 v[88:91], v[40:43], v[16:19], v[88:91]\n\t" \
  "s_waitcnt lgkmcnt(7)\n\t" \
  "v_mfma_f32_16x16x32_bf16 v[68:71], v[44:47], v[4:7], v[68:71]\n\t" \
  "ds_read_b64_tr_b16 v[40:41], v61 offset:4096\n\t" \
  "v_mfma_f32_16x16x32_bf16 v[76:79], v[44:47], v[8:11], v[76:79]\n\t" \
  "ds_read_b64_tr_b16 v[42:43], v61 offset:4608\n\t" \
  "v_mfma_f32_16x16x32_bf16 v[84:87], v[44:47], v[12:15], v[84:87]\n\t" \
  "v_mfma_f32_16x16x32_bf16 v[92:95], v[44:47], v[16:19], v[92:95]\n\t" \
  "N3a_%=:\n\t" \
  "s_waitcnt lgkmcnt(4)\n\t" \
  "v_mfma_f32_16x16x32_bf16 v[160:163], v[48:51], v[20:23], v[160:163]\n\t" \
  "ds_read_b64_tr_b16 v[44:45], v61 offset:8192\n\t" \
  "v_mfma_f32_16x16x32_bf16 v[96:99], v[48:51], v[24:27], v[96:99]\n\t" \
  "ds_read_b64_tr_b16 v[46:47], v61 offset:8704\n\t" \
  "v_mfma_f32_16x16x32_bf16 v[104:107], v[48:51], v[28:31], v[104:107]\n\t" \
  "s_barrier\n\t" \
  "v_mfma_f32_16x16x32_bf16 v[112:115], v[48:51], v[32:35], v[112:115]\n\t" \
  "v_mfma_f32_16x16x32_bf16 v[120:123], v[48:51], v[36:39], v[120:123]\n\t" \
  "s_waitcnt lgkmcnt(4)\n\t" \
  "v_mfma_f32_16x16x32_bf16 v[128:131], v[24:27], v[52:55], v[128:131]\n\t" \
  "ds_read_b64_tr_b16 v[48:49], v60 offset:0\n\t" \
  "v_mfma_f32_16x16x32_bf16 v[136:139], v[28:31], v[52:55], v[136:139]\n\t" \
  "ds_read_b64_tr_b16 v[50:51], v60 offset:512\n\t" \
  "v_mfma_f32_16x16x32_bf16 v[144:147], v[32:35], v[52:55], v[144:147]\n\t" \
  "ds_read_b128 v[0:3], v56 offset:36864\n\t" \
  "v_mfma_f32_16x16x32_bf16 v[152:155], v[36:39], v[52:55], v[152:155]\n\t" \
  "ds_read_b128 v[4:7], v56 offset:38912\n\t" \
  "s_waitcnt lgkmcnt(6)\n\t" \
  "v_mfma_f32_16x16x32_bf16 v[164:167], v[40:43], v[20:23], v[164:167]\n\t" \
  "ds_read_b64_tr_b16 v[52:53], v61 offset:0\n\t" \
  "v_mfma_f32_16x16x32_bf16 v[100:103], v[40:43], v[24:27], v[100:103]\n\t" \
  "ds_read_b64_tr_b16 v[54:55], v61 offset:512\n\t" \
  "v_mfma_f32_16x16x32_bf16 v[108:111], v[40:43], v[28:31], v[108:111]\n\t" \
  "ds_read_b128 v[8:11], v56 offset:40960\n\t" \
  "v_mfma_f32_16x16x32_bf16 v[116:119], v[40:43], v[32:35], v[116:119]\n\t" \
  "ds_read_b128 v[12:15], v56 offset:43008\n\t" \
  "v_mfma_f32_16x16x32_bf16 v[124:127], v[40:43], v[36:39], v[124:127]\n\t" \
  "s_waitcnt lgkmcnt(8)\n\t" \
  "v_mfma_f32_16x16x32_bf16 v[132:135], v[24:27], v[44:47], v[132:135]\n\t" \
  "ds_read_b64_tr_b16 v[40:41], v60 offset:16384\n\t" \
  "v_mfma_f32_16x16x32_bf16 v[140:143], v[28:31], v[44:47], v[140:143]\n\t" \
  "ds_read_b64_tr_b16 v[42:43], v60 offset:16896\n\t" \
  "v_mfma_f32_16x16x32_bf16 v[148:151], v[32:35], v[44:47], v[148:151]\n\t" \
  "ds_read_b128 v[16:19], v56 offset:45056\n\t" \
  "v_mfma_f32_16x16x32_bf16 v[156:159], v[36:39], v[44:47], v[156:159]\n\t" \
  "s_waitcnt lgkmcnt(9)\n\t" \
  "v_mfma_f32_16x16x32_bf16 v[64:67], v[48:51], v[24:27], v[64:67]\n\t" \
  "ds_read_b64_tr_b16 v[44:45], v60 offset:20480\n\t" \
  "v_mfma_f32_16x16x32_bf16 v[72:75], v[48:51], v[28:31], v[72:75]\n\t" \
  "ds_read_b64_tr_b16 v[46:47], v60 offset:20992\n\t" \
  "v_mfma_f32_16x16x32_bf16 v[80:83], v[48:51], v[32:35], v[80:83]\n\t" \
  "v_mfma_f32_16x16x32_bf16 v[88:91], v[48:51], v[36:39], v[88:91]\n\t" \
  "s_waitcnt lgkmcnt(7)\n\t" \
  "v_mfma_f32_16x16x32_bf16 v[68:71], v[52:55], v[24:27], v[68:71]\n\t" \
  "ds_read_b64_tr_b16 v[48:49], v61 offset:16384\n\t" \
  "v_mfma_f32_16x16x32_bf16 v[76:79], v[52:55], v[28:31], v[76:79]\n\t" \
  "ds_read_b64_tr_b16 v[50:51], v61 offset:16896\n\t" \
  "v_mfma_f32_16x16x32_bf16 v[84:87], v[52:55], v[32:35], v[84:87]\n\t" \
  "v_mfma_f32_16x16x32_bf16 v[92:95], v[52:55], v[36:39], v[92:95]\n\t" \
  "N4a_%=:\n\t" \
  "s_waitcnt lgkmcnt(4)\n\t" \
  "v_mfma_f32_16x16x32_bf16 v[160:163], v[40:43], v[0:3], v[160:163]\n\t" \
  "ds_read_b64_tr_b16 v[52:53], v61 offset:20480\n\t" \
  "v_mfma_f32_16x16x32_bf16 v[96:99], v[40:43], v[4:7], v[96:99]\n\t" \
  "ds_read_b64_tr_b16 v[54:55], v61 offset:20992\n\t" \
  "v_mfma_f32_16x16x32_bf16 v[104:107], v[40:43], v[8:11], v[104:107]\n\t" \
  "s_barrier\n\t" \
  "v_mfma_f32_16x16x32_bf16 v[112:115], v[40:43], v[12:15], v[112:115]\n\t" \
  "v_mfma_f32_16x16x32_bf16 v[120:123], v[40:43], v[16:19], v[120:123]\n\t" \
  "s_waitcnt lgkmcnt(4)\n\t" \
  "v_mfma_f32_16x16x32_bf16 v[128:131], v[4:7], v[44:47], v[128:131]\n\t" \
  "ds_read_b64_tr_b16 v[40:41], v60 offset:12288\n\t" \
  "v_mfma_f32_16x16x32_bf16 v[136:139], v[8:11], v[44:47], v[136:139]\n\t" \
  "ds_read_b64_tr_b16 v[42:43], v60 offset:12800\n\t" \
  "v_mfma_f32_16x16x32_bf16 v[144:147], v[12:15], v[44:47], v[144:147]\n\t" \
  "ds_read_b128 v[20:23], v57 offset:36864\n\t" \
  "v_mfma_f32_16x16x32_bf16 v[152:155], v[16:19], v[44:47], v[152:155]\n\t" \
  "ds_read_b128 v[24:27], v57 offset:38912\n\t" \
  "s_waitcnt lgkmcnt(6)\n\t" \
  "v_mfma_f32_16x16x32_bf16 v[164:167], v[48:51], v[0:3], v[164:167]\n\t" \
  "ds_read_b64_tr_b16 v[44:45], v61 offset:12288\n\t" \
  "v_mfma_f32_16x16x32_bf16 v[100:103], v[48:51], v[4:7], v[100:103]\n\t" \
  "ds_read_b64_tr_b16 v[46:47], v61 offset:12800\n\t" \
  "v_mfma_f32_16x16x32_bf16 v[108:111], v[48:51], v[8:11], v[108:111]\n\t" \
  "ds_read_b128 v[28:31], v57 offset:40960\n\t" \
  "v_mfma_f32_16x16x32_bf16 v[116:119], v[48:51], v[12:15], v[116:119]\n\t" \
  "ds_read_b128 v[32:35], v57 offset:43008\n\t" \
  "v_mfma_f32_16x16x32_bf16 v[124:127], v[48:51], v[16:19], v[124:127]\n\t" \
  "s_waitcnt lgkmcnt(8)\n\t" \
  "v_mfma_f32_16x16x32_bf16 v[132:135], v[4:7], v[52:55], v[132:135]\n\t" \
  "ds_read_b64_tr_b16 v[48:49], v60 offset:28672\n\t" \
  "v_mfma_f32_16x16x32_bf16 v[140:143], v[8:11], v[52:55], v[140:143]\n\t" \
  "ds_read_b64_tr_b16 v[50:51], v60 offset:29184\n\t" \
  "v_mfma_f32_16x16x32_bf16 v[148:151], v[12:15], v[52:55], v[148:151]\n\t" \
  "ds_read_b128 v[36:39], v57 offset:45056\n\t" \
  "v_mfma_f32_16x16x32_bf16 v[156:159], v[16:19], v[52:55], v[156:159]\n\t" \
  "s_waitcnt lgkmcnt(9)\n\t" \
  "v_mfma_f32_16x16x32_bf16 v[64:67], v[40:43], v[4:7], v[64:67]\n\t" \
  "ds_read_b64_tr_b16 v[52:53], v60 offset:32768\n\t" \
  "v_mfma_f32_16x16x32_bf16 v[72:75], v[40:43], v[8:11], v[72:75]\n\t" \
  "ds_read_b64_tr_b16 v[54:55], v60 offset:33280\n\t" \
  "v_mfma_f32_16x16x32_bf16 v[80:83], v[40:43], v[12:15], v[80:83]\n\t" \
  "v_mfma_f32_16x16x32_bf16 v[88:91], v[40:43], v[16:19], v[88:91]\n\t" \
  "s_waitcnt lgkmcnt(7)\n\t" \
  "v_mfma_f32_16x16x32_bf16 v[68:71], v[44:47], v[4:7], v[68:71]\n\t" \
  "ds_read_b64_tr_b16 v[40:41], v61 offset:28672\n\t" \
  "v_mfma_f32_16x16x32_bf16 v[76:79], v[44:47], v[8:11], v[76:79]\n\t" \
  "ds_read_b64_tr_b16 v[42:43], v61 offset:29184\n\t" \
  "v_mfma_f32_16x16x32_bf16 v[84:87], v[44:47], v[12:15], v[84:87]\n\t" \
  "v_mfma_f32_16x16x32_bf16 v[92:95], v[44:47], v[16:19], v[92:95]\n\t" \
  "N5a_%=:\n\t" \
  "s_waitcnt lgkmcnt(4)\n\t" \
  "v_mfma_f32_16x16x32_bf16 v[160:163], v[48:51], v[20:23], v[160:163]\n\t" \
  "ds_read_b64_tr_b16 v[44:45], v61 offset:32768\n\t" \
  "v_mfma_f32_16x16x32_bf16 v[96:99], v[48:51], v[24:27], v[96:99]\n\t" \
  "ds_read_b64_tr_b16 v[46:47], v61 offset:33280\n\t" \
  "v_mfma_f32_16x16x32_bf16 v[104:107], v[48:51], v[28:31], v[104:107]\n\t" \
  "s_barrier\n\t" \
  "v_mfma_f32_16x16x32_bf16 v[112:115], v[48:51], v[32:35], v[112:115]\n\t" \
  "v_mfma_f32_16x16x32_bf16 v[120:123], v[48:51], v[36:39], v[120:123]\n\t" \
  "s_waitcnt lgkmcnt(4)\n\t" \
  "v_mfma_f32_16x16x32_bf16 v[128:131], v[24:27], v[52:55], v[128:131]\n\t" \
  "ds_read_b64_tr_b16 v[48:49], v60 offset:24576\n\t" \
  "v_mfma_f32_16x16x32_bf16 v[136:139], v[28:31], v[52:55], v[136:139]\n\t" \
  "ds_read_b64_tr_b16 v[50:51], v60 offset:25088\n\t" \
  "v_mfma_f32_16x16x32_bf16 v[144:147], v[32:35], v[52:55], v[144:147]\n\t" \
  "ds_read_b128 v[0:3], v56 offset:0\n\t" \
  "v_mfma_f32_16x16x32_bf16 v[152:155], v[36:39], v[52:55], v[152:155]\n\t" \
  "ds_read_b128 v[4:7], v56 offset:2048\n\t" \
  "s_waitcnt lgkmcnt(6)\n\t" \
  "v_mfma_f32_16x16x32_bf16 v[164:167], v[40:43], v[20:23], v[164:167]\n\t" \
  "ds_read_b64_tr_b16 v[52:53], v61 offset:24576\n\t" \
  "v_mfma_f32_16x16x32_bf16 v[100:103], v[40:43], v[24:27], v[100:103]\n\t" \
  "ds_read_b64_tr_b16 v[54:55], v61 offset:25088\n\t" \
  "v_mfma_f32_16x16x32_bf16 v[108:111], v[40:43], v[28:31], v[108:111]\n\t" \
  "ds_read_b128 v[8:11], v56 offset:4096\n\t" \
  "v_mfma_f32_16x16x32_bf16 v[116:119], v[40:43], v[32:35], v[116:119]\n\t" \
  "ds_read_b128 v[12:15], v56 offset:6144\n\t" \
  "v_mfma_f32_16x16x32_bf16 v[124:127], v[40:43], v[36:39], v[124:127]\n\t" \
  "s_waitcnt lgkmcnt(8)\n\t" \
  "v_mfma_f32_16x16x32_bf16 v[132:135], v[24:27], v[44:47], v[132:135]\n\t" \
  "ds_read_b64_tr_b16 v[40:41], v58 offset:4096\n\t" \
  "v_mfma_f32_16x16x32_bf16 v[140:143], v[28:31], v[44:47], v[140:143]\n\t" \
  "ds_read_b64_tr_b16 v[42:43], v58 offset:4608\n\t" \
  "v_mfma_f32_16x16x32_bf16 v[148:151], v[32:35], v[44:47], v[148:151]\n\t" \
  "ds_read_b128 v[16:19], v56 offset:8192\n\t" \
  "v_mfma_f32_16x16x32_bf16 v[156:159], v[36:39], v[44:47], v[156:159]\n\t" \
  "s_waitcnt lgkmcnt(9)\n\t" \
  "v_mfma_f32_16x16x32_bf16 v[64:67], v[48:51], v[24:27], v[64:67]\n\t" \
  "ds_read_b64_tr_b16 v[44:45], v58 offset:8192\n\t" \
  "v_mfma_f32_16x16x32_bf16 v[72:75], v[48:51], v[28:31], v[72:75]\n\t" \
  "ds_read_b64_tr_b16 v[46:47], v58 offset:8704\n\t" \
  "v_mfma_f32_16x16x32_bf16 v[80:83], v[48:51], v[32:35], v[80:83]\n\t" \
  "v_mfma_f32_16x16x32_bf16 v[88:91], v[48:51], v[36:39], v[88:91]\n\t" \
  "s_waitcnt lgkmcnt(7)\n\t" \
  "v_mfma_f32_16x16x32_bf16 v[68:71], v[52:55], v[24:27], v[68:71]\n\t" \
  "ds_read_b64_tr_b16 v[48:49], v59 offset:4096\n\t" \
  "v_mfma_f32_16x16x32_bf16 v[76:79], v[52:55], v[28:31], v[76:79]\n\t" \
  "ds_read_b64_tr_b16 v[50:51], v59 offset:4608\n\t" \
  "s_addk_i32 %[it], 1\n\t" \
  "v_mfma_f32_16x16x32_bf16 v[84:87], v[52:55], v[32:35], v[84:87]\n\t" \
  "s_cmp_lt_u32 %[it], 6\n\t" \
  "v_mfma_f32_16x16x32_bf16 v[92:95], v[52:55], v[36:39], v[92:95]\n\t" \
  "s_cbranch_scc1 N0a_%=\n\t" \
  "s_setprio 3\n\t" \
  "v_cvt_pk_bf16_f32 v120, v120, v121\n\t" \
  "v_cvt_pk_bf16_f32 v121, v122, v123\n\t" \
  "v_cvt_pk_bf16_f32 v122, v124, v125\n\t" \
  "v_cvt_pk_bf16_f32 v123, v126, v127\n\t" \
  "v_cvt_pk_bf16_f32 v28, v152, v153\n\t" \
  "v_cvt_pk_bf16_f32 v29, v154, v155\n\t" \
  "v_cvt_pk_bf16_f32 v38, v156, v157\n\t" \
  "v_cvt_pk_bf16_f32 v39, v158, v159\n\t" \
  "ds_write_b128 v63, v[120:123]\n\t" \
  "ds_write2_b64 v63, v[28:29], v[38:39] offset0:128 offset1:129\n\t" \
  "v_mov_b32 v57, %[fa]\n\t" \
  "v_mov_b32 v58, 1\n\t" \
  "s_mov_b64 exec, 1\n\t" \
  "ds_write_b32 v57, v58\n\t" \
  "s_mov_b64 exec, -1\n\t" \
  "v_cvt_pk_bf16_f32 v160, v160, v161\n\t" \
  "v_cvt_pk_bf16_f32 v161, v162, v163\n\t" \
  "v_cvt_pk_bf16_f32 v162, v164, v165\n\t" \
  "v_cvt_pk_bf16_f32 v163, v166, v167\n\t" \
  "s_branch NX_%=\n\t" \
  "NB_%=:\n\t" \
  "ds_read_b64_tr_b16 v[40:41], v60 offset:20480\n\t" \
  "ds_read_b64_tr_b16 v[42:43], v60 offset:20992\n\t" \
  "ds_read_b64_tr_b16 v[44:45], v60 offset:16384\n\t" \
  "ds_read_b64_tr_b16 v[46:47], v60 offset:16896\n\t" \
  "ds_read_b64_tr_b16 v[48:49], v61 offset:20480\n\t" \
  "ds_read_b64_tr_b16 v[50:51], v61 offset:20992\n\t" \
  "ds_read_b128 v[0:3], v56 offset:36864\n\t" \
  "ds_read_b128 v[4:7], v56 offset:47104\n\t" \
  "ds_read_b128 v[8:11], v56 offset:49152\n\t" \
  "ds_read_b128 v[12:15], v56 offset:51200\n\t" \
  "ds_read_b128 v[16:19], v56 offset:53248\n\t" \
  "s_waitcnt lgkmcnt(0)\n\t" \
  "s_branch N4b_%=\n\t" \
  "N0b_%=:\n\t" \
  "s_waitcnt lgkmcnt(4)\n\t" \
  "v_mfma_f32_16x16x32_bf16 v[160:163], v[0:3], v[40:43], v[160:163]\n\t" \
  "ds_read_b64_tr_b16 v[52:53], v59 offset:4096\n\t" \
  "v_mfma_f32_16x16x32_bf16 v[128:131], v[4:7], v[40:43], v[128:131]\n\t" \
  "ds_read_b64_tr_b16 v[54:55], v59 offset:4608\n\t" \
  "v_mfma_f32_16x16x32_bf16 v[136:139], v[8:11], v[40:43], v[136:139]\n\t" \
  "s_barrier\n\t" \
  "v_mfma_f32_16x16x32_bf16 v[144:147], v[12:15], v[40:43], v[144:147]\n\t" \
  "v_mfma_f32_16x16x32_bf16 v[152:155], v[16:19], v[40:43], v[152:155]\n\t" \
  "s_waitcnt lgkmcnt(4)\n\t" \
  "v_mfma_f32_16x16x32_bf16 v[96:99], v[44:47], v[4:7], v[96:99]\n\t" \
  "ds_read_b64_tr_b16 v[40:41], v58 offset:0\n\t" \
  "v_mfma_f32_16x16x32_bf16 v[104:107], v[44:47], v[8:11], v[104:107]\n\t" \
  "ds_read_b64_tr_b16 v[42:43], v58 offset:512\n\t" \
  "v_mfma_f32_16x16x32_bf16 v[112:115], v[44:47], v[12:15], v[112:115]\n\t" \
  "ds_read_b128 v[20:23], v57 offset:0\n\t" \
  "v_mfma_f32_16x16x32_bf16 v[120:123], v[44:47], v[16:19], v[120:123]\n\t" \
  "ds_read_b128 v[24:27], v57 offset:10240\n\t" \
  "s_waitcnt lgkmcnt(6)\n\t" \
  "v_mfma_f32_16x16x32_bf16 v[164:167], v[0:3], v[48:51], v[164:167]\n\t" \
  "ds_read_b64_tr_b16 v[44:45], v59 offset:0\n\t" \
  "v_mfma_f32_16x16x32_bf16 v[132:135], v[4:7], v[48:51], v[132:135]\n\t" \
  "ds_read_b64_tr_b16 v[46:47], v59 offset:512\n\t" \
  "v_mfma_f32_16x16x32_bf16 v[140:143], v[8:11], v[48:51], v[140:143]\n\t" \
  "ds_read_b128 v[28:31], v57 offset:12288\n\t" \
  "v_mfma_f32_16x16x32_bf16 v[148:151], v[12:15], v[48:51], v[148:151]\n\t" \
  "ds_read_b128 v[32:35], v57 offset:14336\n\t" \
  "v_mfma_f32_16x16x32_bf16 v[156:159], v[16:19], v[48:51], v[156:159]\n\t" \
  "s_waitcnt lgkmcnt(8)\n\t" \
  "v_mfma_f32_16x16x32_bf16 v[100:103], v[52:55], v[4:7], v[100:103]\n\t" \
  "ds_read_b64_tr_b16 v[48:49], v58 offset:20480\n\t" \
  "v_mfma_f32_16x16x32_bf16 v[108:111], v[52:55], v[8:11], v[108:111]\n\t" \
  "ds_read_b64_tr_b16 v[50:51], v58 offset:20992\n\t" \
  "v_mfma_f32_16x16x32_bf16 v[116:119], v[52:55], v[12:15], v[116:119]\n\t" \
  "ds_read_b128 v[36:39], v57 offset:16384\n\t" \
  "v_mfma_f32_16x16x32_bf16 v[124:127], v[52:55], v[16:19], v[124:127]\n\t" \
  "s_waitcnt lgkmcnt(9)\n\t" \
  "v_mfma_f32_16x16x32_bf16 v[64:67], v[40:43], v[4:7], v[64:67]\n\t" \
  "ds_read_b64_tr_b16 v[52:53], v58 offset:16384\n\t" \
  "v_mfma_f32_16x16x32_bf16 v[72:75], v[40:43], v[8:11], v[72:75]\n\t" \
  "ds_read_b64_tr_b16 v[54:55], v58 offset:16896\n\t" \
  "v_mfma_f32_16x16x32_bf16 v[80:83], v[40:43], v[12:15], v[80:83]\n\t" \
  "v_mfma_f32_16x16x32_bf16 v[88:91], v[40:43], v[16:19], v[88:91]\n\t" \
  "s_waitcnt lgkmcnt(7)\n\t" \
  "v_mfma_f32_16x16x32_bf16 v[68:71], v[44:47], v[4:7], v[68:71]\n\t" \
  "ds_read_b64_tr_b16 v[40:41], v59 offset:20480\n\t" \
  "v_mfma_f32_16x16x32_bf16 v[76:79], v[44:47], v[8:11], v[76:79]\n\t" \
  "ds_read_b64_tr_b16 v[42:43], v59 offset:20992\n\t" \
  "v_mfma_f32_16x16x32_bf16 v[84:87], v[44:47], v[12:15], v[84:87]\n\t" \
  "v_mfma_f32_16x16x32_bf16 v[92:95], v[44:47], v[16:19], v[92:95]\n\t" \
  "N1b_%=:\n\t" \
  "s_waitcnt lgkmcnt(4)\n\t" \
  "v_mfma_f32_16x16x32_bf16 v[160:163], v[20:23], v[48:51], v[160:163]\n\t" \
  "ds_read_b64_tr_b16 v[44:45], v59 offset:16384\n\t" \
  "v_mfma_f32_16x16x32_bf16 v[128:131], v[24:27], v[48:51], v[128:131]\n\t" \
  "ds_read_b64_tr_b16 v[46:47], v59 offset:16896\n\t" \
  "v_mfma_f32_16x16x32_bf16 v[136:139], v[28:31], v[48:51], v[136:139]\n\t" \
  "s_barrier\n\t" \
  "v_mfma_f32_16x16x32_bf16 v[144:147], v[32:35], v[48:51], v[144:147]\n\t" \
  "v_mfma_f32_16x16x32_bf16 v[152:155], v[36:39], v[48:51], v[152:155]\n\t" \
  "s_waitcnt lgkmcnt(4)\n\t" \
  "v_mfma_f32_16x16x32_bf16 v[96:99], v[52:55], v[24:27], v[96:99]\n\t" \
  "ds_read_b64_tr_b16 v[48:49], v58 offset:12288\n\t" \
  "v_mfma_f32_16x16x32_bf16 v[104:107], v[52:55], v[28:31], v[104:107]\n\t" \
  "ds_read_b64_tr_b16 v[50:51], v58 offset:12800\n\t" \
  "v_mfma_f32_16x16x32_bf16 v[112:115], v[52:55], v[32:35], v[112:115]\n\t" \
  "ds_read_b128 v[0:3], v56 offset:18432\n\t" \
  "v_mfma_f32_16x16x32_bf16 v[120:123], v[52:55], v[36:39], v[120:123]\n\t" \
  "ds_read_b128 v[4:7], v56 offset:28672\n\t" \
  "s_waitcnt lgkmcnt(6)\n\t" \
  "v_mfma_f32_16x16x32_bf16 v[164:167], v[20:23], v[40:43], v[164:167]\n\t" \
  "ds_read_b64_tr_b16 v[52:53], v59 offset:12288\n\t" \
  "v_mfma_f32_16x16x32_bf16 v[132:135], v[24:27], v[40:43], v[132:135]\n\t" \
  "ds_read_b64_tr_b16 v[54:55], v59 offset:12800\n\t" \
  "v_mfma_f32_16x16x32_bf16 v[140:143], v[28:31], v[40:43], v[140:143]\n\t" \
  "ds_read_b128 v[8:11], v56 offset:30720\n\t" \
  "v_mfma_f32_16x16x32_bf16 v[148:151], v[32:35], v[40:43], v[148:151]\n\t" \
  "ds_read_b128 v[12:15], v56 offset:32768\n\t" \
  "v_mfma_f32_16x16x32_bf16 v[156:159], v[36:39], v[40:43], v[156:159]\n\t" \
  "s_waitcnt lgkmcnt(8)\n\t" \
  "v_mfma_f32_16x16x32_bf16 v[100:103], v[44:47], v[24:27], v[100:103]\n\t" \
  "ds_read_b64_tr_b16 v[40:41], v58 offset:32768\n\t" \
  "v_mfma_f32_16x16x32_bf16 v[108:111], v[44:47], v[28:31], v[108:111]\n\t" \
  "ds_read_b64_tr_b16 v[42:43], v58 offset:33280\n\t" \
  "v_mfma_f32_16x16x32_bf16 v[116:119], v[44:47], v[32:35], v[116:119]\n\t" \
  "ds_read_b128 v[16:19], v56 offset:34816\n\t" \
  "v_mfma_f32_16x16x32_bf16 v[124:127], v[44:47], v[36:39], v[124:127]\n\t" \
  "s_waitcnt lgkmcnt(9)\n\t" \
  "v_mfma_f32_16x16x32_bf16 v[64:67], v[48:51], v[24:27], v[64:67]\n\t" \
  "ds_read_b64_tr_b16 v[44:45], v58 offset:28672\n\t" \
  "v_mfma_f32_16x16x32_bf16 v[72:75], v[48:51], v[28:31], v[72:75]\n\t" \
  "ds_read_b64_tr_b16 v[46:47], v58 offset:29184\n\t" \
  "v_mfma_f32_16x16x32_bf16 v[80:83], v[48:51], v[32:35], v[80:83]\n\t" \
  "v_mfma_f32_16x16x32_bf16 v[88:91], v[48:51], v[36:39], v[88:91]\n\t" \
  "s_waitcnt lgkmcnt(7)\n\t" \
  "v_mfma_f32_16x16x32_bf16 v[68:71], v[52:55], v[24:27], v[68:71]\n\t" \
  "ds_read_b64_tr_b16 v[48:49], v59 offset:32768\n\t" \
  "v_mfma_f32_16x16x32_bf16 v[76:79], v[52:55], v[28:31], v[76:79]\n\t" \
  "ds_read_b64_tr_b16 v[50:51], v59 offset:33280\n\t" \
  "v_mfma_f32_16x16x32_bf16 v[84:87], v[52:55], v[32:35], v[84:87]\n\t" \
  "v_mfma_f32_16x16x32_bf16 v[92:95], v[52:55], v[36:39], v[92:95]\n\t" \
  "N2b_%=:\n\t" \
  "s_waitcnt lgkmcnt(4)\n\t" \
  "v_mfma_f32_16x16x32_bf16 v[160:163], v[0:3], v[40:43], v[160:163]\n\t" \
  "ds_read_b64_tr_b16 v[52:53], v59 offset:28672\n\t" \
  "v_mfma_f32_16x16x32_bf16 v[128:131], v[4:7], v[40:43], v[128:131]\n\t" \
  "ds_read_b64_tr_b16 v[54:55], v59 offset:29184\n\t" \
  "v_mfma_f32_16x16x32_bf16 v[136:139], v[8:11], v[40:43], v[136:139]\n\t" \
  "s_barrier\n\t" \
  "v_mfma_f32_16x16x32_bf16 v[144:147], v[12:15], v[40:43], v[144:147]\n\t" \
  "v_mfma_f32_16x16x32_bf16 v[152:155], v[16:19], v[40:43], v[152:155]\n\t" \
  "s_waitcnt lgkmcnt(4)\n\t" \
  "v_mfma_f32_16x16x32_bf16 v[96:99], v[44:47], v[4:7], v[96:99]\n\t" \
  "ds_read_b64_tr_b16 v[40:41], v58 offset:24576\n\t" \
  "v_mfma_f32_16x16x32_bf16 v[104:107], v[44:47], v[8:11], v[104:107]\n\t" \
  "ds_read_b64_tr_b16 v[42:43], v58 offset:25088\n\t" \
  "v_mfma_f32_16x16x32_bf16 v[112:115], v[44:47], v[12:15], v[112:115]\n\t" \
  "ds_read_b128 v[20:23], v57 offset:18432\n\t" \
  "v_mfma_f32_16x16x32_bf16 v[120:123], v[44:47], v[16:19], v[120:123]\n\t" \
  "ds_read_b128 v[24:27], v57 offset:28672\n\t" \
  "s_waitcnt lgkmcnt(6)\n\t" \
  "v_mfma_f32_16x16x32_bf16 v[164:167], v[0:3], v[48:51], v[164:167]\n\t" \
  "ds_read_b64_tr_b16 v[44:45], v59 offset:24576\n\t" \
  "v_mfma_f32_16x16x32_bf16 v[132:135], v[4:7], v[48:51], v[132:135]\n\t" \
  "ds_read_b64_tr_b16 v[46:47], v59 offset:25088\n\t" \
  "v_mfma_f32_16x16x32_bf16 v[140:143], v[8:11], v[48:51], v[140:143]\n\t" \
  "ds_read_b128 v[28:31], v57 offset:30720\n\t" \
  "v_mfma_f32_16x16x32_bf16 v[148:151], v[12:15], v[48:51], v[148:151]\n\t" \
  "ds_read_b128 v[32:35], v57 offset:32768\n\t" \
  "v_mfma_f32_16x16x32_bf16 v[156:159], v[16:19], v[48:51], v[156:159]\n\t" \
  "s_waitcnt lgkmcnt(8)\n\t" \
  "v_mfma_f32_16x16x32_bf16 v[100:103], v[52:55], v[4:7], v[100:103]\n\t" \
  "ds_read_b64_tr_b16 v[48:49], v60 offset:8192\n\t" \
  "v_mfma_f32_16x16x32_bf16 v[108:111], v[52:55], v[8:11], v[108:111]\n\t" \
  "ds_read_b64_tr_b16 v[50:51], v60 offset:8704\n\t" \
  "v_mfma_f32_16x16x32_bf16 v[116:119], v[52:55], v[12:15], v[116:119]\n\t" \
  "ds_read_b128 v[36:39], v57 offset:34816\n\t" \
  "v_mfma_f32_16x16x32_bf16 v[124:127], v[52:55], v[16:19], v[124:127]\n\t" \
  "s_waitcnt lgkmcnt(9)\n\t" \
  "v_mfma_f32_16x16x32_bf16 v[64:67], v[40:43], v[4:7], v[64:67]\n\t" \
  "ds_read_b64_tr_b16 v[52:53], v60 offset:4096\n\t" \
  "v_mfma_f32_16x16x32_bf16 v[72:75], v[40:43], v[8:11], v[72:75]\n\t" \
  "ds_read_b64_tr_b16 v[54:55], v60 offset:4608\n\t" \
  "v_mfma_f32_16x16x32_bf16 v[80:83], v[40:43], v[12:15], v[80:83]\n\t" \
  "v_mfma_f32_16x16x32_bf16 v[88:91], v[40:43], v[16:19], v[88:91]\n\t" \
  "s_waitcnt lgkmcnt(7)\n\t" \
  "v_mfma_f32_16x16x32_bf16 v[68:71], v[44:47], v[4:7], v[68:71]\n\t" \
  "ds_read_b64_tr_b16 v[40:41], v61 offset:8192\n\t" \
  "v_mfma_f32_16x16x32_bf16 v[76:79], v[44:47], v[8:11], v[76:79]\n\t" \
  "ds_read_b64_tr_b16 v[42:43], v61 offset:8704\n\t" \
  "v_mfma_f32_16x16x32_bf16 v[84:87], v[44:47], v[12:15], v[84:87]\n\t" \
  "v_mfma_f32_16x16x32_bf16 v[92:95], v[44:47], v[16:19], v[92:95]\n\t" \
  "N3b_%=:\n\t" \
  "s_waitcnt lgkmcnt(4)\n\t" \
  "v_mfma_f32_16x16x32_bf16 v[160:163], v[20:23], v[48:51], v[160:163]\n\t" \
  "ds_read_b64_tr_b16 v[44:45], v61 offset:4096\n\t" \
  "v_mfma_f32_16x16x32_bf16 v[128:131], v[24:27], v[48:51], v[128:131]\n\t" \
  "ds_read_b64_tr_b16 v[46:47], v61 offset:4608\n\t" \
  "v_mfma_f32_16x16x32_bf16 v[136:139], v[28:31], v[48:51], v[136:139]\n\t" \
  "s_barrier\n\t" \
  "v_mfma_f32_16x16x32_bf16 v[144:147], v[32:35], v[48:51], v[144:147]\n\t" \
  "v_mfma_f32_16x16x32_bf16 v[152:155], v[36:39], v[48:51], v[152:155]\n\t" \
  "s_waitcnt lgkmcnt(4)\n\t" \
  "v_mfma_f32_16x16x32_bf16 v[96:99], v[52:55], v[24:27], v[96:99]\n\t" \
  "ds_read_b64_tr_b16 v[48:49], v60 offset:0\n\t" \
  "v_mfma_f32_16x16x32_bf16 v[104:107], v[52:55], v[28:31], v[104:107]\n\t" \
  "ds_read_b64_tr_b16 v[50:51], v60 offset:512\n\t" \
  "v_mfma_f32_16x16x32_bf16 v[112:115], v[52:55], v[32:35], v[112:115]\n\t" \
  "ds_read_b128 v[0:3], v56 offset:36864\n\t" \
  "v_mfma_f32_16x16x32_bf16 v[120:123], v[52:55], v[36:39], v[120:123]\n\t" \
  "ds_read_b128 v[4:7], v56 offset:47104\n\t" \
  "s_waitcnt lgkmcnt(6)\n\t" \
  "v_mfma_f32_16x16x32_bf16 v[164:167], v[20:23], v[40:43], v[164:167]\n\t" \
  "ds_read_b64_tr_b16 v[52:53], v61 offset:0\n\t" \
  "v_mfma_f32_16x16x32_bf16 v[132:135], v[24:27], v[40:43], v[132:135]\n\t" \
  "ds_read_b64_tr_b16 v[54:55], v61 offset:512\n\t" \
  "v_mfma_f32_16x16x32_bf16 v[140:143], v[28:31], v[40:43], v[140:143]\n\t" \
  "ds_read_b128 v[8:11], v56 offset:49152\n\t" \
  "v_mfma_f32_16x16x32_bf16 v[148:151], v[32:35], v[40:43], v[148:151]\n\t" \
  "ds_read_b128 v[12:15], v56 offset:51200\n\t" \
  "v_mfma_f32_16x16x32_bf16 v[156:159], v[36:39], v[40:43], v[156:159]\n\t" \
  "s_waitcnt lgkmcnt(8)\n\t" \
  "v_mfma_f32_16x16x32_bf16 v[100:103], v[44:47], v[24:27], v[100:103]\n\t" \
  "ds_read_b64_tr_b16 v[40:41], v60 offset:20480\n\t" \
  "v_mfma_f32_16x16x32_bf16 v[108:111], v[44:47], v[28:31], v[108:111]\n\t" \
  "ds_read_b64_tr_b16 v[42:43], v60 offset:20992\n\t" \
  "v_mfma_f32_16x16x32_bf16 v[116:119], v[44:47], v[32:35], v[116:119]\n\t" \
  "ds_read_b128 v[16:19], v56 offset:53248\n\t" \
  "v_mfma_f32_16x16x32_bf16 v[124:127], v[44:47], v[36:39], v[124:127]\n\t" \
  "s_waitcnt lgkmcnt(9)\n\t" \
  "v_mfma_f32_16x16x32_bf16 v[64:67], v[48:51], v[24:27], v[64:67]\n\t" \
  "ds_read_b64_tr_b16 v[44:45], v60 offset:16384\n\t" \
  "v_mfma_f32_16x16x32_bf16 v[72:75], v[48:51], v[28:31], v[72:75]\n\t" \
  "ds_read_b64_tr_b16 v[46:47], v60 offset:16896\n\t" \
  "v_mfma_f32_16x16x32_bf16 v[80:83], v[48:51], v[32:35], v[80:83]\n\t" \
  "v_mfma_f32_16x16x32_bf16 v[88:91], v[48:51], v[36:39], v[88:91]\n\t" \
  "s_waitcnt lgkmcnt(7)\n\t" \
  "v_mfma_f32_16x16x32_bf16 v[68:71], v[52:55], v[24:27], v[68:71]\n\t" \
  "ds_read_b64_tr_b16 v[48:49], v61 offset:20480\n\t" \
  "v_mfma_f32_16x16x32_bf16 v[76:79], v[52:55], v[28:31], v[76:79]\n\t" \
  "ds_read_b64_tr_b16 v[50:51], v61 offset:20992\n\t" \
  "v_mfma_f32_16x16x32_bf16 v[84:87], v[52:55], v[32:35], v[84:87]\n\t" \
  "v_mfma_f32_16x16x32_bf16 v[92:95], v[52:55], v[36:39], v[92:95]\n\t" \
  "N4b_%=:\n\t" \
  "s_waitcnt lgkmcnt(4)\n\t" \
  "v_mfma_f32_16x16x32_bf16 v[160:163], v[0:3], v[40:43], v[160:163]\n\t" \
  "ds_read_b64_tr_b16 v[52:53], v61 offset:16384\n\t" \
  "v_mfma_f32_16x16x32_bf16 v[128:131], v[4:7], v[40:43], v[128:131]\n\t" \
  "ds_read_b64_tr_b16 v[54:55], v61 offset:16896\n\t" \
  "v_mfma_f32_16x16x32_bf16 v[136:139], v[8:11], v[40:43], v[136:139]\n\t" \
  "s_barrier\n\t" \
  "v_mfma_f32_16x16x32_bf16 v[144:147], v[12:15], v[40:43], v[144:147]\n\t" \
  "v_mfma_f32_16x16x32_bf16 v[152:155], v[16:19], v[40:43], v[152:155]\n\t" \
  "s_waitcnt lgkmcnt(4)\n\t" \
  "v_mfma_f32_16x16x32_bf16 v[96:99], v[44:47], v[4:7], v[96:99]\n\t" \
  "ds_read_b64_tr_b16 v[40:41], v60 offset:12288\n\t" \
  "v_mfma_f32_16x16x32_bf16 v[104:107], v[44:47], v[8:11], v[104:107]\n\t" \
  "ds_read_b64_tr_b16 v[42:43], v60 offset:12800\n\t" \
  "v_mfma_f32_16x16x32_bf16 v[112:115], v[44:47], v[12:15], v[112:115]\n\t" \
  "ds_read_b128 v[20:23], v57 offset:36864\n\t" \
  "v_mfma_f32_16x16x32_bf16 v[120:123], v[44:47], v[16:19], v[120:123]\n\t" \
  "ds_read_b128 v[24:27], v57 offset:47104\n\t" \
  "s_waitcnt lgkmcnt(6)\n\t" \
  "v_mfma_f32_16x16x32_bf16 v[164:167], v[0:3], v[48:51], v[164:167]\n\t" \
  "ds_read_b64_tr_b16 v[44:45], v61 offset:12288\n\t" \
  "v_mfma_f32_16x16x32_bf16 v[132:135], v[4:7], v[48:51], v[132:135]\n\t" \
  "ds_read_b64_tr_b16 v[46:47], v61 offset:12800\n\t" \
  "v_mfma_f32_16x16x32_bf16 v[140:143], v[8:11], v[48:51], v[140:143]\n\t" \
  "ds_read_b128 v[28:31], v57 offset:49152\n\t" \
  "v_mfma_f32_16x16x32_bf16 v[148:151], v[12:15], v[48:51], v[148:151]\n\t" \
  "ds_read_b128 v[32:35], v57 offset:51200\n\t" \
  "v_mfma_f32_16x16x32_bf16 v[156:159], v[16:19], v[48:51], v[156:159]\n\t" \
  "s_waitcnt lgkmcnt(8)\n\t" \
  "v_mfma_f32_16x16x32_bf16 v[100:103], v[52:55], v[4:7], v[100:103]\n\t" \
  "ds_read_b64_tr_b16 v[48:49], v60 offset:32768\n\t" \
  "v_mfma_f32_16x16x32_bf16 v[108:111], v[52:55], v[8:11], v[108:111]\n\t" \
  "ds_read_b64_tr_b16 v[50:51], v60 offset:33280\n\t" \
  "v_mfma_f32_16x16x32_bf16 v[116:119], v[52:55], v[12:15], v[116:119]\n\t" \
  "ds_read_b128 v[36:39], v57 offset:53248\n\t" \
  "v_mfma_f32_16x16x32_bf16 v[124:127], v[52:55], v[16:19], v[124:127]\n\t" \
  "s_waitcnt lgkmcnt(9)\n\t" \
  "v_mfma_f32_16x16x32_bf16 v[64:67], v[40:43], v[4:7], v[64:67]\n\t" \
  "ds_read_b64_tr_b16 v[52:53], v60 offset:28672\n\t" \
  "v_mfma_f32_16x16x32_bf16 v[72:75], v[40:43], v[8:11], v[72:75]\n\t" \
  "ds_read_b64_tr_b16 v[54:55], v60 offset:29184\n\t" \
  "v_mfma_f32_16x16x32_bf16 v[80:83], v[40:43], v[12:15], v[80:83]\n\t" \
  "v_mfma_f32_16x16x32_bf16 v[88:91], v[40:43], v[16:19], v[88:91]\n\t" \
  "s_waitcnt lgkmcnt(7)\n\t" \
  "v_mfma_f32_16x16x32_bf16 v[68:71], v[44:47], v[4:7], v[68:71]\n\t" \
  "ds_read_b64_tr_b16 v[40:41], v61 offset:32768\n\t" \
  "v_mfma_f32_16x16x32_bf16 v[76:79], v[44:47], v[8:11], v[76:79]\n\t" \
  "ds_read_b64_tr_b16 v[42:43], v61 offset:33280\n\t" \
  "v_mfma_f32_16x16x32_bf16 v[84:87], v[44:47], v[12:15], v[84:87]\n\t" \
  "v_mfma_f32_16x16x32_bf16 v[92:95], v[44:47], v[16:19], v[92:95]\n\t" \
  "N5b_%=:\n\t" \
  "s_waitcnt lgkmcnt(4)\n\t" \
  "v_mfma_f32_16x16x32_bf16 v[160:163], v[20:23], v[48:51], v[160:163]\n\t" \
  "ds_read_b64_tr_b16 v[44:45], v61 offset:28672\n\t" \
  "v_mfma_f32_16x16x32_bf16 v[128:131], v[24:27], v[48:51], v[128:131]\n\t" \
  "ds_read_b64_tr_b16 v[46:47], v61 offset:29184\n\t" \
  "v_mfma_f32_16x16x32_bf16 v[136:139], v[28:31], v[48:51], v[136:139]\n\t" \
  "s_barrier\n\t" \
  "v_mfma_f32_16x16x32_bf16 v[144:147], v[32:35], v[48:51], v[144:147]\n\t" \
  "v_mfma_f32_16x16x32_bf16 v[152:155], v[36:39], v[48:51], v[152:155]\n\t" \
  "s_waitcnt lgkmcnt(4)\n\t" \
  "v_mfma_f32_16x16x32_bf16 v[96:99], v[52:55], v[24:27], v[96:99]\n\t" \
  "ds_read_b64_tr_b16 v[48:49], v60 offset:24576\n\t" \
  "v_mfma_f32_16x16x32_bf16 v[104:107], v[52:55], v[28:31], v[104:107]\n\t" \
  "ds_read_b64_tr_b16 v[50:51], v60 offset:25088\n\t" \
  "v_mfma_f32_16x16x32_bf16 v[112:115], v[52:55], v[32:35], v[112:115]\n\t" \
  "ds_read_b128 v[0:3], v56 offset:0\n\t" \
  "v_mfma_f32_16x16x32_bf16 v[120:123], v[52:55], v[36:39], v[120:123]\n\t" \
  "ds_read_b128 v[4:7], v56 offset:10240\n\t" \
  "s_waitcnt lgkmcnt(6)\n\t" \
  "v_mfma_f32_16x16x32_bf16 v[164:167], v[20:23], v[40:43], v[164:167]\n\t" \
  "ds_read_b64_tr_b16 v[52:53], v61 offset:24576\n\t" \
  "v_mfma_f32_16x16x32_bf16 v[132:135], v[24:27], v[40:43], v[132:135]\n\t" \
  "ds_read_b64_tr_b16 v[54:55], v61 offset:25088\n\t" \
  "v_mfma_f32_16x16x32_bf16 v[140:143], v[28:31], v[40:43], v[140:143]\n\t" \
  "ds_read_b128 v[8:11], v56 offset:12288\n\t" \
  "v_mfma_f32_16x16x32_bf16 v[148:151], v[32:35], v[40:43], v[148:151]\n\t" \
  "ds_read_b128 v[12:15], v56 offset:14336\n\t" \
  "v_mfma_f32_16x16x32_bf16 v[156:159], v[36:39], v[40:43], v[156:159]\n\t" \
  "s_waitcnt lgkmcnt(8)\n\t" \
  "v_mfma_f32_16x16x32_bf16 v[100:103], v[44:47], v[24:27], v[100:103]\n\t" \
  "ds_read_b64_tr_b16 v[40:41], v58 offset:8192\n\t" \
  "v_mfma_f32_16x16x32_bf16 v[108:111], v[44:47], v[28:31], v[108:111]\n\t" \
  "ds_read_b64_tr_b16 v[42:43], v58 offset:8704\n\t" \
  "v_mfma_f32_16x16x32_bf16 v[116:119], v[44:47], v[32:35], v[116:119]\n\t" \
  "ds_read_b128 v[16:19], v56 offset:16384\n\t" \
  "v_mfma_f32_16x16x32_bf16 v[124:127], v[44:47], v[36:39], v[124:127]\n\t" \
  "s_waitcnt lgkmcnt(9)\n\t" \
  "v_mfma_f32_16x16x32_bf16 v[64:67], v[48:51], v[24:27], v[64:67]\n\t" \
  "ds_read_b64_tr_b16 v[44:45], v58 offset:4096\n\t" \
  "v_mfma_f32_16x16x32_bf16 v[72:75], v[48:51], v[28:31], v[72:75]\n\t" \
  "ds_read_b64_tr_b16 v[46:47], v58 offset:4608\n\t" \
  "v_mfma_f32_16x16x32_bf16 v[80:83], v[48:51], v[32:35], v[80:83]\n\t" \
  "v_mfma_f32_16x16x32_bf16 v[88:91], v[48:51], v[36:39], v[88:91]\n\t" \
  "s_waitcnt lgkmcnt(7)\n\t" \
  "v_mfma_f32_16x16x32_bf16 v[68:71], v[52:55], v[24:27], v[68:71]\n\t" \
  "ds_read_b64_tr_b16 v[48:49], v59 offset:8192\n\t" \
  "v_mfma_f32_16x16x32_bf16 v[76:79], v[52:55], v[28:31], v[76:79]\n\t" \
  "ds_read_b64_tr_b16 v[50:51], v59 offset:8704\n\t" \
  "s_addk_i32 %[it], 1\n\t" \
  "v_mfma_f32_16x16x32_bf16 v[84:87], v[52:55], v[32:35], v[84:87]\n\t" \
  "s_cmp_lt_u32 %[it], 6\n\t" \
  "v_mfma_f32_16x16x32_bf16 v[92:95], v[52:55], v[36:39], v[92:95]\n\t" \
  "s_cbranch_scc1 N0b_%=\n\t" \
  "s_setprio 3\n\t" \
  "v_cvt_pk_bf16_f32 v160, v160, v161\n\t" \
  "v_cvt_pk_bf16_f32 v161, v162, v163\n\t" \
  "v_cvt_pk_bf16_f32 v162, v164, v165\n\t" \
  "v_cvt_pk_bf16_f32 v163, v166, v167\n\t" \
  "ds_write_b128 v63, v[160:163] offset:2048\n\t" \
  "v_mov_b32 v57, %[fa]\n\t" \
  "v_mov_b32 v58, 1\n\t" \
  "s_mov_b64 exec, 1\n\t" \
  "ds_write_b32 v57, v58\n\t" \
  "s_mov_b64 exec, -1\n\t" \
  "v_cvt_pk_bf16_f32 v120, v120, v121\n\t" \
  "v_cvt_pk_bf16_f32 v121, v122, v123\n\t" \
  "v_cvt_pk_bf16_f32 v122, v124, v125\n\t" \
  "v_cvt_pk_bf16_f32 v123, v126, v127\n\t" \
  "v_cvt_pk_bf16_f32 v28, v152, v153\n\t" \
  "v_cvt_pk_bf16_f32 v29, v154, v155\n\t" \
  "v_cvt_pk_bf16_f32 v38, v156, v157\n\t" \
  "v_cvt_pk_bf16_f32 v39, v158, v159\n\t" \
  "NX_%=:\n\t" \
  "v_cvt_pk_bf16_f32 v96, v96, v97\n\t" \
  "v_cvt_pk_bf16_f32 v97, v98, v99\n\t" \
  "v_cvt_pk_bf16_f32 v98, v100, v101\n\t" \
  "v_cvt_pk_bf16_f32 v99, v102, v103\n\t" \
  "v_cvt_pk_bf16_f32 v104, v104, v105\n\t" \
  "v_cvt_pk_bf16_f32 v105, v106, v107\n\t" \
  "v_cvt_pk_bf16_f32 v106, v108, v109\n\t" \
  "v_cvt_pk_bf16_f32 v107, v110, v111\n\t" \
  "v_mov_b32 v60, 0xff61b1e6\n\t" \
  "v_cndmask_b32_e64 v56, v60, 0, s[68:69]\n\t" \
  "v_cndmask_b32_e64 v57, v60, 0, s[70:71]\n\t" \
  "v_cndmask_b32_e64 v58, v60, 0, s[72:73]\n\t" \
  "v_cndmask_b32_e64 v59, v60, 0, s[74:75]\n\t" \
  "v_cvt_pk_bf16_f32 v72, v72, v73\n\t" \
  "v_cvt_pk_bf16_f32 v73, v74, v75\n\t" \
  "v_cvt_pk_bf16_f32 v74, v76, v77\n\t" \
  "v_cvt_pk_bf16_f32 v75, v78, v79\n\t" \
  "v_cndmask_b32_e64 v52, v60, 0, s[76:77]\n\t" \
  "v_cndmask_b32_e64 v53, v60, 0, s[78:79]\n\t" \
  "v_cndmask_b32_e64 v54, v60, 0, s[80:81]\n\t" \
  "v_cndmask_b32_e64 v55, v60, 0, s[82:83]\n\t" \
  "v_cvt_pk_bf16_f32 v24, v136, v137\n\t" \
  "v_cvt_pk_bf16_f32 v25, v138, v139\n\t" \
  "v_cvt_pk_bf16_f32 v34, v140, v141\n\t" \
  "v_cvt_pk_bf16_f32 v35, v142, v143\n\t" \
  "v_cvt_pk_bf16_f32 v26, v144, v145\n\t" \
  "v_cvt_pk_bf16_f32 v27, v146, v147\n\t" \
  "v_cvt_pk_bf16_f32 v36, v148, v149\n\t" \
  "v_cvt_pk_bf16_f32 v37, v150, v151\n\t" \
  "v_mfma_f32_16x16x32_bf16 v[136:139], v[96:99], v[72:75], v[56:59]\n\t" \
  "v_cvt_pk_bf16_f32 v112, v112, v113\n\t" \
  "v_cvt_pk_bf16_f32 v113, v114, v115\n\t" \
  "v_mfma_f32_16x16x32_bf16 v[140:143], v[104:107], v[72:75], v[52:55]\n\t" \
  "v_cvt_pk_bf16_f32 v114, v116, v117\n\t" \
  "v_cvt_pk_bf16_f32 v115, v118, v119\n\t" \
  "v_cvt_pk_bf16_f32 v80, v80, v81\n\t" \
  "v_cvt_pk_bf16_f32 v81, v82, v83\n\t" \
  "v_cvt_pk_bf16_f32 v82, v84, v85\n\t" \
  "v_cvt_pk_bf16_f32 v83, v86, v87\n\t" \
  "v_cvt_pk_bf16_f32 v22, v128, v129\n\t" \
  "v_cvt_pk_bf16_f32 v23, v130, v131\n\t" \
  "v_mfma_f32_16x16x32_bf16 v[144:147], v[104:107], v[80:83], v[56:59]\n\t" \
  "v_cvt_pk_bf16_f32 v32, v132, v133\n\t" \
  "v_cvt_pk_bf16_f32 v33, v134, v135\n\t" \
  "v_mfma_f32_16x16x32_bf16 v[148:151], v[112:115], v[80:83], v[52:55]\n\t" \
  "v_cvt_pk_bf16_f32 v88, v88, v89\n\t" \
  "v_cvt_pk_bf16_f32 v89, v90, v91\n\t" \
  "v_cvt_pk_bf16_f32 v90, v92, v93\n\t" \
  "v_cvt_pk_bf16_f32 v91, v94, v95\n\t" \
  "v_mov_b32 v61, %[fp]\n\t" \
  "v_add_u32 v60, %[vo], v63\n\t" \
  "v_mfma_f32_16x16x32_bf16 v[152:155], v[112:115], v[88:91], v[56:59]\n\t" \
  "ds_read_b32 v0, v61\n\t" \
  "ds_read_b64 v[20:21], v60\n\t" \
  "ds_read_b64 v[30:31], v60 offset:8\n\t" \
  "s_mov_b64 exec, %[rm]\n\t" \
  "ds_read_b128 v[160:163], v63\n\t" \
  "s_mov_b64 exec, -1\n\t" \
  "v_mfma_f32_16x16x32_bf16 v[156:159], v[120:123], v[88:91], v[52:55]\n\t" \
  "v_cvt_pk_bf16_f32 v64, v64, v65\n\t" \
  "v_cvt_pk_bf16_f32 v65, v66, v67\n\t" \
  "v_cvt_pk_bf16_f32 v66, v68, v69\n\t" \
  "v_cvt_pk_bf16_f32 v67, v70, v71\n\t" \
  "v_max_f32 v136, v136, v137\n\t" \
  "v_max_f32 v138, v138, v139\n\t" \
  "v_mfma_f32_16x16x32_bf16 v[132:135], v[96:99], v[64:67], v[52:55]\n\t" \
  "v_max_f32 v140, v140, v141\n\t" \
  "v_max_f32 v142, v142, v143\n\t" \
  "AW_%=:\n\t" \
  "s_waitcnt lgkmcnt(0)\n\t" \
  "v_cmp_eq_u32 vcc, 0, v0\n\t" \
  "v_max3_f32 v5, v136, v138, v140\n\t" \
  "v_max_f32 v5, v5, v142\n\t" \
  "s_cbranch_vccnz AR_%=\n\t" \
  "s_cmp_eq_u32 %[z], 0\n\t" \
  "s_cbranch_scc1 AZ_%=\n\t" \
  "AY_%=:\n\t" \
  "v_mfma_f32_16x16x32_bf16 v[128:131], v[160:163], v[64:67], v[56:59]\n\t" \
  "v_max_f32 v144, v144, v145\n\t" \
  "v_max_f32 v146, v146, v147\n\t" \
  "v_max_f32 v148, v148, v149\n\t" \
  "v_max_f32 v150, v150, v151\n\t" \
  "v_max3_f32 v6, v144, v146, v148\n\t" \
  "v_max_f32 v6, v6, v150\n\t" \
  "v_max_f32 v152, v152, v153\n\t" \
  "v_max_f32 v154, v154, v155\n\t" \
  "v_max_f32 v156, v156, v157\n\t" \
  "v_max_f32 v158, v158, v159\n\t" \
  "v_max3_f32 v7, v152, v154, v156\n\t" \
  "v_max_f32 v7, v7, v158\n\t" \
  "v_mov_b32 v8, 0xc0c0100\n\t" \
  "v_mov_b32 v9, 0x1000c0c\n\t" \
  "v_max_f32 v128, v128, v129\n\t" \
  "v_max_f32 v130, v130, v131\n\t" \
  "v_max_f32 v132, v132, v133\n\t" \
  "v_max_f32 v134, v134, v135\n\t" \
  "v_max3_f32 v4, v128, v130, v132\n\t" \
  "v_max_f32 v4, v4, v134\n\t" \
  "v_cndmask_b32_e64 v60, v8, v9, s[84:85]\n\t" \
  "v_mov_b32 v8, 0xc0c0302\n\t" \
  "v_permlane16_swap_b32 v6, v7\n\t" \
  "v_permlane16_swap_b32 v4, v5\n\t" \
  "v_max_f32 v6, v6, v7\n\t" \
  "v_max_f32 v4, v4, v5\n\t" \
  "v_mov_b32 v9, 0x3020c0c\n\t" \
  "v_cndmask_b32_e64 v61, v8, v9, s[84:85]\n\t" \
  "v_permlane32_swap_b32 v4, v6\n\t" \
  "v_max_f32 v4, v4, v6\n\t" \
  "v_mul_f32 v6, 0xbe8293ee, v4\n\t" \
  "v_mul_f32 v4, 0xbe8293ee, v4\n\t" \
  "s_nop 1\n\t" \
  "v_permlane32_swap_b32 v4, v6\n\t" \
  "v_mov_b32 v5, v4\n\t" \
  "v_mov_b32 v7, v6\n\t" \
  "s_nop 0\n\t" \
  "v_permlane16_swap_b32 v4, v5\n\t" \
  "v_permlane16_swap_b32 v6, v7\n\t" \
  "v_fmamk_f32 v128, v128, 0x3e8293ee, v4\n\t" \
  "v_fmamk_f32 v130, v130, 0x3e8293ee, v4\n\t" \
  "v_fmamk_f32 v132, v132, 0x3e8293ee, v4\n\t" \
  "v_fmamk_f32 v134, v134, 0x3e8293ee, v4\n\t" \
  "v_fmamk_f32 v136, v136, 0x3e8293ee, v5\n\t" \
  "v_fmamk_f32 v138, v138, 0x3e8293ee, v5\n\t" \
  "v_fmamk_f32 v140, v140, 0x3e8293ee, v5\n\t" \
  "v_fmamk_f32 v142, v142, 0x3e8293ee, v5\n\t" \
  "v_fmamk_f32 v144, v144, 0x3e8293ee, v6\n\t" \
  "v_fmamk_f32 v146, v146, 0x3e8293ee, v6\n\t" \
  "v_fmamk_f32 v148, v148, 0x3e8293ee, v6\n\t" \
  "v_fmamk_f32 v150, v150, 0x3e8293ee, v6\n\t" \
  "v_fmamk_f32 v152, v152, 0x3e8293ee, v7\n\t" \
  "v_fmamk_f32 v154, v154, 0x3e8293ee, v7\n\t" \
  "v_fmamk_f32 v156, v156, 0x3e8293ee, v7\n\t" \
  "v_fmamk_f32 v158, v158, 0x3e8293ee, v7\n\t" \
  "v_exp_f32 v128, v128\n\t" \
  "v_exp_f32 v130, v130\n\t" \
  "v_exp_f32 v132, v132\n\t" \
  "v_exp_f32 v134, v134\n\t" \
  "v_exp_f32 v136, v136\n\t" \
  "v_exp_f32 v138, v138\n\t" \
  "v_exp_f32 v140, v140\n\t" \
  "v_exp_f32 v142, v142\n\t" \
  "v_exp_f32 v144, v144\n\t" \
  "v_exp_f32 v146, v146\n\t" \
  "v_exp_f32 v148, v148\n\t" \
  "v_exp_f32 v150, v150\n\t" \
  "v_exp_f32 v152, v152\n\t" \
  "v_exp_f32 v154, v154\n\t" \
  "v_exp_f32 v156, v156\n\t" \
  "v_exp_f32 v158, v158\n\t" \
  "v_add_f32 v4, v128, v130\n\t" \
  "v_add_f32 v8, v132, v134\n\t" \
  "v_add_f32 v5, v136, v138\n\t" \
  "v_add_f32 v9, v140, v142\n\t" \
  "v_add_f32 v6, v144, v146\n\t" \
  "v_add_f32 v10, v148, v150\n\t" \
  "v_add_f32 v7, v152, v154\n\t" \
  "v_add_f32 v11, v156, v158\n\t" \
  "v_add_f32 v4, v4, v8\n\t" \
  "v_add_f32 v5, v5, v9\n\t" \
  "v_add_f32 v6, v6, v10\n\t" \
  "v_add_f32 v7, v7, v11\n\t" \
  "v_permlane16_swap_b32 v4, v5\n\t" \
  "s_nop 0\n\t" \
  "v_permlane16_swap_b32 v6, v7\n\t" \
  "v_add_f32 v4, v4, v5\n\t" \
  "v_add_f32 v6, v6, v7\n\t" \
  "s_nop 1\n\t" \
  "v_permlane32_swap_b32 v4, v6\n\t" \
  "v_add_f32 v4, v4, v6\n\t" \
  "v_rcp_f32 v4, v4\n\t" \
  "s_nop 0\n\t" \
  "v_mov_b32 v6, v4\n\t" \
  "s_nop 1\n\t" \
  "v_permlane32_swap_b32 v4, v6\n\t" \
  "v_mov_b32 v5, v4\n\t" \
  "v_mov_b32 v7, v6\n\t" \
  "s_nop 0\n\t" \
  "v_permlane16_swap_b32 v4, v5\n\t" \
  "v_permlane16_swap_b32 v6, v7\n\t" \
  "v_mul_f32 v128, v128, v4\n\t" \
  "v_mul_f32 v130, v130, v4\n\t" \
  "v_mul_f32 v132, v132, v4\n\t" \
  "v_mul_f32 v134, v134, v4\n\t" \
  "v_cvt_pk_bf16_f32 v128, v128, v130\n\t" \
  "v_cvt_pk_bf16_f32 v132, v132, v134\n\t" \
  "v_perm_b32 v68, 0, v128, v60\n\t" \
  "v_perm_b32 v69, 0, v128, v61\n\t" \
  "v_perm_b32 v70, 0, v132, v60\n\t" \
  "v_perm_b32 v71, 0, v132, v61\n\t" \
  "v_mul_f32 v136, v136, v5\n\t" \
  "v_mul_f32 v138, v138, v5\n\t" \
  "v_mfma_f32_16x16x32_bf16 v[128:131], v[20:23], v[68:71], 0\n\t" \
  "v_mul_f32 v140, v140, v5\n\t" \
  "v_mul_f32 v142, v142, v5\n\t" \
  "v_cvt_pk_bf16_f32 v136, v136, v138\n\t" \
  "v_mfma_f32_16x16x32_bf16 v[132:135], v[30:33], v[68:71], 0\n\t" \
  "v_cvt_pk_bf16_f32 v140, v140, v142\n\t" \
  "v_perm_b32 v76, 0, v136, v60\n\t" \
  "v_perm_b32 v77, 0, v136, v61\n\t" \
  "v_perm_b32 v78, 0, v140, v60\n\t" \
  "v_perm_b32 v79, 0, v140, v61\n\t" \
  "v_mul_f32 v144, v144, v6\n\t" \
  "v_mul_f32 v146, v146, v6\n\t" \
  "v_mfma_f32_16x16x32_bf16 v[136:139], v[22:25], v[76:79], 0\n\t" \
  "v_mul_f32 v148, v148, v6\n\t" \
  "v_mul_f32 v150, v150, v6\n\t" \
  "v_cvt_pk_bf16_f32 v144, v144, v146\n\t" \
  "v_mfma_f32_16x16x32_bf16 v[140:143], v[32:35], v[76:79], 0\n\t" \
  "v_cvt_pk_bf16_f32 v148, v148, v150\n\t" \
  "v_perm_b32 v84, 0, v144, v60\n\t" \
  "v_perm_b32 v85, 0, v144, v61\n\t" \
  "v_perm_b32 v86, 0, v148, v60\n\t" \
  "v_perm_b32 v87, 0, v148, v61\n\t" \
  "v_cvt_pk_bf16_f32 v100, v128, v129\n\t" \
  "v_cvt_pk_bf16_f32 v101, v130, v131\n\t" \
  "v_mfma_f32_16x16x32_bf16 v[144:147], v[24:27], v[84:87], 0\n\t" \
  "v_cvt_pk_bf16_f32 v102, v132, v133\n\t" \
  "v_cvt_pk_bf16_f32 v103, v134, v135\n\t" \
  "global_store_dwordx4 v62, v[100:103], %[ot] offset:0 sc1\n\t" \
  "v_mfma_f32_16x16x32_bf16 v[148:151], v[34:37], v[84:87], 0\n\t" \
  "v_mul_f32 v152, v152, v7\n\t" \
  "v_mul_f32 v154, v154, v7\n\t" \
  "v_mul_f32 v156, v156, v7\n\t" \
  "v_mul_f32 v158, v158, v7\n\t" \
  "v_cvt_pk_bf16_f32 v152, v152, v154\n\t" \
  "v_cvt_pk_bf16_f32 v156, v156, v158\n\t" \
  "v_perm_b32 v92, 0, v152, v60\n\t" \
  "v_perm_b32 v93, 0, v152, v61\n\t" \
  "v_perm_b32 v94, 0, v156, v60\n\t" \
  "v_perm_b32 v95, 0, v156, v61\n\t" \
  "v_cvt_pk_bf16_f32 v108, v136, v137\n\t" \
  "v_cvt_pk_bf16_f32 v109, v138, v139\n\t" \
  "v_mfma_f32_16x16x32_bf16 v[152:155], v[26:29], v[92:95], 0\n\t" \
  "v_cvt_pk_bf16_f32 v110, v140, v141\n\t" \
  "v_cvt_pk_bf16_f32 v111, v142, v143\n\t" \
  "global_store_dwordx4 v62, v[108:111], %[ot] offset:1024 sc1\n\t" \
  "v_mfma_f32_16x16x32_bf16 v[156:159], v[36:39], v[92:95], 0\n\t" \
  "v_cvt_pk_bf16_f32 v116, v144, v145\n\t" \
  "v_cvt_pk_bf16_f32 v117, v146, v147\n\t" \
  "v_cvt_pk_bf16_f32 v118, v148, v149\n\t" \
  "v_cvt_pk_bf16_f32 v119, v150, v151\n\t" \
  "global_store_dwordx4 v62, v[116:119], %[ou] offset:0 sc1\n\t" \
  "v_cvt_pk_bf16_f32 v124, v152, v153\n\t" \
  "v_cvt_pk_bf16_f32 v125, v154, v155\n\t" \
  "s_nop 0\n\t" \
  "v_cvt_pk_bf16_f32 v126, v156, v157\n\t" \
  "v_cvt_pk_bf16_f32 v127, v158, v159\n\t" \
  "global_store_dwordx4 v62, v[124:127], %[ou] offset:1024 sc1\n\t" \
  "s_branch AE_%=\n\t" \
  "AR_%=:\n\t" \
  "s_addk_i32 %[it], 1\n\t" \
  "s_cmp_gt_u32 %[it], 0x400000\n\t" \
  "s_cbranch_scc1 AY_%=\n\t" \
  "ds_read_b32 v0, v61\n\t" \
  "ds_read_b64 v[20:21], v60\n\t" \
  "ds_read_b64 v[30:31], v60 offset:8\n\t" \
  "s_mov_b64 exec, %[rm]\n\t" \
  "ds_read_b128 v[160:163], v63\n\t" \
  "s_mov_b64 exec, -1\n\t" \
  "s_branch AW_%=\n\t" \
  "AZ_%=:\n\t" \
  "v_mov_b32 v0, 0xff61b1e6\n\t" \
  "v_mov_b32 v1, 0xcfa8965f\n\t" \
  "s_nop 3\n\t" \
  "v_cndmask_b32_e64 v56, v0, v1, s[68:69]\n\t" \
  "v_cndmask_b32_e64 v57, v0, v1, s[70:71]\n\t" \
  "v_cndmask_b32_e64 v58, v0, v1, s[72:73]\n\t" \
  "v_cndmask_b32_e64 v59, v0, v1, s[74:75]\n\t" \
  "s_nop 1\n\t" \
  "s_branch AY_%=\n\t" \
  "AE_%=:\n\t" \
  "s_nop 1\n\t"
#define N2_CLOB "v0", "v1", "v2", "v3", "v4", "v5", "v6", "v7", "v8", "v9", "v10", "v11", "v12", "v13", "v14", "v15", "v16", "v17", "v18", "v19", "v20", "v21", "v22", "v23", "v24", "v25", "v26", "v27", "v28", "v29", "v30", "v31", "v32", "v33", "v34", "v35", "v36", "v37", "v38", "v39", "v40", "v41", "v42", "v43", "v44", "v45", "v46", "v47", "v48", "v49", "v50", "v51", "v52", "v53", "v54", "v55", "v60", "v61", "v64", "v65", "v66", "v67", "v68", "v69", "v70", "v71", "v72", "v73", "v74", "v75", "v76", "v77", "v78", "v79", "v80", "v81", "v82", "v83", "v84", "v85", "v86", "v87", "v88", "v89", "v90", "v91", "v92", "v93", "v94", "v95", "v96", "v97", "v98", "v99", "v100", "v101", "v102", "v103", "v104", "v105", "v106", "v107", "v108", "v109", "v110", "v111", "v112", "v113", "v114", "v115", "v116", "v117", "v118", "v119", "v120", "v121", "v122", "v123", "v124", "v125", "v126", "v127", "v128", "v129", "v130", "v131", "v132", "v133", "v134", "v135", "v136", "v137", "v138", "v139", "v140", "v141", "v142", "v143", "v144", "v145", "v146", "v147", "v148", "v149", "v150", "v151", "v152", "v153", "v154", "v155", "v156", "v157", "v158", "v159", "v160", "v161", "v162", "v163", "v164", "v165", "v166", "v167", "s68", "s69", "s70", "s71", "s72", "s73", "s74", "s75", "s76", "s77", "s78", "s79", "s80", "s81", "s82", "s83", "s84", "s85"

#define CV_ASM \
  "ds_read_b64 v[0:1], v28 offset:0\n\t" \
  "ds_read_b64 v[2:3], v29 offset:0\n\t" \
  "ds_read_b64 v[4:5], v30 offset:1024\n\t" \
  "ds_read_b64 v[6:7], v31 offset:1024\n\t" \
  "ds_read_b64 v[8:9], v24 offset:2048\n\t" \
  "ds_read_b64 v[10:11], v25 offset:2048\n\t" \
  "ds_read_b64 v[12:13], v26 offset:3072\n\t" \
  "ds_read_b64 v[14:15], v27 offset:3072\n\t" \
  "ds_read_b64 v[16:17], v28 offset:4096\n\t" \
  "ds_read_b64 v[18:19], v29 offset:4096\n\t" \
  "ds_read_b64 v[20:21], v30 offset:5120\n\t" \
  "ds_read_b64 v[22:23], v31 offset:5120\n\t" \
  "s_waitcnt lgkmcnt(10)\n\t" \
  "v_mfma_f32_16x16x32_bf16 v[128:131], v[68:71], v[0:3], 0\n\t" \
  "v_mfma_f32_16x16x32_bf16 v[132:135], v[76:79], v[0:3], 0\n\t" \
  "v_mfma_f32_16x16x32_bf16 v[136:139], v[84:87], v[0:3], 0\n\t" \
  "v_mfma_f32_16x16x32_bf16 v[140:143], v[92:95], v[0:3], 0\n\t" \
  "v_mfma_f32_16x16x32_bf16 v[144:147], v[100:103], v[0:3], 0\n\t" \
  "v_mfma_f32_16x16x32_bf16 v[148:151], v[108:111], v[0:3], 0\n\t" \
  "v_mfma_f32_16x16x32_bf16 v[152:155], v[116:119], v[0:3], 0\n\t" \
  "v_mfma_f32_16x16x32_bf16 v[156:159], v[124:127], v[0:3], 0\n\t" \
  "ds_read_b64 v[0:1], v24 offset:6144\n\t" \
  "s_waitcnt lgkmcnt(9)\n\t" \
  "v_mfma_f32_16x16x32_bf16 v[128:131], v[64:67], v[4:7], v[128:131]\n\t" \
  "ds_read_b64 v[2:3], v25 offset:6144\n\t" \
  "v_mfma_f32_16x16x32_bf16 v[132:135], v[72:75], v[4:7], v[132:135]\n\t" \
  "v_mfma_f32_16x16x32_bf16 v[136:139], v[80:83], v[4:7], v[136:139]\n\t" \
  "v_mfma_f32_16x16x32_bf16 v[140:143], v[88:91], v[4:7], v[140:143]\n\t" \
  "v_mfma_f32_16x16x32_bf16 v[144:147], v[96:99], v[4:7], v[144:147]\n\t" \
  "v_mfma_f32_16x16x32_bf16 v[148:151], v[104:107], v[4:7], v[148:151]\n\t" \
  "v_mfma_f32_16x16x32_bf16 v[152:155], v[112:115], v[4:7], v[152:155]\n\t" \
  "v_mfma_f32_16x16x32_bf16 v[156:159], v[120:123], v[4:7], v[156:159]\n\t" \
  "ds_read_b64 v[4:5], v26 offset:7168\n\t" \
  "ds_bpermute_b32 v34, v32, v131\n\t" \
  "v_readlane_b32 %[p0], v131, 63\n\t" \
  "s_waitcnt lgkmcnt(10)\n\t" \
  "v_mfma_f32_16x16x32_bf16 v[132:135], v[68:71], v[8:11], v[132:135]\n\t" \
  "ds_read_b64 v[6:7], v27 offset:7168\n\t" \
  "v_mov_b32 v131, v130\n\t" \
  "v_mfma_f32_16x16x32_bf16 v[136:139], v[76:79], v[8:11], v[136:139]\n\t" \
  "v_mov_b32 v130, v129\n\t" \
  "v_mfma_f32_16x16x32_bf16 v[140:143], v[84:87], v[8:11], v[140:143]\n\t" \
  "v_mov_b32 v129, v128\n\t" \
  "v_mfma_f32_16x16x32_bf16 v[144:147], v[92:95], v[8:11], v[144:147]\n\t" \
  "v_mfma_f32_16x16x32_bf16 v[148:151], v[100:103], v[8:11], v[148:151]\n\t" \
  "v_mfma_f32_16x16x32_bf16 v[152:155], v[108:111], v[8:11], v[152:155]\n\t" \
  "v_mfma_f32_16x16x32_bf16 v[156:159], v[116:119], v[8:11], v[156:159]\n\t" \
  "ds_read_b64 v[8:9], v28 offset:8192\n\t" \
  "s_waitcnt lgkmcnt(10)\n\t" \
  "v_mfma_f32_16x16x32_bf16 v[132:135], v[64:67], v[12:15], v[132:135]\n\t" \
  "ds_read_b64 v[10:11], v29 offset:8192\n\t" \
  "v_mfma_f32_16x16x32_bf16 v[136:139], v[72:75], v[12:15], v[136:139]\n\t" \
  "v_mfma_f32_16x16x32_bf16 v[140:143], v[80:83], v[12:15], v[140:143]\n\t" \
  "v_mfma_f32_16x16x32_bf16 v[144:147], v[88:91], v[12:15], v[144:147]\n\t" \
  "v_mfma_f32_16x16x32_bf16 v[148:151], v[96:99], v[12:15], v[148:151]\n\t" \
  "s_waitcnt lgkmcnt(3)\n\t" \
  "v_mov_b32 v128, v34\n\t" \
  "v_writelane_b32 v128, 0, 0\n\t" \
  "v_mfma_f32_16x16x32_bf16 v[152:155], v[104:107], v[12:15], v[152:155]\n\t" \
  "v_mfma_f32_16x16x32_bf16 v[156:159], v[112:115], v[12:15], v[156:159]\n\t" \
  "ds_read_b64 v[12:13], v30 offset:9216\n\t" \
  "s_waitcnt lgkmcnt(10)\n\t" \
  "v_mfma_f32_16x16x32_bf16 v[136:139], v[68:71], v[16:19], v[136:139]\n\t" \
  "ds_read_b64 v[14:15], v31 offset:9216\n\t" \
  "ds_bpermute_b32 v35, v32, v135\n\t" \
  "v_readlane_b32 %[p1], v135, 63\n\t" \
  "v_mfma_f32_16x16x32_bf16 v[140:143], v[76:79], v[16:19], v[140:143]\n\t" \
  "v_mov_b32 v135, v134\n\t" \
  "v_mfma_f32_16x16x32_bf16 v[144:147], v[84:87], v[16:19], v[144:147]\n\t" \
  "v_mov_b32 v134, v133\n\t" \
  "v_mfma_f32_16x16x32_bf16 v[148:151], v[92:95], v[16:19], v[148:151]\n\t" \
  "v_mov_b32 v133, v132\n\t" \
  "v_mfma_f32_16x16x32_bf16 v[152:155], v[100:103], v[16:19], v[152:155]\n\t" \
  "v_mfma_f32_16x16x32_bf16 v[156:159], v[108:111], v[16:19], v[156:159]\n\t" \
  "ds_read_b64 v[16:17], v24 offset:10240\n\t" \
  "s_waitcnt lgkmcnt(11)\n\t" \
  "v_mfma_f32_16x16x32_bf16 v[136:139], v[64:67], v[20:23], v[136:139]\n\t" \
  "ds_read_b64 v[18:19], v25 offset:10240\n\t" \
  "v_mfma_f32_16x16x32_bf16 v[140:143], v[72:75], v[20:23], v[140:143]\n\t" \
  "v_mfma_f32_16x16x32_bf16 v[144:147], v[80:83], v[20:23], v[144:147]\n\t" \
  "v_mfma_f32_16x16x32_bf16 v[148:151], v[88:91], v[20:23], v[148:151]\n\t" \
  "v_mfma_f32_16x16x32_bf16 v[152:155], v[96:99], v[20:23], v[152:155]\n\t" \
  "v_mfma_f32_16x16x32_bf16 v[156:159], v[104:107], v[20:23], v[156:159]\n\t" \
  "ds_read_b64 v[20:21], v26 offset:11264\n\t" \
  "s_waitcnt lgkmcnt(11)\n\t" \
  "v_mfma_f32_16x16x32_bf16 v[140:143], v[68:71], v[0:3], v[140:143]\n\t" \
  "ds_read_b64 v[22:23], v27 offset:11264\n\t" \
  "s_waitcnt lgkmcnt(4)\n\t" \
  "v_mov_b32 v132, v35\n\t" \
  "v_writelane_b32 v132, %[p0], 0\n\t" \
  "v_mfma_f32_16x16x32_bf16 v[144:147], v[76:79], v[0:3], v[144:147]\n\t" \
  "ds_bpermute_b32 v36, v32, v139\n\t" \
  "v_readlane_b32 %[p2], v139, 63\n\t" \
  "v_mfma_f32_16x16x32_bf16 v[148:151], v[84:87], v[0:3], v[148:151]\n\t" \
  "v_mov_b32 v139, v138\n\t" \
  "v_mfma_f32_16x16x32_bf16 v[152:155], v[92:95], v[0:3], v[152:155]\n\t" \
  "v_mov_b32 v138, v137\n\t" \
  "v_mfma_f32_16x16x32_bf16 v[156:159], v[100:103], v[0:3], v[156:159]\n\t" \
  "ds_read_b64 v[0:1], v28 offset:12288\n\t" \
  "v_mov_b32 v137, v136\n\t" \
  "s_waitcnt lgkmcnt(11)\n\t" \
  "v_mfma_f32_16x16x32_bf16 v[140:143], v[64:67], v[4:7], v[140:143]\n\t" \
  "ds_read_b64 v[2:3], v29 offset:12288\n\t" \
  "v_mfma_f32_16x16x32_bf16 v[144:147], v[72:75], v[4:7], v[144:147]\n\t" \
  "v_mfma_f32_16x16x32_bf16 v[148:151], v[80:83], v[4:7], v[148:151]\n\t" \
  "v_mfma_f32_16x16x32_bf16 v[152:155], v[88:91], v[4:7], v[152:155]\n\t" \
  "v_mfma_f32_16x16x32_bf16 v[156:159], v[96:99], v[4:7], v[156:159]\n\t" \
  "ds_read_b64 v[4:5], v30 offset:13312\n\t" \
  "s_waitcnt lgkmcnt(11)\n\t" \
  "v_mfma_f32_16x16x32_bf16 v[144:147], v[68:71], v[8:11], v[144:147]\n\t" \
  "ds_read_b64 v[6:7], v31 offset:13312\n\t" \
  "v_mfma_f32_16x16x32_bf16 v[148:151], v[76:79], v[8:11], v[148:151]\n\t" \
  "v_mfma_f32_16x16x32_bf16 v[152:155], v[84:87], v[8:11], v[152:155]\n\t" \
  "ds_bpermute_b32 v37, v32, v143\n\t" \
  "v_readlane_b32 %[p3], v143, 63\n\t" \
  "v_mfma_f32_16x16x32_bf16 v[156:159], v[92:95], v[8:11], v[156:159]\n\t" \
  "ds_read_b64 v[8:9], v24 offset:14336\n\t" \
  "s_waitcnt lgkmcnt(6)\n\t" \
  "v_mov_b32 v136, v36\n\t" \
  "v_writelane_b32 v136, %[p1], 0\n\t" \
  "v_mov_b32 v143, v142\n\t" \
  "s_waitcnt lgkmcnt(12)\n\t" \
  "v_mfma_f32_16x16x32_bf16 v[144:147], v[64:67], v[12:15], v[144:147]\n\t" \
  "ds_read_b64 v[10:11], v25 offset:14336\n\t" \
  "v_mov_b32 v142, v141\n\t" \
  "v_mfma_f32_16x16x32_bf16 v[148:151], v[72:75], v[12:15], v[148:151]\n\t" \
  "v_mov_b32 v141, v140\n\t" \
  "v_mfma_f32_16x16x32_bf16 v[152:155], v[80:83], v[12:15], v[152:155]\n\t" \
  "v_mfma_f32_16x16x32_bf16 v[156:159], v[88:91], v[12:15], v[156:159]\n\t" \
  "ds_read_b64 v[12:13], v26 offset:15360\n\t" \
  "s_waitcnt lgkmcnt(11)\n\t" \
  "v_mfma_f32_16x16x32_bf16 v[148:151], v[68:71], v[16:19], v[148:151]\n\t" \
  "ds_read_b64 v[14:15], v27 offset:15360\n\t" \
  "v_mfma_f32_16x16x32_bf16 v[152:155], v[76:79], v[16:19], v[152:155]\n\t" \
  "v_mfma_f32_16x16x32_bf16 v[156:159], v[84:87], v[16:19], v[156:159]\n\t" \
  "ds_read_b64 v[16:17], v26 offset:0\n\t" \
  "s_waitcnt lgkmcnt(11)\n\t" \
  "v_mfma_f32_16x16x32_bf16 v[148:151], v[64:67], v[20:23], v[148:151]\n\t" \
  "ds_read_b64 v[18:19], v27 offset:0\n\t" \
  "ds_bpermute_b32 v38, v32, v147\n\t" \
  "v_readlane_b32 %[p4], v147, 63\n\t" \
  "v_mfma_f32_16x16x32_bf16 v[152:155], v[72:75], v[20:23], v[152:155]\n\t" \
  "v_mov_b32 v147, v146\n\t" \
  "v_mfma_f32_16x16x32_bf16 v[156:159], v[80:83], v[20:23], v[156:159]\n\t" \
  "ds_read_b64 v[20:21], v28 offset:1024\n\t" \
  "v_mov_b32 v146, v145\n\t" \
  "s_waitcnt lgkmcnt(11)\n\t" \
  "v_mfma_f32_16x16x32_bf16 v[152:155], v[68:71], v[0:3], v[152:155]\n\t" \
  "ds_read_b64 v[22:23], v29 offset:1024\n\t" \
  "s_waitcnt lgkmcnt(9)\n\t" \
  "v_mov_b32 v140, v37\n\t" \
  "v_writelane_b32 v140, %[p2], 0\n\t" \
  "v_mov_b32 v145, v144\n\t" \
  "v_mfma_f32_16x16x32_bf16 v[156:159], v[76:79], v[0:3], v[156:159]\n\t" \
  "ds_read_b64 v[0:1], v30 offset:2048\n\t" \
  "ds_bpermute_b32 v39, v32, v151\n\t" \
  "v_readlane_b32 %[p5], v151, 63\n\t" \
  "s_waitcnt lgkmcnt(12)\n\t" \
  "v_mfma_f32_16x16x32_bf16 v[152:155], v[64:67], v[4:7], v[152:155]\n\t" \
  "ds_read_b64 v[2:3], v31 offset:2048\n\t" \
  "v_mov_b32 v151, v150\n\t" \
  "v_mfma_f32_16x16x32_bf16 v[156:159], v[72:75], v[4:7], v[156:159]\n\t" \
  "ds_read_b64 v[4:5], v24 offset:3072\n\t" \
  "v_mov_b32 v150, v149\n\t" \
  "s_waitcnt lgkmcnt(11)\n\t" \
  "v_mfma_f32_16x16x32_bf16 v[156:159], v[68:71], v[8:11], v[156:159]\n\t" \
  "ds_read_b64 v[6:7], v25 offset:3072\n\t" \
  "ds_read_b64 v[8:9], v26 offset:4096\n\t" \
  "v_mov_b32 v149, v148\n\t" \
  "ds_bpermute_b32 v40, v32, v155\n\t" \
  "v_readlane_b32 %[p6], v155, 63\n\t" \
  "s_waitcnt lgkmcnt(12)\n\t" \
  "v_mfma_f32_16x16x32_bf16 v[156:159], v[64:67], v[12:15], v[156:159]\n\t" \
  "ds_read_b64 v[10:11], v27 offset:4096\n\t" \
  "ds_read_b64 v[12:13], v28 offset:5120\n\t" \
  "v_mov_b32 v155, v154\n\t" \
  "s_waitcnt lgkmcnt(12)\n\t" \
  "v_mfma_f32_16x16x32_bf16 v[128:131], v[68:71], v[16:19], v[128:131]\n\t" \
  "ds_read_b64 v[14:15], v29 offset:5120\n\t" \
  "v_mov_b32 v154, v153\n\t" \
  "v_mfma_f32_16x16x32_bf16 v[132:135], v[76:79], v[16:19], v[132:135]\n\t" \
  "s_waitcnt lgkmcnt(12)\n\t" \
  "v_mov_b32 v144, v38\n\t" \
  "v_writelane_b32 v144, %[p3], 0\n\t" \
  "v_mov_b32 v153, v152\n\t" \
  "v_mfma_f32_16x16x32_bf16 v[136:139], v[84:87], v[16:19], v[136:139]\n\t" \
  "s_waitcnt lgkmcnt(8)\n\t" \
  "v_mov_b32 v148, v39\n\t" \
  "v_writelane_b32 v148, %[p4], 0\n\t" \
  "v_mfma_f32_16x16x32_bf16 v[140:143], v[92:95], v[16:19], v[140:143]\n\t" \
  "s_waitcnt lgkmcnt(3)\n\t" \
  "v_mov_b32 v152, v40\n\t" \
  "v_writelane_b32 v152, %[p5], 0\n\t" \
  "v_mfma_f32_16x16x32_bf16 v[144:147], v[100:103], v[16:19], v[144:147]\n\t" \
  "ds_bpermute_b32 v41, v32, v159\n\t" \
  "v_mfma_f32_16x16x32_bf16 v[148:151], v[108:111], v[16:19], v[148:151]\n\t" \
  "v_mov_b32 v159, v158\n\t" \
  "v_mfma_f32_16x16x32_bf16 v[152:155], v[116:119], v[16:19], v[152:155]\n\t" \
  "v_mov_b32 v158, v157\n\t" \
  "s_waitcnt lgkmcnt(11)\n\t" \
  "v_mfma_f32_16x16x32_bf16 v[128:131], v[64:67], v[20:23], v[128:131]\n\t" \
  "v_mov_b32 v157, v156\n\t" \
  "v_mfma_f32_16x16x32_bf16 v[132:135], v[72:75], v[20:23], v[132:135]\n\t" \
  "v_mfma_f32_16x16x32_bf16 v[136:139], v[80:83], v[20:23], v[136:139]\n\t" \
  "v_mfma_f32_16x16x32_bf16 v[140:143], v[88:91], v[20:23], v[140:143]\n\t" \
  "v_mfma_f32_16x16x32_bf16 v[144:147], v[96:99], v[20:23], v[144:147]\n\t" \
  "s_waitcnt lgkmcnt(0)\n\t" \
  "v_mov_b32 v156, v41\n\t" \
  "v_writelane_b32 v156, %[p6], 0\n\t" \
  "v_mfma_f32_16x16x32_bf16 v[148:151], v[104:107], v[20:23], v[148:151]\n\t" \
  "v_mfma_f32_16x16x32_bf16 v[152:155], v[112:115], v[20:23], v[152:155]\n\t" \
  "v_mfma_f32_16x16x32_bf16 v[156:159], v[124:127], v[16:19], v[156:159]\n\t" \
  "ds_read_b64 v[16:17], v30 offset:6144\n\t" \
  "ds_bpermute_b32 v34, v32, v131\n\t" \
  "v_readlane_b32 %[p0], v131, 63\n\t" \
  "v_mfma_f32_16x16x32_bf16 v[156:159], v[120:123], v[20:23], v[156:159]\n\t" \
  "ds_read_b64 v[18:19], v31 offset:6144\n\t" \
  "ds_read_b64 v[20:21], v24 offset:7168\n\t" \
  "v_mov_b32 v131, v130\n\t" \
  "s_waitcnt lgkmcnt(12)\n\t" \
  "v_mfma_f32_16x16x32_bf16 v[132:135], v[68:71], v[0:3], v[132:135]\n\t" \
  "ds_read_b64 v[22:23], v25 offset:7168\n\t" \
  "v_mov_b32 v130, v129\n\t" \
  "v_mfma_f32_16x16x32_bf16 v[136:139], v[76:79], v[0:3], v[136:139]\n\t" \
  "v_mov_b32 v129, v128\n\t" \
  "v_mfma_f32_16x16x32_bf16 v[140:143], v[84:87], v[0:3], v[140:143]\n\t" \
  "v_mfma_f32_16x16x32_bf16 v[144:147], v[92:95], v[0:3], v[144:147]\n\t" \
  "v_mfma_f32_16x16x32_bf16 v[148:151], v[100:103], v[0:3], v[148:151]\n\t" \
  "v_mfma_f32_16x16x32_bf16 v[152:155], v[108:111], v[0:3], v[152:155]\n\t" \
  "v_mfma_f32_16x16x32_bf16 v[156:159], v[116:119], v[0:3], v[156:159]\n\t" \
  "ds_read_b64 v[0:1], v26 offset:8192\n\t" \
  "s_waitcnt lgkmcnt(12)\n\t" \
  "v_mfma_f32_16x16x32_bf16 v[132:135], v[64:67], v[4:7], v[132:135]\n\t" \
  "ds_read_b64 v[2:3], v27 offset:8192\n\t" \
  "v_mfma_f32_16x16x32_bf16 v[136:139], v[72:75], v[4:7], v[136:139]\n\t" \
  "v_mfma_f32_16x16x32_bf16 v[140:143], v[80:83], v[4:7], v[140:143]\n\t" \
  "v_mfma_f32_16x16x32_bf16 v[144:147], v[88:91], v[4:7], v[144:147]\n\t" \
  "s_waitcnt lgkmcnt(5)\n\t" \
  "v_mov_b32 v128, v34\n\t" \
  "v_writelane_b32 v128, 0, 0\n\t" \
  "v_mfma_f32_16x16x32_bf16 v[148:151], v[96:99], v[4:7], v[148:151]\n\t" \
  "v_mfma_f32_16x16x32_bf16 v[152:155], v[104:107], v[4:7], v[152:155]\n\t" \
  "v_mfma_f32_16x16x32_bf16 v[156:159], v[112:115], v[4:7], v[156:159]\n\t" \
  "ds_read_b64 v[4:5], v28 offset:9216\n\t" \
  "s_waitcnt lgkmcnt(11)\n\t" \
  "v_mfma_f32_16x16x32_bf16 v[136:139], v[68:71], v[8:11], v[136:139]\n\t" \
  "ds_read_b64 v[6:7], v29 offset:9216\n\t" \
  "ds_bpermute_b32 v35, v32, v135\n\t" \
  "v_readlane_b32 %[p1], v135, 63\n\t" \
  "v_mfma_f32_16x16x32_bf16 v[140:143], v[76:79], v[8:11], v[140:143]\n\t" \
  "v_mov_b32 v135, v134\n\t" \
  "v_mfma_f32_16x16x32_bf16 v[144:147], v[84:87], v[8:11], v[144:147]\n\t" \
  "v_mov_b32 v134, v133\n\t" \
  "v_mfma_f32_16x16x32_bf16 v[148:151], v[92:95], v[8:11], v[148:151]\n\t" \
  "v_mov_b32 v133, v132\n\t" \
  "v_mfma_f32_16x16x32_bf16 v[152:155], v[100:103], v[8:11], v[152:155]\n\t" \
  "v_mfma_f32_16x16x32_bf16 v[156:159], v[108:111], v[8:11], v[156:159]\n\t" \
  "ds_read_b64 v[8:9], v30 offset:10240\n\t" \
  "s_waitcnt lgkmcnt(12)\n\t" \
  "v_mfma_f32_16x16x32_bf16 v[136:139], v[64:67], v[12:15], v[136:139]\n\t" \
  "ds_read_b64 v[10:11], v31 offset:10240\n\t" \
  "v_mfma_f32_16x16x32_bf16 v[140:143], v[72:75], v[12:15], v[140:143]\n\t" \
  "v_mfma_f32_16x16x32_bf16 v[144:147], v[80:83], v[12:15], v[144:147]\n\t" \
  "v_mfma_f32_16x16x32_bf16 v[148:151], v[88:91], v[12:15], v[148:151]\n\t" \
  "v_mfma_f32_16x16x32_bf16 v[152:155], v[96:99], v[12:15], v[152:155]\n\t" \
  "v_mfma_f32_16x16x32_bf16 v[156:159], v[104:107], v[12:15], v[156:159]\n\t" \
  "ds_read_b64 v[12:13], v24 offset:11264\n\t" \
  "s_waitcnt lgkmcnt(10)\n\t" \
  "v_mfma_f32_16x16x32_bf16 v[140:143], v[68:71], v[16:19], v[140:143]\n\t" \
  "ds_read_b64 v[14:15], v25 offset:11264\n\t" \
  "s_waitcnt lgkmcnt(4)\n\t" \
  "v_mov_b32 v132, v35\n\t" \
  "v_writelane_b32 v132, %[p0], 0\n\t" \
  "v_mfma_f32_16x16x32_bf16 v[144:147], v[76:79], v[16:19], v[144:147]\n\t" \
  "ds_bpermute_b32 v36, v32, v139\n\t" \
  "v_readlane_b32 %[p2], v139, 63\n\t" \
  "v_mfma_f32_16x16x32_bf16 v[148:151], v[84:87], v[16:19], v[148:151]\n\t" \
  "v_mov_b32 v139, v138\n\t" \
  "v_mfma_f32_16x16x32_bf16 v[152:155], v[92:95], v[16:19], v[152:155]\n\t" \
  "v_mov_b32 v138, v137\n\t" \
  "v_mfma_f32_16x16x32_bf16 v[156:159], v[100:103], v[16:19], v[156:159]\n\t" \
  "ds_read_b64 v[16:17], v26 offset:12288\n\t" \
  "v_mov_b32 v137, v136\n\t" \
  "s_waitcnt lgkmcnt(11)\n\t" \
  "v_mfma_f32_16x16x32_bf16 v[140:143], v[64:67], v[20:23], v[140:143]\n\t" \
  "ds_read_b64 v[18:19], v27 offset:12288\n\t" \
  "v_mfma_f32_16x16x32_bf16 v[144:147], v[72:75], v[20:23], v[144:147]\n\t" \
  "v_mfma_f32_16x16x32_bf16 v[148:151], v[80:83], v[20:23], v[148:151]\n\t" \
  "v_mfma_f32_16x16x32_bf16 v[152:155], v[88:91], v[20:23], v[152:155]\n\t" \
  "v_mfma_f32_16x16x32_bf16 v[156:159], v[96:99], v[20:23], v[156:159]\n\t" \
  "ds_read_b64 v[20:21], v28 offset:13312\n\t" \
  "s_waitcnt lgkmcnt(11)\n\t" \
  "v_mfma_f32_16x16x32_bf16 v[144:147], v[68:71], v[0:3], v[144:147]\n\t" \
  "ds_read_b64 v[22:23], v29 offset:13312\n\t" \
  "v_mfma_f32_16x16x32_bf16 v[148:151], v[76:79], v[0:3], v[148:151]\n\t" \
  "v_mfma_f32_16x16x32_bf16 v[152:155], v[84:87], v[0:3], v[152:155]\n\t" \
  "ds_bpermute_b32 v37, v32, v143\n\t" \
  "v_readlane_b32 %[p3], v143, 63\n\t" \
  "v_mfma_f32_16x16x32_bf16 v[156:159], v[92:95], v[0:3], v[156:159]\n\t" \
  "ds_read_b64 v[0:1], v30 offset:14336\n\t" \
  "s_waitcnt lgkmcnt(6)\n\t" \
  "v_mov_b32 v136, v36\n\t" \
  "v_writelane_b32 v136, %[p1], 0\n\t" \
  "v_mov_b32 v143, v142\n\t" \
  "s_waitcnt lgkmcnt(12)\n\t" \
  "v_mfma_f32_16x16x32_bf16 v[144:147], v[64:67], v[4:7], v[144:147]\n\t" \
  "ds_read_b64 v[2:3], v31 offset:14336\n\t" \
  "v_mov_b32 v142, v141\n\t" \
  "v_mfma_f32_16x16x32_bf16 v[148:151], v[72:75], v[4:7], v[148:151]\n\t" \
  "v_mov_b32 v141, v140\n\t" \
  "v_mfma_f32_16x16x32_bf16 v[152:155], v[80:83], v[4:7], v[152:155]\n\t" \
  "v_mfma_f32_16x16x32_bf16 v[156:159], v[88:91], v[4:7], v[156:159]\n\t" \
  "ds_read_b64 v[4:5], v24 offset:15360\n\t" \
  "s_waitcnt lgkmcnt(11)\n\t" \
  "v_mfma_f32_16x16x32_bf16 v[148:151], v[68:71], v[8:11], v[148:151]\n\t" \
  "ds_read_b64 v[6:7], v25 offset:15360\n\t" \
  "v_mfma_f32_16x16x32_bf16 v[152:155], v[76:79], v[8:11], v[152:155]\n\t" \
  "v_mfma_f32_16x16x32_bf16 v[156:159], v[84:87], v[8:11], v[156:159]\n\t" \
  "ds_read_b64 v[8:9], v24 offset:0\n\t" \
  "s_waitcnt lgkmcnt(11)\n\t" \
  "v_mfma_f32_16x16x32_bf16 v[148:151], v[64:67], v[12:15], v[148:151]\n\t" \
  "ds_read_b64 v[10:11], v25 offset:0\n\t" \
  "ds_bpermute_b32 v38, v32, v147\n\t" \
  "v_readlane_b32 %[p4], v147, 63\n\t" \
  "v_mfma_f32_16x16x32_bf16 v[152:155], v[72:75], v[12:15], v[152:155]\n\t" \
  "v_mov_b32 v147, v146\n\t" \
  "v_mfma_f32_16x16x32_bf16 v[156:159], v[80:83], v[12:15], v[156:159]\n\t" \
  "ds_read_b64 v[12:13], v26 offset:1024\n\t" \
  "v_mov_b32 v146, v145\n\t" \
  "s_waitcnt lgkmcnt(11)\n\t" \
  "v_mfma_f32_16x16x32_bf16 v[152:155], v[68:71], v[16:19], v[152:155]\n\t" \
  "ds_read_b64 v[14:15], v27 offset:1024\n\t" \
  "s_waitcnt lgkmcnt(9)\n\t" \
  "v_mov_b32 v140, v37\n\t" \
  "v_writelane_b32 v140, %[p2], 0\n\t" \
  "v_mov_b32 v145, v144\n\t" \
  "v_mfma_f32_16x16x32_bf16 v[156:159], v[76:79], v[16:19], v[156:159]\n\t" \
  "ds_read_b64 v[16:17], v28 offset:2048\n\t" \
  "ds_bpermute_b32 v39, v32, v151\n\t" \
  "v_readlane_b32 %[p5], v151, 63\n\t" \
  "s_waitcnt lgkmcnt(12)\n\t" \
  "v_mfma_f32_16x16x32_bf16 v[152:155], v[64:67], v[20:23], v[152:155]\n\t" \
  "ds_read_b64 v[18:19], v29 offset:2048\n\t" \
  "v_mov_b32 v151, v150\n\t" \
  "v_mfma_f32_16x16x32_bf16 v[156:159], v[72:75], v[20:23], v[156:159]\n\t" \
  "ds_read_b64 v[20:21], v30 offset:3072\n\t" \
  "v_mov_b32 v150, v149\n\t" \
  "s_waitcnt lgkmcnt(11)\n\t" \
  "v_mfma_f32_16x16x32_bf16 v[156:159], v[68:71], v[0:3], v[156:159]\n\t" \
  "ds_read_b64 v[22:23], v31 offset:3072\n\t" \
  "ds_read_b64 v[0:1], v24 offset:4096\n\t" \
  "v_mov_b32 v149, v148\n\t" \
  "ds_bpermute_b32 v40, v32, v155\n\t" \
  "v_readlane_b32 %[p6], v155, 63\n\t" \
  "s_waitcnt lgkmcnt(12)\n\t" \
  "v_mfma_f32_16x16x32_bf16 v[156:159], v[64:67], v[4:7], v[156:159]\n\t" \
  "ds_read_b64 v[2:3], v25 offset:4096\n\t" \
  "ds_read_b64 v[4:5], v26 offset:5120\n\t" \
  "v_mov_b32 v155, v154\n\t" \
  "s_waitcnt lgkmcnt(12)\n\t" \
  "v_mfma_f32_16x16x32_bf16 v[128:131], v[68:71], v[8:11], v[128:131]\n\t" \
  "ds_read_b64 v[6:7], v27 offset:5120\n\t" \
  "v_mov_b32 v154, v153\n\t" \
  "v_mfma_f32_16x16x32_bf16 v[132:135], v[76:79], v[8:11], v[132:135]\n\t" \
  "s_waitcnt lgkmcnt(12)\n\t" \
  "v_mov_b32 v144, v38\n\t" \
  "v_writelane_b32 v144, %[p3], 0\n\t" \
  "v_mov_b32 v153, v152\n\t" \
  "v_mfma_f32_16x16x32_bf16 v[136:139], v[84:87], v[8:11], v[136:139]\n\t" \
  "s_waitcnt lgkmcnt(8)\n\t" \
  "v_mov_b32 v148, v39\n\t" \
  "v_writelane_b32 v148, %[p4], 0\n\t" \
  "v_mfma_f32_16x16x32_bf16 v[140:143], v[92:95], v[8:11], v[140:143]\n\t" \
  "s_waitcnt lgkmcnt(3)\n\t" \
  "v_mov_b32 v152, v40\n\t" \
  "v_writelane_b32 v152, %[p5], 0\n\t" \
  "v_mfma_f32_16x16x32_bf16 v[144:147], v[100:103], v[8:11], v[144:147]\n\t" \
  "ds_bpermute_b32 v41, v32, v159\n\t" \
  "v_mfma_f32_16x16x32_bf16 v[148:151], v[108:111], v[8:11], v[148:151]\n\t" \
  "v_mov_b32 v159, v158\n\t" \
  "v_mfma_f32_16x16x32_bf16 v[152:155], v[116:119], v[8:11], v[152:155]\n\t" \
  "v_mov_b32 v158, v157\n\t" \
  "s_waitcnt lgkmcnt(11)\n\t" \
  "v_mfma_f32_16x16x32_bf16 v[128:131], v[64:67], v[12:15], v[128:131]\n\t" \
  "v_mov_b32 v157, v156\n\t" \
  "v_mfma_f32_16x16x32_bf16 v[132:135], v[72:75], v[12:15], v[132:135]\n\t" \
  "v_mfma_f32_16x16x32_bf16 v[136:139], v[80:83], v[12:15], v[136:139]\n\t" \
  "v_mfma_f32_16x16x32_bf16 v[140:143], v[88:91], v[12:15], v[140:143]\n\t" \
  "v_mfma_f32_16x16x32_bf16 v[144:147], v[96:99], v[12:15], v[144:147]\n\t" \
  "s_waitcnt lgkmcnt(0)\n\t" \
  "v_mov_b32 v156, v41\n\t" \
  "v_writelane_b32 v156, %[p6], 0\n\t" \
  "v_mfma_f32_16x16x32_bf16 v[148:151], v[104:107], v[12:15], v[148:151]\n\t" \
  "v_mfma_f32_16x16x32_bf16 v[152:155], v[112:115], v[12:15], v[152:155]\n\t" \
  "v_mfma_f32_16x16x32_bf16 v[156:159], v[124:127], v[8:11], v[156:159]\n\t" \
  "ds_read_b64 v[8:9], v28 offset:6144\n\t" \
  "ds_bpermute_b32 v34, v32, v131\n\t" \
  "v_readlane_b32 %[p0], v131, 63\n\t" \
  "v_mfma_f32_16x16x32_bf16 v[156:159], v[120:123], v[12:15], v[156:159]\n\t" \
  "ds_read_b64 v[10:11], v29 offset:6144\n\t" \
  "ds_read_b64 v[12:13], v30 offset:7168\n\t" \
  "v_mov_b32 v131, v130\n\t" \
  "s_waitcnt lgkmcnt(12)\n\t" \
  "v_mfma_f32_16x16x32_bf16 v[132:135], v[68:71], v[16:19], v[132:135]\n\t" \
  "ds_read_b64 v[14:15], v31 offset:7168\n\t" \
  "v_mov_b32 v130, v129\n\t" \
  "v_mfma_f32_16x16x32_bf16 v[136:139], v[76:79], v[16:19], v[136:139]\n\t" \
  "v_mov_b32 v129, v128\n\t" \
  "v_mfma_f32_16x16x32_bf16 v[140:143], v[84:87], v[16:19], v[140:143]\n\t" \
  "v_mfma_f32_16x16x32_bf16 v[144:147], v[92:95], v[16:19], v[144:147]\n\t" \
  "v_mfma_f32_16x16x32_bf16 v[148:151], v[100:103], v[16:19], v[148:151]\n\t" \
  "v_mfma_f32_16x16x32_bf16 v[152:155], v[108:111], v[16:19], v[152:155]\n\t" \
  "v_mfma_f32_16x16x32_bf16 v[156:159], v[116:119], v[16:19], v[156:159]\n\t" \
  "ds_read_b64 v[16:17], v24 offset:8192\n\t" \
  "s_waitcnt lgkmcnt(12)\n\t" \
  "v_mfma_f32_16x16x32_bf16 v[132:135], v[64:67], v[20:23], v[132:135]\n\t" \
  "ds_read_b64 v[18:19], v25 offset:8192\n\t" \
  "v_mfma_f32_16x16x32_bf16 v[136:139], v[72:75], v[20:23], v[136:139]\n\t" \
  "v_mfma_f32_16x16x32_bf16 v[140:143], v[80:83], v[20:23], v[140:143]\n\t" \
  "v_mfma_f32_16x16x32_bf16 v[144:147], v[88:91], v[20:23], v[144:147]\n\t" \
  "s_waitcnt lgkmcnt(5)\n\t" \
  "v_mov_b32 v128, v34\n\t" \
  "v_writelane_b32 v128, 0, 0\n\t" \
  "v_mfma_f32_16x16x32_bf16 v[148:151], v[96:99], v[20:23], v[148:151]\n\t" \
  "v_mfma_f32_16x16x32_bf16 v[152:155], v[104:107], v[20:23], v[152:155]\n\t" \
  "v_mfma_f32_16x16x32_bf16 v[156:159], v[112:115], v[20:23], v[156:159]\n\t" \
  "ds_read_b64 v[20:21], v26 offset:9216\n\t" \
  "s_waitcnt lgkmcnt(11)\n\t" \
  "v_mfma_f32_16x16x32_bf16 v[136:139], v[68:71], v[0:3], v[136:139]\n\t" \
  "ds_read_b64 v[22:23], v27 offset:9216\n\t" \
  "ds_bpermute_b32 v35, v32, v135\n\t" \
  "v_readlane_b32 %[p1], v135, 63\n\t" \
  "v_mfma_f32_16x16x32_bf16 v[140:143], v[76:79], v[0:3], v[140:143]\n\t" \
  "v_mov_b32 v135, v134\n\t" \
  "v_mfma_f32_16x16x32_bf16 v[144:147], v[84:87], v[0:3], v[144:147]\n\t" \
  "v_mov_b32 v134, v133\n\t" \
  "v_mfma_f32_16x16x32_bf16 v[148:151], v[92:95], v[0:3], v[148:151]\n\t" \
  "v_mov_b32 v133, v132\n\t" \
  "v_mfma_f32_16x16x32_bf16 v[152:155], v[100:103], v[0:3], v[152:155]\n\t" \
  "v_mfma_f32_16x16x32_bf16 v[156:159], v[108:111], v[0:3], v[156:159]\n\t" \
  "ds_read_b64 v[0:1], v28 offset:10240\n\t" \
  "s_waitcnt lgkmcnt(12)\n\t" \
  "v_mfma_f32_16x16x32_bf16 v[136:139], v[64:67], v[4:7], v[136:139]\n\t" \
  "ds_read_b64 v[2:3], v29 offset:10240\n\t" \
  "v_mfma_f32_16x16x32_bf16 v[140:143], v[72:75], v[4:7], v[140:143]\n\t" \
  "v_mfma_f32_16x16x32_bf16 v[144:147], v[80:83], v[4:7], v[144:147]\n\t" \
  "v_mfma_f32_16x16x32_bf16 v[148:151], v[88:91], v[4:7], v[148:151]\n\t" \
  "v_mfma_f32_16x16x32_bf16 v[152:155], v[96:99], v[4:7], v[152:155]\n\t" \
  "v_mfma_f32_16x16x32_bf16 v[156:159], v[104:107], v[4:7], v[156:159]\n\t" \
  "ds_read_b64 v[4:5], v30 offset:11264\n\t" \
  "s_waitcnt lgkmcnt(10)\n\t" \
  "v_mfma_f32_16x16x32_bf16 v[140:143], v[68:71], v[8:11], v[140:143]\n\t" \
  "ds_read_b64 v[6:7], v31 offset:11264\n\t" \
  "s_waitcnt lgkmcnt(4)\n\t" \
  "v_mov_b32 v132, v35\n\t" \
  "v_writelane_b32 v132, %[p0], 0\n\t" \
  "v_mfma_f32_16x16x32_bf16 v[144:147], v[76:79], v[8:11], v[144:147]\n\t" \
  "ds_bpermute_b32 v36, v32, v139\n\t" \
  "v_readlane_b32 %[p2], v139, 63\n\t" \
  "v_mfma_f32_16x16x32_bf16 v[148:151], v[84:87], v[8:11], v[148:151]\n\t" \
  "v_mov_b32 v139, v138\n\t" \
  "v_mfma_f32_16x16x32_bf16 v[152:155], v[92:95], v[8:11], v[152:155]\n\t" \
  "v_mov_b32 v138, v137\n\t" \
  "v_mfma_f32_16x16x32_bf16 v[156:159], v[100:103], v[8:11], v[156:159]\n\t" \
  "ds_read_b64 v[8:9], v24 offset:12288\n\t" \
  "v_mov_b32 v137, v136\n\t" \
  "s_waitcnt lgkmcnt(11)\n\t" \
  "v_mfma_f32_16x16x32_bf16 v[140:143], v[64:67], v[12:15], v[140:143]\n\t" \
  "ds_read_b64 v[10:11], v25 offset:12288\n\t" \
  "v_mfma_f32_16x16x32_bf16 v[144:147], v[72:75], v[12:15], v[144:147]\n\t" \
  "v_mfma_f32_16x16x32_bf16 v[148:151], v[80:83], v[12:15], v[148:151]\n\t" \
  "v_mfma_f32_16x16x32_bf16 v[152:155], v[88:91], v[12:15], v[152:155]\n\t" \
  "v_mfma_f32_16x16x32_bf16 v[156:159], v[96:99], v[12:15], v[156:159]\n\t" \
  "ds_read_b64 v[12:13], v26 offset:13312\n\t" \
  "s_waitcnt lgkmcnt(11)\n\t" \
  "v_mfma_f32_16x16x32_bf16 v[144:147], v[68:71], v[16:19], v[144:147]\n\t" \
  "ds_read_b64 v[14:15], v27 offset:13312\n\t" \
  "v_mfma_f32_16x16x32_bf16 v[148:151], v[76:79], v[16:19], v[148:151]\n\t" \
  "v_mfma_f32_16x16x32_bf16 v[152:155], v[84:87], v[16:19], v[152:155]\n\t" \
  "ds_bpermute_b32 v37, v32, v143\n\t" \
  "v_readlane_b32 %[p3], v143, 63\n\t" \
  "v_mfma_f32_16x16x32_bf16 v[156:159], v[92:95], v[16:19], v[156:159]\n\t" \
  "ds_read_b64 v[16:17], v28 offset:14336\n\t" \
  "s_waitcnt lgkmcnt(6)\n\t" \
  "v_mov_b32 v136, v36\n\t" \
  "v_writelane_b32 v136, %[p1], 0\n\t" \
  "v_mov_b32 v143, v142\n\t" \
  "s_waitcnt lgkmcnt(12)\n\t" \
  "v_mfma_f32_16x16x32_bf16 v[144:147], v[64:67], v[20:23], v[144:147]\n\t" \
  "ds_read_b64 v[18:19], v29 offset:14336\n\t" \
  "v_mov_b32 v142, v141\n\t" \
  "v_mfma_f32_16x16x32_bf16 v[148:151], v[72:75], v[20:23], v[148:151]\n\t" \
  "v_mov_b32 v141, v140\n\t" \
  "v_mfma_f32_16x16x32_bf16 v[152:155], v[80:83], v[20:23], v[152:155]\n\t" \
  "v_mfma_f32_16x16x32_bf16 v[156:159], v[88:91], v[20:23], v[156:159]\n\t" \
  "ds_read_b64 v[20:21], v30 offset:15360\n\t" \
  "s_waitcnt lgkmcnt(11)\n\t" \
  "v_mfma_f32_16x16x32_bf16 v[148:151], v[68:71], v[0:3], v[148:151]\n\t" \
  "ds_read_b64 v[22:23], v31 offset:15360\n\t" \
  "v_mfma_f32_16x16x32_bf16 v[152:155], v[76:79], v[0:3], v[152:155]\n\t" \
  "v_mfma_f32_16x16x32_bf16 v[156:159], v[84:87], v[0:3], v[156:159]\n\t" \
  "ds_read_b64 v[0:1], v30 offset:0\n\t" \
  "s_waitcnt lgkmcnt(11)\n\t" \
  "v_mfma_f32_16x16x32_bf16 v[148:151], v[64:67], v[4:7], v[148:151]\n\t" \
  "ds_read_b64 v[2:3], v31 offset:0\n\t" \
  "ds_bpermute_b32 v38, v32, v147\n\t" \
  "v_readlane_b32 %[p4], v147, 63\n\t" \
  "v_mfma_f32_16x16x32_bf16 v[152:155], v[72:75], v[4:7], v[152:155]\n\t" \
  "v_mov_b32 v147, v146\n\t" \
  "v_mfma_f32_16x16x32_bf16 v[156:159], v[80:83], v[4:7], v[156:159]\n\t" \
  "ds_read_b64 v[4:5], v24 offset:1024\n\t" \
  "v_mov_b32 v146, v145\n\t" \
  "s_waitcnt lgkmcnt(11)\n\t" \
  "v_mfma_f32_16x16x32_bf16 v[152:155], v[68:71], v[8:11], v[152:155]\n\t" \
  "ds_read_b64 v[6:7], v25 offset:1024\n\t" \
  "s_waitcnt lgkmcnt(9)\n\t" \
  "v_mov_b32 v140, v37\n\t" \
  "v_writelane_b32 v140, %[p2], 0\n\t" \
  "v_mov_b32 v145, v144\n\t" \
  "v_mfma_f32_16x16x32_bf16 v[156:159], v[76:79], v[8:11], v[156:159]\n\t" \
  "ds_read_b64 v[8:9], v26 offset:2048\n\t" \
  "ds_bpermute_b32 v39, v32, v151\n\t" \
  "v_readlane_b32 %[p5], v151, 63\n\t" \
  "s_waitcnt lgkmcnt(12)\n\t" \
  "v_mfma_f32_16x16x32_bf16 v[152:155], v[64:67], v[12:15], v[152:155]\n\t" \
  "ds_read_b64 v[10:11], v27 offset:2048\n\t" \
  "v_mov_b32 v151, v150\n\t" \
  "v_mfma_f32_16x16x32_bf16 v[156:159], v[72:75], v[12:15], v[156:159]\n\t" \
  "ds_read_b64 v[12:13], v28 offset:3072\n\t" \
  "v_mov_b32 v150, v149\n\t" \
  "s_waitcnt lgkmcnt(11)\n\t" \
  "v_mfma_f32_16x16x32_bf16 v[156:159], v[68:71], v[16:19], v[156:159]\n\t" \
  "ds_read_b64 v[14:15], v29 offset:3072\n\t" \
  "ds_read_b64 v[16:17], v30 offset:4096\n\t" \
  "v_mov_b32 v149, v148\n\t" \
  "ds_bpermute_b32 v40, v32, v155\n\t" \
  "v_readlane_b32 %[p6], v155, 63\n\t" \
  "s_waitcnt lgkmcnt(12)\n\t" \
  "v_mfma_f32_16x16x32_bf16 v[156:159], v[64:67], v[20:23], v[156:159]\n\t" \
  "ds_read_b64 v[18:19], v31 offset:4096\n\t" \
  "ds_read_b64 v[20:21], v24 offset:5120\n\t" \
  "v_mov_b32 v155, v154\n\t" \
  "s_waitcnt lgkmcnt(12)\n\t" \
  "v_mfma_f32_16x16x32_bf16 v[128:131], v[68:71], v[0:3], v[128:131]\n\t" \
  "ds_read_b64 v[22:23], v25 offset:5120\n\t" \
  "v_mov_b32 v154, v153\n\t" \
  "v_mfma_f32_16x16x32_bf16 v[132:135], v[76:79], v[0:3], v[132:135]\n\t" \
  "s_waitcnt lgkmcnt(12)\n\t" \
  "v_mov_b32 v144, v38\n\t" \
  "v_writelane_b32 v144, %[p3], 0\n\t" \
  "v_mov_b32 v153, v152\n\t" \
  "v_mfma_f32_16x16x32_bf16 v[136:139], v[84:87], v[0:3], v[136:139]\n\t" \
  "s_waitcnt lgkmcnt(8)\n\t" \
  "v_mov_b32 v148, v39\n\t" \
  "v_writelane_b32 v148, %[p4], 0\n\t" \
  "v_mfma_f32_16x16x32_bf16 v[140:143], v[92:95], v[0:3], v[140:143]\n\t" \
  "s_waitcnt lgkmcnt(3)\n\t" \
  "v_mov_b32 v152, v40\n\t" \
  "v_writelane_b32 v152, %[p5], 0\n\t" \
  "v_mfma_f32_16x16x32_bf16 v[144:147], v[100:103], v[0:3], v[144:147]\n\t" \
  "ds_bpermute_b32 v41, v32, v159\n\t" \
  "v_mfma_f32_16x16x32_bf16 v[148:151], v[108:111], v[0:3], v[148:151]\n\t" \
  "v_mov_b32 v159, v158\n\t" \
  "v_mfma_f32_16x16x32_bf16 v[152:155], v[116:119], v[0:3], v[152:155]\n\t" \
  "v_mov_b32 v158, v157\n\t" \
  "s_waitcnt lgkmcnt(11)\n\t" \
  "v_mfma_f32_16x16x32_bf16 v[128:131], v[64:67], v[4:7], v[128:131]\n\t" \
  "v_mov_b32 v157, v156\n\t" \
  "v_mfma_f32_16x16x32_bf16 v[132:135], v[72:75], v[4:7], v[132:135]\n\t" \
  "v_mfma_f32_16x16x32_bf16 v[136:139], v[80:83], v[4:7], v[136:139]\n\t" \
  "v_mfma_f32_16x16x32_bf16 v[140:143], v[88:91], v[4:7], v[140:143]\n\t" \
  "v_mfma_f32_16x16x32_bf16 v[144:147], v[96:99], v[4:7], v[144:147]\n\t" \
  "s_waitcnt lgkmcnt(0)\n\t" \
  "v_mov_b32 v156, v41\n\t" \
  "v_writelane_b32 v156, %[p6], 0\n\t" \
  "v_mfma_f32_16x16x32_bf16 v[148:151], v[104:107], v[4:7], v[148:151]\n\t" \
  "v_mfma_f32_16x16x32_bf16 v[152:155], v[112:115], v[4:7], v[152:155]\n\t" \
  "s_mov_b64 exec, 1\n\t" \
  "ds_write_b32 v33, v33\n\t" \
  "s_mov_b64 exec, -1\n\t" \
  "v_mfma_f32_16x16x32_bf16 v[156:159], v[124:127], v[0:3], v[156:159]\n\t" \
  "ds_read_b64 v[0:1], v26 offset:6144\n\t" \
  "v_mfma_f32_16x16x32_bf16 v[156:159], v[120:123], v[4:7], v[156:159]\n\t" \
  "ds_read_b64 v[2:3], v27 offset:6144\n\t" \
  "ds_read_b64 v[4:5], v28 offset:7168\n\t" \
  "s_waitcnt lgkmcnt(12)\n\t" \
  "v_mfma_f32_16x16x32_bf16 v[132:135], v[68:71], v[8:11], v[132:135]\n\t" \
  "ds_read_b64 v[6:7], v29 offset:7168\n\t" \
  "v_mfma_f32_16x16x32_bf16 v[136:139], v[76:79], v[8:11], v[136:139]\n\t" \
  "v_mfma_f32_16x16x32_bf16 v[140:143], v[84:87], v[8:11], v[140:143]\n\t" \
  "v_mfma_f32_16x16x32_bf16 v[144:147], v[92:95], v[8:11], v[144:147]\n\t" \
  "v_mfma_f32_16x16x32_bf16 v[148:151], v[100:103], v[8:11], v[148:151]\n\t" \
  "v_mfma_f32_16x16x32_bf16 v[152:155], v[108:111], v[8:11], v[152:155]\n\t" \
  "v_mfma_f32_16x16x32_bf16 v[156:159], v[116:119], v[8:11], v[156:159]\n\t" \
  "ds_read_b64 v[8:9], v30 offset:8192\n\t" \
  "s_waitcnt lgkmcnt(12)\n\t" \
  "v_mfma_f32_16x16x32_bf16 v[132:135], v[64:67], v[12:15], v[132:135]\n\t" \
  "ds_read_b64 v[10:11], v31 offset:8192\n\t" \
  "v_mfma_f32_16x16x32_bf16 v[136:139], v[72:75], v[12:15], v[136:139]\n\t" \
  "v_mfma_f32_16x16x32_bf16 v[140:143], v[80:83], v[12:15], v[140:143]\n\t" \
  "v_mfma_f32_16x16x32_bf16 v[144:147], v[88:91], v[12:15], v[144:147]\n\t" \
  "v_mfma_f32_16x16x32_bf16 v[148:151], v[96:99], v[12:15], v[148:151]\n\t" \
  "v_mfma_f32_16x16x32_bf16 v[152:155], v[104:107], v[12:15], v[152:155]\n\t" \
  "v_mfma_f32_16x16x32_bf16 v[156:159], v[112:115], v[12:15], v[156:159]\n\t" \
  "ds_read_b64 v[12:13], v24 offset:9216\n\t" \
  "s_waitcnt lgkmcnt(11)\n\t" \
  "v_mfma_f32_16x16x32_bf16 v[136:139], v[68:71], v[16:19], v[136:139]\n\t" \
  "ds_read_b64 v[14:15], v25 offset:9216\n\t" \
  "v_mfma_f32_16x16x32_bf16 v[140:143], v[76:79], v[16:19], v[140:143]\n\t" \
  "v_mfma_f32_16x16x32_bf16 v[144:147], v[84:87], v[16:19], v[144:147]\n\t" \
  "v_mfma_f32_16x16x32_bf16 v[148:151], v[92:95], v[16:19], v[148:151]\n\t" \
  "v_mfma_f32_16x16x32_bf16 v[152:155], v[100:103], v[16:19], v[152:155]\n\t" \
  "v_mfma_f32_16x16x32_bf16 v[156:159], v[108:111], v[16:19], v[156:159]\n\t" \
  "ds_read_b64 v[16:17], v26 offset:10240\n\t" \
  "s_waitcnt lgkmcnt(11)\n\t" \
  "v_mfma_f32_16x16x32_bf16 v[136:139], v[64:67], v[20:23], v[136:139]\n\t" \
  "ds_read_b64 v[18:19], v27 offset:10240\n\t" \
  "v_mfma_f32_16x16x32_bf16 v[140:143], v[72:75], v[20:23], v[140:143]\n\t" \
  "v_mfma_f32_16x16x32_bf16 v[144:147], v[80:83], v[20:23], v[144:147]\n\t" \
  "v_mfma_f32_16x16x32_bf16 v[148:151], v[88:91], v[20:23], v[148:151]\n\t" \
  "v_mfma_f32_16x16x32_bf16 v[152:155], v[96:99], v[20:23], v[152:155]\n\t" \
  "v_mfma_f32_16x16x32_bf16 v[156:159], v[104:107], v[20:23], v[156:159]\n\t" \
  "ds_read_b64 v[20:21], v28 offset:11264\n\t" \
  "s_waitcnt lgkmcnt(9)\n\t" \
  "v_mfma_f32_16x16x32_bf16 v[140:143], v[68:71], v[0:3], v[140:143]\n\t" \
  "ds_read_b64 v[22:23], v29 offset:11264\n\t" \
  "v_mfma_f32_16x16x32_bf16 v[144:147], v[76:79], v[0:3], v[144:147]\n\t" \
  "v_mfma_f32_16x16x32_bf16 v[148:151], v[84:87], v[0:3], v[148:151]\n\t" \
  "v_mfma_f32_16x16x32_bf16 v[152:155], v[92:95], v[0:3], v[152:155]\n\t" \
  "v_mfma_f32_16x16x32_bf16 v[156:159], v[100:103], v[0:3], v[156:159]\n\t" \
  "ds_read_b64 v[0:1], v30 offset:12288\n\t" \
  "s_waitcnt lgkmcnt(9)\n\t" \
  "v_mfma_f32_16x16x32_bf16 v[140:143], v[64:67], v[4:7], v[140:143]\n\t" \
  "ds_read_b64 v[2:3], v31 offset:12288\n\t" \
  "v_mfma_f32_16x16x32_bf16 v[144:147], v[72:75], v[4:7], v[144:147]\n\t" \
  "v_mfma_f32_16x16x32_bf16 v[148:151], v[80:83], v[4:7], v[148:151]\n\t" \
  "v_mfma_f32_16x16x32_bf16 v[152:155], v[88:91], v[4:7], v[152:155]\n\t" \
  "v_mfma_f32_16x16x32_bf16 v[156:159], v[96:99], v[4:7], v[156:159]\n\t" \
  "ds_read_b64 v[4:5], v24 offset:13312\n\t" \
  "s_waitcnt lgkmcnt(9)\n\t" \
  "v_mfma_f32_16x16x32_bf16 v[144:147], v[68:71], v[8:11], v[144:147]\n\t" \
  "ds_read_b64 v[6:7], v25 offset:13312\n\t" \
  "v_mfma_f32_16x16x32_bf16 v[148:151], v[76:79], v[8:11], v[148:151]\n\t" \
  "v_mfma_f32_16x16x32_bf16 v[152:155], v[84:87], v[8:11], v[152:155]\n\t" \
  "v_mfma_f32_16x16x32_bf16 v[156:159], v[92:95], v[8:11], v[156:159]\n\t" \
  "ds_read_b64 v[8:9], v26 offset:14336\n\t" \
  "s_waitcnt lgkmcnt(9)\n\t" \
  "v_mfma_f32_16x16x32_bf16 v[144:147], v[64:67], v[12:15], v[144:147]\n\t" \
  "ds_read_b64 v[10:11], v27 offset:14336\n\t" \
  "v_mfma_f32_16x16x32_bf16 v[148:151], v[72:75], v[12:15], v[148:151]\n\t" \
  "v_mfma_f32_16x16x32_bf16 v[152:155], v[80:83], v[12:15], v[152:155]\n\t" \
  "v_mfma_f32_16x16x32_bf16 v[156:159], v[88:91], v[12:15], v[156:159]\n\t" \
  "ds_read_b64 v[12:13], v28 offset:15360\n\t" \
  "s_waitcnt lgkmcnt(9)\n\t" \
  "v_mfma_f32_16x16x32_bf16 v[148:151], v[68:71], v[16:19], v[148:151]\n\t" \
  "ds_read_b64 v[14:15], v29 offset:15360\n\t" \
  "v_mfma_f32_16x16x32_bf16 v[152:155], v[76:79], v[16:19], v[152:155]\n\t" \
  "v_mfma_f32_16x16x32_bf16 v[156:159], v[84:87], v[16:19], v[156:159]\n\t" \
  "s_waitcnt lgkmcnt(8)\n\t" \
  "v_mfma_f32_16x16x32_bf16 v[148:151], v[64:67], v[20:23], v[148:151]\n\t" \
  "v_mfma_f32_16x16x32_bf16 v[152:155], v[72:75], v[20:23], v[152:155]\n\t" \
  "v_mfma_f32_16x16x32_bf16 v[156:159], v[80:83], v[20:23], v[156:159]\n\t" \
  "s_waitcnt lgkmcnt(6)\n\t" \
  "v_mfma_f32_16x16x32_bf16 v[152:155], v[68:71], v[0:3], v[152:155]\n\t" \
  "v_mfma_f32_16x16x32_bf16 v[156:159], v[76:79], v[0:3], v[156:159]\n\t" \
  "s_waitcnt lgkmcnt(4)\n\t" \
  "v_mfma_f32_16x16x32_bf16 v[152:155], v[64:67], v[4:7], v[152:155]\n\t" \
  "v_mfma_f32_16x16x32_bf16 v[156:159], v[72:75], v[4:7], v[156:159]\n\t" \
  "s_waitcnt lgkmcnt(2)\n\t" \
  "v_mfma_f32_16x16x32_bf16 v[156:159], v[68:71], v[8:11], v[156:159]\n\t" \
  "s_waitcnt lgkmcnt(0)\n\t" \
  "v_mfma_f32_16x16x32_bf16 v[156:159], v[64:67], v[12:15], v[156:159]\n\t" \
  "s_waitcnt lgkmcnt(0)\n\t" \
  "s_nop 7\n\t"
#define CV_CLOB "v0", "v1", "v2", "v3", "v4", "v5", "v6", "v7", "v8", "v9", "v10", "v11", "v12", "v13", "v14", "v15", "v16", "v17", "v18", "v19", "v20", "v21", "v22", "v23", "v34", "v35", "v36", "v37", "v38", "v39", "v40", "v41"

#define G3_ASM \
  "s_getpc_b64 s[92:93]\n\t" \
  "s_branch QC_%=\n\t" \
  "ds_read_b64_tr_b16 v[96:97], v160 offset:0\n\t" \
  "ds_read_b64_tr_b16 v[98:99], v160 offset:2048\n\t" \
  "ds_read_b64_tr_b16 v[100:101], v161 offset:0\n\t" \
  "ds_read_b64_tr_b16 v[102:103], v161 offset:2048\n\t" \
  "ds_read_b64_tr_b16 v[104:105], v162 offset:0\n\t" \
  "ds_read_b64_tr_b16 v[106:107], v162 offset:2048\n\t" \
  "ds_read_b64_tr_b16 v[108:109], v163 offset:0\n\t" \
  "ds_read_b64_tr_b16 v[110:111], v163 offset:2048\n\t" \
  "ds_read_b64_tr_b16 v[112:113], v160 offset:4096\n\t" \
  "ds_read_b64_tr_b16 v[114:115], v160 offset:6144\n\t" \
  "ds_read_b64_tr_b16 v[116:117], v161 offset:4096\n\t" \
  "ds_read_b64_tr_b16 v[118:119], v161 offset:6144\n\t" \
  "ds_read_b64_tr_b16 v[120:121], v162 offset:4096\n\t" \
  "ds_read_b64_tr_b16 v[122:123], v162 offset:6144\n\t" \
  "QH_%=:\n\t" \
  "v_add_u32 v166, %[go], v165\n\t" \
  "ds_read_b32 v166, v166\n\t" \
  "s_addk_i32 %[cn], 1\n\t" \
  "s_cmp_gt_u32 %[cn], 0x400000\n\t" \
  "s_cbranch_scc1 QI_%=\n\t" \
  "s_waitcnt lgkmcnt(0)\n\t" \
  "v_cmp_eq_u32 vcc, 0, v166\n\t" \
  "s_cbranch_vccz QI_%=\n\t" \
  "s_sleep 2\n\t" \
  "s_getpc_b64 s[92:93]\n\t" \
  "s_branch QC_%=\n\t" \
  "s_branch QH_%=\n\t" \
  "QI_%=:\n\t" \
  "ds_read_b32 v166, v165\n\t" \
  "s_waitcnt lgkmcnt(11)\n\t" \
  "v_mfma_f32_16x16x32_bf16 v[128:131], v[96:99], v[0:3], 0\n\t" \
  "ds_read_b64_tr_b16 v[124:125], v163 offset:4096\n\t" \
  "v_readlane_b32 s94, v167, 5\n\t" \
  "v_mfma_f32_16x16x32_bf16 v[144:147], v[96:99], v[4:7], 0\n\t" \
  "ds_read_b64_tr_b16 v[126:127], v163 offset:6144\n\t" \
  "s_waitcnt lgkmcnt(2)\n\t" \
  "v_cmp_lt_u32 vcc, %[th], v166\n\t" \
  "v_mfma_f32_16x16x32_bf16 v[132:135], v[100:103], v[0:3], 0\n\t" \
  "s_mov_b64 exec, vcc\n\t" \
  "buffer_load_dwordx4 v[80:83], v164, %[ob], s94 offen offset:0 sc1\n\t" \
  "s_mov_b64 exec, -1\n\t" \
  "v_mfma_f32_16x16x32_bf16 v[148:151], v[100:103], v[4:7], 0\n\t" \
  "ds_read_b64_tr_b16 v[96:97], v160 offset:8192\n\t" \
  "ds_read_b64_tr_b16 v[98:99], v160 offset:10240\n\t" \
  "v_mfma_f32_16x16x32_bf16 v[136:139], v[104:107], v[0:3], 0\n\t" \
  "s_mov_b64 exec, vcc\n\t" \
  "buffer_load_dwordx4 v[84:87], v164, %[ob], s94 offen offset:1024 sc1\n\t" \
  "s_mov_b64 exec, -1\n\t" \
  "v_mfma_f32_16x16x32_bf16 v[152:155], v[104:107], v[4:7], 0\n\t" \
  "ds_read_b64_tr_b16 v[100:101], v161 offset:8192\n\t" \
  "ds_read_b64_tr_b16 v[102:103], v161 offset:10240\n\t" \
  "v_mfma_f32_16x16x32_bf16 v[140:143], v[108:111], v[0:3], 0\n\t" \
  "s_mov_b64 exec, vcc\n\t" \
  "buffer_load_dwordx4 v[88:91], v164, %[ob], s94 offen offset:2048 sc1\n\t" \
  "s_mov_b64 exec, -1\n\t" \
  "v_mfma_f32_16x16x32_bf16 v[156:159], v[108:111], v[4:7], 0\n\t" \
  "ds_read_b64_tr_b16 v[104:105], v162 offset:8192\n\t" \
  "ds_read_b64_tr_b16 v[106:107], v162 offset:10240\n\t" \
  "v_mfma_f32_16x16x32_bf16 v[128:131], v[112:115], v[8:11], v[128:131]\n\t" \
  "s_mov_b64 exec, vcc\n\t" \
  "buffer_load_dwordx4 v[92:95], v164, %[ob], s94 offen offset:3072 sc1\n\t" \
  "s_mov_b64 exec, -1\n\t" \
  "v_mfma_f32_16x16x32_bf16 v[144:147], v[112:115], v[12:15], v[144:147]\n\t" \
  "ds_read_b64_tr_b16 v[108:109], v163 offset:8192\n\t" \
  "ds_read_b64_tr_b16 v[110:111], v163 offset:10240\n\t" \
  "v_mfma_f32_16x16x32_bf16 v[132:135], v[116:119], v[8:11], v[132:135]\n\t" \
  "ds_read_b64_tr_b16 v[112:113], v160 offset:12288\n\t" \
  "ds_read_b32 v166, v165\n\t" \
  "v_mfma_f32_16x16x32_bf16 v[148:151], v[116:119], v[12:15], v[148:151]\n\t" \
  "ds_read_b64_tr_b16 v[114:115], v160 offset:14336\n\t" \
  "s_waitcnt lgkmcnt(11)\n\t" \
  "v_mfma_f32_16x16x32_bf16 v[136:139], v[120:123], v[8:11], v[136:139]\n\t" \
  "ds_read_b64_tr_b16 v[116:117], v161 offset:12288\n\t" \
  "v_mfma_f32_16x16x32_bf16 v[152:155], v[120:123], v[12:15], v[152:155]\n\t" \
  "ds_read_b64_tr_b16 v[118:119], v161 offset:14336\n\t" \
  "s_sub_u32 %[iss], %[iss], vcc_lo\n\t" \
  "v_mfma_f32_16x16x32_bf16 v[140:143], v[124:127], v[8:11], v[140:143]\n\t" \
  "ds_read_b64_tr_b16 v[120:121], v162 offset:12288\n\t" \
  "v_mfma_f32_16x16x32_bf16 v[156:159], v[124:127], v[12:15], v[156:159]\n\t" \
  "ds_read_b64_tr_b16 v[122:123], v162 offset:14336\n\t" \
  "s_add_u32 %[th], %[th], 1\n\t" \
  "s_cbranch_vccz QF1_%=\n\t" \
  "s_waitcnt vmcnt(16)\n\t" \
  "QG1_%=:\n\t" \
  "s_waitcnt lgkmcnt(11)\n\t" \
  "v_mfma_f32_16x16x32_bf16 v[128:131], v[96:99], v[16:19], v[128:131]\n\t" \
  "ds_read_b64_tr_b16 v[124:125], v163 offset:12288\n\t" \
  "v_readlane_b32 s94, v167, 6\n\t" \
  "v_mfma_f32_16x16x32_bf16 v[144:147], v[96:99], v[20:23], v[144:147]\n\t" \
  "ds_read_b64_tr_b16 v[126:127], v163 offset:14336\n\t" \
  "s_waitcnt lgkmcnt(7)\n\t" \
  "v_cmp_lt_u32 vcc, %[th], v166\n\t" \
  "v_mfma_f32_16x16x32_bf16 v[132:135], v[100:103], v[16:19], v[132:135]\n\t" \
  "s_mov_b64 exec, vcc\n\t" \
  "buffer_load_dwordx4 v[0:3], v164, %[ob], s94 offen offset:0 sc1\n\t" \
  "s_mov_b64 exec, -1\n\t" \
  "v_mfma_f32_16x16x32_bf16 v[148:151], v[100:103], v[20:23], v[148:151]\n\t" \
  "ds_read_b64_tr_b16 v[96:97], v160 offset:16384\n\t" \
  "ds_read_b64_tr_b16 v[98:99], v160 offset:18432\n\t" \
  "v_mfma_f32_16x16x32_bf16 v[136:139], v[104:107], v[16:19], v[136:139]\n\t" \
  "s_mov_b64 exec, vcc\n\t" \
  "buffer_load_dwordx4 v[4:7], v164, %[ob], s94 offen offset:1024 sc1\n\t" \
  "s_mov_b64 exec, -1\n\t" \
  "v_mfma_f32_16x16x32_bf16 v[152:155], v[104:107], v[20:23], v[152:155]\n\t" \
  "ds_read_b64_tr_b16 v[100:101], v161 offset:16384\n\t" \
  "ds_read_b64_tr_b16 v[102:103], v161 offset:18432\n\t" \
  "v_mfma_f32_16x16x32_bf16 v[140:143], v[108:111], v[16:19], v[140:143]\n\t" \
  "s_mov_b64 exec, vcc\n\t" \
  "buffer_load_dwordx4 v[8:11], v164, %[ob], s94 offen offset:2048 sc1\n\t" \
  "s_mov_b64 exec, -1\n\t" \
  "v_mfma_f32_16x16x32_bf16 v[156:159], v[108:111], v[20:23], v[156:159]\n\t" \
  "ds_read_b64_tr_b16 v[104:105], v162 offset:16384\n\t" \
  "ds_read_b64_tr_b16 v[106:107], v162 offset:18432\n\t" \
  "s_waitcnt lgkmcnt(10)\n\t" \
  "v_mfma_f32_16x16x32_bf16 v[128:131], v[112:115], v[24:27], v[128:131]\n\t" \
  "s_mov_b64 exec, vcc\n\t" \
  "buffer_load_dwordx4 v[12:15], v164, %[ob], s94 offen offset:3072 sc1\n\t" \
  "s_mov_b64 exec, -1\n\t" \
  "v_mfma_f32_16x16x32_bf16 v[144:147], v[112:115], v[28:31], v[144:147]\n\t" \
  "ds_read_b64_tr_b16 v[108:109], v163 offset:16384\n\t" \
  "ds_read_b64_tr_b16 v[110:111], v163 offset:18432\n\t" \
  "v_mfma_f32_16x16x32_bf16 v[132:135], v[116:119], v[24:27], v[132:135]\n\t" \
  "ds_read_b64_tr_b16 v[112:113], v160 offset:20480\n\t" \
  "ds_read_b32 v166, v165\n\t" \
  "v_mfma_f32_16x16x32_bf16 v[148:151], v[116:119], v[28:31], v[148:151]\n\t" \
  "ds_read_b64_tr_b16 v[114:115], v160 offset:22528\n\t" \
  "s_waitcnt lgkmcnt(11)\n\t" \
  "v_mfma_f32_16x16x32_bf16 v[136:139], v[120:123], v[24:27], v[136:139]\n\t" \
  "ds_read_b64_tr_b16 v[116:117], v161 offset:20480\n\t" \
  "v_mfma_f32_16x16x32_bf16 v[152:155], v[120:123], v[28:31], v[152:155]\n\t" \
  "ds_read_b64_tr_b16 v[118:119], v161 offset:22528\n\t" \
  "s_sub_u32 %[iss], %[iss], vcc_lo\n\t" \
  "v_mfma_f32_16x16x32_bf16 v[140:143], v[124:127], v[24:27], v[140:143]\n\t" \
  "ds_read_b64_tr_b16 v[120:121], v162 offset:20480\n\t" \
  "v_mfma_f32_16x16x32_bf16 v[156:159], v[124:127], v[28:31], v[156:159]\n\t" \
  "ds_read_b64_tr_b16 v[122:123], v162 offset:22528\n\t" \
  "s_add_u32 %[th], %[th], 1\n\t" \
  "s_cbranch_vccz QF2_%=\n\t" \
  "s_waitcnt vmcnt(16)\n\t" \
  "QG2_%=:\n\t" \
  "s_waitcnt lgkmcnt(11)\n\t" \
  "v_mfma_f32_16x16x32_bf16 v[128:131], v[96:99], v[32:35], v[128:131]\n\t" \
  "ds_read_b64_tr_b16 v[124:125], v163 offset:20480\n\t" \
  "v_readlane_b32 s94, v167, 7\n\t" \
  "v_mfma_f32_16x16x32_bf16 v[144:147], v[96:99], v[36:39], v[144:147]\n\t" \
  "ds_read_b64_tr_b16 v[126:127], v163 offset:22528\n\t" \
  "s_waitcnt lgkmcnt(7)\n\t" \
  "v_cmp_lt_u32 vcc, %[th], v166\n\t" \
  "v_mfma_f32_16x16x32_bf16 v[132:135], v[100:103], v[32:35], v[132:135]\n\t" \
  "s_mov_b64 exec, vcc\n\t" \
  "buffer_load_dwordx4 v[16:19], v164, %[ob], s94 offen offset:0 sc1\n\t" \
  "s_mov_b64 exec, -1\n\t" \
  "v_mfma_f32_16x16x32_bf16 v[148:151], v[100:103], v[36:39], v[148:151]\n\t" \
  "ds_read_b64_tr_b16 v[96:97], v160 offset:24576\n\t" \
  "ds_read_b64_tr_b16 v[98:99], v160 offset:26624\n\t" \
  "v_mfma_f32_16x16x32_bf16 v[136:139], v[104:107], v[32:35], v[136:139]\n\t" \
  "s_mov_b64 exec, vcc\n\t" \
  "buffer_load_dwordx4 v[20:23], v164, %[ob], s94 offen offset:1024 sc1\n\t" \
  "s_mov_b64 exec, -1\n\t" \
  "v_mfma_f32_16x16x32_bf16 v[152:155], v[104:107], v[36:39], v[152:155]\n\t" \
  "ds_read_b64_tr_b16 v[100:101], v161 offset:24576\n\t" \
  "ds_read_b64_tr_b16 v[102:103], v161 offset:26624\n\t" \
  "v_mfma_f32_16x16x32_bf16 v[140:143], v[108:111], v[32:35], v[140:143]\n\t" \
  "s_mov_b64 exec, vcc\n\t" \
  "buffer_load_dwordx4 v[24:27], v164, %[ob], s94 offen offset:2048 sc1\n\t" \
  "s_mov_b64 exec, -1\n\t" \
  "v_mfma_f32_16x16x32_bf16 v[156:159], v[108:111], v[36:39], v[156:159]\n\t" \
  "ds_read_b64_tr_b16 v[104:105], v162 offset:24576\n\t" \
  "ds_read_b64_tr_b16 v[106:107], v162 offset:26624\n\t" \
  "s_waitcnt lgkmcnt(10)\n\t" \
  "v_mfma_f32_16x16x32_bf16 v[128:131], v[112:115], v[40:43], v[128:131]\n\t" \
  "s_mov_b64 exec, vcc\n\t" \
  "buffer_load_dwordx4 v[28:31], v164, %[ob], s94 offen offset:3072 sc1\n\t" \
  "s_mov_b64 exec, -1\n\t" \
  "v_mfma_f32_16x16x32_bf16 v[144:147], v[112:115], v[44:47], v[144:147]\n\t" \
  "ds_read_b64_tr_b16 v[108:109], v163 offset:24576\n\t" \
  "ds_read_b64_tr_b16 v[110:111], v163 offset:26624\n\t" \
  "v_mfma_f32_16x16x32_bf16 v[132:135], v[116:119], v[40:43], v[132:135]\n\t" \
  "ds_read_b64_tr_b16 v[112:113], v160 offset:28672\n\t" \
  "ds_read_b32 v166, v165\n\t" \
  "v_mfma_f32_16x16x32_bf16 v[148:151], v[116:119], v[44:47], v[148:151]\n\t" \
  "ds_read_b64_tr_b16 v[114:115], v160 offset:30720\n\t" \
  "s_waitcnt lgkmcnt(11)\n\t" \
  "v_mfma_f32_16x16x32_bf16 v[136:139], v[120:123], v[40:43], v[136:139]\n\t" \
  "ds_read_b64_tr_b16 v[116:117], v161 offset:28672\n\t" \
  "v_mfma_f32_16x16x32_bf16 v[152:155], v[120:123], v[44:47], v[152:155]\n\t" \
  "ds_read_b64_tr_b16 v[118:119], v161 offset:30720\n\t" \
  "s_sub_u32 %[iss], %[iss], vcc_lo\n\t" \
  "v_mfma_f32_16x16x32_bf16 v[140:143], v[124:127], v[40:43], v[140:143]\n\t" \
  "ds_read_b64_tr_b16 v[120:121], v162 offset:28672\n\t" \
  "v_mfma_f32_16x16x32_bf16 v[156:159], v[124:127], v[44:47], v[156:159]\n\t" \
  "ds_read_b64_tr_b16 v[122:123], v162 offset:30720\n\t" \
  "s_add_u32 %[th], %[th], 1\n\t" \
  "s_cbranch_vccz QF3_%=\n\t" \
  "s_waitcnt vmcnt(16)\n\t" \
  "QG3_%=:\n\t" \
  "s_waitcnt lgkmcnt(11)\n\t" \
  "v_mfma_f32_16x16x32_bf16 v[128:131], v[96:99], v[48:51], v[128:131]\n\t" \
  "ds_read_b64_tr_b16 v[124:125], v163 offset:28672\n\t" \
  "v_readlane_b32 s94, v167, 8\n\t" \
  "v_mfma_f32_16x16x32_bf16 v[144:147], v[96:99], v[52:55], v[144:147]\n\t" \
  "ds_read_b64_tr_b16 v[126:127], v163 offset:30720\n\t" \
  "s_waitcnt lgkmcnt(7)\n\t" \
  "v_cmp_lt_u32 vcc, %[th], v166\n\t" \
  "v_mfma_f32_16x16x32_bf16 v[132:135], v[100:103], v[48:51], v[132:135]\n\t" \
  "s_mov_b64 exec, vcc\n\t" \
  "buffer_load_dwordx4 v[32:35], v164, %[ob], s94 offen offset:0 sc1\n\t" \
  "s_mov_b64 exec, -1\n\t" \
  "v_mfma_f32_16x16x32_bf16 v[148:151], v[100:103], v[52:55], v[148:151]\n\t" \
  "ds_read_b64_tr_b16 v[96:97], v160 offset:32768\n\t" \
  "ds_read_b64_tr_b16 v[98:99], v160 offset:34816\n\t" \
  "v_mfma_f32_16x16x32_bf16 v[136:139], v[104:107], v[48:51], v[136:139]\n\t" \
  "s_mov_b64 exec, vcc\n\t" \
  "buffer_load_dwordx4 v[36:39], v164, %[ob], s94 offen offset:1024 sc1\n\t" \
  "s_mov_b64 exec, -1\n\t" \
  "v_mfma_f32_16x16x32_bf16 v[152:155], v[104:107], v[52:55], v[152:155]\n\t" \
  "ds_read_b64_tr_b16 v[100:101], v161 offset:32768\n\t" \
  "ds_read_b64_tr_b16 v[102:103], v161 offset:34816\n\t" \
  "v_mfma_f32_16x16x32_bf16 v[140:143], v[108:111], v[48:51], v[140:143]\n\t" \
  "s_mov_b64 exec, vcc\n\t" \
  "buffer_load_dwordx4 v[40:43], v164, %[ob], s94 offen offset:2048 sc1\n\t" \
  "s_mov_b64 exec, -1\n\t" \
  "v_mfma_f32_16x16x32_bf16 v[156:159], v[108:111], v[52:55], v[156:159]\n\t" \
  "ds_read_b64_tr_b16 v[104:105], v162 offset:32768\n\t" \
  "ds_read_b64_tr_b16 v[106:107], v162 offset:34816\n\t" \
  "s_waitcnt lgkmcnt(10)\n\t" \
  "v_mfma_f32_16x16x32_bf16 v[128:131], v[112:115], v[56:59], v[128:131]\n\t" \
  "s_mov_b64 exec, vcc\n\t" \
  "buffer_load_dwordx4 v[44:47], v164, %[ob], s94 offen offset:3072 sc1\n\t" \
  "s_mov_b64 exec, -1\n\t" \
  "v_mfma_f32_16x16x32_bf16 v[144:147], v[112:115], v[60:63], v[144:147]\n\t" \
  "ds_read_b64_tr_b16 v[108:109], v163 offset:32768\n\t" \
  "ds_read_b64_tr_b16 v[110:111], v163 offset:34816\n\t" \
  "v_mfma_f32_16x16x32_bf16 v[132:135], v[116:119], v[56:59], v[132:135]\n\t" \
  "ds_read_b64_tr_b16 v[112:113], v160 offset:36864\n\t" \
  "ds_read_b32 v166, v165\n\t" \
  "v_mfma_f32_16x16x32_bf16 v[148:151], v[116:119], v[60:63], v[148:151]\n\t" \
  "ds_read_b64_tr_b16 v[114:115], v160 offset:38912\n\t" \
  "s_waitcnt lgkmcnt(11)\n\t" \
  "v_mfma_f32_16x16x32_bf16 v[136:139], v[120:123], v[56:59], v[136:139]\n\t" \
  "ds_read_b64_tr_b16 v[116:117], v161 offset:36864\n\t" \
  "v_mfma_f32_16x16x32_bf16 v[152:155], v[120:123], v[60:63], v[152:155]\n\t" \
  "ds_read_b64_tr_b16 v[118:119], v161 offset:38912\n\t" \
  "s_sub_u32 %[iss], %[iss], vcc_lo\n\t" \
  "v_mfma_f32_16x16x32_bf16 v[140:143], v[124:127], v[56:59], v[140:143]\n\t" \
  "ds_read_b64_tr_b16 v[120:121], v162 offset:36864\n\t" \
  "v_mfma_f32_16x16x32_bf16 v[156:159], v[124:127], v[60:63], v[156:159]\n\t" \
  "ds_read_b64_tr_b16 v[122:123], v162 offset:38912\n\t" \
  "s_add_u32 %[th], %[th], 1\n\t" \
  "s_cbranch_vccz QF4_%=\n\t" \
  "s_waitcnt vmcnt(16)\n\t" \
  "QG4_%=:\n\t" \
  "s_waitcnt lgkmcnt(11)\n\t" \
  "v_mfma_f32_16x16x32_bf16 v[128:131], v[96:99], v[64:67], v[128:131]\n\t" \
  "ds_read_b64_tr_b16 v[124:125], v163 offset:36864\n\t" \
  "v_readlane_b32 s94, v167, 9\n\t" \
  "v_mfma_f32_16x16x32_bf16 v[144:147], v[96:99], v[68:71], v[144:147]\n\t" \
  "ds_read_b64_tr_b16 v[126:127], v163 offset:38912\n\t" \
  "s_waitcnt lgkmcnt(7)\n\t" \
  "v_cmp_lt_u32 vcc, %[th], v166\n\t" \
  "v_mfma_f32_16x16x32_bf16 v[132:135], v[100:103], v[64:67], v[132:135]\n\t" \
  "s_mov_b64 exec, vcc\n\t" \
  "buffer_load_dwordx4 v[48:51], v164, %[ob], s94 offen offset:0 sc1\n\t" \
  "s_mov_b64 exec, -1\n\t" \
  "v_mfma_f32_16x16x32_bf16 v[148:151], v[100:103], v[68:71], v[148:151]\n\t" \
  "ds_read_b64_tr_b16 v[96:97], v160 offset:40960\n\t" \
  "ds_read_b64_tr_b16 v[98:99], v160 offset:43008\n\t" \
  "v_mfma_f32_16x16x32_bf16 v[136:139], v[104:107], v[64:67], v[136:139]\n\t" \
  "s_mov_b64 exec, vcc\n\t" \
  "buffer_load_dwordx4 v[52:55], v164, %[ob], s94 offen offset:1024 sc1\n\t" \
  "s_mov_b64 exec, -1\n\t" \
  "v_mfma_f32_16x16x32_bf16 v[152:155], v[104:107], v[68:71], v[152:155]\n\t" \
  "ds_read_b64_tr_b16 v[100:101], v161 offset:40960\n\t" \
  "ds_read_b64_tr_b16 v[102:103], v161 offset:43008\n\t" \
  "v_mfma_f32_16x16x32_bf16 v[140:143], v[108:111], v[64:67], v[140:143]\n\t" \
  "s_mov_b64 exec, vcc\n\t" \
  "buffer_load_dwordx4 v[56:59], v164, %[ob], s94 offen offset:2048 sc1\n\t" \
  "s_mov_b64 exec, -1\n\t" \
  "v_mfma_f32_16x16x32_bf16 v[156:159], v[108:111], v[68:71], v[156:159]\n\t" \
  "ds_read_b64_tr_b16 v[104:105], v162 offset:40960\n\t" \
  "ds_read_b64_tr_b16 v[106:107], v162 offset:43008\n\t" \
  "s_waitcnt lgkmcnt(10)\n\t" \
  "v_mfma_f32_16x16x32_bf16 v[128:131], v[112:115], v[72:75], v[128:131]\n\t" \
  "s_mov_b64 exec, vcc\n\t" \
  "buffer_load_dwordx4 v[60:63], v164, %[ob], s94 offen offset:3072 sc1\n\t" \
  "s_mov_b64 exec, -1\n\t" \
  "v_mfma_f32_16x16x32_bf16 v[144:147], v[112:115], v[76:79], v[144:147]\n\t" \
  "ds_read_b64_tr_b16 v[108:109], v163 offset:40960\n\t" \
  "ds_read_b64_tr_b16 v[110:111], v163 offset:43008\n\t" \
  "v_mfma_f32_16x16x32_bf16 v[132:135], v[116:119], v[72:75], v[132:135]\n\t" \
  "ds_read_b64_tr_b16 v[112:113], v160 offset:45056\n\t" \
  "ds_read_b32 v166, v165\n\t" \
  "v_mfma_f32_16x16x32_bf16 v[148:151], v[116:119], v[76:79], v[148:151]\n\t" \
  "ds_read_b64_tr_b16 v[114:115], v160 offset:47104\n\t" \
  "s_waitcnt lgkmcnt(11)\n\t" \
  "v_mfma_f32_16x16x32_bf16 v[136:139], v[120:123], v[72:75], v[136:139]\n\t" \
  "ds_read_b64_tr_b16 v[116:117], v161 offset:45056\n\t" \
  "v_mfma_f32_16x16x32_bf16 v[152:155], v[120:123], v[76:79], v[152:155]\n\t" \
  "ds_read_b64_tr_b16 v[118:119], v161 offset:47104\n\t" \
  "s_sub_u32 %[iss], %[iss], vcc_lo\n\t" \
  "v_mfma_f32_16x16x32_bf16 v[140:143], v[124:127], v[72:75], v[140:143]\n\t" \
  "ds_read_b64_tr_b16 v[120:121], v162 offset:45056\n\t" \
  "v_mfma_f32_16x16x32_bf16 v[156:159], v[124:127], v[76:79], v[156:159]\n\t" \
  "ds_read_b64_tr_b16 v[122:123], v162 offset:47104\n\t" \
  "s_add_u32 %[th], %[th], 1\n\t" \
  "s_cbranch_vccz QF5_%=\n\t" \
  "s_waitcnt vmcnt(16)\n\t" \
  "QG5_%=:\n\t" \
  "s_waitcnt lgkmcnt(11)\n\t" \
  "v_mfma_f32_16x16x32_bf16 v[128:131], v[96:99], v[80:83], v[128:131]\n\t" \
  "ds_read_b64_tr_b16 v[124:125], v163 offset:45056\n\t" \
  "v_readlane_b32 s94, v167, 10\n\t" \
  "v_mfma_f32_16x16x32_bf16 v[144:147], v[96:99], v[84:87], v[144:147]\n\t" \
  "ds_read_b64_tr_b16 v[126:127], v163 offset:47104\n\t" \
  "s_waitcnt lgkmcnt(7)\n\t" \
  "v_cmp_lt_u32 vcc, %[th], v166\n\t" \
  "v_mfma_f32_16x16x32_bf16 v[132:135], v[100:103], v[80:83], v[132:135]\n\t" \
  "s_mov_b64 exec, vcc\n\t" \
  "buffer_load_dwordx4 v[64:67], v164, %[ob], s94 offen offset:0 sc1\n\t" \
  "s_mov_b64 exec, -1\n\t" \
  "v_mfma_f32_16x16x32_bf16 v[148:151], v[100:103], v[84:87], v[148:151]\n\t" \
  "ds_read_b64_tr_b16 v[96:97], v160 offset:49152\n\t" \
  "ds_read_b64_tr_b16 v[98:99], v160 offset:51200\n\t" \
  "v_mfma_f32_16x16x32_bf16 v[136:139], v[104:107], v[80:83], v[136:139]\n\t" \
  "s_mov_b64 exec, vcc\n\t" \
  "buffer_load_dwordx4 v[68:71], v164, %[ob], s94 offen offset:1024 sc1\n\t" \
  "s_mov_b64 exec, -1\n\t" \
  "v_mfma_f32_16x16x32_bf16 v[152:155], v[104:107], v[84:87], v[152:155]\n\t" \
  "ds_read_b64_tr_b16 v[100:101], v161 offset:49152\n\t" \
  "ds_read_b64_tr_b16 v[102:103], v161 offset:51200\n\t" \
  "v_mfma_f32_16x16x32_bf16 v[140:143], v[108:111], v[80:83], v[140:143]\n\t" \
  "s_mov_b64 exec, vcc\n\t" \
  "buffer_load_dwordx4 v[72:75], v164, %[ob], s94 offen offset:2048 sc1\n\t" \
  "s_mov_b64 exec, -1\n\t" \
  "v_mfma_f32_16x16x32_bf16 v[156:159], v[108:111], v[84:87], v[156:159]\n\t" \
  "ds_read_b64_tr_b16 v[104:105], v162 offset:49152\n\t" \
  "ds_read_b64_tr_b16 v[106:107], v162 offset:51200\n\t" \
  "s_waitcnt lgkmcnt(10)\n\t" \
  "v_mfma_f32_16x16x32_bf16 v[128:131], v[112:115], v[88:91], v[128:131]\n\t" \
  "s_mov_b64 exec, vcc\n\t" \
  "buffer_load_dwordx4 v[76:79], v164, %[ob], s94 offen offset:3072 sc1\n\t" \
  "s_mov_b64 exec, -1\n\t" \
  "v_mfma_f32_16x16x32_bf16 v[144:147], v[112:115], v[92:95], v[144:147]\n\t" \
  "ds_read_b64_tr_b16 v[108:109], v163 offset:49152\n\t" \
  "ds_read_b64_tr_b16 v[110:111], v163 offset:51200\n\t" \
  "v_mfma_f32_16x16x32_bf16 v[132:135], v[116:119], v[88:91], v[132:135]\n\t" \
  "ds_read_b64_tr_b16 v[112:113], v160 offset:53248\n\t" \
  "ds_read_b32 v166, v165\n\t" \
  "v_mfma_f32_16x16x32_bf16 v[148:151], v[116:119], v[92:95], v[148:151]\n\t" \
  "ds_read_b64_tr_b16 v[114:115], v160 offset:55296\n\t" \
  "s_waitcnt lgkmcnt(11)\n\t" \
  "v_mfma_f32_16x16x32_bf16 v[136:139], v[120:123], v[88:91], v[136:139]\n\t" \
  "ds_read_b64_tr_b16 v[116:117], v161 offset:53248\n\t" \
  "v_mfma_f32_16x16x32_bf16 v[152:155], v[120:123], v[92:95], v[152:155]\n\t" \
  "ds_read_b64_tr_b16 v[118:119], v161 offset:55296\n\t" \
  "s_sub_u32 %[iss], %[iss], vcc_lo\n\t" \
  "v_mfma_f32_16x16x32_bf16 v[140:143], v[124:127], v[88:91], v[140:143]\n\t" \
  "ds_read_b64_tr_b16 v[120:121], v162 offset:53248\n\t" \
  "v_mfma_f32_16x16x32_bf16 v[156:159], v[124:127], v[92:95], v[156:159]\n\t" \
  "ds_read_b64_tr_b16 v[122:123], v162 offset:55296\n\t" \
  "s_add_u32 %[th], %[th], 1\n\t" \
  "s_cbranch_vccz QF6_%=\n\t" \
  "s_waitcnt vmcnt(16)\n\t" \
  "QG6_%=:\n\t" \
  "s_waitcnt lgkmcnt(11)\n\t" \
  "v_mfma_f32_16x16x32_bf16 v[128:131], v[96:99], v[0:3], v[128:131]\n\t" \
  "ds_read_b64_tr_b16 v[124:125], v163 offset:53248\n\t" \
  "v_readlane_b32 s94, v167, 11\n\t" \
  "v_mfma_f32_16x16x32_bf16 v[144:147], v[96:99], v[4:7], v[144:147]\n\t" \
  "ds_read_b64_tr_b16 v[126:127], v163 offset:55296\n\t" \
  "s_waitcnt lgkmcnt(7)\n\t" \
  "v_cmp_lt_u32 vcc, %[th], v166\n\t" \
  "v_mfma_f32_16x16x32_bf16 v[132:135], v[100:103], v[0:3], v[132:135]\n\t" \
  "s_mov_b64 exec, vcc\n\t" \
  "buffer_load_dwordx4 v[80:83], v164, %[ob], s94 offen offset:0 sc1\n\t" \
  "s_mov_b64 exec, -1\n\t" \
  "v_mfma_f32_16x16x32_bf16 v[148:151], v[100:103], v[4:7], v[148:151]\n\t" \
  "ds_read_b64_tr_b16 v[96:97], v160 offset:57344\n\t" \
  "ds_read_b64_tr_b16 v[98:99], v160 offset:59392\n\t" \
  "v_mfma_f32_16x16x32_bf16 v[136:139], v[104:107], v[0:3], v[136:139]\n\t" \
  "s_mov_b64 exec, vcc\n\t" \
  "buffer_load_dwordx4 v[84:87], v164, %[ob], s94 offen offset:1024 sc1\n\t" \
  "s_mov_b64 exec, -1\n\t" \
  "v_mfma_f32_16x16x32_bf16 v[152:155], v[104:107], v[4:7], v[152:155]\n\t" \
  "ds_read_b64_tr_b16 v[100:101], v161 offset:57344\n\t" \
  "ds_read_b64_tr_b16 v[102:103], v161 offset:59392\n\t" \
  "v_mfma_f32_16x16x32_bf16 v[140:143], v[108:111], v[0:3], v[140:143]\n\t" \
  "s_mov_b64 exec, vcc\n\t" \
  "buffer_load_dwordx4 v[88:91], v164, %[ob], s94 offen offset:2048 sc1\n\t" \
  "s_mov_b64 exec, -1\n\t" \
  "v_mfma_f32_16x16x32_bf16 v[156:159], v[108:111], v[4:7], v[156:159]\n\t" \
  "ds_read_b64_tr_b16 v[104:105], v162 offset:57344\n\t" \
  "ds_read_b64_tr_b16 v[106:107], v162 offset:59392\n\t" \
  "s_waitcnt lgkmcnt(10)\n\t" \
  "v_mfma_f32_16x16x32_bf16 v[128:131], v[112:115], v[8:11], v[128:131]\n\t" \
  "s_mov_b64 exec, vcc\n\t" \
  "buffer_load_dwordx4 v[92:95], v164, %[ob], s94 offen offset:3072 sc1\n\t" \
  "s_mov_b64 exec, -1\n\t" \
  "v_mfma_f32_16x16x32_bf16 v[144:147], v[112:115], v[12:15], v[144:147]\n\t" \
  "ds_read_b64_tr_b16 v[108:109], v163 offset:57344\n\t" \
  "ds_read_b64_tr_b16 v[110:111], v163 offset:59392\n\t" \
  "v_mfma_f32_16x16x32_bf16 v[132:135], v[116:119], v[8:11], v[132:135]\n\t" \
  "ds_read_b64_tr_b16 v[112:113], v160 offset:61440\n\t" \
  "ds_read_b32 v166, v165\n\t" \
  "v_mfma_f32_16x16x32_bf16 v[148:151], v[116:119], v[12:15], v[148:151]\n\t" \
  "ds_read_b64_tr_b16 v[114:115], v160 offset:63488\n\t" \
  "s_waitcnt lgkmcnt(11)\n\t" \
  "v_mfma_f32_16x16x32_bf16 v[136:139], v[120:123], v[8:11], v[136:139]\n\t" \
  "ds_read_b64_tr_b16 v[116:117], v161 offset:61440\n\t" \
  "v_mfma_f32_16x16x32_bf16 v[152:155], v[120:123], v[12:15], v[152:155]\n\t" \
  "ds_read_b64_tr_b16 v[118:119], v161 offset:63488\n\t" \
  "s_sub_u32 %[iss], %[iss], vcc_lo\n\t" \
  "v_mfma_f32_16x16x32_bf16 v[140:143], v[124:127], v[8:11], v[140:143]\n\t" \
  "ds_read_b64_tr_b16 v[120:121], v162 offset:61440\n\t" \
  "s_mov_b64 exec, 1\n\t" \
  "ds_write_b32 v165, v165 offset:76\n\t" \
  "s_mov_b64 exec, -1\n\t" \
  "v_mfma_f32_16x16x32_bf16 v[156:159], v[124:127], v[12:15], v[156:159]\n\t" \
  "ds_read_b64_tr_b16 v[122:123], v162 offset:63488\n\t" \
  "s_add_u32 %[th], %[th], 1\n\t" \
  "s_cbranch_vccz QF7_%=\n\t" \
  "s_waitcnt vmcnt(16)\n\t" \
  "QG7_%=:\n\t" \
  "s_waitcnt lgkmcnt(12)\n\t" \
  "v_mfma_f32_16x16x32_bf16 v[128:131], v[96:99], v[16:19], v[128:131]\n\t" \
  "ds_read_b64_tr_b16 v[124:125], v163 offset:61440\n\t" \
  "v_readlane_b32 s94, v167, 12\n\t" \
  "v_mfma_f32_16x16x32_bf16 v[144:147], v[96:99], v[20:23], v[144:147]\n\t" \
  "ds_read_b64_tr_b16 v[126:127], v163 offset:63488\n\t" \
  "s_waitcnt lgkmcnt(8)\n\t" \
  "v_cmp_lt_u32 vcc, %[th], v166\n\t" \
  "v_mfma_f32_16x16x32_bf16 v[132:135], v[100:103], v[16:19], v[132:135]\n\t" \
  "v_add_u32 v160, 0x10000, v160\n\t" \
  "s_mov_b64 exec, vcc\n\t" \
  "buffer_load_dwordx4 v[0:3], v164, %[ob], s94 offen offset:0 sc1\n\t" \
  "s_mov_b64 exec, -1\n\t" \
  "v_mfma_f32_16x16x32_bf16 v[148:151], v[100:103], v[20:23], v[148:151]\n\t" \
  "ds_read_b64_tr_b16 v[96:97], v160 offset:0\n\t" \
  "ds_read_b64_tr_b16 v[98:99], v160 offset:2048\n\t" \
  "v_mfma_f32_16x16x32_bf16 v[136:139], v[104:107], v[16:19], v[136:139]\n\t" \
  "v_add_u32 v161, 0x10000, v161\n\t" \
  "s_mov_b64 exec, vcc\n\t" \
  "buffer_load_dwordx4 v[4:7], v164, %[ob], s94 offen offset:1024 sc1\n\t" \
  "s_mov_b64 exec, -1\n\t" \
  "v_mfma_f32_16x16x32_bf16 v[152:155], v[104:107], v[20:23], v[152:155]\n\t" \
  "ds_read_b64_tr_b16 v[100:101], v161 offset:0\n\t" \
  "ds_read_b64_tr_b16 v[102:103], v161 offset:2048\n\t" \
  "v_mfma_f32_16x16x32_bf16 v[140:143], v[108:111], v[16:19], v[140:143]\n\t" \
  "v_add_u32 v162, 0x10000, v162\n\t" \
  "s_mov_b64 exec, vcc\n\t" \
  "buffer_load_dwordx4 v[8:11], v164, %[ob], s94 offen offset:2048 sc1\n\t" \
  "s_mov_b64 exec, -1\n\t" \
  "v_mfma_f32_16x16x32_bf16 v[156:159], v[108:111], v[20:23], v[156:159]\n\t" \
  "ds_read_b64_tr_b16 v[104:105], v162 offset:0\n\t" \
  "ds_read_b64_tr_b16 v[106:107], v162 offset:2048\n\t" \
  "s_waitcnt lgkmcnt(11)\n\t" \
  "v_mfma_f32_16x16x32_bf16 v[128:131], v[112:115], v[24:27], v[128:131]\n\t" \
  "v_add_u32 v163, 0x10000, v163\n\t" \
  "s_mov_b64 exec, vcc\n\t" \
  "buffer_load_dwordx4 v[12:15], v164, %[ob], s94 offen offset:3072 sc1\n\t" \
  "s_mov_b64 exec, -1\n\t" \
  "v_mfma_f32_16x16x32_bf16 v[144:147], v[112:115], v[28:31], v[144:147]\n\t" \
  "ds_read_b64_tr_b16 v[108:109], v163 offset:0\n\t" \
  "ds_read_b64_tr_b16 v[110:111], v163 offset:2048\n\t" \
  "v_mfma_f32_16x16x32_bf16 v[132:135], v[116:119], v[24:27], v[132:135]\n\t" \
  "ds_read_b64_tr_b16 v[112:113], v160 offset:4096\n\t" \
  "ds_read_b32 v166, v165\n\t" \
  "v_mfma_f32_16x16x32_bf16 v[148:151], v[116:119], v[28:31], v[148:151]\n\t" \
  "ds_read_b64_tr_b16 v[114:115], v160 offset:6144\n\t" \
  "s_waitcnt lgkmcnt(11)\n\t" \
  "v_mfma_f32_16x16x32_bf16 v[136:139], v[120:123], v[24:27], v[136:139]\n\t" \
  "ds_read_b64_tr_b16 v[116:117], v161 offset:4096\n\t" \
  "v_mfma_f32_16x16x32_bf16 v[152:155], v[120:123], v[28:31], v[152:155]\n\t" \
  "ds_read_b64_tr_b16 v[118:119], v161 offset:6144\n\t" \
  "s_sub_u32 %[iss], %[iss], vcc_lo\n\t" \
  "v_mfma_f32_16x16x32_bf16 v[140:143], v[124:127], v[24:27], v[140:143]\n\t" \
  "ds_read_b64_tr_b16 v[120:121], v162 offset:4096\n\t" \
  "v_mfma_f32_16x16x32_bf16 v[156:159], v[124:127], v[28:31], v[156:159]\n\t" \
  "ds_read_b64_tr_b16 v[122:123], v162 offset:6144\n\t" \
  "s_add_u32 %[th], %[th], 1\n\t" \
  "s_cbranch_vccz QF8_%=\n\t" \
  "s_waitcnt vmcnt(16)\n\t" \
  "QG8_%=:\n\t" \
  "s_waitcnt lgkmcnt(11)\n\t" \
  "v_mfma_f32_16x16x32_bf16 v[128:131], v[96:99], v[32:35], v[128:131]\n\t" \
  "ds_read_b64_tr_b16 v[124:125], v163 offset:4096\n\t" \
  "v_readlane_b32 s94, v167, 13\n\t" \
  "v_mfma_f32_16x16x32_bf16 v[144:147], v[96:99], v[36:39], v[144:147]\n\t" \
  "ds_read_b64_tr_b16 v[126:127], v163 offset:6144\n\t" \
  "s_waitcnt lgkmcnt(7)\n\t" \
  "v_cmp_lt_u32 vcc, %[th], v166\n\t" \
  "v_mfma_f32_16x16x32_bf16 v[132:135], v[100:103], v[32:35], v[132:135]\n\t" \
  "s_mov_b64 exec, vcc\n\t" \
  "buffer_load_dwordx4 v[16:19], v164, %[ob], s94 offen offset:0 sc1\n\t" \
  "s_mov_b64 exec, -1\n\t" \
  "v_mfma_f32_16x16x32_bf16 v[148:151], v[100:103], v[36:39], v[148:151]\n\t" \
  "ds_read_b64_tr_b16 v[96:97], v160 offset:8192\n\t" \
  "ds_read_b64_tr_b16 v[98:99], v160 offset:10240\n\t" \
  "v_mfma_f32_16x16x32_bf16 v[136:139], v[104:107], v[32:35], v[136:139]\n\t" \
  "s_mov_b64 exec, vcc\n\t" \
  "buffer_load_dwordx4 v[20:23], v164, %[ob], s94 offen offset:1024 sc1\n\t" \
  "s_mov_b64 exec, -1\n\t" \
  "v_mfma_f32_16x16x32_bf16 v[152:155], v[104:107], v[36:39], v[152:155]\n\t" \
  "ds_read_b64_tr_b16 v[100:101], v161 offset:8192\n\t" \
  "ds_read_b64_tr_b16 v[102:103], v161 offset:10240\n\t" \
  "v_mfma_f32_16x16x32_bf16 v[140:143], v[108:111], v[32:35], v[140:143]\n\t" \
  "s_mov_b64 exec, vcc\n\t" \
  "buffer_load_dwordx4 v[24:27], v164, %[ob], s94 offen offset:2048 sc1\n\t" \
  "s_mov_b64 exec, -1\n\t" \
  "v_mfma_f32_16x16x32_bf16 v[156:159], v[108:111], v[36:39], v[156:159]\n\t" \
  "ds_read_b64_tr_b16 v[104:105], v162 offset:8192\n\t" \
  "ds_read_b64_tr_b16 v[106:107], v162 offset:10240\n\t" \
  "s_waitcnt lgkmcnt(10)\n\t" \
  "v_mfma_f32_16x16x32_bf16 v[128:131], v[112:115], v[40:43], v[128:131]\n\t" \
  "s_mov_b64 exec, vcc\n\t" \
  "buffer_load_dwordx4 v[28:31], v164, %[ob], s94 offen offset:3072 sc1\n\t" \
  "s_mov_b64 exec, -1\n\t" \
  "v_mfma_f32_16x16x32_bf16 v[144:147], v[112:115], v[44:47], v[144:147]\n\t" \
  "ds_read_b64_tr_b16 v[108:109], v163 offset:8192\n\t" \
  "ds_read_b64_tr_b16 v[110:111], v163 offset:10240\n\t" \
  "v_mfma_f32_16x16x32_bf16 v[132:135], v[116:119], v[40:43], v[132:135]\n\t" \
  "ds_read_b64_tr_b16 v[112:113], v160 offset:12288\n\t" \
  "ds_read_b32 v166, v165\n\t" \
  "v_mfma_f32_16x16x32_bf16 v[148:151], v[116:119], v[44:47], v[148:151]\n\t" \
  "ds_read_b64_tr_b16 v[114:115], v160 offset:14336\n\t" \
  "s_waitcnt lgkmcnt(11)\n\t" \
  "v_mfma_f32_16x16x32_bf16 v[136:139], v[120:123], v[40:43], v[136:139]\n\t" \
  "ds_read_b64_tr_b16 v[116:117], v161 offset:12288\n\t" \
  "v_mfma_f32_16x16x32_bf16 v[152:155], v[120:123], v[44:47], v[152:155]\n\t" \
  "ds_read_b64_tr_b16 v[118:119], v161 offset:14336\n\t" \
  "s_sub_u32 %[iss], %[iss], vcc_lo\n\t" \
  "v_mfma_f32_16x16x32_bf16 v[140:143], v[124:127], v[40:43], v[140:143]\n\t" \
  "ds_read_b64_tr_b16 v[120:121], v162 offset:12288\n\t" \
  "v_mfma_f32_16x16x32_bf16 v[156:159], v[124:127], v[44:47], v[156:159]\n\t" \
  "ds_read_b64_tr_b16 v[122:123], v162 offset:14336\n\t" \
  "s_add_u32 %[th], %[th], 1\n\t" \
  "s_cbranch_vccz QF9_%=\n\t" \
  "s_waitcnt vmcnt(16)\n\t" \
  "QG9_%=:\n\t" \
  "s_waitcnt lgkmcnt(11)\n\t" \
  "v_mfma_f32_16x16x32_bf16 v[128:131], v[96:99], v[48:51], v[128:131]\n\t" \
  "ds_read_b64_tr_b16 v[124:125], v163 offset:12288\n\t" \
  "v_readlane_b32 s94, v167, 14\n\t" \
  "v_mfma_f32_16x16x32_bf16 v[144:147], v[96:99], v[52:55], v[144:147]\n\t" \
  "ds_read_b64_tr_b16 v[126:127], v163 offset:14336\n\t" \
  "s_waitcnt lgkmcnt(7)\n\t" \
  "v_cmp_lt_u32 vcc, %[th], v166\n\t" \
  "v_mfma_f32_16x16x32_bf16 v[132:135], v[100:103], v[48:51], v[132:135]\n\t" \
  "s_mov_b64 exec, vcc\n\t" \
  "buffer_load_dwordx4 v[32:35], v164, %[ob], s94 offen offset:0 sc1\n\t" \
  "s_mov_b64 exec, -1\n\t" \
  "v_mfma_f32_16x16x32_bf16 v[148:151], v[100:103], v[52:55], v[148:151]\n\t" \
  "ds_read_b64_tr_b16 v[96:97], v160 offset:16384\n\t" \
  "ds_read_b64_tr_b16 v[98:99], v160 offset:18432\n\t" \
  "v_mfma_f32_16x16x32_bf16 v[136:139], v[104:107], v[48:51], v[136:139]\n\t" \
  "s_mov_b64 exec, vcc\n\t" \
  "buffer_load_dwordx4 v[36:39], v164, %[ob], s94 offen offset:1024 sc1\n\t" \
  "s_mov_b64 exec, -1\n\t" \
  "v_mfma_f32_16x16x32_bf16 v[152:155], v[104:107], v[52:55], v[152:155]\n\t" \
  "ds_read_b64_tr_b16 v[100:101], v161 offset:16384\n\t" \
  "ds_read_b64_tr_b16 v[102:103], v161 offset:18432\n\t" \
  "v_mfma_f32_16x16x32_bf16 v[140:143], v[108:111], v[48:51], v[140:143]\n\t" \
  "s_mov_b64 exec, vcc\n\t" \
  "buffer_load_dwordx4 v[40:43], v164, %[ob], s94 offen offset:2048 sc1\n\t" \
  "s_mov_b64 exec, -1\n\t" \
  "v_mfma_f32_16x16x32_bf16 v[156:159], v[108:111], v[52:55], v[156:159]\n\t" \
  "ds_read_b64_tr_b16 v[104:105], v162 offset:16384\n\t" \
  "ds_read_b64_tr_b16 v[106:107], v162 offset:18432\n\t" \
  "s_waitcnt lgkmcnt(10)\n\t" \
  "v_mfma_f32_16x16x32_bf16 v[128:131], v[112:115], v[56:59], v[128:131]\n\t" \
  "s_mov_b64 exec, vcc\n\t" \
  "buffer_load_dwordx4 v[44:47], v164, %[ob], s94 offen offset:3072 sc1\n\t" \
  "s_mov_b64 exec, -1\n\t" \
  "v_mfma_f32_16x16x32_bf16 v[144:147], v[112:115], v[60:63], v[144:147]\n\t" \
  "ds_read_b64_tr_b16 v[108:109], v163 offset:16384\n\t" \
  "ds_read_b64_tr_b16 v[110:111], v163 offset:18432\n\t" \
  "v_mfma_f32_16x16x32_bf16 v[132:135], v[116:119], v[56:59], v[132:135]\n\t" \
  "ds_read_b64_tr_b16 v[112:113], v160 offset:20480\n\t" \
  "ds_read_b32 v166, v165\n\t" \
  "v_mfma_f32_16x16x32_bf16 v[148:151], v[116:119], v[60:63], v[148:151]\n\t" \
  "ds_read_b64_tr_b16 v[114:115], v160 offset:22528\n\t" \
  "s_waitcnt lgkmcnt(11)\n\t" \
  "v_mfma_f32_16x16x32_bf16 v[136:139], v[120:123], v[56:59], v[136:139]\n\t" \
  "ds_read_b64_tr_b16 v[116:117], v161 offset:20480\n\t" \
  "v_mfma_f32_16x16x32_bf16 v[152:155], v[120:123], v[60:63], v[152:155]\n\t" \
  "ds_read_b64_tr_b16 v[118:119], v161 offset:22528\n\t" \
  "s_sub_u32 %[iss], %[iss], vcc_lo\n\t" \
  "v_mfma_f32_16x16x32_bf16 v[140:143], v[124:127], v[56:59], v[140:143]\n\t" \
  "ds_read_b64_tr_b16 v[120:121], v162 offset:20480\n\t" \
  "v_mfma_f32_16x16x32_bf16 v[156:159], v[124:127], v[60:63], v[156:159]\n\t" \
  "ds_read_b64_tr_b16 v[122:123], v162 offset:22528\n\t" \
  "s_add_u32 %[th], %[th], 1\n\t" \
  "s_cbranch_vccz QF10_%=\n\t" \
  "s_waitcnt vmcnt(16)\n\t" \
  "QG10_%=:\n\t" \
  "s_waitcnt lgkmcnt(11)\n\t" \
  "v_mfma_f32_16x16x32_bf16 v[128:131], v[96:99], v[64:67], v[128:131]\n\t" \
  "ds_read_b64_tr_b16 v[124:125], v163 offset:20480\n\t" \
  "v_readlane_b32 s94, v167, 15\n\t" \
  "v_mfma_f32_16x16x32_bf16 v[144:147], v[96:99], v[68:71], v[144:147]\n\t" \
  "ds_read_b64_tr_b16 v[126:127], v163 offset:22528\n\t" \
  "s_waitcnt lgkmcnt(7)\n\t" \
  "v_cmp_lt_u32 vcc, %[th], v166\n\t" \
  "v_mfma_f32_16x16x32_bf16 v[132:135], v[100:103], v[64:67], v[132:135]\n\t" \
  "s_mov_b64 exec, vcc\n\t" \
  "buffer_load_dwordx4 v[48:51], v164, %[ob], s94 offen offset:0 sc1\n\t" \
  "s_mov_b64 exec, -1\n\t" \
  "v_mfma_f32_16x16x32_bf16 v[148:151], v[100:103], v[68:71], v[148:151]\n\t" \
  "ds_read_b64_tr_b16 v[96:97], v160 offset:24576\n\t" \
  "ds_read_b64_tr_b16 v[98:99], v160 offset:26624\n\t" \
  "v_mfma_f32_16x16x32_bf16 v[136:139], v[104:107], v[64:67], v[136:139]\n\t" \
  "s_mov_b64 exec, vcc\n\t" \
  "buffer_load_dwordx4 v[52:55], v164, %[ob], s94 offen offset:1024 sc1\n\t" \
  "s_mov_b64 exec, -1\n\t" \
  "v_mfma_f32_16x16x32_bf16 v[152:155], v[104:107], v[68:71], v[152:155]\n\t" \
  "ds_read_b64_tr_b16 v[100:101], v161 offset:24576\n\t" \
  "ds_read_b64_tr_b16 v[102:103], v161 offset:26624\n\t" \
  "v_mfma_f32_16x16x32_bf16 v[140:143], v[108:111], v[64:67], v[140:143]\n\t" \
  "s_mov_b64 exec, vcc\n\t" \
  "buffer_load_dwordx4 v[56:59], v164, %[ob], s94 offen offset:2048 sc1\n\t" \
  "s_mov_b64 exec, -1\n\t" \
  "v_mfma_f32_16x16x32_bf16 v[156:159], v[108:111], v[68:71], v[156:159]\n\t" \
  "ds_read_b64_tr_b16 v[104:105], v162 offset:24576\n\t" \
  "ds_read_b64_tr_b16 v[106:107], v162 offset:26624\n\t" \
  "s_waitcnt lgkmcnt(10)\n\t" \
  "v_mfma_f32_16x16x32_bf16 v[128:131], v[112:115], v[72:75], v[128:131]\n\t" \
  "s_mov_b64 exec, vcc\n\t" \
  "buffer_load_dwordx4 v[60:63], v164, %[ob], s94 offen offset:3072 sc1\n\t" \
  "s_mov_b64 exec, -1\n\t" \
  "v_mfma_f32_16x16x32_bf16 v[144:147], v[112:115], v[76:79], v[144:147]\n\t" \
  "ds_read_b64_tr_b16 v[108:109], v163 offset:24576\n\t" \
  "ds_read_b64_tr_b16 v[110:111], v163 offset:26624\n\t" \
  "v_mfma_f32_16x16x32_bf16 v[132:135], v[116:119], v[72:75], v[132:135]\n\t" \
  "ds_read_b64_tr_b16 v[112:113], v160 offset:28672\n\t" \
  "v_mfma_f32_16x16x32_bf16 v[148:151], v[116:119], v[76:79], v[148:151]\n\t" \
  "ds_read_b64_tr_b16 v[114:115], v160 offset:30720\n\t" \
  "s_waitcnt lgkmcnt(10)\n\t" \
  "v_mfma_f32_16x16x32_bf16 v[136:139], v[120:123], v[72:75], v[136:139]\n\t" \
  "ds_read_b64_tr_b16 v[116:117], v161 offset:28672\n\t" \
  "v_mfma_f32_16x16x32_bf16 v[152:155], v[120:123], v[76:79], v[152:155]\n\t" \
  "ds_read_b64_tr_b16 v[118:119], v161 offset:30720\n\t" \
  "s_sub_u32 %[iss], %[iss], vcc_lo\n\t" \
  "v_mfma_f32_16x16x32_bf16 v[140:143], v[124:127], v[72:75], v[140:143]\n\t" \
  "ds_read_b64_tr_b16 v[120:121], v162 offset:28672\n\t" \
  "v_mfma_f32_16x16x32_bf16 v[156:159], v[124:127], v[76:79], v[156:159]\n\t" \
  "ds_read_b64_tr_b16 v[122:123], v162 offset:30720\n\t" \
  "s_add_u32 %[th], %[th], 1\n\t" \
  "s_cbranch_vccz QF11_%=\n\t" \
  "s_waitcnt vmcnt(16)\n\t" \
  "QG11_%=:\n\t" \
  "s_waitcnt lgkmcnt(10)\n\t" \
  "v_mfma_f32_16x16x32_bf16 v[128:131], v[96:99], v[80:83], v[128:131]\n\t" \
  "ds_read_b64_tr_b16 v[124:125], v163 offset:28672\n\t" \
  "v_mfma_f32_16x16x32_bf16 v[144:147], v[96:99], v[84:87], v[144:147]\n\t" \
  "ds_read_b64_tr_b16 v[126:127], v163 offset:30720\n\t" \
  "v_mfma_f32_16x16x32_bf16 v[132:135], v[100:103], v[80:83], v[132:135]\n\t" \
  "ds_read_b64_tr_b16 v[96:97], v160 offset:32768\n\t" \
  "v_mfma_f32_16x16x32_bf16 v[148:151], v[100:103], v[84:87], v[148:151]\n\t" \
  "ds_read_b64_tr_b16 v[98:99], v160 offset:34816\n\t" \
  "s_waitcnt lgkmcnt(10)\n\t" \
  "v_mfma_f32_16x16x32_bf16 v[136:139], v[104:107], v[80:83], v[136:139]\n\t" \
  "ds_read_b64_tr_b16 v[100:101], v161 offset:32768\n\t" \
  "v_mfma_f32_16x16x32_bf16 v[152:155], v[104:107], v[84:87], v[152:155]\n\t" \
  "ds_read_b64_tr_b16 v[102:103], v161 offset:34816\n\t" \
  "v_mfma_f32_16x16x32_bf16 v[140:143], v[108:111], v[80:83], v[140:143]\n\t" \
  "ds_read_b64_tr_b16 v[104:105], v162 offset:32768\n\t" \
  "v_mfma_f32_16x16x32_bf16 v[156:159], v[108:111], v[84:87], v[156:159]\n\t" \
  "ds_read_b64_tr_b16 v[106:107], v162 offset:34816\n\t" \
  "s_waitcnt lgkmcnt(10)\n\t" \
  "v_mfma_f32_16x16x32_bf16 v[128:131], v[112:115], v[88:91], v[128:131]\n\t" \
  "ds_read_b64_tr_b16 v[108:109], v163 offset:32768\n\t" \
  "v_mfma_f32_16x16x32_bf16 v[144:147], v[112:115], v[92:95], v[144:147]\n\t" \
  "ds_read_b64_tr_b16 v[110:111], v163 offset:34816\n\t" \
  "v_mfma_f32_16x16x32_bf16 v[132:135], v[116:119], v[88:91], v[132:135]\n\t" \
  "ds_read_b64_tr_b16 v[112:113], v160 offset:36864\n\t" \
  "v_mfma_f32_16x16x32_bf16 v[148:151], v[116:119], v[92:95], v[148:151]\n\t" \
  "ds_read_b64_tr_b16 v[114:115], v160 offset:38912\n\t" \
  "s_waitcnt lgkmcnt(10)\n\t" \
  "v_mfma_f32_16x16x32_bf16 v[136:139], v[120:123], v[88:91], v[136:139]\n\t" \
  "ds_read_b64_tr_b16 v[116:117], v161 offset:36864\n\t" \
  "v_mfma_f32_16x16x32_bf16 v[152:155], v[120:123], v[92:95], v[152:155]\n\t" \
  "ds_read_b64_tr_b16 v[118:119], v161 offset:38912\n\t" \
  "v_mfma_f32_16x16x32_bf16 v[140:143], v[124:127], v[88:91], v[140:143]\n\t" \
  "ds_read_b64_tr_b16 v[120:121], v162 offset:36864\n\t" \
  "v_mfma_f32_16x16x32_bf16 v[156:159], v[124:127], v[92:95], v[156:159]\n\t" \
  "ds_read_b64_tr_b16 v[122:123], v162 offset:38912\n\t" \
  "s_cbranch_vccz QF12_%=\n\t" \
  "s_waitcnt vmcnt(12)\n\t" \
  "QG12_%=:\n\t" \
  "s_waitcnt lgkmcnt(10)\n\t" \
  "v_mfma_f32_16x16x32_bf16 v[128:131], v[96:99], v[0:3], v[128:131]\n\t" \
  "ds_read_b64_tr_b16 v[124:125], v163 offset:36864\n\t" \
  "v_mfma_f32_16x16x32_bf16 v[144:147], v[96:99], v[4:7], v[144:147]\n\t" \
  "ds_read_b64_tr_b16 v[126:127], v163 offset:38912\n\t" \
  "v_mfma_f32_16x16x32_bf16 v[132:135], v[100:103], v[0:3], v[132:135]\n\t" \
  "ds_read_b64_tr_b16 v[96:97], v160 offset:40960\n\t" \
  "v_mfma_f32_16x16x32_bf16 v[148:151], v[100:103], v[4:7], v[148:151]\n\t" \
  "ds_read_b64_tr_b16 v[98:99], v160 offset:43008\n\t" \
  "s_waitcnt lgkmcnt(10)\n\t" \
  "v_mfma_f32_16x16x32_bf16 v[136:139], v[104:107], v[0:3], v[136:139]\n\t" \
  "ds_read_b64_tr_b16 v[100:101], v161 offset:40960\n\t" \
  "v_mfma_f32_16x16x32_bf16 v[152:155], v[104:107], v[4:7], v[152:155]\n\t" \
  "ds_read_b64_tr_b16 v[102:103], v161 offset:43008\n\t" \
  "v_mfma_f32_16x16x32_bf16 v[140:143], v[108:111], v[0:3], v[140:143]\n\t" \
  "ds_read_b64_tr_b16 v[104:105], v162 offset:40960\n\t" \
  "v_mfma_f32_16x16x32_bf16 v[156:159], v[108:111], v[4:7], v[156:159]\n\t" \
  "ds_read_b64_tr_b16 v[106:107], v162 offset:43008\n\t" \
  "s_waitcnt lgkmcnt(10)\n\t" \
  "v_mfma_f32_16x16x32_bf16 v[128:131], v[112:115], v[8:11], v[128:131]\n\t" \
  "ds_read_b64_tr_b16 v[108:109], v163 offset:40960\n\t" \
  "v_mfma_f32_16x16x32_bf16 v[144:147], v[112:115], v[12:15], v[144:147]\n\t" \
  "ds_read_b64_tr_b16 v[110:111], v163 offset:43008\n\t" \
  "v_mfma_f32_16x16x32_bf16 v[132:135], v[116:119], v[8:11], v[132:135]\n\t" \
  "ds_read_b64_tr_b16 v[112:113], v160 offset:45056\n\t" \
  "v_mfma_f32_16x16x32_bf16 v[148:151], v[116:119], v[12:15], v[148:151]\n\t" \
  "ds_read_b64_tr_b16 v[114:115], v160 offset:47104\n\t" \
  "s_waitcnt lgkmcnt(10)\n\t" \
  "v_mfma_f32_16x16x32_bf16 v[136:139], v[120:123], v[8:11], v[136:139]\n\t" \
  "ds_read_b64_tr_b16 v[116:117], v161 offset:45056\n\t" \
  "v_mfma_f32_16x16x32_bf16 v[152:155], v[120:123], v[12:15], v[152:155]\n\t" \
  "ds_read_b64_tr_b16 v[118:119], v161 offset:47104\n\t" \
  "v_mfma_f32_16x16x32_bf16 v[140:143], v[124:127], v[8:11], v[140:143]\n\t" \
  "ds_read_b64_tr_b16 v[120:121], v162 offset:45056\n\t" \
  "v_mfma_f32_16x16x32_bf16 v[156:159], v[124:127], v[12:15], v[156:159]\n\t" \
  "ds_read_b64_tr_b16 v[122:123], v162 offset:47104\n\t" \
  "s_cbranch_vccz QF13_%=\n\t" \
  "s_waitcnt vmcnt(8)\n\t" \
  "QG13_%=:\n\t" \
  "s_waitcnt lgkmcnt(10)\n\t" \
  "v_mfma_f32_16x16x32_bf16 v[128:131], v[96:99], v[16:19], v[128:131]\n\t" \
  "ds_read_b64_tr_b16 v[124:125], v163 offset:45056\n\t" \
  "v_mfma_f32_16x16x32_bf16 v[144:147], v[96:99], v[20:23], v[144:147]\n\t" \
  "ds_read_b64_tr_b16 v[126:127], v163 offset:47104\n\t" \
  "v_mfma_f32_16x16x32_bf16 v[132:135], v[100:103], v[16:19], v[132:135]\n\t" \
  "ds_read_b64_tr_b16 v[96:97], v160 offset:49152\n\t" \
  "v_mfma_f32_16x16x32_bf16 v[148:151], v[100:103], v[20:23], v[148:151]\n\t" \
  "ds_read_b64_tr_b16 v[98:99], v160 offset:51200\n\t" \
  "s_waitcnt lgkmcnt(10)\n\t" \
  "v_mfma_f32_16x16x32_bf16 v[136:139], v[104:107], v[16:19], v[136:139]\n\t" \
  "ds_read_b64_tr_b16 v[100:101], v161 offset:49152\n\t" \
  "v_mfma_f32_16x16x32_bf16 v[152:155], v[104:107], v[20:23], v[152:155]\n\t" \
  "ds_read_b64_tr_b16 v[102:103], v161 offset:51200\n\t" \
  "v_mfma_f32_16x16x32_bf16 v[140:143], v[108:111], v[16:19], v[140:143]\n\t" \
  "ds_read_b64_tr_b16 v[104:105], v162 offset:49152\n\t" \
  "v_mfma_f32_16x16x32_bf16 v[156:159], v[108:111], v[20:23], v[156:159]\n\t" \
  "ds_read_b64_tr_b16 v[106:107], v162 offset:51200\n\t" \
  "s_waitcnt lgkmcnt(10)\n\t" \
  "v_mfma_f32_16x16x32_bf16 v[128:131], v[112:115], v[24:27], v[128:131]\n\t" \
  "ds_read_b64_tr_b16 v[108:109], v163 offset:49152\n\t" \
  "v_mfma_f32_16x16x32_bf16 v[144:147], v[112:115], v[28:31], v[144:147]\n\t" \
  "ds_read_b64_tr_b16 v[110:111], v163 offset:51200\n\t" \
  "v_mfma_f32_16x16x32_bf16 v[132:135], v[116:119], v[24:27], v[132:135]\n\t" \
  "ds_read_b64_tr_b16 v[112:113], v160 offset:53248\n\t" \
  "v_mfma_f32_16x16x32_bf16 v[148:151], v[116:119], v[28:31], v[148:151]\n\t" \
  "ds_read_b64_tr_b16 v[114:115], v160 offset:55296\n\t" \
  "s_waitcnt lgkmcnt(10)\n\t" \
  "v_mfma_f32_16x16x32_bf16 v[136:139], v[120:123], v[24:27], v[136:139]\n\t" \
  "ds_read_b64_tr_b16 v[116:117], v161 offset:53248\n\t" \
  "v_mfma_f32_16x16x32_bf16 v[152:155], v[120:123], v[28:31], v[152:155]\n\t" \
  "ds_read_b64_tr_b16 v[118:119], v161 offset:55296\n\t" \
  "v_mfma_f32_16x16x32_bf16 v[140:143], v[124:127], v[24:27], v[140:143]\n\t" \
  "ds_read_b64_tr_b16 v[120:121], v162 offset:53248\n\t" \
  "v_mfma_f32_16x16x32_bf16 v[156:159], v[124:127], v[28:31], v[156:159]\n\t" \
  "ds_read_b64_tr_b16 v[122:123], v162 offset:55296\n\t" \
  "s_cbranch_vccz QF14_%=\n\t" \
  "s_waitcnt vmcnt(4)\n\t" \
  "QG14_%=:\n\t" \
  "s_waitcnt lgkmcnt(10)\n\t" \
  "v_mfma_f32_16x16x32_bf16 v[128:131], v[96:99], v[32:35], v[128:131]\n\t" \
  "ds_read_b64_tr_b16 v[124:125], v163 offset:53248\n\t" \
  "v_mfma_f32_16x16x32_bf16 v[144:147], v[96:99], v[36:39], v[144:147]\n\t" \
  "ds_read_b64_tr_b16 v[126:127], v163 offset:55296\n\t" \
  "v_mfma_f32_16x16x32_bf16 v[132:135], v[100:103], v[32:35], v[132:135]\n\t" \
  "ds_read_b64_tr_b16 v[96:97], v160 offset:57344\n\t" \
  "v_mfma_f32_16x16x32_bf16 v[148:151], v[100:103], v[36:39], v[148:151]\n\t" \
  "ds_read_b64_tr_b16 v[98:99], v160 offset:59392\n\t" \
  "s_waitcnt lgkmcnt(10)\n\t" \
  "v_mfma_f32_16x16x32_bf16 v[136:139], v[104:107], v[32:35], v[136:139]\n\t" \
  "ds_read_b64_tr_b16 v[100:101], v161 offset:57344\n\t" \
  "v_mfma_f32_16x16x32_bf16 v[152:155], v[104:107], v[36:39], v[152:155]\n\t" \
  "ds_read_b64_tr_b16 v[102:103], v161 offset:59392\n\t" \
  "v_mfma_f32_16x16x32_bf16 v[140:143], v[108:111], v[32:35], v[140:143]\n\t" \
  "ds_read_b64_tr_b16 v[104:105], v162 offset:57344\n\t" \
  "v_mfma_f32_16x16x32_bf16 v[156:159], v[108:111], v[36:39], v[156:159]\n\t" \
  "ds_read_b64_tr_b16 v[106:107], v162 offset:59392\n\t" \
  "s_waitcnt lgkmcnt(10)\n\t" \
  "v_mfma_f32_16x16x32_bf16 v[128:131], v[112:115], v[40:43], v[128:131]\n\t" \
  "ds_read_b64_tr_b16 v[108:109], v163 offset:57344\n\t" \
  "v_mfma_f32_16x16x32_bf16 v[144:147], v[112:115], v[44:47], v[144:147]\n\t" \
  "ds_read_b64_tr_b16 v[110:111], v163 offset:59392\n\t" \
  "v_mfma_f32_16x16x32_bf16 v[132:135], v[116:119], v[40:43], v[132:135]\n\t" \
  "ds_read_b64_tr_b16 v[112:113], v160 offset:61440\n\t" \
  "v_mfma_f32_16x16x32_bf16 v[148:151], v[116:119], v[44:47], v[148:151]\n\t" \
  "ds_read_b64_tr_b16 v[114:115], v160 offset:63488\n\t" \
  "s_waitcnt lgkmcnt(10)\n\t" \
  "v_mfma_f32_16x16x32_bf16 v[136:139], v[120:123], v[40:43], v[136:139]\n\t" \
  "ds_read_b64_tr_b16 v[116:117], v161 offset:61440\n\t" \
  "v_mfma_f32_16x16x32_bf16 v[152:155], v[120:123], v[44:47], v[152:155]\n\t" \
  "ds_read_b64_tr_b16 v[118:119], v161 offset:63488\n\t" \
  "v_mfma_f32_16x16x32_bf16 v[140:143], v[124:127], v[40:43], v[140:143]\n\t" \
  "ds_read_b64_tr_b16 v[120:121], v162 offset:61440\n\t" \
  "v_mfma_f32_16x16x32_bf16 v[156:159], v[124:127], v[44:47], v[156:159]\n\t" \
  "ds_read_b64_tr_b16 v[122:123], v162 offset:63488\n\t" \
  "s_cbranch_vccz QF15_%=\n\t" \
  "s_waitcnt vmcnt(0)\n\t" \
  "QG15_%=:\n\t" \
  "s_waitcnt lgkmcnt(10)\n\t" \
  "v_mfma_f32_16x16x32_bf16 v[128:131], v[96:99], v[48:51], v[128:131]\n\t" \
  "ds_read_b64_tr_b16 v[124:125], v163 offset:61440\n\t" \
  "v_mfma_f32_16x16x32_bf16 v[144:147], v[96:99], v[52:55], v[144:147]\n\t" \
  "ds_read_b64_tr_b16 v[126:127], v163 offset:63488\n\t" \
  "v_mfma_f32_16x16x32_bf16 v[132:135], v[100:103], v[48:51], v[132:135]\n\t" \
  "v_mfma_f32_16x16x32_bf16 v[148:151], v[100:103], v[52:55], v[148:151]\n\t" \
  "s_waitcnt lgkmcnt(8)\n\t" \
  "v_mfma_f32_16x16x32_bf16 v[136:139], v[104:107], v[48:51], v[136:139]\n\t" \
  "v_mfma_f32_16x16x32_bf16 v[152:155], v[104:107], v[52:55], v[152:155]\n\t" \
  "v_mfma_f32_16x16x32_bf16 v[140:143], v[108:111], v[48:51], v[140:143]\n\t" \
  "v_mfma_f32_16x16x32_bf16 v[156:159], v[108:111], v[52:55], v[156:159]\n\t" \
  "s_waitcnt lgkmcnt(4)\n\t" \
  "v_mfma_f32_16x16x32_bf16 v[128:131], v[112:115], v[56:59], v[128:131]\n\t" \
  "v_mfma_f32_16x16x32_bf16 v[144:147], v[112:115], v[60:63], v[144:147]\n\t" \
  "v_mfma_f32_16x16x32_bf16 v[132:135], v[116:119], v[56:59], v[132:135]\n\t" \
  "v_mfma_f32_16x16x32_bf16 v[148:151], v[116:119], v[60:63], v[148:151]\n\t" \
  "s_waitcnt lgkmcnt(0)\n\t" \
  "v_mfma_f32_16x16x32_bf16 v[136:139], v[120:123], v[56:59], v[136:139]\n\t" \
  "v_mfma_f32_16x16x32_bf16 v[152:155], v[120:123], v[60:63], v[152:155]\n\t" \
  "v_mfma_f32_16x16x32_bf16 v[140:143], v[124:127], v[56:59], v[140:143]\n\t" \
  "v_mfma_f32_16x16x32_bf16 v[156:159], v[124:127], v[60:63], v[156:159]\n\t" \
  "s_branch QX_%=\n\t" \
  "QF1_%=:\n\t" \
  "s_mov_b32 %[c], 1\n\t" \
  "s_getpc_b64 s[92:93]\n\t" \
  "s_branch QC_%=\n\t" \
  "s_branch QG1_%=\n\t" \
  "QF2_%=:\n\t" \
  "s_mov_b32 %[c], 2\n\t" \
  "s_getpc_b64 s[92:93]\n\t" \
  "s_branch QC_%=\n\t" \
  "s_branch QG2_%=\n\t" \
  "QF3_%=:\n\t" \
  "s_mov_b32 %[c], 3\n\t" \
  "s_getpc_b64 s[92:93]\n\t" \
  "s_branch QC_%=\n\t" \
  "s_branch QG3_%=\n\t" \
  "QF4_%=:\n\t" \
  "s_mov_b32 %[c], 4\n\t" \
  "s_getpc_b64 s[92:93]\n\t" \
  "s_branch QC_%=\n\t" \
  "s_branch QG4_%=\n\t" \
  "QF5_%=:\n\t" \
  "s_mov_b32 %[c], 5\n\t" \
  "s_getpc_b64 s[92:93]\n\t" \
  "s_branch QC_%=\n\t" \
  "s_branch QG5_%=\n\t" \
  "QF6_%=:\n\t" \
  "s_mov_b32 %[c], 6\n\t" \
  "s_getpc_b64 s[92:93]\n\t" \
  "s_branch QC_%=\n\t" \
  "s_branch QG6_%=\n\t" \
  "QF7_%=:\n\t" \
  "s_mov_b32 %[c], 7\n\t" \
  "s_getpc_b64 s[92:93]\n\t" \
  "s_branch QC_%=\n\t" \
  "s_branch QG7_%=\n\t" \
  "QF8_%=:\n\t" \
  "s_mov_b32 %[c], 8\n\t" \
  "s_getpc_b64 s[92:93]\n\t" \
  "s_branch QC_%=\n\t" \
  "s_branch QG8_%=\n\t" \
  "QF9_%=:\n\t" \
  "s_mov_b32 %[c], 9\n\t" \
  "s_getpc_b64 s[92:93]\n\t" \
  "s_branch QC_%=\n\t" \
  "s_branch QG9_%=\n\t" \
  "QF10_%=:\n\t" \
  "s_mov_b32 %[c], 10\n\t" \
  "s_getpc_b64 s[92:93]\n\t" \
  "s_branch QC_%=\n\t" \
  "s_branch QG10_%=\n\t" \
  "QF11_%=:\n\t" \
  "s_mov_b32 %[c], 11\n\t" \
  "s_getpc_b64 s[92:93]\n\t" \
  "s_branch QC_%=\n\t" \
  "s_branch QG11_%=\n\t" \
  "QF12_%=:\n\t" \
  "s_mov_b32 %[c], 12\n\t" \
  "s_getpc_b64 s[92:93]\n\t" \
  "s_branch QC_%=\n\t" \
  "s_branch QG12_%=\n\t" \
  "QF13_%=:\n\t" \
  "s_mov_b32 %[c], 13\n\t" \
  "s_getpc_b64 s[92:93]\n\t" \
  "s_branch QC_%=\n\t" \
  "s_branch QG13_%=\n\t" \
  "QF14_%=:\n\t" \
  "s_mov_b32 %[c], 14\n\t" \
  "s_getpc_b64 s[92:93]\n\t" \
  "s_branch QC_%=\n\t" \
  "s_branch QG14_%=\n\t" \
  "QF15_%=:\n\t" \
  "s_mov_b32 %[c], 15\n\t" \
  "s_getpc_b64 s[92:93]\n\t" \
  "s_branch QC_%=\n\t" \
  "s_branch QG15_%=\n\t" \
  "QC_%=:\n\t" \
  "s_add_u32 s92, s92, 4\n\t" \
  "s_addc_u32 s93, s93, 0\n\t" \
  "QU_%=:\n\t" \
  "ds_read_b32 v166, v165\n\t" \
  "s_add_u32 %[t1], %[c], 5\n\t" \
  "s_waitcnt lgkmcnt(0)\n\t" \
  "v_readfirstlane_b32 %[t0], v166\n\t" \
  "s_nop 0\n\t" \
  "s_min_u32 %[t1], %[t1], %[t0]\n\t" \
  "QL_%=:\n\t" \
  "s_cmp_ge_u32 %[iss], %[t1]\n\t" \
  "s_cbranch_scc1 QD_%=\n\t" \
  "s_mov_b32 %[t0], %[iss]\n\t" \
  "s_sub_u32 %[th], %[t0], 6\n\t" \
  "s_cselect_b32 %[t0], %[t0], %[th]\n\t" \
  "s_sub_u32 %[th], %[t0], 6\n\t" \
  "s_cselect_b32 %[t0], %[t0], %[th]\n\t" \
  "s_cmp_eq_u32 %[t0], 1\n\t" \
  "s_cbranch_scc1 QS1_%=\n\t" \
  "s_cmp_eq_u32 %[t0], 2\n\t" \
  "s_cbranch_scc1 QS2_%=\n\t" \
  "s_cmp_eq_u32 %[t0], 3\n\t" \
  "s_cbranch_scc1 QS3_%=\n\t" \
  "s_cmp_eq_u32 %[t0], 4\n\t" \
  "s_cbranch_scc1 QS4_%=\n\t" \
  "s_cmp_eq_u32 %[t0], 5\n\t" \
  "s_cbranch_scc1 QS5_%=\n\t" \
  "QS0_%=:\n\t" \
  "v_readlane_b32 s94, v167, %[iss]\n\t" \
  "s_nop 4\n\t" \
  "buffer_load_dwordx4 v[0:3], v164, %[ob], s94 offen offset:0 sc1\n\t" \
  "buffer_load_dwordx4 v[4:7], v164, %[ob], s94 offen offset:1024 sc1\n\t" \
  "buffer_load_dwordx4 v[8:11], v164, %[ob], s94 offen offset:2048 sc1\n\t" \
  "buffer_load_dwordx4 v[12:15], v164, %[ob], s94 offen offset:3072 sc1\n\t" \
  "s_add_u32 %[iss], %[iss], 1\n\t" \
  "s_cmp_ge_u32 %[iss], %[t1]\n\t" \
  "s_cbranch_scc1 QD_%=\n\t" \
  "QS1_%=:\n\t" \
  "v_readlane_b32 s94, v167, %[iss]\n\t" \
  "s_nop 4\n\t" \
  "buffer_load_dwordx4 v[16:19], v164, %[ob], s94 offen offset:0 sc1\n\t" \
  "buffer_load_dwordx4 v[20:23], v164, %[ob], s94 offen offset:1024 sc1\n\t" \
  "buffer_load_dwordx4 v[24:27], v164, %[ob], s94 offen offset:2048 sc1\n\t" \
  "buffer_load_dwordx4 v[28:31], v164, %[ob], s94 offen offset:3072 sc1\n\t" \
  "s_add_u32 %[iss], %[iss], 1\n\t" \
  "s_cmp_ge_u32 %[iss], %[t1]\n\t" \
  "s_cbranch_scc1 QD_%=\n\t" \
  "QS2_%=:\n\t" \
  "v_readlane_b32 s94, v167, %[iss]\n\t" \
  "s_nop 4\n\t" \
  "buffer_load_dwordx4 v[32:35], v164, %[ob], s94 offen offset:0 sc1\n\t" \
  "buffer_load_dwordx4 v[36:39], v164, %[ob], s94 offen offset:1024 sc1\n\t" \
  "buffer_load_dwordx4 v[40:43], v164, %[ob], s94 offen offset:2048 sc1\n\t" \
  "buffer_load_dwordx4 v[44:47], v164, %[ob], s94 offen offset:3072 sc1\n\t" \
  "s_add_u32 %[iss], %[iss], 1\n\t" \
  "s_cmp_ge_u32 %[iss], %[t1]\n\t" \
  "s_cbranch_scc1 QD_%=\n\t" \
  "QS3_%=:\n\t" \
  "v_readlane_b32 s94, v167, %[iss]\n\t" \
  "s_nop 4\n\t" \
  "buffer_load_dwordx4 v[48:51], v164, %[ob], s94 offen offset:0 sc1\n\t" \
  "buffer_load_dwordx4 v[52:55], v164, %[ob], s94 offen offset:1024 sc1\n\t" \
  "buffer_load_dwordx4 v[56:59], v164, %[ob], s94 offen offset:2048 sc1\n\t" \
  "buffer_load_dwordx4 v[60:63], v164, %[ob], s94 offen offset:3072 sc1\n\t" \
  "s_add_u32 %[iss], %[iss], 1\n\t" \
  "s_cmp_ge_u32 %[iss], %[t1]\n\t" \
  "s_cbranch_scc1 QD_%=\n\t" \
  "QS4_%=:\n\t" \
  "v_readlane_b32 s94, v167, %[iss]\n\t" \
  "s_nop 4\n\t" \
  "buffer_load_dwordx4 v[64:67], v164, %[ob], s94 offen offset:0 sc1\n\t" \
  "buffer_load_dwordx4 v[68:71], v164, %[ob], s94 offen offset:1024 sc1\n\t" \
  "buffer_load_dwordx4 v[72:75], v164, %[ob], s94 offen offset:2048 sc1\n\t" \
  "buffer_load_dwordx4 v[76:79], v164, %[ob], s94 offen offset:3072 sc1\n\t" \
  "s_add_u32 %[iss], %[iss], 1\n\t" \
  "s_cmp_ge_u32 %[iss], %[t1]\n\t" \
  "s_cbranch_scc1 QD_%=\n\t" \
  "QS5_%=:\n\t" \
  "v_readlane_b32 s94, v167, %[iss]\n\t" \
  "s_nop 4\n\t" \
  "buffer_load_dwordx4 v[80:83], v164, %[ob], s94 offen offset:0 sc1\n\t" \
  "buffer_load_dwordx4 v[84:87], v164, %[ob], s94 offen offset:1024 sc1\n\t" \
  "buffer_load_dwordx4 v[88:91], v164, %[ob], s94 offen offset:2048 sc1\n\t" \
  "buffer_load_dwordx4 v[92:95], v164, %[ob], s94 offen offset:3072 sc1\n\t" \
  "s_add_u32 %[iss], %[iss], 1\n\t" \
  "s_cmp_ge_u32 %[iss], %[t1]\n\t" \
  "s_cbranch_scc0 QS0_%=\n\t" \
  "QD_%=:\n\t" \
  "s_cmp_gt_u32 %[iss], %[c]\n\t" \
  "s_cbranch_scc1 QW_%=\n\t" \
  "s_addk_i32 %[cn], 1\n\t" \
  "s_cmp_gt_u32 %[cn], 0x400000\n\t" \
  "s_cbranch_scc1 QT_%=\n\t" \
  "s_sleep 1\n\t" \
  "s_branch QU_%=\n\t" \
  "QT_%=:\n\t" \
  "s_add_u32 %[t1], %[c], 1\n\t" \
  "s_branch QL_%=\n\t" \
  "QW_%=:\n\t" \
  "s_cmp_eq_u32 %[iss], 16\n\t" \
  "s_cselect_b64 vcc, -1, 0\n\t" \
  "s_sub_u32 %[t0], %[iss], %[c]\n\t" \
  "s_cmp_eq_u32 %[t0], 5\n\t" \
  "s_cselect_b32 %[th], %[iss], 99\n\t" \
  "s_waitcnt vmcnt(16)\n\t" \
  "s_cmp_ge_u32 %[t0], 5\n\t" \
  "s_cbranch_scc1 QR_%=\n\t" \
  "s_waitcnt vmcnt(12)\n\t" \
  "s_cmp_ge_u32 %[t0], 4\n\t" \
  "s_cbranch_scc1 QR_%=\n\t" \
  "s_waitcnt vmcnt(8)\n\t" \
  "s_cmp_ge_u32 %[t0], 3\n\t" \
  "s_cbranch_scc1 QR_%=\n\t" \
  "s_waitcnt vmcnt(4)\n\t" \
  "s_cmp_ge_u32 %[t0], 2\n\t" \
  "s_cbranch_scc1 QR_%=\n\t" \
  "s_waitcnt vmcnt(0)\n\t" \
  "QR_%=:\n\t" \
  "s_setpc_b64 s[92:93]\n\t" \
  "QX_%=:\n\t" \
  "s_nop 7\n\t"
#define G3_CLOB "v0", "v1", "v2", "v3", "v4", "v5", "v6", "v7", "v8", "v9", "v10", "v11", "v12", "v13", "v14", "v15", "v16", "v17", "v18", "v19", "v20", "v21", "v22", "v23", "v24", "v25", "v26", "v27", "v28", "v29", "v30", "v31", "v32", "v33", "v34", "v35", "v36", "v37", "v38", "v39", "v40", "v41", "v42", "v43", "v44", "v45", "v46", "v47", "v48", "v49", "v50", "v51", "v52", "v53", "v54", "v55", "v56", "v57", "v58", "v59", "v60", "v61", "v62", "v63", "v64", "v65", "v66", "v67", "v68", "v69", "v70", "v71", "v72", "v73", "v74", "v75", "v76", "v77", "v78", "v79", "v80", "v81", "v82", "v83", "v84", "v85", "v86", "v87", "v88", "v89", "v90", "v91", "v92", "v93", "v94", "v95", "v96", "v97", "v98", "v99", "v100", "v101", "v102", "v103", "v104", "v105", "v106", "v107", "v108", "v109", "v110", "v111", "v112", "v113", "v114", "v115", "v116", "v117", "v118", "v119", "v120", "v121", "v122", "v123", "v124", "v125", "v126", "v127", "v166", "s88", "s89", "s92", "s93", "s94"

#define PP_ASM \
  "v_mov_b32 v60, 0\n\t" \
  "s_mov_b32 %[t0], %[n0]\n\t" \
  "s_cmp_eq_u32 %[t0], 0\n\t" \
  "s_cbranch_scc1 PAx_%=\n\t" \
  "PA_%=:\n\t" \
  "s_barrier\n\t" \
  "s_sub_u32 %[t0], %[t0], 1\n\t" \
  "s_cmp_lg_u32 %[t0], 0\n\t" \
  "s_cbranch_scc1 PA_%=\n\t" \
  "PAx_%=:\n\t" \
  "s_mov_b32 m0, %[shd]\n\t" \
  "global_load_dword v160, v60, %[fl] sc1\n\t" \
  "global_load_lds_dwordx4 v163, %[shp]\n\t" \
  "s_mov_b32 m0, %[tgd]\n\t" \
  "s_mov_b64 exec, %[em]\n\t" \
  "global_load_lds_dword v60, %[fl] sc1\n\t" \
  "s_mov_b64 exec, -1\n\t" \
  "global_load_ushort v161, v60, %[hp]\n\t" \
  "global_load_ushort v162, v60, %[dp]\n\t" \
  "global_load_dwordx4 v[0:3], v152, %[kb0]\n\t" \
  "global_load_dwordx4 v[4:7], v153, %[kb0]\n\t" \
  "global_load_dwordx4 v[8:11], v154, %[kb0]\n\t" \
  "global_load_dwordx4 v[12:15], v155, %[kb3] offset:0\n\t" \
  "global_load_dwordx4 v[28:31], v155, %[vb0] offset:0\n\t" \
  "global_load_dwordx4 v[16:19], v155, %[kb3] offset:2048\n\t" \
  "global_load_dwordx4 v[32:35], v155, %[vb0] offset:2048\n\t" \
  "global_load_dwordx4 v[20:23], v155, %[kb5] offset:0\n\t" \
  "global_load_dwordx4 v[36:39], v155, %[vb2] offset:0\n\t" \
  "global_load_dwordx4 v[24:27], v155, %[kb5] offset:2048\n\t" \
  "global_load_dwordx4 v[40:43], v155, %[vb2] offset:2048\n\t" \
  "s_setprio 2\n\t" \
  "s_barrier\n\t" \
  "s_barrier\n\t" \
  "s_waitcnt vmcnt(11)\n\t" \
  "ds_read_b128 v[128:131], v156 offset:0\n\t" \
  "ds_read_b128 v[132:135], v156 offset:16\n\t" \
  "ds_read_b128 v[136:139], v156 offset:32\n\t" \
  "ds_read_b128 v[140:143], v156 offset:48\n\t" \
  "s_waitcnt lgkmcnt(0)\n\t" \
  "v_lshlrev_b32 v64, 16, v128\n\t" \
  "v_and_b32 v65, 0xffff0000, v128\n\t" \
  "v_lshlrev_b32 v66, 16, v129\n\t" \
  "v_and_b32 v67, 0xffff0000, v129\n\t" \
  "v_lshlrev_b32 v68, 16, v130\n\t" \
  "v_and_b32 v69, 0xffff0000, v130\n\t" \
  "v_lshlrev_b32 v70, 16, v131\n\t" \
  "v_and_b32 v71, 0xffff0000, v131\n\t" \
  "v_lshlrev_b32 v72, 16, v132\n\t" \
  "v_and_b32 v73, 0xffff0000, v132\n\t" \
  "v_lshlrev_b32 v74, 16, v133\n\t" \
  "v_and_b32 v75, 0xffff0000, v133\n\t" \
  "v_lshlrev_b32 v76, 16, v134\n\t" \
  "v_and_b32 v77, 0xffff0000, v134\n\t" \
  "v_lshlrev_b32 v78, 16, v135\n\t" \
  "v_and_b32 v79, 0xffff0000, v135\n\t" \
  "v_lshlrev_b32 v80, 16, v136\n\t" \
  "v_and_b32 v81, 0xffff0000, v136\n\t" \
  "v_lshlrev_b32 v82, 16, v137\n\t" \
  "v_and_b32 v83, 0xffff0000, v137\n\t" \
  "v_lshlrev_b32 v84, 16, v138\n\t" \
  "v_and_b32 v85, 0xffff0000, v138\n\t" \
  "v_lshlrev_b32 v86, 16, v139\n\t" \
  "v_and_b32 v87, 0xffff0000, v139\n\t" \
  "v_lshlrev_b32 v88, 16, v140\n\t" \
  "v_and_b32 v89, 0xffff0000, v140\n\t" \
  "v_lshlrev_b32 v90, 16, v141\n\t" \
  "v_and_b32 v91, 0xffff0000, v141\n\t" \
  "v_lshlrev_b32 v92, 16, v142\n\t" \
  "v_and_b32 v93, 0xffff0000, v142\n\t" \
  "v_lshlrev_b32 v94, 16, v143\n\t" \
  "v_and_b32 v95, 0xffff0000, v143\n\t" \
  "s_waitcnt vmcnt(10)\n\t" \
  "s_mov_b64 exec, %[zm]\n\t" \
  "v_mov_b32 v0, 0\n\t" \
  "v_mov_b32 v1, 0\n\t" \
  "v_mov_b32 v2, 0\n\t" \
  "v_mov_b32 v3, 0\n\t" \
  "s_mov_b64 exec, -1\n\t" \
  "v_lshlrev_b32 v128, 16, v0\n\t" \
  "v_and_b32 v129, 0xffff0000, v0\n\t" \
  "s_barrier\n\t" \
  "v_lshlrev_b32 v130, 16, v1\n\t" \
  "v_and_b32 v131, 0xffff0000, v1\n\t" \
  "v_lshlrev_b32 v132, 16, v2\n\t" \
  "v_and_b32 v133, 0xffff0000, v2\n\t" \
  "v_lshlrev_b32 v134, 16, v3\n\t" \
  "v_and_b32 v135, 0xffff0000, v3\n\t" \
  "v_mul_f32 v96, v128, v67\n\t" \
  "v_mul_f32 v97, v129, v71\n\t" \
  "v_mul_f32 v98, v130, v75\n\t" \
  "v_mul_f32 v99, v131, v79\n\t" \
  "v_mul_f32 v100, v132, v83\n\t" \
  "v_mul_f32 v101, v133, v87\n\t" \
  "v_mul_f32 v102, v134, v91\n\t" \
  "v_mul_f32 v103, v135, v95\n\t" \
  "s_waitcnt vmcnt(9)\n\t" \
  "s_mov_b64 exec, %[zm]\n\t" \
  "v_mov_b32 v4, 0\n\t" \
  "v_mov_b32 v5, 0\n\t" \
  "v_mov_b32 v6, 0\n\t" \
  "v_mov_b32 v7, 0\n\t" \
  "s_mov_b64 exec, -1\n\t" \
  "v_lshlrev_b32 v128, 16, v4\n\t" \
  "v_and_b32 v129, 0xffff0000, v4\n\t" \
  "v_lshlrev_b32 v130, 16, v5\n\t" \
  "v_and_b32 v131, 0xffff0000, v5\n\t" \
  "v_lshlrev_b32 v132, 16, v6\n\t" \
  "v_and_b32 v133, 0xffff0000, v6\n\t" \
  "v_lshlrev_b32 v134, 16, v7\n\t" \
  "v_and_b32 v135, 0xffff0000, v7\n\t" \
  "v_fmac_f32 v96, v128, v66\n\t" \
  "v_fmac_f32 v97, v129, v70\n\t" \
  "v_fmac_f32 v98, v130, v74\n\t" \
  "v_fmac_f32 v99, v131, v78\n\t" \
  "v_fmac_f32 v100, v132, v82\n\t" \
  "v_fmac_f32 v101, v133, v86\n\t" \
  "v_fmac_f32 v102, v134, v90\n\t" \
  "v_fmac_f32 v103, v135, v94\n\t" \
  "v_mul_f32 v104, v128, v67\n\t" \
  "v_mul_f32 v105, v129, v71\n\t" \
  "v_mul_f32 v106, v130, v75\n\t" \
  "v_mul_f32 v107, v131, v79\n\t" \
  "v_mul_f32 v108, v132, v83\n\t" \
  "v_mul_f32 v109, v133, v87\n\t" \
  "v_mul_f32 v110, v134, v91\n\t" \
  "v_mul_f32 v111, v135, v95\n\t" \
  "s_waitcnt vmcnt(8)\n\t" \
  "s_mov_b64 exec, %[zm]\n\t" \
  "v_mov_b32 v8, 0\n\t" \
  "v_mov_b32 v9, 0\n\t" \
  "v_mov_b32 v10, 0\n\t" \
  "v_mov_b32 v11, 0\n\t" \
  "s_mov_b64 exec, -1\n\t" \
  "s_setprio 3\n\t" \
  "s_barrier\n\t" \
  "s_mov_b32 m0, %[hd]\n\t" \
  "s_nop 0\n\t" \
  "global_load_lds_dwordx4 v163, %[hp] offset:0\n\t" \
  "global_load_lds_dwordx4 v163, %[hp] offset:1024\n\t" \
  "global_load_lds_dwordx4 v163, %[hp] offset:2048\n\t" \
  "global_load_lds_dwordx4 v163, %[hp] offset:3072\n\t" \
  "global_load_dwordx4 v[44:47], v155, %[qb0] offset:0\n\t" \
  "global_load_dwordx4 v[48:51], v155, %[qb0] offset:2048\n\t" \
  "global_load_dwordx4 v[52:55], v155, %[qb2] offset:0\n\t" \
  "global_load_dwordx4 v[56:59], v155, %[qb2] offset:2048\n\t" \
  "s_setprio 2\n\t" \
  "v_lshlrev_b32 v128, 16, v8\n\t" \
  "v_and_b32 v129, 0xffff0000, v8\n\t" \
  "v_lshlrev_b32 v130, 16, v9\n\t" \
  "v_and_b32 v131, 0xffff0000, v9\n\t" \
  "v_lshlrev_b32 v132, 16, v10\n\t" \
  "v_and_b32 v133, 0xffff0000, v10\n\t" \
  "v_lshlrev_b32 v134, 16, v11\n\t" \
  "v_and_b32 v135, 0xffff0000, v11\n\t" \
  "v_fmac_f32 v96, v128, v65\n\t" \
  "v_fmac_f32 v97, v129, v69\n\t" \
  "v_fmac_f32 v98, v130, v73\n\t" \
  "v_fmac_f32 v99, v131, v77\n\t" \
  "v_fmac_f32 v100, v132, v81\n\t" \
  "v_fmac_f32 v101, v133, v85\n\t" \
  "v_fmac_f32 v102, v134, v89\n\t" \
  "v_fmac_f32 v103, v135, v93\n\t" \
  "v_fmac_f32 v104, v128, v66\n\t" \
  "v_fmac_f32 v105, v129, v70\n\t" \
  "v_fmac_f32 v106, v130, v74\n\t" \
  "v_fmac_f32 v107, v131, v78\n\t" \
  "v_fmac_f32 v108, v132, v82\n\t" \
  "v_fmac_f32 v109, v133, v86\n\t" \
  "v_fmac_f32 v110, v134, v90\n\t" \
  "v_fmac_f32 v111, v135, v94\n\t" \
  "v_mul_f32 v112, v128, v67\n\t" \
  "v_mul_f32 v113, v129, v71\n\t" \
  "v_mul_f32 v114, v130, v75\n\t" \
  "v_mul_f32 v115, v131, v79\n\t" \
  "v_mul_f32 v116, v132, v83\n\t" \
  "v_mul_f32 v117, v133, v87\n\t" \
  "v_mul_f32 v118, v134, v91\n\t" \
  "v_mul_f32 v119, v135, v95\n\t" \
  "s_waitcnt vmcnt(15)\n\t" \
  "v_lshlrev_b32 v128, 16, v12\n\t" \
  "v_and_b32 v129, 0xffff0000, v12\n\t" \
  "v_lshlrev_b32 v130, 16, v13\n\t" \
  "v_and_b32 v131, 0xffff0000, v13\n\t" \
  "v_lshlrev_b32 v132, 16, v14\n\t" \
  "v_and_b32 v133, 0xffff0000, v14\n\t" \
  "v_lshlrev_b32 v134, 16, v15\n\t" \
  "v_and_b32 v135, 0xffff0000, v15\n\t" \
  "s_barrier\n\t" \
  "v_fmac_f32 v96, v128, v64\n\t" \
  "v_fmac_f32 v97, v129, v68\n\t" \
  "v_fmac_f32 v98, v130, v72\n\t" \
  "v_fmac_f32 v99, v131, v76\n\t" \
  "v_fmac_f32 v100, v132, v80\n\t" \
  "v_fmac_f32 v101, v133, v84\n\t" \
  "v_fmac_f32 v102, v134, v88\n\t" \
  "v_fmac_f32 v103, v135, v92\n\t" \
  "v_fmac_f32 v104, v128, v65\n\t" \
  "v_fmac_f32 v105, v129, v69\n\t" \
  "v_fmac_f32 v106, v130, v73\n\t" \
  "v_fmac_f32 v107, v131, v77\n\t" \
  "v_fmac_f32 v108, v132, v81\n\t" \
  "v_fmac_f32 v109, v133, v85\n\t" \
  "v_fmac_f32 v110, v134, v89\n\t" \
  "v_fmac_f32 v111, v135, v93\n\t" \
  "v_fmac_f32 v112, v128, v66\n\t" \
  "v_fmac_f32 v113, v129, v70\n\t" \
  "v_fmac_f32 v114, v130, v74\n\t" \
  "v_fmac_f32 v115, v131, v78\n\t" \
  "v_fmac_f32 v116, v132, v82\n\t" \
  "v_fmac_f32 v117, v133, v86\n\t" \
  "v_fmac_f32 v118, v134, v90\n\t" \
  "v_fmac_f32 v119, v135, v94\n\t" \
  "v_mul_f32 v120, v128, v67\n\t" \
  "v_mul_f32 v121, v129, v71\n\t" \
  "v_mul_f32 v122, v130, v75\n\t" \
  "v_mul_f32 v123, v131, v79\n\t" \
  "v_mul_f32 v124, v132, v83\n\t" \
  "v_mul_f32 v125, v133, v87\n\t" \
  "v_mul_f32 v126, v134, v91\n\t" \
  "v_mul_f32 v127, v135, v95\n\t" \
  "s_waitcnt vmcnt(14)\n\t" \
  "v_lshlrev_b32 v128, 16, v28\n\t" \
  "v_and_b32 v129, 0xffff0000, v28\n\t" \
  "v_lshlrev_b32 v130, 16, v29\n\t" \
  "v_and_b32 v131, 0xffff0000, v29\n\t" \
  "v_lshlrev_b32 v132, 16, v30\n\t" \
  "v_and_b32 v133, 0xffff0000, v30\n\t" \
  "v_lshlrev_b32 v134, 16, v31\n\t" \
  "v_and_b32 v135, 0xffff0000, v31\n\t" \
  "s_barrier\n\t" \
  "v_mul_f32 v96, v96, v128\n\t" \
  "v_mul_f32 v97, v97, v129\n\t" \
  "v_mul_f32 v98, v98, v130\n\t" \
  "v_mul_f32 v99, v99, v131\n\t" \
  "v_mul_f32 v100, v100, v132\n\t" \
  "v_mul_f32 v101, v101, v133\n\t" \
  "v_mul_f32 v102, v102, v134\n\t" \
  "v_mul_f32 v103, v103, v135\n\t" \
  "v_cvt_pk_bf16_f32 v136, v96, v97\n\t" \
  "v_cvt_pk_bf16_f32 v137, v98, v99\n\t" \
  "v_cvt_pk_bf16_f32 v138, v100, v101\n\t" \
  "v_cvt_pk_bf16_f32 v139, v102, v103\n\t" \
  "v_mov_b32_dpp v128, v136 row_ror:8 row_mask:0xf bank_mask:0xf\n\t" \
  "v_mov_b32_dpp v129, v137 row_ror:8 row_mask:0xf bank_mask:0xf\n\t" \
  "v_mov_b32_dpp v130, v138 row_ror:8 row_mask:0xf bank_mask:0xf\n\t" \
  "v_mov_b32_dpp v131, v139 row_ror:8 row_mask:0xf bank_mask:0xf\n\t" \
  "v_perm_b32 v136, v128, v136, v157\n\t" \
  "v_perm_b32 v137, v129, v137, v157\n\t" \
  "v_perm_b32 v138, v130, v138, v157\n\t" \
  "v_perm_b32 v139, v131, v139, v157\n\t" \
  "v_permlane16_swap_b32 v136, v137\n\t" \
  "s_nop 0\n\t" \
  "v_permlane16_swap_b32 v138, v139\n\t" \
  "s_nop 1\n\t" \
  "v_permlane32_swap_b32 v136, v138\n\t" \
  "v_permlane32_swap_b32 v137, v139\n\t" \
  "s_waitcnt vmcnt(13)\n\t" \
  "v_lshlrev_b32 v128, 16, v16\n\t" \
  "v_and_b32 v129, 0xffff0000, v16\n\t" \
  "v_lshlrev_b32 v130, 16, v17\n\t" \
  "v_and_b32 v131, 0xffff0000, v17\n\t" \
  "v_lshlrev_b32 v132, 16, v18\n\t" \
  "v_and_b32 v133, 0xffff0000, v18\n\t" \
  "v_lshlrev_b32 v134, 16, v19\n\t" \
  "v_and_b32 v135, 0xffff0000, v19\n\t" \
  "v_fmac_f32 v104, v128, v64\n\t" \
  "v_fmac_f32 v105, v129, v68\n\t" \
  "v_fmac_f32 v106, v130, v72\n\t" \
  "v_fmac_f32 v107, v131, v76\n\t" \
  "v_fmac_f32 v108, v132, v80\n\t" \
  "v_fmac_f32 v109, v133, v84\n\t" \
  "s_setprio 3\n\t" \
  "s_barrier\n\t" \
  "global_store_dwordx4 v159, v[136:139], %[sb] sc1\n\t" \
  "v_add_u32 v158, 0x80, v159\n\t" \
  "v_bfi_b32 v159, %[mk], v158, v159\n\t" \
  "s_setprio 2\n\t" \
  "v_fmac_f32 v110, v134, v88\n\t" \
  "v_fmac_f32 v111, v135, v92\n\t" \
  "v_fmac_f32 v112, v128, v65\n\t" \
  "v_fmac_f32 v113, v129, v69\n\t" \
  "v_fmac_f32 v114, v130, v73\n\t" \
  "v_fmac_f32 v115, v131, v77\n\t" \
  "v_fmac_f32 v116, v132, v81\n\t" \
  "v_fmac_f32 v117, v133, v85\n\t" \
  "v_fmac_f32 v118, v134, v89\n\t" \
  "v_fmac_f32 v119, v135, v93\n\t" \
  "v_fmac_f32 v120, v128, v66\n\t" \
  "v_fmac_f32 v121, v129, v70\n\t" \
  "v_fmac_f32 v122, v130, v74\n\t" \
  "v_fmac_f32 v123, v131, v78\n\t" \
  "v_fmac_f32 v124, v132, v82\n\t" \
  "v_fmac_f32 v125, v133, v86\n\t" \
  "v_fmac_f32 v126, v134, v90\n\t" \
  "v_fmac_f32 v127, v135, v94\n\t" \
  "s_waitcnt vmcnt(13)\n\t" \
  "v_lshlrev_b32 v128, 16, v32\n\t" \
  "v_and_b32 v129, 0xffff0000, v32\n\t" \
  "v_lshlrev_b32 v130, 16, v33\n\t" \
  "v_and_b32 v131, 0xffff0000, v33\n\t" \
  "v_lshlrev_b32 v132, 16, v34\n\t" \
  "v_and_b32 v133, 0xffff0000, v34\n\t" \
  "v_lshlrev_b32 v134, 16, v35\n\t" \
  "v_and_b32 v135, 0xffff0000, v35\n\t" \
  "v_mul_f32 v104, v104, v128\n\t" \
  "v_mul_f32 v105, v105, v129\n\t" \
  "v_mul_f32 v106, v106, v130\n\t" \
  "v_mul_f32 v107, v107, v131\n\t" \
  "v_mul_f32 v108, v108, v132\n\t" \
  "v_mul_f32 v109, v109, v133\n\t" \
  "v_mul_f32 v110, v110, v134\n\t" \
  "v_mul_f32 v111, v111, v135\n\t" \
  "v_cvt_pk_bf16_f32 v140, v104, v105\n\t" \
  "v_cvt_pk_bf16_f32 v141, v106, v107\n\t" \
  "v_cvt_pk_bf16_f32 v142, v108, v109\n\t" \
  "v_cvt_pk_bf16_f32 v143, v110, v111\n\t" \
  "v_mov_b32_dpp v128, v140 row_ror:8 row_mask:0xf bank_mask:0xf\n\t" \
  "v_mov_b32_dpp v129, v141 row_ror:8 row_mask:0xf bank_mask:0xf\n\t" \
  "s_barrier\n\t" \
  "v_mov_b32_dpp v130, v142 row_ror:8 row_mask:0xf bank_mask:0xf\n\t" \
  "v_mov_b32_dpp v131, v143 row_ror:8 row_mask:0xf bank_mask:0xf\n\t" \
  "v_perm_b32 v140, v128, v140, v157\n\t" \
  "v_perm_b32 v141, v129, v141, v157\n\t" \
  "v_perm_b32 v142, v130, v142, v157\n\t" \
  "v_perm_b32 v143, v131, v143, v157\n\t" \
  "v_permlane16_swap_b32 v140, v141\n\t" \
  "s_nop 0\n\t" \
  "v_permlane16_swap_b32 v142, v143\n\t" \
  "s_nop 1\n\t" \
  "v_permlane32_swap_b32 v140, v142\n\t" \
  "v_permlane32_swap_b32 v141, v143\n\t" \
  "s_waitcnt vmcnt(12)\n\t" \
  "v_lshlrev_b32 v128, 16, v20\n\t" \
  "v_and_b32 v129, 0xffff0000, v20\n\t" \
  "v_lshlrev_b32 v130, 16, v21\n\t" \
  "v_and_b32 v131, 0xffff0000, v21\n\t" \
  "v_lshlrev_b32 v132, 16, v22\n\t" \
  "v_and_b32 v133, 0xffff0000, v22\n\t" \
  "v_lshlrev_b32 v134, 16, v23\n\t" \
  "v_and_b32 v135, 0xffff0000, v23\n\t" \
  "v_fmac_f32 v112, v128, v64\n\t" \
  "v_fmac_f32 v113, v129, v68\n\t" \
  "v_fmac_f32 v114, v130, v72\n\t" \
  "v_fmac_f32 v115, v131, v76\n\t" \
  "v_fmac_f32 v116, v132, v80\n\t" \
  "v_fmac_f32 v117, v133, v84\n\t" \
  "v_fmac_f32 v118, v134, v88\n\t" \
  "v_fmac_f32 v119, v135, v92\n\t" \
  "v_fmac_f32 v120, v128, v65\n\t" \
  "v_fmac_f32 v121, v129, v69\n\t" \
  "v_fmac_f32 v122, v130, v73\n\t" \
  "v_fmac_f32 v123, v131, v77\n\t" \
  "v_fmac_f32 v124, v132, v81\n\t" \
  "v_fmac_f32 v125, v133, v85\n\t" \
  "v_fmac_f32 v126, v134, v89\n\t" \
  "v_fmac_f32 v127, v135, v93\n\t" \
  "s_waitcnt vmcnt(11)\n\t" \
  "v_lshlrev_b32 v128, 16, v36\n\t" \
  "v_and_b32 v129, 0xffff0000, v36\n\t" \
  "v_lshlrev_b32 v130, 16, v37\n\t" \
  "v_and_b32 v131, 0xffff0000, v37\n\t" \
  "s_setprio 3\n\t" \
  "s_barrier\n\t" \
  "global_store_dwordx4 v159, v[140:143], %[sb] sc1\n\t" \
  "v_add_u32 v158, 0x80, v159\n\t" \
  "v_bfi_b32 v159, %[mk], v158, v159\n\t" \
  "s_setprio 2\n\t" \
  "v_lshlrev_b32 v132, 16, v38\n\t" \
  "v_and_b32 v133, 0xffff0000, v38\n\t" \
  "v_lshlrev_b32 v134, 16, v39\n\t" \
  "v_and_b32 v135, 0xffff0000, v39\n\t" \
  "v_mul_f32 v112, v112, v128\n\t" \
  "v_mul_f32 v113, v113, v129\n\t" \
  "v_mul_f32 v114, v114, v130\n\t" \
  "v_mul_f32 v115, v115, v131\n\t" \
  "v_mul_f32 v116, v116, v132\n\t" \
  "v_mul_f32 v117, v117, v133\n\t" \
  "v_mul_f32 v118, v118, v134\n\t" \
  "v_mul_f32 v119, v119, v135\n\t" \
  "v_cvt_pk_bf16_f32 v144, v112, v113\n\t" \
  "v_cvt_pk_bf16_f32 v145, v114, v115\n\t" \
  "v_cvt_pk_bf16_f32 v146, v116, v117\n\t" \
  "v_cvt_pk_bf16_f32 v147, v118, v119\n\t" \
  "v_mov_b32_dpp v128, v144 row_ror:8 row_mask:0xf bank_mask:0xf\n\t" \
  "v_mov_b32_dpp v129, v145 row_ror:8 row_mask:0xf bank_mask:0xf\n\t" \
  "v_mov_b32_dpp v130, v146 row_ror:8 row_mask:0xf bank_mask:0xf\n\t" \
  "v_mov_b32_dpp v131, v147 row_ror:8 row_mask:0xf bank_mask:0xf\n\t" \
  "v_perm_b32 v144, v128, v144, v157\n\t" \
  "v_perm_b32 v145, v129, v145, v157\n\t" \
  "v_perm_b32 v146, v130, v146, v157\n\t" \
  "v_perm_b32 v147, v131, v147, v157\n\t" \
  "v_permlane16_swap_b32 v144, v145\n\t" \
  "s_nop 0\n\t" \
  "v_permlane16_swap_b32 v146, v147\n\t" \
  "s_nop 1\n\t" \
  "v_permlane32_swap_b32 v144, v146\n\t" \
  "v_permlane32_swap_b32 v145, v147\n\t" \
  "s_waitcnt vmcnt(11)\n\t" \
  "v_lshlrev_b32 v128, 16, v24\n\t" \
  "v_and_b32 v129, 0xffff0000, v24\n\t" \
  "v_lshlrev_b32 v130, 16, v25\n\t" \
  "v_and_b32 v131, 0xffff0000, v25\n\t" \
  "v_lshlrev_b32 v132, 16, v26\n\t" \
  "v_and_b32 v133, 0xffff0000, v26\n\t" \
  "v_lshlrev_b32 v134, 16, v27\n\t" \
  "v_and_b32 v135, 0xffff0000, v27\n\t" \
  "v_fmac_f32 v120, v128, v64\n\t" \
  "v_fmac_f32 v121, v129, v68\n\t" \
  "s_setprio 3\n\t" \
  "s_barrier\n\t" \
  "global_store_dwordx4 v159, v[144:147], %[sb] sc1\n\t" \
  "v_add_u32 v158, 0x80, v159\n\t" \
  "v_bfi_b32 v159, %[mk], v158, v159\n\t" \
  "s_setprio 2\n\t" \
  "v_fmac_f32 v122, v130, v72\n\t" \
  "v_fmac_f32 v123, v131, v76\n\t" \
  "v_fmac_f32 v124, v132, v80\n\t" \
  "v_fmac_f32 v125, v133, v84\n\t" \
  "v_fmac_f32 v126, v134, v88\n\t" \
  "v_fmac_f32 v127, v135, v92\n\t" \
  "s_waitcnt vmcnt(11)\n\t" \
  "v_lshlrev_b32 v128, 16, v40\n\t" \
  "v_and_b32 v129, 0xffff0000, v40\n\t" \
  "v_lshlrev_b32 v130, 16, v41\n\t" \
  "v_and_b32 v131, 0xffff0000, v41\n\t" \
  "v_lshlrev_b32 v132, 16, v42\n\t" \
  "v_and_b32 v133, 0xffff0000, v42\n\t" \
  "v_lshlrev_b32 v134, 16, v43\n\t" \
  "v_and_b32 v135, 0xffff0000, v43\n\t" \
  "v_mul_f32 v120, v120, v128\n\t" \
  "v_mul_f32 v121, v121, v129\n\t" \
  "v_mul_f32 v122, v122, v130\n\t" \
  "v_mul_f32 v123, v123, v131\n\t" \
  "v_mul_f32 v124, v124, v132\n\t" \
  "v_mul_f32 v125, v125, v133\n\t" \
  "v_mul_f32 v126, v126, v134\n\t" \
  "v_mul_f32 v127, v127, v135\n\t" \
  "v_cvt_pk_bf16_f32 v148, v120, v121\n\t" \
  "v_cvt_pk_bf16_f32 v149, v122, v123\n\t" \
  "v_cvt_pk_bf16_f32 v150, v124, v125\n\t" \
  "v_cvt_pk_bf16_f32 v151, v126, v127\n\t" \
  "v_mov_b32_dpp v128, v148 row_ror:8 row_mask:0xf bank_mask:0xf\n\t" \
  "v_mov_b32_dpp v129, v149 row_ror:8 row_mask:0xf bank_mask:0xf\n\t" \
  "v_mov_b32_dpp v130, v150 row_ror:8 row_mask:0xf bank_mask:0xf\n\t" \
  "v_mov_b32_dpp v131, v151 row_ror:8 row_mask:0xf bank_mask:0xf\n\t" \
  "v_perm_b32 v148, v128, v148, v157\n\t" \
  "v_perm_b32 v149, v129, v149, v157\n\t" \
  "v_perm_b32 v150, v130, v150, v157\n\t" \
  "v_perm_b32 v151, v131, v151, v157\n\t" \
  "v_permlane16_swap_b32 v148, v149\n\t" \
  "s_nop 0\n\t" \
  "v_permlane16_swap_b32 v150, v151\n\t" \
  "s_nop 1\n\t" \
  "v_permlane32_swap_b32 v148, v150\n\t" \
  "v_permlane32_swap_b32 v149, v151\n\t" \
  "s_setprio 3\n\t" \
  "s_barrier\n\t" \
  "global_store_dwordx4 v159, v[148:151], %[sb] sc1\n\t" \
  "s_setprio 2\n\t" \
  "s_waitcnt vmcnt(8)\n\t" \
  "ds_read_b64_tr_b16 v[64:65], v165 offset:32\n\t" \
  "ds_read_b64_tr_b16 v[66:67], v165 offset:0\n\t" \
  "ds_read_b64_tr_b16 v[68:69], v165 offset:288\n\t" \
  "ds_read_b64_tr_b16 v[70:71], v165 offset:256\n\t" \
  "ds_read_b64_tr_b16 v[72:73], v165 offset:544\n\t" \
  "ds_read_b64_tr_b16 v[74:75], v165 offset:512\n\t" \
  "ds_read_b64_tr_b16 v[76:77], v165 offset:800\n\t" \
  "ds_read_b64_tr_b16 v[78:79], v165 offset:768\n\t" \
  "v_mov_b32 v24, v166\n\t" \
  "v_mov_b32 v25, v167\n\t" \
  "v_mov_b32 v63, 0xc0\n\t" \
  "v_add_u32 v158, 64, v24\n\t" \
  "v_bfi_b32 v26, v63, v158, v24\n\t" \
  "v_add_u32 v158, 64, v25\n\t" \
  "v_bfi_b32 v27, v63, v158, v25\n\t" \
  "v_add_u32 v158, 64, v26\n\t" \
  "v_bfi_b32 v28, v63, v158, v26\n\t" \
  "v_add_u32 v158, 64, v27\n\t" \
  "v_bfi_b32 v29, v63, v158, v27\n\t" \
  "v_add_u32 v158, 64, v28\n\t" \
  "v_bfi_b32 v30, v63, v158, v28\n\t" \
  "v_add_u32 v158, 64, v29\n\t" \
  "v_bfi_b32 v31, v63, v158, v29\n\t" \
  "s_barrier\n\t" \
  "ds_read_b64_tr_b16 v[80:81], v165 offset:1056\n\t" \
  "ds_read_b64_tr_b16 v[82:83], v165 offset:1024\n\t" \
  "ds_read_b64_tr_b16 v[84:85], v165 offset:1312\n\t" \
  "ds_read_b64_tr_b16 v[86:87], v165 offset:1280\n\t" \
  "ds_read_b64_tr_b16 v[88:89], v165 offset:1568\n\t" \
  "ds_read_b64_tr_b16 v[90:91], v165 offset:1536\n\t" \
  "ds_read_b64_tr_b16 v[92:93], v165 offset:1824\n\t" \
  "ds_read_b64_tr_b16 v[94:95], v165 offset:1792\n\t" \
  "s_setprio 3\n\t" \
  "s_barrier\n\t" \
  "s_waitcnt vmcnt(0)\n\t" \
  "v_add_u32 v61, 1, v160\n\t" \
  "s_mov_b64 exec, 1\n\t" \
  "global_store_dword v60, v61, %[ff] sc1\n\t" \
  "s_mov_b64 exec, -1\n\t" \
  "s_setprio 2\n\t" \
  "ds_read_b64_tr_b16 v[96:97], v165 offset:2080\n\t" \
  "ds_read_b64_tr_b16 v[98:99], v165 offset:2048\n\t" \
  "ds_read_b64_tr_b16 v[100:101], v165 offset:2336\n\t" \
  "ds_read_b64_tr_b16 v[102:103], v165 offset:2304\n\t" \
  "ds_read_b64_tr_b16 v[104:105], v165 offset:2592\n\t" \
  "ds_read_b64_tr_b16 v[106:107], v165 offset:2560\n\t" \
  "ds_read_b64_tr_b16 v[108:109], v165 offset:2848\n\t" \
  "ds_read_b64_tr_b16 v[110:111], v165 offset:2816\n\t" \
  "s_barrier\n\t" \
  "ds_read_b64_tr_b16 v[112:113], v165 offset:3104\n\t" \
  "ds_read_b64_tr_b16 v[114:115], v165 offset:3072\n\t" \
  "ds_read_b64_tr_b16 v[116:117], v165 offset:3360\n\t" \
  "ds_read_b64_tr_b16 v[118:119], v165 offset:3328\n\t" \
  "ds_read_b64_tr_b16 v[120:121], v165 offset:3616\n\t" \
  "ds_read_b64_tr_b16 v[122:123], v165 offset:3584\n\t" \
  "ds_read_b64_tr_b16 v[124:125], v165 offset:3872\n\t" \
  "ds_read_b64_tr_b16 v[126:127], v165 offset:3840\n\t" \
  "s_barrier\n\t" \
  "s_waitcnt lgkmcnt(0)\n\t" \
  "v_lshlrev_b32 v61, 16, v161\n\t" \
  "v_lshlrev_b32 v62, 16, v162\n\t" \
  "v_add_f32 v61, v61, v62\n\t" \
  "v_mov_b32 v62, 0xffff\n\t" \
  "v_cvt_pk_bf16_f32 v61, v61, v61\n\t" \
  "s_mov_b64 exec, 1\n\t" \
  "v_bfi_b32 v66, v62, v61, v66\n\t" \
  "s_mov_b64 exec, 15\n\t" \
  "v_and_b32 v66, v62, v66\n\t" \
  "s_mov_b64 exec, 16\n\t" \
  "v_bfi_b32 v66, v62, v66, v61\n\t" \
  "s_mov_b64 exec, 0xff\n\t" \
  "v_mov_b32 v67, 0\n\t" \
  "s_mov_b64 exec, 0x100\n\t" \
  "v_mov_b32 v67, v61\n\t" \
  "s_mov_b64 exec, 0xf00\n\t" \
  "v_and_b32 v67, v62, v67\n\t" \
  "s_mov_b64 exec, 0x1000\n\t" \
  "v_bfi_b32 v67, v62, v67, v61\n\t" \
  "s_mov_b64 exec, -1\n\t" \
  "s_waitcnt lgkmcnt(0)\n\t"
#define PP_NB 15
#define PP_Q 1
#define PP_CLOB "v0", "v1", "v2", "v3", "v4", "v5", "v6", "v7", "v8", "v9", "v10", "v11", "v12", "v13", "v14", "v15", "v16", "v17", "v18", "v19", "v20", "v21", "v22", "v23", "v32", "v33", "v34", "v35", "v36", "v37", "v38", "v39", "v40", "v41", "v42", "v43", "v60", "v61", "v62", "v63", "v128", "v129", "v130", "v131", "v132", "v133", "v134", "v135", "v136", "v137", "v138", "v139", "v140", "v141", "v142", "v143", "v144", "v145", "v146", "v147", "v148", "v149", "v150", "v151", "v158", "v161", "v162"

#define LD_ASM1 \
  "s_add_u32 m0, %[wb], 49152\n\t" \
  "s_add_u32 s68, %[rw0], 0\n\t" \
  "buffer_load_dwordx4 v5, %[sw], s68 offen offset:0 lds\n\t" \
  "buffer_load_dwordx4 v6, %[sw], s68 offen offset:1024 lds\n\t" \
  "buffer_load_dwordx4 v7, %[sw], s68 offen offset:2048 lds\n\t" \
  "s_add_u32 m0, %[xb], 36864\n\t" \
  "s_add_u32 s68, %[rx0], 0\n\t" \
  "buffer_load_dwordx4 v0, %[sx], s68 offen offset:0 lds\n\t" \
  "buffer_load_dwordx4 v1, %[sx], s68 offen offset:1024 lds\n\t" \
  "s_mov_b64 exec, 0xffff\n\t" \
  "buffer_load_dwordx4 v4, %[sx], s68 offen offset:2048 lds\n\t" \
  "s_mov_b64 exec, -1\n\t" \
  "s_add_u32 m0, %[xb], 46080\n\t" \
  "s_add_u32 s68, %[rx0], 147456\n\t" \
  "buffer_load_dwordx4 v2, %[sx], s68 offen offset:0 lds\n\t" \
  "buffer_load_dwordx4 v3, %[sx], s68 offen offset:1024 lds\n\t" \
  "s_mov_b64 exec, 0xffff\n\t" \
  "buffer_load_dwordx4 v4, %[sx], s68 offen offset:2048 lds\n\t" \
  "s_mov_b64 exec, -1\n\t" \
  "s_add_u32 m0, %[wb], 61440\n\t" \
  "s_add_u32 s68, %[rw0], 196608\n\t" \
  "buffer_load_dwordx4 v5, %[sw], s68 offen offset:0 lds\n\t" \
  "buffer_load_dwordx4 v6, %[sw], s68 offen offset:1024 lds\n\t" \
  "buffer_load_dwordx4 v7, %[sw], s68 offen offset:2048 lds\n\t"
#define LD_ASM2 \
  "s_mov_b32 s85, 0x180000\n\t" \
  "s_load_dword s84, %[pw], s85\n\t" \
  "s_mov_b32 s85, 0x180800\n\t" \
  "s_load_dword s84, %[pw], s85\n\t" \
  "s_mov_b32 s85, 0x181000\n\t" \
  "s_load_dword s84, %[pw], s85\n\t" \
  "s_mov_b32 s85, 0x1b0000\n\t" \
  "s_load_dword s84, %[pw], s85\n\t" \
  "s_mov_b32 s85, 0x1b0800\n\t" \
  "s_load_dword s84, %[pw], s85\n\t" \
  "s_mov_b32 s85, 0x1b1000\n\t" \
  "s_load_dword s84, %[pw], s85\n\t" \
  "s_mov_b32 s85, 0x1e0000\n\t" \
  "s_load_dword s84, %[pw], s85\n\t" \
  "s_mov_b32 s85, 0x1e0800\n\t" \
  "s_load_dword s84, %[pw], s85\n\t" \
  "s_add_u32 s68, %[rx1], 0\n\t" \
  "s_add_u32 s69, %[rx1], 147456\n\t" \
  "s_add_u32 s70, %[rw1], 0\n\t" \
  "s_add_u32 m0, %[xb], 0\n\t" \
  "s_waitcnt vmcnt(3)\n\t" \
  "s_barrier\n\t" \
  "buffer_load_dwordx4 v0, %[sx], s68 offen offset:0 lds\n\t" \
  "s_mov_b32 s85, 0x1e1000\n\t" \
  "s_load_dword s84, %[pw], s85\n\t" \
  "buffer_load_dwordx4 v1, %[sx], s68 offen offset:1024 lds\n\t" \
  "s_mov_b32 s85, 0x210000\n\t" \
  "s_load_dword s84, %[pw], s85\n\t" \
  "s_mov_b64 exec, 0xffff\n\t" \
  "buffer_load_dwordx4 v4, %[sx], s68 offen offset:2048 lds\n\t" \
  "s_add_u32 m0, %[xb], 9216\n\t" \
  "s_mov_b64 exec, -1\n\t" \
  "buffer_load_dwordx4 v2, %[sx], s69 offen offset:0 lds\n\t" \
  "s_mov_b32 s85, 0x210800\n\t" \
  "s_load_dword s84, %[pw], s85\n\t" \
  "buffer_load_dwordx4 v3, %[sx], s69 offen offset:1024 lds\n\t" \
  "s_mov_b64 exec, 0xffff\n\t" \
  "buffer_load_dwordx4 v4, %[sx], s69 offen offset:2048 lds\n\t" \
  "s_add_u32 m0, %[wb], 0\n\t" \
  "s_mov_b64 exec, -1\n\t" \
  "buffer_load_dwordx4 v5, %[sw], s70 offen offset:0 lds\n\t" \
  "buffer_load_dwordx4 v6, %[sw], s70 offen offset:1024 lds\n\t" \
  "buffer_load_dwordx4 v7, %[sw], s70 offen offset:2048 lds\n\t" \
  "s_add_u32 s68, %[rw1], 196608\n\t" \
  "s_add_u32 s69, %[rx2], 0\n\t" \
  "s_add_u32 s70, %[rx2], 147456\n\t" \
  "s_add_u32 m0, %[wb], 12288\n\t" \
  "s_waitcnt vmcnt(9)\n\t" \
  "s_barrier\n\t" \
  "buffer_load_dwordx4 v5, %[sw], s68 offen offset:0 lds\n\t" \
  "s_mov_b32 s85, 0x211000\n\t" \
  "s_load_dword s84, %[pw], s85\n\t" \
  "buffer_load_dwordx4 v6, %[sw], s68 offen offset:1024 lds\n\t" \
  "s_mov_b32 s85, 0x240000\n\t" \
  "s_load_dword s84, %[pw], s85\n\t" \
  "buffer_load_dwordx4 v7, %[sw], s68 offen offset:2048 lds\n\t" \
  "s_mov_b32 s85, 0x240800\n\t" \
  "s_load_dword s84, %[pw], s85\n\t" \
  "s_add_u32 m0, %[xb], 18432\n\t" \
  "s_nop 0\n\t" \
  "buffer_load_dwordx4 v0, %[sx], s69 offen offset:0 lds\n\t" \
  "buffer_load_dwordx4 v1, %[sx], s69 offen offset:1024 lds\n\t" \
  "s_mov_b64 exec, 0xffff\n\t" \
  "buffer_load_dwordx4 v4, %[sx], s69 offen offset:2048 lds\n\t" \
  "s_add_u32 m0, %[xb], 27648\n\t" \
  "s_mov_b64 exec, -1\n\t" \
  "buffer_load_dwordx4 v2, %[sx], s70 offen offset:0 lds\n\t" \
  "buffer_load_dwordx4 v3, %[sx], s70 offen offset:1024 lds\n\t" \
  "s_mov_b64 exec, 0xffff\n\t" \
  "buffer_load_dwordx4 v4, %[sx], s70 offen offset:2048 lds\n\t" \
  "s_mov_b64 exec, -1\n\t" \
  "s_add_u32 s68, %[rw2], 0\n\t" \
  "s_add_u32 s69, %[rw2], 196608\n\t" \
  "s_add_u32 s70, %[rx3], 0\n\t" \
  "s_add_u32 m0, %[wb], 24576\n\t" \
  "s_waitcnt vmcnt(9)\n\t" \
  "s_barrier\n\t" \
  "buffer_load_dwordx4 v5, %[sw], s68 offen offset:0 lds\n\t" \
  "s_mov_b32 s85, 0x241000\n\t" \
  "s_load_dword s84, %[pw], s85\n\t" \
  "buffer_load_dwordx4 v6, %[sw], s68 offen offset:1024 lds\n\t" \
  "s_mov_b32 s85, 0x270000\n\t" \
  "s_load_dword s84, %[pw], s85\n\t" \
  "buffer_load_dwordx4 v7, %[sw], s68 offen offset:2048 lds\n\t" \
  "s_mov_b32 s85, 0x270800\n\t" \
  "s_load_dword s84, %[pw], s85\n\t" \
  "s_add_u32 m0, %[wb], 36864\n\t" \
  "s_nop 0\n\t" \
  "buffer_load_dwordx4 v5, %[sw], s69 offen offset:0 lds\n\t" \
  "buffer_load_dwordx4 v6, %[sw], s69 offen offset:1024 lds\n\t" \
  "buffer_load_dwordx4 v7, %[sw], s69 offen offset:2048 lds\n\t" \
  "s_add_u32 m0, %[xb], 36864\n\t" \
  "s_nop 0\n\t" \
  "buffer_load_dwordx4 v0, %[sx], s70 offen offset:0 lds\n\t" \
  "buffer_load_dwordx4 v1, %[sx], s70 offen offset:1024 lds\n\t" \
  "s_mov_b64 exec, 0xffff\n\t" \
  "buffer_load_dwordx4 v4, %[sx], s70 offen offset:2048 lds\n\t" \
  "s_mov_b64 exec, -1\n\t" \
  "s_add_u32 s68, %[rw3], 0\n\t" \
  "s_add_u32 s69, %[rx3], 147456\n\t" \
  "s_add_u32 s70, %[rw3], 196608\n\t" \
  "s_add_u32 m0, %[wb], 49152\n\t" \
  "s_waitcnt vmcnt(15)\n\t" \
  "s_barrier\n\t" \
  "buffer_load_dwordx4 v5, %[sw], s68 offen offset:0 lds\n\t" \
  "s_mov_b32 s85, 0x271000\n\t" \
  "s_load_dword s84, %[pw], s85\n\t" \
  "buffer_load_dwordx4 v6, %[sw], s68 offen offset:1024 lds\n\t" \
  "s_mov_b32 s85, 0x2a0000\n\t" \
  "s_load_dword s84, %[pw], s85\n\t" \
  "buffer_load_dwordx4 v7, %[sw], s68 offen offset:2048 lds\n\t" \
  "s_mov_b32 s85, 0x2a0800\n\t" \
  "s_load_dword s84, %[pw], s85\n\t" \
  "s_add_u32 m0, %[xb], 46080\n\t" \
  "s_nop 0\n\t" \
  "buffer_load_dwordx4 v2, %[sx], s69 offen offset:0 lds\n\t" \
  "buffer_load_dwordx4 v3, %[sx], s69 offen offset:1024 lds\n\t" \
  "s_mov_b64 exec, 0xffff\n\t" \
  "buffer_load_dwordx4 v4, %[sx], s69 offen offset:2048 lds\n\t" \
  "s_add_u32 m0, %[wb], 61440\n\t" \
  "s_mov_b64 exec, -1\n\t" \
  "buffer_load_dwordx4 v5, %[sw], s70 offen offset:0 lds\n\t" \
  "buffer_load_dwordx4 v6, %[sw], s70 offen offset:1024 lds\n\t" \
  "buffer_load_dwordx4 v7, %[sw], s70 offen offset:2048 lds\n\t" \
  "s_add_u32 s68, %[rx0], 512\n\t" \
  "s_add_u32 s69, %[rw0], 1572864\n\t" \
  "s_add_u32 s70, %[rx0], 147968\n\t" \
  "s_add_u32 m0, %[xb], 0\n\t" \
  "s_waitcnt vmcnt(15)\n\t" \
  "s_barrier\n\t" \
  "buffer_load_dwordx4 v0, %[sx], s68 offen offset:0 lds\n\t" \
  "s_mov_b32 s85, 0x2a1000\n\t" \
  "s_load_dword s84, %[pw], s85\n\t" \
  "buffer_load_dwordx4 v1, %[sx], s68 offen offset:1024 lds\n\t" \
  "s_mov_b32 s85, 0x2d0000\n\t" \
  "s_load_dword s84, %[pw], s85\n\t" \
  "s_mov_b64 exec, 0xffff\n\t" \
  "buffer_load_dwordx4 v4, %[sx], s68 offen offset:2048 lds\n\t" \
  "s_add_u32 m0, %[wb], 0\n\t" \
  "s_mov_b64 exec, -1\n\t" \
  "buffer_load_dwordx4 v5, %[sw], s69 offen offset:0 lds\n\t" \
  "s_mov_b32 s85, 0x2d0800\n\t" \
  "s_load_dword s84, %[pw], s85\n\t" \
  "buffer_load_dwordx4 v6, %[sw], s69 offen offset:1024 lds\n\t" \
  "buffer_load_dwordx4 v7, %[sw], s69 offen offset:2048 lds\n\t" \
  "s_add_u32 m0, %[xb], 9216\n\t" \
  "s_nop 0\n\t" \
  "buffer_load_dwordx4 v2, %[sx], s70 offen offset:0 lds\n\t" \
  "buffer_load_dwordx4 v3, %[sx], s70 offen offset:1024 lds\n\t" \
  "s_mov_b64 exec, 0xffff\n\t" \
  "buffer_load_dwordx4 v4, %[sx], s70 offen offset:2048 lds\n\t" \
  "s_mov_b64 exec, -1\n\t" \
  "s_add_u32 s68, %[rw0], 1769472\n\t" \
  "s_add_u32 m0, %[wb], 12288\n\t" \
  "s_waitcnt vmcnt(21)\n\t" \
  "s_barrier\n\t" \
  "buffer_load_dwordx4 v5, %[sw], s68 offen offset:0 lds\n\t" \
  "s_mov_b32 s85, 0x2d1000\n\t" \
  "s_load_dword s84, %[pw], s85\n\t" \
  "buffer_load_dwordx4 v6, %[sw], s68 offen offset:1024 lds\n\t" \
  "s_mov_b32 s85, 0x300000\n\t" \
  "s_load_dword s84, %[pw], s85\n\t" \
  "buffer_load_dwordx4 v7, %[sw], s68 offen offset:2048 lds\n\t" \
  "s_mov_b32 s85, 0x300800\n\t" \
  "s_load_dword s84, %[pw], s85\n\t" \
  "s_add_u32 s68, %[rx1], 512\n\t" \
  "s_add_u32 s69, %[rw1], 1572864\n\t" \
  "s_add_u32 s70, %[rx1], 147968\n\t" \
  "s_add_u32 m0, %[xb], 18432\n\t" \
  "s_waitcnt vmcnt(15)\n\t" \
  "s_barrier\n\t" \
  "buffer_load_dwordx4 v0, %[sx], s68 offen offset:0 lds\n\t" \
  "s_mov_b32 s85, 0x301000\n\t" \
  "s_load_dword s84, %[pw], s85\n\t" \
  "buffer_load_dwordx4 v1, %[sx], s68 offen offset:1024 lds\n\t" \
  "s_mov_b32 s85, 0x330000\n\t" \
  "s_load_dword s84, %[pw], s85\n\t" \
  "s_mov_b64 exec, 0xffff\n\t" \
  "buffer_load_dwordx4 v4, %[sx], s68 offen offset:2048 lds\n\t" \
  "s_add_u32 m0, %[wb], 24576\n\t" \
  "s_mov_b64 exec, -1\n\t" \
  "buffer_load_dwordx4 v5, %[sw], s69 offen offset:0 lds\n\t" \
  "s_mov_b32 s85, 0x330800\n\t" \
  "s_load_dword s84, %[pw], s85\n\t" \
  "buffer_load_dwordx4 v6, %[sw], s69 offen offset:1024 lds\n\t" \
  "buffer_load_dwordx4 v7, %[sw], s69 offen offset:2048 lds\n\t" \
  "s_add_u32 m0, %[xb], 27648\n\t" \
  "s_nop 0\n\t" \
  "buffer_load_dwordx4 v2, %[sx], s70 offen offset:0 lds\n\t" \
  "buffer_load_dwordx4 v3, %[sx], s70 offen offset:1024 lds\n\t" \
  "s_mov_b64 exec, 0xffff\n\t" \
  "buffer_load_dwordx4 v4, %[sx], s70 offen offset:2048 lds\n\t" \
  "s_mov_b64 exec, -1\n\t" \
  "s_add_u32 s68, %[rw1], 1769472\n\t" \
  "s_add_u32 m0, %[wb], 36864\n\t" \
  "s_waitcnt vmcnt(21)\n\t" \
  "s_barrier\n\t" \
  "buffer_load_dwordx4 v5, %[sw], s68 offen offset:0 lds\n\t" \
  "s_mov_b32 s85, 0x331000\n\t" \
  "s_load_dword s84, %[pw], s85\n\t" \
  "buffer_load_dwordx4 v6, %[sw], s68 offen offset:1024 lds\n\t" \
  "s_mov_b32 s85, 0x360000\n\t" \
  "s_load_dword s84, %[pw], s85\n\t" \
  "buffer_load_dwordx4 v7, %[sw], s68 offen offset:2048 lds\n\t" \
  "s_mov_b32 s85, 0x360800\n\t" \
  "s_load_dword s84, %[pw], s85\n\t" \
  "s_add_u32 s68, %[rx2], 512\n\t" \
  "s_add_u32 s69, %[rw2], 1572864\n\t" \
  "s_add_u32 m0, %[xb], 36864\n\t" \
  "s_waitcnt vmcnt(15)\n\t" \
  "s_barrier\n\t" \
  "buffer_load_dwordx4 v0, %[sx], s68 offen offset:0 lds\n\t" \
  "s_mov_b32 s85, 0x361000\n\t" \
  "s_load_dword s84, %[pw], s85\n\t" \
  "buffer_load_dwordx4 v1, %[sx], s68 offen offset:1024 lds\n\t" \
  "s_mov_b32 s85, 0x390000\n\t" \
  "s_load_dword s84, %[pw], s85\n\t" \
  "s_mov_b64 exec, 0xffff\n\t" \
  "buffer_load_dwordx4 v4, %[sx], s68 offen offset:2048 lds\n\t" \
  "s_add_u32 m0, %[wb], 49152\n\t" \
  "s_mov_b64 exec, -1\n\t" \
  "buffer_load_dwordx4 v5, %[sw], s69 offen offset:0 lds\n\t" \
  "s_mov_b32 s85, 0x390800\n\t" \
  "s_load_dword s84, %[pw], s85\n\t" \
  "buffer_load_dwordx4 v6, %[sw], s69 offen offset:1024 lds\n\t" \
  "buffer_load_dwordx4 v7, %[sw], s69 offen offset:2048 lds\n\t" \
  "s_add_u32 s68, %[rx2], 147968\n\t" \
  "s_add_u32 s69, %[rw2], 1769472\n\t" \
  "s_add_u32 m0, %[xb], 46080\n\t" \
  "s_waitcnt vmcnt(18)\n\t" \
  "s_barrier\n\t" \
  "buffer_load_dwordx4 v2, %[sx], s68 offen offset:0 lds\n\t" \
  "s_mov_b32 s85, 0x391000\n\t" \
  "s_load_dword s84, %[pw], s85\n\t" \
  "buffer_load_dwordx4 v3, %[sx], s68 offen offset:1024 lds\n\t" \
  "s_mov_b32 s85, 0x3c0000\n\t" \
  "s_load_dword s84, %[pw], s85\n\t" \
  "s_mov_b64 exec, 0xffff\n\t" \
  "buffer_load_dwordx4 v4, %[sx], s68 offen offset:2048 lds\n\t" \
  "s_add_u32 m0, %[wb], 61440\n\t" \
  "s_mov_b64 exec, -1\n\t" \
  "buffer_load_dwordx4 v5, %[sw], s69 offen offset:0 lds\n\t" \
  "s_mov_b32 s85, 0x3c0800\n\t" \
  "s_load_dword s84, %[pw], s85\n\t" \
  "buffer_load_dwordx4 v6, %[sw], s69 offen offset:1024 lds\n\t" \
  "buffer_load_dwordx4 v7, %[sw], s69 offen offset:2048 lds\n\t" \
  "s_add_u32 s68, %[rx3], 512\n\t" \
  "s_add_u32 s69, %[rw3], 1572864\n\t" \
  "s_add_u32 m0, %[xb], 0\n\t" \
  "s_waitcnt vmcnt(15)\n\t" \
  "s_barrier\n\t" \
  "buffer_load_dwordx4 v0, %[sx], s68 offen offset:0 lds\n\t" \
  "s_mov_b32 s85, 0x3c1000\n\t" \
  "s_load_dword s84, %[pw], s85\n\t" \
  "buffer_load_dwordx4 v1, %[sx], s68 offen offset:1024 lds\n\t" \
  "s_mov_b32 s85, 0x3f0000\n\t" \
  "s_load_dword s84, %[pw], s85\n\t" \
  "s_mov_b64 exec, 0xffff\n\t" \
  "buffer_load_dwordx4 v4, %[sx], s68 offen offset:2048 lds\n\t" \
  "s_add_u32 m0, %[wb], 0\n\t" \
  "s_mov_b64 exec, -1\n\t" \
  "buffer_load_dwordx4 v5, %[sw], s69 offen offset:0 lds\n\t" \
  "s_mov_b32 s85, 0x3f0800\n\t" \
  "s_load_dword s84, %[pw], s85\n\t" \
  "buffer_load_dwordx4 v6, %[sw], s69 offen offset:1024 lds\n\t" \
  "buffer_load_dwordx4 v7, %[sw], s69 offen offset:2048 lds\n\t" \
  "s_add_u32 s68, %[rx3], 147968\n\t" \
  "s_add_u32 s69, %[rw3], 1769472\n\t" \
  "s_add_u32 m0, %[xb], 9216\n\t" \
  "s_waitcnt vmcnt(18)\n\t" \
  "s_barrier\n\t" \
  "buffer_load_dwordx4 v2, %[sx], s68 offen offset:0 lds\n\t" \
  "s_mov_b32 s85, 0x3f1000\n\t" \
  "s_load_dword s84, %[pw], s85\n\t" \
  "buffer_load_dwordx4 v3, %[sx], s68 offen offset:1024 lds\n\t" \
  "s_mov_b32 s85, 0x420000\n\t" \
  "s_load_dword s84, %[pw], s85\n\t" \
  "s_mov_b64 exec, 0xffff\n\t" \
  "buffer_load_dwordx4 v4, %[sx], s68 offen offset:2048 lds\n\t" \
  "s_add_u32 m0, %[wb], 12288\n\t" \
  "s_mov_b64 exec, -1\n\t" \
  "buffer_load_dwordx4 v5, %[sw], s69 offen offset:0 lds\n\t" \
  "s_mov_b32 s85, 0x420800\n\t" \
  "s_load_dword s84, %[pw], s85\n\t" \
  "buffer_load_dwordx4 v6, %[sw], s69 offen offset:1024 lds\n\t" \
  "buffer_load_dwordx4 v7, %[sw], s69 offen offset:2048 lds\n\t" \
  "s_add_u32 s68, %[rx0], 1024\n\t" \
  "s_add_u32 s69, %[rw0], 3145728\n\t" \
  "s_add_u32 m0, %[xb], 18432\n\t" \
  "s_waitcnt vmcnt(15)\n\t" \
  "s_barrier\n\t" \
  "buffer_load_dwordx4 v0, %[sx], s68 offen offset:0 lds\n\t" \
  "s_mov_b32 s85, 0x421000\n\t" \
  "s_load_dword s84, %[pw], s85\n\t" \
  "buffer_load_dwordx4 v1, %[sx], s68 offen offset:1024 lds\n\t" \
  "s_mov_b32 s85, 0x450000\n\t" \
  "s_load_dword s84, %[pw], s85\n\t" \
  "s_mov_b64 exec, 0xffff\n\t" \
  "buffer_load_dwordx4 v4, %[sx], s68 offen offset:2048 lds\n\t" \
  "s_add_u32 m0, %[wb], 24576\n\t" \
  "s_mov_b64 exec, -1\n\t" \
  "buffer_load_dwordx4 v5, %[sw], s69 offen offset:0 lds\n\t" \
  "s_mov_b32 s85, 0x450800\n\t" \
  "s_load_dword s84, %[pw], s85\n\t" \
  "buffer_load_dwordx4 v6, %[sw], s69 offen offset:1024 lds\n\t" \
  "buffer_load_dwordx4 v7, %[sw], s69 offen offset:2048 lds\n\t" \
  "s_add_u32 s68, %[rx0], 148480\n\t" \
  "s_add_u32 s69, %[rw0], 3342336\n\t" \
  "s_add_u32 m0, %[xb], 27648\n\t" \
  "s_waitcnt vmcnt(18)\n\t" \
  "s_barrier\n\t" \
  "buffer_load_dwordx4 v2, %[sx], s68 offen offset:0 lds\n\t" \
  "s_mov_b32 s85, 0x451000\n\t" \
  "s_load_dword s84, %[pw], s85\n\t" \
  "buffer_load_dwordx4 v3, %[sx], s68 offen offset:1024 lds\n\t" \
  "s_mov_b32 s85, 0x480000\n\t" \
  "s_load_dword s84, %[pw], s85\n\t" \
  "s_mov_b64 exec, 0xffff\n\t" \
  "buffer_load_dwordx4 v4, %[sx], s68 offen offset:2048 lds\n\t" \
  "s_add_u32 m0, %[wb], 36864\n\t" \
  "s_mov_b64 exec, -1\n\t" \
  "buffer_load_dwordx4 v5, %[sw], s69 offen offset:0 lds\n\t" \
  "s_mov_b32 s85, 0x480800\n\t" \
  "s_load_dword s84, %[pw], s85\n\t" \
  "buffer_load_dwordx4 v6, %[sw], s69 offen offset:1024 lds\n\t" \
  "buffer_load_dwordx4 v7, %[sw], s69 offen offset:2048 lds\n\t" \
  "s_add_u32 s68, %[rx1], 1024\n\t" \
  "s_add_u32 s69, %[rw1], 3145728\n\t" \
  "s_add_u32 m0, %[xb], 36864\n\t" \
  "s_waitcnt vmcnt(15)\n\t" \
  "s_barrier\n\t" \
  "buffer_load_dwordx4 v0, %[sx], s68 offen offset:0 lds\n\t" \
  "s_mov_b32 s85, 0x481000\n\t" \
  "s_load_dword s84, %[pw], s85\n\t" \
  "buffer_load_dwordx4 v1, %[sx], s68 offen offset:1024 lds\n\t" \
  "s_mov_b32 s85, 0x4b0000\n\t" \
  "s_load_dword s84, %[pw], s85\n\t" \
  "s_mov_b64 exec, 0xffff\n\t" \
  "buffer_load_dwordx4 v4, %[sx], s68 offen offset:2048 lds\n\t" \
  "s_add_u32 m0, %[wb], 49152\n\t" \
  "s_mov_b64 exec, -1\n\t" \
  "buffer_load_dwordx4 v5, %[sw], s69 offen offset:0 lds\n\t" \
  "s_mov_b32 s85, 0x4b0800\n\t" \
  "s_load_dword s84, %[pw], s85\n\t" \
  "buffer_load_dwordx4 v6, %[sw], s69 offen offset:1024 lds\n\t" \
  "buffer_load_dwordx4 v7, %[sw], s69 offen offset:2048 lds\n\t" \
  "s_add_u32 s68, %[rx1], 148480\n\t" \
  "s_add_u32 s69, %[rw1], 3342336\n\t" \
  "s_add_u32 m0, %[xb], 46080\n\t" \
  "s_waitcnt vmcnt(18)\n\t" \
  "s_barrier\n\t" \
  "buffer_load_dwordx4 v2, %[sx], s68 offen offset:0 lds\n\t" \
  "s_mov_b32 s85, 0x4b1000\n\t" \
  "s_load_dword s84, %[pw], s85\n\t" \
  "buffer_load_dwordx4 v3, %[sx], s68 offen offset:1024 lds\n\t" \
  "s_mov_b32 s85, 0x4e0000\n\t" \
  "s_load_dword s84, %[pw], s85\n\t" \
  "s_mov_b64 exec, 0xffff\n\t" \
  "buffer_load_dwordx4 v4, %[sx], s68 offen offset:2048 lds\n\t" \
  "s_add_u32 m0, %[wb], 61440\n\t" \
  "s_mov_b64 exec, -1\n\t" \
  "buffer_load_dwordx4 v5, %[sw], s69 offen offset:0 lds\n\t" \
  "s_mov_b32 s85, 0x4e0800\n\t" \
  "s_load_dword s84, %[pw], s85\n\t" \
  "buffer_load_dwordx4 v6, %[sw], s69 offen offset:1024 lds\n\t" \
  "buffer_load_dwordx4 v7, %[sw], s69 offen offset:2048 lds\n\t" \
  "s_add_u32 s68, %[rx2], 1024\n\t" \
  "s_add_u32 s69, %[rw2], 3145728\n\t" \
  "s_add_u32 m0, %[xb], 0\n\t" \
  "s_waitcnt vmcnt(15)\n\t" \
  "s_barrier\n\t" \
  "buffer_load_dwordx4 v0, %[sx], s68 offen offset:0 lds\n\t" \
  "s_mov_b32 s85, 0x4e1000\n\t" \
  "s_load_dword s84, %[pw], s85\n\t" \
  "buffer_load_dwordx4 v1, %[sx], s68 offen offset:1024 lds\n\t" \
  "s_mov_b32 s85, 0x510000\n\t" \
  "s_load_dword s84, %[pw], s85\n\t" \
  "s_mov_b64 exec, 0xffff\n\t" \
  "buffer_load_dwordx4 v4, %[sx], s68 offen offset:2048 lds\n\t" \
  "s_add_u32 m0, %[wb], 0\n\t" \
  "s_mov_b64 exec, -1\n\t" \
  "buffer_load_dwordx4 v5, %[sw], s69 offen offset:0 lds\n\t" \
  "s_mov_b32 s85, 0x510800\n\t" \
  "s_load_dword s84, %[pw], s85\n\t" \
  "buffer_load_dwordx4 v6, %[sw], s69 offen offset:1024 lds\n\t" \
  "buffer_load_dwordx4 v7, %[sw], s69 offen offset:2048 lds\n\t" \
  "s_add_u32 s68, %[rx2], 148480\n\t" \
  "s_add_u32 s69, %[rw2], 3342336\n\t" \
  "s_add_u32 m0, %[xb], 9216\n\t" \
  "s_waitcnt vmcnt(18)\n\t" \
  "s_barrier\n\t" \
  "buffer_load_dwordx4 v2, %[sx], s68 offen offset:0 lds\n\t" \
  "s_mov_b32 s85, 0x511000\n\t" \
  "s_load_dword s84, %[pw], s85\n\t" \
  "buffer_load_dwordx4 v3, %[sx], s68 offen offset:1024 lds\n\t" \
  "s_mov_b32 s85, 0x540000\n\t" \
  "s_load_dword s84, %[pw], s85\n\t" \
  "s_mov_b64 exec, 0xffff\n\t" \
  "buffer_load_dwordx4 v4, %[sx], s68 offen offset:2048 lds\n\t" \
  "s_add_u32 m0, %[wb], 12288\n\t" \
  "s_mov_b64 exec, -1\n\t" \
  "buffer_load_dwordx4 v5, %[sw], s69 offen offset:0 lds\n\t" \
  "s_mov_b32 s85, 0x540800\n\t" \
  "s_load_dword s84, %[pw], s85\n\t" \
  "buffer_load_dwordx4 v6, %[sw], s69 offen offset:1024 lds\n\t" \
  "buffer_load_dwordx4 v7, %[sw], s69 offen offset:2048 lds\n\t" \
  "s_mov_b32 m0, %[tsk]\n\t" \
  "s_mov_b64 exec, %[tm]\n\t" \
  "global_load_lds_dword v12, %[tp]\n\t" \
  "s_mov_b64 exec, -1\n\t" \
  "s_add_u32 s68, %[rx3], 1024\n\t" \
  "s_add_u32 s69, %[rw3], 3145728\n\t" \
  "s_add_u32 m0, %[xb], 18432\n\t" \
  "s_waitcnt vmcnt(15)\n\t" \
  "s_barrier\n\t" \
  "buffer_load_dwordx4 v0, %[sx], s68 offen offset:0 lds\n\t" \
  "s_mov_b32 s85, 0x541000\n\t" \
  "s_load_dword s84, %[pw], s85\n\t" \
  "buffer_load_dwordx4 v1, %[sx], s68 offen offset:1024 lds\n\t" \
  "s_mov_b32 s85, 0x570000\n\t" \
  "s_load_dword s84, %[pw], s85\n\t" \
  "s_mov_b64 exec, 0xffff\n\t" \
  "buffer_load_dwordx4 v4, %[sx], s68 offen offset:2048 lds\n\t" \
  "s_add_u32 m0, %[wb], 24576\n\t" \
  "s_mov_b64 exec, -1\n\t" \
  "buffer_load_dwordx4 v5, %[sw], s69 offen offset:0 lds\n\t" \
  "s_mov_b32 s85, 0x570800\n\t" \
  "s_load_dword s84, %[pw], s85\n\t" \
  "buffer_load_dwordx4 v6, %[sw], s69 offen offset:1024 lds\n\t" \
  "buffer_load_dwordx4 v7, %[sw], s69 offen offset:2048 lds\n\t" \
  "s_add_u32 s68, %[rx3], 148480\n\t" \
  "s_add_u32 s69, %[rw3], 3342336\n\t" \
  "s_add_u32 m0, %[xb], 27648\n\t" \
  "s_waitcnt vmcnt(18)\n\t" \
  "s_barrier\n\t" \
  "buffer_load_dwordx4 v2, %[sx], s68 offen offset:0 lds\n\t" \
  "s_mov_b32 s85, 0x571000\n\t" \
  "s_load_dword s84, %[pw], s85\n\t" \
  "buffer_load_dwordx4 v3, %[sx], s68 offen offset:1024 lds\n\t" \
  "s_mov_b32 s85, 0x5a0000\n\t" \
  "s_load_dword s84, %[pw], s85\n\t" \
  "s_mov_b64 exec, 0xffff\n\t" \
  "buffer_load_dwordx4 v4, %[sx], s68 offen offset:2048 lds\n\t" \
  "s_add_u32 m0, %[wb], 36864\n\t" \
  "s_mov_b64 exec, -1\n\t" \
  "buffer_load_dwordx4 v5, %[sw], s69 offen offset:0 lds\n\t" \
  "s_mov_b32 s85, 0x5a0800\n\t" \
  "s_load_dword s84, %[pw], s85\n\t" \
  "buffer_load_dwordx4 v6, %[sw], s69 offen offset:1024 lds\n\t" \
  "buffer_load_dwordx4 v7, %[sw], s69 offen offset:2048 lds\n\t" \
  "s_add_u32 s68, %[rx0], 1536\n\t" \
  "s_add_u32 s69, %[rw0], 4718592\n\t" \
  "s_add_u32 m0, %[xb], 36864\n\t" \
  "s_waitcnt vmcnt(15)\n\t" \
  "s_barrier\n\t" \
  "buffer_load_dwordx4 v0, %[sx], s68 offen offset:0 lds\n\t" \
  "s_mov_b32 s85, 0x5a1000\n\t" \
  "s_load_dword s84, %[pw], s85\n\t" \
  "buffer_load_dwordx4 v1, %[sx], s68 offen offset:1024 lds\n\t" \
  "s_mov_b32 s85, 0x5d0000\n\t" \
  "s_load_dword s84, %[pw], s85\n\t" \
  "s_mov_b64 exec, 0xffff\n\t" \
  "buffer_load_dwordx4 v4, %[sx], s68 offen offset:2048 lds\n\t" \
  "s_add_u32 m0, %[wb], 49152\n\t" \
  "s_mov_b64 exec, -1\n\t" \
  "buffer_load_dwordx4 v5, %[sw], s69 offen offset:0 lds\n\t" \
  "s_mov_b32 s85, 0x5d0800\n\t" \
  "s_load_dword s84, %[pw], s85\n\t" \
  "buffer_load_dwordx4 v6, %[sw], s69 offen offset:1024 lds\n\t" \
  "buffer_load_dwordx4 v7, %[sw], s69 offen offset:2048 lds\n\t" \
  "s_add_u32 s68, %[rx0], 148992\n\t" \
  "s_add_u32 s69, %[rw0], 4915200\n\t" \
  "s_add_u32 m0, %[xb], 46080\n\t" \
  "s_waitcnt vmcnt(18)\n\t" \
  "s_barrier\n\t" \
  "buffer_load_dwordx4 v2, %[sx], s68 offen offset:0 lds\n\t" \
  "s_mov_b32 s85, 0x5d1000\n\t" \
  "s_load_dword s84, %[pw], s85\n\t" \
  "buffer_load_dwordx4 v3, %[sx], s68 offen offset:1024 lds\n\t" \
  "s_load_dword s84, %[po], 0x0\n\t" \
  "s_mov_b64 exec, 0xffff\n\t" \
  "buffer_load_dwordx4 v4, %[sx], s68 offen offset:2048 lds\n\t" \
  "s_add_u32 m0, %[wb], 61440\n\t" \
  "s_mov_b64 exec, -1\n\t" \
  "buffer_load_dwordx4 v5, %[sw], s69 offen offset:0 lds\n\t" \
  "s_load_dword s84, %[po], 0x40\n\t" \
  "buffer_load_dwordx4 v6, %[sw], s69 offen offset:1024 lds\n\t" \
  "buffer_load_dwordx4 v7, %[sw], s69 offen offset:2048 lds\n\t" \
  "s_add_u32 s68, %[rx1], 1536\n\t" \
  "s_add_u32 s69, %[rw1], 4718592\n\t" \
  "s_add_u32 m0, %[xb], 0\n\t" \
  "s_waitcnt vmcnt(15)\n\t" \
  "s_barrier\n\t" \
  "buffer_load_dwordx4 v0, %[sx], s68 offen offset:0 lds\n\t" \
  "s_load_dword s84, %[po], 0x80\n\t" \
  "buffer_load_dwordx4 v1, %[sx], s68 offen offset:1024 lds\n\t" \
  "s_load_dword s84, %[po], 0xc0\n\t" \
  "s_mov_b64 exec, 0xffff\n\t" \
  "buffer_load_dwordx4 v4, %[sx], s68 offen offset:2048 lds\n\t" \
  "s_add_u32 m0, %[wb], 0\n\t" \
  "s_mov_b64 exec, -1\n\t" \
  "buffer_load_dwordx4 v5, %[sw], s69 offen offset:0 lds\n\t" \
  "s_load_dword s84, %[po], 0x100\n\t" \
  "buffer_load_dwordx4 v6, %[sw], s69 offen offset:1024 lds\n\t" \
  "buffer_load_dwordx4 v7, %[sw], s69 offen offset:2048 lds\n\t" \
  "s_add_u32 s68, %[rx1], 148992\n\t" \
  "s_add_u32 s69, %[rw1], 4915200\n\t" \
  "s_add_u32 m0, %[xb], 9216\n\t" \
  "s_waitcnt vmcnt(18)\n\t" \
  "s_barrier\n\t" \
  "buffer_load_dwordx4 v2, %[sx], s68 offen offset:0 lds\n\t" \
  "s_load_dword s84, %[po], 0x140\n\t" \
  "buffer_load_dwordx4 v3, %[sx], s68 offen offset:1024 lds\n\t" \
  "s_load_dword s84, %[po], 0x180\n\t" \
  "s_mov_b64 exec, 0xffff\n\t" \
  "buffer_load_dwordx4 v4, %[sx], s68 offen offset:2048 lds\n\t" \
  "s_add_u32 m0, %[wb], 12288\n\t" \
  "s_mov_b64 exec, -1\n\t" \
  "buffer_load_dwordx4 v5, %[sw], s69 offen offset:0 lds\n\t" \
  "s_load_dword s84, %[po], 0x1c0\n\t" \
  "buffer_load_dwordx4 v6, %[sw], s69 offen offset:1024 lds\n\t" \
  "buffer_load_dwordx4 v7, %[sw], s69 offen offset:2048 lds\n\t" \
  "s_add_u32 s68, %[rx2], 1536\n\t" \
  "s_add_u32 s69, %[rw2], 4718592\n\t" \
  "s_add_u32 m0, %[xb], 18432\n\t" \
  "s_waitcnt vmcnt(15)\n\t" \
  "s_barrier\n\t" \
  "buffer_load_dwordx4 v0, %[sx], s68 offen offset:0 lds\n\t" \
  "s_load_dword s84, %[po], 0x200\n\t" \
  "buffer_load_dwordx4 v1, %[sx], s68 offen offset:1024 lds\n\t" \
  "s_load_dword s84, %[po], 0x240\n\t" \
  "s_mov_b64 exec, 0xffff\n\t" \
  "buffer_load_dwordx4 v4, %[sx], s68 offen offset:2048 lds\n\t" \
  "s_add_u32 m0, %[wb], 24576\n\t" \
  "s_mov_b64 exec, -1\n\t" \
  "buffer_load_dwordx4 v5, %[sw], s69 offen offset:0 lds\n\t" \
  "s_load_dword s84, %[po], 0x280\n\t" \
  "buffer_load_dwordx4 v6, %[sw], s69 offen offset:1024 lds\n\t" \
  "buffer_load_dwordx4 v7, %[sw], s69 offen offset:2048 lds\n\t" \
  "s_add_u32 s68, %[rx2], 148992\n\t" \
  "s_add_u32 s69, %[rw2], 4915200\n\t" \
  "s_add_u32 m0, %[xb], 27648\n\t" \
  "s_waitcnt vmcnt(18)\n\t" \
  "s_barrier\n\t" \
  "buffer_load_dwordx4 v2, %[sx], s68 offen offset:0 lds\n\t" \
  "s_load_dword s84, %[po], 0x2c0\n\t" \
  "buffer_load_dwordx4 v3, %[sx], s68 offen offset:1024 lds\n\t" \
  "s_load_dword s84, %[po], 0x300\n\t" \
  "s_mov_b64 exec, 0xffff\n\t" \
  "buffer_load_dwordx4 v4, %[sx], s68 offen offset:2048 lds\n\t" \
  "s_add_u32 m0, %[wb], 36864\n\t" \
  "s_mov_b64 exec, -1\n\t" \
  "buffer_load_dwordx4 v5, %[sw], s69 offen offset:0 lds\n\t" \
  "s_load_dword s84, %[po], 0x340\n\t" \
  "buffer_load_dwordx4 v6, %[sw], s69 offen offset:1024 lds\n\t" \
  "buffer_load_dwordx4 v7, %[sw], s69 offen offset:2048 lds\n\t" \
  "s_add_u32 s68, %[rx3], 1536\n\t" \
  "s_add_u32 s69, %[rw3], 4718592\n\t" \
  "s_add_u32 m0, %[xb], 36864\n\t" \
  "s_waitcnt vmcnt(15)\n\t" \
  "s_barrier\n\t" \
  "buffer_load_dwordx4 v0, %[sx], s68 offen offset:0 lds\n\t" \
  "s_load_dword s84, %[po], 0x380\n\t" \
  "buffer_load_dwordx4 v1, %[sx], s68 offen offset:1024 lds\n\t" \
  "s_load_dword s84, %[po], 0x3c0\n\t" \
  "s_mov_b64 exec, 0xffff\n\t" \
  "buffer_load_dwordx4 v4, %[sx], s68 offen offset:2048 lds\n\t" \
  "s_add_u32 m0, %[wb], 49152\n\t" \
  "s_mov_b64 exec, -1\n\t" \
  "buffer_load_dwordx4 v5, %[sw], s69 offen offset:0 lds\n\t" \
  "s_load_dword s84, %[po], 0x400\n\t" \
  "buffer_load_dwordx4 v6, %[sw], s69 offen offset:1024 lds\n\t" \
  "buffer_load_dwordx4 v7, %[sw], s69 offen offset:2048 lds\n\t" \
  "s_add_u32 s68, %[rx3], 148992\n\t" \
  "s_add_u32 s69, %[rw3], 4915200\n\t" \
  "s_add_u32 m0, %[xb], 46080\n\t" \
  "s_waitcnt vmcnt(18)\n\t" \
  "s_barrier\n\t" \
  "buffer_load_dwordx4 v2, %[sx], s68 offen offset:0 lds\n\t" \
  "s_load_dword s84, %[po], 0x440\n\t" \
  "buffer_load_dwordx4 v3, %[sx], s68 offen offset:1024 lds\n\t" \
  "s_load_dword s84, %[po], 0x480\n\t" \
  "s_mov_b64 exec, 0xffff\n\t" \
  "buffer_load_dwordx4 v4, %[sx], s68 offen offset:2048 lds\n\t" \
  "s_add_u32 m0, %[wb], 61440\n\t" \
  "s_mov_b64 exec, -1\n\t" \
  "buffer_load_dwordx4 v5, %[sw], s69 offen offset:0 lds\n\t" \
  "s_load_dword s84, %[po], 0x4c0\n\t" \
  "buffer_load_dwordx4 v6, %[sw], s69 offen offset:1024 lds\n\t" \
  "buffer_load_dwordx4 v7, %[sw], s69 offen offset:2048 lds\n\t" \
  "v_readlane_b32 s68, v9, 0\n\t" \
  "v_readlane_b32 s69, v9, 1\n\t" \
  "v_readlane_b32 s70, v9, 2\n\t" \
  "v_readlane_b32 s71, v9, 18\n\t" \
  "v_readlane_b32 s72, v9, 19\n\t" \
  "s_add_u32 m0, %[ob], 0\n\t" \
  "s_waitcnt vmcnt(15)\n\t" \
  "s_barrier\n\t" \
  "s_nop 2\n\t" \
  "buffer_load_dwordx4 v8, %[so], s68 offen lds\n\t" \
  "s_load_dword s84, %[po], 0x500\n\t" \
  "s_add_u32 m0, %[ob], 4096\n\t" \
  "s_nop 0\n\t" \
  "buffer_load_dwordx4 v8, %[so], s69 offen lds\n\t" \
  "s_load_dword s84, %[po], 0x540\n\t" \
  "s_add_u32 m0, %[ob], 8192\n\t" \
  "s_nop 0\n\t" \
  "buffer_load_dwordx4 v8, %[so], s70 offen lds\n\t" \
  "s_load_dword s84, %[po], 0x580\n\t" \
  "s_add_u32 m0, %[ob], 73728\n\t" \
  "s_nop 0\n\t" \
  "buffer_load_dwordx4 v8, %[so], s71 offen lds\n\t" \
  "s_add_u32 m0, %[ob], 77824\n\t" \
  "s_nop 0\n\t" \
  "buffer_load_dwordx4 v8, %[so], s72 offen lds\n\t" \
  "v_readlane_b32 s68, v9, 3\n\t" \
  "v_readlane_b32 s69, v9, 4\n\t" \
  "v_readlane_b32 s70, v9, 5\n\t" \
  "v_readlane_b32 s71, v9, 20\n\t" \
  "v_readlane_b32 s72, v9, 21\n\t" \
  "s_add_u32 m0, %[ob], 12288\n\t" \
  "s_waitcnt vmcnt(17)\n\t" \
  "s_barrier\n\t" \
  "s_nop 2\n\t" \
  "buffer_load_dwordx4 v8, %[so], s68 offen lds\n\t" \
  "s_load_dword s84, %[po], 0x5c0\n\t" \
  "s_add_u32 m0, %[ob], 16384\n\t" \
  "s_nop 0\n\t" \
  "buffer_load_dwordx4 v8, %[so], s69 offen lds\n\t" \
  "s_load_dword s84, %[po], 0x600\n\t" \
  "s_add_u32 m0, %[ob], 20480\n\t" \
  "s_nop 0\n\t" \
  "buffer_load_dwordx4 v8, %[so], s70 offen lds\n\t" \
  "s_load_dword s84, %[po], 0x640\n\t" \
  "s_add_u32 m0, %[ob], 81920\n\t" \
  "s_nop 0\n\t" \
  "buffer_load_dwordx4 v8, %[so], s71 offen lds\n\t" \
  "s_add_u32 m0, %[ob], 86016\n\t" \
  "s_nop 0\n\t" \
  "buffer_load_dwordx4 v8, %[so], s72 offen lds\n\t" \
  "v_readlane_b32 s68, v9, 6\n\t" \
  "v_readlane_b32 s69, v9, 7\n\t" \
  "v_readlane_b32 s70, v9, 8\n\t" \
  "v_readlane_b32 s71, v9, 22\n\t" \
  "v_readlane_b32 s72, v9, 23\n\t" \
  "v_readlane_b32 s73, v9, 24\n\t" \
  "s_add_u32 m0, %[ob], 24576\n\t" \
  "s_waitcnt vmcnt(13)\n\t" \
  "s_barrier\n\t" \
  "s_nop 2\n\t" \
  "buffer_load_dwordx4 v8, %[so], s68 offen lds\n\t" \
  "s_load_dword s84, %[po], 0x680\n\t" \
  "s_add_u32 m0, %[ob], 28672\n\t" \
  "s_nop 0\n\t" \
  "buffer_load_dwordx4 v8, %[so], s69 offen lds\n\t" \
  "s_load_dword s84, %[po], 0x6c0\n\t" \
  "s_add_u32 m0, %[ob], 32768\n\t" \
  "s_nop 0\n\t" \
  "buffer_load_dwordx4 v8, %[so], s70 offen lds\n\t" \
  "s_load_dword s84, %[po], 0x700\n\t" \
  "s_add_u32 m0, %[ob], 90112\n\t" \
  "s_nop 0\n\t" \
  "buffer_load_dwordx4 v8, %[so], s71 offen lds\n\t" \
  "s_add_u32 m0, %[ob], 94208\n\t" \
  "s_nop 0\n\t" \
  "buffer_load_dwordx4 v8, %[so], s72 offen lds\n\t" \
  "s_add_u32 m0, %[ob], 98304\n\t" \
  "s_nop 0\n\t" \
  "buffer_load_dwordx4 v8, %[so], s73 offen lds\n\t" \
  "v_readlane_b32 s68, v9, 9\n\t" \
  "v_readlane_b32 s69, v9, 10\n\t" \
  "v_readlane_b32 s70, v9, 11\n\t" \
  "v_readlane_b32 s71, v9, 25\n\t" \
  "v_readlane_b32 s72, v9, 26\n\t" \
  "s_add_u32 m0, %[ob], 36864\n\t" \
  "s_waitcnt vmcnt(16)\n\t" \
  "s_barrier\n\t" \
  "s_nop 2\n\t" \
  "buffer_load_dwordx4 v8, %[so], s68 offen lds\n\t" \
  "s_load_dword s84, %[po], 0x740\n\t" \
  "s_add_u32 m0, %[ob], 40960\n\t" \
  "s_nop 0\n\t" \
  "buffer_load_dwordx4 v8, %[so], s69 offen lds\n\t" \
  "s_load_dword s84, %[po], 0x780\n\t" \
  "s_add_u32 m0, %[ob], 45056\n\t" \
  "s_nop 0\n\t" \
  "buffer_load_dwordx4 v8, %[so], s70 offen lds\n\t" \
  "s_load_dword s84, %[po], 0x7c0\n\t" \
  "s_add_u32 m0, %[ob], 102400\n\t" \
  "s_nop 0\n\t" \
  "buffer_load_dwordx4 v8, %[so], s71 offen lds\n\t" \
  "s_add_u32 m0, %[ob], 106496\n\t" \
  "s_nop 0\n\t" \
  "buffer_load_dwordx4 v8, %[so], s72 offen lds\n\t" \
  "v_readlane_b32 s68, v9, 12\n\t" \
  "v_readlane_b32 s69, v9, 13\n\t" \
  "v_readlane_b32 s70, v9, 14\n\t" \
  "v_readlane_b32 s71, v9, 27\n\t" \
  "v_readlane_b32 s72, v9, 28\n\t" \
  "v_readlane_b32 s73, v9, 29\n\t" \
  "s_add_u32 m0, %[ob], 49152\n\t" \
  "s_nop 3\n\t" \
  "s_barrier\n\t" \
  "s_nop 2\n\t" \
  "buffer_load_dwordx4 v8, %[so], s68 offen lds\n\t" \
  "s_add_u32 m0, %[ob], 53248\n\t" \
  "s_nop 0\n\t" \
  "buffer_load_dwordx4 v8, %[so], s69 offen lds\n\t" \
  "s_add_u32 m0, %[ob], 57344\n\t" \
  "s_nop 0\n\t" \
  "buffer_load_dwordx4 v8, %[so], s70 offen lds\n\t" \
  "s_add_u32 m0, %[ob], 110592\n\t" \
  "s_nop 0\n\t" \
  "buffer_load_dwordx4 v8, %[so], s71 offen lds\n\t" \
  "s_add_u32 m0, %[ob], 114688\n\t" \
  "s_nop 0\n\t" \
  "buffer_load_dwordx4 v8, %[so], s72 offen lds\n\t" \
  "s_add_u32 m0, %[ob], 118784\n\t" \
  "s_nop 0\n\t" \
  "buffer_load_dwordx4 v8, %[so], s73 offen lds\n\t" \
  "v_readlane_b32 s68, v9, 15\n\t" \
  "v_readlane_b32 s69, v9, 16\n\t" \
  "v_readlane_b32 s70, v9, 17\n\t" \
  "v_readlane_b32 s71, v9, 30\n\t" \
  "v_readlane_b32 s72, v9, 31\n\t" \
  "s_add_u32 m0, %[ob], 61440\n\t" \
  "LS_%=:\n\t" \
  "ds_read_b32 v11, v10\n\t" \
  "s_addk_i32 %[cn], 1\n\t" \
  "s_cmp_gt_u32 %[cn], 0x400000\n\t" \
  "s_cbranch_scc1 LX_%=\n\t" \
  "s_waitcnt lgkmcnt(0)\n\t" \
  "v_cmp_eq_u32 vcc, 0, v11\n\t" \
  "s_cbranch_vccz LX_%=\n\t" \
  "s_sleep 1\n\t" \
  "s_branch LS_%=\n\t" \
  "LX_%=:\n\t" \
  "s_nop 2\n\t" \
  "buffer_load_dwordx4 v8, %[so], s68 offen lds\n\t" \
  "s_add_u32 m0, %[ob], 65536\n\t" \
  "s_nop 0\n\t" \
  "buffer_load_dwordx4 v8, %[so], s69 offen lds\n\t" \
  "s_add_u32 m0, %[ob], 69632\n\t" \
  "s_nop 0\n\t" \
  "buffer_load_dwordx4 v8, %[so], s70 offen lds\n\t" \
  "s_add_u32 m0, %[ob], 122880\n\t" \
  "s_nop 0\n\t" \
  "buffer_load_dwordx4 v8, %[so], s71 offen lds\n\t" \
  "s_add_u32 m0, %[ob], 126976\n\t" \
  "s_nop 0\n\t" \
  "buffer_load_dwordx4 v8, %[so], s72 offen lds\n\t" \
  "s_waitcnt vmcnt(0) lgkmcnt(0)\n\t"
#define LD_CLOB "v11", "s68", "s69", "s70", "s71", "s72", "s73", "s74", "s75", "s76", "s77", "s78", "s79", "s80", "s81", "s82", "s83", "s84", "s85"

#define ENDW
#define TS(i)
#define TSS(i)
#define TOUT(n)

#define SPIN(cond,slp) do{ int sp_=0; while(cond){ __builtin_amdgcn_s_sleep(slp); if(++sp_>(1<<22)) break; } }while(0)
__device__ __forceinline__ unsigned rd1(const char* p){ return __builtin_amdgcn_readfirstlane(*(volatile LDS_AS unsigned*)(LDS_AS char*)p); }
__device__ __forceinline__ unsigned lds_inc(const char* p, int l){ unsigned r=0; if(l==0) r=__hip_atomic_fetch_add((LDS_AS unsigned*)(LDS_AS char*)p, 1u, __ATOMIC_RELAXED, __HIP_MEMORY_SCOPE_WORKGROUP); return __builtin_amdgcn_readfirstlane(r); }
__device__ __forceinline__ void lds_add(unsigned adr){ asm volatile("s_mov_b64 exec, 1\n\tds_add_u32 %0, %1\n\ts_mov_b64 exec, -1" :: "v"(adr), "v"(1u) : "memory"); }
__device__ __forceinline__ void bars(int n){
  asm volatile("BL_%=:\n\ts_barrier\n\ts_sub_u32 %0, %0, 1\n\ts_cmp_lg_u32 %0, 0\n\ts_cbranch_scc1 BL_%=" : "+s"(n) :: "memory", "scc");
}
#define barc(rem) barc_(rem)
#define CTRA
#define CTRP
__device__ __forceinline__ void barc_(unsigned& rem){
  asm volatile("s_cmp_eq_u32 %0, 0\n\ts_cbranch_scc1 BC_%=\n\ts_barrier\n\ts_sub_u32 %0, %0, 1\n\tBC_%=:" : "+s"(rem) :: "memory", "scc");
}
template<int SLP> __device__ __forceinline__ void wait_flag_b(const unsigned* p, unsigned voff, unsigned tag, unsigned& rem CTRA){
  int spins=0;
  while(true){
    unsigned v; asm volatile("global_load_dword %0, %1, %2 sc1" : "=v"(v) : "v"(voff), "s"(p) : "memory");
    barc(rem); if(PB2) barc(rem);
    asm volatile("s_waitcnt vmcnt(0)" : "+v"(v) :: "memory");
    if(__all(v==tag) || ++spins>(1<<21)) break;
    if(rem==0) __builtin_amdgcn_s_sleep(SLP);
  }
}
__device__ __forceinline__ void stout(void* sbase, unsigned voff, u32x4 v){
  st16cs(sbase,voff,v);
}

typedef unsigned u32x8 __attribute__((ext_vector_type(8)));
typedef unsigned u32x16 __attribute__((ext_vector_type(16)));
__device__ __forceinline__ char* ptr64(unsigned lo, unsigned hi){ return (char*)(((unsigned long long)hi<<32)|lo); }
__device__ __forceinline__ u32x4 srd(const char* p, unsigned bytes){ return u32x4{(unsigned)(size_t)p,(unsigned)((size_t)p>>32)&0xffffu,bytes,0x20000u}; }
constexpr size_t WS_O=0, WS_KV=(size_t)S*DM*2, WS_Y=(size_t)S*DM*4, WS_FL=(size_t)S*DM*12;

__global__ __launch_bounds__(768,3) void fused_kernel(const u16* x_, const u16* wqkv_, const u16* wo_, char* ws_, u16* out_,
    const u16* kin_, const u16* vin_, const u16* shk_, const u16* hk_, const u16* qin_, const u16* sD_)
{
  __shared__ __attribute__((aligned(128))) char smem[ALL_LDS];
  TS(0);
  const auto ka=__builtin_amdgcn_kernarg_segment_ptr();
  int w8;
  {
    unsigned t=threadIdx.x;
    asm volatile(
      "s_getpc_b64 s[72:73]\n\tv_readfirstlane_b32 %[w8], %[t]\n\ts_lshr_b32 s74, %[b], 3\n\ts_mul_i32 s74, s74, 12\n\ts_lshr_b32 %[w8], %[w8], 6\n\t"
      "s_add_u32 s74, s74, %[w8]\n\ts_min_u32 s74, s74, %[cl]\n\ts_lshl_b32 s74, s74, 7\n\ts_mov_b32 m0, %[sk]\n\tv_mov_b32 v1, s74\n\ts_mov_b64 exec, 1\n\tglobal_load_lds_dword v1, s[72:73]\n\ts_mov_b64 exec, -1\n\t"
      "s_bitcmp1_b32 %[w8], 2\n\ts_cbranch_scc1 EX_%=\n\t"
      "s_load_dwordx4 s[68:71], %[ka], 0x0\n\tv_and_b32 v1, 0xff, %[t]\n\ts_bitcmp1_b32 %[w8], 3\n\ts_cbranch_scc1 EC_%=\n\t"
      "s_mul_i32 s74, %[b], %[nw]\n\tv_cmp_gt_u32 vcc, %[nw], v1\n\tv_add_lshl_u32 v1, v1, s74, 7\n\ts_and_b64 exec, vcc, exec\n\ts_cbranch_execz EE_%=\n\ts_waitcnt lgkmcnt(0)\n\tglobal_load_lds_dword v1, s[70:71]\n\ts_branch EE_%=\n\t"
      "EC_%=:\n\tv_cmp_gt_u32 vcc, %[nx], v1\n\tv_and_b32 v2, 7, v1\n\tv_lshlrev_b32 v1, 4, v1\n\ts_lshl_b32 s74, %[b], 14\n\tv_and_b32 v1, 0xf80, v1\n\tv_lshl_add_u32 v1, v2, 11, v1\n\tv_add_u32 v1, s74, v1\n\t"
      "s_and_b64 exec, vcc, exec\n\ts_cbranch_execz EE_%=\n\ts_waitcnt lgkmcnt(0)\n\tglobal_load_lds_dword v1, s[68:69]\n\t"
      "EE_%=:\n\ts_mov_b64 exec, -1\n\ts_waitcnt lgkmcnt(0)\n\tEX_%=:"
      : [w8]"=&s"(w8) : [ka]"s"(ka), [t]"v"(t), [b]"s"(blockIdx.x), [cl]"n"(CODEL-1), [sk]"s"(lds_addr(smem)+SINK+256), [nw]"s"(12*TPW), [nx]"s"(8*TPX)
      : "memory", "scc", "vcc", "v1", "v2", "s68", "s69", "s70", "s71", "s72", "s73", "s74");
  }
  #define ROLE u32x8 a0; int bid=blockIdx.x; asm volatile("s_load_dwordx8 %0, %2, 0x0" : "=&s"(a0), "+s"(bid) : "s"(ka)); \
    const int w=w8&3, xcd=bid&7, idx=bid>>3, rb=(xcd>>1)*4+(idx&3), hp=(xcd&1)*8+(idx>>2), s0=rb*128; (void)s0; (void)hp;
  #define LANEVARS int l; asm volatile("v_mbcnt_lo_u32_b32 %0, -1, 0\n\tv_mbcnt_hi_u32_b32 %0, -1, %0" : "=v"(l)); const int g=l>>4, fr=l&15, tq=(l&15)>>2, tp=l&3; (void)g; (void)fr; (void)tq; (void)tp;
  #define ARGS0 asm volatile("s_waitcnt lgkmcnt(0)" : "+s"(a0)); const char* x=ptr64(a0[0],a0[1]); const char* wqkv=ptr64(a0[2],a0[3]); const char* wo=ptr64(a0[4],a0[5]); char* ws=ptr64(a0[6],a0[7]); \
    unsigned* flags=(unsigned*)(ws+WS_FL); const int hot0=(3-(int)(a0[2]>>7))&7; (void)x; (void)wqkv; (void)wo; (void)flags; (void)hot0;
  #define ORA ((unsigned)(hp-hot0-1)&7u)
  #define ORD(c) ((((ORA==7u? ((c)==0u||(c)==15u? 7u : ((c)-1u)>>1) : ((c)<14u? (ORA+((c)>>1))%7u : 7u))+hot0+1)&7u)+8u*(((hp>>3)^(c))&1u))
  const unsigned sb0=__builtin_amdgcn_readfirstlane(lds_addr(smem));
  const char* syn=smem+SYN;

  if(__builtin_expect((w8>>2)==1,1)){
    ROLE
    LANEVARS
    __builtin_amdgcn_s_setprio(LPR);
    unsigned ltr=0, ltr2=0, ltr3=0; (void)ltr; (void)ltr2; (void)ltr3;
    const int xr=18*w+(l>>3);
    const unsigned ch=(unsigned)(((l&7)^(xr&7))*16);
    unsigned xo=(unsigned)(xr*2048)+ch;
    unsigned xoA=(s0==0 && xr<16)? 16*2048+ch : xo, xoB=((s0==0 && xr+8<16)? 8*2048+ch : xo)+15360;
    unsigned xo1=xo+15360, xo2=xo+30720;
    unsigned wof[3];
    #pragma unroll
    for(int i=0;i<3;i++){ const int P=3*w+i, wr=8*(P&3)+(l>>3); wof[i]=(unsigned)(wr*6144+(P>>2)*2048-1024*i+((((l&7)>>1)^wswz(wr))*32)+(l&1)*16); }
    unsigned xl=sb0+X_OFF+w*2304, wl=sb0+w*3072, xd=(unsigned)(s0-16)*2048u, wd=hp*128, hq=hp&7, rq=rb&3;
    asm volatile("" : "+{v0}"(xoA), "+{v1}"(xoB), "+{v2}"(xo), "+{v3}"(xo1), "+{v4}"(xo2), "+{v5}"(wof[0]), "+{v6}"(wof[1]), "+{v7}"(wof[2]), "+s"(xl), "+s"(wl), "+s"(xd), "+s"(wd), "+s"(hq), "+s"(rq));
    TSS(2);
    ARGS0
    const u32x4 sx=srd(x+(int)xd,144u*2048u), sw=srd(wqkv+wd,6291456u-wd);
    const int jj=(ROT && (int)hq==hot0)? rq : 0;
    TSS(3);
    asm volatile(LD_ASM1
      :: "{v0}"(xoA), "{v1}"(xoB), "{v2}"(xo), "{v3}"(xo1), "{v4}"(xo2), "{v5}"(wof[0]), "{v6}"(wof[1]), "{v7}"(wof[2]),
        [sx]"s"(sx), [sw]"s"(sw), [xb]"s"(xl), [wb]"s"(wl), [rx0]"s"(jj*128), [rw0]"s"(jj*393216)
      : "memory", "scc", "s68");
    {
      const unsigned wof2=(unsigned)(w*16384+(l>>3)*2048+((((l&7)>>1)^((l>>4)&3))*32)+(l&1)*16);
      const unsigned tab=ORD((unsigned)(l>>1)&15u)*131072u+(l&1)*65536;
      unsigned long long tm; asm("s_cmp_eq_u32 %1, 0\n\ts_cselect_b64 %0, %2, 0" : "=s"(tm) : "s"(w), "s"(WTL==2? 0xffffffffull : 0ull) : "scc");
      unsigned cn=0;
      asm volatile(LD_ASM2
        : [cn]"+s"(cn)
        : "{v0}"(xoA), "{v1}"(xoB), "{v2}"(xo), "{v3}"(xo1), "{v4}"(xo2), "{v5}"(wof[0]), "{v6}"(wof[1]), "{v7}"(wof[2]), "{v8}"(wof2), "{v9}"(tab),
          "{v10}"(sb0+SYN+32+(l&3)*4), "{v12}"((l&31)*2048),
          [sx]"s"(sx), [sw]"s"(sw), [so]"s"(srd(wo+wd,2097152u-wd)), [xb]"s"(xl), [wb]"s"(wl), [ob]"s"(sb0+w*1024),
          [rx0]"s"(jj*128), [rx1]"s"(((jj+1)&3)*128), [rx2]"s"(((jj+2)&3)*128), [rx3]"s"(((jj+3)&3)*128),
          [rw0]"s"(jj*393216), [rw1]"s"(((jj+1)&3)*393216), [rw2]"s"(((jj+2)&3)*393216), [rw3]"s"(((jj+3)&3)*393216),
          [pw]"s"(wqkv+idx*6144+(xcd&1)*1024+(xcd>>1)*256+w*64), [px]"s"(x+(xcd*256+idx*8+w*2)*2048), [po]"s"(wo+(bid*4+w)*2048),
          [tsk]"s"(sb0+SINK+256), [tm]"s"(tm), [tp]"s"(wo+idx*65536+(xcd&1)*1024+((3-(int)(a0[4]>>7))&7)*128)
        : "memory", "scc", "vcc", LD_CLOB);
    }
    const unsigned tag=rd1(syn+64)+1u;
    lds_add(sb0+SYN+12);
    TS(2);
    if(w==3){
      const unsigned* fp=flags+F_O+(rb*16+hp)*16; const unsigned fo=ORD((unsigned)l&15u)*4u;
      const unsigned madr=sb0+(l==0? SYN+20 : SINK+l*4);
      SPIN(rd1(syn+12)<4u || rd1(syn+16)<4u,1);
      PUB(madr,1u);
      unsigned rdy=1; int spins=0;
      while(rdy!=0xFFFFu){
        unsigned v;
        asm volatile("s_mov_b64 exec, 0xffff\n\tglobal_atomic_or %0, %1, %3, %2 sc0 sc1\n\ts_mov_b64 exec, -1\n\ts_waitcnt vmcnt(0)" : "=&v"(v) : "v"(fo), "s"(fp), "v"(0u) : "memory");
        unsigned r=((unsigned)__ballot(v==tag)&0xFFFFu)|1u;
        if(++spins>(1<<21)) r=0xFFFFu;
        if(r!=rdy){ PUB(madr,(unsigned)__builtin_ctz(~r)); rdy=r; }
        else __builtin_amdgcn_s_sleep(OSLP);
      }
      TS(3);
      if(bid==0){
        int sp=0;
        while(true){
          u32x4 v; asm volatile("global_load_dwordx4 %0, %1, %2 sc1\n\ts_waitcnt vmcnt(0)" : "=&v"(v) : "v"(l*16), "s"(flags+F_Y) : "memory");
          if(__all(v[0]==tag && v[1]==tag && v[2]==tag && v[3]==tag) || ++sp>(1<<20)) break;
          __builtin_amdgcn_s_sleep(12);
        }
        if(l==0) stflag(flags, tag);
      }
    } else { TS(3); }
    TOUT(4);
    return;
  }

  if(w8<4){
    ROLE
    u32x2 a1; asm volatile("s_load_dwordx2 %0, %1, 0x20" : "=&s"(a1) : "s"(ka));
    __builtin_amdgcn_s_setprio(GPR);
    const int h=w&1, r=w>>1;
    f32x16 e0, e1;
    LANEVARS
    TSS(3);
    ARGS0
    asm volatile("" : "+s"(a1));
    if(TWO && w==2) touch(wo+bid*8192, l*128, sb0+SINK+512);
    TSS(4);
    {
      unsigned lx0=sb0+X_OFF+fr*128+((g^(fr&7))*16), lx1=sb0+X_OFF+fr*128+(((4+g)^(fr&7))*16);
      const int sw=wswz(8*g+tq);
      unsigned lw0=sb0+(8*g+tq)*128+(unsigned)((((2*h)^sw)*32)+8*tp), lw1=sb0+(8*g+tq)*128+(unsigned)((((2*h+1)^sw)*32)+8*tp);
      char* ot=ws+WS_O+(rb*16+hp)*16384+r*8192+h*2048;
      unsigned it; unsigned long long rm; asm("s_cmp_lg_u32 %1, 0\n\ts_cselect_b64 %0, -1, 0" : "=s"(rm) : "s"(r) : "scc");
      asm volatile(N2_ASM
        : [it]"=&s"(it), "+{v56}"(lx0), "+{v57}"(lx1), "+{v58}"(lw0), "+{v59}"(lw1)
        : "{v62}"(l*16), "{v63}"(sb0+EX_OFF+h*3072+l*16), [r]"s"(r), [ot]"s"(ot), [ou]"s"(ot+4096), [fa]"s"(sb0+SYN+32+w*4), [fp]"s"(sb0+SYN+32+(w^2)*4),
          [vo]"s"(2048-1024*r), [rm]"s"(rm), [z]"s"(rb|r)
        : "memory", "scc", "vcc", N2_CLOB);
    }
    TS(3);
    unsigned tb;
    {
      LANEVARS
      const unsigned tag=rd1(syn+64)+1u;
      const unsigned ra=sb0+(4*g+tq)*128+8*tp; const int sw=(2*g+(tq>>1))&3;
      unsigned b0=ra+((0^sw)*32), b1=ra+((1^sw)*32), b2=ra+((2^sw)*32), b3=ra+((3^sw)*32), vo=(unsigned)(w*4096+l*16);
      tb=ORD((unsigned)l&15u)*16384u;
      asm volatile("" : "+{v160}"(b0), "+{v161}"(b1), "+{v162}"(b2), "+{v163}"(b3), "+{v164}"(vo), "+{v167}"(tb));
      WAITV(0);
      if(lds_inc(syn+16,l)==3u){ if(l<16) stflag(flags+F_O+(rb*16+l)*16+hp, tag); }
      TS(4);
      __builtin_amdgcn_s_setprio(G2P);
      unsigned c=0, iss=0, th, q0, q1, cn=0;
      asm volatile(G3_ASM
        : "={v[128:143]}"(e0), "={v[144:159]}"(e1), [c]"+s"(c), [iss]"+s"(iss), [th]"=&s"(th), [t0]"=&s"(q0), [t1]"=&s"(q1), [cn]"+s"(cn),
          "+{v160}"(b0), "+{v161}"(b1), "+{v162}"(b2), "+{v163}"(b3), "+{v167}"(tb)
        : "{v164}"(vo), "{v165}"(sb0+SYN+20), [go]"s"(60+w*4), [ob]"s"(srd(ws+WS_O+rb*262144,262144u))
        : "memory", "scc", "vcc", G3_CLOB);
    }
    {
      LANEVARS
      char* sg=smem+EX_OFF+w*2048;
      char* op=ptr64(a1[0],a1[1])+(s0+w*32)*4096+2048+hp*128; const unsigned oo=(unsigned)((l>>3)*4096+(l&7)*16);
      u32x4 ov[4];
      #pragma unroll
      for(int t=0;t<2;t++){
        #pragma unroll
        for(int j=0;j<4;j++){
          #define E2(i) (t? e1[(i)&15] : e0[(i)&15])
          *(volatile LDS_AS u32x2*)(LDS_AS char*)(sg+fr*128+(((2*j+(g>>1))^(fr&7))*16)+(g&1)*8)=u32x2{pack2(E2(4*j),E2(4*j+1)),pack2(E2(4*j+2),E2(4*j+3))};
        }
        #pragma unroll
        for(int i=0;i<2;i++) ov[2*t+i]=*(volatile LDS_AS u32x4*)(LDS_AS char*)(sg+(i*8+(l>>3))*128+(((l&7)^(l>>3))*16));
      }
      #pragma unroll
      for(int i=0;i<4;i++) stout(op+i*32768, oo, ov[i]);
      ENDW TS(6); TOUT(7);
    }
    return;
  }

  {
    ROLE
    u32x8 a1; u32x4 a2; u32x2 a3;
    asm volatile("s_load_dwordx8 %0, %3, 0x20\n\ts_load_dwordx4 %1, %3, 0x40\n\ts_load_dwordx2 %2, %3, 0x50" : "=&s"(a1), "=&s"(a2), "=&s"(a3) : "s"(ka));
    char* cr=smem+C_OFF+2048+w*4096;
    const unsigned crs=sb0+C_OFF+2048+w*4096;
    const int sblk=bid>>3, td0=(bid&7)*128+(w&1)*64;
    const int d=(bid>>4)*64+(((bid>>3)&1)*4+w)*8+(bid&7);
    const int kv0=KV0+(sblk&KVS);
    static_assert(KV0+KVS+PP_NB<=33, "barrier budget");
    unsigned tagv, ctr; f32x8 RB;
    const int fd0=(bid&15)*64, fs0=(bid>>4)*128+w*32;
    u32x4 qv[4];
    f32x16 A0,A1,A2,A3;
    const int rw=sblk*64+(w>>1)*32, j=rw>>5;
    unsigned k0,k1,k2,ko,ta,se,so,ha,l16,rb0[2];
    {
      LANEVARS
      if(w==0 && l<25 && l!=16) ((unsigned*)(smem+SYN))[l]=0u;
      if(l<32) *(u32x4*)(smem+C_OFF+w*512+l*16)=u32x4{0,0,0,0};
      asm volatile("" ::: "memory");
      __builtin_amdgcn_s_setprio(CPR);
      const int dg=l&7, rg=l>>3;
      ko=(unsigned)(rg*8192+dg*16);
      const bool top=(rw==0 && rg==0);
      k0=top? ko+6144:ko; k1=top? ko+6144:ko+2048; k2=top? ko+6144:ko+4096;
      ta=sb0+EX_OFF+w*1024+(w&1)*512+dg*64; se=(l&8)? 0x03020706u : 0x05040100u; ha=crs+(32*g-4*tq+4*tp)*2; l16=l*16;
      so=(unsigned)(rg*32768+((j>>2)&3)*128+((dg&4)|((dg+j)&3))*16);
      #pragma unroll
      for(int k=0;k<2;k++){ const int q=fr-1-2*g+k; rb0[k]=sb0+C_OFF+(4+(q>>1))*256+((q>>3)&3)*64+((w+(q>>1))&3)*16+(q&1)*8; }
      asm volatile("" : "+{v152}"(k0), "+{v153}"(k1), "+{v154}"(k2), "+{v155}"(ko), "+{v156}"(ta), "+{v157}"(se), "+{v159}"(so), "+{v163}"(l16), "+{v165}"(ha), "+{v166}"(rb0[0]), "+{v167}"(rb0[1]));
    }
    TSS(3);
    ARGS0
    asm volatile("" : "+s"(a1), "+s"(a2), "+s"(a3));
    char* out=ptr64(a1[0],a1[1]); const char* qp=ptr64(a2[2],a2[3])+(fs0*DM+fd0)*2;
    {
      const char* kb=ptr64(a1[2],a1[3])+(rw*DM+td0)*2; const char* vb=ptr64(a1[4],a1[5])+(rw*DM+td0)*2;
      unsigned long long zm; asm("s_cmp_eq_u32 %1, 0\n\ts_cselect_b64 %0, 0xff, 0" : "=s"(zm) : "s"(rw) : "scc");
      unsigned t0;
      TSS(5);
      asm volatile(PP_ASM
        : [t0]"=&s"(t0), "=&{v164}"(ctr), "+{v159}"(so), "=&{v160}"(tagv), "=&{v[24:31]}"(RB), "=&{v[44:47]}"(qv[0]), "=&{v[48:51]}"(qv[1]), "=&{v[52:55]}"(qv[2]), "=&{v[56:59]}"(qv[3]),
          "=&{v[64:79]}"(A0), "=&{v[80:95]}"(A1), "=&{v[96:111]}"(A2), "=&{v[112:127]}"(A3)
        : "{v152}"(k0), "{v153}"(k1), "{v154}"(k2), "{v155}"(ko), "{v156}"(ta), "{v157}"(se), "{v166}"(rb0[0]), "{v167}"(rb0[1]), "{v165}"(ha), "{v163}"(l16),
          [qb0]"s"(qp), [qb2]"s"(qp+4096), [mk]"s"(0x180u), [em]"s"(1ull|(512ull<<w)), [hd]"s"(crs),
          [kb0]"s"(kb-6144), [kb3]"s"(kb), [kb5]"s"(kb+4096), [vb0]"s"(vb), [vb2]"s"(vb+4096), [sb]"s"(ws+WS_KV+td0*4096+j*512),
          [n0]"s"(kv0), [ff]"s"(flags+F_PP+(td0>>6)*64+j), [zm]"s"(zm), [shd]"s"(sb0+EX_OFF+w*1024), [tgd]"s"(sb0+SYN+64), [shp]"s"(ptr64(a1[6],a1[7])+(bid&7)*1024), [fl]"s"(flags),
          [hp]"s"(ptr64(a2[0],a2[1])+d*4096), [dp]"s"(ptr64(a3[0],a3[1])+d*2)
        : "memory", "scc", PP_CLOB);
    }
    LANEVARS
    const unsigned tag=__builtin_amdgcn_readfirstlane(tagv)+1u;
    unsigned rem=33-PP_NB-kv0;
    barc(rem);
    const unsigned* pf=flags+F_PP+(d>>6)*64;
    const char* kvp=ws+WS_KV+((bid>>4)*8+(bid&7))*32768+((bid>>3)&1)*64+w*8192;
    const unsigned kvo=(unsigned)((l>>2)*128+(l&3)*16);
    int bidx=((l>=16)? l-16 : (l==0? 63 : l+47))*4;
    asm volatile("" : "+{v32}"(bidx));
    __builtin_amdgcn_s_setprio(3);
    while(rem>PREM) barc(rem);
    wait_flag_b<8>(pf, l*4, tag, rem CTRP);
    #pragma unroll
    for(int i=0;i<4;i++){ if(i%KTH==0 && i) barc(rem); gldsc(kvp+i*2048, kvo, crs+i*1024); }
    barc(rem); barc(rem);
    WAITV(0);
    asm volatile("s_mov_b32 m0, %2\n\ts_mov_b64 exec, 1\n\tglobal_load_lds_dword %0, %1 sc1\n\ts_mov_b64 exec, -1" :: "v"(0u), "s"(pf), "s"(sb0+SYN+100+w*4) : "memory");
    while(rem) barc(rem);
    { int sp=0; while(!__all(*(volatile LDS_AS unsigned*)(LDS_AS char*)(syn+100+(l&3)*4)==tag) && ++sp<(1<<22)) __builtin_amdgcn_s_sleep(1); }
    __builtin_amdgcn_s_setprio(CVP);
    TS(3);
    f32x16 ac0, ac1;
    {
      unsigned p0,p1,p2,p3,p4,p5,p6;
      asm volatile("s_nop 1\n\t" CV_ASM
        : "=&{v[128:143]}"(ac0), "=&{v[144:159]}"(ac1), [p0]"=&s"(p0), [p1]"=&s"(p1), [p2]"=&s"(p2), [p3]"=&s"(p3), [p4]"=&s"(p4), [p5]"=&s"(p5), [p6]"=&s"(p6)
          , "+{v[44:47]}"(qv[0]), "+{v[48:51]}"(qv[1]), "+{v[52:55]}"(qv[2]), "+{v[56:59]}"(qv[3])
        : "{v[64:79]}"(A0), "{v[80:95]}"(A1), "{v[96:111]}"(A2), "{v[112:127]}"(A3),
          "{v[24:31]}"(RB), "{v32}"(bidx), "{v33}"(sb0+SYN+80+w*4)
        : "memory", "scc", CV_CLOB);
    }
    TS(4);
    __builtin_amdgcn_s_setprio(3);
    {
      const unsigned yo=(unsigned)((256*(g&1)+16*fr+4*(g&2))*2);
      #pragma unroll
      for(int J=0;J<8;J+=2){
        #define YA(i) ((i)<16? ac0[(i)&15] : ac1[(i)&15])
        const u32x2 a={pack2(YA(4*J),YA(4*J+1)),pack2(YA(4*J+2),YA(4*J+3))}, b={pack2(YA(4*J+4),YA(4*J+5)),pack2(YA(4*J+6),YA(4*J+7))};
        st16cs(ws+WS_Y+d*4096+512*J, yo, widen(a,b));
      }
    }
    WAITV(0);
    if(lds_inc(syn+24,l)==3u){ if(l<16) stflag(flags+F_Y+(l*16+(bid>>4))*16+(bid&15), tag); }
    TS(5);
    if(w==0){
      SPIN(rd1(syn+96)==0u,2);
      wait_flag<YSLP>(flags+F_Y+bid*16+(l&15), tag);
      SPIN(rd1(syn+24)<4u,1);
      PUB(sb0+(l==0? SYN+28 : SINK+l*4), 1u);
    } else SPIN(rd1(syn+28)==0u,4);
    #pragma unroll
    for(int i=0;i<4;i++) gldsc(ws+WS_Y+(fd0+i*16)*4096+fs0*2, (l>>2)*4096+(l&3)*16, crs+i*1024);
    WAITV(0);
    {
      u32x4 y2[4];
      #pragma unroll
      for(int i=0;i<4;i++){ const char* yp=cr+((i>>1)*32+g*8+tq)*64+(i&1)*32+tp*8; y2[i]=__builtin_bit_cast(u32x4,cat44(trread(yp),trread(yp+256))); }
      asm volatile("" : "+v"(y2[0]), "+v"(y2[1]), "+v"(y2[2]), "+v"(y2[3]) :: "memory");
      #pragma unroll
      for(int i=0;i<4;i++) *(u32x4*)(cr+((i&1)*16+fr)*128+((((i>>1)*4+g)^(fr&7))*16))=y2[i];
      asm volatile("" ::: "memory");
      char* op=out+fs0*4096+fd0*2; const unsigned oo=(unsigned)((l>>3)*16384+(l&7)*16);
      #pragma unroll
      for(int i=0;i<4;i++){
        const int row=4*(l>>3)+i;
        const u32x4 y4=*(const u32x4*)(cr+row*128+(((l&7)^(row&7))*16)), q4=qv[i]; u32x4 ov;
        #pragma unroll
        for(int e=0;e<4;e++) ov[e]=pack2(bflo(q4[e])*bflo(y4[e]), bfhi(q4[e])*bfhi(y4[e]));
        stout(op+i*4096, oo, ov);
      }
    }
    ENDW TS(6); TOUT(7);
  }
}

extern "C" void kernel_launch(void* const* d_in, const int* in_sizes, int n_in, void* d_out, int out_size, void* d_ws, size_t ws_size, hipStream_t stream){
  const u16* q=(const u16*)d_in[0]; const u16* k=(const u16*)d_in[1]; const u16* v=(const u16*)d_in[2];
  const u16* shk=(const u16*)d_in[3]; const u16* hk=(const u16*)d_in[4]; const u16* sD=(const u16*)d_in[5];
  const u16* x=(const u16*)d_in[6]; const u16* wqkv=(const u16*)d_in[7]; const u16* wo=(const u16*)d_in[8];
  fused_kernel<<<256,768,0,stream>>>(x,wqkv,wo,(char*)d_ws,(u16*)d_out,k,v,shk,hk,q,sD);
}
```

```cpp
#include <hip/hip_runtime.h>
#include <hip/hip_bf16.h>
#include <stdint.h>

typedef unsigned short u16;
typedef short bf16x8 __attribute__((ext_vector_type(8)));
typedef short s16x4 __attribute__((ext_vector_type(4)));
typedef float f32x2 __attribute__((ext_vector_type(2)));
typedef float f32x4 __attribute__((ext_vector_type(4)));
typedef float f32x8 __attribute__((ext_vector_type(8)));
typedef float f32x16 __attribute__((ext_vector_type(16)));
typedef unsigned u32x4 __attribute__((ext_vector_type(4)));
typedef unsigned u32x2 __attribute__((ext_vector_type(2)));
typedef __bf16 bf16x2_t __attribute__((ext_vector_type(2)));
#define LDS_AS __attribute__((address_space(3)))

__device__ __forceinline__ float bflo(unsigned x){ return __uint_as_float(x<<16); }
__device__ __forceinline__ float bfhi(unsigned x){ return __uint_as_float(x&0xffff0000u); }
__device__ __forceinline__ unsigned pack2(float lo, float hi){ return __builtin_bit_cast(unsigned, __builtin_convertvector(f32x2{lo,hi}, bf16x2_t)); }
__device__ __forceinline__ float mul1(float a, float b){ float r; asm("v_mul_f32 %0, %1, %2" : "=v"(r) : "v"(a), "v"(b)); return r; }
__device__ __forceinline__ float fma1(float a, float b, float c){ float r; asm("v_fma_f32 %0, %1, %2, %3" : "=v"(r) : "v"(a), "v"(b), "v"(c)); return r; }

__device__ __forceinline__ void glds(const void* sbase, unsigned voff, unsigned ldsaddr){
  asm volatile("s_mov_b32 m0, %2\n\ts_nop 0\n\tglobal_load_lds_dwordx4 %0, %1" :: "v"(voff), "s"(sbase), "s"(ldsaddr) : "memory");
}
__device__ __forceinline__ void gldsc(const void* sbase, unsigned voff, unsigned ldsaddr){
  asm volatile("s_mov_b32 m0, %2\n\ts_nop 0\n\tglobal_load_lds_dwordx4 %0, %1 sc1" :: "v"(voff), "s"(sbase), "s"(ldsaddr) : "memory");
}
__device__ __forceinline__ void touch(const void* sbase, unsigned voff, unsigned ldsaddr){
  asm volatile("s_mov_b32 m0, %2\n\ts_nop 0\n\tglobal_load_lds_dword %0, %1" :: "v"(voff), "s"(sbase), "s"(ldsaddr) : "memory");
}
__device__ __forceinline__ void st16c(void* p, u32x4 v){ asm volatile("global_store_dwordx4 %0, %1, off sc1\n\ts_nop 1" :: "v"(p), "v"(v) : "memory"); }
__device__ __forceinline__ void st16cs(void* sbase, unsigned voff, u32x4 v){ asm volatile("global_store_dwordx4 %0, %1, %2 sc1\n\ts_nop 1" :: "v"(voff), "v"(v), "s"(sbase) : "memory"); }
#define GLB_AS __attribute__((address_space(1)))
__device__ __forceinline__ unsigned ldflag(const unsigned* p){ return __hip_atomic_load((const GLB_AS unsigned*)p, __ATOMIC_RELAXED, __HIP_MEMORY_SCOPE_AGENT); }
__device__ __forceinline__ void stflag(unsigned* p, unsigned v){ __hip_atomic_store((GLB_AS unsigned*)p, v, __ATOMIC_RELAXED, __HIP_MEMORY_SCOPE_AGENT); }
template<int SLP> __device__ __forceinline__ void wait_flag(const unsigned* p, unsigned tag){
  int spins=0;
  while(true){ unsigned v=ldflag(p); if(__all(v==tag)) break; __builtin_amdgcn_s_sleep(SLP); if(++spins>(1<<21)) break; }
}
__device__ __forceinline__ unsigned lds_addr(const void* p){ return (unsigned)(size_t)(const LDS_AS void*)p; }
#define WAITV(n) asm volatile("s_waitcnt vmcnt(" #n ")":::"memory")
#define WAITL() asm volatile("s_waitcnt lgkmcnt(0)":::"memory")
#define SBAR() asm volatile("s_barrier":::"memory")
#define PUB(addr,val) asm volatile("ds_write_b32 %0, %1" :: "v"(addr), "v"(val) : "memory")
__device__ __forceinline__ s16x4 trread(const char* p){ return __builtin_amdgcn_ds_read_tr16_b64_v4i16((LDS_AS s16x4*)p); }
__device__ __forceinline__ bf16x8 cat44(s16x4 a, s16x4 b){ return __builtin_shufflevector(a,b,0,1,2,3,4,5,6,7); }
__device__ __forceinline__ int wswz(int row){ return ((row>>1)&1) | (((row>>3)&1)<<1); }
__device__ __forceinline__ u32x4 widen(u32x2 a, u32x2 b){
  auto r0=__builtin_amdgcn_permlane16_swap(a[0],b[0],false,false);
  auto r1=__builtin_amdgcn_permlane16_swap(a[1],b[1],false,false);
  return u32x4{r0[0],r1[0],r0[1],r1[1]};
}

constexpr int S=2048, DM=1024;
constexpr int WS=12288, XS=18432, X_OFF=6*WS;
constexpr int CREG=4608, C_OFF=131072;
constexpr int EX_OFF=C_OFF+4*CREG;
constexpr int SYN=EX_OFF+8192;
constexpr int SINK=SYN+256;
constexpr int ALL_LDS=SINK+768;
constexpr int F_PP=1024, F_O=4096, F_Y=8192;
#define KV0 1
#define KVS 3
#define LPR 2
#define CPR 2
#define GPR 1
#define CGATE 0
#define OSLP 2
#define YSLP 8
#define TPX 16
#define TPW 0
#define TWO 0
#define CVP 0
#define G2P 1
#define OUTC 1
#define ROT 1
#define WTL 2
#define PB2 1
#define PREM 9
#define KTH 1
#define YDEF 3
#define OPOL 1
#define CODEL 280
#define HLAST 1

#define N2_ASM \
  "v_mov_b32 v64, 0\n\t" \
  "v_mov_b32 v65, 0\n\t" \
  "v_mov_b32 v66, 0\n\t" \
  "v_mov_b32 v67, 0\n\t" \
  "v_mov_b32 v68, 0\n\t" \
  "v_mov_b32 v69, 0\n\t" \
  "v_mov_b32 v70, 0\n\t" \
  "v_mov_b32 v71, 0\n\t" \
  "v_mov_b32 v72, 0\n\t" \
  "v_mov_b32 v73, 0\n\t" \
  "v_mov_b32 v74, 0\n\t" \
  "v_mov_b32 v75, 0\n\t" \
  "v_mov_b32 v76, 0\n\t" \
  "v_mov_b32 v77, 0\n\t" \
  "v_mov_b32 v78, 0\n\t" \
  "v_mov_b32 v79, 0\n\t" \
  "v_mov_b32 v80, 0\n\t" \
  "v_mov_b32 v81, 0\n\t" \
  "v_mov_b32 v82, 0\n\t" \
  "v_mov_b32 v83, 0\n\t" \
  "v_mov_b32 v84, 0\n\t" \
  "v_mov_b32 v85, 0\n\t" \
  "v_mov_b32 v86, 0\n\t" \
  "v_mov_b32 v87, 0\n\t" \
  "v_mov_b32 v88, 0\n\t" \
  "v_mov_b32 v89, 0\n\t" \
  "v_mov_b32 v90, 0\n\t" \
  "v_mov_b32 v91, 0\n\t" \
  "v_mov_b32 v92, 0\n\t" \
  "v_mov_b32 v93, 0\n\t" \
  "v_mov_b32 v94, 0\n\t" \
  "v_mov_b32 v95, 0\n\t" \
  "v_mov_b32 v96, 0\n\t" \
  "v_mov_b32 v97, 0\n\t" \
  "v_mov_b32 v98, 0\n\t" \
  "v_mov_b32 v99, 0\n\t" \
  "v_mov_b32 v100, 0\n\t" \
  "v_mov_b32 v101, 0\n\t" \
  "v_mov_b32 v102, 0\n\t" \
  "v_mov_b32 v103, 0\n\t" \
  "v_mov_b32 v104, 0\n\t" \
  "v_mov_b32 v105, 0\n\t" \
  "v_mov_b32 v106, 0\n\t" \
  "v_mov_b32 v107, 0\n\t" \
  "v_mov_b32 v108, 0\n\t" \
  "v_mov_b32 v109, 0\n\t" \
  "v_mov_b32 v110, 0\n\t" \
  "v_mov_b32 v111, 0\n\t" \
  "v_mov_b32 v112, 0\n\t" \
  "v_mov_b32 v113, 0\n\t" \
  "v_mov_b32 v114, 0\n\t" \
  "v_mov_b32 v115, 0\n\t" \
  "v_mov_b32 v116, 0\n\t" \
  "v_mov_b32 v117, 0\n\t" \
  "v_mov_b32 v118, 0\n\t" \
  "v_mov_b32 v119, 0\n\t" \
  "v_mov_b32 v120, 0\n\t" \
  "v_mov_b32 v121, 0\n\t" \
  "v_mov_b32 v122, 0\n\t" \
  "v_mov_b32 v123, 0\n\t" \
  "v_mov_b32 v124, 0\n\t" \
  "v_mov_b32 v125, 0\n\t" \
  "v_mov_b32 v126, 0\n\t" \
  "v_mov_b32 v127, 0\n\t" \
  "v_mov_b32 v128, 0\n\t" \
  "v_mov_b32 v129, 0\n\t" \
  "v_mov_b32 v130, 0\n\t" \
  "v_mov_b32 v131, 0\n\t" \
  "v_mov_b32 v132, 0\n\t" \
  "v_mov_b32 v133, 0\n\t" \
  "v_mov_b32 v134, 0\n\t" \
  "v_mov_b32 v135, 0\n\t" \
  "v_mov_b32 v136, 0\n\t" \
  "v_mov_b32 v137, 0\n\t" \
  "v_mov_b32 v138, 0\n\t" \
  "v_mov_b32 v139, 0\n\t" \
  "v_mov_b32 v140, 0\n\t" \
  "v_mov_b32 v141, 0\n\t" \
  "v_mov_b32 v142, 0\n\t" \
  "v_mov_b32 v143, 0\n\t" \
  "v_mov_b32 v144, 0\n\t" \
  "v_mov_b32 v145, 0\n\t" \
  "v_mov_b32 v146, 0\n\t" \
  "v_mov_b32 v147, 0\n\t" \
  "v_mov_b32 v148, 0\n\t" \
  "v_mov_b32 v149, 0\n\t" \
  "v_mov_b32 v150, 0\n\t" \
  "v_mov_b32 v151, 0\n\t" \
  "v_mov_b32 v152, 0\n\t" \
  "v_mov_b32 v153, 0\n\t" \
  "v_mov_b32 v154, 0\n\t" \
  "v_mov_b32 v155, 0\n\t" \
  "v_mov_b32 v156, 0\n\t" \
  "v_mov_b32 v157, 0\n\t" \
  "v_mov_b32 v158, 0\n\t" \
  "v_mov_b32 v159, 0\n\t" \
  "v_mov_b32 v160, 0\n\t" \
  "v_mov_b32 v161, 0\n\t" \
  "v_mov_b32 v162, 0\n\t" \
  "v_mov_b32 v163, 0\n\t" \
  "v_mov_b32 v164, 0\n\t" \
  "v_mov_b32 v165, 0\n\t" \
  "v_mov_b32 v166, 0\n\t" \
  "v_mov_b32 v167, 0\n\t" \
  "s_mov_b32 s68, 0x50000\n\t" \
  "s_mov_b32 s69, 0x5550055\n\t" \
  "s_mov_b32 s70, 0xa0000\n\t" \
  "s_mov_b32 s71, 0xaaa00aa\n\t" \
  "s_mov_b32 s72, 0x150001\n\t" \
  "s_mov_b32 s73, 0x15550155\n\t" \
  "s_mov_b32 s74, 0x2a0002\n\t" \
  "s_mov_b32 s75, 0x2aaa02aa\n\t" \
  "s_mov_b32 s76, 0x55505555\n\t" \
  "s_mov_b32 s77, 0x50005500\n\t" \
  "s_mov_b32 s78, 0xaaa0aaaa\n\t" \
  "s_mov_b32 s79, 0xa000aa00\n\t" \
  "s_mov_b32 s80, 0x55405554\n\t" \
  "s_mov_b32 s81, 0x40005400\n\t" \
  "s_mov_b32 s82, 0xaa80aaa8\n\t" \
  "s_mov_b32 s83, 0x8000a800\n\t" \
  "s_mov_b32 s84, 0xaaaaaaaa\n\t" \
  "s_mov_b32 s85, 0xaaaaaaaa\n\t" \
  "v_add_u32 v60, 36864, v58\n\t" \
  "v_add_u32 v61, 36864, v59\n\t" \
  "s_mov_b32 %[it], 0\n\t" \
  "s_cmp_lg_u32 %[r], 0\n\t" \
  "s_barrier\n\t" \
  "s_cbranch_scc1 NB_%=\n\t" \
  "ds_read_b64_tr_b16 v[40:41], v60 offset:16384\n\t" \
  "ds_read_b64_tr_b16 v[42:43], v60 offset:16896\n\t" \
  "ds_read_b64_tr_b16 v[44:45], v60 offset:20480\n\t" \
  "ds_read_b64_tr_b16 v[46:47], v60 offset:20992\n\t" \
  "ds_read_b64_tr_b16 v[48:49], v61 offset:16384\n\t" \
  "ds_read_b64_tr_b16 v[50:51], v61 offset:16896\n\t" \
  "ds_read_b128 v[0:3], v56 offset:36864\n\t" \
  "ds_read_b128 v[4:7], v56 offset:38912\n\t" \
  "ds_read_b128 v[8:11], v56 offset:40960\n\t" \
  "ds_read_b128 v[12:15], v56 offset:43008\n\t" \
  "ds_read_b128 v[16:19], v56 offset:45056\n\t" \
  "s_waitcnt lgkmcnt(0)\n\t" \
  "s_branch N4a_%=\n\t" \
  "N0a_%=:\n\t" \
  "s_waitcnt lgkmcnt(4)\n\t" \
  "v_mfma_f32_16x16x32_bf16 v[160:163], v[40:43], v[0:3], v[160:163]\n\t" \
  "ds_read_b64_tr_b16 v[52:53], v59 offset:8192\n\t" \
  "v_mfma_f32_16x16x32_bf16 v[96:99], v[40:43], v[4:7], v[96:99]\n\t" \
  "ds_read_b64_tr_b16 v[54:55], v59 offset:8704\n\t" \
  "v_mfma_f32_16x16x32_bf16 v[104:107], v[40:43], v[8:11], v[104:107]\n\t" \
  "s_barrier\n\t" \
  "v_mfma_f32_16x16x32_bf16 v[112:115], v[40:43], v[12:15], v[112:115]\n\t" \
  "v_mfma_f32_16x16x32_bf16 v[120:123], v[40:43], v[16:19], v[120:123]\n\t" \
  "s_waitcnt lgkmcnt(4)\n\t" \
  "v_mfma_f32_16x16x32_bf16 v[128:131], v[4:7], v[44:47], v[128:131]\n\t" \
  "ds_read_b64_tr_b16 v[40:41], v58 offset:0\n\t" \
  "v_mfma_f32_16x16x32_bf16 v[136:139], v[8:11], v[44:47], v[136:139]\n\t" \
  "ds_read_b64_tr_b16 v[42:43], v58 offset:512\n\t" \
  "v_mfma_f32_16x16x32_bf16 v[144:147], v[12:15], v[44:47], v[144:147]\n\t" \
  "ds_read_b128 v[20:23], v57 offset:0\n\t" \
  "v_mfma_f32_16x16x32_bf16 v[152:155], v[16:19], v[44:47], v[152:155]\n\t" \
  "ds_read_b128 v[24:27], v57 offset:2048\n\t" \
  "s_waitcnt lgkmcnt(6)\n\t" \
  "v_mfma_f32_16x16x32_bf16 v[164:167], v[48:51], v[0:3], v[164:167]\n\t" \
  "ds_read_b64_tr_b16 v[44:45], v59 offset:0\n\t" \
  "v_mfma_f32_16x16x32_bf16 v[100:103], v[48:51], v[4:7], v[100:103]\n\t" \
  "ds_read_b64_tr_b16 v[46:47], v59 offset:512\n\t" \
  "v_mfma_f32_16x16x32_bf16 v[108:111], v[48:51], v[8:11], v[108:111]\n\t" \
  "ds_read_b128 v[28:31], v57 offset:4096\n\t" \
  "v_mfma_f32_16x16x32_bf16 v[116:119], v[48:51], v[12:15], v[116:119]\n\t" \
  "ds_read_b128 v[32:35], v57 offset:6144\n\t" \
  "v_mfma_f32_16x16x32_bf16 v[124:127], v[48:51], v[16:19], v[124:127]\n\t" \
  "s_waitcnt lgkmcnt(8)\n\t" \
  "v_mfma_f32_16x16x32_bf16 v[132:135], v[4:7], v[52:55], v[132:135]\n\t" \
  "ds_read_b64_tr_b16 v[48:49], v58 offset:16384\n\t" \
  "v_mfma_f32_16x16x32_bf16 v[140:143], v[8:11], v[52:55], v[140:143]\n\t" \
  "ds_read_b64_tr_b16 v[50:51], v58 offset:16896\n\t" \
  "v_mfma_f32_16x16x32_bf16 v[148:151], v[12:15], v[52:55], v[148:151]\n\t" \
  "ds_read_b128 v[36:39], v57 offset:8192\n\t" \
  "v_mfma_f32_16x16x32_bf16 v[156:159], v[16:19], v[52:55], v[156:159]\n\t" \
  "s_waitcnt lgkmcnt(9)\n\t" \
  "v_mfma_f32_16x16x32_bf16 v[64:67], v[40:43], v[4:7], v[64:67]\n\t" \
  "ds_read_b64_tr_b16 v[52:53], v58 offset:20480\n\t" \
  "v_mfma_f32_16x16x32_bf16 v[72:75], v[40:43], v[8:11], v[72:75]\n\t" \
  "ds_read_b64_tr_b16 v[54:55], v58 offset:20992\n\t" \
  "v_mfma_f32_16x16x32_bf16 v[80:83], v[40:43], v[12:15], v[80:83]\n\t" \
  "v_mfma_f32_16x16x32_bf16 v[88:91], v[40:43], v[16:19], v[88:91]\n\t" \
  "s_waitcnt lgkmcnt(7)\n\t" \
  "v_mfma_f32_16x16x32_bf16 v[68:71], v[44:47], v[4:7], v[68:71]\n\t" \
  "ds_read_b64_tr_b16 v[40:41], v59 offset:16384\n\t" \
  "v_mfma_f32_16x16x32_bf16 v[76:79], v[44:47], v[8:11], v[76:79]\n\t" \
  "ds_read_b64_tr_b16 v[42:43], v59 offset:16896\n\t" \
  "v_mfma_f32_16x16x32_bf16 v[84:87], v[44:47], v[12:15], v[84:87]\n\t" \
  "v_mfma_f32_16x16x32_bf16 v[92:95], v[44:47], v[16:19], v[92:95]\n\t" \
  "N1a_%=:\n\t" \
  "s_waitcnt lgkmcnt(4)\n\t" \
  "v_mfma_f32_16x16x32_bf16 v[160:163], v[48:51], v[20:23], v[160:163]\n\t" \
  "ds_read_b64_tr_b16 v[44:45], v59 offset:20480\n\t" \
  "v_mfma_f32_16x16x32_bf16 v[96:99], v[48:51], v[24:27], v[96:99]\n\t" \
  "ds_read_b64_tr_b16 v[46:47], v59 offset:20992\n\t" \
  "v_mfma_f32_16x16x32_bf16 v[104:107], v[48:51], v[28:31], v[104:107]\n\t" \
  "s_barrier\n\t" \
  "v_mfma_f32_16x16x32_bf16 v[112:115], v[48:51], v[32:35], v[112:115]\n\t" \
  "v_mfma_f32_16x16x32_bf16 v[120:123], v[48:51], v[36:39], v[120:123]\n\t" \
  "s_waitcnt lgkmcnt(4)\n\t" \
  "v_mfma_f32_16x16x32_bf16 v[128:131], v[24:27], v[52:55], v[128:131]\n\t" \
  "ds_read_b64_tr_b16 v[48:49], v58 offset:12288\n\t" \
  "v_mfma_f32_16x16x32_bf16 v[136:139], v[28:31], v[52:55], v[136:139]\n\t" \
  "ds_read_b64_tr_b16 v[50:51], v58 offset:12800\n\t" \
  "v_mfma_f32_16x16x32_bf16 v[144:147], v[32:35], v[52:55], v[144:147]\n\t" \
  "ds_read_b128 v[0:3], v56 offset:18432\n\t" \
  "v_mfma_f32_16x16x32_bf16 v[152:155], v[36:39], v[52:55], v[152:155]\n\t" \
  "ds_read_b128 v[4:7], v56 offset:20480\n\t" \
  "s_waitcnt lgkmcnt(6)\n\t" \
  "v_mfma_f32_16x16x32_bf16 v[164:167], v[40:43], v[20:23], v[164:167]\n\t" \
  "ds_read_b64_tr_b16 v[52:53], v59 offset:12288\n\t" \
  "v_mfma_f32_16x16x32_bf16 v[100:103], v[40:43], v[24:27], v[100:103]\n\t" \
  "ds_read_b64_tr_b16 v[54:55], v59 offset:12800\n\t" \
  "v_mfma_f32_16x16x32_bf16 v[108:111], v[40:43], v[28:31], v[108:111]\n\t" \
  "ds_read_b128 v[8:11], v56 offset:22528\n\t" \
  "v_mfma_f32_16x16x32_bf16 v[116:119], v[40:43], v[32:35], v[116:119]\n\t" \
  "ds_read_b128 v[12:15], v56 offset:24576\n\t" \
  "v_mfma_f32_16x16x32_bf16 v[124:127], v[40:43], v[36:39], v[124:127]\n\t" \
  "s_waitcnt lgkmcnt(8)\n\t" \
  "v_mfma_f32_16x16x32_bf16 v[132:135], v[24:27], v[44:47], v[132:135]\n\t" \
  "ds_read_b64_tr_b16 v[40:41], v58 offset:28672\n\t" \
  "v_mfma_f32_16x16x32_bf16 v[140:143], v[28:31], v[44:47], v[140:143]\n\t" \
  "ds_read_b64_tr_b16 v[42:43], v58 offset:29184\n\t" \
  "v_mfma_f32_16x16x32_bf16 v[148:151], v[32:35], v[44:47], v[148:151]\n\t" \
  "ds_read_b128 v[16:19], v56 offset:26624\n\t" \
  "v_mfma_f32_16x16x32_bf16 v[156:159], v[36:39], v[44:47], v[156:159]\n\t" \
  "s_waitcnt lgkmcnt(9)\n\t" \
  "v_mfma_f32_16x16x32_bf16 v[64:67], v[48:51], v[24:27], v[64:67]\n\t" \
  "ds_read_b64_tr_b16 v[44:45], v58 offset:32768\n\t" \
  "v_mfma_f32_16x16x32_bf16 v[72:75], v[48:51], v[28:31], v[72:75]\n\t" \
  "ds_read_b64_tr_b16 v[46:47], v58 offset:33280\n\t" \
  "v_mfma_f32_16x16x32_bf16 v[80:83], v[48:51], v[32:35], v[80:83]\n\t" \
  "v_mfma_f32_16x16x32_bf16 v[88:91], v[48:51], v[36:39], v[88:91]\n\t" \
  "s_waitcnt lgkmcnt(7)\n\t" \
  "v_mfma_f32_16x16x32_bf16 v[68:71], v[52:55], v[24:27], v[68:71]\n\t" \
  "ds_read_b64_tr_b16 v[48:49], v59 offset:28672\n\t" \
  "v_mfma_f32_16x16x32_bf16 v[76:79], v[52:55], v[28:31], v[76:79]\n\t" \
  "ds_read_b64_tr_b16 v[50:51], v59 offset:29184\n\t" \
  "v_mfma_f32_16x16x32_bf16 v[84:87], v[52:55], v[32:35], v[84:87]\n\t" \
  "v_mfma_f32_16x16x32_bf16 v[92:95], v[52:55], v[36:39], v[92:95]\n\t" \
  "N2a_%=:\n\t" \
  "s_waitcnt lgkmcnt(4)\n\t" \
  "v_mfma_f32_16x16x32_bf16 v[160:163], v[40:43], v[0:3], v[160:163]\n\t" \
  "ds_read_b64_tr_b16 v[52:53], v59 offset:32768\n\t" \
  "v_mfma_f32_16x16x32_bf16 v[96:99], v[40:43], v[4:7], v[96:99]\n\t" \
  "ds_read_b64_tr_b16 v[54:55], v59 offset:33280\n\t" \
  "v_mfma_f32_16x16x32_bf16 v[104:107], v[40:43], v[8:11], v[104:107]\n\t" \
  "s_barrier\n\t" \
  "v_mfma_f32_16x16x32_bf16 v[112:115], v[40:43], v[12:15], v[112:115]\n\t" \
  "v_mfma_f32_16x16x32_bf16 v[120:123], v[40:43], v[16:19], v[120:123]\n\t" \
  "s_waitcnt lgkmcnt(4)\n\t" \
  "v_mfma_f32_16x16x32_bf16 v[128:131], v[4:7], v[44:47], v[128:131]\n\t" \
  "ds_read_b64_tr_b16 v[40:41], v58 offset:24576\n\t" \
  "v_mfma_f32_16x16x32_bf16 v[136:139], v[8:11], v[44:47], v[136:139]\n\t" \
  "ds_read_b64_tr_b16 v[42:43], v58 offset:25088\n\t" \
  "v_mfma_f32_16x16x32_bf16 v[144:147], v[12:15], v[44:47], v[144:147]\n\t" \
  "ds_read_b128 v[20:23], v57 offset:18432\n\t" \
  "v_mfma_f32_16x16x32_bf16 v[152:155], v[16:19], v[44:47], v[152:155]\n\t" \
  "ds_read_b128 v[24:27], v57 offset:20480\n\t" \
  "s_waitcnt lgkmcnt(6)\n\t" \
  "v_mfma_f32_16x16x32_bf16 v[164:167], v[48:51], v[0:3], v[164:167]\n\t" \
  "ds_read_b64_tr_b16 v[44:45], v59 offset:24576\n\t" \
  "v_mfma_f32_16x16x32_bf16 v[100:103], v[48:51], v[4:7], v[100:103]\n\t" \
  "ds_read_b64_tr_b16 v[46:47], v59 offset:25088\n\t" \
  "v_mfma_f32_16x16x32_bf16 v[108:111], v[48:51], v[8:11], v[108:111]\n\t" \
  "ds_read_b128 v[28:31], v57 offset:22528\n\t" \
  "v_mfma_f32_16x16x32_bf16 v[116:119], v[48:51], v[12:15], v[116:119]\n\t" \
  "ds_read_b128 v[32:35], v57 offset:24576\n\t" \
  "v_mfma_f32_16x16x32_bf16 v[124:127], v[48:51], v[16:19], v[124:127]\n\t" \
  "s_waitcnt lgkmcnt(8)\n\t" \
  "v_mfma_f32_16x16x32_bf16 v[132:135], v[4:7], v[52:55], v[132:135]\n\t" \
  "ds_read_b64_tr_b16 v[48:49], v60 offset:4096\n\t" \
  "v_mfma_f32_16x16x32_bf16 v[140:143], v[8:11], v[52:55], v[140:143]\n\t" \
  "ds_read_b64_tr_b16 v[50:51], v60 offset:4608\n\t" \
  "v_mfma_f32_16x16x32_bf16 v[148:151], v[12:15], v[52:55], v[148:151]\n\t" \
  "ds_read_b128 v[36:39], v57 offset:26624\n\t" \
  "v_mfma_f32_16x16x32_bf16 v[156:159], v[16:19], v[52:55], v[156:159]\n\t" \
  "s_waitcnt lgkmcnt(9)\n\t" \
  "v_mfma_f32_16x16x32_bf16 v[64:67], v[40:43], v[4:7], v[64:67]\n\t" \
  "ds_read_b64_tr_b16 v[52:53], v60 offset:8192\n\t" \
  "v_mfma_f32_16x16x32_bf16 v[72:75], v[40:43], v[8:11], v[72:75]\n\t" \
  "ds_read_b64_tr_b16 v[54:55], v60 offset:8704\n\t" \
  "v_mfma_f32_16x16x32_bf16 v[80:83], v[40:43], v[12:15], v[80:83]\n\t" \
  "v_mfma_f32_16x16x32_bf16 v[88:91], v[40:43], v[16:19], v[88:91]\n\t" \
  "s_waitcnt lgkmcnt(7)\n\t" \
  "v_mfma_f32_16x16x32_bf16 v[68:71], v[44:47], v[4:7], v[68:71]\n\t" \
  "ds_read_b64_tr_b16 v[40:41], v61 offset:4096\n\t" \
  "v_mfma_f32_16x16x32_bf16 v[76:79], v[44:47], v[8:11], v[76:79]\n\t" \
  "ds_read_b64_tr_b16 v[42:43], v61 offset:4608\n\t" \
  "v_mfma_f32_16x16x32_bf16 v[84:87], v[44:47], v[12:15], v[84:87]\n\t" \
  "v_mfma_f32_16x16x32_bf16 v[92:95], v[44:47], v[16:19], v[92:95]\n\t" \
  "N3a_%=:\n\t" \
  "s_waitcnt lgkmcnt(4)\n\t" \
  "v_mfma_f32_16x16x32_bf16 v[160:163], v[48:51], v[20:23], v[160:163]\n\t" \
  "ds_read_b64_tr_b16 v[44:45], v61 offset:8192\n\t" \
  "v_mfma_f32_16x16x32_bf16 v[96:99], v[48:51], v[24:27], v[96:99]\n\t" \
  "ds_read_b64_tr_b16 v[46:47], v61 offset:8704\n\t" \
  "v_mfma_f32_16x16x32_bf16 v[104:107], v[48:51], v[28:31], v[104:107]\n\t" \
  "s_barrier\n\t" \
  "v_mfma_f32_16x16x32_bf16 v[112:115], v[48:51], v[32:35], v[112:115]\n\t" \
  "v_mfma_f32_16x16x32_bf16 v[120:123], v[48:51], v[36:39], v[120:123]\n\t" \
  "s_waitcnt lgkmcnt(4)\n\t" \
  "v_mfma_f32_16x16x32_bf16 v[128:131], v[24:27], v[52:55], v[128:131]\n\t" \
  "ds_read_b64_tr_b16 v[48:49], v60 offset:0\n\t" \
  "v_mfma_f32_16x16x32_bf16 v[136:139], v[28:31], v[52:55], v[136:139]\n\t" \
  "ds_read_b64_tr_b16 v[50:51], v60 offset:512\n\t" \
  "v_mfma_f32_16x16x32_bf16 v[144:147], v[32:35], v[52:55], v[144:147]\n\t" \
  "ds_read_b128 v[0:3], v56 offset:36864\n\t" \
  "v_mfma_f32_16x16x32_bf16 v[152:155], v[36:39], v[52:55], v[152:155]\n\t" \
  "ds_read_b128 v[4:7], v56 offset:38912\n\t" \
  "s_waitcnt lgkmcnt(6)\n\t" \
  "v_mfma_f32_16x16x32_bf16 v[164:167], v[40:43], v[20:23], v[164:167]\n\t" \
  "ds_read_b64_tr_b16 v[52:53], v61 offset:0\n\t" \
  "v_mfma_f32_16x16x32_bf16 v[100:103], v[40:43], v[24:27], v[100:103]\n\t" \
  "ds_read_b64_tr_b16 v[54:55], v61 offset:512\n\t" \
  "v_mfma_f32_16x16x32_bf16 v[108:111], v[40:43], v[28:31], v[108:111]\n\t" \
  "ds_read_b128 v[8:11], v56 offset:40960\n\t" \
  "v_mfma_f32_16x16x32_bf16 v[116:119], v[40:43], v[32:35], v[116:119]\n\t" \
  "ds_read_b128 v[12:15], v56 offset:43008\n\t" \
  "v_mfma_f32_16x16x32_bf16 v[124:127], v[40:43], v[36:39], v[124:127]\n\t" \
  "s_waitcnt lgkmcnt(8)\n\t" \
  "v_mfma_f32_16x16x32_bf16 v[132:135], v[24:27], v[44:47], v[132:135]\n\t" \
  "ds_read_b64_tr_b16 v[40:41], v60 offset:16384\n\t" \
  "v_mfma_f32_16x16x32_bf16 v[140:143], v[28:31], v[44:47], v[140:143]\n\t" \
  "ds_read_b64_tr_b16 v[42:43], v60 offset:16896\n\t" \
  "v_mfma_f32_16x16x32_bf16 v[148:151], v[32:35], v[44:47], v[148:151]\n\t" \
  "ds_read_b128 v[16:19], v56 offset:45056\n\t" \
  "v_mfma_f32_16x16x32_bf16 v[156:159], v[36:39], v[44:47], v[156:159]\n\t" \
  "s_waitcnt lgkmcnt(9)\n\t" \
  "v_mfma_f32_16x16x32_bf16 v[64:67], v[48:51], v[24:27], v[64:67]\n\t" \
  "ds_read_b64_tr_b16 v[44:45], v60 offset:20480\n\t" \
  "v_mfma_f32_16x16x32_bf16 v[72:75], v[48:51], v[28:31], v[72:75]\n\t" \
  "ds_read_b64_tr_b16 v[46:47], v60 offset:20992\n\t" \
  "v_mfma_f32_16x16x32_bf16 v[80:83], v[48:51], v[32:35], v[80:83]\n\t" \
  "v_mfma_f32_16x16x32_bf16 v[88:91], v[48:51], v[36:39], v[88:91]\n\t" \
  "s_waitcnt lgkmcnt(7)\n\t" \
  "v_mfma_f32_16x16x32_bf16 v[68:71], v[52:55], v[24:27], v[68:71]\n\t" \
  "ds_read_b64_tr_b16 v[48:49], v61 offset:16384\n\t" \
  "v_mfma_f32_16x16x32_bf16 v[76:79], v[52:55], v[28:31], v[76:79]\n\t" \
  "ds_read_b64_tr_b16 v[50:51], v61 offset:16896\n\t" \
  "v_mfma_f32_16x16x32_bf16 v[84:87], v[52:55], v[32:35], v[84:87]\n\t" \
  "v_mfma_f32_16x16x32_bf16 v[92:95], v[52:55], v[36:39], v[92:95]\n\t" \
  "N4a_%=:\n\t" \
  "s_waitcnt lgkmcnt(4)\n\t" \
  "v_mfma_f32_16x16x32_bf16 v[160:163], v[40:43], v[0:3], v[160:163]\n\t" \
  "ds_read_b64_tr_b16 v[52:53], v61 offset:20480\n\t" \
  "v_mfma_f32_16x16x32_bf16 v[96:99], v[40:43], v[4:7], v[96:99]\n\t" \
  "ds_read_b64_tr_b16 v[54:55], v61 offset:20992\n\t" \
  "v_mfma_f32_16x16x32_bf16 v[104:107], v[40:43], v[8:11], v[104:107]\n\t" \
  "s_barrier\n\t" \
  "v_mfma_f32_16x16x32_bf16 v[112:115], v[40:43], v[12:15], v[112:115]\n\t" \
  "v_mfma_f32_16x16x32_bf16 v[120:123], v[40:43], v[16:19], v[120:123]\n\t" \
  "s_waitcnt lgkmcnt(4)\n\t" \
  "v_mfma_f32_16x16x32_bf16 v[128:131], v[4:7], v[44:47], v[128:131]\n\t" \
  "ds_read_b64_tr_b16 v[40:41], v60 offset:12288\n\t" \
  "v_mfma_f32_16x16x32_bf16 v[136:139], v[8:11], v[44:47], v[136:139]\n\t" \
  "ds_read_b64_tr_b16 v[42:43], v60 offset:12800\n\t" \
  "v_mfma_f32_16x16x32_bf16 v[144:147], v[12:15], v[44:47], v[144:147]\n\t" \
  "ds_read_b128 v[20:23], v57 offset:36864\n\t" \
  "v_mfma_f32_16x16x32_bf16 v[152:155], v[16:19], v[44:47], v[152:155]\n\t" \
  "ds_read_b128 v[24:27], v57 offset:38912\n\t" \
  "s_waitcnt lgkmcnt(6)\n\t" \
  "v_mfma_f32_16x16x32_bf16 v[164:167], v[48:51], v[0:3], v[164:167]\n\t" \
  "ds_read_b64_tr_b16 v[44:45], v61 offset:12288\n\t" \
  "v_mfma_f32_16x16x32_bf16 v[100:103], v[48:51], v[4:7], v[100:103]\n\t" \
  "ds_read_b64_tr_b16 v[46:47], v61 offset:12800\n\t" \
  "v_mfma_f32_16x16x32_bf16 v[108:111], v[48:51], v[8:11], v[108:111]\n\t" \
  "ds_read_b128 v[28:31], v57 offset:40960\n\t" \
  "v_mfma_f32_16x16x32_bf16 v[116:119], v[48:51], v[12:15], v[116:119]\n\t" \
  "ds_read_b128 v[32:35], v57 offset:43008\n\t" \
  "v_mfma_f32_16x16x32_bf16 v[124:127], v[48:51], v[16:19], v[124:127]\n\t" \
  "s_waitcnt lgkmcnt(8)\n\t" \
  "v_mfma_f32_16x16x32_bf16 v[132:135], v[4:7], v[52:55], v[132:135]\n\t" \
  "ds_read_b64_tr_b16 v[48:49], v60 offset:28672\n\t" \
  "v_mfma_f32_16x16x32_bf16 v[140:143], v[8:11], v[52:55], v[140:143]\n\t" \
  "ds_read_b64_tr_b16 v[50:51], v60 offset:29184\n\t" \
  "v_mfma_f32_16x16x32_bf16 v[148:151], v[12:15], v[52:55], v[148:151]\n\t" \
  "ds_read_b128 v[36:39], v57 offset:45056\n\t" \
  "v_mfma_f32_16x16x32_bf16 v[156:159], v[16:19], v[52:55], v[156:159]\n\t" \
  "s_waitcnt lgkmcnt(9)\n\t" \
  "v_mfma_f32_16x16x32_bf16 v[64:67], v[40:43], v[4:7], v[64:67]\n\t" \
  "ds_read_b64_tr_b16 v[52:53], v60 offset:32768\n\t" \
  "v_mfma_f32_16x16x32_bf16 v[72:75], v[40:43], v[8:11], v[72:75]\n\t" \
  "ds_read_b64_tr_b16 v[54:55], v60 offset:33280\n\t" \
  "v_mfma_f32_16x16x32_bf16 v[80:83], v[40:43], v[12:15], v[80:83]\n\t" \
  "v_mfma_f32_16x16x32_bf16 v[88:91], v[40:43], v[16:19], v[88:91]\n\t" \
  "s_waitcnt lgkmcnt(7)\n\t" \
  "v_mfma_f32_16x16x32_bf16 v[68:71], v[44:47], v[4:7], v[68:71]\n\t" \
  "ds_read_b64_tr_b16 v[40:41], v61 offset:28672\n\t" \
  "v_mfma_f32_16x16x32_bf16 v[76:79], v[44:47], v[8:11], v[76:79]\n\t" \
  "ds_read_b64_tr_b16 v[42:43], v61 offset:29184\n\t" \
  "v_mfma_f32_16x16x32_bf16 v[84:87], v[44:47], v[12:15], v[84:87]\n\t" \
  "v_mfma_f32_16x16x32_bf16 v[92:95], v[44:47], v[16:19], v[92:95]\n\t" \
  "N5a_%=:\n\t" \
  "s_waitcnt lgkmcnt(4)\n\t" \
  "v_mfma_f32_16x16x32_bf16 v[160:163], v[48:51], v[20:23], v[160:163]\n\t" \
  "ds_read_b64_tr_b16 v[44:45], v61 offset:32768\n\t" \
  "v_mfma_f32_16x16x32_bf16 v[96:99], v[48:51], v[24:27], v[96:99]\n\t" \
  "ds_read_b64_tr_b16 v[46:47], v61 offset:33280\n\t" \
  "v_mfma_f32_16x16x32_bf16 v[104:107], v[48:51], v[28:31], v[104:107]\n\t" \
  "s_barrier\n\t" \
  "v_mfma_f32_16x16x32_bf16 v[112:115], v[48:51], v[32:35], v[112:115]\n\t" \
  "v_mfma_f32_16x16x32_bf16 v[120:123], v[48:51], v[36:39], v[120:123]\n\t" \
  "s_waitcnt lgkmcnt(4)\n\t" \
  "v_mfma_f32_16x16x32_bf16 v[128:131], v[24:27], v[52:55], v[128:131]\n\t" \
  "ds_read_b64_tr_b16 v[48:49], v60 offset:24576\n\t" \
  "v_mfma_f32_16x16x32_bf16 v[136:139], v[28:31], v[52:55], v[136:139]\n\t" \
  "ds_read_b64_tr_b16 v[50:51], v60 offset:25088\n\t" \
  "v_mfma_f32_16x16x32_bf16 v[144:147], v[32:35], v[52:55], v[144:147]\n\t" \
  "ds_read_b128 v[0:3], v56 offset:0\n\t" \
  "v_mfma_f32_16x16x32_bf16 v[152:155], v[36:39], v[52:55], v[152:155]\n\t" \
  "ds_read_b128 v[4:7], v56 offset:2048\n\t" \
  "s_waitcnt lgkmcnt(6)\n\t" \
  "v_mfma_f32_16x16x32_bf16 v[164:167], v[40:43], v[20:23], v[164:167]\n\t" \
  "ds_read_b64_tr_b16 v[52:53], v61 offset:24576\n\t" \
  "v_mfma_f32_16x16x32_bf16 v[100:103], v[40:43], v[24:27], v[100:103]\n\t" \
  "ds_read_b64_tr_b16 v[54:55], v61 offset:25088\n\t" \
  "v_mfma_f32_16x16x32_bf16 v[108:111], v[40:43], v[28:31], v[108:111]\n\t" \
  "ds_read_b128 v[8:11], v56 offset:4096\n\t" \
  "v_mfma_f32_16x16x32_bf16 v[116:119], v[40:43], v[32:35], v[116:119]\n\t" \
  "ds_read_b128 v[12:15], v56 offset:6144\n\t" \
  "v_mfma_f32_16x16x32_bf16 v[124:127], v[40:43], v[36:39], v[124:127]\n\t" \
  "s_waitcnt lgkmcnt(8)\n\t" \
  "v_mfma_f32_16x16x32_bf16 v[132:135], v[24:27], v[44:47], v[132:135]\n\t" \
  "ds_read_b64_tr_b16 v[40:41], v58 offset:4096\n\t" \
  "v_mfma_f32_16x16x32_bf16 v[140:143], v[28:31], v[44:47], v[140:143]\n\t" \
  "ds_read_b64_tr_b16 v[42:43], v58 offset:4608\n\t" \
  "v_mfma_f32_16x16x32_bf16 v[148:151], v[32:35], v[44:47], v[148:151]\n\t" \
  "ds_read_b128 v[16:19], v56 offset:8192\n\t" \
  "v_mfma_f32_16x16x32_bf16 v[156:159], v[36:39], v[44:47], v[156:159]\n\t" \
  "s_waitcnt lgkmcnt(9)\n\t" \
  "v_mfma_f32_16x16x32_bf16 v[64:67], v[48:51], v[24:27], v[64:67]\n\t" \
  "ds_read_b64_tr_b16 v[44:45], v58 offset:8192\n\t" \
  "v_mfma_f32_16x16x32_bf16 v[72:75], v[48:51], v[28:31], v[72:75]\n\t" \
  "ds_read_b64_tr_b16 v[46:47], v58 offset:8704\n\t" \
  "v_mfma_f32_16x16x32_bf16 v[80:83], v[48:51], v[32:35], v[80:83]\n\t" \
  "v_mfma_f32_16x16x32_bf16 v[88:91], v[48:51], v[36:39], v[88:91]\n\t" \
  "s_waitcnt lgkmcnt(7)\n\t" \
  "v_mfma_f32_16x16x32_bf16 v[68:71], v[52:55], v[24:27], v[68:71]\n\t" \
  "ds_read_b64_tr_b16 v[48:49], v59 offset:4096\n\t" \
  "v_mfma_f32_16x16x32_bf16 v[76:79], v[52:55], v[28:31], v[76:79]\n\t" \
  "ds_read_b64_tr_b16 v[50:51], v59 offset:4608\n\t" \
  "s_addk_i32 %[it], 1\n\t" \
  "v_mfma_f32_16x16x32_bf16 v[84:87], v[52:55], v[32:35], v[84:87]\n\t" \
  "s_cmp_lt_u32 %[it], 6\n\t" \
  "v_mfma_f32_16x16x32_bf16 v[92:95], v[52:55], v[36:39], v[92:95]\n\t" \
  "s_cbranch_scc1 N0a_%=\n\t" \
  "s_setprio 3\n\t" \
  "v_cvt_pk_bf16_f32 v120, v120, v121\n\t" \
  "v_cvt_pk_bf16_f32 v121, v122, v123\n\t" \
  "v_cvt_pk_bf16_f32 v122, v124, v125\n\t" \
  "v_cvt_pk_bf16_f32 v123, v126, v127\n\t" \
  "v_cvt_pk_bf16_f32 v28, v152, v153\n\t" \
  "v_cvt_pk_bf16_f32 v29, v154, v155\n\t" \
  "v_cvt_pk_bf16_f32 v38, v156, v157\n\t" \
  "v_cvt_pk_bf16_f32 v39, v158, v159\n\t" \
  "ds_write_b128 v63, v[120:123]\n\t" \
  "ds_write2_b64 v63, v[28:29], v[38:39] offset0:128 offset1:129\n\t" \
  "v_mov_b32 v57, %[fa]\n\t" \
  "v_mov_b32 v58, 1\n\t" \
  "s_mov_b64 exec, 1\n\t" \
  "ds_write_b32 v57, v58\n\t" \
  "s_mov_b64 exec, -1\n\t" \
  "v_cvt_pk_bf16_f32 v160, v160, v161\n\t" \
  "v_cvt_pk_bf16_f32 v161, v162, v163\n\t" \
  "v_cvt_pk_bf16_f32 v162, v164, v165\n\t" \
  "v_cvt_pk_bf16_f32 v163, v166, v167\n\t" \
  "s_branch NX_%=\n\t" \
  "NB_%=:\n\t" \
  "ds_read_b64_tr_b16 v[40:41], v60 offset:20480\n\t" \
  "ds_read_b64_tr_b16 v[42:43], v60 offset:20992\n\t" \
  "ds_read_b64_tr_b16 v[44:45], v60 offset:16384\n\t" \
  "ds_read_b64_tr_b16 v[46:47], v60 offset:16896\n\t" \
  "ds_read_b64_tr_b16 v[48:49], v61 offset:20480\n\t" \
  "ds_read_b64_tr_b16 v[50:51], v61 offset:20992\n\t" \
  "ds_read_b128 v[0:3], v56 offset:36864\n\t" \
  "ds_read_b128 v[4:7], v56 offset:47104\n\t" \
  "ds_read_b128 v[8:11], v56 offset:49152\n\t" \
  "ds_read_b128 v[12:15], v56 offset:51200\n\t" \
  "ds_read_b128 v[16:19], v56 offset:53248\n\t" \
  "s_waitcnt lgkmcnt(0)\n\t" \
  "s_branch N4b_%=\n\t" \
  "N0b_%=:\n\t" \
  "s_waitcnt lgkmcnt(4)\n\t" \
  "v_mfma_f32_16x16x32_bf16 v[160:163], v[0:3], v[40:43], v[160:163]\n\t" \
  "ds_read_b64_tr_b16 v[52:53], v59 offset:4096\n\t" \
  "v_mfma_f32_16x16x32_bf16 v[128:131], v[4:7], v[40:43], v[128:131]\n\t" \
  "ds_read_b64_tr_b16 v[54:55], v59 offset:4608\n\t" \
  "v_mfma_f32_16x16x32_bf16 v[136:139], v[8:11], v[40:43], v[136:139]\n\t" \
  "s_barrier\n\t" \
  "v_mfma_f32_16x16x32_bf16 v[144:147], v[12:15], v[40:43], v[144:147]\n\t" \
  "v_mfma_f32_16x16x32_bf16 v[152:155], v[16:19], v[40:43], v[152:155]\n\t" \
  "s_waitcnt lgkmcnt(4)\n\t" \
  "v_mfma_f32_16x16x32_bf16 v[96:99], v[44:47], v[4:7], v[96:99]\n\t" \
  "ds_read_b64_tr_b16 v[40:41], v58 offset:0\n\t" \
  "v_mfma_f32_16x16x32_bf16 v[104:107], v[44:47], v[8:11], v[104:107]\n\t" \
  "ds_read_b64_tr_b16 v[42:43], v58 offset:512\n\t" \
  "v_mfma_f32_16x16x32_bf16 v[112:115], v[44:47], v[12:15], v[112:115]\n\t" \
  "ds_read_b128 v[20:23], v57 offset:0\n\t" \
  "v_mfma_f32_16x16x32_bf16 v[120:123], v[44:47], v[16:19], v[120:123]\n\t" \
  "ds_read_b128 v[24:27], v57 offset:10240\n\t" \
  "s_waitcnt lgkmcnt(6)\n\t" \
  "v_mfma_f32_16x16x32_bf16 v[164:167], v[0:3], v[48:51], v[164:167]\n\t" \
  "ds_read_b64_tr_b16 v[44:45], v59 offset:0\n\t" \
  "v_mfma_f32_16x16x32_bf16 v[132:135], v[4:7], v[48:51], v[132:135]\n\t" \
  "ds_read_b64_tr_b16 v[46:47], v59 offset:512\n\t" \
  "v_mfma_f32_16x16x32_bf16 v[140:143], v[8:11], v[48:51], v[140:143]\n\t" \
  "ds_read_b128 v[28:31], v57 offset:12288\n\t" \
  "v_mfma_f32_16x16x32_bf16 v[148:151], v[12:15], v[48:51], v[148:151]\n\t" \
  "ds_read_b128 v[32:35], v57 offset:14336\n\t" \
  "v_mfma_f32_16x16x32_bf16 v[156:159], v[16:19], v[48:51], v[156:159]\n\t" \
  "s_waitcnt lgkmcnt(8)\n\t" \
  "v_mfma_f32_16x16x32_bf16 v[100:103], v[52:55], v[4:7], v[100:103]\n\t" \
  "ds_read_b64_tr_b16 v[48:49], v58 offset:20480\n\t" \
  "v_mfma_f32_16x16x32_bf16 v[108:111], v[52:55], v[8:11], v[108:111]\n\t" \
  "ds_read_b64_tr_b16 v[50:51], v58 offset:20992\n\t" \
  "v_mfma_f32_16x16x32_bf16 v[116:119], v[52:55], v[12:15], v[116:119]\n\t" \
  "ds_read_b128 v[36:39], v57 offset:16384\n\t" \
  "v_mfma_f32_16x16x32_bf16 v[124:127], v[52:55], v[16:19], v[124:127]\n\t" \
  "s_waitcnt lgkmcnt(9)\n\t" \
  "v_mfma_f32_16x16x32_bf16 v[64:67], v[40:43], v[4:7], v[64:67]\n\t" \
  "ds_read_b64_tr_b16 v[52:53], v58 offset:16384\n\t" \
  "v_mfma_f32_16x16x32_bf16 v[72:75], v[40:43], v[8:11], v[72:75]\n\t" \
  "ds_read_b64_tr_b16 v[54:55], v58 offset:16896\n\t" \
  "v_mfma_f32_16x16x32_bf16 v[80:83], v[40:43], v[12:15], v[80:83]\n\t" \
  "v_mfma_f32_16x16x32_bf16 v[88:91], v[40:43], v[16:19], v[88:91]\n\t" \
  "s_waitcnt lgkmcnt(7)\n\t" \
  "v_mfma_f32_16x16x32_bf16 v[68:71], v[44:47], v[4:7], v[68:71]\n\t" \
  "ds_read_b64_tr_b16 v[40:41], v59 offset:20480\n\t" \
  "v_mfma_f32_16x16x32_bf16 v[76:79], v[44:47], v[8:11], v[76:79]\n\t" \
  "ds_read_b64_tr_b16 v[42:43], v59 offset:20992\n\t" \
  "v_mfma_f32_16x16x32_bf16 v[84:87], v[44:47], v[12:15], v[84:87]\n\t" \
  "v_mfma_f32_16x16x32_bf16 v[92:95], v[44:47], v[16:19], v[92:95]\n\t" \
  "N1b_%=:\n\t" \
  "s_waitcnt lgkmcnt(4)\n\t" \
  "v_mfma_f32_16x16x32_bf16 v[160:163], v[20:23], v[48:51], v[160:163]\n\t" \
  "ds_read_b64_tr_b16 v[44:45], v59 offset:16384\n\t" \
  "v_mfma_f32_16x16x32_bf16 v[128:131], v[24:27], v[48:51], v[128:131]\n\t" \
  "ds_read_b64_tr_b16 v[46:47], v59 offset:16896\n\t" \
  "v_mfma_f32_16x16x32_bf16 v[136:139], v[28:31], v[48:51], v[136:139]\n\t" \
  "s_barrier\n\t" \
  "v_mfma_f32_16x16x32_bf16 v[144:147], v[32:35], v[48:51], v[144:147]\n\t" \
  "v_mfma_f32_16x16x32_bf16 v[152:155], v[36:39], v[48:51], v[152:155]\n\t" \
  "s_waitcnt lgkmcnt(4)\n\t" \
  "v_mfma_f32_16x16x32_bf16 v[96:99], v[52:55], v[24:27], v[96:99]\n\t" \
  "ds_read_b64_tr_b16 v[48:49], v58 offset:12288\n\t" \
  "v_mfma_f32_16x16x32_bf16 v[104:107], v[52:55], v[28:31], v[104:107]\n\t" \
  "ds_read_b64_tr_b16 v[50:51], v58 offset:12800\n\t" \
  "v_mfma_f32_16x16x32_bf16 v[112:115], v[52:55], v[32:35], v[112:115]\n\t" \
  "ds_read_b128 v[0:3], v56 offset:18432\n\t" \
  "v_mfma_f32_16x16x32_bf16 v[120:123], v[52:55], v[36:39], v[120:123]\n\t" \
  "ds_read_b128 v[4:7], v56 offset:28672\n\t" \
  "s_waitcnt lgkmcnt(6)\n\t" \
  "v_mfma_f32_16x16x32_bf16 v[164:167], v[20:23], v[40:43], v[164:167]\n\t" \
  "ds_read_b64_tr_b16 v[52:53], v59 offset:12288\n\t" \
  "v_mfma_f32_16x16x32_bf16 v[132:135], v[24:27], v[40:43], v[132:135]\n\t" \
  "ds_read_b64_tr_b16 v[54:55], v59 offset:12800\n\t" \
  "v_mfma_f32_16x16x32_bf16 v[140:143], v[28:31], v[40:43], v[140:143]\n\t" \
  "ds_read_b128 v[8:11], v56 offset:30720\n\t" \
  "v_mfma_f32_16x16x32_bf16 v[148:151], v[32:35], v[40:43], v[148:151]\n\t" \
  "ds_read_b128 v[12:15], v56 offset:32768\n\t" \
  "v_mfma_f32_16x16x32_bf16 v[156:159], v[36:39], v[40:43], v[156:159]\n\t" \
  "s_waitcnt lgkmcnt(8)\n\t" \
  "v_mfma_f32_16x16x32_bf16 v[100:103], v[44:47], v[24:27], v[100:103]\n\t" \
  "ds_read_b64_tr_b16 v[40:41], v58 offset:32768\n\t" \
  "v_mfma_f32_16x16x32_bf16 v[108:111], v[44:47], v[28:31], v[108:111]\n\t" \
  "ds_read_b64_tr_b16 v[42:43], v58 offset:33280\n\t" \
  "v_mfma_f32_16x16x32_bf16 v[116:119], v[44:47], v[32:35], v[116:119]\n\t" \
  "ds_read_b128 v[16:19], v56 offset:34816\n\t" \
  "v_mfma_f32_16x16x32_bf16 v[124:127], v[44:47], v[36:39], v[124:127]\n\t" \
  "s_waitcnt lgkmcnt(9)\n\t" \
  "v_mfma_f32_16x16x32_bf16 v[64:67], v[48:51], v[24:27], v[64:67]\n\t" \
  "ds_read_b64_tr_b16 v[44:45], v58 offset:28672\n\t" \
  "v_mfma_f32_16x16x32_bf16 v[72:75], v[48:51], v[28:31], v[72:75]\n\t" \
  "ds_read_b64_tr_b16 v[46:47], v58 offset:29184\n\t" \
  "v_mfma_f32_16x16x32_bf16 v[80:83], v[48:51], v[32:35], v[80:83]\n\t" \
  "v_mfma_f32_16x16x32_bf16 v[88:91], v[48:51], v[36:39], v[88:91]\n\t" \
  "s_waitcnt lgkmcnt(7)\n\t" \
  "v_mfma_f32_16x16x32_bf16 v[68:71], v[52:55], v[24:27], v[68:71]\n\t" \
  "ds_read_b64_tr_b16 v[48:49], v59 offset:32768\n\t" \
  "v_mfma_f32_16x16x32_bf16 v[76:79], v[52:55], v[28:31], v[76:79]\n\t" \
  "ds_read_b64_tr_b16 v[50:51], v59 offset:33280\n\t" \
  "v_mfma_f32_16x16x32_bf16 v[84:87], v[52:55], v[32:35], v[84:87]\n\t" \
  "v_mfma_f32_16x16x32_bf16 v[92:95], v[52:55], v[36:39], v[92:95]\n\t" \
  "N2b_%=:\n\t" \
  "s_waitcnt lgkmcnt(4)\n\t" \
  "v_mfma_f32_16x16x32_bf16 v[160:163], v[0:3], v[40:43], v[160:163]\n\t" \
  "ds_read_b64_tr_b16 v[52:53], v59 offset:28672\n\t" \
  "v_mfma_f32_16x16x32_bf16 v[128:131], v[4:7], v[40:43], v[128:131]\n\t" \
  "ds_read_b64_tr_b16 v[54:55], v59 offset:29184\n\t" \
  "v_mfma_f32_16x16x32_bf16 v[136:139], v[8:11], v[40:43], v[136:139]\n\t" \
  "s_barrier\n\t" \
  "v_mfma_f32_16x16x32_bf16 v[144:147], v[12:15], v[40:43], v[144:147]\n\t" \
  "v_mfma_f32_16x16x32_bf16 v[152:155], v[16:19], v[40:43], v[152:155]\n\t" \
  "s_waitcnt lgkmcnt(4)\n\t" \
  "v_mfma_f32_16x16x32_bf16 v[96:99], v[44:47], v[4:7], v[96:99]\n\t" \
  "ds_read_b64_tr_b16 v[40:41], v58 offset:24576\n\t" \
  "v_mfma_f32_16x16x32_bf16 v[104:107], v[44:47], v[8:11], v[104:107]\n\t" \
  "ds_read_b64_tr_b16 v[42:43], v58 offset:25088\n\t" \
  "v_mfma_f32_16x16x32_bf16 v[112:115], v[44:47], v[12:15], v[112:115]\n\t" \
  "ds_read_b128 v[20:23], v57 offset:18432\n\t" \
  "v_mfma_f32_16x16x32_bf16 v[120:123], v[44:47], v[16:19], v[120:123]\n\t" \
  "ds_read_b128 v[24:27], v57 offset:28672\n\t" \
  "s_waitcnt lgkmcnt(6)\n\t" \
  "v_mfma_f32_16x16x32_bf16 v[164:167], v[0:3], v[48:51], v[164:167]\n\t" \
  "ds_read_b64_tr_b16 v[44:45], v59 offset:24576\n\t" \
  "v_mfma_f32_16x16x32_bf16 v[132:135], v[4:7], v[48:51], v[132:135]\n\t" \
  "ds_read_b64_tr_b16 v[46:47], v59 offset:25088\n\t" \
  "v_mfma_f32_16x16x32_bf16 v[140:143], v[8:11], v[48:51], v[140:143]\n\t" \
  "ds_read_b128 v[28:31], v57 offset:30720\n\t" \
  "v_mfma_f32_16x16x32_bf16 v[148:151], v[12:15], v[48:51], v[148:151]\n\t" \
  "ds_read_b128 v[32:35], v57 offset:32768\n\t" \
  "v_mfma_f32_16x16x32_bf16 v[156:159], v[16:19], v[48:51], v[156:159]\n\t" \
  "s_waitcnt lgkmcnt(8)\n\t" \
  "v_mfma_f32_16x16x32_bf16 v[100:103], v[52:55], v[4:7], v[100:103]\n\t" \
  "ds_read_b64_tr_b16 v[48:49], v60 offset:8192\n\t" \
  "v_mfma_f32_16x16x32_bf16 v[108:111], v[52:55], v[8:11], v[108:111]\n\t" \
  "ds_read_b64_tr_b16 v[50:51], v60 offset:8704\n\t" \
  "v_mfma_f32_16x16x32_bf16 v[116:119], v[52:55], v[12:15], v[116:119]\n\t" \
  "ds_read_b128 v[36:39], v57 offset:34816\n\t" \
  "v_mfma_f32_16x16x32_bf16 v[124:127], v[52:55], v[16:19], v[124:127]\n\t" \
  "s_waitcnt lgkmcnt(9)\n\t" \
  "v_mfma_f32_16x16x32_bf16 v[64:67], v[40:43], v[4:7], v[64:67]\n\t" \
  "ds_read_b64_tr_b16 v[52:53], v60 offset:4096\n\t" \
  "v_mfma_f32_16x16x32_bf16 v[72:75], v[40:43], v[8:11], v[72:75]\n\t" \
  "ds_read_b64_tr_b16 v[54:55], v60 offset:4608\n\t" \
  "v_mfma_f32_16x16x32_bf16 v[80:83], v[40:43], v[12:15], v[80:83]\n\t" \
  "v_mfma_f32_16x16x32_bf16 v[88:91], v[40:43], v[16:19], v[88:91]\n\t" \
  "s_waitcnt lgkmcnt(7)\n\t" \
  "v_mfma_f32_16x16x32_bf16 v[68:71], v[44:47], v[4:7], v[68:71]\n\t" \
  "ds_read_b64_tr_b16 v[40:41], v61 offset:8192\n\t" \
  "v_mfma_f32_16x16x32_bf16 v[76:79], v[44:47], v[8:11], v[76:79]\n\t" \
  "ds_read_b64_tr_b16 v[42:43], v61 offset:8704\n\t" \
  "v_mfma_f32_16x16x32_bf16 v[84:87], v[44:47], v[12:15], v[84:87]\n\t" \
  "v_mfma_f32_16x16x32_bf16 v[92:95], v[44:47], v[16:19], v[92:95]\n\t" \
  "N3b_%=:\n\t" \
  "s_waitcnt lgkmcnt(4)\n\t" \
  "v_mfma_f32_16x16x32_bf16 v[160:163], v[20:23], v[48:51], v[160:163]\n\t" \
  "ds_read_b64_tr_b16 v[44:45], v61 offset:4096\n\t" \
  "v_mfma_f32_16x16x32_bf16 v[128:131], v[24:27], v[48:51], v[128:131]\n\t" \
  "ds_read_b64_tr_b16 v[46:47], v61 offset:4608\n\t" \
  "v_mfma_f32_16x16x32_bf16 v[136:139], v[28:31], v[48:51], v[136:139]\n\t" \
  "s_barrier\n\t" \
  "v_mfma_f32_16x16x32_bf16 v[144:147], v[32:35], v[48:51], v[144:147]\n\t" \
  "v_mfma_f32_16x16x32_bf16 v[152:155], v[36:39], v[48:51], v[152:155]\n\t" \
  "s_waitcnt lgkmcnt(4)\n\t" \
  "v_mfma_f32_16x16x32_bf16 v[96:99], v[52:55], v[24:27], v[96:99]\n\t" \
  "ds_read_b64_tr_b16 v[48:49], v60 offset:0\n\t" \
  "v_mfma_f32_16x16x32_bf16 v[104:107], v[52:55], v[28:31], v[104:107]\n\t" \
  "ds_read_b64_tr_b16 v[50:51], v60 offset:512\n\t" \
  "v_mfma_f32_16x16x32_bf16 v[112:115], v[52:55], v[32:35], v[112:115]\n\t" \
  "ds_read_b128 v[0:3], v56 offset:36864\n\t" \
  "v_mfma_f32_16x16x32_bf16 v[120:123], v[52:55], v[36:39], v[120:123]\n\t" \
  "ds_read_b128 v[4:7], v56 offset:47104\n\t" \
  "s_waitcnt lgkmcnt(6)\n\t" \
  "v_mfma_f32_16x16x32_bf16 v[164:167], v[20:23], v[40:43], v[164:167]\n\t" \
  "ds_read_b64_tr_b16 v[52:53], v61 offset:0\n\t" \
  "v_mfma_f32_16x16x32_bf16 v[132:135], v[24:27], v[40:43], v[132:135]\n\t" \
  "ds_read_b64_tr_b16 v[54:55], v61 offset:512\n\t" \
  "v_mfma_f32_16x16x32_bf16 v[140:143], v[28:31], v[40:43], v[140:143]\n\t" \
  "ds_read_b128 v[8:11], v56 offset:49152\n\t" \
  "v_mfma_f32_16x16x32_bf16 v[148:151], v[32:35], v[40:43], v[148:151]\n\t" \
  "ds_read_b128 v[12:15], v56 offset:51200\n\t" \
  "v_mfma_f32_16x16x32_bf16 v[156:159], v[36:39], v[40:43], v[156:159]\n\t" \
  "s_waitcnt lgkmcnt(8)\n\t" \
  "v_mfma_f32_16x16x32_bf16 v[100:103], v[44:47], v[24:27], v[100:103]\n\t" \
  "ds_read_b64_tr_b16 v[40:41], v60 offset:20480\n\t" \
  "v_mfma_f32_16x16x32_bf16 v[108:111], v[44:47], v[28:31], v[108:111]\n\t" \
  "ds_read_b64_tr_b16 v[42:43], v60 offset:20992\n\t" \
  "v_mfma_f32_16x16x32_bf16 v[116:119], v[44:47], v[32:35], v[116:119]\n\t" \
  "ds_read_b128 v[16:19], v56 offset:53248\n\t" \
  "v_mfma_f32_16x16x32_bf16 v[124:127], v[44:47], v[36:39], v[124:127]\n\t" \
  "s_waitcnt lgkmcnt(9)\n\t" \
  "v_mfma_f32_16x16x32_bf16 v[64:67], v[48:51], v[24:27], v[64:67]\n\t" \
  "ds_read_b64_tr_b16 v[44:45], v60 offset:16384\n\t" \
  "v_mfma_f32_16x16x32_bf16 v[72:75], v[48:51], v[28:31], v[72:75]\n\t" \
  "ds_read_b64_tr_b16 v[46:47], v60 offset:16896\n\t" \
  "v_mfma_f32_16x16x32_bf16 v[80:83], v[48:51], v[32:35], v[80:83]\n\t" \
  "v_mfma_f32_16x16x32_bf16 v[88:91], v[48:51], v[36:39], v[88:91]\n\t" \
  "s_waitcnt lgkmcnt(7)\n\t" \
  "v_mfma_f32_16x16x32_bf16 v[68:71], v[52:55], v[24:27], v[68:71]\n\t" \
  "ds_read_b64_tr_b16 v[48:49], v61 offset:20480\n\t" \
  "v_mfma_f32_16x16x32_bf16 v[76:79], v[52:55], v[28:31], v[76:79]\n\t" \
  "ds_read_b64_tr_b16 v[50:51], v61 offset:20992\n\t" \
  "v_mfma_f32_16x16x32_bf16 v[84:87], v[52:55], v[32:35], v[84:87]\n\t" \
  "v_mfma_f32_16x16x32_bf16 v[92:95], v[52:55], v[36:39], v[92:95]\n\t" \
  "N4b_%=:\n\t" \
  "s_waitcnt lgkmcnt(4)\n\t" \
  "v_mfma_f32_16x16x32_bf16 v[160:163], v[0:3], v[40:43], v[160:163]\n\t" \
  "ds_read_b64_tr_b16 v[52:53], v61 offset:16384\n\t" \
  "v_mfma_f32_16x16x32_bf16 v[128:131], v[4:7], v[40:43], v[128:131]\n\t" \
  "ds_read_b64_tr_b16 v[54:55], v61 offset:16896\n\t" \
  "v_mfma_f32_16x16x32_bf16 v[136:139], v[8:11], v[40:43], v[136:139]\n\t" \
  "s_barrier\n\t" \
  "v_mfma_f32_16x16x32_bf16 v[144:147], v[12:15], v[40:43], v[144:147]\n\t" \
  "v_mfma_f32_16x16x32_bf16 v[152:155], v[16:19], v[40:43], v[152:155]\n\t" \
  "s_waitcnt lgkmcnt(4)\n\t" \
  "v_mfma_f32_16x16x32_bf16 v[96:99], v[44:47], v[4:7], v[96:99]\n\t" \
  "ds_read_b64_tr_b16 v[40:41], v60 offset:12288\n\t" \
  "v_mfma_f32_16x16x32_bf16 v[104:107], v[44:47], v[8:11], v[104:107]\n\t" \
  "ds_read_b64_tr_b16 v[42:43], v60 offset:12800\n\t" \
  "v_mfma_f32_16x16x32_bf16 v[112:115], v[44:47], v[12:15], v[112:115]\n\t" \
  "ds_read_b128 v[20:23], v57 offset:36864\n\t" \
  "v_mfma_f32_16x16x32_bf16 v[120:123], v[44:47], v[16:19], v[120:123]\n\t" \
  "ds_read_b128 v[24:27], v57 offset:47104\n\t" \
  "s_waitcnt lgkmcnt(6)\n\t" \
  "v_mfma_f32_16x16x32_bf16 v[164:167], v[0:3], v[48:51], v[164:167]\n\t" \
  "ds_read_b64_tr_b16 v[44:45], v61 offset:12288\n\t" \
  "v_mfma_f32_16x16x32_bf16 v[132:135], v[4:7], v[48:51], v[132:135]\n\t" \
  "ds_read_b64_tr_b16 v[46:47], v61 offset:12800\n\t" \
  "v_mfma_f32_16x16x32_bf16 v[140:143], v[8:11], v[48:51], v[140:143]\n\t" \
  "ds_read_b128 v[28:31], v57 offset:49152\n\t" \
  "v_mfma_f32_16x16x32_bf16 v[148:151], v[12:15], v[48:51], v[148:151]\n\t" \
  "ds_read_b128 v[32:35], v57 offset:51200\n\t" \
  "v_mfma_f32_16x16x32_bf16 v[156:159], v[16:19], v[48:51], v[156:159]\n\t" \
  "s_waitcnt lgkmcnt(8)\n\t" \
  "v_mfma_f32_16x16x32_bf16 v[100:103], v[52:55], v[4:7], v[100:103]\n\t" \
  "ds_read_b64_tr_b16 v[48:49], v60 offset:32768\n\t" \
  "v_mfma_f32_16x16x32_bf16 v[108:111], v[52:55], v[8:11], v[108:111]\n\t" \
  "ds_read_b64_tr_b16 v[50:51], v60 offset:33280\n\t" \
  "v_mfma_f32_16x16x32_bf16 v[116:119], v[52:55], v[12:15], v[116:119]\n\t" \
  "ds_read_b128 v[36:39], v57 offset:53248\n\t" \
  "v_mfma_f32_16x16x32_bf16 v[124:127], v[52:55], v[16:19], v[124:127]\n\t" \
  "s_waitcnt lgkmcnt(9)\n\t" \
  "v_mfma_f32_16x16x32_bf16 v[64:67], v[40:43], v[4:7], v[64:67]\n\t" \
  "ds_read_b64_tr_b16 v[52:53], v60 offset:28672\n\t" \
  "v_mfma_f32_16x16x32_bf16 v[72:75], v[40:43], v[8:11], v[72:75]\n\t" \
  "ds_read_b64_tr_b16 v[54:55], v60 offset:29184\n\t" \
  "v_mfma_f32_16x16x32_bf16 v[80:83], v[40:43], v[12:15], v[80:83]\n\t" \
  "v_mfma_f32_16x16x32_bf16 v[88:91], v[40:43], v[16:19], v[88:91]\n\t" \
  "s_waitcnt lgkmcnt(7)\n\t" \
  "v_mfma_f32_16x16x32_bf16 v[68:71], v[44:47], v[4:7], v[68:71]\n\t" \
  "ds_read_b64_tr_b16 v[40:41], v61 offset:32768\n\t" \
  "v_mfma_f32_16x16x32_bf16 v[76:79], v[44:47], v[8:11], v[76:79]\n\t" \
  "ds_read_b64_tr_b16 v[42:43], v61 offset:33280\n\t" \
  "v_mfma_f32_16x16x32_bf16 v[84:87], v[44:47], v[12:15], v[84:87]\n\t" \
  "v_mfma_f32_16x16x32_bf16 v[92:95], v[44:47], v[16:19], v[92:95]\n\t" \
  "N5b_%=:\n\t" \
  "s_waitcnt lgkmcnt(4)\n\t" \
  "v_mfma_f32_16x16x32_bf16 v[160:163], v[20:23], v[48:51], v[160:163]\n\t" \
  "ds_read_b64_tr_b16 v[44:45], v61 offset:28672\n\t" \
  "v_mfma_f32_16x16x32_bf16 v[128:131], v[24:27], v[48:51], v[128:131]\n\t" \
  "ds_read_b64_tr_b16 v[46:47], v61 offset:29184\n\t" \
  "v_mfma_f32_16x16x32_bf16 v[136:139], v[28:31], v[48:51], v[136:139]\n\t" \
  "s_barrier\n\t" \
  "v_mfma_f32_16x16x32_bf16 v[144:147], v[32:35], v[48:51], v[144:147]\n\t" \
  "v_mfma_f32_16x16x32_bf16 v[152:155], v[36:39], v[48:51], v[152:155]\n\t" \
  "s_waitcnt lgkmcnt(4)\n\t" \
  "v_mfma_f32_16x16x32_bf16 v[96:99], v[52:55], v[24:27], v[96:99]\n\t" \
  "ds_read_b64_tr_b16 v[48:49], v60 offset:24576\n\t" \
  "v_mfma_f32_16x16x32_bf16 v[104:107], v[52:55], v[28:31], v[104:107]\n\t" \
  "ds_read_b64_tr_b16 v[50:51], v60 offset:25088\n\t" \
  "v_mfma_f32_16x16x32_bf16 v[112:115], v[52:55], v[32:35], v[112:115]\n\t" \
  "ds_read_b128 v[0:3], v56 offset:0\n\t" \
  "v_mfma_f32_16x16x32_bf16 v[120:123], v[52:55], v[36:39], v[120:123]\n\t" \
  "ds_read_b128 v[4:7], v56 offset:10240\n\t" \
  "s_waitcnt lgkmcnt(6)\n\t" \
  "v_mfma_f32_16x16x32_bf16 v[164:167], v[20:23], v[40:43], v[164:167]\n\t" \
  "ds_read_b64_tr_b16 v[52:53], v61 offset:24576\n\t" \
  "v_mfma_f32_16x16x32_bf16 v[132:135], v[24:27], v[40:43], v[132:135]\n\t" \
  "ds_read_b64_tr_b16 v[54:55], v61 offset:25088\n\t" \
  "v_mfma_f32_16x16x32_bf16 v[140:143], v[28:31], v[40:43], v[140:143]\n\t" \
  "ds_read_b128 v[8:11], v56 offset:12288\n\t" \
  "v_mfma_f32_16x16x32_bf16 v[148:151], v[32:35], v[40:43], v[148:151]\n\t" \
  "ds_read_b128 v[12:15], v56 offset:14336\n\t" \
  "v_mfma_f32_16x16x32_bf16 v[156:159], v[36:39], v[40:43], v[156:159]\n\t" \
  "s_waitcnt lgkmcnt(8)\n\t" \
  "v_mfma_f32_16x16x32_bf16 v[100:103], v[44:47], v[24:27], v[100:103]\n\t" \
  "ds_read_b64_tr_b16 v[40:41], v58 offset:8192\n\t" \
  "v_mfma_f32_16x16x32_bf16 v[108:111], v[44:47], v[28:31], v[108:111]\n\t" \
  "ds_read_b64_tr_b16 v[42:43], v58 offset:8704\n\t" \
  "v_mfma_f32_16x16x32_bf16 v[116:119], v[44:47], v[32:35], v[116:119]\n\t" \
  "ds_read_b128 v[16:19], v56 offset:16384\n\t" \
  "v_mfma_f32_16x16x32_bf16 v[124:127], v[44:47], v[36:39], v[124:127]\n\t" \
  "s_waitcnt lgkmcnt(9)\n\t" \
  "v_mfma_f32_16x16x32_bf16 v[64:67], v[48:51], v[24:27], v[64:67]\n\t" \
  "ds_read_b64_tr_b16 v[44:45], v58 offset:4096\n\t" \
  "v_mfma_f32_16x16x32_bf16 v[72:75], v[48:51], v[28:31], v[72:75]\n\t" \
  "ds_read_b64_tr_b16 v[46:47], v58 offset:4608\n\t" \
  "v_mfma_f32_16x16x32_bf16 v[80:83], v[48:51], v[32:35], v[80:83]\n\t" \
  "v_mfma_f32_16x16x32_bf16 v[88:91], v[48:51], v[36:39], v[88:91]\n\t" \
  "s_waitcnt lgkmcnt(7)\n\t" \
  "v_mfma_f32_16x16x32_bf16 v[68:71], v[52:55], v[24:27], v[68:71]\n\t" \
  "ds_read_b64_tr_b16 v[48:49], v59 offset:8192\n\t" \
  "v_mfma_f32_16x16x32_bf16 v[76:79], v[52:55], v[28:31], v[76:79]\n\t" \
  "ds_read_b64_tr_b16 v[50:51], v59 offset:8704\n\t" \
  "s_addk_i32 %[it], 1\n\t" \
  "v_mfma_f32_16x16x32_bf16 v[84:87], v[52:55], v[32:35], v[84:87]\n\t" \
  "s_cmp_lt_u32 %[it], 6\n\t" \
  "v_mfma_f32_16x16x32_bf16 v[92:95], v[52:55], v[36:39], v[92:95]\n\t" \
  "s_cbranch_scc1 N0b_%=\n\t" \
  "s_setprio 3\n\t" \
  "v_cvt_pk_bf16_f32 v160, v160, v161\n\t" \
  "v_cvt_pk_bf16_f32 v161, v162, v163\n\t" \
  "v_cvt_pk_bf16_f32 v162, v164, v165\n\t" \
  "v_cvt_pk_bf16_f32 v163, v166, v167\n\t" \
  "ds_write_b128 v63, v[160:163] offset:2048\n\t" \
  "v_mov_b32 v57, %[fa]\n\t" \
  "v_mov_b32 v58, 1\n\t" \
  "s_mov_b64 exec, 1\n\t" \
  "ds_write_b32 v57, v58\n\t" \
  "s_mov_b64 exec, -1\n\t" \
  "v_cvt_pk_bf16_f32 v120, v120, v121\n\t" \
  "v_cvt_pk_bf16_f32 v121, v122, v123\n\t" \
  "v_cvt_pk_bf16_f32 v122, v124, v125\n\t" \
  "v_cvt_pk_bf16_f32 v123, v126, v127\n\t" \
  "v_cvt_pk_bf16_f32 v28, v152, v153\n\t" \
  "v_cvt_pk_bf16_f32 v29, v154, v155\n\t" \
  "v_cvt_pk_bf16_f32 v38, v156, v157\n\t" \
  "v_cvt_pk_bf16_f32 v39, v158, v159\n\t" \
  "NX_%=:\n\t" \
  "v_cvt_pk_bf16_f32 v96, v96, v97\n\t" \
  "v_cvt_pk_bf16_f32 v97, v98, v99\n\t" \
  "v_cvt_pk_bf16_f32 v98, v100, v101\n\t" \
  "v_cvt_pk_bf16_f32 v99, v102, v103\n\t" \
  "v_cvt_pk_bf16_f32 v104, v104, v105\n\t" \
  "v_cvt_pk_bf16_f32 v105, v106, v107\n\t" \
  "v_cvt_pk_bf16_f32 v106, v108, v109\n\t" \
  "v_cvt_pk_bf16_f32 v107, v110, v111\n\t" \
  "v_mov_b32 v60, 0xff61b1e6\n\t" \
  "v_cndmask_b32_e64 v56, v60, 0, s[68:69]\n\t" \
  "v_cndmask_b32_e64 v57, v60, 0, s[70:71]\n\t" \
  "v_cndmask_b32_e64 v58, v60, 0, s[72:73]\n\t" \
  "v_cndmask_b32_e64 v59, v60, 0, s[74:75]\n\t" \
  "v_cvt_pk_bf16_f32 v72, v72, v73\n\t" \
  "v_cvt_pk_bf16_f32 v73, v74, v75\n\t" \
  "v_cvt_pk_bf16_f32 v74, v76, v77\n\t" \
  "v_cvt_pk_bf16_f32 v75, v78, v79\n\t" \
  "v_cndmask_b32_e64 v52, v60, 0, s[76:77]\n\t" \
  "v_cndmask_b32_e64 v53, v60, 0, s[78:79]\n\t" \
  "v_cndmask_b32_e64 v54, v60, 0, s[80:81]\n\t" \
  "v_cndmask_b32_e64 v55, v60, 0, s[82:83]\n\t" \
  "v_cvt_pk_bf16_f32 v24, v136, v137\n\t" \
  "v_cvt_pk_bf16_f32 v25, v138, v139\n\t" \
  "v_cvt_pk_bf16_f32 v34, v140, v141\n\t" \
  "v_cvt_pk_bf16_f32 v35, v142, v143\n\t" \
  "v_cvt_pk_bf16_f32 v26, v144, v145\n\t" \
  "v_cvt_pk_bf16_f32 v27, v146, v147\n\t" \
  "v_cvt_pk_bf16_f32 v36, v148, v149\n\t" \
  "v_cvt_pk_bf16_f32 v37, v150, v151\n\t" \
  "v_mfma_f32_16x16x32_bf16 v[136:139], v[96:99], v[72:75], v[56:59]\n\t" \
  "v_cvt_pk_bf16_f32 v112, v112, v113\n\t" \
  "v_cvt_pk_bf16_f32 v113, v114, v115\n\t" \
  "v_mfma_f32_16x16x32_bf16 v[140:143], v[104:107], v[72:75], v[52:55]\n\t" \
  "v_cvt_pk_bf16_f32 v114, v116, v117\n\t" \
  "v_cvt_pk_bf16_f32 v115, v118, v119\n\t" \
  "v_cvt_pk_bf16_f32 v80, v80, v81\n\t" \
  "v_cvt_pk_bf16_f32 v81, v82, v83\n\t" \
  "v_cvt_pk_bf16_f32 v82, v84, v85\n\t" \
  "v_cvt_pk_bf16_f32 v83, v86, v87\n\t" \
  "v_cvt_pk_bf16_f32 v22, v128, v129\n\t" \
  "v_cvt_pk_bf16_f32 v23, v130, v131\n\t" \
  "v_mfma_f32_16x16x32_bf16 v[144:147], v[104:107], v[80:83], v[56:59]\n\t" \
  "v_cvt_pk_bf16_f32 v32, v132, v133\n\t" \
  "v_cvt_pk_bf16_f32 v33, v134, v135\n\t" \
  "v_mfma_f32_16x16x32_bf16 v[148:151], v[112:115], v[80:83], v[52:55]\n\t" \
  "v_cvt_pk_bf16_f32 v88, v88, v89\n\t" \
  "v_cvt_pk_bf16_f32 v89, v90, v91\n\t" \
  "v_cvt_pk_bf16_f32 v90, v92, v93\n\t" \
  "v_cvt_pk_bf16_f32 v91, v94, v95\n\t" \
  "v_mov_b32 v61, %[fp]\n\t" \
  "v_add_u32 v60, %[vo], v63\n\t" \
  "v_mfma_f32_16x16x32_bf16 v[152:155], v[112:115], v[88:91], v[56:59]\n\t" \
  "ds_read_b32 v0, v61\n\t" \
  "ds_read_b64 v[20:21], v60\n\t" \
  "ds_read_b64 v[30:31], v60 offset:8\n\t" \
  "s_mov_b64 exec, %[rm]\n\t" \
  "ds_read_b128 v[160:163], v63\n\t" \
  "s_mov_b64 exec, -1\n\t" \
  "v_mfma_f32_16x16x32_bf16 v[156:159], v[120:123], v[88:91], v[52:55]\n\t" \
  "v_cvt_pk_bf16_f32 v64, v64, v65\n\t" \
  "v_cvt_pk_bf16_f32 v65, v66, v67\n\t" \
  "v_cvt_pk_bf16_f32 v66, v68, v69\n\t" \
  "v_cvt_pk_bf16_f32 v67, v70, v71\n\t" \
  "v_max_f32 v136, v136, v137\n\t" \
  "v_max_f32 v138, v138, v139\n\t" \
  "v_mfma_f32_16x16x32_bf16 v[132:135], v[96:99], v[64:67], v[52:55]\n\t" \
  "v_max_f32 v140, v140, v141\n\t" \
  "v_max_f32 v142, v142, v143\n\t" \
  "AW_%=:\n\t" \
  "s_waitcnt lgkmcnt(0)\n\t" \
  "v_cmp_eq_u32 vcc, 0, v0\n\t" \
  "v_max3_f32 v5, v136, v138, v140\n\t" \
  "v_max_f32 v5, v5, v142\n\t" \
  "s_cbranch_vccnz AR_%=\n\t" \
  "s_cmp_eq_u32 %[z], 0\n\t" \
  "s_cbranch_scc1 AZ_%=\n\t" \
  "AY_%=:\n\t" \
  "v_mfma_f32_16x16x32_bf16 v[128:131], v[160:163], v[64:67], v[56:59]\n\t" \
  "v_max_f32 v144, v144, v145\n\t" \
  "v_max_f32 v146, v146, v147\n\t" \
  "v_max_f32 v148, v148, v149\n\t" \
  "v_max_f32 v150, v150, v151\n\t" \
  "v_max3_f32 v6, v144, v146, v148\n\t" \
  "v_max_f32 v6, v6, v150\n\t" \
  "v_max_f32 v152, v152, v153\n\t" \
  "v_max_f32 v154, v154, v155\n\t" \
  "v_max_f32 v156, v156, v157\n\t" \
  "v_max_f32 v158, v158, v159\n\t" \
  "v_max3_f32 v7, v152, v154, v156\n\t" \
  "v_max_f32 v7, v7, v158\n\t" \
  "v_mov_b32 v8, 0xc0c0100\n\t" \
  "v_mov_b32 v9, 0x1000c0c\n\t" \
  "v_max_f32 v128, v128, v129\n\t" \
  "v_max_f32 v130, v130, v131\n\t" \
  "v_max_f32 v132, v132, v133\n\t" \
  "v_max_f32 v134, v134, v135\n\t" \
  "v_max3_f32 v4, v128, v130, v132\n\t" \
  "v_max_f32 v4, v4, v134\n\t" \
  "v_cndmask_b32_e64 v60, v8, v9, s[84:85]\n\t" \
  "v_mov_b32 v8, 0xc0c0302\n\t" \
  "v_permlane16_swap_b32 v6, v7\n\t" \
  "v_permlane16_swap_b32 v4, v5\n\t" \
  "v_max_f32 v6, v6, v7\n\t" \
  "v_max_f32 v4, v4, v5\n\t" \
  "v_mov_b32 v9, 0x3020c0c\n\t" \
  "v_cndmask_b32_e64 v61, v8, v9, s[84:85]\n\t" \
  "v_permlane32_swap_b32 v4, v6\n\t" \
  "v_max_f32 v4, v4, v6\n\t" \
  "v_mul_f32 v6, 0xbe8293ee, v4\n\t" \
  "v_mul_f32 v4, 0xbe8293ee, v4\n\t" \
  "s_nop 1\n\t" \
  "v_permlane32_swap_b32 v4, v6\n\t" \
  "v_mov_b32 v5, v4\n\t" \
  "v_mov_b32 v7, v6\n\t" \
  "s_nop 0\n\t" \
  "v_permlane16_swap_b32 v4, v5\n\t" \
  "v_permlane16_swap_b32 v6, v7\n\t" \
  "v_fmamk_f32 v128, v128, 0x3e8293ee, v4\n\t" \
  "v_fmamk_f32 v130, v130, 0x3e8293ee, v4\n\t" \
  "v_fmamk_f32 v132, v132, 0x3e8293ee, v4\n\t" \
  "v_fmamk_f32 v134, v134, 0x3e8293ee, v4\n\t" \
  "v_fmamk_f32 v136, v136, 0x3e8293ee, v5\n\t" \
  "v_fmamk_f32 v138, v138, 0x3e8293ee, v5\n\t" \
  "v_fmamk_f32 v140, v140, 0x3e8293ee, v5\n\t" \
  "v_fmamk_f32 v142, v142, 0x3e8293ee, v5\n\t" \
  "v_fmamk_f32 v144, v144, 0x3e8293ee, v6\n\t" \
  "v_fmamk_f32 v146, v146, 0x3e8293ee, v6\n\t" \
  "v_fmamk_f32 v148, v148, 0x3e8293ee, v6\n\t" \
  "v_fmamk_f32 v150, v150, 0x3e8293ee, v6\n\t" \
  "v_fmamk_f32 v152, v152, 0x3e8293ee, v7\n\t" \
  "v_fmamk_f32 v154, v154, 0x3e8293ee, v7\n\t" \
  "v_fmamk_f32 v156, v156, 0x3e8293ee, v7\n\t" \
  "v_fmamk_f32 v158, v158, 0x3e8293ee, v7\n\t" \
  "v_exp_f32 v128, v128\n\t" \
  "v_exp_f32 v130, v130\n\t" \
  "v_exp_f32 v132, v132\n\t" \
  "v_exp_f32 v134, v134\n\t" \
  "v_exp_f32 v136, v136\n\t" \
  "v_exp_f32 v138, v138\n\t" \
  "v_exp_f32 v140, v140\n\t" \
  "v_exp_f32 v142, v142\n\t" \
  "v_exp_f32 v144, v144\n\t" \
  "v_exp_f32 v146, v146\n\t" \
  "v_exp_f32 v148, v148\n\t" \
  "v_exp_f32 v150, v150\n\t" \
  "v_exp_f32 v152, v152\n\t" \
  "v_exp_f32 v154, v154\n\t" \
  "v_exp_f32 v156, v156\n\t" \
  "v_exp_f32 v158, v158\n\t" \
  "v_add_f32 v4, v128, v130\n\t" \
  "v_add_f32 v8, v132, v134\n\t" \
  "v_add_f32 v5, v136, v138\n\t" \
  "v_add_f32 v9, v140, v142\n\t" \
  "v_add_f32 v6, v144, v146\n\t" \
  "v_add_f32 v10, v148, v150\n\t" \
  "v_add_f32 v7, v152, v154\n\t" \
  "v_add_f32 v11, v156, v158\n\t" \
  "v_add_f32 v4, v4, v8\n\t" \
  "v_add_f32 v5, v5, v9\n\t" \
  "v_add_f32 v6, v6, v10\n\t" \
  "v_add_f32 v7, v7, v11\n\t" \
  "v_permlane16_swap_b32 v4, v5\n\t" \
  "s_nop 0\n\t" \
  "v_permlane16_swap_b32 v6, v7\n\t" \
  "v_add_f32 v4, v4, v5\n\t" \
  "v_add_f32 v6, v6, v7\n\t" \
  "s_nop 1\n\t" \
  "v_permlane32_swap_b32 v4, v6\n\t" \
  "v_add_f32 v4, v4, v6\n\t" \
  "v_rcp_f32 v4, v4\n\t" \
  "s_nop 0\n\t" \
  "v_mov_b32 v6, v4\n\t" \
  "s_nop 1\n\t" \
  "v_permlane32_swap_b32 v4, v6\n\t" \
  "v_mov_b32 v5, v4\n\t" \
  "v_mov_b32 v7, v6\n\t" \
  "s_nop 0\n\t" \
  "v_permlane16_swap_b32 v4, v5\n\t" \
  "v_permlane16_swap_b32 v6, v7\n\t" \
  "v_mul_f32 v128, v128, v4\n\t" \
  "v_mul_f32 v130, v130, v4\n\t" \
  "v_mul_f32 v132, v132, v4\n\t" \
  "v_mul_f32 v134, v134, v4\n\t" \
  "v_cvt_pk_bf16_f32 v128, v128, v130\n\t" \
  "v_cvt_pk_bf16_f32 v132, v132, v134\n\t" \
  "v_perm_b32 v68, 0, v128, v60\n\t" \
  "v_perm_b32 v69, 0, v128, v61\n\t" \
  "v_perm_b32 v70, 0, v132, v60\n\t" \
  "v_perm_b32 v71, 0, v132, v61\n\t" \
  "v_mul_f32 v136, v136, v5\n\t" \
  "v_mul_f32 v138, v138, v5\n\t" \
  "v_mfma_f32_16x16x32_bf16 v[128:131], v[20:23], v[68:71], 0\n\t" \
  "v_mul_f32 v140, v140, v5\n\t" \
  "v_mul_f32 v142, v142, v5\n\t" \
  "v_cvt_pk_bf16_f32 v136, v136, v138\n\t" \
  "v_mfma_f32_16x16x32_bf16 v[132:135], v[30:33], v[68:71], 0\n\t" \
  "v_cvt_pk_bf16_f32 v140, v140, v142\n\t" \
  "v_perm_b32 v76, 0, v136, v60\n\t" \
  "v_perm_b32 v77, 0, v136, v61\n\t" \
  "v_perm_b32 v78, 0, v140, v60\n\t" \
  "v_perm_b32 v79, 0, v140, v61\n\t" \
  "v_mul_f32 v144, v144, v6\n\t" \
  "v_mul_f32 v146, v146, v6\n\t" \
  "v_mfma_f32_16x16x32_bf16 v[136:139], v[22:25], v[76:79], 0\n\t" \
  "v_mul_f32 v148, v148, v6\n\t" \
  "v_mul_f32 v150, v150, v6\n\t" \
  "v_cvt_pk_bf16_f32 v144, v144, v146\n\t" \
  "v_mfma_f32_16x16x32_bf16 v[140:143], v[32:35], v[76:79], 0\n\t" \
  "v_cvt_pk_bf16_f32 v148, v148, v150\n\t" \
  "v_perm_b32 v84, 0, v144, v60\n\t" \
  "v_perm_b32 v85, 0, v144, v61\n\t" \
  "v_perm_b32 v86, 0, v148, v60\n\t" \
  "v_perm_b32 v87, 0, v148, v61\n\t" \
  "v_cvt_pk_bf16_f32 v100, v128, v129\n\t" \
  "v_cvt_pk_bf16_f32 v101, v130, v131\n\t" \
  "v_mfma_f32_16x16x32_bf16 v[144:147], v[24:27], v[84:87], 0\n\t" \
  "v_cvt_pk_bf16_f32 v102, v132, v133\n\t" \
  "v_cvt_pk_bf16_f32 v103, v134, v135\n\t" \
  "global_store_dwordx4 v62, v[100:103], %[ot] offset:0 sc1\n\t" \
  "v_mfma_f32_16x16x32_bf16 v[148:151], v[34:37], v[84:87], 0\n\t" \
  "v_mul_f32 v152, v152, v7\n\t" \
  "v_mul_f32 v154, v154, v7\n\t" \
  "v_mul_f32 v156, v156, v7\n\t" \
  "v_mul_f32 v158, v158, v7\n\t" \
  "v_cvt_pk_bf16_f32 v152, v152, v154\n\t" \
  "v_cvt_pk_bf16_f32 v156, v156, v158\n\t" \
  "v_perm_b32 v92, 0, v152, v60\n\t" \
  "v_perm_b32 v93, 0, v152, v61\n\t" \
  "v_perm_b32 v94, 0, v156, v60\n\t" \
  "v_perm_b32 v95, 0, v156, v61\n\t" \
  "v_cvt_pk_bf16_f32 v108, v136, v137\n\t" \
  "v_cvt_pk_bf16_f32 v109, v138, v139\n\t" \
  "v_mfma_f32_16x16x32_bf16 v[152:155], v[26:29], v[92:95], 0\n\t" \
  "v_cvt_pk_bf16_f32 v110, v140, v141\n\t" \
  "v_cvt_pk_bf16_f32 v111, v142, v143\n\t" \
  "global_store_dwordx4 v62, v[108:111], %[ot] offset:1024 sc1\n\t" \
  "v_mfma_f32_16x16x32_bf16 v[156:159], v[36:39], v[92:95], 0\n\t" \
  "v_cvt_pk_bf16_f32 v116, v144, v145\n\t" \
  "v_cvt_pk_bf16_f32 v117, v146, v147\n\t" \
  "v_cvt_pk_bf16_f32 v118, v148, v149\n\t" \
  "v_cvt_pk_bf16_f32 v119, v150, v151\n\t" \
  "global_store_dwordx4 v62, v[116:119], %[ou] offset:0 sc1\n\t" \
  "v_cvt_pk_bf16_f32 v124, v152, v153\n\t" \
  "v_cvt_pk_bf16_f32 v125, v154, v155\n\t" \
  "s_nop 0\n\t" \
  "v_cvt_pk_bf16_f32 v126, v156, v157\n\t" \
  "v_cvt_pk_bf16_f32 v127, v158, v159\n\t" \
  "global_store_dwordx4 v62, v[124:127], %[ou] offset:1024 sc1\n\t" \
  "s_branch AE_%=\n\t" \
  "AR_%=:\n\t" \
  "s_addk_i32 %[it], 1\n\t" \
  "s_cmp_gt_u32 %[it], 0x400000\n\t" \
  "s_cbranch_scc1 AY_%=\n\t" \
  "ds_read_b32 v0, v61\n\t" \
  "ds_read_b64 v[20:21], v60\n\t" \
  "ds_read_b64 v[30:31], v60 offset:8\n\t" \
  "s_mov_b64 exec, %[rm]\n\t" \
  "ds_read_b128 v[160:163], v63\n\t" \
  "s_mov_b64 exec, -1\n\t" \
  "s_branch AW_%=\n\t" \
  "AZ_%=:\n\t" \
  "v_mov_b32 v0, 0xff61b1e6\n\t" \
  "v_mov_b32 v1, 0xcfa8965f\n\t" \
  "s_nop 3\n\t" \
  "v_cndmask_b32_e64 v56, v0, v1, s[68:69]\n\t" \
  "v_cndmask_b32_e64 v57, v0, v1, s[70:71]\n\t" \
  "v_cndmask_b32_e64 v58, v0, v1, s[72:73]\n\t" \
  "v_cndmask_b32_e64 v59, v0, v1, s[74:75]\n\t" \
  "s_nop 1\n\t" \
  "s_branch AY_%=\n\t" \
  "AE_%=:\n\t" \
  "s_nop 1\n\t"
#define N2_CLOB "v0", "v1", "v2", "v3", "v4", "v5", "v6", "v7", "v8", "v9", "v10", "v11", "v12", "v13", "v14", "v15", "v16", "v17", "v18", "v19", "v20", "v21", "v22", "v23", "v24", "v25", "v26", "v27", "v28", "v29", "v30", "v31", "v32", "v33", "v34", "v35", "v36", "v37", "v38", "v39", "v40", "v41", "v42", "v43", "v44", "v45", "v46", "v47", "v48", "v49", "v50", "v51", "v52", "v53", "v54", "v55", "v60", "v61", "v64", "v65", "v66", "v67", "v68", "v69", "v70", "v71", "v72", "v73", "v74", "v75", "v76", "v77", "v78", "v79", "v80", "v81", "v82", "v83", "v84", "v85", "v86", "v87", "v88", "v89", "v90", "v91", "v92", "v93", "v94", "v95", "v96", "v97", "v98", "v99", "v100", "v101", "v102", "v103", "v104", "v105", "v106", "v107", "v108", "v109", "v110", "v111", "v112", "v113", "v114", "v115", "v116", "v117", "v118", "v119", "v120", "v121", "v122", "v123", "v124", "v125", "v126", "v127", "v128", "v129", "v130", "v131", "v132", "v133", "v134", "v135", "v136", "v137", "v138", "v139", "v140", "v141", "v142", "v143", "v144", "v145", "v146", "v147", "v148", "v149", "v150", "v151", "v152", "v153", "v154", "v155", "v156", "v157", "v158", "v159", "v160", "v161", "v162", "v163", "v164", "v165", "v166", "v167", "s68", "s69", "s70", "s71", "s72", "s73", "s74", "s75", "s76", "s77", "s78", "s79", "s80", "s81", "s82", "s83", "s84", "s85"

#define CV_ASM \
  "ds_read_b64 v[0:1], v28 offset:0\n\t" \
  "ds_read_b64 v[2:3], v29 offset:0\n\t" \
  "ds_read_b64 v[4:5], v30 offset:1024\n\t" \
  "ds_read_b64 v[6:7], v31 offset:1024\n\t" \
  "ds_read_b64 v[8:9], v24 offset:2048\n\t" \
  "ds_read_b64 v[10:11], v25 offset:2048\n\t" \
  "ds_read_b64 v[12:13], v26 offset:3072\n\t" \
  "ds_read_b64 v[14:15], v27 offset:3072\n\t" \
  "ds_read_b64 v[16:17], v28 offset:4096\n\t" \
  "ds_read_b64 v[18:19], v29 offset:4096\n\t" \
  "ds_read_b64 v[20:21], v30 offset:5120\n\t" \
  "ds_read_b64 v[22:23], v31 offset:5120\n\t" \
  "s_waitcnt lgkmcnt(10)\n\t" \
  "v_mfma_f32_16x16x32_bf16 v[128:131], v[68:71], v[0:3], 0\n\t" \
  "v_mfma_f32_16x16x32_bf16 v[132:135], v[76:79], v[0:3], 0\n\t" \
  "v_mfma_f32_16x16x32_bf16 v[136:139], v[84:87], v[0:3], 0\n\t" \
  "v_mfma_f32_16x16x32_bf16 v[140:143], v[92:95], v[0:3], 0\n\t" \
  "v_mfma_f32_16x16x32_bf16 v[144:147], v[100:103], v[0:3], 0\n\t" \
  "v_mfma_f32_16x16x32_bf16 v[148:151], v[108:111], v[0:3], 0\n\t" \
  "v_mfma_f32_16x16x32_bf16 v[152:155], v[116:119], v[0:3], 0\n\t" \
  "v_mfma_f32_16x16x32_bf16 v[156:159], v[124:127], v[0:3], 0\n\t" \
  "ds_read_b64 v[0:1], v24 offset:6144\n\t" \
  "s_waitcnt lgkmcnt(9)\n\t" \
  "v_mfma_f32_16x16x32_bf16 v[128:131], v[64:67], v[4:7], v[128:131]\n\t" \
  "ds_read_b64 v[2:3], v25 offset:6144\n\t" \
  "v_mfma_f32_16x16x32_bf16 v[132:135], v[72:75], v[4:7], v[132:135]\n\t" \
  "v_mfma_f32_16x16x32_bf16 v[136:139], v[80:83], v[4:7], v[136:139]\n\t" \
  "v_mfma_f32_16x16x32_bf16 v[140:143], v[88:91], v[4:7], v[140:143]\n\t" \
  "v_mfma_f32_16x16x32_bf16 v[144:147], v[96:99], v[4:7], v[144:147]\n\t" \
  "v_mfma_f32_16x16x32_bf16 v[148:151], v[104:107], v[4:7], v[148:151]\n\t" \
  "v_mfma_f32_16x16x32_bf16 v[152:155], v[112:115], v[4:7], v[152:155]\n\t" \
  "v_mfma_f32_16x16x32_bf16 v[156:159], v[120:123], v[4:7], v[156:159]\n\t" \
  "ds_read_b64 v[4:5], v26 offset:7168\n\t" \
  "ds_bpermute_b32 v34, v32, v131\n\t" \
  "v_readlane_b32 %[p0], v131, 63\n\t" \
  "s_waitcnt lgkmcnt(10)\n\t" \
  "v_mfma_f32_16x16x32_bf16 v[132:135], v[68:71], v[8:11], v[132:135]\n\t" \
  "ds_read_b64 v[6:7], v27 offset:7168\n\t" \
  "v_mov_b32 v131, v130\n\t" \
  "v_mfma_f32_16x16x32_bf16 v[136:139], v[76:79], v[8:11], v[136:139]\n\t" \
  "v_mov_b32 v130, v129\n\t" \
  "v_mfma_f32_16x16x32_bf16 v[140:143], v[84:87], v[8:11], v[140:143]\n\t" \
  "v_mov_b32 v129, v128\n\t" \
  "v_mfma_f32_16x16x32_bf16 v[144:147], v[92:95], v[8:11], v[144:147]\n\t" \
  "v_mfma_f32_16x16x32_bf16 v[148:151], v[100:103], v[8:11], v[148:151]\n\t" \
  "v_mfma_f32_16x16x32_bf16 v[152:155], v[108:111], v[8:11], v[152:155]\n\t" \
  "v_mfma_f32_16x16x32_bf16 v[156:159], v[116:119], v[8:11], v[156:159]\n\t" \
  "ds_read_b64 v[8:9], v28 offset:8192\n\t" \
  "s_waitcnt lgkmcnt(10)\n\t" \
  "v_mfma_f32_16x16x32_bf16 v[132:135], v[64:67], v[12:15], v[132:135]\n\t" \
  "ds_read_b64 v[10:11], v29 offset:8192\n\t" \
  "v_mfma_f32_16x16x32_bf16 v[136:139], v[72:75], v[12:15], v[136:139]\n\t" \
  "v_mfma_f32_16x16x32_bf16 v[140:143], v[80:83], v[12:15], v[140:143]\n\t" \
  "v_mfma_f32_16x16x32_bf16 v[144:147], v[88:91], v[12:15], v[144:147]\n\t" \
  "v_mfma_f32_16x16x32_bf16 v[148:151], v[96:99], v[12:15], v[148:151]\n\t" \
  "s_waitcnt lgkmcnt(3)\n\t" \
  "v_mov_b32 v128, v34\n\t" \
  "v_writelane_b32 v128, 0, 0\n\t" \
  "v_mfma_f32_16x16x32_bf16 v[152:155], v[104:107], v[12:15], v[152:155]\n\t" \
  "v_mfma_f32_16x16x32_bf16 v[156:159], v[112:115], v[12:15], v[156:159]\n\t" \
  "ds_read_b64 v[12:13], v30 offset:9216\n\t" \
  "s_waitcnt lgkmcnt(10)\n\t" \
  "v_mfma_f32_16x16x32_bf16 v[136:139], v[68:71], v[16:19], v[136:139]\n\t" \
  "ds_read_b64 v[14:15], v31 offset:9216\n\t" \
  "ds_bpermute_b32 v35, v32, v135\n\t" \
  "v_readlane_b32 %[p1], v135, 63\n\t" \
  "v_mfma_f32_16x16x32_bf16 v[140:143], v[76:79], v[16:19], v[140:143]\n\t" \
  "v_mov_b32 v135, v134\n\t" \
  "v_mfma_f32_16x16x32_bf16 v[144:147], v[84:87], v[16:19], v[144:147]\n\t" \
  "v_mov_b32 v134, v133\n\t" \
  "v_mfma_f32_16x16x32_bf16 v[148:151], v[92:95], v[16:19], v[148:151]\n\t" \
  "v_mov_b32 v133, v132\n\t" \
  "v_mfma_f32_16x16x32_bf16 v[152:155], v[100:103], v[16:19], v[152:155]\n\t" \
  "v_mfma_f32_16x16x32_bf16 v[156:159], v[108:111], v[16:19], v[156:159]\n\t" \
  "ds_read_b64 v[16:17], v24 offset:10240\n\t" \
  "s_waitcnt lgkmcnt(11)\n\t" \
  "v_mfma_f32_16x16x32_bf16 v[136:139], v[64:67], v[20:23], v[136:139]\n\t" \
  "ds_read_b64 v[18:19], v25 offset:10240\n\t" \
  "v_mfma_f32_16x16x32_bf16 v[140:143], v[72:75], v[20:23], v[140:143]\n\t" \
  "v_mfma_f32_16x16x32_bf16 v[144:147], v[80:83], v[20:23], v[144:147]\n\t" \
  "v_mfma_f32_16x16x32_bf16 v[148:151], v[88:91], v[20:23], v[148:151]\n\t" \
  "v_mfma_f32_16x16x32_bf16 v[152:155], v[96:99], v[20:23], v[152:155]\n\t" \
  "v_mfma_f32_16x16x32_bf16 v[156:159], v[104:107], v[20:23], v[156:159]\n\t" \
  "ds_read_b64 v[20:21], v26 offset:11264\n\t" \
  "s_waitcnt lgkmcnt(11)\n\t" \
  "v_mfma_f32_16x16x32_bf16 v[140:143], v[68:71], v[0:3], v[140:143]\n\t" \
  "ds_read_b64 v[22:23], v27 offset:11264\n\t" \
  "s_waitcnt lgkmcnt(4)\n\t" \
  "v_mov_b32 v132, v35\n\t" \
  "v_writelane_b32 v132, %[p0], 0\n\t" \
  "v_mfma_f32_16x16x32_bf16 v[144:147], v[76:79], v[0:3], v[144:147]\n\t" \
  "ds_bpermute_b32 v36, v32, v139\n\t" \
  "v_readlane_b32 %[p2], v139, 63\n\t" \
  "v_mfma_f32_16x16x32_bf16 v[148:151], v[84:87], v[0:3], v[148:151]\n\t" \
  "v_mov_b32 v139, v138\n\t" \
  "v_mfma_f32_16x16x32_bf16 v[152:155], v[92:95], v[0:3], v[152:155]\n\t" \
  "v_mov_b32 v138, v137\n\t" \
  "v_mfma_f32_16x16x32_bf16 v[156:159], v[100:103], v[0:3], v[156:159]\n\t" \
  "ds_read_b64 v[0:1], v28 offset:12288\n\t" \
  "v_mov_b32 v137, v136\n\t" \
  "s_waitcnt lgkmcnt(11)\n\t" \
  "v_mfma_f32_16x16x32_bf16 v[140:143], v[64:67], v[4:7], v[140:143]\n\t" \
  "ds_read_b64 v[2:3], v29 offset:12288\n\t" \
  "v_mfma_f32_16x16x32_bf16 v[144:147], v[72:75], v[4:7], v[144:147]\n\t" \
  "v_mfma_f32_16x16x32_bf16 v[148:151], v[80:83], v[4:7], v[148:151]\n\t" \
  "v_mfma_f32_16x16x32_bf16 v[152:155], v[88:91], v[4:7], v[152:155]\n\t" \
  "v_mfma_f32_16x16x32_bf16 v[156:159], v[96:99], v[4:7], v[156:159]\n\t" \
  "ds_read_b64 v[4:5], v30 offset:13312\n\t" \
  "s_waitcnt lgkmcnt(11)\n\t" \
  "v_mfma_f32_16x16x32_bf16 v[144:147], v[68:71], v[8:11], v[144:147]\n\t" \
  "ds_read_b64 v[6:7], v31 offset:13312\n\t" \
  "v_mfma_f32_16x16x32_bf16 v[148:151], v[76:79], v[8:11], v[148:151]\n\t" \
  "v_mfma_f32_16x16x32_bf16 v[152:155], v[84:87], v[8:11], v[152:155]\n\t" \
  "ds_bpermute_b32 v37, v32, v143\n\t" \
  "v_readlane_b32 %[p3], v143, 63\n\t" \
  "v_mfma_f32_16x16x32_bf16 v[156:159], v[92:95], v[8:11], v[156:159]\n\t" \
  "ds_read_b64 v[8:9], v24 offset:14336\n\t" \
  "s_waitcnt lgkmcnt(6)\n\t" \
  "v_mov_b32 v136, v36\n\t" \
  "v_writelane_b32 v136, %[p1], 0\n\t" \
  "v_mov_b32 v143, v142\n\t" \
  "s_waitcnt lgkmcnt(12)\n\t" \
  "v_mfma_f32_16x16x32_bf16 v[144:147], v[64:67], v[12:15], v[144:147]\n\t" \
  "ds_read_b64 v[10:11], v25 offset:14336\n\t" \
  "v_mov_b32 v142, v141\n\t" \
  "v_mfma_f32_16x16x32_bf16 v[148:151], v[72:75], v[12:15], v[148:151]\n\t" \
  "v_mov_b32 v141, v140\n\t" \
  "v_mfma_f32_16x16x32_bf16 v[152:155], v[80:83], v[12:15], v[152:155]\n\t" \
  "v_mfma_f32_16x16x32_bf16 v[156:159], v[88:91], v[12:15], v[156:159]\n\t" \
  "ds_read_b64 v[12:13], v26 offset:15360\n\t" \
  "s_waitcnt lgkmcnt(11)\n\t" \
  "v_mfma_f32_16x16x32_bf16 v[148:151], v[68:71], v[16:19], v[148:151]\n\t" \
  "ds_read_b64 v[14:15], v27 offset:15360\n\t" \
  "v_mfma_f32_16x16x32_bf16 v[152:155], v[76:79], v[16:19], v[152:155]\n\t" \
  "v_mfma_f32_16x16x32_bf16 v[156:159], v[84:87], v[16:19], v[156:159]\n\t" \
  "ds_read_b64 v[16:17], v26 offset:0\n\t" \
  "s_waitcnt lgkmcnt(11)\n\t" \
  "v_mfma_f32_16x16x32_bf16 v[148:151], v[64:67], v[20:23], v[148:151]\n\t" \
  "ds_read_b64 v[18:19], v27 offset:0\n\t" \
  "ds_bpermute_b32 v38, v32, v147\n\t" \
  "v_readlane_b32 %[p4], v147, 63\n\t" \
  "v_mfma_f32_16x16x32_bf16 v[152:155], v[72:75], v[20:23], v[152:155]\n\t" \
  "v_mov_b32 v147, v146\n\t" \
  "v_mfma_f32_16x16x32_bf16 v[156:159], v[80:83], v[20:23], v[156:159]\n\t" \
  "ds_read_b64 v[20:21], v28 offset:1024\n\t" \
  "v_mov_b32 v146, v145\n\t" \
  "s_waitcnt lgkmcnt(11)\n\t" \
  "v_mfma_f32_16x16x32_bf16 v[152:155], v[68:71], v[0:3], v[152:155]\n\t" \
  "ds_read_b64 v[22:23], v29 offset:1024\n\t" \
  "s_waitcnt lgkmcnt(9)\n\t" \
  "v_mov_b32 v140, v37\n\t" \
  "v_writelane_b32 v140, %[p2], 0\n\t" \
  "v_mov_b32 v145, v144\n\t" \
  "v_mfma_f32_16x16x32_bf16 v[156:159], v[76:79], v[0:3], v[156:159]\n\t" \
  "ds_read_b64 v[0:1], v30 offset:2048\n\t" \
  "ds_bpermute_b32 v39, v32, v151\n\t" \
  "v_readlane_b32 %[p5], v151, 63\n\t" \
  "s_waitcnt lgkmcnt(12)\n\t" \
  "v_mfma_f32_16x16x32_bf16 v[152:155], v[64:67], v[4:7], v[152:155]\n\t" \
  "ds_read_b64 v[2:3], v31 offset:2048\n\t" \
  "v_mov_b32 v151, v150\n\t" \
  "v_mfma_f32_16x16x32_bf16 v[156:159], v[72:75], v[4:7], v[156:159]\n\t" \
  "ds_read_b64 v[4:5], v24 offset:3072\n\t" \
  "v_mov_b32 v150, v149\n\t" \
  "s_waitcnt lgkmcnt(11)\n\t" \
  "v_mfma_f32_16x16x32_bf16 v[156:159], v[68:71], v[8:11], v[156:159]\n\t" \
  "ds_read_b64 v[6:7], v25 offset:3072\n\t" \
  "ds_read_b64 v[8:9], v26 offset:4096\n\t" \
  "v_mov_b32 v149, v148\n\t" \
  "ds_bpermute_b32 v40, v32, v155\n\t" \
  "v_readlane_b32 %[p6], v155, 63\n\t" \
  "s_waitcnt lgkmcnt(12)\n\t" \
  "v_mfma_f32_16x16x32_bf16 v[156:159], v[64:67], v[12:15], v[156:159]\n\t" \
  "ds_read_b64 v[10:11], v27 offset:4096\n\t" \
  "ds_read_b64 v[12:13], v28 offset:5120\n\t" \
  "v_mov_b32 v155, v154\n\t" \
  "s_waitcnt lgkmcnt(12)\n\t" \
  "v_mfma_f32_16x16x32_bf16 v[128:131], v[68:71], v[16:19], v[128:131]\n\t" \
  "ds_read_b64 v[14:15], v29 offset:5120\n\t" \
  "v_mov_b32 v154, v153\n\t" \
  "v_mfma_f32_16x16x32_bf16 v[132:135], v[76:79], v[16:19], v[132:135]\n\t" \
  "s_waitcnt lgkmcnt(12)\n\t" \
  "v_mov_b32 v144, v38\n\t" \
  "v_writelane_b32 v144, %[p3], 0\n\t" \
  "v_mov_b32 v153, v152\n\t" \
  "v_mfma_f32_16x16x32_bf16 v[136:139], v[84:87], v[16:19], v[136:139]\n\t" \
  "s_waitcnt lgkmcnt(8)\n\t" \
  "v_mov_b32 v148, v39\n\t" \
  "v_writelane_b32 v148, %[p4], 0\n\t" \
  "v_mfma_f32_16x16x32_bf16 v[140:143], v[92:95], v[16:19], v[140:143]\n\t" \
  "s_waitcnt lgkmcnt(3)\n\t" \
  "v_mov_b32 v152, v40\n\t" \
  "v_writelane_b32 v152, %[p5], 0\n\t" \
  "v_mfma_f32_16x16x32_bf16 v[144:147], v[100:103], v[16:19], v[144:147]\n\t" \
  "ds_bpermute_b32 v41, v32, v159\n\t" \
  "v_mfma_f32_16x16x32_bf16 v[148:151], v[108:111], v[16:19], v[148:151]\n\t" \
  "v_mov_b32 v159, v158\n\t" \
  "v_mfma_f32_16x16x32_bf16 v[152:155], v[116:119], v[16:19], v[152:155]\n\t" \
  "v_mov_b32 v158, v157\n\t" \
  "s_waitcnt lgkmcnt(11)\n\t" \
  "v_mfma_f32_16x16x32_bf16 v[128:131], v[64:67], v[20:23], v[128:131]\n\t" \
  "v_mov_b32 v157, v156\n\t" \
  "v_mfma_f32_16x16x32_bf16 v[132:135], v[72:75], v[20:23], v[132:135]\n\t" \
  "v_mfma_f32_16x16x32_bf16 v[136:139], v[80:83], v[20:23], v[136:139]\n\t" \
  "v_mfma_f32_16x16x32_bf16 v[140:143], v[88:91], v[20:23], v[140:143]\n\t" \
  "v_mfma_f32_16x16x32_bf16 v[144:147], v[96:99], v[20:23], v[144:147]\n\t" \
  "s_waitcnt lgkmcnt(0)\n\t" \
  "v_mov_b32 v156, v41\n\t" \
  "v_writelane_b32 v156, %[p6], 0\n\t" \
  "v_mfma_f32_16x16x32_bf16 v[148:151], v[104:107], v[20:23], v[148:151]\n\t" \
  "v_mfma_f32_16x16x32_bf16 v[152:155], v[112:115], v[20:23], v[152:155]\n\t" \
  "v_mfma_f32_16x16x32_bf16 v[156:159], v[124:127], v[16:19], v[156:159]\n\t" \
  "ds_read_b64 v[16:17], v30 offset:6144\n\t" \
  "ds_bpermute_b32 v34, v32, v131\n\t" \
  "v_readlane_b32 %[p0], v131, 63\n\t" \
  "v_mfma_f32_16x16x32_bf16 v[156:159], v[120:123], v[20:23], v[156:159]\n\t" \
  "ds_read_b64 v[18:19], v31 offset:6144\n\t" \
  "ds_read_b64 v[20:21], v24 offset:7168\n\t" \
  "v_mov_b32 v131, v130\n\t" \
  "s_waitcnt lgkmcnt(12)\n\t" \
  "v_mfma_f32_16x16x32_bf16 v[132:135], v[68:71], v[0:3], v[132:135]\n\t" \
  "ds_read_b64 v[22:23], v25 offset:7168\n\t" \
  "v_mov_b32 v130, v129\n\t" \
  "v_mfma_f32_16x16x32_bf16 v[136:139], v[76:79], v[0:3], v[136:139]\n\t" \
  "v_mov_b32 v129, v128\n\t" \
  "v_mfma_f32_16x16x32_bf16 v[140:143], v[84:87], v[0:3], v[140:143]\n\t" \
  "v_mfma_f32_16x16x32_bf16 v[144:147], v[92:95], v[0:3], v[144:147]\n\t" \
  "v_mfma_f32_16x16x32_bf16 v[148:151], v[100:103], v[0:3], v[148:151]\n\t" \
  "v_mfma_f32_16x16x32_bf16 v[152:155], v[108:111], v[0:3], v[152:155]\n\t" \
  "v_mfma_f32_16x16x32_bf16 v[156:159], v[116:119], v[0:3], v[156:159]\n\t" \
  "ds_read_b64 v[0:1], v26 offset:8192\n\t" \
  "s_waitcnt lgkmcnt(12)\n\t" \
  "v_mfma_f32_16x16x32_bf16 v[132:135], v[64:67], v[4:7], v[132:135]\n\t" \
  "ds_read_b64 v[2:3], v27 offset:8192\n\t" \
  "v_mfma_f32_16x16x32_bf16 v[136:139], v[72:75], v[4:7], v[136:139]\n\t" \
  "v_mfma_f32_16x16x32_bf16 v[140:143], v[80:83], v[4:7], v[140:143]\n\t" \
  "v_mfma_f32_16x16x32_bf16 v[144:147], v[88:91], v[4:7], v[144:147]\n\t" \
  "s_waitcnt lgkmcnt(5)\n\t" \
  "v_mov_b32 v128, v34\n\t" \
  "v_writelane_b32 v128, 0, 0\n\t" \
  "v_mfma_f32_16x16x32_bf16 v[148:151], v[96:99], v[4:7], v[148:151]\n\t" \
  "v_mfma_f32_16x16x32_bf16 v[152:155], v[104:107], v[4:7], v[152:155]\n\t" \
  "v_mfma_f32_16x16x32_bf16 v[156:159], v[112:115], v[4:7], v[156:159]\n\t" \
  "ds_read_b64 v[4:5], v28 offset:9216\n\t" \
  "s_waitcnt lgkmcnt(11)\n\t" \
  "v_mfma_f32_16x16x32_bf16 v[136:139], v[68:71], v[8:11], v[136:139]\n\t" \
  "ds_read_b64 v[6:7], v29 offset:9216\n\t" \
  "ds_bpermute_b32 v35, v32, v135\n\t" \
  "v_readlane_b32 %[p1], v135, 63\n\t" \
  "v_mfma_f32_16x16x32_bf16 v[140:143], v[76:79], v[8:11], v[140:143]\n\t" \
  "v_mov_b32 v135, v134\n\t" \
  "v_mfma_f32_16x16x32_bf16 v[144:147], v[84:87], v[8:11], v[144:147]\n\t" \
  "v_mov_b32 v134, v133\n\t" \
  "v_mfma_f32_16x16x32_bf16 v[148:151], v[92:95], v[8:11], v[148:151]\n\t" \
  "v_mov_b32 v133, v132\n\t" \
  "v_mfma_f32_16x16x32_bf16 v[152:155], v[100:103], v[8:11], v[152:155]\n\t" \
  "v_mfma_f32_16x16x32_bf16 v[156:159], v[108:111], v[8:11], v[156:159]\n\t" \
  "ds_read_b64 v[8:9], v30 offset:10240\n\t" \
  "s_waitcnt lgkmcnt(12)\n\t" \
  "v_mfma_f32_16x16x32_bf16 v[136:139], v[64:67], v[12:15], v[136:139]\n\t" \
  "ds_read_b64 v[10:11], v31 offset:10240\n\t" \
  "v_mfma_f32_16x16x32_bf16 v[140:143], v[72:75], v[12:15], v[140:143]\n\t" \
  "v_mfma_f32_16x16x32_bf16 v[144:147], v[80:83], v[12:15], v[144:147]\n\t" \
  "v_mfma_f32_16x16x32_bf16 v[148:151], v[88:91], v[12:15], v[148:151]\n\t" \
  "v_mfma_f32_16x16x32_bf16 v[152:155], v[96:99], v[12:15], v[152:155]\n\t" \
  "v_mfma_f32_16x16x32_bf16 v[156:159], v[104:107], v[12:15], v[156:159]\n\t" \
  "ds_read_b64 v[12:13], v24 offset:11264\n\t" \
  "s_waitcnt lgkmcnt(10)\n\t" \
  "v_mfma_f32_16x16x32_bf16 v[140:143], v[68:71], v[16:19], v[140:143]\n\t" \
  "ds_read_b64 v[14:15], v25 offset:11264\n\t" \
  "s_waitcnt lgkmcnt(4)\n\t" \
  "v_mov_b32 v132, v35\n\t" \
  "v_writelane_b32 v132, %[p0], 0\n\t" \
  "v_mfma_f32_16x16x32_bf16 v[144:147], v[76:79], v[16:19], v[144:147]\n\t" \
  "ds_bpermute_b32 v36, v32, v139\n\t" \
  "v_readlane_b32 %[p2], v139, 63\n\t" \
  "v_mfma_f32_16x16x32_bf16 v[148:151], v[84:87], v[16:19], v[148:151]\n\t" \
  "v_mov_b32 v139, v138\n\t" \
  "v_mfma_f32_16x16x32_bf16 v[152:155], v[92:95], v[16:19], v[152:155]\n\t" \
  "v_mov_b32 v138, v137\n\t" \
  "v_mfma_f32_16x16x32_bf16 v[156:159], v[100:103], v[16:19], v[156:159]\n\t" \
  "ds_read_b64 v[16:17], v26 offset:12288\n\t" \
  "v_mov_b32 v137, v136\n\t" \
  "s_waitcnt lgkmcnt(11)\n\t" \
  "v_mfma_f32_16x16x32_bf16 v[140:143], v[64:67], v[20:23], v[140:143]\n\t" \
  "ds_read_b64 v[18:19], v27 offset:12288\n\t" \
  "v_mfma_f32_16x16x32_bf16 v[144:147], v[72:75], v[20:23], v[144:147]\n\t" \
  "v_mfma_f32_16x16x32_bf16 v[148:151], v[80:83], v[20:23], v[148:151]\n\t" \
  "v_mfma_f32_16x16x32_bf16 v[152:155], v[88:91], v[20:23], v[152:155]\n\t" \
  "v_mfma_f32_16x16x32_bf16 v[156:159], v[96:99], v[20:23], v[156:159]\n\t" \
  "ds_read_b64 v[20:21], v28 offset:13312\n\t" \
  "s_waitcnt lgkmcnt(11)\n\t" \
  "v_mfma_f32_16x16x32_bf16 v[144:147], v[68:71], v[0:3], v[144:147]\n\t" \
  "ds_read_b64 v[22:23], v29 offset:13312\n\t" \
  "v_mfma_f32_16x16x32_bf16 v[148:151], v[76:79], v[0:3], v[148:151]\n\t" \
  "v_mfma_f32_16x16x32_bf16 v[152:155], v[84:87], v[0:3], v[152:155]\n\t" \
  "ds_bpermute_b32 v37, v32, v143\n\t" \
  "v_readlane_b32 %[p3], v143, 63\n\t" \
  "v_mfma_f32_16x16x32_bf16 v[156:159], v[92:95], v[0:3], v[156:159]\n\t" \
  "ds_read_b64 v[0:1], v30 offset:14336\n\t" \
  "s_waitcnt lgkmcnt(6)\n\t" \
  "v_mov_b32 v136, v36\n\t" \
  "v_writelane_b32 v136, %[p1], 0\n\t" \
  "v_mov_b32 v143, v142\n\t" \
  "s_waitcnt lgkmcnt(12)\n\t" \
  "v_mfma_f32_16x16x32_bf16 v[144:147], v[64:67], v[4:7], v[144:147]\n\t" \
  "ds_read_b64 v[2:3], v31 offset:14336\n\t" \
  "v_mov_b32 v142, v141\n\t" \
  "v_mfma_f32_16x16x32_bf16 v[148:151], v[72:75], v[4:7], v[148:151]\n\t" \
  "v_mov_b32 v141, v140\n\t" \
  "v_mfma_f32_16x16x32_bf16 v[152:155], v[80:83], v[4:7], v[152:155]\n\t" \
  "v_mfma_f32_16x16x32_bf16 v[156:159], v[88:91], v[4:7], v[156:159]\n\t" \
  "ds_read_b64 v[4:5], v24 offset:15360\n\t" \
  "s_waitcnt lgkmcnt(11)\n\t" \
  "v_mfma_f32_16x16x32_bf16 v[148:151], v[68:71], v[8:11], v[148:151]\n\t" \
  "ds_read_b64 v[6:7], v25 offset:15360\n\t" \
  "v_mfma_f32_16x16x32_bf16 v[152:155], v[76:79], v[8:11], v[152:155]\n\t" \
  "v_mfma_f32_16x16x32_bf16 v[156:159], v[84:87], v[8:11], v[156:159]\n\t" \
  "ds_read_b64 v[8:9], v24 offset:0\n\t" \
  "s_waitcnt lgkmcnt(11)\n\t" \
  "v_mfma_f32_16x16x32_bf16 v[148:151], v[64:67], v[12:15], v[148:151]\n\t" \
  "ds_read_b64 v[10:11], v25 offset:0\n\t" \
  "ds_bpermute_b32 v38, v32, v147\n\t" \
  "v_readlane_b32 %[p4], v147, 63\n\t" \
  "v_mfma_f32_16x16x32_bf16 v[152:155], v[72:75], v[12:15], v[152:155]\n\t" \
  "v_mov_b32 v147, v146\n\t" \
  "v_mfma_f32_16x16x32_bf16 v[156:159], v[80:83], v[12:15], v[156:159]\n\t" \
  "ds_read_b64 v[12:13], v26 offset:1024\n\t" \
  "v_mov_b32 v146, v145\n\t" \
  "s_waitcnt lgkmcnt(11)\n\t" \
  "v_mfma_f32_16x16x32_bf16 v[152:155], v[68:71], v[16:19], v[152:155]\n\t" \
  "ds_read_b64 v[14:15], v27 offset:1024\n\t" \
  "s_waitcnt lgkmcnt(9)\n\t" \
  "v_mov_b32 v140, v37\n\t" \
  "v_writelane_b32 v140, %[p2], 0\n\t" \
  "v_mov_b32 v145, v144\n\t" \
  "v_mfma_f32_16x16x32_bf16 v[156:159], v[76:79], v[16:19], v[156:159]\n\t" \
  "ds_read_b64 v[16:17], v28 offset:2048\n\t" \
  "ds_bpermute_b32 v39, v32, v151\n\t" \
  "v_readlane_b32 %[p5], v151, 63\n\t" \
  "s_waitcnt lgkmcnt(12)\n\t" \
  "v_mfma_f32_16x16x32_bf16 v[152:155], v[64:67], v[20:23], v[152:155]\n\t" \
  "ds_read_b64 v[18:19], v29 offset:2048\n\t" \
  "v_mov_b32 v151, v150\n\t" \
  "v_mfma_f32_16x16x32_bf16 v[156:159], v[72:75], v[20:23], v[156:159]\n\t" \
  "ds_read_b64 v[20:21], v30 offset:3072\n\t" \
  "v_mov_b32 v150, v149\n\t" \
  "s_waitcnt lgkmcnt(11)\n\t" \
  "v_mfma_f32_16x16x32_bf16 v[156:159], v[68:71], v[0:3], v[156:159]\n\t" \
  "ds_read_b64 v[22:23], v31 offset:3072\n\t" \
  "ds_read_b64 v[0:1], v24 offset:4096\n\t" \
  "v_mov_b32 v149, v148\n\t" \
  "ds_bpermute_b32 v40, v32, v155\n\t" \
  "v_readlane_b32 %[p6], v155, 63\n\t" \
  "s_waitcnt lgkmcnt(12)\n\t" \
  "v_mfma_f32_16x16x32_bf16 v[156:159], v[64:67], v[4:7], v[156:159]\n\t" \
  "ds_read_b64 v[2:3], v25 offset:4096\n\t" \
  "ds_read_b64 v[4:5], v26 offset:5120\n\t" \
  "v_mov_b32 v155, v154\n\t" \
  "s_waitcnt lgkmcnt(12)\n\t" \
  "v_mfma_f32_16x16x32_bf16 v[128:131], v[68:71], v[8:11], v[128:131]\n\t" \
  "ds_read_b64 v[6:7], v27 offset:5120\n\t" \
  "v_mov_b32 v154, v153\n\t" \
  "v_mfma_f32_16x16x32_bf16 v[132:135], v[76:79], v[8:11], v[132:135]\n\t" \
  "s_waitcnt lgkmcnt(12)\n\t" \
  "v_mov_b32 v144, v38\n\t" \
  "v_writelane_b32 v144, %[p3], 0\n\t" \
  "v_mov_b32 v153, v152\n\t" \
  "v_mfma_f32_16x16x32_bf16 v[136:139], v[84:87], v[8:11], v[136:139]\n\t" \
  "s_waitcnt lgkmcnt(8)\n\t" \
  "v_mov_b32 v148, v39\n\t" \
  "v_writelane_b32 v148, %[p4], 0\n\t" \
  "v_mfma_f32_16x16x32_bf16 v[140:143], v[92:95], v[8:11], v[140:143]\n\t" \
  "s_waitcnt lgkmcnt(3)\n\t" \
  "v_mov_b32 v152, v40\n\t" \
  "v_writelane_b32 v152, %[p5], 0\n\t" \
  "v_mfma_f32_16x16x32_bf16 v[144:147], v[100:103], v[8:11], v[144:147]\n\t" \
  "ds_bpermute_b32 v41, v32, v159\n\t" \
  "v_mfma_f32_16x16x32_bf16 v[148:151], v[108:111], v[8:11], v[148:151]\n\t" \
  "v_mov_b32 v159, v158\n\t" \
  "v_mfma_f32_16x16x32_bf16 v[152:155], v[116:119], v[8:11], v[152:155]\n\t" \
  "v_mov_b32 v158, v157\n\t" \
  "s_waitcnt lgkmcnt(11)\n\t" \
  "v_mfma_f32_16x16x32_bf16 v[128:131], v[64:67], v[12:15], v[128:131]\n\t" \
  "v_mov_b32 v157, v156\n\t" \
  "v_mfma_f32_16x16x32_bf16 v[132:135], v[72:75], v[12:15], v[132:135]\n\t" \
  "v_mfma_f32_16x16x32_bf16 v[136:139], v[80:83], v[12:15], v[136:139]\n\t" \
  "v_mfma_f32_16x16x32_bf16 v[140:143], v[88:91], v[12:15], v[140:143]\n\t" \
  "v_mfma_f32_16x16x32_bf16 v[144:147], v[96:99], v[12:15], v[144:147]\n\t" \
  "s_waitcnt lgkmcnt(0)\n\t" \
  "v_mov_b32 v156, v41\n\t" \
  "v_writelane_b32 v156, %[p6], 0\n\t" \
  "v_mfma_f32_16x16x32_bf16 v[148:151], v[104:107], v[12:15], v[148:151]\n\t" \
  "v_mfma_f32_16x16x32_bf16 v[152:155], v[112:115], v[12:15], v[152:155]\n\t" \
  "v_mfma_f32_16x16x32_bf16 v[156:159], v[124:127], v[8:11], v[156:159]\n\t" \
  "ds_read_b64 v[8:9], v28 offset:6144\n\t" \
  "ds_bpermute_b32 v34, v32, v131\n\t" \
  "v_readlane_b32 %[p0], v131, 63\n\t" \
  "v_mfma_f32_16x16x32_bf16 v[156:159], v[120:123], v[12:15], v[156:159]\n\t" \
  "ds_read_b64 v[10:11], v29 offset:6144\n\t" \
  "ds_read_b64 v[12:13], v30 offset:7168\n\t" \
  "v_mov_b32 v131, v130\n\t" \
  "s_waitcnt lgkmcnt(12)\n\t" \
  "v_mfma_f32_16x16x32_bf16 v[132:135], v[68:71], v[16:19], v[132:135]\n\t" \
  "ds_read_b64 v[14:15], v31 offset:7168\n\t" \
  "v_mov_b32 v130, v129\n\t" \
  "v_mfma_f32_16x16x32_bf16 v[136:139], v[76:79], v[16:19], v[136:139]\n\t" \
  "v_mov_b32 v129, v128\n\t" \
  "v_mfma_f32_16x16x32_bf16 v[140:143], v[84:87], v[16:19], v[140:143]\n\t" \
  "v_mfma_f32_16x16x32_bf16 v[144:147], v[92:95], v[16:19], v[144:147]\n\t" \
  "v_mfma_f32_16x16x32_bf16 v[148:151], v[100:103], v[16:19], v[148:151]\n\t" \
  "v_mfma_f32_16x16x32_bf16 v[152:155], v[108:111], v[16:19], v[152:155]\n\t" \
  "v_mfma_f32_16x16x32_bf16 v[156:159], v[116:119], v[16:19], v[156:159]\n\t" \
  "ds_read_b64 v[16:17], v24 offset:8192\n\t" \
  "s_waitcnt lgkmcnt(12)\n\t" \
  "v_mfma_f32_16x16x32_bf16 v[132:135], v[64:67], v[20:23], v[132:135]\n\t" \
  "ds_read_b64 v[18:19], v25 offset:8192\n\t" \
  "v_mfma_f32_16x16x32_bf16 v[136:139], v[72:75], v[20:23], v[136:139]\n\t" \
  "v_mfma_f32_16x16x32_bf16 v[140:143], v[80:83], v[20:23], v[140:143]\n\t" \
  "v_mfma_f32_16x16x32_bf16 v[144:147], v[88:91], v[20:23], v[144:147]\n\t" \
  "s_waitcnt lgkmcnt(5)\n\t" \
  "v_mov_b32 v128, v34\n\t" \
  "v_writelane_b32 v128, 0, 0\n\t" \
  "v_mfma_f32_16x16x32_bf16 v[148:151], v[96:99], v[20:23], v[148:151]\n\t" \
  "v_mfma_f32_16x16x32_bf16 v[152:155], v[104:107], v[20:23], v[152:155]\n\t" \
  "v_mfma_f32_16x16x32_bf16 v[156:159], v[112:115], v[20:23], v[156:159]\n\t" \
  "ds_read_b64 v[20:21], v26 offset:9216\n\t" \
  "s_waitcnt lgkmcnt(11)\n\t" \
  "v_mfma_f32_16x16x32_bf16 v[136:139], v[68:71], v[0:3], v[136:139]\n\t" \
  "ds_read_b64 v[22:23], v27 offset:9216\n\t" \
  "ds_bpermute_b32 v35, v32, v135\n\t" \
  "v_readlane_b32 %[p1], v135, 63\n\t" \
  "v_mfma_f32_16x16x32_bf16 v[140:143], v[76:79], v[0:3], v[140:143]\n\t" \
  "v_mov_b32 v135, v134\n\t" \
  "v_mfma_f32_16x16x32_bf16 v[144:147], v[84:87], v[0:3], v[144:147]\n\t" \
  "v_mov_b32 v134, v133\n\t" \
  "v_mfma_f32_16x16x32_bf16 v[148:151], v[92:95], v[0:3], v[148:151]\n\t" \
  "v_mov_b32 v133, v132\n\t" \
  "v_mfma_f32_16x16x32_bf16 v[152:155], v[100:103], v[0:3], v[152:155]\n\t" \
  "v_mfma_f32_16x16x32_bf16 v[156:159], v[108:111], v[0:3], v[156:159]\n\t" \
  "ds_read_b64 v[0:1], v28 offset:10240\n\t" \
  "s_waitcnt lgkmcnt(12)\n\t" \
  "v_mfma_f32_16x16x32_bf16 v[136:139], v[64:67], v[4:7], v[136:139]\n\t" \
  "ds_read_b64 v[2:3], v29 offset:10240\n\t" \
  "v_mfma_f32_16x16x32_bf16 v[140:143], v[72:75], v[4:7], v[140:143]\n\t" \
  "v_mfma_f32_16x16x32_bf16 v[144:147], v[80:83], v[4:7], v[144:147]\n\t" \
  "v_mfma_f32_16x16x32_bf16 v[148:151], v[88:91], v[4:7], v[148:151]\n\t" \
  "v_mfma_f32_16x16x32_bf16 v[152:155], v[96:99], v[4:7], v[152:155]\n\t" \
  "v_mfma_f32_16x16x32_bf16 v[156:159], v[104:107], v[4:7], v[156:159]\n\t" \
  "ds_read_b64 v[4:5], v30 offset:11264\n\t" \
  "s_waitcnt lgkmcnt(10)\n\t" \
  "v_mfma_f32_16x16x32_bf16 v[140:143], v[68:71], v[8:11], v[140:143]\n\t" \
  "ds_read_b64 v[6:7], v31 offset:11264\n\t" \
  "s_waitcnt lgkmcnt(4)\n\t" \
  "v_mov_b32 v132, v35\n\t" \
  "v_writelane_b32 v132, %[p0], 0\n\t" \
  "v_mfma_f32_16x16x32_bf16 v[144:147], v[76:79], v[8:11], v[144:147]\n\t" \
  "ds_bpermute_b32 v36, v32, v139\n\t" \
  "v_readlane_b32 %[p2], v139, 63\n\t" \
  "v_mfma_f32_16x16x32_bf16 v[148:151], v[84:87], v[8:11], v[148:151]\n\t" \
  "v_mov_b32 v139, v138\n\t" \
  "v_mfma_f32_16x16x32_bf16 v[152:155], v[92:95], v[8:11], v[152:155]\n\t" \
  "v_mov_b32 v138, v137\n\t" \
  "v_mfma_f32_16x16x32_bf16 v[156:159], v[100:103], v[8:11], v[156:159]\n\t" \
  "ds_read_b64 v[8:9], v24 offset:12288\n\t" \
  "v_mov_b32 v137, v136\n\t" \
  "s_waitcnt lgkmcnt(11)\n\t" \
  "v_mfma_f32_16x16x32_bf16 v[140:143], v[64:67], v[12:15], v[140:143]\n\t" \
  "ds_read_b64 v[10:11], v25 offset:12288\n\t" \
  "v_mfma_f32_16x16x32_bf16 v[144:147], v[72:75], v[12:15], v[144:147]\n\t" \
  "v_mfma_f32_16x16x32_bf16 v[148:151], v[80:83], v[12:15], v[148:151]\n\t" \
  "v_mfma_f32_16x16x32_bf16 v[152:155], v[88:91], v[12:15], v[152:155]\n\t" \
  "v_mfma_f32_16x16x32_bf16 v[156:159], v[96:99], v[12:15], v[156:159]\n\t" \
  "ds_read_b64 v[12:13], v26 offset:13312\n\t" \
  "s_waitcnt lgkmcnt(11)\n\t" \
  "v_mfma_f32_16x16x32_bf16 v[144:147], v[68:71], v[16:19], v[144:147]\n\t" \
  "ds_read_b64 v[14:15], v27 offset:13312\n\t" \
  "v_mfma_f32_16x16x32_bf16 v[148:151], v[76:79], v[16:19], v[148:151]\n\t" \
  "v_mfma_f32_16x16x32_bf16 v[152:155], v[84:87], v[16:19], v[152:155]\n\t" \
  "ds_bpermute_b32 v37, v32, v143\n\t" \
  "v_readlane_b32 %[p3], v143, 63\n\t" \
  "v_mfma_f32_16x16x32_bf16 v[156:159], v[92:95], v[16:19], v[156:159]\n\t" \
  "ds_read_b64 v[16:17], v28 offset:14336\n\t" \
  "s_waitcnt lgkmcnt(6)\n\t" \
  "v_mov_b32 v136, v36\n\t" \
  "v_writelane_b32 v136, %[p1], 0\n\t" \
  "v_mov_b32 v143, v142\n\t" \
  "s_waitcnt lgkmcnt(12)\n\t" \
  "v_mfma_f32_16x16x32_bf16 v[144:147], v[64:67], v[20:23], v[144:147]\n\t" \
  "ds_read_b64 v[18:19], v29 offset:14336\n\t" \
  "v_mov_b32 v142, v141\n\t" \
  "v_mfma_f32_16x16x32_bf16 v[148:151], v[72:75], v[20:23], v[148:151]\n\t" \
  "v_mov_b32 v141, v140\n\t" \
  "v_mfma_f32_16x16x32_bf16 v[152:155], v[80:83], v[20:23], v[152:155]\n\t" \
  "v_mfma_f32_16x16x32_bf16 v[156:159], v[88:91], v[20:23], v[156:159]\n\t" \
  "ds_read_b64 v[20:21], v30 offset:15360\n\t" \
  "s_waitcnt lgkmcnt(11)\n\t" \
  "v_mfma_f32_16x16x32_bf16 v[148:151], v[68:71], v[0:3], v[148:151]\n\t" \
  "ds_read_b64 v[22:23], v31 offset:15360\n\t" \
  "v_mfma_f32_16x16x32_bf16 v[152:155], v[76:79], v[0:3], v[152:155]\n\t" \
  "v_mfma_f32_16x16x32_bf16 v[156:159], v[84:87], v[0:3], v[156:159]\n\t" \
  "ds_read_b64 v[0:1], v30 offset:0\n\t" \
  "s_waitcnt lgkmcnt(11)\n\t" \
  "v_mfma_f32_16x16x32_bf16 v[148:151], v[64:67], v[4:7], v[148:151]\n\t" \
  "ds_read_b64 v[2:3], v31 offset:0\n\t" \
  "ds_bpermute_b32 v38, v32, v147\n\t" \
  "v_readlane_b32 %[p4], v147, 63\n\t" \
  "v_mfma_f32_16x16x32_bf16 v[152:155], v[72:75], v[4:7], v[152:155]\n\t" \
  "v_mov_b32 v147, v146\n\t" \
  "v_mfma_f32_16x16x32_bf16 v[156:159], v[80:83], v[4:7], v[156:159]\n\t" \
  "ds_read_b64 v[4:5], v24 offset:1024\n\t" \
  "v_mov_b32 v146, v145\n\t" \
  "s_waitcnt lgkmcnt(11)\n\t" \
  "v_mfma_f32_16x16x32_bf16 v[152:155], v[68:71], v[8:11], v[152:155]\n\t" \
  "ds_read_b64 v[6:7], v25 offset:1024\n\t" \
  "s_waitcnt lgkmcnt(9)\n\t" \
  "v_mov_b32 v140, v37\n\t" \
  "v_writelane_b32 v140, %[p2], 0\n\t" \
  "v_mov_b32 v145, v144\n\t" \
  "v_mfma_f32_16x16x32_bf16 v[156:159], v[76:79], v[8:11], v[156:159]\n\t" \
  "ds_read_b64 v[8:9], v26 offset:2048\n\t" \
  "ds_bpermute_b32 v39, v32, v151\n\t" \
  "v_readlane_b32 %[p5], v151, 63\n\t" \
  "s_waitcnt lgkmcnt(12)\n\t" \
  "v_mfma_f32_16x16x32_bf16 v[152:155], v[64:67], v[12:15], v[152:155]\n\t" \
  "ds_read_b64 v[10:11], v27 offset:2048\n\t" \
  "v_mov_b32 v151, v150\n\t" \
  "v_mfma_f32_16x16x32_bf16 v[156:159], v[72:75], v[12:15], v[156:159]\n\t" \
  "ds_read_b64 v[12:13], v28 offset:3072\n\t" \
  "v_mov_b32 v150, v149\n\t" \
  "s_waitcnt lgkmcnt(11)\n\t" \
  "v_mfma_f32_16x16x32_bf16 v[156:159], v[68:71], v[16:19], v[156:159]\n\t" \
  "ds_read_b64 v[14:15], v29 offset:3072\n\t" \
  "ds_read_b64 v[16:17], v30 offset:4096\n\t" \
  "v_mov_b32 v149, v148\n\t" \
  "ds_bpermute_b32 v40, v32, v155\n\t" \
  "v_readlane_b32 %[p6], v155, 63\n\t" \
  "s_waitcnt lgkmcnt(12)\n\t" \
  "v_mfma_f32_16x16x32_bf16 v[156:159], v[64:67], v[20:23], v[156:159]\n\t" \
  "ds_read_b64 v[18:19], v31 offset:4096\n\t" \
  "ds_read_b64 v[20:21], v24 offset:5120\n\t" \
  "v_mov_b32 v155, v154\n\t" \
  "s_waitcnt lgkmcnt(12)\n\t" \
  "v_mfma_f32_16x16x32_bf16 v[128:131], v[68:71], v[0:3], v[128:131]\n\t" \
  "ds_read_b64 v[22:23], v25 offset:5120\n\t" \
  "v_mov_b32 v154, v153\n\t" \
  "v_mfma_f32_16x16x32_bf16 v[132:135], v[76:79], v[0:3], v[132:135]\n\t" \
  "s_waitcnt lgkmcnt(12)\n\t" \
  "v_mov_b32 v144, v38\n\t" \
  "v_writelane_b32 v144, %[p3], 0\n\t" \
  "v_mov_b32 v153, v152\n\t" \
  "v_mfma_f32_16x16x32_bf16 v[136:139], v[84:87], v[0:3], v[136:139]\n\t" \
  "s_waitcnt lgkmcnt(8)\n\t" \
  "v_mov_b32 v148, v39\n\t" \
  "v_writelane_b32 v148, %[p4], 0\n\t" \
  "v_mfma_f32_16x16x32_bf16 v[140:143], v[92:95], v[0:3], v[140:143]\n\t" \
  "s_waitcnt lgkmcnt(3)\n\t" \
  "v_mov_b32 v152, v40\n\t" \
  "v_writelane_b32 v152, %[p5], 0\n\t" \
  "v_mfma_f32_16x16x32_bf16 v[144:147], v[100:103], v[0:3], v[144:147]\n\t" \
  "ds_bpermute_b32 v41, v32, v159\n\t" \
  "v_mfma_f32_16x16x32_bf16 v[148:151], v[108:111], v[0:3], v[148:151]\n\t" \
  "v_mov_b32 v159, v158\n\t" \
  "v_mfma_f32_16x16x32_bf16 v[152:155], v[116:119], v[0:3], v[152:155]\n\t" \
  "v_mov_b32 v158, v157\n\t" \
  "s_waitcnt lgkmcnt(11)\n\t" \
  "v_mfma_f32_16x16x32_bf16 v[128:131], v[64:67], v[4:7], v[128:131]\n\t" \
  "v_mov_b32 v157, v156\n\t" \
  "v_mfma_f32_16x16x32_bf16 v[132:135], v[72:75], v[4:7], v[132:135]\n\t" \
  "v_mfma_f32_16x16x32_bf16 v[136:139], v[80:83], v[4:7], v[136:139]\n\t" \
  "v_mfma_f32_16x16x32_bf16 v[140:143], v[88:91], v[4:7], v[140:143]\n\t" \
  "v_mfma_f32_16x16x32_bf16 v[144:147], v[96:99], v[4:7], v[144:147]\n\t" \
  "s_waitcnt lgkmcnt(0)\n\t" \
  "v_mov_b32 v156, v41\n\t" \
  "v_writelane_b32 v156, %[p6], 0\n\t" \
  "v_mfma_f32_16x16x32_bf16 v[148:151], v[104:107], v[4:7], v[148:151]\n\t" \
  "v_mfma_f32_16x16x32_bf16 v[152:155], v[112:115], v[4:7], v[152:155]\n\t" \
  "s_mov_b64 exec, 1\n\t" \
  "ds_write_b32 v33, v33\n\t" \
  "s_mov_b64 exec, -1\n\t" \
  "v_mfma_f32_16x16x32_bf16 v[156:159], v[124:127], v[0:3], v[156:159]\n\t" \
  "ds_read_b64 v[0:1], v26 offset:6144\n\t" \
  "v_mfma_f32_16x16x32_bf16 v[156:159], v[120:123], v[4:7], v[156:159]\n\t" \
  "ds_read_b64 v[2:3], v27 offset:6144\n\t" \
  "ds_read_b64 v[4:5], v28 offset:7168\n\t" \
  "s_waitcnt lgkmcnt(12)\n\t" \
  "v_mfma_f32_16x16x32_bf16 v[132:135], v[68:71], v[8:11], v[132:135]\n\t" \
  "ds_read_b64 v[6:7], v29 offset:7168\n\t" \
  "v_mfma_f32_16x16x32_bf16 v[136:139], v[76:79], v[8:11], v[136:139]\n\t" \
  "v_mfma_f32_16x16x32_bf16 v[140:143], v[84:87], v[8:11], v[140:143]\n\t" \
  "v_mfma_f32_16x16x32_bf16 v[144:147], v[92:95], v[8:11], v[144:147]\n\t" \
  "v_mfma_f32_16x16x32_bf16 v[148:151], v[100:103], v[8:11], v[148:151]\n\t" \
  "v_mfma_f32_16x16x32_bf16 v[152:155], v[108:111], v[8:11], v[152:155]\n\t" \
  "v_mfma_f32_16x16x32_bf16 v[156:159], v[116:119], v[8:11], v[156:159]\n\t" \
  "ds_read_b64 v[8:9], v30 offset:8192\n\t" \
  "s_waitcnt lgkmcnt(12)\n\t" \
  "v_mfma_f32_16x16x32_bf16 v[132:135], v[64:67], v[12:15], v[132:135]\n\t" \
  "ds_read_b64 v[10:11], v31 offset:8192\n\t" \
  "v_mfma_f32_16x16x32_bf16 v[136:139], v[72:75], v[12:15], v[136:139]\n\t" \
  "v_mfma_f32_16x16x32_bf16 v[140:143], v[80:83], v[12:15], v[140:143]\n\t" \
  "v_mfma_f32_16x16x32_bf16 v[144:147], v[88:91], v[12:15], v[144:147]\n\t" \
  "v_mfma_f32_16x16x32_bf16 v[148:151], v[96:99], v[12:15], v[148:151]\n\t" \
  "v_mfma_f32_16x16x32_bf16 v[152:155], v[104:107], v[12:15], v[152:155]\n\t" \
  "v_mfma_f32_16x16x32_bf16 v[156:159], v[112:115], v[12:15], v[156:159]\n\t" \
  "ds_read_b64 v[12:13], v24 offset:9216\n\t" \
  "s_waitcnt lgkmcnt(11)\n\t" \
  "v_mfma_f32_16x16x32_bf16 v[136:139], v[68:71], v[16:19], v[136:139]\n\t" \
  "ds_read_b64 v[14:15], v25 offset:9216\n\t" \
  "v_mfma_f32_16x16x32_bf16 v[140:143], v[76:79], v[16:19], v[140:143]\n\t" \
  "v_mfma_f32_16x16x32_bf16 v[144:147], v[84:87], v[16:19], v[144:147]\n\t" \
  "v_mfma_f32_16x16x32_bf16 v[148:151], v[92:95], v[16:19], v[148:151]\n\t" \
  "v_mfma_f32_16x16x32_bf16 v[152:155], v[100:103], v[16:19], v[152:155]\n\t" \
  "v_mfma_f32_16x16x32_bf16 v[156:159], v[108:111], v[16:19], v[156:159]\n\t" \
  "ds_read_b64 v[16:17], v26 offset:10240\n\t" \
  "s_waitcnt lgkmcnt(11)\n\t" \
  "v_mfma_f32_16x16x32_bf16 v[136:139], v[64:67], v[20:23], v[136:139]\n\t" \
  "ds_read_b64 v[18:19], v27 offset:10240\n\t" \
  "v_mfma_f32_16x16x32_bf16 v[140:143], v[72:75], v[20:23], v[140:143]\n\t" \
  "v_mfma_f32_16x16x32_bf16 v[144:147], v[80:83], v[20:23], v[144:147]\n\t" \
  "v_mfma_f32_16x16x32_bf16 v[148:151], v[88:91], v[20:23], v[148:151]\n\t" \
  "v_mfma_f32_16x16x32_bf16 v[152:155], v[96:99], v[20:23], v[152:155]\n\t" \
  "v_mfma_f32_16x16x32_bf16 v[156:159], v[104:107], v[20:23], v[156:159]\n\t" \
  "ds_read_b64 v[20:21], v28 offset:11264\n\t" \
  "s_waitcnt lgkmcnt(9)\n\t" \
  "v_mfma_f32_16x16x32_bf16 v[140:143], v[68:71], v[0:3], v[140:143]\n\t" \
  "ds_read_b64 v[22:23], v29 offset:11264\n\t" \
  "v_mfma_f32_16x16x32_bf16 v[144:147], v[76:79], v[0:3], v[144:147]\n\t" \
  "v_mfma_f32_16x16x32_bf16 v[148:151], v[84:87], v[0:3], v[148:151]\n\t" \
  "v_mfma_f32_16x16x32_bf16 v[152:155], v[92:95], v[0:3], v[152:155]\n\t" \
  "v_mfma_f32_16x16x32_bf16 v[156:159], v[100:103], v[0:3], v[156:159]\n\t" \
  "ds_read_b64 v[0:1], v30 offset:12288\n\t" \
  "s_waitcnt lgkmcnt(9)\n\t" \
  "v_mfma_f32_16x16x32_bf16 v[140:143], v[64:67], v[4:7], v[140:143]\n\t" \
  "ds_read_b64 v[2:3], v31 offset:12288\n\t" \
  "v_mfma_f32_16x16x32_bf16 v[144:147], v[72:75], v[4:7], v[144:147]\n\t" \
  "v_mfma_f32_16x16x32_bf16 v[148:151], v[80:83], v[4:7], v[148:151]\n\t" \
  "v_mfma_f32_16x16x32_bf16 v[152:155], v[88:91], v[4:7], v[152:155]\n\t" \
  "v_mfma_f32_16x16x32_bf16 v[156:159], v[96:99], v[4:7], v[156:159]\n\t" \
  "ds_read_b64 v[4:5], v24 offset:13312\n\t" \
  "s_waitcnt lgkmcnt(9)\n\t" \
  "v_mfma_f32_16x16x32_bf16 v[144:147], v[68:71], v[8:11], v[144:147]\n\t" \
  "ds_read_b64 v[6:7], v25 offset:13312\n\t" \
  "v_mfma_f32_16x16x32_bf16 v[148:151], v[76:79], v[8:11], v[148:151]\n\t" \
  "v_mfma_f32_16x16x32_bf16 v[152:155], v[84:87], v[8:11], v[152:155]\n\t" \
  "v_mfma_f32_16x16x32_bf16 v[156:159], v[92:95], v[8:11], v[156:159]\n\t" \
  "ds_read_b64 v[8:9], v26 offset:14336\n\t" \
  "s_waitcnt lgkmcnt(9)\n\t" \
  "v_mfma_f32_16x16x32_bf16 v[144:147], v[64:67], v[12:15], v[144:147]\n\t" \
  "ds_read_b64 v[10:11], v27 offset:14336\n\t" \
  "v_mfma_f32_16x16x32_bf16 v[148:151], v[72:75], v[12:15], v[148:151]\n\t" \
  "v_mfma_f32_16x16x32_bf16 v[152:155], v[80:83], v[12:15], v[152:155]\n\t" \
  "v_mfma_f32_16x16x32_bf16 v[156:159], v[88:91], v[12:15], v[156:159]\n\t" \
  "ds_read_b64 v[12:13], v28 offset:15360\n\t" \
  "s_waitcnt lgkmcnt(9)\n\t" \
  "v_mfma_f32_16x16x32_bf16 v[148:151], v[68:71], v[16:19], v[148:151]\n\t" \
  "ds_read_b64 v[14:15], v29 offset:15360\n\t" \
  "v_mfma_f32_16x16x32_bf16 v[152:155], v[76:79], v[16:19], v[152:155]\n\t" \
  "v_mfma_f32_16x16x32_bf16 v[156:159], v[84:87], v[16:19], v[156:159]\n\t" \
  "s_waitcnt lgkmcnt(8)\n\t" \
  "v_mfma_f32_16x16x32_bf16 v[148:151], v[64:67], v[20:23], v[148:151]\n\t" \
  "v_mfma_f32_16x16x32_bf16 v[152:155], v[72:75], v[20:23], v[152:155]\n\t" \
  "v_mfma_f32_16x16x32_bf16 v[156:159], v[80:83], v[20:23], v[156:159]\n\t" \
  "s_waitcnt lgkmcnt(6)\n\t" \
  "v_mfma_f32_16x16x32_bf16 v[152:155], v[68:71], v[0:3], v[152:155]\n\t" \
  "v_mfma_f32_16x16x32_bf16 v[156:159], v[76:79], v[0:3], v[156:159]\n\t" \
  "s_waitcnt lgkmcnt(4)\n\t" \
  "v_mfma_f32_16x16x32_bf16 v[152:155], v[64:67], v[4:7], v[152:155]\n\t" \
  "v_mfma_f32_16x16x32_bf16 v[156:159], v[72:75], v[4:7], v[156:159]\n\t" \
  "s_waitcnt lgkmcnt(2)\n\t" \
  "v_mfma_f32_16x16x32_bf16 v[156:159], v[68:71], v[8:11], v[156:159]\n\t" \
  "s_waitcnt lgkmcnt(0)\n\t" \
  "v_mfma_f32_16x16x32_bf16 v[156:159], v[64:67], v[12:15], v[156:159]\n\t" \
  "s_waitcnt lgkmcnt(0)\n\t" \
  "s_nop 7\n\t"
#define CV_CLOB "v0", "v1", "v2", "v3", "v4", "v5", "v6", "v7", "v8", "v9", "v10", "v11", "v12", "v13", "v14", "v15", "v16", "v17", "v18", "v19", "v20", "v21", "v22", "v23", "v34", "v35", "v36", "v37", "v38", "v39", "v40", "v41"

#define G3_ASM \
  "s_getpc_b64 s[92:93]\n\t" \
  "s_branch QC_%=\n\t" \
  "ds_read_b64_tr_b16 v[96:97], v160 offset:0\n\t" \
  "ds_read_b64_tr_b16 v[98:99], v160 offset:2048\n\t" \
  "ds_read_b64_tr_b16 v[100:101], v161 offset:0\n\t" \
  "ds_read_b64_tr_b16 v[102:103], v161 offset:2048\n\t" \
  "ds_read_b64_tr_b16 v[104:105], v162 offset:0\n\t" \
  "ds_read_b64_tr_b16 v[106:107], v162 offset:2048\n\t" \
  "ds_read_b64_tr_b16 v[108:109], v163 offset:0\n\t" \
  "ds_read_b64_tr_b16 v[110:111], v163 offset:2048\n\t" \
  "ds_read_b64_tr_b16 v[112:113], v160 offset:4096\n\t" \
  "ds_read_b64_tr_b16 v[114:115], v160 offset:6144\n\t" \
  "ds_read_b64_tr_b16 v[116:117], v161 offset:4096\n\t" \
  "ds_read_b64_tr_b16 v[118:119], v161 offset:6144\n\t" \
  "ds_read_b64_tr_b16 v[120:121], v162 offset:4096\n\t" \
  "ds_read_b64_tr_b16 v[122:123], v162 offset:6144\n\t" \
  "QH_%=:\n\t" \
  "v_add_u32 v166, %[go], v165\n\t" \
  "ds_read_b32 v166, v166\n\t" \
  "s_addk_i32 %[cn], 1\n\t" \
  "s_cmp_gt_u32 %[cn], 0x400000\n\t" \
  "s_cbranch_scc1 QI_%=\n\t" \
  "s_waitcnt lgkmcnt(0)\n\t" \
  "v_cmp_eq_u32 vcc, 0, v166\n\t" \
  "s_cbranch_vccz QI_%=\n\t" \
  "s_sleep 2\n\t" \
  "s_getpc_b64 s[92:93]\n\t" \
  "s_branch QC_%=\n\t" \
  "s_branch QH_%=\n\t" \
  "QI_%=:\n\t" \
  "ds_read_b32 v166, v165\n\t" \
  "s_waitcnt lgkmcnt(11)\n\t" \
  "v_mfma_f32_16x16x32_bf16 v[128:131], v[96:99], v[0:3], 0\n\t" \
  "ds_read_b64_tr_b16 v[124:125], v163 offset:4096\n\t" \
  "v_readlane_b32 s94, v167, 5\n\t" \
  "v_mfma_f32_16x16x32_bf16 v[144:147], v[96:99], v[4:7], 0\n\t" \
  "ds_read_b64_tr_b16 v[126:127], v163 offset:6144\n\t" \
  "s_waitcnt lgkmcnt(2)\n\t" \
  "v_cmp_lt_u32 vcc, %[th], v166\n\t" \
  "v_mfma_f32_16x16x32_bf16 v[132:135], v[100:103], v[0:3], 0\n\t" \
  "s_mov_b64 exec, vcc\n\t" \
  "buffer_load_dwordx4 v[80:83], v164, %[ob], s94 offen offset:0 sc1\n\t" \
  "s_mov_b64 exec, -1\n\t" \
  "v_mfma_f32_16x16x32_bf16 v[148:151], v[100:103], v[4:7], 0\n\t" \
  "ds_read_b64_tr_b16 v[96:97], v160 offset:8192\n\t" \
  "ds_read_b64_tr_b16 v[98:99], v160 offset:10240\n\t" \
  "v_mfma_f32_16x16x32_bf16 v[136:139], v[104:107], v[0:3], 0\n\t" \
  "s_mov_b64 exec, vcc\n\t" \
  "buffer_load_dwordx4 v[84:87], v164, %[ob], s94 offen offset:1024 sc1\n\t" \
  "s_mov_b64 exec, -1\n\t" \
  "v_mfma_f32_16x16x32_bf16 v[152:155], v[104:107], v[4:7], 0\n\t" \
  "ds_read_b64_tr_b16 v[100:101], v161 offset:8192\n\t" \
  "ds_read_b64_tr_b16 v[102:103], v161 offset:10240\n\t" \
  "v_mfma_f32_16x16x32_bf16 v[140:143], v[108:111], v[0:3], 0\n\t" \
  "s_mov_b64 exec, vcc\n\t" \
  "buffer_load_dwordx4 v[88:91], v164, %[ob], s94 offen offset:2048 sc1\n\t" \
  "s_mov_b64 exec, -1\n\t" \
  "v_mfma_f32_16x16x32_bf16 v[156:159], v[108:111], v[4:7], 0\n\t" \
  "ds_read_b64_tr_b16 v[104:105], v162 offset:8192\n\t" \
  "ds_read_b64_tr_b16 v[106:107], v162 offset:10240\n\t" \
  "v_mfma_f32_16x16x32_bf16 v[128:131], v[112:115], v[8:11], v[128:131]\n\t" \
  "s_mov_b64 exec, vcc\n\t" \
  "buffer_load_dwordx4 v[92:95], v164, %[ob], s94 offen offset:3072 sc1\n\t" \
  "s_mov_b64 exec, -1\n\t" \
  "v_mfma_f32_16x16x32_bf16 v[144:147], v[112:115], v[12:15], v[144:147]\n\t" \
  "ds_read_b64_tr_b16 v[108:109], v163 offset:8192\n\t" \
  "ds_read_b64_tr_b16 v[110:111], v163 offset:10240\n\t" \
  "v_mfma_f32_16x16x32_bf16 v[132:135], v[116:119], v[8:11], v[132:135]\n\t" \
  "ds_read_b64_tr_b16 v[112:113], v160 offset:12288\n\t" \
  "ds_read_b32 v166, v165\n\t" \
  "v_mfma_f32_16x16x32_bf16 v[148:151], v[116:119], v[12:15], v[148:151]\n\t" \
  "ds_read_b64_tr_b16 v[114:115], v160 offset:14336\n\t" \
  "s_waitcnt lgkmcnt(11)\n\t" \
  "v_mfma_f32_16x16x32_bf16 v[136:139], v[120:123], v[8:11], v[136:139]\n\t" \
  "ds_read_b64_tr_b16 v[116:117], v161 offset:12288\n\t" \
  "v_mfma_f32_16x16x32_bf16 v[152:155], v[120:123], v[12:15], v[152:155]\n\t" \
  "ds_read_b64_tr_b16 v[118:119], v161 offset:14336\n\t" \
  "s_sub_u32 %[iss], %[iss], vcc_lo\n\t" \
  "v_mfma_f32_16x16x32_bf16 v[140:143], v[124:127], v[8:11], v[140:143]\n\t" \
  "ds_read_b64_tr_b16 v[120:121], v162 offset:12288\n\t" \
  "v_mfma_f32_16x16x32_bf16 v[156:159], v[124:127], v[12:15], v[156:159]\n\t" \
  "ds_read_b64_tr_b16 v[122:123], v162 offset:14336\n\t" \
  "s_add_u32 %[th], %[th], 1\n\t" \
  "s_cbranch_vccz QF1_%=\n\t" \
  "s_waitcnt vmcnt(16)\n\t" \
  "QG1_%=:\n\t" \
  "s_waitcnt lgkmcnt(11)\n\t" \
  "v_mfma_f32_16x16x32_bf16 v[128:131], v[96:99], v[16:19], v[128:131]\n\t" \
  "ds_read_b64_tr_b16 v[124:125], v163 offset:12288\n\t" \
  "v_readlane_b32 s94, v167, 6\n\t" \
  "v_mfma_f32_16x16x32_bf16 v[144:147], v[96:99], v[20:23], v[144:147]\n\t" \
  "ds_read_b64_tr_b16 v[126:127], v163 offset:14336\n\t" \
  "s_waitcnt lgkmcnt(7)\n\t" \
  "v_cmp_lt_u32 vcc, %[th], v166\n\t" \
  "v_mfma_f32_16x16x32_bf16 v[132:135], v[100:103], v[16:19], v[132:135]\n\t" \
  "s_mov_b64 exec, vcc\n\t" \
  "buffer_load_dwordx4 v[0:3], v164, %[ob], s94 offen offset:0 sc1\n\t" \
  "s_mov_b64 exec, -1\n\t" \
  "v_mfma_f32_16x16x32_bf16 v[148:151], v[100:103], v[20:23], v[148:151]\n\t" \
  "ds_read_b64_tr_b16 v[96:97], v160 offset:16384\n\t" \
  "ds_read_b64_tr_b16 v[98:99], v160 offset:18432\n\t" \
  "v_mfma_f32_16x16x32_bf16 v[136:139], v[104:107], v[16:19], v[136:139]\n\t" \
  "s_mov_b64 exec, vcc\n\t" \
  "buffer_load_dwordx4 v[4:7], v164, %[ob], s94 offen offset:1024 sc1\n\t" \
  "s_mov_b64 exec, -1\n\t" \
  "v_mfma_f32_16x16x32_bf16 v[152:155], v[104:107], v[20:23], v[152:155]\n\t" \
  "ds_read_b64_tr_b16 v[100:101], v161 offset:16384\n\t" \
  "ds_read_b64_tr_b16 v[102:103], v161 offset:18432\n\t" \
  "v_mfma_f32_16x16x32_bf16 v[140:143], v[108:111], v[16:19], v[140:143]\n\t" \
  "s_mov_b64 exec, vcc\n\t" \
  "buffer_load_dwordx4 v[8:11], v164, %[ob], s94 offen offset:2048 sc1\n\t" \
  "s_mov_b64 exec, -1\n\t" \
  "v_mfma_f32_16x16x32_bf16 v[156:159], v[108:111], v[20:23], v[156:159]\n\t" \
  "ds_read_b64_tr_b16 v[104:105], v162 offset:16384\n\t" \
  "ds_read_b64_tr_b16 v[106:107], v162 offset:18432\n\t" \
  "s_waitcnt lgkmcnt(10)\n\t" \
  "v_mfma_f32_16x16x32_bf16 v[128:131], v[112:115], v[24:27], v[128:131]\n\t" \
  "s_mov_b64 exec, vcc\n\t" \
  "buffer_load_dwordx4 v[12:15], v164, %[ob], s94 offen offset:3072 sc1\n\t" \
  "s_mov_b64 exec, -1\n\t" \
  "v_mfma_f32_16x16x32_bf16 v[144:147], v[112:115], v[28:31], v[144:147]\n\t" \
  "ds_read_b64_tr_b16 v[108:109], v163 offset:16384\n\t" \
  "ds_read_b64_tr_b16 v[110:111], v163 offset:18432\n\t" \
  "v_mfma_f32_16x16x32_bf16 v[132:135], v[116:119], v[24:27], v[132:135]\n\t" \
  "ds_read_b64_tr_b16 v[112:113], v160 offset:20480\n\t" \
  "ds_read_b32 v166, v165\n\t" \
  "v_mfma_f32_16x16x32_bf16 v[148:151], v[116:119], v[28:31], v[148:151]\n\t" \
  "ds_read_b64_tr_b16 v[114:115], v160 offset:22528\n\t" \
  "s_waitcnt lgkmcnt(11)\n\t" \
  "v_mfma_f32_16x16x32_bf16 v[136:139], v[120:123], v[24:27], v[136:139]\n\t" \
  "ds_read_b64_tr_b16 v[116:117], v161 offset:20480\n\t" \
  "v_mfma_f32_16x16x32_bf16 v[152:155], v[120:123], v[28:31], v[152:155]\n\t" \
  "ds_read_b64_tr_b16 v[118:119], v161 offset:22528\n\t" \
  "s_sub_u32 %[iss], %[iss], vcc_lo\n\t" \
  "v_mfma_f32_16x16x32_bf16 v[140:143], v[124:127], v[24:27], v[140:143]\n\t" \
  "ds_read_b64_tr_b16 v[120:121], v162 offset:20480\n\t" \
  "v_mfma_f32_16x16x32_bf16 v[156:159], v[124:127], v[28:31], v[156:159]\n\t" \
  "ds_read_b64_tr_b16 v[122:123], v162 offset:22528\n\t" \
  "s_add_u32 %[th], %[th], 1\n\t" \
  "s_cbranch_vccz QF2_%=\n\t" \
  "s_waitcnt vmcnt(16)\n\t" \
  "QG2_%=:\n\t" \
  "s_waitcnt lgkmcnt(11)\n\t" \
  "v_mfma_f32_16x16x32_bf16 v[128:131], v[96:99], v[32:35], v[128:131]\n\t" \
  "ds_read_b64_tr_b16 v[124:125], v163 offset:20480\n\t" \
  "v_readlane_b32 s94, v167, 7\n\t" \
  "v_mfma_f32_16x16x32_bf16 v[144:147], v[96:99], v[36:39], v[144:147]\n\t" \
  "ds_read_b64_tr_b16 v[126:127], v163 offset:22528\n\t" \
  "s_waitcnt lgkmcnt(7)\n\t" \
  "v_cmp_lt_u32 vcc, %[th], v166\n\t" \
  "v_mfma_f32_16x16x32_bf16 v[132:135], v[100:103], v[32:35], v[132:135]\n\t" \
  "s_mov_b64 exec, vcc\n\t" \
  "buffer_load_dwordx4 v[16:19], v164, %[ob], s94 offen offset:0 sc1\n\t" \
  "s_mov_b64 exec, -1\n\t" \
  "v_mfma_f32_16x16x32_bf16 v[148:151], v[100:103], v[36:39], v[148:151]\n\t" \
  "ds_read_b64_tr_b16 v[96:97], v160 offset:24576\n\t" \
  "ds_read_b64_tr_b16 v[98:99], v160 offset:26624\n\t" \
  "v_mfma_f32_16x16x32_bf16 v[136:139], v[104:107], v[32:35], v[136:139]\n\t" \
  "s_mov_b64 exec, vcc\n\t" \
  "buffer_load_dwordx4 v[20:23], v164, %[ob], s94 offen offset:1024 sc1\n\t" \
  "s_mov_b64 exec, -1\n\t" \
  "v_mfma_f32_16x16x32_bf16 v[152:155], v[104:107], v[36:39], v[152:155]\n\t" \
  "ds_read_b64_tr_b16 v[100:101], v161 offset:24576\n\t" \
  "ds_read_b64_tr_b16 v[102:103], v161 offset:26624\n\t" \
  "v_mfma_f32_16x16x32_bf16 v[140:143], v[108:111], v[32:35], v[140:143]\n\t" \
  "s_mov_b64 exec, vcc\n\t" \
  "buffer_load_dwordx4 v[24:27], v164, %[ob], s94 offen offset:2048 sc1\n\t" \
  "s_mov_b64 exec, -1\n\t" \
  "v_mfma_f32_16x16x32_bf16 v[156:159], v[108:111], v[36:39], v[156:159]\n\t" \
  "ds_read_b64_tr_b16 v[104:105], v162 offset:24576\n\t" \
  "ds_read_b64_tr_b16 v[106:107], v162 offset:26624\n\t" \
  "s_waitcnt lgkmcnt(10)\n\t" \
  "v_mfma_f32_16x16x32_bf16 v[128:131], v[112:115], v[40:43], v[128:131]\n\t" \
  "s_mov_b64 exec, vcc\n\t" \
  "buffer_load_dwordx4 v[28:31], v164, %[ob], s94 offen offset:3072 sc1\n\t" \
  "s_mov_b64 exec, -1\n\t" \
  "v_mfma_f32_16x16x32_bf16 v[144:147], v[112:115], v[44:47], v[144:147]\n\t" \
  "ds_read_b64_tr_b16 v[108:109], v163 offset:24576\n\t" \
  "ds_read_b64_tr_b16 v[110:111], v163 offset:26624\n\t" \
  "v_mfma_f32_16x16x32_bf16 v[132:135], v[116:119], v[40:43], v[132:135]\n\t" \
  "ds_read_b64_tr_b16 v[112:113], v160 offset:28672\n\t" \
  "ds_read_b32 v166, v165\n\t" \
  "v_mfma_f32_16x16x32_bf16 v[148:151], v[116:119], v[44:47], v[148:151]\n\t" \
  "ds_read_b64_tr_b16 v[114:115], v160 offset:30720\n\t" \
  "s_waitcnt lgkmcnt(11)\n\t" \
  "v_mfma_f32_16x16x32_bf16 v[136:139], v[120:123], v[40:43], v[136:139]\n\t" \
  "ds_read_b64_tr_b16 v[116:117], v161 offset:28672\n\t" \
  "v_mfma_f32_16x16x32_bf16 v[152:155], v[120:123], v[44:47], v[152:155]\n\t" \
  "ds_read_b64_tr_b16 v[118:119], v161 offset:30720\n\t" \
  "s_sub_u32 %[iss], %[iss], vcc_lo\n\t" \
  "v_mfma_f32_16x16x32_bf16 v[140:143], v[124:127], v[40:43], v[140:143]\n\t" \
  "ds_read_b64_tr_b16 v[120:121], v162 offset:28672\n\t" \
  "v_mfma_f32_16x16x32_bf16 v[156:159], v[124:127], v[44:47], v[156:159]\n\t" \
  "ds_read_b64_tr_b16 v[122:123], v162 offset:30720\n\t" \
  "s_add_u32 %[th], %[th], 1\n\t" \
  "s_cbranch_vccz QF3_%=\n\t" \
  "s_waitcnt vmcnt(16)\n\t" \
  "QG3_%=:\n\t" \
  "s_waitcnt lgkmcnt(11)\n\t" \
  "v_mfma_f32_16x16x32_bf16 v[128:131], v[96:99], v[48:51], v[128:131]\n\t" \
  "ds_read_b64_tr_b16 v[124:125], v163 offset:28672\n\t" \
  "v_readlane_b32 s94, v167, 8\n\t" \
  "v_mfma_f32_16x16x32_bf16 v[144:147], v[96:99], v[52:55], v[144:147]\n\t" \
  "ds_read_b64_tr_b16 v[126:127], v163 offset:30720\n\t" \
  "s_waitcnt lgkmcnt(7)\n\t" \
  "v_cmp_lt_u32 vcc, %[th], v166\n\t" \
  "v_mfma_f32_16x16x32_bf16 v[132:135], v[100:103], v[48:51], v[132:135]\n\t" \
  "s_mov_b64 exec, vcc\n\t" \
  "buffer_load_dwordx4 v[32:35], v164, %[ob], s94 offen offset:0 sc1\n\t" \
  "s_mov_b64 exec, -1\n\t" \
  "v_mfma_f32_16x16x32_bf16 v[148:151], v[100:103], v[52:55], v[148:151]\n\t" \
  "ds_read_b64_tr_b16 v[96:97], v160 offset:32768\n\t" \
  "ds_read_b64_tr_b16 v[98:99], v160 offset:34816\n\t" \
  "v_mfma_f32_16x16x32_bf16 v[136:139], v[104:107], v[48:51], v[136:139]\n\t" \
  "s_mov_b64 exec, vcc\n\t" \
  "buffer_load_dwordx4 v[36:39], v164, %[ob], s94 offen offset:1024 sc1\n\t" \
  "s_mov_b64 exec, -1\n\t" \
  "v_mfma_f32_16x16x32_bf16 v[152:155], v[104:107], v[52:55], v[152:155]\n\t" \
  "ds_read_b64_tr_b16 v[100:101], v161 offset:32768\n\t" \
  "ds_read_b64_tr_b16 v[102:103], v161 offset:34816\n\t" \
  "v_mfma_f32_16x16x32_bf16 v[140:143], v[108:111], v[48:51], v[140:143]\n\t" \
  "s_mov_b64 exec, vcc\n\t" \
  "buffer_load_dwordx4 v[40:43], v164, %[ob], s94 offen offset:2048 sc1\n\t" \
  "s_mov_b64 exec, -1\n\t" \
  "v_mfma_f32_16x16x32_bf16 v[156:159], v[108:111], v[52:55], v[156:159]\n\t" \
  "ds_read_b64_tr_b16 v[104:105], v162 offset:32768\n\t" \
  "ds_read_b64_tr_b16 v[106:107], v162 offset:34816\n\t" \
  "s_waitcnt lgkmcnt(10)\n\t" \
  "v_mfma_f32_16x16x32_bf16 v[128:131], v[112:115], v[56:59], v[128:131]\n\t" \
  "s_mov_b64 exec, vcc\n\t" \
  "buffer_load_dwordx4 v[44:47], v164, %[ob], s94 offen offset:3072 sc1\n\t" \
  "s_mov_b64 exec, -1\n\t" \
  "v_mfma_f32_16x16x32_bf16 v[144:147], v[112:115], v[60:63], v[144:147]\n\t" \
  "ds_read_b64_tr_b16 v[108:109], v163 offset:32768\n\t" \
  "ds_read_b64_tr_b16 v[110:111], v163 offset:34816\n\t" \
  "v_mfma_f32_16x16x32_bf16 v[132:135], v[116:119], v[56:59], v[132:135]\n\t" \
  "ds_read_b64_tr_b16 v[112:113], v160 offset:36864\n\t" \
  "ds_read_b32 v166, v165\n\t" \
  "v_mfma_f32_16x16x32_bf16 v[148:151], v[116:119], v[60:63], v[148:151]\n\t" \
  "ds_read_b64_tr_b16 v[114:115], v160 offset:38912\n\t" \
  "s_waitcnt lgkmcnt(11)\n\t" \
  "v_mfma_f32_16x16x32_bf16 v[136:139], v[120:123], v[56:59], v[136:139]\n\t" \
  "ds_read_b64_tr_b16 v[116:117], v161 offset:36864\n\t" \
  "v_mfma_f32_16x16x32_bf16 v[152:155], v[120:123], v[60:63], v[152:155]\n\t" \
  "ds_read_b64_tr_b16 v[118:119], v161 offset:38912\n\t" \
  "s_sub_u32 %[iss], %[iss], vcc_lo\n\t" \
  "v_mfma_f32_16x16x32_bf16 v[140:143], v[124:127], v[56:59], v[140:143]\n\t" \
  "ds_read_b64_tr_b16 v[120:121], v162 offset:36864\n\t" \
  "v_mfma_f32_16x16x32_bf16 v[156:159], v[124:127], v[60:63], v[156:159]\n\t" \
  "ds_read_b64_tr_b16 v[122:123], v162 offset:38912\n\t" \
  "s_add_u32 %[th], %[th], 1\n\t" \
  "s_cbranch_vccz QF4_%=\n\t" \
  "s_waitcnt vmcnt(16)\n\t" \
  "QG4_%=:\n\t" \
  "s_waitcnt lgkmcnt(11)\n\t" \
  "v_mfma_f32_16x16x32_bf16 v[128:131], v[96:99], v[64:67], v[128:131]\n\t" \
  "ds_read_b64_tr_b16 v[124:125], v163 offset:36864\n\t" \
  "v_readlane_b32 s94, v167, 9\n\t" \
  "v_mfma_f32_16x16x32_bf16 v[144:147], v[96:99], v[68:71], v[144:147]\n\t" \
  "ds_read_b64_tr_b16 v[126:127], v163 offset:38912\n\t" \
  "s_waitcnt lgkmcnt(7)\n\t" \
  "v_cmp_lt_u32 vcc, %[th], v166\n\t" \
  "v_mfma_f32_16x16x32_bf16 v[132:135], v[100:103], v[64:67], v[132:135]\n\t" \
  "s_mov_b64 exec, vcc\n\t" \
  "buffer_load_dwordx4 v[48:51], v164, %[ob], s94 offen offset:0 sc1\n\t" \
  "s_mov_b64 exec, -1\n\t" \
  "v_mfma_f32_16x16x32_bf16 v[148:151], v[100:103], v[68:71], v[148:151]\n\t" \
  "ds_read_b64_tr_b16 v[96:97], v160 offset:40960\n\t" \
  "ds_read_b64_tr_b16 v[98:99], v160 offset:43008\n\t" \
  "v_mfma_f32_16x16x32_bf16 v[136:139], v[104:107], v[64:67], v[136:139]\n\t" \
  "s_mov_b64 exec, vcc\n\t" \
  "buffer_load_dwordx4 v[52:55], v164, %[ob], s94 offen offset:1024 sc1\n\t" \
  "s_mov_b64 exec, -1\n\t" \
  "v_mfma_f32_16x16x32_bf16 v[152:155], v[104:107], v[68:71], v[152:155]\n\t" \
  "ds_read_b64_tr_b16 v[100:101], v161 offset:40960\n\t" \
  "ds_read_b64_tr_b16 v[102:103], v161 offset:43008\n\t" \
  "v_mfma_f32_16x16x32_bf16 v[140:143], v[108:111], v[64:67], v[140:143]\n\t" \
  "s_mov_b64 exec, vcc\n\t" \
  "buffer_load_dwordx4 v[56:59], v164, %[ob], s94 offen offset:2048 sc1\n\t" \
  "s_mov_b64 exec, -1\n\t" \
  "v_mfma_f32_16x16x32_bf16 v[156:159], v[108:111], v[68:71], v[156:159]\n\t" \
  "ds_read_b64_tr_b16 v[104:105], v162 offset:40960\n\t" \
  "ds_read_b64_tr_b16 v[106:107], v162 offset:43008\n\t" \
  "s_waitcnt lgkmcnt(10)\n\t" \
  "v_mfma_f32_16x16x32_bf16 v[128:131], v[112:115], v[72:75], v[128:131]\n\t" \
  "s_mov_b64 exec, vcc\n\t" \
  "buffer_load_dwordx4 v[60:63], v164, %[ob], s94 offen offset:3072 sc1\n\t" \
  "s_mov_b64 exec, -1\n\t" \
  "v_mfma_f32_16x16x32_bf16 v[144:147], v[112:115], v[76:79], v[144:147]\n\t" \
  "ds_read_b64_tr_b16 v[108:109], v163 offset:40960\n\t" \
  "ds_read_b64_tr_b16 v[110:111], v163 offset:43008\n\t" \
  "v_mfma_f32_16x16x32_bf16 v[132:135], v[116:119], v[72:75], v[132:135]\n\t" \
  "ds_read_b64_tr_b16 v[112:113], v160 offset:45056\n\t" \
  "ds_read_b32 v166, v165\n\t" \
  "v_mfma_f32_16x16x32_bf16 v[148:151], v[116:119], v[76:79], v[148:151]\n\t" \
  "ds_read_b64_tr_b16 v[114:115], v160 offset:47104\n\t" \
  "s_waitcnt lgkmcnt(11)\n\t" \
  "v_mfma_f32_16x16x32_bf16 v[136:139], v[120:123], v[72:75], v[136:139]\n\t" \
  "ds_read_b64_tr_b16 v[116:117], v161 offset:45056\n\t" \
  "v_mfma_f32_16x16x32_bf16 v[152:155], v[120:123], v[76:79], v[152:155]\n\t" \
  "ds_read_b64_tr_b16 v[118:119], v161 offset:47104\n\t" \
  "s_sub_u32 %[iss], %[iss], vcc_lo\n\t" \
  "v_mfma_f32_16x16x32_bf16 v[140:143], v[124:127], v[72:75], v[140:143]\n\t" \
  "ds_read_b64_tr_b16 v[120:121], v162 offset:45056\n\t" \
  "v_mfma_f32_16x16x32_bf16 v[156:159], v[124:127], v[76:79], v[156:159]\n\t" \
  "ds_read_b64_tr_b16 v[122:123], v162 offset:47104\n\t" \
  "s_add_u32 %[th], %[th], 1\n\t" \
  "s_cbranch_vccz QF5_%=\n\t" \
  "s_waitcnt vmcnt(16)\n\t" \
  "QG5_%=:\n\t" \
  "s_waitcnt lgkmcnt(11)\n\t" \
  "v_mfma_f32_16x16x32_bf16 v[128:131], v[96:99], v[80:83], v[128:131]\n\t" \
  "ds_read_b64_tr_b16 v[124:125], v163 offset:45056\n\t" \
  "v_readlane_b32 s94, v167, 10\n\t" \
  "v_mfma_f32_16x16x32_bf16 v[144:147], v[96:99], v[84:87], v[144:147]\n\t" \
  "ds_read_b64_tr_b16 v[126:127], v163 offset:47104\n\t" \
  "s_waitcnt lgkmcnt(7)\n\t" \
  "v_cmp_lt_u32 vcc, %[th], v166\n\t" \
  "v_mfma_f32_16x16x32_bf16 v[132:135], v[100:103], v[80:83], v[132:135]\n\t" \
  "s_mov_b64 exec, vcc\n\t" \
  "buffer_load_dwordx4 v[64:67], v164, %[ob], s94 offen offset:0 sc1\n\t" \
  "s_mov_b64 exec, -1\n\t" \
  "v_mfma_f32_16x16x32_bf16 v[148:151], v[100:103], v[84:87], v[148:151]\n\t" \
  "ds_read_b64_tr_b16 v[96:97], v160 offset:49152\n\t" \
  "ds_read_b64_tr_b16 v[98:99], v160 offset:51200\n\t" \
  "v_mfma_f32_16x16x32_bf16 v[136:139], v[104:107], v[80:83], v[136:139]\n\t" \
  "s_mov_b64 exec, vcc\n\t" \
  "buffer_load_dwordx4 v[68:71], v164, %[ob], s94 offen offset:1024 sc1\n\t" \
  "s_mov_b64 exec, -1\n\t" \
  "v_mfma_f32_16x16x32_bf16 v[152:155], v[104:107], v[84:87], v[152:155]\n\t" \
  "ds_read_b64_tr_b16 v[100:101], v161 offset:49152\n\t" \
  "ds_read_b64_tr_b16 v[102:103], v161 offset:51200\n\t" \
  "v_mfma_f32_16x16x32_bf16 v[140:143], v[108:111], v[80:83], v[140:143]\n\t" \
  "s_mov_b64 exec, vcc\n\t" \
  "buffer_load_dwordx4 v[72:75], v164, %[ob], s94 offen offset:2048 sc1\n\t" \
  "s_mov_b64 exec, -1\n\t" \
  "v_mfma_f32_16x16x32_bf16 v[156:159], v[108:111], v[84:87], v[156:159]\n\t" \
  "ds_read_b64_tr_b16 v[104:105], v162 offset:49152\n\t" \
  "ds_read_b64_tr_b16 v[106:107], v162 offset:51200\n\t" \
  "s_waitcnt lgkmcnt(10)\n\t" \
  "v_mfma_f32_16x16x32_bf16 v[128:131], v[112:115], v[88:91], v[128:131]\n\t" \
  "s_mov_b64 exec, vcc\n\t" \
  "buffer_load_dwordx4 v[76:79], v164, %[ob], s94 offen offset:3072 sc1\n\t" \
  "s_mov_b64 exec, -1\n\t" \
  "v_mfma_f32_16x16x32_bf16 v[144:147], v[112:115], v[92:95], v[144:147]\n\t" \
  "ds_read_b64_tr_b16 v[108:109], v163 offset:49152\n\t" \
  "ds_read_b64_tr_b16 v[110:111], v163 offset:51200\n\t" \
  "v_mfma_f32_16x16x32_bf16 v[132:135], v[116:119], v[88:91], v[132:135]\n\t" \
  "ds_read_b64_tr_b16 v[112:113], v160 offset:53248\n\t" \
  "ds_read_b32 v166, v165\n\t" \
  "v_mfma_f32_16x16x32_bf16 v[148:151], v[116:119], v[92:95], v[148:151]\n\t" \
  "ds_read_b64_tr_b16 v[114:115], v160 offset:55296\n\t" \
  "s_waitcnt lgkmcnt(11)\n\t" \
  "v_mfma_f32_16x16x32_bf16 v[136:139], v[120:123], v[88:91], v[136:139]\n\t" \
  "ds_read_b64_tr_b16 v[116:117], v161 offset:53248\n\t" \
  "v_mfma_f32_16x16x32_bf16 v[152:155], v[120:123], v[92:95], v[152:155]\n\t" \
  "ds_read_b64_tr_b16 v[118:119], v161 offset:55296\n\t" \
  "s_sub_u32 %[iss], %[iss], vcc_lo\n\t" \
  "v_mfma_f32_16x16x32_bf16 v[140:143], v[124:127], v[88:91], v[140:143]\n\t" \
  "ds_read_b64_tr_b16 v[120:121], v162 offset:53248\n\t" \
  "v_mfma_f32_16x16x32_bf16 v[156:159], v[124:127], v[92:95], v[156:159]\n\t" \
  "ds_read_b64_tr_b16 v[122:123], v162 offset:55296\n\t" \
  "s_add_u32 %[th], %[th], 1\n\t" \
  "s_cbranch_vccz QF6_%=\n\t" \
  "s_waitcnt vmcnt(16)\n\t" \
  "QG6_%=:\n\t" \
  "s_waitcnt lgkmcnt(11)\n\t" \
  "v_mfma_f32_16x16x32_bf16 v[128:131], v[96:99], v[0:3], v[128:131]\n\t" \
  "ds_read_b64_tr_b16 v[124:125], v163 offset:53248\n\t" \
  "v_readlane_b32 s94, v167, 11\n\t" \
  "v_mfma_f32_16x16x32_bf16 v[144:147], v[96:99], v[4:7], v[144:147]\n\t" \
  "ds_read_b64_tr_b16 v[126:127], v163 offset:55296\n\t" \
  "s_waitcnt lgkmcnt(7)\n\t" \
  "v_cmp_lt_u32 vcc, %[th], v166\n\t" \
  "v_mfma_f32_16x16x32_bf16 v[132:135], v[100:103], v[0:3], v[132:135]\n\t" \
  "s_mov_b64 exec, vcc\n\t" \
  "buffer_load_dwordx4 v[80:83], v164, %[ob], s94 offen offset:0 sc1\n\t" \
  "s_mov_b64 exec, -1\n\t" \
  "v_mfma_f32_16x16x32_bf16 v[148:151], v[100:103], v[4:7], v[148:151]\n\t" \
  "ds_read_b64_tr_b16 v[96:97], v160 offset:57344\n\t" \
  "ds_read_b64_tr_b16 v[98:99], v160 offset:59392\n\t" \
  "v_mfma_f32_16x16x32_bf16 v[136:139], v[104:107], v[0:3], v[136:139]\n\t" \
  "s_mov_b64 exec, vcc\n\t" \
  "buffer_load_dwordx4 v[84:87], v164, %[ob], s94 offen offset:1024 sc1\n\t" \
  "s_mov_b64 exec, -1\n\t" \
  "v_mfma_f32_16x16x32_bf16 v[152:155], v[104:107], v[4:7], v[152:155]\n\t" \
  "ds_read_b64_tr_b16 v[100:101], v161 offset:57344\n\t" \
  "ds_read_b64_tr_b16 v[102:103], v161 offset:59392\n\t" \
  "v_mfma_f32_16x16x32_bf16 v[140:143], v[108:111], v[0:3], v[140:143]\n\t" \
  "s_mov_b64 exec, vcc\n\t" \
  "buffer_load_dwordx4 v[88:91], v164, %[ob], s94 offen offset:2048 sc1\n\t" \
  "s_mov_b64 exec, -1\n\t" \
  "v_mfma_f32_16x16x32_bf16 v[156:159], v[108:111], v[4:7], v[156:159]\n\t" \
  "ds_read_b64_tr_b16 v[104:105], v162 offset:57344\n\t" \
  "ds_read_b64_tr_b16 v[106:107], v162 offset:59392\n\t" \
  "s_waitcnt lgkmcnt(10)\n\t" \
  "v_mfma_f32_16x16x32_bf16 v[128:131], v[112:115], v[8:11], v[128:131]\n\t" \
  "s_mov_b64 exec, vcc\n\t" \
  "buffer_load_dwordx4 v[92:95], v164, %[ob], s94 offen offset:3072 sc1\n\t" \
  "s_mov_b64 exec, -1\n\t" \
  "v_mfma_f32_16x16x32_bf16 v[144:147], v[112:115], v[12:15], v[144:147]\n\t" \
  "ds_read_b64_tr_b16 v[108:109], v163 offset:57344\n\t" \
  "ds_read_b64_tr_b16 v[110:111], v163 offset:59392\n\t" \
  "v_mfma_f32_16x16x32_bf16 v[132:135], v[116:119], v[8:11], v[132:135]\n\t" \
  "ds_read_b64_tr_b16 v[112:113], v160 offset:61440\n\t" \
  "ds_read_b32 v166, v165\n\t" \
  "v_mfma_f32_16x16x32_bf16 v[148:151], v[116:119], v[12:15], v[148:151]\n\t" \
  "ds_read_b64_tr_b16 v[114:115], v160 offset:63488\n\t" \
  "s_waitcnt lgkmcnt(11)\n\t" \
  "v_mfma_f32_16x16x32_bf16 v[136:139], v[120:123], v[8:11], v[136:139]\n\t" \
  "ds_read_b64_tr_b16 v[116:117], v161 offset:61440\n\t" \
  "v_mfma_f32_16x16x32_bf16 v[152:155], v[120:123], v[12:15], v[152:155]\n\t" \
  "ds_read_b64_tr_b16 v[118:119], v161 offset:63488\n\t" \
  "s_sub_u32 %[iss], %[iss], vcc_lo\n\t" \
  "v_mfma_f32_16x16x32_bf16 v[140:143], v[124:127], v[8:11], v[140:143]\n\t" \
  "ds_read_b64_tr_b16 v[120:121], v162 offset:61440\n\t" \
  "v_mfma_f32_16x16x32_bf16 v[156:159], v[124:127], v[12:15], v[156:159]\n\t" \
  "ds_read_b64_tr_b16 v[122:123], v162 offset:63488\n\t" \
  "s_add_u32 %[th], %[th], 1\n\t" \
  "s_cbranch_vccz QF7_%=\n\t" \
  "s_waitcnt vmcnt(16)\n\t" \
  "QG7_%=:\n\t" \
  "s_waitcnt lgkmcnt(11)\n\t" \
  "v_mfma_f32_16x16x32_bf16 v[128:131], v[96:99], v[16:19], v[128:131]\n\t" \
  "ds_read_b64_tr_b16 v[124:125], v163 offset:61440\n\t" \
  "v_readlane_b32 s94, v167, 12\n\t" \
  "v_mfma_f32_16x16x32_bf16 v[144:147], v[96:99], v[20:23], v[144:147]\n\t" \
  "ds_read_b64_tr_b16 v[126:127], v163 offset:63488\n\t" \
  "s_waitcnt lgkmcnt(7)\n\t" \
  "v_cmp_lt_u32 vcc, %[th], v166\n\t" \
  "v_mfma_f32_16x16x32_bf16 v[132:135], v[100:103], v[16:19], v[132:135]\n\t" \
  "v_add_u32 v160, 0x10000, v160\n\t" \
  "s_mov_b64 exec, vcc\n\t" \
  "buffer_load_dwordx4 v[0:3], v164, %[ob], s94 offen offset:0 sc1\n\t" \
  "s_mov_b64 exec, -1\n\t" \
  "v_mfma_f32_16x16x32_bf16 v[148:151], v[100:103], v[20:23], v[148:151]\n\t" \
  "ds_read_b64_tr_b16 v[96:97], v160 offset:0\n\t" \
  "ds_read_b64_tr_b16 v[98:99], v160 offset:2048\n\t" \
  "v_mfma_f32_16x16x32_bf16 v[136:139], v[104:107], v[16:19], v[136:139]\n\t" \
  "v_add_u32 v161, 0x10000, v161\n\t" \
  "s_mov_b64 exec, vcc\n\t" \
  "buffer_load_dwordx4 v[4:7], v164, %[ob], s94 offen offset:1024 sc1\n\t" \
  "s_mov_b64 exec, -1\n\t" \
  "v_mfma_f32_16x16x32_bf16 v[152:155], v[104:107], v[20:23], v[152:155]\n\t" \
  "ds_read_b64_tr_b16 v[100:101], v161 offset:0\n\t" \
  "ds_read_b64_tr_b16 v[102:103], v161 offset:2048\n\t" \
  "v_mfma_f32_16x16x32_bf16 v[140:143], v[108:111], v[16:19], v[140:143]\n\t" \
  "v_add_u32 v162, 0x10000, v162\n\t" \
  "s_mov_b64 exec, vcc\n\t" \
  "buffer_load_dwordx4 v[8:11], v164, %[ob], s94 offen offset:2048 sc1\n\t" \
  "s_mov_b64 exec, -1\n\t" \
  "v_mfma_f32_16x16x32_bf16 v[156:159], v[108:111], v[20:23], v[156:159]\n\t" \
  "ds_read_b64_tr_b16 v[104:105], v162 offset:0\n\t" \
  "ds_read_b64_tr_b16 v[106:107], v162 offset:2048\n\t" \
  "s_waitcnt lgkmcnt(10)\n\t" \
  "v_mfma_f32_16x16x32_bf16 v[128:131], v[112:115], v[24:27], v[128:131]\n\t" \
  "v_add_u32 v163, 0x10000, v163\n\t" \
  "s_mov_b64 exec, vcc\n\t" \
  "buffer_load_dwordx4 v[12:15], v164, %[ob], s94 offen offset:3072 sc1\n\t" \
  "s_mov_b64 exec, -1\n\t" \
  "v_mfma_f32_16x16x32_bf16 v[144:147], v[112:115], v[28:31], v[144:147]\n\t" \
  "ds_read_b64_tr_b16 v[108:109], v163 offset:0\n\t" \
  "ds_read_b64_tr_b16 v[110:111], v163 offset:2048\n\t" \
  "v_mfma_f32_16x16x32_bf16 v[132:135], v[116:119], v[24:27], v[132:135]\n\t" \
  "ds_read_b64_tr_b16 v[112:113], v160 offset:4096\n\t" \
  "ds_read_b32 v166, v165\n\t" \
  "v_mfma_f32_16x16x32_bf16 v[148:151], v[116:119], v[28:31], v[148:151]\n\t" \
  "ds_read_b64_tr_b16 v[114:115], v160 offset:6144\n\t" \
  "s_waitcnt lgkmcnt(11)\n\t" \
  "v_mfma_f32_16x16x32_bf16 v[136:139], v[120:123], v[24:27], v[136:139]\n\t" \
  "ds_read_b64_tr_b16 v[116:117], v161 offset:4096\n\t" \
  "v_mfma_f32_16x16x32_bf16 v[152:155], v[120:123], v[28:31], v[152:155]\n\t" \
  "ds_read_b64_tr_b16 v[118:119], v161 offset:6144\n\t" \
  "s_sub_u32 %[iss], %[iss], vcc_lo\n\t" \
  "v_mfma_f32_16x16x32_bf16 v[140:143], v[124:127], v[24:27], v[140:143]\n\t" \
  "ds_read_b64_tr_b16 v[120:121], v162 offset:4096\n\t" \
  "v_mfma_f32_16x16x32_bf16 v[156:159], v[124:127], v[28:31], v[156:159]\n\t" \
  "ds_read_b64_tr_b16 v[122:123], v162 offset:6144\n\t" \
  "s_add_u32 %[th], %[th], 1\n\t" \
  "s_cbranch_vccz QF8_%=\n\t" \
  "s_waitcnt vmcnt(16)\n\t" \
  "QG8_%=:\n\t" \
  "s_waitcnt lgkmcnt(11)\n\t" \
  "v_mfma_f32_16x16x32_bf16 v[128:131], v[96:99], v[32:35], v[128:131]\n\t" \
  "ds_read_b64_tr_b16 v[124:125], v163 offset:4096\n\t" \
  "v_readlane_b32 s94, v167, 13\n\t" \
  "v_mfma_f32_16x16x32_bf16 v[144:147], v[96:99], v[36:39], v[144:147]\n\t" \
  "ds_read_b64_tr_b16 v[126:127], v163 offset:6144\n\t" \
  "s_waitcnt lgkmcnt(7)\n\t" \
  "v_cmp_lt_u32 vcc, %[th], v166\n\t" \
  "v_mfma_f32_16x16x32_bf16 v[132:135], v[100:103], v[32:35], v[132:135]\n\t" \
  "s_mov_b64 exec, vcc\n\t" \
  "buffer_load_dwordx4 v[16:19], v164, %[ob], s94 offen offset:0 sc1\n\t" \
  "s_mov_b64 exec, -1\n\t" \
  "v_mfma_f32_16x16x32_bf16 v[148:151], v[100:103], v[36:39], v[148:151]\n\t" \
  "ds_read_b64_tr_b16 v[96:97], v160 offset:8192\n\t" \
  "ds_read_b64_tr_b16 v[98:99], v160 offset:10240\n\t" \
  "v_mfma_f32_16x16x32_bf16 v[136:139], v[104:107], v[32:35], v[136:139]\n\t" \
  "s_mov_b64 exec, vcc\n\t" \
  "buffer_load_dwordx4 v[20:23], v164, %[ob], s94 offen offset:1024 sc1\n\t" \
  "s_mov_b64 exec, -1\n\t" \
  "v_mfma_f32_16x16x32_bf16 v[152:155], v[104:107], v[36:39], v[152:155]\n\t" \
  "ds_read_b64_tr_b16 v[100:101], v161 offset:8192\n\t" \
  "ds_read_b64_tr_b16 v[102:103], v161 offset:10240\n\t" \
  "v_mfma_f32_16x16x32_bf16 v[140:143], v[108:111], v[32:35], v[140:143]\n\t" \
  "s_mov_b64 exec, vcc\n\t" \
  "buffer_load_dwordx4 v[24:27], v164, %[ob], s94 offen offset:2048 sc1\n\t" \
  "s_mov_b64 exec, -1\n\t" \
  "v_mfma_f32_16x16x32_bf16 v[156:159], v[108:111], v[36:39], v[156:159]\n\t" \
  "ds_read_b64_tr_b16 v[104:105], v162 offset:8192\n\t" \
  "ds_read_b64_tr_b16 v[106:107], v162 offset:10240\n\t" \
  "s_waitcnt lgkmcnt(10)\n\t" \
  "v_mfma_f32_16x16x32_bf16 v[128:131], v[112:115], v[40:43], v[128:131]\n\t" \
  "s_mov_b64 exec, vcc\n\t" \
  "buffer_load_dwordx4 v[28:31], v164, %[ob], s94 offen offset:3072 sc1\n\t" \
  "s_mov_b64 exec, -1\n\t" \
  "v_mfma_f32_16x16x32_bf16 v[144:147], v[112:115], v[44:47], v[144:147]\n\t" \
  "ds_read_b64_tr_b16 v[108:109], v163 offset:8192\n\t" \
  "ds_read_b64_tr_b16 v[110:111], v163 offset:10240\n\t" \
  "v_mfma_f32_16x16x32_bf16 v[132:135], v[116:119], v[40:43], v[132:135]\n\t" \
  "ds_read_b64_tr_b16 v[112:113], v160 offset:12288\n\t" \
  "ds_read_b32 v166, v165\n\t" \
  "v_mfma_f32_16x16x32_bf16 v[148:151], v[116:119], v[44:47], v[148:151]\n\t" \
  "ds_read_b64_tr_b16 v[114:115], v160 offset:14336\n\t" \
  "s_waitcnt lgkmcnt(11)\n\t" \
  "v_mfma_f32_16x16x32_bf16 v[136:139], v[120:123], v[40:43], v[136:139]\n\t" \
  "ds_read_b64_tr_b16 v[116:117], v161 offset:12288\n\t" \
  "v_mfma_f32_16x16x32_bf16 v[152:155], v[120:123], v[44:47], v[152:155]\n\t" \
  "ds_read_b64_tr_b16 v[118:119], v161 offset:14336\n\t" \
  "s_sub_u32 %[iss], %[iss], vcc_lo\n\t" \
  "v_mfma_f32_16x16x32_bf16 v[140:143], v[124:127], v[40:43], v[140:143]\n\t" \
  "ds_read_b64_tr_b16 v[120:121], v162 offset:12288\n\t" \
  "v_mfma_f32_16x16x32_bf16 v[156:159], v[124:127], v[44:47], v[156:159]\n\t" \
  "ds_read_b64_tr_b16 v[122:123], v162 offset:14336\n\t" \
  "s_add_u32 %[th], %[th], 1\n\t" \
  "s_cbranch_vccz QF9_%=\n\t" \
  "s_waitcnt vmcnt(16)\n\t" \
  "QG9_%=:\n\t" \
  "s_waitcnt lgkmcnt(11)\n\t" \
  "v_mfma_f32_16x16x32_bf16 v[128:131], v[96:99], v[48:51], v[128:131]\n\t" \
  "ds_read_b64_tr_b16 v[124:125], v163 offset:12288\n\t" \
  "v_readlane_b32 s94, v167, 14\n\t" \
  "v_mfma_f32_16x16x32_bf16 v[144:147], v[96:99], v[52:55], v[144:147]\n\t" \
  "ds_read_b64_tr_b16 v[126:127], v163 offset:14336\n\t" \
  "s_waitcnt lgkmcnt(7)\n\t" \
  "v_cmp_lt_u32 vcc, %[th], v166\n\t" \
  "v_mfma_f32_16x16x32_bf16 v[132:135], v[100:103], v[48:51], v[132:135]\n\t" \
  "s_mov_b64 exec, vcc\n\t" \
  "buffer_load_dwordx4 v[32:35], v164, %[ob], s94 offen offset:0 sc1\n\t" \
  "s_mov_b64 exec, -1\n\t" \
  "v_mfma_f32_16x16x32_bf16 v[148:151], v[100:103], v[52:55], v[148:151]\n\t" \
  "ds_read_b64_tr_b16 v[96:97], v160 offset:16384\n\t" \
  "ds_read_b64_tr_b16 v[98:99], v160 offset:18432\n\t" \
  "v_mfma_f32_16x16x32_bf16 v[136:139], v[104:107], v[48:51], v[136:139]\n\t" \
  "s_mov_b64 exec, vcc\n\t" \
  "buffer_load_dwordx4 v[36:39], v164, %[ob], s94 offen offset:1024 sc1\n\t" \
  "s_mov_b64 exec, -1\n\t" \
  "v_mfma_f32_16x16x32_bf16 v[152:155], v[104:107], v[52:55], v[152:155]\n\t" \
  "ds_read_b64_tr_b16 v[100:101], v161 offset:16384\n\t" \
  "ds_read_b64_tr_b16 v[102:103], v161 offset:18432\n\t" \
  "v_mfma_f32_16x16x32_bf16 v[140:143], v[108:111], v[48:51], v[140:143]\n\t" \
  "s_mov_b64 exec, vcc\n\t" \
  "buffer_load_dwordx4 v[40:43], v164, %[ob], s94 offen offset:2048 sc1\n\t" \
  "s_mov_b64 exec, -1\n\t" \
  "v_mfma_f32_16x16x32_bf16 v[156:159], v[108:111], v[52:55], v[156:159]\n\t" \
  "ds_read_b64_tr_b16 v[104:105], v162 offset:16384\n\t" \
  "ds_read_b64_tr_b16 v[106:107], v162 offset:18432\n\t" \
  "s_waitcnt lgkmcnt(10)\n\t" \
  "v_mfma_f32_16x16x32_bf16 v[128:131], v[112:115], v[56:59], v[128:131]\n\t" \
  "s_mov_b64 exec, vcc\n\t" \
  "buffer_load_dwordx4 v[44:47], v164, %[ob], s94 offen offset:3072 sc1\n\t" \
  "s_mov_b64 exec, -1\n\t" \
  "v_mfma_f32_16x16x32_bf16 v[144:147], v[112:115], v[60:63], v[144:147]\n\t" \
  "ds_read_b64_tr_b16 v[108:109], v163 offset:16384\n\t" \
  "ds_read_b64_tr_b16 v[110:111], v163 offset:18432\n\t" \
  "v_mfma_f32_16x16x32_bf16 v[132:135], v[116:119], v[56:59], v[132:135]\n\t" \
  "ds_read_b64_tr_b16 v[112:113], v160 offset:20480\n\t" \
  "ds_read_b32 v166, v165\n\t" \
  "v_mfma_f32_16x16x32_bf16 v[148:151], v[116:119], v[60:63], v[148:151]\n\t" \
  "ds_read_b64_tr_b16 v[114:115], v160 offset:22528\n\t" \
  "s_waitcnt lgkmcnt(11)\n\t" \
  "v_mfma_f32_16x16x32_bf16 v[136:139], v[120:123], v[56:59], v[136:139]\n\t" \
  "ds_read_b64_tr_b16 v[116:117], v161 offset:20480\n\t" \
  "v_mfma_f32_16x16x32_bf16 v[152:155], v[120:123], v[60:63], v[152:155]\n\t" \
  "ds_read_b64_tr_b16 v[118:119], v161 offset:22528\n\t" \
  "s_sub_u32 %[iss], %[iss], vcc_lo\n\t" \
  "v_mfma_f32_16x16x32_bf16 v[140:143], v[124:127], v[56:59], v[140:143]\n\t" \
  "ds_read_b64_tr_b16 v[120:121], v162 offset:20480\n\t" \
  "s_mov_b64 exec, 1\n\t" \
  "ds_write_b32 v165, v165 offset:76\n\t" \
  "s_mov_b64 exec, -1\n\t" \
  "v_mfma_f32_16x16x32_bf16 v[156:159], v[124:127], v[60:63], v[156:159]\n\t" \
  "ds_read_b64_tr_b16 v[122:123], v162 offset:22528\n\t" \
  "s_add_u32 %[th], %[th], 1\n\t" \
  "s_cbranch_vccz QF10_%=\n\t" \
  "s_waitcnt vmcnt(16)\n\t" \
  "QG10_%=:\n\t" \
  "s_waitcnt lgkmcnt(12)\n\t" \
  "v_mfma_f32_16x16x32_bf16 v[128:131], v[96:99], v[64:67], v[128:131]\n\t" \
  "ds_read_b64_tr_b16 v[124:125], v163 offset:20480\n\t" \
  "v_readlane_b32 s94, v167, 15\n\t" \
  "v_mfma_f32_16x16x32_bf16 v[144:147], v[96:99], v[68:71], v[144:147]\n\t" \
  "ds_read_b64_tr_b16 v[126:127], v163 offset:22528\n\t" \
  "s_waitcnt lgkmcnt(8)\n\t" \
  "v_cmp_lt_u32 vcc, %[th], v166\n\t" \
  "v_mfma_f32_16x16x32_bf16 v[132:135], v[100:103], v[64:67], v[132:135]\n\t" \
  "s_mov_b64 exec, vcc\n\t" \
  "buffer_load_dwordx4 v[48:51], v164, %[ob], s94 offen offset:0 sc1\n\t" \
  "s_mov_b64 exec, -1\n\t" \
  "v_mfma_f32_16x16x32_bf16 v[148:151], v[100:103], v[68:71], v[148:151]\n\t" \
  "ds_read_b64_tr_b16 v[96:97], v160 offset:24576\n\t" \
  "ds_read_b64_tr_b16 v[98:99], v160 offset:26624\n\t" \
  "v_mfma_f32_16x16x32_bf16 v[136:139], v[104:107], v[64:67], v[136:139]\n\t" \
  "s_mov_b64 exec, vcc\n\t" \
  "buffer_load_dwordx4 v[52:55], v164, %[ob], s94 offen offset:1024 sc1\n\t" \
  "s_mov_b64 exec, -1\n\t" \
  "v_mfma_f32_16x16x32_bf16 v[152:155], v[104:107], v[68:71], v[152:155]\n\t" \
  "ds_read_b64_tr_b16 v[100:101], v161 offset:24576\n\t" \
  "ds_read_b64_tr_b16 v[102:103], v161 offset:26624\n\t" \
  "v_mfma_f32_16x16x32_bf16 v[140:143], v[108:111], v[64:67], v[140:143]\n\t" \
  "s_mov_b64 exec, vcc\n\t" \
  "buffer_load_dwordx4 v[56:59], v164, %[ob], s94 offen offset:2048 sc1\n\t" \
  "s_mov_b64 exec, -1\n\t" \
  "v_mfma_f32_16x16x32_bf16 v[156:159], v[108:111], v[68:71], v[156:159]\n\t" \
  "ds_read_b64_tr_b16 v[104:105], v162 offset:24576\n\t" \
  "ds_read_b64_tr_b16 v[106:107], v162 offset:26624\n\t" \
  "s_waitcnt lgkmcnt(11)\n\t" \
  "v_mfma_f32_16x16x32_bf16 v[128:131], v[112:115], v[72:75], v[128:131]\n\t" \
  "s_mov_b64 exec, vcc\n\t" \
  "buffer_load_dwordx4 v[60:63], v164, %[ob], s94 offen offset:3072 sc1\n\t" \
  "s_mov_b64 exec, -1\n\t" \
  "v_mfma_f32_16x16x32_bf16 v[144:147], v[112:115], v[76:79], v[144:147]\n\t" \
  "ds_read_b64_tr_b16 v[108:109], v163 offset:24576\n\t" \
  "ds_read_b64_tr_b16 v[110:111], v163 offset:26624\n\t" \
  "v_mfma_f32_16x16x32_bf16 v[132:135], v[116:119], v[72:75], v[132:135]\n\t" \
  "ds_read_b64_tr_b16 v[112:113], v160 offset:28672\n\t" \
  "v_mfma_f32_16x16x32_bf16 v[148:151], v[116:119], v[76:79], v[148:151]\n\t" \
  "ds_read_b64_tr_b16 v[114:115], v160 offset:30720\n\t" \
  "s_waitcnt lgkmcnt(10)\n\t" \
  "v_mfma_f32_16x16x32_bf16 v[136:139], v[120:123], v[72:75], v[136:139]\n\t" \
  "ds_read_b64_tr_b16 v[116:117], v161 offset:28672\n\t" \
  "v_mfma_f32_16x16x32_bf16 v[152:155], v[120:123], v[76:79], v[152:155]\n\t" \
  "ds_read_b64_tr_b16 v[118:119], v161 offset:30720\n\t" \
  "s_sub_u32 %[iss], %[iss], vcc_lo\n\t" \
  "v_mfma_f32_16x16x32_bf16 v[140:143], v[124:127], v[72:75], v[140:143]\n\t" \
  "ds_read_b64_tr_b16 v[120:121], v162 offset:28672\n\t" \
  "v_mfma_f32_16x16x32_bf16 v[156:159], v[124:127], v[76:79], v[156:159]\n\t" \
  "ds_read_b64_tr_b16 v[122:123], v162 offset:30720\n\t" \
  "s_add_u32 %[th], %[th], 1\n\t" \
  "s_cbranch_vccz QF11_%=\n\t" \
  "s_waitcnt vmcnt(16)\n\t" \
  "QG11_%=:\n\t" \
  "s_waitcnt lgkmcnt(10)\n\t" \
  "v_mfma_f32_16x16x32_bf16 v[128:131], v[96:99], v[80:83], v[128:131]\n\t" \
  "ds_read_b64_tr_b16 v[124:125], v163 offset:28672\n\t" \
  "v_mfma_f32_16x16x32_bf16 v[144:147], v[96:99], v[84:87], v[144:147]\n\t" \
  "ds_read_b64_tr_b16 v[126:127], v163 offset:30720\n\t" \
  "v_mfma_f32_16x16x32_bf16 v[132:135], v[100:103], v[80:83], v[132:135]\n\t" \
  "ds_read_b64_tr_b16 v[96:97], v160 offset:32768\n\t" \
  "v_mfma_f32_16x16x32_bf16 v[148:151], v[100:103], v[84:87], v[148:151]\n\t" \
  "ds_read_b64_tr_b16 v[98:99], v160 offset:34816\n\t" \
  "s_waitcnt lgkmcnt(10)\n\t" \
  "v_mfma_f32_16x16x32_bf16 v[136:139], v[104:107], v[80:83], v[136:139]\n\t" \
  "ds_read_b64_tr_b16 v[100:101], v161 offset:32768\n\t" \
  "v_mfma_f32_16x16x32_bf16 v[152:155], v[104:107], v[84:87], v[152:155]\n\t" \
  "ds_read_b64_tr_b16 v[102:103], v161 offset:34816\n\t" \
  "v_mfma_f32_16x16x32_bf16 v[140:143], v[108:111], v[80:83], v[140:143]\n\t" \
  "ds_read_b64_tr_b16 v[104:105], v162 offset:32768\n\t" \
  "v_mfma_f32_16x16x32_bf16 v[156:159], v[108:111], v[84:87], v[156:159]\n\t" \
  "ds_read_b64_tr_b16 v[106:107], v162 offset:34816\n\t" \
  "s_waitcnt lgkmcnt(10)\n\t" \
  "v_mfma_f32_16x16x32_bf16 v[128:131], v[112:115], v[88:91], v[128:131]\n\t" \
  "ds_read_b64_tr_b16 v[108:109], v163 offset:32768\n\t" \
  "v_mfma_f32_16x16x32_bf16 v[144:147], v[112:115], v[92:95], v[144:147]\n\t" \
  "ds_read_b64_tr_b16 v[110:111], v163 offset:34816\n\t" \
  "v_mfma_f32_16x16x32_bf16 v[132:135], v[116:119], v[88:91], v[132:135]\n\t" \
  "ds_read_b64_tr_b16 v[112:113], v160 offset:36864\n\t" \
  "v_mfma_f32_16x16x32_bf16 v[148:151], v[116:119], v[92:95], v[148:151]\n\t" \
  "ds_read_b64_tr_b16 v[114:115], v160 offset:38912\n\t" \
  "s_waitcnt lgkmcnt(10)\n\t" \
  "v_mfma_f32_16x16x32_bf16 v[136:139], v[120:123], v[88:91], v[136:139]\n\t" \
  "ds_read_b64_tr_b16 v[116:117], v161 offset:36864\n\t" \
  "v_mfma_f32_16x16x32_bf16 v[152:155], v[120:123], v[92:95], v[152:155]\n\t" \
  "ds_read_b64_tr_b16 v[118:119], v161 offset:38912\n\t" \
  "v_mfma_f32_16x16x32_bf16 v[140:143], v[124:127], v[88:91], v[140:143]\n\t" \
  "ds_read_b64_tr_b16 v[120:121], v162 offset:36864\n\t" \
  "v_mfma_f32_16x16x32_bf16 v[156:159], v[124:127], v[92:95], v[156:159]\n\t" \
  "ds_read_b64_tr_b16 v[122:123], v162 offset:38912\n\t" \
  "s_cbranch_vccz QF12_%=\n\t" \
  "s_waitcnt vmcnt(12)\n\t" \
  "QG12_%=:\n\t" \
  "s_waitcnt lgkmcnt(10)\n\t" \
  "v_mfma_f32_16x16x32_bf16 v[128:131], v[96:99], v[0:3], v[128:131]\n\t" \
  "ds_read_b64_tr_b16 v[124:125], v163 offset:36864\n\t" \
  "v_mfma_f32_16x16x32_bf16 v[144:147], v[96:99], v[4:7], v[144:147]\n\t" \
  "ds_read_b64_tr_b16 v[126:127], v163 offset:38912\n\t" \
  "v_mfma_f32_16x16x32_bf16 v[132:135], v[100:103], v[0:3], v[132:135]\n\t" \
  "ds_read_b64_tr_b16 v[96:97], v160 offset:40960\n\t" \
  "v_mfma_f32_16x16x32_bf16 v[148:151], v[100:103], v[4:7], v[148:151]\n\t" \
  "ds_read_b64_tr_b16 v[98:99], v160 offset:43008\n\t" \
  "s_waitcnt lgkmcnt(10)\n\t" \
  "v_mfma_f32_16x16x32_bf16 v[136:139], v[104:107], v[0:3], v[136:139]\n\t" \
  "ds_read_b64_tr_b16 v[100:101], v161 offset:40960\n\t" \
  "v_mfma_f32_16x16x32_bf16 v[152:155], v[104:107], v[4:7], v[152:155]\n\t" \
  "ds_read_b64_tr_b16 v[102:103], v161 offset:43008\n\t" \
  "v_mfma_f32_16x16x32_bf16 v[140:143], v[108:111], v[0:3], v[140:143]\n\t" \
  "ds_read_b64_tr_b16 v[104:105], v162 offset:40960\n\t" \
  "v_mfma_f32_16x16x32_bf16 v[156:159], v[108:111], v[4:7], v[156:159]\n\t" \
  "ds_read_b64_tr_b16 v[106:107], v162 offset:43008\n\t" \
  "s_waitcnt lgkmcnt(10)\n\t" \
  "v_mfma_f32_16x16x32_bf16 v[128:131], v[112:115], v[8:11], v[128:131]\n\t" \
  "ds_read_b64_tr_b16 v[108:109], v163 offset:40960\n\t" \
  "v_mfma_f32_16x16x32_bf16 v[144:147], v[112:115], v[12:15], v[144:147]\n\t" \
  "ds_read_b64_tr_b16 v[110:111], v163 offset:43008\n\t" \
  "v_mfma_f32_16x16x32_bf16 v[132:135], v[116:119], v[8:11], v[132:135]\n\t" \
  "ds_read_b64_tr_b16 v[112:113], v160 offset:45056\n\t" \
  "v_mfma_f32_16x16x32_bf16 v[148:151], v[116:119], v[12:15], v[148:151]\n\t" \
  "ds_read_b64_tr_b16 v[114:115], v160 offset:47104\n\t" \
  "s_waitcnt lgkmcnt(10)\n\t" \
  "v_mfma_f32_16x16x32_bf16 v[136:139], v[120:123], v[8:11], v[136:139]\n\t" \
  "ds_read_b64_tr_b16 v[116:117], v161 offset:45056\n\t" \
  "v_mfma_f32_16x16x32_bf16 v[152:155], v[120:123], v[12:15], v[152:155]\n\t" \
  "ds_read_b64_tr_b16 v[118:119], v161 offset:47104\n\t" \
  "v_mfma_f32_16x16x32_bf16 v[140:143], v[124:127], v[8:11], v[140:143]\n\t" \
  "ds_read_b64_tr_b16 v[120:121], v162 offset:45056\n\t" \
  "v_mfma_f32_16x16x32_bf16 v[156:159], v[124:127], v[12:15], v[156:159]\n\t" \
  "ds_read_b64_tr_b16 v[122:123], v162 offset:47104\n\t" \
  "s_cbranch_vccz QF13_%=\n\t" \
  "s_waitcnt vmcnt(8)\n\t" \
  "QG13_%=:\n\t" \
  "s_waitcnt lgkmcnt(10)\n\t" \
  "v_mfma_f32_16x16x32_bf16 v[128:131], v[96:99], v[16:19], v[128:131]\n\t" \
  "ds_read_b64_tr_b16 v[124:125], v163 offset:45056\n\t" \
  "v_mfma_f32_16x16x32_bf16 v[144:147], v[96:99], v[20:23], v[144:147]\n\t" \
  "ds_read_b64_tr_b16 v[126:127], v163 offset:47104\n\t" \
  "v_mfma_f32_16x16x32_bf16 v[132:135], v[100:103], v[16:19], v[132:135]\n\t" \
  "ds_read_b64_tr_b16 v[96:97], v160 offset:49152\n\t" \
  "v_mfma_f32_16x16x32_bf16 v[148:151], v[100:103], v[20:23], v[148:151]\n\t" \
  "ds_read_b64_tr_b16 v[98:99], v160 offset:51200\n\t" \
  "s_waitcnt lgkmcnt(10)\n\t" \
  "v_mfma_f32_16x16x32_bf16 v[136:139], v[104:107], v[16:19], v[136:139]\n\t" \
  "ds_read_b64_tr_b16 v[100:101], v161 offset:49152\n\t" \
  "v_mfma_f32_16x16x32_bf16 v[152:155], v[104:107], v[20:23], v[152:155]\n\t" \
  "ds_read_b64_tr_b16 v[102:103], v161 offset:51200\n\t" \
  "v_mfma_f32_16x16x32_bf16 v[140:143], v[108:111], v[16:19], v[140:143]\n\t" \
  "ds_read_b64_tr_b16 v[104:105], v162 offset:49152\n\t" \
  "v_mfma_f32_16x16x32_bf16 v[156:159], v[108:111], v[20:23], v[156:159]\n\t" \
  "ds_read_b64_tr_b16 v[106:107], v162 offset:51200\n\t" \
  "s_waitcnt lgkmcnt(10)\n\t" \
  "v_mfma_f32_16x16x32_bf16 v[128:131], v[112:115], v[24:27], v[128:131]\n\t" \
  "ds_read_b64_tr_b16 v[108:109], v163 offset:49152\n\t" \
  "v_mfma_f32_16x16x32_bf16 v[144:147], v[112:115], v[28:31], v[144:147]\n\t" \
  "ds_read_b64_tr_b16 v[110:111], v163 offset:51200\n\t" \
  "v_mfma_f32_16x16x32_bf16 v[132:135], v[116:119], v[24:27], v[132:135]\n\t" \
  "ds_read_b64_tr_b16 v[112:113], v160 offset:53248\n\t" \
  "v_mfma_f32_16x16x32_bf16 v[148:151], v[116:119], v[28:31], v[148:151]\n\t" \
  "ds_read_b64_tr_b16 v[114:115], v160 offset:55296\n\t" \
  "s_waitcnt lgkmcnt(10)\n\t" \
  "v_mfma_f32_16x16x32_bf16 v[136:139], v[120:123], v[24:27], v[136:139]\n\t" \
  "ds_read_b64_tr_b16 v[116:117], v161 offset:53248\n\t" \
  "v_mfma_f32_16x16x32_bf16 v[152:155], v[120:123], v[28:31], v[152:155]\n\t" \
  "ds_read_b64_tr_b16 v[118:119], v161 offset:55296\n\t" \
  "v_mfma_f32_16x16x32_bf16 v[140:143], v[124:127], v[24:27], v[140:143]\n\t" \
  "ds_read_b64_tr_b16 v[120:121], v162 offset:53248\n\t" \
  "v_mfma_f32_16x16x32_bf16 v[156:159], v[124:127], v[28:31], v[156:159]\n\t" \
  "ds_read_b64_tr_b16 v[122:123], v162 offset:55296\n\t" \
  "s_cbranch_vccz QF14_%=\n\t" \
  "s_waitcnt vmcnt(4)\n\t" \
  "QG14_%=:\n\t" \
  "s_waitcnt lgkmcnt(10)\n\t" \
  "v_mfma_f32_16x16x32_bf16 v[128:131], v[96:99], v[32:35], v[128:131]\n\t" \
  "ds_read_b64_tr_b16 v[124:125], v163 offset:53248\n\t" \
  "v_mfma_f32_16x16x32_bf16 v[144:147], v[96:99], v[36:39], v[144:147]\n\t" \
  "ds_read_b64_tr_b16 v[126:127], v163 offset:55296\n\t" \
  "v_mfma_f32_16x16x32_bf16 v[132:135], v[100:103], v[32:35], v[132:135]\n\t" \
  "ds_read_b64_tr_b16 v[96:97], v160 offset:57344\n\t" \
  "v_mfma_f32_16x16x32_bf16 v[148:151], v[100:103], v[36:39], v[148:151]\n\t" \
  "ds_read_b64_tr_b16 v[98:99], v160 offset:59392\n\t" \
  "s_waitcnt lgkmcnt(10)\n\t" \
  "v_mfma_f32_16x16x32_bf16 v[136:139], v[104:107], v[32:35], v[136:139]\n\t" \
  "ds_read_b64_tr_b16 v[100:101], v161 offset:57344\n\t" \
  "v_mfma_f32_16x16x32_bf16 v[152:155], v[104:107], v[36:39], v[152:155]\n\t" \
  "ds_read_b64_tr_b16 v[102:103], v161 offset:59392\n\t" \
  "v_mfma_f32_16x16x32_bf16 v[140:143], v[108:111], v[32:35], v[140:143]\n\t" \
  "ds_read_b64_tr_b16 v[104:105], v162 offset:57344\n\t" \
  "v_mfma_f32_16x16x32_bf16 v[156:159], v[108:111], v[36:39], v[156:159]\n\t" \
  "ds_read_b64_tr_b16 v[106:107], v162 offset:59392\n\t" \
  "s_waitcnt lgkmcnt(10)\n\t" \
  "v_mfma_f32_16x16x32_bf16 v[128:131], v[112:115], v[40:43], v[128:131]\n\t" \
  "ds_read_b64_tr_b16 v[108:109], v163 offset:57344\n\t" \
  "v_mfma_f32_16x16x32_bf16 v[144:147], v[112:115], v[44:47], v[144:147]\n\t" \
  "ds_read_b64_tr_b16 v[110:111], v163 offset:59392\n\t" \
  "v_mfma_f32_16x16x32_bf16 v[132:135], v[116:119], v[40:43], v[132:135]\n\t" \
  "ds_read_b64_tr_b16 v[112:113], v160 offset:61440\n\t" \
  "v_mfma_f32_16x16x32_bf16 v[148:151], v[116:119], v[44:47], v[148:151]\n\t" \
  "ds_read_b64_tr_b16 v[114:115], v160 offset:63488\n\t" \
  "s_waitcnt lgkmcnt(10)\n\t" \
  "v_mfma_f32_16x16x32_bf16 v[136:139], v[120:123], v[40:43], v[136:139]\n\t" \
  "ds_read_b64_tr_b16 v[116:117], v161 offset:61440\n\t" \
  "v_mfma_f32_16x16x32_bf16 v[152:155], v[120:123], v[44:47], v[152:155]\n\t" \
  "ds_read_b64_tr_b16 v[118:119], v161 offset:63488\n\t" \
  "v_mfma_f32_16x16x32_bf16 v[140:143], v[124:127], v[40:43], v[140:143]\n\t" \
  "ds_read_b64_tr_b16 v[120:121], v162 offset:61440\n\t" \
  "v_mfma_f32_16x16x32_bf16 v[156:159], v[124:127], v[44:47], v[156:159]\n\t" \
  "ds_read_b64_tr_b16 v[122:123], v162 offset:63488\n\t" \
  "s_cbranch_vccz QF15_%=\n\t" \
  "s_waitcnt vmcnt(0)\n\t" \
  "QG15_%=:\n\t" \
  "s_waitcnt lgkmcnt(10)\n\t" \
  "v_mfma_f32_16x16x32_bf16 v[128:131], v[96:99], v[48:51], v[128:131]\n\t" \
  "ds_read_b64_tr_b16 v[124:125], v163 offset:61440\n\t" \
  "v_mfma_f32_16x16x32_bf16 v[144:147], v[96:99], v[52:55], v[144:147]\n\t" \
  "ds_read_b64_tr_b16 v[126:127], v163 offset:63488\n\t" \
  "v_mfma_f32_16x16x32_bf16 v[132:135], v[100:103], v[48:51], v[132:135]\n\t" \
  "v_mfma_f32_16x16x32_bf16 v[148:151], v[100:103], v[52:55], v[148:151]\n\t" \
  "s_waitcnt lgkmcnt(8)\n\t" \
  "v_mfma_f32_16x16x32_bf16 v[136:139], v[104:107], v[48:51], v[136:139]\n\t" \
  "v_mfma_f32_16x16x32_bf16 v[152:155], v[104:107], v[52:55], v[152:155]\n\t" \
  "v_mfma_f32_16x16x32_bf16 v[140:143], v[108:111], v[48:51], v[140:143]\n\t" \
  "v_mfma_f32_16x16x32_bf16 v[156:159], v[108:111], v[52:55], v[156:159]\n\t" \
  "s_waitcnt lgkmcnt(4)\n\t" \
  "v_mfma_f32_16x16x32_bf16 v[128:131], v[112:115], v[56:59], v[128:131]\n\t" \
  "v_mfma_f32_16x16x32_bf16 v[144:147], v[112:115], v[60:63], v[144:147]\n\t" \
  "v_mfma_f32_16x16x32_bf16 v[132:135], v[116:119], v[56:59], v[132:135]\n\t" \
  "v_mfma_f32_16x16x32_bf16 v[148:151], v[116:119], v[60:63], v[148:151]\n\t" \
  "s_waitcnt lgkmcnt(0)\n\t" \
  "v_mfma_f32_16x16x32_bf16 v[136:139], v[120:123], v[56:59], v[136:139]\n\t" \
  "v_mfma_f32_16x16x32_bf16 v[152:155], v[120:123], v[60:63], v[152:155]\n\t" \
  "v_mfma_f32_16x16x32_bf16 v[140:143], v[124:127], v[56:59], v[140:143]\n\t" \
  "v_mfma_f32_16x16x32_bf16 v[156:159], v[124:127], v[60:63], v[156:159]\n\t" \
  "s_branch QX_%=\n\t" \
  "QF1_%=:\n\t" \
  "s_mov_b32 %[c], 1\n\t" \
  "s_getpc_b64 s[92:93]\n\t" \
  "s_branch QC_%=\n\t" \
  "s_branch QG1_%=\n\t" \
  "QF2_%=:\n\t" \
  "s_mov_b32 %[c], 2\n\t" \
  "s_getpc_b64 s[92:93]\n\t" \
  "s_branch QC_%=\n\t" \
  "s_branch QG2_%=\n\t" \
  "QF3_%=:\n\t" \
  "s_mov_b32 %[c], 3\n\t" \
  "s_getpc_b64 s[92:93]\n\t" \
  "s_branch QC_%=\n\t" \
  "s_branch QG3_%=\n\t" \
  "QF4_%=:\n\t" \
  "s_mov_b32 %[c], 4\n\t" \
  "s_getpc_b64 s[92:93]\n\t" \
  "s_branch QC_%=\n\t" \
  "s_branch QG4_%=\n\t" \
  "QF5_%=:\n\t" \
  "s_mov_b32 %[c], 5\n\t" \
  "s_getpc_b64 s[92:93]\n\t" \
  "s_branch QC_%=\n\t" \
  "s_branch QG5_%=\n\t" \
  "QF6_%=:\n\t" \
  "s_mov_b32 %[c], 6\n\t" \
  "s_getpc_b64 s[92:93]\n\t" \
  "s_branch QC_%=\n\t" \
  "s_branch QG6_%=\n\t" \
  "QF7_%=:\n\t" \
  "s_mov_b32 %[c], 7\n\t" \
  "s_getpc_b64 s[92:93]\n\t" \
  "s_branch QC_%=\n\t" \
  "s_branch QG7_%=\n\t" \
  "QF8_%=:\n\t" \
  "s_mov_b32 %[c], 8\n\t" \
  "s_getpc_b64 s[92:93]\n\t" \
  "s_branch QC_%=\n\t" \
  "s_branch QG8_%=\n\t" \
  "QF9_%=:\n\t" \
  "s_mov_b32 %[c], 9\n\t" \
  "s_getpc_b64 s[92:93]\n\t" \
  "s_branch QC_%=\n\t" \
  "s_branch QG9_%=\n\t" \
  "QF10_%=:\n\t" \
  "s_mov_b32 %[c], 10\n\t" \
  "s_getpc_b64 s[92:93]\n\t" \
  "s_branch QC_%=\n\t" \
  "s_branch QG10_%=\n\t" \
  "QF11_%=:\n\t" \
  "s_mov_b32 %[c], 11\n\t" \
  "s_getpc_b64 s[92:93]\n\t" \
  "s_branch QC_%=\n\t" \
  "s_branch QG11_%=\n\t" \
  "QF12_%=:\n\t" \
  "s_mov_b32 %[c], 12\n\t" \
  "s_getpc_b64 s[92:93]\n\t" \
  "s_branch QC_%=\n\t" \
  "s_branch QG12_%=\n\t" \
  "QF13_%=:\n\t" \
  "s_mov_b32 %[c], 13\n\t" \
  "s_getpc_b64 s[92:93]\n\t" \
  "s_branch QC_%=\n\t" \
  "s_branch QG13_%=\n\t" \
  "QF14_%=:\n\t" \
  "s_mov_b32 %[c], 14\n\t" \
  "s_getpc_b64 s[92:93]\n\t" \
  "s_branch QC_%=\n\t" \
  "s_branch QG14_%=\n\t" \
  "QF15_%=:\n\t" \
  "s_mov_b32 %[c], 15\n\t" \
  "s_getpc_b64 s[92:93]\n\t" \
  "s_branch QC_%=\n\t" \
  "s_branch QG15_%=\n\t" \
  "QC_%=:\n\t" \
  "s_add_u32 s92, s92, 4\n\t" \
  "s_addc_u32 s93, s93, 0\n\t" \
  "QU_%=:\n\t" \
  "ds_read_b32 v166, v165\n\t" \
  "s_add_u32 %[t1], %[c], 5\n\t" \
  "s_waitcnt lgkmcnt(0)\n\t" \
  "v_readfirstlane_b32 %[t0], v166\n\t" \
  "s_nop 0\n\t" \
  "s_min_u32 %[t1], %[t1], %[t0]\n\t" \
  "QL_%=:\n\t" \
  "s_cmp_ge_u32 %[iss], %[t1]\n\t" \
  "s_cbranch_scc1 QD_%=\n\t" \
  "s_mov_b32 %[t0], %[iss]\n\t" \
  "s_sub_u32 %[th], %[t0], 6\n\t" \
  "s_cselect_b32 %[t0], %[t0], %[th]\n\t" \
  "s_sub_u32 %[th], %[t0], 6\n\t" \
  "s_cselect_b32 %[t0], %[t0], %[th]\n\t" \
  "s_cmp_eq_u32 %[t0], 1\n\t" \
  "s_cbranch_scc1 QS1_%=\n\t" \
  "s_cmp_eq_u32 %[t0], 2\n\t" \
  "s_cbranch_scc1 QS2_%=\n\t" \
  "s_cmp_eq_u32 %[t0], 3\n\t" \
  "s_cbranch_scc1 QS3_%=\n\t" \
  "s_cmp_eq_u32 %[t0], 4\n\t" \
  "s_cbranch_scc1 QS4_%=\n\t" \
  "s_cmp_eq_u32 %[t0], 5\n\t" \
  "s_cbranch_scc1 QS5_%=\n\t" \
  "QS0_%=:\n\t" \
  "v_readlane_b32 s94, v167, %[iss]\n\t" \
  "s_nop 4\n\t" \
  "buffer_load_dwordx4 v[0:3], v164, %[ob], s94 offen offset:0 sc1\n\t" \
  "buffer_load_dwordx4 v[4:7], v164, %[ob], s94 offen offset:1024 sc1\n\t" \
  "buffer_load_dwordx4 v[8:11], v164, %[ob], s94 offen offset:2048 sc1\n\t" \
  "buffer_load_dwordx4 v[12:15], v164, %[ob], s94 offen offset:3072 sc1\n\t" \
  "s_add_u32 %[iss], %[iss], 1\n\t" \
  "s_cmp_ge_u32 %[iss], %[t1]\n\t" \
  "s_cbranch_scc1 QD_%=\n\t" \
  "QS1_%=:\n\t" \
  "v_readlane_b32 s94, v167, %[iss]\n\t" \
  "s_nop 4\n\t" \
  "buffer_load_dwordx4 v[16:19], v164, %[ob], s94 offen offset:0 sc1\n\t" \
  "buffer_load_dwordx4 v[20:23], v164, %[ob], s94 offen offset:1024 sc1\n\t" \
  "buffer_load_dwordx4 v[24:27], v164, %[ob], s94 offen offset:2048 sc1\n\t" \
  "buffer_load_dwordx4 v[28:31], v164, %[ob], s94 offen offset:3072 sc1\n\t" \
  "s_add_u32 %[iss], %[iss], 1\n\t" \
  "s_cmp_ge_u32 %[iss], %[t1]\n\t" \
  "s_cbranch_scc1 QD_%=\n\t" \
  "QS2_%=:\n\t" \
  "v_readlane_b32 s94, v167, %[iss]\n\t" \
  "s_nop 4\n\t" \
  "buffer_load_dwordx4 v[32:35], v164, %[ob], s94 offen offset:0 sc1\n\t" \
  "buffer_load_dwordx4 v[36:39], v164, %[ob], s94 offen offset:1024 sc1\n\t" \
  "buffer_load_dwordx4 v[40:43], v164, %[ob], s94 offen offset:2048 sc1\n\t" \
  "buffer_load_dwordx4 v[44:47], v164, %[ob], s94 offen offset:3072 sc1\n\t" \
  "s_add_u32 %[iss], %[iss], 1\n\t" \
  "s_cmp_ge_u32 %[iss], %[t1]\n\t" \
  "s_cbranch_scc1 QD_%=\n\t" \
  "QS3_%=:\n\t" \
  "v_readlane_b32 s94, v167, %[iss]\n\t" \
  "s_nop 4\n\t" \
  "buffer_load_dwordx4 v[48:51], v164, %[ob], s94 offen offset:0 sc1\n\t" \
  "buffer_load_dwordx4 v[52:55], v164, %[ob], s94 offen offset:1024 sc1\n\t" \
  "buffer_load_dwordx4 v[56:59], v164, %[ob], s94 offen offset:2048 sc1\n\t" \
  "buffer_load_dwordx4 v[60:63], v164, %[ob], s94 offen offset:3072 sc1\n\t" \
  "s_add_u32 %[iss], %[iss], 1\n\t" \
  "s_cmp_ge_u32 %[iss], %[t1]\n\t" \
  "s_cbranch_scc1 QD_%=\n\t" \
  "QS4_%=:\n\t" \
  "v_readlane_b32 s94, v167, %[iss]\n\t" \
  "s_nop 4\n\t" \
  "buffer_load_dwordx4 v[64:67], v164, %[ob], s94 offen offset:0 sc1\n\t" \
  "buffer_load_dwordx4 v[68:71], v164, %[ob], s94 offen offset:1024 sc1\n\t" \
  "buffer_load_dwordx4 v[72:75], v164, %[ob], s94 offen offset:2048 sc1\n\t" \
  "buffer_load_dwordx4 v[76:79], v164, %[ob], s94 offen offset:3072 sc1\n\t" \
  "s_add_u32 %[iss], %[iss], 1\n\t" \
  "s_cmp_ge_u32 %[iss], %[t1]\n\t" \
  "s_cbranch_scc1 QD_%=\n\t" \
  "QS5_%=:\n\t" \
  "v_readlane_b32 s94, v167, %[iss]\n\t" \
  "s_nop 4\n\t" \
  "buffer_load_dwordx4 v[80:83], v164, %[ob], s94 offen offset:0 sc1\n\t" \
  "buffer_load_dwordx4 v[84:87], v164, %[ob], s94 offen offset:1024 sc1\n\t" \
  "buffer_load_dwordx4 v[88:91], v164, %[ob], s94 offen offset:2048 sc1\n\t" \
  "buffer_load_dwordx4 v[92:95], v164, %[ob], s94 offen offset:3072 sc1\n\t" \
  "s_add_u32 %[iss], %[iss], 1\n\t" \
  "s_cmp_ge_u32 %[iss], %[t1]\n\t" \
  "s_cbranch_scc0 QS0_%=\n\t" \
  "QD_%=:\n\t" \
  "s_cmp_gt_u32 %[iss], %[c]\n\t" \
  "s_cbranch_scc1 QW_%=\n\t" \
  "s_addk_i32 %[cn], 1\n\t" \
  "s_cmp_gt_u32 %[cn], 0x400000\n\t" \
  "s_cbranch_scc1 QT_%=\n\t" \
  "s_sleep 1\n\t" \
  "s_branch QU_%=\n\t" \
  "QT_%=:\n\t" \
  "s_add_u32 %[t1], %[c], 1\n\t" \
  "s_branch QL_%=\n\t" \
  "QW_%=:\n\t" \
  "s_cmp_eq_u32 %[iss], 16\n\t" \
  "s_cselect_b64 vcc, -1, 0\n\t" \
  "s_sub_u32 %[t0], %[iss], %[c]\n\t" \
  "s_cmp_eq_u32 %[t0], 5\n\t" \
  "s_cselect_b32 %[th], %[iss], 99\n\t" \
  "s_waitcnt vmcnt(16)\n\t" \
  "s_cmp_ge_u32 %[t0], 5\n\t" \
  "s_cbranch_scc1 QR_%=\n\t" \
  "s_waitcnt vmcnt(12)\n\t" \
  "s_cmp_ge_u32 %[t0], 4\n\t" \
  "s_cbranch_scc1 QR_%=\n\t" \
  "s_waitcnt vmcnt(8)\n\t" \
  "s_cmp_ge_u32 %[t0], 3\n\t" \
  "s_cbranch_scc1 QR_%=\n\t" \
  "s_waitcnt vmcnt(4)\n\t" \
  "s_cmp_ge_u32 %[t0], 2\n\t" \
  "s_cbranch_scc1 QR_%=\n\t" \
  "s_waitcnt vmcnt(0)\n\t" \
  "QR_%=:\n\t" \
  "s_setpc_b64 s[92:93]\n\t" \
  "QX_%=:\n\t" \
  "s_nop 7\n\t"
#define G3_CLOB "v0", "v1", "v2", "v3", "v4", "v5", "v6", "v7", "v8", "v9", "v10", "v11", "v12", "v13", "v14", "v15", "v16", "v17", "v18", "v19", "v20", "v21", "v22", "v23", "v24", "v25", "v26", "v27", "v28", "v29", "v30", "v31", "v32", "v33", "v34", "v35", "v36", "v37", "v38", "v39", "v40", "v41", "v42", "v43", "v44", "v45", "v46", "v47", "v48", "v49", "v50", "v51", "v52", "v53", "v54", "v55", "v56", "v57", "v58", "v59", "v60", "v61", "v62", "v63", "v64", "v65", "v66", "v67", "v68", "v69", "v70", "v71", "v72", "v73", "v74", "v75", "v76", "v77", "v78", "v79", "v80", "v81", "v82", "v83", "v84", "v85", "v86", "v87", "v88", "v89", "v90", "v91", "v92", "v93", "v94", "v95", "v96", "v97", "v98", "v99", "v100", "v101", "v102", "v103", "v104", "v105", "v106", "v107", "v108", "v109", "v110", "v111", "v112", "v113", "v114", "v115", "v116", "v117", "v118", "v119", "v120", "v121", "v122", "v123", "v124", "v125", "v126", "v127", "v166", "s88", "s89", "s92", "s93", "s94"

#define PP_ASM \
  "v_mov_b32 v60, 0\n\t" \
  "s_mov_b32 %[t0], %[n0]\n\t" \
  "s_cmp_eq_u32 %[t0], 0\n\t" \
  "s_cbranch_scc1 PAx_%=\n\t" \
  "PA_%=:\n\t" \
  "s_barrier\n\t" \
  "s_sub_u32 %[t0], %[t0], 1\n\t" \
  "s_cmp_lg_u32 %[t0], 0\n\t" \
  "s_cbranch_scc1 PA_%=\n\t" \
  "PAx_%=:\n\t" \
  "s_mov_b32 m0, %[shd]\n\t" \
  "global_load_dword v160, v60, %[fl] sc1\n\t" \
  "global_load_lds_dwordx4 v163, %[shp]\n\t" \
  "s_mov_b32 m0, %[tgd]\n\t" \
  "s_mov_b64 exec, %[em]\n\t" \
  "global_load_lds_dword v60, %[fl] sc1\n\t" \
  "s_mov_b64 exec, -1\n\t" \
  "global_load_ushort v161, v60, %[hp]\n\t" \
  "global_load_ushort v162, v60, %[dp]\n\t" \
  "global_load_dwordx4 v[0:3], v152, %[kb0]\n\t" \
  "global_load_dwordx4 v[4:7], v153, %[kb0]\n\t" \
  "global_load_dwordx4 v[8:11], v154, %[kb0]\n\t" \
  "global_load_dwordx4 v[12:15], v155, %[kb3] offset:0\n\t" \
  "global_load_dwordx4 v[28:31], v155, %[vb0] offset:0\n\t" \
  "global_load_dwordx4 v[16:19], v155, %[kb3] offset:2048\n\t" \
  "global_load_dwordx4 v[32:35], v155, %[vb0] offset:2048\n\t" \
  "global_load_dwordx4 v[20:23], v155, %[kb5] offset:0\n\t" \
  "global_load_dwordx4 v[36:39], v155, %[vb2] offset:0\n\t" \
  "global_load_dwordx4 v[24:27], v155, %[kb5] offset:2048\n\t" \
  "global_load_dwordx4 v[40:43], v155, %[vb2] offset:2048\n\t" \
  "s_setprio 2\n\t" \
  "s_barrier\n\t" \
  "s_barrier\n\t" \
  "s_waitcnt vmcnt(11)\n\t" \
  "ds_read_b128 v[128:131], v156 offset:0\n\t" \
  "ds_read_b128 v[132:135], v156 offset:16\n\t" \
  "ds_read_b128 v[136:139], v156 offset:32\n\t" \
  "ds_read_b128 v[140:143], v156 offset:48\n\t" \
  "s_waitcnt lgkmcnt(0)\n\t" \
  "v_lshlrev_b32 v64, 16, v128\n\t" \
  "v_and_b32 v65, 0xffff0000, v128\n\t" \
  "v_lshlrev_b32 v66, 16, v129\n\t" \
  "v_and_b32 v67, 0xffff0000, v129\n\t" \
  "v_lshlrev_b32 v68, 16, v130\n\t" \
  "v_and_b32 v69, 0xffff0000, v130\n\t" \
  "v_lshlrev_b32 v70, 16, v131\n\t" \
  "v_and_b32 v71, 0xffff0000, v131\n\t" \
  "v_lshlrev_b32 v72, 16, v132\n\t" \
  "v_and_b32 v73, 0xffff0000, v132\n\t" \
  "v_lshlrev_b32 v74, 16, v133\n\t" \
  "v_and_b32 v75, 0xffff0000, v133\n\t" \
  "v_lshlrev_b32 v76, 16, v134\n\t" \
  "v_and_b32 v77, 0xffff0000, v134\n\t" \
  "v_lshlrev_b32 v78, 16, v135\n\t" \
  "v_and_b32 v79, 0xffff0000, v135\n\t" \
  "v_lshlrev_b32 v80, 16, v136\n\t" \
  "v_and_b32 v81, 0xffff0000, v136\n\t" \
  "v_lshlrev_b32 v82, 16, v137\n\t" \
  "v_and_b32 v83, 0xffff0000, v137\n\t" \
  "v_lshlrev_b32 v84, 16, v138\n\t" \
  "v_and_b32 v85, 0xffff0000, v138\n\t" \
  "v_lshlrev_b32 v86, 16, v139\n\t" \
  "v_and_b32 v87, 0xffff0000, v139\n\t" \
  "v_lshlrev_b32 v88, 16, v140\n\t" \
  "v_and_b32 v89, 0xffff0000, v140\n\t" \
  "v_lshlrev_b32 v90, 16, v141\n\t" \
  "v_and_b32 v91, 0xffff0000, v141\n\t" \
  "v_lshlrev_b32 v92, 16, v142\n\t" \
  "v_and_b32 v93, 0xffff0000, v142\n\t" \
  "v_lshlrev_b32 v94, 16, v143\n\t" \
  "v_and_b32 v95, 0xffff0000, v143\n\t" \
  "s_waitcnt vmcnt(10)\n\t" \
  "s_mov_b64 exec, %[zm]\n\t" \
  "v_mov_b32 v0, 0\n\t" \
  "v_mov_b32 v1, 0\n\t" \
  "v_mov_b32 v2, 0\n\t" \
  "v_mov_b32 v3, 0\n\t" \
  "s_mov_b64 exec, -1\n\t" \
  "v_lshlrev_b32 v128, 16, v0\n\t" \
  "v_and_b32 v129, 0xffff0000, v0\n\t" \
  "s_barrier\n\t" \
  "v_lshlrev_b32 v130, 16, v1\n\t" \
  "v_and_b32 v131, 0xffff0000, v1\n\t" \
  "v_lshlrev_b32 v132, 16, v2\n\t" \
  "v_and_b32 v133, 0xffff0000, v2\n\t" \
  "v_lshlrev_b32 v134, 16, v3\n\t" \
  "v_and_b32 v135, 0xffff0000, v3\n\t" \
  "v_mul_f32 v96, v128, v67\n\t" \
  "v_mul_f32 v97, v129, v71\n\t" \
  "v_mul_f32 v98, v130, v75\n\t" \
  "v_mul_f32 v99, v131, v79\n\t" \
  "v_mul_f32 v100, v132, v83\n\t" \
  "v_mul_f32 v101, v133, v87\n\t" \
  "v_mul_f32 v102, v134, v91\n\t" \
  "v_mul_f32 v103, v135, v95\n\t" \
  "s_waitcnt vmcnt(9)\n\t" \
  "s_mov_b64 exec, %[zm]\n\t" \
  "v_mov_b32 v4, 0\n\t" \
  "v_mov_b32 v5, 0\n\t" \
  "v_mov_b32 v6, 0\n\t" \
  "v_mov_b32 v7, 0\n\t" \
  "s_mov_b64 exec, -1\n\t" \
  "v_lshlrev_b32 v128, 16, v4\n\t" \
  "v_and_b32 v129, 0xffff0000, v4\n\t" \
  "v_lshlrev_b32 v130, 16, v5\n\t" \
  "v_and_b32 v131, 0xffff0000, v5\n\t" \
  "v_lshlrev_b32 v132, 16, v6\n\t" \
  "v_and_b32 v133, 0xffff0000, v6\n\t" \
  "v_lshlrev_b32 v134, 16, v7\n\t" \
  "v_and_b32 v135, 0xffff0000, v7\n\t" \
  "v_fmac_f32 v96, v128, v66\n\t" \
  "v_fmac_f32 v97, v129, v70\n\t" \
  "v_fmac_f32 v98, v130, v74\n\t" \
  "v_fmac_f32 v99, v131, v78\n\t" \
  "v_fmac_f32 v100, v132, v82\n\t" \
  "v_fmac_f32 v101, v133, v86\n\t" \
  "v_fmac_f32 v102, v134, v90\n\t" \
  "v_fmac_f32 v103, v135, v94\n\t" \
  "v_mul_f32 v104, v128, v67\n\t" \
  "v_mul_f32 v105, v129, v71\n\t" \
  "v_mul_f32 v106, v130, v75\n\t" \
  "v_mul_f32 v107, v131, v79\n\t" \
  "v_mul_f32 v108, v132, v83\n\t" \
  "v_mul_f32 v109, v133, v87\n\t" \
  "v_mul_f32 v110, v134, v91\n\t" \
  "v_mul_f32 v111, v135, v95\n\t" \
  "s_waitcnt vmcnt(8)\n\t" \
  "s_mov_b64 exec, %[zm]\n\t" \
  "v_mov_b32 v8, 0\n\t" \
  "v_mov_b32 v9, 0\n\t" \
  "v_mov_b32 v10, 0\n\t" \
  "v_mov_b32 v11, 0\n\t" \
  "s_mov_b64 exec, -1\n\t" \
  "s_setprio 3\n\t" \
  "s_barrier\n\t" \
  "s_mov_b32 m0, %[hd]\n\t" \
  "s_nop 0\n\t" \
  "global_load_lds_dwordx4 v163, %[hp] offset:0\n\t" \
  "global_load_lds_dwordx4 v163, %[hp] offset:1024\n\t" \
  "global_load_lds_dwordx4 v163, %[hp] offset:2048\n\t" \
  "global_load_lds_dwordx4 v163, %[hp] offset:3072\n\t" \
  "global_load_dwordx4 v[44:47], v155, %[qb0] offset:0\n\t" \
  "global_load_dwordx4 v[48:51], v155, %[qb0] offset:2048\n\t" \
  "global_load_dwordx4 v[52:55], v155, %[qb2] offset:0\n\t" \
  "global_load_dwordx4 v[56:59], v155, %[qb2] offset:2048\n\t" \
  "s_setprio 2\n\t" \
  "v_lshlrev_b32 v128, 16, v8\n\t" \
  "v_and_b32 v129, 0xffff0000, v8\n\t" \
  "v_lshlrev_b32 v130, 16, v9\n\t" \
  "v_and_b32 v131, 0xffff0000, v9\n\t" \
  "v_lshlrev_b32 v132, 16, v10\n\t" \
  "v_and_b32 v133, 0xffff0000, v10\n\t" \
  "v_lshlrev_b32 v134, 16, v11\n\t" \
  "v_and_b32 v135, 0xffff0000, v11\n\t" \
  "v_fmac_f32 v96, v128, v65\n\t" \
  "v_fmac_f32 v97, v129, v69\n\t" \
  "v_fmac_f32 v98, v130, v73\n\t" \
  "v_fmac_f32 v99, v131, v77\n\t" \
  "v_fmac_f32 v100, v132, v81\n\t" \
  "v_fmac_f32 v101, v133, v85\n\t" \
  "v_fmac_f32 v102, v134, v89\n\t" \
  "v_fmac_f32 v103, v135, v93\n\t" \
  "v_fmac_f32 v104, v128, v66\n\t" \
  "v_fmac_f32 v105, v129, v70\n\t" \
  "v_fmac_f32 v106, v130, v74\n\t" \
  "v_fmac_f32 v107, v131, v78\n\t" \
  "v_fmac_f32 v108, v132, v82\n\t" \
  "v_fmac_f32 v109, v133, v86\n\t" \
  "v_fmac_f32 v110, v134, v90\n\t" \
  "v_fmac_f32 v111, v135, v94\n\t" \
  "v_mul_f32 v112, v128, v67\n\t" \
  "v_mul_f32 v113, v129, v71\n\t" \
  "v_mul_f32 v114, v130, v75\n\t" \
  "v_mul_f32 v115, v131, v79\n\t" \
  "v_mul_f32 v116, v132, v83\n\t" \
  "v_mul_f32 v117, v133, v87\n\t" \
  "v_mul_f32 v118, v134, v91\n\t" \
  "v_mul_f32 v119, v135, v95\n\t" \
  "s_waitcnt vmcnt(15)\n\t" \
  "v_lshlrev_b32 v128, 16, v12\n\t" \
  "v_and_b32 v129, 0xffff0000, v12\n\t" \
  "v_lshlrev_b32 v130, 16, v13\n\t" \
  "v_and_b32 v131, 0xffff0000, v13\n\t" \
  "v_lshlrev_b32 v132, 16, v14\n\t" \
  "v_and_b32 v133, 0xffff0000, v14\n\t" \
  "v_lshlrev_b32 v134, 16, v15\n\t" \
  "v_and_b32 v135, 0xffff0000, v15\n\t" \
  "s_barrier\n\t" \
  "v_fmac_f32 v96, v128, v64\n\t" \
  "v_fmac_f32 v97, v129, v68\n\t" \
  "v_fmac_f32 v98, v130, v72\n\t" \
  "v_fmac_f32 v99, v131, v76\n\t" \
  "v_fmac_f32 v100, v132, v80\n\t" \
  "v_fmac_f32 v101, v133, v84\n\t" \
  "v_fmac_f32 v102, v134, v88\n\t" \
  "v_fmac_f32 v103, v135, v92\n\t" \
  "v_fmac_f32 v104, v128, v65\n\t" \
  "v_fmac_f32 v105, v129, v69\n\t" \
  "v_fmac_f32 v106, v130, v73\n\t" \
  "v_fmac_f32 v107, v131, v77\n\t" \
  "v_fmac_f32 v108, v132, v81\n\t" \
  "v_fmac_f32 v109, v133, v85\n\t" \
  "v_fmac_f32 v110, v134, v89\n\t" \
  "v_fmac_f32 v111, v135, v93\n\t" \
  "v_fmac_f32 v112, v128, v66\n\t" \
  "v_fmac_f32 v113, v129, v70\n\t" \
  "v_fmac_f32 v114, v130, v74\n\t" \
  "v_fmac_f32 v115, v131, v78\n\t" \
  "v_fmac_f32 v116, v132, v82\n\t" \
  "v_fmac_f32 v117, v133, v86\n\t" \
  "v_fmac_f32 v118, v134, v90\n\t" \
  "v_fmac_f32 v119, v135, v94\n\t" \
  "v_mul_f32 v120, v128, v67\n\t" \
  "v_mul_f32 v121, v129, v71\n\t" \
  "v_mul_f32 v122, v130, v75\n\t" \
  "v_mul_f32 v123, v131, v79\n\t" \
  "v_mul_f32 v124, v132, v83\n\t" \
  "v_mul_f32 v125, v133, v87\n\t" \
  "v_mul_f32 v126, v134, v91\n\t" \
  "v_mul_f32 v127, v135, v95\n\t" \
  "s_waitcnt vmcnt(14)\n\t" \
  "v_lshlrev_b32 v128, 16, v28\n\t" \
  "v_and_b32 v129, 0xffff0000, v28\n\t" \
  "v_lshlrev_b32 v130, 16, v29\n\t" \
  "v_and_b32 v131, 0xffff0000, v29\n\t" \
  "v_lshlrev_b32 v132, 16, v30\n\t" \
  "v_and_b32 v133, 0xffff0000, v30\n\t" \
  "v_lshlrev_b32 v134, 16, v31\n\t" \
  "v_and_b32 v135, 0xffff0000, v31\n\t" \
  "s_barrier\n\t" \
  "v_mul_f32 v96, v96, v128\n\t" \
  "v_mul_f32 v97, v97, v129\n\t" \
  "v_mul_f32 v98, v98, v130\n\t" \
  "v_mul_f32 v99, v99, v131\n\t" \
  "v_mul_f32 v100, v100, v132\n\t" \
  "v_mul_f32 v101, v101, v133\n\t" \
  "v_mul_f32 v102, v102, v134\n\t" \
  "v_mul_f32 v103, v103, v135\n\t" \
  "v_cvt_pk_bf16_f32 v136, v96, v97\n\t" \
  "v_cvt_pk_bf16_f32 v137, v98, v99\n\t" \
  "v_cvt_pk_bf16_f32 v138, v100, v101\n\t" \
  "v_cvt_pk_bf16_f32 v139, v102, v103\n\t" \
  "v_mov_b32_dpp v128, v136 row_ror:8 row_mask:0xf bank_mask:0xf\n\t" \
  "v_mov_b32_dpp v129, v137 row_ror:8 row_mask:0xf bank_mask:0xf\n\t" \
  "v_mov_b32_dpp v130, v138 row_ror:8 row_mask:0xf bank_mask:0xf\n\t" \
  "v_mov_b32_dpp v131, v139 row_ror:8 row_mask:0xf bank_mask:0xf\n\t" \
  "v_perm_b32 v136, v128, v136, v157\n\t" \
  "v_perm_b32 v137, v129, v137, v157\n\t" \
  "v_perm_b32 v138, v130, v138, v157\n\t" \
  "v_perm_b32 v139, v131, v139, v157\n\t" \
  "v_permlane16_swap_b32 v136, v137\n\t" \
  "s_nop 0\n\t" \
  "v_permlane16_swap_b32 v138, v139\n\t" \
  "s_nop 1\n\t" \
  "v_permlane32_swap_b32 v136, v138\n\t" \
  "v_permlane32_swap_b32 v137, v139\n\t" \
  "s_waitcnt vmcnt(13)\n\t" \
  "v_lshlrev_b32 v128, 16, v16\n\t" \
  "v_and_b32 v129, 0xffff0000, v16\n\t" \
  "v_lshlrev_b32 v130, 16, v17\n\t" \
  "v_and_b32 v131, 0xffff0000, v17\n\t" \
  "v_lshlrev_b32 v132, 16, v18\n\t" \
  "v_and_b32 v133, 0xffff0000, v18\n\t" \
  "v_lshlrev_b32 v134, 16, v19\n\t" \
  "v_and_b32 v135, 0xffff0000, v19\n\t" \
  "v_fmac_f32 v104, v128, v64\n\t" \
  "v_fmac_f32 v105, v129, v68\n\t" \
  "v_fmac_f32 v106, v130, v72\n\t" \
  "v_fmac_f32 v107, v131, v76\n\t" \
  "v_fmac_f32 v108, v132, v80\n\t" \
  "v_fmac_f32 v109, v133, v84\n\t" \
  "s_setprio 3\n\t" \
  "s_barrier\n\t" \
  "global_store_dwordx4 v159, v[136:139], %[sb] sc1\n\t" \
  "v_add_u32 v158, 0x80, v159\n\t" \
  "v_bfi_b32 v159, %[mk], v158, v159\n\t" \
  "s_setprio 2\n\t" \
  "v_fmac_f32 v110, v134, v88\n\t" \
  "v_fmac_f32 v111, v135, v92\n\t" \
  "v_fmac_f32 v112, v128, v65\n\t" \
  "v_fmac_f32 v113, v129, v69\n\t" \
  "v_fmac_f32 v114, v130, v73\n\t" \
  "v_fmac_f32 v115, v131, v77\n\t" \
  "v_fmac_f32 v116, v132, v81\n\t" \
  "v_fmac_f32 v117, v133, v85\n\t" \
  "v_fmac_f32 v118, v134, v89\n\t" \
  "v_fmac_f32 v119, v135, v93\n\t" \
  "v_fmac_f32 v120, v128, v66\n\t" \
  "v_fmac_f32 v121, v129, v70\n\t" \
  "v_fmac_f32 v122, v130, v74\n\t" \
  "v_fmac_f32 v123, v131, v78\n\t" \
  "v_fmac_f32 v124, v132, v82\n\t" \
  "v_fmac_f32 v125, v133, v86\n\t" \
  "v_fmac_f32 v126, v134, v90\n\t" \
  "v_fmac_f32 v127, v135, v94\n\t" \
  "s_waitcnt vmcnt(13)\n\t" \
  "v_lshlrev_b32 v128, 16, v32\n\t" \
  "v_and_b32 v129, 0xffff0000, v32\n\t" \
  "v_lshlrev_b32 v130, 16, v33\n\t" \
  "v_and_b32 v131, 0xffff0000, v33\n\t" \
  "v_lshlrev_b32 v132, 16, v34\n\t" \
  "v_and_b32 v133, 0xffff0000, v34\n\t" \
  "v_lshlrev_b32 v134, 16, v35\n\t" \
  "v_and_b32 v135, 0xffff0000, v35\n\t" \
  "v_mul_f32 v104, v104, v128\n\t" \
  "v_mul_f32 v105, v105, v129\n\t" \
  "v_mul_f32 v106, v106, v130\n\t" \
  "v_mul_f32 v107, v107, v131\n\t" \
  "v_mul_f32 v108, v108, v132\n\t" \
  "v_mul_f32 v109, v109, v133\n\t" \
  "v_mul_f32 v110, v110, v134\n\t" \
  "v_mul_f32 v111, v111, v135\n\t" \
  "v_cvt_pk_bf16_f32 v140, v104, v105\n\t" \
  "v_cvt_pk_bf16_f32 v141, v106, v107\n\t" \
  "v_cvt_pk_bf16_f32 v142, v108, v109\n\t" \
  "v_cvt_pk_bf16_f32 v143, v110, v111\n\t" \
  "v_mov_b32_dpp v128, v140 row_ror:8 row_mask:0xf bank_mask:0xf\n\t" \
  "v_mov_b32_dpp v129, v141 row_ror:8 row_mask:0xf bank_mask:0xf\n\t" \
  "s_barrier\n\t" \
  "v_mov_b32_dpp v130, v142 row_ror:8 row_mask:0xf bank_mask:0xf\n\t" \
  "v_mov_b32_dpp v131, v143 row_ror:8 row_mask:0xf bank_mask:0xf\n\t" \
  "v_perm_b32 v140, v128, v140, v157\n\t" \
  "v_perm_b32 v141, v129, v141, v157\n\t" \
  "v_perm_b32 v142, v130, v142, v157\n\t" \
  "v_perm_b32 v143, v131, v143, v157\n\t" \
  "v_permlane16_swap_b32 v140, v141\n\t" \
  "s_nop 0\n\t" \
  "v_permlane16_swap_b32 v142, v143\n\t" \
  "s_nop 1\n\t" \
  "v_permlane32_swap_b32 v140, v142\n\t" \
  "v_permlane32_swap_b32 v141, v143\n\t" \
  "s_waitcnt vmcnt(12)\n\t" \
  "v_lshlrev_b32 v128, 16, v20\n\t" \
  "v_and_b32 v129, 0xffff0000, v20\n\t" \
  "v_lshlrev_b32 v130, 16, v21\n\t" \
  "v_and_b32 v131, 0xffff0000, v21\n\t" \
  "v_lshlrev_b32 v132, 16, v22\n\t" \
  "v_and_b32 v133, 0xffff0000, v22\n\t" \
  "v_lshlrev_b32 v134, 16, v23\n\t" \
  "v_and_b32 v135, 0xffff0000, v23\n\t" \
  "v_fmac_f32 v112, v128, v64\n\t" \
  "v_fmac_f32 v113, v129, v68\n\t" \
  "v_fmac_f32 v114, v130, v72\n\t" \
  "v_fmac_f32 v115, v131, v76\n\t" \
  "v_fmac_f32 v116, v132, v80\n\t" \
  "v_fmac_f32 v117, v133, v84\n\t" \
  "v_fmac_f32 v118, v134, v88\n\t" \
  "v_fmac_f32 v119, v135, v92\n\t" \
  "v_fmac_f32 v120, v128, v65\n\t" \
  "v_fmac_f32 v121, v129, v69\n\t" \
  "v_fmac_f32 v122, v130, v73\n\t" \
  "v_fmac_f32 v123, v131, v77\n\t" \
  "v_fmac_f32 v124, v132, v81\n\t" \
  "v_fmac_f32 v125, v133, v85\n\t" \
  "v_fmac_f32 v126, v134, v89\n\t" \
  "v_fmac_f32 v127, v135, v93\n\t" \
  "s_waitcnt vmcnt(11)\n\t" \
  "v_lshlrev_b32 v128, 16, v36\n\t" \
  "v_and_b32 v129, 0xffff0000, v36\n\t" \
  "v_lshlrev_b32 v130, 16, v37\n\t" \
  "v_and_b32 v131, 0xffff0000, v37\n\t" \
  "s_setprio 3\n\t" \
  "s_barrier\n\t" \
  "global_store_dwordx4 v159, v[140:143], %[sb] sc1\n\t" \
  "v_add_u32 v158, 0x80, v159\n\t" \
  "v_bfi_b32 v159, %[mk], v158, v159\n\t" \
  "s_setprio 2\n\t" \
  "v_lshlrev_b32 v132, 16, v38\n\t" \
  "v_and_b32 v133, 0xffff0000, v38\n\t" \
  "v_lshlrev_b32 v134, 16, v39\n\t" \
  "v_and_b32 v135, 0xffff0000, v39\n\t" \
  "v_mul_f32 v112, v112, v128\n\t" \
  "v_mul_f32 v113, v113, v129\n\t" \
  "v_mul_f32 v114, v114, v130\n\t" \
  "v_mul_f32 v115, v115, v131\n\t" \
  "v_mul_f32 v116, v116, v132\n\t" \
  "v_mul_f32 v117, v117, v133\n\t" \
  "v_mul_f32 v118, v118, v134\n\t" \
  "v_mul_f32 v119, v119, v135\n\t" \
  "v_cvt_pk_bf16_f32 v144, v112, v113\n\t" \
  "v_cvt_pk_bf16_f32 v145, v114, v115\n\t" \
  "v_cvt_pk_bf16_f32 v146, v116, v117\n\t" \
  "v_cvt_pk_bf16_f32 v147, v118, v119\n\t" \
  "v_mov_b32_dpp v128, v144 row_ror:8 row_mask:0xf bank_mask:0xf\n\t" \
  "v_mov_b32_dpp v129, v145 row_ror:8 row_mask:0xf bank_mask:0xf\n\t" \
  "v_mov_b32_dpp v130, v146 row_ror:8 row_mask:0xf bank_mask:0xf\n\t" \
  "v_mov_b32_dpp v131, v147 row_ror:8 row_mask:0xf bank_mask:0xf\n\t" \
  "v_perm_b32 v144, v128, v144, v157\n\t" \
  "v_perm_b32 v145, v129, v145, v157\n\t" \
  "v_perm_b32 v146, v130, v146, v157\n\t" \
  "v_perm_b32 v147, v131, v147, v157\n\t" \
  "v_permlane16_swap_b32 v144, v145\n\t" \
  "s_nop 0\n\t" \
  "v_permlane16_swap_b32 v146, v147\n\t" \
  "s_nop 1\n\t" \
  "v_permlane32_swap_b32 v144, v146\n\t" \
  "v_permlane32_swap_b32 v145, v147\n\t" \
  "s_waitcnt vmcnt(11)\n\t" \
  "v_lshlrev_b32 v128, 16, v24\n\t" \
  "v_and_b32 v129, 0xffff0000, v24\n\t" \
  "v_lshlrev_b32 v130, 16, v25\n\t" \
  "v_and_b32 v131, 0xffff0000, v25\n\t" \
  "v_lshlrev_b32 v132, 16, v26\n\t" \
  "v_and_b32 v133, 0xffff0000, v26\n\t" \
  "v_lshlrev_b32 v134, 16, v27\n\t" \
  "v_and_b32 v135, 0xffff0000, v27\n\t" \
  "v_fmac_f32 v120, v128, v64\n\t" \
  "v_fmac_f32 v121, v129, v68\n\t" \
  "s_setprio 3\n\t" \
  "s_barrier\n\t" \
  "global_store_dwordx4 v159, v[144:147], %[sb] sc1\n\t" \
  "v_add_u32 v158, 0x80, v159\n\t" \
  "v_bfi_b32 v159, %[mk], v158, v159\n\t" \
  "s_setprio 2\n\t" \
  "v_fmac_f32 v122, v130, v72\n\t" \
  "v_fmac_f32 v123, v131, v76\n\t" \
  "v_fmac_f32 v124, v132, v80\n\t" \
  "v_fmac_f32 v125, v133, v84\n\t" \
  "v_fmac_f32 v126, v134, v88\n\t" \
  "v_fmac_f32 v127, v135, v92\n\t" \
  "s_waitcnt vmcnt(11)\n\t" \
  "v_lshlrev_b32 v128, 16, v40\n\t" \
  "v_and_b32 v129, 0xffff0000, v40\n\t" \
  "v_lshlrev_b32 v130, 16, v41\n\t" \
  "v_and_b32 v131, 0xffff0000, v41\n\t" \
  "v_lshlrev_b32 v132, 16, v42\n\t" \
  "v_and_b32 v133, 0xffff0000, v42\n\t" \
  "v_lshlrev_b32 v134, 16, v43\n\t" \
  "v_and_b32 v135, 0xffff0000, v43\n\t" \
  "v_mul_f32 v120, v120, v128\n\t" \
  "v_mul_f32 v121, v121, v129\n\t" \
  "v_mul_f32 v122, v122, v130\n\t" \
  "v_mul_f32 v123, v123, v131\n\t" \
  "v_mul_f32 v124, v124, v132\n\t" \
  "v_mul_f32 v125, v125, v133\n\t" \
  "v_mul_f32 v126, v126, v134\n\t" \
  "v_mul_f32 v127, v127, v135\n\t" \
  "v_cvt_pk_bf16_f32 v148, v120, v121\n\t" \
  "v_cvt_pk_bf16_f32 v149, v122, v123\n\t" \
  "v_cvt_pk_bf16_f32 v150, v124, v125\n\t" \
  "v_cvt_pk_bf16_f32 v151, v126, v127\n\t" \
  "v_mov_b32_dpp v128, v148 row_ror:8 row_mask:0xf bank_mask:0xf\n\t" \
  "v_mov_b32_dpp v129, v149 row_ror:8 row_mask:0xf bank_mask:0xf\n\t" \
  "v_mov_b32_dpp v130, v150 row_ror:8 row_mask:0xf bank_mask:0xf\n\t" \
  "v_mov_b32_dpp v131, v151 row_ror:8 row_mask:0xf bank_mask:0xf\n\t" \
  "v_perm_b32 v148, v128, v148, v157\n\t" \
  "v_perm_b32 v149, v129, v149, v157\n\t" \
  "v_perm_b32 v150, v130, v150, v157\n\t" \
  "v_perm_b32 v151, v131, v151, v157\n\t" \
  "v_permlane16_swap_b32 v148, v149\n\t" \
  "s_nop 0\n\t" \
  "v_permlane16_swap_b32 v150, v151\n\t" \
  "s_nop 1\n\t" \
  "v_permlane32_swap_b32 v148, v150\n\t" \
  "v_permlane32_swap_b32 v149, v151\n\t" \
  "s_setprio 3\n\t" \
  "s_barrier\n\t" \
  "global_store_dwordx4 v159, v[148:151], %[sb] sc1\n\t" \
  "s_setprio 2\n\t" \
  "s_waitcnt vmcnt(8)\n\t" \
  "ds_read_b64_tr_b16 v[64:65], v165 offset:32\n\t" \
  "ds_read_b64_tr_b16 v[66:67], v165 offset:0\n\t" \
  "ds_read_b64_tr_b16 v[68:69], v165 offset:288\n\t" \
  "ds_read_b64_tr_b16 v[70:71], v165 offset:256\n\t" \
  "ds_read_b64_tr_b16 v[72:73], v165 offset:544\n\t" \
  "ds_read_b64_tr_b16 v[74:75], v165 offset:512\n\t" \
  "ds_read_b64_tr_b16 v[76:77], v165 offset:800\n\t" \
  "ds_read_b64_tr_b16 v[78:79], v165 offset:768\n\t" \
  "v_mov_b32 v24, v166\n\t" \
  "v_mov_b32 v25, v167\n\t" \
  "v_mov_b32 v63, 0xc0\n\t" \
  "v_add_u32 v158, 64, v24\n\t" \
  "v_bfi_b32 v26, v63, v158, v24\n\t" \
  "v_add_u32 v158, 64, v25\n\t" \
  "v_bfi_b32 v27, v63, v158, v25\n\t" \
  "v_add_u32 v158, 64, v26\n\t" \
  "v_bfi_b32 v28, v63, v158, v26\n\t" \
  "v_add_u32 v158, 64, v27\n\t" \
  "v_bfi_b32 v29, v63, v158, v27\n\t" \
  "v_add_u32 v158, 64, v28\n\t" \
  "v_bfi_b32 v30, v63, v158, v28\n\t" \
  "v_add_u32 v158, 64, v29\n\t" \
  "v_bfi_b32 v31, v63, v158, v29\n\t" \
  "s_barrier\n\t" \
  "ds_read_b64_tr_b16 v[80:81], v165 offset:1056\n\t" \
  "ds_read_b64_tr_b16 v[82:83], v165 offset:1024\n\t" \
  "ds_read_b64_tr_b16 v[84:85], v165 offset:1312\n\t" \
  "ds_read_b64_tr_b16 v[86:87], v165 offset:1280\n\t" \
  "ds_read_b64_tr_b16 v[88:89], v165 offset:1568\n\t" \
  "ds_read_b64_tr_b16 v[90:91], v165 offset:1536\n\t" \
  "ds_read_b64_tr_b16 v[92:93], v165 offset:1824\n\t" \
  "ds_read_b64_tr_b16 v[94:95], v165 offset:1792\n\t" \
  "s_setprio 3\n\t" \
  "s_barrier\n\t" \
  "s_waitcnt vmcnt(0)\n\t" \
  "v_add_u32 v61, 1, v160\n\t" \
  "s_mov_b64 exec, 1\n\t" \
  "global_store_dword v60, v61, %[ff] sc1\n\t" \
  "s_mov_b64 exec, -1\n\t" \
  "s_setprio 2\n\t" \
  "ds_read_b64_tr_b16 v[96:97], v165 offset:2080\n\t" \
  "ds_read_b64_tr_b16 v[98:99], v165 offset:2048\n\t" \
  "ds_read_b64_tr_b16 v[100:101], v165 offset:2336\n\t" \
  "ds_read_b64_tr_b16 v[102:103], v165 offset:2304\n\t" \
  "ds_read_b64_tr_b16 v[104:105], v165 offset:2592\n\t" \
  "ds_read_b64_tr_b16 v[106:107], v165 offset:2560\n\t" \
  "ds_read_b64_tr_b16 v[108:109], v165 offset:2848\n\t" \
  "ds_read_b64_tr_b16 v[110:111], v165 offset:2816\n\t" \
  "s_barrier\n\t" \
  "ds_read_b64_tr_b16 v[112:113], v165 offset:3104\n\t" \
  "ds_read_b64_tr_b16 v[114:115], v165 offset:3072\n\t" \
  "ds_read_b64_tr_b16 v[116:117], v165 offset:3360\n\t" \
  "ds_read_b64_tr_b16 v[118:119], v165 offset:3328\n\t" \
  "ds_read_b64_tr_b16 v[120:121], v165 offset:3616\n\t" \
  "ds_read_b64_tr_b16 v[122:123], v165 offset:3584\n\t" \
  "ds_read_b64_tr_b16 v[124:125], v165 offset:3872\n\t" \
  "ds_read_b64_tr_b16 v[126:127], v165 offset:3840\n\t" \
  "s_barrier\n\t" \
  "s_waitcnt lgkmcnt(0)\n\t" \
  "v_lshlrev_b32 v61, 16, v161\n\t" \
  "v_lshlrev_b32 v62, 16, v162\n\t" \
  "v_add_f32 v61, v61, v62\n\t" \
  "v_mov_b32 v62, 0xffff\n\t" \
  "v_cvt_pk_bf16_f32 v61, v61, v61\n\t" \
  "s_mov_b64 exec, 1\n\t" \
  "v_bfi_b32 v66, v62, v61, v66\n\t" \
  "s_mov_b64 exec, 15\n\t" \
  "v_and_b32 v66, v62, v66\n\t" \
  "s_mov_b64 exec, 16\n\t" \
  "v_bfi_b32 v66, v62, v66, v61\n\t" \
  "s_mov_b64 exec, 0xff\n\t" \
  "v_mov_b32 v67, 0\n\t" \
  "s_mov_b64 exec, 0x100\n\t" \
  "v_mov_b32 v67, v61\n\t" \
  "s_mov_b64 exec, 0xf00\n\t" \
  "v_and_b32 v67, v62, v67\n\t" \
  "s_mov_b64 exec, 0x1000\n\t" \
  "v_bfi_b32 v67, v62, v67, v61\n\t" \
  "s_mov_b64 exec, -1\n\t" \
  "s_waitcnt lgkmcnt(0)\n\t"
#define PP_NB 15
#define PP_Q 1
#define PP_CLOB "v0", "v1", "v2", "v3", "v4", "v5", "v6", "v7", "v8", "v9", "v10", "v11", "v12", "v13", "v14", "v15", "v16", "v17", "v18", "v19", "v20", "v21", "v22", "v23", "v32", "v33", "v34", "v35", "v36", "v37", "v38", "v39", "v40", "v41", "v42", "v43", "v60", "v61", "v62", "v63", "v128", "v129", "v130", "v131", "v132", "v133", "v134", "v135", "v136", "v137", "v138", "v139", "v140", "v141", "v142", "v143", "v144", "v145", "v146", "v147", "v148", "v149", "v150", "v151", "v158", "v161", "v162"

#define LD_ASM1 \
  "s_add_u32 m0, %[wb], 49152\n\t" \
  "s_add_u32 s68, %[rw0], 0\n\t" \
  "buffer_load_dwordx4 v5, %[sw], s68 offen offset:0 lds\n\t" \
  "buffer_load_dwordx4 v6, %[sw], s68 offen offset:1024 lds\n\t" \
  "buffer_load_dwordx4 v7, %[sw], s68 offen offset:2048 lds\n\t" \
  "s_add_u32 m0, %[xb], 36864\n\t" \
  "s_add_u32 s68, %[rx0], 0\n\t" \
  "buffer_load_dwordx4 v0, %[sx], s68 offen offset:0 lds\n\t" \
  "buffer_load_dwordx4 v1, %[sx], s68 offen offset:1024 lds\n\t" \
  "s_mov_b64 exec, 0xffff\n\t" \
  "buffer_load_dwordx4 v4, %[sx], s68 offen offset:2048 lds\n\t" \
  "s_mov_b64 exec, -1\n\t" \
  "s_add_u32 m0, %[xb], 46080\n\t" \
  "s_add_u32 s68, %[rx0], 147456\n\t" \
  "buffer_load_dwordx4 v2, %[sx], s68 offen offset:0 lds\n\t" \
  "buffer_load_dwordx4 v3, %[sx], s68 offen offset:1024 lds\n\t" \
  "s_mov_b64 exec, 0xffff\n\t" \
  "buffer_load_dwordx4 v4, %[sx], s68 offen offset:2048 lds\n\t" \
  "s_mov_b64 exec, -1\n\t" \
  "s_add_u32 m0, %[wb], 61440\n\t" \
  "s_add_u32 s68, %[rw0], 196608\n\t" \
  "buffer_load_dwordx4 v5, %[sw], s68 offen offset:0 lds\n\t" \
  "buffer_load_dwordx4 v6, %[sw], s68 offen offset:1024 lds\n\t" \
  "buffer_load_dwordx4 v7, %[sw], s68 offen offset:2048 lds\n\t"
#define LD_ASM2 \
  "s_mov_b32 s85, 0x180000\n\t" \
  "s_load_dword s84, %[pw], s85\n\t" \
  "s_mov_b32 s85, 0x180800\n\t" \
  "s_load_dword s84, %[pw], s85\n\t" \
  "s_mov_b32 s85, 0x181000\n\t" \
  "s_load_dword s84, %[pw], s85\n\t" \
  "s_mov_b32 s85, 0x1b0000\n\t" \
  "s_load_dword s84, %[pw], s85\n\t" \
  "s_mov_b32 s85, 0x1b0800\n\t" \
  "s_load_dword s84, %[pw], s85\n\t" \
  "s_mov_b32 s85, 0x1b1000\n\t" \
  "s_load_dword s84, %[pw], s85\n\t" \
  "s_mov_b32 s85, 0x1e0000\n\t" \
  "s_load_dword s84, %[pw], s85\n\t" \
  "s_mov_b32 s85, 0x1e0800\n\t" \
  "s_load_dword s84, %[pw], s85\n\t" \
  "s_add_u32 s68, %[rx1], 0\n\t" \
  "s_add_u32 s69, %[rx1], 147456\n\t" \
  "s_add_u32 s70, %[rw1], 0\n\t" \
  "s_add_u32 m0, %[xb], 0\n\t" \
  "s_waitcnt vmcnt(3)\n\t" \
  "s_barrier\n\t" \
  "buffer_load_dwordx4 v0, %[sx], s68 offen offset:0 lds\n\t" \
  "s_mov_b32 s85, 0x1e1000\n\t" \
  "s_load_dword s84, %[pw], s85\n\t" \
  "buffer_load_dwordx4 v1, %[sx], s68 offen offset:1024 lds\n\t" \
  "s_mov_b32 s85, 0x210000\n\t" \
  "s_load_dword s84, %[pw], s85\n\t" \
  "s_mov_b64 exec, 0xffff\n\t" \
  "buffer_load_dwordx4 v4, %[sx], s68 offen offset:2048 lds\n\t" \
  "s_add_u32 m0, %[xb], 9216\n\t" \
  "s_mov_b64 exec, -1\n\t" \
  "buffer_load_dwordx4 v2, %[sx], s69 offen offset:0 lds\n\t" \
  "s_mov_b32 s85, 0x210800\n\t" \
  "s_load_dword s84, %[pw], s85\n\t" \
  "buffer_load_dwordx4 v3, %[sx], s69 offen offset:1024 lds\n\t" \
  "s_mov_b64 exec, 0xffff\n\t" \
  "buffer_load_dwordx4 v4, %[sx], s69 offen offset:2048 lds\n\t" \
  "s_add_u32 m0, %[wb], 0\n\t" \
  "s_mov_b64 exec, -1\n\t" \
  "buffer_load_dwordx4 v5, %[sw], s70 offen offset:0 lds\n\t" \
  "buffer_load_dwordx4 v6, %[sw], s70 offen offset:1024 lds\n\t" \
  "buffer_load_dwordx4 v7, %[sw], s70 offen offset:2048 lds\n\t" \
  "s_add_u32 s68, %[rw1], 196608\n\t" \
  "s_add_u32 s69, %[rx2], 0\n\t" \
  "s_add_u32 s70, %[rx2], 147456\n\t" \
  "s_add_u32 m0, %[wb], 12288\n\t" \
  "s_waitcnt vmcnt(9)\n\t" \
  "s_barrier\n\t" \
  "buffer_load_dwordx4 v5, %[sw], s68 offen offset:0 lds\n\t" \
  "s_mov_b32 s85, 0x211000\n\t" \
  "s_load_dword s84, %[pw], s85\n\t" \
  "buffer_load_dwordx4 v6, %[sw], s68 offen offset:1024 lds\n\t" \
  "s_mov_b32 s85, 0x240000\n\t" \
  "s_load_dword s84, %[pw], s85\n\t" \
  "buffer_load_dwordx4 v7, %[sw], s68 offen offset:2048 lds\n\t" \
  "s_mov_b32 s85, 0x240800\n\t" \
  "s_load_dword s84, %[pw], s85\n\t" \
  "s_add_u32 m0, %[xb], 18432\n\t" \
  "s_nop 0\n\t" \
  "buffer_load_dwordx4 v0, %[sx], s69 offen offset:0 lds\n\t" \
  "buffer_load_dwordx4 v1, %[sx], s69 offen offset:1024 lds\n\t" \
  "s_mov_b64 exec, 0xffff\n\t" \
  "buffer_load_dwordx4 v4, %[sx], s69 offen offset:2048 lds\n\t" \
  "s_add_u32 m0, %[xb], 27648\n\t" \
  "s_mov_b64 exec, -1\n\t" \
  "buffer_load_dwordx4 v2, %[sx], s70 offen offset:0 lds\n\t" \
  "buffer_load_dwordx4 v3, %[sx], s70 offen offset:1024 lds\n\t" \
  "s_mov_b64 exec, 0xffff\n\t" \
  "buffer_load_dwordx4 v4, %[sx], s70 offen offset:2048 lds\n\t" \
  "s_mov_b64 exec, -1\n\t" \
  "s_add_u32 s68, %[rw2], 0\n\t" \
  "s_add_u32 s69, %[rw2], 196608\n\t" \
  "s_add_u32 s70, %[rx3], 0\n\t" \
  "s_add_u32 m0, %[wb], 24576\n\t" \
  "s_waitcnt vmcnt(9)\n\t" \
  "s_barrier\n\t" \
  "buffer_load_dwordx4 v5, %[sw], s68 offen offset:0 lds\n\t" \
  "s_mov_b32 s85, 0x241000\n\t" \
  "s_load_dword s84, %[pw], s85\n\t" \
  "buffer_load_dwordx4 v6, %[sw], s68 offen offset:1024 lds\n\t" \
  "s_mov_b32 s85, 0x270000\n\t" \
  "s_load_dword s84, %[pw], s85\n\t" \
  "buffer_load_dwordx4 v7, %[sw], s68 offen offset:2048 lds\n\t" \
  "s_mov_b32 s85, 0x270800\n\t" \
  "s_load_dword s84, %[pw], s85\n\t" \
  "s_add_u32 m0, %[wb], 36864\n\t" \
  "s_nop 0\n\t" \
  "buffer_load_dwordx4 v5, %[sw], s69 offen offset:0 lds\n\t" \
  "buffer_load_dwordx4 v6, %[sw], s69 offen offset:1024 lds\n\t" \
  "buffer_load_dwordx4 v7, %[sw], s69 offen offset:2048 lds\n\t" \
  "s_add_u32 m0, %[xb], 36864\n\t" \
  "s_nop 0\n\t" \
  "buffer_load_dwordx4 v0, %[sx], s70 offen offset:0 lds\n\t" \
  "buffer_load_dwordx4 v1, %[sx], s70 offen offset:1024 lds\n\t" \
  "s_mov_b64 exec, 0xffff\n\t" \
  "buffer_load_dwordx4 v4, %[sx], s70 offen offset:2048 lds\n\t" \
  "s_mov_b64 exec, -1\n\t" \
  "s_add_u32 s68, %[rw3], 0\n\t" \
  "s_add_u32 s69, %[rx3], 147456\n\t" \
  "s_add_u32 s70, %[rw3], 196608\n\t" \
  "s_add_u32 m0, %[wb], 49152\n\t" \
  "s_waitcnt vmcnt(15)\n\t" \
  "s_barrier\n\t" \
  "buffer_load_dwordx4 v5, %[sw], s68 offen offset:0 lds\n\t" \
  "s_mov_b32 s85, 0x271000\n\t" \
  "s_load_dword s84, %[pw], s85\n\t" \
  "buffer_load_dwordx4 v6, %[sw], s68 offen offset:1024 lds\n\t" \
  "s_mov_b32 s85, 0x2a0000\n\t" \
  "s_load_dword s84, %[pw], s85\n\t" \
  "buffer_load_dwordx4 v7, %[sw], s68 offen offset:2048 lds\n\t" \
  "s_mov_b32 s85, 0x2a0800\n\t" \
  "s_load_dword s84, %[pw], s85\n\t" \
  "s_add_u32 m0, %[xb], 46080\n\t" \
  "s_nop 0\n\t" \
  "buffer_load_dwordx4 v2, %[sx], s69 offen offset:0 lds\n\t" \
  "buffer_load_dwordx4 v3, %[sx], s69 offen offset:1024 lds\n\t" \
  "s_mov_b64 exec, 0xffff\n\t" \
  "buffer_load_dwordx4 v4, %[sx], s69 offen offset:2048 lds\n\t" \
  "s_add_u32 m0, %[wb], 61440\n\t" \
  "s_mov_b64 exec, -1\n\t" \
  "buffer_load_dwordx4 v5, %[sw], s70 offen offset:0 lds\n\t" \
  "buffer_load_dwordx4 v6, %[sw], s70 offen offset:1024 lds\n\t" \
  "buffer_load_dwordx4 v7, %[sw], s70 offen offset:2048 lds\n\t" \
  "s_add_u32 s68, %[rx0], 512\n\t" \
  "s_add_u32 s69, %[rw0], 1572864\n\t" \
  "s_add_u32 s70, %[rx0], 147968\n\t" \
  "s_add_u32 m0, %[xb], 0\n\t" \
  "s_waitcnt vmcnt(15)\n\t" \
  "s_barrier\n\t" \
  "buffer_load_dwordx4 v0, %[sx], s68 offen offset:0 lds\n\t" \
  "s_mov_b32 s85, 0x2a1000\n\t" \
  "s_load_dword s84, %[pw], s85\n\t" \
  "buffer_load_dwordx4 v1, %[sx], s68 offen offset:1024 lds\n\t" \
  "s_mov_b32 s85, 0x2d0000\n\t" \
  "s_load_dword s84, %[pw], s85\n\t" \
  "s_mov_b64 exec, 0xffff\n\t" \
  "buffer_load_dwordx4 v4, %[sx], s68 offen offset:2048 lds\n\t" \
  "s_add_u32 m0, %[wb], 0\n\t" \
  "s_mov_b64 exec, -1\n\t" \
  "buffer_load_dwordx4 v5, %[sw], s69 offen offset:0 lds\n\t" \
  "s_mov_b32 s85, 0x2d0800\n\t" \
  "s_load_dword s84, %[pw], s85\n\t" \
  "buffer_load_dwordx4 v6, %[sw], s69 offen offset:1024 lds\n\t" \
  "buffer_load_dwordx4 v7, %[sw], s69 offen offset:2048 lds\n\t" \
  "s_add_u32 m0, %[xb], 9216\n\t" \
  "s_nop 0\n\t" \
  "buffer_load_dwordx4 v2, %[sx], s70 offen offset:0 lds\n\t" \
  "buffer_load_dwordx4 v3, %[sx], s70 offen offset:1024 lds\n\t" \
  "s_mov_b64 exec, 0xffff\n\t" \
  "buffer_load_dwordx4 v4, %[sx], s70 offen offset:2048 lds\n\t" \
  "s_mov_b64 exec, -1\n\t" \
  "s_add_u32 s68, %[rw0], 1769472\n\t" \
  "s_add_u32 m0, %[wb], 12288\n\t" \
  "s_waitcnt vmcnt(21)\n\t" \
  "s_barrier\n\t" \
  "buffer_load_dwordx4 v5, %[sw], s68 offen offset:0 lds\n\t" \
  "s_mov_b32 s85, 0x2d1000\n\t" \
  "s_load_dword s84, %[pw], s85\n\t" \
  "buffer_load_dwordx4 v6, %[sw], s68 offen offset:1024 lds\n\t" \
  "s_mov_b32 s85, 0x300000\n\t" \
  "s_load_dword s84, %[pw], s85\n\t" \
  "buffer_load_dwordx4 v7, %[sw], s68 offen offset:2048 lds\n\t" \
  "s_mov_b32 s85, 0x300800\n\t" \
  "s_load_dword s84, %[pw], s85\n\t" \
  "s_add_u32 s68, %[rx1], 512\n\t" \
  "s_add_u32 s69, %[rw1], 1572864\n\t" \
  "s_add_u32 s70, %[rx1], 147968\n\t" \
  "s_add_u32 m0, %[xb], 18432\n\t" \
  "s_waitcnt vmcnt(15)\n\t" \
  "s_barrier\n\t" \
  "buffer_load_dwordx4 v0, %[sx], s68 offen offset:0 lds\n\t" \
  "s_mov_b32 s85, 0x301000\n\t" \
  "s_load_dword s84, %[pw], s85\n\t" \
  "buffer_load_dwordx4 v1, %[sx], s68 offen offset:1024 lds\n\t" \
  "s_mov_b32 s85, 0x330000\n\t" \
  "s_load_dword s84, %[pw], s85\n\t" \
  "s_mov_b64 exec, 0xffff\n\t" \
  "buffer_load_dwordx4 v4, %[sx], s68 offen offset:2048 lds\n\t" \
  "s_add_u32 m0, %[wb], 24576\n\t" \
  "s_mov_b64 exec, -1\n\t" \
  "buffer_load_dwordx4 v5, %[sw], s69 offen offset:0 lds\n\t" \
  "s_mov_b32 s85, 0x330800\n\t" \
  "s_load_dword s84, %[pw], s85\n\t" \
  "buffer_load_dwordx4 v6, %[sw], s69 offen offset:1024 lds\n\t" \
  "buffer_load_dwordx4 v7, %[sw], s69 offen offset:2048 lds\n\t" \
  "s_add_u32 m0, %[xb], 27648\n\t" \
  "s_nop 0\n\t" \
  "buffer_load_dwordx4 v2, %[sx], s70 offen offset:0 lds\n\t" \
  "buffer_load_dwordx4 v3, %[sx], s70 offen offset:1024 lds\n\t" \
  "s_mov_b64 exec, 0xffff\n\t" \
  "buffer_load_dwordx4 v4, %[sx], s70 offen offset:2048 lds\n\t" \
  "s_mov_b64 exec, -1\n\t" \
  "s_add_u32 s68, %[rw1], 1769472\n\t" \
  "s_add_u32 m0, %[wb], 36864\n\t" \
  "s_waitcnt vmcnt(21)\n\t" \
  "s_barrier\n\t" \
  "buffer_load_dwordx4 v5, %[sw], s68 offen offset:0 lds\n\t" \
  "s_mov_b32 s85, 0x331000\n\t" \
  "s_load_dword s84, %[pw], s85\n\t" \
  "buffer_load_dwordx4 v6, %[sw], s68 offen offset:1024 lds\n\t" \
  "s_mov_b32 s85, 0x360000\n\t" \
  "s_load_dword s84, %[pw], s85\n\t" \
  "buffer_load_dwordx4 v7, %[sw], s68 offen offset:2048 lds\n\t" \
  "s_mov_b32 s85, 0x360800\n\t" \
  "s_load_dword s84, %[pw], s85\n\t" \
  "s_add_u32 s68, %[rx2], 512\n\t" \
  "s_add_u32 s69, %[rw2], 1572864\n\t" \
  "s_add_u32 m0, %[xb], 36864\n\t" \
  "s_waitcnt vmcnt(15)\n\t" \
  "s_barrier\n\t" \
  "buffer_load_dwordx4 v0, %[sx], s68 offen offset:0 lds\n\t" \
  "s_mov_b32 s85, 0x361000\n\t" \
  "s_load_dword s84, %[pw], s85\n\t" \
  "buffer_load_dwordx4 v1, %[sx], s68 offen offset:1024 lds\n\t" \
  "s_mov_b32 s85, 0x390000\n\t" \
  "s_load_dword s84, %[pw], s85\n\t" \
  "s_mov_b64 exec, 0xffff\n\t" \
  "buffer_load_dwordx4 v4, %[sx], s68 offen offset:2048 lds\n\t" \
  "s_add_u32 m0, %[wb], 49152\n\t" \
  "s_mov_b64 exec, -1\n\t" \
  "buffer_load_dwordx4 v5, %[sw], s69 offen offset:0 lds\n\t" \
  "s_mov_b32 s85, 0x390800\n\t" \
  "s_load_dword s84, %[pw], s85\n\t" \
  "buffer_load_dwordx4 v6, %[sw], s69 offen offset:1024 lds\n\t" \
  "buffer_load_dwordx4 v7, %[sw], s69 offen offset:2048 lds\n\t" \
  "s_add_u32 s68, %[rx2], 147968\n\t" \
  "s_add_u32 s69, %[rw2], 1769472\n\t" \
  "s_add_u32 m0, %[xb], 46080\n\t" \
  "s_waitcnt vmcnt(18)\n\t" \
  "s_barrier\n\t" \
  "buffer_load_dwordx4 v2, %[sx], s68 offen offset:0 lds\n\t" \
  "s_mov_b32 s85, 0x391000\n\t" \
  "s_load_dword s84, %[pw], s85\n\t" \
  "buffer_load_dwordx4 v3, %[sx], s68 offen offset:1024 lds\n\t" \
  "s_mov_b32 s85, 0x3c0000\n\t" \
  "s_load_dword s84, %[pw], s85\n\t" \
  "s_mov_b64 exec, 0xffff\n\t" \
  "buffer_load_dwordx4 v4, %[sx], s68 offen offset:2048 lds\n\t" \
  "s_add_u32 m0, %[wb], 61440\n\t" \
  "s_mov_b64 exec, -1\n\t" \
  "buffer_load_dwordx4 v5, %[sw], s69 offen offset:0 lds\n\t" \
  "s_mov_b32 s85, 0x3c0800\n\t" \
  "s_load_dword s84, %[pw], s85\n\t" \
  "buffer_load_dwordx4 v6, %[sw], s69 offen offset:1024 lds\n\t" \
  "buffer_load_dwordx4 v7, %[sw], s69 offen offset:2048 lds\n\t" \
  "s_add_u32 s68, %[rx3], 512\n\t" \
  "s_add_u32 s69, %[rw3], 1572864\n\t" \
  "s_add_u32 m0, %[xb], 0\n\t" \
  "s_waitcnt vmcnt(15)\n\t" \
  "s_barrier\n\t" \
  "buffer_load_dwordx4 v0, %[sx], s68 offen offset:0 lds\n\t" \
  "s_mov_b32 s85, 0x3c1000\n\t" \
  "s_load_dword s84, %[pw], s85\n\t" \
  "buffer_load_dwordx4 v1, %[sx], s68 offen offset:1024 lds\n\t" \
  "s_mov_b32 s85, 0x3f0000\n\t" \
  "s_load_dword s84, %[pw], s85\n\t" \
  "s_mov_b64 exec, 0xffff\n\t" \
  "buffer_load_dwordx4 v4, %[sx], s68 offen offset:2048 lds\n\t" \
  "s_add_u32 m0, %[wb], 0\n\t" \
  "s_mov_b64 exec, -1\n\t" \
  "buffer_load_dwordx4 v5, %[sw], s69 offen offset:0 lds\n\t" \
  "s_mov_b32 s85, 0x3f0800\n\t" \
  "s_load_dword s84, %[pw], s85\n\t" \
  "buffer_load_dwordx4 v6, %[sw], s69 offen offset:1024 lds\n\t" \
  "buffer_load_dwordx4 v7, %[sw], s69 offen offset:2048 lds\n\t" \
  "s_add_u32 s68, %[rx3], 147968\n\t" \
  "s_add_u32 s69, %[rw3], 1769472\n\t" \
  "s_add_u32 m0, %[xb], 9216\n\t" \
  "s_waitcnt vmcnt(18)\n\t" \
  "s_barrier\n\t" \
  "buffer_load_dwordx4 v2, %[sx], s68 offen offset:0 lds\n\t" \
  "s_mov_b32 s85, 0x3f1000\n\t" \
  "s_load_dword s84, %[pw], s85\n\t" \
  "buffer_load_dwordx4 v3, %[sx], s68 offen offset:1024 lds\n\t" \
  "s_mov_b32 s85, 0x420000\n\t" \
  "s_load_dword s84, %[pw], s85\n\t" \
  "s_mov_b64 exec, 0xffff\n\t" \
  "buffer_load_dwordx4 v4, %[sx], s68 offen offset:2048 lds\n\t" \
  "s_add_u32 m0, %[wb], 12288\n\t" \
  "s_mov_b64 exec, -1\n\t" \
  "buffer_load_dwordx4 v5, %[sw], s69 offen offset:0 lds\n\t" \
  "s_mov_b32 s85, 0x420800\n\t" \
  "s_load_dword s84, %[pw], s85\n\t" \
  "buffer_load_dwordx4 v6, %[sw], s69 offen offset:1024 lds\n\t" \
  "buffer_load_dwordx4 v7, %[sw], s69 offen offset:2048 lds\n\t" \
  "s_add_u32 s68, %[rx0], 1024\n\t" \
  "s_add_u32 s69, %[rw0], 3145728\n\t" \
  "s_add_u32 m0, %[xb], 18432\n\t" \
  "s_waitcnt vmcnt(15)\n\t" \
  "s_barrier\n\t" \
  "buffer_load_dwordx4 v0, %[sx], s68 offen offset:0 lds\n\t" \
  "s_mov_b32 s85, 0x421000\n\t" \
  "s_load_dword s84, %[pw], s85\n\t" \
  "buffer_load_dwordx4 v1, %[sx], s68 offen offset:1024 lds\n\t" \
  "s_mov_b32 s85, 0x450000\n\t" \
  "s_load_dword s84, %[pw], s85\n\t" \
  "s_mov_b64 exec, 0xffff\n\t" \
  "buffer_load_dwordx4 v4, %[sx], s68 offen offset:2048 lds\n\t" \
  "s_add_u32 m0, %[wb], 24576\n\t" \
  "s_mov_b64 exec, -1\n\t" \
  "buffer_load_dwordx4 v5, %[sw], s69 offen offset:0 lds\n\t" \
  "s_mov_b32 s85, 0x450800\n\t" \
  "s_load_dword s84, %[pw], s85\n\t" \
  "buffer_load_dwordx4 v6, %[sw], s69 offen offset:1024 lds\n\t" \
  "buffer_load_dwordx4 v7, %[sw], s69 offen offset:2048 lds\n\t" \
  "s_add_u32 s68, %[rx0], 148480\n\t" \
  "s_add_u32 s69, %[rw0], 3342336\n\t" \
  "s_add_u32 m0, %[xb], 27648\n\t" \
  "s_waitcnt vmcnt(18)\n\t" \
  "s_barrier\n\t" \
  "buffer_load_dwordx4 v2, %[sx], s68 offen offset:0 lds\n\t" \
  "s_mov_b32 s85, 0x451000\n\t" \
  "s_load_dword s84, %[pw], s85\n\t" \
  "buffer_load_dwordx4 v3, %[sx], s68 offen offset:1024 lds\n\t" \
  "s_mov_b32 s85, 0x480000\n\t" \
  "s_load_dword s84, %[pw], s85\n\t" \
  "s_mov_b64 exec, 0xffff\n\t" \
  "buffer_load_dwordx4 v4, %[sx], s68 offen offset:2048 lds\n\t" \
  "s_add_u32 m0, %[wb], 36864\n\t" \
  "s_mov_b64 exec, -1\n\t" \
  "buffer_load_dwordx4 v5, %[sw], s69 offen offset:0 lds\n\t" \
  "s_mov_b32 s85, 0x480800\n\t" \
  "s_load_dword s84, %[pw], s85\n\t" \
  "buffer_load_dwordx4 v6, %[sw], s69 offen offset:1024 lds\n\t" \
  "buffer_load_dwordx4 v7, %[sw], s69 offen offset:2048 lds\n\t" \
  "s_add_u32 s68, %[rx1], 1024\n\t" \
  "s_add_u32 s69, %[rw1], 3145728\n\t" \
  "s_add_u32 m0, %[xb], 36864\n\t" \
  "s_waitcnt vmcnt(15)\n\t" \
  "s_barrier\n\t" \
  "buffer_load_dwordx4 v0, %[sx], s68 offen offset:0 lds\n\t" \
  "s_mov_b32 s85, 0x481000\n\t" \
  "s_load_dword s84, %[pw], s85\n\t" \
  "buffer_load_dwordx4 v1, %[sx], s68 offen offset:1024 lds\n\t" \
  "s_mov_b32 s85, 0x4b0000\n\t" \
  "s_load_dword s84, %[pw], s85\n\t" \
  "s_mov_b64 exec, 0xffff\n\t" \
  "buffer_load_dwordx4 v4, %[sx], s68 offen offset:2048 lds\n\t" \
  "s_add_u32 m0, %[wb], 49152\n\t" \
  "s_mov_b64 exec, -1\n\t" \
  "buffer_load_dwordx4 v5, %[sw], s69 offen offset:0 lds\n\t" \
  "s_mov_b32 s85, 0x4b0800\n\t" \
  "s_load_dword s84, %[pw], s85\n\t" \
  "buffer_load_dwordx4 v6, %[sw], s69 offen offset:1024 lds\n\t" \
  "buffer_load_dwordx4 v7, %[sw], s69 offen offset:2048 lds\n\t" \
  "s_add_u32 s68, %[rx1], 148480\n\t" \
  "s_add_u32 s69, %[rw1], 3342336\n\t" \
  "s_add_u32 m0, %[xb], 46080\n\t" \
  "s_waitcnt vmcnt(18)\n\t" \
  "s_barrier\n\t" \
  "buffer_load_dwordx4 v2, %[sx], s68 offen offset:0 lds\n\t" \
  "s_mov_b32 s85, 0x4b1000\n\t" \
  "s_load_dword s84, %[pw], s85\n\t" \
  "buffer_load_dwordx4 v3, %[sx], s68 offen offset:1024 lds\n\t" \
  "s_mov_b32 s85, 0x4e0000\n\t" \
  "s_load_dword s84, %[pw], s85\n\t" \
  "s_mov_b64 exec, 0xffff\n\t" \
  "buffer_load_dwordx4 v4, %[sx], s68 offen offset:2048 lds\n\t" \
  "s_add_u32 m0, %[wb], 61440\n\t" \
  "s_mov_b64 exec, -1\n\t" \
  "buffer_load_dwordx4 v5, %[sw], s69 offen offset:0 lds\n\t" \
  "s_mov_b32 s85, 0x4e0800\n\t" \
  "s_load_dword s84, %[pw], s85\n\t" \
  "buffer_load_dwordx4 v6, %[sw], s69 offen offset:1024 lds\n\t" \
  "buffer_load_dwordx4 v7, %[sw], s69 offen offset:2048 lds\n\t" \
  "s_add_u32 s68, %[rx2], 1024\n\t" \
  "s_add_u32 s69, %[rw2], 3145728\n\t" \
  "s_add_u32 m0, %[xb], 0\n\t" \
  "s_waitcnt vmcnt(15)\n\t" \
  "s_barrier\n\t" \
  "buffer_load_dwordx4 v0, %[sx], s68 offen offset:0 lds\n\t" \
  "s_mov_b32 s85, 0x4e1000\n\t" \
  "s_load_dword s84, %[pw], s85\n\t" \
  "buffer_load_dwordx4 v1, %[sx], s68 offen offset:1024 lds\n\t" \
  "s_mov_b32 s85, 0x510000\n\t" \
  "s_load_dword s84, %[pw], s85\n\t" \
  "s_mov_b64 exec, 0xffff\n\t" \
  "buffer_load_dwordx4 v4, %[sx], s68 offen offset:2048 lds\n\t" \
  "s_add_u32 m0, %[wb], 0\n\t" \
  "s_mov_b64 exec, -1\n\t" \
  "buffer_load_dwordx4 v5, %[sw], s69 offen offset:0 lds\n\t" \
  "s_mov_b32 s85, 0x510800\n\t" \
  "s_load_dword s84, %[pw], s85\n\t" \
  "buffer_load_dwordx4 v6, %[sw], s69 offen offset:1024 lds\n\t" \
  "buffer_load_dwordx4 v7, %[sw], s69 offen offset:2048 lds\n\t" \
  "s_add_u32 s68, %[rx2], 148480\n\t" \
  "s_add_u32 s69, %[rw2], 3342336\n\t" \
  "s_add_u32 m0, %[xb], 9216\n\t" \
  "s_waitcnt vmcnt(18)\n\t" \
  "s_barrier\n\t" \
  "buffer_load_dwordx4 v2, %[sx], s68 offen offset:0 lds\n\t" \
  "s_mov_b32 s85, 0x511000\n\t" \
  "s_load_dword s84, %[pw], s85\n\t" \
  "buffer_load_dwordx4 v3, %[sx], s68 offen offset:1024 lds\n\t" \
  "s_mov_b32 s85, 0x540000\n\t" \
  "s_load_dword s84, %[pw], s85\n\t" \
  "s_mov_b64 exec, 0xffff\n\t" \
  "buffer_load_dwordx4 v4, %[sx], s68 offen offset:2048 lds\n\t" \
  "s_add_u32 m0, %[wb], 12288\n\t" \
  "s_mov_b64 exec, -1\n\t" \
  "buffer_load_dwordx4 v5, %[sw], s69 offen offset:0 lds\n\t" \
  "s_mov_b32 s85, 0x540800\n\t" \
  "s_load_dword s84, %[pw], s85\n\t" \
  "buffer_load_dwordx4 v6, %[sw], s69 offen offset:1024 lds\n\t" \
  "buffer_load_dwordx4 v7, %[sw], s69 offen offset:2048 lds\n\t" \
  "s_mov_b32 m0, %[tsk]\n\t" \
  "s_mov_b64 exec, %[tm]\n\t" \
  "global_load_lds_dword v12, %[tp]\n\t" \
  "s_mov_b64 exec, -1\n\t" \
  "s_add_u32 s68, %[rx3], 1024\n\t" \
  "s_add_u32 s69, %[rw3], 3145728\n\t" \
  "s_add_u32 m0, %[xb], 18432\n\t" \
  "s_waitcnt vmcnt(15)\n\t" \
  "s_barrier\n\t" \
  "buffer_load_dwordx4 v0, %[sx], s68 offen offset:0 lds\n\t" \
  "s_mov_b32 s85, 0x541000\n\t" \
  "s_load_dword s84, %[pw], s85\n\t" \
  "buffer_load_dwordx4 v1, %[sx], s68 offen offset:1024 lds\n\t" \
  "s_mov_b32 s85, 0x570000\n\t" \
  "s_load_dword s84, %[pw], s85\n\t" \
  "s_mov_b64 exec, 0xffff\n\t" \
  "buffer_load_dwordx4 v4, %[sx], s68 offen offset:2048 lds\n\t" \
  "s_add_u32 m0, %[wb], 24576\n\t" \
  "s_mov_b64 exec, -1\n\t" \
  "buffer_load_dwordx4 v5, %[sw], s69 offen offset:0 lds\n\t" \
  "s_mov_b32 s85, 0x570800\n\t" \
  "s_load_dword s84, %[pw], s85\n\t" \
  "buffer_load_dwordx4 v6, %[sw], s69 offen offset:1024 lds\n\t" \
  "buffer_load_dwordx4 v7, %[sw], s69 offen offset:2048 lds\n\t" \
  "s_add_u32 s68, %[rx3], 148480\n\t" \
  "s_add_u32 s69, %[rw3], 3342336\n\t" \
  "s_add_u32 m0, %[xb], 27648\n\t" \
  "s_waitcnt vmcnt(18)\n\t" \
  "s_barrier\n\t" \
  "buffer_load_dwordx4 v2, %[sx], s68 offen offset:0 lds\n\t" \
  "s_mov_b32 s85, 0x571000\n\t" \
  "s_load_dword s84, %[pw], s85\n\t" \
  "buffer_load_dwordx4 v3, %[sx], s68 offen offset:1024 lds\n\t" \
  "s_mov_b32 s85, 0x5a0000\n\t" \
  "s_load_dword s84, %[pw], s85\n\t" \
  "s_mov_b64 exec, 0xffff\n\t" \
  "buffer_load_dwordx4 v4, %[sx], s68 offen offset:2048 lds\n\t" \
  "s_add_u32 m0, %[wb], 36864\n\t" \
  "s_mov_b64 exec, -1\n\t" \
  "buffer_load_dwordx4 v5, %[sw], s69 offen offset:0 lds\n\t" \
  "s_mov_b32 s85, 0x5a0800\n\t" \
  "s_load_dword s84, %[pw], s85\n\t" \
  "buffer_load_dwordx4 v6, %[sw], s69 offen offset:1024 lds\n\t" \
  "buffer_load_dwordx4 v7, %[sw], s69 offen offset:2048 lds\n\t" \
  "s_add_u32 s68, %[rx0], 1536\n\t" \
  "s_add_u32 s69, %[rw0], 4718592\n\t" \
  "s_add_u32 m0, %[xb], 36864\n\t" \
  "s_waitcnt vmcnt(15)\n\t" \
  "s_barrier\n\t" \
  "buffer_load_dwordx4 v0, %[sx], s68 offen offset:0 lds\n\t" \
  "s_mov_b32 s85, 0x5a1000\n\t" \
  "s_load_dword s84, %[pw], s85\n\t" \
  "buffer_load_dwordx4 v1, %[sx], s68 offen offset:1024 lds\n\t" \
  "s_mov_b32 s85, 0x5d0000\n\t" \
  "s_load_dword s84, %[pw], s85\n\t" \
  "s_mov_b64 exec, 0xffff\n\t" \
  "buffer_load_dwordx4 v4, %[sx], s68 offen offset:2048 lds\n\t" \
  "s_add_u32 m0, %[wb], 49152\n\t" \
  "s_mov_b64 exec, -1\n\t" \
  "buffer_load_dwordx4 v5, %[sw], s69 offen offset:0 lds\n\t" \
  "s_mov_b32 s85, 0x5d0800\n\t" \
  "s_load_dword s84, %[pw], s85\n\t" \
  "buffer_load_dwordx4 v6, %[sw], s69 offen offset:1024 lds\n\t" \
  "buffer_load_dwordx4 v7, %[sw], s69 offen offset:2048 lds\n\t" \
  "s_add_u32 s68, %[rx0], 148992\n\t" \
  "s_add_u32 s69, %[rw0], 4915200\n\t" \
  "s_add_u32 m0, %[xb], 46080\n\t" \
  "s_waitcnt vmcnt(18)\n\t" \
  "s_barrier\n\t" \
  "buffer_load_dwordx4 v2, %[sx], s68 offen offset:0 lds\n\t" \
  "s_mov_b32 s85, 0x5d1000\n\t" \
  "s_load_dword s84, %[pw], s85\n\t" \
  "buffer_load_dwordx4 v3, %[sx], s68 offen offset:1024 lds\n\t" \
  "s_load_dword s84, %[po], 0x0\n\t" \
  "s_mov_b64 exec, 0xffff\n\t" \
  "buffer_load_dwordx4 v4, %[sx], s68 offen offset:2048 lds\n\t" \
  "s_add_u32 m0, %[wb], 61440\n\t" \
  "s_mov_b64 exec, -1\n\t" \
  "buffer_load_dwordx4 v5, %[sw], s69 offen offset:0 lds\n\t" \
  "s_load_dword s84, %[po], 0x40\n\t" \
  "buffer_load_dwordx4 v6, %[sw], s69 offen offset:1024 lds\n\t" \
  "buffer_load_dwordx4 v7, %[sw], s69 offen offset:2048 lds\n\t" \
  "s_add_u32 s68, %[rx1], 1536\n\t" \
  "s_add_u32 s69, %[rw1], 4718592\n\t" \
  "s_add_u32 m0, %[xb], 0\n\t" \
  "s_waitcnt vmcnt(15)\n\t" \
  "s_barrier\n\t" \
  "buffer_load_dwordx4 v0, %[sx], s68 offen offset:0 lds\n\t" \
  "s_load_dword s84, %[po], 0x80\n\t" \
  "buffer_load_dwordx4 v1, %[sx], s68 offen offset:1024 lds\n\t" \
  "s_load_dword s84, %[po], 0xc0\n\t" \
  "s_mov_b64 exec, 0xffff\n\t" \
  "buffer_load_dwordx4 v4, %[sx], s68 offen offset:2048 lds\n\t" \
  "s_add_u32 m0, %[wb], 0\n\t" \
  "s_mov_b64 exec, -1\n\t" \
  "buffer_load_dwordx4 v5, %[sw], s69 offen offset:0 lds\n\t" \
  "s_load_dword s84, %[po], 0x100\n\t" \
  "buffer_load_dwordx4 v6, %[sw], s69 offen offset:1024 lds\n\t" \
  "buffer_load_dwordx4 v7, %[sw], s69 offen offset:2048 lds\n\t" \
  "s_add_u32 s68, %[rx1], 148992\n\t" \
  "s_add_u32 s69, %[rw1], 4915200\n\t" \
  "s_add_u32 m0, %[xb], 9216\n\t" \
  "s_waitcnt vmcnt(18)\n\t" \
  "s_barrier\n\t" \
  "buffer_load_dwordx4 v2, %[sx], s68 offen offset:0 lds\n\t" \
  "s_load_dword s84, %[po], 0x140\n\t" \
  "buffer_load_dwordx4 v3, %[sx], s68 offen offset:1024 lds\n\t" \
  "s_load_dword s84, %[po], 0x180\n\t" \
  "s_mov_b64 exec, 0xffff\n\t" \
  "buffer_load_dwordx4 v4, %[sx], s68 offen offset:2048 lds\n\t" \
  "s_add_u32 m0, %[wb], 12288\n\t" \
  "s_mov_b64 exec, -1\n\t" \
  "buffer_load_dwordx4 v5, %[sw], s69 offen offset:0 lds\n\t" \
  "s_load_dword s84, %[po], 0x1c0\n\t" \
  "buffer_load_dwordx4 v6, %[sw], s69 offen offset:1024 lds\n\t" \
  "buffer_load_dwordx4 v7, %[sw], s69 offen offset:2048 lds\n\t" \
  "s_add_u32 s68, %[rx2], 1536\n\t" \
  "s_add_u32 s69, %[rw2], 4718592\n\t" \
  "s_add_u32 m0, %[xb], 18432\n\t" \
  "s_waitcnt vmcnt(15)\n\t" \
  "s_barrier\n\t" \
  "buffer_load_dwordx4 v0, %[sx], s68 offen offset:0 lds\n\t" \
  "s_load_dword s84, %[po], 0x200\n\t" \
  "buffer_load_dwordx4 v1, %[sx], s68 offen offset:1024 lds\n\t" \
  "s_load_dword s84, %[po], 0x240\n\t" \
  "s_mov_b64 exec, 0xffff\n\t" \
  "buffer_load_dwordx4 v4, %[sx], s68 offen offset:2048 lds\n\t" \
  "s_add_u32 m0, %[wb], 24576\n\t" \
  "s_mov_b64 exec, -1\n\t" \
  "buffer_load_dwordx4 v5, %[sw], s69 offen offset:0 lds\n\t" \
  "s_load_dword s84, %[po], 0x280\n\t" \
  "buffer_load_dwordx4 v6, %[sw], s69 offen offset:1024 lds\n\t" \
  "buffer_load_dwordx4 v7, %[sw], s69 offen offset:2048 lds\n\t" \
  "s_add_u32 s68, %[rx2], 148992\n\t" \
  "s_add_u32 s69, %[rw2], 4915200\n\t" \
  "s_add_u32 m0, %[xb], 27648\n\t" \
  "s_waitcnt vmcnt(18)\n\t" \
  "s_barrier\n\t" \
  "buffer_load_dwordx4 v2, %[sx], s68 offen offset:0 lds\n\t" \
  "s_load_dword s84, %[po], 0x2c0\n\t" \
  "buffer_load_dwordx4 v3, %[sx], s68 offen offset:1024 lds\n\t" \
  "s_load_dword s84, %[po], 0x300\n\t" \
  "s_mov_b64 exec, 0xffff\n\t" \
  "buffer_load_dwordx4 v4, %[sx], s68 offen offset:2048 lds\n\t" \
  "s_add_u32 m0, %[wb], 36864\n\t" \
  "s_mov_b64 exec, -1\n\t" \
  "buffer_load_dwordx4 v5, %[sw], s69 offen offset:0 lds\n\t" \
  "s_load_dword s84, %[po], 0x340\n\t" \
  "buffer_load_dwordx4 v6, %[sw], s69 offen offset:1024 lds\n\t" \
  "buffer_load_dwordx4 v7, %[sw], s69 offen offset:2048 lds\n\t" \
  "s_add_u32 s68, %[rx3], 1536\n\t" \
  "s_add_u32 s69, %[rw3], 4718592\n\t" \
  "s_add_u32 m0, %[xb], 36864\n\t" \
  "s_waitcnt vmcnt(15)\n\t" \
  "s_barrier\n\t" \
  "buffer_load_dwordx4 v0, %[sx], s68 offen offset:0 lds\n\t" \
  "s_load_dword s84, %[po], 0x380\n\t" \
  "buffer_load_dwordx4 v1, %[sx], s68 offen offset:1024 lds\n\t" \
  "s_load_dword s84, %[po], 0x3c0\n\t" \
  "s_mov_b64 exec, 0xffff\n\t" \
  "buffer_load_dwordx4 v4, %[sx], s68 offen offset:2048 lds\n\t" \
  "s_add_u32 m0, %[wb], 49152\n\t" \
  "s_mov_b64 exec, -1\n\t" \
  "buffer_load_dwordx4 v5, %[sw], s69 offen offset:0 lds\n\t" \
  "s_load_dword s84, %[po], 0x400\n\t" \
  "buffer_load_dwordx4 v6, %[sw], s69 offen offset:1024 lds\n\t" \
  "buffer_load_dwordx4 v7, %[sw], s69 offen offset:2048 lds\n\t" \
  "s_add_u32 s68, %[rx3], 148992\n\t" \
  "s_add_u32 s69, %[rw3], 4915200\n\t" \
  "s_add_u32 m0, %[xb], 46080\n\t" \
  "s_waitcnt vmcnt(18)\n\t" \
  "s_barrier\n\t" \
  "buffer_load_dwordx4 v2, %[sx], s68 offen offset:0 lds\n\t" \
  "s_load_dword s84, %[po], 0x440\n\t" \
  "buffer_load_dwordx4 v3, %[sx], s68 offen offset:1024 lds\n\t" \
  "s_load_dword s84, %[po], 0x480\n\t" \
  "s_mov_b64 exec, 0xffff\n\t" \
  "buffer_load_dwordx4 v4, %[sx], s68 offen offset:2048 lds\n\t" \
  "s_add_u32 m0, %[wb], 61440\n\t" \
  "s_mov_b64 exec, -1\n\t" \
  "buffer_load_dwordx4 v5, %[sw], s69 offen offset:0 lds\n\t" \
  "s_load_dword s84, %[po], 0x4c0\n\t" \
  "buffer_load_dwordx4 v6, %[sw], s69 offen offset:1024 lds\n\t" \
  "buffer_load_dwordx4 v7, %[sw], s69 offen offset:2048 lds\n\t" \
  "v_readlane_b32 s68, v9, 0\n\t" \
  "v_readlane_b32 s69, v9, 1\n\t" \
  "v_readlane_b32 s70, v9, 2\n\t" \
  "v_readlane_b32 s71, v9, 18\n\t" \
  "v_readlane_b32 s72, v9, 19\n\t" \
  "s_add_u32 m0, %[ob], 0\n\t" \
  "s_waitcnt vmcnt(15)\n\t" \
  "s_barrier\n\t" \
  "s_nop 2\n\t" \
  "buffer_load_dwordx4 v8, %[so], s68 offen lds\n\t" \
  "s_load_dword s84, %[po], 0x500\n\t" \
  "s_add_u32 m0, %[ob], 4096\n\t" \
  "s_nop 0\n\t" \
  "buffer_load_dwordx4 v8, %[so], s69 offen lds\n\t" \
  "s_load_dword s84, %[po], 0x540\n\t" \
  "s_add_u32 m0, %[ob], 8192\n\t" \
  "s_nop 0\n\t" \
  "buffer_load_dwordx4 v8, %[so], s70 offen lds\n\t" \
  "s_load_dword s84, %[po], 0x580\n\t" \
  "s_add_u32 m0, %[ob], 73728\n\t" \
  "s_nop 0\n\t" \
  "buffer_load_dwordx4 v8, %[so], s71 offen lds\n\t" \
  "s_add_u32 m0, %[ob], 77824\n\t" \
  "s_nop 0\n\t" \
  "buffer_load_dwordx4 v8, %[so], s72 offen lds\n\t" \
  "v_readlane_b32 s68, v9, 3\n\t" \
  "v_readlane_b32 s69, v9, 4\n\t" \
  "v_readlane_b32 s70, v9, 5\n\t" \
  "v_readlane_b32 s71, v9, 20\n\t" \
  "v_readlane_b32 s72, v9, 21\n\t" \
  "s_add_u32 m0, %[ob], 12288\n\t" \
  "s_waitcnt vmcnt(17)\n\t" \
  "s_barrier\n\t" \
  "s_nop 2\n\t" \
  "buffer_load_dwordx4 v8, %[so], s68 offen lds\n\t" \
  "s_load_dword s84, %[po], 0x5c0\n\t" \
  "s_add_u32 m0, %[ob], 16384\n\t" \
  "s_nop 0\n\t" \
  "buffer_load_dwordx4 v8, %[so], s69 offen lds\n\t" \
  "s_load_dword s84, %[po], 0x600\n\t" \
  "s_add_u32 m0, %[ob], 20480\n\t" \
  "s_nop 0\n\t" \
  "buffer_load_dwordx4 v8, %[so], s70 offen lds\n\t" \
  "s_load_dword s84, %[po], 0x640\n\t" \
  "s_add_u32 m0, %[ob], 81920\n\t" \
  "s_nop 0\n\t" \
  "buffer_load_dwordx4 v8, %[so], s71 offen lds\n\t" \
  "s_add_u32 m0, %[ob], 86016\n\t" \
  "s_nop 0\n\t" \
  "buffer_load_dwordx4 v8, %[so], s72 offen lds\n\t" \
  "v_readlane_b32 s68, v9, 6\n\t" \
  "v_readlane_b32 s69, v9, 7\n\t" \
  "v_readlane_b32 s70, v9, 8\n\t" \
  "v_readlane_b32 s71, v9, 22\n\t" \
  "v_readlane_b32 s72, v9, 23\n\t" \
  "v_readlane_b32 s73, v9, 24\n\t" \
  "s_add_u32 m0, %[ob], 24576\n\t" \
  "s_waitcnt vmcnt(13)\n\t" \
  "s_barrier\n\t" \
  "s_nop 2\n\t" \
  "buffer_load_dwordx4 v8, %[so], s68 offen lds\n\t" \
  "s_load_dword s84, %[po], 0x680\n\t" \
  "s_add_u32 m0, %[ob], 28672\n\t" \
  "s_nop 0\n\t" \
  "buffer_load_dwordx4 v8, %[so], s69 offen lds\n\t" \
  "s_load_dword s84, %[po], 0x6c0\n\t" \
  "s_add_u32 m0, %[ob], 32768\n\t" \
  "s_nop 0\n\t" \
  "buffer_load_dwordx4 v8, %[so], s70 offen lds\n\t" \
  "s_load_dword s84, %[po], 0x700\n\t" \
  "s_add_u32 m0, %[ob], 90112\n\t" \
  "s_nop 0\n\t" \
  "buffer_load_dwordx4 v8, %[so], s71 offen lds\n\t" \
  "s_add_u32 m0, %[ob], 94208\n\t" \
  "s_nop 0\n\t" \
  "buffer_load_dwordx4 v8, %[so], s72 offen lds\n\t" \
  "s_add_u32 m0, %[ob], 98304\n\t" \
  "s_nop 0\n\t" \
  "buffer_load_dwordx4 v8, %[so], s73 offen lds\n\t" \
  "v_readlane_b32 s68, v9, 9\n\t" \
  "v_readlane_b32 s69, v9, 10\n\t" \
  "v_readlane_b32 s70, v9, 11\n\t" \
  "v_readlane_b32 s71, v9, 25\n\t" \
  "v_readlane_b32 s72, v9, 26\n\t" \
  "s_add_u32 m0, %[ob], 36864\n\t" \
  "s_waitcnt vmcnt(16)\n\t" \
  "s_barrier\n\t" \
  "s_nop 2\n\t" \
  "buffer_load_dwordx4 v8, %[so], s68 offen lds\n\t" \
  "s_load_dword s84, %[po], 0x740\n\t" \
  "s_add_u32 m0, %[ob], 40960\n\t" \
  "s_nop 0\n\t" \
  "buffer_load_dwordx4 v8, %[so], s69 offen lds\n\t" \
  "s_load_dword s84, %[po], 0x780\n\t" \
  "s_add_u32 m0, %[ob], 45056\n\t" \
  "s_nop 0\n\t" \
  "buffer_load_dwordx4 v8, %[so], s70 offen lds\n\t" \
  "s_load_dword s84, %[po], 0x7c0\n\t" \
  "s_add_u32 m0, %[ob], 102400\n\t" \
  "s_nop 0\n\t" \
  "buffer_load_dwordx4 v8, %[so], s71 offen lds\n\t" \
  "s_add_u32 m0, %[ob], 106496\n\t" \
  "s_nop 0\n\t" \
  "buffer_load_dwordx4 v8, %[so], s72 offen lds\n\t" \
  "v_readlane_b32 s68, v9, 12\n\t" \
  "v_readlane_b32 s69, v9, 13\n\t" \
  "v_readlane_b32 s70, v9, 14\n\t" \
  "v_readlane_b32 s71, v9, 27\n\t" \
  "v_readlane_b32 s72, v9, 28\n\t" \
  "v_readlane_b32 s73, v9, 29\n\t" \
  "s_add_u32 m0, %[ob], 49152\n\t" \
  "s_nop 3\n\t" \
  "s_barrier\n\t" \
  "s_nop 2\n\t" \
  "buffer_load_dwordx4 v8, %[so], s68 offen lds\n\t" \
  "s_add_u32 m0, %[ob], 53248\n\t" \
  "s_nop 0\n\t" \
  "buffer_load_dwordx4 v8, %[so], s69 offen lds\n\t" \
  "s_add_u32 m0, %[ob], 57344\n\t" \
  "s_nop 0\n\t" \
  "buffer_load_dwordx4 v8, %[so], s70 offen lds\n\t" \
  "s_add_u32 m0, %[ob], 110592\n\t" \
  "s_nop 0\n\t" \
  "buffer_load_dwordx4 v8, %[so], s71 offen lds\n\t" \
  "s_add_u32 m0, %[ob], 114688\n\t" \
  "s_nop 0\n\t" \
  "buffer_load_dwordx4 v8, %[so], s72 offen lds\n\t" \
  "s_add_u32 m0, %[ob], 118784\n\t" \
  "s_nop 0\n\t" \
  "buffer_load_dwordx4 v8, %[so], s73 offen lds\n\t" \
  "v_readlane_b32 s68, v9, 15\n\t" \
  "v_readlane_b32 s69, v9, 16\n\t" \
  "v_readlane_b32 s70, v9, 17\n\t" \
  "v_readlane_b32 s71, v9, 30\n\t" \
  "v_readlane_b32 s72, v9, 31\n\t" \
  "s_add_u32 m0, %[ob], 61440\n\t" \
  "LS_%=:\n\t" \
  "ds_read_b32 v11, v10\n\t" \
  "s_addk_i32 %[cn], 1\n\t" \
  "s_cmp_gt_u32 %[cn], 0x400000\n\t" \
  "s_cbranch_scc1 LX_%=\n\t" \
  "s_waitcnt lgkmcnt(0)\n\t" \
  "v_cmp_eq_u32 vcc, 0, v11\n\t" \
  "s_cbranch_vccz LX_%=\n\t" \
  "s_sleep 1\n\t" \
  "s_branch LS_%=\n\t" \
  "LX_%=:\n\t" \
  "s_nop 2\n\t" \
  "buffer_load_dwordx4 v8, %[so], s68 offen lds\n\t" \
  "s_add_u32 m0, %[ob], 65536\n\t" \
  "s_nop 0\n\t" \
  "buffer_load_dwordx4 v8, %[so], s69 offen lds\n\t" \
  "s_add_u32 m0, %[ob], 69632\n\t" \
  "s_nop 0\n\t" \
  "buffer_load_dwordx4 v8, %[so], s70 offen lds\n\t" \
  "s_add_u32 m0, %[ob], 122880\n\t" \
  "s_nop 0\n\t" \
  "buffer_load_dwordx4 v8, %[so], s71 offen lds\n\t" \
  "s_add_u32 m0, %[ob], 126976\n\t" \
  "s_nop 0\n\t" \
  "buffer_load_dwordx4 v8, %[so], s72 offen lds\n\t" \
  "s_waitcnt vmcnt(0) lgkmcnt(0)\n\t"
#define LD_CLOB "v11", "s68", "s69", "s70", "s71", "s72", "s73", "s74", "s75", "s76", "s77", "s78", "s79", "s80", "s81", "s82", "s83", "s84", "s85"

#define ENDW
#define TS(i)
#define TSS(i)
#define TOUT(n)

#define SPIN(cond,slp) do{ int sp_=0; while(cond){ __builtin_amdgcn_s_sleep(slp); if(++sp_>(1<<22)) break; } }while(0)
__device__ __forceinline__ unsigned rd1(const char* p){ return __builtin_amdgcn_readfirstlane(*(volatile LDS_AS unsigned*)(LDS_AS char*)p); }
__device__ __forceinline__ unsigned lds_inc(const char* p, int l){ unsigned r=0; if(l==0) r=__hip_atomic_fetch_add((LDS_AS unsigned*)(LDS_AS char*)p, 1u, __ATOMIC_RELAXED, __HIP_MEMORY_SCOPE_WORKGROUP); return __builtin_amdgcn_readfirstlane(r); }
__device__ __forceinline__ void lds_add(unsigned adr){ asm volatile("s_mov_b64 exec, 1\n\tds_add_u32 %0, %1\n\ts_mov_b64 exec, -1" :: "v"(adr), "v"(1u) : "memory"); }
__device__ __forceinline__ void bars(int n){
  asm volatile("BL_%=:\n\ts_barrier\n\ts_sub_u32 %0, %0, 1\n\ts_cmp_lg_u32 %0, 0\n\ts_cbranch_scc1 BL_%=" : "+s"(n) :: "memory", "scc");
}
#define barc(rem) barc_(rem)
#define CTRA
#define CTRP
__device__ __forceinline__ void barc_(unsigned& rem){
  asm volatile("s_cmp_eq_u32 %0, 0\n\ts_cbranch_scc1 BC_%=\n\ts_barrier\n\ts_sub_u32 %0, %0, 1\n\tBC_%=:" : "+s"(rem) :: "memory", "scc");
}
template<int SLP> __device__ __forceinline__ void wait_flag_b(const unsigned* p, unsigned voff, unsigned tag, unsigned& rem CTRA){
  int spins=0;
  while(true){
    unsigned v; asm volatile("global_load_dword %0, %1, %2 sc1" : "=v"(v) : "v"(voff), "s"(p) : "memory");
    barc(rem); if(PB2) barc(rem);
    asm volatile("s_waitcnt vmcnt(0)" : "+v"(v) :: "memory");
    if(__all(v==tag) || ++spins>(1<<21)) break;
    if(rem==0) __builtin_amdgcn_s_sleep(SLP);
  }
}
__device__ __forceinline__ void stout(void* sbase, unsigned voff, u32x4 v){
  st16cs(sbase,voff,v);
}

typedef unsigned u32x8 __attribute__((ext_vector_type(8)));
typedef unsigned u32x16 __attribute__((ext_vector_type(16)));
__device__ __forceinline__ char* ptr64(unsigned lo, unsigned hi){ return (char*)(((unsigned long long)hi<<32)|lo); }
__device__ __forceinline__ u32x4 srd(const char* p, unsigned bytes){ return u32x4{(unsigned)(size_t)p,(unsigned)((size_t)p>>32)&0xffffu,bytes,0x20000u}; }
constexpr size_t WS_O=0, WS_KV=(size_t)S*DM*2, WS_Y=(size_t)S*DM*4, WS_FL=(size_t)S*DM*12;

__global__ __launch_bounds__(768,3) void fused_kernel(const u16* x_, const u16* wqkv_, const u16* wo_, char* ws_, u16* out_,
    const u16* kin_, const u16* vin_, const u16* shk_, const u16* hk_, const u16* qin_, const u16* sD_)
{
  __shared__ __attribute__((aligned(128))) char smem[ALL_LDS];
  TS(0);
  const auto ka=__builtin_amdgcn_kernarg_segment_ptr();
  unsigned long long pc; asm volatile("s_getpc_b64 %0" : "=s"(pc));
  const int w8=__builtin_amdgcn_readfirstlane(threadIdx.x>>6);
  #define ROLE u32x8 a0; int bid=blockIdx.x; asm volatile("s_load_dwordx8 %0, %2, 0x0" : "=&s"(a0), "+s"(bid) : "s"(ka)); \
    const int w=w8&3, xcd=bid&7, idx=bid>>3, rb=(xcd>>1)*4+(idx&3), hp=(xcd&1)*8+(idx>>2), s0=rb*128; (void)s0; (void)hp;
  #define LANEVARS int l; asm volatile("v_mbcnt_lo_u32_b32 %0, -1, 0\n\tv_mbcnt_hi_u32_b32 %0, -1, %0" : "=v"(l)); const int g=l>>4, fr=l&15, tq=(l&15)>>2, tp=l&3; (void)g; (void)fr; (void)tq; (void)tp;
  #define ARGS0 asm volatile("s_waitcnt lgkmcnt(0)" : "+s"(a0)); const char* x=ptr64(a0[0],a0[1]); const char* wqkv=ptr64(a0[2],a0[3]); const char* wo=ptr64(a0[4],a0[5]); char* ws=ptr64(a0[6],a0[7]); \
    unsigned* flags=(unsigned*)(ws+WS_FL); const int hot0=(3-(int)(a0[2]>>7))&7; (void)x; (void)wqkv; (void)wo; (void)flags; (void)hot0;
  #define ORD(c) ((((((c)<14)? ((unsigned)(((hp-hot0-1)&7)+1+((c)>>1))%7u) : 7u)+hot0+1)&7u)+8u*(((hp>>3)^(c)^1u)&1u))
  const unsigned sb0=__builtin_amdgcn_readfirstlane(lds_addr(smem));
  const char* syn=smem+SYN;

  if(__builtin_expect((w8>>2)==1,1)){
    ROLE
    LANEVARS
    __builtin_amdgcn_s_setprio(LPR);
    unsigned ltr=0, ltr2=0, ltr3=0; (void)ltr; (void)ltr2; (void)ltr3;
    const int xr=18*w+(l>>3);
    const unsigned ch=(unsigned)(((l&7)^(xr&7))*16);
    unsigned xo=(unsigned)(xr*2048)+ch;
    unsigned xoA=(s0==0 && xr<16)? 16*2048+ch : xo, xoB=((s0==0 && xr+8<16)? 8*2048+ch : xo)+15360;
    unsigned xo1=xo+15360, xo2=xo+30720;
    unsigned wof[3];
    #pragma unroll
    for(int i=0;i<3;i++){ const int P=3*w+i, wr=8*(P&3)+(l>>3); wof[i]=(unsigned)(wr*6144+(P>>2)*2048-1024*i+((((l&7)>>1)^wswz(wr))*32)+(l&1)*16); }
    unsigned xl=sb0+X_OFF+w*2304, wl=sb0+w*3072, xd=(unsigned)(s0-16)*2048u, wd=hp*128, hq=hp&7, rq=rb&3;
    asm volatile("" : "+{v0}"(xoA), "+{v1}"(xoB), "+{v2}"(xo), "+{v3}"(xo1), "+{v4}"(xo2), "+{v5}"(wof[0]), "+{v6}"(wof[1]), "+{v7}"(wof[2]), "+s"(xl), "+s"(wl), "+s"(xd), "+s"(wd), "+s"(hq), "+s"(rq));
    TSS(2);
    ARGS0
    const u32x4 sx=srd(x+(int)xd,144u*2048u), sw=srd(wqkv+wd,6291456u-wd);
    const int jj=(ROT && (int)hq==hot0)? rq : 0;
    TSS(3);
    asm volatile(LD_ASM1
      :: "{v0}"(xoA), "{v1}"(xoB), "{v2}"(xo), "{v3}"(xo1), "{v4}"(xo2), "{v5}"(wof[0]), "{v6}"(wof[1]), "{v7}"(wof[2]),
        [sx]"s"(sx), [sw]"s"(sw), [xb]"s"(xl), [wb]"s"(wl), [rx0]"s"(jj*128), [rw0]"s"(jj*393216)
      : "memory", "scc", "s68");
    {
      const unsigned wof2=(unsigned)(w*16384+(l>>3)*2048+((((l&7)>>1)^((l>>4)&3))*32)+(l&1)*16);
      const unsigned tab=ORD((unsigned)(l>>1)&15u)*131072u+(l&1)*65536;
      unsigned long long tm; asm("s_cmp_eq_u32 %1, 0\n\ts_cselect_b64 %0, %2, 0" : "=s"(tm) : "s"(w), "s"(WTL==2? 0xffffffffull : 0ull) : "scc");
      unsigned cn=0;
      asm volatile(LD_ASM2
        : [cn]"+s"(cn)
        : "{v0}"(xoA), "{v1}"(xoB), "{v2}"(xo), "{v3}"(xo1), "{v4}"(xo2), "{v5}"(wof[0]), "{v6}"(wof[1]), "{v7}"(wof[2]), "{v8}"(wof2), "{v9}"(tab),
          "{v10}"(sb0+SYN+32+(l&3)*4), "{v12}"((l&31)*2048),
          [sx]"s"(sx), [sw]"s"(sw), [so]"s"(srd(wo+wd,2097152u-wd)), [xb]"s"(xl), [wb]"s"(wl), [ob]"s"(sb0+w*1024),
          [rx0]"s"(jj*128), [rx1]"s"(((jj+1)&3)*128), [rx2]"s"(((jj+2)&3)*128), [rx3]"s"(((jj+3)&3)*128),
          [rw0]"s"(jj*393216), [rw1]"s"(((jj+1)&3)*393216), [rw2]"s"(((jj+2)&3)*393216), [rw3]"s"(((jj+3)&3)*393216),
          [pw]"s"(wqkv+idx*6144+(xcd&1)*1024+(xcd>>1)*256+w*64), [px]"s"(x+(xcd*256+idx*8+w*2)*2048), [po]"s"(wo+(bid*4+w)*2048),
          [tsk]"s"(sb0+SINK+256), [tm]"s"(tm), [tp]"s"(wo+idx*65536+(xcd&1)*1024+((3-(int)(a0[4]>>7))&7)*128)
        : "memory", "scc", "vcc", LD_CLOB);
    }
    const unsigned tag=rd1(syn+64)+1u;
    lds_add(sb0+SYN+12);
    TS(2);
    if(w==3){
      const unsigned* fp=flags+F_O+(rb*16+hp)*16; const unsigned fo=ORD((unsigned)l&15u)*4u;
      const unsigned madr=sb0+(l==0? SYN+20 : SINK+l*4);
      SPIN(rd1(syn+12)<4u,1);
      unsigned rdy=0; int spins=0;
      while(rdy!=0xFFFFu){
        unsigned v;
        asm volatile("s_mov_b64 exec, 0xffff\n\tglobal_atomic_or %0, %1, %3, %2 sc0 sc1\n\ts_mov_b64 exec, -1\n\ts_waitcnt vmcnt(0)" : "=&v"(v) : "v"(fo), "s"(fp), "v"(0u) : "memory");
        unsigned r=(unsigned)__ballot(v==tag)&0xFFFFu;
        if(++spins>(1<<21)) r=0xFFFFu;
        if(r!=rdy){ PUB(madr,(unsigned)__builtin_ctz(~r)); rdy=r; }
        else __builtin_amdgcn_s_sleep(OSLP);
      }
      TS(3);
      if(bid==0){
        int sp=0;
        while(true){
          u32x4 v; asm volatile("global_load_dwordx4 %0, %1, %2 sc1\n\ts_waitcnt vmcnt(0)" : "=&v"(v) : "v"(l*16), "s"(flags+F_Y) : "memory");
          if(__all(v[0]==tag && v[1]==tag && v[2]==tag && v[3]==tag) || ++sp>(1<<20)) break;
          __builtin_amdgcn_s_sleep(12);
        }
        if(l==0) stflag(flags, tag);
      }
    } else { TS(3); }
    TOUT(4);
    return;
  }

  if(w8<4){
    ROLE
    u32x2 a1; asm volatile("s_load_dwordx2 %0, %1, 0x20" : "=&s"(a1) : "s"(ka));
    __builtin_amdgcn_s_setprio(GPR);
    const int h=w&1, r=w>>1;
    f32x16 e0, e1;
    LANEVARS
    {
      const unsigned ln=(idx*4+w)*3+l;
      if(CODEL && l<3 && ln<CODEL) touch((const void*)(pc&~127ull), ln*128, sb0+SINK+256);
    }
    constexpr int nx=TPX? TPX:1;
    const int q=w*64+l, q2=(3-w)*64+l, e=q2%12+(idx&1)*12;
    unsigned tx=(q/nx)*2048+(q%nx)*128+(xcd*256+idx*8)*2048, tw=(q2/12)*393216+(e>>3)*2048+(e&7)*128+((xcd>>1)*16+(idx>>1))*6144+(xcd&1)*1024;
    asm volatile("" : "+v"(tx), "+v"(tw));
    TSS(3);
    ARGS0
    asm volatile("" : "+s"(a1));
    if(w*64<8*TPX){ if(q<8*TPX) touch(x, tx, sb0+SINK+256); }
    if((3-w)*64<12*TPW){ if(q2<12*TPW) touch(wqkv, tw, sb0+SINK+512); }
    if(TWO && w==2) touch(wo+bid*8192, l*128, sb0+SINK+512);
    TSS(4);
    {
      unsigned lx0=sb0+X_OFF+fr*128+((g^(fr&7))*16), lx1=sb0+X_OFF+fr*128+(((4+g)^(fr&7))*16);
      const int sw=wswz(8*g+tq);
      unsigned lw0=sb0+(8*g+tq)*128+(unsigned)((((2*h)^sw)*32)+8*tp), lw1=sb0+(8*g+tq)*128+(unsigned)((((2*h+1)^sw)*32)+8*tp);
      char* ot=ws+WS_O+(rb*16+hp)*16384+r*8192+h*2048;
      unsigned it; unsigned long long rm; asm("s_cmp_lg_u32 %1, 0\n\ts_cselect_b64 %0, -1, 0" : "=s"(rm) : "s"(r) : "scc");
      asm volatile(N2_ASM
        : [it]"=&s"(it), "+{v56}"(lx0), "+{v57}"(lx1), "+{v58}"(lw0), "+{v59}"(lw1)
        : "{v62}"(l*16), "{v63}"(sb0+EX_OFF+h*3072+l*16), [r]"s"(r), [ot]"s"(ot), [ou]"s"(ot+4096), [fa]"s"(sb0+SYN+32+w*4), [fp]"s"(sb0+SYN+32+(w^2)*4),
          [vo]"s"(2048-1024*r), [rm]"s"(rm), [z]"s"(rb|r)
        : "memory", "scc", "vcc", N2_CLOB);
    }
    TS(3);
    unsigned tb;
    {
      LANEVARS
      const unsigned tag=rd1(syn+64)+1u;
      const unsigned ra=sb0+(4*g+tq)*128+8*tp; const int sw=(2*g+(tq>>1))&3;
      unsigned b0=ra+((0^sw)*32), b1=ra+((1^sw)*32), b2=ra+((2^sw)*32), b3=ra+((3^sw)*32), vo=(unsigned)(w*4096+l*16);
      tb=ORD((unsigned)l&15u)*16384u;
      asm volatile("" : "+{v160}"(b0), "+{v161}"(b1), "+{v162}"(b2), "+{v163}"(b3), "+{v164}"(vo), "+{v167}"(tb));
      WAITV(0);
      if(lds_inc(syn+16,l)==3u){ if(l<16) stflag(flags+F_O+(rb*16+l)*16+hp, tag); }
      TS(4);
      __builtin_amdgcn_s_setprio(G2P);
      unsigned c=0, iss=0, th, q0, q1, cn=0;
      asm volatile(G3_ASM
        : "={v[128:143]}"(e0), "={v[144:159]}"(e1), [c]"+s"(c), [iss]"+s"(iss), [th]"=&s"(th), [t0]"=&s"(q0), [t1]"=&s"(q1), [cn]"+s"(cn),
          "+{v160}"(b0), "+{v161}"(b1), "+{v162}"(b2), "+{v163}"(b3), "+{v167}"(tb)
        : "{v164}"(vo), "{v165}"(sb0+SYN+20), [go]"s"(60+w*4), [ob]"s"(srd(ws+WS_O+rb*262144,262144u))
        : "memory", "scc", "vcc", G3_CLOB);
    }
    {
      LANEVARS
      char* sg=smem+EX_OFF+w*2048;
      char* op=ptr64(a1[0],a1[1])+(s0+w*32)*4096+2048+hp*128; const unsigned oo=(unsigned)((l>>3)*4096+(l&7)*16);
      u32x4 ov[4];
      #pragma unroll
      for(int t=0;t<2;t++){
        #pragma unroll
        for(int j=0;j<4;j++){
          #define E2(i) (t? e1[(i)&15] : e0[(i)&15])
          *(volatile LDS_AS u32x2*)(LDS_AS char*)(sg+fr*128+(((2*j+(g>>1))^(fr&7))*16)+(g&1)*8)=u32x2{pack2(E2(4*j),E2(4*j+1)),pack2(E2(4*j+2),E2(4*j+3))};
        }
        #pragma unroll
        for(int i=0;i<2;i++) ov[2*t+i]=*(volatile LDS_AS u32x4*)(LDS_AS char*)(sg+(i*8+(l>>3))*128+(((l&7)^(l>>3))*16));
      }
      #pragma unroll
      for(int i=0;i<4;i++) stout(op+i*32768, oo, ov[i]);
      ENDW TS(6); TOUT(7);
    }
    return;
  }

  {
    ROLE
    u32x8 a1; u32x4 a2; u32x2 a3;
    asm volatile("s_load_dwordx8 %0, %3, 0x20\n\ts_load_dwordx4 %1, %3, 0x40\n\ts_load_dwordx2 %2, %3, 0x50" : "=&s"(a1), "=&s"(a2), "=&s"(a3) : "s"(ka));
    char* cr=smem+C_OFF+2048+w*4096;
    const unsigned crs=sb0+C_OFF+2048+w*4096;
    const int sblk=bid>>3, td0=(bid&7)*128+(w&1)*64;
    const int d=(bid>>4)*64+(((bid>>3)&1)*4+w)*8+(bid&7);
    const int kv0=KV0+(sblk&KVS);
    static_assert(KV0+KVS+PP_NB<=33, "barrier budget");
    unsigned tagv, ctr=0; f32x8 RB;
    const int fd0=(bid&15)*64, fs0=(bid>>4)*128+w*32;
    u32x4 qv[4];
    f32x16 A0,A1,A2,A3;
    const int rw=sblk*64+(w>>1)*32, j=rw>>5;
    unsigned k0,k1,k2,ko,ta,se,so,ha,l16,rb0[2];
    {
      LANEVARS
      if(w==0 && l<25 && l!=16) ((unsigned*)(smem+SYN))[l]=0u;
      if(l<32) *(u32x4*)(smem+C_OFF+w*512+l*16)=u32x4{0,0,0,0};
      asm volatile("" ::: "memory");
      __builtin_amdgcn_s_setprio(CPR);
      const int dg=l&7, rg=l>>3;
      ko=(unsigned)(rg*8192+dg*16);
      const bool top=(rw==0 && rg==0);
      k0=top? ko+6144:ko; k1=top? ko+6144:ko+2048; k2=top? ko+6144:ko+4096;
      ta=sb0+EX_OFF+w*1024+(w&1)*512+dg*64; se=(l&8)? 0x03020706u : 0x05040100u; ha=crs+(32*g-4*tq+4*tp)*2; l16=l*16;
      so=(unsigned)(rg*32768+((j>>2)&3)*128+((dg&4)|((dg+j)&3))*16);
      #pragma unroll
      for(int k=0;k<2;k++){ const int q=fr-1-2*g+k; rb0[k]=sb0+C_OFF+(4+(q>>1))*256+((q>>3)&3)*64+((w+(q>>1))&3)*16+(q&1)*8; }
      asm volatile("" : "+{v152}"(k0), "+{v153}"(k1), "+{v154}"(k2), "+{v155}"(ko), "+{v156}"(ta), "+{v157}"(se), "+{v159}"(so), "+{v163}"(l16), "+{v165}"(ha), "+{v166}"(rb0[0]), "+{v167}"(rb0[1]));
    }
    TSS(3);
    ARGS0
    asm volatile("" : "+s"(a1), "+s"(a2), "+s"(a3));
    char* out=ptr64(a1[0],a1[1]); const char* qp=ptr64(a2[2],a2[3])+(fs0*DM+fd0)*2;
    {
      const char* kb=ptr64(a1[2],a1[3])+(rw*DM+td0)*2; const char* vb=ptr64(a1[4],a1[5])+(rw*DM+td0)*2;
      unsigned long long zm; asm("s_cmp_eq_u32 %1, 0\n\ts_cselect_b64 %0, 0xff, 0" : "=s"(zm) : "s"(rw) : "scc");
      unsigned t0;
      TSS(5);
      asm volatile(PP_ASM
        : [t0]"=&s"(t0), "+{v164}"(ctr), "+{v159}"(so), "=&{v160}"(tagv), "=&{v[24:31]}"(RB), "=&{v[44:47]}"(qv[0]), "=&{v[48:51]}"(qv[1]), "=&{v[52:55]}"(qv[2]), "=&{v[56:59]}"(qv[3]),
          "=&{v[64:79]}"(A0), "=&{v[80:95]}"(A1), "=&{v[96:111]}"(A2), "=&{v[112:127]}"(A3)
        : "{v152}"(k0), "{v153}"(k1), "{v154}"(k2), "{v155}"(ko), "{v156}"(ta), "{v157}"(se), "{v166}"(rb0[0]), "{v167}"(rb0[1]), "{v165}"(ha), "{v163}"(l16),
          [qb0]"s"(qp), [qb2]"s"(qp+4096), [mk]"s"(0x180u), [em]"s"(1ull|(512ull<<w)), [hd]"s"(crs),
          [kb0]"s"(kb-6144), [kb3]"s"(kb), [kb5]"s"(kb+4096), [vb0]"s"(vb), [vb2]"s"(vb+4096), [sb]"s"(ws+WS_KV+td0*4096+j*512),
          [n0]"s"(kv0), [ff]"s"(flags+F_PP+(td0>>6)*64+j), [zm]"s"(zm), [shd]"s"(sb0+EX_OFF+w*1024), [tgd]"s"(sb0+SYN+64), [shp]"s"(ptr64(a1[6],a1[7])+(bid&7)*1024), [fl]"s"(flags),
          [hp]"s"(ptr64(a2[0],a2[1])+d*4096), [dp]"s"(ptr64(a3[0],a3[1])+d*2)
        : "memory", "scc", PP_CLOB);
    }
    LANEVARS
    const unsigned tag=__builtin_amdgcn_readfirstlane(tagv)+1u;
    unsigned rem=33-PP_NB-kv0;
    barc(rem);
    const unsigned* pf=flags+F_PP+(d>>6)*64;
    const char* kvp=ws+WS_KV+((bid>>4)*8+(bid&7))*32768+((bid>>3)&1)*64+w*8192;
    const unsigned kvo=(unsigned)((l>>2)*128+(l&3)*16);
    int bidx=((l>=16)? l-16 : (l==0? 63 : l+47))*4;
    asm volatile("" : "+{v32}"(bidx));
    __builtin_amdgcn_s_setprio(3);
    while(rem>PREM) barc(rem);
    wait_flag_b<8>(pf, l*4, tag, rem CTRP);
    #pragma unroll
    for(int i=0;i<4;i++){ if(i%KTH==0 && i) barc(rem); gldsc(kvp+i*2048, kvo, crs+i*1024); }
    barc(rem); barc(rem);
    WAITV(0);
    asm volatile("s_mov_b32 m0, %2\n\ts_mov_b64 exec, 1\n\tglobal_load_lds_dword %0, %1 sc1\n\ts_mov_b64 exec, -1" :: "v"(0u), "s"(pf), "s"(sb0+SYN+100+w*4) : "memory");
    while(rem) barc(rem);
    { int sp=0; while(!__all(*(volatile LDS_AS unsigned*)(LDS_AS char*)(syn+100+(l&3)*4)==tag) && ++sp<(1<<22)) __builtin_amdgcn_s_sleep(1); }
    __builtin_amdgcn_s_setprio(CVP);
    TS(3);
    f32x16 ac0, ac1;
    {
      unsigned p0,p1,p2,p3,p4,p5,p6;
      asm volatile("s_nop 1\n\t" CV_ASM
        : "=&{v[128:143]}"(ac0), "=&{v[144:159]}"(ac1), [p0]"=&s"(p0), [p1]"=&s"(p1), [p2]"=&s"(p2), [p3]"=&s"(p3), [p4]"=&s"(p4), [p5]"=&s"(p5), [p6]"=&s"(p6)
          , "+{v[44:47]}"(qv[0]), "+{v[48:51]}"(qv[1]), "+{v[52:55]}"(qv[2]), "+{v[56:59]}"(qv[3])
        : "{v[64:79]}"(A0), "{v[80:95]}"(A1), "{v[96:111]}"(A2), "{v[112:127]}"(A3),
          "{v[24:31]}"(RB), "{v32}"(bidx), "{v33}"(sb0+SYN+80+w*4)
        : "memory", "scc", CV_CLOB);
    }
    TS(4);
    __builtin_amdgcn_s_setprio(3);
    {
      const unsigned yo=(unsigned)((256*(g&1)+16*fr+4*(g&2))*2);
      #pragma unroll
      for(int J=0;J<8;J+=2){
        #define YA(i) ((i)<16? ac0[(i)&15] : ac1[(i)&15])
        const u32x2 a={pack2(YA(4*J),YA(4*J+1)),pack2(YA(4*J+2),YA(4*J+3))}, b={pack2(YA(4*J+4),YA(4*J+5)),pack2(YA(4*J+6),YA(4*J+7))};
        st16cs(ws+WS_Y+d*4096+512*J, yo, widen(a,b));
      }
    }
    WAITV(0);
    if(lds_inc(syn+24,l)==3u){ if(l<16) stflag(flags+F_Y+(l*16+(bid>>4))*16+(bid&15), tag); }
    TS(5);
    if(w==0){
      SPIN(rd1(syn+96)==0u,2);
      wait_flag<YSLP>(flags+F_Y+bid*16+(l&15), tag);
      SPIN(rd1(syn+24)<4u,1);
      PUB(sb0+(l==0? SYN+28 : SINK+l*4), 1u);
    } else SPIN(rd1(syn+28)==0u,4);
    #pragma unroll
    for(int i=0;i<4;i++) gldsc(ws+WS_Y+(fd0+i*16)*4096+fs0*2, (l>>2)*4096+(l&3)*16, crs+i*1024);
    WAITV(0);
    {
      u32x4 y2[4];
      #pragma unroll
      for(int i=0;i<4;i++){ const char* yp=cr+((i>>1)*32+g*8+tq)*64+(i&1)*32+tp*8; y2[i]=__builtin_bit_cast(u32x4,cat44(trread(yp),trread(yp+256))); }
      asm volatile("" : "+v"(y2[0]), "+v"(y2[1]), "+v"(y2[2]), "+v"(y2[3]) :: "memory");
      #pragma unroll
      for(int i=0;i<4;i++) *(u32x4*)(cr+((i&1)*16+fr)*128+((((i>>1)*4+g)^(fr&7))*16))=y2[i];
      asm volatile("" ::: "memory");
      char* op=out+fs0*4096+fd0*2; const unsigned oo=(unsigned)((l>>3)*16384+(l&7)*16);
      #pragma unroll
      for(int i=0;i<4;i++){
        const int row=4*(l>>3)+i;
        const u32x4 y4=*(const u32x4*)(cr+row*128+(((l&7)^(row&7))*16)), q4=qv[i]; u32x4 ov;
        #pragma unroll
        for(int e=0;e<4;e++) ov[e]=pack2(bflo(q4[e])*bflo(y4[e]), bfhi(q4[e])*bfhi(y4[e]));
        stout(op+i*4096, oo, ov);
      }
    }
    ENDW TS(6); TOUT(7);
  }
}

extern "C" void kernel_launch(void* const* d_in, const int* in_sizes, int n_in, void* d_out, int out_size, void* d_ws, size_t ws_size, hipStream_t stream){
  const u16* q=(const u16*)d_in[0]; const u16* k=(const u16*)d_in[1]; const u16* v=(const u16*)d_in[2];
  const u16* shk=(const u16*)d_in[3]; const u16* hk=(const u16*)d_in[4]; const u16* sD=(const u16*)d_in[5];
  const u16* x=(const u16*)d_in[6]; const u16* wqkv=(const u16*)d_in[7]; const u16* wo=(const u16*)d_in[8];
  fused_kernel<<<256,768,0,stream>>>(x,wqkv,wo,(char*)d_ws,(u16*)d_out,k,v,shk,hk,q,sD);
}
```

```cpp
#include <hip/hip_runtime.h>
#include <hip/hip_bf16.h>
#include <stdint.h>

typedef unsigned short u16;
typedef short bf16x8 __attribute__((ext_vector_type(8)));
typedef short s16x4 __attribute__((ext_vector_type(4)));
typedef float f32x2 __attribute__((ext_vector_type(2)));
typedef float f32x4 __attribute__((ext_vector_type(4)));
typedef float f32x8 __attribute__((ext_vector_type(8)));
typedef float f32x16 __attribute__((ext_vector_type(16)));
typedef unsigned u32x4 __attribute__((ext_vector_type(4)));
typedef unsigned u32x2 __attribute__((ext_vector_type(2)));
typedef __bf16 bf16x2_t __attribute__((ext_vector_type(2)));
#define LDS_AS __attribute__((address_space(3)))

__device__ __forceinline__ float bflo(unsigned x){ return __uint_as_float(x<<16); }
__device__ __forceinline__ float bfhi(unsigned x){ return __uint_as_float(x&0xffff0000u); }
__device__ __forceinline__ unsigned pack2(float lo, float hi){ return __builtin_bit_cast(unsigned, __builtin_convertvector(f32x2{lo,hi}, bf16x2_t)); }
__device__ __forceinline__ float mul1(float a, float b){ float r; asm("v_mul_f32 %0, %1, %2" : "=v"(r) : "v"(a), "v"(b)); return r; }
__device__ __forceinline__ float fma1(float a, float b, float c){ float r; asm("v_fma_f32 %0, %1, %2, %3" : "=v"(r) : "v"(a), "v"(b), "v"(c)); return r; }

__device__ __forceinline__ void glds(const void* sbase, unsigned voff, unsigned ldsaddr){
  asm volatile("s_mov_b32 m0, %2\n\ts_nop 0\n\tglobal_load_lds_dwordx4 %0, %1" :: "v"(voff), "s"(sbase), "s"(ldsaddr) : "memory");
}
__device__ __forceinline__ void gldsc(const void* sbase, unsigned voff, unsigned ldsaddr){
  asm volatile("s_mov_b32 m0, %2\n\ts_nop 0\n\tglobal_load_lds_dwordx4 %0, %1 sc1" :: "v"(voff), "s"(sbase), "s"(ldsaddr) : "memory");
}
__device__ __forceinline__ void touch(const void* sbase, unsigned voff, unsigned ldsaddr){
  asm volatile("s_mov_b32 m0, %2\n\ts_nop 0\n\tglobal_load_lds_dword %0, %1" :: "v"(voff), "s"(sbase), "s"(ldsaddr) : "memory");
}
__device__ __forceinline__ void st16c(void* p, u32x4 v){ asm volatile("global_store_dwordx4 %0, %1, off sc1\n\ts_nop 1" :: "v"(p), "v"(v) : "memory"); }
__device__ __forceinline__ void st16cs(void* sbase, unsigned voff, u32x4 v){ asm volatile("global_store_dwordx4 %0, %1, %2 sc1\n\ts_nop 1" :: "v"(voff), "v"(v), "s"(sbase) : "memory"); }
#define GLB_AS __attribute__((address_space(1)))
__device__ __forceinline__ unsigned ldflag(const unsigned* p){ return __hip_atomic_load((const GLB_AS unsigned*)p, __ATOMIC_RELAXED, __HIP_MEMORY_SCOPE_AGENT); }
__device__ __forceinline__ void stflag(unsigned* p, unsigned v){ __hip_atomic_store((GLB_AS unsigned*)p, v, __ATOMIC_RELAXED, __HIP_MEMORY_SCOPE_AGENT); }
template<int SLP> __device__ __forceinline__ void wait_flag(const unsigned* p, unsigned tag){
  int spins=0;
  while(true){ unsigned v=ldflag(p); if(__all(v==tag)) break; __builtin_amdgcn_s_sleep(SLP); if(++spins>(1<<21)) break; }
}
__device__ __forceinline__ unsigned lds_addr(const void* p){ return (unsigned)(size_t)(const LDS_AS void*)p; }
#define WAITV(n) asm volatile("s_waitcnt vmcnt(" #n ")":::"memory")
#define WAITL() asm volatile("s_waitcnt lgkmcnt(0)":::"memory")
#define SBAR() asm volatile("s_barrier":::"memory")
#define PUB(addr,val) asm volatile("ds_write_b32 %0, %1" :: "v"(addr), "v"(val) : "memory")
__device__ __forceinline__ s16x4 trread(const char* p){ return __builtin_amdgcn_ds_read_tr16_b64_v4i16((LDS_AS s16x4*)p); }
__device__ __forceinline__ bf16x8 cat44(s16x4 a, s16x4 b){ return __builtin_shufflevector(a,b,0,1,2,3,4,5,6,7); }
__device__ __forceinline__ int wswz(int row){ return ((row>>1)&1) | (((row>>3)&1)<<1); }
__device__ __forceinline__ u32x4 widen(u32x2 a, u32x2 b){
  auto r0=__builtin_amdgcn_permlane16_swap(a[0],b[0],false,false);
  auto r1=__builtin_amdgcn_permlane16_swap(a[1],b[1],false,false);
  return u32x4{r0[0],r1[0],r0[1],r1[1]};
}

constexpr int S=2048, DM=1024;
constexpr int WS=12288, XS=18432, X_OFF=6*WS;
constexpr int CREG=4608, C_OFF=131072;
constexpr int EX_OFF=C_OFF+4*CREG;
constexpr int SYN=EX_OFF+8192;
constexpr int SINK=SYN+256;
constexpr int ALL_LDS=SINK+768;
constexpr int F_PP=1024, F_O=4096, F_Y=8192;
#define KV0 1
#define KVS 3
#define LPR 2
#define CPR 2
#define GPR 1
#define CGATE 0
#define OSLP 2
#define YSLP 8
#define TPX 16
#define TPW 2
#define TWO 0
#define CVP 1
#define G2P 3
#define OUTC 1
#define ROT 0
#define WTL 2
#define PB2 1
#define PREM 9
#define KTH 1
#define YDEF 3
#define OPOL 1
#define CODEL 280
#define MIX 0
#define LOCA 3
#define XLOC 1
#define PGT 0
#define PGO 0
#define HLAST 1

#define N2_ASM \
  "v_mov_b32 v64, 0\n\t" \
  "v_mov_b32 v65, 0\n\t" \
  "v_mov_b32 v66, 0\n\t" \
  "v_mov_b32 v67, 0\n\t" \
  "v_mov_b32 v68, 0\n\t" \
  "v_mov_b32 v69, 0\n\t" \
  "v_mov_b32 v70, 0\n\t" \
  "v_mov_b32 v71, 0\n\t" \
  "v_mov_b32 v72, 0\n\t" \
  "v_mov_b32 v73, 0\n\t" \
  "v_mov_b32 v74, 0\n\t" \
  "v_mov_b32 v75, 0\n\t" \
  "v_mov_b32 v76, 0\n\t" \
  "v_mov_b32 v77, 0\n\t" \
  "v_mov_b32 v78, 0\n\t" \
  "v_mov_b32 v79, 0\n\t" \
  "v_mov_b32 v80, 0\n\t" \
  "v_mov_b32 v81, 0\n\t" \
  "v_mov_b32 v82, 0\n\t" \
  "v_mov_b32 v83, 0\n\t" \
  "v_mov_b32 v84, 0\n\t" \
  "v_mov_b32 v85, 0\n\t" \
  "v_mov_b32 v86, 0\n\t" \
  "v_mov_b32 v87, 0\n\t" \
  "v_mov_b32 v88, 0\n\t" \
  "v_mov_b32 v89, 0\n\t" \
  "v_mov_b32 v90, 0\n\t" \
  "v_mov_b32 v91, 0\n\t" \
  "v_mov_b32 v92, 0\n\t" \
  "v_mov_b32 v93, 0\n\t" \
  "v_mov_b32 v94, 0\n\t" \
  "v_mov_b32 v95, 0\n\t" \
  "v_mov_b32 v96, 0\n\t" \
  "v_mov_b32 v97, 0\n\t" \
  "v_mov_b32 v98, 0\n\t" \
  "v_mov_b32 v99, 0\n\t" \
  "v_mov_b32 v100, 0\n\t" \
  "v_mov_b32 v101, 0\n\t" \
  "v_mov_b32 v102, 0\n\t" \
  "v_mov_b32 v103, 0\n\t" \
  "v_mov_b32 v104, 0\n\t" \
  "v_mov_b32 v105, 0\n\t" \
  "v_mov_b32 v106, 0\n\t" \
  "v_mov_b32 v107, 0\n\t" \
  "v_mov_b32 v108, 0\n\t" \
  "v_mov_b32 v109, 0\n\t" \
  "v_mov_b32 v110, 0\n\t" \
  "v_mov_b32 v111, 0\n\t" \
  "v_mov_b32 v112, 0\n\t" \
  "v_mov_b32 v113, 0\n\t" \
  "v_mov_b32 v114, 0\n\t" \
  "v_mov_b32 v115, 0\n\t" \
  "v_mov_b32 v116, 0\n\t" \
  "v_mov_b32 v117, 0\n\t" \
  "v_mov_b32 v118, 0\n\t" \
  "v_mov_b32 v119, 0\n\t" \
  "v_mov_b32 v120, 0\n\t" \
  "v_mov_b32 v121, 0\n\t" \
  "v_mov_b32 v122, 0\n\t" \
  "v_mov_b32 v123, 0\n\t" \
  "v_mov_b32 v124, 0\n\t" \
  "v_mov_b32 v125, 0\n\t" \
  "v_mov_b32 v126, 0\n\t" \
  "v_mov_b32 v127, 0\n\t" \
  "v_mov_b32 v128, 0\n\t" \
  "v_mov_b32 v129, 0\n\t" \
  "v_mov_b32 v130, 0\n\t" \
  "v_mov_b32 v131, 0\n\t" \
  "v_mov_b32 v132, 0\n\t" \
  "v_mov_b32 v133, 0\n\t" \
  "v_mov_b32 v134, 0\n\t" \
  "v_mov_b32 v135, 0\n\t" \
  "v_mov_b32 v136, 0\n\t" \
  "v_mov_b32 v137, 0\n\t" \
  "v_mov_b32 v138, 0\n\t" \
  "v_mov_b32 v139, 0\n\t" \
  "v_mov_b32 v140, 0\n\t" \
  "v_mov_b32 v141, 0\n\t" \
  "v_mov_b32 v142, 0\n\t" \
  "v_mov_b32 v143, 0\n\t" \
  "v_mov_b32 v144, 0\n\t" \
  "v_mov_b32 v145, 0\n\t" \
  "v_mov_b32 v146, 0\n\t" \
  "v_mov_b32 v147, 0\n\t" \
  "v_mov_b32 v148, 0\n\t" \
  "v_mov_b32 v149, 0\n\t" \
  "v_mov_b32 v150, 0\n\t" \
  "v_mov_b32 v151, 0\n\t" \
  "v_mov_b32 v152, 0\n\t" \
  "v_mov_b32 v153, 0\n\t" \
  "v_mov_b32 v154, 0\n\t" \
  "v_mov_b32 v155, 0\n\t" \
  "v_mov_b32 v156, 0\n\t" \
  "v_mov_b32 v157, 0\n\t" \
  "v_mov_b32 v158, 0\n\t" \
  "v_mov_b32 v159, 0\n\t" \
  "v_mov_b32 v160, 0\n\t" \
  "v_mov_b32 v161, 0\n\t" \
  "v_mov_b32 v162, 0\n\t" \
  "v_mov_b32 v163, 0\n\t" \
  "v_mov_b32 v164, 0\n\t" \
  "v_mov_b32 v165, 0\n\t" \
  "v_mov_b32 v166, 0\n\t" \
  "v_mov_b32 v167, 0\n\t" \
  "s_mov_b32 s68, 0x50000\n\t" \
  "s_mov_b32 s69, 0x5550055\n\t" \
  "s_mov_b32 s70, 0xa0000\n\t" \
  "s_mov_b32 s71, 0xaaa00aa\n\t" \
  "s_mov_b32 s72, 0x150001\n\t" \
  "s_mov_b32 s73, 0x15550155\n\t" \
  "s_mov_b32 s74, 0x2a0002\n\t" \
  "s_mov_b32 s75, 0x2aaa02aa\n\t" \
  "s_mov_b32 s76, 0x55505555\n\t" \
  "s_mov_b32 s77, 0x50005500\n\t" \
  "s_mov_b32 s78, 0xaaa0aaaa\n\t" \
  "s_mov_b32 s79, 0xa000aa00\n\t" \
  "s_mov_b32 s80, 0x55405554\n\t" \
  "s_mov_b32 s81, 0x40005400\n\t" \
  "s_mov_b32 s82, 0xaa80aaa8\n\t" \
  "s_mov_b32 s83, 0x8000a800\n\t" \
  "s_mov_b32 s84, 0xaaaaaaaa\n\t" \
  "s_mov_b32 s85, 0xaaaaaaaa\n\t" \
  "v_add_u32 v60, 36864, v58\n\t" \
  "v_add_u32 v61, 36864, v59\n\t" \
  "s_mov_b32 %[it], 0\n\t" \
  "s_cmp_lg_u32 %[r], 0\n\t" \
  "s_barrier\n\t" \
  "s_cbranch_scc1 NB_%=\n\t" \
  "ds_read_b64_tr_b16 v[40:41], v60 offset:16384\n\t" \
  "ds_read_b64_tr_b16 v[42:43], v60 offset:16896\n\t" \
  "ds_read_b64_tr_b16 v[44:45], v60 offset:20480\n\t" \
  "ds_read_b64_tr_b16 v[46:47], v60 offset:20992\n\t" \
  "ds_read_b64_tr_b16 v[48:49], v61 offset:16384\n\t" \
  "ds_read_b64_tr_b16 v[50:51], v61 offset:16896\n\t" \
  "ds_read_b128 v[0:3], v56 offset:36864\n\t" \
  "ds_read_b128 v[4:7], v56 offset:38912\n\t" \
  "ds_read_b128 v[8:11], v56 offset:40960\n\t" \
  "ds_read_b128 v[12:15], v56 offset:43008\n\t" \
  "ds_read_b128 v[16:19], v56 offset:45056\n\t" \
  "s_waitcnt lgkmcnt(0)\n\t" \
  "s_branch N4a_%=\n\t" \
  "N0a_%=:\n\t" \
  "s_waitcnt lgkmcnt(4)\n\t" \
  "v_mfma_f32_16x16x32_bf16 v[160:163], v[40:43], v[0:3], v[160:163]\n\t" \
  "ds_read_b64_tr_b16 v[52:53], v59 offset:8192\n\t" \
  "v_mfma_f32_16x16x32_bf16 v[96:99], v[40:43], v[4:7], v[96:99]\n\t" \
  "ds_read_b64_tr_b16 v[54:55], v59 offset:8704\n\t" \
  "v_mfma_f32_16x16x32_bf16 v[104:107], v[40:43], v[8:11], v[104:107]\n\t" \
  "s_barrier\n\t" \
  "v_mfma_f32_16x16x32_bf16 v[112:115], v[40:43], v[12:15], v[112:115]\n\t" \
  "v_mfma_f32_16x16x32_bf16 v[120:123], v[40:43], v[16:19], v[120:123]\n\t" \
  "s_waitcnt lgkmcnt(4)\n\t" \
  "v_mfma_f32_16x16x32_bf16 v[128:131], v[4:7], v[44:47], v[128:131]\n\t" \
  "ds_read_b64_tr_b16 v[40:41], v58 offset:0\n\t" \
  "v_mfma_f32_16x16x32_bf16 v[136:139], v[8:11], v[44:47], v[136:139]\n\t" \
  "ds_read_b64_tr_b16 v[42:43], v58 offset:512\n\t" \
  "v_mfma_f32_16x16x32_bf16 v[144:147], v[12:15], v[44:47], v[144:147]\n\t" \
  "ds_read_b128 v[20:23], v57 offset:0\n\t" \
  "v_mfma_f32_16x16x32_bf16 v[152:155], v[16:19], v[44:47], v[152:155]\n\t" \
  "ds_read_b128 v[24:27], v57 offset:2048\n\t" \
  "s_waitcnt lgkmcnt(6)\n\t" \
  "v_mfma_f32_16x16x32_bf16 v[164:167], v[48:51], v[0:3], v[164:167]\n\t" \
  "ds_read_b64_tr_b16 v[44:45], v59 offset:0\n\t" \
  "v_mfma_f32_16x16x32_bf16 v[100:103], v[48:51], v[4:7], v[100:103]\n\t" \
  "ds_read_b64_tr_b16 v[46:47], v59 offset:512\n\t" \
  "v_mfma_f32_16x16x32_bf16 v[108:111], v[48:51], v[8:11], v[108:111]\n\t" \
  "ds_read_b128 v[28:31], v57 offset:4096\n\t" \
  "v_mfma_f32_16x16x32_bf16 v[116:119], v[48:51], v[12:15], v[116:119]\n\t" \
  "ds_read_b128 v[32:35], v57 offset:6144\n\t" \
  "v_mfma_f32_16x16x32_bf16 v[124:127], v[48:51], v[16:19], v[124:127]\n\t" \
  "s_waitcnt lgkmcnt(8)\n\t" \
  "v_mfma_f32_16x16x32_bf16 v[132:135], v[4:7], v[52:55], v[132:135]\n\t" \
  "ds_read_b64_tr_b16 v[48:49], v58 offset:16384\n\t" \
  "v_mfma_f32_16x16x32_bf16 v[140:143], v[8:11], v[52:55], v[140:143]\n\t" \
  "ds_read_b64_tr_b16 v[50:51], v58 offset:16896\n\t" \
  "v_mfma_f32_16x16x32_bf16 v[148:151], v[12:15], v[52:55], v[148:151]\n\t" \
  "ds_read_b128 v[36:39], v57 offset:8192\n\t" \
  "v_mfma_f32_16x16x32_bf16 v[156:159], v[16:19], v[52:55], v[156:159]\n\t" \
  "s_waitcnt lgkmcnt(9)\n\t" \
  "v_mfma_f32_16x16x32_bf16 v[64:67], v[40:43], v[4:7], v[64:67]\n\t" \
  "ds_read_b64_tr_b16 v[52:53], v58 offset:20480\n\t" \
  "v_mfma_f32_16x16x32_bf16 v[72:75], v[40:43], v[8:11], v[72:75]\n\t" \
  "ds_read_b64_tr_b16 v[54:55], v58 offset:20992\n\t" \
  "v_mfma_f32_16x16x32_bf16 v[80:83], v[40:43], v[12:15], v[80:83]\n\t" \
  "v_mfma_f32_16x16x32_bf16 v[88:91], v[40:43], v[16:19], v[88:91]\n\t" \
  "s_waitcnt lgkmcnt(7)\n\t" \
  "v_mfma_f32_16x16x32_bf16 v[68:71], v[44:47], v[4:7], v[68:71]\n\t" \
  "ds_read_b64_tr_b16 v[40:41], v59 offset:16384\n\t" \
  "v_mfma_f32_16x16x32_bf16 v[76:79], v[44:47], v[8:11], v[76:79]\n\t" \
  "ds_read_b64_tr_b16 v[42:43], v59 offset:16896\n\t" \
  "v_mfma_f32_16x16x32_bf16 v[84:87], v[44:47], v[12:15], v[84:87]\n\t" \
  "v_mfma_f32_16x16x32_bf16 v[92:95], v[44:47], v[16:19], v[92:95]\n\t" \
  "N1a_%=:\n\t" \
  "s_waitcnt lgkmcnt(4)\n\t" \
  "v_mfma_f32_16x16x32_bf16 v[160:163], v[48:51], v[20:23], v[160:163]\n\t" \
  "ds_read_b64_tr_b16 v[44:45], v59 offset:20480\n\t" \
  "v_mfma_f32_16x16x32_bf16 v[96:99], v[48:51], v[24:27], v[96:99]\n\t" \
  "ds_read_b64_tr_b16 v[46:47], v59 offset:20992\n\t" \
  "v_mfma_f32_16x16x32_bf16 v[104:107], v[48:51], v[28:31], v[104:107]\n\t" \
  "s_barrier\n\t" \
  "v_mfma_f32_16x16x32_bf16 v[112:115], v[48:51], v[32:35], v[112:115]\n\t" \
  "v_mfma_f32_16x16x32_bf16 v[120:123], v[48:51], v[36:39], v[120:123]\n\t" \
  "s_waitcnt lgkmcnt(4)\n\t" \
  "v_mfma_f32_16x16x32_bf16 v[128:131], v[24:27], v[52:55], v[128:131]\n\t" \
  "ds_read_b64_tr_b16 v[48:49], v58 offset:12288\n\t" \
  "v_mfma_f32_16x16x32_bf16 v[136:139], v[28:31], v[52:55], v[136:139]\n\t" \
  "ds_read_b64_tr_b16 v[50:51], v58 offset:12800\n\t" \
  "v_mfma_f32_16x16x32_bf16 v[144:147], v[32:35], v[52:55], v[144:147]\n\t" \
  "ds_read_b128 v[0:3], v56 offset:18432\n\t" \
  "v_mfma_f32_16x16x32_bf16 v[152:155], v[36:39], v[52:55], v[152:155]\n\t" \
  "ds_read_b128 v[4:7], v56 offset:20480\n\t" \
  "s_waitcnt lgkmcnt(6)\n\t" \
  "v_mfma_f32_16x16x32_bf16 v[164:167], v[40:43], v[20:23], v[164:167]\n\t" \
  "ds_read_b64_tr_b16 v[52:53], v59 offset:12288\n\t" \
  "v_mfma_f32_16x16x32_bf16 v[100:103], v[40:43], v[24:27], v[100:103]\n\t" \
  "ds_read_b64_tr_b16 v[54:55], v59 offset:12800\n\t" \
  "v_mfma_f32_16x16x32_bf16 v[108:111], v[40:43], v[28:31], v[108:111]\n\t" \
  "ds_read_b128 v[8:11], v56 offset:22528\n\t" \
  "v_mfma_f32_16x16x32_bf16 v[116:119], v[40:43], v[32:35], v[116:119]\n\t" \
  "ds_read_b128 v[12:15], v56 offset:24576\n\t" \
  "v_mfma_f32_16x16x32_bf16 v[124:127], v[40:43], v[36:39], v[124:127]\n\t" \
  "s_waitcnt lgkmcnt(8)\n\t" \
  "v_mfma_f32_16x16x32_bf16 v[132:135], v[24:27], v[44:47], v[132:135]\n\t" \
  "ds_read_b64_tr_b16 v[40:41], v58 offset:28672\n\t" \
  "v_mfma_f32_16x16x32_bf16 v[140:143], v[28:31], v[44:47], v[140:143]\n\t" \
  "ds_read_b64_tr_b16 v[42:43], v58 offset:29184\n\t" \
  "v_mfma_f32_16x16x32_bf16 v[148:151], v[32:35], v[44:47], v[148:151]\n\t" \
  "ds_read_b128 v[16:19], v56 offset:26624\n\t" \
  "v_mfma_f32_16x16x32_bf16 v[156:159], v[36:39], v[44:47], v[156:159]\n\t" \
  "s_waitcnt lgkmcnt(9)\n\t" \
  "v_mfma_f32_16x16x32_bf16 v[64:67], v[48:51], v[24:27], v[64:67]\n\t" \
  "ds_read_b64_tr_b16 v[44:45], v58 offset:32768\n\t" \
  "v_mfma_f32_16x16x32_bf16 v[72:75], v[48:51], v[28:31], v[72:75]\n\t" \
  "ds_read_b64_tr_b16 v[46:47], v58 offset:33280\n\t" \
  "v_mfma_f32_16x16x32_bf16 v[80:83], v[48:51], v[32:35], v[80:83]\n\t" \
  "v_mfma_f32_16x16x32_bf16 v[88:91], v[48:51], v[36:39], v[88:91]\n\t" \
  "s_waitcnt lgkmcnt(7)\n\t" \
  "v_mfma_f32_16x16x32_bf16 v[68:71], v[52:55], v[24:27], v[68:71]\n\t" \
  "ds_read_b64_tr_b16 v[48:49], v59 offset:28672\n\t" \
  "v_mfma_f32_16x16x32_bf16 v[76:79], v[52:55], v[28:31], v[76:79]\n\t" \
  "ds_read_b64_tr_b16 v[50:51], v59 offset:29184\n\t" \
  "v_mfma_f32_16x16x32_bf16 v[84:87], v[52:55], v[32:35], v[84:87]\n\t" \
  "v_mfma_f32_16x16x32_bf16 v[92:95], v[52:55], v[36:39], v[92:95]\n\t" \
  "N2a_%=:\n\t" \
  "s_waitcnt lgkmcnt(4)\n\t" \
  "v_mfma_f32_16x16x32_bf16 v[160:163], v[40:43], v[0:3], v[160:163]\n\t" \
  "ds_read_b64_tr_b16 v[52:53], v59 offset:32768\n\t" \
  "v_mfma_f32_16x16x32_bf16 v[96:99], v[40:43], v[4:7], v[96:99]\n\t" \
  "ds_read_b64_tr_b16 v[54:55], v59 offset:33280\n\t" \
  "v_mfma_f32_16x16x32_bf16 v[104:107], v[40:43], v[8:11], v[104:107]\n\t" \
  "s_barrier\n\t" \
  "v_mfma_f32_16x16x32_bf16 v[112:115], v[40:43], v[12:15], v[112:115]\n\t" \
  "v_mfma_f32_16x16x32_bf16 v[120:123], v[40:43], v[16:19], v[120:123]\n\t" \
  "s_waitcnt lgkmcnt(4)\n\t" \
  "v_mfma_f32_16x16x32_bf16 v[128:131], v[4:7], v[44:47], v[128:131]\n\t" \
  "ds_read_b64_tr_b16 v[40:41], v58 offset:24576\n\t" \
  "v_mfma_f32_16x16x32_bf16 v[136:139], v[8:11], v[44:47], v[136:139]\n\t" \
  "ds_read_b64_tr_b16 v[42:43], v58 offset:25088\n\t" \
  "v_mfma_f32_16x16x32_bf16 v[144:147], v[12:15], v[44:47], v[144:147]\n\t" \
  "ds_read_b128 v[20:23], v57 offset:18432\n\t" \
  "v_mfma_f32_16x16x32_bf16 v[152:155], v[16:19], v[44:47], v[152:155]\n\t" \
  "ds_read_b128 v[24:27], v57 offset:20480\n\t" \
  "s_waitcnt lgkmcnt(6)\n\t" \
  "v_mfma_f32_16x16x32_bf16 v[164:167], v[48:51], v[0:3], v[164:167]\n\t" \
  "ds_read_b64_tr_b16 v[44:45], v59 offset:24576\n\t" \
  "v_mfma_f32_16x16x32_bf16 v[100:103], v[48:51], v[4:7], v[100:103]\n\t" \
  "ds_read_b64_tr_b16 v[46:47], v59 offset:25088\n\t" \
  "v_mfma_f32_16x16x32_bf16 v[108:111], v[48:51], v[8:11], v[108:111]\n\t" \
  "ds_read_b128 v[28:31], v57 offset:22528\n\t" \
  "v_mfma_f32_16x16x32_bf16 v[116:119], v[48:51], v[12:15], v[116:119]\n\t" \
  "ds_read_b128 v[32:35], v57 offset:24576\n\t" \
  "v_mfma_f32_16x16x32_bf16 v[124:127], v[48:51], v[16:19], v[124:127]\n\t" \
  "s_waitcnt lgkmcnt(8)\n\t" \
  "v_mfma_f32_16x16x32_bf16 v[132:135], v[4:7], v[52:55], v[132:135]\n\t" \
  "ds_read_b64_tr_b16 v[48:49], v60 offset:4096\n\t" \
  "v_mfma_f32_16x16x32_bf16 v[140:143], v[8:11], v[52:55], v[140:143]\n\t" \
  "ds_read_b64_tr_b16 v[50:51], v60 offset:4608\n\t" \
  "v_mfma_f32_16x16x32_bf16 v[148:151], v[12:15], v[52:55], v[148:151]\n\t" \
  "ds_read_b128 v[36:39], v57 offset:26624\n\t" \
  "v_mfma_f32_16x16x32_bf16 v[156:159], v[16:19], v[52:55], v[156:159]\n\t" \
  "s_waitcnt lgkmcnt(9)\n\t" \
  "v_mfma_f32_16x16x32_bf16 v[64:67], v[40:43], v[4:7], v[64:67]\n\t" \
  "ds_read_b64_tr_b16 v[52:53], v60 offset:8192\n\t" \
  "v_mfma_f32_16x16x32_bf16 v[72:75], v[40:43], v[8:11], v[72:75]\n\t" \
  "ds_read_b64_tr_b16 v[54:55], v60 offset:8704\n\t" \
  "v_mfma_f32_16x16x32_bf16 v[80:83], v[40:43], v[12:15], v[80:83]\n\t" \
  "v_mfma_f32_16x16x32_bf16 v[88:91], v[40:43], v[16:19], v[88:91]\n\t" \
  "s_waitcnt lgkmcnt(7)\n\t" \
  "v_mfma_f32_16x16x32_bf16 v[68:71], v[44:47], v[4:7], v[68:71]\n\t" \
  "ds_read_b64_tr_b16 v[40:41], v61 offset:4096\n\t" \
  "v_mfma_f32_16x16x32_bf16 v[76:79], v[44:47], v[8:11], v[76:79]\n\t" \
  "ds_read_b64_tr_b16 v[42:43], v61 offset:4608\n\t" \
  "v_mfma_f32_16x16x32_bf16 v[84:87], v[44:47], v[12:15], v[84:87]\n\t" \
  "v_mfma_f32_16x16x32_bf16 v[92:95], v[44:47], v[16:19], v[92:95]\n\t" \
  "N3a_%=:\n\t" \
  "s_waitcnt lgkmcnt(4)\n\t" \
  "v_mfma_f32_16x16x32_bf16 v[160:163], v[48:51], v[20:23], v[160:163]\n\t" \
  "ds_read_b64_tr_b16 v[44:45], v61 offset:8192\n\t" \
  "v_mfma_f32_16x16x32_bf16 v[96:99], v[48:51], v[24:27], v[96:99]\n\t" \
  "ds_read_b64_tr_b16 v[46:47], v61 offset:8704\n\t" \
  "v_mfma_f32_16x16x32_bf16 v[104:107], v[48:51], v[28:31], v[104:107]\n\t" \
  "s_barrier\n\t" \
  "v_mfma_f32_16x16x32_bf16 v[112:115], v[48:51], v[32:35], v[112:115]\n\t" \
  "v_mfma_f32_16x16x32_bf16 v[120:123], v[48:51], v[36:39], v[120:123]\n\t" \
  "s_waitcnt lgkmcnt(4)\n\t" \
  "v_mfma_f32_16x16x32_bf16 v[128:131], v[24:27], v[52:55], v[128:131]\n\t" \
  "ds_read_b64_tr_b16 v[48:49], v60 offset:0\n\t" \
  "v_mfma_f32_16x16x32_bf16 v[136:139], v[28:31], v[52:55], v[136:139]\n\t" \
  "ds_read_b64_tr_b16 v[50:51], v60 offset:512\n\t" \
  "v_mfma_f32_16x16x32_bf16 v[144:147], v[32:35], v[52:55], v[144:147]\n\t" \
  "ds_read_b128 v[0:3], v56 offset:36864\n\t" \
  "v_mfma_f32_16x16x32_bf16 v[152:155], v[36:39], v[52:55], v[152:155]\n\t" \
  "ds_read_b128 v[4:7], v56 offset:38912\n\t" \
  "s_waitcnt lgkmcnt(6)\n\t" \
  "v_mfma_f32_16x16x32_bf16 v[164:167], v[40:43], v[20:23], v[164:167]\n\t" \
  "ds_read_b64_tr_b16 v[52:53], v61 offset:0\n\t" \
  "v_mfma_f32_16x16x32_bf16 v[100:103], v[40:43], v[24:27], v[100:103]\n\t" \
  "ds_read_b64_tr_b16 v[54:55], v61 offset:512\n\t" \
  "v_mfma_f32_16x16x32_bf16 v[108:111], v[40:43], v[28:31], v[108:111]\n\t" \
  "ds_read_b128 v[8:11], v56 offset:40960\n\t" \
  "v_mfma_f32_16x16x32_bf16 v[116:119], v[40:43], v[32:35], v[116:119]\n\t" \
  "ds_read_b128 v[12:15], v56 offset:43008\n\t" \
  "v_mfma_f32_16x16x32_bf16 v[124:127], v[40:43], v[36:39], v[124:127]\n\t" \
  "s_waitcnt lgkmcnt(8)\n\t" \
  "v_mfma_f32_16x16x32_bf16 v[132:135], v[24:27], v[44:47], v[132:135]\n\t" \
  "ds_read_b64_tr_b16 v[40:41], v60 offset:16384\n\t" \
  "v_mfma_f32_16x16x32_bf16 v[140:143], v[28:31], v[44:47], v[140:143]\n\t" \
  "ds_read_b64_tr_b16 v[42:43], v60 offset:16896\n\t" \
  "v_mfma_f32_16x16x32_bf16 v[148:151], v[32:35], v[44:47], v[148:151]\n\t" \
  "ds_read_b128 v[16:19], v56 offset:45056\n\t" \
  "v_mfma_f32_16x16x32_bf16 v[156:159], v[36:39], v[44:47], v[156:159]\n\t" \
  "s_waitcnt lgkmcnt(9)\n\t" \
  "v_mfma_f32_16x16x32_bf16 v[64:67], v[48:51], v[24:27], v[64:67]\n\t" \
  "ds_read_b64_tr_b16 v[44:45], v60 offset:20480\n\t" \
  "v_mfma_f32_16x16x32_bf16 v[72:75], v[48:51], v[28:31], v[72:75]\n\t" \
  "ds_read_b64_tr_b16 v[46:47], v60 offset:20992\n\t" \
  "v_mfma_f32_16x16x32_bf16 v[80:83], v[48:51], v[32:35], v[80:83]\n\t" \
  "v_mfma_f32_16x16x32_bf16 v[88:91], v[48:51], v[36:39], v[88:91]\n\t" \
  "s_waitcnt lgkmcnt(7)\n\t" \
  "v_mfma_f32_16x16x32_bf16 v[68:71], v[52:55], v[24:27], v[68:71]\n\t" \
  "ds_read_b64_tr_b16 v[48:49], v61 offset:16384\n\t" \
  "v_mfma_f32_16x16x32_bf16 v[76:79], v[52:55], v[28:31], v[76:79]\n\t" \
  "ds_read_b64_tr_b16 v[50:51], v61 offset:16896\n\t" \
  "v_mfma_f32_16x16x32_bf16 v[84:87], v[52:55], v[32:35], v[84:87]\n\t" \
  "v_mfma_f32_16x16x32_bf16 v[92:95], v[52:55], v[36:39], v[92:95]\n\t" \
  "N4a_%=:\n\t" \
  "s_waitcnt lgkmcnt(4)\n\t" \
  "v_mfma_f32_16x16x32_bf16 v[160:163], v[40:43], v[0:3], v[160:163]\n\t" \
  "ds_read_b64_tr_b16 v[52:53], v61 offset:20480\n\t" \
  "v_mfma_f32_16x16x32_bf16 v[96:99], v[40:43], v[4:7], v[96:99]\n\t" \
  "ds_read_b64_tr_b16 v[54:55], v61 offset:20992\n\t" \
  "v_mfma_f32_16x16x32_bf16 v[104:107], v[40:43], v[8:11], v[104:107]\n\t" \
  "s_barrier\n\t" \
  "v_mfma_f32_16x16x32_bf16 v[112:115], v[40:43], v[12:15], v[112:115]\n\t" \
  "v_mfma_f32_16x16x32_bf16 v[120:123], v[40:43], v[16:19], v[120:123]\n\t" \
  "s_waitcnt lgkmcnt(4)\n\t" \
  "v_mfma_f32_16x16x32_bf16 v[128:131], v[4:7], v[44:47], v[128:131]\n\t" \
  "ds_read_b64_tr_b16 v[40:41], v60 offset:12288\n\t" \
  "v_mfma_f32_16x16x32_bf16 v[136:139], v[8:11], v[44:47], v[136:139]\n\t" \
  "ds_read_b64_tr_b16 v[42:43], v60 offset:12800\n\t" \
  "v_mfma_f32_16x16x32_bf16 v[144:147], v[12:15], v[44:47], v[144:147]\n\t" \
  "ds_read_b128 v[20:23], v57 offset:36864\n\t" \
  "v_mfma_f32_16x16x32_bf16 v[152:155], v[16:19], v[44:47], v[152:155]\n\t" \
  "ds_read_b128 v[24:27], v57 offset:38912\n\t" \
  "s_waitcnt lgkmcnt(6)\n\t" \
  "v_mfma_f32_16x16x32_bf16 v[164:167], v[48:51], v[0:3], v[164:167]\n\t" \
  "ds_read_b64_tr_b16 v[44:45], v61 offset:12288\n\t" \
  "v_mfma_f32_16x16x32_bf16 v[100:103], v[48:51], v[4:7], v[100:103]\n\t" \
  "ds_read_b64_tr_b16 v[46:47], v61 offset:12800\n\t" \
  "v_mfma_f32_16x16x32_bf16 v[108:111], v[48:51], v[8:11], v[108:111]\n\t" \
  "ds_read_b128 v[28:31], v57 offset:40960\n\t" \
  "v_mfma_f32_16x16x32_bf16 v[116:119], v[48:51], v[12:15], v[116:119]\n\t" \
  "ds_read_b128 v[32:35], v57 offset:43008\n\t" \
  "v_mfma_f32_16x16x32_bf16 v[124:127], v[48:51], v[16:19], v[124:127]\n\t" \
  "s_waitcnt lgkmcnt(8)\n\t" \
  "v_mfma_f32_16x16x32_bf16 v[132:135], v[4:7], v[52:55], v[132:135]\n\t" \
  "ds_read_b64_tr_b16 v[48:49], v60 offset:28672\n\t" \
  "v_mfma_f32_16x16x32_bf16 v[140:143], v[8:11], v[52:55], v[140:143]\n\t" \
  "ds_read_b64_tr_b16 v[50:51], v60 offset:29184\n\t" \
  "v_mfma_f32_16x16x32_bf16 v[148:151], v[12:15], v[52:55], v[148:151]\n\t" \
  "ds_read_b128 v[36:39], v57 offset:45056\n\t" \
  "v_mfma_f32_16x16x32_bf16 v[156:159], v[16:19], v[52:55], v[156:159]\n\t" \
  "s_waitcnt lgkmcnt(9)\n\t" \
  "v_mfma_f32_16x16x32_bf16 v[64:67], v[40:43], v[4:7], v[64:67]\n\t" \
  "ds_read_b64_tr_b16 v[52:53], v60 offset:32768\n\t" \
  "v_mfma_f32_16x16x32_bf16 v[72:75], v[40:43], v[8:11], v[72:75]\n\t" \
  "ds_read_b64_tr_b16 v[54:55], v60 offset:33280\n\t" \
  "v_mfma_f32_16x16x32_bf16 v[80:83], v[40:43], v[12:15], v[80:83]\n\t" \
  "v_mfma_f32_16x16x32_bf16 v[88:91], v[40:43], v[16:19], v[88:91]\n\t" \
  "s_waitcnt lgkmcnt(7)\n\t" \
  "v_mfma_f32_16x16x32_bf16 v[68:71], v[44:47], v[4:7], v[68:71]\n\t" \
  "ds_read_b64_tr_b16 v[40:41], v61 offset:28672\n\t" \
  "v_mfma_f32_16x16x32_bf16 v[76:79], v[44:47], v[8:11], v[76:79]\n\t" \
  "ds_read_b64_tr_b16 v[42:43], v61 offset:29184\n\t" \
  "v_mfma_f32_16x16x32_bf16 v[84:87], v[44:47], v[12:15], v[84:87]\n\t" \
  "v_mfma_f32_16x16x32_bf16 v[92:95], v[44:47], v[16:19], v[92:95]\n\t" \
  "N5a_%=:\n\t" \
  "s_waitcnt lgkmcnt(4)\n\t" \
  "v_mfma_f32_16x16x32_bf16 v[160:163], v[48:51], v[20:23], v[160:163]\n\t" \
  "ds_read_b64_tr_b16 v[44:45], v61 offset:32768\n\t" \
  "v_mfma_f32_16x16x32_bf16 v[96:99], v[48:51], v[24:27], v[96:99]\n\t" \
  "ds_read_b64_tr_b16 v[46:47], v61 offset:33280\n\t" \
  "v_mfma_f32_16x16x32_bf16 v[104:107], v[48:51], v[28:31], v[104:107]\n\t" \
  "s_barrier\n\t" \
  "v_mfma_f32_16x16x32_bf16 v[112:115], v[48:51], v[32:35], v[112:115]\n\t" \
  "v_mfma_f32_16x16x32_bf16 v[120:123], v[48:51], v[36:39], v[120:123]\n\t" \
  "s_waitcnt lgkmcnt(4)\n\t" \
  "v_mfma_f32_16x16x32_bf16 v[128:131], v[24:27], v[52:55], v[128:131]\n\t" \
  "ds_read_b64_tr_b16 v[48:49], v60 offset:24576\n\t" \
  "v_mfma_f32_16x16x32_bf16 v[136:139], v[28:31], v[52:55], v[136:139]\n\t" \
  "ds_read_b64_tr_b16 v[50:51], v60 offset:25088\n\t" \
  "v_mfma_f32_16x16x32_bf16 v[144:147], v[32:35], v[52:55], v[144:147]\n\t" \
  "ds_read_b128 v[0:3], v56 offset:0\n\t" \
  "v_mfma_f32_16x16x32_bf16 v[152:155], v[36:39], v[52:55], v[152:155]\n\t" \
  "ds_read_b128 v[4:7], v56 offset:2048\n\t" \
  "s_waitcnt lgkmcnt(6)\n\t" \
  "v_mfma_f32_16x16x32_bf16 v[164:167], v[40:43], v[20:23], v[164:167]\n\t" \
  "ds_read_b64_tr_b16 v[52:53], v61 offset:24576\n\t" \
  "v_mfma_f32_16x16x32_bf16 v[100:103], v[40:43], v[24:27], v[100:103]\n\t" \
  "ds_read_b64_tr_b16 v[54:55], v61 offset:25088\n\t" \
  "v_mfma_f32_16x16x32_bf16 v[108:111], v[40:43], v[28:31], v[108:111]\n\t" \
  "ds_read_b128 v[8:11], v56 offset:4096\n\t" \
  "v_mfma_f32_16x16x32_bf16 v[116:119], v[40:43], v[32:35], v[116:119]\n\t" \
  "ds_read_b128 v[12:15], v56 offset:6144\n\t" \
  "v_mfma_f32_16x16x32_bf16 v[124:127], v[40:43], v[36:39], v[124:127]\n\t" \
  "s_waitcnt lgkmcnt(8)\n\t" \
  "v_mfma_f32_16x16x32_bf16 v[132:135], v[24:27], v[44:47], v[132:135]\n\t" \
  "ds_read_b64_tr_b16 v[40:41], v58 offset:4096\n\t" \
  "v_mfma_f32_16x16x32_bf16 v[140:143], v[28:31], v[44:47], v[140:143]\n\t" \
  "ds_read_b64_tr_b16 v[42:43], v58 offset:4608\n\t" \
  "v_mfma_f32_16x16x32_bf16 v[148:151], v[32:35], v[44:47], v[148:151]\n\t" \
  "ds_read_b128 v[16:19], v56 offset:8192\n\t" \
  "v_mfma_f32_16x16x32_bf16 v[156:159], v[36:39], v[44:47], v[156:159]\n\t" \
  "s_waitcnt lgkmcnt(9)\n\t" \
  "v_mfma_f32_16x16x32_bf16 v[64:67], v[48:51], v[24:27], v[64:67]\n\t" \
  "ds_read_b64_tr_b16 v[44:45], v58 offset:8192\n\t" \
  "v_mfma_f32_16x16x32_bf16 v[72:75], v[48:51], v[28:31], v[72:75]\n\t" \
  "ds_read_b64_tr_b16 v[46:47], v58 offset:8704\n\t" \
  "v_mfma_f32_16x16x32_bf16 v[80:83], v[48:51], v[32:35], v[80:83]\n\t" \
  "v_mfma_f32_16x16x32_bf16 v[88:91], v[48:51], v[36:39], v[88:91]\n\t" \
  "s_waitcnt lgkmcnt(7)\n\t" \
  "v_mfma_f32_16x16x32_bf16 v[68:71], v[52:55], v[24:27], v[68:71]\n\t" \
  "ds_read_b64_tr_b16 v[48:49], v59 offset:4096\n\t" \
  "v_mfma_f32_16x16x32_bf16 v[76:79], v[52:55], v[28:31], v[76:79]\n\t" \
  "ds_read_b64_tr_b16 v[50:51], v59 offset:4608\n\t" \
  "s_addk_i32 %[it], 1\n\t" \
  "v_mfma_f32_16x16x32_bf16 v[84:87], v[52:55], v[32:35], v[84:87]\n\t" \
  "s_cmp_lt_u32 %[it], 6\n\t" \
  "v_mfma_f32_16x16x32_bf16 v[92:95], v[52:55], v[36:39], v[92:95]\n\t" \
  "s_cbranch_scc1 N0a_%=\n\t" \
  "s_setprio 3\n\t" \
  "v_cvt_pk_bf16_f32 v120, v120, v121\n\t" \
  "v_cvt_pk_bf16_f32 v121, v122, v123\n\t" \
  "v_cvt_pk_bf16_f32 v122, v124, v125\n\t" \
  "v_cvt_pk_bf16_f32 v123, v126, v127\n\t" \
  "v_cvt_pk_bf16_f32 v28, v152, v153\n\t" \
  "v_cvt_pk_bf16_f32 v29, v154, v155\n\t" \
  "v_cvt_pk_bf16_f32 v38, v156, v157\n\t" \
  "v_cvt_pk_bf16_f32 v39, v158, v159\n\t" \
  "ds_write_b128 v63, v[120:123]\n\t" \
  "ds_write2_b64 v63, v[28:29], v[38:39] offset0:128 offset1:129\n\t" \
  "v_mov_b32 v57, %[fa]\n\t" \
  "v_mov_b32 v58, 1\n\t" \
  "s_mov_b64 exec, 1\n\t" \
  "ds_write_b32 v57, v58\n\t" \
  "s_mov_b64 exec, -1\n\t" \
  "v_cvt_pk_bf16_f32 v160, v160, v161\n\t" \
  "v_cvt_pk_bf16_f32 v161, v162, v163\n\t" \
  "v_cvt_pk_bf16_f32 v162, v164, v165\n\t" \
  "v_cvt_pk_bf16_f32 v163, v166, v167\n\t" \
  "s_branch NX_%=\n\t" \
  "NB_%=:\n\t" \
  "ds_read_b64_tr_b16 v[40:41], v60 offset:20480\n\t" \
  "ds_read_b64_tr_b16 v[42:43], v60 offset:20992\n\t" \
  "ds_read_b64_tr_b16 v[44:45], v60 offset:16384\n\t" \
  "ds_read_b64_tr_b16 v[46:47], v60 offset:16896\n\t" \
  "ds_read_b64_tr_b16 v[48:49], v61 offset:20480\n\t" \
  "ds_read_b64_tr_b16 v[50:51], v61 offset:20992\n\t" \
  "ds_read_b128 v[0:3], v56 offset:36864\n\t" \
  "ds_read_b128 v[4:7], v56 offset:47104\n\t" \
  "ds_read_b128 v[8:11], v56 offset:49152\n\t" \
  "ds_read_b128 v[12:15], v56 offset:51200\n\t" \
  "ds_read_b128 v[16:19], v56 offset:53248\n\t" \
  "s_waitcnt lgkmcnt(0)\n\t" \
  "s_branch N4b_%=\n\t" \
  "N0b_%=:\n\t" \
  "s_waitcnt lgkmcnt(4)\n\t" \
  "v_mfma_f32_16x16x32_bf16 v[160:163], v[0:3], v[40:43], v[160:163]\n\t" \
  "ds_read_b64_tr_b16 v[52:53], v59 offset:4096\n\t" \
  "v_mfma_f32_16x16x32_bf16 v[128:131], v[4:7], v[40:43], v[128:131]\n\t" \
  "ds_read_b64_tr_b16 v[54:55], v59 offset:4608\n\t" \
  "v_mfma_f32_16x16x32_bf16 v[136:139], v[8:11], v[40:43], v[136:139]\n\t" \
  "s_barrier\n\t" \
  "v_mfma_f32_16x16x32_bf16 v[144:147], v[12:15], v[40:43], v[144:147]\n\t" \
  "v_mfma_f32_16x16x32_bf16 v[152:155], v[16:19], v[40:43], v[152:155]\n\t" \
  "s_waitcnt lgkmcnt(4)\n\t" \
  "v_mfma_f32_16x16x32_bf16 v[96:99], v[44:47], v[4:7], v[96:99]\n\t" \
  "ds_read_b64_tr_b16 v[40:41], v58 offset:0\n\t" \
  "v_mfma_f32_16x16x32_bf16 v[104:107], v[44:47], v[8:11], v[104:107]\n\t" \
  "ds_read_b64_tr_b16 v[42:43], v58 offset:512\n\t" \
  "v_mfma_f32_16x16x32_bf16 v[112:115], v[44:47], v[12:15], v[112:115]\n\t" \
  "ds_read_b128 v[20:23], v57 offset:0\n\t" \
  "v_mfma_f32_16x16x32_bf16 v[120:123], v[44:47], v[16:19], v[120:123]\n\t" \
  "ds_read_b128 v[24:27], v57 offset:10240\n\t" \
  "s_waitcnt lgkmcnt(6)\n\t" \
  "v_mfma_f32_16x16x32_bf16 v[164:167], v[0:3], v[48:51], v[164:167]\n\t" \
  "ds_read_b64_tr_b16 v[44:45], v59 offset:0\n\t" \
  "v_mfma_f32_16x16x32_bf16 v[132:135], v[4:7], v[48:51], v[132:135]\n\t" \
  "ds_read_b64_tr_b16 v[46:47], v59 offset:512\n\t" \
  "v_mfma_f32_16x16x32_bf16 v[140:143], v[8:11], v[48:51], v[140:143]\n\t" \
  "ds_read_b128 v[28:31], v57 offset:12288\n\t" \
  "v_mfma_f32_16x16x32_bf16 v[148:151], v[12:15], v[48:51], v[148:151]\n\t" \
  "ds_read_b128 v[32:35], v57 offset:14336\n\t" \
  "v_mfma_f32_16x16x32_bf16 v[156:159], v[16:19], v[48:51], v[156:159]\n\t" \
  "s_waitcnt lgkmcnt(8)\n\t" \
  "v_mfma_f32_16x16x32_bf16 v[100:103], v[52:55], v[4:7], v[100:103]\n\t" \
  "ds_read_b64_tr_b16 v[48:49], v58 offset:20480\n\t" \
  "v_mfma_f32_16x16x32_bf16 v[108:111], v[52:55], v[8:11], v[108:111]\n\t" \
  "ds_read_b64_tr_b16 v[50:51], v58 offset:20992\n\t" \
  "v_mfma_f32_16x16x32_bf16 v[116:119], v[52:55], v[12:15], v[116:119]\n\t" \
  "ds_read_b128 v[36:39], v57 offset:16384\n\t" \
  "v_mfma_f32_16x16x32_bf16 v[124:127], v[52:55], v[16:19], v[124:127]\n\t" \
  "s_waitcnt lgkmcnt(9)\n\t" \
  "v_mfma_f32_16x16x32_bf16 v[64:67], v[40:43], v[4:7], v[64:67]\n\t" \
  "ds_read_b64_tr_b16 v[52:53], v58 offset:16384\n\t" \
  "v_mfma_f32_16x16x32_bf16 v[72:75], v[40:43], v[8:11], v[72:75]\n\t" \
  "ds_read_b64_tr_b16 v[54:55], v58 offset:16896\n\t" \
  "v_mfma_f32_16x16x32_bf16 v[80:83], v[40:43], v[12:15], v[80:83]\n\t" \
  "v_mfma_f32_16x16x32_bf16 v[88:91], v[40:43], v[16:19], v[88:91]\n\t" \
  "s_waitcnt lgkmcnt(7)\n\t" \
  "v_mfma_f32_16x16x32_bf16 v[68:71], v[44:47], v[4:7], v[68:71]\n\t" \
  "ds_read_b64_tr_b16 v[40:41], v59 offset:20480\n\t" \
  "v_mfma_f32_16x16x32_bf16 v[76:79], v[44:47], v[8:11], v[76:79]\n\t" \
  "ds_read_b64_tr_b16 v[42:43], v59 offset:20992\n\t" \
  "v_mfma_f32_16x16x32_bf16 v[84:87], v[44:47], v[12:15], v[84:87]\n\t" \
  "v_mfma_f32_16x16x32_bf16 v[92:95], v[44:47], v[16:19], v[92:95]\n\t" \
  "N1b_%=:\n\t" \
  "s_waitcnt lgkmcnt(4)\n\t" \
  "v_mfma_f32_16x16x32_bf16 v[160:163], v[20:23], v[48:51], v[160:163]\n\t" \
  "ds_read_b64_tr_b16 v[44:45], v59 offset:16384\n\t" \
  "v_mfma_f32_16x16x32_bf16 v[128:131], v[24:27], v[48:51], v[128:131]\n\t" \
  "ds_read_b64_tr_b16 v[46:47], v59 offset:16896\n\t" \
  "v_mfma_f32_16x16x32_bf16 v[136:139], v[28:31], v[48:51], v[136:139]\n\t" \
  "s_barrier\n\t" \
  "v_mfma_f32_16x16x32_bf16 v[144:147], v[32:35], v[48:51], v[144:147]\n\t" \
  "v_mfma_f32_16x16x32_bf16 v[152:155], v[36:39], v[48:51], v[152:155]\n\t" \
  "s_waitcnt lgkmcnt(4)\n\t" \
  "v_mfma_f32_16x16x32_bf16 v[96:99], v[52:55], v[24:27], v[96:99]\n\t" \
  "ds_read_b64_tr_b16 v[48:49], v58 offset:12288\n\t" \
  "v_mfma_f32_16x16x32_bf16 v[104:107], v[52:55], v[28:31], v[104:107]\n\t" \
  "ds_read_b64_tr_b16 v[50:51], v58 offset:12800\n\t" \
  "v_mfma_f32_16x16x32_bf16 v[112:115], v[52:55], v[32:35], v[112:115]\n\t" \
  "ds_read_b128 v[0:3], v56 offset:18432\n\t" \
  "v_mfma_f32_16x16x32_bf16 v[120:123], v[52:55], v[36:39], v[120:123]\n\t" \
  "ds_read_b128 v[4:7], v56 offset:28672\n\t" \
  "s_waitcnt lgkmcnt(6)\n\t" \
  "v_mfma_f32_16x16x32_bf16 v[164:167], v[20:23], v[40:43], v[164:167]\n\t" \
  "ds_read_b64_tr_b16 v[52:53], v59 offset:12288\n\t" \
  "v_mfma_f32_16x16x32_bf16 v[132:135], v[24:27], v[40:43], v[132:135]\n\t" \
  "ds_read_b64_tr_b16 v[54:55], v59 offset:12800\n\t" \
  "v_mfma_f32_16x16x32_bf16 v[140:143], v[28:31], v[40:43], v[140:143]\n\t" \
  "ds_read_b128 v[8:11], v56 offset:30720\n\t" \
  "v_mfma_f32_16x16x32_bf16 v[148:151], v[32:35], v[40:43], v[148:151]\n\t" \
  "ds_read_b128 v[12:15], v56 offset:32768\n\t" \
  "v_mfma_f32_16x16x32_bf16 v[156:159], v[36:39], v[40:43], v[156:159]\n\t" \
  "s_waitcnt lgkmcnt(8)\n\t" \
  "v_mfma_f32_16x16x32_bf16 v[100:103], v[44:47], v[24:27], v[100:103]\n\t" \
  "ds_read_b64_tr_b16 v[40:41], v58 offset:32768\n\t" \
  "v_mfma_f32_16x16x32_bf16 v[108:111], v[44:47], v[28:31], v[108:111]\n\t" \
  "ds_read_b64_tr_b16 v[42:43], v58 offset:33280\n\t" \
  "v_mfma_f32_16x16x32_bf16 v[116:119], v[44:47], v[32:35], v[116:119]\n\t" \
  "ds_read_b128 v[16:19], v56 offset:34816\n\t" \
  "v_mfma_f32_16x16x32_bf16 v[124:127], v[44:47], v[36:39], v[124:127]\n\t" \
  "s_waitcnt lgkmcnt(9)\n\t" \
  "v_mfma_f32_16x16x32_bf16 v[64:67], v[48:51], v[24:27], v[64:67]\n\t" \
  "ds_read_b64_tr_b16 v[44:45], v58 offset:28672\n\t" \
  "v_mfma_f32_16x16x32_bf16 v[72:75], v[48:51], v[28:31], v[72:75]\n\t" \
  "ds_read_b64_tr_b16 v[46:47], v58 offset:29184\n\t" \
  "v_mfma_f32_16x16x32_bf16 v[80:83], v[48:51], v[32:35], v[80:83]\n\t" \
  "v_mfma_f32_16x16x32_bf16 v[88:91], v[48:51], v[36:39], v[88:91]\n\t" \
  "s_waitcnt lgkmcnt(7)\n\t" \
  "v_mfma_f32_16x16x32_bf16 v[68:71], v[52:55], v[24:27], v[68:71]\n\t" \
  "ds_read_b64_tr_b16 v[48:49], v59 offset:32768\n\t" \
  "v_mfma_f32_16x16x32_bf16 v[76:79], v[52:55], v[28:31], v[76:79]\n\t" \
  "ds_read_b64_tr_b16 v[50:51], v59 offset:33280\n\t" \
  "v_mfma_f32_16x16x32_bf16 v[84:87], v[52:55], v[32:35], v[84:87]\n\t" \
  "v_mfma_f32_16x16x32_bf16 v[92:95], v[52:55], v[36:39], v[92:95]\n\t" \
  "N2b_%=:\n\t" \
  "s_waitcnt lgkmcnt(4)\n\t" \
  "v_mfma_f32_16x16x32_bf16 v[160:163], v[0:3], v[40:43], v[160:163]\n\t" \
  "ds_read_b64_tr_b16 v[52:53], v59 offset:28672\n\t" \
  "v_mfma_f32_16x16x32_bf16 v[128:131], v[4:7], v[40:43], v[128:131]\n\t" \
  "ds_read_b64_tr_b16 v[54:55], v59 offset:29184\n\t" \
  "v_mfma_f32_16x16x32_bf16 v[136:139], v[8:11], v[40:43], v[136:139]\n\t" \
  "s_barrier\n\t" \
  "v_mfma_f32_16x16x32_bf16 v[144:147], v[12:15], v[40:43], v[144:147]\n\t" \
  "v_mfma_f32_16x16x32_bf16 v[152:155], v[16:19], v[40:43], v[152:155]\n\t" \
  "s_waitcnt lgkmcnt(4)\n\t" \
  "v_mfma_f32_16x16x32_bf16 v[96:99], v[44:47], v[4:7], v[96:99]\n\t" \
  "ds_read_b64_tr_b16 v[40:41], v58 offset:24576\n\t" \
  "v_mfma_f32_16x16x32_bf16 v[104:107], v[44:47], v[8:11], v[104:107]\n\t" \
  "ds_read_b64_tr_b16 v[42:43], v58 offset:25088\n\t" \
  "v_mfma_f32_16x16x32_bf16 v[112:115], v[44:47], v[12:15], v[112:115]\n\t" \
  "ds_read_b128 v[20:23], v57 offset:18432\n\t" \
  "v_mfma_f32_16x16x32_bf16 v[120:123], v[44:47], v[16:19], v[120:123]\n\t" \
  "ds_read_b128 v[24:27], v57 offset:28672\n\t" \
  "s_waitcnt lgkmcnt(6)\n\t" \
  "v_mfma_f32_16x16x32_bf16 v[164:167], v[0:3], v[48:51], v[164:167]\n\t" \
  "ds_read_b64_tr_b16 v[44:45], v59 offset:24576\n\t" \
  "v_mfma_f32_16x16x32_bf16 v[132:135], v[4:7], v[48:51], v[132:135]\n\t" \
  "ds_read_b64_tr_b16 v[46:47], v59 offset:25088\n\t" \
  "v_mfma_f32_16x16x32_bf16 v[140:143], v[8:11], v[48:51], v[140:143]\n\t" \
  "ds_read_b128 v[28:31], v57 offset:30720\n\t" \
  "v_mfma_f32_16x16x32_bf16 v[148:151], v[12:15], v[48:51], v[148:151]\n\t" \
  "ds_read_b128 v[32:35], v57 offset:32768\n\t" \
  "v_mfma_f32_16x16x32_bf16 v[156:159], v[16:19], v[48:51], v[156:159]\n\t" \
  "s_waitcnt lgkmcnt(8)\n\t" \
  "v_mfma_f32_16x16x32_bf16 v[100:103], v[52:55], v[4:7], v[100:103]\n\t" \
  "ds_read_b64_tr_b16 v[48:49], v60 offset:8192\n\t" \
  "v_mfma_f32_16x16x32_bf16 v[108:111], v[52:55], v[8:11], v[108:111]\n\t" \
  "ds_read_b64_tr_b16 v[50:51], v60 offset:8704\n\t" \
  "v_mfma_f32_16x16x32_bf16 v[116:119], v[52:55], v[12:15], v[116:119]\n\t" \
  "ds_read_b128 v[36:39], v57 offset:34816\n\t" \
  "v_mfma_f32_16x16x32_bf16 v[124:127], v[52:55], v[16:19], v[124:127]\n\t" \
  "s_waitcnt lgkmcnt(9)\n\t" \
  "v_mfma_f32_16x16x32_bf16 v[64:67], v[40:43], v[4:7], v[64:67]\n\t" \
  "ds_read_b64_tr_b16 v[52:53], v60 offset:4096\n\t" \
  "v_mfma_f32_16x16x32_bf16 v[72:75], v[40:43], v[8:11], v[72:75]\n\t" \
  "ds_read_b64_tr_b16 v[54:55], v60 offset:4608\n\t" \
  "v_mfma_f32_16x16x32_bf16 v[80:83], v[40:43], v[12:15], v[80:83]\n\t" \
  "v_mfma_f32_16x16x32_bf16 v[88:91], v[40:43], v[16:19], v[88:91]\n\t" \
  "s_waitcnt lgkmcnt(7)\n\t" \
  "v_mfma_f32_16x16x32_bf16 v[68:71], v[44:47], v[4:7], v[68:71]\n\t" \
  "ds_read_b64_tr_b16 v[40:41], v61 offset:8192\n\t" \
  "v_mfma_f32_16x16x32_bf16 v[76:79], v[44:47], v[8:11], v[76:79]\n\t" \
  "ds_read_b64_tr_b16 v[42:43], v61 offset:8704\n\t" \
  "v_mfma_f32_16x16x32_bf16 v[84:87], v[44:47], v[12:15], v[84:87]\n\t" \
  "v_mfma_f32_16x16x32_bf16 v[92:95], v[44:47], v[16:19], v[92:95]\n\t" \
  "N3b_%=:\n\t" \
  "s_waitcnt lgkmcnt(4)\n\t" \
  "v_mfma_f32_16x16x32_bf16 v[160:163], v[20:23], v[48:51], v[160:163]\n\t" \
  "ds_read_b64_tr_b16 v[44:45], v61 offset:4096\n\t" \
  "v_mfma_f32_16x16x32_bf16 v[128:131], v[24:27], v[48:51], v[128:131]\n\t" \
  "ds_read_b64_tr_b16 v[46:47], v61 offset:4608\n\t" \
  "v_mfma_f32_16x16x32_bf16 v[136:139], v[28:31], v[48:51], v[136:139]\n\t" \
  "s_barrier\n\t" \
  "v_mfma_f32_16x16x32_bf16 v[144:147], v[32:35], v[48:51], v[144:147]\n\t" \
  "v_mfma_f32_16x16x32_bf16 v[152:155], v[36:39], v[48:51], v[152:155]\n\t" \
  "s_waitcnt lgkmcnt(4)\n\t" \
  "v_mfma_f32_16x16x32_bf16 v[96:99], v[52:55], v[24:27], v[96:99]\n\t" \
  "ds_read_b64_tr_b16 v[48:49], v60 offset:0\n\t" \
  "v_mfma_f32_16x16x32_bf16 v[104:107], v[52:55], v[28:31], v[104:107]\n\t" \
  "ds_read_b64_tr_b16 v[50:51], v60 offset:512\n\t" \
  "v_mfma_f32_16x16x32_bf16 v[112:115], v[52:55], v[32:35], v[112:115]\n\t" \
  "ds_read_b128 v[0:3], v56 offset:36864\n\t" \
  "v_mfma_f32_16x16x32_bf16 v[120:123], v[52:55], v[36:39], v[120:123]\n\t" \
  "ds_read_b128 v[4:7], v56 offset:47104\n\t" \
  "s_waitcnt lgkmcnt(6)\n\t" \
  "v_mfma_f32_16x16x32_bf16 v[164:167], v[20:23], v[40:43], v[164:167]\n\t" \
  "ds_read_b64_tr_b16 v[52:53], v61 offset:0\n\t" \
  "v_mfma_f32_16x16x32_bf16 v[132:135], v[24:27], v[40:43], v[132:135]\n\t" \
  "ds_read_b64_tr_b16 v[54:55], v61 offset:512\n\t" \
  "v_mfma_f32_16x16x32_bf16 v[140:143], v[28:31], v[40:43], v[140:143]\n\t" \
  "ds_read_b128 v[8:11], v56 offset:49152\n\t" \
  "v_mfma_f32_16x16x32_bf16 v[148:151], v[32:35], v[40:43], v[148:151]\n\t" \
  "ds_read_b128 v[12:15], v56 offset:51200\n\t" \
  "v_mfma_f32_16x16x32_bf16 v[156:159], v[36:39], v[40:43], v[156:159]\n\t" \
  "s_waitcnt lgkmcnt(8)\n\t" \
  "v_mfma_f32_16x16x32_bf16 v[100:103], v[44:47], v[24:27], v[100:103]\n\t" \
  "ds_read_b64_tr_b16 v[40:41], v60 offset:20480\n\t" \
  "v_mfma_f32_16x16x32_bf16 v[108:111], v[44:47], v[28:31], v[108:111]\n\t" \
  "ds_read_b64_tr_b16 v[42:43], v60 offset:20992\n\t" \
  "v_mfma_f32_16x16x32_bf16 v[116:119], v[44:47], v[32:35], v[116:119]\n\t" \
  "ds_read_b128 v[16:19], v56 offset:53248\n\t" \
  "v_mfma_f32_16x16x32_bf16 v[124:127], v[44:47], v[36:39], v[124:127]\n\t" \
  "s_waitcnt lgkmcnt(9)\n\t" \
  "v_mfma_f32_16x16x32_bf16 v[64:67], v[48:51], v[24:27], v[64:67]\n\t" \
  "ds_read_b64_tr_b16 v[44:45], v60 offset:16384\n\t" \
  "v_mfma_f32_16x16x32_bf16 v[72:75], v[48:51], v[28:31], v[72:75]\n\t" \
  "ds_read_b64_tr_b16 v[46:47], v60 offset:16896\n\t" \
  "v_mfma_f32_16x16x32_bf16 v[80:83], v[48:51], v[32:35], v[80:83]\n\t" \
  "v_mfma_f32_16x16x32_bf16 v[88:91], v[48:51], v[36:39], v[88:91]\n\t" \
  "s_waitcnt lgkmcnt(7)\n\t" \
  "v_mfma_f32_16x16x32_bf16 v[68:71], v[52:55], v[24:27], v[68:71]\n\t" \
  "ds_read_b64_tr_b16 v[48:49], v61 offset:20480\n\t" \
  "v_mfma_f32_16x16x32_bf16 v[76:79], v[52:55], v[28:31], v[76:79]\n\t" \
  "ds_read_b64_tr_b16 v[50:51], v61 offset:20992\n\t" \
  "v_mfma_f32_16x16x32_bf16 v[84:87], v[52:55], v[32:35], v[84:87]\n\t" \
  "v_mfma_f32_16x16x32_bf16 v[92:95], v[52:55], v[36:39], v[92:95]\n\t" \
  "N4b_%=:\n\t" \
  "s_waitcnt lgkmcnt(4)\n\t" \
  "v_mfma_f32_16x16x32_bf16 v[160:163], v[0:3], v[40:43], v[160:163]\n\t" \
  "ds_read_b64_tr_b16 v[52:53], v61 offset:16384\n\t" \
  "v_mfma_f32_16x16x32_bf16 v[128:131], v[4:7], v[40:43], v[128:131]\n\t" \
  "ds_read_b64_tr_b16 v[54:55], v61 offset:16896\n\t" \
  "v_mfma_f32_16x16x32_bf16 v[136:139], v[8:11], v[40:43], v[136:139]\n\t" \
  "s_barrier\n\t" \
  "v_mfma_f32_16x16x32_bf16 v[144:147], v[12:15], v[40:43], v[144:147]\n\t" \
  "v_mfma_f32_16x16x32_bf16 v[152:155], v[16:19], v[40:43], v[152:155]\n\t" \
  "s_waitcnt lgkmcnt(4)\n\t" \
  "v_mfma_f32_16x16x32_bf16 v[96:99], v[44:47], v[4:7], v[96:99]\n\t" \
  "ds_read_b64_tr_b16 v[40:41], v60 offset:12288\n\t" \
  "v_mfma_f32_16x16x32_bf16 v[104:107], v[44:47], v[8:11], v[104:107]\n\t" \
  "ds_read_b64_tr_b16 v[42:43], v60 offset:12800\n\t" \
  "v_mfma_f32_16x16x32_bf16 v[112:115], v[44:47], v[12:15], v[112:115]\n\t" \
  "ds_read_b128 v[20:23], v57 offset:36864\n\t" \
  "v_mfma_f32_16x16x32_bf16 v[120:123], v[44:47], v[16:19], v[120:123]\n\t" \
  "ds_read_b128 v[24:27], v57 offset:47104\n\t" \
  "s_waitcnt lgkmcnt(6)\n\t" \
  "v_mfma_f32_16x16x32_bf16 v[164:167], v[0:3], v[48:51], v[164:167]\n\t" \
  "ds_read_b64_tr_b16 v[44:45], v61 offset:12288\n\t" \
  "v_mfma_f32_16x16x32_bf16 v[132:135], v[4:7], v[48:51], v[132:135]\n\t" \
  "ds_read_b64_tr_b16 v[46:47], v61 offset:12800\n\t" \
  "v_mfma_f32_16x16x32_bf16 v[140:143], v[8:11], v[48:51], v[140:143]\n\t" \
  "ds_read_b128 v[28:31], v57 offset:49152\n\t" \
  "v_mfma_f32_16x16x32_bf16 v[148:151], v[12:15], v[48:51], v[148:151]\n\t" \
  "ds_read_b128 v[32:35], v57 offset:51200\n\t" \
  "v_mfma_f32_16x16x32_bf16 v[156:159], v[16:19], v[48:51], v[156:159]\n\t" \
  "s_waitcnt lgkmcnt(8)\n\t" \
  "v_mfma_f32_16x16x32_bf16 v[100:103], v[52:55], v[4:7], v[100:103]\n\t" \
  "ds_read_b64_tr_b16 v[48:49], v60 offset:32768\n\t" \
  "v_mfma_f32_16x16x32_bf16 v[108:111], v[52:55], v[8:11], v[108:111]\n\t" \
  "ds_read_b64_tr_b16 v[50:51], v60 offset:33280\n\t" \
  "v_mfma_f32_16x16x32_bf16 v[116:119], v[52:55], v[12:15], v[116:119]\n\t" \
  "ds_read_b128 v[36:39], v57 offset:53248\n\t" \
  "v_mfma_f32_16x16x32_bf16 v[124:127], v[52:55], v[16:19], v[124:127]\n\t" \
  "s_waitcnt lgkmcnt(9)\n\t" \
  "v_mfma_f32_16x16x32_bf16 v[64:67], v[40:43], v[4:7], v[64:67]\n\t" \
  "ds_read_b64_tr_b16 v[52:53], v60 offset:28672\n\t" \
  "v_mfma_f32_16x16x32_bf16 v[72:75], v[40:43], v[8:11], v[72:75]\n\t" \
  "ds_read_b64_tr_b16 v[54:55], v60 offset:29184\n\t" \
  "v_mfma_f32_16x16x32_bf16 v[80:83], v[40:43], v[12:15], v[80:83]\n\t" \
  "v_mfma_f32_16x16x32_bf16 v[88:91], v[40:43], v[16:19], v[88:91]\n\t" \
  "s_waitcnt lgkmcnt(7)\n\t" \
  "v_mfma_f32_16x16x32_bf16 v[68:71], v[44:47], v[4:7], v[68:71]\n\t" \
  "ds_read_b64_tr_b16 v[40:41], v61 offset:32768\n\t" \
  "v_mfma_f32_16x16x32_bf16 v[76:79], v[44:47], v[8:11], v[76:79]\n\t" \
  "ds_read_b64_tr_b16 v[42:43], v61 offset:33280\n\t" \
  "v_mfma_f32_16x16x32_bf16 v[84:87], v[44:47], v[12:15], v[84:87]\n\t" \
  "v_mfma_f32_16x16x32_bf16 v[92:95], v[44:47], v[16:19], v[92:95]\n\t" \
  "N5b_%=:\n\t" \
  "s_waitcnt lgkmcnt(4)\n\t" \
  "v_mfma_f32_16x16x32_bf16 v[160:163], v[20:23], v[48:51], v[160:163]\n\t" \
  "ds_read_b64_tr_b16 v[44:45], v61 offset:28672\n\t" \
  "v_mfma_f32_16x16x32_bf16 v[128:131], v[24:27], v[48:51], v[128:131]\n\t" \
  "ds_read_b64_tr_b16 v[46:47], v61 offset:29184\n\t" \
  "v_mfma_f32_16x16x32_bf16 v[136:139], v[28:31], v[48:51], v[136:139]\n\t" \
  "s_barrier\n\t" \
  "v_mfma_f32_16x16x32_bf16 v[144:147], v[32:35], v[48:51], v[144:147]\n\t" \
  "v_mfma_f32_16x16x32_bf16 v[152:155], v[36:39], v[48:51], v[152:155]\n\t" \
  "s_waitcnt lgkmcnt(4)\n\t" \
  "v_mfma_f32_16x16x32_bf16 v[96:99], v[52:55], v[24:27], v[96:99]\n\t" \
  "ds_read_b64_tr_b16 v[48:49], v60 offset:24576\n\t" \
  "v_mfma_f32_16x16x32_bf16 v[104:107], v[52:55], v[28:31], v[104:107]\n\t" \
  "ds_read_b64_tr_b16 v[50:51], v60 offset:25088\n\t" \
  "v_mfma_f32_16x16x32_bf16 v[112:115], v[52:55], v[32:35], v[112:115]\n\t" \
  "ds_read_b128 v[0:3], v56 offset:0\n\t" \
  "v_mfma_f32_16x16x32_bf16 v[120:123], v[52:55], v[36:39], v[120:123]\n\t" \
  "ds_read_b128 v[4:7], v56 offset:10240\n\t" \
  "s_waitcnt lgkmcnt(6)\n\t" \
  "v_mfma_f32_16x16x32_bf16 v[164:167], v[20:23], v[40:43], v[164:167]\n\t" \
  "ds_read_b64_tr_b16 v[52:53], v61 offset:24576\n\t" \
  "v_mfma_f32_16x16x32_bf16 v[132:135], v[24:27], v[40:43], v[132:135]\n\t" \
  "ds_read_b64_tr_b16 v[54:55], v61 offset:25088\n\t" \
  "v_mfma_f32_16x16x32_bf16 v[140:143], v[28:31], v[40:43], v[140:143]\n\t" \
  "ds_read_b128 v[8:11], v56 offset:12288\n\t" \
  "v_mfma_f32_16x16x32_bf16 v[148:151], v[32:35], v[40:43], v[148:151]\n\t" \
  "ds_read_b128 v[12:15], v56 offset:14336\n\t" \
  "v_mfma_f32_16x16x32_bf16 v[156:159], v[36:39], v[40:43], v[156:159]\n\t" \
  "s_waitcnt lgkmcnt(8)\n\t" \
  "v_mfma_f32_16x16x32_bf16 v[100:103], v[44:47], v[24:27], v[100:103]\n\t" \
  "ds_read_b64_tr_b16 v[40:41], v58 offset:8192\n\t" \
  "v_mfma_f32_16x16x32_bf16 v[108:111], v[44:47], v[28:31], v[108:111]\n\t" \
  "ds_read_b64_tr_b16 v[42:43], v58 offset:8704\n\t" \
  "v_mfma_f32_16x16x32_bf16 v[116:119], v[44:47], v[32:35], v[116:119]\n\t" \
  "ds_read_b128 v[16:19], v56 offset:16384\n\t" \
  "v_mfma_f32_16x16x32_bf16 v[124:127], v[44:47], v[36:39], v[124:127]\n\t" \
  "s_waitcnt lgkmcnt(9)\n\t" \
  "v_mfma_f32_16x16x32_bf16 v[64:67], v[48:51], v[24:27], v[64:67]\n\t" \
  "ds_read_b64_tr_b16 v[44:45], v58 offset:4096\n\t" \
  "v_mfma_f32_16x16x32_bf16 v[72:75], v[48:51], v[28:31], v[72:75]\n\t" \
  "ds_read_b64_tr_b16 v[46:47], v58 offset:4608\n\t" \
  "v_mfma_f32_16x16x32_bf16 v[80:83], v[48:51], v[32:35], v[80:83]\n\t" \
  "v_mfma_f32_16x16x32_bf16 v[88:91], v[48:51], v[36:39], v[88:91]\n\t" \
  "s_waitcnt lgkmcnt(7)\n\t" \
  "v_mfma_f32_16x16x32_bf16 v[68:71], v[52:55], v[24:27], v[68:71]\n\t" \
  "ds_read_b64_tr_b16 v[48:49], v59 offset:8192\n\t" \
  "v_mfma_f32_16x16x32_bf16 v[76:79], v[52:55], v[28:31], v[76:79]\n\t" \
  "ds_read_b64_tr_b16 v[50:51], v59 offset:8704\n\t" \
  "s_addk_i32 %[it], 1\n\t" \
  "v_mfma_f32_16x16x32_bf16 v[84:87], v[52:55], v[32:35], v[84:87]\n\t" \
  "s_cmp_lt_u32 %[it], 6\n\t" \
  "v_mfma_f32_16x16x32_bf16 v[92:95], v[52:55], v[36:39], v[92:95]\n\t" \
  "s_cbranch_scc1 N0b_%=\n\t" \
  "s_setprio 3\n\t" \
  "v_cvt_pk_bf16_f32 v160, v160, v161\n\t" \
  "v_cvt_pk_bf16_f32 v161, v162, v163\n\t" \
  "v_cvt_pk_bf16_f32 v162, v164, v165\n\t" \
  "v_cvt_pk_bf16_f32 v163, v166, v167\n\t" \
  "ds_write_b128 v63, v[160:163] offset:2048\n\t" \
  "v_mov_b32 v57, %[fa]\n\t" \
  "v_mov_b32 v58, 1\n\t" \
  "s_mov_b64 exec, 1\n\t" \
  "ds_write_b32 v57, v58\n\t" \
  "s_mov_b64 exec, -1\n\t" \
  "v_cvt_pk_bf16_f32 v120, v120, v121\n\t" \
  "v_cvt_pk_bf16_f32 v121, v122, v123\n\t" \
  "v_cvt_pk_bf16_f32 v122, v124, v125\n\t" \
  "v_cvt_pk_bf16_f32 v123, v126, v127\n\t" \
  "v_cvt_pk_bf16_f32 v28, v152, v153\n\t" \
  "v_cvt_pk_bf16_f32 v29, v154, v155\n\t" \
  "v_cvt_pk_bf16_f32 v38, v156, v157\n\t" \
  "v_cvt_pk_bf16_f32 v39, v158, v159\n\t" \
  "NX_%=:\n\t" \
  "v_cvt_pk_bf16_f32 v96, v96, v97\n\t" \
  "v_cvt_pk_bf16_f32 v97, v98, v99\n\t" \
  "v_cvt_pk_bf16_f32 v98, v100, v101\n\t" \
  "v_cvt_pk_bf16_f32 v99, v102, v103\n\t" \
  "v_cvt_pk_bf16_f32 v104, v104, v105\n\t" \
  "v_cvt_pk_bf16_f32 v105, v106, v107\n\t" \
  "v_cvt_pk_bf16_f32 v106, v108, v109\n\t" \
  "v_cvt_pk_bf16_f32 v107, v110, v111\n\t" \
  "v_mov_b32 v60, 0xff61b1e6\n\t" \
  "v_cndmask_b32_e64 v56, v60, 0, s[68:69]\n\t" \
  "v_cndmask_b32_e64 v57, v60, 0, s[70:71]\n\t" \
  "v_cndmask_b32_e64 v58, v60, 0, s[72:73]\n\t" \
  "v_cndmask_b32_e64 v59, v60, 0, s[74:75]\n\t" \
  "v_cvt_pk_bf16_f32 v72, v72, v73\n\t" \
  "v_cvt_pk_bf16_f32 v73, v74, v75\n\t" \
  "v_cvt_pk_bf16_f32 v74, v76, v77\n\t" \
  "v_cvt_pk_bf16_f32 v75, v78, v79\n\t" \
  "v_cndmask_b32_e64 v52, v60, 0, s[76:77]\n\t" \
  "v_cndmask_b32_e64 v53, v60, 0, s[78:79]\n\t" \
  "v_cndmask_b32_e64 v54, v60, 0, s[80:81]\n\t" \
  "v_cndmask_b32_e64 v55, v60, 0, s[82:83]\n\t" \
  "v_cvt_pk_bf16_f32 v24, v136, v137\n\t" \
  "v_cvt_pk_bf16_f32 v25, v138, v139\n\t" \
  "v_cvt_pk_bf16_f32 v34, v140, v141\n\t" \
  "v_cvt_pk_bf16_f32 v35, v142, v143\n\t" \
  "v_cvt_pk_bf16_f32 v26, v144, v145\n\t" \
  "v_cvt_pk_bf16_f32 v27, v146, v147\n\t" \
  "v_cvt_pk_bf16_f32 v36, v148, v149\n\t" \
  "v_cvt_pk_bf16_f32 v37, v150, v151\n\t" \
  "v_mfma_f32_16x16x32_bf16 v[136:139], v[96:99], v[72:75], v[56:59]\n\t" \
  "v_cvt_pk_bf16_f32 v112, v112, v113\n\t" \
  "v_cvt_pk_bf16_f32 v113, v114, v115\n\t" \
  "v_mfma_f32_16x16x32_bf16 v[140:143], v[104:107], v[72:75], v[52:55]\n\t" \
  "v_cvt_pk_bf16_f32 v114, v116, v117\n\t" \
  "v_cvt_pk_bf16_f32 v115, v118, v119\n\t" \
  "v_cvt_pk_bf16_f32 v80, v80, v81\n\t" \
  "v_cvt_pk_bf16_f32 v81, v82, v83\n\t" \
  "v_cvt_pk_bf16_f32 v82, v84, v85\n\t" \
  "v_cvt_pk_bf16_f32 v83, v86, v87\n\t" \
  "v_cvt_pk_bf16_f32 v22, v128, v129\n\t" \
  "v_cvt_pk_bf16_f32 v23, v130, v131\n\t" \
  "v_mfma_f32_16x16x32_bf16 v[144:147], v[104:107], v[80:83], v[56:59]\n\t" \
  "v_cvt_pk_bf16_f32 v32, v132, v133\n\t" \
  "v_cvt_pk_bf16_f32 v33, v134, v135\n\t" \
  "v_mfma_f32_16x16x32_bf16 v[148:151], v[112:115], v[80:83], v[52:55]\n\t" \
  "v_cvt_pk_bf16_f32 v88, v88, v89\n\t" \
  "v_cvt_pk_bf16_f32 v89, v90, v91\n\t" \
  "v_cvt_pk_bf16_f32 v90, v92, v93\n\t" \
  "v_cvt_pk_bf16_f32 v91, v94, v95\n\t" \
  "v_mov_b32 v61, %[fp]\n\t" \
  "v_add_u32 v60, %[vo], v63\n\t" \
  "v_mfma_f32_16x16x32_bf16 v[152:155], v[112:115], v[88:91], v[56:59]\n\t" \
  "ds_read_b32 v0, v61\n\t" \
  "ds_read_b64 v[20:21], v60\n\t" \
  "ds_read_b64 v[30:31], v60 offset:8\n\t" \
  "s_mov_b64 exec, %[rm]\n\t" \
  "ds_read_b128 v[160:163], v63\n\t" \
  "s_mov_b64 exec, -1\n\t" \
  "v_mfma_f32_16x16x32_bf16 v[156:159], v[120:123], v[88:91], v[52:55]\n\t" \
  "v_cvt_pk_bf16_f32 v64, v64, v65\n\t" \
  "v_cvt_pk_bf16_f32 v65, v66, v67\n\t" \
  "v_cvt_pk_bf16_f32 v66, v68, v69\n\t" \
  "v_cvt_pk_bf16_f32 v67, v70, v71\n\t" \
  "v_max_f32 v136, v136, v137\n\t" \
  "v_max_f32 v138, v138, v139\n\t" \
  "v_mfma_f32_16x16x32_bf16 v[132:135], v[96:99], v[64:67], v[52:55]\n\t" \
  "v_max_f32 v140, v140, v141\n\t" \
  "v_max_f32 v142, v142, v143\n\t" \
  "AW_%=:\n\t" \
  "s_waitcnt lgkmcnt(0)\n\t" \
  "v_cmp_eq_u32 vcc, 0, v0\n\t" \
  "v_max3_f32 v5, v136, v138, v140\n\t" \
  "v_max_f32 v5, v5, v142\n\t" \
  "s_cbranch_vccnz AR_%=\n\t" \
  "s_cmp_eq_u32 %[z], 0\n\t" \
  "s_cbranch_scc1 AZ_%=\n\t" \
  "AY_%=:\n\t" \
  "v_mfma_f32_16x16x32_bf16 v[128:131], v[160:163], v[64:67], v[56:59]\n\t" \
  "v_max_f32 v144, v144, v145\n\t" \
  "v_max_f32 v146, v146, v147\n\t" \
  "v_max_f32 v148, v148, v149\n\t" \
  "v_max_f32 v150, v150, v151\n\t" \
  "v_max3_f32 v6, v144, v146, v148\n\t" \
  "v_max_f32 v6, v6, v150\n\t" \
  "v_max_f32 v152, v152, v153\n\t" \
  "v_max_f32 v154, v154, v155\n\t" \
  "v_max_f32 v156, v156, v157\n\t" \
  "v_max_f32 v158, v158, v159\n\t" \
  "v_max3_f32 v7, v152, v154, v156\n\t" \
  "v_max_f32 v7, v7, v158\n\t" \
  "v_mov_b32 v8, 0xc0c0100\n\t" \
  "v_mov_b32 v9, 0x1000c0c\n\t" \
  "v_max_f32 v128, v128, v129\n\t" \
  "v_max_f32 v130, v130, v131\n\t" \
  "v_max_f32 v132, v132, v133\n\t" \
  "v_max_f32 v134, v134, v135\n\t" \
  "v_max3_f32 v4, v128, v130, v132\n\t" \
  "v_max_f32 v4, v4, v134\n\t" \
  "v_cndmask_b32_e64 v60, v8, v9, s[84:85]\n\t" \
  "v_mov_b32 v8, 0xc0c0302\n\t" \
  "v_permlane16_swap_b32 v6, v7\n\t" \
  "v_permlane16_swap_b32 v4, v5\n\t" \
  "v_max_f32 v6, v6, v7\n\t" \
  "v_max_f32 v4, v4, v5\n\t" \
  "v_mov_b32 v9, 0x3020c0c\n\t" \
  "v_cndmask_b32_e64 v61, v8, v9, s[84:85]\n\t" \
  "v_permlane32_swap_b32 v4, v6\n\t" \
  "v_max_f32 v4, v4, v6\n\t" \
  "v_mul_f32 v6, 0xbe8293ee, v4\n\t" \
  "v_mul_f32 v4, 0xbe8293ee, v4\n\t" \
  "s_nop 1\n\t" \
  "v_permlane32_swap_b32 v4, v6\n\t" \
  "v_mov_b32 v5, v4\n\t" \
  "v_mov_b32 v7, v6\n\t" \
  "s_nop 0\n\t" \
  "v_permlane16_swap_b32 v4, v5\n\t" \
  "v_permlane16_swap_b32 v6, v7\n\t" \
  "v_fmamk_f32 v128, v128, 0x3e8293ee, v4\n\t" \
  "v_fmamk_f32 v130, v130, 0x3e8293ee, v4\n\t" \
  "v_fmamk_f32 v132, v132, 0x3e8293ee, v4\n\t" \
  "v_fmamk_f32 v134, v134, 0x3e8293ee, v4\n\t" \
  "v_fmamk_f32 v136, v136, 0x3e8293ee, v5\n\t" \
  "v_fmamk_f32 v138, v138, 0x3e8293ee, v5\n\t" \
  "v_fmamk_f32 v140, v140, 0x3e8293ee, v5\n\t" \
  "v_fmamk_f32 v142, v142, 0x3e8293ee, v5\n\t" \
  "v_fmamk_f32 v144, v144, 0x3e8293ee, v6\n\t" \
  "v_fmamk_f32 v146, v146, 0x3e8293ee, v6\n\t" \
  "v_fmamk_f32 v148, v148, 0x3e8293ee, v6\n\t" \
  "v_fmamk_f32 v150, v150, 0x3e8293ee, v6\n\t" \
  "v_fmamk_f32 v152, v152, 0x3e8293ee, v7\n\t" \
  "v_fmamk_f32 v154, v154, 0x3e8293ee, v7\n\t" \
  "v_fmamk_f32 v156, v156, 0x3e8293ee, v7\n\t" \
  "v_fmamk_f32 v158, v158, 0x3e8293ee, v7\n\t" \
  "v_exp_f32 v128, v128\n\t" \
  "v_exp_f32 v130, v130\n\t" \
  "v_exp_f32 v132, v132\n\t" \
  "v_exp_f32 v134, v134\n\t" \
  "v_exp_f32 v136, v136\n\t" \
  "v_exp_f32 v138, v138\n\t" \
  "v_exp_f32 v140, v140\n\t" \
  "v_exp_f32 v142, v142\n\t" \
  "v_exp_f32 v144, v144\n\t" \
  "v_exp_f32 v146, v146\n\t" \
  "v_exp_f32 v148, v148\n\t" \
  "v_exp_f32 v150, v150\n\t" \
  "v_exp_f32 v152, v152\n\t" \
  "v_exp_f32 v154, v154\n\t" \
  "v_exp_f32 v156, v156\n\t" \
  "v_exp_f32 v158, v158\n\t" \
  "v_add_f32 v4, v128, v130\n\t" \
  "v_add_f32 v8, v132, v134\n\t" \
  "v_add_f32 v5, v136, v138\n\t" \
  "v_add_f32 v9, v140, v142\n\t" \
  "v_add_f32 v6, v144, v146\n\t" \
  "v_add_f32 v10, v148, v150\n\t" \
  "v_add_f32 v7, v152, v154\n\t" \
  "v_add_f32 v11, v156, v158\n\t" \
  "v_add_f32 v4, v4, v8\n\t" \
  "v_add_f32 v5, v5, v9\n\t" \
  "v_add_f32 v6, v6, v10\n\t" \
  "v_add_f32 v7, v7, v11\n\t" \
  "v_permlane16_swap_b32 v4, v5\n\t" \
  "s_nop 0\n\t" \
  "v_permlane16_swap_b32 v6, v7\n\t" \
  "v_add_f32 v4, v4, v5\n\t" \
  "v_add_f32 v6, v6, v7\n\t" \
  "s_nop 1\n\t" \
  "v_permlane32_swap_b32 v4, v6\n\t" \
  "v_add_f32 v4, v4, v6\n\t" \
  "v_rcp_f32 v4, v4\n\t" \
  "s_nop 0\n\t" \
  "v_mov_b32 v6, v4\n\t" \
  "s_nop 1\n\t" \
  "v_permlane32_swap_b32 v4, v6\n\t" \
  "v_mov_b32 v5, v4\n\t" \
  "v_mov_b32 v7, v6\n\t" \
  "s_nop 0\n\t" \
  "v_permlane16_swap_b32 v4, v5\n\t" \
  "v_permlane16_swap_b32 v6, v7\n\t" \
  "v_mul_f32 v128, v128, v4\n\t" \
  "v_mul_f32 v130, v130, v4\n\t" \
  "v_mul_f32 v132, v132, v4\n\t" \
  "v_mul_f32 v134, v134, v4\n\t" \
  "v_cvt_pk_bf16_f32 v128, v128, v130\n\t" \
  "v_cvt_pk_bf16_f32 v132, v132, v134\n\t" \
  "v_perm_b32 v68, 0, v128, v60\n\t" \
  "v_perm_b32 v69, 0, v128, v61\n\t" \
  "v_perm_b32 v70, 0, v132, v60\n\t" \
  "v_perm_b32 v71, 0, v132, v61\n\t" \
  "v_mul_f32 v136, v136, v5\n\t" \
  "v_mul_f32 v138, v138, v5\n\t" \
  "v_mfma_f32_16x16x32_bf16 v[128:131], v[20:23], v[68:71], 0\n\t" \
  "v_mul_f32 v140, v140, v5\n\t" \
  "v_mul_f32 v142, v142, v5\n\t" \
  "v_cvt_pk_bf16_f32 v136, v136, v138\n\t" \
  "v_mfma_f32_16x16x32_bf16 v[132:135], v[30:33], v[68:71], 0\n\t" \
  "v_cvt_pk_bf16_f32 v140, v140, v142\n\t" \
  "v_perm_b32 v76, 0, v136, v60\n\t" \
  "v_perm_b32 v77, 0, v136, v61\n\t" \
  "v_perm_b32 v78, 0, v140, v60\n\t" \
  "v_perm_b32 v79, 0, v140, v61\n\t" \
  "v_mul_f32 v144, v144, v6\n\t" \
  "v_mul_f32 v146, v146, v6\n\t" \
  "v_mfma_f32_16x16x32_bf16 v[136:139], v[22:25], v[76:79], 0\n\t" \
  "v_mul_f32 v148, v148, v6\n\t" \
  "v_mul_f32 v150, v150, v6\n\t" \
  "v_cvt_pk_bf16_f32 v144, v144, v146\n\t" \
  "v_mfma_f32_16x16x32_bf16 v[140:143], v[32:35], v[76:79], 0\n\t" \
  "v_cvt_pk_bf16_f32 v148, v148, v150\n\t" \
  "v_perm_b32 v84, 0, v144, v60\n\t" \
  "v_perm_b32 v85, 0, v144, v61\n\t" \
  "v_perm_b32 v86, 0, v148, v60\n\t" \
  "v_perm_b32 v87, 0, v148, v61\n\t" \
  "v_cvt_pk_bf16_f32 v100, v128, v129\n\t" \
  "v_cvt_pk_bf16_f32 v101, v130, v131\n\t" \
  "v_mfma_f32_16x16x32_bf16 v[144:147], v[24:27], v[84:87], 0\n\t" \
  "v_cvt_pk_bf16_f32 v102, v132, v133\n\t" \
  "v_cvt_pk_bf16_f32 v103, v134, v135\n\t" \
  "global_store_dwordx4 v62, v[100:103], %[ot] offset:0 sc1\n\t" \
  "v_mfma_f32_16x16x32_bf16 v[148:151], v[34:37], v[84:87], 0\n\t" \
  "v_mul_f32 v152, v152, v7\n\t" \
  "v_mul_f32 v154, v154, v7\n\t" \
  "v_mul_f32 v156, v156, v7\n\t" \
  "v_mul_f32 v158, v158, v7\n\t" \
  "v_cvt_pk_bf16_f32 v152, v152, v154\n\t" \
  "v_cvt_pk_bf16_f32 v156, v156, v158\n\t" \
  "v_perm_b32 v92, 0, v152, v60\n\t" \
  "v_perm_b32 v93, 0, v152, v61\n\t" \
  "v_perm_b32 v94, 0, v156, v60\n\t" \
  "v_perm_b32 v95, 0, v156, v61\n\t" \
  "v_cvt_pk_bf16_f32 v108, v136, v137\n\t" \
  "v_cvt_pk_bf16_f32 v109, v138, v139\n\t" \
  "v_mfma_f32_16x16x32_bf16 v[152:155], v[26:29], v[92:95], 0\n\t" \
  "v_cvt_pk_bf16_f32 v110, v140, v141\n\t" \
  "v_cvt_pk_bf16_f32 v111, v142, v143\n\t" \
  "global_store_dwordx4 v62, v[108:111], %[ot] offset:1024 sc1\n\t" \
  "v_mfma_f32_16x16x32_bf16 v[156:159], v[36:39], v[92:95], 0\n\t" \
  "v_cvt_pk_bf16_f32 v116, v144, v145\n\t" \
  "v_cvt_pk_bf16_f32 v117, v146, v147\n\t" \
  "v_cvt_pk_bf16_f32 v118, v148, v149\n\t" \
  "v_cvt_pk_bf16_f32 v119, v150, v151\n\t" \
  "global_store_dwordx4 v62, v[116:119], %[ou] offset:0 sc1\n\t" \
  "v_cvt_pk_bf16_f32 v124, v152, v153\n\t" \
  "v_cvt_pk_bf16_f32 v125, v154, v155\n\t" \
  "s_nop 0\n\t" \
  "v_cvt_pk_bf16_f32 v126, v156, v157\n\t" \
  "v_cvt_pk_bf16_f32 v127, v158, v159\n\t" \
  "global_store_dwordx4 v62, v[124:127], %[ou] offset:1024 sc1\n\t" \
  "s_branch AE_%=\n\t" \
  "AR_%=:\n\t" \
  "s_addk_i32 %[it], 1\n\t" \
  "s_cmp_gt_u32 %[it], 0x400000\n\t" \
  "s_cbranch_scc1 AY_%=\n\t" \
  "ds_read_b32 v0, v61\n\t" \
  "ds_read_b64 v[20:21], v60\n\t" \
  "ds_read_b64 v[30:31], v60 offset:8\n\t" \
  "s_mov_b64 exec, %[rm]\n\t" \
  "ds_read_b128 v[160:163], v63\n\t" \
  "s_mov_b64 exec, -1\n\t" \
  "s_branch AW_%=\n\t" \
  "AZ_%=:\n\t" \
  "v_mov_b32 v0, 0xff61b1e6\n\t" \
  "v_mov_b32 v1, 0xcfa8965f\n\t" \
  "s_nop 3\n\t" \
  "v_cndmask_b32_e64 v56, v0, v1, s[68:69]\n\t" \
  "v_cndmask_b32_e64 v57, v0, v1, s[70:71]\n\t" \
  "v_cndmask_b32_e64 v58, v0, v1, s[72:73]\n\t" \
  "v_cndmask_b32_e64 v59, v0, v1, s[74:75]\n\t" \
  "s_nop 1\n\t" \
  "s_branch AY_%=\n\t" \
  "AE_%=:\n\t" \
  "s_nop 1\n\t"
#define N2_CLOB "v0", "v1", "v2", "v3", "v4", "v5", "v6", "v7", "v8", "v9", "v10", "v11", "v12", "v13", "v14", "v15", "v16", "v17", "v18", "v19", "v20", "v21", "v22", "v23", "v24", "v25", "v26", "v27", "v28", "v29", "v30", "v31", "v32", "v33", "v34", "v35", "v36", "v37", "v38", "v39", "v40", "v41", "v42", "v43", "v44", "v45", "v46", "v47", "v48", "v49", "v50", "v51", "v52", "v53", "v54", "v55", "v60", "v61", "v64", "v65", "v66", "v67", "v68", "v69", "v70", "v71", "v72", "v73", "v74", "v75", "v76", "v77", "v78", "v79", "v80", "v81", "v82", "v83", "v84", "v85", "v86", "v87", "v88", "v89", "v90", "v91", "v92", "v93", "v94", "v95", "v96", "v97", "v98", "v99", "v100", "v101", "v102", "v103", "v104", "v105", "v106", "v107", "v108", "v109", "v110", "v111", "v112", "v113", "v114", "v115", "v116", "v117", "v118", "v119", "v120", "v121", "v122", "v123", "v124", "v125", "v126", "v127", "v128", "v129", "v130", "v131", "v132", "v133", "v134", "v135", "v136", "v137", "v138", "v139", "v140", "v141", "v142", "v143", "v144", "v145", "v146", "v147", "v148", "v149", "v150", "v151", "v152", "v153", "v154", "v155", "v156", "v157", "v158", "v159", "v160", "v161", "v162", "v163", "v164", "v165", "v166", "v167", "s68", "s69", "s70", "s71", "s72", "s73", "s74", "s75", "s76", "s77", "s78", "s79", "s80", "s81", "s82", "s83", "s84", "s85"

#define CV_ASM \
  "ds_read_b64 v[0:1], v28 offset:0\n\t" \
  "ds_read_b64 v[2:3], v29 offset:0\n\t" \
  "ds_read_b64 v[4:5], v30 offset:1024\n\t" \
  "ds_read_b64 v[6:7], v31 offset:1024\n\t" \
  "ds_read_b64 v[8:9], v24 offset:2048\n\t" \
  "ds_read_b64 v[10:11], v25 offset:2048\n\t" \
  "ds_read_b64 v[12:13], v26 offset:3072\n\t" \
  "ds_read_b64 v[14:15], v27 offset:3072\n\t" \
  "ds_read_b64 v[16:17], v28 offset:4096\n\t" \
  "ds_read_b64 v[18:19], v29 offset:4096\n\t" \
  "ds_read_b64 v[20:21], v30 offset:5120\n\t" \
  "ds_read_b64 v[22:23], v31 offset:5120\n\t" \
  "s_waitcnt lgkmcnt(10)\n\t" \
  "v_mfma_f32_16x16x32_bf16 v[128:131], v[68:71], v[0:3], 0\n\t" \
  "v_mfma_f32_16x16x32_bf16 v[132:135], v[76:79], v[0:3], 0\n\t" \
  "v_mfma_f32_16x16x32_bf16 v[136:139], v[84:87], v[0:3], 0\n\t" \
  "v_mfma_f32_16x16x32_bf16 v[140:143], v[92:95], v[0:3], 0\n\t" \
  "v_mfma_f32_16x16x32_bf16 v[144:147], v[100:103], v[0:3], 0\n\t" \
  "v_mfma_f32_16x16x32_bf16 v[148:151], v[108:111], v[0:3], 0\n\t" \
  "v_mfma_f32_16x16x32_bf16 v[152:155], v[116:119], v[0:3], 0\n\t" \
  "v_mfma_f32_16x16x32_bf16 v[156:159], v[124:127], v[0:3], 0\n\t" \
  "ds_read_b64 v[0:1], v24 offset:6144\n\t" \
  "s_waitcnt lgkmcnt(9)\n\t" \
  "v_mfma_f32_16x16x32_bf16 v[128:131], v[64:67], v[4:7], v[128:131]\n\t" \
  "ds_read_b64 v[2:3], v25 offset:6144\n\t" \
  "v_mfma_f32_16x16x32_bf16 v[132:135], v[72:75], v[4:7], v[132:135]\n\t" \
  "v_mfma_f32_16x16x32_bf16 v[136:139], v[80:83], v[4:7], v[136:139]\n\t" \
  "v_mfma_f32_16x16x32_bf16 v[140:143], v[88:91], v[4:7], v[140:143]\n\t" \
  "v_mfma_f32_16x16x32_bf16 v[144:147], v[96:99], v[4:7], v[144:147]\n\t" \
  "v_mfma_f32_16x16x32_bf16 v[148:151], v[104:107], v[4:7], v[148:151]\n\t" \
  "v_mfma_f32_16x16x32_bf16 v[152:155], v[112:115], v[4:7], v[152:155]\n\t" \
  "v_mfma_f32_16x16x32_bf16 v[156:159], v[120:123], v[4:7], v[156:159]\n\t" \
  "ds_read_b64 v[4:5], v26 offset:7168\n\t" \
  "ds_bpermute_b32 v34, v32, v131\n\t" \
  "v_readlane_b32 %[p0], v131, 63\n\t" \
  "s_waitcnt lgkmcnt(10)\n\t" \
  "v_mfma_f32_16x16x32_bf16 v[132:135], v[68:71], v[8:11], v[132:135]\n\t" \
  "ds_read_b64 v[6:7], v27 offset:7168\n\t" \
  "v_mov_b32 v131, v130\n\t" \
  "v_mfma_f32_16x16x32_bf16 v[136:139], v[76:79], v[8:11], v[136:139]\n\t" \
  "v_mov_b32 v130, v129\n\t" \
  "v_mfma_f32_16x16x32_bf16 v[140:143], v[84:87], v[8:11], v[140:143]\n\t" \
  "v_mov_b32 v129, v128\n\t" \
  "v_mfma_f32_16x16x32_bf16 v[144:147], v[92:95], v[8:11], v[144:147]\n\t" \
  "v_mfma_f32_16x16x32_bf16 v[148:151], v[100:103], v[8:11], v[148:151]\n\t" \
  "v_mfma_f32_16x16x32_bf16 v[152:155], v[108:111], v[8:11], v[152:155]\n\t" \
  "v_mfma_f32_16x16x32_bf16 v[156:159], v[116:119], v[8:11], v[156:159]\n\t" \
  "ds_read_b64 v[8:9], v28 offset:8192\n\t" \
  "s_waitcnt lgkmcnt(10)\n\t" \
  "v_mfma_f32_16x16x32_bf16 v[132:135], v[64:67], v[12:15], v[132:135]\n\t" \
  "ds_read_b64 v[10:11], v29 offset:8192\n\t" \
  "v_mfma_f32_16x16x32_bf16 v[136:139], v[72:75], v[12:15], v[136:139]\n\t" \
  "v_mfma_f32_16x16x32_bf16 v[140:143], v[80:83], v[12:15], v[140:143]\n\t" \
  "v_mfma_f32_16x16x32_bf16 v[144:147], v[88:91], v[12:15], v[144:147]\n\t" \
  "v_mfma_f32_16x16x32_bf16 v[148:151], v[96:99], v[12:15], v[148:151]\n\t" \
  "s_waitcnt lgkmcnt(3)\n\t" \
  "v_mov_b32 v128, v34\n\t" \
  "v_writelane_b32 v128, 0, 0\n\t" \
  "v_mfma_f32_16x16x32_bf16 v[152:155], v[104:107], v[12:15], v[152:155]\n\t" \
  "v_mfma_f32_16x16x32_bf16 v[156:159], v[112:115], v[12:15], v[156:159]\n\t" \
  "ds_read_b64 v[12:13], v30 offset:9216\n\t" \
  "s_waitcnt lgkmcnt(10)\n\t" \
  "v_mfma_f32_16x16x32_bf16 v[136:139], v[68:71], v[16:19], v[136:139]\n\t" \
  "ds_read_b64 v[14:15], v31 offset:9216\n\t" \
  "ds_bpermute_b32 v35, v32, v135\n\t" \
  "v_readlane_b32 %[p1], v135, 63\n\t" \
  "v_mfma_f32_16x16x32_bf16 v[140:143], v[76:79], v[16:19], v[140:143]\n\t" \
  "v_mov_b32 v135, v134\n\t" \
  "v_mfma_f32_16x16x32_bf16 v[144:147], v[84:87], v[16:19], v[144:147]\n\t" \
  "v_mov_b32 v134, v133\n\t" \
  "v_mfma_f32_16x16x32_bf16 v[148:151], v[92:95], v[16:19], v[148:151]\n\t" \
  "v_mov_b32 v133, v132\n\t" \
  "v_mfma_f32_16x16x32_bf16 v[152:155], v[100:103], v[16:19], v[152:155]\n\t" \
  "v_mfma_f32_16x16x32_bf16 v[156:159], v[108:111], v[16:19], v[156:159]\n\t" \
  "ds_read_b64 v[16:17], v24 offset:10240\n\t" \
  "s_waitcnt lgkmcnt(11)\n\t" \
  "v_mfma_f32_16x16x32_bf16 v[136:139], v[64:67], v[20:23], v[136:139]\n\t" \
  "ds_read_b64 v[18:19], v25 offset:10240\n\t" \
  "v_mfma_f32_16x16x32_bf16 v[140:143], v[72:75], v[20:23], v[140:143]\n\t" \
  "v_mfma_f32_16x16x32_bf16 v[144:147], v[80:83], v[20:23], v[144:147]\n\t" \
  "v_mfma_f32_16x16x32_bf16 v[148:151], v[88:91], v[20:23], v[148:151]\n\t" \
  "v_mfma_f32_16x16x32_bf16 v[152:155], v[96:99], v[20:23], v[152:155]\n\t" \
  "v_mfma_f32_16x16x32_bf16 v[156:159], v[104:107], v[20:23], v[156:159]\n\t" \
  "ds_read_b64 v[20:21], v26 offset:11264\n\t" \
  "s_waitcnt lgkmcnt(11)\n\t" \
  "v_mfma_f32_16x16x32_bf16 v[140:143], v[68:71], v[0:3], v[140:143]\n\t" \
  "ds_read_b64 v[22:23], v27 offset:11264\n\t" \
  "s_waitcnt lgkmcnt(4)\n\t" \
  "v_mov_b32 v132, v35\n\t" \
  "v_writelane_b32 v132, %[p0], 0\n\t" \
  "v_mfma_f32_16x16x32_bf16 v[144:147], v[76:79], v[0:3], v[144:147]\n\t" \
  "ds_bpermute_b32 v36, v32, v139\n\t" \
  "v_readlane_b32 %[p2], v139, 63\n\t" \
  "v_mfma_f32_16x16x32_bf16 v[148:151], v[84:87], v[0:3], v[148:151]\n\t" \
  "v_mov_b32 v139, v138\n\t" \
  "v_mfma_f32_16x16x32_bf16 v[152:155], v[92:95], v[0:3], v[152:155]\n\t" \
  "v_mov_b32 v138, v137\n\t" \
  "v_mfma_f32_16x16x32_bf16 v[156:159], v[100:103], v[0:3], v[156:159]\n\t" \
  "ds_read_b64 v[0:1], v28 offset:12288\n\t" \
  "v_mov_b32 v137, v136\n\t" \
  "s_waitcnt lgkmcnt(11)\n\t" \
  "v_mfma_f32_16x16x32_bf16 v[140:143], v[64:67], v[4:7], v[140:143]\n\t" \
  "ds_read_b64 v[2:3], v29 offset:12288\n\t" \
  "v_mfma_f32_16x16x32_bf16 v[144:147], v[72:75], v[4:7], v[144:147]\n\t" \
  "v_mfma_f32_16x16x32_bf16 v[148:151], v[80:83], v[4:7], v[148:151]\n\t" \
  "v_mfma_f32_16x16x32_bf16 v[152:155], v[88:91], v[4:7], v[152:155]\n\t" \
  "v_mfma_f32_16x16x32_bf16 v[156:159], v[96:99], v[4:7], v[156:159]\n\t" \
  "ds_read_b64 v[4:5], v30 offset:13312\n\t" \
  "s_waitcnt lgkmcnt(11)\n\t" \
  "v_mfma_f32_16x16x32_bf16 v[144:147], v[68:71], v[8:11], v[144:147]\n\t" \
  "ds_read_b64 v[6:7], v31 offset:13312\n\t" \
  "v_mfma_f32_16x16x32_bf16 v[148:151], v[76:79], v[8:11], v[148:151]\n\t" \
  "v_mfma_f32_16x16x32_bf16 v[152:155], v[84:87], v[8:11], v[152:155]\n\t" \
  "ds_bpermute_b32 v37, v32, v143\n\t" \
  "v_readlane_b32 %[p3], v143, 63\n\t" \
  "v_mfma_f32_16x16x32_bf16 v[156:159], v[92:95], v[8:11], v[156:159]\n\t" \
  "ds_read_b64 v[8:9], v24 offset:14336\n\t" \
  "s_waitcnt lgkmcnt(6)\n\t" \
  "v_mov_b32 v136, v36\n\t" \
  "v_writelane_b32 v136, %[p1], 0\n\t" \
  "v_mov_b32 v143, v142\n\t" \
  "s_waitcnt lgkmcnt(12)\n\t" \
  "v_mfma_f32_16x16x32_bf16 v[144:147], v[64:67], v[12:15], v[144:147]\n\t" \
  "ds_read_b64 v[10:11], v25 offset:14336\n\t" \
  "v_mov_b32 v142, v141\n\t" \
  "v_mfma_f32_16x16x32_bf16 v[148:151], v[72:75], v[12:15], v[148:151]\n\t" \
  "v_mov_b32 v141, v140\n\t" \
  "v_mfma_f32_16x16x32_bf16 v[152:155], v[80:83], v[12:15], v[152:155]\n\t" \
  "v_mfma_f32_16x16x32_bf16 v[156:159], v[88:91], v[12:15], v[156:159]\n\t" \
  "ds_read_b64 v[12:13], v26 offset:15360\n\t" \
  "s_waitcnt lgkmcnt(11)\n\t" \
  "v_mfma_f32_16x16x32_bf16 v[148:151], v[68:71], v[16:19], v[148:151]\n\t" \
  "ds_read_b64 v[14:15], v27 offset:15360\n\t" \
  "v_mfma_f32_16x16x32_bf16 v[152:155], v[76:79], v[16:19], v[152:155]\n\t" \
  "v_mfma_f32_16x16x32_bf16 v[156:159], v[84:87], v[16:19], v[156:159]\n\t" \
  "ds_read_b64 v[16:17], v26 offset:0\n\t" \
  "s_waitcnt lgkmcnt(11)\n\t" \
  "v_mfma_f32_16x16x32_bf16 v[148:151], v[64:67], v[20:23], v[148:151]\n\t" \
  "ds_read_b64 v[18:19], v27 offset:0\n\t" \
  "ds_bpermute_b32 v38, v32, v147\n\t" \
  "v_readlane_b32 %[p4], v147, 63\n\t" \
  "v_mfma_f32_16x16x32_bf16 v[152:155], v[72:75], v[20:23], v[152:155]\n\t" \
  "v_mov_b32 v147, v146\n\t" \
  "v_mfma_f32_16x16x32_bf16 v[156:159], v[80:83], v[20:23], v[156:159]\n\t" \
  "ds_read_b64 v[20:21], v28 offset:1024\n\t" \
  "v_mov_b32 v146, v145\n\t" \
  "s_waitcnt lgkmcnt(11)\n\t" \
  "v_mfma_f32_16x16x32_bf16 v[152:155], v[68:71], v[0:3], v[152:155]\n\t" \
  "ds_read_b64 v[22:23], v29 offset:1024\n\t" \
  "s_waitcnt lgkmcnt(9)\n\t" \
  "v_mov_b32 v140, v37\n\t" \
  "v_writelane_b32 v140, %[p2], 0\n\t" \
  "v_mov_b32 v145, v144\n\t" \
  "v_mfma_f32_16x16x32_bf16 v[156:159], v[76:79], v[0:3], v[156:159]\n\t" \
  "ds_read_b64 v[0:1], v30 offset:2048\n\t" \
  "ds_bpermute_b32 v39, v32, v151\n\t" \
  "v_readlane_b32 %[p5], v151, 63\n\t" \
  "s_waitcnt lgkmcnt(12)\n\t" \
  "v_mfma_f32_16x16x32_bf16 v[152:155], v[64:67], v[4:7], v[152:155]\n\t" \
  "ds_read_b64 v[2:3], v31 offset:2048\n\t" \
  "v_mov_b32 v151, v150\n\t" \
  "v_mfma_f32_16x16x32_bf16 v[156:159], v[72:75], v[4:7], v[156:159]\n\t" \
  "ds_read_b64 v[4:5], v24 offset:3072\n\t" \
  "v_mov_b32 v150, v149\n\t" \
  "s_waitcnt lgkmcnt(11)\n\t" \
  "v_mfma_f32_16x16x32_bf16 v[156:159], v[68:71], v[8:11], v[156:159]\n\t" \
  "ds_read_b64 v[6:7], v25 offset:3072\n\t" \
  "ds_read_b64 v[8:9], v26 offset:4096\n\t" \
  "v_mov_b32 v149, v148\n\t" \
  "ds_bpermute_b32 v40, v32, v155\n\t" \
  "v_readlane_b32 %[p6], v155, 63\n\t" \
  "s_waitcnt lgkmcnt(12)\n\t" \
  "v_mfma_f32_16x16x32_bf16 v[156:159], v[64:67], v[12:15], v[156:159]\n\t" \
  "ds_read_b64 v[10:11], v27 offset:4096\n\t" \
  "ds_read_b64 v[12:13], v28 offset:5120\n\t" \
  "v_mov_b32 v155, v154\n\t" \
  "s_waitcnt lgkmcnt(12)\n\t" \
  "v_mfma_f32_16x16x32_bf16 v[128:131], v[68:71], v[16:19], v[128:131]\n\t" \
  "ds_read_b64 v[14:15], v29 offset:5120\n\t" \
  "v_mov_b32 v154, v153\n\t" \
  "v_mfma_f32_16x16x32_bf16 v[132:135], v[76:79], v[16:19], v[132:135]\n\t" \
  "s_waitcnt lgkmcnt(12)\n\t" \
  "v_mov_b32 v144, v38\n\t" \
  "v_writelane_b32 v144, %[p3], 0\n\t" \
  "v_mov_b32 v153, v152\n\t" \
  "v_mfma_f32_16x16x32_bf16 v[136:139], v[84:87], v[16:19], v[136:139]\n\t" \
  "s_waitcnt lgkmcnt(8)\n\t" \
  "v_mov_b32 v148, v39\n\t" \
  "v_writelane_b32 v148, %[p4], 0\n\t" \
  "v_mfma_f32_16x16x32_bf16 v[140:143], v[92:95], v[16:19], v[140:143]\n\t" \
  "s_waitcnt lgkmcnt(3)\n\t" \
  "v_mov_b32 v152, v40\n\t" \
  "v_writelane_b32 v152, %[p5], 0\n\t" \
  "v_mfma_f32_16x16x32_bf16 v[144:147], v[100:103], v[16:19], v[144:147]\n\t" \
  "ds_bpermute_b32 v41, v32, v159\n\t" \
  "v_mfma_f32_16x16x32_bf16 v[148:151], v[108:111], v[16:19], v[148:151]\n\t" \
  "v_mov_b32 v159, v158\n\t" \
  "v_mfma_f32_16x16x32_bf16 v[152:155], v[116:119], v[16:19], v[152:155]\n\t" \
  "v_mov_b32 v158, v157\n\t" \
  "s_waitcnt lgkmcnt(11)\n\t" \
  "v_mfma_f32_16x16x32_bf16 v[128:131], v[64:67], v[20:23], v[128:131]\n\t" \
  "v_mov_b32 v157, v156\n\t" \
  "v_mfma_f32_16x16x32_bf16 v[132:135], v[72:75], v[20:23], v[132:135]\n\t" \
  "v_mfma_f32_16x16x32_bf16 v[136:139], v[80:83], v[20:23], v[136:139]\n\t" \
  "v_mfma_f32_16x16x32_bf16 v[140:143], v[88:91], v[20:23], v[140:143]\n\t" \
  "v_mfma_f32_16x16x32_bf16 v[144:147], v[96:99], v[20:23], v[144:147]\n\t" \
  "s_waitcnt lgkmcnt(0)\n\t" \
  "v_mov_b32 v156, v41\n\t" \
  "v_writelane_b32 v156, %[p6], 0\n\t" \
  "v_mfma_f32_16x16x32_bf16 v[148:151], v[104:107], v[20:23], v[148:151]\n\t" \
  "v_mfma_f32_16x16x32_bf16 v[152:155], v[112:115], v[20:23], v[152:155]\n\t" \
  "v_mfma_f32_16x16x32_bf16 v[156:159], v[124:127], v[16:19], v[156:159]\n\t" \
  "ds_read_b64 v[16:17], v30 offset:6144\n\t" \
  "ds_bpermute_b32 v34, v32, v131\n\t" \
  "v_readlane_b32 %[p0], v131, 63\n\t" \
  "v_mfma_f32_16x16x32_bf16 v[156:159], v[120:123], v[20:23], v[156:159]\n\t" \
  "ds_read_b64 v[18:19], v31 offset:6144\n\t" \
  "ds_read_b64 v[20:21], v24 offset:7168\n\t" \
  "v_mov_b32 v131, v130\n\t" \
  "s_waitcnt lgkmcnt(12)\n\t" \
  "v_mfma_f32_16x16x32_bf16 v[132:135], v[68:71], v[0:3], v[132:135]\n\t" \
  "ds_read_b64 v[22:23], v25 offset:7168\n\t" \
  "v_mov_b32 v130, v129\n\t" \
  "v_mfma_f32_16x16x32_bf16 v[136:139], v[76:79], v[0:3], v[136:139]\n\t" \
  "v_mov_b32 v129, v128\n\t" \
  "v_mfma_f32_16x16x32_bf16 v[140:143], v[84:87], v[0:3], v[140:143]\n\t" \
  "v_mfma_f32_16x16x32_bf16 v[144:147], v[92:95], v[0:3], v[144:147]\n\t" \
  "v_mfma_f32_16x16x32_bf16 v[148:151], v[100:103], v[0:3], v[148:151]\n\t" \
  "v_mfma_f32_16x16x32_bf16 v[152:155], v[108:111], v[0:3], v[152:155]\n\t" \
  "v_mfma_f32_16x16x32_bf16 v[156:159], v[116:119], v[0:3], v[156:159]\n\t" \
  "ds_read_b64 v[0:1], v26 offset:8192\n\t" \
  "s_waitcnt lgkmcnt(12)\n\t" \
  "v_mfma_f32_16x16x32_bf16 v[132:135], v[64:67], v[4:7], v[132:135]\n\t" \
  "ds_read_b64 v[2:3], v27 offset:8192\n\t" \
  "v_mfma_f32_16x16x32_bf16 v[136:139], v[72:75], v[4:7], v[136:139]\n\t" \
  "v_mfma_f32_16x16x32_bf16 v[140:143], v[80:83], v[4:7], v[140:143]\n\t" \
  "v_mfma_f32_16x16x32_bf16 v[144:147], v[88:91], v[4:7], v[144:147]\n\t" \
  "s_waitcnt lgkmcnt(5)\n\t" \
  "v_mov_b32 v128, v34\n\t" \
  "v_writelane_b32 v128, 0, 0\n\t" \
  "v_mfma_f32_16x16x32_bf16 v[148:151], v[96:99], v[4:7], v[148:151]\n\t" \
  "v_mfma_f32_16x16x32_bf16 v[152:155], v[104:107], v[4:7], v[152:155]\n\t" \
  "v_mfma_f32_16x16x32_bf16 v[156:159], v[112:115], v[4:7], v[156:159]\n\t" \
  "ds_read_b64 v[4:5], v28 offset:9216\n\t" \
  "s_waitcnt lgkmcnt(11)\n\t" \
  "v_mfma_f32_16x16x32_bf16 v[136:139], v[68:71], v[8:11], v[136:139]\n\t" \
  "ds_read_b64 v[6:7], v29 offset:9216\n\t" \
  "ds_bpermute_b32 v35, v32, v135\n\t" \
  "v_readlane_b32 %[p1], v135, 63\n\t" \
  "v_mfma_f32_16x16x32_bf16 v[140:143], v[76:79], v[8:11], v[140:143]\n\t" \
  "v_mov_b32 v135, v134\n\t" \
  "v_mfma_f32_16x16x32_bf16 v[144:147], v[84:87], v[8:11], v[144:147]\n\t" \
  "v_mov_b32 v134, v133\n\t" \
  "v_mfma_f32_16x16x32_bf16 v[148:151], v[92:95], v[8:11], v[148:151]\n\t" \
  "v_mov_b32 v133, v132\n\t" \
  "v_mfma_f32_16x16x32_bf16 v[152:155], v[100:103], v[8:11], v[152:155]\n\t" \
  "v_mfma_f32_16x16x32_bf16 v[156:159], v[108:111], v[8:11], v[156:159]\n\t" \
  "ds_read_b64 v[8:9], v30 offset:10240\n\t" \
  "s_waitcnt lgkmcnt(12)\n\t" \
  "v_mfma_f32_16x16x32_bf16 v[136:139], v[64:67], v[12:15], v[136:139]\n\t" \
  "ds_read_b64 v[10:11], v31 offset:10240\n\t" \
  "v_mfma_f32_16x16x32_bf16 v[140:143], v[72:75], v[12:15], v[140:143]\n\t" \
  "v_mfma_f32_16x16x32_bf16 v[144:147], v[80:83], v[12:15], v[144:147]\n\t" \
  "v_mfma_f32_16x16x32_bf16 v[148:151], v[88:91], v[12:15], v[148:151]\n\t" \
  "v_mfma_f32_16x16x32_bf16 v[152:155], v[96:99], v[12:15], v[152:155]\n\t" \
  "v_mfma_f32_16x16x32_bf16 v[156:159], v[104:107], v[12:15], v[156:159]\n\t" \
  "ds_read_b64 v[12:13], v24 offset:11264\n\t" \
  "s_waitcnt lgkmcnt(10)\n\t" \
  "v_mfma_f32_16x16x32_bf16 v[140:143], v[68:71], v[16:19], v[140:143]\n\t" \
  "ds_read_b64 v[14:15], v25 offset:11264\n\t" \
  "s_waitcnt lgkmcnt(4)\n\t" \
  "v_mov_b32 v132, v35\n\t" \
  "v_writelane_b32 v132, %[p0], 0\n\t" \
  "v_mfma_f32_16x16x32_bf16 v[144:147], v[76:79], v[16:19], v[144:147]\n\t" \
  "ds_bpermute_b32 v36, v32, v139\n\t" \
  "v_readlane_b32 %[p2], v139, 63\n\t" \
  "v_mfma_f32_16x16x32_bf16 v[148:151], v[84:87], v[16:19], v[148:151]\n\t" \
  "v_mov_b32 v139, v138\n\t" \
  "v_mfma_f32_16x16x32_bf16 v[152:155], v[92:95], v[16:19], v[152:155]\n\t" \
  "v_mov_b32 v138, v137\n\t" \
  "v_mfma_f32_16x16x32_bf16 v[156:159], v[100:103], v[16:19], v[156:159]\n\t" \
  "ds_read_b64 v[16:17], v26 offset:12288\n\t" \
  "v_mov_b32 v137, v136\n\t" \
  "s_waitcnt lgkmcnt(11)\n\t" \
  "v_mfma_f32_16x16x32_bf16 v[140:143], v[64:67], v[20:23], v[140:143]\n\t" \
  "ds_read_b64 v[18:19], v27 offset:12288\n\t" \
  "v_mfma_f32_16x16x32_bf16 v[144:147], v[72:75], v[20:23], v[144:147]\n\t" \
  "v_mfma_f32_16x16x32_bf16 v[148:151], v[80:83], v[20:23], v[148:151]\n\t" \
  "v_mfma_f32_16x16x32_bf16 v[152:155], v[88:91], v[20:23], v[152:155]\n\t" \
  "v_mfma_f32_16x16x32_bf16 v[156:159], v[96:99], v[20:23], v[156:159]\n\t" \
  "ds_read_b64 v[20:21], v28 offset:13312\n\t" \
  "s_waitcnt lgkmcnt(11)\n\t" \
  "v_mfma_f32_16x16x32_bf16 v[144:147], v[68:71], v[0:3], v[144:147]\n\t" \
  "ds_read_b64 v[22:23], v29 offset:13312\n\t" \
  "v_mfma_f32_16x16x32_bf16 v[148:151], v[76:79], v[0:3], v[148:151]\n\t" \
  "v_mfma_f32_16x16x32_bf16 v[152:155], v[84:87], v[0:3], v[152:155]\n\t" \
  "ds_bpermute_b32 v37, v32, v143\n\t" \
  "v_readlane_b32 %[p3], v143, 63\n\t" \
  "v_mfma_f32_16x16x32_bf16 v[156:159], v[92:95], v[0:3], v[156:159]\n\t" \
  "ds_read_b64 v[0:1], v30 offset:14336\n\t" \
  "s_waitcnt lgkmcnt(6)\n\t" \
  "v_mov_b32 v136, v36\n\t" \
  "v_writelane_b32 v136, %[p1], 0\n\t" \
  "v_mov_b32 v143, v142\n\t" \
  "s_waitcnt lgkmcnt(12)\n\t" \
  "v_mfma_f32_16x16x32_bf16 v[144:147], v[64:67], v[4:7], v[144:147]\n\t" \
  "ds_read_b64 v[2:3], v31 offset:14336\n\t" \
  "v_mov_b32 v142, v141\n\t" \
  "v_mfma_f32_16x16x32_bf16 v[148:151], v[72:75], v[4:7], v[148:151]\n\t" \
  "v_mov_b32 v141, v140\n\t" \
  "v_mfma_f32_16x16x32_bf16 v[152:155], v[80:83], v[4:7], v[152:155]\n\t" \
  "v_mfma_f32_16x16x32_bf16 v[156:159], v[88:91], v[4:7], v[156:159]\n\t" \
  "ds_read_b64 v[4:5], v24 offset:15360\n\t" \
  "s_waitcnt lgkmcnt(11)\n\t" \
  "v_mfma_f32_16x16x32_bf16 v[148:151], v[68:71], v[8:11], v[148:151]\n\t" \
  "ds_read_b64 v[6:7], v25 offset:15360\n\t" \
  "v_mfma_f32_16x16x32_bf16 v[152:155], v[76:79], v[8:11], v[152:155]\n\t" \
  "v_mfma_f32_16x16x32_bf16 v[156:159], v[84:87], v[8:11], v[156:159]\n\t" \
  "ds_read_b64 v[8:9], v24 offset:0\n\t" \
  "s_waitcnt lgkmcnt(11)\n\t" \
  "v_mfma_f32_16x16x32_bf16 v[148:151], v[64:67], v[12:15], v[148:151]\n\t" \
  "ds_read_b64 v[10:11], v25 offset:0\n\t" \
  "ds_bpermute_b32 v38, v32, v147\n\t" \
  "v_readlane_b32 %[p4], v147, 63\n\t" \
  "v_mfma_f32_16x16x32_bf16 v[152:155], v[72:75], v[12:15], v[152:155]\n\t" \
  "v_mov_b32 v147, v146\n\t" \
  "v_mfma_f32_16x16x32_bf16 v[156:159], v[80:83], v[12:15], v[156:159]\n\t" \
  "ds_read_b64 v[12:13], v26 offset:1024\n\t" \
  "v_mov_b32 v146, v145\n\t" \
  "s_waitcnt lgkmcnt(11)\n\t" \
  "v_mfma_f32_16x16x32_bf16 v[152:155], v[68:71], v[16:19], v[152:155]\n\t" \
  "ds_read_b64 v[14:15], v27 offset:1024\n\t" \
  "s_waitcnt lgkmcnt(9)\n\t" \
  "v_mov_b32 v140, v37\n\t" \
  "v_writelane_b32 v140, %[p2], 0\n\t" \
  "v_mov_b32 v145, v144\n\t" \
  "v_mfma_f32_16x16x32_bf16 v[156:159], v[76:79], v[16:19], v[156:159]\n\t" \
  "ds_read_b64 v[16:17], v28 offset:2048\n\t" \
  "ds_bpermute_b32 v39, v32, v151\n\t" \
  "v_readlane_b32 %[p5], v151, 63\n\t" \
  "s_waitcnt lgkmcnt(12)\n\t" \
  "v_mfma_f32_16x16x32_bf16 v[152:155], v[64:67], v[20:23], v[152:155]\n\t" \
  "ds_read_b64 v[18:19], v29 offset:2048\n\t" \
  "v_mov_b32 v151, v150\n\t" \
  "v_mfma_f32_16x16x32_bf16 v[156:159], v[72:75], v[20:23], v[156:159]\n\t" \
  "ds_read_b64 v[20:21], v30 offset:3072\n\t" \
  "v_mov_b32 v150, v149\n\t" \
  "s_waitcnt lgkmcnt(11)\n\t" \
  "v_mfma_f32_16x16x32_bf16 v[156:159], v[68:71], v[0:3], v[156:159]\n\t" \
  "ds_read_b64 v[22:23], v31 offset:3072\n\t" \
  "ds_read_b64 v[0:1], v24 offset:4096\n\t" \
  "v_mov_b32 v149, v148\n\t" \
  "ds_bpermute_b32 v40, v32, v155\n\t" \
  "v_readlane_b32 %[p6], v155, 63\n\t" \
  "s_waitcnt lgkmcnt(12)\n\t" \
  "v_mfma_f32_16x16x32_bf16 v[156:159], v[64:67], v[4:7], v[156:159]\n\t" \
  "ds_read_b64 v[2:3], v25 offset:4096\n\t" \
  "ds_read_b64 v[4:5], v26 offset:5120\n\t" \
  "v_mov_b32 v155, v154\n\t" \
  "s_waitcnt lgkmcnt(12)\n\t" \
  "v_mfma_f32_16x16x32_bf16 v[128:131], v[68:71], v[8:11], v[128:131]\n\t" \
  "ds_read_b64 v[6:7], v27 offset:5120\n\t" \
  "v_mov_b32 v154, v153\n\t" \
  "v_mfma_f32_16x16x32_bf16 v[132:135], v[76:79], v[8:11], v[132:135]\n\t" \
  "s_waitcnt lgkmcnt(12)\n\t" \
  "v_mov_b32 v144, v38\n\t" \
  "v_writelane_b32 v144, %[p3], 0\n\t" \
  "v_mov_b32 v153, v152\n\t" \
  "v_mfma_f32_16x16x32_bf16 v[136:139], v[84:87], v[8:11], v[136:139]\n\t" \
  "s_waitcnt lgkmcnt(8)\n\t" \
  "v_mov_b32 v148, v39\n\t" \
  "v_writelane_b32 v148, %[p4], 0\n\t" \
  "v_mfma_f32_16x16x32_bf16 v[140:143], v[92:95], v[8:11], v[140:143]\n\t" \
  "s_waitcnt lgkmcnt(3)\n\t" \
  "v_mov_b32 v152, v40\n\t" \
  "v_writelane_b32 v152, %[p5], 0\n\t" \
  "v_mfma_f32_16x16x32_bf16 v[144:147], v[100:103], v[8:11], v[144:147]\n\t" \
  "ds_bpermute_b32 v41, v32, v159\n\t" \
  "v_mfma_f32_16x16x32_bf16 v[148:151], v[108:111], v[8:11], v[148:151]\n\t" \
  "v_mov_b32 v159, v158\n\t" \
  "v_mfma_f32_16x16x32_bf16 v[152:155], v[116:119], v[8:11], v[152:155]\n\t" \
  "v_mov_b32 v158, v157\n\t" \
  "s_waitcnt lgkmcnt(11)\n\t" \
  "v_mfma_f32_16x16x32_bf16 v[128:131], v[64:67], v[12:15], v[128:131]\n\t" \
  "v_mov_b32 v157, v156\n\t" \
  "v_mfma_f32_16x16x32_bf16 v[132:135], v[72:75], v[12:15], v[132:135]\n\t" \
  "v_mfma_f32_16x16x32_bf16 v[136:139], v[80:83], v[12:15], v[136:139]\n\t" \
  "v_mfma_f32_16x16x32_bf16 v[140:143], v[88:91], v[12:15], v[140:143]\n\t" \
  "v_mfma_f32_16x16x32_bf16 v[144:147], v[96:99], v[12:15], v[144:147]\n\t" \
  "s_waitcnt lgkmcnt(0)\n\t" \
  "v_mov_b32 v156, v41\n\t" \
  "v_writelane_b32 v156, %[p6], 0\n\t" \
  "v_mfma_f32_16x16x32_bf16 v[148:151], v[104:107], v[12:15], v[148:151]\n\t" \
  "v_mfma_f32_16x16x32_bf16 v[152:155], v[112:115], v[12:15], v[152:155]\n\t" \
  "v_mfma_f32_16x16x32_bf16 v[156:159], v[124:127], v[8:11], v[156:159]\n\t" \
  "ds_read_b64 v[8:9], v28 offset:6144\n\t" \
  "ds_bpermute_b32 v34, v32, v131\n\t" \
  "v_readlane_b32 %[p0], v131, 63\n\t" \
  "v_mfma_f32_16x16x32_bf16 v[156:159], v[120:123], v[12:15], v[156:159]\n\t" \
  "ds_read_b64 v[10:11], v29 offset:6144\n\t" \
  "ds_read_b64 v[12:13], v30 offset:7168\n\t" \
  "v_mov_b32 v131, v130\n\t" \
  "s_waitcnt lgkmcnt(12)\n\t" \
  "v_mfma_f32_16x16x32_bf16 v[132:135], v[68:71], v[16:19], v[132:135]\n\t" \
  "ds_read_b64 v[14:15], v31 offset:7168\n\t" \
  "v_mov_b32 v130, v129\n\t" \
  "v_mfma_f32_16x16x32_bf16 v[136:139], v[76:79], v[16:19], v[136:139]\n\t" \
  "v_mov_b32 v129, v128\n\t" \
  "v_mfma_f32_16x16x32_bf16 v[140:143], v[84:87], v[16:19], v[140:143]\n\t" \
  "v_mfma_f32_16x16x32_bf16 v[144:147], v[92:95], v[16:19], v[144:147]\n\t" \
  "v_mfma_f32_16x16x32_bf16 v[148:151], v[100:103], v[16:19], v[148:151]\n\t" \
  "v_mfma_f32_16x16x32_bf16 v[152:155], v[108:111], v[16:19], v[152:155]\n\t" \
  "v_mfma_f32_16x16x32_bf16 v[156:159], v[116:119], v[16:19], v[156:159]\n\t" \
  "ds_read_b64 v[16:17], v24 offset:8192\n\t" \
  "s_waitcnt lgkmcnt(12)\n\t" \
  "v_mfma_f32_16x16x32_bf16 v[132:135], v[64:67], v[20:23], v[132:135]\n\t" \
  "ds_read_b64 v[18:19], v25 offset:8192\n\t" \
  "v_mfma_f32_16x16x32_bf16 v[136:139], v[72:75], v[20:23], v[136:139]\n\t" \
  "v_mfma_f32_16x16x32_bf16 v[140:143], v[80:83], v[20:23], v[140:143]\n\t" \
  "v_mfma_f32_16x16x32_bf16 v[144:147], v[88:91], v[20:23], v[144:147]\n\t" \
  "s_waitcnt lgkmcnt(5)\n\t" \
  "v_mov_b32 v128, v34\n\t" \
  "v_writelane_b32 v128, 0, 0\n\t" \
  "v_mfma_f32_16x16x32_bf16 v[148:151], v[96:99], v[20:23], v[148:151]\n\t" \
  "v_mfma_f32_16x16x32_bf16 v[152:155], v[104:107], v[20:23], v[152:155]\n\t" \
  "v_mfma_f32_16x16x32_bf16 v[156:159], v[112:115], v[20:23], v[156:159]\n\t" \
  "ds_read_b64 v[20:21], v26 offset:9216\n\t" \
  "s_waitcnt lgkmcnt(11)\n\t" \
  "v_mfma_f32_16x16x32_bf16 v[136:139], v[68:71], v[0:3], v[136:139]\n\t" \
  "ds_read_b64 v[22:23], v27 offset:9216\n\t" \
  "ds_bpermute_b32 v35, v32, v135\n\t" \
  "v_readlane_b32 %[p1], v135, 63\n\t" \
  "v_mfma_f32_16x16x32_bf16 v[140:143], v[76:79], v[0:3], v[140:143]\n\t" \
  "v_mov_b32 v135, v134\n\t" \
  "v_mfma_f32_16x16x32_bf16 v[144:147], v[84:87], v[0:3], v[144:147]\n\t" \
  "v_mov_b32 v134, v133\n\t" \
  "v_mfma_f32_16x16x32_bf16 v[148:151], v[92:95], v[0:3], v[148:151]\n\t" \
  "v_mov_b32 v133, v132\n\t" \
  "v_mfma_f32_16x16x32_bf16 v[152:155], v[100:103], v[0:3], v[152:155]\n\t" \
  "v_mfma_f32_16x16x32_bf16 v[156:159], v[108:111], v[0:3], v[156:159]\n\t" \
  "ds_read_b64 v[0:1], v28 offset:10240\n\t" \
  "s_waitcnt lgkmcnt(12)\n\t" \
  "v_mfma_f32_16x16x32_bf16 v[136:139], v[64:67], v[4:7], v[136:139]\n\t" \
  "ds_read_b64 v[2:3], v29 offset:10240\n\t" \
  "v_mfma_f32_16x16x32_bf16 v[140:143], v[72:75], v[4:7], v[140:143]\n\t" \
  "v_mfma_f32_16x16x32_bf16 v[144:147], v[80:83], v[4:7], v[144:147]\n\t" \
  "v_mfma_f32_16x16x32_bf16 v[148:151], v[88:91], v[4:7], v[148:151]\n\t" \
  "v_mfma_f32_16x16x32_bf16 v[152:155], v[96:99], v[4:7], v[152:155]\n\t" \
  "v_mfma_f32_16x16x32_bf16 v[156:159], v[104:107], v[4:7], v[156:159]\n\t" \
  "ds_read_b64 v[4:5], v30 offset:11264\n\t" \
  "s_waitcnt lgkmcnt(10)\n\t" \
  "v_mfma_f32_16x16x32_bf16 v[140:143], v[68:71], v[8:11], v[140:143]\n\t" \
  "ds_read_b64 v[6:7], v31 offset:11264\n\t" \
  "s_waitcnt lgkmcnt(4)\n\t" \
  "v_mov_b32 v132, v35\n\t" \
  "v_writelane_b32 v132, %[p0], 0\n\t" \
  "v_mfma_f32_16x16x32_bf16 v[144:147], v[76:79], v[8:11], v[144:147]\n\t" \
  "ds_bpermute_b32 v36, v32, v139\n\t" \
  "v_readlane_b32 %[p2], v139, 63\n\t" \
  "v_mfma_f32_16x16x32_bf16 v[148:151], v[84:87], v[8:11], v[148:151]\n\t" \
  "v_mov_b32 v139, v138\n\t" \
  "v_mfma_f32_16x16x32_bf16 v[152:155], v[92:95], v[8:11], v[152:155]\n\t" \
  "v_mov_b32 v138, v137\n\t" \
  "v_mfma_f32_16x16x32_bf16 v[156:159], v[100:103], v[8:11], v[156:159]\n\t" \
  "ds_read_b64 v[8:9], v24 offset:12288\n\t" \
  "v_mov_b32 v137, v136\n\t" \
  "s_waitcnt lgkmcnt(11)\n\t" \
  "v_mfma_f32_16x16x32_bf16 v[140:143], v[64:67], v[12:15], v[140:143]\n\t" \
  "ds_read_b64 v[10:11], v25 offset:12288\n\t" \
  "v_mfma_f32_16x16x32_bf16 v[144:147], v[72:75], v[12:15], v[144:147]\n\t" \
  "v_mfma_f32_16x16x32_bf16 v[148:151], v[80:83], v[12:15], v[148:151]\n\t" \
  "v_mfma_f32_16x16x32_bf16 v[152:155], v[88:91], v[12:15], v[152:155]\n\t" \
  "v_mfma_f32_16x16x32_bf16 v[156:159], v[96:99], v[12:15], v[156:159]\n\t" \
  "ds_read_b64 v[12:13], v26 offset:13312\n\t" \
  "s_waitcnt lgkmcnt(11)\n\t" \
  "v_mfma_f32_16x16x32_bf16 v[144:147], v[68:71], v[16:19], v[144:147]\n\t" \
  "ds_read_b64 v[14:15], v27 offset:13312\n\t" \
  "v_mfma_f32_16x16x32_bf16 v[148:151], v[76:79], v[16:19], v[148:151]\n\t" \
  "v_mfma_f32_16x16x32_bf16 v[152:155], v[84:87], v[16:19], v[152:155]\n\t" \
  "ds_bpermute_b32 v37, v32, v143\n\t" \
  "v_readlane_b32 %[p3], v143, 63\n\t" \
  "v_mfma_f32_16x16x32_bf16 v[156:159], v[92:95], v[16:19], v[156:159]\n\t" \
  "ds_read_b64 v[16:17], v28 offset:14336\n\t" \
  "s_waitcnt lgkmcnt(6)\n\t" \
  "v_mov_b32 v136, v36\n\t" \
  "v_writelane_b32 v136, %[p1], 0\n\t" \
  "v_mov_b32 v143, v142\n\t" \
  "s_waitcnt lgkmcnt(12)\n\t" \
  "v_mfma_f32_16x16x32_bf16 v[144:147], v[64:67], v[20:23], v[144:147]\n\t" \
  "ds_read_b64 v[18:19], v29 offset:14336\n\t" \
  "v_mov_b32 v142, v141\n\t" \
  "v_mfma_f32_16x16x32_bf16 v[148:151], v[72:75], v[20:23], v[148:151]\n\t" \
  "v_mov_b32 v141, v140\n\t" \
  "v_mfma_f32_16x16x32_bf16 v[152:155], v[80:83], v[20:23], v[152:155]\n\t" \
  "v_mfma_f32_16x16x32_bf16 v[156:159], v[88:91], v[20:23], v[156:159]\n\t" \
  "ds_read_b64 v[20:21], v30 offset:15360\n\t" \
  "s_waitcnt lgkmcnt(11)\n\t" \
  "v_mfma_f32_16x16x32_bf16 v[148:151], v[68:71], v[0:3], v[148:151]\n\t" \
  "ds_read_b64 v[22:23], v31 offset:15360\n\t" \
  "v_mfma_f32_16x16x32_bf16 v[152:155], v[76:79], v[0:3], v[152:155]\n\t" \
  "v_mfma_f32_16x16x32_bf16 v[156:159], v[84:87], v[0:3], v[156:159]\n\t" \
  "ds_read_b64 v[0:1], v30 offset:0\n\t" \
  "s_waitcnt lgkmcnt(11)\n\t" \
  "v_mfma_f32_16x16x32_bf16 v[148:151], v[64:67], v[4:7], v[148:151]\n\t" \
  "ds_read_b64 v[2:3], v31 offset:0\n\t" \
  "ds_bpermute_b32 v38, v32, v147\n\t" \
  "v_readlane_b32 %[p4], v147, 63\n\t" \
  "v_mfma_f32_16x16x32_bf16 v[152:155], v[72:75], v[4:7], v[152:155]\n\t" \
  "v_mov_b32 v147, v146\n\t" \
  "v_mfma_f32_16x16x32_bf16 v[156:159], v[80:83], v[4:7], v[156:159]\n\t" \
  "ds_read_b64 v[4:5], v24 offset:1024\n\t" \
  "v_mov_b32 v146, v145\n\t" \
  "s_waitcnt lgkmcnt(11)\n\t" \
  "v_mfma_f32_16x16x32_bf16 v[152:155], v[68:71], v[8:11], v[152:155]\n\t" \
  "ds_read_b64 v[6:7], v25 offset:1024\n\t" \
  "s_waitcnt lgkmcnt(9)\n\t" \
  "v_mov_b32 v140, v37\n\t" \
  "v_writelane_b32 v140, %[p2], 0\n\t" \
  "v_mov_b32 v145, v144\n\t" \
  "v_mfma_f32_16x16x32_bf16 v[156:159], v[76:79], v[8:11], v[156:159]\n\t" \
  "ds_read_b64 v[8:9], v26 offset:2048\n\t" \
  "ds_bpermute_b32 v39, v32, v151\n\t" \
  "v_readlane_b32 %[p5], v151, 63\n\t" \
  "s_waitcnt lgkmcnt(12)\n\t" \
  "v_mfma_f32_16x16x32_bf16 v[152:155], v[64:67], v[12:15], v[152:155]\n\t" \
  "ds_read_b64 v[10:11], v27 offset:2048\n\t" \
  "v_mov_b32 v151, v150\n\t" \
  "v_mfma_f32_16x16x32_bf16 v[156:159], v[72:75], v[12:15], v[156:159]\n\t" \
  "ds_read_b64 v[12:13], v28 offset:3072\n\t" \
  "v_mov_b32 v150, v149\n\t" \
  "s_waitcnt lgkmcnt(11)\n\t" \
  "v_mfma_f32_16x16x32_bf16 v[156:159], v[68:71], v[16:19], v[156:159]\n\t" \
  "ds_read_b64 v[14:15], v29 offset:3072\n\t" \
  "ds_read_b64 v[16:17], v30 offset:4096\n\t" \
  "v_mov_b32 v149, v148\n\t" \
  "ds_bpermute_b32 v40, v32, v155\n\t" \
  "v_readlane_b32 %[p6], v155, 63\n\t" \
  "s_waitcnt lgkmcnt(12)\n\t" \
  "v_mfma_f32_16x16x32_bf16 v[156:159], v[64:67], v[20:23], v[156:159]\n\t" \
  "ds_read_b64 v[18:19], v31 offset:4096\n\t" \
  "ds_read_b64 v[20:21], v24 offset:5120\n\t" \
  "v_mov_b32 v155, v154\n\t" \
  "s_waitcnt lgkmcnt(12)\n\t" \
  "v_mfma_f32_16x16x32_bf16 v[128:131], v[68:71], v[0:3], v[128:131]\n\t" \
  "ds_read_b64 v[22:23], v25 offset:5120\n\t" \
  "v_mov_b32 v154, v153\n\t" \
  "v_mfma_f32_16x16x32_bf16 v[132:135], v[76:79], v[0:3], v[132:135]\n\t" \
  "s_waitcnt lgkmcnt(12)\n\t" \
  "v_mov_b32 v144, v38\n\t" \
  "v_writelane_b32 v144, %[p3], 0\n\t" \
  "v_mov_b32 v153, v152\n\t" \
  "v_mfma_f32_16x16x32_bf16 v[136:139], v[84:87], v[0:3], v[136:139]\n\t" \
  "s_waitcnt lgkmcnt(8)\n\t" \
  "v_mov_b32 v148, v39\n\t" \
  "v_writelane_b32 v148, %[p4], 0\n\t" \
  "v_mfma_f32_16x16x32_bf16 v[140:143], v[92:95], v[0:3], v[140:143]\n\t" \
  "s_waitcnt lgkmcnt(3)\n\t" \
  "v_mov_b32 v152, v40\n\t" \
  "v_writelane_b32 v152, %[p5], 0\n\t" \
  "v_mfma_f32_16x16x32_bf16 v[144:147], v[100:103], v[0:3], v[144:147]\n\t" \
  "ds_bpermute_b32 v41, v32, v159\n\t" \
  "v_mfma_f32_16x16x32_bf16 v[148:151], v[108:111], v[0:3], v[148:151]\n\t" \
  "v_mov_b32 v159, v158\n\t" \
  "v_mfma_f32_16x16x32_bf16 v[152:155], v[116:119], v[0:3], v[152:155]\n\t" \
  "v_mov_b32 v158, v157\n\t" \
  "s_waitcnt lgkmcnt(11)\n\t" \
  "v_mfma_f32_16x16x32_bf16 v[128:131], v[64:67], v[4:7], v[128:131]\n\t" \
  "v_mov_b32 v157, v156\n\t" \
  "v_mfma_f32_16x16x32_bf16 v[132:135], v[72:75], v[4:7], v[132:135]\n\t" \
  "v_mfma_f32_16x16x32_bf16 v[136:139], v[80:83], v[4:7], v[136:139]\n\t" \
  "v_mfma_f32_16x16x32_bf16 v[140:143], v[88:91], v[4:7], v[140:143]\n\t" \
  "v_mfma_f32_16x16x32_bf16 v[144:147], v[96:99], v[4:7], v[144:147]\n\t" \
  "s_waitcnt lgkmcnt(0)\n\t" \
  "v_mov_b32 v156, v41\n\t" \
  "v_writelane_b32 v156, %[p6], 0\n\t" \
  "v_mfma_f32_16x16x32_bf16 v[148:151], v[104:107], v[4:7], v[148:151]\n\t" \
  "v_mfma_f32_16x16x32_bf16 v[152:155], v[112:115], v[4:7], v[152:155]\n\t" \
  "s_mov_b64 exec, 1\n\t" \
  "ds_write_b32 v33, v33\n\t" \
  "s_mov_b64 exec, -1\n\t" \
  "v_mfma_f32_16x16x32_bf16 v[156:159], v[124:127], v[0:3], v[156:159]\n\t" \
  "ds_read_b64 v[0:1], v26 offset:6144\n\t" \
  "v_mfma_f32_16x16x32_bf16 v[156:159], v[120:123], v[4:7], v[156:159]\n\t" \
  "ds_read_b64 v[2:3], v27 offset:6144\n\t" \
  "ds_read_b64 v[4:5], v28 offset:7168\n\t" \
  "s_waitcnt lgkmcnt(12)\n\t" \
  "v_mfma_f32_16x16x32_bf16 v[132:135], v[68:71], v[8:11], v[132:135]\n\t" \
  "ds_read_b64 v[6:7], v29 offset:7168\n\t" \
  "v_mfma_f32_16x16x32_bf16 v[136:139], v[76:79], v[8:11], v[136:139]\n\t" \
  "v_mfma_f32_16x16x32_bf16 v[140:143], v[84:87], v[8:11], v[140:143]\n\t" \
  "v_mfma_f32_16x16x32_bf16 v[144:147], v[92:95], v[8:11], v[144:147]\n\t" \
  "v_mfma_f32_16x16x32_bf16 v[148:151], v[100:103], v[8:11], v[148:151]\n\t" \
  "v_mfma_f32_16x16x32_bf16 v[152:155], v[108:111], v[8:11], v[152:155]\n\t" \
  "v_mfma_f32_16x16x32_bf16 v[156:159], v[116:119], v[8:11], v[156:159]\n\t" \
  "ds_read_b64 v[8:9], v30 offset:8192\n\t" \
  "s_waitcnt lgkmcnt(12)\n\t" \
  "v_mfma_f32_16x16x32_bf16 v[132:135], v[64:67], v[12:15], v[132:135]\n\t" \
  "ds_read_b64 v[10:11], v31 offset:8192\n\t" \
  "v_mfma_f32_16x16x32_bf16 v[136:139], v[72:75], v[12:15], v[136:139]\n\t" \
  "v_mfma_f32_16x16x32_bf16 v[140:143], v[80:83], v[12:15], v[140:143]\n\t" \
  "v_mfma_f32_16x16x32_bf16 v[144:147], v[88:91], v[12:15], v[144:147]\n\t" \
  "v_mfma_f32_16x16x32_bf16 v[148:151], v[96:99], v[12:15], v[148:151]\n\t" \
  "v_mfma_f32_16x16x32_bf16 v[152:155], v[104:107], v[12:15], v[152:155]\n\t" \
  "v_mfma_f32_16x16x32_bf16 v[156:159], v[112:115], v[12:15], v[156:159]\n\t" \
  "ds_read_b64 v[12:13], v24 offset:9216\n\t" \
  "s_waitcnt lgkmcnt(11)\n\t" \
  "v_mfma_f32_16x16x32_bf16 v[136:139], v[68:71], v[16:19], v[136:139]\n\t" \
  "ds_read_b64 v[14:15], v25 offset:9216\n\t" \
  "v_mfma_f32_16x16x32_bf16 v[140:143], v[76:79], v[16:19], v[140:143]\n\t" \
  "v_mfma_f32_16x16x32_bf16 v[144:147], v[84:87], v[16:19], v[144:147]\n\t" \
  "v_mfma_f32_16x16x32_bf16 v[148:151], v[92:95], v[16:19], v[148:151]\n\t" \
  "v_mfma_f32_16x16x32_bf16 v[152:155], v[100:103], v[16:19], v[152:155]\n\t" \
  "v_mfma_f32_16x16x32_bf16 v[156:159], v[108:111], v[16:19], v[156:159]\n\t" \
  "ds_read_b64 v[16:17], v26 offset:10240\n\t" \
  "s_waitcnt lgkmcnt(11)\n\t" \
  "v_mfma_f32_16x16x32_bf16 v[136:139], v[64:67], v[20:23], v[136:139]\n\t" \
  "ds_read_b64 v[18:19], v27 offset:10240\n\t" \
  "v_mfma_f32_16x16x32_bf16 v[140:143], v[72:75], v[20:23], v[140:143]\n\t" \
  "v_mfma_f32_16x16x32_bf16 v[144:147], v[80:83], v[20:23], v[144:147]\n\t" \
  "v_mfma_f32_16x16x32_bf16 v[148:151], v[88:91], v[20:23], v[148:151]\n\t" \
  "v_mfma_f32_16x16x32_bf16 v[152:155], v[96:99], v[20:23], v[152:155]\n\t" \
  "v_mfma_f32_16x16x32_bf16 v[156:159], v[104:107], v[20:23], v[156:159]\n\t" \
  "ds_read_b64 v[20:21], v28 offset:11264\n\t" \
  "s_waitcnt lgkmcnt(9)\n\t" \
  "v_mfma_f32_16x16x32_bf16 v[140:143], v[68:71], v[0:3], v[140:143]\n\t" \
  "ds_read_b64 v[22:23], v29 offset:11264\n\t" \
  "v_mfma_f32_16x16x32_bf16 v[144:147], v[76:79], v[0:3], v[144:147]\n\t" \
  "v_mfma_f32_16x16x32_bf16 v[148:151], v[84:87], v[0:3], v[148:151]\n\t" \
  "v_mfma_f32_16x16x32_bf16 v[152:155], v[92:95], v[0:3], v[152:155]\n\t" \
  "v_mfma_f32_16x16x32_bf16 v[156:159], v[100:103], v[0:3], v[156:159]\n\t" \
  "ds_read_b64 v[0:1], v30 offset:12288\n\t" \
  "s_waitcnt lgkmcnt(9)\n\t" \
  "v_mfma_f32_16x16x32_bf16 v[140:143], v[64:67], v[4:7], v[140:143]\n\t" \
  "ds_read_b64 v[2:3], v31 offset:12288\n\t" \
  "v_mfma_f32_16x16x32_bf16 v[144:147], v[72:75], v[4:7], v[144:147]\n\t" \
  "v_mfma_f32_16x16x32_bf16 v[148:151], v[80:83], v[4:7], v[148:151]\n\t" \
  "v_mfma_f32_16x16x32_bf16 v[152:155], v[88:91], v[4:7], v[152:155]\n\t" \
  "v_mfma_f32_16x16x32_bf16 v[156:159], v[96:99], v[4:7], v[156:159]\n\t" \
  "ds_read_b64 v[4:5], v24 offset:13312\n\t" \
  "s_waitcnt lgkmcnt(9)\n\t" \
  "v_mfma_f32_16x16x32_bf16 v[144:147], v[68:71], v[8:11], v[144:147]\n\t" \
  "ds_read_b64 v[6:7], v25 offset:13312\n\t" \
  "v_mfma_f32_16x16x32_bf16 v[148:151], v[76:79], v[8:11], v[148:151]\n\t" \
  "v_mfma_f32_16x16x32_bf16 v[152:155], v[84:87], v[8:11], v[152:155]\n\t" \
  "v_mfma_f32_16x16x32_bf16 v[156:159], v[92:95], v[8:11], v[156:159]\n\t" \
  "ds_read_b64 v[8:9], v26 offset:14336\n\t" \
  "s_waitcnt lgkmcnt(9)\n\t" \
  "v_mfma_f32_16x16x32_bf16 v[144:147], v[64:67], v[12:15], v[144:147]\n\t" \
  "ds_read_b64 v[10:11], v27 offset:14336\n\t" \
  "v_mfma_f32_16x16x32_bf16 v[148:151], v[72:75], v[12:15], v[148:151]\n\t" \
  "v_mfma_f32_16x16x32_bf16 v[152:155], v[80:83], v[12:15], v[152:155]\n\t" \
  "v_mfma_f32_16x16x32_bf16 v[156:159], v[88:91], v[12:15], v[156:159]\n\t" \
  "ds_read_b64 v[12:13], v28 offset:15360\n\t" \
  "s_waitcnt lgkmcnt(9)\n\t" \
  "v_mfma_f32_16x16x32_bf16 v[148:151], v[68:71], v[16:19], v[148:151]\n\t" \
  "ds_read_b64 v[14:15], v29 offset:15360\n\t" \
  "v_mfma_f32_16x16x32_bf16 v[152:155], v[76:79], v[16:19], v[152:155]\n\t" \
  "v_mfma_f32_16x16x32_bf16 v[156:159], v[84:87], v[16:19], v[156:159]\n\t" \
  "s_waitcnt lgkmcnt(8)\n\t" \
  "v_mfma_f32_16x16x32_bf16 v[148:151], v[64:67], v[20:23], v[148:151]\n\t" \
  "v_mfma_f32_16x16x32_bf16 v[152:155], v[72:75], v[20:23], v[152:155]\n\t" \
  "v_mfma_f32_16x16x32_bf16 v[156:159], v[80:83], v[20:23], v[156:159]\n\t" \
  "s_waitcnt lgkmcnt(6)\n\t" \
  "v_mfma_f32_16x16x32_bf16 v[152:155], v[68:71], v[0:3], v[152:155]\n\t" \
  "v_mfma_f32_16x16x32_bf16 v[156:159], v[76:79], v[0:3], v[156:159]\n\t" \
  "s_waitcnt lgkmcnt(4)\n\t" \
  "v_mfma_f32_16x16x32_bf16 v[152:155], v[64:67], v[4:7], v[152:155]\n\t" \
  "v_mfma_f32_16x16x32_bf16 v[156:159], v[72:75], v[4:7], v[156:159]\n\t" \
  "s_waitcnt lgkmcnt(2)\n\t" \
  "v_mfma_f32_16x16x32_bf16 v[156:159], v[68:71], v[8:11], v[156:159]\n\t" \
  "s_waitcnt lgkmcnt(0)\n\t" \
  "v_mfma_f32_16x16x32_bf16 v[156:159], v[64:67], v[12:15], v[156:159]\n\t" \
  "s_waitcnt lgkmcnt(0)\n\t" \
  "s_nop 7\n\t"
#define CV_CLOB "v0", "v1", "v2", "v3", "v4", "v5", "v6", "v7", "v8", "v9", "v10", "v11", "v12", "v13", "v14", "v15", "v16", "v17", "v18", "v19", "v20", "v21", "v22", "v23", "v34", "v35", "v36", "v37", "v38", "v39", "v40", "v41"

#define G3_ASM \
  "s_getpc_b64 s[92:93]\n\t" \
  "s_branch QC_%=\n\t" \
  "ds_read_b64_tr_b16 v[96:97], v160 offset:0\n\t" \
  "ds_read_b64_tr_b16 v[98:99], v160 offset:2048\n\t" \
  "ds_read_b64_tr_b16 v[100:101], v161 offset:0\n\t" \
  "ds_read_b64_tr_b16 v[102:103], v161 offset:2048\n\t" \
  "ds_read_b64_tr_b16 v[104:105], v162 offset:0\n\t" \
  "ds_read_b64_tr_b16 v[106:107], v162 offset:2048\n\t" \
  "ds_read_b64_tr_b16 v[108:109], v163 offset:0\n\t" \
  "ds_read_b64_tr_b16 v[110:111], v163 offset:2048\n\t" \
  "ds_read_b64_tr_b16 v[112:113], v160 offset:4096\n\t" \
  "ds_read_b64_tr_b16 v[114:115], v160 offset:6144\n\t" \
  "ds_read_b64_tr_b16 v[116:117], v161 offset:4096\n\t" \
  "ds_read_b64_tr_b16 v[118:119], v161 offset:6144\n\t" \
  "ds_read_b64_tr_b16 v[120:121], v162 offset:4096\n\t" \
  "ds_read_b64_tr_b16 v[122:123], v162 offset:6144\n\t" \
  "QH_%=:\n\t" \
  "v_add_u32 v166, %[go], v165\n\t" \
  "ds_read_b32 v166, v166\n\t" \
  "s_addk_i32 %[cn], 1\n\t" \
  "s_cmp_gt_u32 %[cn], 0x400000\n\t" \
  "s_cbranch_scc1 QI_%=\n\t" \
  "s_waitcnt lgkmcnt(0)\n\t" \
  "v_cmp_eq_u32 vcc, 0, v166\n\t" \
  "s_cbranch_vccz QI_%=\n\t" \
  "s_sleep 2\n\t" \
  "s_getpc_b64 s[92:93]\n\t" \
  "s_branch QC_%=\n\t" \
  "s_branch QH_%=\n\t" \
  "QI_%=:\n\t" \
  "s_setprio 0\n\t" \
  "ds_read_b32 v166, v165\n\t" \
  "s_waitcnt lgkmcnt(11)\n\t" \
  "v_mfma_f32_16x16x32_bf16 v[128:131], v[96:99], v[0:3], 0\n\t" \
  "ds_read_b64_tr_b16 v[124:125], v163 offset:4096\n\t" \
  "v_readlane_b32 s94, v167, 5\n\t" \
  "v_mfma_f32_16x16x32_bf16 v[144:147], v[96:99], v[4:7], 0\n\t" \
  "ds_read_b64_tr_b16 v[126:127], v163 offset:6144\n\t" \
  "s_waitcnt lgkmcnt(2)\n\t" \
  "v_cmp_lt_u32 vcc, %[th], v166\n\t" \
  "v_mfma_f32_16x16x32_bf16 v[132:135], v[100:103], v[0:3], 0\n\t" \
  "s_mov_b64 exec, vcc\n\t" \
  "buffer_load_dwordx4 v[80:83], v164, %[ob], s94 offen offset:0 sc1\n\t" \
  "s_mov_b64 exec, -1\n\t" \
  "v_mfma_f32_16x16x32_bf16 v[148:151], v[100:103], v[4:7], 0\n\t" \
  "ds_read_b64_tr_b16 v[96:97], v160 offset:8192\n\t" \
  "ds_read_b64_tr_b16 v[98:99], v160 offset:10240\n\t" \
  "v_mfma_f32_16x16x32_bf16 v[136:139], v[104:107], v[0:3], 0\n\t" \
  "s_mov_b64 exec, vcc\n\t" \
  "buffer_load_dwordx4 v[84:87], v164, %[ob], s94 offen offset:1024 sc1\n\t" \
  "s_mov_b64 exec, -1\n\t" \
  "v_mfma_f32_16x16x32_bf16 v[152:155], v[104:107], v[4:7], 0\n\t" \
  "ds_read_b64_tr_b16 v[100:101], v161 offset:8192\n\t" \
  "ds_read_b64_tr_b16 v[102:103], v161 offset:10240\n\t" \
  "v_mfma_f32_16x16x32_bf16 v[140:143], v[108:111], v[0:3], 0\n\t" \
  "s_mov_b64 exec, vcc\n\t" \
  "buffer_load_dwordx4 v[88:91], v164, %[ob], s94 offen offset:2048 sc1\n\t" \
  "s_mov_b64 exec, -1\n\t" \
  "v_mfma_f32_16x16x32_bf16 v[156:159], v[108:111], v[4:7], 0\n\t" \
  "ds_read_b64_tr_b16 v[104:105], v162 offset:8192\n\t" \
  "ds_read_b64_tr_b16 v[106:107], v162 offset:10240\n\t" \
  "v_mfma_f32_16x16x32_bf16 v[128:131], v[112:115], v[8:11], v[128:131]\n\t" \
  "s_mov_b64 exec, vcc\n\t" \
  "buffer_load_dwordx4 v[92:95], v164, %[ob], s94 offen offset:3072 sc1\n\t" \
  "s_mov_b64 exec, -1\n\t" \
  "v_mfma_f32_16x16x32_bf16 v[144:147], v[112:115], v[12:15], v[144:147]\n\t" \
  "ds_read_b64_tr_b16 v[108:109], v163 offset:8192\n\t" \
  "ds_read_b64_tr_b16 v[110:111], v163 offset:10240\n\t" \
  "v_mfma_f32_16x16x32_bf16 v[132:135], v[116:119], v[8:11], v[132:135]\n\t" \
  "ds_read_b64_tr_b16 v[112:113], v160 offset:12288\n\t" \
  "ds_read_b32 v166, v165\n\t" \
  "v_mfma_f32_16x16x32_bf16 v[148:151], v[116:119], v[12:15], v[148:151]\n\t" \
  "ds_read_b64_tr_b16 v[114:115], v160 offset:14336\n\t" \
  "s_waitcnt lgkmcnt(11)\n\t" \
  "v_mfma_f32_16x16x32_bf16 v[136:139], v[120:123], v[8:11], v[136:139]\n\t" \
  "ds_read_b64_tr_b16 v[116:117], v161 offset:12288\n\t" \
  "v_mfma_f32_16x16x32_bf16 v[152:155], v[120:123], v[12:15], v[152:155]\n\t" \
  "ds_read_b64_tr_b16 v[118:119], v161 offset:14336\n\t" \
  "s_sub_u32 %[iss], %[iss], vcc_lo\n\t" \
  "v_mfma_f32_16x16x32_bf16 v[140:143], v[124:127], v[8:11], v[140:143]\n\t" \
  "ds_read_b64_tr_b16 v[120:121], v162 offset:12288\n\t" \
  "v_mfma_f32_16x16x32_bf16 v[156:159], v[124:127], v[12:15], v[156:159]\n\t" \
  "ds_read_b64_tr_b16 v[122:123], v162 offset:14336\n\t" \
  "s_add_u32 %[th], %[th], 1\n\t" \
  "s_cbranch_vccz QF1_%=\n\t" \
  "s_waitcnt vmcnt(16)\n\t" \
  "QG1_%=:\n\t" \
  "s_waitcnt lgkmcnt(11)\n\t" \
  "v_mfma_f32_16x16x32_bf16 v[128:131], v[96:99], v[16:19], v[128:131]\n\t" \
  "ds_read_b64_tr_b16 v[124:125], v163 offset:12288\n\t" \
  "v_readlane_b32 s94, v167, 6\n\t" \
  "v_mfma_f32_16x16x32_bf16 v[144:147], v[96:99], v[20:23], v[144:147]\n\t" \
  "ds_read_b64_tr_b16 v[126:127], v163 offset:14336\n\t" \
  "s_waitcnt lgkmcnt(7)\n\t" \
  "v_cmp_lt_u32 vcc, %[th], v166\n\t" \
  "v_mfma_f32_16x16x32_bf16 v[132:135], v[100:103], v[16:19], v[132:135]\n\t" \
  "s_mov_b64 exec, vcc\n\t" \
  "buffer_load_dwordx4 v[0:3], v164, %[ob], s94 offen offset:0 sc1\n\t" \
  "s_mov_b64 exec, -1\n\t" \
  "v_mfma_f32_16x16x32_bf16 v[148:151], v[100:103], v[20:23], v[148:151]\n\t" \
  "ds_read_b64_tr_b16 v[96:97], v160 offset:16384\n\t" \
  "ds_read_b64_tr_b16 v[98:99], v160 offset:18432\n\t" \
  "v_mfma_f32_16x16x32_bf16 v[136:139], v[104:107], v[16:19], v[136:139]\n\t" \
  "s_mov_b64 exec, vcc\n\t" \
  "buffer_load_dwordx4 v[4:7], v164, %[ob], s94 offen offset:1024 sc1\n\t" \
  "s_mov_b64 exec, -1\n\t" \
  "v_mfma_f32_16x16x32_bf16 v[152:155], v[104:107], v[20:23], v[152:155]\n\t" \
  "ds_read_b64_tr_b16 v[100:101], v161 offset:16384\n\t" \
  "ds_read_b64_tr_b16 v[102:103], v161 offset:18432\n\t" \
  "v_mfma_f32_16x16x32_bf16 v[140:143], v[108:111], v[16:19], v[140:143]\n\t" \
  "s_mov_b64 exec, vcc\n\t" \
  "buffer_load_dwordx4 v[8:11], v164, %[ob], s94 offen offset:2048 sc1\n\t" \
  "s_mov_b64 exec, -1\n\t" \
  "v_mfma_f32_16x16x32_bf16 v[156:159], v[108:111], v[20:23], v[156:159]\n\t" \
  "ds_read_b64_tr_b16 v[104:105], v162 offset:16384\n\t" \
  "ds_read_b64_tr_b16 v[106:107], v162 offset:18432\n\t" \
  "s_waitcnt lgkmcnt(10)\n\t" \
  "v_mfma_f32_16x16x32_bf16 v[128:131], v[112:115], v[24:27], v[128:131]\n\t" \
  "s_mov_b64 exec, vcc\n\t" \
  "buffer_load_dwordx4 v[12:15], v164, %[ob], s94 offen offset:3072 sc1\n\t" \
  "s_mov_b64 exec, -1\n\t" \
  "v_mfma_f32_16x16x32_bf16 v[144:147], v[112:115], v[28:31], v[144:147]\n\t" \
  "ds_read_b64_tr_b16 v[108:109], v163 offset:16384\n\t" \
  "ds_read_b64_tr_b16 v[110:111], v163 offset:18432\n\t" \
  "v_mfma_f32_16x16x32_bf16 v[132:135], v[116:119], v[24:27], v[132:135]\n\t" \
  "ds_read_b64_tr_b16 v[112:113], v160 offset:20480\n\t" \
  "ds_read_b32 v166, v165\n\t" \
  "v_mfma_f32_16x16x32_bf16 v[148:151], v[116:119], v[28:31], v[148:151]\n\t" \
  "ds_read_b64_tr_b16 v[114:115], v160 offset:22528\n\t" \
  "s_waitcnt lgkmcnt(11)\n\t" \
  "v_mfma_f32_16x16x32_bf16 v[136:139], v[120:123], v[24:27], v[136:139]\n\t" \
  "ds_read_b64_tr_b16 v[116:117], v161 offset:20480\n\t" \
  "v_mfma_f32_16x16x32_bf16 v[152:155], v[120:123], v[28:31], v[152:155]\n\t" \
  "ds_read_b64_tr_b16 v[118:119], v161 offset:22528\n\t" \
  "s_sub_u32 %[iss], %[iss], vcc_lo\n\t" \
  "v_mfma_f32_16x16x32_bf16 v[140:143], v[124:127], v[24:27], v[140:143]\n\t" \
  "ds_read_b64_tr_b16 v[120:121], v162 offset:20480\n\t" \
  "v_mfma_f32_16x16x32_bf16 v[156:159], v[124:127], v[28:31], v[156:159]\n\t" \
  "ds_read_b64_tr_b16 v[122:123], v162 offset:22528\n\t" \
  "s_add_u32 %[th], %[th], 1\n\t" \
  "s_cbranch_vccz QF2_%=\n\t" \
  "s_waitcnt vmcnt(16)\n\t" \
  "QG2_%=:\n\t" \
  "s_waitcnt lgkmcnt(11)\n\t" \
  "v_mfma_f32_16x16x32_bf16 v[128:131], v[96:99], v[32:35], v[128:131]\n\t" \
  "ds_read_b64_tr_b16 v[124:125], v163 offset:20480\n\t" \
  "v_readlane_b32 s94, v167, 7\n\t" \
  "v_mfma_f32_16x16x32_bf16 v[144:147], v[96:99], v[36:39], v[144:147]\n\t" \
  "ds_read_b64_tr_b16 v[126:127], v163 offset:22528\n\t" \
  "s_waitcnt lgkmcnt(7)\n\t" \
  "v_cmp_lt_u32 vcc, %[th], v166\n\t" \
  "v_mfma_f32_16x16x32_bf16 v[132:135], v[100:103], v[32:35], v[132:135]\n\t" \
  "s_mov_b64 exec, vcc\n\t" \
  "buffer_load_dwordx4 v[16:19], v164, %[ob], s94 offen offset:0 sc1\n\t" \
  "s_mov_b64 exec, -1\n\t" \
  "v_mfma_f32_16x16x32_bf16 v[148:151], v[100:103], v[36:39], v[148:151]\n\t" \
  "ds_read_b64_tr_b16 v[96:97], v160 offset:24576\n\t" \
  "ds_read_b64_tr_b16 v[98:99], v160 offset:26624\n\t" \
  "v_mfma_f32_16x16x32_bf16 v[136:139], v[104:107], v[32:35], v[136:139]\n\t" \
  "s_mov_b64 exec, vcc\n\t" \
  "buffer_load_dwordx4 v[20:23], v164, %[ob], s94 offen offset:1024 sc1\n\t" \
  "s_mov_b64 exec, -1\n\t" \
  "v_mfma_f32_16x16x32_bf16 v[152:155], v[104:107], v[36:39], v[152:155]\n\t" \
  "ds_read_b64_tr_b16 v[100:101], v161 offset:24576\n\t" \
  "ds_read_b64_tr_b16 v[102:103], v161 offset:26624\n\t" \
  "v_mfma_f32_16x16x32_bf16 v[140:143], v[108:111], v[32:35], v[140:143]\n\t" \
  "s_mov_b64 exec, vcc\n\t" \
  "buffer_load_dwordx4 v[24:27], v164, %[ob], s94 offen offset:2048 sc1\n\t" \
  "s_mov_b64 exec, -1\n\t" \
  "v_mfma_f32_16x16x32_bf16 v[156:159], v[108:111], v[36:39], v[156:159]\n\t" \
  "ds_read_b64_tr_b16 v[104:105], v162 offset:24576\n\t" \
  "ds_read_b64_tr_b16 v[106:107], v162 offset:26624\n\t" \
  "s_waitcnt lgkmcnt(10)\n\t" \
  "v_mfma_f32_16x16x32_bf16 v[128:131], v[112:115], v[40:43], v[128:131]\n\t" \
  "s_mov_b64 exec, vcc\n\t" \
  "buffer_load_dwordx4 v[28:31], v164, %[ob], s94 offen offset:3072 sc1\n\t" \
  "s_mov_b64 exec, -1\n\t" \
  "v_mfma_f32_16x16x32_bf16 v[144:147], v[112:115], v[44:47], v[144:147]\n\t" \
  "ds_read_b64_tr_b16 v[108:109], v163 offset:24576\n\t" \
  "ds_read_b64_tr_b16 v[110:111], v163 offset:26624\n\t" \
  "v_mfma_f32_16x16x32_bf16 v[132:135], v[116:119], v[40:43], v[132:135]\n\t" \
  "ds_read_b64_tr_b16 v[112:113], v160 offset:28672\n\t" \
  "ds_read_b32 v166, v165\n\t" \
  "v_mfma_f32_16x16x32_bf16 v[148:151], v[116:119], v[44:47], v[148:151]\n\t" \
  "ds_read_b64_tr_b16 v[114:115], v160 offset:30720\n\t" \
  "s_waitcnt lgkmcnt(11)\n\t" \
  "v_mfma_f32_16x16x32_bf16 v[136:139], v[120:123], v[40:43], v[136:139]\n\t" \
  "ds_read_b64_tr_b16 v[116:117], v161 offset:28672\n\t" \
  "v_mfma_f32_16x16x32_bf16 v[152:155], v[120:123], v[44:47], v[152:155]\n\t" \
  "ds_read_b64_tr_b16 v[118:119], v161 offset:30720\n\t" \
  "s_sub_u32 %[iss], %[iss], vcc_lo\n\t" \
  "v_mfma_f32_16x16x32_bf16 v[140:143], v[124:127], v[40:43], v[140:143]\n\t" \
  "ds_read_b64_tr_b16 v[120:121], v162 offset:28672\n\t" \
  "v_mfma_f32_16x16x32_bf16 v[156:159], v[124:127], v[44:47], v[156:159]\n\t" \
  "ds_read_b64_tr_b16 v[122:123], v162 offset:30720\n\t" \
  "s_add_u32 %[th], %[th], 1\n\t" \
  "s_cbranch_vccz QF3_%=\n\t" \
  "s_waitcnt vmcnt(16)\n\t" \
  "QG3_%=:\n\t" \
  "s_waitcnt lgkmcnt(11)\n\t" \
  "v_mfma_f32_16x16x32_bf16 v[128:131], v[96:99], v[48:51], v[128:131]\n\t" \
  "ds_read_b64_tr_b16 v[124:125], v163 offset:28672\n\t" \
  "v_readlane_b32 s94, v167, 8\n\t" \
  "v_mfma_f32_16x16x32_bf16 v[144:147], v[96:99], v[52:55], v[144:147]\n\t" \
  "ds_read_b64_tr_b16 v[126:127], v163 offset:30720\n\t" \
  "s_waitcnt lgkmcnt(7)\n\t" \
  "v_cmp_lt_u32 vcc, %[th], v166\n\t" \
  "v_mfma_f32_16x16x32_bf16 v[132:135], v[100:103], v[48:51], v[132:135]\n\t" \
  "s_mov_b64 exec, vcc\n\t" \
  "buffer_load_dwordx4 v[32:35], v164, %[ob], s94 offen offset:0 sc1\n\t" \
  "s_mov_b64 exec, -1\n\t" \
  "v_mfma_f32_16x16x32_bf16 v[148:151], v[100:103], v[52:55], v[148:151]\n\t" \
  "ds_read_b64_tr_b16 v[96:97], v160 offset:32768\n\t" \
  "ds_read_b64_tr_b16 v[98:99], v160 offset:34816\n\t" \
  "v_mfma_f32_16x16x32_bf16 v[136:139], v[104:107], v[48:51], v[136:139]\n\t" \
  "s_mov_b64 exec, vcc\n\t" \
  "buffer_load_dwordx4 v[36:39], v164, %[ob], s94 offen offset:1024 sc1\n\t" \
  "s_mov_b64 exec, -1\n\t" \
  "v_mfma_f32_16x16x32_bf16 v[152:155], v[104:107], v[52:55], v[152:155]\n\t" \
  "ds_read_b64_tr_b16 v[100:101], v161 offset:32768\n\t" \
  "ds_read_b64_tr_b16 v[102:103], v161 offset:34816\n\t" \
  "v_mfma_f32_16x16x32_bf16 v[140:143], v[108:111], v[48:51], v[140:143]\n\t" \
  "s_mov_b64 exec, vcc\n\t" \
  "buffer_load_dwordx4 v[40:43], v164, %[ob], s94 offen offset:2048 sc1\n\t" \
  "s_mov_b64 exec, -1\n\t" \
  "v_mfma_f32_16x16x32_bf16 v[156:159], v[108:111], v[52:55], v[156:159]\n\t" \
  "ds_read_b64_tr_b16 v[104:105], v162 offset:32768\n\t" \
  "ds_read_b64_tr_b16 v[106:107], v162 offset:34816\n\t" \
  "s_waitcnt lgkmcnt(10)\n\t" \
  "v_mfma_f32_16x16x32_bf16 v[128:131], v[112:115], v[56:59], v[128:131]\n\t" \
  "s_mov_b64 exec, vcc\n\t" \
  "buffer_load_dwordx4 v[44:47], v164, %[ob], s94 offen offset:3072 sc1\n\t" \
  "s_mov_b64 exec, -1\n\t" \
  "v_mfma_f32_16x16x32_bf16 v[144:147], v[112:115], v[60:63], v[144:147]\n\t" \
  "ds_read_b64_tr_b16 v[108:109], v163 offset:32768\n\t" \
  "ds_read_b64_tr_b16 v[110:111], v163 offset:34816\n\t" \
  "v_mfma_f32_16x16x32_bf16 v[132:135], v[116:119], v[56:59], v[132:135]\n\t" \
  "ds_read_b64_tr_b16 v[112:113], v160 offset:36864\n\t" \
  "ds_read_b32 v166, v165\n\t" \
  "v_mfma_f32_16x16x32_bf16 v[148:151], v[116:119], v[60:63], v[148:151]\n\t" \
  "ds_read_b64_tr_b16 v[114:115], v160 offset:38912\n\t" \
  "s_waitcnt lgkmcnt(11)\n\t" \
  "v_mfma_f32_16x16x32_bf16 v[136:139], v[120:123], v[56:59], v[136:139]\n\t" \
  "ds_read_b64_tr_b16 v[116:117], v161 offset:36864\n\t" \
  "v_mfma_f32_16x16x32_bf16 v[152:155], v[120:123], v[60:63], v[152:155]\n\t" \
  "ds_read_b64_tr_b16 v[118:119], v161 offset:38912\n\t" \
  "s_sub_u32 %[iss], %[iss], vcc_lo\n\t" \
  "v_mfma_f32_16x16x32_bf16 v[140:143], v[124:127], v[56:59], v[140:143]\n\t" \
  "ds_read_b64_tr_b16 v[120:121], v162 offset:36864\n\t" \
  "v_mfma_f32_16x16x32_bf16 v[156:159], v[124:127], v[60:63], v[156:159]\n\t" \
  "ds_read_b64_tr_b16 v[122:123], v162 offset:38912\n\t" \
  "s_add_u32 %[th], %[th], 1\n\t" \
  "s_cbranch_vccz QF4_%=\n\t" \
  "s_waitcnt vmcnt(16)\n\t" \
  "QG4_%=:\n\t" \
  "s_waitcnt lgkmcnt(11)\n\t" \
  "v_mfma_f32_16x16x32_bf16 v[128:131], v[96:99], v[64:67], v[128:131]\n\t" \
  "ds_read_b64_tr_b16 v[124:125], v163 offset:36864\n\t" \
  "v_readlane_b32 s94, v167, 9\n\t" \
  "v_mfma_f32_16x16x32_bf16 v[144:147], v[96:99], v[68:71], v[144:147]\n\t" \
  "ds_read_b64_tr_b16 v[126:127], v163 offset:38912\n\t" \
  "s_waitcnt lgkmcnt(7)\n\t" \
  "v_cmp_lt_u32 vcc, %[th], v166\n\t" \
  "v_mfma_f32_16x16x32_bf16 v[132:135], v[100:103], v[64:67], v[132:135]\n\t" \
  "s_mov_b64 exec, vcc\n\t" \
  "buffer_load_dwordx4 v[48:51], v164, %[ob], s94 offen offset:0 sc1\n\t" \
  "s_mov_b64 exec, -1\n\t" \
  "v_mfma_f32_16x16x32_bf16 v[148:151], v[100:103], v[68:71], v[148:151]\n\t" \
  "ds_read_b64_tr_b16 v[96:97], v160 offset:40960\n\t" \
  "ds_read_b64_tr_b16 v[98:99], v160 offset:43008\n\t" \
  "v_mfma_f32_16x16x32_bf16 v[136:139], v[104:107], v[64:67], v[136:139]\n\t" \
  "s_mov_b64 exec, vcc\n\t" \
  "buffer_load_dwordx4 v[52:55], v164, %[ob], s94 offen offset:1024 sc1\n\t" \
  "s_mov_b64 exec, -1\n\t" \
  "v_mfma_f32_16x16x32_bf16 v[152:155], v[104:107], v[68:71], v[152:155]\n\t" \
  "ds_read_b64_tr_b16 v[100:101], v161 offset:40960\n\t" \
  "ds_read_b64_tr_b16 v[102:103], v161 offset:43008\n\t" \
  "v_mfma_f32_16x16x32_bf16 v[140:143], v[108:111], v[64:67], v[140:143]\n\t" \
  "s_mov_b64 exec, vcc\n\t" \
  "buffer_load_dwordx4 v[56:59], v164, %[ob], s94 offen offset:2048 sc1\n\t" \
  "s_mov_b64 exec, -1\n\t" \
  "v_mfma_f32_16x16x32_bf16 v[156:159], v[108:111], v[68:71], v[156:159]\n\t" \
  "ds_read_b64_tr_b16 v[104:105], v162 offset:40960\n\t" \
  "ds_read_b64_tr_b16 v[106:107], v162 offset:43008\n\t" \
  "s_waitcnt lgkmcnt(10)\n\t" \
  "v_mfma_f32_16x16x32_bf16 v[128:131], v[112:115], v[72:75], v[128:131]\n\t" \
  "s_mov_b64 exec, vcc\n\t" \
  "buffer_load_dwordx4 v[60:63], v164, %[ob], s94 offen offset:3072 sc1\n\t" \
  "s_mov_b64 exec, -1\n\t" \
  "v_mfma_f32_16x16x32_bf16 v[144:147], v[112:115], v[76:79], v[144:147]\n\t" \
  "ds_read_b64_tr_b16 v[108:109], v163 offset:40960\n\t" \
  "ds_read_b64_tr_b16 v[110:111], v163 offset:43008\n\t" \
  "v_mfma_f32_16x16x32_bf16 v[132:135], v[116:119], v[72:75], v[132:135]\n\t" \
  "ds_read_b64_tr_b16 v[112:113], v160 offset:45056\n\t" \
  "ds_read_b32 v166, v165\n\t" \
  "v_mfma_f32_16x16x32_bf16 v[148:151], v[116:119], v[76:79], v[148:151]\n\t" \
  "ds_read_b64_tr_b16 v[114:115], v160 offset:47104\n\t" \
  "s_waitcnt lgkmcnt(11)\n\t" \
  "v_mfma_f32_16x16x32_bf16 v[136:139], v[120:123], v[72:75], v[136:139]\n\t" \
  "ds_read_b64_tr_b16 v[116:117], v161 offset:45056\n\t" \
  "v_mfma_f32_16x16x32_bf16 v[152:155], v[120:123], v[76:79], v[152:155]\n\t" \
  "ds_read_b64_tr_b16 v[118:119], v161 offset:47104\n\t" \
  "s_sub_u32 %[iss], %[iss], vcc_lo\n\t" \
  "v_mfma_f32_16x16x32_bf16 v[140:143], v[124:127], v[72:75], v[140:143]\n\t" \
  "ds_read_b64_tr_b16 v[120:121], v162 offset:45056\n\t" \
  "v_mfma_f32_16x16x32_bf16 v[156:159], v[124:127], v[76:79], v[156:159]\n\t" \
  "ds_read_b64_tr_b16 v[122:123], v162 offset:47104\n\t" \
  "s_add_u32 %[th], %[th], 1\n\t" \
  "s_cbranch_vccz QF5_%=\n\t" \
  "s_waitcnt vmcnt(16)\n\t" \
  "QG5_%=:\n\t" \
  "s_waitcnt lgkmcnt(11)\n\t" \
  "v_mfma_f32_16x16x32_bf16 v[128:131], v[96:99], v[80:83], v[128:131]\n\t" \
  "ds_read_b64_tr_b16 v[124:125], v163 offset:45056\n\t" \
  "v_readlane_b32 s94, v167, 10\n\t" \
  "v_mfma_f32_16x16x32_bf16 v[144:147], v[96:99], v[84:87], v[144:147]\n\t" \
  "ds_read_b64_tr_b16 v[126:127], v163 offset:47104\n\t" \
  "s_waitcnt lgkmcnt(7)\n\t" \
  "v_cmp_lt_u32 vcc, %[th], v166\n\t" \
  "v_mfma_f32_16x16x32_bf16 v[132:135], v[100:103], v[80:83], v[132:135]\n\t" \
  "s_mov_b64 exec, vcc\n\t" \
  "buffer_load_dwordx4 v[64:67], v164, %[ob], s94 offen offset:0 sc1\n\t" \
  "s_mov_b64 exec, -1\n\t" \
  "v_mfma_f32_16x16x32_bf16 v[148:151], v[100:103], v[84:87], v[148:151]\n\t" \
  "ds_read_b64_tr_b16 v[96:97], v160 offset:49152\n\t" \
  "ds_read_b64_tr_b16 v[98:99], v160 offset:51200\n\t" \
  "v_mfma_f32_16x16x32_bf16 v[136:139], v[104:107], v[80:83], v[136:139]\n\t" \
  "s_mov_b64 exec, vcc\n\t" \
  "buffer_load_dwordx4 v[68:71], v164, %[ob], s94 offen offset:1024 sc1\n\t" \
  "s_mov_b64 exec, -1\n\t" \
  "v_mfma_f32_16x16x32_bf16 v[152:155], v[104:107], v[84:87], v[152:155]\n\t" \
  "ds_read_b64_tr_b16 v[100:101], v161 offset:49152\n\t" \
  "ds_read_b64_tr_b16 v[102:103], v161 offset:51200\n\t" \
  "v_mfma_f32_16x16x32_bf16 v[140:143], v[108:111], v[80:83], v[140:143]\n\t" \
  "s_mov_b64 exec, vcc\n\t" \
  "buffer_load_dwordx4 v[72:75], v164, %[ob], s94 offen offset:2048 sc1\n\t" \
  "s_mov_b64 exec, -1\n\t" \
  "v_mfma_f32_16x16x32_bf16 v[156:159], v[108:111], v[84:87], v[156:159]\n\t" \
  "ds_read_b64_tr_b16 v[104:105], v162 offset:49152\n\t" \
  "ds_read_b64_tr_b16 v[106:107], v162 offset:51200\n\t" \
  "s_waitcnt lgkmcnt(10)\n\t" \
  "v_mfma_f32_16x16x32_bf16 v[128:131], v[112:115], v[88:91], v[128:131]\n\t" \
  "s_mov_b64 exec, vcc\n\t" \
  "buffer_load_dwordx4 v[76:79], v164, %[ob], s94 offen offset:3072 sc1\n\t" \
  "s_mov_b64 exec, -1\n\t" \
  "v_mfma_f32_16x16x32_bf16 v[144:147], v[112:115], v[92:95], v[144:147]\n\t" \
  "ds_read_b64_tr_b16 v[108:109], v163 offset:49152\n\t" \
  "ds_read_b64_tr_b16 v[110:111], v163 offset:51200\n\t" \
  "v_mfma_f32_16x16x32_bf16 v[132:135], v[116:119], v[88:91], v[132:135]\n\t" \
  "ds_read_b64_tr_b16 v[112:113], v160 offset:53248\n\t" \
  "ds_read_b32 v166, v165\n\t" \
  "v_mfma_f32_16x16x32_bf16 v[148:151], v[116:119], v[92:95], v[148:151]\n\t" \
  "ds_read_b64_tr_b16 v[114:115], v160 offset:55296\n\t" \
  "s_waitcnt lgkmcnt(11)\n\t" \
  "v_mfma_f32_16x16x32_bf16 v[136:139], v[120:123], v[88:91], v[136:139]\n\t" \
  "ds_read_b64_tr_b16 v[116:117], v161 offset:53248\n\t" \
  "v_mfma_f32_16x16x32_bf16 v[152:155], v[120:123], v[92:95], v[152:155]\n\t" \
  "ds_read_b64_tr_b16 v[118:119], v161 offset:55296\n\t" \
  "s_sub_u32 %[iss], %[iss], vcc_lo\n\t" \
  "v_mfma_f32_16x16x32_bf16 v[140:143], v[124:127], v[88:91], v[140:143]\n\t" \
  "ds_read_b64_tr_b16 v[120:121], v162 offset:53248\n\t" \
  "v_mfma_f32_16x16x32_bf16 v[156:159], v[124:127], v[92:95], v[156:159]\n\t" \
  "ds_read_b64_tr_b16 v[122:123], v162 offset:55296\n\t" \
  "s_add_u32 %[th], %[th], 1\n\t" \
  "s_cbranch_vccz QF6_%=\n\t" \
  "s_waitcnt vmcnt(16)\n\t" \
  "QG6_%=:\n\t" \
  "s_waitcnt lgkmcnt(11)\n\t" \
  "v_mfma_f32_16x16x32_bf16 v[128:131], v[96:99], v[0:3], v[128:131]\n\t" \
  "ds_read_b64_tr_b16 v[124:125], v163 offset:53248\n\t" \
  "v_readlane_b32 s94, v167, 11\n\t" \
  "v_mfma_f32_16x16x32_bf16 v[144:147], v[96:99], v[4:7], v[144:147]\n\t" \
  "ds_read_b64_tr_b16 v[126:127], v163 offset:55296\n\t" \
  "s_waitcnt lgkmcnt(7)\n\t" \
  "v_cmp_lt_u32 vcc, %[th], v166\n\t" \
  "v_mfma_f32_16x16x32_bf16 v[132:135], v[100:103], v[0:3], v[132:135]\n\t" \
  "s_mov_b64 exec, vcc\n\t" \
  "buffer_load_dwordx4 v[80:83], v164, %[ob], s94 offen offset:0 sc1\n\t" \
  "s_mov_b64 exec, -1\n\t" \
  "v_mfma_f32_16x16x32_bf16 v[148:151], v[100:103], v[4:7], v[148:151]\n\t" \
  "ds_read_b64_tr_b16 v[96:97], v160 offset:57344\n\t" \
  "ds_read_b64_tr_b16 v[98:99], v160 offset:59392\n\t" \
  "v_mfma_f32_16x16x32_bf16 v[136:139], v[104:107], v[0:3], v[136:139]\n\t" \
  "s_mov_b64 exec, vcc\n\t" \
  "buffer_load_dwordx4 v[84:87], v164, %[ob], s94 offen offset:1024 sc1\n\t" \
  "s_mov_b64 exec, -1\n\t" \
  "v_mfma_f32_16x16x32_bf16 v[152:155], v[104:107], v[4:7], v[152:155]\n\t" \
  "ds_read_b64_tr_b16 v[100:101], v161 offset:57344\n\t" \
  "ds_read_b64_tr_b16 v[102:103], v161 offset:59392\n\t" \
  "v_mfma_f32_16x16x32_bf16 v[140:143], v[108:111], v[0:3], v[140:143]\n\t" \
  "s_mov_b64 exec, vcc\n\t" \
  "buffer_load_dwordx4 v[88:91], v164, %[ob], s94 offen offset:2048 sc1\n\t" \
  "s_mov_b64 exec, -1\n\t" \
  "v_mfma_f32_16x16x32_bf16 v[156:159], v[108:111], v[4:7], v[156:159]\n\t" \
  "ds_read_b64_tr_b16 v[104:105], v162 offset:57344\n\t" \
  "ds_read_b64_tr_b16 v[106:107], v162 offset:59392\n\t" \
  "s_waitcnt lgkmcnt(10)\n\t" \
  "v_mfma_f32_16x16x32_bf16 v[128:131], v[112:115], v[8:11], v[128:131]\n\t" \
  "s_mov_b64 exec, vcc\n\t" \
  "buffer_load_dwordx4 v[92:95], v164, %[ob], s94 offen offset:3072 sc1\n\t" \
  "s_mov_b64 exec, -1\n\t" \
  "v_mfma_f32_16x16x32_bf16 v[144:147], v[112:115], v[12:15], v[144:147]\n\t" \
  "ds_read_b64_tr_b16 v[108:109], v163 offset:57344\n\t" \
  "ds_read_b64_tr_b16 v[110:111], v163 offset:59392\n\t" \
  "v_mfma_f32_16x16x32_bf16 v[132:135], v[116:119], v[8:11], v[132:135]\n\t" \
  "ds_read_b64_tr_b16 v[112:113], v160 offset:61440\n\t" \
  "ds_read_b32 v166, v165\n\t" \
  "v_mfma_f32_16x16x32_bf16 v[148:151], v[116:119], v[12:15], v[148:151]\n\t" \
  "ds_read_b64_tr_b16 v[114:115], v160 offset:63488\n\t" \
  "s_waitcnt lgkmcnt(11)\n\t" \
  "v_mfma_f32_16x16x32_bf16 v[136:139], v[120:123], v[8:11], v[136:139]\n\t" \
  "ds_read_b64_tr_b16 v[116:117], v161 offset:61440\n\t" \
  "v_mfma_f32_16x16x32_bf16 v[152:155], v[120:123], v[12:15], v[152:155]\n\t" \
  "ds_read_b64_tr_b16 v[118:119], v161 offset:63488\n\t" \
  "s_sub_u32 %[iss], %[iss], vcc_lo\n\t" \
  "v_mfma_f32_16x16x32_bf16 v[140:143], v[124:127], v[8:11], v[140:143]\n\t" \
  "ds_read_b64_tr_b16 v[120:121], v162 offset:61440\n\t" \
  "s_mov_b64 exec, 1\n\t" \
  "ds_write_b32 v165, v165 offset:76\n\t" \
  "s_mov_b64 exec, -1\n\t" \
  "v_mfma_f32_16x16x32_bf16 v[156:159], v[124:127], v[12:15], v[156:159]\n\t" \
  "ds_read_b64_tr_b16 v[122:123], v162 offset:63488\n\t" \
  "s_add_u32 %[th], %[th], 1\n\t" \
  "s_cbranch_vccz QF7_%=\n\t" \
  "s_waitcnt vmcnt(16)\n\t" \
  "QG7_%=:\n\t" \
  "s_waitcnt lgkmcnt(12)\n\t" \
  "v_mfma_f32_16x16x32_bf16 v[128:131], v[96:99], v[16:19], v[128:131]\n\t" \
  "ds_read_b64_tr_b16 v[124:125], v163 offset:61440\n\t" \
  "v_readlane_b32 s94, v167, 12\n\t" \
  "v_mfma_f32_16x16x32_bf16 v[144:147], v[96:99], v[20:23], v[144:147]\n\t" \
  "ds_read_b64_tr_b16 v[126:127], v163 offset:63488\n\t" \
  "s_waitcnt lgkmcnt(8)\n\t" \
  "v_cmp_lt_u32 vcc, %[th], v166\n\t" \
  "v_mfma_f32_16x16x32_bf16 v[132:135], v[100:103], v[16:19], v[132:135]\n\t" \
  "v_add_u32 v160, 0x10000, v160\n\t" \
  "s_mov_b64 exec, vcc\n\t" \
  "buffer_load_dwordx4 v[0:3], v164, %[ob], s94 offen offset:0 sc1\n\t" \
  "s_mov_b64 exec, -1\n\t" \
  "v_mfma_f32_16x16x32_bf16 v[148:151], v[100:103], v[20:23], v[148:151]\n\t" \
  "ds_read_b64_tr_b16 v[96:97], v160 offset:0\n\t" \
  "ds_read_b64_tr_b16 v[98:99], v160 offset:2048\n\t" \
  "v_mfma_f32_16x16x32_bf16 v[136:139], v[104:107], v[16:19], v[136:139]\n\t" \
  "v_add_u32 v161, 0x10000, v161\n\t" \
  "s_mov_b64 exec, vcc\n\t" \
  "buffer_load_dwordx4 v[4:7], v164, %[ob], s94 offen offset:1024 sc1\n\t" \
  "s_mov_b64 exec, -1\n\t" \
  "v_mfma_f32_16x16x32_bf16 v[152:155], v[104:107], v[20:23], v[152:155]\n\t" \
  "ds_read_b64_tr_b16 v[100:101], v161 offset:0\n\t" \
  "ds_read_b64_tr_b16 v[102:103], v161 offset:2048\n\t" \
  "v_mfma_f32_16x16x32_bf16 v[140:143], v[108:111], v[16:19], v[140:143]\n\t" \
  "v_add_u32 v162, 0x10000, v162\n\t" \
  "s_mov_b64 exec, vcc\n\t" \
  "buffer_load_dwordx4 v[8:11], v164, %[ob], s94 offen offset:2048 sc1\n\t" \
  "s_mov_b64 exec, -1\n\t" \
  "v_mfma_f32_16x16x32_bf16 v[156:159], v[108:111], v[20:23], v[156:159]\n\t" \
  "ds_read_b64_tr_b16 v[104:105], v162 offset:0\n\t" \
  "ds_read_b64_tr_b16 v[106:107], v162 offset:2048\n\t" \
  "s_waitcnt lgkmcnt(11)\n\t" \
  "v_mfma_f32_16x16x32_bf16 v[128:131], v[112:115], v[24:27], v[128:131]\n\t" \
  "v_add_u32 v163, 0x10000, v163\n\t" \
  "s_mov_b64 exec, vcc\n\t" \
  "buffer_load_dwordx4 v[12:15], v164, %[ob], s94 offen offset:3072 sc1\n\t" \
  "s_mov_b64 exec, -1\n\t" \
  "v_mfma_f32_16x16x32_bf16 v[144:147], v[112:115], v[28:31], v[144:147]\n\t" \
  "ds_read_b64_tr_b16 v[108:109], v163 offset:0\n\t" \
  "ds_read_b64_tr_b16 v[110:111], v163 offset:2048\n\t" \
  "v_mfma_f32_16x16x32_bf16 v[132:135], v[116:119], v[24:27], v[132:135]\n\t" \
  "ds_read_b64_tr_b16 v[112:113], v160 offset:4096\n\t" \
  "ds_read_b32 v166, v165\n\t" \
  "v_mfma_f32_16x16x32_bf16 v[148:151], v[116:119], v[28:31], v[148:151]\n\t" \
  "ds_read_b64_tr_b16 v[114:115], v160 offset:6144\n\t" \
  "s_waitcnt lgkmcnt(11)\n\t" \
  "v_mfma_f32_16x16x32_bf16 v[136:139], v[120:123], v[24:27], v[136:139]\n\t" \
  "ds_read_b64_tr_b16 v[116:117], v161 offset:4096\n\t" \
  "v_mfma_f32_16x16x32_bf16 v[152:155], v[120:123], v[28:31], v[152:155]\n\t" \
  "ds_read_b64_tr_b16 v[118:119], v161 offset:6144\n\t" \
  "s_sub_u32 %[iss], %[iss], vcc_lo\n\t" \
  "v_mfma_f32_16x16x32_bf16 v[140:143], v[124:127], v[24:27], v[140:143]\n\t" \
  "ds_read_b64_tr_b16 v[120:121], v162 offset:4096\n\t" \
  "v_mfma_f32_16x16x32_bf16 v[156:159], v[124:127], v[28:31], v[156:159]\n\t" \
  "ds_read_b64_tr_b16 v[122:123], v162 offset:6144\n\t" \
  "s_add_u32 %[th], %[th], 1\n\t" \
  "s_cbranch_vccz QF8_%=\n\t" \
  "s_waitcnt vmcnt(16)\n\t" \
  "QG8_%=:\n\t" \
  "s_waitcnt lgkmcnt(11)\n\t" \
  "v_mfma_f32_16x16x32_bf16 v[128:131], v[96:99], v[32:35], v[128:131]\n\t" \
  "ds_read_b64_tr_b16 v[124:125], v163 offset:4096\n\t" \
  "v_readlane_b32 s94, v167, 13\n\t" \
  "v_mfma_f32_16x16x32_bf16 v[144:147], v[96:99], v[36:39], v[144:147]\n\t" \
  "ds_read_b64_tr_b16 v[126:127], v163 offset:6144\n\t" \
  "s_waitcnt lgkmcnt(7)\n\t" \
  "v_cmp_lt_u32 vcc, %[th], v166\n\t" \
  "v_mfma_f32_16x16x32_bf16 v[132:135], v[100:103], v[32:35], v[132:135]\n\t" \
  "s_mov_b64 exec, vcc\n\t" \
  "buffer_load_dwordx4 v[16:19], v164, %[ob], s94 offen offset:0 sc1\n\t" \
  "s_mov_b64 exec, -1\n\t" \
  "v_mfma_f32_16x16x32_bf16 v[148:151], v[100:103], v[36:39], v[148:151]\n\t" \
  "ds_read_b64_tr_b16 v[96:97], v160 offset:8192\n\t" \
  "ds_read_b64_tr_b16 v[98:99], v160 offset:10240\n\t" \
  "v_mfma_f32_16x16x32_bf16 v[136:139], v[104:107], v[32:35], v[136:139]\n\t" \
  "s_mov_b64 exec, vcc\n\t" \
  "buffer_load_dwordx4 v[20:23], v164, %[ob], s94 offen offset:1024 sc1\n\t" \
  "s_mov_b64 exec, -1\n\t" \
  "v_mfma_f32_16x16x32_bf16 v[152:155], v[104:107], v[36:39], v[152:155]\n\t" \
  "ds_read_b64_tr_b16 v[100:101], v161 offset:8192\n\t" \
  "ds_read_b64_tr_b16 v[102:103], v161 offset:10240\n\t" \
  "v_mfma_f32_16x16x32_bf16 v[140:143], v[108:111], v[32:35], v[140:143]\n\t" \
  "s_mov_b64 exec, vcc\n\t" \
  "buffer_load_dwordx4 v[24:27], v164, %[ob], s94 offen offset:2048 sc1\n\t" \
  "s_mov_b64 exec, -1\n\t" \
  "v_mfma_f32_16x16x32_bf16 v[156:159], v[108:111], v[36:39], v[156:159]\n\t" \
  "ds_read_b64_tr_b16 v[104:105], v162 offset:8192\n\t" \
  "ds_read_b64_tr_b16 v[106:107], v162 offset:10240\n\t" \
  "s_waitcnt lgkmcnt(10)\n\t" \
  "v_mfma_f32_16x16x32_bf16 v[128:131], v[112:115], v[40:43], v[128:131]\n\t" \
  "s_mov_b64 exec, vcc\n\t" \
  "buffer_load_dwordx4 v[28:31], v164, %[ob], s94 offen offset:3072 sc1\n\t" \
  "s_mov_b64 exec, -1\n\t" \
  "v_mfma_f32_16x16x32_bf16 v[144:147], v[112:115], v[44:47], v[144:147]\n\t" \
  "ds_read_b64_tr_b16 v[108:109], v163 offset:8192\n\t" \
  "ds_read_b64_tr_b16 v[110:111], v163 offset:10240\n\t" \
  "v_mfma_f32_16x16x32_bf16 v[132:135], v[116:119], v[40:43], v[132:135]\n\t" \
  "ds_read_b64_tr_b16 v[112:113], v160 offset:12288\n\t" \
  "ds_read_b32 v166, v165\n\t" \
  "v_mfma_f32_16x16x32_bf16 v[148:151], v[116:119], v[44:47], v[148:151]\n\t" \
  "ds_read_b64_tr_b16 v[114:115], v160 offset:14336\n\t" \
  "s_waitcnt lgkmcnt(11)\n\t" \
  "v_mfma_f32_16x16x32_bf16 v[136:139], v[120:123], v[40:43], v[136:139]\n\t" \
  "ds_read_b64_tr_b16 v[116:117], v161 offset:12288\n\t" \
  "v_mfma_f32_16x16x32_bf16 v[152:155], v[120:123], v[44:47], v[152:155]\n\t" \
  "ds_read_b64_tr_b16 v[118:119], v161 offset:14336\n\t" \
  "s_sub_u32 %[iss], %[iss], vcc_lo\n\t" \
  "v_mfma_f32_16x16x32_bf16 v[140:143], v[124:127], v[40:43], v[140:143]\n\t" \
  "ds_read_b64_tr_b16 v[120:121], v162 offset:12288\n\t" \
  "v_mfma_f32_16x16x32_bf16 v[156:159], v[124:127], v[44:47], v[156:159]\n\t" \
  "ds_read_b64_tr_b16 v[122:123], v162 offset:14336\n\t" \
  "s_add_u32 %[th], %[th], 1\n\t" \
  "s_cbranch_vccz QF9_%=\n\t" \
  "s_waitcnt vmcnt(16)\n\t" \
  "QG9_%=:\n\t" \
  "s_waitcnt lgkmcnt(11)\n\t" \
  "v_mfma_f32_16x16x32_bf16 v[128:131], v[96:99], v[48:51], v[128:131]\n\t" \
  "ds_read_b64_tr_b16 v[124:125], v163 offset:12288\n\t" \
  "v_readlane_b32 s94, v167, 14\n\t" \
  "v_mfma_f32_16x16x32_bf16 v[144:147], v[96:99], v[52:55], v[144:147]\n\t" \
  "ds_read_b64_tr_b16 v[126:127], v163 offset:14336\n\t" \
  "s_waitcnt lgkmcnt(7)\n\t" \
  "v_cmp_lt_u32 vcc, %[th], v166\n\t" \
  "v_mfma_f32_16x16x32_bf16 v[132:135], v[100:103], v[48:51], v[132:135]\n\t" \
  "s_mov_b64 exec, vcc\n\t" \
  "buffer_load_dwordx4 v[32:35], v164, %[ob], s94 offen offset:0 sc1\n\t" \
  "s_mov_b64 exec, -1\n\t" \
  "v_mfma_f32_16x16x32_bf16 v[148:151], v[100:103], v[52:55], v[148:151]\n\t" \
  "ds_read_b64_tr_b16 v[96:97], v160 offset:16384\n\t" \
  "ds_read_b64_tr_b16 v[98:99], v160 offset:18432\n\t" \
  "v_mfma_f32_16x16x32_bf16 v[136:139], v[104:107], v[48:51], v[136:139]\n\t" \
  "s_mov_b64 exec, vcc\n\t" \
  "buffer_load_dwordx4 v[36:39], v164, %[ob], s94 offen offset:1024 sc1\n\t" \
  "s_mov_b64 exec, -1\n\t" \
  "v_mfma_f32_16x16x32_bf16 v[152:155], v[104:107], v[52:55], v[152:155]\n\t" \
  "ds_read_b64_tr_b16 v[100:101], v161 offset:16384\n\t" \
  "ds_read_b64_tr_b16 v[102:103], v161 offset:18432\n\t" \
  "v_mfma_f32_16x16x32_bf16 v[140:143], v[108:111], v[48:51], v[140:143]\n\t" \
  "s_mov_b64 exec, vcc\n\t" \
  "buffer_load_dwordx4 v[40:43], v164, %[ob], s94 offen offset:2048 sc1\n\t" \
  "s_mov_b64 exec, -1\n\t" \
  "v_mfma_f32_16x16x32_bf16 v[156:159], v[108:111], v[52:55], v[156:159]\n\t" \
  "ds_read_b64_tr_b16 v[104:105], v162 offset:16384\n\t" \
  "ds_read_b64_tr_b16 v[106:107], v162 offset:18432\n\t" \
  "s_waitcnt lgkmcnt(10)\n\t" \
  "v_mfma_f32_16x16x32_bf16 v[128:131], v[112:115], v[56:59], v[128:131]\n\t" \
  "s_mov_b64 exec, vcc\n\t" \
  "buffer_load_dwordx4 v[44:47], v164, %[ob], s94 offen offset:3072 sc1\n\t" \
  "s_mov_b64 exec, -1\n\t" \
  "v_mfma_f32_16x16x32_bf16 v[144:147], v[112:115], v[60:63], v[144:147]\n\t" \
  "ds_read_b64_tr_b16 v[108:109], v163 offset:16384\n\t" \
  "ds_read_b64_tr_b16 v[110:111], v163 offset:18432\n\t" \
  "v_mfma_f32_16x16x32_bf16 v[132:135], v[116:119], v[56:59], v[132:135]\n\t" \
  "ds_read_b64_tr_b16 v[112:113], v160 offset:20480\n\t" \
  "ds_read_b32 v166, v165\n\t" \
  "v_mfma_f32_16x16x32_bf16 v[148:151], v[116:119], v[60:63], v[148:151]\n\t" \
  "ds_read_b64_tr_b16 v[114:115], v160 offset:22528\n\t" \
  "s_waitcnt lgkmcnt(11)\n\t" \
  "v_mfma_f32_16x16x32_bf16 v[136:139], v[120:123], v[56:59], v[136:139]\n\t" \
  "ds_read_b64_tr_b16 v[116:117], v161 offset:20480\n\t" \
  "v_mfma_f32_16x16x32_bf16 v[152:155], v[120:123], v[60:63], v[152:155]\n\t" \
  "ds_read_b64_tr_b16 v[118:119], v161 offset:22528\n\t" \
  "s_sub_u32 %[iss], %[iss], vcc_lo\n\t" \
  "v_mfma_f32_16x16x32_bf16 v[140:143], v[124:127], v[56:59], v[140:143]\n\t" \
  "ds_read_b64_tr_b16 v[120:121], v162 offset:20480\n\t" \
  "v_mfma_f32_16x16x32_bf16 v[156:159], v[124:127], v[60:63], v[156:159]\n\t" \
  "ds_read_b64_tr_b16 v[122:123], v162 offset:22528\n\t" \
  "s_add_u32 %[th], %[th], 1\n\t" \
  "s_cbranch_vccz QF10_%=\n\t" \
  "s_waitcnt vmcnt(16)\n\t" \
  "QG10_%=:\n\t" \
  "s_waitcnt lgkmcnt(11)\n\t" \
  "v_mfma_f32_16x16x32_bf16 v[128:131], v[96:99], v[64:67], v[128:131]\n\t" \
  "ds_read_b64_tr_b16 v[124:125], v163 offset:20480\n\t" \
  "v_readlane_b32 s94, v167, 15\n\t" \
  "v_mfma_f32_16x16x32_bf16 v[144:147], v[96:99], v[68:71], v[144:147]\n\t" \
  "ds_read_b64_tr_b16 v[126:127], v163 offset:22528\n\t" \
  "s_waitcnt lgkmcnt(7)\n\t" \
  "v_cmp_lt_u32 vcc, %[th], v166\n\t" \
  "v_mfma_f32_16x16x32_bf16 v[132:135], v[100:103], v[64:67], v[132:135]\n\t" \
  "s_mov_b64 exec, vcc\n\t" \
  "buffer_load_dwordx4 v[48:51], v164, %[ob], s94 offen offset:0 sc1\n\t" \
  "s_mov_b64 exec, -1\n\t" \
  "v_mfma_f32_16x16x32_bf16 v[148:151], v[100:103], v[68:71], v[148:151]\n\t" \
  "ds_read_b64_tr_b16 v[96:97], v160 offset:24576\n\t" \
  "ds_read_b64_tr_b16 v[98:99], v160 offset:26624\n\t" \
  "v_mfma_f32_16x16x32_bf16 v[136:139], v[104:107], v[64:67], v[136:139]\n\t" \
  "s_mov_b64 exec, vcc\n\t" \
  "buffer_load_dwordx4 v[52:55], v164, %[ob], s94 offen offset:1024 sc1\n\t" \
  "s_mov_b64 exec, -1\n\t" \
  "v_mfma_f32_16x16x32_bf16 v[152:155], v[104:107], v[68:71], v[152:155]\n\t" \
  "ds_read_b64_tr_b16 v[100:101], v161 offset:24576\n\t" \
  "ds_read_b64_tr_b16 v[102:103], v161 offset:26624\n\t" \
  "v_mfma_f32_16x16x32_bf16 v[140:143], v[108:111], v[64:67], v[140:143]\n\t" \
  "s_mov_b64 exec, vcc\n\t" \
  "buffer_load_dwordx4 v[56:59], v164, %[ob], s94 offen offset:2048 sc1\n\t" \
  "s_mov_b64 exec, -1\n\t" \
  "v_mfma_f32_16x16x32_bf16 v[156:159], v[108:111], v[68:71], v[156:159]\n\t" \
  "ds_read_b64_tr_b16 v[104:105], v162 offset:24576\n\t" \
  "ds_read_b64_tr_b16 v[106:107], v162 offset:26624\n\t" \
  "s_waitcnt lgkmcnt(10)\n\t" \
  "v_mfma_f32_16x16x32_bf16 v[128:131], v[112:115], v[72:75], v[128:131]\n\t" \
  "s_mov_b64 exec, vcc\n\t" \
  "buffer_load_dwordx4 v[60:63], v164, %[ob], s94 offen offset:3072 sc1\n\t" \
  "s_mov_b64 exec, -1\n\t" \
  "v_mfma_f32_16x16x32_bf16 v[144:147], v[112:115], v[76:79], v[144:147]\n\t" \
  "ds_read_b64_tr_b16 v[108:109], v163 offset:24576\n\t" \
  "ds_read_b64_tr_b16 v[110:111], v163 offset:26624\n\t" \
  "v_mfma_f32_16x16x32_bf16 v[132:135], v[116:119], v[72:75], v[132:135]\n\t" \
  "ds_read_b64_tr_b16 v[112:113], v160 offset:28672\n\t" \
  "v_mfma_f32_16x16x32_bf16 v[148:151], v[116:119], v[76:79], v[148:151]\n\t" \
  "ds_read_b64_tr_b16 v[114:115], v160 offset:30720\n\t" \
  "s_waitcnt lgkmcnt(10)\n\t" \
  "v_mfma_f32_16x16x32_bf16 v[136:139], v[120:123], v[72:75], v[136:139]\n\t" \
  "ds_read_b64_tr_b16 v[116:117], v161 offset:28672\n\t" \
  "v_mfma_f32_16x16x32_bf16 v[152:155], v[120:123], v[76:79], v[152:155]\n\t" \
  "ds_read_b64_tr_b16 v[118:119], v161 offset:30720\n\t" \
  "s_sub_u32 %[iss], %[iss], vcc_lo\n\t" \
  "v_mfma_f32_16x16x32_bf16 v[140:143], v[124:127], v[72:75], v[140:143]\n\t" \
  "ds_read_b64_tr_b16 v[120:121], v162 offset:28672\n\t" \
  "v_mfma_f32_16x16x32_bf16 v[156:159], v[124:127], v[76:79], v[156:159]\n\t" \
  "ds_read_b64_tr_b16 v[122:123], v162 offset:30720\n\t" \
  "s_add_u32 %[th], %[th], 1\n\t" \
  "s_cbranch_vccz QF11_%=\n\t" \
  "s_waitcnt vmcnt(16)\n\t" \
  "QG11_%=:\n\t" \
  "s_waitcnt lgkmcnt(10)\n\t" \
  "v_mfma_f32_16x16x32_bf16 v[128:131], v[96:99], v[80:83], v[128:131]\n\t" \
  "ds_read_b64_tr_b16 v[124:125], v163 offset:28672\n\t" \
  "v_mfma_f32_16x16x32_bf16 v[144:147], v[96:99], v[84:87], v[144:147]\n\t" \
  "ds_read_b64_tr_b16 v[126:127], v163 offset:30720\n\t" \
  "v_mfma_f32_16x16x32_bf16 v[132:135], v[100:103], v[80:83], v[132:135]\n\t" \
  "ds_read_b64_tr_b16 v[96:97], v160 offset:32768\n\t" \
  "v_mfma_f32_16x16x32_bf16 v[148:151], v[100:103], v[84:87], v[148:151]\n\t" \
  "ds_read_b64_tr_b16 v[98:99], v160 offset:34816\n\t" \
  "s_waitcnt lgkmcnt(10)\n\t" \
  "v_mfma_f32_16x16x32_bf16 v[136:139], v[104:107], v[80:83], v[136:139]\n\t" \
  "ds_read_b64_tr_b16 v[100:101], v161 offset:32768\n\t" \
  "v_mfma_f32_16x16x32_bf16 v[152:155], v[104:107], v[84:87], v[152:155]\n\t" \
  "ds_read_b64_tr_b16 v[102:103], v161 offset:34816\n\t" \
  "v_mfma_f32_16x16x32_bf16 v[140:143], v[108:111], v[80:83], v[140:143]\n\t" \
  "ds_read_b64_tr_b16 v[104:105], v162 offset:32768\n\t" \
  "v_mfma_f32_16x16x32_bf16 v[156:159], v[108:111], v[84:87], v[156:159]\n\t" \
  "ds_read_b64_tr_b16 v[106:107], v162 offset:34816\n\t" \
  "s_waitcnt lgkmcnt(10)\n\t" \
  "v_mfma_f32_16x16x32_bf16 v[128:131], v[112:115], v[88:91], v[128:131]\n\t" \
  "ds_read_b64_tr_b16 v[108:109], v163 offset:32768\n\t" \
  "v_mfma_f32_16x16x32_bf16 v[144:147], v[112:115], v[92:95], v[144:147]\n\t" \
  "ds_read_b64_tr_b16 v[110:111], v163 offset:34816\n\t" \
  "v_mfma_f32_16x16x32_bf16 v[132:135], v[116:119], v[88:91], v[132:135]\n\t" \
  "ds_read_b64_tr_b16 v[112:113], v160 offset:36864\n\t" \
  "v_mfma_f32_16x16x32_bf16 v[148:151], v[116:119], v[92:95], v[148:151]\n\t" \
  "ds_read_b64_tr_b16 v[114:115], v160 offset:38912\n\t" \
  "s_waitcnt lgkmcnt(10)\n\t" \
  "v_mfma_f32_16x16x32_bf16 v[136:139], v[120:123], v[88:91], v[136:139]\n\t" \
  "ds_read_b64_tr_b16 v[116:117], v161 offset:36864\n\t" \
  "v_mfma_f32_16x16x32_bf16 v[152:155], v[120:123], v[92:95], v[152:155]\n\t" \
  "ds_read_b64_tr_b16 v[118:119], v161 offset:38912\n\t" \
  "v_mfma_f32_16x16x32_bf16 v[140:143], v[124:127], v[88:91], v[140:143]\n\t" \
  "ds_read_b64_tr_b16 v[120:121], v162 offset:36864\n\t" \
  "v_mfma_f32_16x16x32_bf16 v[156:159], v[124:127], v[92:95], v[156:159]\n\t" \
  "ds_read_b64_tr_b16 v[122:123], v162 offset:38912\n\t" \
  "s_cbranch_vccz QF12_%=\n\t" \
  "s_waitcnt vmcnt(12)\n\t" \
  "QG12_%=:\n\t" \
  "s_waitcnt lgkmcnt(10)\n\t" \
  "v_mfma_f32_16x16x32_bf16 v[128:131], v[96:99], v[0:3], v[128:131]\n\t" \
  "ds_read_b64_tr_b16 v[124:125], v163 offset:36864\n\t" \
  "v_mfma_f32_16x16x32_bf16 v[144:147], v[96:99], v[4:7], v[144:147]\n\t" \
  "ds_read_b64_tr_b16 v[126:127], v163 offset:38912\n\t" \
  "v_mfma_f32_16x16x32_bf16 v[132:135], v[100:103], v[0:3], v[132:135]\n\t" \
  "ds_read_b64_tr_b16 v[96:97], v160 offset:40960\n\t" \
  "v_mfma_f32_16x16x32_bf16 v[148:151], v[100:103], v[4:7], v[148:151]\n\t" \
  "ds_read_b64_tr_b16 v[98:99], v160 offset:43008\n\t" \
  "s_waitcnt lgkmcnt(10)\n\t" \
  "v_mfma_f32_16x16x32_bf16 v[136:139], v[104:107], v[0:3], v[136:139]\n\t" \
  "ds_read_b64_tr_b16 v[100:101], v161 offset:40960\n\t" \
  "v_mfma_f32_16x16x32_bf16 v[152:155], v[104:107], v[4:7], v[152:155]\n\t" \
  "ds_read_b64_tr_b16 v[102:103], v161 offset:43008\n\t" \
  "v_mfma_f32_16x16x32_bf16 v[140:143], v[108:111], v[0:3], v[140:143]\n\t" \
  "ds_read_b64_tr_b16 v[104:105], v162 offset:40960\n\t" \
  "v_mfma_f32_16x16x32_bf16 v[156:159], v[108:111], v[4:7], v[156:159]\n\t" \
  "ds_read_b64_tr_b16 v[106:107], v162 offset:43008\n\t" \
  "s_waitcnt lgkmcnt(10)\n\t" \
  "v_mfma_f32_16x16x32_bf16 v[128:131], v[112:115], v[8:11], v[128:131]\n\t" \
  "ds_read_b64_tr_b16 v[108:109], v163 offset:40960\n\t" \
  "v_mfma_f32_16x16x32_bf16 v[144:147], v[112:115], v[12:15], v[144:147]\n\t" \
  "ds_read_b64_tr_b16 v[110:111], v163 offset:43008\n\t" \
  "v_mfma_f32_16x16x32_bf16 v[132:135], v[116:119], v[8:11], v[132:135]\n\t" \
  "ds_read_b64_tr_b16 v[112:113], v160 offset:45056\n\t" \
  "v_mfma_f32_16x16x32_bf16 v[148:151], v[116:119], v[12:15], v[148:151]\n\t" \
  "ds_read_b64_tr_b16 v[114:115], v160 offset:47104\n\t" \
  "s_waitcnt lgkmcnt(10)\n\t" \
  "v_mfma_f32_16x16x32_bf16 v[136:139], v[120:123], v[8:11], v[136:139]\n\t" \
  "ds_read_b64_tr_b16 v[116:117], v161 offset:45056\n\t" \
  "v_mfma_f32_16x16x32_bf16 v[152:155], v[120:123], v[12:15], v[152:155]\n\t" \
  "ds_read_b64_tr_b16 v[118:119], v161 offset:47104\n\t" \
  "v_mfma_f32_16x16x32_bf16 v[140:143], v[124:127], v[8:11], v[140:143]\n\t" \
  "ds_read_b64_tr_b16 v[120:121], v162 offset:45056\n\t" \
  "v_mfma_f32_16x16x32_bf16 v[156:159], v[124:127], v[12:15], v[156:159]\n\t" \
  "ds_read_b64_tr_b16 v[122:123], v162 offset:47104\n\t" \
  "s_cbranch_vccz QF13_%=\n\t" \
  "s_waitcnt vmcnt(8)\n\t" \
  "QG13_%=:\n\t" \
  "s_waitcnt lgkmcnt(10)\n\t" \
  "v_mfma_f32_16x16x32_bf16 v[128:131], v[96:99], v[16:19], v[128:131]\n\t" \
  "ds_read_b64_tr_b16 v[124:125], v163 offset:45056\n\t" \
  "v_mfma_f32_16x16x32_bf16 v[144:147], v[96:99], v[20:23], v[144:147]\n\t" \
  "ds_read_b64_tr_b16 v[126:127], v163 offset:47104\n\t" \
  "v_mfma_f32_16x16x32_bf16 v[132:135], v[100:103], v[16:19], v[132:135]\n\t" \
  "ds_read_b64_tr_b16 v[96:97], v160 offset:49152\n\t" \
  "v_mfma_f32_16x16x32_bf16 v[148:151], v[100:103], v[20:23], v[148:151]\n\t" \
  "ds_read_b64_tr_b16 v[98:99], v160 offset:51200\n\t" \
  "s_waitcnt lgkmcnt(10)\n\t" \
  "v_mfma_f32_16x16x32_bf16 v[136:139], v[104:107], v[16:19], v[136:139]\n\t" \
  "ds_read_b64_tr_b16 v[100:101], v161 offset:49152\n\t" \
  "v_mfma_f32_16x16x32_bf16 v[152:155], v[104:107], v[20:23], v[152:155]\n\t" \
  "ds_read_b64_tr_b16 v[102:103], v161 offset:51200\n\t" \
  "v_mfma_f32_16x16x32_bf16 v[140:143], v[108:111], v[16:19], v[140:143]\n\t" \
  "ds_read_b64_tr_b16 v[104:105], v162 offset:49152\n\t" \
  "v_mfma_f32_16x16x32_bf16 v[156:159], v[108:111], v[20:23], v[156:159]\n\t" \
  "ds_read_b64_tr_b16 v[106:107], v162 offset:51200\n\t" \
  "s_waitcnt lgkmcnt(10)\n\t" \
  "v_mfma_f32_16x16x32_bf16 v[128:131], v[112:115], v[24:27], v[128:131]\n\t" \
  "ds_read_b64_tr_b16 v[108:109], v163 offset:49152\n\t" \
  "v_mfma_f32_16x16x32_bf16 v[144:147], v[112:115], v[28:31], v[144:147]\n\t" \
  "ds_read_b64_tr_b16 v[110:111], v163 offset:51200\n\t" \
  "v_mfma_f32_16x16x32_bf16 v[132:135], v[116:119], v[24:27], v[132:135]\n\t" \
  "ds_read_b64_tr_b16 v[112:113], v160 offset:53248\n\t" \
  "v_mfma_f32_16x16x32_bf16 v[148:151], v[116:119], v[28:31], v[148:151]\n\t" \
  "ds_read_b64_tr_b16 v[114:115], v160 offset:55296\n\t" \
  "s_waitcnt lgkmcnt(10)\n\t" \
  "v_mfma_f32_16x16x32_bf16 v[136:139], v[120:123], v[24:27], v[136:139]\n\t" \
  "ds_read_b64_tr_b16 v[116:117], v161 offset:53248\n\t" \
  "v_mfma_f32_16x16x32_bf16 v[152:155], v[120:123], v[28:31], v[152:155]\n\t" \
  "ds_read_b64_tr_b16 v[118:119], v161 offset:55296\n\t" \
  "v_mfma_f32_16x16x32_bf16 v[140:143], v[124:127], v[24:27], v[140:143]\n\t" \
  "ds_read_b64_tr_b16 v[120:121], v162 offset:53248\n\t" \
  "v_mfma_f32_16x16x32_bf16 v[156:159], v[124:127], v[28:31], v[156:159]\n\t" \
  "ds_read_b64_tr_b16 v[122:123], v162 offset:55296\n\t" \
  "s_cbranch_vccz QF14_%=\n\t" \
  "s_waitcnt vmcnt(4)\n\t" \
  "QG14_%=:\n\t" \
  "s_waitcnt lgkmcnt(10)\n\t" \
  "v_mfma_f32_16x16x32_bf16 v[128:131], v[96:99], v[32:35], v[128:131]\n\t" \
  "ds_read_b64_tr_b16 v[124:125], v163 offset:53248\n\t" \
  "v_mfma_f32_16x16x32_bf16 v[144:147], v[96:99], v[36:39], v[144:147]\n\t" \
  "ds_read_b64_tr_b16 v[126:127], v163 offset:55296\n\t" \
  "v_mfma_f32_16x16x32_bf16 v[132:135], v[100:103], v[32:35], v[132:135]\n\t" \
  "ds_read_b64_tr_b16 v[96:97], v160 offset:57344\n\t" \
  "v_mfma_f32_16x16x32_bf16 v[148:151], v[100:103], v[36:39], v[148:151]\n\t" \
  "ds_read_b64_tr_b16 v[98:99], v160 offset:59392\n\t" \
  "s_waitcnt lgkmcnt(10)\n\t" \
  "v_mfma_f32_16x16x32_bf16 v[136:139], v[104:107], v[32:35], v[136:139]\n\t" \
  "ds_read_b64_tr_b16 v[100:101], v161 offset:57344\n\t" \
  "v_mfma_f32_16x16x32_bf16 v[152:155], v[104:107], v[36:39], v[152:155]\n\t" \
  "ds_read_b64_tr_b16 v[102:103], v161 offset:59392\n\t" \
  "v_mfma_f32_16x16x32_bf16 v[140:143], v[108:111], v[32:35], v[140:143]\n\t" \
  "ds_read_b64_tr_b16 v[104:105], v162 offset:57344\n\t" \
  "v_mfma_f32_16x16x32_bf16 v[156:159], v[108:111], v[36:39], v[156:159]\n\t" \
  "ds_read_b64_tr_b16 v[106:107], v162 offset:59392\n\t" \
  "s_waitcnt lgkmcnt(10)\n\t" \
  "v_mfma_f32_16x16x32_bf16 v[128:131], v[112:115], v[40:43], v[128:131]\n\t" \
  "ds_read_b64_tr_b16 v[108:109], v163 offset:57344\n\t" \
  "v_mfma_f32_16x16x32_bf16 v[144:147], v[112:115], v[44:47], v[144:147]\n\t" \
  "ds_read_b64_tr_b16 v[110:111], v163 offset:59392\n\t" \
  "v_mfma_f32_16x16x32_bf16 v[132:135], v[116:119], v[40:43], v[132:135]\n\t" \
  "ds_read_b64_tr_b16 v[112:113], v160 offset:61440\n\t" \
  "v_mfma_f32_16x16x32_bf16 v[148:151], v[116:119], v[44:47], v[148:151]\n\t" \
  "ds_read_b64_tr_b16 v[114:115], v160 offset:63488\n\t" \
  "s_waitcnt lgkmcnt(10)\n\t" \
  "v_mfma_f32_16x16x32_bf16 v[136:139], v[120:123], v[40:43], v[136:139]\n\t" \
  "ds_read_b64_tr_b16 v[116:117], v161 offset:61440\n\t" \
  "v_mfma_f32_16x16x32_bf16 v[152:155], v[120:123], v[44:47], v[152:155]\n\t" \
  "ds_read_b64_tr_b16 v[118:119], v161 offset:63488\n\t" \
  "v_mfma_f32_16x16x32_bf16 v[140:143], v[124:127], v[40:43], v[140:143]\n\t" \
  "ds_read_b64_tr_b16 v[120:121], v162 offset:61440\n\t" \
  "v_mfma_f32_16x16x32_bf16 v[156:159], v[124:127], v[44:47], v[156:159]\n\t" \
  "ds_read_b64_tr_b16 v[122:123], v162 offset:63488\n\t" \
  "s_cbranch_vccz QF15_%=\n\t" \
  "s_waitcnt vmcnt(0)\n\t" \
  "QG15_%=:\n\t" \
  "s_waitcnt lgkmcnt(10)\n\t" \
  "v_mfma_f32_16x16x32_bf16 v[128:131], v[96:99], v[48:51], v[128:131]\n\t" \
  "ds_read_b64_tr_b16 v[124:125], v163 offset:61440\n\t" \
  "v_mfma_f32_16x16x32_bf16 v[144:147], v[96:99], v[52:55], v[144:147]\n\t" \
  "ds_read_b64_tr_b16 v[126:127], v163 offset:63488\n\t" \
  "v_mfma_f32_16x16x32_bf16 v[132:135], v[100:103], v[48:51], v[132:135]\n\t" \
  "v_mfma_f32_16x16x32_bf16 v[148:151], v[100:103], v[52:55], v[148:151]\n\t" \
  "s_waitcnt lgkmcnt(8)\n\t" \
  "v_mfma_f32_16x16x32_bf16 v[136:139], v[104:107], v[48:51], v[136:139]\n\t" \
  "v_mfma_f32_16x16x32_bf16 v[152:155], v[104:107], v[52:55], v[152:155]\n\t" \
  "v_mfma_f32_16x16x32_bf16 v[140:143], v[108:111], v[48:51], v[140:143]\n\t" \
  "v_mfma_f32_16x16x32_bf16 v[156:159], v[108:111], v[52:55], v[156:159]\n\t" \
  "s_waitcnt lgkmcnt(4)\n\t" \
  "v_mfma_f32_16x16x32_bf16 v[128:131], v[112:115], v[56:59], v[128:131]\n\t" \
  "v_mfma_f32_16x16x32_bf16 v[144:147], v[112:115], v[60:63], v[144:147]\n\t" \
  "v_mfma_f32_16x16x32_bf16 v[132:135], v[116:119], v[56:59], v[132:135]\n\t" \
  "v_mfma_f32_16x16x32_bf16 v[148:151], v[116:119], v[60:63], v[148:151]\n\t" \
  "s_waitcnt lgkmcnt(0)\n\t" \
  "v_mfma_f32_16x16x32_bf16 v[136:139], v[120:123], v[56:59], v[136:139]\n\t" \
  "v_mfma_f32_16x16x32_bf16 v[152:155], v[120:123], v[60:63], v[152:155]\n\t" \
  "v_mfma_f32_16x16x32_bf16 v[140:143], v[124:127], v[56:59], v[140:143]\n\t" \
  "v_mfma_f32_16x16x32_bf16 v[156:159], v[124:127], v[60:63], v[156:159]\n\t" \
  "s_branch QX_%=\n\t" \
  "QF1_%=:\n\t" \
  "s_mov_b32 %[c], 1\n\t" \
  "s_getpc_b64 s[92:93]\n\t" \
  "s_branch QC_%=\n\t" \
  "s_branch QG1_%=\n\t" \
  "QF2_%=:\n\t" \
  "s_mov_b32 %[c], 2\n\t" \
  "s_getpc_b64 s[92:93]\n\t" \
  "s_branch QC_%=\n\t" \
  "s_branch QG2_%=\n\t" \
  "QF3_%=:\n\t" \
  "s_mov_b32 %[c], 3\n\t" \
  "s_getpc_b64 s[92:93]\n\t" \
  "s_branch QC_%=\n\t" \
  "s_branch QG3_%=\n\t" \
  "QF4_%=:\n\t" \
  "s_mov_b32 %[c], 4\n\t" \
  "s_getpc_b64 s[92:93]\n\t" \
  "s_branch QC_%=\n\t" \
  "s_branch QG4_%=\n\t" \
  "QF5_%=:\n\t" \
  "s_mov_b32 %[c], 5\n\t" \
  "s_getpc_b64 s[92:93]\n\t" \
  "s_branch QC_%=\n\t" \
  "s_branch QG5_%=\n\t" \
  "QF6_%=:\n\t" \
  "s_mov_b32 %[c], 6\n\t" \
  "s_getpc_b64 s[92:93]\n\t" \
  "s_branch QC_%=\n\t" \
  "s_branch QG6_%=\n\t" \
  "QF7_%=:\n\t" \
  "s_mov_b32 %[c], 7\n\t" \
  "s_getpc_b64 s[92:93]\n\t" \
  "s_branch QC_%=\n\t" \
  "s_branch QG7_%=\n\t" \
  "QF8_%=:\n\t" \
  "s_mov_b32 %[c], 8\n\t" \
  "s_getpc_b64 s[92:93]\n\t" \
  "s_branch QC_%=\n\t" \
  "s_branch QG8_%=\n\t" \
  "QF9_%=:\n\t" \
  "s_mov_b32 %[c], 9\n\t" \
  "s_getpc_b64 s[92:93]\n\t" \
  "s_branch QC_%=\n\t" \
  "s_branch QG9_%=\n\t" \
  "QF10_%=:\n\t" \
  "s_mov_b32 %[c], 10\n\t" \
  "s_getpc_b64 s[92:93]\n\t" \
  "s_branch QC_%=\n\t" \
  "s_branch QG10_%=\n\t" \
  "QF11_%=:\n\t" \
  "s_mov_b32 %[c], 11\n\t" \
  "s_getpc_b64 s[92:93]\n\t" \
  "s_branch QC_%=\n\t" \
  "s_branch QG11_%=\n\t" \
  "QF12_%=:\n\t" \
  "s_mov_b32 %[c], 12\n\t" \
  "s_getpc_b64 s[92:93]\n\t" \
  "s_branch QC_%=\n\t" \
  "s_branch QG12_%=\n\t" \
  "QF13_%=:\n\t" \
  "s_mov_b32 %[c], 13\n\t" \
  "s_getpc_b64 s[92:93]\n\t" \
  "s_branch QC_%=\n\t" \
  "s_branch QG13_%=\n\t" \
  "QF14_%=:\n\t" \
  "s_mov_b32 %[c], 14\n\t" \
  "s_getpc_b64 s[92:93]\n\t" \
  "s_branch QC_%=\n\t" \
  "s_branch QG14_%=\n\t" \
  "QF15_%=:\n\t" \
  "s_mov_b32 %[c], 15\n\t" \
  "s_getpc_b64 s[92:93]\n\t" \
  "s_branch QC_%=\n\t" \
  "s_branch QG15_%=\n\t" \
  "QC_%=:\n\t" \
  "s_add_u32 s92, s92, 4\n\t" \
  "s_addc_u32 s93, s93, 0\n\t" \
  "QU_%=:\n\t" \
  "ds_read_b32 v166, v165\n\t" \
  "s_add_u32 %[t1], %[c], 5\n\t" \
  "s_waitcnt lgkmcnt(0)\n\t" \
  "v_readfirstlane_b32 %[t0], v166\n\t" \
  "s_nop 0\n\t" \
  "s_min_u32 %[t1], %[t1], %[t0]\n\t" \
  "QL_%=:\n\t" \
  "s_cmp_ge_u32 %[iss], %[t1]\n\t" \
  "s_cbranch_scc1 QD_%=\n\t" \
  "s_mov_b32 %[t0], %[iss]\n\t" \
  "s_sub_u32 %[th], %[t0], 6\n\t" \
  "s_cselect_b32 %[t0], %[t0], %[th]\n\t" \
  "s_sub_u32 %[th], %[t0], 6\n\t" \
  "s_cselect_b32 %[t0], %[t0], %[th]\n\t" \
  "s_cmp_eq_u32 %[t0], 1\n\t" \
  "s_cbranch_scc1 QS1_%=\n\t" \
  "s_cmp_eq_u32 %[t0], 2\n\t" \
  "s_cbranch_scc1 QS2_%=\n\t" \
  "s_cmp_eq_u32 %[t0], 3\n\t" \
  "s_cbranch_scc1 QS3_%=\n\t" \
  "s_cmp_eq_u32 %[t0], 4\n\t" \
  "s_cbranch_scc1 QS4_%=\n\t" \
  "s_cmp_eq_u32 %[t0], 5\n\t" \
  "s_cbranch_scc1 QS5_%=\n\t" \
  "QS0_%=:\n\t" \
  "v_readlane_b32 s94, v167, %[iss]\n\t" \
  "s_nop 4\n\t" \
  "buffer_load_dwordx4 v[0:3], v164, %[ob], s94 offen offset:0 sc1\n\t" \
  "buffer_load_dwordx4 v[4:7], v164, %[ob], s94 offen offset:1024 sc1\n\t" \
  "buffer_load_dwordx4 v[8:11], v164, %[ob], s94 offen offset:2048 sc1\n\t" \
  "buffer_load_dwordx4 v[12:15], v164, %[ob], s94 offen offset:3072 sc1\n\t" \
  "s_add_u32 %[iss], %[iss], 1\n\t" \
  "s_cmp_ge_u32 %[iss], %[t1]\n\t" \
  "s_cbranch_scc1 QD_%=\n\t" \
  "QS1_%=:\n\t" \
  "v_readlane_b32 s94, v167, %[iss]\n\t" \
  "s_nop 4\n\t" \
  "buffer_load_dwordx4 v[16:19], v164, %[ob], s94 offen offset:0 sc1\n\t" \
  "buffer_load_dwordx4 v[20:23], v164, %[ob], s94 offen offset:1024 sc1\n\t" \
  "buffer_load_dwordx4 v[24:27], v164, %[ob], s94 offen offset:2048 sc1\n\t" \
  "buffer_load_dwordx4 v[28:31], v164, %[ob], s94 offen offset:3072 sc1\n\t" \
  "s_add_u32 %[iss], %[iss], 1\n\t" \
  "s_cmp_ge_u32 %[iss], %[t1]\n\t" \
  "s_cbranch_scc1 QD_%=\n\t" \
  "QS2_%=:\n\t" \
  "v_readlane_b32 s94, v167, %[iss]\n\t" \
  "s_nop 4\n\t" \
  "buffer_load_dwordx4 v[32:35], v164, %[ob], s94 offen offset:0 sc1\n\t" \
  "buffer_load_dwordx4 v[36:39], v164, %[ob], s94 offen offset:1024 sc1\n\t" \
  "buffer_load_dwordx4 v[40:43], v164, %[ob], s94 offen offset:2048 sc1\n\t" \
  "buffer_load_dwordx4 v[44:47], v164, %[ob], s94 offen offset:3072 sc1\n\t" \
  "s_add_u32 %[iss], %[iss], 1\n\t" \
  "s_cmp_ge_u32 %[iss], %[t1]\n\t" \
  "s_cbranch_scc1 QD_%=\n\t" \
  "QS3_%=:\n\t" \
  "v_readlane_b32 s94, v167, %[iss]\n\t" \
  "s_nop 4\n\t" \
  "buffer_load_dwordx4 v[48:51], v164, %[ob], s94 offen offset:0 sc1\n\t" \
  "buffer_load_dwordx4 v[52:55], v164, %[ob], s94 offen offset:1024 sc1\n\t" \
  "buffer_load_dwordx4 v[56:59], v164, %[ob], s94 offen offset:2048 sc1\n\t" \
  "buffer_load_dwordx4 v[60:63], v164, %[ob], s94 offen offset:3072 sc1\n\t" \
  "s_add_u32 %[iss], %[iss], 1\n\t" \
  "s_cmp_ge_u32 %[iss], %[t1]\n\t" \
  "s_cbranch_scc1 QD_%=\n\t" \
  "QS4_%=:\n\t" \
  "v_readlane_b32 s94, v167, %[iss]\n\t" \
  "s_nop 4\n\t" \
  "buffer_load_dwordx4 v[64:67], v164, %[ob], s94 offen offset:0 sc1\n\t" \
  "buffer_load_dwordx4 v[68:71], v164, %[ob], s94 offen offset:1024 sc1\n\t" \
  "buffer_load_dwordx4 v[72:75], v164, %[ob], s94 offen offset:2048 sc1\n\t" \
  "buffer_load_dwordx4 v[76:79], v164, %[ob], s94 offen offset:3072 sc1\n\t" \
  "s_add_u32 %[iss], %[iss], 1\n\t" \
  "s_cmp_ge_u32 %[iss], %[t1]\n\t" \
  "s_cbranch_scc1 QD_%=\n\t" \
  "QS5_%=:\n\t" \
  "v_readlane_b32 s94, v167, %[iss]\n\t" \
  "s_nop 4\n\t" \
  "buffer_load_dwordx4 v[80:83], v164, %[ob], s94 offen offset:0 sc1\n\t" \
  "buffer_load_dwordx4 v[84:87], v164, %[ob], s94 offen offset:1024 sc1\n\t" \
  "buffer_load_dwordx4 v[88:91], v164, %[ob], s94 offen offset:2048 sc1\n\t" \
  "buffer_load_dwordx4 v[92:95], v164, %[ob], s94 offen offset:3072 sc1\n\t" \
  "s_add_u32 %[iss], %[iss], 1\n\t" \
  "s_cmp_ge_u32 %[iss], %[t1]\n\t" \
  "s_cbranch_scc0 QS0_%=\n\t" \
  "QD_%=:\n\t" \
  "s_cmp_gt_u32 %[iss], %[c]\n\t" \
  "s_cbranch_scc1 QW_%=\n\t" \
  "s_addk_i32 %[cn], 1\n\t" \
  "s_cmp_gt_u32 %[cn], 0x400000\n\t" \
  "s_cbranch_scc1 QT_%=\n\t" \
  "s_sleep 1\n\t" \
  "s_branch QU_%=\n\t" \
  "QT_%=:\n\t" \
  "s_add_u32 %[t1], %[c], 1\n\t" \
  "s_branch QL_%=\n\t" \
  "QW_%=:\n\t" \
  "s_cmp_eq_u32 %[iss], 16\n\t" \
  "s_cselect_b64 vcc, -1, 0\n\t" \
  "s_sub_u32 %[t0], %[iss], %[c]\n\t" \
  "s_cmp_eq_u32 %[t0], 5\n\t" \
  "s_cselect_b32 %[th], %[iss], 99\n\t" \
  "s_waitcnt vmcnt(16)\n\t" \
  "s_cmp_ge_u32 %[t0], 5\n\t" \
  "s_cbranch_scc1 QR_%=\n\t" \
  "s_waitcnt vmcnt(12)\n\t" \
  "s_cmp_ge_u32 %[t0], 4\n\t" \
  "s_cbranch_scc1 QR_%=\n\t" \
  "s_waitcnt vmcnt(8)\n\t" \
  "s_cmp_ge_u32 %[t0], 3\n\t" \
  "s_cbranch_scc1 QR_%=\n\t" \
  "s_waitcnt vmcnt(4)\n\t" \
  "s_cmp_ge_u32 %[t0], 2\n\t" \
  "s_cbranch_scc1 QR_%=\n\t" \
  "s_waitcnt vmcnt(0)\n\t" \
  "QR_%=:\n\t" \
  "s_setpc_b64 s[92:93]\n\t" \
  "QX_%=:\n\t" \
  "s_nop 7\n\t"
#define G3_CLOB "v0", "v1", "v2", "v3", "v4", "v5", "v6", "v7", "v8", "v9", "v10", "v11", "v12", "v13", "v14", "v15", "v16", "v17", "v18", "v19", "v20", "v21", "v22", "v23", "v24", "v25", "v26", "v27", "v28", "v29", "v30", "v31", "v32", "v33", "v34", "v35", "v36", "v37", "v38", "v39", "v40", "v41", "v42", "v43", "v44", "v45", "v46", "v47", "v48", "v49", "v50", "v51", "v52", "v53", "v54", "v55", "v56", "v57", "v58", "v59", "v60", "v61", "v62", "v63", "v64", "v65", "v66", "v67", "v68", "v69", "v70", "v71", "v72", "v73", "v74", "v75", "v76", "v77", "v78", "v79", "v80", "v81", "v82", "v83", "v84", "v85", "v86", "v87", "v88", "v89", "v90", "v91", "v92", "v93", "v94", "v95", "v96", "v97", "v98", "v99", "v100", "v101", "v102", "v103", "v104", "v105", "v106", "v107", "v108", "v109", "v110", "v111", "v112", "v113", "v114", "v115", "v116", "v117", "v118", "v119", "v120", "v121", "v122", "v123", "v124", "v125", "v126", "v127", "v166", "s88", "s89", "s92", "s93", "s94"

#define PP_ASM \
  "v_mov_b32 v60, 0\n\t" \
  "s_mov_b32 %[t0], %[n0]\n\t" \
  "s_cmp_eq_u32 %[t0], 0\n\t" \
  "s_cbranch_scc1 PAx_%=\n\t" \
  "PA_%=:\n\t" \
  "s_barrier\n\t" \
  "s_sub_u32 %[t0], %[t0], 1\n\t" \
  "s_cmp_lg_u32 %[t0], 0\n\t" \
  "s_cbranch_scc1 PA_%=\n\t" \
  "PAx_%=:\n\t" \
  "s_mov_b32 m0, %[shd]\n\t" \
  "global_load_dword v160, v60, %[fl] sc1\n\t" \
  "global_load_lds_dwordx4 v163, %[shp]\n\t" \
  "s_mov_b32 m0, %[tgd]\n\t" \
  "s_mov_b64 exec, %[em]\n\t" \
  "global_load_lds_dword v60, %[fl] sc1\n\t" \
  "s_mov_b64 exec, -1\n\t" \
  "global_load_ushort v161, v60, %[hp]\n\t" \
  "global_load_ushort v162, v60, %[dp]\n\t" \
  "global_load_dwordx4 v[0:3], v152, %[kb0]\n\t" \
  "global_load_dwordx4 v[4:7], v153, %[kb0]\n\t" \
  "global_load_dwordx4 v[8:11], v154, %[kb0]\n\t" \
  "global_load_dwordx4 v[12:15], v155, %[kb3] offset:0\n\t" \
  "global_load_dwordx4 v[28:31], v155, %[vb0] offset:0\n\t" \
  "global_load_dwordx4 v[16:19], v155, %[kb3] offset:2048\n\t" \
  "global_load_dwordx4 v[32:35], v155, %[vb0] offset:2048\n\t" \
  "global_load_dwordx4 v[20:23], v155, %[kb5] offset:0\n\t" \
  "global_load_dwordx4 v[36:39], v155, %[vb2] offset:0\n\t" \
  "global_load_dwordx4 v[24:27], v155, %[kb5] offset:2048\n\t" \
  "global_load_dwordx4 v[40:43], v155, %[vb2] offset:2048\n\t" \
  "s_setprio 2\n\t" \
  "s_barrier\n\t" \
  "s_barrier\n\t" \
  "s_waitcnt vmcnt(11)\n\t" \
  "ds_read_b128 v[128:131], v156 offset:0\n\t" \
  "ds_read_b128 v[132:135], v156 offset:16\n\t" \
  "ds_read_b128 v[136:139], v156 offset:32\n\t" \
  "ds_read_b128 v[140:143], v156 offset:48\n\t" \
  "s_waitcnt lgkmcnt(0)\n\t" \
  "v_lshlrev_b32 v64, 16, v128\n\t" \
  "v_and_b32 v65, 0xffff0000, v128\n\t" \
  "v_lshlrev_b32 v66, 16, v129\n\t" \
  "v_and_b32 v67, 0xffff0000, v129\n\t" \
  "v_lshlrev_b32 v68, 16, v130\n\t" \
  "v_and_b32 v69, 0xffff0000, v130\n\t" \
  "v_lshlrev_b32 v70, 16, v131\n\t" \
  "v_and_b32 v71, 0xffff0000, v131\n\t" \
  "v_lshlrev_b32 v72, 16, v132\n\t" \
  "v_and_b32 v73, 0xffff0000, v132\n\t" \
  "v_lshlrev_b32 v74, 16, v133\n\t" \
  "v_and_b32 v75, 0xffff0000, v133\n\t" \
  "v_lshlrev_b32 v76, 16, v134\n\t" \
  "v_and_b32 v77, 0xffff0000, v134\n\t" \
  "v_lshlrev_b32 v78, 16, v135\n\t" \
  "v_and_b32 v79, 0xffff0000, v135\n\t" \
  "v_lshlrev_b32 v80, 16, v136\n\t" \
  "v_and_b32 v81, 0xffff0000, v136\n\t" \
  "v_lshlrev_b32 v82, 16, v137\n\t" \
  "v_and_b32 v83, 0xffff0000, v137\n\t" \
  "v_lshlrev_b32 v84, 16, v138\n\t" \
  "v_and_b32 v85, 0xffff0000, v138\n\t" \
  "v_lshlrev_b32 v86, 16, v139\n\t" \
  "v_and_b32 v87, 0xffff0000, v139\n\t" \
  "v_lshlrev_b32 v88, 16, v140\n\t" \
  "v_and_b32 v89, 0xffff0000, v140\n\t" \
  "v_lshlrev_b32 v90, 16, v141\n\t" \
  "v_and_b32 v91, 0xffff0000, v141\n\t" \
  "v_lshlrev_b32 v92, 16, v142\n\t" \
  "v_and_b32 v93, 0xffff0000, v142\n\t" \
  "v_lshlrev_b32 v94, 16, v143\n\t" \
  "v_and_b32 v95, 0xffff0000, v143\n\t" \
  "s_waitcnt vmcnt(10)\n\t" \
  "s_mov_b64 exec, %[zm]\n\t" \
  "v_mov_b32 v0, 0\n\t" \
  "v_mov_b32 v1, 0\n\t" \
  "v_mov_b32 v2, 0\n\t" \
  "v_mov_b32 v3, 0\n\t" \
  "s_mov_b64 exec, -1\n\t" \
  "v_lshlrev_b32 v128, 16, v0\n\t" \
  "v_and_b32 v129, 0xffff0000, v0\n\t" \
  "s_barrier\n\t" \
  "v_lshlrev_b32 v130, 16, v1\n\t" \
  "v_and_b32 v131, 0xffff0000, v1\n\t" \
  "v_lshlrev_b32 v132, 16, v2\n\t" \
  "v_and_b32 v133, 0xffff0000, v2\n\t" \
  "v_lshlrev_b32 v134, 16, v3\n\t" \
  "v_and_b32 v135, 0xffff0000, v3\n\t" \
  "v_mul_f32 v96, v128, v67\n\t" \
  "v_mul_f32 v97, v129, v71\n\t" \
  "v_mul_f32 v98, v130, v75\n\t" \
  "v_mul_f32 v99, v131, v79\n\t" \
  "v_mul_f32 v100, v132, v83\n\t" \
  "v_mul_f32 v101, v133, v87\n\t" \
  "v_mul_f32 v102, v134, v91\n\t" \
  "v_mul_f32 v103, v135, v95\n\t" \
  "s_waitcnt vmcnt(9)\n\t" \
  "s_mov_b64 exec, %[zm]\n\t" \
  "v_mov_b32 v4, 0\n\t" \
  "v_mov_b32 v5, 0\n\t" \
  "v_mov_b32 v6, 0\n\t" \
  "v_mov_b32 v7, 0\n\t" \
  "s_mov_b64 exec, -1\n\t" \
  "v_lshlrev_b32 v128, 16, v4\n\t" \
  "v_and_b32 v129, 0xffff0000, v4\n\t" \
  "v_lshlrev_b32 v130, 16, v5\n\t" \
  "v_and_b32 v131, 0xffff0000, v5\n\t" \
  "v_lshlrev_b32 v132, 16, v6\n\t" \
  "v_and_b32 v133, 0xffff0000, v6\n\t" \
  "v_lshlrev_b32 v134, 16, v7\n\t" \
  "v_and_b32 v135, 0xffff0000, v7\n\t" \
  "v_fmac_f32 v96, v128, v66\n\t" \
  "v_fmac_f32 v97, v129, v70\n\t" \
  "v_fmac_f32 v98, v130, v74\n\t" \
  "v_fmac_f32 v99, v131, v78\n\t" \
  "v_fmac_f32 v100, v132, v82\n\t" \
  "v_fmac_f32 v101, v133, v86\n\t" \
  "v_fmac_f32 v102, v134, v90\n\t" \
  "v_fmac_f32 v103, v135, v94\n\t" \
  "v_mul_f32 v104, v128, v67\n\t" \
  "v_mul_f32 v105, v129, v71\n\t" \
  "v_mul_f32 v106, v130, v75\n\t" \
  "v_mul_f32 v107, v131, v79\n\t" \
  "v_mul_f32 v108, v132, v83\n\t" \
  "v_mul_f32 v109, v133, v87\n\t" \
  "v_mul_f32 v110, v134, v91\n\t" \
  "v_mul_f32 v111, v135, v95\n\t" \
  "s_waitcnt vmcnt(8)\n\t" \
  "s_mov_b64 exec, %[zm]\n\t" \
  "v_mov_b32 v8, 0\n\t" \
  "v_mov_b32 v9, 0\n\t" \
  "v_mov_b32 v10, 0\n\t" \
  "v_mov_b32 v11, 0\n\t" \
  "s_mov_b64 exec, -1\n\t" \
  "s_setprio 3\n\t" \
  "s_barrier\n\t" \
  "s_mov_b32 m0, %[hd]\n\t" \
  "s_nop 0\n\t" \
  "global_load_lds_dwordx4 v163, %[hp] offset:0\n\t" \
  "global_load_lds_dwordx4 v163, %[hp] offset:1024\n\t" \
  "global_load_lds_dwordx4 v163, %[hp] offset:2048\n\t" \
  "global_load_lds_dwordx4 v163, %[hp] offset:3072\n\t" \
  "global_load_dwordx4 v[44:47], v155, %[qb0] offset:0\n\t" \
  "global_load_dwordx4 v[48:51], v155, %[qb0] offset:2048\n\t" \
  "global_load_dwordx4 v[52:55], v155, %[qb2] offset:0\n\t" \
  "global_load_dwordx4 v[56:59], v155, %[qb2] offset:2048\n\t" \
  "s_setprio 2\n\t" \
  "v_lshlrev_b32 v128, 16, v8\n\t" \
  "v_and_b32 v129, 0xffff0000, v8\n\t" \
  "v_lshlrev_b32 v130, 16, v9\n\t" \
  "v_and_b32 v131, 0xffff0000, v9\n\t" \
  "v_lshlrev_b32 v132, 16, v10\n\t" \
  "v_and_b32 v133, 0xffff0000, v10\n\t" \
  "v_lshlrev_b32 v134, 16, v11\n\t" \
  "v_and_b32 v135, 0xffff0000, v11\n\t" \
  "v_fmac_f32 v96, v128, v65\n\t" \
  "v_fmac_f32 v97, v129, v69\n\t" \
  "v_fmac_f32 v98, v130, v73\n\t" \
  "v_fmac_f32 v99, v131, v77\n\t" \
  "v_fmac_f32 v100, v132, v81\n\t" \
  "v_fmac_f32 v101, v133, v85\n\t" \
  "v_fmac_f32 v102, v134, v89\n\t" \
  "v_fmac_f32 v103, v135, v93\n\t" \
  "v_fmac_f32 v104, v128, v66\n\t" \
  "v_fmac_f32 v105, v129, v70\n\t" \
  "v_fmac_f32 v106, v130, v74\n\t" \
  "v_fmac_f32 v107, v131, v78\n\t" \
  "v_fmac_f32 v108, v132, v82\n\t" \
  "v_fmac_f32 v109, v133, v86\n\t" \
  "v_fmac_f32 v110, v134, v90\n\t" \
  "v_fmac_f32 v111, v135, v94\n\t" \
  "v_mul_f32 v112, v128, v67\n\t" \
  "v_mul_f32 v113, v129, v71\n\t" \
  "v_mul_f32 v114, v130, v75\n\t" \
  "v_mul_f32 v115, v131, v79\n\t" \
  "v_mul_f32 v116, v132, v83\n\t" \
  "v_mul_f32 v117, v133, v87\n\t" \
  "v_mul_f32 v118, v134, v91\n\t" \
  "v_mul_f32 v119, v135, v95\n\t" \
  "s_waitcnt vmcnt(15)\n\t" \
  "v_lshlrev_b32 v128, 16, v12\n\t" \
  "v_and_b32 v129, 0xffff0000, v12\n\t" \
  "v_lshlrev_b32 v130, 16, v13\n\t" \
  "v_and_b32 v131, 0xffff0000, v13\n\t" \
  "v_lshlrev_b32 v132, 16, v14\n\t" \
  "v_and_b32 v133, 0xffff0000, v14\n\t" \
  "v_lshlrev_b32 v134, 16, v15\n\t" \
  "v_and_b32 v135, 0xffff0000, v15\n\t" \
  "s_barrier\n\t" \
  "v_fmac_f32 v96, v128, v64\n\t" \
  "v_fmac_f32 v97, v129, v68\n\t" \
  "v_fmac_f32 v98, v130, v72\n\t" \
  "v_fmac_f32 v99, v131, v76\n\t" \
  "v_fmac_f32 v100, v132, v80\n\t" \
  "v_fmac_f32 v101, v133, v84\n\t" \
  "v_fmac_f32 v102, v134, v88\n\t" \
  "v_fmac_f32 v103, v135, v92\n\t" \
  "v_fmac_f32 v104, v128, v65\n\t" \
  "v_fmac_f32 v105, v129, v69\n\t" \
  "v_fmac_f32 v106, v130, v73\n\t" \
  "v_fmac_f32 v107, v131, v77\n\t" \
  "v_fmac_f32 v108, v132, v81\n\t" \
  "v_fmac_f32 v109, v133, v85\n\t" \
  "v_fmac_f32 v110, v134, v89\n\t" \
  "v_fmac_f32 v111, v135, v93\n\t" \
  "v_fmac_f32 v112, v128, v66\n\t" \
  "v_fmac_f32 v113, v129, v70\n\t" \
  "v_fmac_f32 v114, v130, v74\n\t" \
  "v_fmac_f32 v115, v131, v78\n\t" \
  "v_fmac_f32 v116, v132, v82\n\t" \
  "v_fmac_f32 v117, v133, v86\n\t" \
  "v_fmac_f32 v118, v134, v90\n\t" \
  "v_fmac_f32 v119, v135, v94\n\t" \
  "v_mul_f32 v120, v128, v67\n\t" \
  "v_mul_f32 v121, v129, v71\n\t" \
  "v_mul_f32 v122, v130, v75\n\t" \
  "v_mul_f32 v123, v131, v79\n\t" \
  "v_mul_f32 v124, v132, v83\n\t" \
  "v_mul_f32 v125, v133, v87\n\t" \
  "v_mul_f32 v126, v134, v91\n\t" \
  "v_mul_f32 v127, v135, v95\n\t" \
  "s_waitcnt vmcnt(14)\n\t" \
  "v_lshlrev_b32 v128, 16, v28\n\t" \
  "v_and_b32 v129, 0xffff0000, v28\n\t" \
  "v_lshlrev_b32 v130, 16, v29\n\t" \
  "v_and_b32 v131, 0xffff0000, v29\n\t" \
  "v_lshlrev_b32 v132, 16, v30\n\t" \
  "v_and_b32 v133, 0xffff0000, v30\n\t" \
  "v_lshlrev_b32 v134, 16, v31\n\t" \
  "v_and_b32 v135, 0xffff0000, v31\n\t" \
  "s_barrier\n\t" \
  "v_mul_f32 v96, v96, v128\n\t" \
  "v_mul_f32 v97, v97, v129\n\t" \
  "v_mul_f32 v98, v98, v130\n\t" \
  "v_mul_f32 v99, v99, v131\n\t" \
  "v_mul_f32 v100, v100, v132\n\t" \
  "v_mul_f32 v101, v101, v133\n\t" \
  "v_mul_f32 v102, v102, v134\n\t" \
  "v_mul_f32 v103, v103, v135\n\t" \
  "v_cvt_pk_bf16_f32 v136, v96, v97\n\t" \
  "v_cvt_pk_bf16_f32 v137, v98, v99\n\t" \
  "v_cvt_pk_bf16_f32 v138, v100, v101\n\t" \
  "v_cvt_pk_bf16_f32 v139, v102, v103\n\t" \
  "v_mov_b32_dpp v128, v136 row_ror:8 row_mask:0xf bank_mask:0xf\n\t" \
  "v_mov_b32_dpp v129, v137 row_ror:8 row_mask:0xf bank_mask:0xf\n\t" \
  "v_mov_b32_dpp v130, v138 row_ror:8 row_mask:0xf bank_mask:0xf\n\t" \
  "v_mov_b32_dpp v131, v139 row_ror:8 row_mask:0xf bank_mask:0xf\n\t" \
  "v_perm_b32 v136, v128, v136, v157\n\t" \
  "v_perm_b32 v137, v129, v137, v157\n\t" \
  "v_perm_b32 v138, v130, v138, v157\n\t" \
  "v_perm_b32 v139, v131, v139, v157\n\t" \
  "v_permlane16_swap_b32 v136, v137\n\t" \
  "s_nop 0\n\t" \
  "v_permlane16_swap_b32 v138, v139\n\t" \
  "s_nop 1\n\t" \
  "v_permlane32_swap_b32 v136, v138\n\t" \
  "v_permlane32_swap_b32 v137, v139\n\t" \
  "s_waitcnt vmcnt(13)\n\t" \
  "v_lshlrev_b32 v128, 16, v16\n\t" \
  "v_and_b32 v129, 0xffff0000, v16\n\t" \
  "v_lshlrev_b32 v130, 16, v17\n\t" \
  "v_and_b32 v131, 0xffff0000, v17\n\t" \
  "v_lshlrev_b32 v132, 16, v18\n\t" \
  "v_and_b32 v133, 0xffff0000, v18\n\t" \
  "v_lshlrev_b32 v134, 16, v19\n\t" \
  "v_and_b32 v135, 0xffff0000, v19\n\t" \
  "v_fmac_f32 v104, v128, v64\n\t" \
  "v_fmac_f32 v105, v129, v68\n\t" \
  "v_fmac_f32 v106, v130, v72\n\t" \
  "v_fmac_f32 v107, v131, v76\n\t" \
  "v_fmac_f32 v108, v132, v80\n\t" \
  "v_fmac_f32 v109, v133, v84\n\t" \
  "s_setprio 3\n\t" \
  "s_barrier\n\t" \
  "global_store_dwordx4 v159, v[136:139], %[sb] sc1\n\t" \
  "v_add_u32 v158, 0x80, v159\n\t" \
  "v_bfi_b32 v159, %[mk], v158, v159\n\t" \
  "s_setprio 2\n\t" \
  "v_fmac_f32 v110, v134, v88\n\t" \
  "v_fmac_f32 v111, v135, v92\n\t" \
  "v_fmac_f32 v112, v128, v65\n\t" \
  "v_fmac_f32 v113, v129, v69\n\t" \
  "v_fmac_f32 v114, v130, v73\n\t" \
  "v_fmac_f32 v115, v131, v77\n\t" \
  "v_fmac_f32 v116, v132, v81\n\t" \
  "v_fmac_f32 v117, v133, v85\n\t" \
  "v_fmac_f32 v118, v134, v89\n\t" \
  "v_fmac_f32 v119, v135, v93\n\t" \
  "v_fmac_f32 v120, v128, v66\n\t" \
  "v_fmac_f32 v121, v129, v70\n\t" \
  "v_fmac_f32 v122, v130, v74\n\t" \
  "v_fmac_f32 v123, v131, v78\n\t" \
  "v_fmac_f32 v124, v132, v82\n\t" \
  "v_fmac_f32 v125, v133, v86\n\t" \
  "v_fmac_f32 v126, v134, v90\n\t" \
  "v_fmac_f32 v127, v135, v94\n\t" \
  "s_waitcnt vmcnt(13)\n\t" \
  "v_lshlrev_b32 v128, 16, v32\n\t" \
  "v_and_b32 v129, 0xffff0000, v32\n\t" \
  "v_lshlrev_b32 v130, 16, v33\n\t" \
  "v_and_b32 v131, 0xffff0000, v33\n\t" \
  "v_lshlrev_b32 v132, 16, v34\n\t" \
  "v_and_b32 v133, 0xffff0000, v34\n\t" \
  "v_lshlrev_b32 v134, 16, v35\n\t" \
  "v_and_b32 v135, 0xffff0000, v35\n\t" \
  "v_mul_f32 v104, v104, v128\n\t" \
  "v_mul_f32 v105, v105, v129\n\t" \
  "v_mul_f32 v106, v106, v130\n\t" \
  "v_mul_f32 v107, v107, v131\n\t" \
  "v_mul_f32 v108, v108, v132\n\t" \
  "v_mul_f32 v109, v109, v133\n\t" \
  "v_mul_f32 v110, v110, v134\n\t" \
  "v_mul_f32 v111, v111, v135\n\t" \
  "v_cvt_pk_bf16_f32 v140, v104, v105\n\t" \
  "v_cvt_pk_bf16_f32 v141, v106, v107\n\t" \
  "v_cvt_pk_bf16_f32 v142, v108, v109\n\t" \
  "v_cvt_pk_bf16_f32 v143, v110, v111\n\t" \
  "v_mov_b32_dpp v128, v140 row_ror:8 row_mask:0xf bank_mask:0xf\n\t" \
  "v_mov_b32_dpp v129, v141 row_ror:8 row_mask:0xf bank_mask:0xf\n\t" \
  "s_barrier\n\t" \
  "v_mov_b32_dpp v130, v142 row_ror:8 row_mask:0xf bank_mask:0xf\n\t" \
  "v_mov_b32_dpp v131, v143 row_ror:8 row_mask:0xf bank_mask:0xf\n\t" \
  "v_perm_b32 v140, v128, v140, v157\n\t" \
  "v_perm_b32 v141, v129, v141, v157\n\t" \
  "v_perm_b32 v142, v130, v142, v157\n\t" \
  "v_perm_b32 v143, v131, v143, v157\n\t" \
  "v_permlane16_swap_b32 v140, v141\n\t" \
  "s_nop 0\n\t" \
  "v_permlane16_swap_b32 v142, v143\n\t" \
  "s_nop 1\n\t" \
  "v_permlane32_swap_b32 v140, v142\n\t" \
  "v_permlane32_swap_b32 v141, v143\n\t" \
  "s_waitcnt vmcnt(12)\n\t" \
  "v_lshlrev_b32 v128, 16, v20\n\t" \
  "v_and_b32 v129, 0xffff0000, v20\n\t" \
  "v_lshlrev_b32 v130, 16, v21\n\t" \
  "v_and_b32 v131, 0xffff0000, v21\n\t" \
  "v_lshlrev_b32 v132, 16, v22\n\t" \
  "v_and_b32 v133, 0xffff0000, v22\n\t" \
  "v_lshlrev_b32 v134, 16, v23\n\t" \
  "v_and_b32 v135, 0xffff0000, v23\n\t" \
  "v_fmac_f32 v112, v128, v64\n\t" \
  "v_fmac_f32 v113, v129, v68\n\t" \
  "v_fmac_f32 v114, v130, v72\n\t" \
  "v_fmac_f32 v115, v131, v76\n\t" \
  "v_fmac_f32 v116, v132, v80\n\t" \
  "v_fmac_f32 v117, v133, v84\n\t" \
  "v_fmac_f32 v118, v134, v88\n\t" \
  "v_fmac_f32 v119, v135, v92\n\t" \
  "v_fmac_f32 v120, v128, v65\n\t" \
  "v_fmac_f32 v121, v129, v69\n\t" \
  "v_fmac_f32 v122, v130, v73\n\t" \
  "v_fmac_f32 v123, v131, v77\n\t" \
  "v_fmac_f32 v124, v132, v81\n\t" \
  "v_fmac_f32 v125, v133, v85\n\t" \
  "v_fmac_f32 v126, v134, v89\n\t" \
  "v_fmac_f32 v127, v135, v93\n\t" \
  "s_waitcnt vmcnt(11)\n\t" \
  "v_lshlrev_b32 v128, 16, v36\n\t" \
  "v_and_b32 v129, 0xffff0000, v36\n\t" \
  "v_lshlrev_b32 v130, 16, v37\n\t" \
  "v_and_b32 v131, 0xffff0000, v37\n\t" \
  "s_setprio 3\n\t" \
  "s_barrier\n\t" \
  "global_store_dwordx4 v159, v[140:143], %[sb] sc1\n\t" \
  "v_add_u32 v158, 0x80, v159\n\t" \
  "v_bfi_b32 v159, %[mk], v158, v159\n\t" \
  "s_setprio 2\n\t" \
  "v_lshlrev_b32 v132, 16, v38\n\t" \
  "v_and_b32 v133, 0xffff0000, v38\n\t" \
  "v_lshlrev_b32 v134, 16, v39\n\t" \
  "v_and_b32 v135, 0xffff0000, v39\n\t" \
  "v_mul_f32 v112, v112, v128\n\t" \
  "v_mul_f32 v113, v113, v129\n\t" \
  "v_mul_f32 v114, v114, v130\n\t" \
  "v_mul_f32 v115, v115, v131\n\t" \
  "v_mul_f32 v116, v116, v132\n\t" \
  "v_mul_f32 v117, v117, v133\n\t" \
  "v_mul_f32 v118, v118, v134\n\t" \
  "v_mul_f32 v119, v119, v135\n\t" \
  "v_cvt_pk_bf16_f32 v144, v112, v113\n\t" \
  "v_cvt_pk_bf16_f32 v145, v114, v115\n\t" \
  "v_cvt_pk_bf16_f32 v146, v116, v117\n\t" \
  "v_cvt_pk_bf16_f32 v147, v118, v119\n\t" \
  "v_mov_b32_dpp v128, v144 row_ror:8 row_mask:0xf bank_mask:0xf\n\t" \
  "v_mov_b32_dpp v129, v145 row_ror:8 row_mask:0xf bank_mask:0xf\n\t" \
  "v_mov_b32_dpp v130, v146 row_ror:8 row_mask:0xf bank_mask:0xf\n\t" \
  "v_mov_b32_dpp v131, v147 row_ror:8 row_mask:0xf bank_mask:0xf\n\t" \
  "v_perm_b32 v144, v128, v144, v157\n\t" \
  "v_perm_b32 v145, v129, v145, v157\n\t" \
  "v_perm_b32 v146, v130, v146, v157\n\t" \
  "v_perm_b32 v147, v131, v147, v157\n\t" \
  "v_permlane16_swap_b32 v144, v145\n\t" \
  "s_nop 0\n\t" \
  "v_permlane16_swap_b32 v146, v147\n\t" \
  "s_nop 1\n\t" \
  "v_permlane32_swap_b32 v144, v146\n\t" \
  "v_permlane32_swap_b32 v145, v147\n\t" \
  "s_waitcnt vmcnt(11)\n\t" \
  "v_lshlrev_b32 v128, 16, v24\n\t" \
  "v_and_b32 v129, 0xffff0000, v24\n\t" \
  "v_lshlrev_b32 v130, 16, v25\n\t" \
  "v_and_b32 v131, 0xffff0000, v25\n\t" \
  "v_lshlrev_b32 v132, 16, v26\n\t" \
  "v_and_b32 v133, 0xffff0000, v26\n\t" \
  "v_lshlrev_b32 v134, 16, v27\n\t" \
  "v_and_b32 v135, 0xffff0000, v27\n\t" \
  "v_fmac_f32 v120, v128, v64\n\t" \
  "v_fmac_f32 v121, v129, v68\n\t" \
  "s_setprio 3\n\t" \
  "s_barrier\n\t" \
  "global_store_dwordx4 v159, v[144:147], %[sb] sc1\n\t" \
  "v_add_u32 v158, 0x80, v159\n\t" \
  "v_bfi_b32 v159, %[mk], v158, v159\n\t" \
  "s_setprio 2\n\t" \
  "v_fmac_f32 v122, v130, v72\n\t" \
  "v_fmac_f32 v123, v131, v76\n\t" \
  "v_fmac_f32 v124, v132, v80\n\t" \
  "v_fmac_f32 v125, v133, v84\n\t" \
  "v_fmac_f32 v126, v134, v88\n\t" \
  "v_fmac_f32 v127, v135, v92\n\t" \
  "s_waitcnt vmcnt(11)\n\t" \
  "v_lshlrev_b32 v128, 16, v40\n\t" \
  "v_and_b32 v129, 0xffff0000, v40\n\t" \
  "v_lshlrev_b32 v130, 16, v41\n\t" \
  "v_and_b32 v131, 0xffff0000, v41\n\t" \
  "v_lshlrev_b32 v132, 16, v42\n\t" \
  "v_and_b32 v133, 0xffff0000, v42\n\t" \
  "v_lshlrev_b32 v134, 16, v43\n\t" \
  "v_and_b32 v135, 0xffff0000, v43\n\t" \
  "v_mul_f32 v120, v120, v128\n\t" \
  "v_mul_f32 v121, v121, v129\n\t" \
  "v_mul_f32 v122, v122, v130\n\t" \
  "v_mul_f32 v123, v123, v131\n\t" \
  "v_mul_f32 v124, v124, v132\n\t" \
  "v_mul_f32 v125, v125, v133\n\t" \
  "v_mul_f32 v126, v126, v134\n\t" \
  "v_mul_f32 v127, v127, v135\n\t" \
  "v_cvt_pk_bf16_f32 v148, v120, v121\n\t" \
  "v_cvt_pk_bf16_f32 v149, v122, v123\n\t" \
  "v_cvt_pk_bf16_f32 v150, v124, v125\n\t" \
  "v_cvt_pk_bf16_f32 v151, v126, v127\n\t" \
  "v_mov_b32_dpp v128, v148 row_ror:8 row_mask:0xf bank_mask:0xf\n\t" \
  "v_mov_b32_dpp v129, v149 row_ror:8 row_mask:0xf bank_mask:0xf\n\t" \
  "v_mov_b32_dpp v130, v150 row_ror:8 row_mask:0xf bank_mask:0xf\n\t" \
  "v_mov_b32_dpp v131, v151 row_ror:8 row_mask:0xf bank_mask:0xf\n\t" \
  "v_perm_b32 v148, v128, v148, v157\n\t" \
  "v_perm_b32 v149, v129, v149, v157\n\t" \
  "v_perm_b32 v150, v130, v150, v157\n\t" \
  "v_perm_b32 v151, v131, v151, v157\n\t" \
  "v_permlane16_swap_b32 v148, v149\n\t" \
  "s_nop 0\n\t" \
  "v_permlane16_swap_b32 v150, v151\n\t" \
  "s_nop 1\n\t" \
  "v_permlane32_swap_b32 v148, v150\n\t" \
  "v_permlane32_swap_b32 v149, v151\n\t" \
  "s_setprio 3\n\t" \
  "s_barrier\n\t" \
  "global_store_dwordx4 v159, v[148:151], %[sb] sc1\n\t" \
  "s_setprio 2\n\t" \
  "s_waitcnt vmcnt(8)\n\t" \
  "ds_read_b64_tr_b16 v[64:65], v165 offset:32\n\t" \
  "ds_read_b64_tr_b16 v[66:67], v165 offset:0\n\t" \
  "ds_read_b64_tr_b16 v[68:69], v165 offset:288\n\t" \
  "ds_read_b64_tr_b16 v[70:71], v165 offset:256\n\t" \
  "ds_read_b64_tr_b16 v[72:73], v165 offset:544\n\t" \
  "ds_read_b64_tr_b16 v[74:75], v165 offset:512\n\t" \
  "ds_read_b64_tr_b16 v[76:77], v165 offset:800\n\t" \
  "ds_read_b64_tr_b16 v[78:79], v165 offset:768\n\t" \
  "v_mov_b32 v24, v166\n\t" \
  "v_mov_b32 v25, v167\n\t" \
  "v_mov_b32 v63, 0xc0\n\t" \
  "v_add_u32 v158, 64, v24\n\t" \
  "v_bfi_b32 v26, v63, v158, v24\n\t" \
  "v_add_u32 v158, 64, v25\n\t" \
  "v_bfi_b32 v27, v63, v158, v25\n\t" \
  "v_add_u32 v158, 64, v26\n\t" \
  "v_bfi_b32 v28, v63, v158, v26\n\t" \
  "v_add_u32 v158, 64, v27\n\t" \
  "v_bfi_b32 v29, v63, v158, v27\n\t" \
  "v_add_u32 v158, 64, v28\n\t" \
  "v_bfi_b32 v30, v63, v158, v28\n\t" \
  "v_add_u32 v158, 64, v29\n\t" \
  "v_bfi_b32 v31, v63, v158, v29\n\t" \
  "s_barrier\n\t" \
  "ds_read_b64_tr_b16 v[80:81], v165 offset:1056\n\t" \
  "ds_read_b64_tr_b16 v[82:83], v165 offset:1024\n\t" \
  "ds_read_b64_tr_b16 v[84:85], v165 offset:1312\n\t" \
  "ds_read_b64_tr_b16 v[86:87], v165 offset:1280\n\t" \
  "ds_read_b64_tr_b16 v[88:89], v165 offset:1568\n\t" \
  "ds_read_b64_tr_b16 v[90:91], v165 offset:1536\n\t" \
  "ds_read_b64_tr_b16 v[92:93], v165 offset:1824\n\t" \
  "ds_read_b64_tr_b16 v[94:95], v165 offset:1792\n\t" \
  "s_setprio 3\n\t" \
  "s_barrier\n\t" \
  "s_waitcnt vmcnt(0)\n\t" \
  "v_add_u32 v61, 1, v160\n\t" \
  "s_mov_b64 exec, 1\n\t" \
  "global_store_dword v60, v61, %[ff] sc1\n\t" \
  "s_mov_b64 exec, -1\n\t" \
  "s_setprio 2\n\t" \
  "ds_read_b64_tr_b16 v[96:97], v165 offset:2080\n\t" \
  "ds_read_b64_tr_b16 v[98:99], v165 offset:2048\n\t" \
  "ds_read_b64_tr_b16 v[100:101], v165 offset:2336\n\t" \
  "ds_read_b64_tr_b16 v[102:103], v165 offset:2304\n\t" \
  "ds_read_b64_tr_b16 v[104:105], v165 offset:2592\n\t" \
  "ds_read_b64_tr_b16 v[106:107], v165 offset:2560\n\t" \
  "ds_read_b64_tr_b16 v[108:109], v165 offset:2848\n\t" \
  "ds_read_b64_tr_b16 v[110:111], v165 offset:2816\n\t" \
  "s_barrier\n\t" \
  "ds_read_b64_tr_b16 v[112:113], v165 offset:3104\n\t" \
  "ds_read_b64_tr_b16 v[114:115], v165 offset:3072\n\t" \
  "ds_read_b64_tr_b16 v[116:117], v165 offset:3360\n\t" \
  "ds_read_b64_tr_b16 v[118:119], v165 offset:3328\n\t" \
  "ds_read_b64_tr_b16 v[120:121], v165 offset:3616\n\t" \
  "ds_read_b64_tr_b16 v[122:123], v165 offset:3584\n\t" \
  "ds_read_b64_tr_b16 v[124:125], v165 offset:3872\n\t" \
  "ds_read_b64_tr_b16 v[126:127], v165 offset:3840\n\t" \
  "s_barrier\n\t" \
  "s_waitcnt lgkmcnt(0)\n\t" \
  "v_lshlrev_b32 v61, 16, v161\n\t" \
  "v_lshlrev_b32 v62, 16, v162\n\t" \
  "v_add_f32 v61, v61, v62\n\t" \
  "v_mov_b32 v62, 0xffff\n\t" \
  "v_cvt_pk_bf16_f32 v61, v61, v61\n\t" \
  "s_mov_b64 exec, 1\n\t" \
  "v_bfi_b32 v66, v62, v61, v66\n\t" \
  "s_mov_b64 exec, 15\n\t" \
  "v_and_b32 v66, v62, v66\n\t" \
  "s_mov_b64 exec, 16\n\t" \
  "v_bfi_b32 v66, v62, v66, v61\n\t" \
  "s_mov_b64 exec, 0xff\n\t" \
  "v_mov_b32 v67, 0\n\t" \
  "s_mov_b64 exec, 0x100\n\t" \
  "v_mov_b32 v67, v61\n\t" \
  "s_mov_b64 exec, 0xf00\n\t" \
  "v_and_b32 v67, v62, v67\n\t" \
  "s_mov_b64 exec, 0x1000\n\t" \
  "v_bfi_b32 v67, v62, v67, v61\n\t" \
  "s_mov_b64 exec, -1\n\t" \
  "s_waitcnt lgkmcnt(0)\n\t"
#define PP_NB 15
#define PP_Q 1
#define PP_CLOB "v0", "v1", "v2", "v3", "v4", "v5", "v6", "v7", "v8", "v9", "v10", "v11", "v12", "v13", "v14", "v15", "v16", "v17", "v18", "v19", "v20", "v21", "v22", "v23", "v32", "v33", "v34", "v35", "v36", "v37", "v38", "v39", "v40", "v41", "v42", "v43", "v60", "v61", "v62", "v63", "v128", "v129", "v130", "v131", "v132", "v133", "v134", "v135", "v136", "v137", "v138", "v139", "v140", "v141", "v142", "v143", "v144", "v145", "v146", "v147", "v148", "v149", "v150", "v151", "v158", "v161", "v162"

#define LD_ASM1 \
  "s_add_u32 m0, %[wb], 49152\n\t" \
  "s_add_u32 s68, %[rw0], 0\n\t" \
  "buffer_load_dwordx4 v5, %[sw], s68 offen offset:0 lds\n\t" \
  "buffer_load_dwordx4 v6, %[sw], s68 offen offset:1024 lds\n\t" \
  "buffer_load_dwordx4 v7, %[sw], s68 offen offset:2048 lds\n\t" \
  "s_add_u32 m0, %[xb], 36864\n\t" \
  "s_add_u32 s68, %[rx0], 0\n\t" \
  "buffer_load_dwordx4 v0, %[sx], s68 offen offset:0 lds\n\t" \
  "buffer_load_dwordx4 v1, %[sx], s68 offen offset:1024 lds\n\t" \
  "s_mov_b64 exec, 0xffff\n\t" \
  "buffer_load_dwordx4 v4, %[sx], s68 offen offset:2048 lds\n\t" \
  "s_mov_b64 exec, -1\n\t" \
  "s_add_u32 m0, %[xb], 46080\n\t" \
  "s_add_u32 s68, %[rx0], 147456\n\t" \
  "buffer_load_dwordx4 v2, %[sx], s68 offen offset:0 lds\n\t" \
  "buffer_load_dwordx4 v3, %[sx], s68 offen offset:1024 lds\n\t" \
  "s_mov_b64 exec, 0xffff\n\t" \
  "buffer_load_dwordx4 v4, %[sx], s68 offen offset:2048 lds\n\t" \
  "s_mov_b64 exec, -1\n\t" \
  "s_add_u32 m0, %[wb], 61440\n\t" \
  "s_add_u32 s68, %[rw0], 196608\n\t" \
  "buffer_load_dwordx4 v5, %[sw], s68 offen offset:0 lds\n\t" \
  "buffer_load_dwordx4 v6, %[sw], s68 offen offset:1024 lds\n\t" \
  "buffer_load_dwordx4 v7, %[sw], s68 offen offset:2048 lds\n\t"
#define LD_ASM2 \
  "s_mov_b32 s85, 0x180000\n\t" \
  "s_load_dword s84, %[pw], s85\n\t" \
  "s_mov_b32 s85, 0x180800\n\t" \
  "s_load_dword s84, %[pw], s85\n\t" \
  "s_mov_b32 s85, 0x181000\n\t" \
  "s_load_dword s84, %[pw], s85\n\t" \
  "s_mov_b32 s85, 0x1b0000\n\t" \
  "s_load_dword s84, %[pw], s85\n\t" \
  "s_mov_b32 s85, 0x1b0800\n\t" \
  "s_load_dword s84, %[pw], s85\n\t" \
  "s_mov_b32 s85, 0x1b1000\n\t" \
  "s_load_dword s84, %[pw], s85\n\t" \
  "s_mov_b32 s85, 0x1e0000\n\t" \
  "s_load_dword s84, %[pw], s85\n\t" \
  "s_mov_b32 s85, 0x1e0800\n\t" \
  "s_load_dword s84, %[pw], s85\n\t" \
  "s_mov_b32 m0, %[tsk]\n\t" \
  "s_mov_b32 s85, 0x0\n\t" \
  "s_mov_b64 exec, 63\n\t" \
  "buffer_load_dword v16, %[sh], s85 offen lds\n\t" \
  "s_mov_b64 exec, -1\n\t" \
  "s_add_u32 s68, %[rx1], 0\n\t" \
  "s_add_u32 s69, %[rx1], 147456\n\t" \
  "s_add_u32 s70, %[rw1], 0\n\t" \
  "s_add_u32 m0, %[xb], 0\n\t" \
  "s_waitcnt vmcnt(3)\n\t" \
  "s_barrier\n\t" \
  "buffer_load_dwordx4 v0, %[sx], s68 offen offset:0 lds\n\t" \
  "s_mov_b32 s85, 0x1e1000\n\t" \
  "s_load_dword s84, %[pw], s85\n\t" \
  "buffer_load_dwordx4 v1, %[sx], s68 offen offset:1024 lds\n\t" \
  "s_mov_b32 s85, 0x210000\n\t" \
  "s_load_dword s84, %[pw], s85\n\t" \
  "s_mov_b64 exec, 0xffff\n\t" \
  "buffer_load_dwordx4 v4, %[sx], s68 offen offset:2048 lds\n\t" \
  "s_add_u32 m0, %[xb], 9216\n\t" \
  "s_mov_b64 exec, -1\n\t" \
  "buffer_load_dwordx4 v2, %[sx], s69 offen offset:0 lds\n\t" \
  "s_mov_b32 s85, 0x210800\n\t" \
  "s_load_dword s84, %[pw], s85\n\t" \
  "buffer_load_dwordx4 v3, %[sx], s69 offen offset:1024 lds\n\t" \
  "s_mov_b64 exec, 0xffff\n\t" \
  "buffer_load_dwordx4 v4, %[sx], s69 offen offset:2048 lds\n\t" \
  "s_add_u32 m0, %[wb], 0\n\t" \
  "s_mov_b64 exec, -1\n\t" \
  "buffer_load_dwordx4 v5, %[sw], s70 offen offset:0 lds\n\t" \
  "buffer_load_dwordx4 v6, %[sw], s70 offen offset:1024 lds\n\t" \
  "buffer_load_dwordx4 v7, %[sw], s70 offen offset:2048 lds\n\t" \
  "s_mov_b32 m0, %[tsk]\n\t" \
  "s_mov_b32 s85, 0x180000\n\t" \
  "s_mov_b64 exec, 63\n\t" \
  "buffer_load_dword v16, %[sh], s85 offen lds\n\t" \
  "s_mov_b64 exec, -1\n\t" \
  "s_add_u32 s68, %[rw1], 196608\n\t" \
  "s_add_u32 s69, %[rx2], 0\n\t" \
  "s_add_u32 s70, %[rx2], 147456\n\t" \
  "s_add_u32 m0, %[wb], 12288\n\t" \
  "s_waitcnt vmcnt(9)\n\t" \
  "s_barrier\n\t" \
  "buffer_load_dwordx4 v5, %[sw], s68 offen offset:0 lds\n\t" \
  "s_mov_b32 s85, 0x211000\n\t" \
  "s_load_dword s84, %[pw], s85\n\t" \
  "buffer_load_dwordx4 v6, %[sw], s68 offen offset:1024 lds\n\t" \
  "s_mov_b32 s85, 0x240000\n\t" \
  "s_load_dword s84, %[pw], s85\n\t" \
  "buffer_load_dwordx4 v7, %[sw], s68 offen offset:2048 lds\n\t" \
  "s_mov_b32 s85, 0x240800\n\t" \
  "s_load_dword s84, %[pw], s85\n\t" \
  "s_add_u32 m0, %[xb], 18432\n\t" \
  "s_nop 0\n\t" \
  "buffer_load_dwordx4 v0, %[sx], s69 offen offset:0 lds\n\t" \
  "buffer_load_dwordx4 v1, %[sx], s69 offen offset:1024 lds\n\t" \
  "s_mov_b64 exec, 0xffff\n\t" \
  "buffer_load_dwordx4 v4, %[sx], s69 offen offset:2048 lds\n\t" \
  "s_add_u32 m0, %[xb], 27648\n\t" \
  "s_mov_b64 exec, -1\n\t" \
  "buffer_load_dwordx4 v2, %[sx], s70 offen offset:0 lds\n\t" \
  "buffer_load_dwordx4 v3, %[sx], s70 offen offset:1024 lds\n\t" \
  "s_mov_b64 exec, 0xffff\n\t" \
  "buffer_load_dwordx4 v4, %[sx], s70 offen offset:2048 lds\n\t" \
  "s_mov_b64 exec, -1\n\t" \
  "s_add_u32 s68, %[rw2], 0\n\t" \
  "s_add_u32 s69, %[rw2], 196608\n\t" \
  "s_add_u32 s70, %[rx3], 0\n\t" \
  "s_add_u32 m0, %[wb], 24576\n\t" \
  "s_waitcnt vmcnt(9)\n\t" \
  "s_barrier\n\t" \
  "buffer_load_dwordx4 v5, %[sw], s68 offen offset:0 lds\n\t" \
  "s_mov_b32 s85, 0x241000\n\t" \
  "s_load_dword s84, %[pw], s85\n\t" \
  "buffer_load_dwordx4 v6, %[sw], s68 offen offset:1024 lds\n\t" \
  "s_mov_b32 s85, 0x270000\n\t" \
  "s_load_dword s84, %[pw], s85\n\t" \
  "buffer_load_dwordx4 v7, %[sw], s68 offen offset:2048 lds\n\t" \
  "s_mov_b32 s85, 0x270800\n\t" \
  "s_load_dword s84, %[pw], s85\n\t" \
  "s_add_u32 m0, %[wb], 36864\n\t" \
  "s_nop 0\n\t" \
  "buffer_load_dwordx4 v5, %[sw], s69 offen offset:0 lds\n\t" \
  "buffer_load_dwordx4 v6, %[sw], s69 offen offset:1024 lds\n\t" \
  "buffer_load_dwordx4 v7, %[sw], s69 offen offset:2048 lds\n\t" \
  "s_add_u32 m0, %[xb], 36864\n\t" \
  "s_nop 0\n\t" \
  "buffer_load_dwordx4 v0, %[sx], s70 offen offset:0 lds\n\t" \
  "buffer_load_dwordx4 v1, %[sx], s70 offen offset:1024 lds\n\t" \
  "s_mov_b64 exec, 0xffff\n\t" \
  "buffer_load_dwordx4 v4, %[sx], s70 offen offset:2048 lds\n\t" \
  "s_mov_b64 exec, -1\n\t" \
  "s_add_u32 s68, %[rw3], 0\n\t" \
  "s_add_u32 s69, %[rx3], 147456\n\t" \
  "s_add_u32 s70, %[rw3], 196608\n\t" \
  "s_add_u32 m0, %[wb], 49152\n\t" \
  "s_waitcnt vmcnt(15)\n\t" \
  "s_barrier\n\t" \
  "buffer_load_dwordx4 v5, %[sw], s68 offen offset:0 lds\n\t" \
  "s_mov_b32 s85, 0x271000\n\t" \
  "s_load_dword s84, %[pw], s85\n\t" \
  "buffer_load_dwordx4 v6, %[sw], s68 offen offset:1024 lds\n\t" \
  "s_mov_b32 s85, 0x2a0000\n\t" \
  "s_load_dword s84, %[pw], s85\n\t" \
  "buffer_load_dwordx4 v7, %[sw], s68 offen offset:2048 lds\n\t" \
  "s_mov_b32 s85, 0x2a0800\n\t" \
  "s_load_dword s84, %[pw], s85\n\t" \
  "s_add_u32 m0, %[xb], 46080\n\t" \
  "s_nop 0\n\t" \
  "buffer_load_dwordx4 v2, %[sx], s69 offen offset:0 lds\n\t" \
  "buffer_load_dwordx4 v3, %[sx], s69 offen offset:1024 lds\n\t" \
  "s_mov_b64 exec, 0xffff\n\t" \
  "buffer_load_dwordx4 v4, %[sx], s69 offen offset:2048 lds\n\t" \
  "s_add_u32 m0, %[wb], 61440\n\t" \
  "s_mov_b64 exec, -1\n\t" \
  "buffer_load_dwordx4 v5, %[sw], s70 offen offset:0 lds\n\t" \
  "buffer_load_dwordx4 v6, %[sw], s70 offen offset:1024 lds\n\t" \
  "buffer_load_dwordx4 v7, %[sw], s70 offen offset:2048 lds\n\t" \
  "s_add_u32 s68, %[rx0], 512\n\t" \
  "s_add_u32 s69, %[rw0], 1572864\n\t" \
  "s_add_u32 s70, %[rx0], 147968\n\t" \
  "s_add_u32 m0, %[xb], 0\n\t" \
  "s_waitcnt vmcnt(15)\n\t" \
  "s_barrier\n\t" \
  "buffer_load_dwordx4 v0, %[sx], s68 offen offset:0 lds\n\t" \
  "s_mov_b32 s85, 0x2a1000\n\t" \
  "s_load_dword s84, %[pw], s85\n\t" \
  "buffer_load_dwordx4 v1, %[sx], s68 offen offset:1024 lds\n\t" \
  "s_mov_b32 s85, 0x2d0000\n\t" \
  "s_load_dword s84, %[pw], s85\n\t" \
  "s_mov_b64 exec, 0xffff\n\t" \
  "buffer_load_dwordx4 v4, %[sx], s68 offen offset:2048 lds\n\t" \
  "s_add_u32 m0, %[wb], 0\n\t" \
  "s_mov_b64 exec, -1\n\t" \
  "buffer_load_dwordx4 v5, %[sw], s69 offen offset:0 lds\n\t" \
  "s_mov_b32 s85, 0x2d0800\n\t" \
  "s_load_dword s84, %[pw], s85\n\t" \
  "buffer_load_dwordx4 v6, %[sw], s69 offen offset:1024 lds\n\t" \
  "buffer_load_dwordx4 v7, %[sw], s69 offen offset:2048 lds\n\t" \
  "s_add_u32 m0, %[xb], 9216\n\t" \
  "s_nop 0\n\t" \
  "buffer_load_dwordx4 v2, %[sx], s70 offen offset:0 lds\n\t" \
  "buffer_load_dwordx4 v3, %[sx], s70 offen offset:1024 lds\n\t" \
  "s_mov_b64 exec, 0xffff\n\t" \
  "buffer_load_dwordx4 v4, %[sx], s70 offen offset:2048 lds\n\t" \
  "s_mov_b64 exec, -1\n\t" \
  "s_add_u32 s68, %[rw0], 1769472\n\t" \
  "s_add_u32 m0, %[wb], 12288\n\t" \
  "s_waitcnt vmcnt(21)\n\t" \
  "s_barrier\n\t" \
  "buffer_load_dwordx4 v5, %[sw], s68 offen offset:0 lds\n\t" \
  "s_mov_b32 s85, 0x2d1000\n\t" \
  "s_load_dword s84, %[pw], s85\n\t" \
  "buffer_load_dwordx4 v6, %[sw], s68 offen offset:1024 lds\n\t" \
  "s_mov_b32 s85, 0x300000\n\t" \
  "s_load_dword s84, %[pw], s85\n\t" \
  "buffer_load_dwordx4 v7, %[sw], s68 offen offset:2048 lds\n\t" \
  "s_mov_b32 s85, 0x300800\n\t" \
  "s_load_dword s84, %[pw], s85\n\t" \
  "s_add_u32 s68, %[rx1], 512\n\t" \
  "s_add_u32 s69, %[rw1], 1572864\n\t" \
  "s_add_u32 s70, %[rx1], 147968\n\t" \
  "s_add_u32 m0, %[xb], 18432\n\t" \
  "s_waitcnt vmcnt(15)\n\t" \
  "s_barrier\n\t" \
  "buffer_load_dwordx4 v0, %[sx], s68 offen offset:0 lds\n\t" \
  "s_mov_b32 s85, 0x301000\n\t" \
  "s_load_dword s84, %[pw], s85\n\t" \
  "buffer_load_dwordx4 v1, %[sx], s68 offen offset:1024 lds\n\t" \
  "s_mov_b32 s85, 0x330000\n\t" \
  "s_load_dword s84, %[pw], s85\n\t" \
  "s_mov_b64 exec, 0xffff\n\t" \
  "buffer_load_dwordx4 v4, %[sx], s68 offen offset:2048 lds\n\t" \
  "s_add_u32 m0, %[wb], 24576\n\t" \
  "s_mov_b64 exec, -1\n\t" \
  "buffer_load_dwordx4 v5, %[sw], s69 offen offset:0 lds\n\t" \
  "s_mov_b32 s85, 0x330800\n\t" \
  "s_load_dword s84, %[pw], s85\n\t" \
  "buffer_load_dwordx4 v6, %[sw], s69 offen offset:1024 lds\n\t" \
  "buffer_load_dwordx4 v7, %[sw], s69 offen offset:2048 lds\n\t" \
  "s_add_u32 m0, %[xb], 27648\n\t" \
  "s_nop 0\n\t" \
  "buffer_load_dwordx4 v2, %[sx], s70 offen offset:0 lds\n\t" \
  "buffer_load_dwordx4 v3, %[sx], s70 offen offset:1024 lds\n\t" \
  "s_mov_b64 exec, 0xffff\n\t" \
  "buffer_load_dwordx4 v4, %[sx], s70 offen offset:2048 lds\n\t" \
  "s_mov_b64 exec, -1\n\t" \
  "s_add_u32 s68, %[rw1], 1769472\n\t" \
  "s_add_u32 m0, %[wb], 36864\n\t" \
  "s_waitcnt vmcnt(21)\n\t" \
  "s_barrier\n\t" \
  "buffer_load_dwordx4 v5, %[sw], s68 offen offset:0 lds\n\t" \
  "s_mov_b32 s85, 0x331000\n\t" \
  "s_load_dword s84, %[pw], s85\n\t" \
  "buffer_load_dwordx4 v6, %[sw], s68 offen offset:1024 lds\n\t" \
  "s_mov_b32 s85, 0x360000\n\t" \
  "s_load_dword s84, %[pw], s85\n\t" \
  "buffer_load_dwordx4 v7, %[sw], s68 offen offset:2048 lds\n\t" \
  "s_mov_b32 s85, 0x360800\n\t" \
  "s_load_dword s84, %[pw], s85\n\t" \
  "s_add_u32 s68, %[rx2], 512\n\t" \
  "s_add_u32 s69, %[rw2], 1572864\n\t" \
  "s_add_u32 m0, %[xb], 36864\n\t" \
  "s_waitcnt vmcnt(15)\n\t" \
  "s_barrier\n\t" \
  "buffer_load_dwordx4 v0, %[sx], s68 offen offset:0 lds\n\t" \
  "s_mov_b32 s85, 0x361000\n\t" \
  "s_load_dword s84, %[pw], s85\n\t" \
  "buffer_load_dwordx4 v1, %[sx], s68 offen offset:1024 lds\n\t" \
  "s_mov_b32 s85, 0x390000\n\t" \
  "s_load_dword s84, %[pw], s85\n\t" \
  "s_mov_b64 exec, 0xffff\n\t" \
  "buffer_load_dwordx4 v4, %[sx], s68 offen offset:2048 lds\n\t" \
  "s_add_u32 m0, %[wb], 49152\n\t" \
  "s_mov_b64 exec, -1\n\t" \
  "buffer_load_dwordx4 v5, %[sw], s69 offen offset:0 lds\n\t" \
  "s_mov_b32 s85, 0x390800\n\t" \
  "s_load_dword s84, %[pw], s85\n\t" \
  "buffer_load_dwordx4 v6, %[sw], s69 offen offset:1024 lds\n\t" \
  "buffer_load_dwordx4 v7, %[sw], s69 offen offset:2048 lds\n\t" \
  "s_mov_b32 m0, %[tsk]\n\t" \
  "s_mov_b32 s85, 0x300000\n\t" \
  "s_mov_b64 exec, 63\n\t" \
  "buffer_load_dword v16, %[sh], s85 offen lds\n\t" \
  "s_mov_b64 exec, -1\n\t" \
  "s_add_u32 s68, %[rx2], 147968\n\t" \
  "s_add_u32 s69, %[rw2], 1769472\n\t" \
  "s_add_u32 m0, %[xb], 46080\n\t" \
  "s_waitcnt vmcnt(18)\n\t" \
  "s_barrier\n\t" \
  "buffer_load_dwordx4 v2, %[sx], s68 offen offset:0 lds\n\t" \
  "s_mov_b32 s85, 0x391000\n\t" \
  "s_load_dword s84, %[pw], s85\n\t" \
  "buffer_load_dwordx4 v3, %[sx], s68 offen offset:1024 lds\n\t" \
  "s_mov_b32 s85, 0x3c0000\n\t" \
  "s_load_dword s84, %[pw], s85\n\t" \
  "s_mov_b64 exec, 0xffff\n\t" \
  "buffer_load_dwordx4 v4, %[sx], s68 offen offset:2048 lds\n\t" \
  "s_add_u32 m0, %[wb], 61440\n\t" \
  "s_mov_b64 exec, -1\n\t" \
  "buffer_load_dwordx4 v5, %[sw], s69 offen offset:0 lds\n\t" \
  "s_mov_b32 s85, 0x3c0800\n\t" \
  "s_load_dword s84, %[pw], s85\n\t" \
  "buffer_load_dwordx4 v6, %[sw], s69 offen offset:1024 lds\n\t" \
  "buffer_load_dwordx4 v7, %[sw], s69 offen offset:2048 lds\n\t" \
  "s_add_u32 s68, %[rx3], 512\n\t" \
  "s_add_u32 s69, %[rw3], 1572864\n\t" \
  "s_add_u32 m0, %[xb], 0\n\t" \
  "s_waitcnt vmcnt(15)\n\t" \
  "s_barrier\n\t" \
  "buffer_load_dwordx4 v0, %[sx], s68 offen offset:0 lds\n\t" \
  "s_mov_b32 s85, 0x3c1000\n\t" \
  "s_load_dword s84, %[pw], s85\n\t" \
  "buffer_load_dwordx4 v1, %[sx], s68 offen offset:1024 lds\n\t" \
  "s_mov_b32 s85, 0x3f0000\n\t" \
  "s_load_dword s84, %[pw], s85\n\t" \
  "s_mov_b64 exec, 0xffff\n\t" \
  "buffer_load_dwordx4 v4, %[sx], s68 offen offset:2048 lds\n\t" \
  "s_add_u32 m0, %[wb], 0\n\t" \
  "s_mov_b64 exec, -1\n\t" \
  "buffer_load_dwordx4 v5, %[sw], s69 offen offset:0 lds\n\t" \
  "s_mov_b32 s85, 0x3f0800\n\t" \
  "s_load_dword s84, %[pw], s85\n\t" \
  "buffer_load_dwordx4 v6, %[sw], s69 offen offset:1024 lds\n\t" \
  "buffer_load_dwordx4 v7, %[sw], s69 offen offset:2048 lds\n\t" \
  "s_add_u32 s68, %[rx3], 147968\n\t" \
  "s_add_u32 s69, %[rw3], 1769472\n\t" \
  "s_add_u32 m0, %[xb], 9216\n\t" \
  "s_waitcnt vmcnt(18)\n\t" \
  "s_barrier\n\t" \
  "buffer_load_dwordx4 v2, %[sx], s68 offen offset:0 lds\n\t" \
  "s_mov_b32 s85, 0x3f1000\n\t" \
  "s_load_dword s84, %[pw], s85\n\t" \
  "buffer_load_dwordx4 v3, %[sx], s68 offen offset:1024 lds\n\t" \
  "s_mov_b32 s85, 0x420000\n\t" \
  "s_load_dword s84, %[pw], s85\n\t" \
  "s_mov_b64 exec, 0xffff\n\t" \
  "buffer_load_dwordx4 v4, %[sx], s68 offen offset:2048 lds\n\t" \
  "s_add_u32 m0, %[wb], 12288\n\t" \
  "s_mov_b64 exec, -1\n\t" \
  "buffer_load_dwordx4 v5, %[sw], s69 offen offset:0 lds\n\t" \
  "s_mov_b32 s85, 0x420800\n\t" \
  "s_load_dword s84, %[pw], s85\n\t" \
  "buffer_load_dwordx4 v6, %[sw], s69 offen offset:1024 lds\n\t" \
  "buffer_load_dwordx4 v7, %[sw], s69 offen offset:2048 lds\n\t" \
  "s_add_u32 s68, %[rx0], 1024\n\t" \
  "s_add_u32 s69, %[rw0], 3145728\n\t" \
  "s_add_u32 m0, %[xb], 18432\n\t" \
  "s_waitcnt vmcnt(15)\n\t" \
  "s_barrier\n\t" \
  "buffer_load_dwordx4 v0, %[sx], s68 offen offset:0 lds\n\t" \
  "s_mov_b32 s85, 0x421000\n\t" \
  "s_load_dword s84, %[pw], s85\n\t" \
  "buffer_load_dwordx4 v1, %[sx], s68 offen offset:1024 lds\n\t" \
  "s_mov_b32 s85, 0x450000\n\t" \
  "s_load_dword s84, %[pw], s85\n\t" \
  "s_mov_b64 exec, 0xffff\n\t" \
  "buffer_load_dwordx4 v4, %[sx], s68 offen offset:2048 lds\n\t" \
  "s_add_u32 m0, %[wb], 24576\n\t" \
  "s_mov_b64 exec, -1\n\t" \
  "buffer_load_dwordx4 v5, %[sw], s69 offen offset:0 lds\n\t" \
  "s_mov_b32 s85, 0x450800\n\t" \
  "s_load_dword s84, %[pw], s85\n\t" \
  "buffer_load_dwordx4 v6, %[sw], s69 offen offset:1024 lds\n\t" \
  "buffer_load_dwordx4 v7, %[sw], s69 offen offset:2048 lds\n\t" \
  "s_add_u32 s68, %[rx0], 148480\n\t" \
  "s_add_u32 s69, %[rw0], 3342336\n\t" \
  "s_add_u32 m0, %[xb], 27648\n\t" \
  "s_waitcnt vmcnt(18)\n\t" \
  "s_barrier\n\t" \
  "buffer_load_dwordx4 v2, %[sx], s68 offen offset:0 lds\n\t" \
  "s_mov_b32 s85, 0x451000\n\t" \
  "s_load_dword s84, %[pw], s85\n\t" \
  "buffer_load_dwordx4 v3, %[sx], s68 offen offset:1024 lds\n\t" \
  "s_mov_b32 s85, 0x480000\n\t" \
  "s_load_dword s84, %[pw], s85\n\t" \
  "s_mov_b64 exec, 0xffff\n\t" \
  "buffer_load_dwordx4 v4, %[sx], s68 offen offset:2048 lds\n\t" \
  "s_add_u32 m0, %[wb], 36864\n\t" \
  "s_mov_b64 exec, -1\n\t" \
  "buffer_load_dwordx4 v5, %[sw], s69 offen offset:0 lds\n\t" \
  "s_mov_b32 s85, 0x480800\n\t" \
  "s_load_dword s84, %[pw], s85\n\t" \
  "buffer_load_dwordx4 v6, %[sw], s69 offen offset:1024 lds\n\t" \
  "buffer_load_dwordx4 v7, %[sw], s69 offen offset:2048 lds\n\t" \
  "s_add_u32 s68, %[rx1], 1024\n\t" \
  "s_add_u32 s69, %[rw1], 3145728\n\t" \
  "s_add_u32 m0, %[xb], 36864\n\t" \
  "s_waitcnt vmcnt(15)\n\t" \
  "s_barrier\n\t" \
  "buffer_load_dwordx4 v0, %[sx], s68 offen offset:0 lds\n\t" \
  "s_mov_b32 s85, 0x481000\n\t" \
  "s_load_dword s84, %[pw], s85\n\t" \
  "buffer_load_dwordx4 v1, %[sx], s68 offen offset:1024 lds\n\t" \
  "s_mov_b32 s85, 0x4b0000\n\t" \
  "s_load_dword s84, %[pw], s85\n\t" \
  "s_mov_b64 exec, 0xffff\n\t" \
  "buffer_load_dwordx4 v4, %[sx], s68 offen offset:2048 lds\n\t" \
  "s_add_u32 m0, %[wb], 49152\n\t" \
  "s_mov_b64 exec, -1\n\t" \
  "buffer_load_dwordx4 v5, %[sw], s69 offen offset:0 lds\n\t" \
  "s_mov_b32 s85, 0x4b0800\n\t" \
  "s_load_dword s84, %[pw], s85\n\t" \
  "buffer_load_dwordx4 v6, %[sw], s69 offen offset:1024 lds\n\t" \
  "buffer_load_dwordx4 v7, %[sw], s69 offen offset:2048 lds\n\t" \
  "s_add_u32 s68, %[rx1], 148480\n\t" \
  "s_add_u32 s69, %[rw1], 3342336\n\t" \
  "s_add_u32 m0, %[xb], 46080\n\t" \
  "s_waitcnt vmcnt(18)\n\t" \
  "s_barrier\n\t" \
  "buffer_load_dwordx4 v2, %[sx], s68 offen offset:0 lds\n\t" \
  "s_mov_b32 s85, 0x4b1000\n\t" \
  "s_load_dword s84, %[pw], s85\n\t" \
  "buffer_load_dwordx4 v3, %[sx], s68 offen offset:1024 lds\n\t" \
  "s_mov_b32 s85, 0x4e0000\n\t" \
  "s_load_dword s84, %[pw], s85\n\t" \
  "s_mov_b64 exec, 0xffff\n\t" \
  "buffer_load_dwordx4 v4, %[sx], s68 offen offset:2048 lds\n\t" \
  "s_add_u32 m0, %[wb], 61440\n\t" \
  "s_mov_b64 exec, -1\n\t" \
  "buffer_load_dwordx4 v5, %[sw], s69 offen offset:0 lds\n\t" \
  "s_mov_b32 s85, 0x4e0800\n\t" \
  "s_load_dword s84, %[pw], s85\n\t" \
  "buffer_load_dwordx4 v6, %[sw], s69 offen offset:1024 lds\n\t" \
  "buffer_load_dwordx4 v7, %[sw], s69 offen offset:2048 lds\n\t" \
  "s_add_u32 s68, %[rx2], 1024\n\t" \
  "s_add_u32 s69, %[rw2], 3145728\n\t" \
  "s_add_u32 m0, %[xb], 0\n\t" \
  "s_waitcnt vmcnt(15)\n\t" \
  "s_barrier\n\t" \
  "buffer_load_dwordx4 v0, %[sx], s68 offen offset:0 lds\n\t" \
  "s_mov_b32 s85, 0x4e1000\n\t" \
  "s_load_dword s84, %[pw], s85\n\t" \
  "buffer_load_dwordx4 v1, %[sx], s68 offen offset:1024 lds\n\t" \
  "s_mov_b32 s85, 0x510000\n\t" \
  "s_load_dword s84, %[pw], s85\n\t" \
  "s_mov_b64 exec, 0xffff\n\t" \
  "buffer_load_dwordx4 v4, %[sx], s68 offen offset:2048 lds\n\t" \
  "s_add_u32 m0, %[wb], 0\n\t" \
  "s_mov_b64 exec, -1\n\t" \
  "buffer_load_dwordx4 v5, %[sw], s69 offen offset:0 lds\n\t" \
  "s_mov_b32 s85, 0x510800\n\t" \
  "s_load_dword s84, %[pw], s85\n\t" \
  "buffer_load_dwordx4 v6, %[sw], s69 offen offset:1024 lds\n\t" \
  "buffer_load_dwordx4 v7, %[sw], s69 offen offset:2048 lds\n\t" \
  "s_mov_b32 m0, %[tsk]\n\t" \
  "s_mov_b32 s85, 0x480000\n\t" \
  "s_mov_b64 exec, 63\n\t" \
  "buffer_load_dword v16, %[sh], s85 offen lds\n\t" \
  "s_mov_b64 exec, -1\n\t" \
  "s_add_u32 s68, %[rx2], 148480\n\t" \
  "s_add_u32 s69, %[rw2], 3342336\n\t" \
  "s_add_u32 m0, %[xb], 9216\n\t" \
  "s_waitcnt vmcnt(18)\n\t" \
  "s_barrier\n\t" \
  "buffer_load_dwordx4 v2, %[sx], s68 offen offset:0 lds\n\t" \
  "s_mov_b32 s85, 0x511000\n\t" \
  "s_load_dword s84, %[pw], s85\n\t" \
  "buffer_load_dwordx4 v3, %[sx], s68 offen offset:1024 lds\n\t" \
  "s_mov_b32 s85, 0x540000\n\t" \
  "s_load_dword s84, %[pw], s85\n\t" \
  "s_mov_b64 exec, 0xffff\n\t" \
  "buffer_load_dwordx4 v4, %[sx], s68 offen offset:2048 lds\n\t" \
  "s_add_u32 m0, %[wb], 12288\n\t" \
  "s_mov_b64 exec, -1\n\t" \
  "buffer_load_dwordx4 v5, %[sw], s69 offen offset:0 lds\n\t" \
  "s_mov_b32 s85, 0x540800\n\t" \
  "s_load_dword s84, %[pw], s85\n\t" \
  "buffer_load_dwordx4 v6, %[sw], s69 offen offset:1024 lds\n\t" \
  "buffer_load_dwordx4 v7, %[sw], s69 offen offset:2048 lds\n\t" \
  "s_mov_b32 m0, %[tsk]\n\t" \
  "s_mov_b64 exec, %[tm]\n\t" \
  "global_load_lds_dword v12, %[tp]\n\t" \
  "s_mov_b64 exec, -1\n\t" \
  "s_add_u32 s68, %[rx3], 1024\n\t" \
  "s_add_u32 s69, %[rw3], 3145728\n\t" \
  "s_add_u32 m0, %[xb], 18432\n\t" \
  "s_waitcnt vmcnt(15)\n\t" \
  "s_barrier\n\t" \
  "buffer_load_dwordx4 v0, %[sx], s68 offen offset:0 lds\n\t" \
  "s_mov_b32 s85, 0x541000\n\t" \
  "s_load_dword s84, %[pw], s85\n\t" \
  "buffer_load_dwordx4 v1, %[sx], s68 offen offset:1024 lds\n\t" \
  "s_mov_b32 s85, 0x570000\n\t" \
  "s_load_dword s84, %[pw], s85\n\t" \
  "s_mov_b64 exec, 0xffff\n\t" \
  "buffer_load_dwordx4 v4, %[sx], s68 offen offset:2048 lds\n\t" \
  "s_add_u32 m0, %[wb], 24576\n\t" \
  "s_mov_b64 exec, -1\n\t" \
  "buffer_load_dwordx4 v5, %[sw], s69 offen offset:0 lds\n\t" \
  "s_mov_b32 s85, 0x570800\n\t" \
  "s_load_dword s84, %[pw], s85\n\t" \
  "buffer_load_dwordx4 v6, %[sw], s69 offen offset:1024 lds\n\t" \
  "buffer_load_dwordx4 v7, %[sw], s69 offen offset:2048 lds\n\t" \
  "s_add_u32 s68, %[rx3], 148480\n\t" \
  "s_add_u32 s69, %[rw3], 3342336\n\t" \
  "s_add_u32 m0, %[xb], 27648\n\t" \
  "s_waitcnt vmcnt(18)\n\t" \
  "s_barrier\n\t" \
  "buffer_load_dwordx4 v2, %[sx], s68 offen offset:0 lds\n\t" \
  "s_mov_b32 s85, 0x571000\n\t" \
  "s_load_dword s84, %[pw], s85\n\t" \
  "buffer_load_dwordx4 v3, %[sx], s68 offen offset:1024 lds\n\t" \
  "s_mov_b32 s85, 0x5a0000\n\t" \
  "s_load_dword s84, %[pw], s85\n\t" \
  "s_mov_b64 exec, 0xffff\n\t" \
  "buffer_load_dwordx4 v4, %[sx], s68 offen offset:2048 lds\n\t" \
  "s_add_u32 m0, %[wb], 36864\n\t" \
  "s_mov_b64 exec, -1\n\t" \
  "buffer_load_dwordx4 v5, %[sw], s69 offen offset:0 lds\n\t" \
  "s_mov_b32 s85, 0x5a0800\n\t" \
  "s_load_dword s84, %[pw], s85\n\t" \
  "buffer_load_dwordx4 v6, %[sw], s69 offen offset:1024 lds\n\t" \
  "buffer_load_dwordx4 v7, %[sw], s69 offen offset:2048 lds\n\t" \
  "s_add_u32 s68, %[rx0], 1536\n\t" \
  "s_add_u32 s69, %[rw0], 4718592\n\t" \
  "s_add_u32 m0, %[xb], 36864\n\t" \
  "s_waitcnt vmcnt(15)\n\t" \
  "s_barrier\n\t" \
  "buffer_load_dwordx4 v0, %[sx], s68 offen offset:0 lds\n\t" \
  "s_mov_b32 s85, 0x5a1000\n\t" \
  "s_load_dword s84, %[pw], s85\n\t" \
  "buffer_load_dwordx4 v1, %[sx], s68 offen offset:1024 lds\n\t" \
  "s_mov_b32 s85, 0x5d0000\n\t" \
  "s_load_dword s84, %[pw], s85\n\t" \
  "s_mov_b64 exec, 0xffff\n\t" \
  "buffer_load_dwordx4 v4, %[sx], s68 offen offset:2048 lds\n\t" \
  "s_add_u32 m0, %[wb], 49152\n\t" \
  "s_mov_b64 exec, -1\n\t" \
  "buffer_load_dwordx4 v5, %[sw], s69 offen offset:0 lds\n\t" \
  "s_mov_b32 s85, 0x5d0800\n\t" \
  "s_load_dword s84, %[pw], s85\n\t" \
  "buffer_load_dwordx4 v6, %[sw], s69 offen offset:1024 lds\n\t" \
  "buffer_load_dwordx4 v7, %[sw], s69 offen offset:2048 lds\n\t" \
  "s_add_u32 s68, %[rx0], 148992\n\t" \
  "s_add_u32 s69, %[rw0], 4915200\n\t" \
  "s_add_u32 m0, %[xb], 46080\n\t" \
  "s_waitcnt vmcnt(18)\n\t" \
  "s_barrier\n\t" \
  "buffer_load_dwordx4 v2, %[sx], s68 offen offset:0 lds\n\t" \
  "s_mov_b32 s85, 0x5d1000\n\t" \
  "s_load_dword s84, %[pw], s85\n\t" \
  "buffer_load_dwordx4 v3, %[sx], s68 offen offset:1024 lds\n\t" \
  "s_load_dword s84, %[po], 0x0\n\t" \
  "s_mov_b64 exec, 0xffff\n\t" \
  "buffer_load_dwordx4 v4, %[sx], s68 offen offset:2048 lds\n\t" \
  "s_add_u32 m0, %[wb], 61440\n\t" \
  "s_mov_b64 exec, -1\n\t" \
  "buffer_load_dwordx4 v5, %[sw], s69 offen offset:0 lds\n\t" \
  "s_load_dword s84, %[po], 0x40\n\t" \
  "buffer_load_dwordx4 v6, %[sw], s69 offen offset:1024 lds\n\t" \
  "buffer_load_dwordx4 v7, %[sw], s69 offen offset:2048 lds\n\t" \
  "s_add_u32 s68, %[rx1], 1536\n\t" \
  "s_add_u32 s69, %[rw1], 4718592\n\t" \
  "s_add_u32 m0, %[xb], 0\n\t" \
  "s_waitcnt vmcnt(15)\n\t" \
  "s_barrier\n\t" \
  "buffer_load_dwordx4 v0, %[sx], s68 offen offset:0 lds\n\t" \
  "s_load_dword s84, %[po], 0x80\n\t" \
  "buffer_load_dwordx4 v1, %[sx], s68 offen offset:1024 lds\n\t" \
  "s_load_dword s84, %[po], 0xc0\n\t" \
  "s_mov_b64 exec, 0xffff\n\t" \
  "buffer_load_dwordx4 v4, %[sx], s68 offen offset:2048 lds\n\t" \
  "s_add_u32 m0, %[wb], 0\n\t" \
  "s_mov_b64 exec, -1\n\t" \
  "buffer_load_dwordx4 v5, %[sw], s69 offen offset:0 lds\n\t" \
  "s_load_dword s84, %[po], 0x100\n\t" \
  "buffer_load_dwordx4 v6, %[sw], s69 offen offset:1024 lds\n\t" \
  "buffer_load_dwordx4 v7, %[sw], s69 offen offset:2048 lds\n\t" \
  "s_add_u32 s68, %[rx1], 148992\n\t" \
  "s_add_u32 s69, %[rw1], 4915200\n\t" \
  "s_add_u32 m0, %[xb], 9216\n\t" \
  "s_waitcnt vmcnt(18)\n\t" \
  "s_barrier\n\t" \
  "buffer_load_dwordx4 v2, %[sx], s68 offen offset:0 lds\n\t" \
  "s_load_dword s84, %[po], 0x140\n\t" \
  "buffer_load_dwordx4 v3, %[sx], s68 offen offset:1024 lds\n\t" \
  "s_load_dword s84, %[po], 0x180\n\t" \
  "s_mov_b64 exec, 0xffff\n\t" \
  "buffer_load_dwordx4 v4, %[sx], s68 offen offset:2048 lds\n\t" \
  "s_add_u32 m0, %[wb], 12288\n\t" \
  "s_mov_b64 exec, -1\n\t" \
  "buffer_load_dwordx4 v5, %[sw], s69 offen offset:0 lds\n\t" \
  "s_load_dword s84, %[po], 0x1c0\n\t" \
  "buffer_load_dwordx4 v6, %[sw], s69 offen offset:1024 lds\n\t" \
  "buffer_load_dwordx4 v7, %[sw], s69 offen offset:2048 lds\n\t" \
  "s_add_u32 s68, %[rx2], 1536\n\t" \
  "s_add_u32 s69, %[rw2], 4718592\n\t" \
  "s_add_u32 m0, %[xb], 18432\n\t" \
  "s_waitcnt vmcnt(15)\n\t" \
  "s_barrier\n\t" \
  "buffer_load_dwordx4 v0, %[sx], s68 offen offset:0 lds\n\t" \
  "s_load_dword s84, %[po], 0x200\n\t" \
  "buffer_load_dwordx4 v1, %[sx], s68 offen offset:1024 lds\n\t" \
  "s_load_dword s84, %[po], 0x240\n\t" \
  "s_mov_b64 exec, 0xffff\n\t" \
  "buffer_load_dwordx4 v4, %[sx], s68 offen offset:2048 lds\n\t" \
  "s_add_u32 m0, %[wb], 24576\n\t" \
  "s_mov_b64 exec, -1\n\t" \
  "buffer_load_dwordx4 v5, %[sw], s69 offen offset:0 lds\n\t" \
  "s_load_dword s84, %[po], 0x280\n\t" \
  "buffer_load_dwordx4 v6, %[sw], s69 offen offset:1024 lds\n\t" \
  "buffer_load_dwordx4 v7, %[sw], s69 offen offset:2048 lds\n\t" \
  "s_add_u32 s68, %[rx2], 148992\n\t" \
  "s_add_u32 s69, %[rw2], 4915200\n\t" \
  "s_add_u32 m0, %[xb], 27648\n\t" \
  "s_waitcnt vmcnt(18)\n\t" \
  "s_barrier\n\t" \
  "buffer_load_dwordx4 v2, %[sx], s68 offen offset:0 lds\n\t" \
  "s_load_dword s84, %[po], 0x2c0\n\t" \
  "buffer_load_dwordx4 v3, %[sx], s68 offen offset:1024 lds\n\t" \
  "s_load_dword s84, %[po], 0x300\n\t" \
  "s_mov_b64 exec, 0xffff\n\t" \
  "buffer_load_dwordx4 v4, %[sx], s68 offen offset:2048 lds\n\t" \
  "s_add_u32 m0, %[wb], 36864\n\t" \
  "s_mov_b64 exec, -1\n\t" \
  "buffer_load_dwordx4 v5, %[sw], s69 offen offset:0 lds\n\t" \
  "s_load_dword s84, %[po], 0x340\n\t" \
  "buffer_load_dwordx4 v6, %[sw], s69 offen offset:1024 lds\n\t" \
  "buffer_load_dwordx4 v7, %[sw], s69 offen offset:2048 lds\n\t" \
  "s_add_u32 s68, %[rx3], 1536\n\t" \
  "s_add_u32 s69, %[rw3], 4718592\n\t" \
  "s_add_u32 m0, %[xb], 36864\n\t" \
  "s_waitcnt vmcnt(15)\n\t" \
  "s_barrier\n\t" \
  "buffer_load_dwordx4 v0, %[sx], s68 offen offset:0 lds\n\t" \
  "s_load_dword s84, %[po], 0x380\n\t" \
  "buffer_load_dwordx4 v1, %[sx], s68 offen offset:1024 lds\n\t" \
  "s_load_dword s84, %[po], 0x3c0\n\t" \
  "s_mov_b64 exec, 0xffff\n\t" \
  "buffer_load_dwordx4 v4, %[sx], s68 offen offset:2048 lds\n\t" \
  "s_add_u32 m0, %[wb], 49152\n\t" \
  "s_mov_b64 exec, -1\n\t" \
  "buffer_load_dwordx4 v5, %[sw], s69 offen offset:0 lds\n\t" \
  "s_load_dword s84, %[po], 0x400\n\t" \
  "buffer_load_dwordx4 v6, %[sw], s69 offen offset:1024 lds\n\t" \
  "buffer_load_dwordx4 v7, %[sw], s69 offen offset:2048 lds\n\t" \
  "s_add_u32 s68, %[rx3], 148992\n\t" \
  "s_add_u32 s69, %[rw3], 4915200\n\t" \
  "s_add_u32 m0, %[xb], 46080\n\t" \
  "s_waitcnt vmcnt(18)\n\t" \
  "s_barrier\n\t" \
  "buffer_load_dwordx4 v2, %[sx], s68 offen offset:0 lds\n\t" \
  "s_load_dword s84, %[po], 0x440\n\t" \
  "buffer_load_dwordx4 v3, %[sx], s68 offen offset:1024 lds\n\t" \
  "s_load_dword s84, %[po], 0x480\n\t" \
  "s_mov_b64 exec, 0xffff\n\t" \
  "buffer_load_dwordx4 v4, %[sx], s68 offen offset:2048 lds\n\t" \
  "s_add_u32 m0, %[wb], 61440\n\t" \
  "s_mov_b64 exec, -1\n\t" \
  "buffer_load_dwordx4 v5, %[sw], s69 offen offset:0 lds\n\t" \
  "s_load_dword s84, %[po], 0x4c0\n\t" \
  "buffer_load_dwordx4 v6, %[sw], s69 offen offset:1024 lds\n\t" \
  "buffer_load_dwordx4 v7, %[sw], s69 offen offset:2048 lds\n\t" \
  "v_readlane_b32 s68, v9, 0\n\t" \
  "v_readlane_b32 s69, v9, 1\n\t" \
  "v_readlane_b32 s70, v9, 2\n\t" \
  "v_readlane_b32 s71, v9, 18\n\t" \
  "v_readlane_b32 s72, v9, 19\n\t" \
  "s_add_u32 m0, %[ob], 0\n\t" \
  "s_waitcnt vmcnt(15)\n\t" \
  "s_barrier\n\t" \
  "s_nop 2\n\t" \
  "buffer_load_dwordx4 v8, %[so], s68 offen lds\n\t" \
  "s_load_dword s84, %[po], 0x500\n\t" \
  "s_add_u32 m0, %[ob], 4096\n\t" \
  "s_nop 0\n\t" \
  "buffer_load_dwordx4 v8, %[so], s69 offen lds\n\t" \
  "s_load_dword s84, %[po], 0x540\n\t" \
  "s_add_u32 m0, %[ob], 8192\n\t" \
  "s_nop 0\n\t" \
  "buffer_load_dwordx4 v8, %[so], s70 offen lds\n\t" \
  "s_load_dword s84, %[po], 0x580\n\t" \
  "s_add_u32 m0, %[ob], 73728\n\t" \
  "s_nop 0\n\t" \
  "buffer_load_dwordx4 v8, %[so], s71 offen lds\n\t" \
  "s_add_u32 m0, %[ob], 77824\n\t" \
  "s_nop 0\n\t" \
  "buffer_load_dwordx4 v8, %[so], s72 offen lds\n\t" \
  "v_readlane_b32 s68, v9, 3\n\t" \
  "v_readlane_b32 s69, v9, 4\n\t" \
  "v_readlane_b32 s70, v9, 5\n\t" \
  "v_readlane_b32 s71, v9, 20\n\t" \
  "v_readlane_b32 s72, v9, 21\n\t" \
  "s_add_u32 m0, %[ob], 12288\n\t" \
  "s_waitcnt vmcnt(17)\n\t" \
  "s_barrier\n\t" \
  "s_nop 2\n\t" \
  "buffer_load_dwordx4 v8, %[so], s68 offen lds\n\t" \
  "s_load_dword s84, %[po], 0x5c0\n\t" \
  "s_add_u32 m0, %[ob], 16384\n\t" \
  "s_nop 0\n\t" \
  "buffer_load_dwordx4 v8, %[so], s69 offen lds\n\t" \
  "s_load_dword s84, %[po], 0x600\n\t" \
  "s_add_u32 m0, %[ob], 20480\n\t" \
  "s_nop 0\n\t" \
  "buffer_load_dwordx4 v8, %[so], s70 offen lds\n\t" \
  "s_load_dword s84, %[po], 0x640\n\t" \
  "s_add_u32 m0, %[ob], 81920\n\t" \
  "s_nop 0\n\t" \
  "buffer_load_dwordx4 v8, %[so], s71 offen lds\n\t" \
  "s_add_u32 m0, %[ob], 86016\n\t" \
  "s_nop 0\n\t" \
  "buffer_load_dwordx4 v8, %[so], s72 offen lds\n\t" \
  "v_readlane_b32 s68, v9, 6\n\t" \
  "v_readlane_b32 s69, v9, 7\n\t" \
  "v_readlane_b32 s70, v9, 8\n\t" \
  "v_readlane_b32 s71, v9, 22\n\t" \
  "v_readlane_b32 s72, v9, 23\n\t" \
  "v_readlane_b32 s73, v9, 24\n\t" \
  "s_add_u32 m0, %[ob], 24576\n\t" \
  "s_waitcnt vmcnt(13)\n\t" \
  "s_barrier\n\t" \
  "s_nop 2\n\t" \
  "buffer_load_dwordx4 v8, %[so], s68 offen lds\n\t" \
  "s_load_dword s84, %[po], 0x680\n\t" \
  "s_add_u32 m0, %[ob], 28672\n\t" \
  "s_nop 0\n\t" \
  "buffer_load_dwordx4 v8, %[so], s69 offen lds\n\t" \
  "s_load_dword s84, %[po], 0x6c0\n\t" \
  "s_add_u32 m0, %[ob], 32768\n\t" \
  "s_nop 0\n\t" \
  "buffer_load_dwordx4 v8, %[so], s70 offen lds\n\t" \
  "s_load_dword s84, %[po], 0x700\n\t" \
  "s_add_u32 m0, %[ob], 90112\n\t" \
  "s_nop 0\n\t" \
  "buffer_load_dwordx4 v8, %[so], s71 offen lds\n\t" \
  "s_add_u32 m0, %[ob], 94208\n\t" \
  "s_nop 0\n\t" \
  "buffer_load_dwordx4 v8, %[so], s72 offen lds\n\t" \
  "s_add_u32 m0, %[ob], 98304\n\t" \
  "s_nop 0\n\t" \
  "buffer_load_dwordx4 v8, %[so], s73 offen lds\n\t" \
  "v_readlane_b32 s68, v9, 9\n\t" \
  "v_readlane_b32 s69, v9, 10\n\t" \
  "v_readlane_b32 s70, v9, 11\n\t" \
  "v_readlane_b32 s71, v9, 25\n\t" \
  "v_readlane_b32 s72, v9, 26\n\t" \
  "s_add_u32 m0, %[ob], 36864\n\t" \
  "s_waitcnt vmcnt(16)\n\t" \
  "s_barrier\n\t" \
  "s_nop 2\n\t" \
  "buffer_load_dwordx4 v8, %[so], s68 offen lds\n\t" \
  "s_load_dword s84, %[po], 0x740\n\t" \
  "s_add_u32 m0, %[ob], 40960\n\t" \
  "s_nop 0\n\t" \
  "buffer_load_dwordx4 v8, %[so], s69 offen lds\n\t" \
  "s_load_dword s84, %[po], 0x780\n\t" \
  "s_add_u32 m0, %[ob], 45056\n\t" \
  "s_nop 0\n\t" \
  "buffer_load_dwordx4 v8, %[so], s70 offen lds\n\t" \
  "s_load_dword s84, %[po], 0x7c0\n\t" \
  "s_add_u32 m0, %[ob], 102400\n\t" \
  "s_nop 0\n\t" \
  "buffer_load_dwordx4 v8, %[so], s71 offen lds\n\t" \
  "s_add_u32 m0, %[ob], 106496\n\t" \
  "s_nop 0\n\t" \
  "buffer_load_dwordx4 v8, %[so], s72 offen lds\n\t" \
  "v_readlane_b32 s68, v9, 12\n\t" \
  "v_readlane_b32 s69, v9, 13\n\t" \
  "v_readlane_b32 s70, v9, 14\n\t" \
  "v_readlane_b32 s71, v9, 27\n\t" \
  "v_readlane_b32 s72, v9, 28\n\t" \
  "v_readlane_b32 s73, v9, 29\n\t" \
  "s_add_u32 m0, %[ob], 49152\n\t" \
  "s_nop 3\n\t" \
  "s_barrier\n\t" \
  "s_nop 2\n\t" \
  "buffer_load_dwordx4 v8, %[so], s68 offen lds\n\t" \
  "s_add_u32 m0, %[ob], 53248\n\t" \
  "s_nop 0\n\t" \
  "buffer_load_dwordx4 v8, %[so], s69 offen lds\n\t" \
  "s_add_u32 m0, %[ob], 57344\n\t" \
  "s_nop 0\n\t" \
  "buffer_load_dwordx4 v8, %[so], s70 offen lds\n\t" \
  "s_add_u32 m0, %[ob], 110592\n\t" \
  "s_nop 0\n\t" \
  "buffer_load_dwordx4 v8, %[so], s71 offen lds\n\t" \
  "s_add_u32 m0, %[ob], 114688\n\t" \
  "s_nop 0\n\t" \
  "buffer_load_dwordx4 v8, %[so], s72 offen lds\n\t" \
  "s_add_u32 m0, %[ob], 118784\n\t" \
  "s_nop 0\n\t" \
  "buffer_load_dwordx4 v8, %[so], s73 offen lds\n\t" \
  "v_readlane_b32 s68, v9, 15\n\t" \
  "v_readlane_b32 s69, v9, 16\n\t" \
  "v_readlane_b32 s70, v9, 17\n\t" \
  "v_readlane_b32 s71, v9, 30\n\t" \
  "v_readlane_b32 s72, v9, 31\n\t" \
  "s_add_u32 m0, %[ob], 61440\n\t" \
  "LS_%=:\n\t" \
  "ds_read_b32 v11, v10\n\t" \
  "s_addk_i32 %[cn], 1\n\t" \
  "s_cmp_gt_u32 %[cn], 0x400000\n\t" \
  "s_cbranch_scc1 LX_%=\n\t" \
  "s_waitcnt lgkmcnt(0)\n\t" \
  "v_cmp_eq_u32 vcc, 0, v11\n\t" \
  "s_cbranch_vccz LX_%=\n\t" \
  "s_sleep 1\n\t" \
  "s_branch LS_%=\n\t" \
  "LX_%=:\n\t" \
  "s_nop 2\n\t" \
  "buffer_load_dwordx4 v8, %[so], s68 offen lds\n\t" \
  "s_add_u32 m0, %[ob], 65536\n\t" \
  "s_nop 0\n\t" \
  "buffer_load_dwordx4 v8, %[so], s69 offen lds\n\t" \
  "s_add_u32 m0, %[ob], 69632\n\t" \
  "s_nop 0\n\t" \
  "buffer_load_dwordx4 v8, %[so], s70 offen lds\n\t" \
  "s_add_u32 m0, %[ob], 122880\n\t" \
  "s_nop 0\n\t" \
  "buffer_load_dwordx4 v8, %[so], s71 offen lds\n\t" \
  "s_add_u32 m0, %[ob], 126976\n\t" \
  "s_nop 0\n\t" \
  "buffer_load_dwordx4 v8, %[so], s72 offen lds\n\t" \
  "s_waitcnt vmcnt(0) lgkmcnt(0)\n\t"
#define LD_HT 8
#define LD_CLOB "v11", "s68", "s69", "s70", "s71", "s72", "s73", "s74", "s75", "s76", "s77", "s78", "s79", "s80", "s81", "s82", "s83", "s84", "s85"

#define ENDW
#define TS(i)
#define TSS(i)
#define TOUT(n)

#define SPIN(cond,slp) do{ int sp_=0; while(cond){ __builtin_amdgcn_s_sleep(slp); if(++sp_>(1<<22)) break; } }while(0)
__device__ __forceinline__ unsigned rd1(const char* p){ return __builtin_amdgcn_readfirstlane(*(volatile LDS_AS unsigned*)(LDS_AS char*)p); }
__device__ __forceinline__ unsigned lds_inc(const char* p, int l){ unsigned r=0; if(l==0) r=__hip_atomic_fetch_add((LDS_AS unsigned*)(LDS_AS char*)p, 1u, __ATOMIC_RELAXED, __HIP_MEMORY_SCOPE_WORKGROUP); return __builtin_amdgcn_readfirstlane(r); }
__device__ __forceinline__ void lds_add(unsigned adr){ asm volatile("s_mov_b64 exec, 1\n\tds_add_u32 %0, %1\n\ts_mov_b64 exec, -1" :: "v"(adr), "v"(1u) : "memory"); }
__device__ __forceinline__ void bars(int n){
  asm volatile("BL_%=:\n\ts_barrier\n\ts_sub_u32 %0, %0, 1\n\ts_cmp_lg_u32 %0, 0\n\ts_cbranch_scc1 BL_%=" : "+s"(n) :: "memory", "scc");
}
#define barc(rem) barc_(rem)
#define CTRA
#define CTRP
__device__ __forceinline__ void barc_(unsigned& rem){
  asm volatile("s_cmp_eq_u32 %0, 0\n\ts_cbranch_scc1 BC_%=\n\ts_barrier\n\ts_sub_u32 %0, %0, 1\n\tBC_%=:" : "+s"(rem) :: "memory", "scc");
}
template<int SLP> __device__ __forceinline__ void wait_flag_b(const unsigned* p, unsigned voff, unsigned tag, unsigned& rem CTRA){
  int spins=0;
  while(true){
    unsigned v; asm volatile("global_load_dword %0, %1, %2 sc1" : "=v"(v) : "v"(voff), "s"(p) : "memory");
    barc(rem); if(PB2) barc(rem);
    asm volatile("s_waitcnt vmcnt(0)" : "+v"(v) :: "memory");
    if(__all(v==tag) || ++spins>(1<<21)) break;
    if(rem==0) __builtin_amdgcn_s_sleep(SLP);
  }
}
__device__ __forceinline__ void stout(void* sbase, unsigned voff, u32x4 v){
  st16cs(sbase,voff,v);
}

typedef unsigned u32x8 __attribute__((ext_vector_type(8)));
typedef unsigned u32x16 __attribute__((ext_vector_type(16)));
__device__ __forceinline__ char* ptr64(unsigned lo, unsigned hi){ return (char*)(((unsigned long long)hi<<32)|lo); }
__device__ __forceinline__ u32x4 srd(const char* p, unsigned bytes){ return u32x4{(unsigned)(size_t)p,(unsigned)((size_t)p>>32)&0xffffu,bytes,0x20000u}; }
constexpr size_t WS_O=0, WS_KV=(size_t)S*DM*2, WS_Y=(size_t)S*DM*4, WS_FL=(size_t)S*DM*12;

__global__ __launch_bounds__(768,3) void fused_kernel(const u16* x_, const u16* wqkv_, const u16* wo_, char* ws_, u16* out_,
    const u16* kin_, const u16* vin_, const u16* shk_, const u16* hk_, const u16* qin_, const u16* sD_)
{
  __shared__ __attribute__((aligned(128))) char smem[ALL_LDS];
  TS(0);
  const auto ka=__builtin_amdgcn_kernarg_segment_ptr();
  int w8;
  {
    unsigned t=threadIdx.x;
    asm volatile(
      "s_getpc_b64 s[72:73]\n\tv_readfirstlane_b32 %[w8], %[t]\n\ts_lshr_b32 s74, %[b], 3\n\ts_mul_i32 s74, s74, 12\n\ts_lshr_b32 %[w8], %[w8], 6\n\t"
      "s_add_u32 s74, s74, %[w8]\n\ts_min_u32 s74, s74, %[cl]\n\ts_lshl_b32 s74, s74, 7\n\ts_mov_b32 m0, %[sk]\n\tv_mov_b32 v1, s74\n\ts_mov_b64 exec, 1\n\tglobal_load_lds_dword v1, s[72:73]\n\ts_mov_b64 exec, -1\n\t"
      "s_bitcmp1_b32 %[w8], 2\n\ts_cbranch_scc1 EX_%=\n\t"
      "s_load_dwordx4 s[68:71], %[ka], 0x0\n\tv_and_b32 v1, 0xff, %[t]\n\ts_bitcmp1_b32 %[w8], 3\n\ts_cbranch_scc1 EC_%=\n\t"
      "s_mul_i32 s74, %[b], %[nw]\n\tv_cmp_gt_u32 vcc, %[nw], v1\n\tv_add_lshl_u32 v1, v1, s74, 7\n\ts_and_b64 exec, vcc, exec\n\ts_cbranch_execz EE_%=\n\ts_waitcnt lgkmcnt(0)\n\tglobal_load_lds_dword v1, s[70:71]\n\ts_branch EE_%=\n\t"
      "EC_%=:\n\tv_cmp_gt_u32 vcc, %[nx], v1\n\tv_and_b32 v2, 7, v1\n\tv_lshlrev_b32 v1, 4, v1\n\t"
      "s_lshl_b32 s74, %[b], 19\n\ts_lshl_b32 s73, %[b], 11\n\ts_or_b32 s74, s74, s73\n\ts_and_b32 s74, s74, 0x3fc000\n\t"
      "v_and_b32 v1, 0xf80, v1\n\tv_lshl_add_u32 v1, v2, 11, v1\n\tv_add_u32 v1, s74, v1\n\t"
      "s_and_b64 exec, vcc, exec\n\ts_cbranch_execz EE_%=\n\ts_waitcnt lgkmcnt(0)\n\tglobal_load_lds_dword v1, s[68:69]\n\t"
      "EE_%=:\n\ts_mov_b64 exec, -1\n\ts_waitcnt lgkmcnt(0)\n\tEX_%=:"
      : [w8]"=&s"(w8) : [ka]"s"(ka), [t]"v"(t), [b]"s"(blockIdx.x), [cl]"n"(CODEL-1), [sk]"s"(lds_addr(smem)+SINK+256), [nw]"s"(12*TPW), [nx]"s"(8*TPX)
      : "memory", "scc", "vcc", "v1", "v2", "s68", "s69", "s70", "s71", "s72", "s73", "s74");
  }
  #define ROLE u32x8 a0; int bid=blockIdx.x; asm volatile("s_load_dwordx8 %0, %2, 0x0" : "=&s"(a0), "+s"(bid) : "s"(ka)); \
    const int w=w8&3, xcd=bid&7, idx=bid>>3, rb=(xcd>>1)*4+(idx&3), hp=(xcd&1)*8+(idx>>2), s0=rb*128; (void)s0; (void)hp;
  #define LANEVARS int l; asm volatile("v_mbcnt_lo_u32_b32 %0, -1, 0\n\tv_mbcnt_hi_u32_b32 %0, -1, %0" : "=v"(l)); const int g=l>>4, fr=l&15, tq=(l&15)>>2, tp=l&3; (void)g; (void)fr; (void)tq; (void)tp;
  #define ARGS0 asm volatile("s_waitcnt lgkmcnt(0)" : "+s"(a0)); const char* x=ptr64(a0[0],a0[1]); const char* wqkv=ptr64(a0[2],a0[3]); const char* wo=ptr64(a0[4],a0[5]); char* ws=ptr64(a0[6],a0[7]); \
    unsigned* flags=(unsigned*)(ws+WS_FL); const int hot0=(3-(int)(a0[2]>>7))&7; (void)x; (void)wqkv; (void)wo; (void)flags; (void)hot0;
  #define ORA ((unsigned)(hp-hot0-1)&7u)
  #define ORD(c) ((((ORA==7u? ((c)==0u||(c)==15u? 7u : ((c)-1u)>>1) : ((c)<14u? (ORA+((c)>>1))%7u : 7u))+hot0+1)&7u)+8u*(((hp>>3)^(c))&1u))
  const unsigned sb0=__builtin_amdgcn_readfirstlane(lds_addr(smem));
  const char* syn=smem+SYN;

  if(__builtin_expect((w8>>2)==1,1)){
    ROLE
    LANEVARS
    __builtin_amdgcn_s_setprio(LPR);
    unsigned ltr=0, ltr2=0, ltr3=0; (void)ltr; (void)ltr2; (void)ltr3;
    const int xr=18*w+(l>>3);
    const unsigned ch=(unsigned)(((l&7)^(xr&7))*16);
    unsigned xo=(unsigned)(xr*2048)+ch;
    unsigned xoA=(s0==0 && xr<16)? 16*2048+ch : xo, xoB=((s0==0 && xr+8<16)? 8*2048+ch : xo)+15360;
    unsigned xo1=xo+15360, xo2=xo+30720;
    unsigned wof[3];
    #pragma unroll
    for(int i=0;i<3;i++){ const int P=3*w+i, wr=8*(P&3)+(l>>3); const int ci=((l&7)>>1)^wswz(wr); wof[i]=(unsigned)(wr*6144+(P>>2)*2048-1024*i+(MIX? (ci>>1)*128+(ci&1)*32 : ci*32)+(l&1)*16); }
    unsigned xl=sb0+X_OFF+w*2304, wl=sb0+w*3072, xd=(unsigned)(s0-16)*2048u, wd=hp*128, hq=hp&7, rq=rb&3;
    asm volatile("" : "+{v0}"(xoA), "+{v1}"(xoB), "+{v2}"(xo), "+{v3}"(xo1), "+{v4}"(xo2), "+{v5}"(wof[0]), "+{v6}"(wof[1]), "+{v7}"(wof[2]), "+s"(xl), "+s"(wl), "+s"(xd), "+s"(wd), "+s"(hq), "+s"(rq));
    TSS(2);
    ARGS0
    const unsigned wm=MIX? (wd&~255u)+((wd>>1)&64u) : wd;
    const u32x4 sx=srd(x+(int)xd,144u*2048u), sw=srd(wqkv+wm,6291456u-wm);
    const int jj=(ROT && (MIX? ((int)hq|1)==(hot0|1) : (int)hq==hot0))? rq : 0;
    TSS(3);
    asm volatile(LD_ASM1
      :: "{v0}"(xoA), "{v1}"(xoB), "{v2}"(xo), "{v3}"(xo1), "{v4}"(xo2), "{v5}"(wof[0]), "{v6}"(wof[1]), "{v7}"(wof[2]),
        [sx]"s"(sx), [sw]"s"(sw), [xb]"s"(xl), [wb]"s"(wl), [rx0]"s"(jj*128), [rw0]"s"(jj*393216)
      : "memory", "scc", "s68");
    {
      const unsigned wof2=(unsigned)(w*16384+(l>>3)*2048+((((l&7)>>1)^((l>>4)&3))*32)+(l&1)*16);
      const unsigned to=ORD((unsigned)(l>>1)&15u);
      const unsigned tab=MIX? (to&~1u)*131072u+(l&1)*131072u+(to&1u)*65536u : to*131072u+(l&1)*65536;
      unsigned long long tm; asm("s_cmp_eq_u32 %1, 0\n\ts_cselect_b64 %0, %2, 0" : "=s"(tm) : "s"(w), "s"(WTL==2? 0xffffffffull : 0ull) : "scc");
      unsigned cn=0;
      const unsigned hn=(unsigned)(w*(3*LD_HT/4)+l), hto=(hn/6u)*393216u+(hn%6u)*2048u;
      asm volatile(LD_ASM2
        : [cn]"+&s"(cn)
        : "{v0}"(xoA), "{v1}"(xoB), "{v2}"(xo), "{v3}"(xo1), "{v4}"(xo2), "{v5}"(wof[0]), "{v6}"(wof[1]), "{v7}"(wof[2]), "{v8}"(wof2), "{v9}"(tab),
          "{v10}"(sb0+SYN+32+(l&3)*4), "{v12}"((l&31)*2048),
          "{v16}"(hto), [sh]"s"(srd(wqkv+((xcd&1)*8+hot0)*128+idx*12288,6291456u-2048u-idx*12288u+128u)),
          [sx]"s"(sx), [sw]"s"(sw), [so]"s"(srd(wo+wd,2097152u-wd)), [xb]"s"(xl), [wb]"s"(wl), [ob]"s"(sb0+w*1024),
          [rx0]"s"(jj*128), [rx1]"s"(((jj+1)&3)*128), [rx2]"s"(((jj+2)&3)*128), [rx3]"s"(((jj+3)&3)*128),
          [rw0]"s"(jj*393216), [rw1]"s"(((jj+1)&3)*393216), [rw2]"s"(((jj+2)&3)*393216), [rw3]"s"(((jj+3)&3)*393216),
          [pw]"s"(wqkv+idx*6144+(xcd&1)*1024+(xcd>>1)*256+w*64), [px]"s"(x+(xcd*256+idx*8+w*2)*2048), [po]"s"(wo+(bid*4+w)*2048),
          [tsk]"s"(sb0+SINK+256), [tm]"s"(tm), [tp]"s"(wo+idx*65536+(xcd&1)*1024+((3-(int)(a0[4]>>7))&7)*128)
        : "memory", "scc", "vcc", LD_CLOB);
    }
    const unsigned tag=rd1(syn+64)+1u;
    lds_add(sb0+SYN+12);
    TS(2);
    if(w==3){
      const unsigned* fp=flags+F_O+(rb*16+hp)*16; const unsigned fo=ORD((unsigned)l&15u)*4u;
      const unsigned madr=sb0+(l==0? SYN+20 : SINK+l*4);
      SPIN(rd1(syn+12)<4u || rd1(syn+16)<4u,1);
      PUB(madr,1u);
      unsigned rdy=1; int spins=0;
      while(rdy!=0xFFFFu){
        unsigned v;
        asm volatile("s_mov_b64 exec, 0xffff\n\tglobal_atomic_or %0, %1, %3, %2 sc0 sc1\n\ts_mov_b64 exec, -1\n\ts_waitcnt vmcnt(0)" : "=&v"(v) : "v"(fo), "s"(fp), "v"(0u) : "memory");
        unsigned r=((unsigned)__ballot(v==tag)&0xFFFFu)|1u;
        if(++spins>(1<<21)) r=0xFFFFu;
        if(r!=rdy){ PUB(madr,(unsigned)__builtin_ctz(~r)); rdy=r; }
        else __builtin_amdgcn_s_sleep(OSLP);
      }
      TS(3);
      if(bid==0){
        int sp=0;
        while(true){
          u32x4 v; asm volatile("global_load_dwordx4 %0, %1, %2 sc1\n\ts_waitcnt vmcnt(0)" : "=&v"(v) : "v"(l*16), "s"(flags+F_Y) : "memory");
          if(__all(v[0]==tag && v[1]==tag && v[2]==tag && v[3]==tag) || ++sp>(1<<20)) break;
          __builtin_amdgcn_s_sleep(12);
        }
        if(l==0) stflag(flags, tag);
      }
    } else { TS(3); }
    TOUT(4);
    return;
  }

  if(w8<4){
    ROLE
    u32x2 a1; asm volatile("s_load_dwordx2 %0, %1, 0x20" : "=&s"(a1) : "s"(ka));
    __builtin_amdgcn_s_setprio(GPR);
    const int h=w&1, r=w>>1;
    f32x16 e0, e1;
    LANEVARS
    TSS(3);
    ARGS0
    asm volatile("" : "+s"(a1));
    if(PGT&1){ if(w==1 && l<7) touch(ws, l<6? l*2097152+PGO : (unsigned)WS_FL+2048, sb0+SINK+512); }
    if(PGT&2){ if(w==2 && l<4) touch(ptr64(a1[0],a1[1]), l*2097152+PGO, sb0+SINK+512); }
    if(PGT&4){ if(w==3 && l<1) touch(wo, PGO, sb0+SINK+512); if(w==0 && l<2) touch(wqkv, l*2097152+2097152, sb0+SINK+512); }
    TSS(4);
    {
      unsigned lx0=sb0+X_OFF+fr*128+((g^(fr&7))*16), lx1=sb0+X_OFF+fr*128+(((4+g)^(fr&7))*16);
      const int sw=wswz(8*g+tq);
      unsigned lw0=sb0+(8*g+tq)*128+(unsigned)((((2*h)^sw)*32)+8*tp), lw1=sb0+(8*g+tq)*128+(unsigned)((((2*h+1)^sw)*32)+8*tp);
      char* ot=ws+WS_O+(rb*16+hp)*16384+r*8192+h*2048;
      unsigned it; unsigned long long rm; asm("s_cmp_lg_u32 %1, 0\n\ts_cselect_b64 %0, -1, 0" : "=s"(rm) : "s"(r) : "scc");
      asm volatile(N2_ASM
        : [it]"=&s"(it), "+{v56}"(lx0), "+{v57}"(lx1), "+{v58}"(lw0), "+{v59}"(lw1)
        : "{v62}"(l*16), "{v63}"(sb0+EX_OFF+h*3072+l*16), [r]"s"(r), [ot]"s"(ot), [ou]"s"(ot+4096), [fa]"s"(sb0+SYN+32+w*4), [fp]"s"(sb0+SYN+32+(w^2)*4),
          [vo]"s"(2048-1024*r), [rm]"s"(rm), [z]"s"(rb|r)
        : "memory", "scc", "vcc", N2_CLOB);
    }
    TS(3);
    unsigned tb;
    {
      LANEVARS
      const unsigned tag=rd1(syn+64)+1u;
      const unsigned ra=sb0+(4*g+tq)*128+8*tp; const int sw=(2*g+(tq>>1))&3;
      unsigned b0=ra+((0^sw)*32), b1=ra+((1^sw)*32), b2=ra+((2^sw)*32), b3=ra+((3^sw)*32), vo=(unsigned)(w*4096+l*16);
      tb=ORD((unsigned)l&15u)*16384u;
      asm volatile("" : "+{v160}"(b0), "+{v161}"(b1), "+{v162}"(b2), "+{v163}"(b3), "+{v164}"(vo), "+{v167}"(tb));
      WAITV(0);
      if(lds_inc(syn+16,l)==3u){ if(l<16) stflag(flags+F_O+(rb*16+l)*16+hp, tag); }
      TS(4);
      __builtin_amdgcn_s_setprio(G2P);
      unsigned c=0, iss=0, th, q0, q1, cn=0;
      asm volatile(G3_ASM
        : "={v[128:143]}"(e0), "={v[144:159]}"(e1), [c]"+s"(c), [iss]"+s"(iss), [th]"=&s"(th), [t0]"=&s"(q0), [t1]"=&s"(q1), [cn]"+&s"(cn),
          "+{v160}"(b0), "+{v161}"(b1), "+{v162}"(b2), "+{v163}"(b3), "+{v167}"(tb)
        : "{v164}"(vo), "{v165}"(sb0+SYN+20), [go]"s"(60+w*4), [ob]"s"(srd(ws+WS_O+rb*262144,262144u))
        : "memory", "scc", "vcc", G3_CLOB);
    }
    {
      LANEVARS
      char* sg=smem+EX_OFF+w*2048;
      char* op=ptr64(a1[0],a1[1])+(s0+w*32)*4096+2048+hp*128; const unsigned oo=(unsigned)((l>>3)*4096+(l&7)*16);
      u32x4 ov[4];
      #pragma unroll
      for(int t=0;t<2;t++){
        #pragma unroll
        for(int j=0;j<4;j++){
          #define E2(i) (t? e1[(i)&15] : e0[(i)&15])
          *(volatile LDS_AS u32x2*)(LDS_AS char*)(sg+fr*128+(((2*j+(g>>1))^(fr&7))*16)+(g&1)*8)=u32x2{pack2(E2(4*j),E2(4*j+1)),pack2(E2(4*j+2),E2(4*j+3))};
        }
        #pragma unroll
        for(int i=0;i<2;i++) ov[2*t+i]=*(volatile LDS_AS u32x4*)(LDS_AS char*)(sg+(i*8+(l>>3))*128+(((l&7)^(l>>3))*16));
      }
      #pragma unroll
      for(int i=0;i<4;i++) stout(op+i*32768, oo, ov[i]);
      ENDW TS(6); TOUT(7);
    }
    return;
  }

  {
    ROLE
    u32x8 a1; u32x4 a2; u32x2 a3;
    asm volatile("s_load_dwordx8 %0, %3, 0x20\n\ts_load_dwordx4 %1, %3, 0x40\n\ts_load_dwordx2 %2, %3, 0x50" : "=&s"(a1), "=&s"(a2), "=&s"(a3) : "s"(ka));
    char* cr=smem+C_OFF+2048+w*4096;
    const unsigned crs=sb0+C_OFF+2048+w*4096;
    const int sblk=bid>>3, td0=(bid&7)*128+(w&1)*64;
    const int vb=(LOCA&1)? xcd*32+(idx&16)+(idx&1)*8+((idx>>1)&7) : bid, fb=(LOCA&2)? (idx&15)*16+xcd*2+(idx>>4) : bid;
    const int d=(vb>>4)*64+(((vb>>3)&1)*4+w)*8+(vb&7);
    const int kv0=KV0+(sblk&KVS);
    static_assert(KV0+KVS+PP_NB<=33, "barrier budget");
    unsigned tagv, ctr; f32x8 RB;
    const int fd0=(fb&15)*64, fs0=(fb>>4)*128+w*32;
    u32x4 qv[4];
    f32x16 A0,A1,A2,A3;
    const int rw=sblk*64+(w>>1)*32, j=rw>>5;
    unsigned k0,k1,k2,ko,ta,se,so,ha,l16,rb0[2];
    {
      LANEVARS
      if(w==0 && l<25 && l!=16) ((unsigned*)(smem+SYN))[l]=0u;
      if(l<32) *(u32x4*)(smem+C_OFF+w*512+l*16)=u32x4{0,0,0,0};
      asm volatile("" ::: "memory");
      __builtin_amdgcn_s_setprio(CPR);
      const int dg=l&7, rg=l>>3;
      ko=(unsigned)(rg*8192+dg*16);
      const bool top=(rw==0 && rg==0);
      k0=top? ko+6144:ko; k1=top? ko+6144:ko+2048; k2=top? ko+6144:ko+4096;
      ta=sb0+EX_OFF+w*1024+(w&1)*512+dg*64; se=(l&8)? 0x03020706u : 0x05040100u; ha=crs+(32*g-4*tq+4*tp)*2; l16=l*16;
      so=(unsigned)(rg*32768+((j>>2)&3)*128+((dg&4)|((dg+j)&3))*16);
      #pragma unroll
      for(int k=0;k<2;k++){ const int q=fr-1-2*g+k; rb0[k]=sb0+C_OFF+(4+(q>>1))*256+((q>>3)&3)*64+((w+(q>>1))&3)*16+(q&1)*8; }
      asm volatile("" : "+{v152}"(k0), "+{v153}"(k1), "+{v154}"(k2), "+{v155}"(ko), "+{v156}"(ta), "+{v157}"(se), "+{v159}"(so), "+{v163}"(l16), "+{v165}"(ha), "+{v166}"(rb0[0]), "+{v167}"(rb0[1]));
    }
    TSS(3);
    ARGS0
    asm volatile("" : "+s"(a1), "+s"(a2), "+s"(a3));
    char* out=ptr64(a1[0],a1[1]); const char* qp=ptr64(a2[2],a2[3])+(fs0*DM+fd0)*2;
    {
      const char* kb=ptr64(a1[2],a1[3])+(rw*DM+td0)*2; const char* vb=ptr64(a1[4],a1[5])+(rw*DM+td0)*2;
      unsigned long long zm; asm("s_cmp_eq_u32 %1, 0\n\ts_cselect_b64 %0, 0xff, 0" : "=s"(zm) : "s"(rw) : "scc");
      unsigned t0;
      if(PGT&8){ const char* tp= w==0? kb : w==1? vb : w==2? qp : ptr64(a2[0],a2[1])+d*4096;
        asm volatile("s_mov_b32 m0, %1\n\ts_mov_b64 exec, 1\n\tglobal_load_lds_dword v163, %0\n\ts_mov_b64 exec, -1" :: "s"(tp), "s"(sb0+SINK+512), "{v163}"(l16) : "memory"); }
      TSS(5);
      asm volatile(PP_ASM
        : [t0]"=&s"(t0), "=&{v164}"(ctr), "+{v159}"(so), "=&{v160}"(tagv), "=&{v[24:31]}"(RB), "=&{v[44:47]}"(qv[0]), "=&{v[48:51]}"(qv[1]), "=&{v[52:55]}"(qv[2]), "=&{v[56:59]}"(qv[3]),
          "=&{v[64:79]}"(A0), "=&{v[80:95]}"(A1), "=&{v[96:111]}"(A2), "=&{v[112:127]}"(A3)
        : "{v152}"(k0), "{v153}"(k1), "{v154}"(k2), "{v155}"(ko), "{v156}"(ta), "{v157}"(se), "{v166}"(rb0[0]), "{v167}"(rb0[1]), "{v165}"(ha), "{v163}"(l16),
          [qb0]"s"(qp), [qb2]"s"(qp+4096), [mk]"s"(0x180u), [em]"s"(1ull|(512ull<<w)), [hd]"s"(crs),
          [kb0]"s"(kb-6144), [kb3]"s"(kb), [kb5]"s"(kb+4096), [vb0]"s"(vb), [vb2]"s"(vb+4096), [sb]"s"(ws+WS_KV+td0*4096+j*512),
          [n0]"s"(kv0), [ff]"s"(flags+F_PP+(td0>>6)*64+j), [zm]"s"(zm), [shd]"s"(sb0+EX_OFF+w*1024), [tgd]"s"(sb0+SYN+64), [shp]"s"(ptr64(a1[6],a1[7])+(bid&7)*1024), [fl]"s"(flags),
          [hp]"s"(ptr64(a2[0],a2[1])+d*4096), [dp]"s"(ptr64(a3[0],a3[1])+d*2)
        : "memory", "scc", PP_CLOB);
    }
    LANEVARS
    const unsigned tag=__builtin_amdgcn_readfirstlane(tagv)+1u;
    unsigned rem=33-PP_NB-kv0;
    barc(rem);
    const unsigned* pf=flags+F_PP+(d>>6)*64;
    const char* kvp=ws+WS_KV+((vb>>4)*8+(vb&7))*32768+((vb>>3)&1)*64+w*8192;
    const unsigned kvo=(unsigned)((l>>2)*128+(l&3)*16);
    int bidx=((l>=16)? l-16 : (l==0? 63 : l+47))*4;
    asm volatile("" : "+{v32}"(bidx));
    __builtin_amdgcn_s_setprio(3);
    while(rem>PREM) barc(rem);
    wait_flag_b<8>(pf, l*4, tag, rem CTRP);
    #pragma unroll
    for(int i=0;i<4;i++){ if(i%KTH==0 && i) barc(rem); gldsc(kvp+i*2048, kvo, crs+i*1024); }
    barc(rem); barc(rem);
    WAITV(0);
    asm volatile("s_mov_b32 m0, %2\n\ts_mov_b64 exec, 1\n\tglobal_load_lds_dword %0, %1 sc1\n\ts_mov_b64 exec, -1" :: "v"(0u), "s"(pf), "s"(sb0+SYN+100+w*4) : "memory");
    while(rem) barc(rem);
    { int sp=0; while(!__all(*(volatile LDS_AS unsigned*)(LDS_AS char*)(syn+100+(l&3)*4)==tag) && ++sp<(1<<22)) __builtin_amdgcn_s_sleep(1); }
    __builtin_amdgcn_s_setprio(CVP);
    TS(3);
    f32x16 ac0, ac1;
    {
      unsigned p0,p1,p2,p3,p4,p5,p6;
      asm volatile("s_nop 1\n\t" CV_ASM
        : "=&{v[128:143]}"(ac0), "=&{v[144:159]}"(ac1), [p0]"=&s"(p0), [p1]"=&s"(p1), [p2]"=&s"(p2), [p3]"=&s"(p3), [p4]"=&s"(p4), [p5]"=&s"(p5), [p6]"=&s"(p6)
          , "+{v[44:47]}"(qv[0]), "+{v[48:51]}"(qv[1]), "+{v[52:55]}"(qv[2]), "+{v[56:59]}"(qv[3])
        : "{v[64:79]}"(A0), "{v[80:95]}"(A1), "{v[96:111]}"(A2), "{v[112:127]}"(A3),
          "{v[24:31]}"(RB), "{v32}"(bidx), "{v33}"(sb0+SYN+80+w*4)
        : "memory", "scc", CV_CLOB);
    }
    TS(4);
    __builtin_amdgcn_s_setprio(3);
    {
      const unsigned yo=(unsigned)((256*(g&1)+16*fr+4*(g&2))*2);
      #pragma unroll
      for(int J=0;J<8;J+=2){
        #define YA(i) ((i)<16? ac0[(i)&15] : ac1[(i)&15])
        const u32x2 a={pack2(YA(4*J),YA(4*J+1)),pack2(YA(4*J+2),YA(4*J+3))}, b={pack2(YA(4*J+4),YA(4*J+5)),pack2(YA(4*J+6),YA(4*J+7))};
        st16cs(ws+WS_Y+d*4096+512*J, yo, widen(a,b));
      }
    }
    WAITV(0);
    if(lds_inc(syn+24,l)==3u){ if(l<16) stflag(flags+F_Y+(l*16+(vb>>4))*16+(vb&15), tag); }
    TS(5);
    if(w==0){
      SPIN(rd1(syn+96)==0u,2);
      wait_flag<YSLP>(flags+F_Y+fb*16+(l&15), tag);
      SPIN(rd1(syn+24)<4u,1);
      PUB(sb0+(l==0? SYN+28 : SINK+l*4), 1u);
    } else SPIN(rd1(syn+28)==0u,4);
    #pragma unroll
    for(int i=0;i<4;i++) gldsc(ws+WS_Y+(fd0+i*16)*4096+fs0*2, (l>>2)*4096+(l&3)*16, crs+i*1024);
    WAITV(0);
    {
      u32x4 y2[4];
      #pragma unroll
      for(int i=0;i<4;i++){ const char* yp=cr+((i>>1)*32+g*8+tq)*64+(i&1)*32+tp*8; y2[i]=__builtin_bit_cast(u32x4,cat44(trread(yp),trread(yp+256))); }
      asm volatile("" : "+v"(y2[0]), "+v"(y2[1]), "+v"(y2[2]), "+v"(y2[3]) :: "memory");
      #pragma unroll
      for(int i=0;i<4;i++) *(u32x4*)(cr+((i&1)*16+fr)*128+((((i>>1)*4+g)^(fr&7))*16))=y2[i];
      asm volatile("" ::: "memory");
      char* op=out+fs0*4096+fd0*2; const unsigned oo=(unsigned)((l>>3)*16384+(l&7)*16);
      #pragma unroll
      for(int i=0;i<4;i++){
        const int row=4*(l>>3)+i;
        const u32x4 y4=*(const u32x4*)(cr+row*128+(((l&7)^(row&7))*16)), q4=qv[i]; u32x4 ov;
        #pragma unroll
        for(int e=0;e<4;e++) ov[e]=pack2(bflo(q4[e])*bflo(y4[e]), bfhi(q4[e])*bfhi(y4[e]));
        stout(op+i*4096, oo, ov);
      }
    }
    ENDW TS(6); TOUT(7);
  }
}

extern "C" void kernel_launch(void* const* d_in, const int* in_sizes, int n_in, void* d_out, int out_size, void* d_ws, size_t ws_size, hipStream_t stream){
  const u16* q=(const u16*)d_in[0]; const u16* k=(const u16*)d_in[1]; const u16* v=(const u16*)d_in[2];
  const u16* shk=(const u16*)d_in[3]; const u16* hk=(const u16*)d_in[4]; const u16* sD=(const u16*)d_in[5];
  const u16* x=(const u16*)d_in[6]; const u16* wqkv=(const u16*)d_in[7]; const u16* wo=(const u16*)d_in[8];
  fused_kernel<<<256,768,0,stream>>>(x,wqkv,wo,(char*)d_ws,(u16*)d_out,k,v,shk,hk,q,sD);
}
```

```cpp
#include <hip/hip_runtime.h>
#include <hip/hip_bf16.h>
#include <stdint.h>

typedef unsigned short u16;
typedef short bf16x8 __attribute__((ext_vector_type(8)));
typedef short s16x4 __attribute__((ext_vector_type(4)));
typedef float f32x2 __attribute__((ext_vector_type(2)));
typedef float f32x4 __attribute__((ext_vector_type(4)));
typedef float f32x8 __attribute__((ext_vector_type(8)));
typedef float f32x16 __attribute__((ext_vector_type(16)));
typedef unsigned u32x4 __attribute__((ext_vector_type(4)));
typedef unsigned u32x2 __attribute__((ext_vector_type(2)));
typedef __bf16 bf16x2_t __attribute__((ext_vector_type(2)));
#define LDS_AS __attribute__((address_space(3)))

__device__ __forceinline__ float bflo(unsigned x){ return __uint_as_float(x<<16); }
__device__ __forceinline__ float bfhi(unsigned x){ return __uint_as_float(x&0xffff0000u); }
__device__ __forceinline__ unsigned pack2(float lo, float hi){ return __builtin_bit_cast(unsigned, __builtin_convertvector(f32x2{lo,hi}, bf16x2_t)); }
__device__ __forceinline__ float mul1(float a, float b){ float r; asm("v_mul_f32 %0, %1, %2" : "=v"(r) : "v"(a), "v"(b)); return r; }
__device__ __forceinline__ float fma1(float a, float b, float c){ float r; asm("v_fma_f32 %0, %1, %2, %3" : "=v"(r) : "v"(a), "v"(b), "v"(c)); return r; }

__device__ __forceinline__ void glds(const void* sbase, unsigned voff, unsigned ldsaddr){
  asm volatile("s_mov_b32 m0, %2\n\ts_nop 0\n\tglobal_load_lds_dwordx4 %0, %1" :: "v"(voff), "s"(sbase), "s"(ldsaddr) : "memory");
}
__device__ __forceinline__ void gldsc(const void* sbase, unsigned voff, unsigned ldsaddr){
  asm volatile("s_mov_b32 m0, %2\n\ts_nop 0\n\tglobal_load_lds_dwordx4 %0, %1 sc1" :: "v"(voff), "s"(sbase), "s"(ldsaddr) : "memory");
}
__device__ __forceinline__ void touch(const void* sbase, unsigned voff, unsigned ldsaddr){
  asm volatile("s_mov_b32 m0, %2\n\ts_nop 0\n\tglobal_load_lds_dword %0, %1" :: "v"(voff), "s"(sbase), "s"(ldsaddr) : "memory");
}
__device__ __forceinline__ void st16c(void* p, u32x4 v){ asm volatile("global_store_dwordx4 %0, %1, off sc1\n\ts_nop 1" :: "v"(p), "v"(v) : "memory"); }
__device__ __forceinline__ void st16cs(void* sbase, unsigned voff, u32x4 v){ asm volatile("global_store_dwordx4 %0, %1, %2 sc1\n\ts_nop 1" :: "v"(voff), "v"(v), "s"(sbase) : "memory"); }
#define GLB_AS __attribute__((address_space(1)))
__device__ __forceinline__ unsigned ldflag(const unsigned* p){ return __hip_atomic_load((const GLB_AS unsigned*)p, __ATOMIC_RELAXED, __HIP_MEMORY_SCOPE_AGENT); }
__device__ __forceinline__ void stflag(unsigned* p, unsigned v){ __hip_atomic_store((GLB_AS unsigned*)p, v, __ATOMIC_RELAXED, __HIP_MEMORY_SCOPE_AGENT); }
template<int SLP> __device__ __forceinline__ void wait_flag(const unsigned* p, unsigned tag){
  int spins=0;
  while(true){ unsigned v=ldflag(p); if(__all(v==tag)) break; __builtin_amdgcn_s_sleep(SLP); if(++spins>(1<<21)) break; }
}
__device__ __forceinline__ unsigned lds_addr(const void* p){ return (unsigned)(size_t)(const LDS_AS void*)p; }
#define WAITV(n) asm volatile("s_waitcnt vmcnt(" #n ")":::"memory")
#define WAITL() asm volatile("s_waitcnt lgkmcnt(0)":::"memory")
#define SBAR() asm volatile("s_barrier":::"memory")
#define PUB(addr,val) asm volatile("ds_write_b32 %0, %1" :: "v"(addr), "v"(val) : "memory")
__device__ __forceinline__ s16x4 trread(const char* p){ return __builtin_amdgcn_ds_read_tr16_b64_v4i16((LDS_AS s16x4*)p); }
__device__ __forceinline__ bf16x8 cat44(s16x4 a, s16x4 b){ return __builtin_shufflevector(a,b,0,1,2,3,4,5,6,7); }
__device__ __forceinline__ int wswz(int row){ return ((row>>1)&1) | (((row>>3)&1)<<1); }
__device__ __forceinline__ u32x4 widen(u32x2 a, u32x2 b){
  auto r0=__builtin_amdgcn_permlane16_swap(a[0],b[0],false,false);
  auto r1=__builtin_amdgcn_permlane16_swap(a[1],b[1],false,false);
  return u32x4{r0[0],r1[0],r0[1],r1[1]};
}

constexpr int S=2048, DM=1024;
constexpr int WS=12288, XS=18432, X_OFF=6*WS;
constexpr int CREG=4608, C_OFF=131072;
constexpr int EX_OFF=C_OFF+4*CREG;
constexpr int SYN=EX_OFF+8192;
constexpr int SINK=SYN+256;
constexpr int ALL_LDS=SINK+768;
constexpr int F_PP=1024, F_O=4096, F_Y=8192;
#define KV0 1
#define KVS 3
#define LPR 2
#define CPR 2
#define GPR 1
#define CGATE 0
#define OSLP 2
#define YSLP 8
#define TPX 16
#define TPW 2
#define TWO 0
#define CVP 1
#define G2P 3
#define OUTC 1
#define ROT 0
#define WTL 2
#define PB2 1
#define PREM 9
#define KTH 1
#define YDEF 3
#define OPOL 1
#define CODEL 280
#define MIX 0
#define LOCA 3
#define XLOC 1
#define PGT 0
#define PGO 0
#define QREM 0
#define HLAST 1

#define N2_ASM \
  "v_mov_b32 v64, 0\n\t" \
  "v_mov_b32 v65, 0\n\t" \
  "v_mov_b32 v66, 0\n\t" \
  "v_mov_b32 v67, 0\n\t" \
  "v_mov_b32 v68, 0\n\t" \
  "v_mov_b32 v69, 0\n\t" \
  "v_mov_b32 v70, 0\n\t" \
  "v_mov_b32 v71, 0\n\t" \
  "v_mov_b32 v72, 0\n\t" \
  "v_mov_b32 v73, 0\n\t" \
  "v_mov_b32 v74, 0\n\t" \
  "v_mov_b32 v75, 0\n\t" \
  "v_mov_b32 v76, 0\n\t" \
  "v_mov_b32 v77, 0\n\t" \
  "v_mov_b32 v78, 0\n\t" \
  "v_mov_b32 v79, 0\n\t" \
  "v_mov_b32 v80, 0\n\t" \
  "v_mov_b32 v81, 0\n\t" \
  "v_mov_b32 v82, 0\n\t" \
  "v_mov_b32 v83, 0\n\t" \
  "v_mov_b32 v84, 0\n\t" \
  "v_mov_b32 v85, 0\n\t" \
  "v_mov_b32 v86, 0\n\t" \
  "v_mov_b32 v87, 0\n\t" \
  "v_mov_b32 v88, 0\n\t" \
  "v_mov_b32 v89, 0\n\t" \
  "v_mov_b32 v90, 0\n\t" \
  "v_mov_b32 v91, 0\n\t" \
  "v_mov_b32 v92, 0\n\t" \
  "v_mov_b32 v93, 0\n\t" \
  "v_mov_b32 v94, 0\n\t" \
  "v_mov_b32 v95, 0\n\t" \
  "v_mov_b32 v96, 0\n\t" \
  "v_mov_b32 v97, 0\n\t" \
  "v_mov_b32 v98, 0\n\t" \
  "v_mov_b32 v99, 0\n\t" \
  "v_mov_b32 v100, 0\n\t" \
  "v_mov_b32 v101, 0\n\t" \
  "v_mov_b32 v102, 0\n\t" \
  "v_mov_b32 v103, 0\n\t" \
  "v_mov_b32 v104, 0\n\t" \
  "v_mov_b32 v105, 0\n\t" \
  "v_mov_b32 v106, 0\n\t" \
  "v_mov_b32 v107, 0\n\t" \
  "v_mov_b32 v108, 0\n\t" \
  "v_mov_b32 v109, 0\n\t" \
  "v_mov_b32 v110, 0\n\t" \
  "v_mov_b32 v111, 0\n\t" \
  "v_mov_b32 v112, 0\n\t" \
  "v_mov_b32 v113, 0\n\t" \
  "v_mov_b32 v114, 0\n\t" \
  "v_mov_b32 v115, 0\n\t" \
  "v_mov_b32 v116, 0\n\t" \
  "v_mov_b32 v117, 0\n\t" \
  "v_mov_b32 v118, 0\n\t" \
  "v_mov_b32 v119, 0\n\t" \
  "v_mov_b32 v120, 0\n\t" \
  "v_mov_b32 v121, 0\n\t" \
  "v_mov_b32 v122, 0\n\t" \
  "v_mov_b32 v123, 0\n\t" \
  "v_mov_b32 v124, 0\n\t" \
  "v_mov_b32 v125, 0\n\t" \
  "v_mov_b32 v126, 0\n\t" \
  "v_mov_b32 v127, 0\n\t" \
  "v_mov_b32 v128, 0\n\t" \
  "v_mov_b32 v129, 0\n\t" \
  "v_mov_b32 v130, 0\n\t" \
  "v_mov_b32 v131, 0\n\t" \
  "v_mov_b32 v132, 0\n\t" \
  "v_mov_b32 v133, 0\n\t" \
  "v_mov_b32 v134, 0\n\t" \
  "v_mov_b32 v135, 0\n\t" \
  "v_mov_b32 v136, 0\n\t" \
  "v_mov_b32 v137, 0\n\t" \
  "v_mov_b32 v138, 0\n\t" \
  "v_mov_b32 v139, 0\n\t" \
  "v_mov_b32 v140, 0\n\t" \
  "v_mov_b32 v141, 0\n\t" \
  "v_mov_b32 v142, 0\n\t" \
  "v_mov_b32 v143, 0\n\t" \
  "v_mov_b32 v144, 0\n\t" \
  "v_mov_b32 v145, 0\n\t" \
  "v_mov_b32 v146, 0\n\t" \
  "v_mov_b32 v147, 0\n\t" \
  "v_mov_b32 v148, 0\n\t" \
  "v_mov_b32 v149, 0\n\t" \
  "v_mov_b32 v150, 0\n\t" \
  "v_mov_b32 v151, 0\n\t" \
  "v_mov_b32 v152, 0\n\t" \
  "v_mov_b32 v153, 0\n\t" \
  "v_mov_b32 v154, 0\n\t" \
  "v_mov_b32 v155, 0\n\t" \
  "v_mov_b32 v156, 0\n\t" \
  "v_mov_b32 v157, 0\n\t" \
  "v_mov_b32 v158, 0\n\t" \
  "v_mov_b32 v159, 0\n\t" \
  "v_mov_b32 v160, 0\n\t" \
  "v_mov_b32 v161, 0\n\t" \
  "v_mov_b32 v162, 0\n\t" \
  "v_mov_b32 v163, 0\n\t" \
  "v_mov_b32 v164, 0\n\t" \
  "v_mov_b32 v165, 0\n\t" \
  "v_mov_b32 v166, 0\n\t" \
  "v_mov_b32 v167, 0\n\t" \
  "s_mov_b32 s68, 0x50000\n\t" \
  "s_mov_b32 s69, 0x5550055\n\t" \
  "s_mov_b32 s70, 0xa0000\n\t" \
  "s_mov_b32 s71, 0xaaa00aa\n\t" \
  "s_mov_b32 s72, 0x150001\n\t" \
  "s_mov_b32 s73, 0x15550155\n\t" \
  "s_mov_b32 s74, 0x2a0002\n\t" \
  "s_mov_b32 s75, 0x2aaa02aa\n\t" \
  "s_mov_b32 s76, 0x55505555\n\t" \
  "s_mov_b32 s77, 0x50005500\n\t" \
  "s_mov_b32 s78, 0xaaa0aaaa\n\t" \
  "s_mov_b32 s79, 0xa000aa00\n\t" \
  "s_mov_b32 s80, 0x55405554\n\t" \
  "s_mov_b32 s81, 0x40005400\n\t" \
  "s_mov_b32 s82, 0xaa80aaa8\n\t" \
  "s_mov_b32 s83, 0x8000a800\n\t" \
  "s_mov_b32 s84, 0xaaaaaaaa\n\t" \
  "s_mov_b32 s85, 0xaaaaaaaa\n\t" \
  "v_add_u32 v60, 36864, v58\n\t" \
  "v_add_u32 v61, 36864, v59\n\t" \
  "s_mov_b32 %[it], 0\n\t" \
  "s_cmp_lg_u32 %[r], 0\n\t" \
  "s_barrier\n\t" \
  "s_cbranch_scc1 NB_%=\n\t" \
  "ds_read_b64_tr_b16 v[40:41], v60 offset:16384\n\t" \
  "ds_read_b64_tr_b16 v[42:43], v60 offset:16896\n\t" \
  "ds_read_b64_tr_b16 v[44:45], v60 offset:20480\n\t" \
  "ds_read_b64_tr_b16 v[46:47], v60 offset:20992\n\t" \
  "ds_read_b64_tr_b16 v[48:49], v61 offset:16384\n\t" \
  "ds_read_b64_tr_b16 v[50:51], v61 offset:16896\n\t" \
  "ds_read_b128 v[0:3], v56 offset:36864\n\t" \
  "ds_read_b128 v[4:7], v56 offset:38912\n\t" \
  "ds_read_b128 v[8:11], v56 offset:40960\n\t" \
  "ds_read_b128 v[12:15], v56 offset:43008\n\t" \
  "ds_read_b128 v[16:19], v56 offset:45056\n\t" \
  "s_waitcnt lgkmcnt(0)\n\t" \
  "s_branch N4a_%=\n\t" \
  "N0a_%=:\n\t" \
  "s_waitcnt lgkmcnt(4)\n\t" \
  "v_mfma_f32_16x16x32_bf16 v[160:163], v[40:43], v[0:3], v[160:163]\n\t" \
  "ds_read_b64_tr_b16 v[52:53], v59 offset:8192\n\t" \
  "v_mfma_f32_16x16x32_bf16 v[96:99], v[40:43], v[4:7], v[96:99]\n\t" \
  "ds_read_b64_tr_b16 v[54:55], v59 offset:8704\n\t" \
  "v_mfma_f32_16x16x32_bf16 v[104:107], v[40:43], v[8:11], v[104:107]\n\t" \
  "s_barrier\n\t" \
  "v_mfma_f32_16x16x32_bf16 v[112:115], v[40:43], v[12:15], v[112:115]\n\t" \
  "v_mfma_f32_16x16x32_bf16 v[120:123], v[40:43], v[16:19], v[120:123]\n\t" \
  "s_waitcnt lgkmcnt(4)\n\t" \
  "v_mfma_f32_16x16x32_bf16 v[128:131], v[4:7], v[44:47], v[128:131]\n\t" \
  "ds_read_b64_tr_b16 v[40:41], v58 offset:0\n\t" \
  "v_mfma_f32_16x16x32_bf16 v[136:139], v[8:11], v[44:47], v[136:139]\n\t" \
  "ds_read_b64_tr_b16 v[42:43], v58 offset:512\n\t" \
  "v_mfma_f32_16x16x32_bf16 v[144:147], v[12:15], v[44:47], v[144:147]\n\t" \
  "ds_read_b128 v[20:23], v57 offset:0\n\t" \
  "v_mfma_f32_16x16x32_bf16 v[152:155], v[16:19], v[44:47], v[152:155]\n\t" \
  "ds_read_b128 v[24:27], v57 offset:2048\n\t" \
  "s_waitcnt lgkmcnt(6)\n\t" \
  "v_mfma_f32_16x16x32_bf16 v[164:167], v[48:51], v[0:3], v[164:167]\n\t" \
  "ds_read_b64_tr_b16 v[44:45], v59 offset:0\n\t" \
  "v_mfma_f32_16x16x32_bf16 v[100:103], v[48:51], v[4:7], v[100:103]\n\t" \
  "ds_read_b64_tr_b16 v[46:47], v59 offset:512\n\t" \
  "v_mfma_f32_16x16x32_bf16 v[108:111], v[48:51], v[8:11], v[108:111]\n\t" \
  "ds_read_b128 v[28:31], v57 offset:4096\n\t" \
  "v_mfma_f32_16x16x32_bf16 v[116:119], v[48:51], v[12:15], v[116:119]\n\t" \
  "ds_read_b128 v[32:35], v57 offset:6144\n\t" \
  "v_mfma_f32_16x16x32_bf16 v[124:127], v[48:51], v[16:19], v[124:127]\n\t" \
  "s_waitcnt lgkmcnt(8)\n\t" \
  "v_mfma_f32_16x16x32_bf16 v[132:135], v[4:7], v[52:55], v[132:135]\n\t" \
  "ds_read_b64_tr_b16 v[48:49], v58 offset:16384\n\t" \
  "v_mfma_f32_16x16x32_bf16 v[140:143], v[8:11], v[52:55], v[140:143]\n\t" \
  "ds_read_b64_tr_b16 v[50:51], v58 offset:16896\n\t" \
  "v_mfma_f32_16x16x32_bf16 v[148:151], v[12:15], v[52:55], v[148:151]\n\t" \
  "ds_read_b128 v[36:39], v57 offset:8192\n\t" \
  "v_mfma_f32_16x16x32_bf16 v[156:159], v[16:19], v[52:55], v[156:159]\n\t" \
  "s_waitcnt lgkmcnt(9)\n\t" \
  "v_mfma_f32_16x16x32_bf16 v[64:67], v[40:43], v[4:7], v[64:67]\n\t" \
  "ds_read_b64_tr_b16 v[52:53], v58 offset:20480\n\t" \
  "v_mfma_f32_16x16x32_bf16 v[72:75], v[40:43], v[8:11], v[72:75]\n\t" \
  "ds_read_b64_tr_b16 v[54:55], v58 offset:20992\n\t" \
  "v_mfma_f32_16x16x32_bf16 v[80:83], v[40:43], v[12:15], v[80:83]\n\t" \
  "v_mfma_f32_16x16x32_bf16 v[88:91], v[40:43], v[16:19], v[88:91]\n\t" \
  "s_waitcnt lgkmcnt(7)\n\t" \
  "v_mfma_f32_16x16x32_bf16 v[68:71], v[44:47], v[4:7], v[68:71]\n\t" \
  "ds_read_b64_tr_b16 v[40:41], v59 offset:16384\n\t" \
  "v_mfma_f32_16x16x32_bf16 v[76:79], v[44:47], v[8:11], v[76:79]\n\t" \
  "ds_read_b64_tr_b16 v[42:43], v59 offset:16896\n\t" \
  "v_mfma_f32_16x16x32_bf16 v[84:87], v[44:47], v[12:15], v[84:87]\n\t" \
  "v_mfma_f32_16x16x32_bf16 v[92:95], v[44:47], v[16:19], v[92:95]\n\t" \
  "N1a_%=:\n\t" \
  "s_waitcnt lgkmcnt(4)\n\t" \
  "v_mfma_f32_16x16x32_bf16 v[160:163], v[48:51], v[20:23], v[160:163]\n\t" \
  "ds_read_b64_tr_b16 v[44:45], v59 offset:20480\n\t" \
  "v_mfma_f32_16x16x32_bf16 v[96:99], v[48:51], v[24:27], v[96:99]\n\t" \
  "ds_read_b64_tr_b16 v[46:47], v59 offset:20992\n\t" \
  "v_mfma_f32_16x16x32_bf16 v[104:107], v[48:51], v[28:31], v[104:107]\n\t" \
  "s_barrier\n\t" \
  "v_mfma_f32_16x16x32_bf16 v[112:115], v[48:51], v[32:35], v[112:115]\n\t" \
  "v_mfma_f32_16x16x32_bf16 v[120:123], v[48:51], v[36:39], v[120:123]\n\t" \
  "s_waitcnt lgkmcnt(4)\n\t" \
  "v_mfma_f32_16x16x32_bf16 v[128:131], v[24:27], v[52:55], v[128:131]\n\t" \
  "ds_read_b64_tr_b16 v[48:49], v58 offset:12288\n\t" \
  "v_mfma_f32_16x16x32_bf16 v[136:139], v[28:31], v[52:55], v[136:139]\n\t" \
  "ds_read_b64_tr_b16 v[50:51], v58 offset:12800\n\t" \
  "v_mfma_f32_16x16x32_bf16 v[144:147], v[32:35], v[52:55], v[144:147]\n\t" \
  "ds_read_b128 v[0:3], v56 offset:18432\n\t" \
  "v_mfma_f32_16x16x32_bf16 v[152:155], v[36:39], v[52:55], v[152:155]\n\t" \
  "ds_read_b128 v[4:7], v56 offset:20480\n\t" \
  "s_waitcnt lgkmcnt(6)\n\t" \
  "v_mfma_f32_16x16x32_bf16 v[164:167], v[40:43], v[20:23], v[164:167]\n\t" \
  "ds_read_b64_tr_b16 v[52:53], v59 offset:12288\n\t" \
  "v_mfma_f32_16x16x32_bf16 v[100:103], v[40:43], v[24:27], v[100:103]\n\t" \
  "ds_read_b64_tr_b16 v[54:55], v59 offset:12800\n\t" \
  "v_mfma_f32_16x16x32_bf16 v[108:111], v[40:43], v[28:31], v[108:111]\n\t" \
  "ds_read_b128 v[8:11], v56 offset:22528\n\t" \
  "v_mfma_f32_16x16x32_bf16 v[116:119], v[40:43], v[32:35], v[116:119]\n\t" \
  "ds_read_b128 v[12:15], v56 offset:24576\n\t" \
  "v_mfma_f32_16x16x32_bf16 v[124:127], v[40:43], v[36:39], v[124:127]\n\t" \
  "s_waitcnt lgkmcnt(8)\n\t" \
  "v_mfma_f32_16x16x32_bf16 v[132:135], v[24:27], v[44:47], v[132:135]\n\t" \
  "ds_read_b64_tr_b16 v[40:41], v58 offset:28672\n\t" \
  "v_mfma_f32_16x16x32_bf16 v[140:143], v[28:31], v[44:47], v[140:143]\n\t" \
  "ds_read_b64_tr_b16 v[42:43], v58 offset:29184\n\t" \
  "v_mfma_f32_16x16x32_bf16 v[148:151], v[32:35], v[44:47], v[148:151]\n\t" \
  "ds_read_b128 v[16:19], v56 offset:26624\n\t" \
  "v_mfma_f32_16x16x32_bf16 v[156:159], v[36:39], v[44:47], v[156:159]\n\t" \
  "s_waitcnt lgkmcnt(9)\n\t" \
  "v_mfma_f32_16x16x32_bf16 v[64:67], v[48:51], v[24:27], v[64:67]\n\t" \
  "ds_read_b64_tr_b16 v[44:45], v58 offset:32768\n\t" \
  "v_mfma_f32_16x16x32_bf16 v[72:75], v[48:51], v[28:31], v[72:75]\n\t" \
  "ds_read_b64_tr_b16 v[46:47], v58 offset:33280\n\t" \
  "v_mfma_f32_16x16x32_bf16 v[80:83], v[48:51], v[32:35], v[80:83]\n\t" \
  "v_mfma_f32_16x16x32_bf16 v[88:91], v[48:51], v[36:39], v[88:91]\n\t" \
  "s_waitcnt lgkmcnt(7)\n\t" \
  "v_mfma_f32_16x16x32_bf16 v[68:71], v[52:55], v[24:27], v[68:71]\n\t" \
  "ds_read_b64_tr_b16 v[48:49], v59 offset:28672\n\t" \
  "v_mfma_f32_16x16x32_bf16 v[76:79], v[52:55], v[28:31], v[76:79]\n\t" \
  "ds_read_b64_tr_b16 v[50:51], v59 offset:29184\n\t" \
  "v_mfma_f32_16x16x32_bf16 v[84:87], v[52:55], v[32:35], v[84:87]\n\t" \
  "v_mfma_f32_16x16x32_bf16 v[92:95], v[52:55], v[36:39], v[92:95]\n\t" \
  "N2a_%=:\n\t" \
  "s_waitcnt lgkmcnt(4)\n\t" \
  "v_mfma_f32_16x16x32_bf16 v[160:163], v[40:43], v[0:3], v[160:163]\n\t" \
  "ds_read_b64_tr_b16 v[52:53], v59 offset:32768\n\t" \
  "v_mfma_f32_16x16x32_bf16 v[96:99], v[40:43], v[4:7], v[96:99]\n\t" \
  "ds_read_b64_tr_b16 v[54:55], v59 offset:33280\n\t" \
  "v_mfma_f32_16x16x32_bf16 v[104:107], v[40:43], v[8:11], v[104:107]\n\t" \
  "s_barrier\n\t" \
  "v_mfma_f32_16x16x32_bf16 v[112:115], v[40:43], v[12:15], v[112:115]\n\t" \
  "v_mfma_f32_16x16x32_bf16 v[120:123], v[40:43], v[16:19], v[120:123]\n\t" \
  "s_waitcnt lgkmcnt(4)\n\t" \
  "v_mfma_f32_16x16x32_bf16 v[128:131], v[4:7], v[44:47], v[128:131]\n\t" \
  "ds_read_b64_tr_b16 v[40:41], v58 offset:24576\n\t" \
  "v_mfma_f32_16x16x32_bf16 v[136:139], v[8:11], v[44:47], v[136:139]\n\t" \
  "ds_read_b64_tr_b16 v[42:43], v58 offset:25088\n\t" \
  "v_mfma_f32_16x16x32_bf16 v[144:147], v[12:15], v[44:47], v[144:147]\n\t" \
  "ds_read_b128 v[20:23], v57 offset:18432\n\t" \
  "v_mfma_f32_16x16x32_bf16 v[152:155], v[16:19], v[44:47], v[152:155]\n\t" \
  "ds_read_b128 v[24:27], v57 offset:20480\n\t" \
  "s_waitcnt lgkmcnt(6)\n\t" \
  "v_mfma_f32_16x16x32_bf16 v[164:167], v[48:51], v[0:3], v[164:167]\n\t" \
  "ds_read_b64_tr_b16 v[44:45], v59 offset:24576\n\t" \
  "v_mfma_f32_16x16x32_bf16 v[100:103], v[48:51], v[4:7], v[100:103]\n\t" \
  "ds_read_b64_tr_b16 v[46:47], v59 offset:25088\n\t" \
  "v_mfma_f32_16x16x32_bf16 v[108:111], v[48:51], v[8:11], v[108:111]\n\t" \
  "ds_read_b128 v[28:31], v57 offset:22528\n\t" \
  "v_mfma_f32_16x16x32_bf16 v[116:119], v[48:51], v[12:15], v[116:119]\n\t" \
  "ds_read_b128 v[32:35], v57 offset:24576\n\t" \
  "v_mfma_f32_16x16x32_bf16 v[124:127], v[48:51], v[16:19], v[124:127]\n\t" \
  "s_waitcnt lgkmcnt(8)\n\t" \
  "v_mfma_f32_16x16x32_bf16 v[132:135], v[4:7], v[52:55], v[132:135]\n\t" \
  "ds_read_b64_tr_b16 v[48:49], v60 offset:4096\n\t" \
  "v_mfma_f32_16x16x32_bf16 v[140:143], v[8:11], v[52:55], v[140:143]\n\t" \
  "ds_read_b64_tr_b16 v[50:51], v60 offset:4608\n\t" \
  "v_mfma_f32_16x16x32_bf16 v[148:151], v[12:15], v[52:55], v[148:151]\n\t" \
  "ds_read_b128 v[36:39], v57 offset:26624\n\t" \
  "v_mfma_f32_16x16x32_bf16 v[156:159], v[16:19], v[52:55], v[156:159]\n\t" \
  "s_waitcnt lgkmcnt(9)\n\t" \
  "v_mfma_f32_16x16x32_bf16 v[64:67], v[40:43], v[4:7], v[64:67]\n\t" \
  "ds_read_b64_tr_b16 v[52:53], v60 offset:8192\n\t" \
  "v_mfma_f32_16x16x32_bf16 v[72:75], v[40:43], v[8:11], v[72:75]\n\t" \
  "ds_read_b64_tr_b16 v[54:55], v60 offset:8704\n\t" \
  "v_mfma_f32_16x16x32_bf16 v[80:83], v[40:43], v[12:15], v[80:83]\n\t" \
  "v_mfma_f32_16x16x32_bf16 v[88:91], v[40:43], v[16:19], v[88:91]\n\t" \
  "s_waitcnt lgkmcnt(7)\n\t" \
  "v_mfma_f32_16x16x32_bf16 v[68:71], v[44:47], v[4:7], v[68:71]\n\t" \
  "ds_read_b64_tr_b16 v[40:41], v61 offset:4096\n\t" \
  "v_mfma_f32_16x16x32_bf16 v[76:79], v[44:47], v[8:11], v[76:79]\n\t" \
  "ds_read_b64_tr_b16 v[42:43], v61 offset:4608\n\t" \
  "v_mfma_f32_16x16x32_bf16 v[84:87], v[44:47], v[12:15], v[84:87]\n\t" \
  "v_mfma_f32_16x16x32_bf16 v[92:95], v[44:47], v[16:19], v[92:95]\n\t" \
  "N3a_%=:\n\t" \
  "s_waitcnt lgkmcnt(4)\n\t" \
  "v_mfma_f32_16x16x32_bf16 v[160:163], v[48:51], v[20:23], v[160:163]\n\t" \
  "ds_read_b64_tr_b16 v[44:45], v61 offset:8192\n\t" \
  "v_mfma_f32_16x16x32_bf16 v[96:99], v[48:51], v[24:27], v[96:99]\n\t" \
  "ds_read_b64_tr_b16 v[46:47], v61 offset:8704\n\t" \
  "v_mfma_f32_16x16x32_bf16 v[104:107], v[48:51], v[28:31], v[104:107]\n\t" \
  "s_barrier\n\t" \
  "v_mfma_f32_16x16x32_bf16 v[112:115], v[48:51], v[32:35], v[112:115]\n\t" \
  "v_mfma_f32_16x16x32_bf16 v[120:123], v[48:51], v[36:39], v[120:123]\n\t" \
  "s_waitcnt lgkmcnt(4)\n\t" \
  "v_mfma_f32_16x16x32_bf16 v[128:131], v[24:27], v[52:55], v[128:131]\n\t" \
  "ds_read_b64_tr_b16 v[48:49], v60 offset:0\n\t" \
  "v_mfma_f32_16x16x32_bf16 v[136:139], v[28:31], v[52:55], v[136:139]\n\t" \
  "ds_read_b64_tr_b16 v[50:51], v60 offset:512\n\t" \
  "v_mfma_f32_16x16x32_bf16 v[144:147], v[32:35], v[52:55], v[144:147]\n\t" \
  "ds_read_b128 v[0:3], v56 offset:36864\n\t" \
  "v_mfma_f32_16x16x32_bf16 v[152:155], v[36:39], v[52:55], v[152:155]\n\t" \
  "ds_read_b128 v[4:7], v56 offset:38912\n\t" \
  "s_waitcnt lgkmcnt(6)\n\t" \
  "v_mfma_f32_16x16x32_bf16 v[164:167], v[40:43], v[20:23], v[164:167]\n\t" \
  "ds_read_b64_tr_b16 v[52:53], v61 offset:0\n\t" \
  "v_mfma_f32_16x16x32_bf16 v[100:103], v[40:43], v[24:27], v[100:103]\n\t" \
  "ds_read_b64_tr_b16 v[54:55], v61 offset:512\n\t" \
  "v_mfma_f32_16x16x32_bf16 v[108:111], v[40:43], v[28:31], v[108:111]\n\t" \
  "ds_read_b128 v[8:11], v56 offset:40960\n\t" \
  "v_mfma_f32_16x16x32_bf16 v[116:119], v[40:43], v[32:35], v[116:119]\n\t" \
  "ds_read_b128 v[12:15], v56 offset:43008\n\t" \
  "v_mfma_f32_16x16x32_bf16 v[124:127], v[40:43], v[36:39], v[124:127]\n\t" \
  "s_waitcnt lgkmcnt(8)\n\t" \
  "v_mfma_f32_16x16x32_bf16 v[132:135], v[24:27], v[44:47], v[132:135]\n\t" \
  "ds_read_b64_tr_b16 v[40:41], v60 offset:16384\n\t" \
  "v_mfma_f32_16x16x32_bf16 v[140:143], v[28:31], v[44:47], v[140:143]\n\t" \
  "ds_read_b64_tr_b16 v[42:43], v60 offset:16896\n\t" \
  "v_mfma_f32_16x16x32_bf16 v[148:151], v[32:35], v[44:47], v[148:151]\n\t" \
  "ds_read_b128 v[16:19], v56 offset:45056\n\t" \
  "v_mfma_f32_16x16x32_bf16 v[156:159], v[36:39], v[44:47], v[156:159]\n\t" \
  "s_waitcnt lgkmcnt(9)\n\t" \
  "v_mfma_f32_16x16x32_bf16 v[64:67], v[48:51], v[24:27], v[64:67]\n\t" \
  "ds_read_b64_tr_b16 v[44:45], v60 offset:20480\n\t" \
  "v_mfma_f32_16x16x32_bf16 v[72:75], v[48:51], v[28:31], v[72:75]\n\t" \
  "ds_read_b64_tr_b16 v[46:47], v60 offset:20992\n\t" \
  "v_mfma_f32_16x16x32_bf16 v[80:83], v[48:51], v[32:35], v[80:83]\n\t" \
  "v_mfma_f32_16x16x32_bf16 v[88:91], v[48:51], v[36:39], v[88:91]\n\t" \
  "s_waitcnt lgkmcnt(7)\n\t" \
  "v_mfma_f32_16x16x32_bf16 v[68:71], v[52:55], v[24:27], v[68:71]\n\t" \
  "ds_read_b64_tr_b16 v[48:49], v61 offset:16384\n\t" \
  "v_mfma_f32_16x16x32_bf16 v[76:79], v[52:55], v[28:31], v[76:79]\n\t" \
  "ds_read_b64_tr_b16 v[50:51], v61 offset:16896\n\t" \
  "v_mfma_f32_16x16x32_bf16 v[84:87], v[52:55], v[32:35], v[84:87]\n\t" \
  "v_mfma_f32_16x16x32_bf16 v[92:95], v[52:55], v[36:39], v[92:95]\n\t" \
  "N4a_%=:\n\t" \
  "s_waitcnt lgkmcnt(4)\n\t" \
  "v_mfma_f32_16x16x32_bf16 v[160:163], v[40:43], v[0:3], v[160:163]\n\t" \
  "ds_read_b64_tr_b16 v[52:53], v61 offset:20480\n\t" \
  "v_mfma_f32_16x16x32_bf16 v[96:99], v[40:43], v[4:7], v[96:99]\n\t" \
  "ds_read_b64_tr_b16 v[54:55], v61 offset:20992\n\t" \
  "v_mfma_f32_16x16x32_bf16 v[104:107], v[40:43], v[8:11], v[104:107]\n\t" \
  "s_barrier\n\t" \
  "v_mfma_f32_16x16x32_bf16 v[112:115], v[40:43], v[12:15], v[112:115]\n\t" \
  "v_mfma_f32_16x16x32_bf16 v[120:123], v[40:43], v[16:19], v[120:123]\n\t" \
  "s_waitcnt lgkmcnt(4)\n\t" \
  "v_mfma_f32_16x16x32_bf16 v[128:131], v[4:7], v[44:47], v[128:131]\n\t" \
  "ds_read_b64_tr_b16 v[40:41], v60 offset:12288\n\t" \
  "v_mfma_f32_16x16x32_bf16 v[136:139], v[8:11], v[44:47], v[136:139]\n\t" \
  "ds_read_b64_tr_b16 v[42:43], v60 offset:12800\n\t" \
  "v_mfma_f32_16x16x32_bf16 v[144:147], v[12:15], v[44:47], v[144:147]\n\t" \
  "ds_read_b128 v[20:23], v57 offset:36864\n\t" \
  "v_mfma_f32_16x16x32_bf16 v[152:155], v[16:19], v[44:47], v[152:155]\n\t" \
  "ds_read_b128 v[24:27], v57 offset:38912\n\t" \
  "s_waitcnt lgkmcnt(6)\n\t" \
  "v_mfma_f32_16x16x32_bf16 v[164:167], v[48:51], v[0:3], v[164:167]\n\t" \
  "ds_read_b64_tr_b16 v[44:45], v61 offset:12288\n\t" \
  "v_mfma_f32_16x16x32_bf16 v[100:103], v[48:51], v[4:7], v[100:103]\n\t" \
  "ds_read_b64_tr_b16 v[46:47], v61 offset:12800\n\t" \
  "v_mfma_f32_16x16x32_bf16 v[108:111], v[48:51], v[8:11], v[108:111]\n\t" \
  "ds_read_b128 v[28:31], v57 offset:40960\n\t" \
  "v_mfma_f32_16x16x32_bf16 v[116:119], v[48:51], v[12:15], v[116:119]\n\t" \
  "ds_read_b128 v[32:35], v57 offset:43008\n\t" \
  "v_mfma_f32_16x16x32_bf16 v[124:127], v[48:51], v[16:19], v[124:127]\n\t" \
  "s_waitcnt lgkmcnt(8)\n\t" \
  "v_mfma_f32_16x16x32_bf16 v[132:135], v[4:7], v[52:55], v[132:135]\n\t" \
  "ds_read_b64_tr_b16 v[48:49], v60 offset:28672\n\t" \
  "v_mfma_f32_16x16x32_bf16 v[140:143], v[8:11], v[52:55], v[140:143]\n\t" \
  "ds_read_b64_tr_b16 v[50:51], v60 offset:29184\n\t" \
  "v_mfma_f32_16x16x32_bf16 v[148:151], v[12:15], v[52:55], v[148:151]\n\t" \
  "ds_read_b128 v[36:39], v57 offset:45056\n\t" \
  "v_mfma_f32_16x16x32_bf16 v[156:159], v[16:19], v[52:55], v[156:159]\n\t" \
  "s_waitcnt lgkmcnt(9)\n\t" \
  "v_mfma_f32_16x16x32_bf16 v[64:67], v[40:43], v[4:7], v[64:67]\n\t" \
  "ds_read_b64_tr_b16 v[52:53], v60 offset:32768\n\t" \
  "v_mfma_f32_16x16x32_bf16 v[72:75], v[40:43], v[8:11], v[72:75]\n\t" \
  "ds_read_b64_tr_b16 v[54:55], v60 offset:33280\n\t" \
  "v_mfma_f32_16x16x32_bf16 v[80:83], v[40:43], v[12:15], v[80:83]\n\t" \
  "v_mfma_f32_16x16x32_bf16 v[88:91], v[40:43], v[16:19], v[88:91]\n\t" \
  "s_waitcnt lgkmcnt(7)\n\t" \
  "v_mfma_f32_16x16x32_bf16 v[68:71], v[44:47], v[4:7], v[68:71]\n\t" \
  "ds_read_b64_tr_b16 v[40:41], v61 offset:28672\n\t" \
  "v_mfma_f32_16x16x32_bf16 v[76:79], v[44:47], v[8:11], v[76:79]\n\t" \
  "ds_read_b64_tr_b16 v[42:43], v61 offset:29184\n\t" \
  "v_mfma_f32_16x16x32_bf16 v[84:87], v[44:47], v[12:15], v[84:87]\n\t" \
  "v_mfma_f32_16x16x32_bf16 v[92:95], v[44:47], v[16:19], v[92:95]\n\t" \
  "N5a_%=:\n\t" \
  "s_waitcnt lgkmcnt(4)\n\t" \
  "v_mfma_f32_16x16x32_bf16 v[160:163], v[48:51], v[20:23], v[160:163]\n\t" \
  "ds_read_b64_tr_b16 v[44:45], v61 offset:32768\n\t" \
  "v_mfma_f32_16x16x32_bf16 v[96:99], v[48:51], v[24:27], v[96:99]\n\t" \
  "ds_read_b64_tr_b16 v[46:47], v61 offset:33280\n\t" \
  "v_mfma_f32_16x16x32_bf16 v[104:107], v[48:51], v[28:31], v[104:107]\n\t" \
  "s_barrier\n\t" \
  "v_mfma_f32_16x16x32_bf16 v[112:115], v[48:51], v[32:35], v[112:115]\n\t" \
  "v_mfma_f32_16x16x32_bf16 v[120:123], v[48:51], v[36:39], v[120:123]\n\t" \
  "s_waitcnt lgkmcnt(4)\n\t" \
  "v_mfma_f32_16x16x32_bf16 v[128:131], v[24:27], v[52:55], v[128:131]\n\t" \
  "ds_read_b64_tr_b16 v[48:49], v60 offset:24576\n\t" \
  "v_mfma_f32_16x16x32_bf16 v[136:139], v[28:31], v[52:55], v[136:139]\n\t" \
  "ds_read_b64_tr_b16 v[50:51], v60 offset:25088\n\t" \
  "v_mfma_f32_16x16x32_bf16 v[144:147], v[32:35], v[52:55], v[144:147]\n\t" \
  "ds_read_b128 v[0:3], v56 offset:0\n\t" \
  "v_mfma_f32_16x16x32_bf16 v[152:155], v[36:39], v[52:55], v[152:155]\n\t" \
  "ds_read_b128 v[4:7], v56 offset:2048\n\t" \
  "s_waitcnt lgkmcnt(6)\n\t" \
  "v_mfma_f32_16x16x32_bf16 v[164:167], v[40:43], v[20:23], v[164:167]\n\t" \
  "ds_read_b64_tr_b16 v[52:53], v61 offset:24576\n\t" \
  "v_mfma_f32_16x16x32_bf16 v[100:103], v[40:43], v[24:27], v[100:103]\n\t" \
  "ds_read_b64_tr_b16 v[54:55], v61 offset:25088\n\t" \
  "v_mfma_f32_16x16x32_bf16 v[108:111], v[40:43], v[28:31], v[108:111]\n\t" \
  "ds_read_b128 v[8:11], v56 offset:4096\n\t" \
  "v_mfma_f32_16x16x32_bf16 v[116:119], v[40:43], v[32:35], v[116:119]\n\t" \
  "ds_read_b128 v[12:15], v56 offset:6144\n\t" \
  "v_mfma_f32_16x16x32_bf16 v[124:127], v[40:43], v[36:39], v[124:127]\n\t" \
  "s_waitcnt lgkmcnt(8)\n\t" \
  "v_mfma_f32_16x16x32_bf16 v[132:135], v[24:27], v[44:47], v[132:135]\n\t" \
  "ds_read_b64_tr_b16 v[40:41], v58 offset:4096\n\t" \
  "v_mfma_f32_16x16x32_bf16 v[140:143], v[28:31], v[44:47], v[140:143]\n\t" \
  "ds_read_b64_tr_b16 v[42:43], v58 offset:4608\n\t" \
  "v_mfma_f32_16x16x32_bf16 v[148:151], v[32:35], v[44:47], v[148:151]\n\t" \
  "ds_read_b128 v[16:19], v56 offset:8192\n\t" \
  "v_mfma_f32_16x16x32_bf16 v[156:159], v[36:39], v[44:47], v[156:159]\n\t" \
  "s_waitcnt lgkmcnt(9)\n\t" \
  "v_mfma_f32_16x16x32_bf16 v[64:67], v[48:51], v[24:27], v[64:67]\n\t" \
  "ds_read_b64_tr_b16 v[44:45], v58 offset:8192\n\t" \
  "v_mfma_f32_16x16x32_bf16 v[72:75], v[48:51], v[28:31], v[72:75]\n\t" \
  "ds_read_b64_tr_b16 v[46:47], v58 offset:8704\n\t" \
  "v_mfma_f32_16x16x32_bf16 v[80:83], v[48:51], v[32:35], v[80:83]\n\t" \
  "v_mfma_f32_16x16x32_bf16 v[88:91], v[48:51], v[36:39], v[88:91]\n\t" \
  "s_waitcnt lgkmcnt(7)\n\t" \
  "v_mfma_f32_16x16x32_bf16 v[68:71], v[52:55], v[24:27], v[68:71]\n\t" \
  "ds_read_b64_tr_b16 v[48:49], v59 offset:4096\n\t" \
  "v_mfma_f32_16x16x32_bf16 v[76:79], v[52:55], v[28:31], v[76:79]\n\t" \
  "ds_read_b64_tr_b16 v[50:51], v59 offset:4608\n\t" \
  "s_addk_i32 %[it], 1\n\t" \
  "v_mfma_f32_16x16x32_bf16 v[84:87], v[52:55], v[32:35], v[84:87]\n\t" \
  "s_cmp_lt_u32 %[it], 6\n\t" \
  "v_mfma_f32_16x16x32_bf16 v[92:95], v[52:55], v[36:39], v[92:95]\n\t" \
  "s_cbranch_scc1 N0a_%=\n\t" \
  "s_setprio 3\n\t" \
  "v_cvt_pk_bf16_f32 v120, v120, v121\n\t" \
  "v_cvt_pk_bf16_f32 v121, v122, v123\n\t" \
  "v_cvt_pk_bf16_f32 v122, v124, v125\n\t" \
  "v_cvt_pk_bf16_f32 v123, v126, v127\n\t" \
  "v_cvt_pk_bf16_f32 v28, v152, v153\n\t" \
  "v_cvt_pk_bf16_f32 v29, v154, v155\n\t" \
  "v_cvt_pk_bf16_f32 v38, v156, v157\n\t" \
  "v_cvt_pk_bf16_f32 v39, v158, v159\n\t" \
  "ds_write_b128 v63, v[120:123]\n\t" \
  "ds_write2_b64 v63, v[28:29], v[38:39] offset0:128 offset1:129\n\t" \
  "v_mov_b32 v57, %[fa]\n\t" \
  "v_mov_b32 v58, 1\n\t" \
  "s_mov_b64 exec, 1\n\t" \
  "ds_write_b32 v57, v58\n\t" \
  "s_mov_b64 exec, -1\n\t" \
  "v_cvt_pk_bf16_f32 v160, v160, v161\n\t" \
  "v_cvt_pk_bf16_f32 v161, v162, v163\n\t" \
  "v_cvt_pk_bf16_f32 v162, v164, v165\n\t" \
  "v_cvt_pk_bf16_f32 v163, v166, v167\n\t" \
  "s_branch NX_%=\n\t" \
  "NB_%=:\n\t" \
  "ds_read_b64_tr_b16 v[40:41], v60 offset:20480\n\t" \
  "ds_read_b64_tr_b16 v[42:43], v60 offset:20992\n\t" \
  "ds_read_b64_tr_b16 v[44:45], v60 offset:16384\n\t" \
  "ds_read_b64_tr_b16 v[46:47], v60 offset:16896\n\t" \
  "ds_read_b64_tr_b16 v[48:49], v61 offset:20480\n\t" \
  "ds_read_b64_tr_b16 v[50:51], v61 offset:20992\n\t" \
  "ds_read_b128 v[0:3], v56 offset:36864\n\t" \
  "ds_read_b128 v[4:7], v56 offset:47104\n\t" \
  "ds_read_b128 v[8:11], v56 offset:49152\n\t" \
  "ds_read_b128 v[12:15], v56 offset:51200\n\t" \
  "ds_read_b128 v[16:19], v56 offset:53248\n\t" \
  "s_waitcnt lgkmcnt(0)\n\t" \
  "s_branch N4b_%=\n\t" \
  "N0b_%=:\n\t" \
  "s_waitcnt lgkmcnt(4)\n\t" \
  "v_mfma_f32_16x16x32_bf16 v[160:163], v[0:3], v[40:43], v[160:163]\n\t" \
  "ds_read_b64_tr_b16 v[52:53], v59 offset:4096\n\t" \
  "v_mfma_f32_16x16x32_bf16 v[128:131], v[4:7], v[40:43], v[128:131]\n\t" \
  "ds_read_b64_tr_b16 v[54:55], v59 offset:4608\n\t" \
  "v_mfma_f32_16x16x32_bf16 v[136:139], v[8:11], v[40:43], v[136:139]\n\t" \
  "s_barrier\n\t" \
  "v_mfma_f32_16x16x32_bf16 v[144:147], v[12:15], v[40:43], v[144:147]\n\t" \
  "v_mfma_f32_16x16x32_bf16 v[152:155], v[16:19], v[40:43], v[152:155]\n\t" \
  "s_waitcnt lgkmcnt(4)\n\t" \
  "v_mfma_f32_16x16x32_bf16 v[96:99], v[44:47], v[4:7], v[96:99]\n\t" \
  "ds_read_b64_tr_b16 v[40:41], v58 offset:0\n\t" \
  "v_mfma_f32_16x16x32_bf16 v[104:107], v[44:47], v[8:11], v[104:107]\n\t" \
  "ds_read_b64_tr_b16 v[42:43], v58 offset:512\n\t" \
  "v_mfma_f32_16x16x32_bf16 v[112:115], v[44:47], v[12:15], v[112:115]\n\t" \
  "ds_read_b128 v[20:23], v57 offset:0\n\t" \
  "v_mfma_f32_16x16x32_bf16 v[120:123], v[44:47], v[16:19], v[120:123]\n\t" \
  "ds_read_b128 v[24:27], v57 offset:10240\n\t" \
  "s_waitcnt lgkmcnt(6)\n\t" \
  "v_mfma_f32_16x16x32_bf16 v[164:167], v[0:3], v[48:51], v[164:167]\n\t" \
  "ds_read_b64_tr_b16 v[44:45], v59 offset:0\n\t" \
  "v_mfma_f32_16x16x32_bf16 v[132:135], v[4:7], v[48:51], v[132:135]\n\t" \
  "ds_read_b64_tr_b16 v[46:47], v59 offset:512\n\t" \
  "v_mfma_f32_16x16x32_bf16 v[140:143], v[8:11], v[48:51], v[140:143]\n\t" \
  "ds_read_b128 v[28:31], v57 offset:12288\n\t" \
  "v_mfma_f32_16x16x32_bf16 v[148:151], v[12:15], v[48:51], v[148:151]\n\t" \
  "ds_read_b128 v[32:35], v57 offset:14336\n\t" \
  "v_mfma_f32_16x16x32_bf16 v[156:159], v[16:19], v[48:51], v[156:159]\n\t" \
  "s_waitcnt lgkmcnt(8)\n\t" \
  "v_mfma_f32_16x16x32_bf16 v[100:103], v[52:55], v[4:7], v[100:103]\n\t" \
  "ds_read_b64_tr_b16 v[48:49], v58 offset:20480\n\t" \
  "v_mfma_f32_16x16x32_bf16 v[108:111], v[52:55], v[8:11], v[108:111]\n\t" \
  "ds_read_b64_tr_b16 v[50:51], v58 offset:20992\n\t" \
  "v_mfma_f32_16x16x32_bf16 v[116:119], v[52:55], v[12:15], v[116:119]\n\t" \
  "ds_read_b128 v[36:39], v57 offset:16384\n\t" \
  "v_mfma_f32_16x16x32_bf16 v[124:127], v[52:55], v[16:19], v[124:127]\n\t" \
  "s_waitcnt lgkmcnt(9)\n\t" \
  "v_mfma_f32_16x16x32_bf16 v[64:67], v[40:43], v[4:7], v[64:67]\n\t" \
  "ds_read_b64_tr_b16 v[52:53], v58 offset:16384\n\t" \
  "v_mfma_f32_16x16x32_bf16 v[72:75], v[40:43], v[8:11], v[72:75]\n\t" \
  "ds_read_b64_tr_b16 v[54:55], v58 offset:16896\n\t" \
  "v_mfma_f32_16x16x32_bf16 v[80:83], v[40:43], v[12:15], v[80:83]\n\t" \
  "v_mfma_f32_16x16x32_bf16 v[88:91], v[40:43], v[16:19], v[88:91]\n\t" \
  "s_waitcnt lgkmcnt(7)\n\t" \
  "v_mfma_f32_16x16x32_bf16 v[68:71], v[44:47], v[4:7], v[68:71]\n\t" \
  "ds_read_b64_tr_b16 v[40:41], v59 offset:20480\n\t" \
  "v_mfma_f32_16x16x32_bf16 v[76:79], v[44:47], v[8:11], v[76:79]\n\t" \
  "ds_read_b64_tr_b16 v[42:43], v59 offset:20992\n\t" \
  "v_mfma_f32_16x16x32_bf16 v[84:87], v[44:47], v[12:15], v[84:87]\n\t" \
  "v_mfma_f32_16x16x32_bf16 v[92:95], v[44:47], v[16:19], v[92:95]\n\t" \
  "N1b_%=:\n\t" \
  "s_waitcnt lgkmcnt(4)\n\t" \
  "v_mfma_f32_16x16x32_bf16 v[160:163], v[20:23], v[48:51], v[160:163]\n\t" \
  "ds_read_b64_tr_b16 v[44:45], v59 offset:16384\n\t" \
  "v_mfma_f32_16x16x32_bf16 v[128:131], v[24:27], v[48:51], v[128:131]\n\t" \
  "ds_read_b64_tr_b16 v[46:47], v59 offset:16896\n\t" \
  "v_mfma_f32_16x16x32_bf16 v[136:139], v[28:31], v[48:51], v[136:139]\n\t" \
  "s_barrier\n\t" \
  "v_mfma_f32_16x16x32_bf16 v[144:147], v[32:35], v[48:51], v[144:147]\n\t" \
  "v_mfma_f32_16x16x32_bf16 v[152:155], v[36:39], v[48:51], v[152:155]\n\t" \
  "s_waitcnt lgkmcnt(4)\n\t" \
  "v_mfma_f32_16x16x32_bf16 v[96:99], v[52:55], v[24:27], v[96:99]\n\t" \
  "ds_read_b64_tr_b16 v[48:49], v58 offset:12288\n\t" \
  "v_mfma_f32_16x16x32_bf16 v[104:107], v[52:55], v[28:31], v[104:107]\n\t" \
  "ds_read_b64_tr_b16 v[50:51], v58 offset:12800\n\t" \
  "v_mfma_f32_16x16x32_bf16 v[112:115], v[52:55], v[32:35], v[112:115]\n\t" \
  "ds_read_b128 v[0:3], v56 offset:18432\n\t" \
  "v_mfma_f32_16x16x32_bf16 v[120:123], v[52:55], v[36:39], v[120:123]\n\t" \
  "ds_read_b128 v[4:7], v56 offset:28672\n\t" \
  "s_waitcnt lgkmcnt(6)\n\t" \
  "v_mfma_f32_16x16x32_bf16 v[164:167], v[20:23], v[40:43], v[164:167]\n\t" \
  "ds_read_b64_tr_b16 v[52:53], v59 offset:12288\n\t" \
  "v_mfma_f32_16x16x32_bf16 v[132:135], v[24:27], v[40:43], v[132:135]\n\t" \
  "ds_read_b64_tr_b16 v[54:55], v59 offset:12800\n\t" \
  "v_mfma_f32_16x16x32_bf16 v[140:143], v[28:31], v[40:43], v[140:143]\n\t" \
  "ds_read_b128 v[8:11], v56 offset:30720\n\t" \
  "v_mfma_f32_16x16x32_bf16 v[148:151], v[32:35], v[40:43], v[148:151]\n\t" \
  "ds_read_b128 v[12:15], v56 offset:32768\n\t" \
  "v_mfma_f32_16x16x32_bf16 v[156:159], v[36:39], v[40:43], v[156:159]\n\t" \
  "s_waitcnt lgkmcnt(8)\n\t" \
  "v_mfma_f32_16x16x32_bf16 v[100:103], v[44:47], v[24:27], v[100:103]\n\t" \
  "ds_read_b64_tr_b16 v[40:41], v58 offset:32768\n\t" \
  "v_mfma_f32_16x16x32_bf16 v[108:111], v[44:47], v[28:31], v[108:111]\n\t" \
  "ds_read_b64_tr_b16 v[42:43], v58 offset:33280\n\t" \
  "v_mfma_f32_16x16x32_bf16 v[116:119], v[44:47], v[32:35], v[116:119]\n\t" \
  "ds_read_b128 v[16:19], v56 offset:34816\n\t" \
  "v_mfma_f32_16x16x32_bf16 v[124:127], v[44:47], v[36:39], v[124:127]\n\t" \
  "s_waitcnt lgkmcnt(9)\n\t" \
  "v_mfma_f32_16x16x32_bf16 v[64:67], v[48:51], v[24:27], v[64:67]\n\t" \
  "ds_read_b64_tr_b16 v[44:45], v58 offset:28672\n\t" \
  "v_mfma_f32_16x16x32_bf16 v[72:75], v[48:51], v[28:31], v[72:75]\n\t" \
  "ds_read_b64_tr_b16 v[46:47], v58 offset:29184\n\t" \
  "v_mfma_f32_16x16x32_bf16 v[80:83], v[48:51], v[32:35], v[80:83]\n\t" \
  "v_mfma_f32_16x16x32_bf16 v[88:91], v[48:51], v[36:39], v[88:91]\n\t" \
  "s_waitcnt lgkmcnt(7)\n\t" \
  "v_mfma_f32_16x16x32_bf16 v[68:71], v[52:55], v[24:27], v[68:71]\n\t" \
  "ds_read_b64_tr_b16 v[48:49], v59 offset:32768\n\t" \
  "v_mfma_f32_16x16x32_bf16 v[76:79], v[52:55], v[28:31], v[76:79]\n\t" \
  "ds_read_b64_tr_b16 v[50:51], v59 offset:33280\n\t" \
  "v_mfma_f32_16x16x32_bf16 v[84:87], v[52:55], v[32:35], v[84:87]\n\t" \
  "v_mfma_f32_16x16x32_bf16 v[92:95], v[52:55], v[36:39], v[92:95]\n\t" \
  "N2b_%=:\n\t" \
  "s_waitcnt lgkmcnt(4)\n\t" \
  "v_mfma_f32_16x16x32_bf16 v[160:163], v[0:3], v[40:43], v[160:163]\n\t" \
  "ds_read_b64_tr_b16 v[52:53], v59 offset:28672\n\t" \
  "v_mfma_f32_16x16x32_bf16 v[128:131], v[4:7], v[40:43], v[128:131]\n\t" \
  "ds_read_b64_tr_b16 v[54:55], v59 offset:29184\n\t" \
  "v_mfma_f32_16x16x32_bf16 v[136:139], v[8:11], v[40:43], v[136:139]\n\t" \
  "s_barrier\n\t" \
  "v_mfma_f32_16x16x32_bf16 v[144:147], v[12:15], v[40:43], v[144:147]\n\t" \
  "v_mfma_f32_16x16x32_bf16 v[152:155], v[16:19], v[40:43], v[152:155]\n\t" \
  "s_waitcnt lgkmcnt(4)\n\t" \
  "v_mfma_f32_16x16x32_bf16 v[96:99], v[44:47], v[4:7], v[96:99]\n\t" \
  "ds_read_b64_tr_b16 v[40:41], v58 offset:24576\n\t" \
  "v_mfma_f32_16x16x32_bf16 v[104:107], v[44:47], v[8:11], v[104:107]\n\t" \
  "ds_read_b64_tr_b16 v[42:43], v58 offset:25088\n\t" \
  "v_mfma_f32_16x16x32_bf16 v[112:115], v[44:47], v[12:15], v[112:115]\n\t" \
  "ds_read_b128 v[20:23], v57 offset:18432\n\t" \
  "v_mfma_f32_16x16x32_bf16 v[120:123], v[44:47], v[16:19], v[120:123]\n\t" \
  "ds_read_b128 v[24:27], v57 offset:28672\n\t" \
  "s_waitcnt lgkmcnt(6)\n\t" \
  "v_mfma_f32_16x16x32_bf16 v[164:167], v[0:3], v[48:51], v[164:167]\n\t" \
  "ds_read_b64_tr_b16 v[44:45], v59 offset:24576\n\t" \
  "v_mfma_f32_16x16x32_bf16 v[132:135], v[4:7], v[48:51], v[132:135]\n\t" \
  "ds_read_b64_tr_b16 v[46:47], v59 offset:25088\n\t" \
  "v_mfma_f32_16x16x32_bf16 v[140:143], v[8:11], v[48:51], v[140:143]\n\t" \
  "ds_read_b128 v[28:31], v57 offset:30720\n\t" \
  "v_mfma_f32_16x16x32_bf16 v[148:151], v[12:15], v[48:51], v[148:151]\n\t" \
  "ds_read_b128 v[32:35], v57 offset:32768\n\t" \
  "v_mfma_f32_16x16x32_bf16 v[156:159], v[16:19], v[48:51], v[156:159]\n\t" \
  "s_waitcnt lgkmcnt(8)\n\t" \
  "v_mfma_f32_16x16x32_bf16 v[100:103], v[52:55], v[4:7], v[100:103]\n\t" \
  "ds_read_b64_tr_b16 v[48:49], v60 offset:8192\n\t" \
  "v_mfma_f32_16x16x32_bf16 v[108:111], v[52:55], v[8:11], v[108:111]\n\t" \
  "ds_read_b64_tr_b16 v[50:51], v60 offset:8704\n\t" \
  "v_mfma_f32_16x16x32_bf16 v[116:119], v[52:55], v[12:15], v[116:119]\n\t" \
  "ds_read_b128 v[36:39], v57 offset:34816\n\t" \
  "v_mfma_f32_16x16x32_bf16 v[124:127], v[52:55], v[16:19], v[124:127]\n\t" \
  "s_waitcnt lgkmcnt(9)\n\t" \
  "v_mfma_f32_16x16x32_bf16 v[64:67], v[40:43], v[4:7], v[64:67]\n\t" \
  "ds_read_b64_tr_b16 v[52:53], v60 offset:4096\n\t" \
  "v_mfma_f32_16x16x32_bf16 v[72:75], v[40:43], v[8:11], v[72:75]\n\t" \
  "ds_read_b64_tr_b16 v[54:55], v60 offset:4608\n\t" \
  "v_mfma_f32_16x16x32_bf16 v[80:83], v[40:43], v[12:15], v[80:83]\n\t" \
  "v_mfma_f32_16x16x32_bf16 v[88:91], v[40:43], v[16:19], v[88:91]\n\t" \
  "s_waitcnt lgkmcnt(7)\n\t" \
  "v_mfma_f32_16x16x32_bf16 v[68:71], v[44:47], v[4:7], v[68:71]\n\t" \
  "ds_read_b64_tr_b16 v[40:41], v61 offset:8192\n\t" \
  "v_mfma_f32_16x16x32_bf16 v[76:79], v[44:47], v[8:11], v[76:79]\n\t" \
  "ds_read_b64_tr_b16 v[42:43], v61 offset:8704\n\t" \
  "v_mfma_f32_16x16x32_bf16 v[84:87], v[44:47], v[12:15], v[84:87]\n\t" \
  "v_mfma_f32_16x16x32_bf16 v[92:95], v[44:47], v[16:19], v[92:95]\n\t" \
  "N3b_%=:\n\t" \
  "s_waitcnt lgkmcnt(4)\n\t" \
  "v_mfma_f32_16x16x32_bf16 v[160:163], v[20:23], v[48:51], v[160:163]\n\t" \
  "ds_read_b64_tr_b16 v[44:45], v61 offset:4096\n\t" \
  "v_mfma_f32_16x16x32_bf16 v[128:131], v[24:27], v[48:51], v[128:131]\n\t" \
  "ds_read_b64_tr_b16 v[46:47], v61 offset:4608\n\t" \
  "v_mfma_f32_16x16x32_bf16 v[136:139], v[28:31], v[48:51], v[136:139]\n\t" \
  "s_barrier\n\t" \
  "v_mfma_f32_16x16x32_bf16 v[144:147], v[32:35], v[48:51], v[144:147]\n\t" \
  "v_mfma_f32_16x16x32_bf16 v[152:155], v[36:39], v[48:51], v[152:155]\n\t" \
  "s_waitcnt lgkmcnt(4)\n\t" \
  "v_mfma_f32_16x16x32_bf16 v[96:99], v[52:55], v[24:27], v[96:99]\n\t" \
  "ds_read_b64_tr_b16 v[48:49], v60 offset:0\n\t" \
  "v_mfma_f32_16x16x32_bf16 v[104:107], v[52:55], v[28:31], v[104:107]\n\t" \
  "ds_read_b64_tr_b16 v[50:51], v60 offset:512\n\t" \
  "v_mfma_f32_16x16x32_bf16 v[112:115], v[52:55], v[32:35], v[112:115]\n\t" \
  "ds_read_b128 v[0:3], v56 offset:36864\n\t" \
  "v_mfma_f32_16x16x32_bf16 v[120:123], v[52:55], v[36:39], v[120:123]\n\t" \
  "ds_read_b128 v[4:7], v56 offset:47104\n\t" \
  "s_waitcnt lgkmcnt(6)\n\t" \
  "v_mfma_f32_16x16x32_bf16 v[164:167], v[20:23], v[40:43], v[164:167]\n\t" \
  "ds_read_b64_tr_b16 v[52:53], v61 offset:0\n\t" \
  "v_mfma_f32_16x16x32_bf16 v[132:135], v[24:27], v[40:43], v[132:135]\n\t" \
  "ds_read_b64_tr_b16 v[54:55], v61 offset:512\n\t" \
  "v_mfma_f32_16x16x32_bf16 v[140:143], v[28:31], v[40:43], v[140:143]\n\t" \
  "ds_read_b128 v[8:11], v56 offset:49152\n\t" \
  "v_mfma_f32_16x16x32_bf16 v[148:151], v[32:35], v[40:43], v[148:151]\n\t" \
  "ds_read_b128 v[12:15], v56 offset:51200\n\t" \
  "v_mfma_f32_16x16x32_bf16 v[156:159], v[36:39], v[40:43], v[156:159]\n\t" \
  "s_waitcnt lgkmcnt(8)\n\t" \
  "v_mfma_f32_16x16x32_bf16 v[100:103], v[44:47], v[24:27], v[100:103]\n\t" \
  "ds_read_b64_tr_b16 v[40:41], v60 offset:20480\n\t" \
  "v_mfma_f32_16x16x32_bf16 v[108:111], v[44:47], v[28:31], v[108:111]\n\t" \
  "ds_read_b64_tr_b16 v[42:43], v60 offset:20992\n\t" \
  "v_mfma_f32_16x16x32_bf16 v[116:119], v[44:47], v[32:35], v[116:119]\n\t" \
  "ds_read_b128 v[16:19], v56 offset:53248\n\t" \
  "v_mfma_f32_16x16x32_bf16 v[124:127], v[44:47], v[36:39], v[124:127]\n\t" \
  "s_waitcnt lgkmcnt(9)\n\t" \
  "v_mfma_f32_16x16x32_bf16 v[64:67], v[48:51], v[24:27], v[64:67]\n\t" \
  "ds_read_b64_tr_b16 v[44:45], v60 offset:16384\n\t" \
  "v_mfma_f32_16x16x32_bf16 v[72:75], v[48:51], v[28:31], v[72:75]\n\t" \
  "ds_read_b64_tr_b16 v[46:47], v60 offset:16896\n\t" \
  "v_mfma_f32_16x16x32_bf16 v[80:83], v[48:51], v[32:35], v[80:83]\n\t" \
  "v_mfma_f32_16x16x32_bf16 v[88:91], v[48:51], v[36:39], v[88:91]\n\t" \
  "s_waitcnt lgkmcnt(7)\n\t" \
  "v_mfma_f32_16x16x32_bf16 v[68:71], v[52:55], v[24:27], v[68:71]\n\t" \
  "ds_read_b64_tr_b16 v[48:49], v61 offset:20480\n\t" \
  "v_mfma_f32_16x16x32_bf16 v[76:79], v[52:55], v[28:31], v[76:79]\n\t" \
  "ds_read_b64_tr_b16 v[50:51], v61 offset:20992\n\t" \
  "v_mfma_f32_16x16x32_bf16 v[84:87], v[52:55], v[32:35], v[84:87]\n\t" \
  "v_mfma_f32_16x16x32_bf16 v[92:95], v[52:55], v[36:39], v[92:95]\n\t" \
  "N4b_%=:\n\t" \
  "s_waitcnt lgkmcnt(4)\n\t" \
  "v_mfma_f32_16x16x32_bf16 v[160:163], v[0:3], v[40:43], v[160:163]\n\t" \
  "ds_read_b64_tr_b16 v[52:53], v61 offset:16384\n\t" \
  "v_mfma_f32_16x16x32_bf16 v[128:131], v[4:7], v[40:43], v[128:131]\n\t" \
  "ds_read_b64_tr_b16 v[54:55], v61 offset:16896\n\t" \
  "v_mfma_f32_16x16x32_bf16 v[136:139], v[8:11], v[40:43], v[136:139]\n\t" \
  "s_barrier\n\t" \
  "v_mfma_f32_16x16x32_bf16 v[144:147], v[12:15], v[40:43], v[144:147]\n\t" \
  "v_mfma_f32_16x16x32_bf16 v[152:155], v[16:19], v[40:43], v[152:155]\n\t" \
  "s_waitcnt lgkmcnt(4)\n\t" \
  "v_mfma_f32_16x16x32_bf16 v[96:99], v[44:47], v[4:7], v[96:99]\n\t" \
  "ds_read_b64_tr_b16 v[40:41], v60 offset:12288\n\t" \
  "v_mfma_f32_16x16x32_bf16 v[104:107], v[44:47], v[8:11], v[104:107]\n\t" \
  "ds_read_b64_tr_b16 v[42:43], v60 offset:12800\n\t" \
  "v_mfma_f32_16x16x32_bf16 v[112:115], v[44:47], v[12:15], v[112:115]\n\t" \
  "ds_read_b128 v[20:23], v57 offset:36864\n\t" \
  "v_mfma_f32_16x16x32_bf16 v[120:123], v[44:47], v[16:19], v[120:123]\n\t" \
  "ds_read_b128 v[24:27], v57 offset:47104\n\t" \
  "s_waitcnt lgkmcnt(6)\n\t" \
  "v_mfma_f32_16x16x32_bf16 v[164:167], v[0:3], v[48:51], v[164:167]\n\t" \
  "ds_read_b64_tr_b16 v[44:45], v61 offset:12288\n\t" \
  "v_mfma_f32_16x16x32_bf16 v[132:135], v[4:7], v[48:51], v[132:135]\n\t" \
  "ds_read_b64_tr_b16 v[46:47], v61 offset:12800\n\t" \
  "v_mfma_f32_16x16x32_bf16 v[140:143], v[8:11], v[48:51], v[140:143]\n\t" \
  "ds_read_b128 v[28:31], v57 offset:49152\n\t" \
  "v_mfma_f32_16x16x32_bf16 v[148:151], v[12:15], v[48:51], v[148:151]\n\t" \
  "ds_read_b128 v[32:35], v57 offset:51200\n\t" \
  "v_mfma_f32_16x16x32_bf16 v[156:159], v[16:19], v[48:51], v[156:159]\n\t" \
  "s_waitcnt lgkmcnt(8)\n\t" \
  "v_mfma_f32_16x16x32_bf16 v[100:103], v[52:55], v[4:7], v[100:103]\n\t" \
  "ds_read_b64_tr_b16 v[48:49], v60 offset:32768\n\t" \
  "v_mfma_f32_16x16x32_bf16 v[108:111], v[52:55], v[8:11], v[108:111]\n\t" \
  "ds_read_b64_tr_b16 v[50:51], v60 offset:33280\n\t" \
  "v_mfma_f32_16x16x32_bf16 v[116:119], v[52:55], v[12:15], v[116:119]\n\t" \
  "ds_read_b128 v[36:39], v57 offset:53248\n\t" \
  "v_mfma_f32_16x16x32_bf16 v[124:127], v[52:55], v[16:19], v[124:127]\n\t" \
  "s_waitcnt lgkmcnt(9)\n\t" \
  "v_mfma_f32_16x16x32_bf16 v[64:67], v[40:43], v[4:7], v[64:67]\n\t" \
  "ds_read_b64_tr_b16 v[52:53], v60 offset:28672\n\t" \
  "v_mfma_f32_16x16x32_bf16 v[72:75], v[40:43], v[8:11], v[72:75]\n\t" \
  "ds_read_b64_tr_b16 v[54:55], v60 offset:29184\n\t" \
  "v_mfma_f32_16x16x32_bf16 v[80:83], v[40:43], v[12:15], v[80:83]\n\t" \
  "v_mfma_f32_16x16x32_bf16 v[88:91], v[40:43], v[16:19], v[88:91]\n\t" \
  "s_waitcnt lgkmcnt(7)\n\t" \
  "v_mfma_f32_16x16x32_bf16 v[68:71], v[44:47], v[4:7], v[68:71]\n\t" \
  "ds_read_b64_tr_b16 v[40:41], v61 offset:32768\n\t" \
  "v_mfma_f32_16x16x32_bf16 v[76:79], v[44:47], v[8:11], v[76:79]\n\t" \
  "ds_read_b64_tr_b16 v[42:43], v61 offset:33280\n\t" \
  "v_mfma_f32_16x16x32_bf16 v[84:87], v[44:47], v[12:15], v[84:87]\n\t" \
  "v_mfma_f32_16x16x32_bf16 v[92:95], v[44:47], v[16:19], v[92:95]\n\t" \
  "N5b_%=:\n\t" \
  "s_waitcnt lgkmcnt(4)\n\t" \
  "v_mfma_f32_16x16x32_bf16 v[160:163], v[20:23], v[48:51], v[160:163]\n\t" \
  "ds_read_b64_tr_b16 v[44:45], v61 offset:28672\n\t" \
  "v_mfma_f32_16x16x32_bf16 v[128:131], v[24:27], v[48:51], v[128:131]\n\t" \
  "ds_read_b64_tr_b16 v[46:47], v61 offset:29184\n\t" \
  "v_mfma_f32_16x16x32_bf16 v[136:139], v[28:31], v[48:51], v[136:139]\n\t" \
  "s_barrier\n\t" \
  "v_mfma_f32_16x16x32_bf16 v[144:147], v[32:35], v[48:51], v[144:147]\n\t" \
  "v_mfma_f32_16x16x32_bf16 v[152:155], v[36:39], v[48:51], v[152:155]\n\t" \
  "s_waitcnt lgkmcnt(4)\n\t" \
  "v_mfma_f32_16x16x32_bf16 v[96:99], v[52:55], v[24:27], v[96:99]\n\t" \
  "ds_read_b64_tr_b16 v[48:49], v60 offset:24576\n\t" \
  "v_mfma_f32_16x16x32_bf16 v[104:107], v[52:55], v[28:31], v[104:107]\n\t" \
  "ds_read_b64_tr_b16 v[50:51], v60 offset:25088\n\t" \
  "v_mfma_f32_16x16x32_bf16 v[112:115], v[52:55], v[32:35], v[112:115]\n\t" \
  "ds_read_b128 v[0:3], v56 offset:0\n\t" \
  "v_mfma_f32_16x16x32_bf16 v[120:123], v[52:55], v[36:39], v[120:123]\n\t" \
  "ds_read_b128 v[4:7], v56 offset:10240\n\t" \
  "s_waitcnt lgkmcnt(6)\n\t" \
  "v_mfma_f32_16x16x32_bf16 v[164:167], v[20:23], v[40:43], v[164:167]\n\t" \
  "ds_read_b64_tr_b16 v[52:53], v61 offset:24576\n\t" \
  "v_mfma_f32_16x16x32_bf16 v[132:135], v[24:27], v[40:43], v[132:135]\n\t" \
  "ds_read_b64_tr_b16 v[54:55], v61 offset:25088\n\t" \
  "v_mfma_f32_16x16x32_bf16 v[140:143], v[28:31], v[40:43], v[140:143]\n\t" \
  "ds_read_b128 v[8:11], v56 offset:12288\n\t" \
  "v_mfma_f32_16x16x32_bf16 v[148:151], v[32:35], v[40:43], v[148:151]\n\t" \
  "ds_read_b128 v[12:15], v56 offset:14336\n\t" \
  "v_mfma_f32_16x16x32_bf16 v[156:159], v[36:39], v[40:43], v[156:159]\n\t" \
  "s_waitcnt lgkmcnt(8)\n\t" \
  "v_mfma_f32_16x16x32_bf16 v[100:103], v[44:47], v[24:27], v[100:103]\n\t" \
  "ds_read_b64_tr_b16 v[40:41], v58 offset:8192\n\t" \
  "v_mfma_f32_16x16x32_bf16 v[108:111], v[44:47], v[28:31], v[108:111]\n\t" \
  "ds_read_b64_tr_b16 v[42:43], v58 offset:8704\n\t" \
  "v_mfma_f32_16x16x32_bf16 v[116:119], v[44:47], v[32:35], v[116:119]\n\t" \
  "ds_read_b128 v[16:19], v56 offset:16384\n\t" \
  "v_mfma_f32_16x16x32_bf16 v[124:127], v[44:47], v[36:39], v[124:127]\n\t" \
  "s_waitcnt lgkmcnt(9)\n\t" \
  "v_mfma_f32_16x16x32_bf16 v[64:67], v[48:51], v[24:27], v[64:67]\n\t" \
  "ds_read_b64_tr_b16 v[44:45], v58 offset:4096\n\t" \
  "v_mfma_f32_16x16x32_bf16 v[72:75], v[48:51], v[28:31], v[72:75]\n\t" \
  "ds_read_b64_tr_b16 v[46:47], v58 offset:4608\n\t" \
  "v_mfma_f32_16x16x32_bf16 v[80:83], v[48:51], v[32:35], v[80:83]\n\t" \
  "v_mfma_f32_16x16x32_bf16 v[88:91], v[48:51], v[36:39], v[88:91]\n\t" \
  "s_waitcnt lgkmcnt(7)\n\t" \
  "v_mfma_f32_16x16x32_bf16 v[68:71], v[52:55], v[24:27], v[68:71]\n\t" \
  "ds_read_b64_tr_b16 v[48:49], v59 offset:8192\n\t" \
  "v_mfma_f32_16x16x32_bf16 v[76:79], v[52:55], v[28:31], v[76:79]\n\t" \
  "ds_read_b64_tr_b16 v[50:51], v59 offset:8704\n\t" \
  "s_addk_i32 %[it], 1\n\t" \
  "v_mfma_f32_16x16x32_bf16 v[84:87], v[52:55], v[32:35], v[84:87]\n\t" \
  "s_cmp_lt_u32 %[it], 6\n\t" \
  "v_mfma_f32_16x16x32_bf16 v[92:95], v[52:55], v[36:39], v[92:95]\n\t" \
  "s_cbranch_scc1 N0b_%=\n\t" \
  "s_setprio 3\n\t" \
  "v_cvt_pk_bf16_f32 v160, v160, v161\n\t" \
  "v_cvt_pk_bf16_f32 v161, v162, v163\n\t" \
  "v_cvt_pk_bf16_f32 v162, v164, v165\n\t" \
  "v_cvt_pk_bf16_f32 v163, v166, v167\n\t" \
  "ds_write_b128 v63, v[160:163] offset:2048\n\t" \
  "v_mov_b32 v57, %[fa]\n\t" \
  "v_mov_b32 v58, 1\n\t" \
  "s_mov_b64 exec, 1\n\t" \
  "ds_write_b32 v57, v58\n\t" \
  "s_mov_b64 exec, -1\n\t" \
  "v_cvt_pk_bf16_f32 v120, v120, v121\n\t" \
  "v_cvt_pk_bf16_f32 v121, v122, v123\n\t" \
  "v_cvt_pk_bf16_f32 v122, v124, v125\n\t" \
  "v_cvt_pk_bf16_f32 v123, v126, v127\n\t" \
  "v_cvt_pk_bf16_f32 v28, v152, v153\n\t" \
  "v_cvt_pk_bf16_f32 v29, v154, v155\n\t" \
  "v_cvt_pk_bf16_f32 v38, v156, v157\n\t" \
  "v_cvt_pk_bf16_f32 v39, v158, v159\n\t" \
  "NX_%=:\n\t" \
  "v_cvt_pk_bf16_f32 v96, v96, v97\n\t" \
  "v_cvt_pk_bf16_f32 v97, v98, v99\n\t" \
  "v_cvt_pk_bf16_f32 v98, v100, v101\n\t" \
  "v_cvt_pk_bf16_f32 v99, v102, v103\n\t" \
  "v_cvt_pk_bf16_f32 v104, v104, v105\n\t" \
  "v_cvt_pk_bf16_f32 v105, v106, v107\n\t" \
  "v_cvt_pk_bf16_f32 v106, v108, v109\n\t" \
  "v_cvt_pk_bf16_f32 v107, v110, v111\n\t" \
  "v_mov_b32 v60, 0xff61b1e6\n\t" \
  "v_cndmask_b32_e64 v56, v60, 0, s[68:69]\n\t" \
  "v_cndmask_b32_e64 v57, v60, 0, s[70:71]\n\t" \
  "v_cndmask_b32_e64 v58, v60, 0, s[72:73]\n\t" \
  "v_cndmask_b32_e64 v59, v60, 0, s[74:75]\n\t" \
  "v_cvt_pk_bf16_f32 v72, v72, v73\n\t" \
  "v_cvt_pk_bf16_f32 v73, v74, v75\n\t" \
  "v_cvt_pk_bf16_f32 v74, v76, v77\n\t" \
  "v_cvt_pk_bf16_f32 v75, v78, v79\n\t" \
  "v_cndmask_b32_e64 v52, v60, 0, s[76:77]\n\t" \
  "v_cndmask_b32_e64 v53, v60, 0, s[78:79]\n\t" \
  "v_cndmask_b32_e64 v54, v60, 0, s[80:81]\n\t" \
  "v_cndmask_b32_e64 v55, v60, 0, s[82:83]\n\t" \
  "v_cvt_pk_bf16_f32 v24, v136, v137\n\t" \
  "v_cvt_pk_bf16_f32 v25, v138, v139\n\t" \
  "v_cvt_pk_bf16_f32 v34, v140, v141\n\t" \
  "v_cvt_pk_bf16_f32 v35, v142, v143\n\t" \
  "v_cvt_pk_bf16_f32 v26, v144, v145\n\t" \
  "v_cvt_pk_bf16_f32 v27, v146, v147\n\t" \
  "v_cvt_pk_bf16_f32 v36, v148, v149\n\t" \
  "v_cvt_pk_bf16_f32 v37, v150, v151\n\t" \
  "v_mfma_f32_16x16x32_bf16 v[136:139], v[96:99], v[72:75], v[56:59]\n\t" \
  "v_cvt_pk_bf16_f32 v112, v112, v113\n\t" \
  "v_cvt_pk_bf16_f32 v113, v114, v115\n\t" \
  "v_mfma_f32_16x16x32_bf16 v[140:143], v[104:107], v[72:75], v[52:55]\n\t" \
  "v_cvt_pk_bf16_f32 v114, v116, v117\n\t" \
  "v_cvt_pk_bf16_f32 v115, v118, v119\n\t" \
  "v_cvt_pk_bf16_f32 v80, v80, v81\n\t" \
  "v_cvt_pk_bf16_f32 v81, v82, v83\n\t" \
  "v_cvt_pk_bf16_f32 v82, v84, v85\n\t" \
  "v_cvt_pk_bf16_f32 v83, v86, v87\n\t" \
  "v_cvt_pk_bf16_f32 v22, v128, v129\n\t" \
  "v_cvt_pk_bf16_f32 v23, v130, v131\n\t" \
  "v_mfma_f32_16x16x32_bf16 v[144:147], v[104:107], v[80:83], v[56:59]\n\t" \
  "v_cvt_pk_bf16_f32 v32, v132, v133\n\t" \
  "v_cvt_pk_bf16_f32 v33, v134, v135\n\t" \
  "v_mfma_f32_16x16x32_bf16 v[148:151], v[112:115], v[80:83], v[52:55]\n\t" \
  "v_cvt_pk_bf16_f32 v88, v88, v89\n\t" \
  "v_cvt_pk_bf16_f32 v89, v90, v91\n\t" \
  "v_cvt_pk_bf16_f32 v90, v92, v93\n\t" \
  "v_cvt_pk_bf16_f32 v91, v94, v95\n\t" \
  "v_mov_b32 v61, %[fp]\n\t" \
  "v_add_u32 v60, %[vo], v63\n\t" \
  "v_mfma_f32_16x16x32_bf16 v[152:155], v[112:115], v[88:91], v[56:59]\n\t" \
  "ds_read_b32 v0, v61\n\t" \
  "ds_read_b64 v[20:21], v60\n\t" \
  "ds_read_b64 v[30:31], v60 offset:8\n\t" \
  "s_mov_b64 exec, %[rm]\n\t" \
  "ds_read_b128 v[160:163], v63\n\t" \
  "s_mov_b64 exec, -1\n\t" \
  "v_mfma_f32_16x16x32_bf16 v[156:159], v[120:123], v[88:91], v[52:55]\n\t" \
  "v_cvt_pk_bf16_f32 v64, v64, v65\n\t" \
  "v_cvt_pk_bf16_f32 v65, v66, v67\n\t" \
  "v_cvt_pk_bf16_f32 v66, v68, v69\n\t" \
  "v_cvt_pk_bf16_f32 v67, v70, v71\n\t" \
  "v_max_f32 v136, v136, v137\n\t" \
  "v_max_f32 v138, v138, v139\n\t" \
  "v_mfma_f32_16x16x32_bf16 v[132:135], v[96:99], v[64:67], v[52:55]\n\t" \
  "v_max_f32 v140, v140, v141\n\t" \
  "v_max_f32 v142, v142, v143\n\t" \
  "AW_%=:\n\t" \
  "s_waitcnt lgkmcnt(0)\n\t" \
  "v_cmp_eq_u32 vcc, 0, v0\n\t" \
  "v_max3_f32 v5, v136, v138, v140\n\t" \
  "v_max_f32 v5, v5, v142\n\t" \
  "s_cbranch_vccnz AR_%=\n\t" \
  "s_cmp_eq_u32 %[z], 0\n\t" \
  "s_cbranch_scc1 AZ_%=\n\t" \
  "AY_%=:\n\t" \
  "v_mfma_f32_16x16x32_bf16 v[128:131], v[160:163], v[64:67], v[56:59]\n\t" \
  "v_max_f32 v144, v144, v145\n\t" \
  "v_max_f32 v146, v146, v147\n\t" \
  "v_max_f32 v148, v148, v149\n\t" \
  "v_max_f32 v150, v150, v151\n\t" \
  "v_max3_f32 v6, v144, v146, v148\n\t" \
  "v_max_f32 v6, v6, v150\n\t" \
  "v_max_f32 v152, v152, v153\n\t" \
  "v_max_f32 v154, v154, v155\n\t" \
  "v_max_f32 v156, v156, v157\n\t" \
  "v_max_f32 v158, v158, v159\n\t" \
  "v_max3_f32 v7, v152, v154, v156\n\t" \
  "v_max_f32 v7, v7, v158\n\t" \
  "v_mov_b32 v8, 0xc0c0100\n\t" \
  "v_mov_b32 v9, 0x1000c0c\n\t" \
  "v_max_f32 v128, v128, v129\n\t" \
  "v_max_f32 v130, v130, v131\n\t" \
  "v_max_f32 v132, v132, v133\n\t" \
  "v_max_f32 v134, v134, v135\n\t" \
  "v_max3_f32 v4, v128, v130, v132\n\t" \
  "v_max_f32 v4, v4, v134\n\t" \
  "v_cndmask_b32_e64 v60, v8, v9, s[84:85]\n\t" \
  "v_mov_b32 v8, 0xc0c0302\n\t" \
  "v_permlane16_swap_b32 v6, v7\n\t" \
  "v_permlane16_swap_b32 v4, v5\n\t" \
  "v_max_f32 v6, v6, v7\n\t" \
  "v_max_f32 v4, v4, v5\n\t" \
  "v_mov_b32 v9, 0x3020c0c\n\t" \
  "v_cndmask_b32_e64 v61, v8, v9, s[84:85]\n\t" \
  "v_permlane32_swap_b32 v4, v6\n\t" \
  "v_max_f32 v4, v4, v6\n\t" \
  "v_mul_f32 v6, 0xbe8293ee, v4\n\t" \
  "v_mul_f32 v4, 0xbe8293ee, v4\n\t" \
  "s_nop 1\n\t" \
  "v_permlane32_swap_b32 v4, v6\n\t" \
  "v_mov_b32 v5, v4\n\t" \
  "v_mov_b32 v7, v6\n\t" \
  "s_nop 0\n\t" \
  "v_permlane16_swap_b32 v4, v5\n\t" \
  "v_permlane16_swap_b32 v6, v7\n\t" \
  "v_fmamk_f32 v128, v128, 0x3e8293ee, v4\n\t" \
  "v_fmamk_f32 v130, v130, 0x3e8293ee, v4\n\t" \
  "v_fmamk_f32 v132, v132, 0x3e8293ee, v4\n\t" \
  "v_fmamk_f32 v134, v134, 0x3e8293ee, v4\n\t" \
  "v_fmamk_f32 v136, v136, 0x3e8293ee, v5\n\t" \
  "v_fmamk_f32 v138, v138, 0x3e8293ee, v5\n\t" \
  "v_fmamk_f32 v140, v140, 0x3e8293ee, v5\n\t" \
  "v_fmamk_f32 v142, v142, 0x3e8293ee, v5\n\t" \
  "v_fmamk_f32 v144, v144, 0x3e8293ee, v6\n\t" \
  "v_fmamk_f32 v146, v146, 0x3e8293ee, v6\n\t" \
  "v_fmamk_f32 v148, v148, 0x3e8293ee, v6\n\t" \
  "v_fmamk_f32 v150, v150, 0x3e8293ee, v6\n\t" \
  "v_fmamk_f32 v152, v152, 0x3e8293ee, v7\n\t" \
  "v_fmamk_f32 v154, v154, 0x3e8293ee, v7\n\t" \
  "v_fmamk_f32 v156, v156, 0x3e8293ee, v7\n\t" \
  "v_fmamk_f32 v158, v158, 0x3e8293ee, v7\n\t" \
  "v_exp_f32 v128, v128\n\t" \
  "v_exp_f32 v130, v130\n\t" \
  "v_exp_f32 v132, v132\n\t" \
  "v_exp_f32 v134, v134\n\t" \
  "v_exp_f32 v136, v136\n\t" \
  "v_exp_f32 v138, v138\n\t" \
  "v_exp_f32 v140, v140\n\t" \
  "v_exp_f32 v142, v142\n\t" \
  "v_exp_f32 v144, v144\n\t" \
  "v_exp_f32 v146, v146\n\t" \
  "v_exp_f32 v148, v148\n\t" \
  "v_exp_f32 v150, v150\n\t" \
  "v_exp_f32 v152, v152\n\t" \
  "v_exp_f32 v154, v154\n\t" \
  "v_exp_f32 v156, v156\n\t" \
  "v_exp_f32 v158, v158\n\t" \
  "v_add_f32 v4, v128, v130\n\t" \
  "v_add_f32 v8, v132, v134\n\t" \
  "v_add_f32 v5, v136, v138\n\t" \
  "v_add_f32 v9, v140, v142\n\t" \
  "v_add_f32 v6, v144, v146\n\t" \
  "v_add_f32 v10, v148, v150\n\t" \
  "v_add_f32 v7, v152, v154\n\t" \
  "v_add_f32 v11, v156, v158\n\t" \
  "v_add_f32 v4, v4, v8\n\t" \
  "v_add_f32 v5, v5, v9\n\t" \
  "v_add_f32 v6, v6, v10\n\t" \
  "v_add_f32 v7, v7, v11\n\t" \
  "v_permlane16_swap_b32 v4, v5\n\t" \
  "s_nop 0\n\t" \
  "v_permlane16_swap_b32 v6, v7\n\t" \
  "v_add_f32 v4, v4, v5\n\t" \
  "v_add_f32 v6, v6, v7\n\t" \
  "s_nop 1\n\t" \
  "v_permlane32_swap_b32 v4, v6\n\t" \
  "v_add_f32 v4, v4, v6\n\t" \
  "v_rcp_f32 v4, v4\n\t" \
  "s_nop 0\n\t" \
  "v_mov_b32 v6, v4\n\t" \
  "s_nop 1\n\t" \
  "v_permlane32_swap_b32 v4, v6\n\t" \
  "v_mov_b32 v5, v4\n\t" \
  "v_mov_b32 v7, v6\n\t" \
  "s_nop 0\n\t" \
  "v_permlane16_swap_b32 v4, v5\n\t" \
  "v_permlane16_swap_b32 v6, v7\n\t" \
  "v_mul_f32 v128, v128, v4\n\t" \
  "v_mul_f32 v130, v130, v4\n\t" \
  "v_mul_f32 v132, v132, v4\n\t" \
  "v_mul_f32 v134, v134, v4\n\t" \
  "v_cvt_pk_bf16_f32 v128, v128, v130\n\t" \
  "v_cvt_pk_bf16_f32 v132, v132, v134\n\t" \
  "v_perm_b32 v68, 0, v128, v60\n\t" \
  "v_perm_b32 v69, 0, v128, v61\n\t" \
  "v_perm_b32 v70, 0, v132, v60\n\t" \
  "v_perm_b32 v71, 0, v132, v61\n\t" \
  "v_mul_f32 v136, v136, v5\n\t" \
  "v_mul_f32 v138, v138, v5\n\t" \
  "v_mfma_f32_16x16x32_bf16 v[128:131], v[20:23], v[68:71], 0\n\t" \
  "v_mul_f32 v140, v140, v5\n\t" \
  "v_mul_f32 v142, v142, v5\n\t" \
  "v_cvt_pk_bf16_f32 v136, v136, v138\n\t" \
  "v_mfma_f32_16x16x32_bf16 v[132:135], v[30:33], v[68:71], 0\n\t" \
  "v_cvt_pk_bf16_f32 v140, v140, v142\n\t" \
  "v_perm_b32 v76, 0, v136, v60\n\t" \
  "v_perm_b32 v77, 0, v136, v61\n\t" \
  "v_perm_b32 v78, 0, v140, v60\n\t" \
  "v_perm_b32 v79, 0, v140, v61\n\t" \
  "v_mul_f32 v144, v144, v6\n\t" \
  "v_mul_f32 v146, v146, v6\n\t" \
  "v_mfma_f32_16x16x32_bf16 v[136:139], v[22:25], v[76:79], 0\n\t" \
  "v_mul_f32 v148, v148, v6\n\t" \
  "v_mul_f32 v150, v150, v6\n\t" \
  "v_cvt_pk_bf16_f32 v144, v144, v146\n\t" \
  "v_mfma_f32_16x16x32_bf16 v[140:143], v[32:35], v[76:79], 0\n\t" \
  "v_cvt_pk_bf16_f32 v148, v148, v150\n\t" \
  "v_perm_b32 v84, 0, v144, v60\n\t" \
  "v_perm_b32 v85, 0, v144, v61\n\t" \
  "v_perm_b32 v86, 0, v148, v60\n\t" \
  "v_perm_b32 v87, 0, v148, v61\n\t" \
  "v_cvt_pk_bf16_f32 v100, v128, v129\n\t" \
  "v_cvt_pk_bf16_f32 v101, v130, v131\n\t" \
  "v_mfma_f32_16x16x32_bf16 v[144:147], v[24:27], v[84:87], 0\n\t" \
  "v_cvt_pk_bf16_f32 v102, v132, v133\n\t" \
  "v_cvt_pk_bf16_f32 v103, v134, v135\n\t" \
  "global_store_dwordx4 v62, v[100:103], %[ot] offset:0 sc1\n\t" \
  "v_mfma_f32_16x16x32_bf16 v[148:151], v[34:37], v[84:87], 0\n\t" \
  "v_mul_f32 v152, v152, v7\n\t" \
  "v_mul_f32 v154, v154, v7\n\t" \
  "v_mul_f32 v156, v156, v7\n\t" \
  "v_mul_f32 v158, v158, v7\n\t" \
  "v_cvt_pk_bf16_f32 v152, v152, v154\n\t" \
  "v_cvt_pk_bf16_f32 v156, v156, v158\n\t" \
  "v_perm_b32 v92, 0, v152, v60\n\t" \
  "v_perm_b32 v93, 0, v152, v61\n\t" \
  "v_perm_b32 v94, 0, v156, v60\n\t" \
  "v_perm_b32 v95, 0, v156, v61\n\t" \
  "v_cvt_pk_bf16_f32 v108, v136, v137\n\t" \
  "v_cvt_pk_bf16_f32 v109, v138, v139\n\t" \
  "v_mfma_f32_16x16x32_bf16 v[152:155], v[26:29], v[92:95], 0\n\t" \
  "v_cvt_pk_bf16_f32 v110, v140, v141\n\t" \
  "v_cvt_pk_bf16_f32 v111, v142, v143\n\t" \
  "global_store_dwordx4 v62, v[108:111], %[ot] offset:1024 sc1\n\t" \
  "v_mfma_f32_16x16x32_bf16 v[156:159], v[36:39], v[92:95], 0\n\t" \
  "v_cvt_pk_bf16_f32 v116, v144, v145\n\t" \
  "v_cvt_pk_bf16_f32 v117, v146, v147\n\t" \
  "v_cvt_pk_bf16_f32 v118, v148, v149\n\t" \
  "v_cvt_pk_bf16_f32 v119, v150, v151\n\t" \
  "global_store_dwordx4 v62, v[116:119], %[ou] offset:0 sc1\n\t" \
  "v_cvt_pk_bf16_f32 v124, v152, v153\n\t" \
  "v_cvt_pk_bf16_f32 v125, v154, v155\n\t" \
  "s_nop 0\n\t" \
  "v_cvt_pk_bf16_f32 v126, v156, v157\n\t" \
  "v_cvt_pk_bf16_f32 v127, v158, v159\n\t" \
  "global_store_dwordx4 v62, v[124:127], %[ou] offset:1024 sc1\n\t" \
  "s_branch AE_%=\n\t" \
  "AR_%=:\n\t" \
  "s_addk_i32 %[it], 1\n\t" \
  "s_cmp_gt_u32 %[it], 0x400000\n\t" \
  "s_cbranch_scc1 AY_%=\n\t" \
  "ds_read_b32 v0, v61\n\t" \
  "ds_read_b64 v[20:21], v60\n\t" \
  "ds_read_b64 v[30:31], v60 offset:8\n\t" \
  "s_mov_b64 exec, %[rm]\n\t" \
  "ds_read_b128 v[160:163], v63\n\t" \
  "s_mov_b64 exec, -1\n\t" \
  "s_branch AW_%=\n\t" \
  "AZ_%=:\n\t" \
  "v_mov_b32 v0, 0xff61b1e6\n\t" \
  "v_mov_b32 v1, 0xcfa8965f\n\t" \
  "s_nop 3\n\t" \
  "v_cndmask_b32_e64 v56, v0, v1, s[68:69]\n\t" \
  "v_cndmask_b32_e64 v57, v0, v1, s[70:71]\n\t" \
  "v_cndmask_b32_e64 v58, v0, v1, s[72:73]\n\t" \
  "v_cndmask_b32_e64 v59, v0, v1, s[74:75]\n\t" \
  "s_nop 1\n\t" \
  "s_branch AY_%=\n\t" \
  "AE_%=:\n\t" \
  "s_nop 1\n\t"
#define N2_CLOB "v0", "v1", "v2", "v3", "v4", "v5", "v6", "v7", "v8", "v9", "v10", "v11", "v12", "v13", "v14", "v15", "v16", "v17", "v18", "v19", "v20", "v21", "v22", "v23", "v24", "v25", "v26", "v27", "v28", "v29", "v30", "v31", "v32", "v33", "v34", "v35", "v36", "v37", "v38", "v39", "v40", "v41", "v42", "v43", "v44", "v45", "v46", "v47", "v48", "v49", "v50", "v51", "v52", "v53", "v54", "v55", "v60", "v61", "v64", "v65", "v66", "v67", "v68", "v69", "v70", "v71", "v72", "v73", "v74", "v75", "v76", "v77", "v78", "v79", "v80", "v81", "v82", "v83", "v84", "v85", "v86", "v87", "v88", "v89", "v90", "v91", "v92", "v93", "v94", "v95", "v96", "v97", "v98", "v99", "v100", "v101", "v102", "v103", "v104", "v105", "v106", "v107", "v108", "v109", "v110", "v111", "v112", "v113", "v114", "v115", "v116", "v117", "v118", "v119", "v120", "v121", "v122", "v123", "v124", "v125", "v126", "v127", "v128", "v129", "v130", "v131", "v132", "v133", "v134", "v135", "v136", "v137", "v138", "v139", "v140", "v141", "v142", "v143", "v144", "v145", "v146", "v147", "v148", "v149", "v150", "v151", "v152", "v153", "v154", "v155", "v156", "v157", "v158", "v159", "v160", "v161", "v162", "v163", "v164", "v165", "v166", "v167", "s68", "s69", "s70", "s71", "s72", "s73", "s74", "s75", "s76", "s77", "s78", "s79", "s80", "s81", "s82", "s83", "s84", "s85"

#define CV_ASM \
  "ds_read_b64 v[0:1], v28 offset:0\n\t" \
  "ds_read_b64 v[2:3], v29 offset:0\n\t" \
  "ds_read_b64 v[4:5], v30 offset:1024\n\t" \
  "ds_read_b64 v[6:7], v31 offset:1024\n\t" \
  "ds_read_b64 v[8:9], v24 offset:2048\n\t" \
  "ds_read_b64 v[10:11], v25 offset:2048\n\t" \
  "ds_read_b64 v[12:13], v26 offset:3072\n\t" \
  "ds_read_b64 v[14:15], v27 offset:3072\n\t" \
  "ds_read_b64 v[16:17], v28 offset:4096\n\t" \
  "ds_read_b64 v[18:19], v29 offset:4096\n\t" \
  "ds_read_b64 v[20:21], v30 offset:5120\n\t" \
  "ds_read_b64 v[22:23], v31 offset:5120\n\t" \
  "s_waitcnt lgkmcnt(10)\n\t" \
  "v_mfma_f32_16x16x32_bf16 v[128:131], v[68:71], v[0:3], 0\n\t" \
  "v_mfma_f32_16x16x32_bf16 v[132:135], v[76:79], v[0:3], 0\n\t" \
  "v_mfma_f32_16x16x32_bf16 v[136:139], v[84:87], v[0:3], 0\n\t" \
  "v_mfma_f32_16x16x32_bf16 v[140:143], v[92:95], v[0:3], 0\n\t" \
  "v_mfma_f32_16x16x32_bf16 v[144:147], v[100:103], v[0:3], 0\n\t" \
  "v_mfma_f32_16x16x32_bf16 v[148:151], v[108:111], v[0:3], 0\n\t" \
  "v_mfma_f32_16x16x32_bf16 v[152:155], v[116:119], v[0:3], 0\n\t" \
  "v_mfma_f32_16x16x32_bf16 v[156:159], v[124:127], v[0:3], 0\n\t" \
  "ds_read_b64 v[0:1], v24 offset:6144\n\t" \
  "s_waitcnt lgkmcnt(9)\n\t" \
  "v_mfma_f32_16x16x32_bf16 v[128:131], v[64:67], v[4:7], v[128:131]\n\t" \
  "ds_read_b64 v[2:3], v25 offset:6144\n\t" \
  "v_mfma_f32_16x16x32_bf16 v[132:135], v[72:75], v[4:7], v[132:135]\n\t" \
  "v_mfma_f32_16x16x32_bf16 v[136:139], v[80:83], v[4:7], v[136:139]\n\t" \
  "v_mfma_f32_16x16x32_bf16 v[140:143], v[88:91], v[4:7], v[140:143]\n\t" \
  "v_mfma_f32_16x16x32_bf16 v[144:147], v[96:99], v[4:7], v[144:147]\n\t" \
  "v_mfma_f32_16x16x32_bf16 v[148:151], v[104:107], v[4:7], v[148:151]\n\t" \
  "v_mfma_f32_16x16x32_bf16 v[152:155], v[112:115], v[4:7], v[152:155]\n\t" \
  "v_mfma_f32_16x16x32_bf16 v[156:159], v[120:123], v[4:7], v[156:159]\n\t" \
  "ds_read_b64 v[4:5], v26 offset:7168\n\t" \
  "ds_bpermute_b32 v34, v32, v131\n\t" \
  "v_readlane_b32 %[p0], v131, 63\n\t" \
  "s_waitcnt lgkmcnt(10)\n\t" \
  "v_mfma_f32_16x16x32_bf16 v[132:135], v[68:71], v[8:11], v[132:135]\n\t" \
  "ds_read_b64 v[6:7], v27 offset:7168\n\t" \
  "v_mov_b32 v131, v130\n\t" \
  "v_mfma_f32_16x16x32_bf16 v[136:139], v[76:79], v[8:11], v[136:139]\n\t" \
  "v_mov_b32 v130, v129\n\t" \
  "v_mfma_f32_16x16x32_bf16 v[140:143], v[84:87], v[8:11], v[140:143]\n\t" \
  "v_mov_b32 v129, v128\n\t" \
  "v_mfma_f32_16x16x32_bf16 v[144:147], v[92:95], v[8:11], v[144:147]\n\t" \
  "v_mfma_f32_16x16x32_bf16 v[148:151], v[100:103], v[8:11], v[148:151]\n\t" \
  "v_mfma_f32_16x16x32_bf16 v[152:155], v[108:111], v[8:11], v[152:155]\n\t" \
  "v_mfma_f32_16x16x32_bf16 v[156:159], v[116:119], v[8:11], v[156:159]\n\t" \
  "ds_read_b64 v[8:9], v28 offset:8192\n\t" \
  "s_waitcnt lgkmcnt(10)\n\t" \
  "v_mfma_f32_16x16x32_bf16 v[132:135], v[64:67], v[12:15], v[132:135]\n\t" \
  "ds_read_b64 v[10:11], v29 offset:8192\n\t" \
  "v_mfma_f32_16x16x32_bf16 v[136:139], v[72:75], v[12:15], v[136:139]\n\t" \
  "v_mfma_f32_16x16x32_bf16 v[140:143], v[80:83], v[12:15], v[140:143]\n\t" \
  "v_mfma_f32_16x16x32_bf16 v[144:147], v[88:91], v[12:15], v[144:147]\n\t" \
  "v_mfma_f32_16x16x32_bf16 v[148:151], v[96:99], v[12:15], v[148:151]\n\t" \
  "s_waitcnt lgkmcnt(3)\n\t" \
  "v_mov_b32 v128, v34\n\t" \
  "v_writelane_b32 v128, 0, 0\n\t" \
  "v_mfma_f32_16x16x32_bf16 v[152:155], v[104:107], v[12:15], v[152:155]\n\t" \
  "v_mfma_f32_16x16x32_bf16 v[156:159], v[112:115], v[12:15], v[156:159]\n\t" \
  "ds_read_b64 v[12:13], v30 offset:9216\n\t" \
  "s_waitcnt lgkmcnt(10)\n\t" \
  "v_mfma_f32_16x16x32_bf16 v[136:139], v[68:71], v[16:19], v[136:139]\n\t" \
  "ds_read_b64 v[14:15], v31 offset:9216\n\t" \
  "ds_bpermute_b32 v35, v32, v135\n\t" \
  "v_readlane_b32 %[p1], v135, 63\n\t" \
  "v_mfma_f32_16x16x32_bf16 v[140:143], v[76:79], v[16:19], v[140:143]\n\t" \
  "v_mov_b32 v135, v134\n\t" \
  "v_mfma_f32_16x16x32_bf16 v[144:147], v[84:87], v[16:19], v[144:147]\n\t" \
  "v_mov_b32 v134, v133\n\t" \
  "v_mfma_f32_16x16x32_bf16 v[148:151], v[92:95], v[16:19], v[148:151]\n\t" \
  "v_mov_b32 v133, v132\n\t" \
  "v_mfma_f32_16x16x32_bf16 v[152:155], v[100:103], v[16:19], v[152:155]\n\t" \
  "v_mfma_f32_16x16x32_bf16 v[156:159], v[108:111], v[16:19], v[156:159]\n\t" \
  "ds_read_b64 v[16:17], v24 offset:10240\n\t" \
  "s_waitcnt lgkmcnt(11)\n\t" \
  "v_mfma_f32_16x16x32_bf16 v[136:139], v[64:67], v[20:23], v[136:139]\n\t" \
  "ds_read_b64 v[18:19], v25 offset:10240\n\t" \
  "v_mfma_f32_16x16x32_bf16 v[140:143], v[72:75], v[20:23], v[140:143]\n\t" \
  "v_mfma_f32_16x16x32_bf16 v[144:147], v[80:83], v[20:23], v[144:147]\n\t" \
  "v_mfma_f32_16x16x32_bf16 v[148:151], v[88:91], v[20:23], v[148:151]\n\t" \
  "v_mfma_f32_16x16x32_bf16 v[152:155], v[96:99], v[20:23], v[152:155]\n\t" \
  "v_mfma_f32_16x16x32_bf16 v[156:159], v[104:107], v[20:23], v[156:159]\n\t" \
  "ds_read_b64 v[20:21], v26 offset:11264\n\t" \
  "s_waitcnt lgkmcnt(11)\n\t" \
  "v_mfma_f32_16x16x32_bf16 v[140:143], v[68:71], v[0:3], v[140:143]\n\t" \
  "ds_read_b64 v[22:23], v27 offset:11264\n\t" \
  "s_waitcnt lgkmcnt(4)\n\t" \
  "v_mov_b32 v132, v35\n\t" \
  "v_writelane_b32 v132, %[p0], 0\n\t" \
  "v_mfma_f32_16x16x32_bf16 v[144:147], v[76:79], v[0:3], v[144:147]\n\t" \
  "ds_bpermute_b32 v36, v32, v139\n\t" \
  "v_readlane_b32 %[p2], v139, 63\n\t" \
  "v_mfma_f32_16x16x32_bf16 v[148:151], v[84:87], v[0:3], v[148:151]\n\t" \
  "v_mov_b32 v139, v138\n\t" \
  "v_mfma_f32_16x16x32_bf16 v[152:155], v[92:95], v[0:3], v[152:155]\n\t" \
  "v_mov_b32 v138, v137\n\t" \
  "v_mfma_f32_16x16x32_bf16 v[156:159], v[100:103], v[0:3], v[156:159]\n\t" \
  "ds_read_b64 v[0:1], v28 offset:12288\n\t" \
  "v_mov_b32 v137, v136\n\t" \
  "s_waitcnt lgkmcnt(11)\n\t" \
  "v_mfma_f32_16x16x32_bf16 v[140:143], v[64:67], v[4:7], v[140:143]\n\t" \
  "ds_read_b64 v[2:3], v29 offset:12288\n\t" \
  "v_mfma_f32_16x16x32_bf16 v[144:147], v[72:75], v[4:7], v[144:147]\n\t" \
  "v_mfma_f32_16x16x32_bf16 v[148:151], v[80:83], v[4:7], v[148:151]\n\t" \
  "global_load_dwordx4 v[44:47], v43, %[q0] offset:0\n\t" \
  "v_mfma_f32_16x16x32_bf16 v[152:155], v[88:91], v[4:7], v[152:155]\n\t" \
  "v_mfma_f32_16x16x32_bf16 v[156:159], v[96:99], v[4:7], v[156:159]\n\t" \
  "ds_read_b64 v[4:5], v30 offset:13312\n\t" \
  "s_waitcnt lgkmcnt(11)\n\t" \
  "v_mfma_f32_16x16x32_bf16 v[144:147], v[68:71], v[8:11], v[144:147]\n\t" \
  "ds_read_b64 v[6:7], v31 offset:13312\n\t" \
  "v_mfma_f32_16x16x32_bf16 v[148:151], v[76:79], v[8:11], v[148:151]\n\t" \
  "v_mfma_f32_16x16x32_bf16 v[152:155], v[84:87], v[8:11], v[152:155]\n\t" \
  "ds_bpermute_b32 v37, v32, v143\n\t" \
  "v_readlane_b32 %[p3], v143, 63\n\t" \
  "v_mfma_f32_16x16x32_bf16 v[156:159], v[92:95], v[8:11], v[156:159]\n\t" \
  "ds_read_b64 v[8:9], v24 offset:14336\n\t" \
  "s_waitcnt lgkmcnt(6)\n\t" \
  "v_mov_b32 v136, v36\n\t" \
  "v_writelane_b32 v136, %[p1], 0\n\t" \
  "v_mov_b32 v143, v142\n\t" \
  "global_load_dwordx4 v[48:51], v43, %[q0] offset:2048\n\t" \
  "s_waitcnt lgkmcnt(12)\n\t" \
  "v_mfma_f32_16x16x32_bf16 v[144:147], v[64:67], v[12:15], v[144:147]\n\t" \
  "ds_read_b64 v[10:11], v25 offset:14336\n\t" \
  "v_mov_b32 v142, v141\n\t" \
  "v_mfma_f32_16x16x32_bf16 v[148:151], v[72:75], v[12:15], v[148:151]\n\t" \
  "v_mov_b32 v141, v140\n\t" \
  "v_mfma_f32_16x16x32_bf16 v[152:155], v[80:83], v[12:15], v[152:155]\n\t" \
  "v_mfma_f32_16x16x32_bf16 v[156:159], v[88:91], v[12:15], v[156:159]\n\t" \
  "ds_read_b64 v[12:13], v26 offset:15360\n\t" \
  "s_waitcnt lgkmcnt(11)\n\t" \
  "v_mfma_f32_16x16x32_bf16 v[148:151], v[68:71], v[16:19], v[148:151]\n\t" \
  "ds_read_b64 v[14:15], v27 offset:15360\n\t" \
  "v_mfma_f32_16x16x32_bf16 v[152:155], v[76:79], v[16:19], v[152:155]\n\t" \
  "global_load_dwordx4 v[52:55], v43, %[q2] offset:0\n\t" \
  "v_mfma_f32_16x16x32_bf16 v[156:159], v[84:87], v[16:19], v[156:159]\n\t" \
  "ds_read_b64 v[16:17], v26 offset:0\n\t" \
  "s_waitcnt lgkmcnt(11)\n\t" \
  "v_mfma_f32_16x16x32_bf16 v[148:151], v[64:67], v[20:23], v[148:151]\n\t" \
  "ds_read_b64 v[18:19], v27 offset:0\n\t" \
  "ds_bpermute_b32 v38, v32, v147\n\t" \
  "v_readlane_b32 %[p4], v147, 63\n\t" \
  "v_mfma_f32_16x16x32_bf16 v[152:155], v[72:75], v[20:23], v[152:155]\n\t" \
  "v_mov_b32 v147, v146\n\t" \
  "v_mfma_f32_16x16x32_bf16 v[156:159], v[80:83], v[20:23], v[156:159]\n\t" \
  "ds_read_b64 v[20:21], v28 offset:1024\n\t" \
  "v_mov_b32 v146, v145\n\t" \
  "s_waitcnt lgkmcnt(11)\n\t" \
  "v_mfma_f32_16x16x32_bf16 v[152:155], v[68:71], v[0:3], v[152:155]\n\t" \
  "ds_read_b64 v[22:23], v29 offset:1024\n\t" \
  "s_waitcnt lgkmcnt(9)\n\t" \
  "v_mov_b32 v140, v37\n\t" \
  "v_writelane_b32 v140, %[p2], 0\n\t" \
  "v_mov_b32 v145, v144\n\t" \
  "v_mfma_f32_16x16x32_bf16 v[156:159], v[76:79], v[0:3], v[156:159]\n\t" \
  "ds_read_b64 v[0:1], v30 offset:2048\n\t" \
  "ds_bpermute_b32 v39, v32, v151\n\t" \
  "v_readlane_b32 %[p5], v151, 63\n\t" \
  "global_load_dwordx4 v[56:59], v43, %[q2] offset:2048\n\t" \
  "s_waitcnt lgkmcnt(12)\n\t" \
  "v_mfma_f32_16x16x32_bf16 v[152:155], v[64:67], v[4:7], v[152:155]\n\t" \
  "ds_read_b64 v[2:3], v31 offset:2048\n\t" \
  "v_mov_b32 v151, v150\n\t" \
  "v_mfma_f32_16x16x32_bf16 v[156:159], v[72:75], v[4:7], v[156:159]\n\t" \
  "ds_read_b64 v[4:5], v24 offset:3072\n\t" \
  "v_mov_b32 v150, v149\n\t" \
  "s_waitcnt lgkmcnt(11)\n\t" \
  "v_mfma_f32_16x16x32_bf16 v[156:159], v[68:71], v[8:11], v[156:159]\n\t" \
  "ds_read_b64 v[6:7], v25 offset:3072\n\t" \
  "ds_read_b64 v[8:9], v26 offset:4096\n\t" \
  "v_mov_b32 v149, v148\n\t" \
  "ds_bpermute_b32 v40, v32, v155\n\t" \
  "v_readlane_b32 %[p6], v155, 63\n\t" \
  "s_waitcnt lgkmcnt(12)\n\t" \
  "v_mfma_f32_16x16x32_bf16 v[156:159], v[64:67], v[12:15], v[156:159]\n\t" \
  "ds_read_b64 v[10:11], v27 offset:4096\n\t" \
  "ds_read_b64 v[12:13], v28 offset:5120\n\t" \
  "v_mov_b32 v155, v154\n\t" \
  "s_waitcnt lgkmcnt(12)\n\t" \
  "v_mfma_f32_16x16x32_bf16 v[128:131], v[68:71], v[16:19], v[128:131]\n\t" \
  "ds_read_b64 v[14:15], v29 offset:5120\n\t" \
  "v_mov_b32 v154, v153\n\t" \
  "v_mfma_f32_16x16x32_bf16 v[132:135], v[76:79], v[16:19], v[132:135]\n\t" \
  "s_waitcnt lgkmcnt(12)\n\t" \
  "v_mov_b32 v144, v38\n\t" \
  "v_writelane_b32 v144, %[p3], 0\n\t" \
  "v_mov_b32 v153, v152\n\t" \
  "v_mfma_f32_16x16x32_bf16 v[136:139], v[84:87], v[16:19], v[136:139]\n\t" \
  "s_waitcnt lgkmcnt(8)\n\t" \
  "v_mov_b32 v148, v39\n\t" \
  "v_writelane_b32 v148, %[p4], 0\n\t" \
  "v_mfma_f32_16x16x32_bf16 v[140:143], v[92:95], v[16:19], v[140:143]\n\t" \
  "s_waitcnt lgkmcnt(3)\n\t" \
  "v_mov_b32 v152, v40\n\t" \
  "v_writelane_b32 v152, %[p5], 0\n\t" \
  "v_mfma_f32_16x16x32_bf16 v[144:147], v[100:103], v[16:19], v[144:147]\n\t" \
  "ds_bpermute_b32 v41, v32, v159\n\t" \
  "v_mfma_f32_16x16x32_bf16 v[148:151], v[108:111], v[16:19], v[148:151]\n\t" \
  "v_mov_b32 v159, v158\n\t" \
  "v_mfma_f32_16x16x32_bf16 v[152:155], v[116:119], v[16:19], v[152:155]\n\t" \
  "v_mov_b32 v158, v157\n\t" \
  "s_waitcnt lgkmcnt(11)\n\t" \
  "v_mfma_f32_16x16x32_bf16 v[128:131], v[64:67], v[20:23], v[128:131]\n\t" \
  "v_mov_b32 v157, v156\n\t" \
  "v_mfma_f32_16x16x32_bf16 v[132:135], v[72:75], v[20:23], v[132:135]\n\t" \
  "v_mfma_f32_16x16x32_bf16 v[136:139], v[80:83], v[20:23], v[136:139]\n\t" \
  "v_mfma_f32_16x16x32_bf16 v[140:143], v[88:91], v[20:23], v[140:143]\n\t" \
  "v_mfma_f32_16x16x32_bf16 v[144:147], v[96:99], v[20:23], v[144:147]\n\t" \
  "s_waitcnt lgkmcnt(0)\n\t" \
  "v_mov_b32 v156, v41\n\t" \
  "v_writelane_b32 v156, %[p6], 0\n\t" \
  "v_mfma_f32_16x16x32_bf16 v[148:151], v[104:107], v[20:23], v[148:151]\n\t" \
  "v_mfma_f32_16x16x32_bf16 v[152:155], v[112:115], v[20:23], v[152:155]\n\t" \
  "v_mfma_f32_16x16x32_bf16 v[156:159], v[124:127], v[16:19], v[156:159]\n\t" \
  "ds_read_b64 v[16:17], v30 offset:6144\n\t" \
  "ds_bpermute_b32 v34, v32, v131\n\t" \
  "v_readlane_b32 %[p0], v131, 63\n\t" \
  "v_mfma_f32_16x16x32_bf16 v[156:159], v[120:123], v[20:23], v[156:159]\n\t" \
  "ds_read_b64 v[18:19], v31 offset:6144\n\t" \
  "ds_read_b64 v[20:21], v24 offset:7168\n\t" \
  "v_mov_b32 v131, v130\n\t" \
  "s_waitcnt lgkmcnt(12)\n\t" \
  "v_mfma_f32_16x16x32_bf16 v[132:135], v[68:71], v[0:3], v[132:135]\n\t" \
  "ds_read_b64 v[22:23], v25 offset:7168\n\t" \
  "v_mov_b32 v130, v129\n\t" \
  "v_mfma_f32_16x16x32_bf16 v[136:139], v[76:79], v[0:3], v[136:139]\n\t" \
  "v_mov_b32 v129, v128\n\t" \
  "v_mfma_f32_16x16x32_bf16 v[140:143], v[84:87], v[0:3], v[140:143]\n\t" \
  "v_mfma_f32_16x16x32_bf16 v[144:147], v[92:95], v[0:3], v[144:147]\n\t" \
  "v_mfma_f32_16x16x32_bf16 v[148:151], v[100:103], v[0:3], v[148:151]\n\t" \
  "v_mfma_f32_16x16x32_bf16 v[152:155], v[108:111], v[0:3], v[152:155]\n\t" \
  "v_mfma_f32_16x16x32_bf16 v[156:159], v[116:119], v[0:3], v[156:159]\n\t" \
  "ds_read_b64 v[0:1], v26 offset:8192\n\t" \
  "s_waitcnt lgkmcnt(12)\n\t" \
  "v_mfma_f32_16x16x32_bf16 v[132:135], v[64:67], v[4:7], v[132:135]\n\t" \
  "ds_read_b64 v[2:3], v27 offset:8192\n\t" \
  "v_mfma_f32_16x16x32_bf16 v[136:139], v[72:75], v[4:7], v[136:139]\n\t" \
  "v_mfma_f32_16x16x32_bf16 v[140:143], v[80:83], v[4:7], v[140:143]\n\t" \
  "v_mfma_f32_16x16x32_bf16 v[144:147], v[88:91], v[4:7], v[144:147]\n\t" \
  "s_waitcnt lgkmcnt(5)\n\t" \
  "v_mov_b32 v128, v34\n\t" \
  "v_writelane_b32 v128, 0, 0\n\t" \
  "v_mfma_f32_16x16x32_bf16 v[148:151], v[96:99], v[4:7], v[148:151]\n\t" \
  "v_mfma_f32_16x16x32_bf16 v[152:155], v[104:107], v[4:7], v[152:155]\n\t" \
  "v_mfma_f32_16x16x32_bf16 v[156:159], v[112:115], v[4:7], v[156:159]\n\t" \
  "ds_read_b64 v[4:5], v28 offset:9216\n\t" \
  "s_waitcnt lgkmcnt(11)\n\t" \
  "v_mfma_f32_16x16x32_bf16 v[136:139], v[68:71], v[8:11], v[136:139]\n\t" \
  "ds_read_b64 v[6:7], v29 offset:9216\n\t" \
  "ds_bpermute_b32 v35, v32, v135\n\t" \
  "v_readlane_b32 %[p1], v135, 63\n\t" \
  "v_mfma_f32_16x16x32_bf16 v[140:143], v[76:79], v[8:11], v[140:143]\n\t" \
  "v_mov_b32 v135, v134\n\t" \
  "v_mfma_f32_16x16x32_bf16 v[144:147], v[84:87], v[8:11], v[144:147]\n\t" \
  "v_mov_b32 v134, v133\n\t" \
  "v_mfma_f32_16x16x32_bf16 v[148:151], v[92:95], v[8:11], v[148:151]\n\t" \
  "v_mov_b32 v133, v132\n\t" \
  "v_mfma_f32_16x16x32_bf16 v[152:155], v[100:103], v[8:11], v[152:155]\n\t" \
  "v_mfma_f32_16x16x32_bf16 v[156:159], v[108:111], v[8:11], v[156:159]\n\t" \
  "ds_read_b64 v[8:9], v30 offset:10240\n\t" \
  "s_waitcnt lgkmcnt(12)\n\t" \
  "v_mfma_f32_16x16x32_bf16 v[136:139], v[64:67], v[12:15], v[136:139]\n\t" \
  "ds_read_b64 v[10:11], v31 offset:10240\n\t" \
  "v_mfma_f32_16x16x32_bf16 v[140:143], v[72:75], v[12:15], v[140:143]\n\t" \
  "v_mfma_f32_16x16x32_bf16 v[144:147], v[80:83], v[12:15], v[144:147]\n\t" \
  "v_mfma_f32_16x16x32_bf16 v[148:151], v[88:91], v[12:15], v[148:151]\n\t" \
  "v_mfma_f32_16x16x32_bf16 v[152:155], v[96:99], v[12:15], v[152:155]\n\t" \
  "v_mfma_f32_16x16x32_bf16 v[156:159], v[104:107], v[12:15], v[156:159]\n\t" \
  "ds_read_b64 v[12:13], v24 offset:11264\n\t" \
  "s_waitcnt lgkmcnt(10)\n\t" \
  "v_mfma_f32_16x16x32_bf16 v[140:143], v[68:71], v[16:19], v[140:143]\n\t" \
  "ds_read_b64 v[14:15], v25 offset:11264\n\t" \
  "s_waitcnt lgkmcnt(4)\n\t" \
  "v_mov_b32 v132, v35\n\t" \
  "v_writelane_b32 v132, %[p0], 0\n\t" \
  "v_mfma_f32_16x16x32_bf16 v[144:147], v[76:79], v[16:19], v[144:147]\n\t" \
  "ds_bpermute_b32 v36, v32, v139\n\t" \
  "v_readlane_b32 %[p2], v139, 63\n\t" \
  "v_mfma_f32_16x16x32_bf16 v[148:151], v[84:87], v[16:19], v[148:151]\n\t" \
  "v_mov_b32 v139, v138\n\t" \
  "v_mfma_f32_16x16x32_bf16 v[152:155], v[92:95], v[16:19], v[152:155]\n\t" \
  "v_mov_b32 v138, v137\n\t" \
  "v_mfma_f32_16x16x32_bf16 v[156:159], v[100:103], v[16:19], v[156:159]\n\t" \
  "ds_read_b64 v[16:17], v26 offset:12288\n\t" \
  "v_mov_b32 v137, v136\n\t" \
  "s_waitcnt lgkmcnt(11)\n\t" \
  "v_mfma_f32_16x16x32_bf16 v[140:143], v[64:67], v[20:23], v[140:143]\n\t" \
  "ds_read_b64 v[18:19], v27 offset:12288\n\t" \
  "v_mfma_f32_16x16x32_bf16 v[144:147], v[72:75], v[20:23], v[144:147]\n\t" \
  "v_mfma_f32_16x16x32_bf16 v[148:151], v[80:83], v[20:23], v[148:151]\n\t" \
  "v_mfma_f32_16x16x32_bf16 v[152:155], v[88:91], v[20:23], v[152:155]\n\t" \
  "v_mfma_f32_16x16x32_bf16 v[156:159], v[96:99], v[20:23], v[156:159]\n\t" \
  "ds_read_b64 v[20:21], v28 offset:13312\n\t" \
  "s_waitcnt lgkmcnt(11)\n\t" \
  "v_mfma_f32_16x16x32_bf16 v[144:147], v[68:71], v[0:3], v[144:147]\n\t" \
  "ds_read_b64 v[22:23], v29 offset:13312\n\t" \
  "v_mfma_f32_16x16x32_bf16 v[148:151], v[76:79], v[0:3], v[148:151]\n\t" \
  "v_mfma_f32_16x16x32_bf16 v[152:155], v[84:87], v[0:3], v[152:155]\n\t" \
  "ds_bpermute_b32 v37, v32, v143\n\t" \
  "v_readlane_b32 %[p3], v143, 63\n\t" \
  "v_mfma_f32_16x16x32_bf16 v[156:159], v[92:95], v[0:3], v[156:159]\n\t" \
  "ds_read_b64 v[0:1], v30 offset:14336\n\t" \
  "s_waitcnt lgkmcnt(6)\n\t" \
  "v_mov_b32 v136, v36\n\t" \
  "v_writelane_b32 v136, %[p1], 0\n\t" \
  "v_mov_b32 v143, v142\n\t" \
  "s_waitcnt lgkmcnt(12)\n\t" \
  "v_mfma_f32_16x16x32_bf16 v[144:147], v[64:67], v[4:7], v[144:147]\n\t" \
  "ds_read_b64 v[2:3], v31 offset:14336\n\t" \
  "v_mov_b32 v142, v141\n\t" \
  "v_mfma_f32_16x16x32_bf16 v[148:151], v[72:75], v[4:7], v[148:151]\n\t" \
  "v_mov_b32 v141, v140\n\t" \
  "v_mfma_f32_16x16x32_bf16 v[152:155], v[80:83], v[4:7], v[152:155]\n\t" \
  "v_mfma_f32_16x16x32_bf16 v[156:159], v[88:91], v[4:7], v[156:159]\n\t" \
  "ds_read_b64 v[4:5], v24 offset:15360\n\t" \
  "s_waitcnt lgkmcnt(11)\n\t" \
  "v_mfma_f32_16x16x32_bf16 v[148:151], v[68:71], v[8:11], v[148:151]\n\t" \
  "ds_read_b64 v[6:7], v25 offset:15360\n\t" \
  "v_mfma_f32_16x16x32_bf16 v[152:155], v[76:79], v[8:11], v[152:155]\n\t" \
  "v_mfma_f32_16x16x32_bf16 v[156:159], v[84:87], v[8:11], v[156:159]\n\t" \
  "ds_read_b64 v[8:9], v24 offset:0\n\t" \
  "s_waitcnt lgkmcnt(11)\n\t" \
  "v_mfma_f32_16x16x32_bf16 v[148:151], v[64:67], v[12:15], v[148:151]\n\t" \
  "ds_read_b64 v[10:11], v25 offset:0\n\t" \
  "ds_bpermute_b32 v38, v32, v147\n\t" \
  "v_readlane_b32 %[p4], v147, 63\n\t" \
  "v_mfma_f32_16x16x32_bf16 v[152:155], v[72:75], v[12:15], v[152:155]\n\t" \
  "v_mov_b32 v147, v146\n\t" \
  "v_mfma_f32_16x16x32_bf16 v[156:159], v[80:83], v[12:15], v[156:159]\n\t" \
  "ds_read_b64 v[12:13], v26 offset:1024\n\t" \
  "v_mov_b32 v146, v145\n\t" \
  "s_waitcnt lgkmcnt(11)\n\t" \
  "v_mfma_f32_16x16x32_bf16 v[152:155], v[68:71], v[16:19], v[152:155]\n\t" \
  "ds_read_b64 v[14:15], v27 offset:1024\n\t" \
  "s_waitcnt lgkmcnt(9)\n\t" \
  "v_mov_b32 v140, v37\n\t" \
  "v_writelane_b32 v140, %[p2], 0\n\t" \
  "v_mov_b32 v145, v144\n\t" \
  "v_mfma_f32_16x16x32_bf16 v[156:159], v[76:79], v[16:19], v[156:159]\n\t" \
  "ds_read_b64 v[16:17], v28 offset:2048\n\t" \
  "ds_bpermute_b32 v39, v32, v151\n\t" \
  "v_readlane_b32 %[p5], v151, 63\n\t" \
  "s_waitcnt lgkmcnt(12)\n\t" \
  "v_mfma_f32_16x16x32_bf16 v[152:155], v[64:67], v[20:23], v[152:155]\n\t" \
  "ds_read_b64 v[18:19], v29 offset:2048\n\t" \
  "v_mov_b32 v151, v150\n\t" \
  "v_mfma_f32_16x16x32_bf16 v[156:159], v[72:75], v[20:23], v[156:159]\n\t" \
  "ds_read_b64 v[20:21], v30 offset:3072\n\t" \
  "v_mov_b32 v150, v149\n\t" \
  "s_waitcnt lgkmcnt(11)\n\t" \
  "v_mfma_f32_16x16x32_bf16 v[156:159], v[68:71], v[0:3], v[156:159]\n\t" \
  "ds_read_b64 v[22:23], v31 offset:3072\n\t" \
  "ds_read_b64 v[0:1], v24 offset:4096\n\t" \
  "v_mov_b32 v149, v148\n\t" \
  "ds_bpermute_b32 v40, v32, v155\n\t" \
  "v_readlane_b32 %[p6], v155, 63\n\t" \
  "s_waitcnt lgkmcnt(12)\n\t" \
  "v_mfma_f32_16x16x32_bf16 v[156:159], v[64:67], v[4:7], v[156:159]\n\t" \
  "ds_read_b64 v[2:3], v25 offset:4096\n\t" \
  "ds_read_b64 v[4:5], v26 offset:5120\n\t" \
  "v_mov_b32 v155, v154\n\t" \
  "s_waitcnt lgkmcnt(12)\n\t" \
  "v_mfma_f32_16x16x32_bf16 v[128:131], v[68:71], v[8:11], v[128:131]\n\t" \
  "ds_read_b64 v[6:7], v27 offset:5120\n\t" \
  "v_mov_b32 v154, v153\n\t" \
  "v_mfma_f32_16x16x32_bf16 v[132:135], v[76:79], v[8:11], v[132:135]\n\t" \
  "s_waitcnt lgkmcnt(12)\n\t" \
  "v_mov_b32 v144, v38\n\t" \
  "v_writelane_b32 v144, %[p3], 0\n\t" \
  "v_mov_b32 v153, v152\n\t" \
  "v_mfma_f32_16x16x32_bf16 v[136:139], v[84:87], v[8:11], v[136:139]\n\t" \
  "s_waitcnt lgkmcnt(8)\n\t" \
  "v_mov_b32 v148, v39\n\t" \
  "v_writelane_b32 v148, %[p4], 0\n\t" \
  "v_mfma_f32_16x16x32_bf16 v[140:143], v[92:95], v[8:11], v[140:143]\n\t" \
  "s_waitcnt lgkmcnt(3)\n\t" \
  "v_mov_b32 v152, v40\n\t" \
  "v_writelane_b32 v152, %[p5], 0\n\t" \
  "v_mfma_f32_16x16x32_bf16 v[144:147], v[100:103], v[8:11], v[144:147]\n\t" \
  "ds_bpermute_b32 v41, v32, v159\n\t" \
  "v_mfma_f32_16x16x32_bf16 v[148:151], v[108:111], v[8:11], v[148:151]\n\t" \
  "v_mov_b32 v159, v158\n\t" \
  "v_mfma_f32_16x16x32_bf16 v[152:155], v[116:119], v[8:11], v[152:155]\n\t" \
  "v_mov_b32 v158, v157\n\t" \
  "s_waitcnt lgkmcnt(11)\n\t" \
  "v_mfma_f32_16x16x32_bf16 v[128:131], v[64:67], v[12:15], v[128:131]\n\t" \
  "v_mov_b32 v157, v156\n\t" \
  "v_mfma_f32_16x16x32_bf16 v[132:135], v[72:75], v[12:15], v[132:135]\n\t" \
  "v_mfma_f32_16x16x32_bf16 v[136:139], v[80:83], v[12:15], v[136:139]\n\t" \
  "v_mfma_f32_16x16x32_bf16 v[140:143], v[88:91], v[12:15], v[140:143]\n\t" \
  "v_mfma_f32_16x16x32_bf16 v[144:147], v[96:99], v[12:15], v[144:147]\n\t" \
  "s_waitcnt lgkmcnt(0)\n\t" \
  "v_mov_b32 v156, v41\n\t" \
  "v_writelane_b32 v156, %[p6], 0\n\t" \
  "v_mfma_f32_16x16x32_bf16 v[148:151], v[104:107], v[12:15], v[148:151]\n\t" \
  "v_mfma_f32_16x16x32_bf16 v[152:155], v[112:115], v[12:15], v[152:155]\n\t" \
  "v_mfma_f32_16x16x32_bf16 v[156:159], v[124:127], v[8:11], v[156:159]\n\t" \
  "ds_read_b64 v[8:9], v28 offset:6144\n\t" \
  "ds_bpermute_b32 v34, v32, v131\n\t" \
  "v_readlane_b32 %[p0], v131, 63\n\t" \
  "v_mfma_f32_16x16x32_bf16 v[156:159], v[120:123], v[12:15], v[156:159]\n\t" \
  "ds_read_b64 v[10:11], v29 offset:6144\n\t" \
  "ds_read_b64 v[12:13], v30 offset:7168\n\t" \
  "v_mov_b32 v131, v130\n\t" \
  "s_waitcnt lgkmcnt(12)\n\t" \
  "v_mfma_f32_16x16x32_bf16 v[132:135], v[68:71], v[16:19], v[132:135]\n\t" \
  "ds_read_b64 v[14:15], v31 offset:7168\n\t" \
  "v_mov_b32 v130, v129\n\t" \
  "v_mfma_f32_16x16x32_bf16 v[136:139], v[76:79], v[16:19], v[136:139]\n\t" \
  "v_mov_b32 v129, v128\n\t" \
  "v_mfma_f32_16x16x32_bf16 v[140:143], v[84:87], v[16:19], v[140:143]\n\t" \
  "v_mfma_f32_16x16x32_bf16 v[144:147], v[92:95], v[16:19], v[144:147]\n\t" \
  "v_mfma_f32_16x16x32_bf16 v[148:151], v[100:103], v[16:19], v[148:151]\n\t" \
  "v_mfma_f32_16x16x32_bf16 v[152:155], v[108:111], v[16:19], v[152:155]\n\t" \
  "v_mfma_f32_16x16x32_bf16 v[156:159], v[116:119], v[16:19], v[156:159]\n\t" \
  "ds_read_b64 v[16:17], v24 offset:8192\n\t" \
  "s_waitcnt lgkmcnt(12)\n\t" \
  "v_mfma_f32_16x16x32_bf16 v[132:135], v[64:67], v[20:23], v[132:135]\n\t" \
  "ds_read_b64 v[18:19], v25 offset:8192\n\t" \
  "v_mfma_f32_16x16x32_bf16 v[136:139], v[72:75], v[20:23], v[136:139]\n\t" \
  "v_mfma_f32_16x16x32_bf16 v[140:143], v[80:83], v[20:23], v[140:143]\n\t" \
  "v_mfma_f32_16x16x32_bf16 v[144:147], v[88:91], v[20:23], v[144:147]\n\t" \
  "s_waitcnt lgkmcnt(5)\n\t" \
  "v_mov_b32 v128, v34\n\t" \
  "v_writelane_b32 v128, 0, 0\n\t" \
  "v_mfma_f32_16x16x32_bf16 v[148:151], v[96:99], v[20:23], v[148:151]\n\t" \
  "v_mfma_f32_16x16x32_bf16 v[152:155], v[104:107], v[20:23], v[152:155]\n\t" \
  "v_mfma_f32_16x16x32_bf16 v[156:159], v[112:115], v[20:23], v[156:159]\n\t" \
  "ds_read_b64 v[20:21], v26 offset:9216\n\t" \
  "s_waitcnt lgkmcnt(11)\n\t" \
  "v_mfma_f32_16x16x32_bf16 v[136:139], v[68:71], v[0:3], v[136:139]\n\t" \
  "ds_read_b64 v[22:23], v27 offset:9216\n\t" \
  "ds_bpermute_b32 v35, v32, v135\n\t" \
  "v_readlane_b32 %[p1], v135, 63\n\t" \
  "v_mfma_f32_16x16x32_bf16 v[140:143], v[76:79], v[0:3], v[140:143]\n\t" \
  "v_mov_b32 v135, v134\n\t" \
  "v_mfma_f32_16x16x32_bf16 v[144:147], v[84:87], v[0:3], v[144:147]\n\t" \
  "v_mov_b32 v134, v133\n\t" \
  "v_mfma_f32_16x16x32_bf16 v[148:151], v[92:95], v[0:3], v[148:151]\n\t" \
  "v_mov_b32 v133, v132\n\t" \
  "v_mfma_f32_16x16x32_bf16 v[152:155], v[100:103], v[0:3], v[152:155]\n\t" \
  "v_mfma_f32_16x16x32_bf16 v[156:159], v[108:111], v[0:3], v[156:159]\n\t" \
  "ds_read_b64 v[0:1], v28 offset:10240\n\t" \
  "s_waitcnt lgkmcnt(12)\n\t" \
  "v_mfma_f32_16x16x32_bf16 v[136:139], v[64:67], v[4:7], v[136:139]\n\t" \
  "ds_read_b64 v[2:3], v29 offset:10240\n\t" \
  "v_mfma_f32_16x16x32_bf16 v[140:143], v[72:75], v[4:7], v[140:143]\n\t" \
  "v_mfma_f32_16x16x32_bf16 v[144:147], v[80:83], v[4:7], v[144:147]\n\t" \
  "v_mfma_f32_16x16x32_bf16 v[148:151], v[88:91], v[4:7], v[148:151]\n\t" \
  "v_mfma_f32_16x16x32_bf16 v[152:155], v[96:99], v[4:7], v[152:155]\n\t" \
  "v_mfma_f32_16x16x32_bf16 v[156:159], v[104:107], v[4:7], v[156:159]\n\t" \
  "ds_read_b64 v[4:5], v30 offset:11264\n\t" \
  "s_waitcnt lgkmcnt(10)\n\t" \
  "v_mfma_f32_16x16x32_bf16 v[140:143], v[68:71], v[8:11], v[140:143]\n\t" \
  "ds_read_b64 v[6:7], v31 offset:11264\n\t" \
  "s_waitcnt lgkmcnt(4)\n\t" \
  "v_mov_b32 v132, v35\n\t" \
  "v_writelane_b32 v132, %[p0], 0\n\t" \
  "v_mfma_f32_16x16x32_bf16 v[144:147], v[76:79], v[8:11], v[144:147]\n\t" \
  "ds_bpermute_b32 v36, v32, v139\n\t" \
  "v_readlane_b32 %[p2], v139, 63\n\t" \
  "v_mfma_f32_16x16x32_bf16 v[148:151], v[84:87], v[8:11], v[148:151]\n\t" \
  "v_mov_b32 v139, v138\n\t" \
  "v_mfma_f32_16x16x32_bf16 v[152:155], v[92:95], v[8:11], v[152:155]\n\t" \
  "v_mov_b32 v138, v137\n\t" \
  "v_mfma_f32_16x16x32_bf16 v[156:159], v[100:103], v[8:11], v[156:159]\n\t" \
  "ds_read_b64 v[8:9], v24 offset:12288\n\t" \
  "v_mov_b32 v137, v136\n\t" \
  "s_waitcnt lgkmcnt(11)\n\t" \
  "v_mfma_f32_16x16x32_bf16 v[140:143], v[64:67], v[12:15], v[140:143]\n\t" \
  "ds_read_b64 v[10:11], v25 offset:12288\n\t" \
  "v_mfma_f32_16x16x32_bf16 v[144:147], v[72:75], v[12:15], v[144:147]\n\t" \
  "v_mfma_f32_16x16x32_bf16 v[148:151], v[80:83], v[12:15], v[148:151]\n\t" \
  "v_mfma_f32_16x16x32_bf16 v[152:155], v[88:91], v[12:15], v[152:155]\n\t" \
  "v_mfma_f32_16x16x32_bf16 v[156:159], v[96:99], v[12:15], v[156:159]\n\t" \
  "ds_read_b64 v[12:13], v26 offset:13312\n\t" \
  "s_waitcnt lgkmcnt(11)\n\t" \
  "v_mfma_f32_16x16x32_bf16 v[144:147], v[68:71], v[16:19], v[144:147]\n\t" \
  "ds_read_b64 v[14:15], v27 offset:13312\n\t" \
  "v_mfma_f32_16x16x32_bf16 v[148:151], v[76:79], v[16:19], v[148:151]\n\t" \
  "v_mfma_f32_16x16x32_bf16 v[152:155], v[84:87], v[16:19], v[152:155]\n\t" \
  "ds_bpermute_b32 v37, v32, v143\n\t" \
  "v_readlane_b32 %[p3], v143, 63\n\t" \
  "v_mfma_f32_16x16x32_bf16 v[156:159], v[92:95], v[16:19], v[156:159]\n\t" \
  "ds_read_b64 v[16:17], v28 offset:14336\n\t" \
  "s_waitcnt lgkmcnt(6)\n\t" \
  "v_mov_b32 v136, v36\n\t" \
  "v_writelane_b32 v136, %[p1], 0\n\t" \
  "v_mov_b32 v143, v142\n\t" \
  "s_waitcnt lgkmcnt(12)\n\t" \
  "v_mfma_f32_16x16x32_bf16 v[144:147], v[64:67], v[20:23], v[144:147]\n\t" \
  "ds_read_b64 v[18:19], v29 offset:14336\n\t" \
  "v_mov_b32 v142, v141\n\t" \
  "v_mfma_f32_16x16x32_bf16 v[148:151], v[72:75], v[20:23], v[148:151]\n\t" \
  "v_mov_b32 v141, v140\n\t" \
  "v_mfma_f32_16x16x32_bf16 v[152:155], v[80:83], v[20:23], v[152:155]\n\t" \
  "v_mfma_f32_16x16x32_bf16 v[156:159], v[88:91], v[20:23], v[156:159]\n\t" \
  "ds_read_b64 v[20:21], v30 offset:15360\n\t" \
  "s_waitcnt lgkmcnt(11)\n\t" \
  "v_mfma_f32_16x16x32_bf16 v[148:151], v[68:71], v[0:3], v[148:151]\n\t" \
  "ds_read_b64 v[22:23], v31 offset:15360\n\t" \
  "v_mfma_f32_16x16x32_bf16 v[152:155], v[76:79], v[0:3], v[152:155]\n\t" \
  "v_mfma_f32_16x16x32_bf16 v[156:159], v[84:87], v[0:3], v[156:159]\n\t" \
  "ds_read_b64 v[0:1], v30 offset:0\n\t" \
  "s_waitcnt lgkmcnt(11)\n\t" \
  "v_mfma_f32_16x16x32_bf16 v[148:151], v[64:67], v[4:7], v[148:151]\n\t" \
  "ds_read_b64 v[2:3], v31 offset:0\n\t" \
  "ds_bpermute_b32 v38, v32, v147\n\t" \
  "v_readlane_b32 %[p4], v147, 63\n\t" \
  "v_mfma_f32_16x16x32_bf16 v[152:155], v[72:75], v[4:7], v[152:155]\n\t" \
  "v_mov_b32 v147, v146\n\t" \
  "v_mfma_f32_16x16x32_bf16 v[156:159], v[80:83], v[4:7], v[156:159]\n\t" \
  "ds_read_b64 v[4:5], v24 offset:1024\n\t" \
  "v_mov_b32 v146, v145\n\t" \
  "s_waitcnt lgkmcnt(11)\n\t" \
  "v_mfma_f32_16x16x32_bf16 v[152:155], v[68:71], v[8:11], v[152:155]\n\t" \
  "ds_read_b64 v[6:7], v25 offset:1024\n\t" \
  "s_waitcnt lgkmcnt(9)\n\t" \
  "v_mov_b32 v140, v37\n\t" \
  "v_writelane_b32 v140, %[p2], 0\n\t" \
  "v_mov_b32 v145, v144\n\t" \
  "v_mfma_f32_16x16x32_bf16 v[156:159], v[76:79], v[8:11], v[156:159]\n\t" \
  "ds_read_b64 v[8:9], v26 offset:2048\n\t" \
  "ds_bpermute_b32 v39, v32, v151\n\t" \
  "v_readlane_b32 %[p5], v151, 63\n\t" \
  "s_waitcnt lgkmcnt(12)\n\t" \
  "v_mfma_f32_16x16x32_bf16 v[152:155], v[64:67], v[12:15], v[152:155]\n\t" \
  "ds_read_b64 v[10:11], v27 offset:2048\n\t" \
  "v_mov_b32 v151, v150\n\t" \
  "v_mfma_f32_16x16x32_bf16 v[156:159], v[72:75], v[12:15], v[156:159]\n\t" \
  "ds_read_b64 v[12:13], v28 offset:3072\n\t" \
  "v_mov_b32 v150, v149\n\t" \
  "s_waitcnt lgkmcnt(11)\n\t" \
  "v_mfma_f32_16x16x32_bf16 v[156:159], v[68:71], v[16:19], v[156:159]\n\t" \
  "ds_read_b64 v[14:15], v29 offset:3072\n\t" \
  "ds_read_b64 v[16:17], v30 offset:4096\n\t" \
  "v_mov_b32 v149, v148\n\t" \
  "ds_bpermute_b32 v40, v32, v155\n\t" \
  "v_readlane_b32 %[p6], v155, 63\n\t" \
  "s_waitcnt lgkmcnt(12)\n\t" \
  "v_mfma_f32_16x16x32_bf16 v[156:159], v[64:67], v[20:23], v[156:159]\n\t" \
  "ds_read_b64 v[18:19], v31 offset:4096\n\t" \
  "ds_read_b64 v[20:21], v24 offset:5120\n\t" \
  "v_mov_b32 v155, v154\n\t" \
  "s_waitcnt lgkmcnt(12)\n\t" \
  "v_mfma_f32_16x16x32_bf16 v[128:131], v[68:71], v[0:3], v[128:131]\n\t" \
  "ds_read_b64 v[22:23], v25 offset:5120\n\t" \
  "v_mov_b32 v154, v153\n\t" \
  "v_mfma_f32_16x16x32_bf16 v[132:135], v[76:79], v[0:3], v[132:135]\n\t" \
  "s_waitcnt lgkmcnt(12)\n\t" \
  "v_mov_b32 v144, v38\n\t" \
  "v_writelane_b32 v144, %[p3], 0\n\t" \
  "v_mov_b32 v153, v152\n\t" \
  "v_mfma_f32_16x16x32_bf16 v[136:139], v[84:87], v[0:3], v[136:139]\n\t" \
  "s_waitcnt lgkmcnt(8)\n\t" \
  "v_mov_b32 v148, v39\n\t" \
  "v_writelane_b32 v148, %[p4], 0\n\t" \
  "v_mfma_f32_16x16x32_bf16 v[140:143], v[92:95], v[0:3], v[140:143]\n\t" \
  "s_waitcnt lgkmcnt(3)\n\t" \
  "v_mov_b32 v152, v40\n\t" \
  "v_writelane_b32 v152, %[p5], 0\n\t" \
  "v_mfma_f32_16x16x32_bf16 v[144:147], v[100:103], v[0:3], v[144:147]\n\t" \
  "ds_bpermute_b32 v41, v32, v159\n\t" \
  "v_mfma_f32_16x16x32_bf16 v[148:151], v[108:111], v[0:3], v[148:151]\n\t" \
  "v_mov_b32 v159, v158\n\t" \
  "v_mfma_f32_16x16x32_bf16 v[152:155], v[116:119], v[0:3], v[152:155]\n\t" \
  "v_mov_b32 v158, v157\n\t" \
  "s_waitcnt lgkmcnt(11)\n\t" \
  "v_mfma_f32_16x16x32_bf16 v[128:131], v[64:67], v[4:7], v[128:131]\n\t" \
  "v_mov_b32 v157, v156\n\t" \
  "v_mfma_f32_16x16x32_bf16 v[132:135], v[72:75], v[4:7], v[132:135]\n\t" \
  "v_mfma_f32_16x16x32_bf16 v[136:139], v[80:83], v[4:7], v[136:139]\n\t" \
  "v_mfma_f32_16x16x32_bf16 v[140:143], v[88:91], v[4:7], v[140:143]\n\t" \
  "v_mfma_f32_16x16x32_bf16 v[144:147], v[96:99], v[4:7], v[144:147]\n\t" \
  "s_waitcnt lgkmcnt(0)\n\t" \
  "v_mov_b32 v156, v41\n\t" \
  "v_writelane_b32 v156, %[p6], 0\n\t" \
  "v_mfma_f32_16x16x32_bf16 v[148:151], v[104:107], v[4:7], v[148:151]\n\t" \
  "v_mfma_f32_16x16x32_bf16 v[152:155], v[112:115], v[4:7], v[152:155]\n\t" \
  "s_mov_b64 exec, 1\n\t" \
  "ds_write_b32 v33, v33\n\t" \
  "s_mov_b64 exec, -1\n\t" \
  "v_mfma_f32_16x16x32_bf16 v[156:159], v[124:127], v[0:3], v[156:159]\n\t" \
  "ds_read_b64 v[0:1], v26 offset:6144\n\t" \
  "v_mfma_f32_16x16x32_bf16 v[156:159], v[120:123], v[4:7], v[156:159]\n\t" \
  "ds_read_b64 v[2:3], v27 offset:6144\n\t" \
  "ds_read_b64 v[4:5], v28 offset:7168\n\t" \
  "s_waitcnt lgkmcnt(12)\n\t" \
  "v_mfma_f32_16x16x32_bf16 v[132:135], v[68:71], v[8:11], v[132:135]\n\t" \
  "ds_read_b64 v[6:7], v29 offset:7168\n\t" \
  "v_mfma_f32_16x16x32_bf16 v[136:139], v[76:79], v[8:11], v[136:139]\n\t" \
  "v_mfma_f32_16x16x32_bf16 v[140:143], v[84:87], v[8:11], v[140:143]\n\t" \
  "v_mfma_f32_16x16x32_bf16 v[144:147], v[92:95], v[8:11], v[144:147]\n\t" \
  "v_mfma_f32_16x16x32_bf16 v[148:151], v[100:103], v[8:11], v[148:151]\n\t" \
  "v_mfma_f32_16x16x32_bf16 v[152:155], v[108:111], v[8:11], v[152:155]\n\t" \
  "v_mfma_f32_16x16x32_bf16 v[156:159], v[116:119], v[8:11], v[156:159]\n\t" \
  "ds_read_b64 v[8:9], v30 offset:8192\n\t" \
  "s_waitcnt lgkmcnt(12)\n\t" \
  "v_mfma_f32_16x16x32_bf16 v[132:135], v[64:67], v[12:15], v[132:135]\n\t" \
  "ds_read_b64 v[10:11], v31 offset:8192\n\t" \
  "v_mfma_f32_16x16x32_bf16 v[136:139], v[72:75], v[12:15], v[136:139]\n\t" \
  "v_mfma_f32_16x16x32_bf16 v[140:143], v[80:83], v[12:15], v[140:143]\n\t" \
  "v_mfma_f32_16x16x32_bf16 v[144:147], v[88:91], v[12:15], v[144:147]\n\t" \
  "v_mfma_f32_16x16x32_bf16 v[148:151], v[96:99], v[12:15], v[148:151]\n\t" \
  "v_mfma_f32_16x16x32_bf16 v[152:155], v[104:107], v[12:15], v[152:155]\n\t" \
  "v_mfma_f32_16x16x32_bf16 v[156:159], v[112:115], v[12:15], v[156:159]\n\t" \
  "ds_read_b64 v[12:13], v24 offset:9216\n\t" \
  "s_waitcnt lgkmcnt(11)\n\t" \
  "v_mfma_f32_16x16x32_bf16 v[136:139], v[68:71], v[16:19], v[136:139]\n\t" \
  "ds_read_b64 v[14:15], v25 offset:9216\n\t" \
  "v_mfma_f32_16x16x32_bf16 v[140:143], v[76:79], v[16:19], v[140:143]\n\t" \
  "v_mfma_f32_16x16x32_bf16 v[144:147], v[84:87], v[16:19], v[144:147]\n\t" \
  "v_mfma_f32_16x16x32_bf16 v[148:151], v[92:95], v[16:19], v[148:151]\n\t" \
  "v_mfma_f32_16x16x32_bf16 v[152:155], v[100:103], v[16:19], v[152:155]\n\t" \
  "v_mfma_f32_16x16x32_bf16 v[156:159], v[108:111], v[16:19], v[156:159]\n\t" \
  "ds_read_b64 v[16:17], v26 offset:10240\n\t" \
  "s_waitcnt lgkmcnt(11)\n\t" \
  "v_mfma_f32_16x16x32_bf16 v[136:139], v[64:67], v[20:23], v[136:139]\n\t" \
  "ds_read_b64 v[18:19], v27 offset:10240\n\t" \
  "v_mfma_f32_16x16x32_bf16 v[140:143], v[72:75], v[20:23], v[140:143]\n\t" \
  "v_mfma_f32_16x16x32_bf16 v[144:147], v[80:83], v[20:23], v[144:147]\n\t" \
  "v_mfma_f32_16x16x32_bf16 v[148:151], v[88:91], v[20:23], v[148:151]\n\t" \
  "v_mfma_f32_16x16x32_bf16 v[152:155], v[96:99], v[20:23], v[152:155]\n\t" \
  "v_mfma_f32_16x16x32_bf16 v[156:159], v[104:107], v[20:23], v[156:159]\n\t" \
  "ds_read_b64 v[20:21], v28 offset:11264\n\t" \
  "s_waitcnt lgkmcnt(9)\n\t" \
  "v_mfma_f32_16x16x32_bf16 v[140:143], v[68:71], v[0:3], v[140:143]\n\t" \
  "ds_read_b64 v[22:23], v29 offset:11264\n\t" \
  "v_mfma_f32_16x16x32_bf16 v[144:147], v[76:79], v[0:3], v[144:147]\n\t" \
  "v_mfma_f32_16x16x32_bf16 v[148:151], v[84:87], v[0:3], v[148:151]\n\t" \
  "v_mfma_f32_16x16x32_bf16 v[152:155], v[92:95], v[0:3], v[152:155]\n\t" \
  "v_mfma_f32_16x16x32_bf16 v[156:159], v[100:103], v[0:3], v[156:159]\n\t" \
  "ds_read_b64 v[0:1], v30 offset:12288\n\t" \
  "s_waitcnt lgkmcnt(9)\n\t" \
  "v_mfma_f32_16x16x32_bf16 v[140:143], v[64:67], v[4:7], v[140:143]\n\t" \
  "ds_read_b64 v[2:3], v31 offset:12288\n\t" \
  "v_mfma_f32_16x16x32_bf16 v[144:147], v[72:75], v[4:7], v[144:147]\n\t" \
  "v_mfma_f32_16x16x32_bf16 v[148:151], v[80:83], v[4:7], v[148:151]\n\t" \
  "v_mfma_f32_16x16x32_bf16 v[152:155], v[88:91], v[4:7], v[152:155]\n\t" \
  "v_mfma_f32_16x16x32_bf16 v[156:159], v[96:99], v[4:7], v[156:159]\n\t" \
  "ds_read_b64 v[4:5], v24 offset:13312\n\t" \
  "s_waitcnt lgkmcnt(9)\n\t" \
  "v_mfma_f32_16x16x32_bf16 v[144:147], v[68:71], v[8:11], v[144:147]\n\t" \
  "ds_read_b64 v[6:7], v25 offset:13312\n\t" \
  "v_mfma_f32_16x16x32_bf16 v[148:151], v[76:79], v[8:11], v[148:151]\n\t" \
  "v_mfma_f32_16x16x32_bf16 v[152:155], v[84:87], v[8:11], v[152:155]\n\t" \
  "v_mfma_f32_16x16x32_bf16 v[156:159], v[92:95], v[8:11], v[156:159]\n\t" \
  "ds_read_b64 v[8:9], v26 offset:14336\n\t" \
  "s_waitcnt lgkmcnt(9)\n\t" \
  "v_mfma_f32_16x16x32_bf16 v[144:147], v[64:67], v[12:15], v[144:147]\n\t" \
  "ds_read_b64 v[10:11], v27 offset:14336\n\t" \
  "v_mfma_f32_16x16x32_bf16 v[148:151], v[72:75], v[12:15], v[148:151]\n\t" \
  "v_mfma_f32_16x16x32_bf16 v[152:155], v[80:83], v[12:15], v[152:155]\n\t" \
  "v_mfma_f32_16x16x32_bf16 v[156:159], v[88:91], v[12:15], v[156:159]\n\t" \
  "ds_read_b64 v[12:13], v28 offset:15360\n\t" \
  "s_waitcnt lgkmcnt(9)\n\t" \
  "v_mfma_f32_16x16x32_bf16 v[148:151], v[68:71], v[16:19], v[148:151]\n\t" \
  "ds_read_b64 v[14:15], v29 offset:15360\n\t" \
  "v_mfma_f32_16x16x32_bf16 v[152:155], v[76:79], v[16:19], v[152:155]\n\t" \
  "v_mfma_f32_16x16x32_bf16 v[156:159], v[84:87], v[16:19], v[156:159]\n\t" \
  "s_waitcnt lgkmcnt(8)\n\t" \
  "v_mfma_f32_16x16x32_bf16 v[148:151], v[64:67], v[20:23], v[148:151]\n\t" \
  "v_mfma_f32_16x16x32_bf16 v[152:155], v[72:75], v[20:23], v[152:155]\n\t" \
  "v_mfma_f32_16x16x32_bf16 v[156:159], v[80:83], v[20:23], v[156:159]\n\t" \
  "s_waitcnt lgkmcnt(6)\n\t" \
  "v_mfma_f32_16x16x32_bf16 v[152:155], v[68:71], v[0:3], v[152:155]\n\t" \
  "v_mfma_f32_16x16x32_bf16 v[156:159], v[76:79], v[0:3], v[156:159]\n\t" \
  "s_waitcnt lgkmcnt(4)\n\t" \
  "v_mfma_f32_16x16x32_bf16 v[152:155], v[64:67], v[4:7], v[152:155]\n\t" \
  "v_mfma_f32_16x16x32_bf16 v[156:159], v[72:75], v[4:7], v[156:159]\n\t" \
  "s_waitcnt lgkmcnt(2)\n\t" \
  "v_mfma_f32_16x16x32_bf16 v[156:159], v[68:71], v[8:11], v[156:159]\n\t" \
  "s_waitcnt lgkmcnt(0)\n\t" \
  "v_mfma_f32_16x16x32_bf16 v[156:159], v[64:67], v[12:15], v[156:159]\n\t" \
  "s_waitcnt lgkmcnt(0)\n\t" \
  "s_nop 7\n\t"
#define CV_Q 1
#define CV_CLOB "v0", "v1", "v2", "v3", "v4", "v5", "v6", "v7", "v8", "v9", "v10", "v11", "v12", "v13", "v14", "v15", "v16", "v17", "v18", "v19", "v20", "v21", "v22", "v23", "v34", "v35", "v36", "v37", "v38", "v39", "v40", "v41"

#define G3_ASM \
  "s_getpc_b64 s[92:93]\n\t" \
  "s_branch QC_%=\n\t" \
  "ds_read_b64_tr_b16 v[96:97], v160 offset:0\n\t" \
  "ds_read_b64_tr_b16 v[98:99], v160 offset:2048\n\t" \
  "ds_read_b64_tr_b16 v[100:101], v161 offset:0\n\t" \
  "ds_read_b64_tr_b16 v[102:103], v161 offset:2048\n\t" \
  "ds_read_b64_tr_b16 v[104:105], v162 offset:0\n\t" \
  "ds_read_b64_tr_b16 v[106:107], v162 offset:2048\n\t" \
  "ds_read_b64_tr_b16 v[108:109], v163 offset:0\n\t" \
  "ds_read_b64_tr_b16 v[110:111], v163 offset:2048\n\t" \
  "ds_read_b64_tr_b16 v[112:113], v160 offset:4096\n\t" \
  "ds_read_b64_tr_b16 v[114:115], v160 offset:6144\n\t" \
  "ds_read_b64_tr_b16 v[116:117], v161 offset:4096\n\t" \
  "ds_read_b64_tr_b16 v[118:119], v161 offset:6144\n\t" \
  "ds_read_b64_tr_b16 v[120:121], v162 offset:4096\n\t" \
  "ds_read_b64_tr_b16 v[122:123], v162 offset:6144\n\t" \
  "QH_%=:\n\t" \
  "v_add_u32 v166, %[go], v165\n\t" \
  "ds_read_b32 v166, v166\n\t" \
  "s_addk_i32 %[cn], 1\n\t" \
  "s_cmp_gt_u32 %[cn], 0x400000\n\t" \
  "s_cbranch_scc1 QI_%=\n\t" \
  "s_waitcnt lgkmcnt(0)\n\t" \
  "v_cmp_eq_u32 vcc, 0, v166\n\t" \
  "s_cbranch_vccz QI_%=\n\t" \
  "s_sleep 2\n\t" \
  "s_getpc_b64 s[92:93]\n\t" \
  "s_branch QC_%=\n\t" \
  "s_branch QH_%=\n\t" \
  "QI_%=:\n\t" \
  "s_setprio 0\n\t" \
  "ds_read_b32 v166, v165\n\t" \
  "s_waitcnt lgkmcnt(11)\n\t" \
  "v_mfma_f32_16x16x32_bf16 v[128:131], v[96:99], v[0:3], 0\n\t" \
  "ds_read_b64_tr_b16 v[124:125], v163 offset:4096\n\t" \
  "v_readlane_b32 s94, v167, 5\n\t" \
  "v_mfma_f32_16x16x32_bf16 v[144:147], v[96:99], v[4:7], 0\n\t" \
  "ds_read_b64_tr_b16 v[126:127], v163 offset:6144\n\t" \
  "s_waitcnt lgkmcnt(2)\n\t" \
  "v_cmp_lt_u32 vcc, %[th], v166\n\t" \
  "v_mfma_f32_16x16x32_bf16 v[132:135], v[100:103], v[0:3], 0\n\t" \
  "s_mov_b64 exec, vcc\n\t" \
  "buffer_load_dwordx4 v[80:83], v164, %[ob], s94 offen offset:0 sc1\n\t" \
  "s_mov_b64 exec, -1\n\t" \
  "v_mfma_f32_16x16x32_bf16 v[148:151], v[100:103], v[4:7], 0\n\t" \
  "ds_read_b64_tr_b16 v[96:97], v160 offset:8192\n\t" \
  "ds_read_b64_tr_b16 v[98:99], v160 offset:10240\n\t" \
  "v_mfma_f32_16x16x32_bf16 v[136:139], v[104:107], v[0:3], 0\n\t" \
  "s_mov_b64 exec, vcc\n\t" \
  "buffer_load_dwordx4 v[84:87], v164, %[ob], s94 offen offset:1024 sc1\n\t" \
  "s_mov_b64 exec, -1\n\t" \
  "v_mfma_f32_16x16x32_bf16 v[152:155], v[104:107], v[4:7], 0\n\t" \
  "ds_read_b64_tr_b16 v[100:101], v161 offset:8192\n\t" \
  "ds_read_b64_tr_b16 v[102:103], v161 offset:10240\n\t" \
  "v_mfma_f32_16x16x32_bf16 v[140:143], v[108:111], v[0:3], 0\n\t" \
  "s_mov_b64 exec, vcc\n\t" \
  "buffer_load_dwordx4 v[88:91], v164, %[ob], s94 offen offset:2048 sc1\n\t" \
  "s_mov_b64 exec, -1\n\t" \
  "v_mfma_f32_16x16x32_bf16 v[156:159], v[108:111], v[4:7], 0\n\t" \
  "ds_read_b64_tr_b16 v[104:105], v162 offset:8192\n\t" \
  "ds_read_b64_tr_b16 v[106:107], v162 offset:10240\n\t" \
  "v_mfma_f32_16x16x32_bf16 v[128:131], v[112:115], v[8:11], v[128:131]\n\t" \
  "s_mov_b64 exec, vcc\n\t" \
  "buffer_load_dwordx4 v[92:95], v164, %[ob], s94 offen offset:3072 sc1\n\t" \
  "s_mov_b64 exec, -1\n\t" \
  "v_mfma_f32_16x16x32_bf16 v[144:147], v[112:115], v[12:15], v[144:147]\n\t" \
  "ds_read_b64_tr_b16 v[108:109], v163 offset:8192\n\t" \
  "ds_read_b64_tr_b16 v[110:111], v163 offset:10240\n\t" \
  "v_mfma_f32_16x16x32_bf16 v[132:135], v[116:119], v[8:11], v[132:135]\n\t" \
  "ds_read_b64_tr_b16 v[112:113], v160 offset:12288\n\t" \
  "ds_read_b32 v166, v165\n\t" \
  "v_mfma_f32_16x16x32_bf16 v[148:151], v[116:119], v[12:15], v[148:151]\n\t" \
  "ds_read_b64_tr_b16 v[114:115], v160 offset:14336\n\t" \
  "s_waitcnt lgkmcnt(11)\n\t" \
  "v_mfma_f32_16x16x32_bf16 v[136:139], v[120:123], v[8:11], v[136:139]\n\t" \
  "ds_read_b64_tr_b16 v[116:117], v161 offset:12288\n\t" \
  "v_mfma_f32_16x16x32_bf16 v[152:155], v[120:123], v[12:15], v[152:155]\n\t" \
  "ds_read_b64_tr_b16 v[118:119], v161 offset:14336\n\t" \
  "s_sub_u32 %[iss], %[iss], vcc_lo\n\t" \
  "v_mfma_f32_16x16x32_bf16 v[140:143], v[124:127], v[8:11], v[140:143]\n\t" \
  "ds_read_b64_tr_b16 v[120:121], v162 offset:12288\n\t" \
  "v_mfma_f32_16x16x32_bf16 v[156:159], v[124:127], v[12:15], v[156:159]\n\t" \
  "ds_read_b64_tr_b16 v[122:123], v162 offset:14336\n\t" \
  "s_add_u32 %[th], %[th], 1\n\t" \
  "s_cbranch_vccz QF1_%=\n\t" \
  "s_waitcnt vmcnt(16)\n\t" \
  "QG1_%=:\n\t" \
  "s_waitcnt lgkmcnt(11)\n\t" \
  "v_mfma_f32_16x16x32_bf16 v[128:131], v[96:99], v[16:19], v[128:131]\n\t" \
  "ds_read_b64_tr_b16 v[124:125], v163 offset:12288\n\t" \
  "v_readlane_b32 s94, v167, 6\n\t" \
  "v_mfma_f32_16x16x32_bf16 v[144:147], v[96:99], v[20:23], v[144:147]\n\t" \
  "ds_read_b64_tr_b16 v[126:127], v163 offset:14336\n\t" \
  "s_waitcnt lgkmcnt(7)\n\t" \
  "v_cmp_lt_u32 vcc, %[th], v166\n\t" \
  "v_mfma_f32_16x16x32_bf16 v[132:135], v[100:103], v[16:19], v[132:135]\n\t" \
  "s_mov_b64 exec, vcc\n\t" \
  "buffer_load_dwordx4 v[0:3], v164, %[ob], s94 offen offset:0 sc1\n\t" \
  "s_mov_b64 exec, -1\n\t" \
  "v_mfma_f32_16x16x32_bf16 v[148:151], v[100:103], v[20:23], v[148:151]\n\t" \
  "ds_read_b64_tr_b16 v[96:97], v160 offset:16384\n\t" \
  "ds_read_b64_tr_b16 v[98:99], v160 offset:18432\n\t" \
  "v_mfma_f32_16x16x32_bf16 v[136:139], v[104:107], v[16:19], v[136:139]\n\t" \
  "s_mov_b64 exec, vcc\n\t" \
  "buffer_load_dwordx4 v[4:7], v164, %[ob], s94 offen offset:1024 sc1\n\t" \
  "s_mov_b64 exec, -1\n\t" \
  "v_mfma_f32_16x16x32_bf16 v[152:155], v[104:107], v[20:23], v[152:155]\n\t" \
  "ds_read_b64_tr_b16 v[100:101], v161 offset:16384\n\t" \
  "ds_read_b64_tr_b16 v[102:103], v161 offset:18432\n\t" \
  "v_mfma_f32_16x16x32_bf16 v[140:143], v[108:111], v[16:19], v[140:143]\n\t" \
  "s_mov_b64 exec, vcc\n\t" \
  "buffer_load_dwordx4 v[8:11], v164, %[ob], s94 offen offset:2048 sc1\n\t" \
  "s_mov_b64 exec, -1\n\t" \
  "v_mfma_f32_16x16x32_bf16 v[156:159], v[108:111], v[20:23], v[156:159]\n\t" \
  "ds_read_b64_tr_b16 v[104:105], v162 offset:16384\n\t" \
  "ds_read_b64_tr_b16 v[106:107], v162 offset:18432\n\t" \
  "s_waitcnt lgkmcnt(10)\n\t" \
  "v_mfma_f32_16x16x32_bf16 v[128:131], v[112:115], v[24:27], v[128:131]\n\t" \
  "s_mov_b64 exec, vcc\n\t" \
  "buffer_load_dwordx4 v[12:15], v164, %[ob], s94 offen offset:3072 sc1\n\t" \
  "s_mov_b64 exec, -1\n\t" \
  "v_mfma_f32_16x16x32_bf16 v[144:147], v[112:115], v[28:31], v[144:147]\n\t" \
  "ds_read_b64_tr_b16 v[108:109], v163 offset:16384\n\t" \
  "ds_read_b64_tr_b16 v[110:111], v163 offset:18432\n\t" \
  "v_mfma_f32_16x16x32_bf16 v[132:135], v[116:119], v[24:27], v[132:135]\n\t" \
  "ds_read_b64_tr_b16 v[112:113], v160 offset:20480\n\t" \
  "ds_read_b32 v166, v165\n\t" \
  "v_mfma_f32_16x16x32_bf16 v[148:151], v[116:119], v[28:31], v[148:151]\n\t" \
  "ds_read_b64_tr_b16 v[114:115], v160 offset:22528\n\t" \
  "s_waitcnt lgkmcnt(11)\n\t" \
  "v_mfma_f32_16x16x32_bf16 v[136:139], v[120:123], v[24:27], v[136:139]\n\t" \
  "ds_read_b64_tr_b16 v[116:117], v161 offset:20480\n\t" \
  "v_mfma_f32_16x16x32_bf16 v[152:155], v[120:123], v[28:31], v[152:155]\n\t" \
  "ds_read_b64_tr_b16 v[118:119], v161 offset:22528\n\t" \
  "s_sub_u32 %[iss], %[iss], vcc_lo\n\t" \
  "v_mfma_f32_16x16x32_bf16 v[140:143], v[124:127], v[24:27], v[140:143]\n\t" \
  "ds_read_b64_tr_b16 v[120:121], v162 offset:20480\n\t" \
  "v_mfma_f32_16x16x32_bf16 v[156:159], v[124:127], v[28:31], v[156:159]\n\t" \
  "ds_read_b64_tr_b16 v[122:123], v162 offset:22528\n\t" \
  "s_add_u32 %[th], %[th], 1\n\t" \
  "s_cbranch_vccz QF2_%=\n\t" \
  "s_waitcnt vmcnt(16)\n\t" \
  "QG2_%=:\n\t" \
  "s_waitcnt lgkmcnt(11)\n\t" \
  "v_mfma_f32_16x16x32_bf16 v[128:131], v[96:99], v[32:35], v[128:131]\n\t" \
  "ds_read_b64_tr_b16 v[124:125], v163 offset:20480\n\t" \
  "v_readlane_b32 s94, v167, 7\n\t" \
  "v_mfma_f32_16x16x32_bf16 v[144:147], v[96:99], v[36:39], v[144:147]\n\t" \
  "ds_read_b64_tr_b16 v[126:127], v163 offset:22528\n\t" \
  "s_waitcnt lgkmcnt(7)\n\t" \
  "v_cmp_lt_u32 vcc, %[th], v166\n\t" \
  "v_mfma_f32_16x16x32_bf16 v[132:135], v[100:103], v[32:35], v[132:135]\n\t" \
  "s_mov_b64 exec, vcc\n\t" \
  "buffer_load_dwordx4 v[16:19], v164, %[ob], s94 offen offset:0 sc1\n\t" \
  "s_mov_b64 exec, -1\n\t" \
  "v_mfma_f32_16x16x32_bf16 v[148:151], v[100:103], v[36:39], v[148:151]\n\t" \
  "ds_read_b64_tr_b16 v[96:97], v160 offset:24576\n\t" \
  "ds_read_b64_tr_b16 v[98:99], v160 offset:26624\n\t" \
  "v_mfma_f32_16x16x32_bf16 v[136:139], v[104:107], v[32:35], v[136:139]\n\t" \
  "s_mov_b64 exec, vcc\n\t" \
  "buffer_load_dwordx4 v[20:23], v164, %[ob], s94 offen offset:1024 sc1\n\t" \
  "s_mov_b64 exec, -1\n\t" \
  "v_mfma_f32_16x16x32_bf16 v[152:155], v[104:107], v[36:39], v[152:155]\n\t" \
  "ds_read_b64_tr_b16 v[100:101], v161 offset:24576\n\t" \
  "ds_read_b64_tr_b16 v[102:103], v161 offset:26624\n\t" \
  "v_mfma_f32_16x16x32_bf16 v[140:143], v[108:111], v[32:35], v[140:143]\n\t" \
  "s_mov_b64 exec, vcc\n\t" \
  "buffer_load_dwordx4 v[24:27], v164, %[ob], s94 offen offset:2048 sc1\n\t" \
  "s_mov_b64 exec, -1\n\t" \
  "v_mfma_f32_16x16x32_bf16 v[156:159], v[108:111], v[36:39], v[156:159]\n\t" \
  "ds_read_b64_tr_b16 v[104:105], v162 offset:24576\n\t" \
  "ds_read_b64_tr_b16 v[106:107], v162 offset:26624\n\t" \
  "s_waitcnt lgkmcnt(10)\n\t" \
  "v_mfma_f32_16x16x32_bf16 v[128:131], v[112:115], v[40:43], v[128:131]\n\t" \
  "s_mov_b64 exec, vcc\n\t" \
  "buffer_load_dwordx4 v[28:31], v164, %[ob], s94 offen offset:3072 sc1\n\t" \
  "s_mov_b64 exec, -1\n\t" \
  "v_mfma_f32_16x16x32_bf16 v[144:147], v[112:115], v[44:47], v[144:147]\n\t" \
  "ds_read_b64_tr_b16 v[108:109], v163 offset:24576\n\t" \
  "ds_read_b64_tr_b16 v[110:111], v163 offset:26624\n\t" \
  "v_mfma_f32_16x16x32_bf16 v[132:135], v[116:119], v[40:43], v[132:135]\n\t" \
  "ds_read_b64_tr_b16 v[112:113], v160 offset:28672\n\t" \
  "ds_read_b32 v166, v165\n\t" \
  "v_mfma_f32_16x16x32_bf16 v[148:151], v[116:119], v[44:47], v[148:151]\n\t" \
  "ds_read_b64_tr_b16 v[114:115], v160 offset:30720\n\t" \
  "s_waitcnt lgkmcnt(11)\n\t" \
  "v_mfma_f32_16x16x32_bf16 v[136:139], v[120:123], v[40:43], v[136:139]\n\t" \
  "ds_read_b64_tr_b16 v[116:117], v161 offset:28672\n\t" \
  "v_mfma_f32_16x16x32_bf16 v[152:155], v[120:123], v[44:47], v[152:155]\n\t" \
  "ds_read_b64_tr_b16 v[118:119], v161 offset:30720\n\t" \
  "s_sub_u32 %[iss], %[iss], vcc_lo\n\t" \
  "v_mfma_f32_16x16x32_bf16 v[140:143], v[124:127], v[40:43], v[140:143]\n\t" \
  "ds_read_b64_tr_b16 v[120:121], v162 offset:28672\n\t" \
  "v_mfma_f32_16x16x32_bf16 v[156:159], v[124:127], v[44:47], v[156:159]\n\t" \
  "ds_read_b64_tr_b16 v[122:123], v162 offset:30720\n\t" \
  "s_add_u32 %[th], %[th], 1\n\t" \
  "s_cbranch_vccz QF3_%=\n\t" \
  "s_waitcnt vmcnt(16)\n\t" \
  "QG3_%=:\n\t" \
  "s_waitcnt lgkmcnt(11)\n\t" \
  "v_mfma_f32_16x16x32_bf16 v[128:131], v[96:99], v[48:51], v[128:131]\n\t" \
  "ds_read_b64_tr_b16 v[124:125], v163 offset:28672\n\t" \
  "v_readlane_b32 s94, v167, 8\n\t" \
  "v_mfma_f32_16x16x32_bf16 v[144:147], v[96:99], v[52:55], v[144:147]\n\t" \
  "ds_read_b64_tr_b16 v[126:127], v163 offset:30720\n\t" \
  "s_waitcnt lgkmcnt(7)\n\t" \
  "v_cmp_lt_u32 vcc, %[th], v166\n\t" \
  "v_mfma_f32_16x16x32_bf16 v[132:135], v[100:103], v[48:51], v[132:135]\n\t" \
  "s_mov_b64 exec, vcc\n\t" \
  "buffer_load_dwordx4 v[32:35], v164, %[ob], s94 offen offset:0 sc1\n\t" \
  "s_mov_b64 exec, -1\n\t" \
  "v_mfma_f32_16x16x32_bf16 v[148:151], v[100:103], v[52:55], v[148:151]\n\t" \
  "ds_read_b64_tr_b16 v[96:97], v160 offset:32768\n\t" \
  "ds_read_b64_tr_b16 v[98:99], v160 offset:34816\n\t" \
  "v_mfma_f32_16x16x32_bf16 v[136:139], v[104:107], v[48:51], v[136:139]\n\t" \
  "s_mov_b64 exec, vcc\n\t" \
  "buffer_load_dwordx4 v[36:39], v164, %[ob], s94 offen offset:1024 sc1\n\t" \
  "s_mov_b64 exec, -1\n\t" \
  "v_mfma_f32_16x16x32_bf16 v[152:155], v[104:107], v[52:55], v[152:155]\n\t" \
  "ds_read_b64_tr_b16 v[100:101], v161 offset:32768\n\t" \
  "ds_read_b64_tr_b16 v[102:103], v161 offset:34816\n\t" \
  "v_mfma_f32_16x16x32_bf16 v[140:143], v[108:111], v[48:51], v[140:143]\n\t" \
  "s_mov_b64 exec, vcc\n\t" \
  "buffer_load_dwordx4 v[40:43], v164, %[ob], s94 offen offset:2048 sc1\n\t" \
  "s_mov_b64 exec, -1\n\t" \
  "v_mfma_f32_16x16x32_bf16 v[156:159], v[108:111], v[52:55], v[156:159]\n\t" \
  "ds_read_b64_tr_b16 v[104:105], v162 offset:32768\n\t" \
  "ds_read_b64_tr_b16 v[106:107], v162 offset:34816\n\t" \
  "s_waitcnt lgkmcnt(10)\n\t" \
  "v_mfma_f32_16x16x32_bf16 v[128:131], v[112:115], v[56:59], v[128:131]\n\t" \
  "s_mov_b64 exec, vcc\n\t" \
  "buffer_load_dwordx4 v[44:47], v164, %[ob], s94 offen offset:3072 sc1\n\t" \
  "s_mov_b64 exec, -1\n\t" \
  "v_mfma_f32_16x16x32_bf16 v[144:147], v[112:115], v[60:63], v[144:147]\n\t" \
  "ds_read_b64_tr_b16 v[108:109], v163 offset:32768\n\t" \
  "ds_read_b64_tr_b16 v[110:111], v163 offset:34816\n\t" \
  "v_mfma_f32_16x16x32_bf16 v[132:135], v[116:119], v[56:59], v[132:135]\n\t" \
  "ds_read_b64_tr_b16 v[112:113], v160 offset:36864\n\t" \
  "ds_read_b32 v166, v165\n\t" \
  "v_mfma_f32_16x16x32_bf16 v[148:151], v[116:119], v[60:63], v[148:151]\n\t" \
  "ds_read_b64_tr_b16 v[114:115], v160 offset:38912\n\t" \
  "s_waitcnt lgkmcnt(11)\n\t" \
  "v_mfma_f32_16x16x32_bf16 v[136:139], v[120:123], v[56:59], v[136:139]\n\t" \
  "ds_read_b64_tr_b16 v[116:117], v161 offset:36864\n\t" \
  "v_mfma_f32_16x16x32_bf16 v[152:155], v[120:123], v[60:63], v[152:155]\n\t" \
  "ds_read_b64_tr_b16 v[118:119], v161 offset:38912\n\t" \
  "s_sub_u32 %[iss], %[iss], vcc_lo\n\t" \
  "v_mfma_f32_16x16x32_bf16 v[140:143], v[124:127], v[56:59], v[140:143]\n\t" \
  "ds_read_b64_tr_b16 v[120:121], v162 offset:36864\n\t" \
  "v_mfma_f32_16x16x32_bf16 v[156:159], v[124:127], v[60:63], v[156:159]\n\t" \
  "ds_read_b64_tr_b16 v[122:123], v162 offset:38912\n\t" \
  "s_add_u32 %[th], %[th], 1\n\t" \
  "s_cbranch_vccz QF4_%=\n\t" \
  "s_waitcnt vmcnt(16)\n\t" \
  "QG4_%=:\n\t" \
  "s_waitcnt lgkmcnt(11)\n\t" \
  "v_mfma_f32_16x16x32_bf16 v[128:131], v[96:99], v[64:67], v[128:131]\n\t" \
  "ds_read_b64_tr_b16 v[124:125], v163 offset:36864\n\t" \
  "v_readlane_b32 s94, v167, 9\n\t" \
  "v_mfma_f32_16x16x32_bf16 v[144:147], v[96:99], v[68:71], v[144:147]\n\t" \
  "ds_read_b64_tr_b16 v[126:127], v163 offset:38912\n\t" \
  "s_waitcnt lgkmcnt(7)\n\t" \
  "v_cmp_lt_u32 vcc, %[th], v166\n\t" \
  "v_mfma_f32_16x16x32_bf16 v[132:135], v[100:103], v[64:67], v[132:135]\n\t" \
  "s_mov_b64 exec, vcc\n\t" \
  "buffer_load_dwordx4 v[48:51], v164, %[ob], s94 offen offset:0 sc1\n\t" \
  "s_mov_b64 exec, -1\n\t" \
  "v_mfma_f32_16x16x32_bf16 v[148:151], v[100:103], v[68:71], v[148:151]\n\t" \
  "ds_read_b64_tr_b16 v[96:97], v160 offset:40960\n\t" \
  "ds_read_b64_tr_b16 v[98:99], v160 offset:43008\n\t" \
  "v_mfma_f32_16x16x32_bf16 v[136:139], v[104:107], v[64:67], v[136:139]\n\t" \
  "s_mov_b64 exec, vcc\n\t" \
  "buffer_load_dwordx4 v[52:55], v164, %[ob], s94 offen offset:1024 sc1\n\t" \
  "s_mov_b64 exec, -1\n\t" \
  "v_mfma_f32_16x16x32_bf16 v[152:155], v[104:107], v[68:71], v[152:155]\n\t" \
  "ds_read_b64_tr_b16 v[100:101], v161 offset:40960\n\t" \
  "ds_read_b64_tr_b16 v[102:103], v161 offset:43008\n\t" \
  "v_mfma_f32_16x16x32_bf16 v[140:143], v[108:111], v[64:67], v[140:143]\n\t" \
  "s_mov_b64 exec, vcc\n\t" \
  "buffer_load_dwordx4 v[56:59], v164, %[ob], s94 offen offset:2048 sc1\n\t" \
  "s_mov_b64 exec, -1\n\t" \
  "v_mfma_f32_16x16x32_bf16 v[156:159], v[108:111], v[68:71], v[156:159]\n\t" \
  "ds_read_b64_tr_b16 v[104:105], v162 offset:40960\n\t" \
  "ds_read_b64_tr_b16 v[106:107], v162 offset:43008\n\t" \
  "s_waitcnt lgkmcnt(10)\n\t" \
  "v_mfma_f32_16x16x32_bf16 v[128:131], v[112:115], v[72:75], v[128:131]\n\t" \
  "s_mov_b64 exec, vcc\n\t" \
  "buffer_load_dwordx4 v[60:63], v164, %[ob], s94 offen offset:3072 sc1\n\t" \
  "s_mov_b64 exec, -1\n\t" \
  "v_mfma_f32_16x16x32_bf16 v[144:147], v[112:115], v[76:79], v[144:147]\n\t" \
  "ds_read_b64_tr_b16 v[108:109], v163 offset:40960\n\t" \
  "ds_read_b64_tr_b16 v[110:111], v163 offset:43008\n\t" \
  "v_mfma_f32_16x16x32_bf16 v[132:135], v[116:119], v[72:75], v[132:135]\n\t" \
  "ds_read_b64_tr_b16 v[112:113], v160 offset:45056\n\t" \
  "ds_read_b32 v166, v165\n\t" \
  "v_mfma_f32_16x16x32_bf16 v[148:151], v[116:119], v[76:79], v[148:151]\n\t" \
  "ds_read_b64_tr_b16 v[114:115], v160 offset:47104\n\t" \
  "s_waitcnt lgkmcnt(11)\n\t" \
  "v_mfma_f32_16x16x32_bf16 v[136:139], v[120:123], v[72:75], v[136:139]\n\t" \
  "ds_read_b64_tr_b16 v[116:117], v161 offset:45056\n\t" \
  "v_mfma_f32_16x16x32_bf16 v[152:155], v[120:123], v[76:79], v[152:155]\n\t" \
  "ds_read_b64_tr_b16 v[118:119], v161 offset:47104\n\t" \
  "s_sub_u32 %[iss], %[iss], vcc_lo\n\t" \
  "v_mfma_f32_16x16x32_bf16 v[140:143], v[124:127], v[72:75], v[140:143]\n\t" \
  "ds_read_b64_tr_b16 v[120:121], v162 offset:45056\n\t" \
  "v_mfma_f32_16x16x32_bf16 v[156:159], v[124:127], v[76:79], v[156:159]\n\t" \
  "ds_read_b64_tr_b16 v[122:123], v162 offset:47104\n\t" \
  "s_add_u32 %[th], %[th], 1\n\t" \
  "s_cbranch_vccz QF5_%=\n\t" \
  "s_waitcnt vmcnt(16)\n\t" \
  "QG5_%=:\n\t" \
  "s_waitcnt lgkmcnt(11)\n\t" \
  "v_mfma_f32_16x16x32_bf16 v[128:131], v[96:99], v[80:83], v[128:131]\n\t" \
  "ds_read_b64_tr_b16 v[124:125], v163 offset:45056\n\t" \
  "v_readlane_b32 s94, v167, 10\n\t" \
  "v_mfma_f32_16x16x32_bf16 v[144:147], v[96:99], v[84:87], v[144:147]\n\t" \
  "ds_read_b64_tr_b16 v[126:127], v163 offset:47104\n\t" \
  "s_waitcnt lgkmcnt(7)\n\t" \
  "v_cmp_lt_u32 vcc, %[th], v166\n\t" \
  "v_mfma_f32_16x16x32_bf16 v[132:135], v[100:103], v[80:83], v[132:135]\n\t" \
  "s_mov_b64 exec, vcc\n\t" \
  "buffer_load_dwordx4 v[64:67], v164, %[ob], s94 offen offset:0 sc1\n\t" \
  "s_mov_b64 exec, -1\n\t" \
  "v_mfma_f32_16x16x32_bf16 v[148:151], v[100:103], v[84:87], v[148:151]\n\t" \
  "ds_read_b64_tr_b16 v[96:97], v160 offset:49152\n\t" \
  "ds_read_b64_tr_b16 v[98:99], v160 offset:51200\n\t" \
  "v_mfma_f32_16x16x32_bf16 v[136:139], v[104:107], v[80:83], v[136:139]\n\t" \
  "s_mov_b64 exec, vcc\n\t" \
  "buffer_load_dwordx4 v[68:71], v164, %[ob], s94 offen offset:1024 sc1\n\t" \
  "s_mov_b64 exec, -1\n\t" \
  "v_mfma_f32_16x16x32_bf16 v[152:155], v[104:107], v[84:87], v[152:155]\n\t" \
  "ds_read_b64_tr_b16 v[100:101], v161 offset:49152\n\t" \
  "ds_read_b64_tr_b16 v[102:103], v161 offset:51200\n\t" \
  "v_mfma_f32_16x16x32_bf16 v[140:143], v[108:111], v[80:83], v[140:143]\n\t" \
  "s_mov_b64 exec, vcc\n\t" \
  "buffer_load_dwordx4 v[72:75], v164, %[ob], s94 offen offset:2048 sc1\n\t" \
  "s_mov_b64 exec, -1\n\t" \
  "v_mfma_f32_16x16x32_bf16 v[156:159], v[108:111], v[84:87], v[156:159]\n\t" \
  "ds_read_b64_tr_b16 v[104:105], v162 offset:49152\n\t" \
  "ds_read_b64_tr_b16 v[106:107], v162 offset:51200\n\t" \
  "s_waitcnt lgkmcnt(10)\n\t" \
  "v_mfma_f32_16x16x32_bf16 v[128:131], v[112:115], v[88:91], v[128:131]\n\t" \
  "s_mov_b64 exec, vcc\n\t" \
  "buffer_load_dwordx4 v[76:79], v164, %[ob], s94 offen offset:3072 sc1\n\t" \
  "s_mov_b64 exec, -1\n\t" \
  "v_mfma_f32_16x16x32_bf16 v[144:147], v[112:115], v[92:95], v[144:147]\n\t" \
  "ds_read_b64_tr_b16 v[108:109], v163 offset:49152\n\t" \
  "ds_read_b64_tr_b16 v[110:111], v163 offset:51200\n\t" \
  "v_mfma_f32_16x16x32_bf16 v[132:135], v[116:119], v[88:91], v[132:135]\n\t" \
  "ds_read_b64_tr_b16 v[112:113], v160 offset:53248\n\t" \
  "ds_read_b32 v166, v165\n\t" \
  "v_mfma_f32_16x16x32_bf16 v[148:151], v[116:119], v[92:95], v[148:151]\n\t" \
  "ds_read_b64_tr_b16 v[114:115], v160 offset:55296\n\t" \
  "s_waitcnt lgkmcnt(11)\n\t" \
  "v_mfma_f32_16x16x32_bf16 v[136:139], v[120:123], v[88:91], v[136:139]\n\t" \
  "ds_read_b64_tr_b16 v[116:117], v161 offset:53248\n\t" \
  "v_mfma_f32_16x16x32_bf16 v[152:155], v[120:123], v[92:95], v[152:155]\n\t" \
  "ds_read_b64_tr_b16 v[118:119], v161 offset:55296\n\t" \
  "s_sub_u32 %[iss], %[iss], vcc_lo\n\t" \
  "v_mfma_f32_16x16x32_bf16 v[140:143], v[124:127], v[88:91], v[140:143]\n\t" \
  "ds_read_b64_tr_b16 v[120:121], v162 offset:53248\n\t" \
  "v_mfma_f32_16x16x32_bf16 v[156:159], v[124:127], v[92:95], v[156:159]\n\t" \
  "ds_read_b64_tr_b16 v[122:123], v162 offset:55296\n\t" \
  "s_add_u32 %[th], %[th], 1\n\t" \
  "s_cbranch_vccz QF6_%=\n\t" \
  "s_waitcnt vmcnt(16)\n\t" \
  "QG6_%=:\n\t" \
  "s_waitcnt lgkmcnt(11)\n\t" \
  "v_mfma_f32_16x16x32_bf16 v[128:131], v[96:99], v[0:3], v[128:131]\n\t" \
  "ds_read_b64_tr_b16 v[124:125], v163 offset:53248\n\t" \
  "v_readlane_b32 s94, v167, 11\n\t" \
  "v_mfma_f32_16x16x32_bf16 v[144:147], v[96:99], v[4:7], v[144:147]\n\t" \
  "ds_read_b64_tr_b16 v[126:127], v163 offset:55296\n\t" \
  "s_waitcnt lgkmcnt(7)\n\t" \
  "v_cmp_lt_u32 vcc, %[th], v166\n\t" \
  "v_mfma_f32_16x16x32_bf16 v[132:135], v[100:103], v[0:3], v[132:135]\n\t" \
  "s_mov_b64 exec, vcc\n\t" \
  "buffer_load_dwordx4 v[80:83], v164, %[ob], s94 offen offset:0 sc1\n\t" \
  "s_mov_b64 exec, -1\n\t" \
  "v_mfma_f32_16x16x32_bf16 v[148:151], v[100:103], v[4:7], v[148:151]\n\t" \
  "ds_read_b64_tr_b16 v[96:97], v160 offset:57344\n\t" \
  "ds_read_b64_tr_b16 v[98:99], v160 offset:59392\n\t" \
  "v_mfma_f32_16x16x32_bf16 v[136:139], v[104:107], v[0:3], v[136:139]\n\t" \
  "s_mov_b64 exec, vcc\n\t" \
  "buffer_load_dwordx4 v[84:87], v164, %[ob], s94 offen offset:1024 sc1\n\t" \
  "s_mov_b64 exec, -1\n\t" \
  "v_mfma_f32_16x16x32_bf16 v[152:155], v[104:107], v[4:7], v[152:155]\n\t" \
  "ds_read_b64_tr_b16 v[100:101], v161 offset:57344\n\t" \
  "ds_read_b64_tr_b16 v[102:103], v161 offset:59392\n\t" \
  "v_mfma_f32_16x16x32_bf16 v[140:143], v[108:111], v[0:3], v[140:143]\n\t" \
  "s_mov_b64 exec, vcc\n\t" \
  "buffer_load_dwordx4 v[88:91], v164, %[ob], s94 offen offset:2048 sc1\n\t" \
  "s_mov_b64 exec, -1\n\t" \
  "v_mfma_f32_16x16x32_bf16 v[156:159], v[108:111], v[4:7], v[156:159]\n\t" \
  "ds_read_b64_tr_b16 v[104:105], v162 offset:57344\n\t" \
  "ds_read_b64_tr_b16 v[106:107], v162 offset:59392\n\t" \
  "s_waitcnt lgkmcnt(10)\n\t" \
  "v_mfma_f32_16x16x32_bf16 v[128:131], v[112:115], v[8:11], v[128:131]\n\t" \
  "s_mov_b64 exec, vcc\n\t" \
  "buffer_load_dwordx4 v[92:95], v164, %[ob], s94 offen offset:3072 sc1\n\t" \
  "s_mov_b64 exec, -1\n\t" \
  "v_mfma_f32_16x16x32_bf16 v[144:147], v[112:115], v[12:15], v[144:147]\n\t" \
  "ds_read_b64_tr_b16 v[108:109], v163 offset:57344\n\t" \
  "ds_read_b64_tr_b16 v[110:111], v163 offset:59392\n\t" \
  "v_mfma_f32_16x16x32_bf16 v[132:135], v[116:119], v[8:11], v[132:135]\n\t" \
  "ds_read_b64_tr_b16 v[112:113], v160 offset:61440\n\t" \
  "ds_read_b32 v166, v165\n\t" \
  "v_mfma_f32_16x16x32_bf16 v[148:151], v[116:119], v[12:15], v[148:151]\n\t" \
  "ds_read_b64_tr_b16 v[114:115], v160 offset:63488\n\t" \
  "s_waitcnt lgkmcnt(11)\n\t" \
  "v_mfma_f32_16x16x32_bf16 v[136:139], v[120:123], v[8:11], v[136:139]\n\t" \
  "ds_read_b64_tr_b16 v[116:117], v161 offset:61440\n\t" \
  "v_mfma_f32_16x16x32_bf16 v[152:155], v[120:123], v[12:15], v[152:155]\n\t" \
  "ds_read_b64_tr_b16 v[118:119], v161 offset:63488\n\t" \
  "s_sub_u32 %[iss], %[iss], vcc_lo\n\t" \
  "v_mfma_f32_16x16x32_bf16 v[140:143], v[124:127], v[8:11], v[140:143]\n\t" \
  "ds_read_b64_tr_b16 v[120:121], v162 offset:61440\n\t" \
  "s_mov_b64 exec, 1\n\t" \
  "ds_write_b32 v165, v165 offset:76\n\t" \
  "s_mov_b64 exec, -1\n\t" \
  "v_mfma_f32_16x16x32_bf16 v[156:159], v[124:127], v[12:15], v[156:159]\n\t" \
  "ds_read_b64_tr_b16 v[122:123], v162 offset:63488\n\t" \
  "s_add_u32 %[th], %[th], 1\n\t" \
  "s_cbranch_vccz QF7_%=\n\t" \
  "s_waitcnt vmcnt(16)\n\t" \
  "QG7_%=:\n\t" \
  "s_waitcnt lgkmcnt(12)\n\t" \
  "v_mfma_f32_16x16x32_bf16 v[128:131], v[96:99], v[16:19], v[128:131]\n\t" \
  "ds_read_b64_tr_b16 v[124:125], v163 offset:61440\n\t" \
  "v_readlane_b32 s94, v167, 12\n\t" \
  "v_mfma_f32_16x16x32_bf16 v[144:147], v[96:99], v[20:23], v[144:147]\n\t" \
  "ds_read_b64_tr_b16 v[126:127], v163 offset:63488\n\t" \
  "s_waitcnt lgkmcnt(8)\n\t" \
  "v_cmp_lt_u32 vcc, %[th], v166\n\t" \
  "v_mfma_f32_16x16x32_bf16 v[132:135], v[100:103], v[16:19], v[132:135]\n\t" \
  "v_add_u32 v160, 0x10000, v160\n\t" \
  "s_mov_b64 exec, vcc\n\t" \
  "buffer_load_dwordx4 v[0:3], v164, %[ob], s94 offen offset:0 sc1\n\t" \
  "s_mov_b64 exec, -1\n\t" \
  "v_mfma_f32_16x16x32_bf16 v[148:151], v[100:103], v[20:23], v[148:151]\n\t" \
  "ds_read_b64_tr_b16 v[96:97], v160 offset:0\n\t" \
  "ds_read_b64_tr_b16 v[98:99], v160 offset:2048\n\t" \
  "v_mfma_f32_16x16x32_bf16 v[136:139], v[104:107], v[16:19], v[136:139]\n\t" \
  "v_add_u32 v161, 0x10000, v161\n\t" \
  "s_mov_b64 exec, vcc\n\t" \
  "buffer_load_dwordx4 v[4:7], v164, %[ob], s94 offen offset:1024 sc1\n\t" \
  "s_mov_b64 exec, -1\n\t" \
  "v_mfma_f32_16x16x32_bf16 v[152:155], v[104:107], v[20:23], v[152:155]\n\t" \
  "ds_read_b64_tr_b16 v[100:101], v161 offset:0\n\t" \
  "ds_read_b64_tr_b16 v[102:103], v161 offset:2048\n\t" \
  "v_mfma_f32_16x16x32_bf16 v[140:143], v[108:111], v[16:19], v[140:143]\n\t" \
  "v_add_u32 v162, 0x10000, v162\n\t" \
  "s_mov_b64 exec, vcc\n\t" \
  "buffer_load_dwordx4 v[8:11], v164, %[ob], s94 offen offset:2048 sc1\n\t" \
  "s_mov_b64 exec, -1\n\t" \
  "v_mfma_f32_16x16x32_bf16 v[156:159], v[108:111], v[20:23], v[156:159]\n\t" \
  "ds_read_b64_tr_b16 v[104:105], v162 offset:0\n\t" \
  "ds_read_b64_tr_b16 v[106:107], v162 offset:2048\n\t" \
  "s_waitcnt lgkmcnt(11)\n\t" \
  "v_mfma_f32_16x16x32_bf16 v[128:131], v[112:115], v[24:27], v[128:131]\n\t" \
  "v_add_u32 v163, 0x10000, v163\n\t" \
  "s_mov_b64 exec, vcc\n\t" \
  "buffer_load_dwordx4 v[12:15], v164, %[ob], s94 offen offset:3072 sc1\n\t" \
  "s_mov_b64 exec, -1\n\t" \
  "v_mfma_f32_16x16x32_bf16 v[144:147], v[112:115], v[28:31], v[144:147]\n\t" \
  "ds_read_b64_tr_b16 v[108:109], v163 offset:0\n\t" \
  "ds_read_b64_tr_b16 v[110:111], v163 offset:2048\n\t" \
  "v_mfma_f32_16x16x32_bf16 v[132:135], v[116:119], v[24:27], v[132:135]\n\t" \
  "ds_read_b64_tr_b16 v[112:113], v160 offset:4096\n\t" \
  "ds_read_b32 v166, v165\n\t" \
  "v_mfma_f32_16x16x32_bf16 v[148:151], v[116:119], v[28:31], v[148:151]\n\t" \
  "ds_read_b64_tr_b16 v[114:115], v160 offset:6144\n\t" \
  "s_waitcnt lgkmcnt(11)\n\t" \
  "v_mfma_f32_16x16x32_bf16 v[136:139], v[120:123], v[24:27], v[136:139]\n\t" \
  "ds_read_b64_tr_b16 v[116:117], v161 offset:4096\n\t" \
  "v_mfma_f32_16x16x32_bf16 v[152:155], v[120:123], v[28:31], v[152:155]\n\t" \
  "ds_read_b64_tr_b16 v[118:119], v161 offset:6144\n\t" \
  "s_sub_u32 %[iss], %[iss], vcc_lo\n\t" \
  "v_mfma_f32_16x16x32_bf16 v[140:143], v[124:127], v[24:27], v[140:143]\n\t" \
  "ds_read_b64_tr_b16 v[120:121], v162 offset:4096\n\t" \
  "v_mfma_f32_16x16x32_bf16 v[156:159], v[124:127], v[28:31], v[156:159]\n\t" \
  "ds_read_b64_tr_b16 v[122:123], v162 offset:6144\n\t" \
  "s_add_u32 %[th], %[th], 1\n\t" \
  "s_cbranch_vccz QF8_%=\n\t" \
  "s_waitcnt vmcnt(16)\n\t" \
  "QG8_%=:\n\t" \
  "s_waitcnt lgkmcnt(11)\n\t" \
  "v_mfma_f32_16x16x32_bf16 v[128:131], v[96:99], v[32:35], v[128:131]\n\t" \
  "ds_read_b64_tr_b16 v[124:125], v163 offset:4096\n\t" \
  "v_readlane_b32 s94, v167, 13\n\t" \
  "v_mfma_f32_16x16x32_bf16 v[144:147], v[96:99], v[36:39], v[144:147]\n\t" \
  "ds_read_b64_tr_b16 v[126:127], v163 offset:6144\n\t" \
  "s_waitcnt lgkmcnt(7)\n\t" \
  "v_cmp_lt_u32 vcc, %[th], v166\n\t" \
  "v_mfma_f32_16x16x32_bf16 v[132:135], v[100:103], v[32:35], v[132:135]\n\t" \
  "s_mov_b64 exec, vcc\n\t" \
  "buffer_load_dwordx4 v[16:19], v164, %[ob], s94 offen offset:0 sc1\n\t" \
  "s_mov_b64 exec, -1\n\t" \
  "v_mfma_f32_16x16x32_bf16 v[148:151], v[100:103], v[36:39], v[148:151]\n\t" \
  "ds_read_b64_tr_b16 v[96:97], v160 offset:8192\n\t" \
  "ds_read_b64_tr_b16 v[98:99], v160 offset:10240\n\t" \
  "v_mfma_f32_16x16x32_bf16 v[136:139], v[104:107], v[32:35], v[136:139]\n\t" \
  "s_mov_b64 exec, vcc\n\t" \
  "buffer_load_dwordx4 v[20:23], v164, %[ob], s94 offen offset:1024 sc1\n\t" \
  "s_mov_b64 exec, -1\n\t" \
  "v_mfma_f32_16x16x32_bf16 v[152:155], v[104:107], v[36:39], v[152:155]\n\t" \
  "ds_read_b64_tr_b16 v[100:101], v161 offset:8192\n\t" \
  "ds_read_b64_tr_b16 v[102:103], v161 offset:10240\n\t" \
  "v_mfma_f32_16x16x32_bf16 v[140:143], v[108:111], v[32:35], v[140:143]\n\t" \
  "s_mov_b64 exec, vcc\n\t" \
  "buffer_load_dwordx4 v[24:27], v164, %[ob], s94 offen offset:2048 sc1\n\t" \
  "s_mov_b64 exec, -1\n\t" \
  "v_mfma_f32_16x16x32_bf16 v[156:159], v[108:111], v[36:39], v[156:159]\n\t" \
  "ds_read_b64_tr_b16 v[104:105], v162 offset:8192\n\t" \
  "ds_read_b64_tr_b16 v[106:107], v162 offset:10240\n\t" \
  "s_waitcnt lgkmcnt(10)\n\t" \
  "v_mfma_f32_16x16x32_bf16 v[128:131], v[112:115], v[40:43], v[128:131]\n\t" \
  "s_mov_b64 exec, vcc\n\t" \
  "buffer_load_dwordx4 v[28:31], v164, %[ob], s94 offen offset:3072 sc1\n\t" \
  "s_mov_b64 exec, -1\n\t" \
  "v_mfma_f32_16x16x32_bf16 v[144:147], v[112:115], v[44:47], v[144:147]\n\t" \
  "ds_read_b64_tr_b16 v[108:109], v163 offset:8192\n\t" \
  "ds_read_b64_tr_b16 v[110:111], v163 offset:10240\n\t" \
  "v_mfma_f32_16x16x32_bf16 v[132:135], v[116:119], v[40:43], v[132:135]\n\t" \
  "ds_read_b64_tr_b16 v[112:113], v160 offset:12288\n\t" \
  "ds_read_b32 v166, v165\n\t" \
  "v_mfma_f32_16x16x32_bf16 v[148:151], v[116:119], v[44:47], v[148:151]\n\t" \
  "ds_read_b64_tr_b16 v[114:115], v160 offset:14336\n\t" \
  "s_waitcnt lgkmcnt(11)\n\t" \
  "v_mfma_f32_16x16x32_bf16 v[136:139], v[120:123], v[40:43], v[136:139]\n\t" \
  "ds_read_b64_tr_b16 v[116:117], v161 offset:12288\n\t" \
  "v_mfma_f32_16x16x32_bf16 v[152:155], v[120:123], v[44:47], v[152:155]\n\t" \
  "ds_read_b64_tr_b16 v[118:119], v161 offset:14336\n\t" \
  "s_sub_u32 %[iss], %[iss], vcc_lo\n\t" \
  "v_mfma_f32_16x16x32_bf16 v[140:143], v[124:127], v[40:43], v[140:143]\n\t" \
  "ds_read_b64_tr_b16 v[120:121], v162 offset:12288\n\t" \
  "v_mfma_f32_16x16x32_bf16 v[156:159], v[124:127], v[44:47], v[156:159]\n\t" \
  "ds_read_b64_tr_b16 v[122:123], v162 offset:14336\n\t" \
  "s_add_u32 %[th], %[th], 1\n\t" \
  "s_cbranch_vccz QF9_%=\n\t" \
  "s_waitcnt vmcnt(16)\n\t" \
  "QG9_%=:\n\t" \
  "s_waitcnt lgkmcnt(11)\n\t" \
  "v_mfma_f32_16x16x32_bf16 v[128:131], v[96:99], v[48:51], v[128:131]\n\t" \
  "ds_read_b64_tr_b16 v[124:125], v163 offset:12288\n\t" \
  "v_readlane_b32 s94, v167, 14\n\t" \
  "v_mfma_f32_16x16x32_bf16 v[144:147], v[96:99], v[52:55], v[144:147]\n\t" \
  "ds_read_b64_tr_b16 v[126:127], v163 offset:14336\n\t" \
  "s_waitcnt lgkmcnt(7)\n\t" \
  "v_cmp_lt_u32 vcc, %[th], v166\n\t" \
  "v_mfma_f32_16x16x32_bf16 v[132:135], v[100:103], v[48:51], v[132:135]\n\t" \
  "s_mov_b64 exec, vcc\n\t" \
  "buffer_load_dwordx4 v[32:35], v164, %[ob], s94 offen offset:0 sc1\n\t" \
  "s_mov_b64 exec, -1\n\t" \
  "v_mfma_f32_16x16x32_bf16 v[148:151], v[100:103], v[52:55], v[148:151]\n\t" \
  "ds_read_b64_tr_b16 v[96:97], v160 offset:16384\n\t" \
  "ds_read_b64_tr_b16 v[98:99], v160 offset:18432\n\t" \
  "v_mfma_f32_16x16x32_bf16 v[136:139], v[104:107], v[48:51], v[136:139]\n\t" \
  "s_mov_b64 exec, vcc\n\t" \
  "buffer_load_dwordx4 v[36:39], v164, %[ob], s94 offen offset:1024 sc1\n\t" \
  "s_mov_b64 exec, -1\n\t" \
  "v_mfma_f32_16x16x32_bf16 v[152:155], v[104:107], v[52:55], v[152:155]\n\t" \
  "ds_read_b64_tr_b16 v[100:101], v161 offset:16384\n\t" \
  "ds_read_b64_tr_b16 v[102:103], v161 offset:18432\n\t" \
  "v_mfma_f32_16x16x32_bf16 v[140:143], v[108:111], v[48:51], v[140:143]\n\t" \
  "s_mov_b64 exec, vcc\n\t" \
  "buffer_load_dwordx4 v[40:43], v164, %[ob], s94 offen offset:2048 sc1\n\t" \
  "s_mov_b64 exec, -1\n\t" \
  "v_mfma_f32_16x16x32_bf16 v[156:159], v[108:111], v[52:55], v[156:159]\n\t" \
  "ds_read_b64_tr_b16 v[104:105], v162 offset:16384\n\t" \
  "ds_read_b64_tr_b16 v[106:107], v162 offset:18432\n\t" \
  "s_waitcnt lgkmcnt(10)\n\t" \
  "v_mfma_f32_16x16x32_bf16 v[128:131], v[112:115], v[56:59], v[128:131]\n\t" \
  "s_mov_b64 exec, vcc\n\t" \
  "buffer_load_dwordx4 v[44:47], v164, %[ob], s94 offen offset:3072 sc1\n\t" \
  "s_mov_b64 exec, -1\n\t" \
  "v_mfma_f32_16x16x32_bf16 v[144:147], v[112:115], v[60:63], v[144:147]\n\t" \
  "ds_read_b64_tr_b16 v[108:109], v163 offset:16384\n\t" \
  "ds_read_b64_tr_b16 v[110:111], v163 offset:18432\n\t" \
  "v_mfma_f32_16x16x32_bf16 v[132:135], v[116:119], v[56:59], v[132:135]\n\t" \
  "ds_read_b64_tr_b16 v[112:113], v160 offset:20480\n\t" \
  "ds_read_b32 v166, v165\n\t" \
  "v_mfma_f32_16x16x32_bf16 v[148:151], v[116:119], v[60:63], v[148:151]\n\t" \
  "ds_read_b64_tr_b16 v[114:115], v160 offset:22528\n\t" \
  "s_waitcnt lgkmcnt(11)\n\t" \
  "v_mfma_f32_16x16x32_bf16 v[136:139], v[120:123], v[56:59], v[136:139]\n\t" \
  "ds_read_b64_tr_b16 v[116:117], v161 offset:20480\n\t" \
  "v_mfma_f32_16x16x32_bf16 v[152:155], v[120:123], v[60:63], v[152:155]\n\t" \
  "ds_read_b64_tr_b16 v[118:119], v161 offset:22528\n\t" \
  "s_sub_u32 %[iss], %[iss], vcc_lo\n\t" \
  "v_mfma_f32_16x16x32_bf16 v[140:143], v[124:127], v[56:59], v[140:143]\n\t" \
  "ds_read_b64_tr_b16 v[120:121], v162 offset:20480\n\t" \
  "v_mfma_f32_16x16x32_bf16 v[156:159], v[124:127], v[60:63], v[156:159]\n\t" \
  "ds_read_b64_tr_b16 v[122:123], v162 offset:22528\n\t" \
  "s_add_u32 %[th], %[th], 1\n\t" \
  "s_cbranch_vccz QF10_%=\n\t" \
  "s_waitcnt vmcnt(16)\n\t" \
  "QG10_%=:\n\t" \
  "s_waitcnt lgkmcnt(11)\n\t" \
  "v_mfma_f32_16x16x32_bf16 v[128:131], v[96:99], v[64:67], v[128:131]\n\t" \
  "ds_read_b64_tr_b16 v[124:125], v163 offset:20480\n\t" \
  "v_readlane_b32 s94, v167, 15\n\t" \
  "v_mfma_f32_16x16x32_bf16 v[144:147], v[96:99], v[68:71], v[144:147]\n\t" \
  "ds_read_b64_tr_b16 v[126:127], v163 offset:22528\n\t" \
  "s_waitcnt lgkmcnt(7)\n\t" \
  "v_cmp_lt_u32 vcc, %[th], v166\n\t" \
  "v_mfma_f32_16x16x32_bf16 v[132:135], v[100:103], v[64:67], v[132:135]\n\t" \
  "s_mov_b64 exec, vcc\n\t" \
  "buffer_load_dwordx4 v[48:51], v164, %[ob], s94 offen offset:0 sc1\n\t" \
  "s_mov_b64 exec, -1\n\t" \
  "v_mfma_f32_16x16x32_bf16 v[148:151], v[100:103], v[68:71], v[148:151]\n\t" \
  "ds_read_b64_tr_b16 v[96:97], v160 offset:24576\n\t" \
  "ds_read_b64_tr_b16 v[98:99], v160 offset:26624\n\t" \
  "v_mfma_f32_16x16x32_bf16 v[136:139], v[104:107], v[64:67], v[136:139]\n\t" \
  "s_mov_b64 exec, vcc\n\t" \
  "buffer_load_dwordx4 v[52:55], v164, %[ob], s94 offen offset:1024 sc1\n\t" \
  "s_mov_b64 exec, -1\n\t" \
  "v_mfma_f32_16x16x32_bf16 v[152:155], v[104:107], v[68:71], v[152:155]\n\t" \
  "ds_read_b64_tr_b16 v[100:101], v161 offset:24576\n\t" \
  "ds_read_b64_tr_b16 v[102:103], v161 offset:26624\n\t" \
  "v_mfma_f32_16x16x32_bf16 v[140:143], v[108:111], v[64:67], v[140:143]\n\t" \
  "s_mov_b64 exec, vcc\n\t" \
  "buffer_load_dwordx4 v[56:59], v164, %[ob], s94 offen offset:2048 sc1\n\t" \
  "s_mov_b64 exec, -1\n\t" \
  "v_mfma_f32_16x16x32_bf16 v[156:159], v[108:111], v[68:71], v[156:159]\n\t" \
  "ds_read_b64_tr_b16 v[104:105], v162 offset:24576\n\t" \
  "ds_read_b64_tr_b16 v[106:107], v162 offset:26624\n\t" \
  "s_waitcnt lgkmcnt(10)\n\t" \
  "v_mfma_f32_16x16x32_bf16 v[128:131], v[112:115], v[72:75], v[128:131]\n\t" \
  "s_mov_b64 exec, vcc\n\t" \
  "buffer_load_dwordx4 v[60:63], v164, %[ob], s94 offen offset:3072 sc1\n\t" \
  "s_mov_b64 exec, -1\n\t" \
  "v_mfma_f32_16x16x32_bf16 v[144:147], v[112:115], v[76:79], v[144:147]\n\t" \
  "ds_read_b64_tr_b16 v[108:109], v163 offset:24576\n\t" \
  "ds_read_b64_tr_b16 v[110:111], v163 offset:26624\n\t" \
  "v_mfma_f32_16x16x32_bf16 v[132:135], v[116:119], v[72:75], v[132:135]\n\t" \
  "ds_read_b64_tr_b16 v[112:113], v160 offset:28672\n\t" \
  "v_mfma_f32_16x16x32_bf16 v[148:151], v[116:119], v[76:79], v[148:151]\n\t" \
  "ds_read_b64_tr_b16 v[114:115], v160 offset:30720\n\t" \
  "s_waitcnt lgkmcnt(10)\n\t" \
  "v_mfma_f32_16x16x32_bf16 v[136:139], v[120:123], v[72:75], v[136:139]\n\t" \
  "ds_read_b64_tr_b16 v[116:117], v161 offset:28672\n\t" \
  "v_mfma_f32_16x16x32_bf16 v[152:155], v[120:123], v[76:79], v[152:155]\n\t" \
  "ds_read_b64_tr_b16 v[118:119], v161 offset:30720\n\t" \
  "s_sub_u32 %[iss], %[iss], vcc_lo\n\t" \
  "v_mfma_f32_16x16x32_bf16 v[140:143], v[124:127], v[72:75], v[140:143]\n\t" \
  "ds_read_b64_tr_b16 v[120:121], v162 offset:28672\n\t" \
  "v_mfma_f32_16x16x32_bf16 v[156:159], v[124:127], v[76:79], v[156:159]\n\t" \
  "ds_read_b64_tr_b16 v[122:123], v162 offset:30720\n\t" \
  "s_add_u32 %[th], %[th], 1\n\t" \
  "s_cbranch_vccz QF11_%=\n\t" \
  "s_waitcnt vmcnt(16)\n\t" \
  "QG11_%=:\n\t" \
  "s_waitcnt lgkmcnt(10)\n\t" \
  "v_mfma_f32_16x16x32_bf16 v[128:131], v[96:99], v[80:83], v[128:131]\n\t" \
  "ds_read_b64_tr_b16 v[124:125], v163 offset:28672\n\t" \
  "v_mfma_f32_16x16x32_bf16 v[144:147], v[96:99], v[84:87], v[144:147]\n\t" \
  "ds_read_b64_tr_b16 v[126:127], v163 offset:30720\n\t" \
  "v_mfma_f32_16x16x32_bf16 v[132:135], v[100:103], v[80:83], v[132:135]\n\t" \
  "ds_read_b64_tr_b16 v[96:97], v160 offset:32768\n\t" \
  "v_mfma_f32_16x16x32_bf16 v[148:151], v[100:103], v[84:87], v[148:151]\n\t" \
  "ds_read_b64_tr_b16 v[98:99], v160 offset:34816\n\t" \
  "s_waitcnt lgkmcnt(10)\n\t" \
  "v_mfma_f32_16x16x32_bf16 v[136:139], v[104:107], v[80:83], v[136:139]\n\t" \
  "ds_read_b64_tr_b16 v[100:101], v161 offset:32768\n\t" \
  "v_mfma_f32_16x16x32_bf16 v[152:155], v[104:107], v[84:87], v[152:155]\n\t" \
  "ds_read_b64_tr_b16 v[102:103], v161 offset:34816\n\t" \
  "v_mfma_f32_16x16x32_bf16 v[140:143], v[108:111], v[80:83], v[140:143]\n\t" \
  "ds_read_b64_tr_b16 v[104:105], v162 offset:32768\n\t" \
  "v_mfma_f32_16x16x32_bf16 v[156:159], v[108:111], v[84:87], v[156:159]\n\t" \
  "ds_read_b64_tr_b16 v[106:107], v162 offset:34816\n\t" \
  "s_waitcnt lgkmcnt(10)\n\t" \
  "v_mfma_f32_16x16x32_bf16 v[128:131], v[112:115], v[88:91], v[128:131]\n\t" \
  "ds_read_b64_tr_b16 v[108:109], v163 offset:32768\n\t" \
  "v_mfma_f32_16x16x32_bf16 v[144:147], v[112:115], v[92:95], v[144:147]\n\t" \
  "ds_read_b64_tr_b16 v[110:111], v163 offset:34816\n\t" \
  "v_mfma_f32_16x16x32_bf16 v[132:135], v[116:119], v[88:91], v[132:135]\n\t" \
  "ds_read_b64_tr_b16 v[112:113], v160 offset:36864\n\t" \
  "v_mfma_f32_16x16x32_bf16 v[148:151], v[116:119], v[92:95], v[148:151]\n\t" \
  "ds_read_b64_tr_b16 v[114:115], v160 offset:38912\n\t" \
  "s_waitcnt lgkmcnt(10)\n\t" \
  "v_mfma_f32_16x16x32_bf16 v[136:139], v[120:123], v[88:91], v[136:139]\n\t" \
  "ds_read_b64_tr_b16 v[116:117], v161 offset:36864\n\t" \
  "v_mfma_f32_16x16x32_bf16 v[152:155], v[120:123], v[92:95], v[152:155]\n\t" \
  "ds_read_b64_tr_b16 v[118:119], v161 offset:38912\n\t" \
  "v_mfma_f32_16x16x32_bf16 v[140:143], v[124:127], v[88:91], v[140:143]\n\t" \
  "ds_read_b64_tr_b16 v[120:121], v162 offset:36864\n\t" \
  "v_mfma_f32_16x16x32_bf16 v[156:159], v[124:127], v[92:95], v[156:159]\n\t" \
  "ds_read_b64_tr_b16 v[122:123], v162 offset:38912\n\t" \
  "s_cbranch_vccz QF12_%=\n\t" \
  "s_waitcnt vmcnt(12)\n\t" \
  "QG12_%=:\n\t" \
  "s_waitcnt lgkmcnt(10)\n\t" \
  "v_mfma_f32_16x16x32_bf16 v[128:131], v[96:99], v[0:3], v[128:131]\n\t" \
  "ds_read_b64_tr_b16 v[124:125], v163 offset:36864\n\t" \
  "v_mfma_f32_16x16x32_bf16 v[144:147], v[96:99], v[4:7], v[144:147]\n\t" \
  "ds_read_b64_tr_b16 v[126:127], v163 offset:38912\n\t" \
  "v_mfma_f32_16x16x32_bf16 v[132:135], v[100:103], v[0:3], v[132:135]\n\t" \
  "ds_read_b64_tr_b16 v[96:97], v160 offset:40960\n\t" \
  "v_mfma_f32_16x16x32_bf16 v[148:151], v[100:103], v[4:7], v[148:151]\n\t" \
  "ds_read_b64_tr_b16 v[98:99], v160 offset:43008\n\t" \
  "s_waitcnt lgkmcnt(10)\n\t" \
  "v_mfma_f32_16x16x32_bf16 v[136:139], v[104:107], v[0:3], v[136:139]\n\t" \
  "ds_read_b64_tr_b16 v[100:101], v161 offset:40960\n\t" \
  "v_mfma_f32_16x16x32_bf16 v[152:155], v[104:107], v[4:7], v[152:155]\n\t" \
  "ds_read_b64_tr_b16 v[102:103], v161 offset:43008\n\t" \
  "v_mfma_f32_16x16x32_bf16 v[140:143], v[108:111], v[0:3], v[140:143]\n\t" \
  "ds_read_b64_tr_b16 v[104:105], v162 offset:40960\n\t" \
  "v_mfma_f32_16x16x32_bf16 v[156:159], v[108:111], v[4:7], v[156:159]\n\t" \
  "ds_read_b64_tr_b16 v[106:107], v162 offset:43008\n\t" \
  "s_waitcnt lgkmcnt(10)\n\t" \
  "v_mfma_f32_16x16x32_bf16 v[128:131], v[112:115], v[8:11], v[128:131]\n\t" \
  "ds_read_b64_tr_b16 v[108:109], v163 offset:40960\n\t" \
  "v_mfma_f32_16x16x32_bf16 v[144:147], v[112:115], v[12:15], v[144:147]\n\t" \
  "ds_read_b64_tr_b16 v[110:111], v163 offset:43008\n\t" \
  "v_mfma_f32_16x16x32_bf16 v[132:135], v[116:119], v[8:11], v[132:135]\n\t" \
  "ds_read_b64_tr_b16 v[112:113], v160 offset:45056\n\t" \
  "v_mfma_f32_16x16x32_bf16 v[148:151], v[116:119], v[12:15], v[148:151]\n\t" \
  "ds_read_b64_tr_b16 v[114:115], v160 offset:47104\n\t" \
  "s_waitcnt lgkmcnt(10)\n\t" \
  "v_mfma_f32_16x16x32_bf16 v[136:139], v[120:123], v[8:11], v[136:139]\n\t" \
  "ds_read_b64_tr_b16 v[116:117], v161 offset:45056\n\t" \
  "v_mfma_f32_16x16x32_bf16 v[152:155], v[120:123], v[12:15], v[152:155]\n\t" \
  "ds_read_b64_tr_b16 v[118:119], v161 offset:47104\n\t" \
  "v_mfma_f32_16x16x32_bf16 v[140:143], v[124:127], v[8:11], v[140:143]\n\t" \
  "ds_read_b64_tr_b16 v[120:121], v162 offset:45056\n\t" \
  "v_mfma_f32_16x16x32_bf16 v[156:159], v[124:127], v[12:15], v[156:159]\n\t" \
  "ds_read_b64_tr_b16 v[122:123], v162 offset:47104\n\t" \
  "s_cbranch_vccz QF13_%=\n\t" \
  "s_waitcnt vmcnt(8)\n\t" \
  "QG13_%=:\n\t" \
  "s_waitcnt lgkmcnt(10)\n\t" \
  "v_mfma_f32_16x16x32_bf16 v[128:131], v[96:99], v[16:19], v[128:131]\n\t" \
  "ds_read_b64_tr_b16 v[124:125], v163 offset:45056\n\t" \
  "v_mfma_f32_16x16x32_bf16 v[144:147], v[96:99], v[20:23], v[144:147]\n\t" \
  "ds_read_b64_tr_b16 v[126:127], v163 offset:47104\n\t" \
  "v_mfma_f32_16x16x32_bf16 v[132:135], v[100:103], v[16:19], v[132:135]\n\t" \
  "ds_read_b64_tr_b16 v[96:97], v160 offset:49152\n\t" \
  "v_mfma_f32_16x16x32_bf16 v[148:151], v[100:103], v[20:23], v[148:151]\n\t" \
  "ds_read_b64_tr_b16 v[98:99], v160 offset:51200\n\t" \
  "s_waitcnt lgkmcnt(10)\n\t" \
  "v_mfma_f32_16x16x32_bf16 v[136:139], v[104:107], v[16:19], v[136:139]\n\t" \
  "ds_read_b64_tr_b16 v[100:101], v161 offset:49152\n\t" \
  "v_mfma_f32_16x16x32_bf16 v[152:155], v[104:107], v[20:23], v[152:155]\n\t" \
  "ds_read_b64_tr_b16 v[102:103], v161 offset:51200\n\t" \
  "v_mfma_f32_16x16x32_bf16 v[140:143], v[108:111], v[16:19], v[140:143]\n\t" \
  "ds_read_b64_tr_b16 v[104:105], v162 offset:49152\n\t" \
  "v_mfma_f32_16x16x32_bf16 v[156:159], v[108:111], v[20:23], v[156:159]\n\t" \
  "ds_read_b64_tr_b16 v[106:107], v162 offset:51200\n\t" \
  "s_waitcnt lgkmcnt(10)\n\t" \
  "v_mfma_f32_16x16x32_bf16 v[128:131], v[112:115], v[24:27], v[128:131]\n\t" \
  "ds_read_b64_tr_b16 v[108:109], v163 offset:49152\n\t" \
  "v_mfma_f32_16x16x32_bf16 v[144:147], v[112:115], v[28:31], v[144:147]\n\t" \
  "ds_read_b64_tr_b16 v[110:111], v163 offset:51200\n\t" \
  "v_mfma_f32_16x16x32_bf16 v[132:135], v[116:119], v[24:27], v[132:135]\n\t" \
  "ds_read_b64_tr_b16 v[112:113], v160 offset:53248\n\t" \
  "v_mfma_f32_16x16x32_bf16 v[148:151], v[116:119], v[28:31], v[148:151]\n\t" \
  "ds_read_b64_tr_b16 v[114:115], v160 offset:55296\n\t" \
  "s_waitcnt lgkmcnt(10)\n\t" \
  "v_mfma_f32_16x16x32_bf16 v[136:139], v[120:123], v[24:27], v[136:139]\n\t" \
  "ds_read_b64_tr_b16 v[116:117], v161 offset:53248\n\t" \
  "v_mfma_f32_16x16x32_bf16 v[152:155], v[120:123], v[28:31], v[152:155]\n\t" \
  "ds_read_b64_tr_b16 v[118:119], v161 offset:55296\n\t" \
  "v_mfma_f32_16x16x32_bf16 v[140:143], v[124:127], v[24:27], v[140:143]\n\t" \
  "ds_read_b64_tr_b16 v[120:121], v162 offset:53248\n\t" \
  "v_mfma_f32_16x16x32_bf16 v[156:159], v[124:127], v[28:31], v[156:159]\n\t" \
  "ds_read_b64_tr_b16 v[122:123], v162 offset:55296\n\t" \
  "s_cbranch_vccz QF14_%=\n\t" \
  "s_waitcnt vmcnt(4)\n\t" \
  "QG14_%=:\n\t" \
  "s_waitcnt lgkmcnt(10)\n\t" \
  "v_mfma_f32_16x16x32_bf16 v[128:131], v[96:99], v[32:35], v[128:131]\n\t" \
  "ds_read_b64_tr_b16 v[124:125], v163 offset:53248\n\t" \
  "v_mfma_f32_16x16x32_bf16 v[144:147], v[96:99], v[36:39], v[144:147]\n\t" \
  "ds_read_b64_tr_b16 v[126:127], v163 offset:55296\n\t" \
  "v_mfma_f32_16x16x32_bf16 v[132:135], v[100:103], v[32:35], v[132:135]\n\t" \
  "ds_read_b64_tr_b16 v[96:97], v160 offset:57344\n\t" \
  "v_mfma_f32_16x16x32_bf16 v[148:151], v[100:103], v[36:39], v[148:151]\n\t" \
  "ds_read_b64_tr_b16 v[98:99], v160 offset:59392\n\t" \
  "s_waitcnt lgkmcnt(10)\n\t" \
  "v_mfma_f32_16x16x32_bf16 v[136:139], v[104:107], v[32:35], v[136:139]\n\t" \
  "ds_read_b64_tr_b16 v[100:101], v161 offset:57344\n\t" \
  "v_mfma_f32_16x16x32_bf16 v[152:155], v[104:107], v[36:39], v[152:155]\n\t" \
  "ds_read_b64_tr_b16 v[102:103], v161 offset:59392\n\t" \
  "v_mfma_f32_16x16x32_bf16 v[140:143], v[108:111], v[32:35], v[140:143]\n\t" \
  "ds_read_b64_tr_b16 v[104:105], v162 offset:57344\n\t" \
  "v_mfma_f32_16x16x32_bf16 v[156:159], v[108:111], v[36:39], v[156:159]\n\t" \
  "ds_read_b64_tr_b16 v[106:107], v162 offset:59392\n\t" \
  "s_waitcnt lgkmcnt(10)\n\t" \
  "v_mfma_f32_16x16x32_bf16 v[128:131], v[112:115], v[40:43], v[128:131]\n\t" \
  "ds_read_b64_tr_b16 v[108:109], v163 offset:57344\n\t" \
  "v_mfma_f32_16x16x32_bf16 v[144:147], v[112:115], v[44:47], v[144:147]\n\t" \
  "ds_read_b64_tr_b16 v[110:111], v163 offset:59392\n\t" \
  "v_mfma_f32_16x16x32_bf16 v[132:135], v[116:119], v[40:43], v[132:135]\n\t" \
  "ds_read_b64_tr_b16 v[112:113], v160 offset:61440\n\t" \
  "v_mfma_f32_16x16x32_bf16 v[148:151], v[116:119], v[44:47], v[148:151]\n\t" \
  "ds_read_b64_tr_b16 v[114:115], v160 offset:63488\n\t" \
  "s_waitcnt lgkmcnt(10)\n\t" \
  "v_mfma_f32_16x16x32_bf16 v[136:139], v[120:123], v[40:43], v[136:139]\n\t" \
  "ds_read_b64_tr_b16 v[116:117], v161 offset:61440\n\t" \
  "v_mfma_f32_16x16x32_bf16 v[152:155], v[120:123], v[44:47], v[152:155]\n\t" \
  "ds_read_b64_tr_b16 v[118:119], v161 offset:63488\n\t" \
  "v_mfma_f32_16x16x32_bf16 v[140:143], v[124:127], v[40:43], v[140:143]\n\t" \
  "ds_read_b64_tr_b16 v[120:121], v162 offset:61440\n\t" \
  "v_mfma_f32_16x16x32_bf16 v[156:159], v[124:127], v[44:47], v[156:159]\n\t" \
  "ds_read_b64_tr_b16 v[122:123], v162 offset:63488\n\t" \
  "s_cbranch_vccz QF15_%=\n\t" \
  "s_waitcnt vmcnt(0)\n\t" \
  "QG15_%=:\n\t" \
  "s_waitcnt lgkmcnt(10)\n\t" \
  "v_mfma_f32_16x16x32_bf16 v[128:131], v[96:99], v[48:51], v[128:131]\n\t" \
  "ds_read_b64_tr_b16 v[124:125], v163 offset:61440\n\t" \
  "v_mfma_f32_16x16x32_bf16 v[144:147], v[96:99], v[52:55], v[144:147]\n\t" \
  "ds_read_b64_tr_b16 v[126:127], v163 offset:63488\n\t" \
  "v_mfma_f32_16x16x32_bf16 v[132:135], v[100:103], v[48:51], v[132:135]\n\t" \
  "v_mfma_f32_16x16x32_bf16 v[148:151], v[100:103], v[52:55], v[148:151]\n\t" \
  "s_waitcnt lgkmcnt(8)\n\t" \
  "v_mfma_f32_16x16x32_bf16 v[136:139], v[104:107], v[48:51], v[136:139]\n\t" \
  "v_mfma_f32_16x16x32_bf16 v[152:155], v[104:107], v[52:55], v[152:155]\n\t" \
  "v_mfma_f32_16x16x32_bf16 v[140:143], v[108:111], v[48:51], v[140:143]\n\t" \
  "v_mfma_f32_16x16x32_bf16 v[156:159], v[108:111], v[52:55], v[156:159]\n\t" \
  "s_waitcnt lgkmcnt(4)\n\t" \
  "v_mfma_f32_16x16x32_bf16 v[128:131], v[112:115], v[56:59], v[128:131]\n\t" \
  "v_mfma_f32_16x16x32_bf16 v[144:147], v[112:115], v[60:63], v[144:147]\n\t" \
  "v_mfma_f32_16x16x32_bf16 v[132:135], v[116:119], v[56:59], v[132:135]\n\t" \
  "v_mfma_f32_16x16x32_bf16 v[148:151], v[116:119], v[60:63], v[148:151]\n\t" \
  "s_waitcnt lgkmcnt(0)\n\t" \
  "v_mfma_f32_16x16x32_bf16 v[136:139], v[120:123], v[56:59], v[136:139]\n\t" \
  "v_mfma_f32_16x16x32_bf16 v[152:155], v[120:123], v[60:63], v[152:155]\n\t" \
  "v_mfma_f32_16x16x32_bf16 v[140:143], v[124:127], v[56:59], v[140:143]\n\t" \
  "v_mfma_f32_16x16x32_bf16 v[156:159], v[124:127], v[60:63], v[156:159]\n\t" \
  "s_branch QX_%=\n\t" \
  "QF1_%=:\n\t" \
  "s_mov_b32 %[c], 1\n\t" \
  "s_getpc_b64 s[92:93]\n\t" \
  "s_branch QC_%=\n\t" \
  "s_branch QG1_%=\n\t" \
  "QF2_%=:\n\t" \
  "s_mov_b32 %[c], 2\n\t" \
  "s_getpc_b64 s[92:93]\n\t" \
  "s_branch QC_%=\n\t" \
  "s_branch QG2_%=\n\t" \
  "QF3_%=:\n\t" \
  "s_mov_b32 %[c], 3\n\t" \
  "s_getpc_b64 s[92:93]\n\t" \
  "s_branch QC_%=\n\t" \
  "s_branch QG3_%=\n\t" \
  "QF4_%=:\n\t" \
  "s_mov_b32 %[c], 4\n\t" \
  "s_getpc_b64 s[92:93]\n\t" \
  "s_branch QC_%=\n\t" \
  "s_branch QG4_%=\n\t" \
  "QF5_%=:\n\t" \
  "s_mov_b32 %[c], 5\n\t" \
  "s_getpc_b64 s[92:93]\n\t" \
  "s_branch QC_%=\n\t" \
  "s_branch QG5_%=\n\t" \
  "QF6_%=:\n\t" \
  "s_mov_b32 %[c], 6\n\t" \
  "s_getpc_b64 s[92:93]\n\t" \
  "s_branch QC_%=\n\t" \
  "s_branch QG6_%=\n\t" \
  "QF7_%=:\n\t" \
  "s_mov_b32 %[c], 7\n\t" \
  "s_getpc_b64 s[92:93]\n\t" \
  "s_branch QC_%=\n\t" \
  "s_branch QG7_%=\n\t" \
  "QF8_%=:\n\t" \
  "s_mov_b32 %[c], 8\n\t" \
  "s_getpc_b64 s[92:93]\n\t" \
  "s_branch QC_%=\n\t" \
  "s_branch QG8_%=\n\t" \
  "QF9_%=:\n\t" \
  "s_mov_b32 %[c], 9\n\t" \
  "s_getpc_b64 s[92:93]\n\t" \
  "s_branch QC_%=\n\t" \
  "s_branch QG9_%=\n\t" \
  "QF10_%=:\n\t" \
  "s_mov_b32 %[c], 10\n\t" \
  "s_getpc_b64 s[92:93]\n\t" \
  "s_branch QC_%=\n\t" \
  "s_branch QG10_%=\n\t" \
  "QF11_%=:\n\t" \
  "s_mov_b32 %[c], 11\n\t" \
  "s_getpc_b64 s[92:93]\n\t" \
  "s_branch QC_%=\n\t" \
  "s_branch QG11_%=\n\t" \
  "QF12_%=:\n\t" \
  "s_mov_b32 %[c], 12\n\t" \
  "s_getpc_b64 s[92:93]\n\t" \
  "s_branch QC_%=\n\t" \
  "s_branch QG12_%=\n\t" \
  "QF13_%=:\n\t" \
  "s_mov_b32 %[c], 13\n\t" \
  "s_getpc_b64 s[92:93]\n\t" \
  "s_branch QC_%=\n\t" \
  "s_branch QG13_%=\n\t" \
  "QF14_%=:\n\t" \
  "s_mov_b32 %[c], 14\n\t" \
  "s_getpc_b64 s[92:93]\n\t" \
  "s_branch QC_%=\n\t" \
  "s_branch QG14_%=\n\t" \
  "QF15_%=:\n\t" \
  "s_mov_b32 %[c], 15\n\t" \
  "s_getpc_b64 s[92:93]\n\t" \
  "s_branch QC_%=\n\t" \
  "s_branch QG15_%=\n\t" \
  "QC_%=:\n\t" \
  "s_add_u32 s92, s92, 4\n\t" \
  "s_addc_u32 s93, s93, 0\n\t" \
  "QU_%=:\n\t" \
  "ds_read_b32 v166, v165\n\t" \
  "s_add_u32 %[t1], %[c], 5\n\t" \
  "s_waitcnt lgkmcnt(0)\n\t" \
  "v_readfirstlane_b32 %[t0], v166\n\t" \
  "s_nop 0\n\t" \
  "s_min_u32 %[t1], %[t1], %[t0]\n\t" \
  "QL_%=:\n\t" \
  "s_cmp_ge_u32 %[iss], %[t1]\n\t" \
  "s_cbranch_scc1 QD_%=\n\t" \
  "s_mov_b32 %[t0], %[iss]\n\t" \
  "s_sub_u32 %[th], %[t0], 6\n\t" \
  "s_cselect_b32 %[t0], %[t0], %[th]\n\t" \
  "s_sub_u32 %[th], %[t0], 6\n\t" \
  "s_cselect_b32 %[t0], %[t0], %[th]\n\t" \
  "s_cmp_eq_u32 %[t0], 1\n\t" \
  "s_cbranch_scc1 QS1_%=\n\t" \
  "s_cmp_eq_u32 %[t0], 2\n\t" \
  "s_cbranch_scc1 QS2_%=\n\t" \
  "s_cmp_eq_u32 %[t0], 3\n\t" \
  "s_cbranch_scc1 QS3_%=\n\t" \
  "s_cmp_eq_u32 %[t0], 4\n\t" \
  "s_cbranch_scc1 QS4_%=\n\t" \
  "s_cmp_eq_u32 %[t0], 5\n\t" \
  "s_cbranch_scc1 QS5_%=\n\t" \
  "QS0_%=:\n\t" \
  "v_readlane_b32 s94, v167, %[iss]\n\t" \
  "s_nop 4\n\t" \
  "buffer_load_dwordx4 v[0:3], v164, %[ob], s94 offen offset:0 sc1\n\t" \
  "buffer_load_dwordx4 v[4:7], v164, %[ob], s94 offen offset:1024 sc1\n\t" \
  "buffer_load_dwordx4 v[8:11], v164, %[ob], s94 offen offset:2048 sc1\n\t" \
  "buffer_load_dwordx4 v[12:15], v164, %[ob], s94 offen offset:3072 sc1\n\t" \
  "s_add_u32 %[iss], %[iss], 1\n\t" \
  "s_cmp_ge_u32 %[iss], %[t1]\n\t" \
  "s_cbranch_scc1 QD_%=\n\t" \
  "QS1_%=:\n\t" \
  "v_readlane_b32 s94, v167, %[iss]\n\t" \
  "s_nop 4\n\t" \
  "buffer_load_dwordx4 v[16:19], v164, %[ob], s94 offen offset:0 sc1\n\t" \
  "buffer_load_dwordx4 v[20:23], v164, %[ob], s94 offen offset:1024 sc1\n\t" \
  "buffer_load_dwordx4 v[24:27], v164, %[ob], s94 offen offset:2048 sc1\n\t" \
  "buffer_load_dwordx4 v[28:31], v164, %[ob], s94 offen offset:3072 sc1\n\t" \
  "s_add_u32 %[iss], %[iss], 1\n\t" \
  "s_cmp_ge_u32 %[iss], %[t1]\n\t" \
  "s_cbranch_scc1 QD_%=\n\t" \
  "QS2_%=:\n\t" \
  "v_readlane_b32 s94, v167, %[iss]\n\t" \
  "s_nop 4\n\t" \
  "buffer_load_dwordx4 v[32:35], v164, %[ob], s94 offen offset:0 sc1\n\t" \
  "buffer_load_dwordx4 v[36:39], v164, %[ob], s94 offen offset:1024 sc1\n\t" \
  "buffer_load_dwordx4 v[40:43], v164, %[ob], s94 offen offset:2048 sc1\n\t" \
  "buffer_load_dwordx4 v[44:47], v164, %[ob], s94 offen offset:3072 sc1\n\t" \
  "s_add_u32 %[iss], %[iss], 1\n\t" \
  "s_cmp_ge_u32 %[iss], %[t1]\n\t" \
  "s_cbranch_scc1 QD_%=\n\t" \
  "QS3_%=:\n\t" \
  "v_readlane_b32 s94, v167, %[iss]\n\t" \
  "s_nop 4\n\t" \
  "buffer_load_dwordx4 v[48:51], v164, %[ob], s94 offen offset:0 sc1\n\t" \
  "buffer_load_dwordx4 v[52:55], v164, %[ob], s94 offen offset:1024 sc1\n\t" \
  "buffer_load_dwordx4 v[56:59], v164, %[ob], s94 offen offset:2048 sc1\n\t" \
  "buffer_load_dwordx4 v[60:63], v164, %[ob], s94 offen offset:3072 sc1\n\t" \
  "s_add_u32 %[iss], %[iss], 1\n\t" \
  "s_cmp_ge_u32 %[iss], %[t1]\n\t" \
  "s_cbranch_scc1 QD_%=\n\t" \
  "QS4_%=:\n\t" \
  "v_readlane_b32 s94, v167, %[iss]\n\t" \
  "s_nop 4\n\t" \
  "buffer_load_dwordx4 v[64:67], v164, %[ob], s94 offen offset:0 sc1\n\t" \
  "buffer_load_dwordx4 v[68:71], v164, %[ob], s94 offen offset:1024 sc1\n\t" \
  "buffer_load_dwordx4 v[72:75], v164, %[ob], s94 offen offset:2048 sc1\n\t" \
  "buffer_load_dwordx4 v[76:79], v164, %[ob], s94 offen offset:3072 sc1\n\t" \
  "s_add_u32 %[iss], %[iss], 1\n\t" \
  "s_cmp_ge_u32 %[iss], %[t1]\n\t" \
  "s_cbranch_scc1 QD_%=\n\t" \
  "QS5_%=:\n\t" \
  "v_readlane_b32 s94, v167, %[iss]\n\t" \
  "s_nop 4\n\t" \
  "buffer_load_dwordx4 v[80:83], v164, %[ob], s94 offen offset:0 sc1\n\t" \
  "buffer_load_dwordx4 v[84:87], v164, %[ob], s94 offen offset:1024 sc1\n\t" \
  "buffer_load_dwordx4 v[88:91], v164, %[ob], s94 offen offset:2048 sc1\n\t" \
  "buffer_load_dwordx4 v[92:95], v164, %[ob], s94 offen offset:3072 sc1\n\t" \
  "s_add_u32 %[iss], %[iss], 1\n\t" \
  "s_cmp_ge_u32 %[iss], %[t1]\n\t" \
  "s_cbranch_scc0 QS0_%=\n\t" \
  "QD_%=:\n\t" \
  "s_cmp_gt_u32 %[iss], %[c]\n\t" \
  "s_cbranch_scc1 QW_%=\n\t" \
  "s_addk_i32 %[cn], 1\n\t" \
  "s_cmp_gt_u32 %[cn], 0x400000\n\t" \
  "s_cbranch_scc1 QT_%=\n\t" \
  "s_sleep 1\n\t" \
  "s_branch QU_%=\n\t" \
  "QT_%=:\n\t" \
  "s_add_u32 %[t1], %[c], 1\n\t" \
  "s_branch QL_%=\n\t" \
  "QW_%=:\n\t" \
  "s_cmp_eq_u32 %[iss], 16\n\t" \
  "s_cselect_b64 vcc, -1, 0\n\t" \
  "s_sub_u32 %[t0], %[iss], %[c]\n\t" \
  "s_cmp_eq_u32 %[t0], 5\n\t" \
  "s_cselect_b32 %[th], %[iss], 99\n\t" \
  "s_waitcnt vmcnt(16)\n\t" \
  "s_cmp_ge_u32 %[t0], 5\n\t" \
  "s_cbranch_scc1 QR_%=\n\t" \
  "s_waitcnt vmcnt(12)\n\t" \
  "s_cmp_ge_u32 %[t0], 4\n\t" \
  "s_cbranch_scc1 QR_%=\n\t" \
  "s_waitcnt vmcnt(8)\n\t" \
  "s_cmp_ge_u32 %[t0], 3\n\t" \
  "s_cbranch_scc1 QR_%=\n\t" \
  "s_waitcnt vmcnt(4)\n\t" \
  "s_cmp_ge_u32 %[t0], 2\n\t" \
  "s_cbranch_scc1 QR_%=\n\t" \
  "s_waitcnt vmcnt(0)\n\t" \
  "QR_%=:\n\t" \
  "s_setpc_b64 s[92:93]\n\t" \
  "QX_%=:\n\t" \
  "s_nop 7\n\t"
#define G3_CLOB "v0", "v1", "v2", "v3", "v4", "v5", "v6", "v7", "v8", "v9", "v10", "v11", "v12", "v13", "v14", "v15", "v16", "v17", "v18", "v19", "v20", "v21", "v22", "v23", "v24", "v25", "v26", "v27", "v28", "v29", "v30", "v31", "v32", "v33", "v34", "v35", "v36", "v37", "v38", "v39", "v40", "v41", "v42", "v43", "v44", "v45", "v46", "v47", "v48", "v49", "v50", "v51", "v52", "v53", "v54", "v55", "v56", "v57", "v58", "v59", "v60", "v61", "v62", "v63", "v64", "v65", "v66", "v67", "v68", "v69", "v70", "v71", "v72", "v73", "v74", "v75", "v76", "v77", "v78", "v79", "v80", "v81", "v82", "v83", "v84", "v85", "v86", "v87", "v88", "v89", "v90", "v91", "v92", "v93", "v94", "v95", "v96", "v97", "v98", "v99", "v100", "v101", "v102", "v103", "v104", "v105", "v106", "v107", "v108", "v109", "v110", "v111", "v112", "v113", "v114", "v115", "v116", "v117", "v118", "v119", "v120", "v121", "v122", "v123", "v124", "v125", "v126", "v127", "v166", "s88", "s89", "s92", "s93", "s94"

#define PP_ASM \
  "v_mov_b32 v60, 0\n\t" \
  "s_mov_b32 %[t0], %[n0]\n\t" \
  "s_cmp_eq_u32 %[t0], 0\n\t" \
  "s_cbranch_scc1 PAx_%=\n\t" \
  "PA_%=:\n\t" \
  "s_barrier\n\t" \
  "s_sub_u32 %[t0], %[t0], 1\n\t" \
  "s_cmp_lg_u32 %[t0], 0\n\t" \
  "s_cbranch_scc1 PA_%=\n\t" \
  "PAx_%=:\n\t" \
  "s_mov_b32 m0, %[shd]\n\t" \
  "global_load_dword v160, v60, %[fl] sc1\n\t" \
  "global_load_lds_dwordx4 v163, %[shp]\n\t" \
  "s_mov_b32 m0, %[tgd]\n\t" \
  "s_mov_b64 exec, %[em]\n\t" \
  "global_load_lds_dword v60, %[fl] sc1\n\t" \
  "s_mov_b64 exec, -1\n\t" \
  "global_load_ushort v161, v60, %[hp]\n\t" \
  "global_load_ushort v162, v60, %[dp]\n\t" \
  "global_load_dwordx4 v[0:3], v152, %[kb0]\n\t" \
  "global_load_dwordx4 v[4:7], v153, %[kb0]\n\t" \
  "global_load_dwordx4 v[8:11], v154, %[kb0]\n\t" \
  "global_load_dwordx4 v[12:15], v155, %[kb3] offset:0\n\t" \
  "global_load_dwordx4 v[28:31], v155, %[vb0] offset:0\n\t" \
  "global_load_dwordx4 v[16:19], v155, %[kb3] offset:2048\n\t" \
  "global_load_dwordx4 v[32:35], v155, %[vb0] offset:2048\n\t" \
  "global_load_dwordx4 v[20:23], v155, %[kb5] offset:0\n\t" \
  "global_load_dwordx4 v[36:39], v155, %[vb2] offset:0\n\t" \
  "global_load_dwordx4 v[24:27], v155, %[kb5] offset:2048\n\t" \
  "global_load_dwordx4 v[40:43], v155, %[vb2] offset:2048\n\t" \
  "s_setprio 2\n\t" \
  "s_barrier\n\t" \
  "s_barrier\n\t" \
  "s_waitcnt vmcnt(11)\n\t" \
  "ds_read_b128 v[128:131], v156 offset:0\n\t" \
  "ds_read_b128 v[132:135], v156 offset:16\n\t" \
  "ds_read_b128 v[136:139], v156 offset:32\n\t" \
  "ds_read_b128 v[140:143], v156 offset:48\n\t" \
  "s_waitcnt lgkmcnt(0)\n\t" \
  "v_lshlrev_b32 v64, 16, v128\n\t" \
  "v_and_b32 v65, 0xffff0000, v128\n\t" \
  "v_lshlrev_b32 v66, 16, v129\n\t" \
  "v_and_b32 v67, 0xffff0000, v129\n\t" \
  "v_lshlrev_b32 v68, 16, v130\n\t" \
  "v_and_b32 v69, 0xffff0000, v130\n\t" \
  "v_lshlrev_b32 v70, 16, v131\n\t" \
  "v_and_b32 v71, 0xffff0000, v131\n\t" \
  "v_lshlrev_b32 v72, 16, v132\n\t" \
  "v_and_b32 v73, 0xffff0000, v132\n\t" \
  "v_lshlrev_b32 v74, 16, v133\n\t" \
  "v_and_b32 v75, 0xffff0000, v133\n\t" \
  "v_lshlrev_b32 v76, 16, v134\n\t" \
  "v_and_b32 v77, 0xffff0000, v134\n\t" \
  "v_lshlrev_b32 v78, 16, v135\n\t" \
  "v_and_b32 v79, 0xffff0000, v135\n\t" \
  "v_lshlrev_b32 v80, 16, v136\n\t" \
  "v_and_b32 v81, 0xffff0000, v136\n\t" \
  "v_lshlrev_b32 v82, 16, v137\n\t" \
  "v_and_b32 v83, 0xffff0000, v137\n\t" \
  "v_lshlrev_b32 v84, 16, v138\n\t" \
  "v_and_b32 v85, 0xffff0000, v138\n\t" \
  "v_lshlrev_b32 v86, 16, v139\n\t" \
  "v_and_b32 v87, 0xffff0000, v139\n\t" \
  "v_lshlrev_b32 v88, 16, v140\n\t" \
  "v_and_b32 v89, 0xffff0000, v140\n\t" \
  "v_lshlrev_b32 v90, 16, v141\n\t" \
  "v_and_b32 v91, 0xffff0000, v141\n\t" \
  "v_lshlrev_b32 v92, 16, v142\n\t" \
  "v_and_b32 v93, 0xffff0000, v142\n\t" \
  "v_lshlrev_b32 v94, 16, v143\n\t" \
  "v_and_b32 v95, 0xffff0000, v143\n\t" \
  "s_waitcnt vmcnt(10)\n\t" \
  "s_mov_b64 exec, %[zm]\n\t" \
  "v_mov_b32 v0, 0\n\t" \
  "v_mov_b32 v1, 0\n\t" \
  "v_mov_b32 v2, 0\n\t" \
  "v_mov_b32 v3, 0\n\t" \
  "s_mov_b64 exec, -1\n\t" \
  "v_lshlrev_b32 v128, 16, v0\n\t" \
  "v_and_b32 v129, 0xffff0000, v0\n\t" \
  "s_barrier\n\t" \
  "v_lshlrev_b32 v130, 16, v1\n\t" \
  "v_and_b32 v131, 0xffff0000, v1\n\t" \
  "v_lshlrev_b32 v132, 16, v2\n\t" \
  "v_and_b32 v133, 0xffff0000, v2\n\t" \
  "v_lshlrev_b32 v134, 16, v3\n\t" \
  "v_and_b32 v135, 0xffff0000, v3\n\t" \
  "v_mul_f32 v96, v128, v67\n\t" \
  "v_mul_f32 v97, v129, v71\n\t" \
  "v_mul_f32 v98, v130, v75\n\t" \
  "v_mul_f32 v99, v131, v79\n\t" \
  "v_mul_f32 v100, v132, v83\n\t" \
  "v_mul_f32 v101, v133, v87\n\t" \
  "v_mul_f32 v102, v134, v91\n\t" \
  "v_mul_f32 v103, v135, v95\n\t" \
  "s_waitcnt vmcnt(9)\n\t" \
  "s_mov_b64 exec, %[zm]\n\t" \
  "v_mov_b32 v4, 0\n\t" \
  "v_mov_b32 v5, 0\n\t" \
  "v_mov_b32 v6, 0\n\t" \
  "v_mov_b32 v7, 0\n\t" \
  "s_mov_b64 exec, -1\n\t" \
  "v_lshlrev_b32 v128, 16, v4\n\t" \
  "v_and_b32 v129, 0xffff0000, v4\n\t" \
  "v_lshlrev_b32 v130, 16, v5\n\t" \
  "v_and_b32 v131, 0xffff0000, v5\n\t" \
  "v_lshlrev_b32 v132, 16, v6\n\t" \
  "v_and_b32 v133, 0xffff0000, v6\n\t" \
  "v_lshlrev_b32 v134, 16, v7\n\t" \
  "v_and_b32 v135, 0xffff0000, v7\n\t" \
  "v_fmac_f32 v96, v128, v66\n\t" \
  "v_fmac_f32 v97, v129, v70\n\t" \
  "v_fmac_f32 v98, v130, v74\n\t" \
  "v_fmac_f32 v99, v131, v78\n\t" \
  "v_fmac_f32 v100, v132, v82\n\t" \
  "v_fmac_f32 v101, v133, v86\n\t" \
  "v_fmac_f32 v102, v134, v90\n\t" \
  "v_fmac_f32 v103, v135, v94\n\t" \
  "v_mul_f32 v104, v128, v67\n\t" \
  "v_mul_f32 v105, v129, v71\n\t" \
  "v_mul_f32 v106, v130, v75\n\t" \
  "v_mul_f32 v107, v131, v79\n\t" \
  "v_mul_f32 v108, v132, v83\n\t" \
  "v_mul_f32 v109, v133, v87\n\t" \
  "v_mul_f32 v110, v134, v91\n\t" \
  "v_mul_f32 v111, v135, v95\n\t" \
  "s_waitcnt vmcnt(8)\n\t" \
  "s_mov_b64 exec, %[zm]\n\t" \
  "v_mov_b32 v8, 0\n\t" \
  "v_mov_b32 v9, 0\n\t" \
  "v_mov_b32 v10, 0\n\t" \
  "v_mov_b32 v11, 0\n\t" \
  "s_mov_b64 exec, -1\n\t" \
  "s_barrier\n\t" \
  "v_lshlrev_b32 v128, 16, v8\n\t" \
  "v_and_b32 v129, 0xffff0000, v8\n\t" \
  "v_lshlrev_b32 v130, 16, v9\n\t" \
  "v_and_b32 v131, 0xffff0000, v9\n\t" \
  "v_lshlrev_b32 v132, 16, v10\n\t" \
  "v_and_b32 v133, 0xffff0000, v10\n\t" \
  "v_lshlrev_b32 v134, 16, v11\n\t" \
  "v_and_b32 v135, 0xffff0000, v11\n\t" \
  "v_fmac_f32 v96, v128, v65\n\t" \
  "v_fmac_f32 v97, v129, v69\n\t" \
  "v_fmac_f32 v98, v130, v73\n\t" \
  "v_fmac_f32 v99, v131, v77\n\t" \
  "v_fmac_f32 v100, v132, v81\n\t" \
  "v_fmac_f32 v101, v133, v85\n\t" \
  "v_fmac_f32 v102, v134, v89\n\t" \
  "v_fmac_f32 v103, v135, v93\n\t" \
  "v_fmac_f32 v104, v128, v66\n\t" \
  "v_fmac_f32 v105, v129, v70\n\t" \
  "v_fmac_f32 v106, v130, v74\n\t" \
  "v_fmac_f32 v107, v131, v78\n\t" \
  "v_fmac_f32 v108, v132, v82\n\t" \
  "v_fmac_f32 v109, v133, v86\n\t" \
  "v_fmac_f32 v110, v134, v90\n\t" \
  "v_fmac_f32 v111, v135, v94\n\t" \
  "v_mul_f32 v112, v128, v67\n\t" \
  "v_mul_f32 v113, v129, v71\n\t" \
  "v_mul_f32 v114, v130, v75\n\t" \
  "v_mul_f32 v115, v131, v79\n\t" \
  "v_mul_f32 v116, v132, v83\n\t" \
  "v_mul_f32 v117, v133, v87\n\t" \
  "v_mul_f32 v118, v134, v91\n\t" \
  "v_mul_f32 v119, v135, v95\n\t" \
  "s_waitcnt vmcnt(7)\n\t" \
  "v_lshlrev_b32 v128, 16, v12\n\t" \
  "v_and_b32 v129, 0xffff0000, v12\n\t" \
  "v_lshlrev_b32 v130, 16, v13\n\t" \
  "v_and_b32 v131, 0xffff0000, v13\n\t" \
  "v_lshlrev_b32 v132, 16, v14\n\t" \
  "v_and_b32 v133, 0xffff0000, v14\n\t" \
  "v_lshlrev_b32 v134, 16, v15\n\t" \
  "v_and_b32 v135, 0xffff0000, v15\n\t" \
  "s_barrier\n\t" \
  "v_fmac_f32 v96, v128, v64\n\t" \
  "v_fmac_f32 v97, v129, v68\n\t" \
  "v_fmac_f32 v98, v130, v72\n\t" \
  "v_fmac_f32 v99, v131, v76\n\t" \
  "v_fmac_f32 v100, v132, v80\n\t" \
  "v_fmac_f32 v101, v133, v84\n\t" \
  "v_fmac_f32 v102, v134, v88\n\t" \
  "v_fmac_f32 v103, v135, v92\n\t" \
  "v_fmac_f32 v104, v128, v65\n\t" \
  "v_fmac_f32 v105, v129, v69\n\t" \
  "v_fmac_f32 v106, v130, v73\n\t" \
  "v_fmac_f32 v107, v131, v77\n\t" \
  "v_fmac_f32 v108, v132, v81\n\t" \
  "v_fmac_f32 v109, v133, v85\n\t" \
  "v_fmac_f32 v110, v134, v89\n\t" \
  "v_fmac_f32 v111, v135, v93\n\t" \
  "v_fmac_f32 v112, v128, v66\n\t" \
  "v_fmac_f32 v113, v129, v70\n\t" \
  "v_fmac_f32 v114, v130, v74\n\t" \
  "v_fmac_f32 v115, v131, v78\n\t" \
  "v_fmac_f32 v116, v132, v82\n\t" \
  "v_fmac_f32 v117, v133, v86\n\t" \
  "v_fmac_f32 v118, v134, v90\n\t" \
  "v_fmac_f32 v119, v135, v94\n\t" \
  "v_mul_f32 v120, v128, v67\n\t" \
  "v_mul_f32 v121, v129, v71\n\t" \
  "v_mul_f32 v122, v130, v75\n\t" \
  "v_mul_f32 v123, v131, v79\n\t" \
  "v_mul_f32 v124, v132, v83\n\t" \
  "v_mul_f32 v125, v133, v87\n\t" \
  "v_mul_f32 v126, v134, v91\n\t" \
  "v_mul_f32 v127, v135, v95\n\t" \
  "s_waitcnt vmcnt(6)\n\t" \
  "v_lshlrev_b32 v128, 16, v28\n\t" \
  "v_and_b32 v129, 0xffff0000, v28\n\t" \
  "v_lshlrev_b32 v130, 16, v29\n\t" \
  "v_and_b32 v131, 0xffff0000, v29\n\t" \
  "v_lshlrev_b32 v132, 16, v30\n\t" \
  "v_and_b32 v133, 0xffff0000, v30\n\t" \
  "v_lshlrev_b32 v134, 16, v31\n\t" \
  "v_and_b32 v135, 0xffff0000, v31\n\t" \
  "s_barrier\n\t" \
  "v_mul_f32 v96, v96, v128\n\t" \
  "v_mul_f32 v97, v97, v129\n\t" \
  "v_mul_f32 v98, v98, v130\n\t" \
  "v_mul_f32 v99, v99, v131\n\t" \
  "v_mul_f32 v100, v100, v132\n\t" \
  "v_mul_f32 v101, v101, v133\n\t" \
  "v_mul_f32 v102, v102, v134\n\t" \
  "v_mul_f32 v103, v103, v135\n\t" \
  "v_cvt_pk_bf16_f32 v136, v96, v97\n\t" \
  "v_cvt_pk_bf16_f32 v137, v98, v99\n\t" \
  "v_cvt_pk_bf16_f32 v138, v100, v101\n\t" \
  "v_cvt_pk_bf16_f32 v139, v102, v103\n\t" \
  "v_mov_b32_dpp v128, v136 row_ror:8 row_mask:0xf bank_mask:0xf\n\t" \
  "v_mov_b32_dpp v129, v137 row_ror:8 row_mask:0xf bank_mask:0xf\n\t" \
  "v_mov_b32_dpp v130, v138 row_ror:8 row_mask:0xf bank_mask:0xf\n\t" \
  "v_mov_b32_dpp v131, v139 row_ror:8 row_mask:0xf bank_mask:0xf\n\t" \
  "v_perm_b32 v136, v128, v136, v157\n\t" \
  "v_perm_b32 v137, v129, v137, v157\n\t" \
  "v_perm_b32 v138, v130, v138, v157\n\t" \
  "v_perm_b32 v139, v131, v139, v157\n\t" \
  "v_permlane16_swap_b32 v136, v137\n\t" \
  "s_nop 0\n\t" \
  "v_permlane16_swap_b32 v138, v139\n\t" \
  "s_nop 1\n\t" \
  "v_permlane32_swap_b32 v136, v138\n\t" \
  "v_permlane32_swap_b32 v137, v139\n\t" \
  "s_waitcnt vmcnt(5)\n\t" \
  "v_lshlrev_b32 v128, 16, v16\n\t" \
  "v_and_b32 v129, 0xffff0000, v16\n\t" \
  "v_lshlrev_b32 v130, 16, v17\n\t" \
  "v_and_b32 v131, 0xffff0000, v17\n\t" \
  "v_lshlrev_b32 v132, 16, v18\n\t" \
  "v_and_b32 v133, 0xffff0000, v18\n\t" \
  "v_lshlrev_b32 v134, 16, v19\n\t" \
  "v_and_b32 v135, 0xffff0000, v19\n\t" \
  "v_fmac_f32 v104, v128, v64\n\t" \
  "v_fmac_f32 v105, v129, v68\n\t" \
  "v_fmac_f32 v106, v130, v72\n\t" \
  "v_fmac_f32 v107, v131, v76\n\t" \
  "v_fmac_f32 v108, v132, v80\n\t" \
  "v_fmac_f32 v109, v133, v84\n\t" \
  "s_setprio 3\n\t" \
  "s_barrier\n\t" \
  "global_store_dwordx4 v159, v[136:139], %[sb] sc1\n\t" \
  "v_add_u32 v158, 0x80, v159\n\t" \
  "v_bfi_b32 v159, %[mk], v158, v159\n\t" \
  "s_setprio 2\n\t" \
  "v_fmac_f32 v110, v134, v88\n\t" \
  "v_fmac_f32 v111, v135, v92\n\t" \
  "v_fmac_f32 v112, v128, v65\n\t" \
  "v_fmac_f32 v113, v129, v69\n\t" \
  "v_fmac_f32 v114, v130, v73\n\t" \
  "v_fmac_f32 v115, v131, v77\n\t" \
  "v_fmac_f32 v116, v132, v81\n\t" \
  "v_fmac_f32 v117, v133, v85\n\t" \
  "v_fmac_f32 v118, v134, v89\n\t" \
  "v_fmac_f32 v119, v135, v93\n\t" \
  "v_fmac_f32 v120, v128, v66\n\t" \
  "v_fmac_f32 v121, v129, v70\n\t" \
  "v_fmac_f32 v122, v130, v74\n\t" \
  "v_fmac_f32 v123, v131, v78\n\t" \
  "v_fmac_f32 v124, v132, v82\n\t" \
  "v_fmac_f32 v125, v133, v86\n\t" \
  "v_fmac_f32 v126, v134, v90\n\t" \
  "v_fmac_f32 v127, v135, v94\n\t" \
  "s_waitcnt vmcnt(5)\n\t" \
  "v_lshlrev_b32 v128, 16, v32\n\t" \
  "v_and_b32 v129, 0xffff0000, v32\n\t" \
  "v_lshlrev_b32 v130, 16, v33\n\t" \
  "v_and_b32 v131, 0xffff0000, v33\n\t" \
  "v_lshlrev_b32 v132, 16, v34\n\t" \
  "v_and_b32 v133, 0xffff0000, v34\n\t" \
  "v_lshlrev_b32 v134, 16, v35\n\t" \
  "v_and_b32 v135, 0xffff0000, v35\n\t" \
  "v_mul_f32 v104, v104, v128\n\t" \
  "v_mul_f32 v105, v105, v129\n\t" \
  "v_mul_f32 v106, v106, v130\n\t" \
  "v_mul_f32 v107, v107, v131\n\t" \
  "v_mul_f32 v108, v108, v132\n\t" \
  "v_mul_f32 v109, v109, v133\n\t" \
  "v_mul_f32 v110, v110, v134\n\t" \
  "v_mul_f32 v111, v111, v135\n\t" \
  "v_cvt_pk_bf16_f32 v140, v104, v105\n\t" \
  "v_cvt_pk_bf16_f32 v141, v106, v107\n\t" \
  "v_cvt_pk_bf16_f32 v142, v108, v109\n\t" \
  "v_cvt_pk_bf16_f32 v143, v110, v111\n\t" \
  "v_mov_b32_dpp v128, v140 row_ror:8 row_mask:0xf bank_mask:0xf\n\t" \
  "v_mov_b32_dpp v129, v141 row_ror:8 row_mask:0xf bank_mask:0xf\n\t" \
  "s_barrier\n\t" \
  "v_mov_b32_dpp v130, v142 row_ror:8 row_mask:0xf bank_mask:0xf\n\t" \
  "v_mov_b32_dpp v131, v143 row_ror:8 row_mask:0xf bank_mask:0xf\n\t" \
  "v_perm_b32 v140, v128, v140, v157\n\t" \
  "v_perm_b32 v141, v129, v141, v157\n\t" \
  "v_perm_b32 v142, v130, v142, v157\n\t" \
  "v_perm_b32 v143, v131, v143, v157\n\t" \
  "v_permlane16_swap_b32 v140, v141\n\t" \
  "s_nop 0\n\t" \
  "v_permlane16_swap_b32 v142, v143\n\t" \
  "s_nop 1\n\t" \
  "v_permlane32_swap_b32 v140, v142\n\t" \
  "v_permlane32_swap_b32 v141, v143\n\t" \
  "s_waitcnt vmcnt(4)\n\t" \
  "v_lshlrev_b32 v128, 16, v20\n\t" \
  "v_and_b32 v129, 0xffff0000, v20\n\t" \
  "v_lshlrev_b32 v130, 16, v21\n\t" \
  "v_and_b32 v131, 0xffff0000, v21\n\t" \
  "v_lshlrev_b32 v132, 16, v22\n\t" \
  "v_and_b32 v133, 0xffff0000, v22\n\t" \
  "v_lshlrev_b32 v134, 16, v23\n\t" \
  "v_and_b32 v135, 0xffff0000, v23\n\t" \
  "v_fmac_f32 v112, v128, v64\n\t" \
  "v_fmac_f32 v113, v129, v68\n\t" \
  "v_fmac_f32 v114, v130, v72\n\t" \
  "v_fmac_f32 v115, v131, v76\n\t" \
  "v_fmac_f32 v116, v132, v80\n\t" \
  "v_fmac_f32 v117, v133, v84\n\t" \
  "v_fmac_f32 v118, v134, v88\n\t" \
  "v_fmac_f32 v119, v135, v92\n\t" \
  "v_fmac_f32 v120, v128, v65\n\t" \
  "v_fmac_f32 v121, v129, v69\n\t" \
  "v_fmac_f32 v122, v130, v73\n\t" \
  "v_fmac_f32 v123, v131, v77\n\t" \
  "v_fmac_f32 v124, v132, v81\n\t" \
  "v_fmac_f32 v125, v133, v85\n\t" \
  "v_fmac_f32 v126, v134, v89\n\t" \
  "v_fmac_f32 v127, v135, v93\n\t" \
  "s_waitcnt vmcnt(3)\n\t" \
  "v_lshlrev_b32 v128, 16, v36\n\t" \
  "v_and_b32 v129, 0xffff0000, v36\n\t" \
  "v_lshlrev_b32 v130, 16, v37\n\t" \
  "v_and_b32 v131, 0xffff0000, v37\n\t" \
  "s_setprio 3\n\t" \
  "s_barrier\n\t" \
  "global_store_dwordx4 v159, v[140:143], %[sb] sc1\n\t" \
  "v_add_u32 v158, 0x80, v159\n\t" \
  "v_bfi_b32 v159, %[mk], v158, v159\n\t" \
  "s_mov_b32 m0, %[hd]\n\t" \
  "s_nop 0\n\t" \
  "global_load_lds_dwordx4 v163, %[hp] offset:0\n\t" \
  "global_load_lds_dwordx4 v163, %[hp] offset:1024\n\t" \
  "global_load_lds_dwordx4 v163, %[hp] offset:2048\n\t" \
  "global_load_lds_dwordx4 v163, %[hp] offset:3072\n\t" \
  "s_setprio 2\n\t" \
  "v_lshlrev_b32 v132, 16, v38\n\t" \
  "v_and_b32 v133, 0xffff0000, v38\n\t" \
  "v_lshlrev_b32 v134, 16, v39\n\t" \
  "v_and_b32 v135, 0xffff0000, v39\n\t" \
  "v_mul_f32 v112, v112, v128\n\t" \
  "v_mul_f32 v113, v113, v129\n\t" \
  "v_mul_f32 v114, v114, v130\n\t" \
  "v_mul_f32 v115, v115, v131\n\t" \
  "v_mul_f32 v116, v116, v132\n\t" \
  "v_mul_f32 v117, v117, v133\n\t" \
  "v_mul_f32 v118, v118, v134\n\t" \
  "v_mul_f32 v119, v119, v135\n\t" \
  "v_cvt_pk_bf16_f32 v144, v112, v113\n\t" \
  "v_cvt_pk_bf16_f32 v145, v114, v115\n\t" \
  "v_cvt_pk_bf16_f32 v146, v116, v117\n\t" \
  "v_cvt_pk_bf16_f32 v147, v118, v119\n\t" \
  "v_mov_b32_dpp v128, v144 row_ror:8 row_mask:0xf bank_mask:0xf\n\t" \
  "v_mov_b32_dpp v129, v145 row_ror:8 row_mask:0xf bank_mask:0xf\n\t" \
  "v_mov_b32_dpp v130, v146 row_ror:8 row_mask:0xf bank_mask:0xf\n\t" \
  "v_mov_b32_dpp v131, v147 row_ror:8 row_mask:0xf bank_mask:0xf\n\t" \
  "v_perm_b32 v144, v128, v144, v157\n\t" \
  "v_perm_b32 v145, v129, v145, v157\n\t" \
  "v_perm_b32 v146, v130, v146, v157\n\t" \
  "v_perm_b32 v147, v131, v147, v157\n\t" \
  "v_permlane16_swap_b32 v144, v145\n\t" \
  "s_nop 0\n\t" \
  "v_permlane16_swap_b32 v146, v147\n\t" \
  "s_nop 1\n\t" \
  "v_permlane32_swap_b32 v144, v146\n\t" \
  "v_permlane32_swap_b32 v145, v147\n\t" \
  "s_waitcnt vmcnt(7)\n\t" \
  "v_lshlrev_b32 v128, 16, v24\n\t" \
  "v_and_b32 v129, 0xffff0000, v24\n\t" \
  "v_lshlrev_b32 v130, 16, v25\n\t" \
  "v_and_b32 v131, 0xffff0000, v25\n\t" \
  "v_lshlrev_b32 v132, 16, v26\n\t" \
  "v_and_b32 v133, 0xffff0000, v26\n\t" \
  "v_lshlrev_b32 v134, 16, v27\n\t" \
  "v_and_b32 v135, 0xffff0000, v27\n\t" \
  "v_fmac_f32 v120, v128, v64\n\t" \
  "v_fmac_f32 v121, v129, v68\n\t" \
  "s_setprio 3\n\t" \
  "s_barrier\n\t" \
  "global_store_dwordx4 v159, v[144:147], %[sb] sc1\n\t" \
  "v_add_u32 v158, 0x80, v159\n\t" \
  "v_bfi_b32 v159, %[mk], v158, v159\n\t" \
  "s_setprio 2\n\t" \
  "v_fmac_f32 v122, v130, v72\n\t" \
  "v_fmac_f32 v123, v131, v76\n\t" \
  "v_fmac_f32 v124, v132, v80\n\t" \
  "v_fmac_f32 v125, v133, v84\n\t" \
  "v_fmac_f32 v126, v134, v88\n\t" \
  "v_fmac_f32 v127, v135, v92\n\t" \
  "s_waitcnt vmcnt(7)\n\t" \
  "v_lshlrev_b32 v128, 16, v40\n\t" \
  "v_and_b32 v129, 0xffff0000, v40\n\t" \
  "v_lshlrev_b32 v130, 16, v41\n\t" \
  "v_and_b32 v131, 0xffff0000, v41\n\t" \
  "v_lshlrev_b32 v132, 16, v42\n\t" \
  "v_and_b32 v133, 0xffff0000, v42\n\t" \
  "v_lshlrev_b32 v134, 16, v43\n\t" \
  "v_and_b32 v135, 0xffff0000, v43\n\t" \
  "v_mul_f32 v120, v120, v128\n\t" \
  "v_mul_f32 v121, v121, v129\n\t" \
  "v_mul_f32 v122, v122, v130\n\t" \
  "v_mul_f32 v123, v123, v131\n\t" \
  "v_mul_f32 v124, v124, v132\n\t" \
  "v_mul_f32 v125, v125, v133\n\t" \
  "v_mul_f32 v126, v126, v134\n\t" \
  "v_mul_f32 v127, v127, v135\n\t" \
  "v_cvt_pk_bf16_f32 v148, v120, v121\n\t" \
  "v_cvt_pk_bf16_f32 v149, v122, v123\n\t" \
  "v_cvt_pk_bf16_f32 v150, v124, v125\n\t" \
  "v_cvt_pk_bf16_f32 v151, v126, v127\n\t" \
  "v_mov_b32_dpp v128, v148 row_ror:8 row_mask:0xf bank_mask:0xf\n\t" \
  "v_mov_b32_dpp v129, v149 row_ror:8 row_mask:0xf bank_mask:0xf\n\t" \
  "v_mov_b32_dpp v130, v150 row_ror:8 row_mask:0xf bank_mask:0xf\n\t" \
  "v_mov_b32_dpp v131, v151 row_ror:8 row_mask:0xf bank_mask:0xf\n\t" \
  "v_perm_b32 v148, v128, v148, v157\n\t" \
  "v_perm_b32 v149, v129, v149, v157\n\t" \
  "v_perm_b32 v150, v130, v150, v157\n\t" \
  "v_perm_b32 v151, v131, v151, v157\n\t" \
  "v_permlane16_swap_b32 v148, v149\n\t" \
  "s_nop 0\n\t" \
  "v_permlane16_swap_b32 v150, v151\n\t" \
  "s_nop 1\n\t" \
  "v_permlane32_swap_b32 v148, v150\n\t" \
  "v_permlane32_swap_b32 v149, v151\n\t" \
  "s_setprio 3\n\t" \
  "s_barrier\n\t" \
  "global_store_dwordx4 v159, v[148:151], %[sb] sc1\n\t" \
  "s_setprio 2\n\t" \
  "s_waitcnt vmcnt(2)\n\t" \
  "ds_read_b64_tr_b16 v[64:65], v165 offset:32\n\t" \
  "ds_read_b64_tr_b16 v[66:67], v165 offset:0\n\t" \
  "ds_read_b64_tr_b16 v[68:69], v165 offset:288\n\t" \
  "ds_read_b64_tr_b16 v[70:71], v165 offset:256\n\t" \
  "ds_read_b64_tr_b16 v[72:73], v165 offset:544\n\t" \
  "ds_read_b64_tr_b16 v[74:75], v165 offset:512\n\t" \
  "ds_read_b64_tr_b16 v[76:77], v165 offset:800\n\t" \
  "ds_read_b64_tr_b16 v[78:79], v165 offset:768\n\t" \
  "v_mov_b32 v24, v166\n\t" \
  "v_mov_b32 v25, v167\n\t" \
  "v_mov_b32 v63, 0xc0\n\t" \
  "v_add_u32 v158, 64, v24\n\t" \
  "v_bfi_b32 v26, v63, v158, v24\n\t" \
  "v_add_u32 v158, 64, v25\n\t" \
  "v_bfi_b32 v27, v63, v158, v25\n\t" \
  "v_add_u32 v158, 64, v26\n\t" \
  "v_bfi_b32 v28, v63, v158, v26\n\t" \
  "v_add_u32 v158, 64, v27\n\t" \
  "v_bfi_b32 v29, v63, v158, v27\n\t" \
  "v_add_u32 v158, 64, v28\n\t" \
  "v_bfi_b32 v30, v63, v158, v28\n\t" \
  "v_add_u32 v158, 64, v29\n\t" \
  "v_bfi_b32 v31, v63, v158, v29\n\t" \
  "s_barrier\n\t" \
  "ds_read_b64_tr_b16 v[80:81], v165 offset:1056\n\t" \
  "ds_read_b64_tr_b16 v[82:83], v165 offset:1024\n\t" \
  "ds_read_b64_tr_b16 v[84:85], v165 offset:1312\n\t" \
  "ds_read_b64_tr_b16 v[86:87], v165 offset:1280\n\t" \
  "ds_read_b64_tr_b16 v[88:89], v165 offset:1568\n\t" \
  "ds_read_b64_tr_b16 v[90:91], v165 offset:1536\n\t" \
  "ds_read_b64_tr_b16 v[92:93], v165 offset:1824\n\t" \
  "ds_read_b64_tr_b16 v[94:95], v165 offset:1792\n\t" \
  "s_setprio 3\n\t" \
  "s_barrier\n\t" \
  "s_waitcnt vmcnt(0)\n\t" \
  "v_add_u32 v61, 1, v160\n\t" \
  "s_mov_b64 exec, 1\n\t" \
  "global_store_dword v60, v61, %[ff] sc1\n\t" \
  "s_mov_b64 exec, -1\n\t" \
  "s_setprio 2\n\t" \
  "ds_read_b64_tr_b16 v[96:97], v165 offset:2080\n\t" \
  "ds_read_b64_tr_b16 v[98:99], v165 offset:2048\n\t" \
  "ds_read_b64_tr_b16 v[100:101], v165 offset:2336\n\t" \
  "ds_read_b64_tr_b16 v[102:103], v165 offset:2304\n\t" \
  "ds_read_b64_tr_b16 v[104:105], v165 offset:2592\n\t" \
  "ds_read_b64_tr_b16 v[106:107], v165 offset:2560\n\t" \
  "ds_read_b64_tr_b16 v[108:109], v165 offset:2848\n\t" \
  "ds_read_b64_tr_b16 v[110:111], v165 offset:2816\n\t" \
  "s_barrier\n\t" \
  "ds_read_b64_tr_b16 v[112:113], v165 offset:3104\n\t" \
  "ds_read_b64_tr_b16 v[114:115], v165 offset:3072\n\t" \
  "ds_read_b64_tr_b16 v[116:117], v165 offset:3360\n\t" \
  "ds_read_b64_tr_b16 v[118:119], v165 offset:3328\n\t" \
  "ds_read_b64_tr_b16 v[120:121], v165 offset:3616\n\t" \
  "ds_read_b64_tr_b16 v[122:123], v165 offset:3584\n\t" \
  "ds_read_b64_tr_b16 v[124:125], v165 offset:3872\n\t" \
  "ds_read_b64_tr_b16 v[126:127], v165 offset:3840\n\t" \
  "s_barrier\n\t" \
  "s_waitcnt lgkmcnt(0)\n\t" \
  "v_lshlrev_b32 v61, 16, v161\n\t" \
  "v_lshlrev_b32 v62, 16, v162\n\t" \
  "v_add_f32 v61, v61, v62\n\t" \
  "v_mov_b32 v62, 0xffff\n\t" \
  "v_cvt_pk_bf16_f32 v61, v61, v61\n\t" \
  "s_mov_b64 exec, 1\n\t" \
  "v_bfi_b32 v66, v62, v61, v66\n\t" \
  "s_mov_b64 exec, 15\n\t" \
  "v_and_b32 v66, v62, v66\n\t" \
  "s_mov_b64 exec, 16\n\t" \
  "v_bfi_b32 v66, v62, v66, v61\n\t" \
  "s_mov_b64 exec, 0xff\n\t" \
  "v_mov_b32 v67, 0\n\t" \
  "s_mov_b64 exec, 0x100\n\t" \
  "v_mov_b32 v67, v61\n\t" \
  "s_mov_b64 exec, 0xf00\n\t" \
  "v_and_b32 v67, v62, v67\n\t" \
  "s_mov_b64 exec, 0x1000\n\t" \
  "v_bfi_b32 v67, v62, v67, v61\n\t" \
  "s_mov_b64 exec, -1\n\t" \
  "s_waitcnt lgkmcnt(0)\n\t"
#define PP_NB 15
#define PP_Q 0
#define PP_CLOB "v0", "v1", "v2", "v3", "v4", "v5", "v6", "v7", "v8", "v9", "v10", "v11", "v12", "v13", "v14", "v15", "v16", "v17", "v18", "v19", "v20", "v21", "v22", "v23", "v32", "v33", "v34", "v35", "v36", "v37", "v38", "v39", "v40", "v41", "v42", "v43", "v60", "v61", "v62", "v63", "v128", "v129", "v130", "v131", "v132", "v133", "v134", "v135", "v136", "v137", "v138", "v139", "v140", "v141", "v142", "v143", "v144", "v145", "v146", "v147", "v148", "v149", "v150", "v151", "v158", "v161", "v162"

#define LD_ASM1 \
  "s_add_u32 m0, %[wb], 49152\n\t" \
  "s_add_u32 s68, %[rw0], 0\n\t" \
  "buffer_load_dwordx4 v5, %[sw], s68 offen offset:0 lds\n\t" \
  "buffer_load_dwordx4 v6, %[sw], s68 offen offset:1024 lds\n\t" \
  "buffer_load_dwordx4 v7, %[sw], s68 offen offset:2048 lds\n\t" \
  "s_add_u32 m0, %[xb], 36864\n\t" \
  "s_add_u32 s68, %[rx0], 0\n\t" \
  "buffer_load_dwordx4 v0, %[sx], s68 offen offset:0 lds\n\t" \
  "buffer_load_dwordx4 v1, %[sx], s68 offen offset:1024 lds\n\t" \
  "s_mov_b64 exec, 0xffff\n\t" \
  "buffer_load_dwordx4 v4, %[sx], s68 offen offset:2048 lds\n\t" \
  "s_mov_b64 exec, -1\n\t" \
  "s_add_u32 m0, %[xb], 46080\n\t" \
  "s_add_u32 s68, %[rx0], 147456\n\t" \
  "buffer_load_dwordx4 v2, %[sx], s68 offen offset:0 lds\n\t" \
  "buffer_load_dwordx4 v3, %[sx], s68 offen offset:1024 lds\n\t" \
  "s_mov_b64 exec, 0xffff\n\t" \
  "buffer_load_dwordx4 v4, %[sx], s68 offen offset:2048 lds\n\t" \
  "s_mov_b64 exec, -1\n\t" \
  "s_add_u32 m0, %[wb], 61440\n\t" \
  "s_add_u32 s68, %[rw0], 196608\n\t" \
  "buffer_load_dwordx4 v5, %[sw], s68 offen offset:0 lds\n\t" \
  "buffer_load_dwordx4 v6, %[sw], s68 offen offset:1024 lds\n\t" \
  "buffer_load_dwordx4 v7, %[sw], s68 offen offset:2048 lds\n\t"
#define LD_ASM2 \
  "s_mov_b32 s85, 0x180000\n\t" \
  "s_load_dword s84, %[pw], s85\n\t" \
  "s_mov_b32 s85, 0x180800\n\t" \
  "s_load_dword s84, %[pw], s85\n\t" \
  "s_mov_b32 s85, 0x181000\n\t" \
  "s_load_dword s84, %[pw], s85\n\t" \
  "s_mov_b32 s85, 0x1b0000\n\t" \
  "s_load_dword s84, %[pw], s85\n\t" \
  "s_mov_b32 s85, 0x1b0800\n\t" \
  "s_load_dword s84, %[pw], s85\n\t" \
  "s_mov_b32 s85, 0x1b1000\n\t" \
  "s_load_dword s84, %[pw], s85\n\t" \
  "s_mov_b32 s85, 0x1e0000\n\t" \
  "s_load_dword s84, %[pw], s85\n\t" \
  "s_mov_b32 s85, 0x1e0800\n\t" \
  "s_load_dword s84, %[pw], s85\n\t" \
  "s_mov_b32 m0, %[tsk]\n\t" \
  "s_mov_b32 s85, 0x0\n\t" \
  "s_mov_b64 exec, 63\n\t" \
  "buffer_load_dword v16, %[sh], s85 offen lds\n\t" \
  "s_mov_b64 exec, -1\n\t" \
  "s_add_u32 s68, %[rx1], 0\n\t" \
  "s_add_u32 s69, %[rx1], 147456\n\t" \
  "s_add_u32 s70, %[rw1], 0\n\t" \
  "s_add_u32 m0, %[xb], 0\n\t" \
  "s_waitcnt vmcnt(3)\n\t" \
  "s_barrier\n\t" \
  "buffer_load_dwordx4 v0, %[sx], s68 offen offset:0 lds\n\t" \
  "s_mov_b32 s85, 0x1e1000\n\t" \
  "s_load_dword s84, %[pw], s85\n\t" \
  "buffer_load_dwordx4 v1, %[sx], s68 offen offset:1024 lds\n\t" \
  "s_mov_b32 s85, 0x210000\n\t" \
  "s_load_dword s84, %[pw], s85\n\t" \
  "s_mov_b64 exec, 0xffff\n\t" \
  "buffer_load_dwordx4 v4, %[sx], s68 offen offset:2048 lds\n\t" \
  "s_add_u32 m0, %[xb], 9216\n\t" \
  "s_mov_b64 exec, -1\n\t" \
  "buffer_load_dwordx4 v2, %[sx], s69 offen offset:0 lds\n\t" \
  "s_mov_b32 s85, 0x210800\n\t" \
  "s_load_dword s84, %[pw], s85\n\t" \
  "buffer_load_dwordx4 v3, %[sx], s69 offen offset:1024 lds\n\t" \
  "s_mov_b64 exec, 0xffff\n\t" \
  "buffer_load_dwordx4 v4, %[sx], s69 offen offset:2048 lds\n\t" \
  "s_add_u32 m0, %[wb], 0\n\t" \
  "s_mov_b64 exec, -1\n\t" \
  "buffer_load_dwordx4 v5, %[sw], s70 offen offset:0 lds\n\t" \
  "buffer_load_dwordx4 v6, %[sw], s70 offen offset:1024 lds\n\t" \
  "buffer_load_dwordx4 v7, %[sw], s70 offen offset:2048 lds\n\t" \
  "s_mov_b32 m0, %[tsk]\n\t" \
  "s_mov_b32 s85, 0x180000\n\t" \
  "s_mov_b64 exec, 63\n\t" \
  "buffer_load_dword v16, %[sh], s85 offen lds\n\t" \
  "s_mov_b64 exec, -1\n\t" \
  "s_add_u32 s68, %[rw1], 196608\n\t" \
  "s_add_u32 s69, %[rx2], 0\n\t" \
  "s_add_u32 s70, %[rx2], 147456\n\t" \
  "s_add_u32 m0, %[wb], 12288\n\t" \
  "s_waitcnt vmcnt(9)\n\t" \
  "s_barrier\n\t" \
  "buffer_load_dwordx4 v5, %[sw], s68 offen offset:0 lds\n\t" \
  "s_mov_b32 s85, 0x211000\n\t" \
  "s_load_dword s84, %[pw], s85\n\t" \
  "buffer_load_dwordx4 v6, %[sw], s68 offen offset:1024 lds\n\t" \
  "s_mov_b32 s85, 0x240000\n\t" \
  "s_load_dword s84, %[pw], s85\n\t" \
  "buffer_load_dwordx4 v7, %[sw], s68 offen offset:2048 lds\n\t" \
  "s_mov_b32 s85, 0x240800\n\t" \
  "s_load_dword s84, %[pw], s85\n\t" \
  "s_add_u32 m0, %[xb], 18432\n\t" \
  "s_nop 0\n\t" \
  "buffer_load_dwordx4 v0, %[sx], s69 offen offset:0 lds\n\t" \
  "buffer_load_dwordx4 v1, %[sx], s69 offen offset:1024 lds\n\t" \
  "s_mov_b64 exec, 0xffff\n\t" \
  "buffer_load_dwordx4 v4, %[sx], s69 offen offset:2048 lds\n\t" \
  "s_add_u32 m0, %[xb], 27648\n\t" \
  "s_mov_b64 exec, -1\n\t" \
  "buffer_load_dwordx4 v2, %[sx], s70 offen offset:0 lds\n\t" \
  "buffer_load_dwordx4 v3, %[sx], s70 offen offset:1024 lds\n\t" \
  "s_mov_b64 exec, 0xffff\n\t" \
  "buffer_load_dwordx4 v4, %[sx], s70 offen offset:2048 lds\n\t" \
  "s_mov_b64 exec, -1\n\t" \
  "s_add_u32 s68, %[rw2], 0\n\t" \
  "s_add_u32 s69, %[rw2], 196608\n\t" \
  "s_add_u32 s70, %[rx3], 0\n\t" \
  "s_add_u32 m0, %[wb], 24576\n\t" \
  "s_waitcnt vmcnt(9)\n\t" \
  "s_barrier\n\t" \
  "buffer_load_dwordx4 v5, %[sw], s68 offen offset:0 lds\n\t" \
  "s_mov_b32 s85, 0x241000\n\t" \
  "s_load_dword s84, %[pw], s85\n\t" \
  "buffer_load_dwordx4 v6, %[sw], s68 offen offset:1024 lds\n\t" \
  "s_mov_b32 s85, 0x270000\n\t" \
  "s_load_dword s84, %[pw], s85\n\t" \
  "buffer_load_dwordx4 v7, %[sw], s68 offen offset:2048 lds\n\t" \
  "s_mov_b32 s85, 0x270800\n\t" \
  "s_load_dword s84, %[pw], s85\n\t" \
  "s_add_u32 m0, %[wb], 36864\n\t" \
  "s_nop 0\n\t" \
  "buffer_load_dwordx4 v5, %[sw], s69 offen offset:0 lds\n\t" \
  "buffer_load_dwordx4 v6, %[sw], s69 offen offset:1024 lds\n\t" \
  "buffer_load_dwordx4 v7, %[sw], s69 offen offset:2048 lds\n\t" \
  "s_add_u32 m0, %[xb], 36864\n\t" \
  "s_nop 0\n\t" \
  "buffer_load_dwordx4 v0, %[sx], s70 offen offset:0 lds\n\t" \
  "buffer_load_dwordx4 v1, %[sx], s70 offen offset:1024 lds\n\t" \
  "s_mov_b64 exec, 0xffff\n\t" \
  "buffer_load_dwordx4 v4, %[sx], s70 offen offset:2048 lds\n\t" \
  "s_mov_b64 exec, -1\n\t" \
  "s_add_u32 s68, %[rw3], 0\n\t" \
  "s_add_u32 s69, %[rx3], 147456\n\t" \
  "s_add_u32 s70, %[rw3], 196608\n\t" \
  "s_add_u32 m0, %[wb], 49152\n\t" \
  "s_waitcnt vmcnt(15)\n\t" \
  "s_barrier\n\t" \
  "buffer_load_dwordx4 v5, %[sw], s68 offen offset:0 lds\n\t" \
  "s_mov_b32 s85, 0x271000\n\t" \
  "s_load_dword s84, %[pw], s85\n\t" \
  "buffer_load_dwordx4 v6, %[sw], s68 offen offset:1024 lds\n\t" \
  "s_mov_b32 s85, 0x2a0000\n\t" \
  "s_load_dword s84, %[pw], s85\n\t" \
  "buffer_load_dwordx4 v7, %[sw], s68 offen offset:2048 lds\n\t" \
  "s_mov_b32 s85, 0x2a0800\n\t" \
  "s_load_dword s84, %[pw], s85\n\t" \
  "s_add_u32 m0, %[xb], 46080\n\t" \
  "s_nop 0\n\t" \
  "buffer_load_dwordx4 v2, %[sx], s69 offen offset:0 lds\n\t" \
  "buffer_load_dwordx4 v3, %[sx], s69 offen offset:1024 lds\n\t" \
  "s_mov_b64 exec, 0xffff\n\t" \
  "buffer_load_dwordx4 v4, %[sx], s69 offen offset:2048 lds\n\t" \
  "s_add_u32 m0, %[wb], 61440\n\t" \
  "s_mov_b64 exec, -1\n\t" \
  "buffer_load_dwordx4 v5, %[sw], s70 offen offset:0 lds\n\t" \
  "buffer_load_dwordx4 v6, %[sw], s70 offen offset:1024 lds\n\t" \
  "buffer_load_dwordx4 v7, %[sw], s70 offen offset:2048 lds\n\t" \
  "s_add_u32 s68, %[rx0], 512\n\t" \
  "s_add_u32 s69, %[rw0], 1572864\n\t" \
  "s_add_u32 s70, %[rx0], 147968\n\t" \
  "s_add_u32 m0, %[xb], 0\n\t" \
  "s_waitcnt vmcnt(15)\n\t" \
  "s_barrier\n\t" \
  "buffer_load_dwordx4 v0, %[sx], s68 offen offset:0 lds\n\t" \
  "s_mov_b32 s85, 0x2a1000\n\t" \
  "s_load_dword s84, %[pw], s85\n\t" \
  "buffer_load_dwordx4 v1, %[sx], s68 offen offset:1024 lds\n\t" \
  "s_mov_b32 s85, 0x2d0000\n\t" \
  "s_load_dword s84, %[pw], s85\n\t" \
  "s_mov_b64 exec, 0xffff\n\t" \
  "buffer_load_dwordx4 v4, %[sx], s68 offen offset:2048 lds\n\t" \
  "s_add_u32 m0, %[wb], 0\n\t" \
  "s_mov_b64 exec, -1\n\t" \
  "buffer_load_dwordx4 v5, %[sw], s69 offen offset:0 lds\n\t" \
  "s_mov_b32 s85, 0x2d0800\n\t" \
  "s_load_dword s84, %[pw], s85\n\t" \
  "buffer_load_dwordx4 v6, %[sw], s69 offen offset:1024 lds\n\t" \
  "buffer_load_dwordx4 v7, %[sw], s69 offen offset:2048 lds\n\t" \
  "s_add_u32 m0, %[xb], 9216\n\t" \
  "s_nop 0\n\t" \
  "buffer_load_dwordx4 v2, %[sx], s70 offen offset:0 lds\n\t" \
  "buffer_load_dwordx4 v3, %[sx], s70 offen offset:1024 lds\n\t" \
  "s_mov_b64 exec, 0xffff\n\t" \
  "buffer_load_dwordx4 v4, %[sx], s70 offen offset:2048 lds\n\t" \
  "s_mov_b64 exec, -1\n\t" \
  "s_sleep 3\n\t" \
  "s_add_u32 s68, %[rw0], 1769472\n\t" \
  "s_add_u32 m0, %[wb], 12288\n\t" \
  "s_waitcnt vmcnt(21)\n\t" \
  "s_barrier\n\t" \
  "buffer_load_dwordx4 v5, %[sw], s68 offen offset:0 lds\n\t" \
  "s_mov_b32 s85, 0x2d1000\n\t" \
  "s_load_dword s84, %[pw], s85\n\t" \
  "buffer_load_dwordx4 v6, %[sw], s68 offen offset:1024 lds\n\t" \
  "s_mov_b32 s85, 0x300000\n\t" \
  "s_load_dword s84, %[pw], s85\n\t" \
  "buffer_load_dwordx4 v7, %[sw], s68 offen offset:2048 lds\n\t" \
  "s_mov_b32 s85, 0x300800\n\t" \
  "s_load_dword s84, %[pw], s85\n\t" \
  "s_sleep 3\n\t" \
  "s_add_u32 s68, %[rx1], 512\n\t" \
  "s_add_u32 s69, %[rw1], 1572864\n\t" \
  "s_add_u32 s70, %[rx1], 147968\n\t" \
  "s_add_u32 m0, %[xb], 18432\n\t" \
  "s_waitcnt vmcnt(15)\n\t" \
  "s_barrier\n\t" \
  "buffer_load_dwordx4 v0, %[sx], s68 offen offset:0 lds\n\t" \
  "s_mov_b32 s85, 0x301000\n\t" \
  "s_load_dword s84, %[pw], s85\n\t" \
  "buffer_load_dwordx4 v1, %[sx], s68 offen offset:1024 lds\n\t" \
  "s_mov_b32 s85, 0x330000\n\t" \
  "s_load_dword s84, %[pw], s85\n\t" \
  "s_mov_b64 exec, 0xffff\n\t" \
  "buffer_load_dwordx4 v4, %[sx], s68 offen offset:2048 lds\n\t" \
  "s_add_u32 m0, %[wb], 24576\n\t" \
  "s_mov_b64 exec, -1\n\t" \
  "buffer_load_dwordx4 v5, %[sw], s69 offen offset:0 lds\n\t" \
  "s_mov_b32 s85, 0x330800\n\t" \
  "s_load_dword s84, %[pw], s85\n\t" \
  "buffer_load_dwordx4 v6, %[sw], s69 offen offset:1024 lds\n\t" \
  "buffer_load_dwordx4 v7, %[sw], s69 offen offset:2048 lds\n\t" \
  "s_add_u32 m0, %[xb], 27648\n\t" \
  "s_nop 0\n\t" \
  "buffer_load_dwordx4 v2, %[sx], s70 offen offset:0 lds\n\t" \
  "buffer_load_dwordx4 v3, %[sx], s70 offen offset:1024 lds\n\t" \
  "s_mov_b64 exec, 0xffff\n\t" \
  "buffer_load_dwordx4 v4, %[sx], s70 offen offset:2048 lds\n\t" \
  "s_mov_b64 exec, -1\n\t" \
  "s_sleep 3\n\t" \
  "s_add_u32 s68, %[rw1], 1769472\n\t" \
  "s_add_u32 m0, %[wb], 36864\n\t" \
  "s_waitcnt vmcnt(21)\n\t" \
  "s_barrier\n\t" \
  "buffer_load_dwordx4 v5, %[sw], s68 offen offset:0 lds\n\t" \
  "s_mov_b32 s85, 0x331000\n\t" \
  "s_load_dword s84, %[pw], s85\n\t" \
  "buffer_load_dwordx4 v6, %[sw], s68 offen offset:1024 lds\n\t" \
  "s_mov_b32 s85, 0x360000\n\t" \
  "s_load_dword s84, %[pw], s85\n\t" \
  "buffer_load_dwordx4 v7, %[sw], s68 offen offset:2048 lds\n\t" \
  "s_mov_b32 s85, 0x360800\n\t" \
  "s_load_dword s84, %[pw], s85\n\t" \
  "s_sleep 3\n\t" \
  "s_add_u32 s68, %[rx2], 512\n\t" \
  "s_add_u32 s69, %[rw2], 1572864\n\t" \
  "s_add_u32 m0, %[xb], 36864\n\t" \
  "s_waitcnt vmcnt(15)\n\t" \
  "s_barrier\n\t" \
  "buffer_load_dwordx4 v0, %[sx], s68 offen offset:0 lds\n\t" \
  "s_mov_b32 s85, 0x361000\n\t" \
  "s_load_dword s84, %[pw], s85\n\t" \
  "buffer_load_dwordx4 v1, %[sx], s68 offen offset:1024 lds\n\t" \
  "s_mov_b32 s85, 0x390000\n\t" \
  "s_load_dword s84, %[pw], s85\n\t" \
  "s_mov_b64 exec, 0xffff\n\t" \
  "buffer_load_dwordx4 v4, %[sx], s68 offen offset:2048 lds\n\t" \
  "s_add_u32 m0, %[wb], 49152\n\t" \
  "s_mov_b64 exec, -1\n\t" \
  "buffer_load_dwordx4 v5, %[sw], s69 offen offset:0 lds\n\t" \
  "s_mov_b32 s85, 0x390800\n\t" \
  "s_load_dword s84, %[pw], s85\n\t" \
  "buffer_load_dwordx4 v6, %[sw], s69 offen offset:1024 lds\n\t" \
  "buffer_load_dwordx4 v7, %[sw], s69 offen offset:2048 lds\n\t" \
  "s_mov_b32 m0, %[tsk]\n\t" \
  "s_mov_b32 s85, 0x300000\n\t" \
  "s_mov_b64 exec, 63\n\t" \
  "buffer_load_dword v16, %[sh], s85 offen lds\n\t" \
  "s_mov_b64 exec, -1\n\t" \
  "s_sleep 3\n\t" \
  "s_add_u32 s68, %[rx2], 147968\n\t" \
  "s_add_u32 s69, %[rw2], 1769472\n\t" \
  "s_add_u32 m0, %[xb], 46080\n\t" \
  "s_waitcnt vmcnt(18)\n\t" \
  "s_barrier\n\t" \
  "buffer_load_dwordx4 v2, %[sx], s68 offen offset:0 lds\n\t" \
  "s_mov_b32 s85, 0x391000\n\t" \
  "s_load_dword s84, %[pw], s85\n\t" \
  "buffer_load_dwordx4 v3, %[sx], s68 offen offset:1024 lds\n\t" \
  "s_mov_b32 s85, 0x3c0000\n\t" \
  "s_load_dword s84, %[pw], s85\n\t" \
  "s_mov_b64 exec, 0xffff\n\t" \
  "buffer_load_dwordx4 v4, %[sx], s68 offen offset:2048 lds\n\t" \
  "s_add_u32 m0, %[wb], 61440\n\t" \
  "s_mov_b64 exec, -1\n\t" \
  "buffer_load_dwordx4 v5, %[sw], s69 offen offset:0 lds\n\t" \
  "s_mov_b32 s85, 0x3c0800\n\t" \
  "s_load_dword s84, %[pw], s85\n\t" \
  "buffer_load_dwordx4 v6, %[sw], s69 offen offset:1024 lds\n\t" \
  "buffer_load_dwordx4 v7, %[sw], s69 offen offset:2048 lds\n\t" \
  "s_sleep 3\n\t" \
  "s_add_u32 s68, %[rx3], 512\n\t" \
  "s_add_u32 s69, %[rw3], 1572864\n\t" \
  "s_add_u32 m0, %[xb], 0\n\t" \
  "s_waitcnt vmcnt(15)\n\t" \
  "s_barrier\n\t" \
  "buffer_load_dwordx4 v0, %[sx], s68 offen offset:0 lds\n\t" \
  "s_mov_b32 s85, 0x3c1000\n\t" \
  "s_load_dword s84, %[pw], s85\n\t" \
  "buffer_load_dwordx4 v1, %[sx], s68 offen offset:1024 lds\n\t" \
  "s_mov_b32 s85, 0x3f0000\n\t" \
  "s_load_dword s84, %[pw], s85\n\t" \
  "s_mov_b64 exec, 0xffff\n\t" \
  "buffer_load_dwordx4 v4, %[sx], s68 offen offset:2048 lds\n\t" \
  "s_add_u32 m0, %[wb], 0\n\t" \
  "s_mov_b64 exec, -1\n\t" \
  "buffer_load_dwordx4 v5, %[sw], s69 offen offset:0 lds\n\t" \
  "s_mov_b32 s85, 0x3f0800\n\t" \
  "s_load_dword s84, %[pw], s85\n\t" \
  "buffer_load_dwordx4 v6, %[sw], s69 offen offset:1024 lds\n\t" \
  "buffer_load_dwordx4 v7, %[sw], s69 offen offset:2048 lds\n\t" \
  "s_add_u32 s68, %[rx3], 147968\n\t" \
  "s_add_u32 s69, %[rw3], 1769472\n\t" \
  "s_add_u32 m0, %[xb], 9216\n\t" \
  "s_waitcnt vmcnt(18)\n\t" \
  "s_barrier\n\t" \
  "buffer_load_dwordx4 v2, %[sx], s68 offen offset:0 lds\n\t" \
  "s_mov_b32 s85, 0x3f1000\n\t" \
  "s_load_dword s84, %[pw], s85\n\t" \
  "buffer_load_dwordx4 v3, %[sx], s68 offen offset:1024 lds\n\t" \
  "s_mov_b32 s85, 0x420000\n\t" \
  "s_load_dword s84, %[pw], s85\n\t" \
  "s_mov_b64 exec, 0xffff\n\t" \
  "buffer_load_dwordx4 v4, %[sx], s68 offen offset:2048 lds\n\t" \
  "s_add_u32 m0, %[wb], 12288\n\t" \
  "s_mov_b64 exec, -1\n\t" \
  "buffer_load_dwordx4 v5, %[sw], s69 offen offset:0 lds\n\t" \
  "s_mov_b32 s85, 0x420800\n\t" \
  "s_load_dword s84, %[pw], s85\n\t" \
  "buffer_load_dwordx4 v6, %[sw], s69 offen offset:1024 lds\n\t" \
  "buffer_load_dwordx4 v7, %[sw], s69 offen offset:2048 lds\n\t" \
  "s_add_u32 s68, %[rx0], 1024\n\t" \
  "s_add_u32 s69, %[rw0], 3145728\n\t" \
  "s_add_u32 m0, %[xb], 18432\n\t" \
  "s_waitcnt vmcnt(15)\n\t" \
  "s_barrier\n\t" \
  "buffer_load_dwordx4 v0, %[sx], s68 offen offset:0 lds\n\t" \
  "s_mov_b32 s85, 0x421000\n\t" \
  "s_load_dword s84, %[pw], s85\n\t" \
  "buffer_load_dwordx4 v1, %[sx], s68 offen offset:1024 lds\n\t" \
  "s_mov_b32 s85, 0x450000\n\t" \
  "s_load_dword s84, %[pw], s85\n\t" \
  "s_mov_b64 exec, 0xffff\n\t" \
  "buffer_load_dwordx4 v4, %[sx], s68 offen offset:2048 lds\n\t" \
  "s_add_u32 m0, %[wb], 24576\n\t" \
  "s_mov_b64 exec, -1\n\t" \
  "buffer_load_dwordx4 v5, %[sw], s69 offen offset:0 lds\n\t" \
  "s_mov_b32 s85, 0x450800\n\t" \
  "s_load_dword s84, %[pw], s85\n\t" \
  "buffer_load_dwordx4 v6, %[sw], s69 offen offset:1024 lds\n\t" \
  "buffer_load_dwordx4 v7, %[sw], s69 offen offset:2048 lds\n\t" \
  "s_add_u32 s68, %[rx0], 148480\n\t" \
  "s_add_u32 s69, %[rw0], 3342336\n\t" \
  "s_add_u32 m0, %[xb], 27648\n\t" \
  "s_waitcnt vmcnt(18)\n\t" \
  "s_barrier\n\t" \
  "buffer_load_dwordx4 v2, %[sx], s68 offen offset:0 lds\n\t" \
  "s_mov_b32 s85, 0x451000\n\t" \
  "s_load_dword s84, %[pw], s85\n\t" \
  "buffer_load_dwordx4 v3, %[sx], s68 offen offset:1024 lds\n\t" \
  "s_mov_b32 s85, 0x480000\n\t" \
  "s_load_dword s84, %[pw], s85\n\t" \
  "s_mov_b64 exec, 0xffff\n\t" \
  "buffer_load_dwordx4 v4, %[sx], s68 offen offset:2048 lds\n\t" \
  "s_add_u32 m0, %[wb], 36864\n\t" \
  "s_mov_b64 exec, -1\n\t" \
  "buffer_load_dwordx4 v5, %[sw], s69 offen offset:0 lds\n\t" \
  "s_mov_b32 s85, 0x480800\n\t" \
  "s_load_dword s84, %[pw], s85\n\t" \
  "buffer_load_dwordx4 v6, %[sw], s69 offen offset:1024 lds\n\t" \
  "buffer_load_dwordx4 v7, %[sw], s69 offen offset:2048 lds\n\t" \
  "s_add_u32 s68, %[rx1], 1024\n\t" \
  "s_add_u32 s69, %[rw1], 3145728\n\t" \
  "s_add_u32 m0, %[xb], 36864\n\t" \
  "s_waitcnt vmcnt(15)\n\t" \
  "s_barrier\n\t" \
  "buffer_load_dwordx4 v0, %[sx], s68 offen offset:0 lds\n\t" \
  "s_mov_b32 s85, 0x481000\n\t" \
  "s_load_dword s84, %[pw], s85\n\t" \
  "buffer_load_dwordx4 v1, %[sx], s68 offen offset:1024 lds\n\t" \
  "s_mov_b32 s85, 0x4b0000\n\t" \
  "s_load_dword s84, %[pw], s85\n\t" \
  "s_mov_b64 exec, 0xffff\n\t" \
  "buffer_load_dwordx4 v4, %[sx], s68 offen offset:2048 lds\n\t" \
  "s_add_u32 m0, %[wb], 49152\n\t" \
  "s_mov_b64 exec, -1\n\t" \
  "buffer_load_dwordx4 v5, %[sw], s69 offen offset:0 lds\n\t" \
  "s_mov_b32 s85, 0x4b0800\n\t" \
  "s_load_dword s84, %[pw], s85\n\t" \
  "buffer_load_dwordx4 v6, %[sw], s69 offen offset:1024 lds\n\t" \
  "buffer_load_dwordx4 v7, %[sw], s69 offen offset:2048 lds\n\t" \
  "s_add_u32 s68, %[rx1], 148480\n\t" \
  "s_add_u32 s69, %[rw1], 3342336\n\t" \
  "s_add_u32 m0, %[xb], 46080\n\t" \
  "s_waitcnt vmcnt(18)\n\t" \
  "s_barrier\n\t" \
  "buffer_load_dwordx4 v2, %[sx], s68 offen offset:0 lds\n\t" \
  "s_mov_b32 s85, 0x4b1000\n\t" \
  "s_load_dword s84, %[pw], s85\n\t" \
  "buffer_load_dwordx4 v3, %[sx], s68 offen offset:1024 lds\n\t" \
  "s_mov_b32 s85, 0x4e0000\n\t" \
  "s_load_dword s84, %[pw], s85\n\t" \
  "s_mov_b64 exec, 0xffff\n\t" \
  "buffer_load_dwordx4 v4, %[sx], s68 offen offset:2048 lds\n\t" \
  "s_add_u32 m0, %[wb], 61440\n\t" \
  "s_mov_b64 exec, -1\n\t" \
  "buffer_load_dwordx4 v5, %[sw], s69 offen offset:0 lds\n\t" \
  "s_mov_b32 s85, 0x4e0800\n\t" \
  "s_load_dword s84, %[pw], s85\n\t" \
  "buffer_load_dwordx4 v6, %[sw], s69 offen offset:1024 lds\n\t" \
  "buffer_load_dwordx4 v7, %[sw], s69 offen offset:2048 lds\n\t" \
  "s_add_u32 s68, %[rx2], 1024\n\t" \
  "s_add_u32 s69, %[rw2], 3145728\n\t" \
  "s_add_u32 m0, %[xb], 0\n\t" \
  "s_waitcnt vmcnt(15)\n\t" \
  "s_barrier\n\t" \
  "buffer_load_dwordx4 v0, %[sx], s68 offen offset:0 lds\n\t" \
  "s_mov_b32 s85, 0x4e1000\n\t" \
  "s_load_dword s84, %[pw], s85\n\t" \
  "buffer_load_dwordx4 v1, %[sx], s68 offen offset:1024 lds\n\t" \
  "s_mov_b32 s85, 0x510000\n\t" \
  "s_load_dword s84, %[pw], s85\n\t" \
  "s_mov_b64 exec, 0xffff\n\t" \
  "buffer_load_dwordx4 v4, %[sx], s68 offen offset:2048 lds\n\t" \
  "s_add_u32 m0, %[wb], 0\n\t" \
  "s_mov_b64 exec, -1\n\t" \
  "buffer_load_dwordx4 v5, %[sw], s69 offen offset:0 lds\n\t" \
  "s_mov_b32 s85, 0x510800\n\t" \
  "s_load_dword s84, %[pw], s85\n\t" \
  "buffer_load_dwordx4 v6, %[sw], s69 offen offset:1024 lds\n\t" \
  "buffer_load_dwordx4 v7, %[sw], s69 offen offset:2048 lds\n\t" \
  "s_mov_b32 m0, %[tsk]\n\t" \
  "s_mov_b32 s85, 0x480000\n\t" \
  "s_mov_b64 exec, 63\n\t" \
  "buffer_load_dword v16, %[sh], s85 offen lds\n\t" \
  "s_mov_b64 exec, -1\n\t" \
  "s_add_u32 s68, %[rx2], 148480\n\t" \
  "s_add_u32 s69, %[rw2], 3342336\n\t" \
  "s_add_u32 m0, %[xb], 9216\n\t" \
  "s_waitcnt vmcnt(18)\n\t" \
  "s_barrier\n\t" \
  "buffer_load_dwordx4 v2, %[sx], s68 offen offset:0 lds\n\t" \
  "s_mov_b32 s85, 0x511000\n\t" \
  "s_load_dword s84, %[pw], s85\n\t" \
  "buffer_load_dwordx4 v3, %[sx], s68 offen offset:1024 lds\n\t" \
  "s_mov_b32 s85, 0x540000\n\t" \
  "s_load_dword s84, %[pw], s85\n\t" \
  "s_mov_b64 exec, 0xffff\n\t" \
  "buffer_load_dwordx4 v4, %[sx], s68 offen offset:2048 lds\n\t" \
  "s_add_u32 m0, %[wb], 12288\n\t" \
  "s_mov_b64 exec, -1\n\t" \
  "buffer_load_dwordx4 v5, %[sw], s69 offen offset:0 lds\n\t" \
  "s_mov_b32 s85, 0x540800\n\t" \
  "s_load_dword s84, %[pw], s85\n\t" \
  "buffer_load_dwordx4 v6, %[sw], s69 offen offset:1024 lds\n\t" \
  "buffer_load_dwordx4 v7, %[sw], s69 offen offset:2048 lds\n\t" \
  "s_mov_b32 m0, %[tsk]\n\t" \
  "s_mov_b64 exec, %[tm]\n\t" \
  "global_load_lds_dword v12, %[tp]\n\t" \
  "s_mov_b64 exec, -1\n\t" \
  "s_add_u32 s68, %[rx3], 1024\n\t" \
  "s_add_u32 s69, %[rw3], 3145728\n\t" \
  "s_add_u32 m0, %[xb], 18432\n\t" \
  "s_waitcnt vmcnt(15)\n\t" \
  "s_barrier\n\t" \
  "buffer_load_dwordx4 v0, %[sx], s68 offen offset:0 lds\n\t" \
  "s_mov_b32 s85, 0x541000\n\t" \
  "s_load_dword s84, %[pw], s85\n\t" \
  "buffer_load_dwordx4 v1, %[sx], s68 offen offset:1024 lds\n\t" \
  "s_mov_b32 s85, 0x570000\n\t" \
  "s_load_dword s84, %[pw], s85\n\t" \
  "s_mov_b64 exec, 0xffff\n\t" \
  "buffer_load_dwordx4 v4, %[sx], s68 offen offset:2048 lds\n\t" \
  "s_add_u32 m0, %[wb], 24576\n\t" \
  "s_mov_b64 exec, -1\n\t" \
  "buffer_load_dwordx4 v5, %[sw], s69 offen offset:0 lds\n\t" \
  "s_mov_b32 s85, 0x570800\n\t" \
  "s_load_dword s84, %[pw], s85\n\t" \
  "buffer_load_dwordx4 v6, %[sw], s69 offen offset:1024 lds\n\t" \
  "buffer_load_dwordx4 v7, %[sw], s69 offen offset:2048 lds\n\t" \
  "s_add_u32 s68, %[rx3], 148480\n\t" \
  "s_add_u32 s69, %[rw3], 3342336\n\t" \
  "s_add_u32 m0, %[xb], 27648\n\t" \
  "s_waitcnt vmcnt(18)\n\t" \
  "s_barrier\n\t" \
  "buffer_load_dwordx4 v2, %[sx], s68 offen offset:0 lds\n\t" \
  "s_mov_b32 s85, 0x571000\n\t" \
  "s_load_dword s84, %[pw], s85\n\t" \
  "buffer_load_dwordx4 v3, %[sx], s68 offen offset:1024 lds\n\t" \
  "s_mov_b32 s85, 0x5a0000\n\t" \
  "s_load_dword s84, %[pw], s85\n\t" \
  "s_mov_b64 exec, 0xffff\n\t" \
  "buffer_load_dwordx4 v4, %[sx], s68 offen offset:2048 lds\n\t" \
  "s_add_u32 m0, %[wb], 36864\n\t" \
  "s_mov_b64 exec, -1\n\t" \
  "buffer_load_dwordx4 v5, %[sw], s69 offen offset:0 lds\n\t" \
  "s_mov_b32 s85, 0x5a0800\n\t" \
  "s_load_dword s84, %[pw], s85\n\t" \
  "buffer_load_dwordx4 v6, %[sw], s69 offen offset:1024 lds\n\t" \
  "buffer_load_dwordx4 v7, %[sw], s69 offen offset:2048 lds\n\t" \
  "s_add_u32 s68, %[rx0], 1536\n\t" \
  "s_add_u32 s69, %[rw0], 4718592\n\t" \
  "s_add_u32 m0, %[xb], 36864\n\t" \
  "s_waitcnt vmcnt(15)\n\t" \
  "s_barrier\n\t" \
  "buffer_load_dwordx4 v0, %[sx], s68 offen offset:0 lds\n\t" \
  "s_mov_b32 s85, 0x5a1000\n\t" \
  "s_load_dword s84, %[pw], s85\n\t" \
  "buffer_load_dwordx4 v1, %[sx], s68 offen offset:1024 lds\n\t" \
  "s_mov_b32 s85, 0x5d0000\n\t" \
  "s_load_dword s84, %[pw], s85\n\t" \
  "s_mov_b64 exec, 0xffff\n\t" \
  "buffer_load_dwordx4 v4, %[sx], s68 offen offset:2048 lds\n\t" \
  "s_add_u32 m0, %[wb], 49152\n\t" \
  "s_mov_b64 exec, -1\n\t" \
  "buffer_load_dwordx4 v5, %[sw], s69 offen offset:0 lds\n\t" \
  "s_mov_b32 s85, 0x5d0800\n\t" \
  "s_load_dword s84, %[pw], s85\n\t" \
  "buffer_load_dwordx4 v6, %[sw], s69 offen offset:1024 lds\n\t" \
  "buffer_load_dwordx4 v7, %[sw], s69 offen offset:2048 lds\n\t" \
  "s_add_u32 s68, %[rx0], 148992\n\t" \
  "s_add_u32 s69, %[rw0], 4915200\n\t" \
  "s_add_u32 m0, %[xb], 46080\n\t" \
  "s_waitcnt vmcnt(18)\n\t" \
  "s_barrier\n\t" \
  "buffer_load_dwordx4 v2, %[sx], s68 offen offset:0 lds\n\t" \
  "s_mov_b32 s85, 0x5d1000\n\t" \
  "s_load_dword s84, %[pw], s85\n\t" \
  "buffer_load_dwordx4 v3, %[sx], s68 offen offset:1024 lds\n\t" \
  "s_load_dword s84, %[po], 0x0\n\t" \
  "s_mov_b64 exec, 0xffff\n\t" \
  "buffer_load_dwordx4 v4, %[sx], s68 offen offset:2048 lds\n\t" \
  "s_add_u32 m0, %[wb], 61440\n\t" \
  "s_mov_b64 exec, -1\n\t" \
  "buffer_load_dwordx4 v5, %[sw], s69 offen offset:0 lds\n\t" \
  "s_load_dword s84, %[po], 0x40\n\t" \
  "buffer_load_dwordx4 v6, %[sw], s69 offen offset:1024 lds\n\t" \
  "buffer_load_dwordx4 v7, %[sw], s69 offen offset:2048 lds\n\t" \
  "s_add_u32 s68, %[rx1], 1536\n\t" \
  "s_add_u32 s69, %[rw1], 4718592\n\t" \
  "s_add_u32 m0, %[xb], 0\n\t" \
  "s_waitcnt vmcnt(15)\n\t" \
  "s_barrier\n\t" \
  "buffer_load_dwordx4 v0, %[sx], s68 offen offset:0 lds\n\t" \
  "s_load_dword s84, %[po], 0x80\n\t" \
  "buffer_load_dwordx4 v1, %[sx], s68 offen offset:1024 lds\n\t" \
  "s_load_dword s84, %[po], 0xc0\n\t" \
  "s_mov_b64 exec, 0xffff\n\t" \
  "buffer_load_dwordx4 v4, %[sx], s68 offen offset:2048 lds\n\t" \
  "s_add_u32 m0, %[wb], 0\n\t" \
  "s_mov_b64 exec, -1\n\t" \
  "buffer_load_dwordx4 v5, %[sw], s69 offen offset:0 lds\n\t" \
  "s_load_dword s84, %[po], 0x100\n\t" \
  "buffer_load_dwordx4 v6, %[sw], s69 offen offset:1024 lds\n\t" \
  "buffer_load_dwordx4 v7, %[sw], s69 offen offset:2048 lds\n\t" \
  "s_add_u32 s68, %[rx1], 148992\n\t" \
  "s_add_u32 s69, %[rw1], 4915200\n\t" \
  "s_add_u32 m0, %[xb], 9216\n\t" \
  "s_waitcnt vmcnt(18)\n\t" \
  "s_barrier\n\t" \
  "buffer_load_dwordx4 v2, %[sx], s68 offen offset:0 lds\n\t" \
  "s_load_dword s84, %[po], 0x140\n\t" \
  "buffer_load_dwordx4 v3, %[sx], s68 offen offset:1024 lds\n\t" \
  "s_load_dword s84, %[po], 0x180\n\t" \
  "s_mov_b64 exec, 0xffff\n\t" \
  "buffer_load_dwordx4 v4, %[sx], s68 offen offset:2048 lds\n\t" \
  "s_add_u32 m0, %[wb], 12288\n\t" \
  "s_mov_b64 exec, -1\n\t" \
  "buffer_load_dwordx4 v5, %[sw], s69 offen offset:0 lds\n\t" \
  "s_load_dword s84, %[po], 0x1c0\n\t" \
  "buffer_load_dwordx4 v6, %[sw], s69 offen offset:1024 lds\n\t" \
  "buffer_load_dwordx4 v7, %[sw], s69 offen offset:2048 lds\n\t" \
  "s_add_u32 s68, %[rx2], 1536\n\t" \
  "s_add_u32 s69, %[rw2], 4718592\n\t" \
  "s_add_u32 m0, %[xb], 18432\n\t" \
  "s_waitcnt vmcnt(15)\n\t" \
  "s_barrier\n\t" \
  "buffer_load_dwordx4 v0, %[sx], s68 offen offset:0 lds\n\t" \
  "s_load_dword s84, %[po], 0x200\n\t" \
  "buffer_load_dwordx4 v1, %[sx], s68 offen offset:1024 lds\n\t" \
  "s_load_dword s84, %[po], 0x240\n\t" \
  "s_mov_b64 exec, 0xffff\n\t" \
  "buffer_load_dwordx4 v4, %[sx], s68 offen offset:2048 lds\n\t" \
  "s_add_u32 m0, %[wb], 24576\n\t" \
  "s_mov_b64 exec, -1\n\t" \
  "buffer_load_dwordx4 v5, %[sw], s69 offen offset:0 lds\n\t" \
  "s_load_dword s84, %[po], 0x280\n\t" \
  "buffer_load_dwordx4 v6, %[sw], s69 offen offset:1024 lds\n\t" \
  "buffer_load_dwordx4 v7, %[sw], s69 offen offset:2048 lds\n\t" \
  "s_add_u32 s68, %[rx2], 148992\n\t" \
  "s_add_u32 s69, %[rw2], 4915200\n\t" \
  "s_add_u32 m0, %[xb], 27648\n\t" \
  "s_waitcnt vmcnt(18)\n\t" \
  "s_barrier\n\t" \
  "buffer_load_dwordx4 v2, %[sx], s68 offen offset:0 lds\n\t" \
  "s_load_dword s84, %[po], 0x2c0\n\t" \
  "buffer_load_dwordx4 v3, %[sx], s68 offen offset:1024 lds\n\t" \
  "s_load_dword s84, %[po], 0x300\n\t" \
  "s_mov_b64 exec, 0xffff\n\t" \
  "buffer_load_dwordx4 v4, %[sx], s68 offen offset:2048 lds\n\t" \
  "s_add_u32 m0, %[wb], 36864\n\t" \
  "s_mov_b64 exec, -1\n\t" \
  "buffer_load_dwordx4 v5, %[sw], s69 offen offset:0 lds\n\t" \
  "s_load_dword s84, %[po], 0x340\n\t" \
  "buffer_load_dwordx4 v6, %[sw], s69 offen offset:1024 lds\n\t" \
  "buffer_load_dwordx4 v7, %[sw], s69 offen offset:2048 lds\n\t" \
  "s_add_u32 s68, %[rx3], 1536\n\t" \
  "s_add_u32 s69, %[rw3], 4718592\n\t" \
  "s_add_u32 m0, %[xb], 36864\n\t" \
  "s_waitcnt vmcnt(15)\n\t" \
  "s_barrier\n\t" \
  "buffer_load_dwordx4 v0, %[sx], s68 offen offset:0 lds\n\t" \
  "s_load_dword s84, %[po], 0x380\n\t" \
  "buffer_load_dwordx4 v1, %[sx], s68 offen offset:1024 lds\n\t" \
  "s_load_dword s84, %[po], 0x3c0\n\t" \
  "s_mov_b64 exec, 0xffff\n\t" \
  "buffer_load_dwordx4 v4, %[sx], s68 offen offset:2048 lds\n\t" \
  "s_add_u32 m0, %[wb], 49152\n\t" \
  "s_mov_b64 exec, -1\n\t" \
  "buffer_load_dwordx4 v5, %[sw], s69 offen offset:0 lds\n\t" \
  "s_load_dword s84, %[po], 0x400\n\t" \
  "buffer_load_dwordx4 v6, %[sw], s69 offen offset:1024 lds\n\t" \
  "buffer_load_dwordx4 v7, %[sw], s69 offen offset:2048 lds\n\t" \
  "s_add_u32 s68, %[rx3], 148992\n\t" \
  "s_add_u32 s69, %[rw3], 4915200\n\t" \
  "s_add_u32 m0, %[xb], 46080\n\t" \
  "s_waitcnt vmcnt(18)\n\t" \
  "s_barrier\n\t" \
  "buffer_load_dwordx4 v2, %[sx], s68 offen offset:0 lds\n\t" \
  "s_load_dword s84, %[po], 0x440\n\t" \
  "buffer_load_dwordx4 v3, %[sx], s68 offen offset:1024 lds\n\t" \
  "s_load_dword s84, %[po], 0x480\n\t" \
  "s_mov_b64 exec, 0xffff\n\t" \
  "buffer_load_dwordx4 v4, %[sx], s68 offen offset:2048 lds\n\t" \
  "s_add_u32 m0, %[wb], 61440\n\t" \
  "s_mov_b64 exec, -1\n\t" \
  "buffer_load_dwordx4 v5, %[sw], s69 offen offset:0 lds\n\t" \
  "s_load_dword s84, %[po], 0x4c0\n\t" \
  "buffer_load_dwordx4 v6, %[sw], s69 offen offset:1024 lds\n\t" \
  "buffer_load_dwordx4 v7, %[sw], s69 offen offset:2048 lds\n\t" \
  "v_readlane_b32 s68, v9, 0\n\t" \
  "v_readlane_b32 s69, v9, 1\n\t" \
  "v_readlane_b32 s70, v9, 2\n\t" \
  "v_readlane_b32 s71, v9, 18\n\t" \
  "v_readlane_b32 s72, v9, 19\n\t" \
  "s_add_u32 m0, %[ob], 0\n\t" \
  "s_waitcnt vmcnt(15)\n\t" \
  "s_barrier\n\t" \
  "s_nop 2\n\t" \
  "buffer_load_dwordx4 v8, %[so], s68 offen lds\n\t" \
  "s_load_dword s84, %[po], 0x500\n\t" \
  "s_add_u32 m0, %[ob], 4096\n\t" \
  "s_nop 0\n\t" \
  "buffer_load_dwordx4 v8, %[so], s69 offen lds\n\t" \
  "s_load_dword s84, %[po], 0x540\n\t" \
  "s_add_u32 m0, %[ob], 8192\n\t" \
  "s_nop 0\n\t" \
  "buffer_load_dwordx4 v8, %[so], s70 offen lds\n\t" \
  "s_load_dword s84, %[po], 0x580\n\t" \
  "s_add_u32 m0, %[ob], 73728\n\t" \
  "s_nop 0\n\t" \
  "buffer_load_dwordx4 v8, %[so], s71 offen lds\n\t" \
  "s_add_u32 m0, %[ob], 77824\n\t" \
  "s_nop 0\n\t" \
  "buffer_load_dwordx4 v8, %[so], s72 offen lds\n\t" \
  "v_readlane_b32 s68, v9, 3\n\t" \
  "v_readlane_b32 s69, v9, 4\n\t" \
  "v_readlane_b32 s70, v9, 5\n\t" \
  "v_readlane_b32 s71, v9, 20\n\t" \
  "v_readlane_b32 s72, v9, 21\n\t" \
  "s_add_u32 m0, %[ob], 12288\n\t" \
  "s_waitcnt vmcnt(17)\n\t" \
  "s_barrier\n\t" \
  "s_nop 2\n\t" \
  "buffer_load_dwordx4 v8, %[so], s68 offen lds\n\t" \
  "s_load_dword s84, %[po], 0x5c0\n\t" \
  "s_add_u32 m0, %[ob], 16384\n\t" \
  "s_nop 0\n\t" \
  "buffer_load_dwordx4 v8, %[so], s69 offen lds\n\t" \
  "s_load_dword s84, %[po], 0x600\n\t" \
  "s_add_u32 m0, %[ob], 20480\n\t" \
  "s_nop 0\n\t" \
  "buffer_load_dwordx4 v8, %[so], s70 offen lds\n\t" \
  "s_load_dword s84, %[po], 0x640\n\t" \
  "s_add_u32 m0, %[ob], 81920\n\t" \
  "s_nop 0\n\t" \
  "buffer_load_dwordx4 v8, %[so], s71 offen lds\n\t" \
  "s_add_u32 m0, %[ob], 86016\n\t" \
  "s_nop 0\n\t" \
  "buffer_load_dwordx4 v8, %[so], s72 offen lds\n\t" \
  "v_readlane_b32 s68, v9, 6\n\t" \
  "v_readlane_b32 s69, v9, 7\n\t" \
  "v_readlane_b32 s70, v9, 8\n\t" \
  "v_readlane_b32 s71, v9, 22\n\t" \
  "v_readlane_b32 s72, v9, 23\n\t" \
  "v_readlane_b32 s73, v9, 24\n\t" \
  "s_add_u32 m0, %[ob], 24576\n\t" \
  "s_waitcnt vmcnt(13)\n\t" \
  "s_barrier\n\t" \
  "s_nop 2\n\t" \
  "buffer_load_dwordx4 v8, %[so], s68 offen lds\n\t" \
  "s_load_dword s84, %[po], 0x680\n\t" \
  "s_add_u32 m0, %[ob], 28672\n\t" \
  "s_nop 0\n\t" \
  "buffer_load_dwordx4 v8, %[so], s69 offen lds\n\t" \
  "s_load_dword s84, %[po], 0x6c0\n\t" \
  "s_add_u32 m0, %[ob], 32768\n\t" \
  "s_nop 0\n\t" \
  "buffer_load_dwordx4 v8, %[so], s70 offen lds\n\t" \
  "s_load_dword s84, %[po], 0x700\n\t" \
  "s_add_u32 m0, %[ob], 90112\n\t" \
  "s_nop 0\n\t" \
  "buffer_load_dwordx4 v8, %[so], s71 offen lds\n\t" \
  "s_add_u32 m0, %[ob], 94208\n\t" \
  "s_nop 0\n\t" \
  "buffer_load_dwordx4 v8, %[so], s72 offen lds\n\t" \
  "s_add_u32 m0, %[ob], 98304\n\t" \
  "s_nop 0\n\t" \
  "buffer_load_dwordx4 v8, %[so], s73 offen lds\n\t" \
  "v_readlane_b32 s68, v9, 9\n\t" \
  "v_readlane_b32 s69, v9, 10\n\t" \
  "v_readlane_b32 s70, v9, 11\n\t" \
  "v_readlane_b32 s71, v9, 25\n\t" \
  "v_readlane_b32 s72, v9, 26\n\t" \
  "s_add_u32 m0, %[ob], 36864\n\t" \
  "s_waitcnt vmcnt(16)\n\t" \
  "s_barrier\n\t" \
  "s_nop 2\n\t" \
  "buffer_load_dwordx4 v8, %[so], s68 offen lds\n\t" \
  "s_load_dword s84, %[po], 0x740\n\t" \
  "s_add_u32 m0, %[ob], 40960\n\t" \
  "s_nop 0\n\t" \
  "buffer_load_dwordx4 v8, %[so], s69 offen lds\n\t" \
  "s_load_dword s84, %[po], 0x780\n\t" \
  "s_add_u32 m0, %[ob], 45056\n\t" \
  "s_nop 0\n\t" \
  "buffer_load_dwordx4 v8, %[so], s70 offen lds\n\t" \
  "s_load_dword s84, %[po], 0x7c0\n\t" \
  "s_add_u32 m0, %[ob], 102400\n\t" \
  "s_nop 0\n\t" \
  "buffer_load_dwordx4 v8, %[so], s71 offen lds\n\t" \
  "s_add_u32 m0, %[ob], 106496\n\t" \
  "s_nop 0\n\t" \
  "buffer_load_dwordx4 v8, %[so], s72 offen lds\n\t" \
  "v_readlane_b32 s68, v9, 12\n\t" \
  "v_readlane_b32 s69, v9, 13\n\t" \
  "v_readlane_b32 s70, v9, 14\n\t" \
  "v_readlane_b32 s71, v9, 27\n\t" \
  "v_readlane_b32 s72, v9, 28\n\t" \
  "v_readlane_b32 s73, v9, 29\n\t" \
  "s_add_u32 m0, %[ob], 49152\n\t" \
  "s_nop 3\n\t" \
  "s_barrier\n\t" \
  "s_nop 2\n\t" \
  "buffer_load_dwordx4 v8, %[so], s68 offen lds\n\t" \
  "s_add_u32 m0, %[ob], 53248\n\t" \
  "s_nop 0\n\t" \
  "buffer_load_dwordx4 v8, %[so], s69 offen lds\n\t" \
  "s_add_u32 m0, %[ob], 57344\n\t" \
  "s_nop 0\n\t" \
  "buffer_load_dwordx4 v8, %[so], s70 offen lds\n\t" \
  "s_add_u32 m0, %[ob], 110592\n\t" \
  "s_nop 0\n\t" \
  "buffer_load_dwordx4 v8, %[so], s71 offen lds\n\t" \
  "s_add_u32 m0, %[ob], 114688\n\t" \
  "s_nop 0\n\t" \
  "buffer_load_dwordx4 v8, %[so], s72 offen lds\n\t" \
  "s_add_u32 m0, %[ob], 118784\n\t" \
  "s_nop 0\n\t" \
  "buffer_load_dwordx4 v8, %[so], s73 offen lds\n\t" \
  "v_readlane_b32 s68, v9, 15\n\t" \
  "v_readlane_b32 s69, v9, 16\n\t" \
  "v_readlane_b32 s70, v9, 17\n\t" \
  "v_readlane_b32 s71, v9, 30\n\t" \
  "v_readlane_b32 s72, v9, 31\n\t" \
  "s_add_u32 m0, %[ob], 61440\n\t" \
  "LS_%=:\n\t" \
  "ds_read_b32 v11, v10\n\t" \
  "s_addk_i32 %[cn], 1\n\t" \
  "s_cmp_gt_u32 %[cn], 0x400000\n\t" \
  "s_cbranch_scc1 LX_%=\n\t" \
  "s_waitcnt lgkmcnt(0)\n\t" \
  "v_cmp_eq_u32 vcc, 0, v11\n\t" \
  "s_cbranch_vccz LX_%=\n\t" \
  "s_sleep 1\n\t" \
  "s_branch LS_%=\n\t" \
  "LX_%=:\n\t" \
  "s_nop 2\n\t" \
  "buffer_load_dwordx4 v8, %[so], s68 offen lds\n\t" \
  "s_add_u32 m0, %[ob], 65536\n\t" \
  "s_nop 0\n\t" \
  "buffer_load_dwordx4 v8, %[so], s69 offen lds\n\t" \
  "s_add_u32 m0, %[ob], 69632\n\t" \
  "s_nop 0\n\t" \
  "buffer_load_dwordx4 v8, %[so], s70 offen lds\n\t" \
  "s_add_u32 m0, %[ob], 122880\n\t" \
  "s_nop 0\n\t" \
  "buffer_load_dwordx4 v8, %[so], s71 offen lds\n\t" \
  "s_add_u32 m0, %[ob], 126976\n\t" \
  "s_nop 0\n\t" \
  "buffer_load_dwordx4 v8, %[so], s72 offen lds\n\t" \
  "s_waitcnt vmcnt(0) lgkmcnt(0)\n\t"
#define LD_HT 8
#define LD_CLOB "v11", "s68", "s69", "s70", "s71", "s72", "s73", "s74", "s75", "s76", "s77", "s78", "s79", "s80", "s81", "s82", "s83", "s84", "s85"

#define ENDW
#define TS(i)
#define TSS(i)
#define TOUT(n)

#define SPIN(cond,slp) do{ int sp_=0; while(cond){ __builtin_amdgcn_s_sleep(slp); if(++sp_>(1<<22)) break; } }while(0)
__device__ __forceinline__ unsigned rd1(const char* p){ return __builtin_amdgcn_readfirstlane(*(volatile LDS_AS unsigned*)(LDS_AS char*)p); }
__device__ __forceinline__ unsigned lds_inc(const char* p, int l){ unsigned r=0; if(l==0) r=__hip_atomic_fetch_add((LDS_AS unsigned*)(LDS_AS char*)p, 1u, __ATOMIC_RELAXED, __HIP_MEMORY_SCOPE_WORKGROUP); return __builtin_amdgcn_readfirstlane(r); }
__device__ __forceinline__ void lds_add(unsigned adr){ asm volatile("s_mov_b64 exec, 1\n\tds_add_u32 %0, %1\n\ts_mov_b64 exec, -1" :: "v"(adr), "v"(1u) : "memory"); }
__device__ __forceinline__ void bars(int n){
  asm volatile("BL_%=:\n\ts_barrier\n\ts_sub_u32 %0, %0, 1\n\ts_cmp_lg_u32 %0, 0\n\ts_cbranch_scc1 BL_%=" : "+s"(n) :: "memory", "scc");
}
#define barc(rem) barc_(rem)
#define CTRA
#define CTRP
__device__ __forceinline__ void barc_(unsigned& rem){
  asm volatile("s_cmp_eq_u32 %0, 0\n\ts_cbranch_scc1 BC_%=\n\ts_barrier\n\ts_sub_u32 %0, %0, 1\n\tBC_%=:" : "+s"(rem) :: "memory", "scc");
}
template<int SLP> __device__ __forceinline__ void wait_flag_b(const unsigned* p, unsigned voff, unsigned tag, unsigned& rem CTRA){
  int spins=0;
  while(true){
    unsigned v; asm volatile("global_load_dword %0, %1, %2 sc1" : "=v"(v) : "v"(voff), "s"(p) : "memory");
    barc(rem); if(PB2) barc(rem);
    asm volatile("s_waitcnt vmcnt(0)" : "+v"(v) :: "memory");
    if(__all(v==tag) || ++spins>(1<<21)) break;
    if(rem==0) __builtin_amdgcn_s_sleep(SLP);
  }
}
__device__ __forceinline__ void stout(void* sbase, unsigned voff, u32x4 v){
  st16cs(sbase,voff,v);
}

typedef unsigned u32x8 __attribute__((ext_vector_type(8)));
typedef unsigned u32x16 __attribute__((ext_vector_type(16)));
__device__ __forceinline__ char* ptr64(unsigned lo, unsigned hi){ return (char*)(((unsigned long long)hi<<32)|lo); }
__device__ __forceinline__ u32x4 srd(const char* p, unsigned bytes){ return u32x4{(unsigned)(size_t)p,(unsigned)((size_t)p>>32)&0xffffu,bytes,0x20000u}; }
constexpr size_t WS_O=0, WS_KV=(size_t)S*DM*2, WS_Y=(size_t)S*DM*4, WS_FL=(size_t)S*DM*12;

__global__ __launch_bounds__(768,3) void fused_kernel(const u16* x_, const u16* wqkv_, const u16* wo_, char* ws_, u16* out_,
    const u16* kin_, const u16* vin_, const u16* shk_, const u16* hk_, const u16* qin_, const u16* sD_)
{
  __shared__ __attribute__((aligned(128))) char smem[ALL_LDS];
  TS(0);
  const auto ka=__builtin_amdgcn_kernarg_segment_ptr();
  int w8;
  {
    unsigned t=threadIdx.x;
    asm volatile(
      "s_getpc_b64 s[72:73]\n\tv_readfirstlane_b32 %[w8], %[t]\n\ts_lshr_b32 s74, %[b], 3\n\ts_mul_i32 s74, s74, 12\n\ts_lshr_b32 %[w8], %[w8], 6\n\t"
      "s_add_u32 s74, s74, %[w8]\n\ts_min_u32 s74, s74, %[cl]\n\ts_lshl_b32 s74, s74, 7\n\ts_mov_b32 m0, %[sk]\n\tv_mov_b32 v1, s74\n\ts_mov_b64 exec, 1\n\tglobal_load_lds_dword v1, s[72:73]\n\ts_mov_b64 exec, -1\n\t"
      "s_bitcmp1_b32 %[w8], 2\n\ts_cbranch_scc1 EX_%=\n\t"
      "s_load_dwordx4 s[68:71], %[ka], 0x0\n\tv_and_b32 v1, 0xff, %[t]\n\ts_bitcmp1_b32 %[w8], 3\n\ts_cbranch_scc1 EC_%=\n\t"
      "s_mul_i32 s74, %[b], %[nw]\n\tv_cmp_gt_u32 vcc, %[nw], v1\n\tv_add_lshl_u32 v1, v1, s74, 7\n\ts_and_b64 exec, vcc, exec\n\ts_cbranch_execz EE_%=\n\ts_waitcnt lgkmcnt(0)\n\tglobal_load_lds_dword v1, s[70:71]\n\ts_branch EE_%=\n\t"
      "EC_%=:\n\tv_cmp_gt_u32 vcc, %[nx], v1\n\tv_and_b32 v2, 7, v1\n\tv_lshlrev_b32 v1, 4, v1\n\t"
      "s_lshl_b32 s74, %[b], 19\n\ts_lshl_b32 s73, %[b], 11\n\ts_or_b32 s74, s74, s73\n\ts_and_b32 s74, s74, 0x3fc000\n\t"
      "v_and_b32 v1, 0xf80, v1\n\tv_lshl_add_u32 v1, v2, 11, v1\n\tv_add_u32 v1, s74, v1\n\t"
      "s_and_b64 exec, vcc, exec\n\ts_cbranch_execz EE_%=\n\ts_waitcnt lgkmcnt(0)\n\tglobal_load_lds_dword v1, s[68:69]\n\t"
      "EE_%=:\n\ts_mov_b64 exec, -1\n\ts_waitcnt lgkmcnt(0)\n\tEX_%=:"
      : [w8]"=&s"(w8) : [ka]"s"(ka), [t]"v"(t), [b]"s"(blockIdx.x), [cl]"n"(CODEL-1), [sk]"s"(lds_addr(smem)+SINK+256), [nw]"s"(12*TPW), [nx]"s"(8*TPX)
      : "memory", "scc", "vcc", "v1", "v2", "s68", "s69", "s70", "s71", "s72", "s73", "s74");
  }
  #define ROLE u32x8 a0; int bid=blockIdx.x; asm volatile("s_load_dwordx8 %0, %2, 0x0" : "=&s"(a0), "+s"(bid) : "s"(ka)); \
    const int w=w8&3, xcd=bid&7, idx=bid>>3, rb=(xcd>>1)*4+(idx&3), hp=(xcd&1)*8+(idx>>2), s0=rb*128; (void)s0; (void)hp;
  #define LANEVARS int l; asm volatile("v_mbcnt_lo_u32_b32 %0, -1, 0\n\tv_mbcnt_hi_u32_b32 %0, -1, %0" : "=v"(l)); const int g=l>>4, fr=l&15, tq=(l&15)>>2, tp=l&3; (void)g; (void)fr; (void)tq; (void)tp;
  #define ARGS0 asm volatile("s_waitcnt lgkmcnt(0)" : "+s"(a0)); const char* x=ptr64(a0[0],a0[1]); const char* wqkv=ptr64(a0[2],a0[3]); const char* wo=ptr64(a0[4],a0[5]); char* ws=ptr64(a0[6],a0[7]); \
    unsigned* flags=(unsigned*)(ws+WS_FL); const int hot0=(3-(int)(a0[2]>>7))&7; (void)x; (void)wqkv; (void)wo; (void)flags; (void)hot0;
  #define ORA ((unsigned)(hp-hot0-1)&7u)
  #define ORD(c) ((((ORA==7u? ((c)==0u||(c)==15u? 7u : ((c)-1u)>>1) : ((c)<14u? (ORA+((c)>>1))%7u : 7u))+hot0+1)&7u)+8u*(((hp>>3)^(c))&1u))
  const unsigned sb0=__builtin_amdgcn_readfirstlane(lds_addr(smem));
  const char* syn=smem+SYN;

  if(__builtin_expect((w8>>2)==1,1)){
    ROLE
    LANEVARS
    __builtin_amdgcn_s_setprio(LPR);
    unsigned ltr=0, ltr2=0, ltr3=0; (void)ltr; (void)ltr2; (void)ltr3;
    const int xr=18*w+(l>>3);
    const unsigned ch=(unsigned)(((l&7)^(xr&7))*16);
    unsigned xo=(unsigned)(xr*2048)+ch;
    unsigned xoA=(s0==0 && xr<16)? 16*2048+ch : xo, xoB=((s0==0 && xr+8<16)? 8*2048+ch : xo)+15360;
    unsigned xo1=xo+15360, xo2=xo+30720;
    unsigned wof[3];
    #pragma unroll
    for(int i=0;i<3;i++){ const int P=3*w+i, wr=8*(P&3)+(l>>3); const int ci=((l&7)>>1)^wswz(wr); wof[i]=(unsigned)(wr*6144+(P>>2)*2048-1024*i+(MIX? (ci>>1)*128+(ci&1)*32 : ci*32)+(l&1)*16); }
    unsigned xl=sb0+X_OFF+w*2304, wl=sb0+w*3072, xd=(unsigned)(s0-16)*2048u, wd=hp*128, hq=hp&7, rq=rb&3;
    asm volatile("" : "+{v0}"(xoA), "+{v1}"(xoB), "+{v2}"(xo), "+{v3}"(xo1), "+{v4}"(xo2), "+{v5}"(wof[0]), "+{v6}"(wof[1]), "+{v7}"(wof[2]), "+s"(xl), "+s"(wl), "+s"(xd), "+s"(wd), "+s"(hq), "+s"(rq));
    TSS(2);
    ARGS0
    const unsigned wm=MIX? (wd&~255u)+((wd>>1)&64u) : wd;
    const u32x4 sx=srd(x+(int)xd,144u*2048u), sw=srd(wqkv+wm,6291456u-wm);
    const int jj=(ROT && (MIX? ((int)hq|1)==(hot0|1) : (int)hq==hot0))? rq : 0;
    TSS(3);
    asm volatile(LD_ASM1
      :: "{v0}"(xoA), "{v1}"(xoB), "{v2}"(xo), "{v3}"(xo1), "{v4}"(xo2), "{v5}"(wof[0]), "{v6}"(wof[1]), "{v7}"(wof[2]),
        [sx]"s"(sx), [sw]"s"(sw), [xb]"s"(xl), [wb]"s"(wl), [rx0]"s"(jj*128), [rw0]"s"(jj*393216)
      : "memory", "scc", "s68");
    {
      const unsigned wof2=(unsigned)(w*16384+(l>>3)*2048+((((l&7)>>1)^((l>>4)&3))*32)+(l&1)*16);
      const unsigned to=ORD((unsigned)(l>>1)&15u);
      const unsigned tab=MIX? (to&~1u)*131072u+(l&1)*131072u+(to&1u)*65536u : to*131072u+(l&1)*65536;
      unsigned long long tm; asm("s_cmp_eq_u32 %1, 0\n\ts_cselect_b64 %0, %2, 0" : "=s"(tm) : "s"(w), "s"(WTL==2? 0xffffffffull : 0ull) : "scc");
      unsigned cn=0;
      const unsigned hn=(unsigned)(w*(3*LD_HT/4)+l), hto=(hn/6u)*393216u+(hn%6u)*2048u;
      asm volatile(LD_ASM2
        : [cn]"+&s"(cn)
        : "{v0}"(xoA), "{v1}"(xoB), "{v2}"(xo), "{v3}"(xo1), "{v4}"(xo2), "{v5}"(wof[0]), "{v6}"(wof[1]), "{v7}"(wof[2]), "{v8}"(wof2), "{v9}"(tab),
          "{v10}"(sb0+SYN+32+(l&3)*4), "{v12}"((l&31)*2048),
          "{v16}"(hto), [sh]"s"(srd(wqkv+((xcd&1)*8+hot0)*128+idx*12288,6291456u-2048u-idx*12288u+128u)),
          [sx]"s"(sx), [sw]"s"(sw), [so]"s"(srd(wo+wd,2097152u-wd)), [xb]"s"(xl), [wb]"s"(wl), [ob]"s"(sb0+w*1024),
          [rx0]"s"(jj*128), [rx1]"s"(((jj+1)&3)*128), [rx2]"s"(((jj+2)&3)*128), [rx3]"s"(((jj+3)&3)*128),
          [rw0]"s"(jj*393216), [rw1]"s"(((jj+1)&3)*393216), [rw2]"s"(((jj+2)&3)*393216), [rw3]"s"(((jj+3)&3)*393216),
          [pw]"s"(wqkv+idx*6144+(xcd&1)*1024+(xcd>>1)*256+w*64), [px]"s"(x+(xcd*256+idx*8+w*2)*2048), [po]"s"(wo+(bid*4+w)*2048),
          [tsk]"s"(sb0+SINK+256), [tm]"s"(tm), [tp]"s"(wo+idx*65536+(xcd&1)*1024+((3-(int)(a0[4]>>7))&7)*128)
        : "memory", "scc", "vcc", LD_CLOB);
    }
    const unsigned tag=rd1(syn+64)+1u;
    lds_add(sb0+SYN+12);
    TS(2);
    if(w==3){
      const unsigned* fp=flags+F_O+(rb*16+hp)*16; const unsigned fo=ORD((unsigned)l&15u)*4u;
      const unsigned madr=sb0+(l==0? SYN+20 : SINK+l*4);
      SPIN(rd1(syn+12)<4u || rd1(syn+16)<4u,1);
      PUB(madr,1u);
      unsigned rdy=1; int spins=0;
      while(rdy!=0xFFFFu){
        unsigned v;
        asm volatile("s_mov_b64 exec, 0xffff\n\tglobal_atomic_or %0, %1, %3, %2 sc0 sc1\n\ts_mov_b64 exec, -1\n\ts_waitcnt vmcnt(0)" : "=&v"(v) : "v"(fo), "s"(fp), "v"(0u) : "memory");
        unsigned r=((unsigned)__ballot(v==tag)&0xFFFFu)|1u;
        if(++spins>(1<<21)) r=0xFFFFu;
        if(r!=rdy){ PUB(madr,(unsigned)__builtin_ctz(~r)); rdy=r; }
        else __builtin_amdgcn_s_sleep(OSLP);
      }
      TS(3);
      if(bid==0){
        int sp=0;
        while(true){
          u32x4 v; asm volatile("global_load_dwordx4 %0, %1, %2 sc1\n\ts_waitcnt vmcnt(0)" : "=&v"(v) : "v"(l*16), "s"(flags+F_Y) : "memory");
          if(__all(v[0]==tag && v[1]==tag && v[2]==tag && v[3]==tag) || ++sp>(1<<20)) break;
          __builtin_amdgcn_s_sleep(12);
        }
        if(l==0) stflag(flags, tag);
      }
    } else { TS(3); }
    TOUT(4);
    return;
  }

  if(w8<4){
    ROLE
    u32x2 a1; asm volatile("s_load_dwordx2 %0, %1, 0x20" : "=&s"(a1) : "s"(ka));
    __builtin_amdgcn_s_setprio(GPR);
    const int h=w&1, r=w>>1;
    f32x16 e0, e1;
    LANEVARS
    TSS(3);
    ARGS0
    asm volatile("" : "+s"(a1));
    if(PGT&1){ if(w==1 && l<7) touch(ws, l<6? l*2097152+PGO : (unsigned)WS_FL+2048, sb0+SINK+512); }
    if(PGT&2){ if(w==2 && l<4) touch(ptr64(a1[0],a1[1]), l*2097152+PGO, sb0+SINK+512); }
    if(PGT&4){ if(w==3 && l<1) touch(wo, PGO, sb0+SINK+512); if(w==0 && l<2) touch(wqkv, l*2097152+2097152, sb0+SINK+512); }
    TSS(4);
    {
      unsigned lx0=sb0+X_OFF+fr*128+((g^(fr&7))*16), lx1=sb0+X_OFF+fr*128+(((4+g)^(fr&7))*16);
      const int sw=wswz(8*g+tq);
      unsigned lw0=sb0+(8*g+tq)*128+(unsigned)((((2*h)^sw)*32)+8*tp), lw1=sb0+(8*g+tq)*128+(unsigned)((((2*h+1)^sw)*32)+8*tp);
      char* ot=ws+WS_O+(rb*16+hp)*16384+r*8192+h*2048;
      unsigned it; unsigned long long rm; asm("s_cmp_lg_u32 %1, 0\n\ts_cselect_b64 %0, -1, 0" : "=s"(rm) : "s"(r) : "scc");
      asm volatile(N2_ASM
        : [it]"=&s"(it), "+{v56}"(lx0), "+{v57}"(lx1), "+{v58}"(lw0), "+{v59}"(lw1)
        : "{v62}"(l*16), "{v63}"(sb0+EX_OFF+h*3072+l*16), [r]"s"(r), [ot]"s"(ot), [ou]"s"(ot+4096), [fa]"s"(sb0+SYN+32+w*4), [fp]"s"(sb0+SYN+32+(w^2)*4),
          [vo]"s"(2048-1024*r), [rm]"s"(rm), [z]"s"(rb|r)
        : "memory", "scc", "vcc", N2_CLOB);
    }
    TS(3);
    unsigned tb;
    {
      LANEVARS
      const unsigned tag=rd1(syn+64)+1u;
      const unsigned ra=sb0+(4*g+tq)*128+8*tp; const int sw=(2*g+(tq>>1))&3;
      unsigned b0=ra+((0^sw)*32), b1=ra+((1^sw)*32), b2=ra+((2^sw)*32), b3=ra+((3^sw)*32), vo=(unsigned)(w*4096+l*16);
      tb=ORD((unsigned)l&15u)*16384u;
      asm volatile("" : "+{v160}"(b0), "+{v161}"(b1), "+{v162}"(b2), "+{v163}"(b3), "+{v164}"(vo), "+{v167}"(tb));
      WAITV(0);
      if(lds_inc(syn+16,l)==3u){ if(l<16) stflag(flags+F_O+(rb*16+l)*16+hp, tag); }
      TS(4);
      __builtin_amdgcn_s_setprio(G2P);
      unsigned c=0, iss=0, th, q0, q1, cn=0;
      asm volatile(G3_ASM
        : "={v[128:143]}"(e0), "={v[144:159]}"(e1), [c]"+s"(c), [iss]"+s"(iss), [th]"=&s"(th), [t0]"=&s"(q0), [t1]"=&s"(q1), [cn]"+&s"(cn),
          "+{v160}"(b0), "+{v161}"(b1), "+{v162}"(b2), "+{v163}"(b3), "+{v167}"(tb)
        : "{v164}"(vo), "{v165}"(sb0+SYN+20), [go]"s"(60+w*4), [ob]"s"(srd(ws+WS_O+rb*262144,262144u))
        : "memory", "scc", "vcc", G3_CLOB);
    }
    {
      LANEVARS
      char* sg=smem+EX_OFF+w*2048;
      char* op=ptr64(a1[0],a1[1])+(s0+w*32)*4096+2048+hp*128; const unsigned oo=(unsigned)((l>>3)*4096+(l&7)*16);
      u32x4 ov[4];
      #pragma unroll
      for(int t=0;t<2;t++){
        #pragma unroll
        for(int j=0;j<4;j++){
          #define E2(i) (t? e1[(i)&15] : e0[(i)&15])
          *(volatile LDS_AS u32x2*)(LDS_AS char*)(sg+fr*128+(((2*j+(g>>1))^(fr&7))*16)+(g&1)*8)=u32x2{pack2(E2(4*j),E2(4*j+1)),pack2(E2(4*j+2),E2(4*j+3))};
        }
        #pragma unroll
        for(int i=0;i<2;i++) ov[2*t+i]=*(volatile LDS_AS u32x4*)(LDS_AS char*)(sg+(i*8+(l>>3))*128+(((l&7)^(l>>3))*16));
      }
      #pragma unroll
      for(int i=0;i<4;i++) stout(op+i*32768, oo, ov[i]);
      ENDW TS(6); TOUT(7);
    }
    return;
  }

  {
    ROLE
    u32x8 a1; u32x4 a2; u32x2 a3;
    asm volatile("s_load_dwordx8 %0, %3, 0x20\n\ts_load_dwordx4 %1, %3, 0x40\n\ts_load_dwordx2 %2, %3, 0x50" : "=&s"(a1), "=&s"(a2), "=&s"(a3) : "s"(ka));
    char* cr=smem+C_OFF+2048+w*4096;
    const unsigned crs=sb0+C_OFF+2048+w*4096;
    const int sblk=bid>>3, td0=(bid&7)*128+(w&1)*64;
    const int vb=(LOCA&1)? xcd*32+(idx&16)+(idx&1)*8+((idx>>1)&7) : bid, fb=(LOCA&2)? (idx&15)*16+xcd*2+(idx>>4) : bid;
    const int d=(vb>>4)*64+(((vb>>3)&1)*4+w)*8+(vb&7);
    const int kv0=KV0+(sblk&KVS);
    static_assert(KV0+KVS+PP_NB<=33, "barrier budget");
    unsigned tagv, ctr; f32x8 RB;
    const int fd0=(fb&15)*64, fs0=(fb>>4)*128+w*32;
    u32x4 qv[4];
    f32x16 A0,A1,A2,A3;
    const int rw=sblk*64+(w>>1)*32, j=rw>>5;
    unsigned k0,k1,k2,ko,ta,se,so,ha,l16,rb0[2];
    {
      LANEVARS
      if(w==0 && l<25 && l!=16) ((unsigned*)(smem+SYN))[l]=0u;
      if(l<32) *(u32x4*)(smem+C_OFF+w*512+l*16)=u32x4{0,0,0,0};
      asm volatile("" ::: "memory");
      __builtin_amdgcn_s_setprio(CPR);
      const int dg=l&7, rg=l>>3;
      ko=(unsigned)(rg*8192+dg*16);
      const bool top=(rw==0 && rg==0);
      k0=top? ko+6144:ko; k1=top? ko+6144:ko+2048; k2=top? ko+6144:ko+4096;
      ta=sb0+EX_OFF+w*1024+(w&1)*512+dg*64; se=(l&8)? 0x03020706u : 0x05040100u; ha=crs+(32*g-4*tq+4*tp)*2; l16=l*16;
      so=(unsigned)(rg*32768+((j>>2)&3)*128+((dg&4)|((dg+j)&3))*16);
      #pragma unroll
      for(int k=0;k<2;k++){ const int q=fr-1-2*g+k; rb0[k]=sb0+C_OFF+(4+(q>>1))*256+((q>>3)&3)*64+((w+(q>>1))&3)*16+(q&1)*8; }
      asm volatile("" : "+{v152}"(k0), "+{v153}"(k1), "+{v154}"(k2), "+{v155}"(ko), "+{v156}"(ta), "+{v157}"(se), "+{v159}"(so), "+{v163}"(l16), "+{v165}"(ha), "+{v166}"(rb0[0]), "+{v167}"(rb0[1]));
    }
    TSS(3);
    ARGS0
    asm volatile("" : "+s"(a1), "+s"(a2), "+s"(a3));
    char* out=ptr64(a1[0],a1[1]); const char* qp=ptr64(a2[2],a2[3])+(fs0*DM+fd0)*2;
    {
      const char* kb=ptr64(a1[2],a1[3])+(rw*DM+td0)*2; const char* vb=ptr64(a1[4],a1[5])+(rw*DM+td0)*2;
      unsigned long long zm; asm("s_cmp_eq_u32 %1, 0\n\ts_cselect_b64 %0, 0xff, 0" : "=s"(zm) : "s"(rw) : "scc");
      unsigned t0;
      if(PGT&8){ const char* tp= w==0? kb : w==1? vb : w==2? qp : ptr64(a2[0],a2[1])+d*4096;
        asm volatile("s_mov_b32 m0, %1\n\ts_mov_b64 exec, 1\n\tglobal_load_lds_dword v163, %0\n\ts_mov_b64 exec, -1" :: "s"(tp), "s"(sb0+SINK+512), "{v163}"(l16) : "memory"); }
      TSS(5);
      asm volatile(PP_ASM
        : [t0]"=&s"(t0), "=&{v164}"(ctr), "+{v159}"(so), "=&{v160}"(tagv), "=&{v[24:31]}"(RB), "=&{v[44:47]}"(qv[0]), "=&{v[48:51]}"(qv[1]), "=&{v[52:55]}"(qv[2]), "=&{v[56:59]}"(qv[3]),
          "=&{v[64:79]}"(A0), "=&{v[80:95]}"(A1), "=&{v[96:111]}"(A2), "=&{v[112:127]}"(A3)
        : "{v152}"(k0), "{v153}"(k1), "{v154}"(k2), "{v155}"(ko), "{v156}"(ta), "{v157}"(se), "{v166}"(rb0[0]), "{v167}"(rb0[1]), "{v165}"(ha), "{v163}"(l16),
          [qb0]"s"(qp), [qb2]"s"(qp+4096), [mk]"s"(0x180u), [em]"s"(1ull|(512ull<<w)), [hd]"s"(crs),
          [kb0]"s"(kb-6144), [kb3]"s"(kb), [kb5]"s"(kb+4096), [vb0]"s"(vb), [vb2]"s"(vb+4096), [sb]"s"(ws+WS_KV+td0*4096+j*512),
          [n0]"s"(kv0), [ff]"s"(flags+F_PP+(td0>>6)*64+j), [zm]"s"(zm), [shd]"s"(sb0+EX_OFF+w*1024), [tgd]"s"(sb0+SYN+64), [shp]"s"(ptr64(a1[6],a1[7])+(bid&7)*1024), [fl]"s"(flags),
          [hp]"s"(ptr64(a2[0],a2[1])+d*4096), [dp]"s"(ptr64(a3[0],a3[1])+d*2)
        : "memory", "scc", PP_CLOB);
    }
    LANEVARS
    const unsigned tag=__builtin_amdgcn_readfirstlane(tagv)+1u;
    unsigned rem=33-PP_NB-kv0;
    barc(rem);
    const unsigned* pf=flags+F_PP+(d>>6)*64;
    const char* kvp=ws+WS_KV+((vb>>4)*8+(vb&7))*32768+((vb>>3)&1)*64+w*8192;
    const unsigned kvo=(unsigned)((l>>2)*128+(l&3)*16);
    int bidx=((l>=16)? l-16 : (l==0? 63 : l+47))*4;
    asm volatile("" : "+{v32}"(bidx));
    __builtin_amdgcn_s_setprio(3);
    while(rem>PREM) barc(rem);
    wait_flag_b<8>(pf, l*4, tag, rem CTRP);
    #pragma unroll
    for(int i=0;i<4;i++){ if(i%KTH==0 && i) barc(rem); gldsc(kvp+i*2048, kvo, crs+i*1024); }
    barc(rem); barc(rem);
    WAITV(0);
    asm volatile("s_mov_b32 m0, %2\n\ts_mov_b64 exec, 1\n\tglobal_load_lds_dword %0, %1 sc1\n\ts_mov_b64 exec, -1" :: "v"(0u), "s"(pf), "s"(sb0+SYN+100+w*4) : "memory");
    while(rem) barc(rem);
    { int sp=0; while(!__all(*(volatile LDS_AS unsigned*)(LDS_AS char*)(syn+100+(l&3)*4)==tag) && ++sp<(1<<22)) __builtin_amdgcn_s_sleep(1); }
    __builtin_amdgcn_s_setprio(CVP);
    TS(3);
    f32x16 ac0, ac1;
    {
      unsigned p0,p1,p2,p3,p4,p5,p6;
      asm volatile("s_nop 1\n\t" CV_ASM
        : "=&{v[128:143]}"(ac0), "=&{v[144:159]}"(ac1), [p0]"=&s"(p0), [p1]"=&s"(p1), [p2]"=&s"(p2), [p3]"=&s"(p3), [p4]"=&s"(p4), [p5]"=&s"(p5), [p6]"=&s"(p6)
          , "+{v[44:47]}"(qv[0]), "+{v[48:51]}"(qv[1]), "+{v[52:55]}"(qv[2]), "+{v[56:59]}"(qv[3])
        : "{v[64:79]}"(A0), "{v[80:95]}"(A1), "{v[96:111]}"(A2), "{v[112:127]}"(A3),
          "{v[24:31]}"(RB), "{v32}"(bidx), "{v33}"(sb0+SYN+80+w*4)
          , "{v43}"((l>>3)*8192+(l&7)*16), [q0]"s"(qp), [q2]"s"(qp+4096)
        : "memory", "scc", CV_CLOB);
    }
    TS(4);
    __builtin_amdgcn_s_setprio(3);
    {
      const unsigned yo=(unsigned)((256*(g&1)+16*fr+4*(g&2))*2);
      #pragma unroll
      for(int J=0;J<8;J+=2){
        #define YA(i) ((i)<16? ac0[(i)&15] : ac1[(i)&15])
        const u32x2 a={pack2(YA(4*J),YA(4*J+1)),pack2(YA(4*J+2),YA(4*J+3))}, b={pack2(YA(4*J+4),YA(4*J+5)),pack2(YA(4*J+6),YA(4*J+7))};
        st16cs(ws+WS_Y+d*4096+512*J, yo, widen(a,b));
      }
    }
    WAITV(0);
    if(lds_inc(syn+24,l)==3u){ if(l<16) stflag(flags+F_Y+(l*16+(vb>>4))*16+(vb&15), tag); }
    TS(5);
    if(w==0){
      SPIN(rd1(syn+96)==0u,2);
      wait_flag<YSLP>(flags+F_Y+fb*16+(l&15), tag);
      SPIN(rd1(syn+24)<4u,1);
      PUB(sb0+(l==0? SYN+28 : SINK+l*4), 1u);
    } else SPIN(rd1(syn+28)==0u,4);
    #pragma unroll
    for(int i=0;i<4;i++) gldsc(ws+WS_Y+(fd0+i*16)*4096+fs0*2, (l>>2)*4096+(l&3)*16, crs+i*1024);
    WAITV(0);
    {
      u32x4 y2[4];
      #pragma unroll
      for(int i=0;i<4;i++){ const char* yp=cr+((i>>1)*32+g*8+tq)*64+(i&1)*32+tp*8; y2[i]=__builtin_bit_cast(u32x4,cat44(trread(yp),trread(yp+256))); }
      asm volatile("" : "+v"(y2[0]), "+v"(y2[1]), "+v"(y2[2]), "+v"(y2[3]) :: "memory");
      #pragma unroll
      for(int i=0;i<4;i++) *(u32x4*)(cr+((i&1)*16+fr)*128+((((i>>1)*4+g)^(fr&7))*16))=y2[i];
      asm volatile("" ::: "memory");
      char* op=out+fs0*4096+fd0*2; const unsigned oo=(unsigned)((l>>3)*16384+(l&7)*16);
      #pragma unroll
      for(int i=0;i<4;i++){
        const int row=4*(l>>3)+i;
        const u32x4 y4=*(const u32x4*)(cr+row*128+(((l&7)^(row&7))*16)), q4=qv[i]; u32x4 ov;
        #pragma unroll
        for(int e=0;e<4;e++) ov[e]=pack2(bflo(q4[e])*bflo(y4[e]), bfhi(q4[e])*bfhi(y4[e]));
        stout(op+i*4096, oo, ov);
      }
    }
    ENDW TS(6); TOUT(7);
  }
}

extern "C" void kernel_launch(void* const* d_in, const int* in_sizes, int n_in, void* d_out, int out_size, void* d_ws, size_t ws_size, hipStream_t stream){
  const u16* q=(const u16*)d_in[0]; const u16* k=(const u16*)d_in[1]; const u16* v=(const u16*)d_in[2];
  const u16* shk=(const u16*)d_in[3]; const u16* hk=(const u16*)d_in[4]; const u16* sD=(const u16*)d_in[5];
  const u16* x=(const u16*)d_in[6]; const u16* wqkv=(const u16*)d_in[7]; const u16* wo=(const u16*)d_in[8];
  fused_kernel<<<256,768,0,stream>>>(x,wqkv,wo,(char*)d_ws,(u16*)d_out,k,v,shk,hk,q,sD);
}
```

```cpp
#include <hip/hip_runtime.h>
#include <hip/hip_bf16.h>
#include <stdint.h>

typedef unsigned short u16;
typedef short bf16x8 __attribute__((ext_vector_type(8)));
typedef short s16x4 __attribute__((ext_vector_type(4)));
typedef float f32x2 __attribute__((ext_vector_type(2)));
typedef float f32x4 __attribute__((ext_vector_type(4)));
typedef float f32x8 __attribute__((ext_vector_type(8)));
typedef float f32x16 __attribute__((ext_vector_type(16)));
typedef unsigned u32x4 __attribute__((ext_vector_type(4)));
typedef unsigned u32x2 __attribute__((ext_vector_type(2)));
typedef __bf16 bf16x2_t __attribute__((ext_vector_type(2)));
#define LDS_AS __attribute__((address_space(3)))

__device__ __forceinline__ float bflo(unsigned x){ return __uint_as_float(x<<16); }
__device__ __forceinline__ float bfhi(unsigned x){ return __uint_as_float(x&0xffff0000u); }
__device__ __forceinline__ unsigned pack2(float lo, float hi){ return __builtin_bit_cast(unsigned, __builtin_convertvector(f32x2{lo,hi}, bf16x2_t)); }
__device__ __forceinline__ float mul1(float a, float b){ float r; asm("v_mul_f32 %0, %1, %2" : "=v"(r) : "v"(a), "v"(b)); return r; }
__device__ __forceinline__ float fma1(float a, float b, float c){ float r; asm("v_fma_f32 %0, %1, %2, %3" : "=v"(r) : "v"(a), "v"(b), "v"(c)); return r; }

__device__ __forceinline__ void glds(const void* sbase, unsigned voff, unsigned ldsaddr){
  asm volatile("s_mov_b32 m0, %2\n\ts_nop 0\n\tglobal_load_lds_dwordx4 %0, %1" :: "v"(voff), "s"(sbase), "s"(ldsaddr) : "memory");
}
__device__ __forceinline__ void gldsc(const void* sbase, unsigned voff, unsigned ldsaddr){
  asm volatile("s_mov_b32 m0, %2\n\ts_nop 0\n\tglobal_load_lds_dwordx4 %0, %1 sc1" :: "v"(voff), "s"(sbase), "s"(ldsaddr) : "memory");
}
__device__ __forceinline__ void touch(const void* sbase, unsigned voff, unsigned ldsaddr){
  asm volatile("s_mov_b32 m0, %2\n\ts_nop 0\n\tglobal_load_lds_dword %0, %1" :: "v"(voff), "s"(sbase), "s"(ldsaddr) : "memory");
}
__device__ __forceinline__ void st16c(void* p, u32x4 v){ asm volatile("global_store_dwordx4 %0, %1, off sc1\n\ts_nop 1" :: "v"(p), "v"(v) : "memory"); }
__device__ __forceinline__ void st16cs(void* sbase, unsigned voff, u32x4 v){ asm volatile("global_store_dwordx4 %0, %1, %2 sc1\n\ts_nop 1" :: "v"(voff), "v"(v), "s"(sbase) : "memory"); }
#define GLB_AS __attribute__((address_space(1)))
__device__ __forceinline__ unsigned ldflag(const unsigned* p){ return __hip_atomic_load((const GLB_AS unsigned*)p, __ATOMIC_RELAXED, __HIP_MEMORY_SCOPE_AGENT); }
__device__ __forceinline__ void stflag(unsigned* p, unsigned v){ __hip_atomic_store((GLB_AS unsigned*)p, v, __ATOMIC_RELAXED, __HIP_MEMORY_SCOPE_AGENT); }
template<int SLP> __device__ __forceinline__ void wait_flag(const unsigned* p, unsigned tag){
  int spins=0;
  while(true){ unsigned v=ldflag(p); if(__all(v==tag)) break; __builtin_amdgcn_s_sleep(SLP); if(++spins>(1<<21)) break; }
}
__device__ __forceinline__ unsigned lds_addr(const void* p){ return (unsigned)(size_t)(const LDS_AS void*)p; }
#define WAITV(n) asm volatile("s_waitcnt vmcnt(" #n ")":::"memory")
#define WAITL() asm volatile("s_waitcnt lgkmcnt(0)":::"memory")
#define SBAR() asm volatile("s_barrier":::"memory")
#define PUB(addr,val) asm volatile("ds_write_b32 %0, %1" :: "v"(addr), "v"(val) : "memory")
__device__ __forceinline__ s16x4 trread(const char* p){ return __builtin_amdgcn_ds_read_tr16_b64_v4i16((LDS_AS s16x4*)p); }
__device__ __forceinline__ bf16x8 cat44(s16x4 a, s16x4 b){ return __builtin_shufflevector(a,b,0,1,2,3,4,5,6,7); }
__device__ __forceinline__ int wswz(int row){ return ((row>>1)&1) | (((row>>3)&1)<<1); }
__device__ __forceinline__ u32x4 widen(u32x2 a, u32x2 b){
  auto r0=__builtin_amdgcn_permlane16_swap(a[0],b[0],false,false);
  auto r1=__builtin_amdgcn_permlane16_swap(a[1],b[1],false,false);
  return u32x4{r0[0],r1[0],r0[1],r1[1]};
}

constexpr int S=2048, DM=1024;
constexpr int WS=12288, XS=18432, X_OFF=6*WS;
constexpr int CREG=4608, C_OFF=131072;
constexpr int EX_OFF=C_OFF+4*CREG;
constexpr int SYN=EX_OFF+8192;
constexpr int SINK=SYN+256;
constexpr int ALL_LDS=SINK+768;
constexpr int F_PP=1024, F_O=4096, F_Y=8192;
#define KV0 1
#define KVS 3
#define LPR 2
#define CPR 2
#define GPR 1
#define CGATE 0
#define OSLP 2
#define YSLP 8
#define TPX 16
#define TPW 2
#define TWO 0
#define CVP 1
#define G2P 3
#define OUTC 1
#define ROT 0
#define WTL 2
#define PB2 1
#define PREM 9
#define KTH 1
#define YDEF 3
#define OPOL 1
#define CODEL 280
#define MIX 0
#define LOCA 3
#define XLOC 1
#define PGT 0
#define PGO 0
#define QREM 0
#define HLAST 1

#define N2_ASM \
  "v_mov_b32 v64, 0\n\t" \
  "v_mov_b32 v65, 0\n\t" \
  "v_mov_b32 v66, 0\n\t" \
  "v_mov_b32 v67, 0\n\t" \
  "v_mov_b32 v68, 0\n\t" \
  "v_mov_b32 v69, 0\n\t" \
  "v_mov_b32 v70, 0\n\t" \
  "v_mov_b32 v71, 0\n\t" \
  "v_mov_b32 v72, 0\n\t" \
  "v_mov_b32 v73, 0\n\t" \
  "v_mov_b32 v74, 0\n\t" \
  "v_mov_b32 v75, 0\n\t" \
  "v_mov_b32 v76, 0\n\t" \
  "v_mov_b32 v77, 0\n\t" \
  "v_mov_b32 v78, 0\n\t" \
  "v_mov_b32 v79, 0\n\t" \
  "v_mov_b32 v80, 0\n\t" \
  "v_mov_b32 v81, 0\n\t" \
  "v_mov_b32 v82, 0\n\t" \
  "v_mov_b32 v83, 0\n\t" \
  "v_mov_b32 v84, 0\n\t" \
  "v_mov_b32 v85, 0\n\t" \
  "v_mov_b32 v86, 0\n\t" \
  "v_mov_b32 v87, 0\n\t" \
  "v_mov_b32 v88, 0\n\t" \
  "v_mov_b32 v89, 0\n\t" \
  "v_mov_b32 v90, 0\n\t" \
  "v_mov_b32 v91, 0\n\t" \
  "v_mov_b32 v92, 0\n\t" \
  "v_mov_b32 v93, 0\n\t" \
  "v_mov_b32 v94, 0\n\t" \
  "v_mov_b32 v95, 0\n\t" \
  "v_mov_b32 v96, 0\n\t" \
  "v_mov_b32 v97, 0\n\t" \
  "v_mov_b32 v98, 0\n\t" \
  "v_mov_b32 v99, 0\n\t" \
  "v_mov_b32 v100, 0\n\t" \
  "v_mov_b32 v101, 0\n\t" \
  "v_mov_b32 v102, 0\n\t" \
  "v_mov_b32 v103, 0\n\t" \
  "v_mov_b32 v104, 0\n\t" \
  "v_mov_b32 v105, 0\n\t" \
  "v_mov_b32 v106, 0\n\t" \
  "v_mov_b32 v107, 0\n\t" \
  "v_mov_b32 v108, 0\n\t" \
  "v_mov_b32 v109, 0\n\t" \
  "v_mov_b32 v110, 0\n\t" \
  "v_mov_b32 v111, 0\n\t" \
  "v_mov_b32 v112, 0\n\t" \
  "v_mov_b32 v113, 0\n\t" \
  "v_mov_b32 v114, 0\n\t" \
  "v_mov_b32 v115, 0\n\t" \
  "v_mov_b32 v116, 0\n\t" \
  "v_mov_b32 v117, 0\n\t" \
  "v_mov_b32 v118, 0\n\t" \
  "v_mov_b32 v119, 0\n\t" \
  "v_mov_b32 v120, 0\n\t" \
  "v_mov_b32 v121, 0\n\t" \
  "v_mov_b32 v122, 0\n\t" \
  "v_mov_b32 v123, 0\n\t" \
  "v_mov_b32 v124, 0\n\t" \
  "v_mov_b32 v125, 0\n\t" \
  "v_mov_b32 v126, 0\n\t" \
  "v_mov_b32 v127, 0\n\t" \
  "v_mov_b32 v128, 0\n\t" \
  "v_mov_b32 v129, 0\n\t" \
  "v_mov_b32 v130, 0\n\t" \
  "v_mov_b32 v131, 0\n\t" \
  "v_mov_b32 v132, 0\n\t" \
  "v_mov_b32 v133, 0\n\t" \
  "v_mov_b32 v134, 0\n\t" \
  "v_mov_b32 v135, 0\n\t" \
  "v_mov_b32 v136, 0\n\t" \
  "v_mov_b32 v137, 0\n\t" \
  "v_mov_b32 v138, 0\n\t" \
  "v_mov_b32 v139, 0\n\t" \
  "v_mov_b32 v140, 0\n\t" \
  "v_mov_b32 v141, 0\n\t" \
  "v_mov_b32 v142, 0\n\t" \
  "v_mov_b32 v143, 0\n\t" \
  "v_mov_b32 v144, 0\n\t" \
  "v_mov_b32 v145, 0\n\t" \
  "v_mov_b32 v146, 0\n\t" \
  "v_mov_b32 v147, 0\n\t" \
  "v_mov_b32 v148, 0\n\t" \
  "v_mov_b32 v149, 0\n\t" \
  "v_mov_b32 v150, 0\n\t" \
  "v_mov_b32 v151, 0\n\t" \
  "v_mov_b32 v152, 0\n\t" \
  "v_mov_b32 v153, 0\n\t" \
  "v_mov_b32 v154, 0\n\t" \
  "v_mov_b32 v155, 0\n\t" \
  "v_mov_b32 v156, 0\n\t" \
  "v_mov_b32 v157, 0\n\t" \
  "v_mov_b32 v158, 0\n\t" \
  "v_mov_b32 v159, 0\n\t" \
  "v_mov_b32 v160, 0\n\t" \
  "v_mov_b32 v161, 0\n\t" \
  "v_mov_b32 v162, 0\n\t" \
  "v_mov_b32 v163, 0\n\t" \
  "v_mov_b32 v164, 0\n\t" \
  "v_mov_b32 v165, 0\n\t" \
  "v_mov_b32 v166, 0\n\t" \
  "v_mov_b32 v167, 0\n\t" \
  "s_mov_b32 s68, 0x50000\n\t" \
  "s_mov_b32 s69, 0x5550055\n\t" \
  "s_mov_b32 s70, 0xa0000\n\t" \
  "s_mov_b32 s71, 0xaaa00aa\n\t" \
  "s_mov_b32 s72, 0x150001\n\t" \
  "s_mov_b32 s73, 0x15550155\n\t" \
  "s_mov_b32 s74, 0x2a0002\n\t" \
  "s_mov_b32 s75, 0x2aaa02aa\n\t" \
  "s_mov_b32 s76, 0x55505555\n\t" \
  "s_mov_b32 s77, 0x50005500\n\t" \
  "s_mov_b32 s78, 0xaaa0aaaa\n\t" \
  "s_mov_b32 s79, 0xa000aa00\n\t" \
  "s_mov_b32 s80, 0x55405554\n\t" \
  "s_mov_b32 s81, 0x40005400\n\t" \
  "s_mov_b32 s82, 0xaa80aaa8\n\t" \
  "s_mov_b32 s83, 0x8000a800\n\t" \
  "s_mov_b32 s84, 0xaaaaaaaa\n\t" \
  "s_mov_b32 s85, 0xaaaaaaaa\n\t" \
  "v_add_u32 v60, 36864, v58\n\t" \
  "v_add_u32 v61, 36864, v59\n\t" \
  "s_mov_b32 %[it], 0\n\t" \
  "s_cmp_lg_u32 %[r], 0\n\t" \
  "s_barrier\n\t" \
  "s_cbranch_scc1 NB_%=\n\t" \
  "ds_read_b64_tr_b16 v[40:41], v60 offset:16384\n\t" \
  "ds_read_b64_tr_b16 v[42:43], v60 offset:16896\n\t" \
  "ds_read_b64_tr_b16 v[44:45], v60 offset:20480\n\t" \
  "ds_read_b64_tr_b16 v[46:47], v60 offset:20992\n\t" \
  "ds_read_b64_tr_b16 v[48:49], v61 offset:16384\n\t" \
  "ds_read_b64_tr_b16 v[50:51], v61 offset:16896\n\t" \
  "ds_read_b128 v[0:3], v56 offset:36864\n\t" \
  "ds_read_b128 v[4:7], v56 offset:38912\n\t" \
  "ds_read_b128 v[8:11], v56 offset:40960\n\t" \
  "ds_read_b128 v[12:15], v56 offset:43008\n\t" \
  "ds_read_b128 v[16:19], v56 offset:45056\n\t" \
  "s_waitcnt lgkmcnt(0)\n\t" \
  "s_branch N4a_%=\n\t" \
  "N0a_%=:\n\t" \
  "s_waitcnt lgkmcnt(4)\n\t" \
  "v_mfma_f32_16x16x32_bf16 v[160:163], v[40:43], v[0:3], v[160:163]\n\t" \
  "ds_read_b64_tr_b16 v[52:53], v59 offset:8192\n\t" \
  "v_mfma_f32_16x16x32_bf16 v[96:99], v[40:43], v[4:7], v[96:99]\n\t" \
  "ds_read_b64_tr_b16 v[54:55], v59 offset:8704\n\t" \
  "v_mfma_f32_16x16x32_bf16 v[104:107], v[40:43], v[8:11], v[104:107]\n\t" \
  "s_barrier\n\t" \
  "v_mfma_f32_16x16x32_bf16 v[112:115], v[40:43], v[12:15], v[112:115]\n\t" \
  "v_mfma_f32_16x16x32_bf16 v[120:123], v[40:43], v[16:19], v[120:123]\n\t" \
  "s_waitcnt lgkmcnt(4)\n\t" \
  "v_mfma_f32_16x16x32_bf16 v[128:131], v[4:7], v[44:47], v[128:131]\n\t" \
  "ds_read_b64_tr_b16 v[40:41], v58 offset:0\n\t" \
  "v_mfma_f32_16x16x32_bf16 v[136:139], v[8:11], v[44:47], v[136:139]\n\t" \
  "ds_read_b64_tr_b16 v[42:43], v58 offset:512\n\t" \
  "v_mfma_f32_16x16x32_bf16 v[144:147], v[12:15], v[44:47], v[144:147]\n\t" \
  "ds_read_b128 v[20:23], v57 offset:0\n\t" \
  "v_mfma_f32_16x16x32_bf16 v[152:155], v[16:19], v[44:47], v[152:155]\n\t" \
  "ds_read_b128 v[24:27], v57 offset:2048\n\t" \
  "s_waitcnt lgkmcnt(6)\n\t" \
  "v_mfma_f32_16x16x32_bf16 v[164:167], v[48:51], v[0:3], v[164:167]\n\t" \
  "ds_read_b64_tr_b16 v[44:45], v59 offset:0\n\t" \
  "v_mfma_f32_16x16x32_bf16 v[100:103], v[48:51], v[4:7], v[100:103]\n\t" \
  "ds_read_b64_tr_b16 v[46:47], v59 offset:512\n\t" \
  "v_mfma_f32_16x16x32_bf16 v[108:111], v[48:51], v[8:11], v[108:111]\n\t" \
  "ds_read_b128 v[28:31], v57 offset:4096\n\t" \
  "v_mfma_f32_16x16x32_bf16 v[116:119], v[48:51], v[12:15], v[116:119]\n\t" \
  "ds_read_b128 v[32:35], v57 offset:6144\n\t" \
  "v_mfma_f32_16x16x32_bf16 v[124:127], v[48:51], v[16:19], v[124:127]\n\t" \
  "s_waitcnt lgkmcnt(8)\n\t" \
  "v_mfma_f32_16x16x32_bf16 v[132:135], v[4:7], v[52:55], v[132:135]\n\t" \
  "ds_read_b64_tr_b16 v[48:49], v58 offset:16384\n\t" \
  "v_mfma_f32_16x16x32_bf16 v[140:143], v[8:11], v[52:55], v[140:143]\n\t" \
  "ds_read_b64_tr_b16 v[50:51], v58 offset:16896\n\t" \
  "v_mfma_f32_16x16x32_bf16 v[148:151], v[12:15], v[52:55], v[148:151]\n\t" \
  "ds_read_b128 v[36:39], v57 offset:8192\n\t" \
  "v_mfma_f32_16x16x32_bf16 v[156:159], v[16:19], v[52:55], v[156:159]\n\t" \
  "s_waitcnt lgkmcnt(9)\n\t" \
  "v_mfma_f32_16x16x32_bf16 v[64:67], v[40:43], v[4:7], v[64:67]\n\t" \
  "ds_read_b64_tr_b16 v[52:53], v58 offset:20480\n\t" \
  "v_mfma_f32_16x16x32_bf16 v[72:75], v[40:43], v[8:11], v[72:75]\n\t" \
  "ds_read_b64_tr_b16 v[54:55], v58 offset:20992\n\t" \
  "v_mfma_f32_16x16x32_bf16 v[80:83], v[40:43], v[12:15], v[80:83]\n\t" \
  "v_mfma_f32_16x16x32_bf16 v[88:91], v[40:43], v[16:19], v[88:91]\n\t" \
  "s_waitcnt lgkmcnt(7)\n\t" \
  "v_mfma_f32_16x16x32_bf16 v[68:71], v[44:47], v[4:7], v[68:71]\n\t" \
  "ds_read_b64_tr_b16 v[40:41], v59 offset:16384\n\t" \
  "v_mfma_f32_16x16x32_bf16 v[76:79], v[44:47], v[8:11], v[76:79]\n\t" \
  "ds_read_b64_tr_b16 v[42:43], v59 offset:16896\n\t" \
  "v_mfma_f32_16x16x32_bf16 v[84:87], v[44:47], v[12:15], v[84:87]\n\t" \
  "v_mfma_f32_16x16x32_bf16 v[92:95], v[44:47], v[16:19], v[92:95]\n\t" \
  "N1a_%=:\n\t" \
  "s_waitcnt lgkmcnt(4)\n\t" \
  "v_mfma_f32_16x16x32_bf16 v[160:163], v[48:51], v[20:23], v[160:163]\n\t" \
  "ds_read_b64_tr_b16 v[44:45], v59 offset:20480\n\t" \
  "v_mfma_f32_16x16x32_bf16 v[96:99], v[48:51], v[24:27], v[96:99]\n\t" \
  "ds_read_b64_tr_b16 v[46:47], v59 offset:20992\n\t" \
  "v_mfma_f32_16x16x32_bf16 v[104:107], v[48:51], v[28:31], v[104:107]\n\t" \
  "s_barrier\n\t" \
  "v_mfma_f32_16x16x32_bf16 v[112:115], v[48:51], v[32:35], v[112:115]\n\t" \
  "v_mfma_f32_16x16x32_bf16 v[120:123], v[48:51], v[36:39], v[120:123]\n\t" \
  "s_waitcnt lgkmcnt(4)\n\t" \
  "v_mfma_f32_16x16x32_bf16 v[128:131], v[24:27], v[52:55], v[128:131]\n\t" \
  "ds_read_b64_tr_b16 v[48:49], v58 offset:12288\n\t" \
  "v_mfma_f32_16x16x32_bf16 v[136:139], v[28:31], v[52:55], v[136:139]\n\t" \
  "ds_read_b64_tr_b16 v[50:51], v58 offset:12800\n\t" \
  "v_mfma_f32_16x16x32_bf16 v[144:147], v[32:35], v[52:55], v[144:147]\n\t" \
  "ds_read_b128 v[0:3], v56 offset:18432\n\t" \
  "v_mfma_f32_16x16x32_bf16 v[152:155], v[36:39], v[52:55], v[152:155]\n\t" \
  "ds_read_b128 v[4:7], v56 offset:20480\n\t" \
  "s_waitcnt lgkmcnt(6)\n\t" \
  "v_mfma_f32_16x16x32_bf16 v[164:167], v[40:43], v[20:23], v[164:167]\n\t" \
  "ds_read_b64_tr_b16 v[52:53], v59 offset:12288\n\t" \
  "v_mfma_f32_16x16x32_bf16 v[100:103], v[40:43], v[24:27], v[100:103]\n\t" \
  "ds_read_b64_tr_b16 v[54:55], v59 offset:12800\n\t" \
  "v_mfma_f32_16x16x32_bf16 v[108:111], v[40:43], v[28:31], v[108:111]\n\t" \
  "ds_read_b128 v[8:11], v56 offset:22528\n\t" \
  "v_mfma_f32_16x16x32_bf16 v[116:119], v[40:43], v[32:35], v[116:119]\n\t" \
  "ds_read_b128 v[12:15], v56 offset:24576\n\t" \
  "v_mfma_f32_16x16x32_bf16 v[124:127], v[40:43], v[36:39], v[124:127]\n\t" \
  "s_waitcnt lgkmcnt(8)\n\t" \
  "v_mfma_f32_16x16x32_bf16 v[132:135], v[24:27], v[44:47], v[132:135]\n\t" \
  "ds_read_b64_tr_b16 v[40:41], v58 offset:28672\n\t" \
  "v_mfma_f32_16x16x32_bf16 v[140:143], v[28:31], v[44:47], v[140:143]\n\t" \
  "ds_read_b64_tr_b16 v[42:43], v58 offset:29184\n\t" \
  "v_mfma_f32_16x16x32_bf16 v[148:151], v[32:35], v[44:47], v[148:151]\n\t" \
  "ds_read_b128 v[16:19], v56 offset:26624\n\t" \
  "v_mfma_f32_16x16x32_bf16 v[156:159], v[36:39], v[44:47], v[156:159]\n\t" \
  "s_waitcnt lgkmcnt(9)\n\t" \
  "v_mfma_f32_16x16x32_bf16 v[64:67], v[48:51], v[24:27], v[64:67]\n\t" \
  "ds_read_b64_tr_b16 v[44:45], v58 offset:32768\n\t" \
  "v_mfma_f32_16x16x32_bf16 v[72:75], v[48:51], v[28:31], v[72:75]\n\t" \
  "ds_read_b64_tr_b16 v[46:47], v58 offset:33280\n\t" \
  "v_mfma_f32_16x16x32_bf16 v[80:83], v[48:51], v[32:35], v[80:83]\n\t" \
  "v_mfma_f32_16x16x32_bf16 v[88:91], v[48:51], v[36:39], v[88:91]\n\t" \
  "s_waitcnt lgkmcnt(7)\n\t" \
  "v_mfma_f32_16x16x32_bf16 v[68:71], v[52:55], v[24:27], v[68:71]\n\t" \
  "ds_read_b64_tr_b16 v[48:49], v59 offset:28672\n\t" \
  "v_mfma_f32_16x16x32_bf16 v[76:79], v[52:55], v[28:31], v[76:79]\n\t" \
  "ds_read_b64_tr_b16 v[50:51], v59 offset:29184\n\t" \
  "v_mfma_f32_16x16x32_bf16 v[84:87], v[52:55], v[32:35], v[84:87]\n\t" \
  "v_mfma_f32_16x16x32_bf16 v[92:95], v[52:55], v[36:39], v[92:95]\n\t" \
  "N2a_%=:\n\t" \
  "s_waitcnt lgkmcnt(4)\n\t" \
  "v_mfma_f32_16x16x32_bf16 v[160:163], v[40:43], v[0:3], v[160:163]\n\t" \
  "ds_read_b64_tr_b16 v[52:53], v59 offset:32768\n\t" \
  "v_mfma_f32_16x16x32_bf16 v[96:99], v[40:43], v[4:7], v[96:99]\n\t" \
  "ds_read_b64_tr_b16 v[54:55], v59 offset:33280\n\t" \
  "v_mfma_f32_16x16x32_bf16 v[104:107], v[40:43], v[8:11], v[104:107]\n\t" \
  "s_barrier\n\t" \
  "v_mfma_f32_16x16x32_bf16 v[112:115], v[40:43], v[12:15], v[112:115]\n\t" \
  "v_mfma_f32_16x16x32_bf16 v[120:123], v[40:43], v[16:19], v[120:123]\n\t" \
  "s_waitcnt lgkmcnt(4)\n\t" \
  "v_mfma_f32_16x16x32_bf16 v[128:131], v[4:7], v[44:47], v[128:131]\n\t" \
  "ds_read_b64_tr_b16 v[40:41], v58 offset:24576\n\t" \
  "v_mfma_f32_16x16x32_bf16 v[136:139], v[8:11], v[44:47], v[136:139]\n\t" \
  "ds_read_b64_tr_b16 v[42:43], v58 offset:25088\n\t" \
  "v_mfma_f32_16x16x32_bf16 v[144:147], v[12:15], v[44:47], v[144:147]\n\t" \
  "ds_read_b128 v[20:23], v57 offset:18432\n\t" \
  "v_mfma_f32_16x16x32_bf16 v[152:155], v[16:19], v[44:47], v[152:155]\n\t" \
  "ds_read_b128 v[24:27], v57 offset:20480\n\t" \
  "s_waitcnt lgkmcnt(6)\n\t" \
  "v_mfma_f32_16x16x32_bf16 v[164:167], v[48:51], v[0:3], v[164:167]\n\t" \
  "ds_read_b64_tr_b16 v[44:45], v59 offset:24576\n\t" \
  "v_mfma_f32_16x16x32_bf16 v[100:103], v[48:51], v[4:7], v[100:103]\n\t" \
  "ds_read_b64_tr_b16 v[46:47], v59 offset:25088\n\t" \
  "v_mfma_f32_16x16x32_bf16 v[108:111], v[48:51], v[8:11], v[108:111]\n\t" \
  "ds_read_b128 v[28:31], v57 offset:22528\n\t" \
  "v_mfma_f32_16x16x32_bf16 v[116:119], v[48:51], v[12:15], v[116:119]\n\t" \
  "ds_read_b128 v[32:35], v57 offset:24576\n\t" \
  "v_mfma_f32_16x16x32_bf16 v[124:127], v[48:51], v[16:19], v[124:127]\n\t" \
  "s_waitcnt lgkmcnt(8)\n\t" \
  "v_mfma_f32_16x16x32_bf16 v[132:135], v[4:7], v[52:55], v[132:135]\n\t" \
  "ds_read_b64_tr_b16 v[48:49], v60 offset:4096\n\t" \
  "v_mfma_f32_16x16x32_bf16 v[140:143], v[8:11], v[52:55], v[140:143]\n\t" \
  "ds_read_b64_tr_b16 v[50:51], v60 offset:4608\n\t" \
  "v_mfma_f32_16x16x32_bf16 v[148:151], v[12:15], v[52:55], v[148:151]\n\t" \
  "ds_read_b128 v[36:39], v57 offset:26624\n\t" \
  "v_mfma_f32_16x16x32_bf16 v[156:159], v[16:19], v[52:55], v[156:159]\n\t" \
  "s_waitcnt lgkmcnt(9)\n\t" \
  "v_mfma_f32_16x16x32_bf16 v[64:67], v[40:43], v[4:7], v[64:67]\n\t" \
  "ds_read_b64_tr_b16 v[52:53], v60 offset:8192\n\t" \
  "v_mfma_f32_16x16x32_bf16 v[72:75], v[40:43], v[8:11], v[72:75]\n\t" \
  "ds_read_b64_tr_b16 v[54:55], v60 offset:8704\n\t" \
  "v_mfma_f32_16x16x32_bf16 v[80:83], v[40:43], v[12:15], v[80:83]\n\t" \
  "v_mfma_f32_16x16x32_bf16 v[88:91], v[40:43], v[16:19], v[88:91]\n\t" \
  "s_waitcnt lgkmcnt(7)\n\t" \
  "v_mfma_f32_16x16x32_bf16 v[68:71], v[44:47], v[4:7], v[68:71]\n\t" \
  "ds_read_b64_tr_b16 v[40:41], v61 offset:4096\n\t" \
  "v_mfma_f32_16x16x32_bf16 v[76:79], v[44:47], v[8:11], v[76:79]\n\t" \
  "ds_read_b64_tr_b16 v[42:43], v61 offset:4608\n\t" \
  "v_mfma_f32_16x16x32_bf16 v[84:87], v[44:47], v[12:15], v[84:87]\n\t" \
  "v_mfma_f32_16x16x32_bf16 v[92:95], v[44:47], v[16:19], v[92:95]\n\t" \
  "N3a_%=:\n\t" \
  "s_waitcnt lgkmcnt(4)\n\t" \
  "v_mfma_f32_16x16x32_bf16 v[160:163], v[48:51], v[20:23], v[160:163]\n\t" \
  "ds_read_b64_tr_b16 v[44:45], v61 offset:8192\n\t" \
  "v_mfma_f32_16x16x32_bf16 v[96:99], v[48:51], v[24:27], v[96:99]\n\t" \
  "ds_read_b64_tr_b16 v[46:47], v61 offset:8704\n\t" \
  "v_mfma_f32_16x16x32_bf16 v[104:107], v[48:51], v[28:31], v[104:107]\n\t" \
  "s_barrier\n\t" \
  "v_mfma_f32_16x16x32_bf16 v[112:115], v[48:51], v[32:35], v[112:115]\n\t" \
  "v_mfma_f32_16x16x32_bf16 v[120:123], v[48:51], v[36:39], v[120:123]\n\t" \
  "s_waitcnt lgkmcnt(4)\n\t" \
  "v_mfma_f32_16x16x32_bf16 v[128:131], v[24:27], v[52:55], v[128:131]\n\t" \
  "ds_read_b64_tr_b16 v[48:49], v60 offset:0\n\t" \
  "v_mfma_f32_16x16x32_bf16 v[136:139], v[28:31], v[52:55], v[136:139]\n\t" \
  "ds_read_b64_tr_b16 v[50:51], v60 offset:512\n\t" \
  "v_mfma_f32_16x16x32_bf16 v[144:147], v[32:35], v[52:55], v[144:147]\n\t" \
  "ds_read_b128 v[0:3], v56 offset:36864\n\t" \
  "v_mfma_f32_16x16x32_bf16 v[152:155], v[36:39], v[52:55], v[152:155]\n\t" \
  "ds_read_b128 v[4:7], v56 offset:38912\n\t" \
  "s_waitcnt lgkmcnt(6)\n\t" \
  "v_mfma_f32_16x16x32_bf16 v[164:167], v[40:43], v[20:23], v[164:167]\n\t" \
  "ds_read_b64_tr_b16 v[52:53], v61 offset:0\n\t" \
  "v_mfma_f32_16x16x32_bf16 v[100:103], v[40:43], v[24:27], v[100:103]\n\t" \
  "ds_read_b64_tr_b16 v[54:55], v61 offset:512\n\t" \
  "v_mfma_f32_16x16x32_bf16 v[108:111], v[40:43], v[28:31], v[108:111]\n\t" \
  "ds_read_b128 v[8:11], v56 offset:40960\n\t" \
  "v_mfma_f32_16x16x32_bf16 v[116:119], v[40:43], v[32:35], v[116:119]\n\t" \
  "ds_read_b128 v[12:15], v56 offset:43008\n\t" \
  "v_mfma_f32_16x16x32_bf16 v[124:127], v[40:43], v[36:39], v[124:127]\n\t" \
  "s_waitcnt lgkmcnt(8)\n\t" \
  "v_mfma_f32_16x16x32_bf16 v[132:135], v[24:27], v[44:47], v[132:135]\n\t" \
  "ds_read_b64_tr_b16 v[40:41], v60 offset:16384\n\t" \
  "v_mfma_f32_16x16x32_bf16 v[140:143], v[28:31], v[44:47], v[140:143]\n\t" \
  "ds_read_b64_tr_b16 v[42:43], v60 offset:16896\n\t" \
  "v_mfma_f32_16x16x32_bf16 v[148:151], v[32:35], v[44:47], v[148:151]\n\t" \
  "ds_read_b128 v[16:19], v56 offset:45056\n\t" \
  "v_mfma_f32_16x16x32_bf16 v[156:159], v[36:39], v[44:47], v[156:159]\n\t" \
  "s_waitcnt lgkmcnt(9)\n\t" \
  "v_mfma_f32_16x16x32_bf16 v[64:67], v[48:51], v[24:27], v[64:67]\n\t" \
  "ds_read_b64_tr_b16 v[44:45], v60 offset:20480\n\t" \
  "v_mfma_f32_16x16x32_bf16 v[72:75], v[48:51], v[28:31], v[72:75]\n\t" \
  "ds_read_b64_tr_b16 v[46:47], v60 offset:20992\n\t" \
  "v_mfma_f32_16x16x32_bf16 v[80:83], v[48:51], v[32:35], v[80:83]\n\t" \
  "v_mfma_f32_16x16x32_bf16 v[88:91], v[48:51], v[36:39], v[88:91]\n\t" \
  "s_waitcnt lgkmcnt(7)\n\t" \
  "v_mfma_f32_16x16x32_bf16 v[68:71], v[52:55], v[24:27], v[68:71]\n\t" \
  "ds_read_b64_tr_b16 v[48:49], v61 offset:16384\n\t" \
  "v_mfma_f32_16x16x32_bf16 v[76:79], v[52:55], v[28:31], v[76:79]\n\t" \
  "ds_read_b64_tr_b16 v[50:51], v61 offset:16896\n\t" \
  "v_mfma_f32_16x16x32_bf16 v[84:87], v[52:55], v[32:35], v[84:87]\n\t" \
  "v_mfma_f32_16x16x32_bf16 v[92:95], v[52:55], v[36:39], v[92:95]\n\t" \
  "N4a_%=:\n\t" \
  "s_waitcnt lgkmcnt(4)\n\t" \
  "v_mfma_f32_16x16x32_bf16 v[160:163], v[40:43], v[0:3], v[160:163]\n\t" \
  "ds_read_b64_tr_b16 v[52:53], v61 offset:20480\n\t" \
  "v_mfma_f32_16x16x32_bf16 v[96:99], v[40:43], v[4:7], v[96:99]\n\t" \
  "ds_read_b64_tr_b16 v[54:55], v61 offset:20992\n\t" \
  "v_mfma_f32_16x16x32_bf16 v[104:107], v[40:43], v[8:11], v[104:107]\n\t" \
  "s_barrier\n\t" \
  "v_mfma_f32_16x16x32_bf16 v[112:115], v[40:43], v[12:15], v[112:115]\n\t" \
  "v_mfma_f32_16x16x32_bf16 v[120:123], v[40:43], v[16:19], v[120:123]\n\t" \
  "s_waitcnt lgkmcnt(4)\n\t" \
  "v_mfma_f32_16x16x32_bf16 v[128:131], v[4:7], v[44:47], v[128:131]\n\t" \
  "ds_read_b64_tr_b16 v[40:41], v60 offset:12288\n\t" \
  "v_mfma_f32_16x16x32_bf16 v[136:139], v[8:11], v[44:47], v[136:139]\n\t" \
  "ds_read_b64_tr_b16 v[42:43], v60 offset:12800\n\t" \
  "v_mfma_f32_16x16x32_bf16 v[144:147], v[12:15], v[44:47], v[144:147]\n\t" \
  "ds_read_b128 v[20:23], v57 offset:36864\n\t" \
  "v_mfma_f32_16x16x32_bf16 v[152:155], v[16:19], v[44:47], v[152:155]\n\t" \
  "ds_read_b128 v[24:27], v57 offset:38912\n\t" \
  "s_waitcnt lgkmcnt(6)\n\t" \
  "v_mfma_f32_16x16x32_bf16 v[164:167], v[48:51], v[0:3], v[164:167]\n\t" \
  "ds_read_b64_tr_b16 v[44:45], v61 offset:12288\n\t" \
  "v_mfma_f32_16x16x32_bf16 v[100:103], v[48:51], v[4:7], v[100:103]\n\t" \
  "ds_read_b64_tr_b16 v[46:47], v61 offset:12800\n\t" \
  "v_mfma_f32_16x16x32_bf16 v[108:111], v[48:51], v[8:11], v[108:111]\n\t" \
  "ds_read_b128 v[28:31], v57 offset:40960\n\t" \
  "v_mfma_f32_16x16x32_bf16 v[116:119], v[48:51], v[12:15], v[116:119]\n\t" \
  "ds_read_b128 v[32:35], v57 offset:43008\n\t" \
  "v_mfma_f32_16x16x32_bf16 v[124:127], v[48:51], v[16:19], v[124:127]\n\t" \
  "s_waitcnt lgkmcnt(8)\n\t" \
  "v_mfma_f32_16x16x32_bf16 v[132:135], v[4:7], v[52:55], v[132:135]\n\t" \
  "ds_read_b64_tr_b16 v[48:49], v60 offset:28672\n\t" \
  "v_mfma_f32_16x16x32_bf16 v[140:143], v[8:11], v[52:55], v[140:143]\n\t" \
  "ds_read_b64_tr_b16 v[50:51], v60 offset:29184\n\t" \
  "v_mfma_f32_16x16x32_bf16 v[148:151], v[12:15], v[52:55], v[148:151]\n\t" \
  "ds_read_b128 v[36:39], v57 offset:45056\n\t" \
  "v_mfma_f32_16x16x32_bf16 v[156:159], v[16:19], v[52:55], v[156:159]\n\t" \
  "s_waitcnt lgkmcnt(9)\n\t" \
  "v_mfma_f32_16x16x32_bf16 v[64:67], v[40:43], v[4:7], v[64:67]\n\t" \
  "ds_read_b64_tr_b16 v[52:53], v60 offset:32768\n\t" \
  "v_mfma_f32_16x16x32_bf16 v[72:75], v[40:43], v[8:11], v[72:75]\n\t" \
  "ds_read_b64_tr_b16 v[54:55], v60 offset:33280\n\t" \
  "v_mfma_f32_16x16x32_bf16 v[80:83], v[40:43], v[12:15], v[80:83]\n\t" \
  "v_mfma_f32_16x16x32_bf16 v[88:91], v[40:43], v[16:19], v[88:91]\n\t" \
  "s_waitcnt lgkmcnt(7)\n\t" \
  "v_mfma_f32_16x16x32_bf16 v[68:71], v[44:47], v[4:7], v[68:71]\n\t" \
  "ds_read_b64_tr_b16 v[40:41], v61 offset:28672\n\t" \
  "v_mfma_f32_16x16x32_bf16 v[76:79], v[44:47], v[8:11], v[76:79]\n\t" \
  "ds_read_b64_tr_b16 v[42:43], v61 offset:29184\n\t" \
  "v_mfma_f32_16x16x32_bf16 v[84:87], v[44:47], v[12:15], v[84:87]\n\t" \
  "v_mfma_f32_16x16x32_bf16 v[92:95], v[44:47], v[16:19], v[92:95]\n\t" \
  "N5a_%=:\n\t" \
  "s_waitcnt lgkmcnt(4)\n\t" \
  "v_mfma_f32_16x16x32_bf16 v[160:163], v[48:51], v[20:23], v[160:163]\n\t" \
  "ds_read_b64_tr_b16 v[44:45], v61 offset:32768\n\t" \
  "v_mfma_f32_16x16x32_bf16 v[96:99], v[48:51], v[24:27], v[96:99]\n\t" \
  "ds_read_b64_tr_b16 v[46:47], v61 offset:33280\n\t" \
  "v_mfma_f32_16x16x32_bf16 v[104:107], v[48:51], v[28:31], v[104:107]\n\t" \
  "s_barrier\n\t" \
  "v_mfma_f32_16x16x32_bf16 v[112:115], v[48:51], v[32:35], v[112:115]\n\t" \
  "v_mfma_f32_16x16x32_bf16 v[120:123], v[48:51], v[36:39], v[120:123]\n\t" \
  "s_waitcnt lgkmcnt(4)\n\t" \
  "v_mfma_f32_16x16x32_bf16 v[128:131], v[24:27], v[52:55], v[128:131]\n\t" \
  "ds_read_b64_tr_b16 v[48:49], v60 offset:24576\n\t" \
  "v_mfma_f32_16x16x32_bf16 v[136:139], v[28:31], v[52:55], v[136:139]\n\t" \
  "ds_read_b64_tr_b16 v[50:51], v60 offset:25088\n\t" \
  "v_mfma_f32_16x16x32_bf16 v[144:147], v[32:35], v[52:55], v[144:147]\n\t" \
  "ds_read_b128 v[0:3], v56 offset:0\n\t" \
  "v_mfma_f32_16x16x32_bf16 v[152:155], v[36:39], v[52:55], v[152:155]\n\t" \
  "ds_read_b128 v[4:7], v56 offset:2048\n\t" \
  "s_waitcnt lgkmcnt(6)\n\t" \
  "v_mfma_f32_16x16x32_bf16 v[164:167], v[40:43], v[20:23], v[164:167]\n\t" \
  "ds_read_b64_tr_b16 v[52:53], v61 offset:24576\n\t" \
  "v_mfma_f32_16x16x32_bf16 v[100:103], v[40:43], v[24:27], v[100:103]\n\t" \
  "ds_read_b64_tr_b16 v[54:55], v61 offset:25088\n\t" \
  "v_mfma_f32_16x16x32_bf16 v[108:111], v[40:43], v[28:31], v[108:111]\n\t" \
  "ds_read_b128 v[8:11], v56 offset:4096\n\t" \
  "v_mfma_f32_16x16x32_bf16 v[116:119], v[40:43], v[32:35], v[116:119]\n\t" \
  "ds_read_b128 v[12:15], v56 offset:6144\n\t" \
  "v_mfma_f32_16x16x32_bf16 v[124:127], v[40:43], v[36:39], v[124:127]\n\t" \
  "s_waitcnt lgkmcnt(8)\n\t" \
  "v_mfma_f32_16x16x32_bf16 v[132:135], v[24:27], v[44:47], v[132:135]\n\t" \
  "ds_read_b64_tr_b16 v[40:41], v58 offset:4096\n\t" \
  "v_mfma_f32_16x16x32_bf16 v[140:143], v[28:31], v[44:47], v[140:143]\n\t" \
  "ds_read_b64_tr_b16 v[42:43], v58 offset:4608\n\t" \
  "v_mfma_f32_16x16x32_bf16 v[148:151], v[32:35], v[44:47], v[148:151]\n\t" \
  "ds_read_b128 v[16:19], v56 offset:8192\n\t" \
  "v_mfma_f32_16x16x32_bf16 v[156:159], v[36:39], v[44:47], v[156:159]\n\t" \
  "s_waitcnt lgkmcnt(9)\n\t" \
  "v_mfma_f32_16x16x32_bf16 v[64:67], v[48:51], v[24:27], v[64:67]\n\t" \
  "ds_read_b64_tr_b16 v[44:45], v58 offset:8192\n\t" \
  "v_mfma_f32_16x16x32_bf16 v[72:75], v[48:51], v[28:31], v[72:75]\n\t" \
  "ds_read_b64_tr_b16 v[46:47], v58 offset:8704\n\t" \
  "v_mfma_f32_16x16x32_bf16 v[80:83], v[48:51], v[32:35], v[80:83]\n\t" \
  "v_mfma_f32_16x16x32_bf16 v[88:91], v[48:51], v[36:39], v[88:91]\n\t" \
  "s_waitcnt lgkmcnt(7)\n\t" \
  "v_mfma_f32_16x16x32_bf16 v[68:71], v[52:55], v[24:27], v[68:71]\n\t" \
  "ds_read_b64_tr_b16 v[48:49], v59 offset:4096\n\t" \
  "v_mfma_f32_16x16x32_bf16 v[76:79], v[52:55], v[28:31], v[76:79]\n\t" \
  "ds_read_b64_tr_b16 v[50:51], v59 offset:4608\n\t" \
  "s_addk_i32 %[it], 1\n\t" \
  "v_mfma_f32_16x16x32_bf16 v[84:87], v[52:55], v[32:35], v[84:87]\n\t" \
  "s_cmp_lt_u32 %[it], 6\n\t" \
  "v_mfma_f32_16x16x32_bf16 v[92:95], v[52:55], v[36:39], v[92:95]\n\t" \
  "s_cbranch_scc1 N0a_%=\n\t" \
  "s_setprio 3\n\t" \
  "v_cvt_pk_bf16_f32 v120, v120, v121\n\t" \
  "v_cvt_pk_bf16_f32 v121, v122, v123\n\t" \
  "v_cvt_pk_bf16_f32 v122, v124, v125\n\t" \
  "v_cvt_pk_bf16_f32 v123, v126, v127\n\t" \
  "v_cvt_pk_bf16_f32 v28, v152, v153\n\t" \
  "v_cvt_pk_bf16_f32 v29, v154, v155\n\t" \
  "v_cvt_pk_bf16_f32 v38, v156, v157\n\t" \
  "v_cvt_pk_bf16_f32 v39, v158, v159\n\t" \
  "ds_write_b128 v63, v[120:123]\n\t" \
  "ds_write2_b64 v63, v[28:29], v[38:39] offset0:128 offset1:129\n\t" \
  "v_mov_b32 v57, %[fa]\n\t" \
  "v_mov_b32 v58, 1\n\t" \
  "s_mov_b64 exec, 1\n\t" \
  "ds_write_b32 v57, v58\n\t" \
  "s_mov_b64 exec, -1\n\t" \
  "v_cvt_pk_bf16_f32 v160, v160, v161\n\t" \
  "v_cvt_pk_bf16_f32 v161, v162, v163\n\t" \
  "v_cvt_pk_bf16_f32 v162, v164, v165\n\t" \
  "v_cvt_pk_bf16_f32 v163, v166, v167\n\t" \
  "s_branch NX_%=\n\t" \
  "NB_%=:\n\t" \
  "ds_read_b64_tr_b16 v[40:41], v60 offset:20480\n\t" \
  "ds_read_b64_tr_b16 v[42:43], v60 offset:20992\n\t" \
  "ds_read_b64_tr_b16 v[44:45], v60 offset:16384\n\t" \
  "ds_read_b64_tr_b16 v[46:47], v60 offset:16896\n\t" \
  "ds_read_b64_tr_b16 v[48:49], v61 offset:20480\n\t" \
  "ds_read_b64_tr_b16 v[50:51], v61 offset:20992\n\t" \
  "ds_read_b128 v[0:3], v56 offset:36864\n\t" \
  "ds_read_b128 v[4:7], v56 offset:47104\n\t" \
  "ds_read_b128 v[8:11], v56 offset:49152\n\t" \
  "ds_read_b128 v[12:15], v56 offset:51200\n\t" \
  "ds_read_b128 v[16:19], v56 offset:53248\n\t" \
  "s_waitcnt lgkmcnt(0)\n\t" \
  "s_branch N4b_%=\n\t" \
  "N0b_%=:\n\t" \
  "s_waitcnt lgkmcnt(4)\n\t" \
  "v_mfma_f32_16x16x32_bf16 v[160:163], v[0:3], v[40:43], v[160:163]\n\t" \
  "ds_read_b64_tr_b16 v[52:53], v59 offset:4096\n\t" \
  "v_mfma_f32_16x16x32_bf16 v[128:131], v[4:7], v[40:43], v[128:131]\n\t" \
  "ds_read_b64_tr_b16 v[54:55], v59 offset:4608\n\t" \
  "v_mfma_f32_16x16x32_bf16 v[136:139], v[8:11], v[40:43], v[136:139]\n\t" \
  "s_barrier\n\t" \
  "v_mfma_f32_16x16x32_bf16 v[144:147], v[12:15], v[40:43], v[144:147]\n\t" \
  "v_mfma_f32_16x16x32_bf16 v[152:155], v[16:19], v[40:43], v[152:155]\n\t" \
  "s_waitcnt lgkmcnt(4)\n\t" \
  "v_mfma_f32_16x16x32_bf16 v[96:99], v[44:47], v[4:7], v[96:99]\n\t" \
  "ds_read_b64_tr_b16 v[40:41], v58 offset:0\n\t" \
  "v_mfma_f32_16x16x32_bf16 v[104:107], v[44:47], v[8:11], v[104:107]\n\t" \
  "ds_read_b64_tr_b16 v[42:43], v58 offset:512\n\t" \
  "v_mfma_f32_16x16x32_bf16 v[112:115], v[44:47], v[12:15], v[112:115]\n\t" \
  "ds_read_b128 v[20:23], v57 offset:0\n\t" \
  "v_mfma_f32_16x16x32_bf16 v[120:123], v[44:47], v[16:19], v[120:123]\n\t" \
  "ds_read_b128 v[24:27], v57 offset:10240\n\t" \
  "s_waitcnt lgkmcnt(6)\n\t" \
  "v_mfma_f32_16x16x32_bf16 v[164:167], v[0:3], v[48:51], v[164:167]\n\t" \
  "ds_read_b64_tr_b16 v[44:45], v59 offset:0\n\t" \
  "v_mfma_f32_16x16x32_bf16 v[132:135], v[4:7], v[48:51], v[132:135]\n\t" \
  "ds_read_b64_tr_b16 v[46:47], v59 offset:512\n\t" \
  "v_mfma_f32_16x16x32_bf16 v[140:143], v[8:11], v[48:51], v[140:143]\n\t" \
  "ds_read_b128 v[28:31], v57 offset:12288\n\t" \
  "v_mfma_f32_16x16x32_bf16 v[148:151], v[12:15], v[48:51], v[148:151]\n\t" \
  "ds_read_b128 v[32:35], v57 offset:14336\n\t" \
  "v_mfma_f32_16x16x32_bf16 v[156:159], v[16:19], v[48:51], v[156:159]\n\t" \
  "s_waitcnt lgkmcnt(8)\n\t" \
  "v_mfma_f32_16x16x32_bf16 v[100:103], v[52:55], v[4:7], v[100:103]\n\t" \
  "ds_read_b64_tr_b16 v[48:49], v58 offset:20480\n\t" \
  "v_mfma_f32_16x16x32_bf16 v[108:111], v[52:55], v[8:11], v[108:111]\n\t" \
  "ds_read_b64_tr_b16 v[50:51], v58 offset:20992\n\t" \
  "v_mfma_f32_16x16x32_bf16 v[116:119], v[52:55], v[12:15], v[116:119]\n\t" \
  "ds_read_b128 v[36:39], v57 offset:16384\n\t" \
  "v_mfma_f32_16x16x32_bf16 v[124:127], v[52:55], v[16:19], v[124:127]\n\t" \
  "s_waitcnt lgkmcnt(9)\n\t" \
  "v_mfma_f32_16x16x32_bf16 v[64:67], v[40:43], v[4:7], v[64:67]\n\t" \
  "ds_read_b64_tr_b16 v[52:53], v58 offset:16384\n\t" \
  "v_mfma_f32_16x16x32_bf16 v[72:75], v[40:43], v[8:11], v[72:75]\n\t" \
  "ds_read_b64_tr_b16 v[54:55], v58 offset:16896\n\t" \
  "v_mfma_f32_16x16x32_bf16 v[80:83], v[40:43], v[12:15], v[80:83]\n\t" \
  "v_mfma_f32_16x16x32_bf16 v[88:91], v[40:43], v[16:19], v[88:91]\n\t" \
  "s_waitcnt lgkmcnt(7)\n\t" \
  "v_mfma_f32_16x16x32_bf16 v[68:71], v[44:47], v[4:7], v[68:71]\n\t" \
  "ds_read_b64_tr_b16 v[40:41], v59 offset:20480\n\t" \
  "v_mfma_f32_16x16x32_bf16 v[76:79], v[44:47], v[8:11], v[76:79]\n\t" \
  "ds_read_b64_tr_b16 v[42:43], v59 offset:20992\n\t" \
  "v_mfma_f32_16x16x32_bf16 v[84:87], v[44:47], v[12:15], v[84:87]\n\t" \
  "v_mfma_f32_16x16x32_bf16 v[92:95], v[44:47], v[16:19], v[92:95]\n\t" \
  "N1b_%=:\n\t" \
  "s_waitcnt lgkmcnt(4)\n\t" \
  "v_mfma_f32_16x16x32_bf16 v[160:163], v[20:23], v[48:51], v[160:163]\n\t" \
  "ds_read_b64_tr_b16 v[44:45], v59 offset:16384\n\t" \
  "v_mfma_f32_16x16x32_bf16 v[128:131], v[24:27], v[48:51], v[128:131]\n\t" \
  "ds_read_b64_tr_b16 v[46:47], v59 offset:16896\n\t" \
  "v_mfma_f32_16x16x32_bf16 v[136:139], v[28:31], v[48:51], v[136:139]\n\t" \
  "s_barrier\n\t" \
  "v_mfma_f32_16x16x32_bf16 v[144:147], v[32:35], v[48:51], v[144:147]\n\t" \
  "v_mfma_f32_16x16x32_bf16 v[152:155], v[36:39], v[48:51], v[152:155]\n\t" \
  "s_waitcnt lgkmcnt(4)\n\t" \
  "v_mfma_f32_16x16x32_bf16 v[96:99], v[52:55], v[24:27], v[96:99]\n\t" \
  "ds_read_b64_tr_b16 v[48:49], v58 offset:12288\n\t" \
  "v_mfma_f32_16x16x32_bf16 v[104:107], v[52:55], v[28:31], v[104:107]\n\t" \
  "ds_read_b64_tr_b16 v[50:51], v58 offset:12800\n\t" \
  "v_mfma_f32_16x16x32_bf16 v[112:115], v[52:55], v[32:35], v[112:115]\n\t" \
  "ds_read_b128 v[0:3], v56 offset:18432\n\t" \
  "v_mfma_f32_16x16x32_bf16 v[120:123], v[52:55], v[36:39], v[120:123]\n\t" \
  "ds_read_b128 v[4:7], v56 offset:28672\n\t" \
  "s_waitcnt lgkmcnt(6)\n\t" \
  "v_mfma_f32_16x16x32_bf16 v[164:167], v[20:23], v[40:43], v[164:167]\n\t" \
  "ds_read_b64_tr_b16 v[52:53], v59 offset:12288\n\t" \
  "v_mfma_f32_16x16x32_bf16 v[132:135], v[24:27], v[40:43], v[132:135]\n\t" \
  "ds_read_b64_tr_b16 v[54:55], v59 offset:12800\n\t" \
  "v_mfma_f32_16x16x32_bf16 v[140:143], v[28:31], v[40:43], v[140:143]\n\t" \
  "ds_read_b128 v[8:11], v56 offset:30720\n\t" \
  "v_mfma_f32_16x16x32_bf16 v[148:151], v[32:35], v[40:43], v[148:151]\n\t" \
  "ds_read_b128 v[12:15], v56 offset:32768\n\t" \
  "v_mfma_f32_16x16x32_bf16 v[156:159], v[36:39], v[40:43], v[156:159]\n\t" \
  "s_waitcnt lgkmcnt(8)\n\t" \
  "v_mfma_f32_16x16x32_bf16 v[100:103], v[44:47], v[24:27], v[100:103]\n\t" \
  "ds_read_b64_tr_b16 v[40:41], v58 offset:32768\n\t" \
  "v_mfma_f32_16x16x32_bf16 v[108:111], v[44:47], v[28:31], v[108:111]\n\t" \
  "ds_read_b64_tr_b16 v[42:43], v58 offset:33280\n\t" \
  "v_mfma_f32_16x16x32_bf16 v[116:119], v[44:47], v[32:35], v[116:119]\n\t" \
  "ds_read_b128 v[16:19], v56 offset:34816\n\t" \
  "v_mfma_f32_16x16x32_bf16 v[124:127], v[44:47], v[36:39], v[124:127]\n\t" \
  "s_waitcnt lgkmcnt(9)\n\t" \
  "v_mfma_f32_16x16x32_bf16 v[64:67], v[48:51], v[24:27], v[64:67]\n\t" \
  "ds_read_b64_tr_b16 v[44:45], v58 offset:28672\n\t" \
  "v_mfma_f32_16x16x32_bf16 v[72:75], v[48:51], v[28:31], v[72:75]\n\t" \
  "ds_read_b64_tr_b16 v[46:47], v58 offset:29184\n\t" \
  "v_mfma_f32_16x16x32_bf16 v[80:83], v[48:51], v[32:35], v[80:83]\n\t" \
  "v_mfma_f32_16x16x32_bf16 v[88:91], v[48:51], v[36:39], v[88:91]\n\t" \
  "s_waitcnt lgkmcnt(7)\n\t" \
  "v_mfma_f32_16x16x32_bf16 v[68:71], v[52:55], v[24:27], v[68:71]\n\t" \
  "ds_read_b64_tr_b16 v[48:49], v59 offset:32768\n\t" \
  "v_mfma_f32_16x16x32_bf16 v[76:79], v[52:55], v[28:31], v[76:79]\n\t" \
  "ds_read_b64_tr_b16 v[50:51], v59 offset:33280\n\t" \
  "v_mfma_f32_16x16x32_bf16 v[84:87], v[52:55], v[32:35], v[84:87]\n\t" \
  "v_mfma_f32_16x16x32_bf16 v[92:95], v[52:55], v[36:39], v[92:95]\n\t" \
  "N2b_%=:\n\t" \
  "s_waitcnt lgkmcnt(4)\n\t" \
  "v_mfma_f32_16x16x32_bf16 v[160:163], v[0:3], v[40:43], v[160:163]\n\t" \
  "ds_read_b64_tr_b16 v[52:53], v59 offset:28672\n\t" \
  "v_mfma_f32_16x16x32_bf16 v[128:131], v[4:7], v[40:43], v[128:131]\n\t" \
  "ds_read_b64_tr_b16 v[54:55], v59 offset:29184\n\t" \
  "v_mfma_f32_16x16x32_bf16 v[136:139], v[8:11], v[40:43], v[136:139]\n\t" \
  "s_barrier\n\t" \
  "v_mfma_f32_16x16x32_bf16 v[144:147], v[12:15], v[40:43], v[144:147]\n\t" \
  "v_mfma_f32_16x16x32_bf16 v[152:155], v[16:19], v[40:43], v[152:155]\n\t" \
  "s_waitcnt lgkmcnt(4)\n\t" \
  "v_mfma_f32_16x16x32_bf16 v[96:99], v[44:47], v[4:7], v[96:99]\n\t" \
  "ds_read_b64_tr_b16 v[40:41], v58 offset:24576\n\t" \
  "v_mfma_f32_16x16x32_bf16 v[104:107], v[44:47], v[8:11], v[104:107]\n\t" \
  "ds_read_b64_tr_b16 v[42:43], v58 offset:25088\n\t" \
  "v_mfma_f32_16x16x32_bf16 v[112:115], v[44:47], v[12:15], v[112:115]\n\t" \
  "ds_read_b128 v[20:23], v57 offset:18432\n\t" \
  "v_mfma_f32_16x16x32_bf16 v[120:123], v[44:47], v[16:19], v[120:123]\n\t" \
  "ds_read_b128 v[24:27], v57 offset:28672\n\t" \
  "s_waitcnt lgkmcnt(6)\n\t" \
  "v_mfma_f32_16x16x32_bf16 v[164:167], v[0:3], v[48:51], v[164:167]\n\t" \
  "ds_read_b64_tr_b16 v[44:45], v59 offset:24576\n\t" \
  "v_mfma_f32_16x16x32_bf16 v[132:135], v[4:7], v[48:51], v[132:135]\n\t" \
  "ds_read_b64_tr_b16 v[46:47], v59 offset:25088\n\t" \
  "v_mfma_f32_16x16x32_bf16 v[140:143], v[8:11], v[48:51], v[140:143]\n\t" \
  "ds_read_b128 v[28:31], v57 offset:30720\n\t" \
  "v_mfma_f32_16x16x32_bf16 v[148:151], v[12:15], v[48:51], v[148:151]\n\t" \
  "ds_read_b128 v[32:35], v57 offset:32768\n\t" \
  "v_mfma_f32_16x16x32_bf16 v[156:159], v[16:19], v[48:51], v[156:159]\n\t" \
  "s_waitcnt lgkmcnt(8)\n\t" \
  "v_mfma_f32_16x16x32_bf16 v[100:103], v[52:55], v[4:7], v[100:103]\n\t" \
  "ds_read_b64_tr_b16 v[48:49], v60 offset:8192\n\t" \
  "v_mfma_f32_16x16x32_bf16 v[108:111], v[52:55], v[8:11], v[108:111]\n\t" \
  "ds_read_b64_tr_b16 v[50:51], v60 offset:8704\n\t" \
  "v_mfma_f32_16x16x32_bf16 v[116:119], v[52:55], v[12:15], v[116:119]\n\t" \
  "ds_read_b128 v[36:39], v57 offset:34816\n\t" \
  "v_mfma_f32_16x16x32_bf16 v[124:127], v[52:55], v[16:19], v[124:127]\n\t" \
  "s_waitcnt lgkmcnt(9)\n\t" \
  "v_mfma_f32_16x16x32_bf16 v[64:67], v[40:43], v[4:7], v[64:67]\n\t" \
  "ds_read_b64_tr_b16 v[52:53], v60 offset:4096\n\t" \
  "v_mfma_f32_16x16x32_bf16 v[72:75], v[40:43], v[8:11], v[72:75]\n\t" \
  "ds_read_b64_tr_b16 v[54:55], v60 offset:4608\n\t" \
  "v_mfma_f32_16x16x32_bf16 v[80:83], v[40:43], v[12:15], v[80:83]\n\t" \
  "v_mfma_f32_16x16x32_bf16 v[88:91], v[40:43], v[16:19], v[88:91]\n\t" \
  "s_waitcnt lgkmcnt(7)\n\t" \
  "v_mfma_f32_16x16x32_bf16 v[68:71], v[44:47], v[4:7], v[68:71]\n\t" \
  "ds_read_b64_tr_b16 v[40:41], v61 offset:8192\n\t" \
  "v_mfma_f32_16x16x32_bf16 v[76:79], v[44:47], v[8:11], v[76:79]\n\t" \
  "ds_read_b64_tr_b16 v[42:43], v61 offset:8704\n\t" \
  "v_mfma_f32_16x16x32_bf16 v[84:87], v[44:47], v[12:15], v[84:87]\n\t" \
  "v_mfma_f32_16x16x32_bf16 v[92:95], v[44:47], v[16:19], v[92:95]\n\t" \
  "N3b_%=:\n\t" \
  "s_waitcnt lgkmcnt(4)\n\t" \
  "v_mfma_f32_16x16x32_bf16 v[160:163], v[20:23], v[48:51], v[160:163]\n\t" \
  "ds_read_b64_tr_b16 v[44:45], v61 offset:4096\n\t" \
  "v_mfma_f32_16x16x32_bf16 v[128:131], v[24:27], v[48:51], v[128:131]\n\t" \
  "ds_read_b64_tr_b16 v[46:47], v61 offset:4608\n\t" \
  "v_mfma_f32_16x16x32_bf16 v[136:139], v[28:31], v[48:51], v[136:139]\n\t" \
  "s_barrier\n\t" \
  "v_mfma_f32_16x16x32_bf16 v[144:147], v[32:35], v[48:51], v[144:147]\n\t" \
  "v_mfma_f32_16x16x32_bf16 v[152:155], v[36:39], v[48:51], v[152:155]\n\t" \
  "s_waitcnt lgkmcnt(4)\n\t" \
  "v_mfma_f32_16x16x32_bf16 v[96:99], v[52:55], v[24:27], v[96:99]\n\t" \
  "ds_read_b64_tr_b16 v[48:49], v60 offset:0\n\t" \
  "v_mfma_f32_16x16x32_bf16 v[104:107], v[52:55], v[28:31], v[104:107]\n\t" \
  "ds_read_b64_tr_b16 v[50:51], v60 offset:512\n\t" \
  "v_mfma_f32_16x16x32_bf16 v[112:115], v[52:55], v[32:35], v[112:115]\n\t" \
  "ds_read_b128 v[0:3], v56 offset:36864\n\t" \
  "v_mfma_f32_16x16x32_bf16 v[120:123], v[52:55], v[36:39], v[120:123]\n\t" \
  "ds_read_b128 v[4:7], v56 offset:47104\n\t" \
  "s_waitcnt lgkmcnt(6)\n\t" \
  "v_mfma_f32_16x16x32_bf16 v[164:167], v[20:23], v[40:43], v[164:167]\n\t" \
  "ds_read_b64_tr_b16 v[52:53], v61 offset:0\n\t" \
  "v_mfma_f32_16x16x32_bf16 v[132:135], v[24:27], v[40:43], v[132:135]\n\t" \
  "ds_read_b64_tr_b16 v[54:55], v61 offset:512\n\t" \
  "v_mfma_f32_16x16x32_bf16 v[140:143], v[28:31], v[40:43], v[140:143]\n\t" \
  "ds_read_b128 v[8:11], v56 offset:49152\n\t" \
  "v_mfma_f32_16x16x32_bf16 v[148:151], v[32:35], v[40:43], v[148:151]\n\t" \
  "ds_read_b128 v[12:15], v56 offset:51200\n\t" \
  "v_mfma_f32_16x16x32_bf16 v[156:159], v[36:39], v[40:43], v[156:159]\n\t" \
  "s_waitcnt lgkmcnt(8)\n\t" \
  "v_mfma_f32_16x16x32_bf16 v[100:103], v[44:47], v[24:27], v[100:103]\n\t" \
  "ds_read_b64_tr_b16 v[40:41], v60 offset:20480\n\t" \
  "v_mfma_f32_16x16x32_bf16 v[108:111], v[44:47], v[28:31], v[108:111]\n\t" \
  "ds_read_b64_tr_b16 v[42:43], v60 offset:20992\n\t" \
  "v_mfma_f32_16x16x32_bf16 v[116:119], v[44:47], v[32:35], v[116:119]\n\t" \
  "ds_read_b128 v[16:19], v56 offset:53248\n\t" \
  "v_mfma_f32_16x16x32_bf16 v[124:127], v[44:47], v[36:39], v[124:127]\n\t" \
  "s_waitcnt lgkmcnt(9)\n\t" \
  "v_mfma_f32_16x16x32_bf16 v[64:67], v[48:51], v[24:27], v[64:67]\n\t" \
  "ds_read_b64_tr_b16 v[44:45], v60 offset:16384\n\t" \
  "v_mfma_f32_16x16x32_bf16 v[72:75], v[48:51], v[28:31], v[72:75]\n\t" \
  "ds_read_b64_tr_b16 v[46:47], v60 offset:16896\n\t" \
  "v_mfma_f32_16x16x32_bf16 v[80:83], v[48:51], v[32:35], v[80:83]\n\t" \
  "v_mfma_f32_16x16x32_bf16 v[88:91], v[48:51], v[36:39], v[88:91]\n\t" \
  "s_waitcnt lgkmcnt(7)\n\t" \
  "v_mfma_f32_16x16x32_bf16 v[68:71], v[52:55], v[24:27], v[68:71]\n\t" \
  "ds_read_b64_tr_b16 v[48:49], v61 offset:20480\n\t" \
  "v_mfma_f32_16x16x32_bf16 v[76:79], v[52:55], v[28:31], v[76:79]\n\t" \
  "ds_read_b64_tr_b16 v[50:51], v61 offset:20992\n\t" \
  "v_mfma_f32_16x16x32_bf16 v[84:87], v[52:55], v[32:35], v[84:87]\n\t" \
  "v_mfma_f32_16x16x32_bf16 v[92:95], v[52:55], v[36:39], v[92:95]\n\t" \
  "N4b_%=:\n\t" \
  "s_waitcnt lgkmcnt(4)\n\t" \
  "v_mfma_f32_16x16x32_bf16 v[160:163], v[0:3], v[40:43], v[160:163]\n\t" \
  "ds_read_b64_tr_b16 v[52:53], v61 offset:16384\n\t" \
  "v_mfma_f32_16x16x32_bf16 v[128:131], v[4:7], v[40:43], v[128:131]\n\t" \
  "ds_read_b64_tr_b16 v[54:55], v61 offset:16896\n\t" \
  "v_mfma_f32_16x16x32_bf16 v[136:139], v[8:11], v[40:43], v[136:139]\n\t" \
  "s_barrier\n\t" \
  "v_mfma_f32_16x16x32_bf16 v[144:147], v[12:15], v[40:43], v[144:147]\n\t" \
  "v_mfma_f32_16x16x32_bf16 v[152:155], v[16:19], v[40:43], v[152:155]\n\t" \
  "s_waitcnt lgkmcnt(4)\n\t" \
  "v_mfma_f32_16x16x32_bf16 v[96:99], v[44:47], v[4:7], v[96:99]\n\t" \
  "ds_read_b64_tr_b16 v[40:41], v60 offset:12288\n\t" \
  "v_mfma_f32_16x16x32_bf16 v[104:107], v[44:47], v[8:11], v[104:107]\n\t" \
  "ds_read_b64_tr_b16 v[42:43], v60 offset:12800\n\t" \
  "v_mfma_f32_16x16x32_bf16 v[112:115], v[44:47], v[12:15], v[112:115]\n\t" \
  "ds_read_b128 v[20:23], v57 offset:36864\n\t" \
  "v_mfma_f32_16x16x32_bf16 v[120:123], v[44:47], v[16:19], v[120:123]\n\t" \
  "ds_read_b128 v[24:27], v57 offset:47104\n\t" \
  "s_waitcnt lgkmcnt(6)\n\t" \
  "v_mfma_f32_16x16x32_bf16 v[164:167], v[0:3], v[48:51], v[164:167]\n\t" \
  "ds_read_b64_tr_b16 v[44:45], v61 offset:12288\n\t" \
  "v_mfma_f32_16x16x32_bf16 v[132:135], v[4:7], v[48:51], v[132:135]\n\t" \
  "ds_read_b64_tr_b16 v[46:47], v61 offset:12800\n\t" \
  "v_mfma_f32_16x16x32_bf16 v[140:143], v[8:11], v[48:51], v[140:143]\n\t" \
  "ds_read_b128 v[28:31], v57 offset:49152\n\t" \
  "v_mfma_f32_16x16x32_bf16 v[148:151], v[12:15], v[48:51], v[148:151]\n\t" \
  "ds_read_b128 v[32:35], v57 offset:51200\n\t" \
  "v_mfma_f32_16x16x32_bf16 v[156:159], v[16:19], v[48:51], v[156:159]\n\t" \
  "s_waitcnt lgkmcnt(8)\n\t" \
  "v_mfma_f32_16x16x32_bf16 v[100:103], v[52:55], v[4:7], v[100:103]\n\t" \
  "ds_read_b64_tr_b16 v[48:49], v60 offset:32768\n\t" \
  "v_mfma_f32_16x16x32_bf16 v[108:111], v[52:55], v[8:11], v[108:111]\n\t" \
  "ds_read_b64_tr_b16 v[50:51], v60 offset:33280\n\t" \
  "v_mfma_f32_16x16x32_bf16 v[116:119], v[52:55], v[12:15], v[116:119]\n\t" \
  "ds_read_b128 v[36:39], v57 offset:53248\n\t" \
  "v_mfma_f32_16x16x32_bf16 v[124:127], v[52:55], v[16:19], v[124:127]\n\t" \
  "s_waitcnt lgkmcnt(9)\n\t" \
  "v_mfma_f32_16x16x32_bf16 v[64:67], v[40:43], v[4:7], v[64:67]\n\t" \
  "ds_read_b64_tr_b16 v[52:53], v60 offset:28672\n\t" \
  "v_mfma_f32_16x16x32_bf16 v[72:75], v[40:43], v[8:11], v[72:75]\n\t" \
  "ds_read_b64_tr_b16 v[54:55], v60 offset:29184\n\t" \
  "v_mfma_f32_16x16x32_bf16 v[80:83], v[40:43], v[12:15], v[80:83]\n\t" \
  "v_mfma_f32_16x16x32_bf16 v[88:91], v[40:43], v[16:19], v[88:91]\n\t" \
  "s_waitcnt lgkmcnt(7)\n\t" \
  "v_mfma_f32_16x16x32_bf16 v[68:71], v[44:47], v[4:7], v[68:71]\n\t" \
  "ds_read_b64_tr_b16 v[40:41], v61 offset:32768\n\t" \
  "v_mfma_f32_16x16x32_bf16 v[76:79], v[44:47], v[8:11], v[76:79]\n\t" \
  "ds_read_b64_tr_b16 v[42:43], v61 offset:33280\n\t" \
  "v_mfma_f32_16x16x32_bf16 v[84:87], v[44:47], v[12:15], v[84:87]\n\t" \
  "v_mfma_f32_16x16x32_bf16 v[92:95], v[44:47], v[16:19], v[92:95]\n\t" \
  "N5b_%=:\n\t" \
  "s_waitcnt lgkmcnt(4)\n\t" \
  "v_mfma_f32_16x16x32_bf16 v[160:163], v[20:23], v[48:51], v[160:163]\n\t" \
  "ds_read_b64_tr_b16 v[44:45], v61 offset:28672\n\t" \
  "v_mfma_f32_16x16x32_bf16 v[128:131], v[24:27], v[48:51], v[128:131]\n\t" \
  "ds_read_b64_tr_b16 v[46:47], v61 offset:29184\n\t" \
  "v_mfma_f32_16x16x32_bf16 v[136:139], v[28:31], v[48:51], v[136:139]\n\t" \
  "s_barrier\n\t" \
  "v_mfma_f32_16x16x32_bf16 v[144:147], v[32:35], v[48:51], v[144:147]\n\t" \
  "v_mfma_f32_16x16x32_bf16 v[152:155], v[36:39], v[48:51], v[152:155]\n\t" \
  "s_waitcnt lgkmcnt(4)\n\t" \
  "v_mfma_f32_16x16x32_bf16 v[96:99], v[52:55], v[24:27], v[96:99]\n\t" \
  "ds_read_b64_tr_b16 v[48:49], v60 offset:24576\n\t" \
  "v_mfma_f32_16x16x32_bf16 v[104:107], v[52:55], v[28:31], v[104:107]\n\t" \
  "ds_read_b64_tr_b16 v[50:51], v60 offset:25088\n\t" \
  "v_mfma_f32_16x16x32_bf16 v[112:115], v[52:55], v[32:35], v[112:115]\n\t" \
  "ds_read_b128 v[0:3], v56 offset:0\n\t" \
  "v_mfma_f32_16x16x32_bf16 v[120:123], v[52:55], v[36:39], v[120:123]\n\t" \
  "ds_read_b128 v[4:7], v56 offset:10240\n\t" \
  "s_waitcnt lgkmcnt(6)\n\t" \
  "v_mfma_f32_16x16x32_bf16 v[164:167], v[20:23], v[40:43], v[164:167]\n\t" \
  "ds_read_b64_tr_b16 v[52:53], v61 offset:24576\n\t" \
  "v_mfma_f32_16x16x32_bf16 v[132:135], v[24:27], v[40:43], v[132:135]\n\t" \
  "ds_read_b64_tr_b16 v[54:55], v61 offset:25088\n\t" \
  "v_mfma_f32_16x16x32_bf16 v[140:143], v[28:31], v[40:43], v[140:143]\n\t" \
  "ds_read_b128 v[8:11], v56 offset:12288\n\t" \
  "v_mfma_f32_16x16x32_bf16 v[148:151], v[32:35], v[40:43], v[148:151]\n\t" \
  "ds_read_b128 v[12:15], v56 offset:14336\n\t" \
  "v_mfma_f32_16x16x32_bf16 v[156:159], v[36:39], v[40:43], v[156:159]\n\t" \
  "s_waitcnt lgkmcnt(8)\n\t" \
  "v_mfma_f32_16x16x32_bf16 v[100:103], v[44:47], v[24:27], v[100:103]\n\t" \
  "ds_read_b64_tr_b16 v[40:41], v58 offset:8192\n\t" \
  "v_mfma_f32_16x16x32_bf16 v[108:111], v[44:47], v[28:31], v[108:111]\n\t" \
  "ds_read_b64_tr_b16 v[42:43], v58 offset:8704\n\t" \
  "v_mfma_f32_16x16x32_bf16 v[116:119], v[44:47], v[32:35], v[116:119]\n\t" \
  "ds_read_b128 v[16:19], v56 offset:16384\n\t" \
  "v_mfma_f32_16x16x32_bf16 v[124:127], v[44:47], v[36:39], v[124:127]\n\t" \
  "s_waitcnt lgkmcnt(9)\n\t" \
  "v_mfma_f32_16x16x32_bf16 v[64:67], v[48:51], v[24:27], v[64:67]\n\t" \
  "ds_read_b64_tr_b16 v[44:45], v58 offset:4096\n\t" \
  "v_mfma_f32_16x16x32_bf16 v[72:75], v[48:51], v[28:31], v[72:75]\n\t" \
  "ds_read_b64_tr_b16 v[46:47], v58 offset:4608\n\t" \
  "v_mfma_f32_16x16x32_bf16 v[80:83], v[48:51], v[32:35], v[80:83]\n\t" \
  "v_mfma_f32_16x16x32_bf16 v[88:91], v[48:51], v[36:39], v[88:91]\n\t" \
  "s_waitcnt lgkmcnt(7)\n\t" \
  "v_mfma_f32_16x16x32_bf16 v[68:71], v[52:55], v[24:27], v[68:71]\n\t" \
  "ds_read_b64_tr_b16 v[48:49], v59 offset:8192\n\t" \
  "v_mfma_f32_16x16x32_bf16 v[76:79], v[52:55], v[28:31], v[76:79]\n\t" \
  "ds_read_b64_tr_b16 v[50:51], v59 offset:8704\n\t" \
  "s_addk_i32 %[it], 1\n\t" \
  "v_mfma_f32_16x16x32_bf16 v[84:87], v[52:55], v[32:35], v[84:87]\n\t" \
  "s_cmp_lt_u32 %[it], 6\n\t" \
  "v_mfma_f32_16x16x32_bf16 v[92:95], v[52:55], v[36:39], v[92:95]\n\t" \
  "s_cbranch_scc1 N0b_%=\n\t" \
  "s_setprio 3\n\t" \
  "v_cvt_pk_bf16_f32 v160, v160, v161\n\t" \
  "v_cvt_pk_bf16_f32 v161, v162, v163\n\t" \
  "v_cvt_pk_bf16_f32 v162, v164, v165\n\t" \
  "v_cvt_pk_bf16_f32 v163, v166, v167\n\t" \
  "ds_write_b128 v63, v[160:163] offset:2048\n\t" \
  "v_mov_b32 v57, %[fa]\n\t" \
  "v_mov_b32 v58, 1\n\t" \
  "s_mov_b64 exec, 1\n\t" \
  "ds_write_b32 v57, v58\n\t" \
  "s_mov_b64 exec, -1\n\t" \
  "v_cvt_pk_bf16_f32 v120, v120, v121\n\t" \
  "v_cvt_pk_bf16_f32 v121, v122, v123\n\t" \
  "v_cvt_pk_bf16_f32 v122, v124, v125\n\t" \
  "v_cvt_pk_bf16_f32 v123, v126, v127\n\t" \
  "v_cvt_pk_bf16_f32 v28, v152, v153\n\t" \
  "v_cvt_pk_bf16_f32 v29, v154, v155\n\t" \
  "v_cvt_pk_bf16_f32 v38, v156, v157\n\t" \
  "v_cvt_pk_bf16_f32 v39, v158, v159\n\t" \
  "NX_%=:\n\t" \
  "v_cvt_pk_bf16_f32 v96, v96, v97\n\t" \
  "v_cvt_pk_bf16_f32 v97, v98, v99\n\t" \
  "v_cvt_pk_bf16_f32 v98, v100, v101\n\t" \
  "v_cvt_pk_bf16_f32 v99, v102, v103\n\t" \
  "v_cvt_pk_bf16_f32 v104, v104, v105\n\t" \
  "v_cvt_pk_bf16_f32 v105, v106, v107\n\t" \
  "v_cvt_pk_bf16_f32 v106, v108, v109\n\t" \
  "v_cvt_pk_bf16_f32 v107, v110, v111\n\t" \
  "v_mov_b32 v60, 0xff61b1e6\n\t" \
  "v_cndmask_b32_e64 v56, v60, 0, s[68:69]\n\t" \
  "v_cndmask_b32_e64 v57, v60, 0, s[70:71]\n\t" \
  "v_cndmask_b32_e64 v58, v60, 0, s[72:73]\n\t" \
  "v_cndmask_b32_e64 v59, v60, 0, s[74:75]\n\t" \
  "v_cvt_pk_bf16_f32 v72, v72, v73\n\t" \
  "v_cvt_pk_bf16_f32 v73, v74, v75\n\t" \
  "v_cvt_pk_bf16_f32 v74, v76, v77\n\t" \
  "v_cvt_pk_bf16_f32 v75, v78, v79\n\t" \
  "v_cndmask_b32_e64 v52, v60, 0, s[76:77]\n\t" \
  "v_cndmask_b32_e64 v53, v60, 0, s[78:79]\n\t" \
  "v_cndmask_b32_e64 v54, v60, 0, s[80:81]\n\t" \
  "v_cndmask_b32_e64 v55, v60, 0, s[82:83]\n\t" \
  "v_cvt_pk_bf16_f32 v24, v136, v137\n\t" \
  "v_cvt_pk_bf16_f32 v25, v138, v139\n\t" \
  "v_cvt_pk_bf16_f32 v34, v140, v141\n\t" \
  "v_cvt_pk_bf16_f32 v35, v142, v143\n\t" \
  "v_cvt_pk_bf16_f32 v26, v144, v145\n\t" \
  "v_cvt_pk_bf16_f32 v27, v146, v147\n\t" \
  "v_cvt_pk_bf16_f32 v36, v148, v149\n\t" \
  "v_cvt_pk_bf16_f32 v37, v150, v151\n\t" \
  "v_mfma_f32_16x16x32_bf16 v[136:139], v[96:99], v[72:75], v[56:59]\n\t" \
  "v_cvt_pk_bf16_f32 v112, v112, v113\n\t" \
  "v_cvt_pk_bf16_f32 v113, v114, v115\n\t" \
  "v_mfma_f32_16x16x32_bf16 v[140:143], v[104:107], v[72:75], v[52:55]\n\t" \
  "v_cvt_pk_bf16_f32 v114, v116, v117\n\t" \
  "v_cvt_pk_bf16_f32 v115, v118, v119\n\t" \
  "v_cvt_pk_bf16_f32 v80, v80, v81\n\t" \
  "v_cvt_pk_bf16_f32 v81, v82, v83\n\t" \
  "v_cvt_pk_bf16_f32 v82, v84, v85\n\t" \
  "v_cvt_pk_bf16_f32 v83, v86, v87\n\t" \
  "v_cvt_pk_bf16_f32 v22, v128, v129\n\t" \
  "v_cvt_pk_bf16_f32 v23, v130, v131\n\t" \
  "v_mfma_f32_16x16x32_bf16 v[144:147], v[104:107], v[80:83], v[56:59]\n\t" \
  "v_cvt_pk_bf16_f32 v32, v132, v133\n\t" \
  "v_cvt_pk_bf16_f32 v33, v134, v135\n\t" \
  "v_mfma_f32_16x16x32_bf16 v[148:151], v[112:115], v[80:83], v[52:55]\n\t" \
  "v_cvt_pk_bf16_f32 v88, v88, v89\n\t" \
  "v_cvt_pk_bf16_f32 v89, v90, v91\n\t" \
  "v_cvt_pk_bf16_f32 v90, v92, v93\n\t" \
  "v_cvt_pk_bf16_f32 v91, v94, v95\n\t" \
  "v_mov_b32 v61, %[fp]\n\t" \
  "v_add_u32 v60, %[vo], v63\n\t" \
  "v_mfma_f32_16x16x32_bf16 v[152:155], v[112:115], v[88:91], v[56:59]\n\t" \
  "ds_read_b32 v0, v61\n\t" \
  "ds_read_b64 v[20:21], v60\n\t" \
  "ds_read_b64 v[30:31], v60 offset:8\n\t" \
  "s_mov_b64 exec, %[rm]\n\t" \
  "ds_read_b128 v[160:163], v63\n\t" \
  "s_mov_b64 exec, -1\n\t" \
  "v_mfma_f32_16x16x32_bf16 v[156:159], v[120:123], v[88:91], v[52:55]\n\t" \
  "v_cvt_pk_bf16_f32 v64, v64, v65\n\t" \
  "v_cvt_pk_bf16_f32 v65, v66, v67\n\t" \
  "v_cvt_pk_bf16_f32 v66, v68, v69\n\t" \
  "v_cvt_pk_bf16_f32 v67, v70, v71\n\t" \
  "v_max_f32 v136, v136, v137\n\t" \
  "v_max_f32 v138, v138, v139\n\t" \
  "v_mfma_f32_16x16x32_bf16 v[132:135], v[96:99], v[64:67], v[52:55]\n\t" \
  "v_max_f32 v140, v140, v141\n\t" \
  "v_max_f32 v142, v142, v143\n\t" \
  "AW_%=:\n\t" \
  "s_waitcnt lgkmcnt(0)\n\t" \
  "v_cmp_eq_u32 vcc, 0, v0\n\t" \
  "v_max3_f32 v5, v136, v138, v140\n\t" \
  "v_max_f32 v5, v5, v142\n\t" \
  "s_cbranch_vccnz AR_%=\n\t" \
  "s_cmp_eq_u32 %[z], 0\n\t" \
  "s_cbranch_scc1 AZ_%=\n\t" \
  "AY_%=:\n\t" \
  "v_mfma_f32_16x16x32_bf16 v[128:131], v[160:163], v[64:67], v[56:59]\n\t" \
  "v_max_f32 v144, v144, v145\n\t" \
  "v_max_f32 v146, v146, v147\n\t" \
  "v_max_f32 v148, v148, v149\n\t" \
  "v_max_f32 v150, v150, v151\n\t" \
  "v_max3_f32 v6, v144, v146, v148\n\t" \
  "v_max_f32 v6, v6, v150\n\t" \
  "v_max_f32 v152, v152, v153\n\t" \
  "v_max_f32 v154, v154, v155\n\t" \
  "v_max_f32 v156, v156, v157\n\t" \
  "v_max_f32 v158, v158, v159\n\t" \
  "v_max3_f32 v7, v152, v154, v156\n\t" \
  "v_max_f32 v7, v7, v158\n\t" \
  "v_mov_b32 v8, 0xc0c0100\n\t" \
  "v_mov_b32 v9, 0x1000c0c\n\t" \
  "v_max_f32 v128, v128, v129\n\t" \
  "v_max_f32 v130, v130, v131\n\t" \
  "v_max_f32 v132, v132, v133\n\t" \
  "v_max_f32 v134, v134, v135\n\t" \
  "v_max3_f32 v4, v128, v130, v132\n\t" \
  "v_max_f32 v4, v4, v134\n\t" \
  "v_cndmask_b32_e64 v60, v8, v9, s[84:85]\n\t" \
  "v_mov_b32 v8, 0xc0c0302\n\t" \
  "v_permlane16_swap_b32 v6, v7\n\t" \
  "v_permlane16_swap_b32 v4, v5\n\t" \
  "v_max_f32 v6, v6, v7\n\t" \
  "v_max_f32 v4, v4, v5\n\t" \
  "v_mov_b32 v9, 0x3020c0c\n\t" \
  "v_cndmask_b32_e64 v61, v8, v9, s[84:85]\n\t" \
  "v_permlane32_swap_b32 v4, v6\n\t" \
  "v_max_f32 v4, v4, v6\n\t" \
  "v_mul_f32 v6, 0xbe8293ee, v4\n\t" \
  "v_mul_f32 v4, 0xbe8293ee, v4\n\t" \
  "s_nop 1\n\t" \
  "v_permlane32_swap_b32 v4, v6\n\t" \
  "v_mov_b32 v5, v4\n\t" \
  "v_mov_b32 v7, v6\n\t" \
  "s_nop 0\n\t" \
  "v_permlane16_swap_b32 v4, v5\n\t" \
  "v_permlane16_swap_b32 v6, v7\n\t" \
  "v_fmamk_f32 v128, v128, 0x3e8293ee, v4\n\t" \
  "v_fmamk_f32 v130, v130, 0x3e8293ee, v4\n\t" \
  "v_fmamk_f32 v132, v132, 0x3e8293ee, v4\n\t" \
  "v_fmamk_f32 v134, v134, 0x3e8293ee, v4\n\t" \
  "v_fmamk_f32 v136, v136, 0x3e8293ee, v5\n\t" \
  "v_fmamk_f32 v138, v138, 0x3e8293ee, v5\n\t" \
  "v_fmamk_f32 v140, v140, 0x3e8293ee, v5\n\t" \
  "v_fmamk_f32 v142, v142, 0x3e8293ee, v5\n\t" \
  "v_fmamk_f32 v144, v144, 0x3e8293ee, v6\n\t" \
  "v_fmamk_f32 v146, v146, 0x3e8293ee, v6\n\t" \
  "v_fmamk_f32 v148, v148, 0x3e8293ee, v6\n\t" \
  "v_fmamk_f32 v150, v150, 0x3e8293ee, v6\n\t" \
  "v_fmamk_f32 v152, v152, 0x3e8293ee, v7\n\t" \
  "v_fmamk_f32 v154, v154, 0x3e8293ee, v7\n\t" \
  "v_fmamk_f32 v156, v156, 0x3e8293ee, v7\n\t" \
  "v_fmamk_f32 v158, v158, 0x3e8293ee, v7\n\t" \
  "v_exp_f32 v128, v128\n\t" \
  "v_exp_f32 v130, v130\n\t" \
  "v_exp_f32 v132, v132\n\t" \
  "v_exp_f32 v134, v134\n\t" \
  "v_exp_f32 v136, v136\n\t" \
  "v_exp_f32 v138, v138\n\t" \
  "v_exp_f32 v140, v140\n\t" \
  "v_exp_f32 v142, v142\n\t" \
  "v_exp_f32 v144, v144\n\t" \
  "v_exp_f32 v146, v146\n\t" \
  "v_exp_f32 v148, v148\n\t" \
  "v_exp_f32 v150, v150\n\t" \
  "v_exp_f32 v152, v152\n\t" \
  "v_exp_f32 v154, v154\n\t" \
  "v_exp_f32 v156, v156\n\t" \
  "v_exp_f32 v158, v158\n\t" \
  "v_add_f32 v4, v128, v130\n\t" \
  "v_add_f32 v8, v132, v134\n\t" \
  "v_add_f32 v5, v136, v138\n\t" \
  "v_add_f32 v9, v140, v142\n\t" \
  "v_add_f32 v6, v144, v146\n\t" \
  "v_add_f32 v10, v148, v150\n\t" \
  "v_add_f32 v7, v152, v154\n\t" \
  "v_add_f32 v11, v156, v158\n\t" \
  "v_add_f32 v4, v4, v8\n\t" \
  "v_add_f32 v5, v5, v9\n\t" \
  "v_add_f32 v6, v6, v10\n\t" \
  "v_add_f32 v7, v7, v11\n\t" \
  "v_permlane16_swap_b32 v4, v5\n\t" \
  "s_nop 0\n\t" \
  "v_permlane16_swap_b32 v6, v7\n\t" \
  "v_add_f32 v4, v4, v5\n\t" \
  "v_add_f32 v6, v6, v7\n\t" \
  "s_nop 1\n\t" \
  "v_permlane32_swap_b32 v4, v6\n\t" \
  "v_add_f32 v4, v4, v6\n\t" \
  "v_rcp_f32 v4, v4\n\t" \
  "s_nop 0\n\t" \
  "v_mov_b32 v6, v4\n\t" \
  "s_nop 1\n\t" \
  "v_permlane32_swap_b32 v4, v6\n\t" \
  "v_mov_b32 v5, v4\n\t" \
  "v_mov_b32 v7, v6\n\t" \
  "s_nop 0\n\t" \
  "v_permlane16_swap_b32 v4, v5\n\t" \
  "v_permlane16_swap_b32 v6, v7\n\t" \
  "v_mul_f32 v128, v128, v4\n\t" \
  "v_mul_f32 v130, v130, v4\n\t" \
  "v_mul_f32 v132, v132, v4\n\t" \
  "v_mul_f32 v134, v134, v4\n\t" \
  "v_cvt_pk_bf16_f32 v128, v128, v130\n\t" \
  "v_cvt_pk_bf16_f32 v132, v132, v134\n\t" \
  "v_perm_b32 v68, 0, v128, v60\n\t" \
  "v_perm_b32 v69, 0, v128, v61\n\t" \
  "v_perm_b32 v70, 0, v132, v60\n\t" \
  "v_perm_b32 v71, 0, v132, v61\n\t" \
  "v_mul_f32 v136, v136, v5\n\t" \
  "v_mul_f32 v138, v138, v5\n\t" \
  "v_mfma_f32_16x16x32_bf16 v[128:131], v[20:23], v[68:71], 0\n\t" \
  "v_mul_f32 v140, v140, v5\n\t" \
  "v_mul_f32 v142, v142, v5\n\t" \
  "v_cvt_pk_bf16_f32 v136, v136, v138\n\t" \
  "v_mfma_f32_16x16x32_bf16 v[132:135], v[30:33], v[68:71], 0\n\t" \
  "v_cvt_pk_bf16_f32 v140, v140, v142\n\t" \
  "v_perm_b32 v76, 0, v136, v60\n\t" \
  "v_perm_b32 v77, 0, v136, v61\n\t" \
  "v_perm_b32 v78, 0, v140, v60\n\t" \
  "v_perm_b32 v79, 0, v140, v61\n\t" \
  "v_mul_f32 v144, v144, v6\n\t" \
  "v_mul_f32 v146, v146, v6\n\t" \
  "v_mfma_f32_16x16x32_bf16 v[136:139], v[22:25], v[76:79], 0\n\t" \
  "v_mul_f32 v148, v148, v6\n\t" \
  "v_mul_f32 v150, v150, v6\n\t" \
  "v_cvt_pk_bf16_f32 v144, v144, v146\n\t" \
  "v_mfma_f32_16x16x32_bf16 v[140:143], v[32:35], v[76:79], 0\n\t" \
  "v_cvt_pk_bf16_f32 v148, v148, v150\n\t" \
  "v_perm_b32 v84, 0, v144, v60\n\t" \
  "v_perm_b32 v85, 0, v144, v61\n\t" \
  "v_perm_b32 v86, 0, v148, v60\n\t" \
  "v_perm_b32 v87, 0, v148, v61\n\t" \
  "v_cvt_pk_bf16_f32 v100, v128, v129\n\t" \
  "v_cvt_pk_bf16_f32 v101, v130, v131\n\t" \
  "v_mfma_f32_16x16x32_bf16 v[144:147], v[24:27], v[84:87], 0\n\t" \
  "v_cvt_pk_bf16_f32 v102, v132, v133\n\t" \
  "v_cvt_pk_bf16_f32 v103, v134, v135\n\t" \
  "global_store_dwordx4 v62, v[100:103], %[ot] offset:0 sc1\n\t" \
  "v_mfma_f32_16x16x32_bf16 v[148:151], v[34:37], v[84:87], 0\n\t" \
  "v_mul_f32 v152, v152, v7\n\t" \
  "v_mul_f32 v154, v154, v7\n\t" \
  "v_mul_f32 v156, v156, v7\n\t" \
  "v_mul_f32 v158, v158, v7\n\t" \
  "v_cvt_pk_bf16_f32 v152, v152, v154\n\t" \
  "v_cvt_pk_bf16_f32 v156, v156, v158\n\t" \
  "v_perm_b32 v92, 0, v152, v60\n\t" \
  "v_perm_b32 v93, 0, v152, v61\n\t" \
  "v_perm_b32 v94, 0, v156, v60\n\t" \
  "v_perm_b32 v95, 0, v156, v61\n\t" \
  "v_cvt_pk_bf16_f32 v108, v136, v137\n\t" \
  "v_cvt_pk_bf16_f32 v109, v138, v139\n\t" \
  "v_mfma_f32_16x16x32_bf16 v[152:155], v[26:29], v[92:95], 0\n\t" \
  "v_cvt_pk_bf16_f32 v110, v140, v141\n\t" \
  "v_cvt_pk_bf16_f32 v111, v142, v143\n\t" \
  "global_store_dwordx4 v62, v[108:111], %[ot] offset:1024 sc1\n\t" \
  "v_mfma_f32_16x16x32_bf16 v[156:159], v[36:39], v[92:95], 0\n\t" \
  "v_cvt_pk_bf16_f32 v116, v144, v145\n\t" \
  "v_cvt_pk_bf16_f32 v117, v146, v147\n\t" \
  "v_cvt_pk_bf16_f32 v118, v148, v149\n\t" \
  "v_cvt_pk_bf16_f32 v119, v150, v151\n\t" \
  "global_store_dwordx4 v62, v[116:119], %[ou] offset:0 sc1\n\t" \
  "v_cvt_pk_bf16_f32 v124, v152, v153\n\t" \
  "v_cvt_pk_bf16_f32 v125, v154, v155\n\t" \
  "s_nop 0\n\t" \
  "v_cvt_pk_bf16_f32 v126, v156, v157\n\t" \
  "v_cvt_pk_bf16_f32 v127, v158, v159\n\t" \
  "global_store_dwordx4 v62, v[124:127], %[ou] offset:1024 sc1\n\t" \
  "s_branch AE_%=\n\t" \
  "AR_%=:\n\t" \
  "s_addk_i32 %[it], 1\n\t" \
  "s_cmp_gt_u32 %[it], 0x400000\n\t" \
  "s_cbranch_scc1 AY_%=\n\t" \
  "ds_read_b32 v0, v61\n\t" \
  "ds_read_b64 v[20:21], v60\n\t" \
  "ds_read_b64 v[30:31], v60 offset:8\n\t" \
  "s_mov_b64 exec, %[rm]\n\t" \
  "ds_read_b128 v[160:163], v63\n\t" \
  "s_mov_b64 exec, -1\n\t" \
  "s_branch AW_%=\n\t" \
  "AZ_%=:\n\t" \
  "v_mov_b32 v0, 0xff61b1e6\n\t" \
  "v_mov_b32 v1, 0xcfa8965f\n\t" \
  "s_nop 3\n\t" \
  "v_cndmask_b32_e64 v56, v0, v1, s[68:69]\n\t" \
  "v_cndmask_b32_e64 v57, v0, v1, s[70:71]\n\t" \
  "v_cndmask_b32_e64 v58, v0, v1, s[72:73]\n\t" \
  "v_cndmask_b32_e64 v59, v0, v1, s[74:75]\n\t" \
  "s_nop 1\n\t" \
  "s_branch AY_%=\n\t" \
  "AE_%=:\n\t" \
  "s_nop 1\n\t"
#define N2_CLOB "v0", "v1", "v2", "v3", "v4", "v5", "v6", "v7", "v8", "v9", "v10", "v11", "v12", "v13", "v14", "v15", "v16", "v17", "v18", "v19", "v20", "v21", "v22", "v23", "v24", "v25", "v26", "v27", "v28", "v29", "v30", "v31", "v32", "v33", "v34", "v35", "v36", "v37", "v38", "v39", "v40", "v41", "v42", "v43", "v44", "v45", "v46", "v47", "v48", "v49", "v50", "v51", "v52", "v53", "v54", "v55", "v60", "v61", "v64", "v65", "v66", "v67", "v68", "v69", "v70", "v71", "v72", "v73", "v74", "v75", "v76", "v77", "v78", "v79", "v80", "v81", "v82", "v83", "v84", "v85", "v86", "v87", "v88", "v89", "v90", "v91", "v92", "v93", "v94", "v95", "v96", "v97", "v98", "v99", "v100", "v101", "v102", "v103", "v104", "v105", "v106", "v107", "v108", "v109", "v110", "v111", "v112", "v113", "v114", "v115", "v116", "v117", "v118", "v119", "v120", "v121", "v122", "v123", "v124", "v125", "v126", "v127", "v128", "v129", "v130", "v131", "v132", "v133", "v134", "v135", "v136", "v137", "v138", "v139", "v140", "v141", "v142", "v143", "v144", "v145", "v146", "v147", "v148", "v149", "v150", "v151", "v152", "v153", "v154", "v155", "v156", "v157", "v158", "v159", "v160", "v161", "v162", "v163", "v164", "v165", "v166", "v167", "s68", "s69", "s70", "s71", "s72", "s73", "s74", "s75", "s76", "s77", "s78", "s79", "s80", "s81", "s82", "s83", "s84", "s85"

#define CV_ASM \
  "s_mov_b64 vcc, 1\n\t" \
  "ds_read_b64 v[0:1], v28 offset:0\n\t" \
  "ds_read_b64 v[2:3], v29 offset:0\n\t" \
  "ds_read_b64 v[4:5], v30 offset:1024\n\t" \
  "ds_read_b64 v[6:7], v31 offset:1024\n\t" \
  "ds_read_b64 v[8:9], v24 offset:2048\n\t" \
  "ds_read_b64 v[10:11], v25 offset:2048\n\t" \
  "ds_read_b64 v[12:13], v26 offset:3072\n\t" \
  "ds_read_b64 v[14:15], v27 offset:3072\n\t" \
  "ds_read_b64 v[16:17], v28 offset:4096\n\t" \
  "ds_read_b64 v[18:19], v29 offset:4096\n\t" \
  "ds_read_b64 v[20:21], v30 offset:5120\n\t" \
  "ds_read_b64 v[22:23], v31 offset:5120\n\t" \
  "s_waitcnt lgkmcnt(0)\n\t" \
  "v_mfma_f32_16x16x32_bf16 v[128:131], v[68:71], v[0:3], 0\n\t" \
  "v_mfma_f32_16x16x32_bf16 v[128:131], v[64:67], v[4:7], v[128:131]\n\t" \
  "v_mfma_f32_16x16x32_bf16 v[132:135], v[76:79], v[0:3], 0\n\t" \
  "v_mfma_f32_16x16x32_bf16 v[132:135], v[72:75], v[4:7], v[132:135]\n\t" \
  "v_mfma_f32_16x16x32_bf16 v[136:139], v[84:87], v[0:3], 0\n\t" \
  "v_mfma_f32_16x16x32_bf16 v[136:139], v[80:83], v[4:7], v[136:139]\n\t" \
  "v_mfma_f32_16x16x32_bf16 v[140:143], v[92:95], v[0:3], 0\n\t" \
  "v_mfma_f32_16x16x32_bf16 v[140:143], v[88:91], v[4:7], v[140:143]\n\t" \
  "v_mfma_f32_16x16x32_bf16 v[144:147], v[100:103], v[0:3], 0\n\t" \
  "v_mfma_f32_16x16x32_bf16 v[144:147], v[96:99], v[4:7], v[144:147]\n\t" \
  "ds_bpermute_b32 v34, v32, v131\n\t" \
  "v_mfma_f32_16x16x32_bf16 v[148:151], v[108:111], v[0:3], 0\n\t" \
  "v_mov_b32 v131, v130\n\t" \
  "v_mov_b32 v130, v129\n\t" \
  "v_mfma_f32_16x16x32_bf16 v[148:151], v[104:107], v[4:7], v[148:151]\n\t" \
  "v_mov_b32 v129, v128\n\t" \
  "v_mfma_f32_16x16x32_bf16 v[152:155], v[116:119], v[0:3], 0\n\t" \
  "v_mfma_f32_16x16x32_bf16 v[152:155], v[112:115], v[4:7], v[152:155]\n\t" \
  "v_mfma_f32_16x16x32_bf16 v[156:159], v[124:127], v[0:3], 0\n\t" \
  "ds_read_b64 v[0:1], v24 offset:6144\n\t" \
  "ds_read_b64 v[2:3], v25 offset:6144\n\t" \
  "v_mfma_f32_16x16x32_bf16 v[156:159], v[120:123], v[4:7], v[156:159]\n\t" \
  "ds_read_b64 v[4:5], v26 offset:7168\n\t" \
  "ds_read_b64 v[6:7], v27 offset:7168\n\t" \
  "v_mfma_f32_16x16x32_bf16 v[132:135], v[68:71], v[8:11], v[132:135]\n\t" \
  "v_mfma_f32_16x16x32_bf16 v[132:135], v[64:67], v[12:15], v[132:135]\n\t" \
  "v_mfma_f32_16x16x32_bf16 v[136:139], v[76:79], v[8:11], v[136:139]\n\t" \
  "v_mfma_f32_16x16x32_bf16 v[136:139], v[72:75], v[12:15], v[136:139]\n\t" \
  "v_mfma_f32_16x16x32_bf16 v[140:143], v[84:87], v[8:11], v[140:143]\n\t" \
  "s_waitcnt lgkmcnt(4)\n\t" \
  "v_cndmask_b32_e64 v128, v34, 0, vcc\n\t" \
  "v_mfma_f32_16x16x32_bf16 v[140:143], v[80:83], v[12:15], v[140:143]\n\t" \
  "v_mfma_f32_16x16x32_bf16 v[144:147], v[92:95], v[8:11], v[144:147]\n\t" \
  "v_mfma_f32_16x16x32_bf16 v[144:147], v[88:91], v[12:15], v[144:147]\n\t" \
  "ds_bpermute_b32 v35, v32, v135\n\t" \
  "v_mfma_f32_16x16x32_bf16 v[148:151], v[100:103], v[8:11], v[148:151]\n\t" \
  "v_mov_b32 v135, v134\n\t" \
  "v_mov_b32 v134, v133\n\t" \
  "v_mfma_f32_16x16x32_bf16 v[148:151], v[96:99], v[12:15], v[148:151]\n\t" \
  "v_mov_b32 v133, v132\n\t" \
  "v_mfma_f32_16x16x32_bf16 v[152:155], v[108:111], v[8:11], v[152:155]\n\t" \
  "v_mfma_f32_16x16x32_bf16 v[152:155], v[104:107], v[12:15], v[152:155]\n\t" \
  "v_mfma_f32_16x16x32_bf16 v[156:159], v[116:119], v[8:11], v[156:159]\n\t" \
  "ds_read_b64 v[8:9], v28 offset:8192\n\t" \
  "ds_read_b64 v[10:11], v29 offset:8192\n\t" \
  "v_mfma_f32_16x16x32_bf16 v[156:159], v[112:115], v[12:15], v[156:159]\n\t" \
  "ds_read_b64 v[12:13], v30 offset:9216\n\t" \
  "ds_read_b64 v[14:15], v31 offset:9216\n\t" \
  "v_mfma_f32_16x16x32_bf16 v[136:139], v[68:71], v[16:19], v[136:139]\n\t" \
  "v_mfma_f32_16x16x32_bf16 v[136:139], v[64:67], v[20:23], v[136:139]\n\t" \
  "v_mfma_f32_16x16x32_bf16 v[140:143], v[76:79], v[16:19], v[140:143]\n\t" \
  "v_mfma_f32_16x16x32_bf16 v[140:143], v[72:75], v[20:23], v[140:143]\n\t" \
  "v_mfma_f32_16x16x32_bf16 v[144:147], v[84:87], v[16:19], v[144:147]\n\t" \
  "s_waitcnt lgkmcnt(4)\n\t" \
  "v_cndmask_b32_e64 v132, v35, v34, vcc\n\t" \
  "v_mfma_f32_16x16x32_bf16 v[144:147], v[80:83], v[20:23], v[144:147]\n\t" \
  "v_mfma_f32_16x16x32_bf16 v[148:151], v[92:95], v[16:19], v[148:151]\n\t" \
  "v_mfma_f32_16x16x32_bf16 v[148:151], v[88:91], v[20:23], v[148:151]\n\t" \
  "ds_bpermute_b32 v36, v32, v139\n\t" \
  "v_mfma_f32_16x16x32_bf16 v[152:155], v[100:103], v[16:19], v[152:155]\n\t" \
  "v_mov_b32 v139, v138\n\t" \
  "v_mov_b32 v138, v137\n\t" \
  "v_mfma_f32_16x16x32_bf16 v[152:155], v[96:99], v[20:23], v[152:155]\n\t" \
  "v_mov_b32 v137, v136\n\t" \
  "v_mfma_f32_16x16x32_bf16 v[156:159], v[108:111], v[16:19], v[156:159]\n\t" \
  "ds_read_b64 v[16:17], v24 offset:10240\n\t" \
  "ds_read_b64 v[18:19], v25 offset:10240\n\t" \
  "v_mfma_f32_16x16x32_bf16 v[156:159], v[104:107], v[20:23], v[156:159]\n\t" \
  "ds_read_b64 v[20:21], v26 offset:11264\n\t" \
  "ds_read_b64 v[22:23], v27 offset:11264\n\t" \
  "v_mfma_f32_16x16x32_bf16 v[140:143], v[68:71], v[0:3], v[140:143]\n\t" \
  "v_mfma_f32_16x16x32_bf16 v[140:143], v[64:67], v[4:7], v[140:143]\n\t" \
  "v_mfma_f32_16x16x32_bf16 v[144:147], v[76:79], v[0:3], v[144:147]\n\t" \
  "v_mfma_f32_16x16x32_bf16 v[144:147], v[72:75], v[4:7], v[144:147]\n\t" \
  "v_mfma_f32_16x16x32_bf16 v[148:151], v[84:87], v[0:3], v[148:151]\n\t" \
  "v_mfma_f32_16x16x32_bf16 v[148:151], v[80:83], v[4:7], v[148:151]\n\t" \
  "v_mfma_f32_16x16x32_bf16 v[152:155], v[92:95], v[0:3], v[152:155]\n\t" \
  "s_waitcnt lgkmcnt(4)\n\t" \
  "v_cndmask_b32_e64 v136, v36, v35, vcc\n\t" \
  "v_mfma_f32_16x16x32_bf16 v[152:155], v[88:91], v[4:7], v[152:155]\n\t" \
  "ds_bpermute_b32 v37, v32, v143\n\t" \
  "v_mfma_f32_16x16x32_bf16 v[156:159], v[100:103], v[0:3], v[156:159]\n\t" \
  "ds_read_b64 v[0:1], v28 offset:12288\n\t" \
  "ds_read_b64 v[2:3], v29 offset:12288\n\t" \
  "v_mfma_f32_16x16x32_bf16 v[156:159], v[96:99], v[4:7], v[156:159]\n\t" \
  "ds_read_b64 v[4:5], v30 offset:13312\n\t" \
  "ds_read_b64 v[6:7], v31 offset:13312\n\t" \
  "v_mfma_f32_16x16x32_bf16 v[144:147], v[68:71], v[8:11], v[144:147]\n\t" \
  "v_mov_b32 v143, v142\n\t" \
  "v_mov_b32 v142, v141\n\t" \
  "v_mfma_f32_16x16x32_bf16 v[144:147], v[64:67], v[12:15], v[144:147]\n\t" \
  "v_mov_b32 v141, v140\n\t" \
  "v_mfma_f32_16x16x32_bf16 v[148:151], v[76:79], v[8:11], v[148:151]\n\t" \
  "v_mfma_f32_16x16x32_bf16 v[148:151], v[72:75], v[12:15], v[148:151]\n\t" \
  "v_mfma_f32_16x16x32_bf16 v[152:155], v[84:87], v[8:11], v[152:155]\n\t" \
  "v_mfma_f32_16x16x32_bf16 v[152:155], v[80:83], v[12:15], v[152:155]\n\t" \
  "v_mfma_f32_16x16x32_bf16 v[156:159], v[92:95], v[8:11], v[156:159]\n\t" \
  "ds_read_b64 v[8:9], v24 offset:14336\n\t" \
  "ds_read_b64 v[10:11], v25 offset:14336\n\t" \
  "v_mfma_f32_16x16x32_bf16 v[156:159], v[88:91], v[12:15], v[156:159]\n\t" \
  "ds_read_b64 v[12:13], v26 offset:15360\n\t" \
  "s_waitcnt lgkmcnt(8)\n\t" \
  "v_mfma_f32_16x16x32_bf16 v[148:151], v[68:71], v[16:19], v[148:151]\n\t" \
  "ds_read_b64 v[14:15], v27 offset:15360\n\t" \
  "ds_bpermute_b32 v38, v32, v147\n\t" \
  "v_mfma_f32_16x16x32_bf16 v[148:151], v[64:67], v[20:23], v[148:151]\n\t" \
  "v_mov_b32 v147, v146\n\t" \
  "v_mov_b32 v146, v145\n\t" \
  "v_mfma_f32_16x16x32_bf16 v[152:155], v[76:79], v[16:19], v[152:155]\n\t" \
  "s_waitcnt lgkmcnt(9)\n\t" \
  "v_cndmask_b32_e64 v140, v37, v36, vcc\n\t" \
  "v_mfma_f32_16x16x32_bf16 v[152:155], v[72:75], v[20:23], v[152:155]\n\t" \
  "v_mov_b32 v145, v144\n\t" \
  "v_mfma_f32_16x16x32_bf16 v[156:159], v[84:87], v[16:19], v[156:159]\n\t" \
  "ds_read_b64 v[16:17], v26 offset:0\n\t" \
  "ds_read_b64 v[18:19], v27 offset:0\n\t" \
  "v_mfma_f32_16x16x32_bf16 v[156:159], v[80:83], v[20:23], v[156:159]\n\t" \
  "ds_read_b64 v[20:21], v28 offset:1024\n\t" \
  "ds_read_b64 v[22:23], v29 offset:1024\n\t" \
  "s_waitcnt lgkmcnt(9)\n\t" \
  "v_mfma_f32_16x16x32_bf16 v[152:155], v[68:71], v[0:3], v[152:155]\n\t" \
  "ds_bpermute_b32 v39, v32, v151\n\t" \
  "v_mfma_f32_16x16x32_bf16 v[152:155], v[64:67], v[4:7], v[152:155]\n\t" \
  "v_mov_b32 v151, v150\n\t" \
  "v_mov_b32 v150, v149\n\t" \
  "v_mfma_f32_16x16x32_bf16 v[156:159], v[76:79], v[0:3], v[156:159]\n\t" \
  "ds_read_b64 v[0:1], v30 offset:2048\n\t" \
  "ds_read_b64 v[2:3], v31 offset:2048\n\t" \
  "v_mfma_f32_16x16x32_bf16 v[156:159], v[72:75], v[4:7], v[156:159]\n\t" \
  "ds_read_b64 v[4:5], v24 offset:3072\n\t" \
  "s_waitcnt lgkmcnt(11)\n\t" \
  "v_mfma_f32_16x16x32_bf16 v[156:159], v[68:71], v[8:11], v[156:159]\n\t" \
  "ds_bpermute_b32 v40, v32, v155\n\t" \
  "s_waitcnt lgkmcnt(10)\n\t" \
  "v_mfma_f32_16x16x32_bf16 v[156:159], v[64:67], v[12:15], v[156:159]\n\t" \
  "ds_read_b64 v[6:7], v25 offset:3072\n\t" \
  "s_waitcnt lgkmcnt(8)\n\t" \
  "v_mfma_f32_16x16x32_bf16 v[128:131], v[68:71], v[16:19], v[128:131]\n\t" \
  "v_cndmask_b32_e64 v144, v38, v37, vcc\n\t" \
  "s_waitcnt lgkmcnt(6)\n\t" \
  "v_mfma_f32_16x16x32_bf16 v[128:131], v[64:67], v[20:23], v[128:131]\n\t" \
  "v_mov_b32 v149, v148\n\t" \
  "v_mov_b32 v155, v154\n\t" \
  "v_mfma_f32_16x16x32_bf16 v[132:135], v[76:79], v[16:19], v[132:135]\n\t" \
  "ds_bpermute_b32 v41, v32, v159\n\t" \
  "v_mov_b32 v154, v153\n\t" \
  "v_mfma_f32_16x16x32_bf16 v[132:135], v[72:75], v[20:23], v[132:135]\n\t" \
  "v_mov_b32 v153, v152\n\t" \
  "v_mov_b32 v159, v158\n\t" \
  "v_mfma_f32_16x16x32_bf16 v[136:139], v[84:87], v[16:19], v[136:139]\n\t" \
  "v_mov_b32 v158, v157\n\t" \
  "v_mov_b32 v157, v156\n\t" \
  "v_mfma_f32_16x16x32_bf16 v[136:139], v[80:83], v[20:23], v[136:139]\n\t" \
  "s_waitcnt lgkmcnt(6)\n\t" \
  "v_cndmask_b32_e64 v148, v39, v38, vcc\n\t" \
  "v_mfma_f32_16x16x32_bf16 v[140:143], v[92:95], v[16:19], v[140:143]\n\t" \
  "global_load_dwordx4 v[44:47], v43, %[q0] offset:0\n\t" \
  "v_mfma_f32_16x16x32_bf16 v[140:143], v[88:91], v[20:23], v[140:143]\n\t" \
  "ds_read_b64 v[8:9], v26 offset:4096\n\t" \
  "ds_read_b64 v[10:11], v27 offset:4096\n\t" \
  "v_mfma_f32_16x16x32_bf16 v[144:147], v[100:103], v[16:19], v[144:147]\n\t" \
  "s_waitcnt lgkmcnt(4)\n\t" \
  "v_cndmask_b32_e64 v152, v40, v39, vcc\n\t" \
  "v_mfma_f32_16x16x32_bf16 v[144:147], v[96:99], v[20:23], v[144:147]\n\t" \
  "ds_read_b64 v[12:13], v28 offset:5120\n\t" \
  "ds_read_b64 v[14:15], v29 offset:5120\n\t" \
  "v_mfma_f32_16x16x32_bf16 v[148:151], v[108:111], v[16:19], v[148:151]\n\t" \
  "s_waitcnt lgkmcnt(4)\n\t" \
  "v_cndmask_b32_e64 v156, v41, v40, vcc\n\t" \
  "v_mfma_f32_16x16x32_bf16 v[148:151], v[104:107], v[20:23], v[148:151]\n\t" \
  "global_load_dwordx4 v[48:51], v43, %[q0] offset:2048\n\t" \
  "v_mfma_f32_16x16x32_bf16 v[152:155], v[116:119], v[16:19], v[152:155]\n\t" \
  "ds_bpermute_b32 v34, v32, v131\n\t" \
  "v_mfma_f32_16x16x32_bf16 v[152:155], v[112:115], v[20:23], v[152:155]\n\t" \
  "v_mov_b32 v131, v130\n\t" \
  "v_mov_b32 v130, v129\n\t" \
  "v_mfma_f32_16x16x32_bf16 v[156:159], v[124:127], v[16:19], v[156:159]\n\t" \
  "global_load_dwordx4 v[52:55], v43, %[q2] offset:0\n\t" \
  "v_mfma_f32_16x16x32_bf16 v[156:159], v[120:123], v[20:23], v[156:159]\n\t" \
  "ds_read_b64 v[16:17], v30 offset:6144\n\t" \
  "ds_read_b64 v[18:19], v31 offset:6144\n\t" \
  "v_mfma_f32_16x16x32_bf16 v[132:135], v[68:71], v[0:3], v[132:135]\n\t" \
  "ds_read_b64 v[20:21], v24 offset:7168\n\t" \
  "ds_read_b64 v[22:23], v25 offset:7168\n\t" \
  "v_mfma_f32_16x16x32_bf16 v[132:135], v[64:67], v[4:7], v[132:135]\n\t" \
  "v_mov_b32 v129, v128\n\t" \
  "v_mfma_f32_16x16x32_bf16 v[136:139], v[76:79], v[0:3], v[136:139]\n\t" \
  "global_load_dwordx4 v[56:59], v43, %[q2] offset:2048\n\t" \
  "v_mfma_f32_16x16x32_bf16 v[136:139], v[72:75], v[4:7], v[136:139]\n\t" \
  "v_mfma_f32_16x16x32_bf16 v[140:143], v[84:87], v[0:3], v[140:143]\n\t" \
  "v_mfma_f32_16x16x32_bf16 v[140:143], v[80:83], v[4:7], v[140:143]\n\t" \
  "v_mfma_f32_16x16x32_bf16 v[144:147], v[92:95], v[0:3], v[144:147]\n\t" \
  "v_mfma_f32_16x16x32_bf16 v[144:147], v[88:91], v[4:7], v[144:147]\n\t" \
  "ds_bpermute_b32 v35, v32, v135\n\t" \
  "v_mfma_f32_16x16x32_bf16 v[148:151], v[100:103], v[0:3], v[148:151]\n\t" \
  "v_mov_b32 v135, v134\n\t" \
  "v_mov_b32 v134, v133\n\t" \
  "v_mfma_f32_16x16x32_bf16 v[148:151], v[96:99], v[4:7], v[148:151]\n\t" \
  "s_waitcnt lgkmcnt(5)\n\t" \
  "v_cndmask_b32_e64 v128, v34, 0, vcc\n\t" \
  "v_mfma_f32_16x16x32_bf16 v[152:155], v[108:111], v[0:3], v[152:155]\n\t" \
  "v_mov_b32 v133, v132\n\t" \
  "v_mfma_f32_16x16x32_bf16 v[152:155], v[104:107], v[4:7], v[152:155]\n\t" \
  "v_mfma_f32_16x16x32_bf16 v[156:159], v[116:119], v[0:3], v[156:159]\n\t" \
  "ds_read_b64 v[0:1], v26 offset:8192\n\t" \
  "ds_read_b64 v[2:3], v27 offset:8192\n\t" \
  "v_mfma_f32_16x16x32_bf16 v[156:159], v[112:115], v[4:7], v[156:159]\n\t" \
  "ds_read_b64 v[4:5], v28 offset:9216\n\t" \
  "ds_read_b64 v[6:7], v29 offset:9216\n\t" \
  "v_mfma_f32_16x16x32_bf16 v[136:139], v[68:71], v[8:11], v[136:139]\n\t" \
  "v_mfma_f32_16x16x32_bf16 v[136:139], v[64:67], v[12:15], v[136:139]\n\t" \
  "v_mfma_f32_16x16x32_bf16 v[140:143], v[76:79], v[8:11], v[140:143]\n\t" \
  "v_mfma_f32_16x16x32_bf16 v[140:143], v[72:75], v[12:15], v[140:143]\n\t" \
  "v_mfma_f32_16x16x32_bf16 v[144:147], v[84:87], v[8:11], v[144:147]\n\t" \
  "s_waitcnt lgkmcnt(4)\n\t" \
  "v_cndmask_b32_e64 v132, v35, v34, vcc\n\t" \
  "v_mfma_f32_16x16x32_bf16 v[144:147], v[80:83], v[12:15], v[144:147]\n\t" \
  "v_mfma_f32_16x16x32_bf16 v[148:151], v[92:95], v[8:11], v[148:151]\n\t" \
  "v_mfma_f32_16x16x32_bf16 v[148:151], v[88:91], v[12:15], v[148:151]\n\t" \
  "ds_bpermute_b32 v36, v32, v139\n\t" \
  "v_mfma_f32_16x16x32_bf16 v[152:155], v[100:103], v[8:11], v[152:155]\n\t" \
  "v_mov_b32 v139, v138\n\t" \
  "v_mov_b32 v138, v137\n\t" \
  "v_mfma_f32_16x16x32_bf16 v[152:155], v[96:99], v[12:15], v[152:155]\n\t" \
  "v_mov_b32 v137, v136\n\t" \
  "v_mfma_f32_16x16x32_bf16 v[156:159], v[108:111], v[8:11], v[156:159]\n\t" \
  "ds_read_b64 v[8:9], v30 offset:10240\n\t" \
  "ds_read_b64 v[10:11], v31 offset:10240\n\t" \
  "v_mfma_f32_16x16x32_bf16 v[156:159], v[104:107], v[12:15], v[156:159]\n\t" \
  "ds_read_b64 v[12:13], v24 offset:11264\n\t" \
  "ds_read_b64 v[14:15], v25 offset:11264\n\t" \
  "v_mfma_f32_16x16x32_bf16 v[140:143], v[68:71], v[16:19], v[140:143]\n\t" \
  "v_mfma_f32_16x16x32_bf16 v[140:143], v[64:67], v[20:23], v[140:143]\n\t" \
  "v_mfma_f32_16x16x32_bf16 v[144:147], v[76:79], v[16:19], v[144:147]\n\t" \
  "v_mfma_f32_16x16x32_bf16 v[144:147], v[72:75], v[20:23], v[144:147]\n\t" \
  "v_mfma_f32_16x16x32_bf16 v[148:151], v[84:87], v[16:19], v[148:151]\n\t" \
  "v_mfma_f32_16x16x32_bf16 v[148:151], v[80:83], v[20:23], v[148:151]\n\t" \
  "v_mfma_f32_16x16x32_bf16 v[152:155], v[92:95], v[16:19], v[152:155]\n\t" \
  "s_waitcnt lgkmcnt(4)\n\t" \
  "v_cndmask_b32_e64 v136, v36, v35, vcc\n\t" \
  "v_mfma_f32_16x16x32_bf16 v[152:155], v[88:91], v[20:23], v[152:155]\n\t" \
  "ds_bpermute_b32 v37, v32, v143\n\t" \
  "v_mfma_f32_16x16x32_bf16 v[156:159], v[100:103], v[16:19], v[156:159]\n\t" \
  "ds_read_b64 v[16:17], v26 offset:12288\n\t" \
  "ds_read_b64 v[18:19], v27 offset:12288\n\t" \
  "v_mfma_f32_16x16x32_bf16 v[156:159], v[96:99], v[20:23], v[156:159]\n\t" \
  "ds_read_b64 v[20:21], v28 offset:13312\n\t" \
  "ds_read_b64 v[22:23], v29 offset:13312\n\t" \
  "v_mfma_f32_16x16x32_bf16 v[144:147], v[68:71], v[0:3], v[144:147]\n\t" \
  "v_mov_b32 v143, v142\n\t" \
  "v_mov_b32 v142, v141\n\t" \
  "v_mfma_f32_16x16x32_bf16 v[144:147], v[64:67], v[4:7], v[144:147]\n\t" \
  "v_mov_b32 v141, v140\n\t" \
  "v_mfma_f32_16x16x32_bf16 v[148:151], v[76:79], v[0:3], v[148:151]\n\t" \
  "v_mfma_f32_16x16x32_bf16 v[148:151], v[72:75], v[4:7], v[148:151]\n\t" \
  "v_mfma_f32_16x16x32_bf16 v[152:155], v[84:87], v[0:3], v[152:155]\n\t" \
  "v_mfma_f32_16x16x32_bf16 v[152:155], v[80:83], v[4:7], v[152:155]\n\t" \
  "v_mfma_f32_16x16x32_bf16 v[156:159], v[92:95], v[0:3], v[156:159]\n\t" \
  "ds_read_b64 v[0:1], v30 offset:14336\n\t" \
  "ds_read_b64 v[2:3], v31 offset:14336\n\t" \
  "v_mfma_f32_16x16x32_bf16 v[156:159], v[88:91], v[4:7], v[156:159]\n\t" \
  "ds_read_b64 v[4:5], v24 offset:15360\n\t" \
  "s_waitcnt lgkmcnt(8)\n\t" \
  "v_mfma_f32_16x16x32_bf16 v[148:151], v[68:71], v[8:11], v[148:151]\n\t" \
  "ds_read_b64 v[6:7], v25 offset:15360\n\t" \
  "ds_bpermute_b32 v38, v32, v147\n\t" \
  "v_mfma_f32_16x16x32_bf16 v[148:151], v[64:67], v[12:15], v[148:151]\n\t" \
  "v_mov_b32 v147, v146\n\t" \
  "v_mov_b32 v146, v145\n\t" \
  "v_mfma_f32_16x16x32_bf16 v[152:155], v[76:79], v[8:11], v[152:155]\n\t" \
  "s_waitcnt lgkmcnt(9)\n\t" \
  "v_cndmask_b32_e64 v140, v37, v36, vcc\n\t" \
  "v_mfma_f32_16x16x32_bf16 v[152:155], v[72:75], v[12:15], v[152:155]\n\t" \
  "v_mov_b32 v145, v144\n\t" \
  "v_mfma_f32_16x16x32_bf16 v[156:159], v[84:87], v[8:11], v[156:159]\n\t" \
  "ds_read_b64 v[8:9], v24 offset:0\n\t" \
  "ds_read_b64 v[10:11], v25 offset:0\n\t" \
  "v_mfma_f32_16x16x32_bf16 v[156:159], v[80:83], v[12:15], v[156:159]\n\t" \
  "ds_read_b64 v[12:13], v26 offset:1024\n\t" \
  "ds_read_b64 v[14:15], v27 offset:1024\n\t" \
  "s_waitcnt lgkmcnt(9)\n\t" \
  "v_mfma_f32_16x16x32_bf16 v[152:155], v[68:71], v[16:19], v[152:155]\n\t" \
  "ds_bpermute_b32 v39, v32, v151\n\t" \
  "v_mfma_f32_16x16x32_bf16 v[152:155], v[64:67], v[20:23], v[152:155]\n\t" \
  "v_mov_b32 v151, v150\n\t" \
  "v_mov_b32 v150, v149\n\t" \
  "v_mfma_f32_16x16x32_bf16 v[156:159], v[76:79], v[16:19], v[156:159]\n\t" \
  "ds_read_b64 v[16:17], v28 offset:2048\n\t" \
  "ds_read_b64 v[18:19], v29 offset:2048\n\t" \
  "v_mfma_f32_16x16x32_bf16 v[156:159], v[72:75], v[20:23], v[156:159]\n\t" \
  "ds_read_b64 v[20:21], v30 offset:3072\n\t" \
  "s_waitcnt lgkmcnt(11)\n\t" \
  "v_mfma_f32_16x16x32_bf16 v[156:159], v[68:71], v[0:3], v[156:159]\n\t" \
  "ds_bpermute_b32 v40, v32, v155\n\t" \
  "s_waitcnt lgkmcnt(10)\n\t" \
  "v_mfma_f32_16x16x32_bf16 v[156:159], v[64:67], v[4:7], v[156:159]\n\t" \
  "ds_read_b64 v[22:23], v31 offset:3072\n\t" \
  "s_waitcnt lgkmcnt(8)\n\t" \
  "v_mfma_f32_16x16x32_bf16 v[128:131], v[68:71], v[8:11], v[128:131]\n\t" \
  "v_cndmask_b32_e64 v144, v38, v37, vcc\n\t" \
  "s_waitcnt lgkmcnt(6)\n\t" \
  "v_mfma_f32_16x16x32_bf16 v[128:131], v[64:67], v[12:15], v[128:131]\n\t" \
  "v_mov_b32 v149, v148\n\t" \
  "v_mov_b32 v155, v154\n\t" \
  "v_mfma_f32_16x16x32_bf16 v[132:135], v[76:79], v[8:11], v[132:135]\n\t" \
  "ds_bpermute_b32 v41, v32, v159\n\t" \
  "v_mov_b32 v154, v153\n\t" \
  "v_mfma_f32_16x16x32_bf16 v[132:135], v[72:75], v[12:15], v[132:135]\n\t" \
  "v_mov_b32 v153, v152\n\t" \
  "v_mov_b32 v159, v158\n\t" \
  "v_mfma_f32_16x16x32_bf16 v[136:139], v[84:87], v[8:11], v[136:139]\n\t" \
  "v_mov_b32 v158, v157\n\t" \
  "v_mov_b32 v157, v156\n\t" \
  "v_mfma_f32_16x16x32_bf16 v[136:139], v[80:83], v[12:15], v[136:139]\n\t" \
  "s_waitcnt lgkmcnt(6)\n\t" \
  "v_cndmask_b32_e64 v148, v39, v38, vcc\n\t" \
  "v_mfma_f32_16x16x32_bf16 v[140:143], v[92:95], v[8:11], v[140:143]\n\t" \
  "ds_read_b64 v[0:1], v24 offset:4096\n\t" \
  "ds_read_b64 v[2:3], v25 offset:4096\n\t" \
  "v_mfma_f32_16x16x32_bf16 v[140:143], v[88:91], v[12:15], v[140:143]\n\t" \
  "ds_read_b64 v[4:5], v26 offset:5120\n\t" \
  "ds_read_b64 v[6:7], v27 offset:5120\n\t" \
  "v_mfma_f32_16x16x32_bf16 v[144:147], v[100:103], v[8:11], v[144:147]\n\t" \
  "s_waitcnt lgkmcnt(6)\n\t" \
  "v_cndmask_b32_e64 v152, v40, v39, vcc\n\t" \
  "v_mfma_f32_16x16x32_bf16 v[144:147], v[96:99], v[12:15], v[144:147]\n\t" \
  "ds_bpermute_b32 v34, v32, v131\n\t" \
  "v_mfma_f32_16x16x32_bf16 v[148:151], v[108:111], v[8:11], v[148:151]\n\t" \
  "s_waitcnt lgkmcnt(5)\n\t" \
  "v_cndmask_b32_e64 v156, v41, v40, vcc\n\t" \
  "v_mfma_f32_16x16x32_bf16 v[148:151], v[104:107], v[12:15], v[148:151]\n\t" \
  "v_mov_b32 v131, v130\n\t" \
  "v_mov_b32 v130, v129\n\t" \
  "v_mfma_f32_16x16x32_bf16 v[152:155], v[116:119], v[8:11], v[152:155]\n\t" \
  "v_mov_b32 v129, v128\n\t" \
  "v_mfma_f32_16x16x32_bf16 v[152:155], v[112:115], v[12:15], v[152:155]\n\t" \
  "v_mfma_f32_16x16x32_bf16 v[156:159], v[124:127], v[8:11], v[156:159]\n\t" \
  "ds_read_b64 v[8:9], v28 offset:6144\n\t" \
  "ds_read_b64 v[10:11], v29 offset:6144\n\t" \
  "v_mfma_f32_16x16x32_bf16 v[156:159], v[120:123], v[12:15], v[156:159]\n\t" \
  "ds_read_b64 v[12:13], v30 offset:7168\n\t" \
  "ds_read_b64 v[14:15], v31 offset:7168\n\t" \
  "v_mfma_f32_16x16x32_bf16 v[132:135], v[68:71], v[16:19], v[132:135]\n\t" \
  "v_mfma_f32_16x16x32_bf16 v[132:135], v[64:67], v[20:23], v[132:135]\n\t" \
  "v_mfma_f32_16x16x32_bf16 v[136:139], v[76:79], v[16:19], v[136:139]\n\t" \
  "v_mfma_f32_16x16x32_bf16 v[136:139], v[72:75], v[20:23], v[136:139]\n\t" \
  "v_mfma_f32_16x16x32_bf16 v[140:143], v[84:87], v[16:19], v[140:143]\n\t" \
  "s_waitcnt lgkmcnt(4)\n\t" \
  "v_cndmask_b32_e64 v128, v34, 0, vcc\n\t" \
  "v_mfma_f32_16x16x32_bf16 v[140:143], v[80:83], v[20:23], v[140:143]\n\t" \
  "v_mfma_f32_16x16x32_bf16 v[144:147], v[92:95], v[16:19], v[144:147]\n\t" \
  "v_mfma_f32_16x16x32_bf16 v[144:147], v[88:91], v[20:23], v[144:147]\n\t" \
  "ds_bpermute_b32 v35, v32, v135\n\t" \
  "v_mfma_f32_16x16x32_bf16 v[148:151], v[100:103], v[16:19], v[148:151]\n\t" \
  "v_mov_b32 v135, v134\n\t" \
  "v_mov_b32 v134, v133\n\t" \
  "v_mfma_f32_16x16x32_bf16 v[148:151], v[96:99], v[20:23], v[148:151]\n\t" \
  "v_mov_b32 v133, v132\n\t" \
  "v_mfma_f32_16x16x32_bf16 v[152:155], v[108:111], v[16:19], v[152:155]\n\t" \
  "v_mfma_f32_16x16x32_bf16 v[152:155], v[104:107], v[20:23], v[152:155]\n\t" \
  "v_mfma_f32_16x16x32_bf16 v[156:159], v[116:119], v[16:19], v[156:159]\n\t" \
  "ds_read_b64 v[16:17], v24 offset:8192\n\t" \
  "ds_read_b64 v[18:19], v25 offset:8192\n\t" \
  "v_mfma_f32_16x16x32_bf16 v[156:159], v[112:115], v[20:23], v[156:159]\n\t" \
  "ds_read_b64 v[20:21], v26 offset:9216\n\t" \
  "ds_read_b64 v[22:23], v27 offset:9216\n\t" \
  "v_mfma_f32_16x16x32_bf16 v[136:139], v[68:71], v[0:3], v[136:139]\n\t" \
  "v_mfma_f32_16x16x32_bf16 v[136:139], v[64:67], v[4:7], v[136:139]\n\t" \
  "v_mfma_f32_16x16x32_bf16 v[140:143], v[76:79], v[0:3], v[140:143]\n\t" \
  "v_mfma_f32_16x16x32_bf16 v[140:143], v[72:75], v[4:7], v[140:143]\n\t" \
  "v_mfma_f32_16x16x32_bf16 v[144:147], v[84:87], v[0:3], v[144:147]\n\t" \
  "s_waitcnt lgkmcnt(4)\n\t" \
  "v_cndmask_b32_e64 v132, v35, v34, vcc\n\t" \
  "v_mfma_f32_16x16x32_bf16 v[144:147], v[80:83], v[4:7], v[144:147]\n\t" \
  "v_mfma_f32_16x16x32_bf16 v[148:151], v[92:95], v[0:3], v[148:151]\n\t" \
  "v_mfma_f32_16x16x32_bf16 v[148:151], v[88:91], v[4:7], v[148:151]\n\t" \
  "ds_bpermute_b32 v36, v32, v139\n\t" \
  "v_mfma_f32_16x16x32_bf16 v[152:155], v[100:103], v[0:3], v[152:155]\n\t" \
  "v_mov_b32 v139, v138\n\t" \
  "v_mov_b32 v138, v137\n\t" \
  "v_mfma_f32_16x16x32_bf16 v[152:155], v[96:99], v[4:7], v[152:155]\n\t" \
  "v_mov_b32 v137, v136\n\t" \
  "v_mfma_f32_16x16x32_bf16 v[156:159], v[108:111], v[0:3], v[156:159]\n\t" \
  "ds_read_b64 v[0:1], v28 offset:10240\n\t" \
  "ds_read_b64 v[2:3], v29 offset:10240\n\t" \
  "v_mfma_f32_16x16x32_bf16 v[156:159], v[104:107], v[4:7], v[156:159]\n\t" \
  "ds_read_b64 v[4:5], v30 offset:11264\n\t" \
  "ds_read_b64 v[6:7], v31 offset:11264\n\t" \
  "v_mfma_f32_16x16x32_bf16 v[140:143], v[68:71], v[8:11], v[140:143]\n\t" \
  "v_mfma_f32_16x16x32_bf16 v[140:143], v[64:67], v[12:15], v[140:143]\n\t" \
  "v_mfma_f32_16x16x32_bf16 v[144:147], v[76:79], v[8:11], v[144:147]\n\t" \
  "v_mfma_f32_16x16x32_bf16 v[144:147], v[72:75], v[12:15], v[144:147]\n\t" \
  "v_mfma_f32_16x16x32_bf16 v[148:151], v[84:87], v[8:11], v[148:151]\n\t" \
  "v_mfma_f32_16x16x32_bf16 v[148:151], v[80:83], v[12:15], v[148:151]\n\t" \
  "v_mfma_f32_16x16x32_bf16 v[152:155], v[92:95], v[8:11], v[152:155]\n\t" \
  "s_waitcnt lgkmcnt(4)\n\t" \
  "v_cndmask_b32_e64 v136, v36, v35, vcc\n\t" \
  "v_mfma_f32_16x16x32_bf16 v[152:155], v[88:91], v[12:15], v[152:155]\n\t" \
  "ds_bpermute_b32 v37, v32, v143\n\t" \
  "v_mfma_f32_16x16x32_bf16 v[156:159], v[100:103], v[8:11], v[156:159]\n\t" \
  "ds_read_b64 v[8:9], v24 offset:12288\n\t" \
  "ds_read_b64 v[10:11], v25 offset:12288\n\t" \
  "v_mfma_f32_16x16x32_bf16 v[156:159], v[96:99], v[12:15], v[156:159]\n\t" \
  "ds_read_b64 v[12:13], v26 offset:13312\n\t" \
  "ds_read_b64 v[14:15], v27 offset:13312\n\t" \
  "v_mfma_f32_16x16x32_bf16 v[144:147], v[68:71], v[16:19], v[144:147]\n\t" \
  "v_mov_b32 v143, v142\n\t" \
  "v_mov_b32 v142, v141\n\t" \
  "v_mfma_f32_16x16x32_bf16 v[144:147], v[64:67], v[20:23], v[144:147]\n\t" \
  "v_mov_b32 v141, v140\n\t" \
  "v_mfma_f32_16x16x32_bf16 v[148:151], v[76:79], v[16:19], v[148:151]\n\t" \
  "v_mfma_f32_16x16x32_bf16 v[148:151], v[72:75], v[20:23], v[148:151]\n\t" \
  "v_mfma_f32_16x16x32_bf16 v[152:155], v[84:87], v[16:19], v[152:155]\n\t" \
  "v_mfma_f32_16x16x32_bf16 v[152:155], v[80:83], v[20:23], v[152:155]\n\t" \
  "v_mfma_f32_16x16x32_bf16 v[156:159], v[92:95], v[16:19], v[156:159]\n\t" \
  "ds_read_b64 v[16:17], v28 offset:14336\n\t" \
  "ds_read_b64 v[18:19], v29 offset:14336\n\t" \
  "v_mfma_f32_16x16x32_bf16 v[156:159], v[88:91], v[20:23], v[156:159]\n\t" \
  "ds_read_b64 v[20:21], v30 offset:15360\n\t" \
  "s_waitcnt lgkmcnt(8)\n\t" \
  "v_mfma_f32_16x16x32_bf16 v[148:151], v[68:71], v[0:3], v[148:151]\n\t" \
  "ds_read_b64 v[22:23], v31 offset:15360\n\t" \
  "ds_bpermute_b32 v38, v32, v147\n\t" \
  "v_mfma_f32_16x16x32_bf16 v[148:151], v[64:67], v[4:7], v[148:151]\n\t" \
  "v_mov_b32 v147, v146\n\t" \
  "v_mov_b32 v146, v145\n\t" \
  "v_mfma_f32_16x16x32_bf16 v[152:155], v[76:79], v[0:3], v[152:155]\n\t" \
  "s_waitcnt lgkmcnt(9)\n\t" \
  "v_cndmask_b32_e64 v140, v37, v36, vcc\n\t" \
  "v_mfma_f32_16x16x32_bf16 v[152:155], v[72:75], v[4:7], v[152:155]\n\t" \
  "v_mov_b32 v145, v144\n\t" \
  "v_mfma_f32_16x16x32_bf16 v[156:159], v[84:87], v[0:3], v[156:159]\n\t" \
  "ds_read_b64 v[0:1], v30 offset:0\n\t" \
  "ds_read_b64 v[2:3], v31 offset:0\n\t" \
  "v_mfma_f32_16x16x32_bf16 v[156:159], v[80:83], v[4:7], v[156:159]\n\t" \
  "ds_read_b64 v[4:5], v24 offset:1024\n\t" \
  "ds_read_b64 v[6:7], v25 offset:1024\n\t" \
  "s_waitcnt lgkmcnt(9)\n\t" \
  "v_mfma_f32_16x16x32_bf16 v[152:155], v[68:71], v[8:11], v[152:155]\n\t" \
  "ds_bpermute_b32 v39, v32, v151\n\t" \
  "v_mfma_f32_16x16x32_bf16 v[152:155], v[64:67], v[12:15], v[152:155]\n\t" \
  "v_mov_b32 v151, v150\n\t" \
  "v_mov_b32 v150, v149\n\t" \
  "v_mfma_f32_16x16x32_bf16 v[156:159], v[76:79], v[8:11], v[156:159]\n\t" \
  "ds_read_b64 v[8:9], v26 offset:2048\n\t" \
  "ds_read_b64 v[10:11], v27 offset:2048\n\t" \
  "v_mfma_f32_16x16x32_bf16 v[156:159], v[72:75], v[12:15], v[156:159]\n\t" \
  "ds_read_b64 v[12:13], v28 offset:3072\n\t" \
  "s_waitcnt lgkmcnt(11)\n\t" \
  "v_mfma_f32_16x16x32_bf16 v[156:159], v[68:71], v[16:19], v[156:159]\n\t" \
  "ds_bpermute_b32 v40, v32, v155\n\t" \
  "s_waitcnt lgkmcnt(10)\n\t" \
  "v_mfma_f32_16x16x32_bf16 v[156:159], v[64:67], v[20:23], v[156:159]\n\t" \
  "ds_read_b64 v[14:15], v29 offset:3072\n\t" \
  "s_waitcnt lgkmcnt(8)\n\t" \
  "v_mfma_f32_16x16x32_bf16 v[128:131], v[68:71], v[0:3], v[128:131]\n\t" \
  "v_cndmask_b32_e64 v144, v38, v37, vcc\n\t" \
  "s_waitcnt lgkmcnt(6)\n\t" \
  "v_mfma_f32_16x16x32_bf16 v[128:131], v[64:67], v[4:7], v[128:131]\n\t" \
  "v_mov_b32 v149, v148\n\t" \
  "v_mov_b32 v155, v154\n\t" \
  "v_mfma_f32_16x16x32_bf16 v[132:135], v[76:79], v[0:3], v[132:135]\n\t" \
  "ds_bpermute_b32 v41, v32, v159\n\t" \
  "v_mov_b32 v154, v153\n\t" \
  "v_mfma_f32_16x16x32_bf16 v[132:135], v[72:75], v[4:7], v[132:135]\n\t" \
  "v_mov_b32 v153, v152\n\t" \
  "v_mov_b32 v159, v158\n\t" \
  "v_mfma_f32_16x16x32_bf16 v[136:139], v[84:87], v[0:3], v[136:139]\n\t" \
  "v_mov_b32 v158, v157\n\t" \
  "v_mov_b32 v157, v156\n\t" \
  "v_mfma_f32_16x16x32_bf16 v[136:139], v[80:83], v[4:7], v[136:139]\n\t" \
  "s_waitcnt lgkmcnt(6)\n\t" \
  "v_cndmask_b32_e64 v148, v39, v38, vcc\n\t" \
  "v_mfma_f32_16x16x32_bf16 v[140:143], v[92:95], v[0:3], v[140:143]\n\t" \
  "ds_read_b64 v[16:17], v30 offset:4096\n\t" \
  "ds_read_b64 v[18:19], v31 offset:4096\n\t" \
  "v_mfma_f32_16x16x32_bf16 v[140:143], v[88:91], v[4:7], v[140:143]\n\t" \
  "ds_read_b64 v[20:21], v24 offset:5120\n\t" \
  "ds_read_b64 v[22:23], v25 offset:5120\n\t" \
  "v_mfma_f32_16x16x32_bf16 v[144:147], v[100:103], v[0:3], v[144:147]\n\t" \
  "s_waitcnt lgkmcnt(6)\n\t" \
  "v_cndmask_b32_e64 v152, v40, v39, vcc\n\t" \
  "v_mfma_f32_16x16x32_bf16 v[144:147], v[96:99], v[4:7], v[144:147]\n\t" \
  "v_mfma_f32_16x16x32_bf16 v[148:151], v[108:111], v[0:3], v[148:151]\n\t" \
  "s_waitcnt lgkmcnt(4)\n\t" \
  "v_cndmask_b32_e64 v156, v41, v40, vcc\n\t" \
  "v_mfma_f32_16x16x32_bf16 v[148:151], v[104:107], v[4:7], v[148:151]\n\t" \
  "v_mfma_f32_16x16x32_bf16 v[152:155], v[116:119], v[0:3], v[152:155]\n\t" \
  "v_mfma_f32_16x16x32_bf16 v[152:155], v[112:115], v[4:7], v[152:155]\n\t" \
  "v_mfma_f32_16x16x32_bf16 v[156:159], v[124:127], v[0:3], v[156:159]\n\t" \
  "ds_read_b64 v[0:1], v26 offset:6144\n\t" \
  "ds_read_b64 v[2:3], v27 offset:6144\n\t" \
  "v_mfma_f32_16x16x32_bf16 v[156:159], v[120:123], v[4:7], v[156:159]\n\t" \
  "ds_read_b64 v[4:5], v28 offset:7168\n\t" \
  "ds_read_b64 v[6:7], v29 offset:7168\n\t" \
  "v_mfma_f32_16x16x32_bf16 v[132:135], v[68:71], v[8:11], v[132:135]\n\t" \
  "v_mfma_f32_16x16x32_bf16 v[132:135], v[64:67], v[12:15], v[132:135]\n\t" \
  "v_mfma_f32_16x16x32_bf16 v[136:139], v[76:79], v[8:11], v[136:139]\n\t" \
  "v_mfma_f32_16x16x32_bf16 v[136:139], v[72:75], v[12:15], v[136:139]\n\t" \
  "v_mfma_f32_16x16x32_bf16 v[140:143], v[84:87], v[8:11], v[140:143]\n\t" \
  "v_mfma_f32_16x16x32_bf16 v[140:143], v[80:83], v[12:15], v[140:143]\n\t" \
  "v_mfma_f32_16x16x32_bf16 v[144:147], v[92:95], v[8:11], v[144:147]\n\t" \
  "v_mfma_f32_16x16x32_bf16 v[144:147], v[88:91], v[12:15], v[144:147]\n\t" \
  "v_mfma_f32_16x16x32_bf16 v[148:151], v[100:103], v[8:11], v[148:151]\n\t" \
  "s_mov_b64 exec, 1\n\t" \
  "ds_write_b32 v33, v33\n\t" \
  "s_mov_b64 exec, -1\n\t" \
  "v_mfma_f32_16x16x32_bf16 v[148:151], v[96:99], v[12:15], v[148:151]\n\t" \
  "v_mfma_f32_16x16x32_bf16 v[152:155], v[108:111], v[8:11], v[152:155]\n\t" \
  "v_mfma_f32_16x16x32_bf16 v[152:155], v[104:107], v[12:15], v[152:155]\n\t" \
  "v_mfma_f32_16x16x32_bf16 v[156:159], v[116:119], v[8:11], v[156:159]\n\t" \
  "ds_read_b64 v[8:9], v30 offset:8192\n\t" \
  "ds_read_b64 v[10:11], v31 offset:8192\n\t" \
  "v_mfma_f32_16x16x32_bf16 v[156:159], v[112:115], v[12:15], v[156:159]\n\t" \
  "ds_read_b64 v[12:13], v24 offset:9216\n\t" \
  "s_waitcnt lgkmcnt(4)\n\t" \
  "v_mfma_f32_16x16x32_bf16 v[136:139], v[68:71], v[16:19], v[136:139]\n\t" \
  "ds_read_b64 v[14:15], v25 offset:9216\n\t" \
  "v_mfma_f32_16x16x32_bf16 v[136:139], v[64:67], v[20:23], v[136:139]\n\t" \
  "v_mfma_f32_16x16x32_bf16 v[140:143], v[76:79], v[16:19], v[140:143]\n\t" \
  "v_mfma_f32_16x16x32_bf16 v[140:143], v[72:75], v[20:23], v[140:143]\n\t" \
  "v_mfma_f32_16x16x32_bf16 v[144:147], v[84:87], v[16:19], v[144:147]\n\t" \
  "v_mfma_f32_16x16x32_bf16 v[144:147], v[80:83], v[20:23], v[144:147]\n\t" \
  "v_mfma_f32_16x16x32_bf16 v[148:151], v[92:95], v[16:19], v[148:151]\n\t" \
  "v_mfma_f32_16x16x32_bf16 v[148:151], v[88:91], v[20:23], v[148:151]\n\t" \
  "v_mfma_f32_16x16x32_bf16 v[152:155], v[100:103], v[16:19], v[152:155]\n\t" \
  "v_mfma_f32_16x16x32_bf16 v[152:155], v[96:99], v[20:23], v[152:155]\n\t" \
  "v_mfma_f32_16x16x32_bf16 v[156:159], v[108:111], v[16:19], v[156:159]\n\t" \
  "ds_read_b64 v[16:17], v26 offset:10240\n\t" \
  "ds_read_b64 v[18:19], v27 offset:10240\n\t" \
  "v_mfma_f32_16x16x32_bf16 v[156:159], v[104:107], v[20:23], v[156:159]\n\t" \
  "ds_read_b64 v[20:21], v28 offset:11264\n\t" \
  "ds_read_b64 v[22:23], v29 offset:11264\n\t" \
  "v_mfma_f32_16x16x32_bf16 v[140:143], v[68:71], v[0:3], v[140:143]\n\t" \
  "v_mfma_f32_16x16x32_bf16 v[140:143], v[64:67], v[4:7], v[140:143]\n\t" \
  "v_mfma_f32_16x16x32_bf16 v[144:147], v[76:79], v[0:3], v[144:147]\n\t" \
  "v_mfma_f32_16x16x32_bf16 v[144:147], v[72:75], v[4:7], v[144:147]\n\t" \
  "v_mfma_f32_16x16x32_bf16 v[148:151], v[84:87], v[0:3], v[148:151]\n\t" \
  "v_mfma_f32_16x16x32_bf16 v[148:151], v[80:83], v[4:7], v[148:151]\n\t" \
  "v_mfma_f32_16x16x32_bf16 v[152:155], v[92:95], v[0:3], v[152:155]\n\t" \
  "v_mfma_f32_16x16x32_bf16 v[152:155], v[88:91], v[4:7], v[152:155]\n\t" \
  "v_mfma_f32_16x16x32_bf16 v[156:159], v[100:103], v[0:3], v[156:159]\n\t" \
  "ds_read_b64 v[0:1], v30 offset:12288\n\t" \
  "ds_read_b64 v[2:3], v31 offset:12288\n\t" \
  "v_mfma_f32_16x16x32_bf16 v[156:159], v[96:99], v[4:7], v[156:159]\n\t" \
  "ds_read_b64 v[4:5], v24 offset:13312\n\t" \
  "s_waitcnt lgkmcnt(5)\n\t" \
  "v_mfma_f32_16x16x32_bf16 v[144:147], v[68:71], v[8:11], v[144:147]\n\t" \
  "ds_read_b64 v[6:7], v25 offset:13312\n\t" \
  "v_mfma_f32_16x16x32_bf16 v[144:147], v[64:67], v[12:15], v[144:147]\n\t" \
  "v_mfma_f32_16x16x32_bf16 v[148:151], v[76:79], v[8:11], v[148:151]\n\t" \
  "v_mfma_f32_16x16x32_bf16 v[148:151], v[72:75], v[12:15], v[148:151]\n\t" \
  "v_mfma_f32_16x16x32_bf16 v[152:155], v[84:87], v[8:11], v[152:155]\n\t" \
  "v_mfma_f32_16x16x32_bf16 v[152:155], v[80:83], v[12:15], v[152:155]\n\t" \
  "v_mfma_f32_16x16x32_bf16 v[156:159], v[92:95], v[8:11], v[156:159]\n\t" \
  "ds_read_b64 v[8:9], v26 offset:14336\n\t" \
  "ds_read_b64 v[10:11], v27 offset:14336\n\t" \
  "v_mfma_f32_16x16x32_bf16 v[156:159], v[88:91], v[12:15], v[156:159]\n\t" \
  "ds_read_b64 v[12:13], v28 offset:15360\n\t" \
  "ds_read_b64 v[14:15], v29 offset:15360\n\t" \
  "v_mfma_f32_16x16x32_bf16 v[148:151], v[68:71], v[16:19], v[148:151]\n\t" \
  "s_waitcnt lgkmcnt(8)\n\t" \
  "v_mfma_f32_16x16x32_bf16 v[148:151], v[64:67], v[20:23], v[148:151]\n\t" \
  "v_mfma_f32_16x16x32_bf16 v[152:155], v[76:79], v[16:19], v[152:155]\n\t" \
  "v_mfma_f32_16x16x32_bf16 v[152:155], v[72:75], v[20:23], v[152:155]\n\t" \
  "v_mfma_f32_16x16x32_bf16 v[156:159], v[84:87], v[16:19], v[156:159]\n\t" \
  "v_mfma_f32_16x16x32_bf16 v[156:159], v[80:83], v[20:23], v[156:159]\n\t" \
  "s_waitcnt lgkmcnt(4)\n\t" \
  "v_mfma_f32_16x16x32_bf16 v[152:155], v[68:71], v[0:3], v[152:155]\n\t" \
  "v_mfma_f32_16x16x32_bf16 v[152:155], v[64:67], v[4:7], v[152:155]\n\t" \
  "v_mfma_f32_16x16x32_bf16 v[156:159], v[76:79], v[0:3], v[156:159]\n\t" \
  "v_mfma_f32_16x16x32_bf16 v[156:159], v[72:75], v[4:7], v[156:159]\n\t" \
  "s_waitcnt lgkmcnt(2)\n\t" \
  "v_mfma_f32_16x16x32_bf16 v[156:159], v[68:71], v[8:11], v[156:159]\n\t" \
  "s_waitcnt lgkmcnt(0)\n\t" \
  "v_mfma_f32_16x16x32_bf16 v[156:159], v[64:67], v[12:15], v[156:159]\n\t" \
  "s_waitcnt lgkmcnt(0)\n\t" \
  "s_nop 7\n\t"
#define CV_Q 1
#define CV_CLOB "vcc", "v0", "v1", "v2", "v3", "v4", "v5", "v6", "v7", "v8", "v9", "v10", "v11", "v12", "v13", "v14", "v15", "v16", "v17", "v18", "v19", "v20", "v21", "v22", "v23", "v34", "v35", "v36", "v37", "v38", "v39", "v40", "v41"

#define G3_ASM \
  "s_getpc_b64 s[92:93]\n\t" \
  "s_branch QC_%=\n\t" \
  "ds_read_b64_tr_b16 v[96:97], v160 offset:0\n\t" \
  "ds_read_b64_tr_b16 v[98:99], v160 offset:2048\n\t" \
  "ds_read_b64_tr_b16 v[100:101], v161 offset:0\n\t" \
  "ds_read_b64_tr_b16 v[102:103], v161 offset:2048\n\t" \
  "ds_read_b64_tr_b16 v[104:105], v162 offset:0\n\t" \
  "ds_read_b64_tr_b16 v[106:107], v162 offset:2048\n\t" \
  "ds_read_b64_tr_b16 v[108:109], v163 offset:0\n\t" \
  "ds_read_b64_tr_b16 v[110:111], v163 offset:2048\n\t" \
  "ds_read_b64_tr_b16 v[112:113], v160 offset:4096\n\t" \
  "ds_read_b64_tr_b16 v[114:115], v160 offset:6144\n\t" \
  "ds_read_b64_tr_b16 v[116:117], v161 offset:4096\n\t" \
  "ds_read_b64_tr_b16 v[118:119], v161 offset:6144\n\t" \
  "ds_read_b64_tr_b16 v[120:121], v162 offset:4096\n\t" \
  "ds_read_b64_tr_b16 v[122:123], v162 offset:6144\n\t" \
  "QH_%=:\n\t" \
  "v_add_u32 v166, %[go], v165\n\t" \
  "ds_read_b32 v166, v166\n\t" \
  "s_addk_i32 %[cn], 1\n\t" \
  "s_cmp_gt_u32 %[cn], 0x400000\n\t" \
  "s_cbranch_scc1 QI_%=\n\t" \
  "s_waitcnt lgkmcnt(0)\n\t" \
  "v_cmp_eq_u32 vcc, 0, v166\n\t" \
  "s_cbranch_vccz QI_%=\n\t" \
  "s_sleep 2\n\t" \
  "s_getpc_b64 s[92:93]\n\t" \
  "s_branch QC_%=\n\t" \
  "s_branch QH_%=\n\t" \
  "QI_%=:\n\t" \
  "s_setprio 0\n\t" \
  "ds_read_b32 v166, v165\n\t" \
  "s_waitcnt lgkmcnt(11)\n\t" \
  "v_mfma_f32_16x16x32_bf16 v[128:131], v[96:99], v[0:3], 0\n\t" \
  "ds_read_b64_tr_b16 v[124:125], v163 offset:4096\n\t" \
  "v_readlane_b32 s94, v167, 5\n\t" \
  "v_mfma_f32_16x16x32_bf16 v[144:147], v[96:99], v[4:7], 0\n\t" \
  "ds_read_b64_tr_b16 v[126:127], v163 offset:6144\n\t" \
  "s_waitcnt lgkmcnt(2)\n\t" \
  "v_cmp_lt_u32 vcc, %[th], v166\n\t" \
  "v_mfma_f32_16x16x32_bf16 v[132:135], v[100:103], v[0:3], 0\n\t" \
  "s_mov_b64 exec, vcc\n\t" \
  "buffer_load_dwordx4 v[80:83], v164, %[ob], s94 offen offset:0 sc1\n\t" \
  "s_mov_b64 exec, -1\n\t" \
  "v_mfma_f32_16x16x32_bf16 v[148:151], v[100:103], v[4:7], 0\n\t" \
  "ds_read_b64_tr_b16 v[96:97], v160 offset:8192\n\t" \
  "ds_read_b64_tr_b16 v[98:99], v160 offset:10240\n\t" \
  "v_mfma_f32_16x16x32_bf16 v[136:139], v[104:107], v[0:3], 0\n\t" \
  "s_mov_b64 exec, vcc\n\t" \
  "buffer_load_dwordx4 v[84:87], v164, %[ob], s94 offen offset:1024 sc1\n\t" \
  "s_mov_b64 exec, -1\n\t" \
  "v_mfma_f32_16x16x32_bf16 v[152:155], v[104:107], v[4:7], 0\n\t" \
  "ds_read_b64_tr_b16 v[100:101], v161 offset:8192\n\t" \
  "ds_read_b64_tr_b16 v[102:103], v161 offset:10240\n\t" \
  "v_mfma_f32_16x16x32_bf16 v[140:143], v[108:111], v[0:3], 0\n\t" \
  "s_mov_b64 exec, vcc\n\t" \
  "buffer_load_dwordx4 v[88:91], v164, %[ob], s94 offen offset:2048 sc1\n\t" \
  "s_mov_b64 exec, -1\n\t" \
  "v_mfma_f32_16x16x32_bf16 v[156:159], v[108:111], v[4:7], 0\n\t" \
  "ds_read_b64_tr_b16 v[104:105], v162 offset:8192\n\t" \
  "ds_read_b64_tr_b16 v[106:107], v162 offset:10240\n\t" \
  "v_mfma_f32_16x16x32_bf16 v[128:131], v[112:115], v[8:11], v[128:131]\n\t" \
  "s_mov_b64 exec, vcc\n\t" \
  "buffer_load_dwordx4 v[92:95], v164, %[ob], s94 offen offset:3072 sc1\n\t" \
  "s_mov_b64 exec, -1\n\t" \
  "v_mfma_f32_16x16x32_bf16 v[144:147], v[112:115], v[12:15], v[144:147]\n\t" \
  "ds_read_b64_tr_b16 v[108:109], v163 offset:8192\n\t" \
  "ds_read_b64_tr_b16 v[110:111], v163 offset:10240\n\t" \
  "v_mfma_f32_16x16x32_bf16 v[132:135], v[116:119], v[8:11], v[132:135]\n\t" \
  "ds_read_b64_tr_b16 v[112:113], v160 offset:12288\n\t" \
  "ds_read_b32 v166, v165\n\t" \
  "v_mfma_f32_16x16x32_bf16 v[148:151], v[116:119], v[12:15], v[148:151]\n\t" \
  "ds_read_b64_tr_b16 v[114:115], v160 offset:14336\n\t" \
  "s_waitcnt lgkmcnt(11)\n\t" \
  "v_mfma_f32_16x16x32_bf16 v[136:139], v[120:123], v[8:11], v[136:139]\n\t" \
  "ds_read_b64_tr_b16 v[116:117], v161 offset:12288\n\t" \
  "v_mfma_f32_16x16x32_bf16 v[152:155], v[120:123], v[12:15], v[152:155]\n\t" \
  "ds_read_b64_tr_b16 v[118:119], v161 offset:14336\n\t" \
  "s_sub_u32 %[iss], %[iss], vcc_lo\n\t" \
  "v_mfma_f32_16x16x32_bf16 v[140:143], v[124:127], v[8:11], v[140:143]\n\t" \
  "ds_read_b64_tr_b16 v[120:121], v162 offset:12288\n\t" \
  "v_mfma_f32_16x16x32_bf16 v[156:159], v[124:127], v[12:15], v[156:159]\n\t" \
  "ds_read_b64_tr_b16 v[122:123], v162 offset:14336\n\t" \
  "s_add_u32 %[th], %[th], 1\n\t" \
  "s_cbranch_vccz QF1_%=\n\t" \
  "s_waitcnt vmcnt(16)\n\t" \
  "QG1_%=:\n\t" \
  "s_waitcnt lgkmcnt(11)\n\t" \
  "v_mfma_f32_16x16x32_bf16 v[128:131], v[96:99], v[16:19], v[128:131]\n\t" \
  "ds_read_b64_tr_b16 v[124:125], v163 offset:12288\n\t" \
  "v_readlane_b32 s94, v167, 6\n\t" \
  "v_mfma_f32_16x16x32_bf16 v[144:147], v[96:99], v[20:23], v[144:147]\n\t" \
  "ds_read_b64_tr_b16 v[126:127], v163 offset:14336\n\t" \
  "s_waitcnt lgkmcnt(7)\n\t" \
  "v_cmp_lt_u32 vcc, %[th], v166\n\t" \
  "v_mfma_f32_16x16x32_bf16 v[132:135], v[100:103], v[16:19], v[132:135]\n\t" \
  "s_mov_b64 exec, vcc\n\t" \
  "buffer_load_dwordx4 v[0:3], v164, %[ob], s94 offen offset:0 sc1\n\t" \
  "s_mov_b64 exec, -1\n\t" \
  "v_mfma_f32_16x16x32_bf16 v[148:151], v[100:103], v[20:23], v[148:151]\n\t" \
  "ds_read_b64_tr_b16 v[96:97], v160 offset:16384\n\t" \
  "ds_read_b64_tr_b16 v[98:99], v160 offset:18432\n\t" \
  "v_mfma_f32_16x16x32_bf16 v[136:139], v[104:107], v[16:19], v[136:139]\n\t" \
  "s_mov_b64 exec, vcc\n\t" \
  "buffer_load_dwordx4 v[4:7], v164, %[ob], s94 offen offset:1024 sc1\n\t" \
  "s_mov_b64 exec, -1\n\t" \
  "v_mfma_f32_16x16x32_bf16 v[152:155], v[104:107], v[20:23], v[152:155]\n\t" \
  "ds_read_b64_tr_b16 v[100:101], v161 offset:16384\n\t" \
  "ds_read_b64_tr_b16 v[102:103], v161 offset:18432\n\t" \
  "v_mfma_f32_16x16x32_bf16 v[140:143], v[108:111], v[16:19], v[140:143]\n\t" \
  "s_mov_b64 exec, vcc\n\t" \
  "buffer_load_dwordx4 v[8:11], v164, %[ob], s94 offen offset:2048 sc1\n\t" \
  "s_mov_b64 exec, -1\n\t" \
  "v_mfma_f32_16x16x32_bf16 v[156:159], v[108:111], v[20:23], v[156:159]\n\t" \
  "ds_read_b64_tr_b16 v[104:105], v162 offset:16384\n\t" \
  "ds_read_b64_tr_b16 v[106:107], v162 offset:18432\n\t" \
  "s_waitcnt lgkmcnt(10)\n\t" \
  "v_mfma_f32_16x16x32_bf16 v[128:131], v[112:115], v[24:27], v[128:131]\n\t" \
  "s_mov_b64 exec, vcc\n\t" \
  "buffer_load_dwordx4 v[12:15], v164, %[ob], s94 offen offset:3072 sc1\n\t" \
  "s_mov_b64 exec, -1\n\t" \
  "v_mfma_f32_16x16x32_bf16 v[144:147], v[112:115], v[28:31], v[144:147]\n\t" \
  "ds_read_b64_tr_b16 v[108:109], v163 offset:16384\n\t" \
  "ds_read_b64_tr_b16 v[110:111], v163 offset:18432\n\t" \
  "v_mfma_f32_16x16x32_bf16 v[132:135], v[116:119], v[24:27], v[132:135]\n\t" \
  "ds_read_b64_tr_b16 v[112:113], v160 offset:20480\n\t" \
  "ds_read_b32 v166, v165\n\t" \
  "v_mfma_f32_16x16x32_bf16 v[148:151], v[116:119], v[28:31], v[148:151]\n\t" \
  "ds_read_b64_tr_b16 v[114:115], v160 offset:22528\n\t" \
  "s_waitcnt lgkmcnt(11)\n\t" \
  "v_mfma_f32_16x16x32_bf16 v[136:139], v[120:123], v[24:27], v[136:139]\n\t" \
  "ds_read_b64_tr_b16 v[116:117], v161 offset:20480\n\t" \
  "v_mfma_f32_16x16x32_bf16 v[152:155], v[120:123], v[28:31], v[152:155]\n\t" \
  "ds_read_b64_tr_b16 v[118:119], v161 offset:22528\n\t" \
  "s_sub_u32 %[iss], %[iss], vcc_lo\n\t" \
  "v_mfma_f32_16x16x32_bf16 v[140:143], v[124:127], v[24:27], v[140:143]\n\t" \
  "ds_read_b64_tr_b16 v[120:121], v162 offset:20480\n\t" \
  "v_mfma_f32_16x16x32_bf16 v[156:159], v[124:127], v[28:31], v[156:159]\n\t" \
  "ds_read_b64_tr_b16 v[122:123], v162 offset:22528\n\t" \
  "s_add_u32 %[th], %[th], 1\n\t" \
  "s_cbranch_vccz QF2_%=\n\t" \
  "s_waitcnt vmcnt(16)\n\t" \
  "QG2_%=:\n\t" \
  "s_waitcnt lgkmcnt(11)\n\t" \
  "v_mfma_f32_16x16x32_bf16 v[128:131], v[96:99], v[32:35], v[128:131]\n\t" \
  "ds_read_b64_tr_b16 v[124:125], v163 offset:20480\n\t" \
  "v_readlane_b32 s94, v167, 7\n\t" \
  "v_mfma_f32_16x16x32_bf16 v[144:147], v[96:99], v[36:39], v[144:147]\n\t" \
  "ds_read_b64_tr_b16 v[126:127], v163 offset:22528\n\t" \
  "s_waitcnt lgkmcnt(7)\n\t" \
  "v_cmp_lt_u32 vcc, %[th], v166\n\t" \
  "v_mfma_f32_16x16x32_bf16 v[132:135], v[100:103], v[32:35], v[132:135]\n\t" \
  "s_mov_b64 exec, vcc\n\t" \
  "buffer_load_dwordx4 v[16:19], v164, %[ob], s94 offen offset:0 sc1\n\t" \
  "s_mov_b64 exec, -1\n\t" \
  "v_mfma_f32_16x16x32_bf16 v[148:151], v[100:103], v[36:39], v[148:151]\n\t" \
  "ds_read_b64_tr_b16 v[96:97], v160 offset:24576\n\t" \
  "ds_read_b64_tr_b16 v[98:99], v160 offset:26624\n\t" \
  "v_mfma_f32_16x16x32_bf16 v[136:139], v[104:107], v[32:35], v[136:139]\n\t" \
  "s_mov_b64 exec, vcc\n\t" \
  "buffer_load_dwordx4 v[20:23], v164, %[ob], s94 offen offset:1024 sc1\n\t" \
  "s_mov_b64 exec, -1\n\t" \
  "v_mfma_f32_16x16x32_bf16 v[152:155], v[104:107], v[36:39], v[152:155]\n\t" \
  "ds_read_b64_tr_b16 v[100:101], v161 offset:24576\n\t" \
  "ds_read_b64_tr_b16 v[102:103], v161 offset:26624\n\t" \
  "v_mfma_f32_16x16x32_bf16 v[140:143], v[108:111], v[32:35], v[140:143]\n\t" \
  "s_mov_b64 exec, vcc\n\t" \
  "buffer_load_dwordx4 v[24:27], v164, %[ob], s94 offen offset:2048 sc1\n\t" \
  "s_mov_b64 exec, -1\n\t" \
  "v_mfma_f32_16x16x32_bf16 v[156:159], v[108:111], v[36:39], v[156:159]\n\t" \
  "ds_read_b64_tr_b16 v[104:105], v162 offset:24576\n\t" \
  "ds_read_b64_tr_b16 v[106:107], v162 offset:26624\n\t" \
  "s_waitcnt lgkmcnt(10)\n\t" \
  "v_mfma_f32_16x16x32_bf16 v[128:131], v[112:115], v[40:43], v[128:131]\n\t" \
  "s_mov_b64 exec, vcc\n\t" \
  "buffer_load_dwordx4 v[28:31], v164, %[ob], s94 offen offset:3072 sc1\n\t" \
  "s_mov_b64 exec, -1\n\t" \
  "v_mfma_f32_16x16x32_bf16 v[144:147], v[112:115], v[44:47], v[144:147]\n\t" \
  "ds_read_b64_tr_b16 v[108:109], v163 offset:24576\n\t" \
  "ds_read_b64_tr_b16 v[110:111], v163 offset:26624\n\t" \
  "v_mfma_f32_16x16x32_bf16 v[132:135], v[116:119], v[40:43], v[132:135]\n\t" \
  "ds_read_b64_tr_b16 v[112:113], v160 offset:28672\n\t" \
  "ds_read_b32 v166, v165\n\t" \
  "v_mfma_f32_16x16x32_bf16 v[148:151], v[116:119], v[44:47], v[148:151]\n\t" \
  "ds_read_b64_tr_b16 v[114:115], v160 offset:30720\n\t" \
  "s_waitcnt lgkmcnt(11)\n\t" \
  "v_mfma_f32_16x16x32_bf16 v[136:139], v[120:123], v[40:43], v[136:139]\n\t" \
  "ds_read_b64_tr_b16 v[116:117], v161 offset:28672\n\t" \
  "v_mfma_f32_16x16x32_bf16 v[152:155], v[120:123], v[44:47], v[152:155]\n\t" \
  "ds_read_b64_tr_b16 v[118:119], v161 offset:30720\n\t" \
  "s_sub_u32 %[iss], %[iss], vcc_lo\n\t" \
  "v_mfma_f32_16x16x32_bf16 v[140:143], v[124:127], v[40:43], v[140:143]\n\t" \
  "ds_read_b64_tr_b16 v[120:121], v162 offset:28672\n\t" \
  "v_mfma_f32_16x16x32_bf16 v[156:159], v[124:127], v[44:47], v[156:159]\n\t" \
  "ds_read_b64_tr_b16 v[122:123], v162 offset:30720\n\t" \
  "s_add_u32 %[th], %[th], 1\n\t" \
  "s_cbranch_vccz QF3_%=\n\t" \
  "s_waitcnt vmcnt(16)\n\t" \
  "QG3_%=:\n\t" \
  "s_waitcnt lgkmcnt(11)\n\t" \
  "v_mfma_f32_16x16x32_bf16 v[128:131], v[96:99], v[48:51], v[128:131]\n\t" \
  "ds_read_b64_tr_b16 v[124:125], v163 offset:28672\n\t" \
  "v_readlane_b32 s94, v167, 8\n\t" \
  "v_mfma_f32_16x16x32_bf16 v[144:147], v[96:99], v[52:55], v[144:147]\n\t" \
  "ds_read_b64_tr_b16 v[126:127], v163 offset:30720\n\t" \
  "s_waitcnt lgkmcnt(7)\n\t" \
  "v_cmp_lt_u32 vcc, %[th], v166\n\t" \
  "v_mfma_f32_16x16x32_bf16 v[132:135], v[100:103], v[48:51], v[132:135]\n\t" \
  "s_mov_b64 exec, vcc\n\t" \
  "buffer_load_dwordx4 v[32:35], v164, %[ob], s94 offen offset:0 sc1\n\t" \
  "s_mov_b64 exec, -1\n\t" \
  "v_mfma_f32_16x16x32_bf16 v[148:151], v[100:103], v[52:55], v[148:151]\n\t" \
  "ds_read_b64_tr_b16 v[96:97], v160 offset:32768\n\t" \
  "ds_read_b64_tr_b16 v[98:99], v160 offset:34816\n\t" \
  "v_mfma_f32_16x16x32_bf16 v[136:139], v[104:107], v[48:51], v[136:139]\n\t" \
  "s_mov_b64 exec, vcc\n\t" \
  "buffer_load_dwordx4 v[36:39], v164, %[ob], s94 offen offset:1024 sc1\n\t" \
  "s_mov_b64 exec, -1\n\t" \
  "v_mfma_f32_16x16x32_bf16 v[152:155], v[104:107], v[52:55], v[152:155]\n\t" \
  "ds_read_b64_tr_b16 v[100:101], v161 offset:32768\n\t" \
  "ds_read_b64_tr_b16 v[102:103], v161 offset:34816\n\t" \
  "v_mfma_f32_16x16x32_bf16 v[140:143], v[108:111], v[48:51], v[140:143]\n\t" \
  "s_mov_b64 exec, vcc\n\t" \
  "buffer_load_dwordx4 v[40:43], v164, %[ob], s94 offen offset:2048 sc1\n\t" \
  "s_mov_b64 exec, -1\n\t" \
  "v_mfma_f32_16x16x32_bf16 v[156:159], v[108:111], v[52:55], v[156:159]\n\t" \
  "ds_read_b64_tr_b16 v[104:105], v162 offset:32768\n\t" \
  "ds_read_b64_tr_b16 v[106:107], v162 offset:34816\n\t" \
  "s_waitcnt lgkmcnt(10)\n\t" \
  "v_mfma_f32_16x16x32_bf16 v[128:131], v[112:115], v[56:59], v[128:131]\n\t" \
  "s_mov_b64 exec, vcc\n\t" \
  "buffer_load_dwordx4 v[44:47], v164, %[ob], s94 offen offset:3072 sc1\n\t" \
  "s_mov_b64 exec, -1\n\t" \
  "v_mfma_f32_16x16x32_bf16 v[144:147], v[112:115], v[60:63], v[144:147]\n\t" \
  "ds_read_b64_tr_b16 v[108:109], v163 offset:32768\n\t" \
  "ds_read_b64_tr_b16 v[110:111], v163 offset:34816\n\t" \
  "v_mfma_f32_16x16x32_bf16 v[132:135], v[116:119], v[56:59], v[132:135]\n\t" \
  "ds_read_b64_tr_b16 v[112:113], v160 offset:36864\n\t" \
  "ds_read_b32 v166, v165\n\t" \
  "v_mfma_f32_16x16x32_bf16 v[148:151], v[116:119], v[60:63], v[148:151]\n\t" \
  "ds_read_b64_tr_b16 v[114:115], v160 offset:38912\n\t" \
  "s_waitcnt lgkmcnt(11)\n\t" \
  "v_mfma_f32_16x16x32_bf16 v[136:139], v[120:123], v[56:59], v[136:139]\n\t" \
  "ds_read_b64_tr_b16 v[116:117], v161 offset:36864\n\t" \
  "v_mfma_f32_16x16x32_bf16 v[152:155], v[120:123], v[60:63], v[152:155]\n\t" \
  "ds_read_b64_tr_b16 v[118:119], v161 offset:38912\n\t" \
  "s_sub_u32 %[iss], %[iss], vcc_lo\n\t" \
  "v_mfma_f32_16x16x32_bf16 v[140:143], v[124:127], v[56:59], v[140:143]\n\t" \
  "ds_read_b64_tr_b16 v[120:121], v162 offset:36864\n\t" \
  "v_mfma_f32_16x16x32_bf16 v[156:159], v[124:127], v[60:63], v[156:159]\n\t" \
  "ds_read_b64_tr_b16 v[122:123], v162 offset:38912\n\t" \
  "s_add_u32 %[th], %[th], 1\n\t" \
  "s_cbranch_vccz QF4_%=\n\t" \
  "s_waitcnt vmcnt(16)\n\t" \
  "QG4_%=:\n\t" \
  "s_waitcnt lgkmcnt(11)\n\t" \
  "v_mfma_f32_16x16x32_bf16 v[128:131], v[96:99], v[64:67], v[128:131]\n\t" \
  "ds_read_b64_tr_b16 v[124:125], v163 offset:36864\n\t" \
  "v_readlane_b32 s94, v167, 9\n\t" \
  "v_mfma_f32_16x16x32_bf16 v[144:147], v[96:99], v[68:71], v[144:147]\n\t" \
  "ds_read_b64_tr_b16 v[126:127], v163 offset:38912\n\t" \
  "s_waitcnt lgkmcnt(7)\n\t" \
  "v_cmp_lt_u32 vcc, %[th], v166\n\t" \
  "v_mfma_f32_16x16x32_bf16 v[132:135], v[100:103], v[64:67], v[132:135]\n\t" \
  "s_mov_b64 exec, vcc\n\t" \
  "buffer_load_dwordx4 v[48:51], v164, %[ob], s94 offen offset:0 sc1\n\t" \
  "s_mov_b64 exec, -1\n\t" \
  "v_mfma_f32_16x16x32_bf16 v[148:151], v[100:103], v[68:71], v[148:151]\n\t" \
  "ds_read_b64_tr_b16 v[96:97], v160 offset:40960\n\t" \
  "ds_read_b64_tr_b16 v[98:99], v160 offset:43008\n\t" \
  "v_mfma_f32_16x16x32_bf16 v[136:139], v[104:107], v[64:67], v[136:139]\n\t" \
  "s_mov_b64 exec, vcc\n\t" \
  "buffer_load_dwordx4 v[52:55], v164, %[ob], s94 offen offset:1024 sc1\n\t" \
  "s_mov_b64 exec, -1\n\t" \
  "v_mfma_f32_16x16x32_bf16 v[152:155], v[104:107], v[68:71], v[152:155]\n\t" \
  "ds_read_b64_tr_b16 v[100:101], v161 offset:40960\n\t" \
  "ds_read_b64_tr_b16 v[102:103], v161 offset:43008\n\t" \
  "v_mfma_f32_16x16x32_bf16 v[140:143], v[108:111], v[64:67], v[140:143]\n\t" \
  "s_mov_b64 exec, vcc\n\t" \
  "buffer_load_dwordx4 v[56:59], v164, %[ob], s94 offen offset:2048 sc1\n\t" \
  "s_mov_b64 exec, -1\n\t" \
  "v_mfma_f32_16x16x32_bf16 v[156:159], v[108:111], v[68:71], v[156:159]\n\t" \
  "ds_read_b64_tr_b16 v[104:105], v162 offset:40960\n\t" \
  "ds_read_b64_tr_b16 v[106:107], v162 offset:43008\n\t" \
  "s_waitcnt lgkmcnt(10)\n\t" \
  "v_mfma_f32_16x16x32_bf16 v[128:131], v[112:115], v[72:75], v[128:131]\n\t" \
  "s_mov_b64 exec, vcc\n\t" \
  "buffer_load_dwordx4 v[60:63], v164, %[ob], s94 offen offset:3072 sc1\n\t" \
  "s_mov_b64 exec, -1\n\t" \
  "v_mfma_f32_16x16x32_bf16 v[144:147], v[112:115], v[76:79], v[144:147]\n\t" \
  "ds_read_b64_tr_b16 v[108:109], v163 offset:40960\n\t" \
  "ds_read_b64_tr_b16 v[110:111], v163 offset:43008\n\t" \
  "v_mfma_f32_16x16x32_bf16 v[132:135], v[116:119], v[72:75], v[132:135]\n\t" \
  "ds_read_b64_tr_b16 v[112:113], v160 offset:45056\n\t" \
  "ds_read_b32 v166, v165\n\t" \
  "v_mfma_f32_16x16x32_bf16 v[148:151], v[116:119], v[76:79], v[148:151]\n\t" \
  "ds_read_b64_tr_b16 v[114:115], v160 offset:47104\n\t" \
  "s_waitcnt lgkmcnt(11)\n\t" \
  "v_mfma_f32_16x16x32_bf16 v[136:139], v[120:123], v[72:75], v[136:139]\n\t" \
  "ds_read_b64_tr_b16 v[116:117], v161 offset:45056\n\t" \
  "v_mfma_f32_16x16x32_bf16 v[152:155], v[120:123], v[76:79], v[152:155]\n\t" \
  "ds_read_b64_tr_b16 v[118:119], v161 offset:47104\n\t" \
  "s_sub_u32 %[iss], %[iss], vcc_lo\n\t" \
  "v_mfma_f32_16x16x32_bf16 v[140:143], v[124:127], v[72:75], v[140:143]\n\t" \
  "ds_read_b64_tr_b16 v[120:121], v162 offset:45056\n\t" \
  "v_mfma_f32_16x16x32_bf16 v[156:159], v[124:127], v[76:79], v[156:159]\n\t" \
  "ds_read_b64_tr_b16 v[122:123], v162 offset:47104\n\t" \
  "s_add_u32 %[th], %[th], 1\n\t" \
  "s_cbranch_vccz QF5_%=\n\t" \
  "s_waitcnt vmcnt(16)\n\t" \
  "QG5_%=:\n\t" \
  "s_waitcnt lgkmcnt(11)\n\t" \
  "v_mfma_f32_16x16x32_bf16 v[128:131], v[96:99], v[80:83], v[128:131]\n\t" \
  "ds_read_b64_tr_b16 v[124:125], v163 offset:45056\n\t" \
  "v_readlane_b32 s94, v167, 10\n\t" \
  "v_mfma_f32_16x16x32_bf16 v[144:147], v[96:99], v[84:87], v[144:147]\n\t" \
  "ds_read_b64_tr_b16 v[126:127], v163 offset:47104\n\t" \
  "s_waitcnt lgkmcnt(7)\n\t" \
  "v_cmp_lt_u32 vcc, %[th], v166\n\t" \
  "v_mfma_f32_16x16x32_bf16 v[132:135], v[100:103], v[80:83], v[132:135]\n\t" \
  "s_mov_b64 exec, vcc\n\t" \
  "buffer_load_dwordx4 v[64:67], v164, %[ob], s94 offen offset:0 sc1\n\t" \
  "s_mov_b64 exec, -1\n\t" \
  "v_mfma_f32_16x16x32_bf16 v[148:151], v[100:103], v[84:87], v[148:151]\n\t" \
  "ds_read_b64_tr_b16 v[96:97], v160 offset:49152\n\t" \
  "ds_read_b64_tr_b16 v[98:99], v160 offset:51200\n\t" \
  "v_mfma_f32_16x16x32_bf16 v[136:139], v[104:107], v[80:83], v[136:139]\n\t" \
  "s_mov_b64 exec, vcc\n\t" \
  "buffer_load_dwordx4 v[68:71], v164, %[ob], s94 offen offset:1024 sc1\n\t" \
  "s_mov_b64 exec, -1\n\t" \
  "v_mfma_f32_16x16x32_bf16 v[152:155], v[104:107], v[84:87], v[152:155]\n\t" \
  "ds_read_b64_tr_b16 v[100:101], v161 offset:49152\n\t" \
  "ds_read_b64_tr_b16 v[102:103], v161 offset:51200\n\t" \
  "v_mfma_f32_16x16x32_bf16 v[140:143], v[108:111], v[80:83], v[140:143]\n\t" \
  "s_mov_b64 exec, vcc\n\t" \
  "buffer_load_dwordx4 v[72:75], v164, %[ob], s94 offen offset:2048 sc1\n\t" \
  "s_mov_b64 exec, -1\n\t" \
  "v_mfma_f32_16x16x32_bf16 v[156:159], v[108:111], v[84:87], v[156:159]\n\t" \
  "ds_read_b64_tr_b16 v[104:105], v162 offset:49152\n\t" \
  "ds_read_b64_tr_b16 v[106:107], v162 offset:51200\n\t" \
  "s_waitcnt lgkmcnt(10)\n\t" \
  "v_mfma_f32_16x16x32_bf16 v[128:131], v[112:115], v[88:91], v[128:131]\n\t" \
  "s_mov_b64 exec, vcc\n\t" \
  "buffer_load_dwordx4 v[76:79], v164, %[ob], s94 offen offset:3072 sc1\n\t" \
  "s_mov_b64 exec, -1\n\t" \
  "v_mfma_f32_16x16x32_bf16 v[144:147], v[112:115], v[92:95], v[144:147]\n\t" \
  "ds_read_b64_tr_b16 v[108:109], v163 offset:49152\n\t" \
  "ds_read_b64_tr_b16 v[110:111], v163 offset:51200\n\t" \
  "v_mfma_f32_16x16x32_bf16 v[132:135], v[116:119], v[88:91], v[132:135]\n\t" \
  "ds_read_b64_tr_b16 v[112:113], v160 offset:53248\n\t" \
  "ds_read_b32 v166, v165\n\t" \
  "v_mfma_f32_16x16x32_bf16 v[148:151], v[116:119], v[92:95], v[148:151]\n\t" \
  "ds_read_b64_tr_b16 v[114:115], v160 offset:55296\n\t" \
  "s_waitcnt lgkmcnt(11)\n\t" \
  "v_mfma_f32_16x16x32_bf16 v[136:139], v[120:123], v[88:91], v[136:139]\n\t" \
  "ds_read_b64_tr_b16 v[116:117], v161 offset:53248\n\t" \
  "v_mfma_f32_16x16x32_bf16 v[152:155], v[120:123], v[92:95], v[152:155]\n\t" \
  "ds_read_b64_tr_b16 v[118:119], v161 offset:55296\n\t" \
  "s_sub_u32 %[iss], %[iss], vcc_lo\n\t" \
  "v_mfma_f32_16x16x32_bf16 v[140:143], v[124:127], v[88:91], v[140:143]\n\t" \
  "ds_read_b64_tr_b16 v[120:121], v162 offset:53248\n\t" \
  "v_mfma_f32_16x16x32_bf16 v[156:159], v[124:127], v[92:95], v[156:159]\n\t" \
  "ds_read_b64_tr_b16 v[122:123], v162 offset:55296\n\t" \
  "s_add_u32 %[th], %[th], 1\n\t" \
  "s_cbranch_vccz QF6_%=\n\t" \
  "s_waitcnt vmcnt(16)\n\t" \
  "QG6_%=:\n\t" \
  "s_waitcnt lgkmcnt(11)\n\t" \
  "v_mfma_f32_16x16x32_bf16 v[128:131], v[96:99], v[0:3], v[128:131]\n\t" \
  "ds_read_b64_tr_b16 v[124:125], v163 offset:53248\n\t" \
  "v_readlane_b32 s94, v167, 11\n\t" \
  "v_mfma_f32_16x16x32_bf16 v[144:147], v[96:99], v[4:7], v[144:147]\n\t" \
  "ds_read_b64_tr_b16 v[126:127], v163 offset:55296\n\t" \
  "s_waitcnt lgkmcnt(7)\n\t" \
  "v_cmp_lt_u32 vcc, %[th], v166\n\t" \
  "v_mfma_f32_16x16x32_bf16 v[132:135], v[100:103], v[0:3], v[132:135]\n\t" \
  "s_mov_b64 exec, vcc\n\t" \
  "buffer_load_dwordx4 v[80:83], v164, %[ob], s94 offen offset:0 sc1\n\t" \
  "s_mov_b64 exec, -1\n\t" \
  "v_mfma_f32_16x16x32_bf16 v[148:151], v[100:103], v[4:7], v[148:151]\n\t" \
  "ds_read_b64_tr_b16 v[96:97], v160 offset:57344\n\t" \
  "ds_read_b64_tr_b16 v[98:99], v160 offset:59392\n\t" \
  "v_mfma_f32_16x16x32_bf16 v[136:139], v[104:107], v[0:3], v[136:139]\n\t" \
  "s_mov_b64 exec, vcc\n\t" \
  "buffer_load_dwordx4 v[84:87], v164, %[ob], s94 offen offset:1024 sc1\n\t" \
  "s_mov_b64 exec, -1\n\t" \
  "v_mfma_f32_16x16x32_bf16 v[152:155], v[104:107], v[4:7], v[152:155]\n\t" \
  "ds_read_b64_tr_b16 v[100:101], v161 offset:57344\n\t" \
  "ds_read_b64_tr_b16 v[102:103], v161 offset:59392\n\t" \
  "v_mfma_f32_16x16x32_bf16 v[140:143], v[108:111], v[0:3], v[140:143]\n\t" \
  "s_mov_b64 exec, vcc\n\t" \
  "buffer_load_dwordx4 v[88:91], v164, %[ob], s94 offen offset:2048 sc1\n\t" \
  "s_mov_b64 exec, -1\n\t" \
  "v_mfma_f32_16x16x32_bf16 v[156:159], v[108:111], v[4:7], v[156:159]\n\t" \
  "ds_read_b64_tr_b16 v[104:105], v162 offset:57344\n\t" \
  "ds_read_b64_tr_b16 v[106:107], v162 offset:59392\n\t" \
  "s_waitcnt lgkmcnt(10)\n\t" \
  "v_mfma_f32_16x16x32_bf16 v[128:131], v[112:115], v[8:11], v[128:131]\n\t" \
  "s_mov_b64 exec, vcc\n\t" \
  "buffer_load_dwordx4 v[92:95], v164, %[ob], s94 offen offset:3072 sc1\n\t" \
  "s_mov_b64 exec, -1\n\t" \
  "v_mfma_f32_16x16x32_bf16 v[144:147], v[112:115], v[12:15], v[144:147]\n\t" \
  "ds_read_b64_tr_b16 v[108:109], v163 offset:57344\n\t" \
  "ds_read_b64_tr_b16 v[110:111], v163 offset:59392\n\t" \
  "v_mfma_f32_16x16x32_bf16 v[132:135], v[116:119], v[8:11], v[132:135]\n\t" \
  "ds_read_b64_tr_b16 v[112:113], v160 offset:61440\n\t" \
  "ds_read_b32 v166, v165\n\t" \
  "v_mfma_f32_16x16x32_bf16 v[148:151], v[116:119], v[12:15], v[148:151]\n\t" \
  "ds_read_b64_tr_b16 v[114:115], v160 offset:63488\n\t" \
  "s_waitcnt lgkmcnt(11)\n\t" \
  "v_mfma_f32_16x16x32_bf16 v[136:139], v[120:123], v[8:11], v[136:139]\n\t" \
  "ds_read_b64_tr_b16 v[116:117], v161 offset:61440\n\t" \
  "v_mfma_f32_16x16x32_bf16 v[152:155], v[120:123], v[12:15], v[152:155]\n\t" \
  "ds_read_b64_tr_b16 v[118:119], v161 offset:63488\n\t" \
  "s_sub_u32 %[iss], %[iss], vcc_lo\n\t" \
  "v_mfma_f32_16x16x32_bf16 v[140:143], v[124:127], v[8:11], v[140:143]\n\t" \
  "ds_read_b64_tr_b16 v[120:121], v162 offset:61440\n\t" \
  "s_mov_b64 exec, 1\n\t" \
  "ds_write_b32 v165, v165 offset:76\n\t" \
  "s_mov_b64 exec, -1\n\t" \
  "v_mfma_f32_16x16x32_bf16 v[156:159], v[124:127], v[12:15], v[156:159]\n\t" \
  "ds_read_b64_tr_b16 v[122:123], v162 offset:63488\n\t" \
  "s_add_u32 %[th], %[th], 1\n\t" \
  "s_cbranch_vccz QF7_%=\n\t" \
  "s_waitcnt vmcnt(16)\n\t" \
  "QG7_%=:\n\t" \
  "s_waitcnt lgkmcnt(12)\n\t" \
  "v_mfma_f32_16x16x32_bf16 v[128:131], v[96:99], v[16:19], v[128:131]\n\t" \
  "ds_read_b64_tr_b16 v[124:125], v163 offset:61440\n\t" \
  "v_readlane_b32 s94, v167, 12\n\t" \
  "v_mfma_f32_16x16x32_bf16 v[144:147], v[96:99], v[20:23], v[144:147]\n\t" \
  "ds_read_b64_tr_b16 v[126:127], v163 offset:63488\n\t" \
  "s_waitcnt lgkmcnt(8)\n\t" \
  "v_cmp_lt_u32 vcc, %[th], v166\n\t" \
  "v_mfma_f32_16x16x32_bf16 v[132:135], v[100:103], v[16:19], v[132:135]\n\t" \
  "v_add_u32 v160, 0x10000, v160\n\t" \
  "s_mov_b64 exec, vcc\n\t" \
  "buffer_load_dwordx4 v[0:3], v164, %[ob], s94 offen offset:0 sc1\n\t" \
  "s_mov_b64 exec, -1\n\t" \
  "v_mfma_f32_16x16x32_bf16 v[148:151], v[100:103], v[20:23], v[148:151]\n\t" \
  "ds_read_b64_tr_b16 v[96:97], v160 offset:0\n\t" \
  "ds_read_b64_tr_b16 v[98:99], v160 offset:2048\n\t" \
  "v_mfma_f32_16x16x32_bf16 v[136:139], v[104:107], v[16:19], v[136:139]\n\t" \
  "v_add_u32 v161, 0x10000, v161\n\t" \
  "s_mov_b64 exec, vcc\n\t" \
  "buffer_load_dwordx4 v[4:7], v164, %[ob], s94 offen offset:1024 sc1\n\t" \
  "s_mov_b64 exec, -1\n\t" \
  "v_mfma_f32_16x16x32_bf16 v[152:155], v[104:107], v[20:23], v[152:155]\n\t" \
  "ds_read_b64_tr_b16 v[100:101], v161 offset:0\n\t" \
  "ds_read_b64_tr_b16 v[102:103], v161 offset:2048\n\t" \
  "v_mfma_f32_16x16x32_bf16 v[140:143], v[108:111], v[16:19], v[140:143]\n\t" \
  "v_add_u32 v162, 0x10000, v162\n\t" \
  "s_mov_b64 exec, vcc\n\t" \
  "buffer_load_dwordx4 v[8:11], v164, %[ob], s94 offen offset:2048 sc1\n\t" \
  "s_mov_b64 exec, -1\n\t" \
  "v_mfma_f32_16x16x32_bf16 v[156:159], v[108:111], v[20:23], v[156:159]\n\t" \
  "ds_read_b64_tr_b16 v[104:105], v162 offset:0\n\t" \
  "ds_read_b64_tr_b16 v[106:107], v162 offset:2048\n\t" \
  "s_waitcnt lgkmcnt(11)\n\t" \
  "v_mfma_f32_16x16x32_bf16 v[128:131], v[112:115], v[24:27], v[128:131]\n\t" \
  "v_add_u32 v163, 0x10000, v163\n\t" \
  "s_mov_b64 exec, vcc\n\t" \
  "buffer_load_dwordx4 v[12:15], v164, %[ob], s94 offen offset:3072 sc1\n\t" \
  "s_mov_b64 exec, -1\n\t" \
  "v_mfma_f32_16x16x32_bf16 v[144:147], v[112:115], v[28:31], v[144:147]\n\t" \
  "ds_read_b64_tr_b16 v[108:109], v163 offset:0\n\t" \
  "ds_read_b64_tr_b16 v[110:111], v163 offset:2048\n\t" \
  "v_mfma_f32_16x16x32_bf16 v[132:135], v[116:119], v[24:27], v[132:135]\n\t" \
  "ds_read_b64_tr_b16 v[112:113], v160 offset:4096\n\t" \
  "ds_read_b32 v166, v165\n\t" \
  "v_mfma_f32_16x16x32_bf16 v[148:151], v[116:119], v[28:31], v[148:151]\n\t" \
  "ds_read_b64_tr_b16 v[114:115], v160 offset:6144\n\t" \
  "s_waitcnt lgkmcnt(11)\n\t" \
  "v_mfma_f32_16x16x32_bf16 v[136:139], v[120:123], v[24:27], v[136:139]\n\t" \
  "ds_read_b64_tr_b16 v[116:117], v161 offset:4096\n\t" \
  "v_mfma_f32_16x16x32_bf16 v[152:155], v[120:123], v[28:31], v[152:155]\n\t" \
  "ds_read_b64_tr_b16 v[118:119], v161 offset:6144\n\t" \
  "s_sub_u32 %[iss], %[iss], vcc_lo\n\t" \
  "v_mfma_f32_16x16x32_bf16 v[140:143], v[124:127], v[24:27], v[140:143]\n\t" \
  "ds_read_b64_tr_b16 v[120:121], v162 offset:4096\n\t" \
  "v_mfma_f32_16x16x32_bf16 v[156:159], v[124:127], v[28:31], v[156:159]\n\t" \
  "ds_read_b64_tr_b16 v[122:123], v162 offset:6144\n\t" \
  "s_add_u32 %[th], %[th], 1\n\t" \
  "s_cbranch_vccz QF8_%=\n\t" \
  "s_waitcnt vmcnt(16)\n\t" \
  "QG8_%=:\n\t" \
  "s_waitcnt lgkmcnt(11)\n\t" \
  "v_mfma_f32_16x16x32_bf16 v[128:131], v[96:99], v[32:35], v[128:131]\n\t" \
  "ds_read_b64_tr_b16 v[124:125], v163 offset:4096\n\t" \
  "v_readlane_b32 s94, v167, 13\n\t" \
  "v_mfma_f32_16x16x32_bf16 v[144:147], v[96:99], v[36:39], v[144:147]\n\t" \
  "ds_read_b64_tr_b16 v[126:127], v163 offset:6144\n\t" \
  "s_waitcnt lgkmcnt(7)\n\t" \
  "v_cmp_lt_u32 vcc, %[th], v166\n\t" \
  "v_mfma_f32_16x16x32_bf16 v[132:135], v[100:103], v[32:35], v[132:135]\n\t" \
  "s_mov_b64 exec, vcc\n\t" \
  "buffer_load_dwordx4 v[16:19], v164, %[ob], s94 offen offset:0 sc1\n\t" \
  "s_mov_b64 exec, -1\n\t" \
  "v_mfma_f32_16x16x32_bf16 v[148:151], v[100:103], v[36:39], v[148:151]\n\t" \
  "ds_read_b64_tr_b16 v[96:97], v160 offset:8192\n\t" \
  "ds_read_b64_tr_b16 v[98:99], v160 offset:10240\n\t" \
  "v_mfma_f32_16x16x32_bf16 v[136:139], v[104:107], v[32:35], v[136:139]\n\t" \
  "s_mov_b64 exec, vcc\n\t" \
  "buffer_load_dwordx4 v[20:23], v164, %[ob], s94 offen offset:1024 sc1\n\t" \
  "s_mov_b64 exec, -1\n\t" \
  "v_mfma_f32_16x16x32_bf16 v[152:155], v[104:107], v[36:39], v[152:155]\n\t" \
  "ds_read_b64_tr_b16 v[100:101], v161 offset:8192\n\t" \
  "ds_read_b64_tr_b16 v[102:103], v161 offset:10240\n\t" \
  "v_mfma_f32_16x16x32_bf16 v[140:143], v[108:111], v[32:35], v[140:143]\n\t" \
  "s_mov_b64 exec, vcc\n\t" \
  "buffer_load_dwordx4 v[24:27], v164, %[ob], s94 offen offset:2048 sc1\n\t" \
  "s_mov_b64 exec, -1\n\t" \
  "v_mfma_f32_16x16x32_bf16 v[156:159], v[108:111], v[36:39], v[156:159]\n\t" \
  "ds_read_b64_tr_b16 v[104:105], v162 offset:8192\n\t" \
  "ds_read_b64_tr_b16 v[106:107], v162 offset:10240\n\t" \
  "s_waitcnt lgkmcnt(10)\n\t" \
  "v_mfma_f32_16x16x32_bf16 v[128:131], v[112:115], v[40:43], v[128:131]\n\t" \
  "s_mov_b64 exec, vcc\n\t" \
  "buffer_load_dwordx4 v[28:31], v164, %[ob], s94 offen offset:3072 sc1\n\t" \
  "s_mov_b64 exec, -1\n\t" \
  "v_mfma_f32_16x16x32_bf16 v[144:147], v[112:115], v[44:47], v[144:147]\n\t" \
  "ds_read_b64_tr_b16 v[108:109], v163 offset:8192\n\t" \
  "ds_read_b64_tr_b16 v[110:111], v163 offset:10240\n\t" \
  "v_mfma_f32_16x16x32_bf16 v[132:135], v[116:119], v[40:43], v[132:135]\n\t" \
  "ds_read_b64_tr_b16 v[112:113], v160 offset:12288\n\t" \
  "ds_read_b32 v166, v165\n\t" \
  "v_mfma_f32_16x16x32_bf16 v[148:151], v[116:119], v[44:47], v[148:151]\n\t" \
  "ds_read_b64_tr_b16 v[114:115], v160 offset:14336\n\t" \
  "s_waitcnt lgkmcnt(11)\n\t" \
  "v_mfma_f32_16x16x32_bf16 v[136:139], v[120:123], v[40:43], v[136:139]\n\t" \
  "ds_read_b64_tr_b16 v[116:117], v161 offset:12288\n\t" \
  "v_mfma_f32_16x16x32_bf16 v[152:155], v[120:123], v[44:47], v[152:155]\n\t" \
  "ds_read_b64_tr_b16 v[118:119], v161 offset:14336\n\t" \
  "s_sub_u32 %[iss], %[iss], vcc_lo\n\t" \
  "v_mfma_f32_16x16x32_bf16 v[140:143], v[124:127], v[40:43], v[140:143]\n\t" \
  "ds_read_b64_tr_b16 v[120:121], v162 offset:12288\n\t" \
  "v_mfma_f32_16x16x32_bf16 v[156:159], v[124:127], v[44:47], v[156:159]\n\t" \
  "ds_read_b64_tr_b16 v[122:123], v162 offset:14336\n\t" \
  "s_add_u32 %[th], %[th], 1\n\t" \
  "s_cbranch_vccz QF9_%=\n\t" \
  "s_waitcnt vmcnt(16)\n\t" \
  "QG9_%=:\n\t" \
  "s_waitcnt lgkmcnt(11)\n\t" \
  "v_mfma_f32_16x16x32_bf16 v[128:131], v[96:99], v[48:51], v[128:131]\n\t" \
  "ds_read_b64_tr_b16 v[124:125], v163 offset:12288\n\t" \
  "v_readlane_b32 s94, v167, 14\n\t" \
  "v_mfma_f32_16x16x32_bf16 v[144:147], v[96:99], v[52:55], v[144:147]\n\t" \
  "ds_read_b64_tr_b16 v[126:127], v163 offset:14336\n\t" \
  "s_waitcnt lgkmcnt(7)\n\t" \
  "v_cmp_lt_u32 vcc, %[th], v166\n\t" \
  "v_mfma_f32_16x16x32_bf16 v[132:135], v[100:103], v[48:51], v[132:135]\n\t" \
  "s_mov_b64 exec, vcc\n\t" \
  "buffer_load_dwordx4 v[32:35], v164, %[ob], s94 offen offset:0 sc1\n\t" \
  "s_mov_b64 exec, -1\n\t" \
  "v_mfma_f32_16x16x32_bf16 v[148:151], v[100:103], v[52:55], v[148:151]\n\t" \
  "ds_read_b64_tr_b16 v[96:97], v160 offset:16384\n\t" \
  "ds_read_b64_tr_b16 v[98:99], v160 offset:18432\n\t" \
  "v_mfma_f32_16x16x32_bf16 v[136:139], v[104:107], v[48:51], v[136:139]\n\t" \
  "s_mov_b64 exec, vcc\n\t" \
  "buffer_load_dwordx4 v[36:39], v164, %[ob], s94 offen offset:1024 sc1\n\t" \
  "s_mov_b64 exec, -1\n\t" \
  "v_mfma_f32_16x16x32_bf16 v[152:155], v[104:107], v[52:55], v[152:155]\n\t" \
  "ds_read_b64_tr_b16 v[100:101], v161 offset:16384\n\t" \
  "ds_read_b64_tr_b16 v[102:103], v161 offset:18432\n\t" \
  "v_mfma_f32_16x16x32_bf16 v[140:143], v[108:111], v[48:51], v[140:143]\n\t" \
  "s_mov_b64 exec, vcc\n\t" \
  "buffer_load_dwordx4 v[40:43], v164, %[ob], s94 offen offset:2048 sc1\n\t" \
  "s_mov_b64 exec, -1\n\t" \
  "v_mfma_f32_16x16x32_bf16 v[156:159], v[108:111], v[52:55], v[156:159]\n\t" \
  "ds_read_b64_tr_b16 v[104:105], v162 offset:16384\n\t" \
  "ds_read_b64_tr_b16 v[106:107], v162 offset:18432\n\t" \
  "s_waitcnt lgkmcnt(10)\n\t" \
  "v_mfma_f32_16x16x32_bf16 v[128:131], v[112:115], v[56:59], v[128:131]\n\t" \
  "s_mov_b64 exec, vcc\n\t" \
  "buffer_load_dwordx4 v[44:47], v164, %[ob], s94 offen offset:3072 sc1\n\t" \
  "s_mov_b64 exec, -1\n\t" \
  "v_mfma_f32_16x16x32_bf16 v[144:147], v[112:115], v[60:63], v[144:147]\n\t" \
  "ds_read_b64_tr_b16 v[108:109], v163 offset:16384\n\t" \
  "ds_read_b64_tr_b16 v[110:111], v163 offset:18432\n\t" \
  "v_mfma_f32_16x16x32_bf16 v[132:135], v[116:119], v[56:59], v[132:135]\n\t" \
  "ds_read_b64_tr_b16 v[112:113], v160 offset:20480\n\t" \
  "ds_read_b32 v166, v165\n\t" \
  "v_mfma_f32_16x16x32_bf16 v[148:151], v[116:119], v[60:63], v[148:151]\n\t" \
  "ds_read_b64_tr_b16 v[114:115], v160 offset:22528\n\t" \
  "s_waitcnt lgkmcnt(11)\n\t" \
  "v_mfma_f32_16x16x32_bf16 v[136:139], v[120:123], v[56:59], v[136:139]\n\t" \
  "ds_read_b64_tr_b16 v[116:117], v161 offset:20480\n\t" \
  "v_mfma_f32_16x16x32_bf16 v[152:155], v[120:123], v[60:63], v[152:155]\n\t" \
  "ds_read_b64_tr_b16 v[118:119], v161 offset:22528\n\t" \
  "s_sub_u32 %[iss], %[iss], vcc_lo\n\t" \
  "v_mfma_f32_16x16x32_bf16 v[140:143], v[124:127], v[56:59], v[140:143]\n\t" \
  "ds_read_b64_tr_b16 v[120:121], v162 offset:20480\n\t" \
  "v_mfma_f32_16x16x32_bf16 v[156:159], v[124:127], v[60:63], v[156:159]\n\t" \
  "ds_read_b64_tr_b16 v[122:123], v162 offset:22528\n\t" \
  "s_add_u32 %[th], %[th], 1\n\t" \
  "s_cbranch_vccz QF10_%=\n\t" \
  "s_waitcnt vmcnt(16)\n\t" \
  "QG10_%=:\n\t" \
  "s_waitcnt lgkmcnt(11)\n\t" \
  "v_mfma_f32_16x16x32_bf16 v[128:131], v[96:99], v[64:67], v[128:131]\n\t" \
  "ds_read_b64_tr_b16 v[124:125], v163 offset:20480\n\t" \
  "v_readlane_b32 s94, v167, 15\n\t" \
  "v_mfma_f32_16x16x32_bf16 v[144:147], v[96:99], v[68:71], v[144:147]\n\t" \
  "ds_read_b64_tr_b16 v[126:127], v163 offset:22528\n\t" \
  "s_waitcnt lgkmcnt(7)\n\t" \
  "v_cmp_lt_u32 vcc, %[th], v166\n\t" \
  "v_mfma_f32_16x16x32_bf16 v[132:135], v[100:103], v[64:67], v[132:135]\n\t" \
  "s_mov_b64 exec, vcc\n\t" \
  "buffer_load_dwordx4 v[48:51], v164, %[ob], s94 offen offset:0 sc1\n\t" \
  "s_mov_b64 exec, -1\n\t" \
  "v_mfma_f32_16x16x32_bf16 v[148:151], v[100:103], v[68:71], v[148:151]\n\t" \
  "ds_read_b64_tr_b16 v[96:97], v160 offset:24576\n\t" \
  "ds_read_b64_tr_b16 v[98:99], v160 offset:26624\n\t" \
  "v_mfma_f32_16x16x32_bf16 v[136:139], v[104:107], v[64:67], v[136:139]\n\t" \
  "s_mov_b64 exec, vcc\n\t" \
  "buffer_load_dwordx4 v[52:55], v164, %[ob], s94 offen offset:1024 sc1\n\t" \
  "s_mov_b64 exec, -1\n\t" \
  "v_mfma_f32_16x16x32_bf16 v[152:155], v[104:107], v[68:71], v[152:155]\n\t" \
  "ds_read_b64_tr_b16 v[100:101], v161 offset:24576\n\t" \
  "ds_read_b64_tr_b16 v[102:103], v161 offset:26624\n\t" \
  "v_mfma_f32_16x16x32_bf16 v[140:143], v[108:111], v[64:67], v[140:143]\n\t" \
  "s_mov_b64 exec, vcc\n\t" \
  "buffer_load_dwordx4 v[56:59], v164, %[ob], s94 offen offset:2048 sc1\n\t" \
  "s_mov_b64 exec, -1\n\t" \
  "v_mfma_f32_16x16x32_bf16 v[156:159], v[108:111], v[68:71], v[156:159]\n\t" \
  "ds_read_b64_tr_b16 v[104:105], v162 offset:24576\n\t" \
  "ds_read_b64_tr_b16 v[106:107], v162 offset:26624\n\t" \
  "s_waitcnt lgkmcnt(10)\n\t" \
  "v_mfma_f32_16x16x32_bf16 v[128:131], v[112:115], v[72:75], v[128:131]\n\t" \
  "s_mov_b64 exec, vcc\n\t" \
  "buffer_load_dwordx4 v[60:63], v164, %[ob], s94 offen offset:3072 sc1\n\t" \
  "s_mov_b64 exec, -1\n\t" \
  "v_mfma_f32_16x16x32_bf16 v[144:147], v[112:115], v[76:79], v[144:147]\n\t" \
  "ds_read_b64_tr_b16 v[108:109], v163 offset:24576\n\t" \
  "ds_read_b64_tr_b16 v[110:111], v163 offset:26624\n\t" \
  "v_mfma_f32_16x16x32_bf16 v[132:135], v[116:119], v[72:75], v[132:135]\n\t" \
  "ds_read_b64_tr_b16 v[112:113], v160 offset:28672\n\t" \
  "v_mfma_f32_16x16x32_bf16 v[148:151], v[116:119], v[76:79], v[148:151]\n\t" \
  "ds_read_b64_tr_b16 v[114:115], v160 offset:30720\n\t" \
  "s_waitcnt lgkmcnt(10)\n\t" \
  "v_mfma_f32_16x16x32_bf16 v[136:139], v[120:123], v[72:75], v[136:139]\n\t" \
  "ds_read_b64_tr_b16 v[116:117], v161 offset:28672\n\t" \
  "v_mfma_f32_16x16x32_bf16 v[152:155], v[120:123], v[76:79], v[152:155]\n\t" \
  "ds_read_b64_tr_b16 v[118:119], v161 offset:30720\n\t" \
  "s_sub_u32 %[iss], %[iss], vcc_lo\n\t" \
  "v_mfma_f32_16x16x32_bf16 v[140:143], v[124:127], v[72:75], v[140:143]\n\t" \
  "ds_read_b64_tr_b16 v[120:121], v162 offset:28672\n\t" \
  "v_mfma_f32_16x16x32_bf16 v[156:159], v[124:127], v[76:79], v[156:159]\n\t" \
  "ds_read_b64_tr_b16 v[122:123], v162 offset:30720\n\t" \
  "s_add_u32 %[th], %[th], 1\n\t" \
  "s_cbranch_vccz QF11_%=\n\t" \
  "s_waitcnt vmcnt(16)\n\t" \
  "QG11_%=:\n\t" \
  "s_waitcnt lgkmcnt(10)\n\t" \
  "v_mfma_f32_16x16x32_bf16 v[128:131], v[96:99], v[80:83], v[128:131]\n\t" \
  "ds_read_b64_tr_b16 v[124:125], v163 offset:28672\n\t" \
  "v_mfma_f32_16x16x32_bf16 v[144:147], v[96:99], v[84:87], v[144:147]\n\t" \
  "ds_read_b64_tr_b16 v[126:127], v163 offset:30720\n\t" \
  "v_mfma_f32_16x16x32_bf16 v[132:135], v[100:103], v[80:83], v[132:135]\n\t" \
  "ds_read_b64_tr_b16 v[96:97], v160 offset:32768\n\t" \
  "v_mfma_f32_16x16x32_bf16 v[148:151], v[100:103], v[84:87], v[148:151]\n\t" \
  "ds_read_b64_tr_b16 v[98:99], v160 offset:34816\n\t" \
  "s_waitcnt lgkmcnt(10)\n\t" \
  "v_mfma_f32_16x16x32_bf16 v[136:139], v[104:107], v[80:83], v[136:139]\n\t" \
  "ds_read_b64_tr_b16 v[100:101], v161 offset:32768\n\t" \
  "v_mfma_f32_16x16x32_bf16 v[152:155], v[104:107], v[84:87], v[152:155]\n\t" \
  "ds_read_b64_tr_b16 v[102:103], v161 offset:34816\n\t" \
  "v_mfma_f32_16x16x32_bf16 v[140:143], v[108:111], v[80:83], v[140:143]\n\t" \
  "ds_read_b64_tr_b16 v[104:105], v162 offset:32768\n\t" \
  "v_mfma_f32_16x16x32_bf16 v[156:159], v[108:111], v[84:87], v[156:159]\n\t" \
  "ds_read_b64_tr_b16 v[106:107], v162 offset:34816\n\t" \
  "s_waitcnt lgkmcnt(10)\n\t" \
  "v_mfma_f32_16x16x32_bf16 v[128:131], v[112:115], v[88:91], v[128:131]\n\t" \
  "ds_read_b64_tr_b16 v[108:109], v163 offset:32768\n\t" \
  "v_mfma_f32_16x16x32_bf16 v[144:147], v[112:115], v[92:95], v[144:147]\n\t" \
  "ds_read_b64_tr_b16 v[110:111], v163 offset:34816\n\t" \
  "v_mfma_f32_16x16x32_bf16 v[132:135], v[116:119], v[88:91], v[132:135]\n\t" \
  "ds_read_b64_tr_b16 v[112:113], v160 offset:36864\n\t" \
  "v_mfma_f32_16x16x32_bf16 v[148:151], v[116:119], v[92:95], v[148:151]\n\t" \
  "ds_read_b64_tr_b16 v[114:115], v160 offset:38912\n\t" \
  "s_waitcnt lgkmcnt(10)\n\t" \
  "v_mfma_f32_16x16x32_bf16 v[136:139], v[120:123], v[88:91], v[136:139]\n\t" \
  "ds_read_b64_tr_b16 v[116:117], v161 offset:36864\n\t" \
  "v_mfma_f32_16x16x32_bf16 v[152:155], v[120:123], v[92:95], v[152:155]\n\t" \
  "ds_read_b64_tr_b16 v[118:119], v161 offset:38912\n\t" \
  "v_mfma_f32_16x16x32_bf16 v[140:143], v[124:127], v[88:91], v[140:143]\n\t" \
  "ds_read_b64_tr_b16 v[120:121], v162 offset:36864\n\t" \
  "v_mfma_f32_16x16x32_bf16 v[156:159], v[124:127], v[92:95], v[156:159]\n\t" \
  "ds_read_b64_tr_b16 v[122:123], v162 offset:38912\n\t" \
  "s_cbranch_vccz QF12_%=\n\t" \
  "s_waitcnt vmcnt(12)\n\t" \
  "QG12_%=:\n\t" \
  "s_waitcnt lgkmcnt(10)\n\t" \
  "v_mfma_f32_16x16x32_bf16 v[128:131], v[96:99], v[0:3], v[128:131]\n\t" \
  "ds_read_b64_tr_b16 v[124:125], v163 offset:36864\n\t" \
  "v_mfma_f32_16x16x32_bf16 v[144:147], v[96:99], v[4:7], v[144:147]\n\t" \
  "ds_read_b64_tr_b16 v[126:127], v163 offset:38912\n\t" \
  "v_mfma_f32_16x16x32_bf16 v[132:135], v[100:103], v[0:3], v[132:135]\n\t" \
  "ds_read_b64_tr_b16 v[96:97], v160 offset:40960\n\t" \
  "v_mfma_f32_16x16x32_bf16 v[148:151], v[100:103], v[4:7], v[148:151]\n\t" \
  "ds_read_b64_tr_b16 v[98:99], v160 offset:43008\n\t" \
  "s_waitcnt lgkmcnt(10)\n\t" \
  "v_mfma_f32_16x16x32_bf16 v[136:139], v[104:107], v[0:3], v[136:139]\n\t" \
  "ds_read_b64_tr_b16 v[100:101], v161 offset:40960\n\t" \
  "v_mfma_f32_16x16x32_bf16 v[152:155], v[104:107], v[4:7], v[152:155]\n\t" \
  "ds_read_b64_tr_b16 v[102:103], v161 offset:43008\n\t" \
  "v_mfma_f32_16x16x32_bf16 v[140:143], v[108:111], v[0:3], v[140:143]\n\t" \
  "ds_read_b64_tr_b16 v[104:105], v162 offset:40960\n\t" \
  "v_mfma_f32_16x16x32_bf16 v[156:159], v[108:111], v[4:7], v[156:159]\n\t" \
  "ds_read_b64_tr_b16 v[106:107], v162 offset:43008\n\t" \
  "s_waitcnt lgkmcnt(10)\n\t" \
  "v_mfma_f32_16x16x32_bf16 v[128:131], v[112:115], v[8:11], v[128:131]\n\t" \
  "ds_read_b64_tr_b16 v[108:109], v163 offset:40960\n\t" \
  "v_mfma_f32_16x16x32_bf16 v[144:147], v[112:115], v[12:15], v[144:147]\n\t" \
  "ds_read_b64_tr_b16 v[110:111], v163 offset:43008\n\t" \
  "v_mfma_f32_16x16x32_bf16 v[132:135], v[116:119], v[8:11], v[132:135]\n\t" \
  "ds_read_b64_tr_b16 v[112:113], v160 offset:45056\n\t" \
  "v_mfma_f32_16x16x32_bf16 v[148:151], v[116:119], v[12:15], v[148:151]\n\t" \
  "ds_read_b64_tr_b16 v[114:115], v160 offset:47104\n\t" \
  "s_waitcnt lgkmcnt(10)\n\t" \
  "v_mfma_f32_16x16x32_bf16 v[136:139], v[120:123], v[8:11], v[136:139]\n\t" \
  "ds_read_b64_tr_b16 v[116:117], v161 offset:45056\n\t" \
  "v_mfma_f32_16x16x32_bf16 v[152:155], v[120:123], v[12:15], v[152:155]\n\t" \
  "ds_read_b64_tr_b16 v[118:119], v161 offset:47104\n\t" \
  "v_mfma_f32_16x16x32_bf16 v[140:143], v[124:127], v[8:11], v[140:143]\n\t" \
  "ds_read_b64_tr_b16 v[120:121], v162 offset:45056\n\t" \
  "v_mfma_f32_16x16x32_bf16 v[156:159], v[124:127], v[12:15], v[156:159]\n\t" \
  "ds_read_b64_tr_b16 v[122:123], v162 offset:47104\n\t" \
  "s_cbranch_vccz QF13_%=\n\t" \
  "s_waitcnt vmcnt(8)\n\t" \
  "QG13_%=:\n\t" \
  "s_waitcnt lgkmcnt(10)\n\t" \
  "v_mfma_f32_16x16x32_bf16 v[128:131], v[96:99], v[16:19], v[128:131]\n\t" \
  "ds_read_b64_tr_b16 v[124:125], v163 offset:45056\n\t" \
  "v_mfma_f32_16x16x32_bf16 v[144:147], v[96:99], v[20:23], v[144:147]\n\t" \
  "ds_read_b64_tr_b16 v[126:127], v163 offset:47104\n\t" \
  "v_mfma_f32_16x16x32_bf16 v[132:135], v[100:103], v[16:19], v[132:135]\n\t" \
  "ds_read_b64_tr_b16 v[96:97], v160 offset:49152\n\t" \
  "v_mfma_f32_16x16x32_bf16 v[148:151], v[100:103], v[20:23], v[148:151]\n\t" \
  "ds_read_b64_tr_b16 v[98:99], v160 offset:51200\n\t" \
  "s_waitcnt lgkmcnt(10)\n\t" \
  "v_mfma_f32_16x16x32_bf16 v[136:139], v[104:107], v[16:19], v[136:139]\n\t" \
  "ds_read_b64_tr_b16 v[100:101], v161 offset:49152\n\t" \
  "v_mfma_f32_16x16x32_bf16 v[152:155], v[104:107], v[20:23], v[152:155]\n\t" \
  "ds_read_b64_tr_b16 v[102:103], v161 offset:51200\n\t" \
  "v_mfma_f32_16x16x32_bf16 v[140:143], v[108:111], v[16:19], v[140:143]\n\t" \
  "ds_read_b64_tr_b16 v[104:105], v162 offset:49152\n\t" \
  "v_mfma_f32_16x16x32_bf16 v[156:159], v[108:111], v[20:23], v[156:159]\n\t" \
  "ds_read_b64_tr_b16 v[106:107], v162 offset:51200\n\t" \
  "s_waitcnt lgkmcnt(10)\n\t" \
  "v_mfma_f32_16x16x32_bf16 v[128:131], v[112:115], v[24:27], v[128:131]\n\t" \
  "ds_read_b64_tr_b16 v[108:109], v163 offset:49152\n\t" \
  "v_mfma_f32_16x16x32_bf16 v[144:147], v[112:115], v[28:31], v[144:147]\n\t" \
  "ds_read_b64_tr_b16 v[110:111], v163 offset:51200\n\t" \
  "v_mfma_f32_16x16x32_bf16 v[132:135], v[116:119], v[24:27], v[132:135]\n\t" \
  "ds_read_b64_tr_b16 v[112:113], v160 offset:53248\n\t" \
  "v_mfma_f32_16x16x32_bf16 v[148:151], v[116:119], v[28:31], v[148:151]\n\t" \
  "ds_read_b64_tr_b16 v[114:115], v160 offset:55296\n\t" \
  "s_waitcnt lgkmcnt(10)\n\t" \
  "v_mfma_f32_16x16x32_bf16 v[136:139], v[120:123], v[24:27], v[136:139]\n\t" \
  "ds_read_b64_tr_b16 v[116:117], v161 offset:53248\n\t" \
  "v_mfma_f32_16x16x32_bf16 v[152:155], v[120:123], v[28:31], v[152:155]\n\t" \
  "ds_read_b64_tr_b16 v[118:119], v161 offset:55296\n\t" \
  "v_mfma_f32_16x16x32_bf16 v[140:143], v[124:127], v[24:27], v[140:143]\n\t" \
  "ds_read_b64_tr_b16 v[120:121], v162 offset:53248\n\t" \
  "v_mfma_f32_16x16x32_bf16 v[156:159], v[124:127], v[28:31], v[156:159]\n\t" \
  "ds_read_b64_tr_b16 v[122:123], v162 offset:55296\n\t" \
  "s_cbranch_vccz QF14_%=\n\t" \
  "s_waitcnt vmcnt(4)\n\t" \
  "QG14_%=:\n\t" \
  "s_waitcnt lgkmcnt(10)\n\t" \
  "v_mfma_f32_16x16x32_bf16 v[128:131], v[96:99], v[32:35], v[128:131]\n\t" \
  "ds_read_b64_tr_b16 v[124:125], v163 offset:53248\n\t" \
  "v_mfma_f32_16x16x32_bf16 v[144:147], v[96:99], v[36:39], v[144:147]\n\t" \
  "ds_read_b64_tr_b16 v[126:127], v163 offset:55296\n\t" \
  "v_mfma_f32_16x16x32_bf16 v[132:135], v[100:103], v[32:35], v[132:135]\n\t" \
  "ds_read_b64_tr_b16 v[96:97], v160 offset:57344\n\t" \
  "v_mfma_f32_16x16x32_bf16 v[148:151], v[100:103], v[36:39], v[148:151]\n\t" \
  "ds_read_b64_tr_b16 v[98:99], v160 offset:59392\n\t" \
  "s_waitcnt lgkmcnt(10)\n\t" \
  "v_mfma_f32_16x16x32_bf16 v[136:139], v[104:107], v[32:35], v[136:139]\n\t" \
  "ds_read_b64_tr_b16 v[100:101], v161 offset:57344\n\t" \
  "v_mfma_f32_16x16x32_bf16 v[152:155], v[104:107], v[36:39], v[152:155]\n\t" \
  "ds_read_b64_tr_b16 v[102:103], v161 offset:59392\n\t" \
  "v_mfma_f32_16x16x32_bf16 v[140:143], v[108:111], v[32:35], v[140:143]\n\t" \
  "ds_read_b64_tr_b16 v[104:105], v162 offset:57344\n\t" \
  "v_mfma_f32_16x16x32_bf16 v[156:159], v[108:111], v[36:39], v[156:159]\n\t" \
  "ds_read_b64_tr_b16 v[106:107], v162 offset:59392\n\t" \
  "s_waitcnt lgkmcnt(10)\n\t" \
  "v_mfma_f32_16x16x32_bf16 v[128:131], v[112:115], v[40:43], v[128:131]\n\t" \
  "ds_read_b64_tr_b16 v[108:109], v163 offset:57344\n\t" \
  "v_mfma_f32_16x16x32_bf16 v[144:147], v[112:115], v[44:47], v[144:147]\n\t" \
  "ds_read_b64_tr_b16 v[110:111], v163 offset:59392\n\t" \
  "v_mfma_f32_16x16x32_bf16 v[132:135], v[116:119], v[40:43], v[132:135]\n\t" \
  "ds_read_b64_tr_b16 v[112:113], v160 offset:61440\n\t" \
  "v_mfma_f32_16x16x32_bf16 v[148:151], v[116:119], v[44:47], v[148:151]\n\t" \
  "ds_read_b64_tr_b16 v[114:115], v160 offset:63488\n\t" \
  "s_waitcnt lgkmcnt(10)\n\t" \
  "v_mfma_f32_16x16x32_bf16 v[136:139], v[120:123], v[40:43], v[136:139]\n\t" \
  "ds_read_b64_tr_b16 v[116:117], v161 offset:61440\n\t" \
  "v_mfma_f32_16x16x32_bf16 v[152:155], v[120:123], v[44:47], v[152:155]\n\t" \
  "ds_read_b64_tr_b16 v[118:119], v161 offset:63488\n\t" \
  "v_mfma_f32_16x16x32_bf16 v[140:143], v[124:127], v[40:43], v[140:143]\n\t" \
  "ds_read_b64_tr_b16 v[120:121], v162 offset:61440\n\t" \
  "v_mfma_f32_16x16x32_bf16 v[156:159], v[124:127], v[44:47], v[156:159]\n\t" \
  "ds_read_b64_tr_b16 v[122:123], v162 offset:63488\n\t" \
  "s_cbranch_vccz QF15_%=\n\t" \
  "s_waitcnt vmcnt(0)\n\t" \
  "QG15_%=:\n\t" \
  "s_waitcnt lgkmcnt(10)\n\t" \
  "v_mfma_f32_16x16x32_bf16 v[128:131], v[96:99], v[48:51], v[128:131]\n\t" \
  "ds_read_b64_tr_b16 v[124:125], v163 offset:61440\n\t" \
  "v_mfma_f32_16x16x32_bf16 v[144:147], v[96:99], v[52:55], v[144:147]\n\t" \
  "ds_read_b64_tr_b16 v[126:127], v163 offset:63488\n\t" \
  "v_mfma_f32_16x16x32_bf16 v[132:135], v[100:103], v[48:51], v[132:135]\n\t" \
  "v_mfma_f32_16x16x32_bf16 v[148:151], v[100:103], v[52:55], v[148:151]\n\t" \
  "s_waitcnt lgkmcnt(8)\n\t" \
  "v_mfma_f32_16x16x32_bf16 v[136:139], v[104:107], v[48:51], v[136:139]\n\t" \
  "v_mfma_f32_16x16x32_bf16 v[152:155], v[104:107], v[52:55], v[152:155]\n\t" \
  "v_mfma_f32_16x16x32_bf16 v[140:143], v[108:111], v[48:51], v[140:143]\n\t" \
  "v_mfma_f32_16x16x32_bf16 v[156:159], v[108:111], v[52:55], v[156:159]\n\t" \
  "s_waitcnt lgkmcnt(4)\n\t" \
  "v_mfma_f32_16x16x32_bf16 v[128:131], v[112:115], v[56:59], v[128:131]\n\t" \
  "v_mfma_f32_16x16x32_bf16 v[144:147], v[112:115], v[60:63], v[144:147]\n\t" \
  "v_mfma_f32_16x16x32_bf16 v[132:135], v[116:119], v[56:59], v[132:135]\n\t" \
  "v_mfma_f32_16x16x32_bf16 v[148:151], v[116:119], v[60:63], v[148:151]\n\t" \
  "s_waitcnt lgkmcnt(0)\n\t" \
  "v_mfma_f32_16x16x32_bf16 v[136:139], v[120:123], v[56:59], v[136:139]\n\t" \
  "v_mfma_f32_16x16x32_bf16 v[152:155], v[120:123], v[60:63], v[152:155]\n\t" \
  "v_mfma_f32_16x16x32_bf16 v[140:143], v[124:127], v[56:59], v[140:143]\n\t" \
  "v_mfma_f32_16x16x32_bf16 v[156:159], v[124:127], v[60:63], v[156:159]\n\t" \
  "s_branch QX_%=\n\t" \
  "QF1_%=:\n\t" \
  "s_mov_b32 %[c], 1\n\t" \
  "s_getpc_b64 s[92:93]\n\t" \
  "s_branch QC_%=\n\t" \
  "s_branch QG1_%=\n\t" \
  "QF2_%=:\n\t" \
  "s_mov_b32 %[c], 2\n\t" \
  "s_getpc_b64 s[92:93]\n\t" \
  "s_branch QC_%=\n\t" \
  "s_branch QG2_%=\n\t" \
  "QF3_%=:\n\t" \
  "s_mov_b32 %[c], 3\n\t" \
  "s_getpc_b64 s[92:93]\n\t" \
  "s_branch QC_%=\n\t" \
  "s_branch QG3_%=\n\t" \
  "QF4_%=:\n\t" \
  "s_mov_b32 %[c], 4\n\t" \
  "s_getpc_b64 s[92:93]\n\t" \
  "s_branch QC_%=\n\t" \
  "s_branch QG4_%=\n\t" \
  "QF5_%=:\n\t" \
  "s_mov_b32 %[c], 5\n\t" \
  "s_getpc_b64 s[92:93]\n\t" \
  "s_branch QC_%=\n\t" \
  "s_branch QG5_%=\n\t" \
  "QF6_%=:\n\t" \
  "s_mov_b32 %[c], 6\n\t" \
  "s_getpc_b64 s[92:93]\n\t" \
  "s_branch QC_%=\n\t" \
  "s_branch QG6_%=\n\t" \
  "QF7_%=:\n\t" \
  "s_mov_b32 %[c], 7\n\t" \
  "s_getpc_b64 s[92:93]\n\t" \
  "s_branch QC_%=\n\t" \
  "s_branch QG7_%=\n\t" \
  "QF8_%=:\n\t" \
  "s_mov_b32 %[c], 8\n\t" \
  "s_getpc_b64 s[92:93]\n\t" \
  "s_branch QC_%=\n\t" \
  "s_branch QG8_%=\n\t" \
  "QF9_%=:\n\t" \
  "s_mov_b32 %[c], 9\n\t" \
  "s_getpc_b64 s[92:93]\n\t" \
  "s_branch QC_%=\n\t" \
  "s_branch QG9_%=\n\t" \
  "QF10_%=:\n\t" \
  "s_mov_b32 %[c], 10\n\t" \
  "s_getpc_b64 s[92:93]\n\t" \
  "s_branch QC_%=\n\t" \
  "s_branch QG10_%=\n\t" \
  "QF11_%=:\n\t" \
  "s_mov_b32 %[c], 11\n\t" \
  "s_getpc_b64 s[92:93]\n\t" \
  "s_branch QC_%=\n\t" \
  "s_branch QG11_%=\n\t" \
  "QF12_%=:\n\t" \
  "s_mov_b32 %[c], 12\n\t" \
  "s_getpc_b64 s[92:93]\n\t" \
  "s_branch QC_%=\n\t" \
  "s_branch QG12_%=\n\t" \
  "QF13_%=:\n\t" \
  "s_mov_b32 %[c], 13\n\t" \
  "s_getpc_b64 s[92:93]\n\t" \
  "s_branch QC_%=\n\t" \
  "s_branch QG13_%=\n\t" \
  "QF14_%=:\n\t" \
  "s_mov_b32 %[c], 14\n\t" \
  "s_getpc_b64 s[92:93]\n\t" \
  "s_branch QC_%=\n\t" \
  "s_branch QG14_%=\n\t" \
  "QF15_%=:\n\t" \
  "s_mov_b32 %[c], 15\n\t" \
  "s_getpc_b64 s[92:93]\n\t" \
  "s_branch QC_%=\n\t" \
  "s_branch QG15_%=\n\t" \
  "QC_%=:\n\t" \
  "s_add_u32 s92, s92, 4\n\t" \
  "s_addc_u32 s93, s93, 0\n\t" \
  "QU_%=:\n\t" \
  "ds_read_b32 v166, v165\n\t" \
  "s_add_u32 %[t1], %[c], 5\n\t" \
  "s_waitcnt lgkmcnt(0)\n\t" \
  "v_readfirstlane_b32 %[t0], v166\n\t" \
  "s_nop 0\n\t" \
  "s_min_u32 %[t1], %[t1], %[t0]\n\t" \
  "QL_%=:\n\t" \
  "s_cmp_ge_u32 %[iss], %[t1]\n\t" \
  "s_cbranch_scc1 QD_%=\n\t" \
  "s_mov_b32 %[t0], %[iss]\n\t" \
  "s_sub_u32 %[th], %[t0], 6\n\t" \
  "s_cselect_b32 %[t0], %[t0], %[th]\n\t" \
  "s_sub_u32 %[th], %[t0], 6\n\t" \
  "s_cselect_b32 %[t0], %[t0], %[th]\n\t" \
  "s_cmp_eq_u32 %[t0], 1\n\t" \
  "s_cbranch_scc1 QS1_%=\n\t" \
  "s_cmp_eq_u32 %[t0], 2\n\t" \
  "s_cbranch_scc1 QS2_%=\n\t" \
  "s_cmp_eq_u32 %[t0], 3\n\t" \
  "s_cbranch_scc1 QS3_%=\n\t" \
  "s_cmp_eq_u32 %[t0], 4\n\t" \
  "s_cbranch_scc1 QS4_%=\n\t" \
  "s_cmp_eq_u32 %[t0], 5\n\t" \
  "s_cbranch_scc1 QS5_%=\n\t" \
  "QS0_%=:\n\t" \
  "v_readlane_b32 s94, v167, %[iss]\n\t" \
  "s_nop 4\n\t" \
  "buffer_load_dwordx4 v[0:3], v164, %[ob], s94 offen offset:0 sc1\n\t" \
  "buffer_load_dwordx4 v[4:7], v164, %[ob], s94 offen offset:1024 sc1\n\t" \
  "buffer_load_dwordx4 v[8:11], v164, %[ob], s94 offen offset:2048 sc1\n\t" \
  "buffer_load_dwordx4 v[12:15], v164, %[ob], s94 offen offset:3072 sc1\n\t" \
  "s_add_u32 %[iss], %[iss], 1\n\t" \
  "s_cmp_ge_u32 %[iss], %[t1]\n\t" \
  "s_cbranch_scc1 QD_%=\n\t" \
  "QS1_%=:\n\t" \
  "v_readlane_b32 s94, v167, %[iss]\n\t" \
  "s_nop 4\n\t" \
  "buffer_load_dwordx4 v[16:19], v164, %[ob], s94 offen offset:0 sc1\n\t" \
  "buffer_load_dwordx4 v[20:23], v164, %[ob], s94 offen offset:1024 sc1\n\t" \
  "buffer_load_dwordx4 v[24:27], v164, %[ob], s94 offen offset:2048 sc1\n\t" \
  "buffer_load_dwordx4 v[28:31], v164, %[ob], s94 offen offset:3072 sc1\n\t" \
  "s_add_u32 %[iss], %[iss], 1\n\t" \
  "s_cmp_ge_u32 %[iss], %[t1]\n\t" \
  "s_cbranch_scc1 QD_%=\n\t" \
  "QS2_%=:\n\t" \
  "v_readlane_b32 s94, v167, %[iss]\n\t" \
  "s_nop 4\n\t" \
  "buffer_load_dwordx4 v[32:35], v164, %[ob], s94 offen offset:0 sc1\n\t" \
  "buffer_load_dwordx4 v[36:39], v164, %[ob], s94 offen offset:1024 sc1\n\t" \
  "buffer_load_dwordx4 v[40:43], v164, %[ob], s94 offen offset:2048 sc1\n\t" \
  "buffer_load_dwordx4 v[44:47], v164, %[ob], s94 offen offset:3072 sc1\n\t" \
  "s_add_u32 %[iss], %[iss], 1\n\t" \
  "s_cmp_ge_u32 %[iss], %[t1]\n\t" \
  "s_cbranch_scc1 QD_%=\n\t" \
  "QS3_%=:\n\t" \
  "v_readlane_b32 s94, v167, %[iss]\n\t" \
  "s_nop 4\n\t" \
  "buffer_load_dwordx4 v[48:51], v164, %[ob], s94 offen offset:0 sc1\n\t" \
  "buffer_load_dwordx4 v[52:55], v164, %[ob], s94 offen offset:1024 sc1\n\t" \
  "buffer_load_dwordx4 v[56:59], v164, %[ob], s94 offen offset:2048 sc1\n\t" \
  "buffer_load_dwordx4 v[60:63], v164, %[ob], s94 offen offset:3072 sc1\n\t" \
  "s_add_u32 %[iss], %[iss], 1\n\t" \
  "s_cmp_ge_u32 %[iss], %[t1]\n\t" \
  "s_cbranch_scc1 QD_%=\n\t" \
  "QS4_%=:\n\t" \
  "v_readlane_b32 s94, v167, %[iss]\n\t" \
  "s_nop 4\n\t" \
  "buffer_load_dwordx4 v[64:67], v164, %[ob], s94 offen offset:0 sc1\n\t" \
  "buffer_load_dwordx4 v[68:71], v164, %[ob], s94 offen offset:1024 sc1\n\t" \
  "buffer_load_dwordx4 v[72:75], v164, %[ob], s94 offen offset:2048 sc1\n\t" \
  "buffer_load_dwordx4 v[76:79], v164, %[ob], s94 offen offset:3072 sc1\n\t" \
  "s_add_u32 %[iss], %[iss], 1\n\t" \
  "s_cmp_ge_u32 %[iss], %[t1]\n\t" \
  "s_cbranch_scc1 QD_%=\n\t" \
  "QS5_%=:\n\t" \
  "v_readlane_b32 s94, v167, %[iss]\n\t" \
  "s_nop 4\n\t" \
  "buffer_load_dwordx4 v[80:83], v164, %[ob], s94 offen offset:0 sc1\n\t" \
  "buffer_load_dwordx4 v[84:87], v164, %[ob], s94 offen offset:1024 sc1\n\t" \
  "buffer_load_dwordx4 v[88:91], v164, %[ob], s94 offen offset:2048 sc1\n\t" \
  "buffer_load_dwordx4 v[92:95], v164, %[ob], s94 offen offset:3072 sc1\n\t" \
  "s_add_u32 %[iss], %[iss], 1\n\t" \
  "s_cmp_ge_u32 %[iss], %[t1]\n\t" \
  "s_cbranch_scc0 QS0_%=\n\t" \
  "QD_%=:\n\t" \
  "s_cmp_gt_u32 %[iss], %[c]\n\t" \
  "s_cbranch_scc1 QW_%=\n\t" \
  "s_addk_i32 %[cn], 1\n\t" \
  "s_cmp_gt_u32 %[cn], 0x400000\n\t" \
  "s_cbranch_scc1 QT_%=\n\t" \
  "s_sleep 1\n\t" \
  "s_branch QU_%=\n\t" \
  "QT_%=:\n\t" \
  "s_add_u32 %[t1], %[c], 1\n\t" \
  "s_branch QL_%=\n\t" \
  "QW_%=:\n\t" \
  "s_cmp_eq_u32 %[iss], 16\n\t" \
  "s_cselect_b64 vcc, -1, 0\n\t" \
  "s_sub_u32 %[t0], %[iss], %[c]\n\t" \
  "s_cmp_eq_u32 %[t0], 5\n\t" \
  "s_cselect_b32 %[th], %[iss], 99\n\t" \
  "s_waitcnt vmcnt(16)\n\t" \
  "s_cmp_ge_u32 %[t0], 5\n\t" \
  "s_cbranch_scc1 QR_%=\n\t" \
  "s_waitcnt vmcnt(12)\n\t" \
  "s_cmp_ge_u32 %[t0], 4\n\t" \
  "s_cbranch_scc1 QR_%=\n\t" \
  "s_waitcnt vmcnt(8)\n\t" \
  "s_cmp_ge_u32 %[t0], 3\n\t" \
  "s_cbranch_scc1 QR_%=\n\t" \
  "s_waitcnt vmcnt(4)\n\t" \
  "s_cmp_ge_u32 %[t0], 2\n\t" \
  "s_cbranch_scc1 QR_%=\n\t" \
  "s_waitcnt vmcnt(0)\n\t" \
  "QR_%=:\n\t" \
  "s_setpc_b64 s[92:93]\n\t" \
  "QX_%=:\n\t" \
  "s_nop 7\n\t"
#define G3_CLOB "v0", "v1", "v2", "v3", "v4", "v5", "v6", "v7", "v8", "v9", "v10", "v11", "v12", "v13", "v14", "v15", "v16", "v17", "v18", "v19", "v20", "v21", "v22", "v23", "v24", "v25", "v26", "v27", "v28", "v29", "v30", "v31", "v32", "v33", "v34", "v35", "v36", "v37", "v38", "v39", "v40", "v41", "v42", "v43", "v44", "v45", "v46", "v47", "v48", "v49", "v50", "v51", "v52", "v53", "v54", "v55", "v56", "v57", "v58", "v59", "v60", "v61", "v62", "v63", "v64", "v65", "v66", "v67", "v68", "v69", "v70", "v71", "v72", "v73", "v74", "v75", "v76", "v77", "v78", "v79", "v80", "v81", "v82", "v83", "v84", "v85", "v86", "v87", "v88", "v89", "v90", "v91", "v92", "v93", "v94", "v95", "v96", "v97", "v98", "v99", "v100", "v101", "v102", "v103", "v104", "v105", "v106", "v107", "v108", "v109", "v110", "v111", "v112", "v113", "v114", "v115", "v116", "v117", "v118", "v119", "v120", "v121", "v122", "v123", "v124", "v125", "v126", "v127", "v166", "s88", "s89", "s92", "s93", "s94"

#define PP_ASM \
  "v_mov_b32 v60, 0\n\t" \
  "s_mov_b32 %[t0], %[n0]\n\t" \
  "s_cmp_eq_u32 %[t0], 0\n\t" \
  "s_cbranch_scc1 PAx_%=\n\t" \
  "PA_%=:\n\t" \
  "s_barrier\n\t" \
  "s_sub_u32 %[t0], %[t0], 1\n\t" \
  "s_cmp_lg_u32 %[t0], 0\n\t" \
  "s_cbranch_scc1 PA_%=\n\t" \
  "PAx_%=:\n\t" \
  "s_mov_b32 m0, %[shd]\n\t" \
  "global_load_dword v160, v60, %[fl] sc1\n\t" \
  "global_load_lds_dwordx4 v163, %[shp]\n\t" \
  "s_mov_b32 m0, %[tgd]\n\t" \
  "s_mov_b64 exec, %[em]\n\t" \
  "global_load_lds_dword v60, %[fl] sc1\n\t" \
  "s_mov_b64 exec, -1\n\t" \
  "global_load_ushort v161, v60, %[hp]\n\t" \
  "global_load_ushort v162, v60, %[dp]\n\t" \
  "global_load_dwordx4 v[0:3], v152, %[kb0]\n\t" \
  "global_load_dwordx4 v[4:7], v153, %[kb0]\n\t" \
  "global_load_dwordx4 v[8:11], v154, %[kb0]\n\t" \
  "global_load_dwordx4 v[12:15], v155, %[kb3] offset:0\n\t" \
  "global_load_dwordx4 v[28:31], v155, %[vb0] offset:0\n\t" \
  "s_setprio 2\n\t" \
  "s_barrier\n\t" \
  "s_barrier\n\t" \
  "s_waitcnt vmcnt(5)\n\t" \
  "ds_read_b128 v[128:131], v156 offset:0\n\t" \
  "ds_read_b128 v[132:135], v156 offset:16\n\t" \
  "ds_read_b128 v[136:139], v156 offset:32\n\t" \
  "ds_read_b128 v[140:143], v156 offset:48\n\t" \
  "s_waitcnt lgkmcnt(0)\n\t" \
  "v_lshlrev_b32 v64, 16, v128\n\t" \
  "v_and_b32 v65, 0xffff0000, v128\n\t" \
  "v_lshlrev_b32 v66, 16, v129\n\t" \
  "v_and_b32 v67, 0xffff0000, v129\n\t" \
  "v_lshlrev_b32 v68, 16, v130\n\t" \
  "v_and_b32 v69, 0xffff0000, v130\n\t" \
  "v_lshlrev_b32 v70, 16, v131\n\t" \
  "v_and_b32 v71, 0xffff0000, v131\n\t" \
  "v_lshlrev_b32 v72, 16, v132\n\t" \
  "v_and_b32 v73, 0xffff0000, v132\n\t" \
  "v_lshlrev_b32 v74, 16, v133\n\t" \
  "v_and_b32 v75, 0xffff0000, v133\n\t" \
  "v_lshlrev_b32 v76, 16, v134\n\t" \
  "v_and_b32 v77, 0xffff0000, v134\n\t" \
  "v_lshlrev_b32 v78, 16, v135\n\t" \
  "v_and_b32 v79, 0xffff0000, v135\n\t" \
  "v_lshlrev_b32 v80, 16, v136\n\t" \
  "v_and_b32 v81, 0xffff0000, v136\n\t" \
  "v_lshlrev_b32 v82, 16, v137\n\t" \
  "v_and_b32 v83, 0xffff0000, v137\n\t" \
  "v_lshlrev_b32 v84, 16, v138\n\t" \
  "v_and_b32 v85, 0xffff0000, v138\n\t" \
  "v_lshlrev_b32 v86, 16, v139\n\t" \
  "v_and_b32 v87, 0xffff0000, v139\n\t" \
  "v_lshlrev_b32 v88, 16, v140\n\t" \
  "v_and_b32 v89, 0xffff0000, v140\n\t" \
  "v_lshlrev_b32 v90, 16, v141\n\t" \
  "v_and_b32 v91, 0xffff0000, v141\n\t" \
  "v_lshlrev_b32 v92, 16, v142\n\t" \
  "v_and_b32 v93, 0xffff0000, v142\n\t" \
  "v_lshlrev_b32 v94, 16, v143\n\t" \
  "v_and_b32 v95, 0xffff0000, v143\n\t" \
  "s_waitcnt vmcnt(4)\n\t" \
  "s_mov_b64 exec, %[zm]\n\t" \
  "v_mov_b32 v0, 0\n\t" \
  "v_mov_b32 v1, 0\n\t" \
  "v_mov_b32 v2, 0\n\t" \
  "v_mov_b32 v3, 0\n\t" \
  "s_mov_b64 exec, -1\n\t" \
  "v_lshlrev_b32 v128, 16, v0\n\t" \
  "v_and_b32 v129, 0xffff0000, v0\n\t" \
  "s_setprio 3\n\t" \
  "s_barrier\n\t" \
  "global_load_dwordx4 v[16:19], v155, %[kb3] offset:2048\n\t" \
  "global_load_dwordx4 v[32:35], v155, %[vb0] offset:2048\n\t" \
  "global_load_dwordx4 v[20:23], v155, %[kb5] offset:0\n\t" \
  "s_setprio 2\n\t" \
  "v_lshlrev_b32 v130, 16, v1\n\t" \
  "v_and_b32 v131, 0xffff0000, v1\n\t" \
  "v_lshlrev_b32 v132, 16, v2\n\t" \
  "v_and_b32 v133, 0xffff0000, v2\n\t" \
  "v_lshlrev_b32 v134, 16, v3\n\t" \
  "v_and_b32 v135, 0xffff0000, v3\n\t" \
  "v_mul_f32 v96, v128, v67\n\t" \
  "v_mul_f32 v97, v129, v71\n\t" \
  "v_mul_f32 v98, v130, v75\n\t" \
  "v_mul_f32 v99, v131, v79\n\t" \
  "v_mul_f32 v100, v132, v83\n\t" \
  "v_mul_f32 v101, v133, v87\n\t" \
  "v_mul_f32 v102, v134, v91\n\t" \
  "v_mul_f32 v103, v135, v95\n\t" \
  "s_waitcnt vmcnt(6)\n\t" \
  "s_mov_b64 exec, %[zm]\n\t" \
  "v_mov_b32 v4, 0\n\t" \
  "v_mov_b32 v5, 0\n\t" \
  "v_mov_b32 v6, 0\n\t" \
  "v_mov_b32 v7, 0\n\t" \
  "s_mov_b64 exec, -1\n\t" \
  "v_lshlrev_b32 v128, 16, v4\n\t" \
  "v_and_b32 v129, 0xffff0000, v4\n\t" \
  "v_lshlrev_b32 v130, 16, v5\n\t" \
  "v_and_b32 v131, 0xffff0000, v5\n\t" \
  "v_lshlrev_b32 v132, 16, v6\n\t" \
  "v_and_b32 v133, 0xffff0000, v6\n\t" \
  "v_lshlrev_b32 v134, 16, v7\n\t" \
  "v_and_b32 v135, 0xffff0000, v7\n\t" \
  "v_fmac_f32 v96, v128, v66\n\t" \
  "v_fmac_f32 v97, v129, v70\n\t" \
  "v_fmac_f32 v98, v130, v74\n\t" \
  "v_fmac_f32 v99, v131, v78\n\t" \
  "v_fmac_f32 v100, v132, v82\n\t" \
  "v_fmac_f32 v101, v133, v86\n\t" \
  "v_fmac_f32 v102, v134, v90\n\t" \
  "v_fmac_f32 v103, v135, v94\n\t" \
  "v_mul_f32 v104, v128, v67\n\t" \
  "v_mul_f32 v105, v129, v71\n\t" \
  "v_mul_f32 v106, v130, v75\n\t" \
  "v_mul_f32 v107, v131, v79\n\t" \
  "v_mul_f32 v108, v132, v83\n\t" \
  "v_mul_f32 v109, v133, v87\n\t" \
  "v_mul_f32 v110, v134, v91\n\t" \
  "v_mul_f32 v111, v135, v95\n\t" \
  "s_waitcnt vmcnt(5)\n\t" \
  "s_mov_b64 exec, %[zm]\n\t" \
  "v_mov_b32 v8, 0\n\t" \
  "v_mov_b32 v9, 0\n\t" \
  "v_mov_b32 v10, 0\n\t" \
  "v_mov_b32 v11, 0\n\t" \
  "s_mov_b64 exec, -1\n\t" \
  "s_barrier\n\t" \
  "v_lshlrev_b32 v128, 16, v8\n\t" \
  "v_and_b32 v129, 0xffff0000, v8\n\t" \
  "v_lshlrev_b32 v130, 16, v9\n\t" \
  "v_and_b32 v131, 0xffff0000, v9\n\t" \
  "v_lshlrev_b32 v132, 16, v10\n\t" \
  "v_and_b32 v133, 0xffff0000, v10\n\t" \
  "v_lshlrev_b32 v134, 16, v11\n\t" \
  "v_and_b32 v135, 0xffff0000, v11\n\t" \
  "v_fmac_f32 v96, v128, v65\n\t" \
  "v_fmac_f32 v97, v129, v69\n\t" \
  "v_fmac_f32 v98, v130, v73\n\t" \
  "v_fmac_f32 v99, v131, v77\n\t" \
  "v_fmac_f32 v100, v132, v81\n\t" \
  "v_fmac_f32 v101, v133, v85\n\t" \
  "v_fmac_f32 v102, v134, v89\n\t" \
  "v_fmac_f32 v103, v135, v93\n\t" \
  "v_fmac_f32 v104, v128, v66\n\t" \
  "v_fmac_f32 v105, v129, v70\n\t" \
  "v_fmac_f32 v106, v130, v74\n\t" \
  "v_fmac_f32 v107, v131, v78\n\t" \
  "v_fmac_f32 v108, v132, v82\n\t" \
  "v_fmac_f32 v109, v133, v86\n\t" \
  "v_fmac_f32 v110, v134, v90\n\t" \
  "v_fmac_f32 v111, v135, v94\n\t" \
  "v_mul_f32 v112, v128, v67\n\t" \
  "v_mul_f32 v113, v129, v71\n\t" \
  "v_mul_f32 v114, v130, v75\n\t" \
  "v_mul_f32 v115, v131, v79\n\t" \
  "v_mul_f32 v116, v132, v83\n\t" \
  "v_mul_f32 v117, v133, v87\n\t" \
  "v_mul_f32 v118, v134, v91\n\t" \
  "v_mul_f32 v119, v135, v95\n\t" \
  "s_waitcnt vmcnt(4)\n\t" \
  "v_lshlrev_b32 v128, 16, v12\n\t" \
  "v_and_b32 v129, 0xffff0000, v12\n\t" \
  "v_lshlrev_b32 v130, 16, v13\n\t" \
  "v_and_b32 v131, 0xffff0000, v13\n\t" \
  "v_lshlrev_b32 v132, 16, v14\n\t" \
  "v_and_b32 v133, 0xffff0000, v14\n\t" \
  "v_lshlrev_b32 v134, 16, v15\n\t" \
  "v_and_b32 v135, 0xffff0000, v15\n\t" \
  "s_setprio 3\n\t" \
  "s_barrier\n\t" \
  "global_load_dwordx4 v[36:39], v155, %[vb2] offset:0\n\t" \
  "global_load_dwordx4 v[24:27], v155, %[kb5] offset:2048\n\t" \
  "global_load_dwordx4 v[40:43], v155, %[vb2] offset:2048\n\t" \
  "s_setprio 2\n\t" \
  "v_fmac_f32 v96, v128, v64\n\t" \
  "v_fmac_f32 v97, v129, v68\n\t" \
  "v_fmac_f32 v98, v130, v72\n\t" \
  "v_fmac_f32 v99, v131, v76\n\t" \
  "v_fmac_f32 v100, v132, v80\n\t" \
  "v_fmac_f32 v101, v133, v84\n\t" \
  "v_fmac_f32 v102, v134, v88\n\t" \
  "v_fmac_f32 v103, v135, v92\n\t" \
  "v_fmac_f32 v104, v128, v65\n\t" \
  "v_fmac_f32 v105, v129, v69\n\t" \
  "v_fmac_f32 v106, v130, v73\n\t" \
  "v_fmac_f32 v107, v131, v77\n\t" \
  "v_fmac_f32 v108, v132, v81\n\t" \
  "v_fmac_f32 v109, v133, v85\n\t" \
  "v_fmac_f32 v110, v134, v89\n\t" \
  "v_fmac_f32 v111, v135, v93\n\t" \
  "v_fmac_f32 v112, v128, v66\n\t" \
  "v_fmac_f32 v113, v129, v70\n\t" \
  "v_fmac_f32 v114, v130, v74\n\t" \
  "v_fmac_f32 v115, v131, v78\n\t" \
  "v_fmac_f32 v116, v132, v82\n\t" \
  "v_fmac_f32 v117, v133, v86\n\t" \
  "v_fmac_f32 v118, v134, v90\n\t" \
  "v_fmac_f32 v119, v135, v94\n\t" \
  "v_mul_f32 v120, v128, v67\n\t" \
  "v_mul_f32 v121, v129, v71\n\t" \
  "v_mul_f32 v122, v130, v75\n\t" \
  "v_mul_f32 v123, v131, v79\n\t" \
  "v_mul_f32 v124, v132, v83\n\t" \
  "v_mul_f32 v125, v133, v87\n\t" \
  "v_mul_f32 v126, v134, v91\n\t" \
  "v_mul_f32 v127, v135, v95\n\t" \
  "s_waitcnt vmcnt(6)\n\t" \
  "v_lshlrev_b32 v128, 16, v28\n\t" \
  "v_and_b32 v129, 0xffff0000, v28\n\t" \
  "v_lshlrev_b32 v130, 16, v29\n\t" \
  "v_and_b32 v131, 0xffff0000, v29\n\t" \
  "v_lshlrev_b32 v132, 16, v30\n\t" \
  "v_and_b32 v133, 0xffff0000, v30\n\t" \
  "v_lshlrev_b32 v134, 16, v31\n\t" \
  "v_and_b32 v135, 0xffff0000, v31\n\t" \
  "s_barrier\n\t" \
  "v_mul_f32 v96, v96, v128\n\t" \
  "v_mul_f32 v97, v97, v129\n\t" \
  "v_mul_f32 v98, v98, v130\n\t" \
  "v_mul_f32 v99, v99, v131\n\t" \
  "v_mul_f32 v100, v100, v132\n\t" \
  "v_mul_f32 v101, v101, v133\n\t" \
  "v_mul_f32 v102, v102, v134\n\t" \
  "v_mul_f32 v103, v103, v135\n\t" \
  "v_cvt_pk_bf16_f32 v136, v96, v97\n\t" \
  "v_cvt_pk_bf16_f32 v137, v98, v99\n\t" \
  "v_cvt_pk_bf16_f32 v138, v100, v101\n\t" \
  "v_cvt_pk_bf16_f32 v139, v102, v103\n\t" \
  "v_mov_b32_dpp v128, v136 row_ror:8 row_mask:0xf bank_mask:0xf\n\t" \
  "v_mov_b32_dpp v129, v137 row_ror:8 row_mask:0xf bank_mask:0xf\n\t" \
  "v_mov_b32_dpp v130, v138 row_ror:8 row_mask:0xf bank_mask:0xf\n\t" \
  "v_mov_b32_dpp v131, v139 row_ror:8 row_mask:0xf bank_mask:0xf\n\t" \
  "v_perm_b32 v136, v128, v136, v157\n\t" \
  "v_perm_b32 v137, v129, v137, v157\n\t" \
  "v_perm_b32 v138, v130, v138, v157\n\t" \
  "v_perm_b32 v139, v131, v139, v157\n\t" \
  "v_permlane16_swap_b32 v136, v137\n\t" \
  "s_nop 0\n\t" \
  "v_permlane16_swap_b32 v138, v139\n\t" \
  "s_nop 1\n\t" \
  "v_permlane32_swap_b32 v136, v138\n\t" \
  "v_permlane32_swap_b32 v137, v139\n\t" \
  "s_waitcnt vmcnt(5)\n\t" \
  "v_lshlrev_b32 v128, 16, v16\n\t" \
  "v_and_b32 v129, 0xffff0000, v16\n\t" \
  "v_lshlrev_b32 v130, 16, v17\n\t" \
  "v_and_b32 v131, 0xffff0000, v17\n\t" \
  "v_lshlrev_b32 v132, 16, v18\n\t" \
  "v_and_b32 v133, 0xffff0000, v18\n\t" \
  "v_lshlrev_b32 v134, 16, v19\n\t" \
  "v_and_b32 v135, 0xffff0000, v19\n\t" \
  "v_fmac_f32 v104, v128, v64\n\t" \
  "v_fmac_f32 v105, v129, v68\n\t" \
  "v_fmac_f32 v106, v130, v72\n\t" \
  "v_fmac_f32 v107, v131, v76\n\t" \
  "v_fmac_f32 v108, v132, v80\n\t" \
  "v_fmac_f32 v109, v133, v84\n\t" \
  "s_setprio 3\n\t" \
  "s_barrier\n\t" \
  "global_store_dwordx4 v159, v[136:139], %[sb] sc1\n\t" \
  "v_add_u32 v158, 0x80, v159\n\t" \
  "v_bfi_b32 v159, %[mk], v158, v159\n\t" \
  "s_setprio 2\n\t" \
  "v_fmac_f32 v110, v134, v88\n\t" \
  "v_fmac_f32 v111, v135, v92\n\t" \
  "v_fmac_f32 v112, v128, v65\n\t" \
  "v_fmac_f32 v113, v129, v69\n\t" \
  "v_fmac_f32 v114, v130, v73\n\t" \
  "v_fmac_f32 v115, v131, v77\n\t" \
  "v_fmac_f32 v116, v132, v81\n\t" \
  "v_fmac_f32 v117, v133, v85\n\t" \
  "v_fmac_f32 v118, v134, v89\n\t" \
  "v_fmac_f32 v119, v135, v93\n\t" \
  "v_fmac_f32 v120, v128, v66\n\t" \
  "v_fmac_f32 v121, v129, v70\n\t" \
  "v_fmac_f32 v122, v130, v74\n\t" \
  "v_fmac_f32 v123, v131, v78\n\t" \
  "v_fmac_f32 v124, v132, v82\n\t" \
  "v_fmac_f32 v125, v133, v86\n\t" \
  "v_fmac_f32 v126, v134, v90\n\t" \
  "v_fmac_f32 v127, v135, v94\n\t" \
  "s_waitcnt vmcnt(5)\n\t" \
  "v_lshlrev_b32 v128, 16, v32\n\t" \
  "v_and_b32 v129, 0xffff0000, v32\n\t" \
  "v_lshlrev_b32 v130, 16, v33\n\t" \
  "v_and_b32 v131, 0xffff0000, v33\n\t" \
  "v_lshlrev_b32 v132, 16, v34\n\t" \
  "v_and_b32 v133, 0xffff0000, v34\n\t" \
  "v_lshlrev_b32 v134, 16, v35\n\t" \
  "v_and_b32 v135, 0xffff0000, v35\n\t" \
  "v_mul_f32 v104, v104, v128\n\t" \
  "v_mul_f32 v105, v105, v129\n\t" \
  "v_mul_f32 v106, v106, v130\n\t" \
  "v_mul_f32 v107, v107, v131\n\t" \
  "v_mul_f32 v108, v108, v132\n\t" \
  "v_mul_f32 v109, v109, v133\n\t" \
  "v_mul_f32 v110, v110, v134\n\t" \
  "v_mul_f32 v111, v111, v135\n\t" \
  "v_cvt_pk_bf16_f32 v140, v104, v105\n\t" \
  "v_cvt_pk_bf16_f32 v141, v106, v107\n\t" \
  "v_cvt_pk_bf16_f32 v142, v108, v109\n\t" \
  "v_cvt_pk_bf16_f32 v143, v110, v111\n\t" \
  "v_mov_b32_dpp v128, v140 row_ror:8 row_mask:0xf bank_mask:0xf\n\t" \
  "v_mov_b32_dpp v129, v141 row_ror:8 row_mask:0xf bank_mask:0xf\n\t" \
  "s_barrier\n\t" \
  "v_mov_b32_dpp v130, v142 row_ror:8 row_mask:0xf bank_mask:0xf\n\t" \
  "v_mov_b32_dpp v131, v143 row_ror:8 row_mask:0xf bank_mask:0xf\n\t" \
  "v_perm_b32 v140, v128, v140, v157\n\t" \
  "v_perm_b32 v141, v129, v141, v157\n\t" \
  "v_perm_b32 v142, v130, v142, v157\n\t" \
  "v_perm_b32 v143, v131, v143, v157\n\t" \
  "v_permlane16_swap_b32 v140, v141\n\t" \
  "s_nop 0\n\t" \
  "v_permlane16_swap_b32 v142, v143\n\t" \
  "s_nop 1\n\t" \
  "v_permlane32_swap_b32 v140, v142\n\t" \
  "v_permlane32_swap_b32 v141, v143\n\t" \
  "s_waitcnt vmcnt(4)\n\t" \
  "v_lshlrev_b32 v128, 16, v20\n\t" \
  "v_and_b32 v129, 0xffff0000, v20\n\t" \
  "v_lshlrev_b32 v130, 16, v21\n\t" \
  "v_and_b32 v131, 0xffff0000, v21\n\t" \
  "v_lshlrev_b32 v132, 16, v22\n\t" \
  "v_and_b32 v133, 0xffff0000, v22\n\t" \
  "v_lshlrev_b32 v134, 16, v23\n\t" \
  "v_and_b32 v135, 0xffff0000, v23\n\t" \
  "v_fmac_f32 v112, v128, v64\n\t" \
  "v_fmac_f32 v113, v129, v68\n\t" \
  "v_fmac_f32 v114, v130, v72\n\t" \
  "v_fmac_f32 v115, v131, v76\n\t" \
  "v_fmac_f32 v116, v132, v80\n\t" \
  "v_fmac_f32 v117, v133, v84\n\t" \
  "v_fmac_f32 v118, v134, v88\n\t" \
  "v_fmac_f32 v119, v135, v92\n\t" \
  "v_fmac_f32 v120, v128, v65\n\t" \
  "v_fmac_f32 v121, v129, v69\n\t" \
  "v_fmac_f32 v122, v130, v73\n\t" \
  "v_fmac_f32 v123, v131, v77\n\t" \
  "v_fmac_f32 v124, v132, v81\n\t" \
  "v_fmac_f32 v125, v133, v85\n\t" \
  "v_fmac_f32 v126, v134, v89\n\t" \
  "v_fmac_f32 v127, v135, v93\n\t" \
  "s_waitcnt vmcnt(3)\n\t" \
  "v_lshlrev_b32 v128, 16, v36\n\t" \
  "v_and_b32 v129, 0xffff0000, v36\n\t" \
  "v_lshlrev_b32 v130, 16, v37\n\t" \
  "v_and_b32 v131, 0xffff0000, v37\n\t" \
  "s_setprio 3\n\t" \
  "s_barrier\n\t" \
  "global_store_dwordx4 v159, v[140:143], %[sb] sc1\n\t" \
  "v_add_u32 v158, 0x80, v159\n\t" \
  "v_bfi_b32 v159, %[mk], v158, v159\n\t" \
  "s_mov_b32 m0, %[hd]\n\t" \
  "s_nop 0\n\t" \
  "global_load_lds_dwordx4 v163, %[hp] offset:0\n\t" \
  "global_load_lds_dwordx4 v163, %[hp] offset:1024\n\t" \
  "global_load_lds_dwordx4 v163, %[hp] offset:2048\n\t" \
  "global_load_lds_dwordx4 v163, %[hp] offset:3072\n\t" \
  "s_setprio 2\n\t" \
  "v_lshlrev_b32 v132, 16, v38\n\t" \
  "v_and_b32 v133, 0xffff0000, v38\n\t" \
  "v_lshlrev_b32 v134, 16, v39\n\t" \
  "v_and_b32 v135, 0xffff0000, v39\n\t" \
  "v_mul_f32 v112, v112, v128\n\t" \
  "v_mul_f32 v113, v113, v129\n\t" \
  "v_mul_f32 v114, v114, v130\n\t" \
  "v_mul_f32 v115, v115, v131\n\t" \
  "v_mul_f32 v116, v116, v132\n\t" \
  "v_mul_f32 v117, v117, v133\n\t" \
  "v_mul_f32 v118, v118, v134\n\t" \
  "v_mul_f32 v119, v119, v135\n\t" \
  "v_cvt_pk_bf16_f32 v144, v112, v113\n\t" \
  "v_cvt_pk_bf16_f32 v145, v114, v115\n\t" \
  "v_cvt_pk_bf16_f32 v146, v116, v117\n\t" \
  "v_cvt_pk_bf16_f32 v147, v118, v119\n\t" \
  "v_mov_b32_dpp v128, v144 row_ror:8 row_mask:0xf bank_mask:0xf\n\t" \
  "v_mov_b32_dpp v129, v145 row_ror:8 row_mask:0xf bank_mask:0xf\n\t" \
  "v_mov_b32_dpp v130, v146 row_ror:8 row_mask:0xf bank_mask:0xf\n\t" \
  "v_mov_b32_dpp v131, v147 row_ror:8 row_mask:0xf bank_mask:0xf\n\t" \
  "v_perm_b32 v144, v128, v144, v157\n\t" \
  "v_perm_b32 v145, v129, v145, v157\n\t" \
  "v_perm_b32 v146, v130, v146, v157\n\t" \
  "v_perm_b32 v147, v131, v147, v157\n\t" \
  "v_permlane16_swap_b32 v144, v145\n\t" \
  "s_nop 0\n\t" \
  "v_permlane16_swap_b32 v146, v147\n\t" \
  "s_nop 1\n\t" \
  "v_permlane32_swap_b32 v144, v146\n\t" \
  "v_permlane32_swap_b32 v145, v147\n\t" \
  "s_waitcnt vmcnt(7)\n\t" \
  "v_lshlrev_b32 v128, 16, v24\n\t" \
  "v_and_b32 v129, 0xffff0000, v24\n\t" \
  "v_lshlrev_b32 v130, 16, v25\n\t" \
  "v_and_b32 v131, 0xffff0000, v25\n\t" \
  "v_lshlrev_b32 v132, 16, v26\n\t" \
  "v_and_b32 v133, 0xffff0000, v26\n\t" \
  "v_lshlrev_b32 v134, 16, v27\n\t" \
  "v_and_b32 v135, 0xffff0000, v27\n\t" \
  "v_fmac_f32 v120, v128, v64\n\t" \
  "v_fmac_f32 v121, v129, v68\n\t" \
  "s_setprio 3\n\t" \
  "s_barrier\n\t" \
  "global_store_dwordx4 v159, v[144:147], %[sb] sc1\n\t" \
  "v_add_u32 v158, 0x80, v159\n\t" \
  "v_bfi_b32 v159, %[mk], v158, v159\n\t" \
  "s_setprio 2\n\t" \
  "v_fmac_f32 v122, v130, v72\n\t" \
  "v_fmac_f32 v123, v131, v76\n\t" \
  "v_fmac_f32 v124, v132, v80\n\t" \
  "v_fmac_f32 v125, v133, v84\n\t" \
  "v_fmac_f32 v126, v134, v88\n\t" \
  "v_fmac_f32 v127, v135, v92\n\t" \
  "s_waitcnt vmcnt(7)\n\t" \
  "v_lshlrev_b32 v128, 16, v40\n\t" \
  "v_and_b32 v129, 0xffff0000, v40\n\t" \
  "v_lshlrev_b32 v130, 16, v41\n\t" \
  "v_and_b32 v131, 0xffff0000, v41\n\t" \
  "v_lshlrev_b32 v132, 16, v42\n\t" \
  "v_and_b32 v133, 0xffff0000, v42\n\t" \
  "v_lshlrev_b32 v134, 16, v43\n\t" \
  "v_and_b32 v135, 0xffff0000, v43\n\t" \
  "v_mul_f32 v120, v120, v128\n\t" \
  "v_mul_f32 v121, v121, v129\n\t" \
  "v_mul_f32 v122, v122, v130\n\t" \
  "v_mul_f32 v123, v123, v131\n\t" \
  "v_mul_f32 v124, v124, v132\n\t" \
  "v_mul_f32 v125, v125, v133\n\t" \
  "v_mul_f32 v126, v126, v134\n\t" \
  "v_mul_f32 v127, v127, v135\n\t" \
  "v_cvt_pk_bf16_f32 v148, v120, v121\n\t" \
  "v_cvt_pk_bf16_f32 v149, v122, v123\n\t" \
  "v_cvt_pk_bf16_f32 v150, v124, v125\n\t" \
  "v_cvt_pk_bf16_f32 v151, v126, v127\n\t" \
  "v_mov_b32_dpp v128, v148 row_ror:8 row_mask:0xf bank_mask:0xf\n\t" \
  "v_mov_b32_dpp v129, v149 row_ror:8 row_mask:0xf bank_mask:0xf\n\t" \
  "v_mov_b32_dpp v130, v150 row_ror:8 row_mask:0xf bank_mask:0xf\n\t" \
  "v_mov_b32_dpp v131, v151 row_ror:8 row_mask:0xf bank_mask:0xf\n\t" \
  "v_perm_b32 v148, v128, v148, v157\n\t" \
  "v_perm_b32 v149, v129, v149, v157\n\t" \
  "v_perm_b32 v150, v130, v150, v157\n\t" \
  "v_perm_b32 v151, v131, v151, v157\n\t" \
  "v_permlane16_swap_b32 v148, v149\n\t" \
  "s_nop 0\n\t" \
  "v_permlane16_swap_b32 v150, v151\n\t" \
  "s_nop 1\n\t" \
  "v_permlane32_swap_b32 v148, v150\n\t" \
  "v_permlane32_swap_b32 v149, v151\n\t" \
  "s_setprio 3\n\t" \
  "s_barrier\n\t" \
  "global_store_dwordx4 v159, v[148:151], %[sb] sc1\n\t" \
  "s_setprio 2\n\t" \
  "s_waitcnt vmcnt(2)\n\t" \
  "ds_read_b64_tr_b16 v[64:65], v165 offset:32\n\t" \
  "ds_read_b64_tr_b16 v[66:67], v165 offset:0\n\t" \
  "ds_read_b64_tr_b16 v[68:69], v165 offset:288\n\t" \
  "ds_read_b64_tr_b16 v[70:71], v165 offset:256\n\t" \
  "ds_read_b64_tr_b16 v[72:73], v165 offset:544\n\t" \
  "ds_read_b64_tr_b16 v[74:75], v165 offset:512\n\t" \
  "ds_read_b64_tr_b16 v[76:77], v165 offset:800\n\t" \
  "ds_read_b64_tr_b16 v[78:79], v165 offset:768\n\t" \
  "v_mov_b32 v24, v166\n\t" \
  "v_mov_b32 v25, v167\n\t" \
  "v_mov_b32 v63, 0xc0\n\t" \
  "v_add_u32 v158, 64, v24\n\t" \
  "v_bfi_b32 v26, v63, v158, v24\n\t" \
  "v_add_u32 v158, 64, v25\n\t" \
  "v_bfi_b32 v27, v63, v158, v25\n\t" \
  "v_add_u32 v158, 64, v26\n\t" \
  "v_bfi_b32 v28, v63, v158, v26\n\t" \
  "v_add_u32 v158, 64, v27\n\t" \
  "v_bfi_b32 v29, v63, v158, v27\n\t" \
  "v_add_u32 v158, 64, v28\n\t" \
  "v_bfi_b32 v30, v63, v158, v28\n\t" \
  "v_add_u32 v158, 64, v29\n\t" \
  "v_bfi_b32 v31, v63, v158, v29\n\t" \
  "s_barrier\n\t" \
  "ds_read_b64_tr_b16 v[80:81], v165 offset:1056\n\t" \
  "ds_read_b64_tr_b16 v[82:83], v165 offset:1024\n\t" \
  "ds_read_b64_tr_b16 v[84:85], v165 offset:1312\n\t" \
  "ds_read_b64_tr_b16 v[86:87], v165 offset:1280\n\t" \
  "ds_read_b64_tr_b16 v[88:89], v165 offset:1568\n\t" \
  "ds_read_b64_tr_b16 v[90:91], v165 offset:1536\n\t" \
  "ds_read_b64_tr_b16 v[92:93], v165 offset:1824\n\t" \
  "ds_read_b64_tr_b16 v[94:95], v165 offset:1792\n\t" \
  "s_setprio 3\n\t" \
  "s_barrier\n\t" \
  "s_waitcnt vmcnt(0)\n\t" \
  "v_add_u32 v61, 1, v160\n\t" \
  "s_mov_b64 exec, 1\n\t" \
  "global_store_dword v60, v61, %[ff] sc1\n\t" \
  "s_mov_b64 exec, -1\n\t" \
  "s_setprio 2\n\t" \
  "ds_read_b64_tr_b16 v[96:97], v165 offset:2080\n\t" \
  "ds_read_b64_tr_b16 v[98:99], v165 offset:2048\n\t" \
  "ds_read_b64_tr_b16 v[100:101], v165 offset:2336\n\t" \
  "ds_read_b64_tr_b16 v[102:103], v165 offset:2304\n\t" \
  "ds_read_b64_tr_b16 v[104:105], v165 offset:2592\n\t" \
  "ds_read_b64_tr_b16 v[106:107], v165 offset:2560\n\t" \
  "ds_read_b64_tr_b16 v[108:109], v165 offset:2848\n\t" \
  "ds_read_b64_tr_b16 v[110:111], v165 offset:2816\n\t" \
  "s_barrier\n\t" \
  "ds_read_b64_tr_b16 v[112:113], v165 offset:3104\n\t" \
  "ds_read_b64_tr_b16 v[114:115], v165 offset:3072\n\t" \
  "ds_read_b64_tr_b16 v[116:117], v165 offset:3360\n\t" \
  "ds_read_b64_tr_b16 v[118:119], v165 offset:3328\n\t" \
  "ds_read_b64_tr_b16 v[120:121], v165 offset:3616\n\t" \
  "ds_read_b64_tr_b16 v[122:123], v165 offset:3584\n\t" \
  "ds_read_b64_tr_b16 v[124:125], v165 offset:3872\n\t" \
  "ds_read_b64_tr_b16 v[126:127], v165 offset:3840\n\t" \
  "s_barrier\n\t" \
  "s_waitcnt lgkmcnt(0)\n\t" \
  "v_lshlrev_b32 v61, 16, v161\n\t" \
  "v_lshlrev_b32 v62, 16, v162\n\t" \
  "v_add_f32 v61, v61, v62\n\t" \
  "v_mov_b32 v62, 0xffff\n\t" \
  "v_cvt_pk_bf16_f32 v61, v61, v61\n\t" \
  "s_mov_b64 exec, 1\n\t" \
  "v_bfi_b32 v66, v62, v61, v66\n\t" \
  "s_mov_b64 exec, 15\n\t" \
  "v_and_b32 v66, v62, v66\n\t" \
  "s_mov_b64 exec, 16\n\t" \
  "v_bfi_b32 v66, v62, v66, v61\n\t" \
  "s_mov_b64 exec, 0xff\n\t" \
  "v_mov_b32 v67, 0\n\t" \
  "s_mov_b64 exec, 0x100\n\t" \
  "v_mov_b32 v67, v61\n\t" \
  "s_mov_b64 exec, 0xf00\n\t" \
  "v_and_b32 v67, v62, v67\n\t" \
  "s_mov_b64 exec, 0x1000\n\t" \
  "v_bfi_b32 v67, v62, v67, v61\n\t" \
  "s_mov_b64 exec, -1\n\t" \
  "s_waitcnt lgkmcnt(0)\n\t"
#define PP_NB 15
#define PP_Q 0
#define PP_CLOB "v0", "v1", "v2", "v3", "v4", "v5", "v6", "v7", "v8", "v9", "v10", "v11", "v12", "v13", "v14", "v15", "v16", "v17", "v18", "v19", "v20", "v21", "v22", "v23", "v32", "v33", "v34", "v35", "v36", "v37", "v38", "v39", "v40", "v41", "v42", "v43", "v60", "v61", "v62", "v63", "v128", "v129", "v130", "v131", "v132", "v133", "v134", "v135", "v136", "v137", "v138", "v139", "v140", "v141", "v142", "v143", "v144", "v145", "v146", "v147", "v148", "v149", "v150", "v151", "v158", "v161", "v162"

#define LD_ASM1 \
  "s_add_u32 m0, %[wb], 49152\n\t" \
  "s_add_u32 s68, %[rw0], 0\n\t" \
  "buffer_load_dwordx4 v5, %[sw], s68 offen offset:0 lds\n\t" \
  "buffer_load_dwordx4 v6, %[sw], s68 offen offset:1024 lds\n\t" \
  "buffer_load_dwordx4 v7, %[sw], s68 offen offset:2048 lds\n\t" \
  "s_add_u32 m0, %[xb], 36864\n\t" \
  "s_add_u32 s68, %[rx0], 0\n\t" \
  "buffer_load_dwordx4 v0, %[sx], s68 offen offset:0 lds\n\t" \
  "buffer_load_dwordx4 v1, %[sx], s68 offen offset:1024 lds\n\t" \
  "s_mov_b64 exec, 0xffff\n\t" \
  "buffer_load_dwordx4 v4, %[sx], s68 offen offset:2048 lds\n\t" \
  "s_mov_b64 exec, -1\n\t" \
  "s_add_u32 m0, %[xb], 46080\n\t" \
  "s_add_u32 s68, %[rx0], 147456\n\t" \
  "buffer_load_dwordx4 v2, %[sx], s68 offen offset:0 lds\n\t" \
  "buffer_load_dwordx4 v3, %[sx], s68 offen offset:1024 lds\n\t" \
  "s_mov_b64 exec, 0xffff\n\t" \
  "buffer_load_dwordx4 v4, %[sx], s68 offen offset:2048 lds\n\t" \
  "s_mov_b64 exec, -1\n\t" \
  "s_add_u32 m0, %[wb], 61440\n\t" \
  "s_add_u32 s68, %[rw0], 196608\n\t" \
  "buffer_load_dwordx4 v5, %[sw], s68 offen offset:0 lds\n\t" \
  "buffer_load_dwordx4 v6, %[sw], s68 offen offset:1024 lds\n\t" \
  "buffer_load_dwordx4 v7, %[sw], s68 offen offset:2048 lds\n\t"
#define LD_ASM2 \
  "s_mov_b32 s85, 0x180000\n\t" \
  "s_load_dword s84, %[pw], s85\n\t" \
  "s_mov_b32 s85, 0x180800\n\t" \
  "s_load_dword s84, %[pw], s85\n\t" \
  "s_mov_b32 s85, 0x181000\n\t" \
  "s_load_dword s84, %[pw], s85\n\t" \
  "s_mov_b32 s85, 0x1b0000\n\t" \
  "s_load_dword s84, %[pw], s85\n\t" \
  "s_mov_b32 s85, 0x1b0800\n\t" \
  "s_load_dword s84, %[pw], s85\n\t" \
  "s_mov_b32 s85, 0x1b1000\n\t" \
  "s_load_dword s84, %[pw], s85\n\t" \
  "s_mov_b32 s85, 0x1e0000\n\t" \
  "s_load_dword s84, %[pw], s85\n\t" \
  "s_mov_b32 s85, 0x1e0800\n\t" \
  "s_load_dword s84, %[pw], s85\n\t" \
  "s_mov_b32 m0, %[tsk]\n\t" \
  "s_mov_b32 s85, 0x0\n\t" \
  "s_mov_b64 exec, 63\n\t" \
  "buffer_load_dword v16, %[sh], s85 offen lds\n\t" \
  "s_mov_b64 exec, -1\n\t" \
  "s_add_u32 s68, %[rx1], 0\n\t" \
  "s_add_u32 s69, %[rx1], 147456\n\t" \
  "s_add_u32 s70, %[rw1], 0\n\t" \
  "s_add_u32 m0, %[xb], 0\n\t" \
  "s_waitcnt vmcnt(3)\n\t" \
  "s_barrier\n\t" \
  "buffer_load_dwordx4 v0, %[sx], s68 offen offset:0 lds\n\t" \
  "s_mov_b32 s85, 0x1e1000\n\t" \
  "s_load_dword s84, %[pw], s85\n\t" \
  "buffer_load_dwordx4 v1, %[sx], s68 offen offset:1024 lds\n\t" \
  "s_mov_b32 s85, 0x210000\n\t" \
  "s_load_dword s84, %[pw], s85\n\t" \
  "s_mov_b64 exec, 0xffff\n\t" \
  "buffer_load_dwordx4 v4, %[sx], s68 offen offset:2048 lds\n\t" \
  "s_add_u32 m0, %[xb], 9216\n\t" \
  "s_mov_b64 exec, -1\n\t" \
  "buffer_load_dwordx4 v2, %[sx], s69 offen offset:0 lds\n\t" \
  "s_mov_b32 s85, 0x210800\n\t" \
  "s_load_dword s84, %[pw], s85\n\t" \
  "buffer_load_dwordx4 v3, %[sx], s69 offen offset:1024 lds\n\t" \
  "s_mov_b64 exec, 0xffff\n\t" \
  "buffer_load_dwordx4 v4, %[sx], s69 offen offset:2048 lds\n\t" \
  "s_add_u32 m0, %[wb], 0\n\t" \
  "s_mov_b64 exec, -1\n\t" \
  "buffer_load_dwordx4 v5, %[sw], s70 offen offset:0 lds\n\t" \
  "buffer_load_dwordx4 v6, %[sw], s70 offen offset:1024 lds\n\t" \
  "buffer_load_dwordx4 v7, %[sw], s70 offen offset:2048 lds\n\t" \
  "s_mov_b32 m0, %[tsk]\n\t" \
  "s_mov_b32 s85, 0x180000\n\t" \
  "s_mov_b64 exec, 63\n\t" \
  "buffer_load_dword v16, %[sh], s85 offen lds\n\t" \
  "s_mov_b64 exec, -1\n\t" \
  "s_add_u32 s68, %[rw1], 196608\n\t" \
  "s_add_u32 s69, %[rx2], 0\n\t" \
  "s_add_u32 s70, %[rx2], 147456\n\t" \
  "s_add_u32 m0, %[wb], 12288\n\t" \
  "s_waitcnt vmcnt(9)\n\t" \
  "s_barrier\n\t" \
  "buffer_load_dwordx4 v5, %[sw], s68 offen offset:0 lds\n\t" \
  "s_mov_b32 s85, 0x211000\n\t" \
  "s_load_dword s84, %[pw], s85\n\t" \
  "buffer_load_dwordx4 v6, %[sw], s68 offen offset:1024 lds\n\t" \
  "s_mov_b32 s85, 0x240000\n\t" \
  "s_load_dword s84, %[pw], s85\n\t" \
  "buffer_load_dwordx4 v7, %[sw], s68 offen offset:2048 lds\n\t" \
  "s_mov_b32 s85, 0x240800\n\t" \
  "s_load_dword s84, %[pw], s85\n\t" \
  "s_add_u32 m0, %[xb], 18432\n\t" \
  "s_nop 0\n\t" \
  "buffer_load_dwordx4 v0, %[sx], s69 offen offset:0 lds\n\t" \
  "buffer_load_dwordx4 v1, %[sx], s69 offen offset:1024 lds\n\t" \
  "s_mov_b64 exec, 0xffff\n\t" \
  "buffer_load_dwordx4 v4, %[sx], s69 offen offset:2048 lds\n\t" \
  "s_add_u32 m0, %[xb], 27648\n\t" \
  "s_mov_b64 exec, -1\n\t" \
  "buffer_load_dwordx4 v2, %[sx], s70 offen offset:0 lds\n\t" \
  "buffer_load_dwordx4 v3, %[sx], s70 offen offset:1024 lds\n\t" \
  "s_mov_b64 exec, 0xffff\n\t" \
  "buffer_load_dwordx4 v4, %[sx], s70 offen offset:2048 lds\n\t" \
  "s_mov_b64 exec, -1\n\t" \
  "s_add_u32 s68, %[rw2], 0\n\t" \
  "s_add_u32 s69, %[rw2], 196608\n\t" \
  "s_add_u32 s70, %[rx3], 0\n\t" \
  "s_add_u32 m0, %[wb], 24576\n\t" \
  "s_waitcnt vmcnt(9)\n\t" \
  "s_barrier\n\t" \
  "buffer_load_dwordx4 v5, %[sw], s68 offen offset:0 lds\n\t" \
  "s_mov_b32 s85, 0x241000\n\t" \
  "s_load_dword s84, %[pw], s85\n\t" \
  "buffer_load_dwordx4 v6, %[sw], s68 offen offset:1024 lds\n\t" \
  "s_mov_b32 s85, 0x270000\n\t" \
  "s_load_dword s84, %[pw], s85\n\t" \
  "buffer_load_dwordx4 v7, %[sw], s68 offen offset:2048 lds\n\t" \
  "s_mov_b32 s85, 0x270800\n\t" \
  "s_load_dword s84, %[pw], s85\n\t" \
  "s_add_u32 m0, %[wb], 36864\n\t" \
  "s_nop 0\n\t" \
  "buffer_load_dwordx4 v5, %[sw], s69 offen offset:0 lds\n\t" \
  "buffer_load_dwordx4 v6, %[sw], s69 offen offset:1024 lds\n\t" \
  "buffer_load_dwordx4 v7, %[sw], s69 offen offset:2048 lds\n\t" \
  "s_add_u32 m0, %[xb], 36864\n\t" \
  "s_nop 0\n\t" \
  "buffer_load_dwordx4 v0, %[sx], s70 offen offset:0 lds\n\t" \
  "buffer_load_dwordx4 v1, %[sx], s70 offen offset:1024 lds\n\t" \
  "s_mov_b64 exec, 0xffff\n\t" \
  "buffer_load_dwordx4 v4, %[sx], s70 offen offset:2048 lds\n\t" \
  "s_mov_b64 exec, -1\n\t" \
  "s_add_u32 s68, %[rw3], 0\n\t" \
  "s_add_u32 s69, %[rx3], 147456\n\t" \
  "s_add_u32 s70, %[rw3], 196608\n\t" \
  "s_add_u32 m0, %[wb], 49152\n\t" \
  "s_waitcnt vmcnt(15)\n\t" \
  "s_barrier\n\t" \
  "buffer_load_dwordx4 v5, %[sw], s68 offen offset:0 lds\n\t" \
  "s_mov_b32 s85, 0x271000\n\t" \
  "s_load_dword s84, %[pw], s85\n\t" \
  "buffer_load_dwordx4 v6, %[sw], s68 offen offset:1024 lds\n\t" \
  "s_mov_b32 s85, 0x2a0000\n\t" \
  "s_load_dword s84, %[pw], s85\n\t" \
  "buffer_load_dwordx4 v7, %[sw], s68 offen offset:2048 lds\n\t" \
  "s_mov_b32 s85, 0x2a0800\n\t" \
  "s_load_dword s84, %[pw], s85\n\t" \
  "s_add_u32 m0, %[xb], 46080\n\t" \
  "s_nop 0\n\t" \
  "buffer_load_dwordx4 v2, %[sx], s69 offen offset:0 lds\n\t" \
  "buffer_load_dwordx4 v3, %[sx], s69 offen offset:1024 lds\n\t" \
  "s_mov_b64 exec, 0xffff\n\t" \
  "buffer_load_dwordx4 v4, %[sx], s69 offen offset:2048 lds\n\t" \
  "s_add_u32 m0, %[wb], 61440\n\t" \
  "s_mov_b64 exec, -1\n\t" \
  "buffer_load_dwordx4 v5, %[sw], s70 offen offset:0 lds\n\t" \
  "buffer_load_dwordx4 v6, %[sw], s70 offen offset:1024 lds\n\t" \
  "buffer_load_dwordx4 v7, %[sw], s70 offen offset:2048 lds\n\t" \
  "s_add_u32 s68, %[rx0], 512\n\t" \
  "s_add_u32 s69, %[rw0], 1572864\n\t" \
  "s_add_u32 s70, %[rx0], 147968\n\t" \
  "s_add_u32 m0, %[xb], 0\n\t" \
  "s_waitcnt vmcnt(15)\n\t" \
  "s_barrier\n\t" \
  "buffer_load_dwordx4 v0, %[sx], s68 offen offset:0 lds\n\t" \
  "s_mov_b32 s85, 0x2a1000\n\t" \
  "s_load_dword s84, %[pw], s85\n\t" \
  "buffer_load_dwordx4 v1, %[sx], s68 offen offset:1024 lds\n\t" \
  "s_mov_b32 s85, 0x2d0000\n\t" \
  "s_load_dword s84, %[pw], s85\n\t" \
  "s_mov_b64 exec, 0xffff\n\t" \
  "buffer_load_dwordx4 v4, %[sx], s68 offen offset:2048 lds\n\t" \
  "s_add_u32 m0, %[wb], 0\n\t" \
  "s_mov_b64 exec, -1\n\t" \
  "buffer_load_dwordx4 v5, %[sw], s69 offen offset:0 lds\n\t" \
  "s_mov_b32 s85, 0x2d0800\n\t" \
  "s_load_dword s84, %[pw], s85\n\t" \
  "buffer_load_dwordx4 v6, %[sw], s69 offen offset:1024 lds\n\t" \
  "buffer_load_dwordx4 v7, %[sw], s69 offen offset:2048 lds\n\t" \
  "s_add_u32 m0, %[xb], 9216\n\t" \
  "s_nop 0\n\t" \
  "buffer_load_dwordx4 v2, %[sx], s70 offen offset:0 lds\n\t" \
  "buffer_load_dwordx4 v3, %[sx], s70 offen offset:1024 lds\n\t" \
  "s_mov_b64 exec, 0xffff\n\t" \
  "buffer_load_dwordx4 v4, %[sx], s70 offen offset:2048 lds\n\t" \
  "s_mov_b64 exec, -1\n\t" \
  "s_sleep 2\n\t" \
  "s_add_u32 s68, %[rw0], 1769472\n\t" \
  "s_add_u32 m0, %[wb], 12288\n\t" \
  "s_waitcnt vmcnt(21)\n\t" \
  "s_barrier\n\t" \
  "buffer_load_dwordx4 v5, %[sw], s68 offen offset:0 lds\n\t" \
  "s_mov_b32 s85, 0x2d1000\n\t" \
  "s_load_dword s84, %[pw], s85\n\t" \
  "buffer_load_dwordx4 v6, %[sw], s68 offen offset:1024 lds\n\t" \
  "s_mov_b32 s85, 0x300000\n\t" \
  "s_load_dword s84, %[pw], s85\n\t" \
  "buffer_load_dwordx4 v7, %[sw], s68 offen offset:2048 lds\n\t" \
  "s_mov_b32 s85, 0x300800\n\t" \
  "s_load_dword s84, %[pw], s85\n\t" \
  "s_sleep 2\n\t" \
  "s_add_u32 s68, %[rx1], 512\n\t" \
  "s_add_u32 s69, %[rw1], 1572864\n\t" \
  "s_add_u32 s70, %[rx1], 147968\n\t" \
  "s_add_u32 m0, %[xb], 18432\n\t" \
  "s_waitcnt vmcnt(15)\n\t" \
  "s_barrier\n\t" \
  "buffer_load_dwordx4 v0, %[sx], s68 offen offset:0 lds\n\t" \
  "s_mov_b32 s85, 0x301000\n\t" \
  "s_load_dword s84, %[pw], s85\n\t" \
  "buffer_load_dwordx4 v1, %[sx], s68 offen offset:1024 lds\n\t" \
  "s_mov_b32 s85, 0x330000\n\t" \
  "s_load_dword s84, %[pw], s85\n\t" \
  "s_mov_b64 exec, 0xffff\n\t" \
  "buffer_load_dwordx4 v4, %[sx], s68 offen offset:2048 lds\n\t" \
  "s_add_u32 m0, %[wb], 24576\n\t" \
  "s_mov_b64 exec, -1\n\t" \
  "buffer_load_dwordx4 v5, %[sw], s69 offen offset:0 lds\n\t" \
  "s_mov_b32 s85, 0x330800\n\t" \
  "s_load_dword s84, %[pw], s85\n\t" \
  "buffer_load_dwordx4 v6, %[sw], s69 offen offset:1024 lds\n\t" \
  "buffer_load_dwordx4 v7, %[sw], s69 offen offset:2048 lds\n\t" \
  "s_add_u32 m0, %[xb], 27648\n\t" \
  "s_nop 0\n\t" \
  "buffer_load_dwordx4 v2, %[sx], s70 offen offset:0 lds\n\t" \
  "buffer_load_dwordx4 v3, %[sx], s70 offen offset:1024 lds\n\t" \
  "s_mov_b64 exec, 0xffff\n\t" \
  "buffer_load_dwordx4 v4, %[sx], s70 offen offset:2048 lds\n\t" \
  "s_mov_b64 exec, -1\n\t" \
  "s_sleep 2\n\t" \
  "s_add_u32 s68, %[rw1], 1769472\n\t" \
  "s_add_u32 m0, %[wb], 36864\n\t" \
  "s_waitcnt vmcnt(21)\n\t" \
  "s_barrier\n\t" \
  "buffer_load_dwordx4 v5, %[sw], s68 offen offset:0 lds\n\t" \
  "s_mov_b32 s85, 0x331000\n\t" \
  "s_load_dword s84, %[pw], s85\n\t" \
  "buffer_load_dwordx4 v6, %[sw], s68 offen offset:1024 lds\n\t" \
  "s_mov_b32 s85, 0x360000\n\t" \
  "s_load_dword s84, %[pw], s85\n\t" \
  "buffer_load_dwordx4 v7, %[sw], s68 offen offset:2048 lds\n\t" \
  "s_mov_b32 s85, 0x360800\n\t" \
  "s_load_dword s84, %[pw], s85\n\t" \
  "s_sleep 2\n\t" \
  "s_add_u32 s68, %[rx2], 512\n\t" \
  "s_add_u32 s69, %[rw2], 1572864\n\t" \
  "s_add_u32 m0, %[xb], 36864\n\t" \
  "s_waitcnt vmcnt(15)\n\t" \
  "s_barrier\n\t" \
  "buffer_load_dwordx4 v0, %[sx], s68 offen offset:0 lds\n\t" \
  "s_mov_b32 s85, 0x361000\n\t" \
  "s_load_dword s84, %[pw], s85\n\t" \
  "buffer_load_dwordx4 v1, %[sx], s68 offen offset:1024 lds\n\t" \
  "s_mov_b32 s85, 0x390000\n\t" \
  "s_load_dword s84, %[pw], s85\n\t" \
  "s_mov_b64 exec, 0xffff\n\t" \
  "buffer_load_dwordx4 v4, %[sx], s68 offen offset:2048 lds\n\t" \
  "s_add_u32 m0, %[wb], 49152\n\t" \
  "s_mov_b64 exec, -1\n\t" \
  "buffer_load_dwordx4 v5, %[sw], s69 offen offset:0 lds\n\t" \
  "s_mov_b32 s85, 0x390800\n\t" \
  "s_load_dword s84, %[pw], s85\n\t" \
  "buffer_load_dwordx4 v6, %[sw], s69 offen offset:1024 lds\n\t" \
  "buffer_load_dwordx4 v7, %[sw], s69 offen offset:2048 lds\n\t" \
  "s_mov_b32 m0, %[tsk]\n\t" \
  "s_mov_b32 s85, 0x300000\n\t" \
  "s_mov_b64 exec, 63\n\t" \
  "buffer_load_dword v16, %[sh], s85 offen lds\n\t" \
  "s_mov_b64 exec, -1\n\t" \
  "s_sleep 2\n\t" \
  "s_add_u32 s68, %[rx2], 147968\n\t" \
  "s_add_u32 s69, %[rw2], 1769472\n\t" \
  "s_add_u32 m0, %[xb], 46080\n\t" \
  "s_waitcnt vmcnt(18)\n\t" \
  "s_barrier\n\t" \
  "buffer_load_dwordx4 v2, %[sx], s68 offen offset:0 lds\n\t" \
  "s_mov_b32 s85, 0x391000\n\t" \
  "s_load_dword s84, %[pw], s85\n\t" \
  "buffer_load_dwordx4 v3, %[sx], s68 offen offset:1024 lds\n\t" \
  "s_mov_b32 s85, 0x3c0000\n\t" \
  "s_load_dword s84, %[pw], s85\n\t" \
  "s_mov_b64 exec, 0xffff\n\t" \
  "buffer_load_dwordx4 v4, %[sx], s68 offen offset:2048 lds\n\t" \
  "s_add_u32 m0, %[wb], 61440\n\t" \
  "s_mov_b64 exec, -1\n\t" \
  "buffer_load_dwordx4 v5, %[sw], s69 offen offset:0 lds\n\t" \
  "s_mov_b32 s85, 0x3c0800\n\t" \
  "s_load_dword s84, %[pw], s85\n\t" \
  "buffer_load_dwordx4 v6, %[sw], s69 offen offset:1024 lds\n\t" \
  "buffer_load_dwordx4 v7, %[sw], s69 offen offset:2048 lds\n\t" \
  "s_sleep 2\n\t" \
  "s_add_u32 s68, %[rx3], 512\n\t" \
  "s_add_u32 s69, %[rw3], 1572864\n\t" \
  "s_add_u32 m0, %[xb], 0\n\t" \
  "s_waitcnt vmcnt(15)\n\t" \
  "s_barrier\n\t" \
  "buffer_load_dwordx4 v0, %[sx], s68 offen offset:0 lds\n\t" \
  "s_mov_b32 s85, 0x3c1000\n\t" \
  "s_load_dword s84, %[pw], s85\n\t" \
  "buffer_load_dwordx4 v1, %[sx], s68 offen offset:1024 lds\n\t" \
  "s_mov_b32 s85, 0x3f0000\n\t" \
  "s_load_dword s84, %[pw], s85\n\t" \
  "s_mov_b64 exec, 0xffff\n\t" \
  "buffer_load_dwordx4 v4, %[sx], s68 offen offset:2048 lds\n\t" \
  "s_add_u32 m0, %[wb], 0\n\t" \
  "s_mov_b64 exec, -1\n\t" \
  "buffer_load_dwordx4 v5, %[sw], s69 offen offset:0 lds\n\t" \
  "s_mov_b32 s85, 0x3f0800\n\t" \
  "s_load_dword s84, %[pw], s85\n\t" \
  "buffer_load_dwordx4 v6, %[sw], s69 offen offset:1024 lds\n\t" \
  "buffer_load_dwordx4 v7, %[sw], s69 offen offset:2048 lds\n\t" \
  "s_add_u32 s68, %[rx3], 147968\n\t" \
  "s_add_u32 s69, %[rw3], 1769472\n\t" \
  "s_add_u32 m0, %[xb], 9216\n\t" \
  "s_waitcnt vmcnt(18)\n\t" \
  "s_barrier\n\t" \
  "buffer_load_dwordx4 v2, %[sx], s68 offen offset:0 lds\n\t" \
  "s_mov_b32 s85, 0x3f1000\n\t" \
  "s_load_dword s84, %[pw], s85\n\t" \
  "buffer_load_dwordx4 v3, %[sx], s68 offen offset:1024 lds\n\t" \
  "s_mov_b32 s85, 0x420000\n\t" \
  "s_load_dword s84, %[pw], s85\n\t" \
  "s_mov_b64 exec, 0xffff\n\t" \
  "buffer_load_dwordx4 v4, %[sx], s68 offen offset:2048 lds\n\t" \
  "s_add_u32 m0, %[wb], 12288\n\t" \
  "s_mov_b64 exec, -1\n\t" \
  "buffer_load_dwordx4 v5, %[sw], s69 offen offset:0 lds\n\t" \
  "s_mov_b32 s85, 0x420800\n\t" \
  "s_load_dword s84, %[pw], s85\n\t" \
  "buffer_load_dwordx4 v6, %[sw], s69 offen offset:1024 lds\n\t" \
  "buffer_load_dwordx4 v7, %[sw], s69 offen offset:2048 lds\n\t" \
  "s_add_u32 s68, %[rx0], 1024\n\t" \
  "s_add_u32 s69, %[rw0], 3145728\n\t" \
  "s_add_u32 m0, %[xb], 18432\n\t" \
  "s_waitcnt vmcnt(15)\n\t" \
  "s_barrier\n\t" \
  "buffer_load_dwordx4 v0, %[sx], s68 offen offset:0 lds\n\t" \
  "s_mov_b32 s85, 0x421000\n\t" \
  "s_load_dword s84, %[pw], s85\n\t" \
  "buffer_load_dwordx4 v1, %[sx], s68 offen offset:1024 lds\n\t" \
  "s_mov_b32 s85, 0x450000\n\t" \
  "s_load_dword s84, %[pw], s85\n\t" \
  "s_mov_b64 exec, 0xffff\n\t" \
  "buffer_load_dwordx4 v4, %[sx], s68 offen offset:2048 lds\n\t" \
  "s_add_u32 m0, %[wb], 24576\n\t" \
  "s_mov_b64 exec, -1\n\t" \
  "buffer_load_dwordx4 v5, %[sw], s69 offen offset:0 lds\n\t" \
  "s_mov_b32 s85, 0x450800\n\t" \
  "s_load_dword s84, %[pw], s85\n\t" \
  "buffer_load_dwordx4 v6, %[sw], s69 offen offset:1024 lds\n\t" \
  "buffer_load_dwordx4 v7, %[sw], s69 offen offset:2048 lds\n\t" \
  "s_add_u32 s68, %[rx0], 148480\n\t" \
  "s_add_u32 s69, %[rw0], 3342336\n\t" \
  "s_add_u32 m0, %[xb], 27648\n\t" \
  "s_waitcnt vmcnt(18)\n\t" \
  "s_barrier\n\t" \
  "buffer_load_dwordx4 v2, %[sx], s68 offen offset:0 lds\n\t" \
  "s_mov_b32 s85, 0x451000\n\t" \
  "s_load_dword s84, %[pw], s85\n\t" \
  "buffer_load_dwordx4 v3, %[sx], s68 offen offset:1024 lds\n\t" \
  "s_mov_b32 s85, 0x480000\n\t" \
  "s_load_dword s84, %[pw], s85\n\t" \
  "s_mov_b64 exec, 0xffff\n\t" \
  "buffer_load_dwordx4 v4, %[sx], s68 offen offset:2048 lds\n\t" \
  "s_add_u32 m0, %[wb], 36864\n\t" \
  "s_mov_b64 exec, -1\n\t" \
  "buffer_load_dwordx4 v5, %[sw], s69 offen offset:0 lds\n\t" \
  "s_mov_b32 s85, 0x480800\n\t" \
  "s_load_dword s84, %[pw], s85\n\t" \
  "buffer_load_dwordx4 v6, %[sw], s69 offen offset:1024 lds\n\t" \
  "buffer_load_dwordx4 v7, %[sw], s69 offen offset:2048 lds\n\t" \
  "s_add_u32 s68, %[rx1], 1024\n\t" \
  "s_add_u32 s69, %[rw1], 3145728\n\t" \
  "s_add_u32 m0, %[xb], 36864\n\t" \
  "s_waitcnt vmcnt(15)\n\t" \
  "s_barrier\n\t" \
  "buffer_load_dwordx4 v0, %[sx], s68 offen offset:0 lds\n\t" \
  "s_mov_b32 s85, 0x481000\n\t" \
  "s_load_dword s84, %[pw], s85\n\t" \
  "buffer_load_dwordx4 v1, %[sx], s68 offen offset:1024 lds\n\t" \
  "s_mov_b32 s85, 0x4b0000\n\t" \
  "s_load_dword s84, %[pw], s85\n\t" \
  "s_mov_b64 exec, 0xffff\n\t" \
  "buffer_load_dwordx4 v4, %[sx], s68 offen offset:2048 lds\n\t" \
  "s_add_u32 m0, %[wb], 49152\n\t" \
  "s_mov_b64 exec, -1\n\t" \
  "buffer_load_dwordx4 v5, %[sw], s69 offen offset:0 lds\n\t" \
  "s_mov_b32 s85, 0x4b0800\n\t" \
  "s_load_dword s84, %[pw], s85\n\t" \
  "buffer_load_dwordx4 v6, %[sw], s69 offen offset:1024 lds\n\t" \
  "buffer_load_dwordx4 v7, %[sw], s69 offen offset:2048 lds\n\t" \
  "s_add_u32 s68, %[rx1], 148480\n\t" \
  "s_add_u32 s69, %[rw1], 3342336\n\t" \
  "s_add_u32 m0, %[xb], 46080\n\t" \
  "s_waitcnt vmcnt(18)\n\t" \
  "s_barrier\n\t" \
  "buffer_load_dwordx4 v2, %[sx], s68 offen offset:0 lds\n\t" \
  "s_mov_b32 s85, 0x4b1000\n\t" \
  "s_load_dword s84, %[pw], s85\n\t" \
  "buffer_load_dwordx4 v3, %[sx], s68 offen offset:1024 lds\n\t" \
  "s_mov_b32 s85, 0x4e0000\n\t" \
  "s_load_dword s84, %[pw], s85\n\t" \
  "s_mov_b64 exec, 0xffff\n\t" \
  "buffer_load_dwordx4 v4, %[sx], s68 offen offset:2048 lds\n\t" \
  "s_add_u32 m0, %[wb], 61440\n\t" \
  "s_mov_b64 exec, -1\n\t" \
  "buffer_load_dwordx4 v5, %[sw], s69 offen offset:0 lds\n\t" \
  "s_mov_b32 s85, 0x4e0800\n\t" \
  "s_load_dword s84, %[pw], s85\n\t" \
  "buffer_load_dwordx4 v6, %[sw], s69 offen offset:1024 lds\n\t" \
  "buffer_load_dwordx4 v7, %[sw], s69 offen offset:2048 lds\n\t" \
  "s_add_u32 s68, %[rx2], 1024\n\t" \
  "s_add_u32 s69, %[rw2], 3145728\n\t" \
  "s_add_u32 m0, %[xb], 0\n\t" \
  "s_waitcnt vmcnt(15)\n\t" \
  "s_barrier\n\t" \
  "buffer_load_dwordx4 v0, %[sx], s68 offen offset:0 lds\n\t" \
  "s_mov_b32 s85, 0x4e1000\n\t" \
  "s_load_dword s84, %[pw], s85\n\t" \
  "buffer_load_dwordx4 v1, %[sx], s68 offen offset:1024 lds\n\t" \
  "s_mov_b32 s85, 0x510000\n\t" \
  "s_load_dword s84, %[pw], s85\n\t" \
  "s_mov_b64 exec, 0xffff\n\t" \
  "buffer_load_dwordx4 v4, %[sx], s68 offen offset:2048 lds\n\t" \
  "s_add_u32 m0, %[wb], 0\n\t" \
  "s_mov_b64 exec, -1\n\t" \
  "buffer_load_dwordx4 v5, %[sw], s69 offen offset:0 lds\n\t" \
  "s_mov_b32 s85, 0x510800\n\t" \
  "s_load_dword s84, %[pw], s85\n\t" \
  "buffer_load_dwordx4 v6, %[sw], s69 offen offset:1024 lds\n\t" \
  "buffer_load_dwordx4 v7, %[sw], s69 offen offset:2048 lds\n\t" \
  "s_mov_b32 m0, %[tsk]\n\t" \
  "s_mov_b32 s85, 0x480000\n\t" \
  "s_mov_b64 exec, 63\n\t" \
  "buffer_load_dword v16, %[sh], s85 offen lds\n\t" \
  "s_mov_b64 exec, -1\n\t" \
  "s_add_u32 s68, %[rx2], 148480\n\t" \
  "s_add_u32 s69, %[rw2], 3342336\n\t" \
  "s_add_u32 m0, %[xb], 9216\n\t" \
  "s_waitcnt vmcnt(18)\n\t" \
  "s_barrier\n\t" \
  "buffer_load_dwordx4 v2, %[sx], s68 offen offset:0 lds\n\t" \
  "s_mov_b32 s85, 0x511000\n\t" \
  "s_load_dword s84, %[pw], s85\n\t" \
  "buffer_load_dwordx4 v3, %[sx], s68 offen offset:1024 lds\n\t" \
  "s_mov_b32 s85, 0x540000\n\t" \
  "s_load_dword s84, %[pw], s85\n\t" \
  "s_mov_b64 exec, 0xffff\n\t" \
  "buffer_load_dwordx4 v4, %[sx], s68 offen offset:2048 lds\n\t" \
  "s_add_u32 m0, %[wb], 12288\n\t" \
  "s_mov_b64 exec, -1\n\t" \
  "buffer_load_dwordx4 v5, %[sw], s69 offen offset:0 lds\n\t" \
  "s_mov_b32 s85, 0x540800\n\t" \
  "s_load_dword s84, %[pw], s85\n\t" \
  "buffer_load_dwordx4 v6, %[sw], s69 offen offset:1024 lds\n\t" \
  "buffer_load_dwordx4 v7, %[sw], s69 offen offset:2048 lds\n\t" \
  "s_mov_b32 m0, %[tsk]\n\t" \
  "s_mov_b64 exec, %[tm]\n\t" \
  "global_load_lds_dword v12, %[tp]\n\t" \
  "s_mov_b64 exec, -1\n\t" \
  "s_add_u32 s68, %[rx3], 1024\n\t" \
  "s_add_u32 s69, %[rw3], 3145728\n\t" \
  "s_add_u32 m0, %[xb], 18432\n\t" \
  "s_waitcnt vmcnt(15)\n\t" \
  "s_barrier\n\t" \
  "buffer_load_dwordx4 v0, %[sx], s68 offen offset:0 lds\n\t" \
  "s_mov_b32 s85, 0x541000\n\t" \
  "s_load_dword s84, %[pw], s85\n\t" \
  "buffer_load_dwordx4 v1, %[sx], s68 offen offset:1024 lds\n\t" \
  "s_mov_b32 s85, 0x570000\n\t" \
  "s_load_dword s84, %[pw], s85\n\t" \
  "s_mov_b64 exec, 0xffff\n\t" \
  "buffer_load_dwordx4 v4, %[sx], s68 offen offset:2048 lds\n\t" \
  "s_add_u32 m0, %[wb], 24576\n\t" \
  "s_mov_b64 exec, -1\n\t" \
  "buffer_load_dwordx4 v5, %[sw], s69 offen offset:0 lds\n\t" \
  "s_mov_b32 s85, 0x570800\n\t" \
  "s_load_dword s84, %[pw], s85\n\t" \
  "buffer_load_dwordx4 v6, %[sw], s69 offen offset:1024 lds\n\t" \
  "buffer_load_dwordx4 v7, %[sw], s69 offen offset:2048 lds\n\t" \
  "s_add_u32 s68, %[rx3], 148480\n\t" \
  "s_add_u32 s69, %[rw3], 3342336\n\t" \
  "s_add_u32 m0, %[xb], 27648\n\t" \
  "s_waitcnt vmcnt(18)\n\t" \
  "s_barrier\n\t" \
  "buffer_load_dwordx4 v2, %[sx], s68 offen offset:0 lds\n\t" \
  "s_mov_b32 s85, 0x571000\n\t" \
  "s_load_dword s84, %[pw], s85\n\t" \
  "buffer_load_dwordx4 v3, %[sx], s68 offen offset:1024 lds\n\t" \
  "s_mov_b32 s85, 0x5a0000\n\t" \
  "s_load_dword s84, %[pw], s85\n\t" \
  "s_mov_b64 exec, 0xffff\n\t" \
  "buffer_load_dwordx4 v4, %[sx], s68 offen offset:2048 lds\n\t" \
  "s_add_u32 m0, %[wb], 36864\n\t" \
  "s_mov_b64 exec, -1\n\t" \
  "buffer_load_dwordx4 v5, %[sw], s69 offen offset:0 lds\n\t" \
  "s_mov_b32 s85, 0x5a0800\n\t" \
  "s_load_dword s84, %[pw], s85\n\t" \
  "buffer_load_dwordx4 v6, %[sw], s69 offen offset:1024 lds\n\t" \
  "buffer_load_dwordx4 v7, %[sw], s69 offen offset:2048 lds\n\t" \
  "s_add_u32 s68, %[rx0], 1536\n\t" \
  "s_add_u32 s69, %[rw0], 4718592\n\t" \
  "s_add_u32 m0, %[xb], 36864\n\t" \
  "s_waitcnt vmcnt(15)\n\t" \
  "s_barrier\n\t" \
  "buffer_load_dwordx4 v0, %[sx], s68 offen offset:0 lds\n\t" \
  "s_mov_b32 s85, 0x5a1000\n\t" \
  "s_load_dword s84, %[pw], s85\n\t" \
  "buffer_load_dwordx4 v1, %[sx], s68 offen offset:1024 lds\n\t" \
  "s_mov_b32 s85, 0x5d0000\n\t" \
  "s_load_dword s84, %[pw], s85\n\t" \
  "s_mov_b64 exec, 0xffff\n\t" \
  "buffer_load_dwordx4 v4, %[sx], s68 offen offset:2048 lds\n\t" \
  "s_add_u32 m0, %[wb], 49152\n\t" \
  "s_mov_b64 exec, -1\n\t" \
  "buffer_load_dwordx4 v5, %[sw], s69 offen offset:0 lds\n\t" \
  "s_mov_b32 s85, 0x5d0800\n\t" \
  "s_load_dword s84, %[pw], s85\n\t" \
  "buffer_load_dwordx4 v6, %[sw], s69 offen offset:1024 lds\n\t" \
  "buffer_load_dwordx4 v7, %[sw], s69 offen offset:2048 lds\n\t" \
  "s_add_u32 s68, %[rx0], 148992\n\t" \
  "s_add_u32 s69, %[rw0], 4915200\n\t" \
  "s_add_u32 m0, %[xb], 46080\n\t" \
  "s_waitcnt vmcnt(18)\n\t" \
  "s_barrier\n\t" \
  "buffer_load_dwordx4 v2, %[sx], s68 offen offset:0 lds\n\t" \
  "s_mov_b32 s85, 0x5d1000\n\t" \
  "s_load_dword s84, %[pw], s85\n\t" \
  "buffer_load_dwordx4 v3, %[sx], s68 offen offset:1024 lds\n\t" \
  "s_load_dword s84, %[po], 0x0\n\t" \
  "s_mov_b64 exec, 0xffff\n\t" \
  "buffer_load_dwordx4 v4, %[sx], s68 offen offset:2048 lds\n\t" \
  "s_add_u32 m0, %[wb], 61440\n\t" \
  "s_mov_b64 exec, -1\n\t" \
  "buffer_load_dwordx4 v5, %[sw], s69 offen offset:0 lds\n\t" \
  "s_load_dword s84, %[po], 0x40\n\t" \
  "buffer_load_dwordx4 v6, %[sw], s69 offen offset:1024 lds\n\t" \
  "buffer_load_dwordx4 v7, %[sw], s69 offen offset:2048 lds\n\t" \
  "s_add_u32 s68, %[rx1], 1536\n\t" \
  "s_add_u32 s69, %[rw1], 4718592\n\t" \
  "s_add_u32 m0, %[xb], 0\n\t" \
  "s_waitcnt vmcnt(15)\n\t" \
  "s_barrier\n\t" \
  "buffer_load_dwordx4 v0, %[sx], s68 offen offset:0 lds\n\t" \
  "s_load_dword s84, %[po], 0x80\n\t" \
  "buffer_load_dwordx4 v1, %[sx], s68 offen offset:1024 lds\n\t" \
  "s_load_dword s84, %[po], 0xc0\n\t" \
  "s_mov_b64 exec, 0xffff\n\t" \
  "buffer_load_dwordx4 v4, %[sx], s68 offen offset:2048 lds\n\t" \
  "s_add_u32 m0, %[wb], 0\n\t" \
  "s_mov_b64 exec, -1\n\t" \
  "buffer_load_dwordx4 v5, %[sw], s69 offen offset:0 lds\n\t" \
  "s_load_dword s84, %[po], 0x100\n\t" \
  "buffer_load_dwordx4 v6, %[sw], s69 offen offset:1024 lds\n\t" \
  "buffer_load_dwordx4 v7, %[sw], s69 offen offset:2048 lds\n\t" \
  "s_add_u32 s68, %[rx1], 148992\n\t" \
  "s_add_u32 s69, %[rw1], 4915200\n\t" \
  "s_add_u32 m0, %[xb], 9216\n\t" \
  "s_waitcnt vmcnt(18)\n\t" \
  "s_barrier\n\t" \
  "buffer_load_dwordx4 v2, %[sx], s68 offen offset:0 lds\n\t" \
  "s_load_dword s84, %[po], 0x140\n\t" \
  "buffer_load_dwordx4 v3, %[sx], s68 offen offset:1024 lds\n\t" \
  "s_load_dword s84, %[po], 0x180\n\t" \
  "s_mov_b64 exec, 0xffff\n\t" \
  "buffer_load_dwordx4 v4, %[sx], s68 offen offset:2048 lds\n\t" \
  "s_add_u32 m0, %[wb], 12288\n\t" \
  "s_mov_b64 exec, -1\n\t" \
  "buffer_load_dwordx4 v5, %[sw], s69 offen offset:0 lds\n\t" \
  "s_load_dword s84, %[po], 0x1c0\n\t" \
  "buffer_load_dwordx4 v6, %[sw], s69 offen offset:1024 lds\n\t" \
  "buffer_load_dwordx4 v7, %[sw], s69 offen offset:2048 lds\n\t" \
  "s_add_u32 s68, %[rx2], 1536\n\t" \
  "s_add_u32 s69, %[rw2], 4718592\n\t" \
  "s_add_u32 m0, %[xb], 18432\n\t" \
  "s_waitcnt vmcnt(15)\n\t" \
  "s_barrier\n\t" \
  "buffer_load_dwordx4 v0, %[sx], s68 offen offset:0 lds\n\t" \
  "s_load_dword s84, %[po], 0x200\n\t" \
  "buffer_load_dwordx4 v1, %[sx], s68 offen offset:1024 lds\n\t" \
  "s_load_dword s84, %[po], 0x240\n\t" \
  "s_mov_b64 exec, 0xffff\n\t" \
  "buffer_load_dwordx4 v4, %[sx], s68 offen offset:2048 lds\n\t" \
  "s_add_u32 m0, %[wb], 24576\n\t" \
  "s_mov_b64 exec, -1\n\t" \
  "buffer_load_dwordx4 v5, %[sw], s69 offen offset:0 lds\n\t" \
  "s_load_dword s84, %[po], 0x280\n\t" \
  "buffer_load_dwordx4 v6, %[sw], s69 offen offset:1024 lds\n\t" \
  "buffer_load_dwordx4 v7, %[sw], s69 offen offset:2048 lds\n\t" \
  "s_add_u32 s68, %[rx2], 148992\n\t" \
  "s_add_u32 s69, %[rw2], 4915200\n\t" \
  "s_add_u32 m0, %[xb], 27648\n\t" \
  "s_waitcnt vmcnt(18)\n\t" \
  "s_barrier\n\t" \
  "buffer_load_dwordx4 v2, %[sx], s68 offen offset:0 lds\n\t" \
  "s_load_dword s84, %[po], 0x2c0\n\t" \
  "buffer_load_dwordx4 v3, %[sx], s68 offen offset:1024 lds\n\t" \
  "s_load_dword s84, %[po], 0x300\n\t" \
  "s_mov_b64 exec, 0xffff\n\t" \
  "buffer_load_dwordx4 v4, %[sx], s68 offen offset:2048 lds\n\t" \
  "s_add_u32 m0, %[wb], 36864\n\t" \
  "s_mov_b64 exec, -1\n\t" \
  "buffer_load_dwordx4 v5, %[sw], s69 offen offset:0 lds\n\t" \
  "s_load_dword s84, %[po], 0x340\n\t" \
  "buffer_load_dwordx4 v6, %[sw], s69 offen offset:1024 lds\n\t" \
  "buffer_load_dwordx4 v7, %[sw], s69 offen offset:2048 lds\n\t" \
  "s_add_u32 s68, %[rx3], 1536\n\t" \
  "s_add_u32 s69, %[rw3], 4718592\n\t" \
  "s_add_u32 m0, %[xb], 36864\n\t" \
  "s_waitcnt vmcnt(15)\n\t" \
  "s_barrier\n\t" \
  "buffer_load_dwordx4 v0, %[sx], s68 offen offset:0 lds\n\t" \
  "s_load_dword s84, %[po], 0x380\n\t" \
  "buffer_load_dwordx4 v1, %[sx], s68 offen offset:1024 lds\n\t" \
  "s_load_dword s84, %[po], 0x3c0\n\t" \
  "s_mov_b64 exec, 0xffff\n\t" \
  "buffer_load_dwordx4 v4, %[sx], s68 offen offset:2048 lds\n\t" \
  "s_add_u32 m0, %[wb], 49152\n\t" \
  "s_mov_b64 exec, -1\n\t" \
  "buffer_load_dwordx4 v5, %[sw], s69 offen offset:0 lds\n\t" \
  "s_load_dword s84, %[po], 0x400\n\t" \
  "buffer_load_dwordx4 v6, %[sw], s69 offen offset:1024 lds\n\t" \
  "buffer_load_dwordx4 v7, %[sw], s69 offen offset:2048 lds\n\t" \
  "s_add_u32 s68, %[rx3], 148992\n\t" \
  "s_add_u32 s69, %[rw3], 4915200\n\t" \
  "s_add_u32 m0, %[xb], 46080\n\t" \
  "s_waitcnt vmcnt(18)\n\t" \
  "s_barrier\n\t" \
  "buffer_load_dwordx4 v2, %[sx], s68 offen offset:0 lds\n\t" \
  "s_load_dword s84, %[po], 0x440\n\t" \
  "buffer_load_dwordx4 v3, %[sx], s68 offen offset:1024 lds\n\t" \
  "s_load_dword s84, %[po], 0x480\n\t" \
  "s_mov_b64 exec, 0xffff\n\t" \
  "buffer_load_dwordx4 v4, %[sx], s68 offen offset:2048 lds\n\t" \
  "s_add_u32 m0, %[wb], 61440\n\t" \
  "s_mov_b64 exec, -1\n\t" \
  "buffer_load_dwordx4 v5, %[sw], s69 offen offset:0 lds\n\t" \
  "s_load_dword s84, %[po], 0x4c0\n\t" \
  "buffer_load_dwordx4 v6, %[sw], s69 offen offset:1024 lds\n\t" \
  "buffer_load_dwordx4 v7, %[sw], s69 offen offset:2048 lds\n\t" \
  "v_readlane_b32 s68, v9, 0\n\t" \
  "v_readlane_b32 s69, v9, 1\n\t" \
  "v_readlane_b32 s70, v9, 2\n\t" \
  "v_readlane_b32 s71, v9, 18\n\t" \
  "v_readlane_b32 s72, v9, 19\n\t" \
  "s_add_u32 m0, %[ob], 0\n\t" \
  "s_waitcnt vmcnt(15)\n\t" \
  "s_barrier\n\t" \
  "s_nop 2\n\t" \
  "buffer_load_dwordx4 v8, %[so], s68 offen lds\n\t" \
  "s_load_dword s84, %[po], 0x500\n\t" \
  "s_add_u32 m0, %[ob], 4096\n\t" \
  "s_nop 0\n\t" \
  "buffer_load_dwordx4 v8, %[so], s69 offen lds\n\t" \
  "s_load_dword s84, %[po], 0x540\n\t" \
  "s_add_u32 m0, %[ob], 8192\n\t" \
  "s_nop 0\n\t" \
  "buffer_load_dwordx4 v8, %[so], s70 offen lds\n\t" \
  "s_load_dword s84, %[po], 0x580\n\t" \
  "s_add_u32 m0, %[ob], 73728\n\t" \
  "s_nop 0\n\t" \
  "buffer_load_dwordx4 v8, %[so], s71 offen lds\n\t" \
  "s_add_u32 m0, %[ob], 77824\n\t" \
  "s_nop 0\n\t" \
  "buffer_load_dwordx4 v8, %[so], s72 offen lds\n\t" \
  "v_readlane_b32 s68, v9, 3\n\t" \
  "v_readlane_b32 s69, v9, 4\n\t" \
  "v_readlane_b32 s70, v9, 5\n\t" \
  "v_readlane_b32 s71, v9, 20\n\t" \
  "v_readlane_b32 s72, v9, 21\n\t" \
  "s_add_u32 m0, %[ob], 12288\n\t" \
  "s_waitcnt vmcnt(17)\n\t" \
  "s_barrier\n\t" \
  "s_nop 2\n\t" \
  "buffer_load_dwordx4 v8, %[so], s68 offen lds\n\t" \
  "s_load_dword s84, %[po], 0x5c0\n\t" \
  "s_add_u32 m0, %[ob], 16384\n\t" \
  "s_nop 0\n\t" \
  "buffer_load_dwordx4 v8, %[so], s69 offen lds\n\t" \
  "s_load_dword s84, %[po], 0x600\n\t" \
  "s_add_u32 m0, %[ob], 20480\n\t" \
  "s_nop 0\n\t" \
  "buffer_load_dwordx4 v8, %[so], s70 offen lds\n\t" \
  "s_load_dword s84, %[po], 0x640\n\t" \
  "s_add_u32 m0, %[ob], 81920\n\t" \
  "s_nop 0\n\t" \
  "buffer_load_dwordx4 v8, %[so], s71 offen lds\n\t" \
  "s_add_u32 m0, %[ob], 86016\n\t" \
  "s_nop 0\n\t" \
  "buffer_load_dwordx4 v8, %[so], s72 offen lds\n\t" \
  "v_readlane_b32 s68, v9, 6\n\t" \
  "v_readlane_b32 s69, v9, 7\n\t" \
  "v_readlane_b32 s70, v9, 8\n\t" \
  "v_readlane_b32 s71, v9, 22\n\t" \
  "v_readlane_b32 s72, v9, 23\n\t" \
  "v_readlane_b32 s73, v9, 24\n\t" \
  "s_add_u32 m0, %[ob], 24576\n\t" \
  "s_waitcnt vmcnt(13)\n\t" \
  "s_barrier\n\t" \
  "s_nop 2\n\t" \
  "buffer_load_dwordx4 v8, %[so], s68 offen lds\n\t" \
  "s_load_dword s84, %[po], 0x680\n\t" \
  "s_add_u32 m0, %[ob], 28672\n\t" \
  "s_nop 0\n\t" \
  "buffer_load_dwordx4 v8, %[so], s69 offen lds\n\t" \
  "s_load_dword s84, %[po], 0x6c0\n\t" \
  "s_add_u32 m0, %[ob], 32768\n\t" \
  "s_nop 0\n\t" \
  "buffer_load_dwordx4 v8, %[so], s70 offen lds\n\t" \
  "s_load_dword s84, %[po], 0x700\n\t" \
  "s_add_u32 m0, %[ob], 90112\n\t" \
  "s_nop 0\n\t" \
  "buffer_load_dwordx4 v8, %[so], s71 offen lds\n\t" \
  "s_add_u32 m0, %[ob], 94208\n\t" \
  "s_nop 0\n\t" \
  "buffer_load_dwordx4 v8, %[so], s72 offen lds\n\t" \
  "s_add_u32 m0, %[ob], 98304\n\t" \
  "s_nop 0\n\t" \
  "buffer_load_dwordx4 v8, %[so], s73 offen lds\n\t" \
  "v_readlane_b32 s68, v9, 9\n\t" \
  "v_readlane_b32 s69, v9, 10\n\t" \
  "v_readlane_b32 s70, v9, 11\n\t" \
  "v_readlane_b32 s71, v9, 25\n\t" \
  "v_readlane_b32 s72, v9, 26\n\t" \
  "s_add_u32 m0, %[ob], 36864\n\t" \
  "s_waitcnt vmcnt(16)\n\t" \
  "s_barrier\n\t" \
  "s_nop 2\n\t" \
  "buffer_load_dwordx4 v8, %[so], s68 offen lds\n\t" \
  "s_load_dword s84, %[po], 0x740\n\t" \
  "s_add_u32 m0, %[ob], 40960\n\t" \
  "s_nop 0\n\t" \
  "buffer_load_dwordx4 v8, %[so], s69 offen lds\n\t" \
  "s_load_dword s84, %[po], 0x780\n\t" \
  "s_add_u32 m0, %[ob], 45056\n\t" \
  "s_nop 0\n\t" \
  "buffer_load_dwordx4 v8, %[so], s70 offen lds\n\t" \
  "s_load_dword s84, %[po], 0x7c0\n\t" \
  "s_add_u32 m0, %[ob], 102400\n\t" \
  "s_nop 0\n\t" \
  "buffer_load_dwordx4 v8, %[so], s71 offen lds\n\t" \
  "s_add_u32 m0, %[ob], 106496\n\t" \
  "s_nop 0\n\t" \
  "buffer_load_dwordx4 v8, %[so], s72 offen lds\n\t" \
  "v_readlane_b32 s68, v9, 12\n\t" \
  "v_readlane_b32 s69, v9, 13\n\t" \
  "v_readlane_b32 s70, v9, 14\n\t" \
  "v_readlane_b32 s71, v9, 27\n\t" \
  "v_readlane_b32 s72, v9, 28\n\t" \
  "v_readlane_b32 s73, v9, 29\n\t" \
  "s_add_u32 m0, %[ob], 49152\n\t" \
  "s_nop 3\n\t" \
  "s_barrier\n\t" \
  "s_nop 2\n\t" \
  "buffer_load_dwordx4 v8, %[so], s68 offen lds\n\t" \
  "s_add_u32 m0, %[ob], 53248\n\t" \
  "s_nop 0\n\t" \
  "buffer_load_dwordx4 v8, %[so], s69 offen lds\n\t" \
  "s_add_u32 m0, %[ob], 57344\n\t" \
  "s_nop 0\n\t" \
  "buffer_load_dwordx4 v8, %[so], s70 offen lds\n\t" \
  "s_add_u32 m0, %[ob], 110592\n\t" \
  "s_nop 0\n\t" \
  "buffer_load_dwordx4 v8, %[so], s71 offen lds\n\t" \
  "s_add_u32 m0, %[ob], 114688\n\t" \
  "s_nop 0\n\t" \
  "buffer_load_dwordx4 v8, %[so], s72 offen lds\n\t" \
  "s_add_u32 m0, %[ob], 118784\n\t" \
  "s_nop 0\n\t" \
  "buffer_load_dwordx4 v8, %[so], s73 offen lds\n\t" \
  "v_readlane_b32 s68, v9, 15\n\t" \
  "v_readlane_b32 s69, v9, 16\n\t" \
  "v_readlane_b32 s70, v9, 17\n\t" \
  "v_readlane_b32 s71, v9, 30\n\t" \
  "v_readlane_b32 s72, v9, 31\n\t" \
  "s_add_u32 m0, %[ob], 61440\n\t" \
  "LS_%=:\n\t" \
  "ds_read_b32 v11, v10\n\t" \
  "s_addk_i32 %[cn], 1\n\t" \
  "s_cmp_gt_u32 %[cn], 0x400000\n\t" \
  "s_cbranch_scc1 LX_%=\n\t" \
  "s_waitcnt lgkmcnt(0)\n\t" \
  "v_cmp_eq_u32 vcc, 0, v11\n\t" \
  "s_cbranch_vccz LX_%=\n\t" \
  "s_sleep 1\n\t" \
  "s_branch LS_%=\n\t" \
  "LX_%=:\n\t" \
  "s_nop 2\n\t" \
  "buffer_load_dwordx4 v8, %[so], s68 offen lds\n\t" \
  "s_add_u32 m0, %[ob], 65536\n\t" \
  "s_nop 0\n\t" \
  "buffer_load_dwordx4 v8, %[so], s69 offen lds\n\t" \
  "s_add_u32 m0, %[ob], 69632\n\t" \
  "s_nop 0\n\t" \
  "buffer_load_dwordx4 v8, %[so], s70 offen lds\n\t" \
  "s_add_u32 m0, %[ob], 122880\n\t" \
  "s_nop 0\n\t" \
  "buffer_load_dwordx4 v8, %[so], s71 offen lds\n\t" \
  "s_add_u32 m0, %[ob], 126976\n\t" \
  "s_nop 0\n\t" \
  "buffer_load_dwordx4 v8, %[so], s72 offen lds\n\t" \
  "s_waitcnt vmcnt(0) lgkmcnt(0)\n\t"
#define LD_HT 8
#define LD_CLOB "v11", "s68", "s69", "s70", "s71", "s72", "s73", "s74", "s75", "s76", "s77", "s78", "s79", "s80", "s81", "s82", "s83", "s84", "s85"

#define ENDW
#define TS(i)
#define TSS(i)
#define TOUT(n)

#define SPIN(cond,slp) do{ int sp_=0; while(cond){ __builtin_amdgcn_s_sleep(slp); if(++sp_>(1<<22)) break; } }while(0)
__device__ __forceinline__ unsigned rd1(const char* p){ return __builtin_amdgcn_readfirstlane(*(volatile LDS_AS unsigned*)(LDS_AS char*)p); }
__device__ __forceinline__ unsigned lds_inc(const char* p, int l){ unsigned r=0; if(l==0) r=__hip_atomic_fetch_add((LDS_AS unsigned*)(LDS_AS char*)p, 1u, __ATOMIC_RELAXED, __HIP_MEMORY_SCOPE_WORKGROUP); return __builtin_amdgcn_readfirstlane(r); }
__device__ __forceinline__ void lds_add(unsigned adr){ asm volatile("s_mov_b64 exec, 1\n\tds_add_u32 %0, %1\n\ts_mov_b64 exec, -1" :: "v"(adr), "v"(1u) : "memory"); }
__device__ __forceinline__ void bars(int n){
  asm volatile("BL_%=:\n\ts_barrier\n\ts_sub_u32 %0, %0, 1\n\ts_cmp_lg_u32 %0, 0\n\ts_cbranch_scc1 BL_%=" : "+s"(n) :: "memory", "scc");
}
#define barc(rem) barc_(rem)
#define CTRA
#define CTRP
__device__ __forceinline__ void barc_(unsigned& rem){
  asm volatile("s_cmp_eq_u32 %0, 0\n\ts_cbranch_scc1 BC_%=\n\ts_barrier\n\ts_sub_u32 %0, %0, 1\n\tBC_%=:" : "+s"(rem) :: "memory", "scc");
}
template<int SLP> __device__ __forceinline__ void wait_flag_b(const unsigned* p, unsigned voff, unsigned tag, unsigned& rem CTRA){
  int spins=0;
  while(true){
    unsigned v; asm volatile("global_load_dword %0, %1, %2 sc1" : "=v"(v) : "v"(voff), "s"(p) : "memory");
    barc(rem); if(PB2) barc(rem);
    asm volatile("s_waitcnt vmcnt(0)" : "+v"(v) :: "memory");
    if(__all(v==tag) || ++spins>(1<<21)) break;
    if(rem==0) __builtin_amdgcn_s_sleep(SLP);
  }
}
__device__ __forceinline__ void stout(void* sbase, unsigned voff, u32x4 v){
  st16cs(sbase,voff,v);
}

typedef unsigned u32x8 __attribute__((ext_vector_type(8)));
typedef unsigned u32x16 __attribute__((ext_vector_type(16)));
__device__ __forceinline__ char* ptr64(unsigned lo, unsigned hi){ return (char*)(((unsigned long long)hi<<32)|lo); }
__device__ __forceinline__ u32x4 srd(const char* p, unsigned bytes){ return u32x4{(unsigned)(size_t)p,(unsigned)((size_t)p>>32)&0xffffu,bytes,0x20000u}; }
constexpr size_t WS_O=0, WS_KV=(size_t)S*DM*2, WS_Y=(size_t)S*DM*4, WS_FL=(size_t)S*DM*12;

__global__ __launch_bounds__(768,3) void fused_kernel(const u16* x_, const u16* wqkv_, const u16* wo_, char* ws_, u16* out_,
    const u16* kin_, const u16* vin_, const u16* shk_, const u16* hk_, const u16* qin_, const u16* sD_)
{
  __shared__ __attribute__((aligned(128))) char smem[ALL_LDS];
  TS(0);
  const auto ka=__builtin_amdgcn_kernarg_segment_ptr();
  int w8;
  {
    unsigned t=threadIdx.x;
    asm volatile(
      "s_getpc_b64 s[72:73]\n\tv_readfirstlane_b32 %[w8], %[t]\n\ts_lshr_b32 s74, %[b], 3\n\ts_mul_i32 s74, s74, 12\n\ts_lshr_b32 %[w8], %[w8], 6\n\t"
      "s_add_u32 s74, s74, %[w8]\n\ts_min_u32 s74, s74, %[cl]\n\ts_lshl_b32 s74, s74, 7\n\ts_mov_b32 m0, %[sk]\n\tv_mov_b32 v1, s74\n\ts_mov_b64 exec, 1\n\tglobal_load_lds_dword v1, s[72:73]\n\ts_mov_b64 exec, -1\n\t"
      "s_bitcmp1_b32 %[w8], 2\n\ts_cbranch_scc1 EX_%=\n\t"
      "s_load_dwordx4 s[68:71], %[ka], 0x0\n\tv_and_b32 v1, 0xff, %[t]\n\ts_bitcmp1_b32 %[w8], 3\n\ts_cbranch_scc1 EC_%=\n\t"
      "s_mul_i32 s74, %[b], %[nw]\n\tv_cmp_gt_u32 vcc, %[nw], v1\n\tv_add_lshl_u32 v1, v1, s74, 7\n\ts_and_b64 exec, vcc, exec\n\ts_cbranch_execz EE_%=\n\ts_waitcnt lgkmcnt(0)\n\tglobal_load_lds_dword v1, s[70:71]\n\ts_branch EE_%=\n\t"
      "EC_%=:\n\tv_cmp_gt_u32 vcc, %[nx], v1\n\tv_and_b32 v2, 7, v1\n\tv_lshlrev_b32 v1, 4, v1\n\t"
      "s_lshl_b32 s74, %[b], 19\n\ts_lshl_b32 s73, %[b], 11\n\ts_or_b32 s74, s74, s73\n\ts_and_b32 s74, s74, 0x3fc000\n\t"
      "v_and_b32 v1, 0xf80, v1\n\tv_lshl_add_u32 v1, v2, 11, v1\n\tv_add_u32 v1, s74, v1\n\t"
      "s_and_b64 exec, vcc, exec\n\ts_cbranch_execz EE_%=\n\ts_waitcnt lgkmcnt(0)\n\tglobal_load_lds_dword v1, s[68:69]\n\t"
      "EE_%=:\n\ts_mov_b64 exec, -1\n\ts_waitcnt lgkmcnt(0)\n\tEX_%=:"
      : [w8]"=&s"(w8) : [ka]"s"(ka), [t]"v"(t), [b]"s"(blockIdx.x), [cl]"n"(CODEL-1), [sk]"s"(lds_addr(smem)+SINK+256), [nw]"s"(12*TPW), [nx]"s"(8*TPX)
      : "memory", "scc", "vcc", "v1", "v2", "s68", "s69", "s70", "s71", "s72", "s73", "s74");
  }
  #define ROLE u32x8 a0; int bid=blockIdx.x; asm volatile("s_load_dwordx8 %0, %2, 0x0" : "=&s"(a0), "+s"(bid) : "s"(ka)); \
    const int w=w8&3, xcd=bid&7, idx=bid>>3, rb=(xcd>>1)*4+(idx&3), hp=(xcd&1)*8+(idx>>2), s0=rb*128; (void)s0; (void)hp;
  #define LANEVARS int l; asm volatile("v_mbcnt_lo_u32_b32 %0, -1, 0\n\tv_mbcnt_hi_u32_b32 %0, -1, %0" : "=v"(l)); const int g=l>>4, fr=l&15, tq=(l&15)>>2, tp=l&3; (void)g; (void)fr; (void)tq; (void)tp;
  #define ARGS0 asm volatile("s_waitcnt lgkmcnt(0)" : "+s"(a0)); const char* x=ptr64(a0[0],a0[1]); const char* wqkv=ptr64(a0[2],a0[3]); const char* wo=ptr64(a0[4],a0[5]); char* ws=ptr64(a0[6],a0[7]); \
    unsigned* flags=(unsigned*)(ws+WS_FL); const int hot0=(3-(int)(a0[2]>>7))&7; (void)x; (void)wqkv; (void)wo; (void)flags; (void)hot0;
  #define ORA ((unsigned)(hp-hot0-1)&7u)
  #define ORD(c) ((((ORA==7u? ((c)==0u||(c)==15u? 7u : ((c)-1u)>>1) : ((c)<14u? (ORA+((c)>>1))%7u : 7u))+hot0+1)&7u)+8u*(((hp>>3)^(c))&1u))
  const unsigned sb0=__builtin_amdgcn_readfirstlane(lds_addr(smem));
  const char* syn=smem+SYN;

  if(__builtin_expect((w8>>2)==1,1)){
    ROLE
    LANEVARS
    __builtin_amdgcn_s_setprio(LPR);
    unsigned ltr=0, ltr2=0, ltr3=0; (void)ltr; (void)ltr2; (void)ltr3;
    const int xr=18*w+(l>>3);
    const unsigned ch=(unsigned)(((l&7)^(xr&7))*16);
    unsigned xo=(unsigned)(xr*2048)+ch;
    unsigned xoA=(s0==0 && xr<16)? 16*2048+ch : xo, xoB=((s0==0 && xr+8<16)? 8*2048+ch : xo)+15360;
    unsigned xo1=xo+15360, xo2=xo+30720;
    unsigned wof[3];
    #pragma unroll
    for(int i=0;i<3;i++){ const int P=3*w+i, wr=8*(P&3)+(l>>3); const int ci=((l&7)>>1)^wswz(wr); wof[i]=(unsigned)(wr*6144+(P>>2)*2048-1024*i+(MIX? (ci>>1)*128+(ci&1)*32 : ci*32)+(l&1)*16); }
    unsigned xl=sb0+X_OFF+w*2304, wl=sb0+w*3072, xd=(unsigned)(s0-16)*2048u, wd=hp*128, hq=hp&7, rq=rb&3;
    asm volatile("" : "+{v0}"(xoA), "+{v1}"(xoB), "+{v2}"(xo), "+{v3}"(xo1), "+{v4}"(xo2), "+{v5}"(wof[0]), "+{v6}"(wof[1]), "+{v7}"(wof[2]), "+s"(xl), "+s"(wl), "+s"(xd), "+s"(wd), "+s"(hq), "+s"(rq));
    TSS(2);
    ARGS0
    const unsigned wm=MIX? (wd&~255u)+((wd>>1)&64u) : wd;
    const u32x4 sx=srd(x+(int)xd,144u*2048u), sw=srd(wqkv+wm,6291456u-wm);
    const int jj=(ROT && (MIX? ((int)hq|1)==(hot0|1) : (int)hq==hot0))? rq : 0;
    TSS(3);
    asm volatile(LD_ASM1
      :: "{v0}"(xoA), "{v1}"(xoB), "{v2}"(xo), "{v3}"(xo1), "{v4}"(xo2), "{v5}"(wof[0]), "{v6}"(wof[1]), "{v7}"(wof[2]),
        [sx]"s"(sx), [sw]"s"(sw), [xb]"s"(xl), [wb]"s"(wl), [rx0]"s"(jj*128), [rw0]"s"(jj*393216)
      : "memory", "scc", "s68");
    {
      const unsigned wof2=(unsigned)(w*16384+(l>>3)*2048+((((l&7)>>1)^((l>>4)&3))*32)+(l&1)*16);
      const unsigned to=ORD((unsigned)(l>>1)&15u);
      const unsigned tab=MIX? (to&~1u)*131072u+(l&1)*131072u+(to&1u)*65536u : to*131072u+(l&1)*65536;
      unsigned long long tm; asm("s_cmp_eq_u32 %1, 0\n\ts_cselect_b64 %0, %2, 0" : "=s"(tm) : "s"(w), "s"(WTL==2? 0xffffffffull : 0ull) : "scc");
      unsigned cn=0;
      const unsigned hn=(unsigned)(w*(3*LD_HT/4)+l), hto=(hn/6u)*393216u+(hn%6u)*2048u;
      asm volatile(LD_ASM2
        : [cn]"+&s"(cn)
        : "{v0}"(xoA), "{v1}"(xoB), "{v2}"(xo), "{v3}"(xo1), "{v4}"(xo2), "{v5}"(wof[0]), "{v6}"(wof[1]), "{v7}"(wof[2]), "{v8}"(wof2), "{v9}"(tab),
          "{v10}"(sb0+SYN+32+(l&3)*4), "{v12}"((l&31)*2048),
          "{v16}"(hto), [sh]"s"(srd(wqkv+((xcd&1)*8+hot0)*128+idx*12288,6291456u-2048u-idx*12288u+128u)),
          [sx]"s"(sx), [sw]"s"(sw), [so]"s"(srd(wo+wd,2097152u-wd)), [xb]"s"(xl), [wb]"s"(wl), [ob]"s"(sb0+w*1024),
          [rx0]"s"(jj*128), [rx1]"s"(((jj+1)&3)*128), [rx2]"s"(((jj+2)&3)*128), [rx3]"s"(((jj+3)&3)*128),
          [rw0]"s"(jj*393216), [rw1]"s"(((jj+1)&3)*393216), [rw2]"s"(((jj+2)&3)*393216), [rw3]"s"(((jj+3)&3)*393216),
          [pw]"s"(wqkv+idx*6144+(xcd&1)*1024+(xcd>>1)*256+w*64), [px]"s"(x+(xcd*256+idx*8+w*2)*2048), [po]"s"(wo+(bid*4+w)*2048),
          [tsk]"s"(sb0+SINK+256), [tm]"s"(tm), [tp]"s"(wo+idx*65536+(xcd&1)*1024+((3-(int)(a0[4]>>7))&7)*128)
        : "memory", "scc", "vcc", LD_CLOB);
    }
    const unsigned tag=rd1(syn+64)+1u;
    lds_add(sb0+SYN+12);
    TS(2);
    if(w==3){
      const unsigned* fp=flags+F_O+(rb*16+hp)*16; const unsigned fo=ORD((unsigned)l&15u)*4u;
      const unsigned madr=sb0+(l==0? SYN+20 : SINK+l*4);
      SPIN(rd1(syn+12)<4u || rd1(syn+16)<4u,1);
      PUB(madr,1u);
      unsigned rdy=1; int spins=0;
      while(rdy!=0xFFFFu){
        unsigned v;
        asm volatile("s_mov_b64 exec, 0xffff\n\tglobal_atomic_or %0, %1, %3, %2 sc0 sc1\n\ts_mov_b64 exec, -1\n\ts_waitcnt vmcnt(0)" : "=&v"(v) : "v"(fo), "s"(fp), "v"(0u) : "memory");
        unsigned r=((unsigned)__ballot(v==tag)&0xFFFFu)|1u;
        if(++spins>(1<<21)) r=0xFFFFu;
        if(r!=rdy){ PUB(madr,(unsigned)__builtin_ctz(~r)); rdy=r; }
        else __builtin_amdgcn_s_sleep(OSLP);
      }
      TS(3);
      if(bid==0){
        int sp=0;
        while(true){
          u32x4 v; asm volatile("global_load_dwordx4 %0, %1, %2 sc1\n\ts_waitcnt vmcnt(0)" : "=&v"(v) : "v"(l*16), "s"(flags+F_Y) : "memory");
          if(__all(v[0]==tag && v[1]==tag && v[2]==tag && v[3]==tag) || ++sp>(1<<20)) break;
          __builtin_amdgcn_s_sleep(12);
        }
        if(l==0) stflag(flags, tag);
      }
    } else { TS(3); }
    TOUT(4);
    return;
  }

  if(w8<4){
    ROLE
    u32x2 a1; asm volatile("s_load_dwordx2 %0, %1, 0x20" : "=&s"(a1) : "s"(ka));
    __builtin_amdgcn_s_setprio(GPR);
    const int h=w&1, r=w>>1;
    f32x16 e0, e1;
    LANEVARS
    TSS(3);
    ARGS0
    asm volatile("" : "+s"(a1));
    if(PGT&1){ if(w==1 && l<7) touch(ws, l<6? l*2097152+PGO : (unsigned)WS_FL+2048, sb0+SINK+512); }
    if(PGT&2){ if(w==2 && l<4) touch(ptr64(a1[0],a1[1]), l*2097152+PGO, sb0+SINK+512); }
    if(PGT&4){ if(w==3 && l<1) touch(wo, PGO, sb0+SINK+512); if(w==0 && l<2) touch(wqkv, l*2097152+2097152, sb0+SINK+512); }
    TSS(4);
    {
      unsigned lx0=sb0+X_OFF+fr*128+((g^(fr&7))*16), lx1=sb0+X_OFF+fr*128+(((4+g)^(fr&7))*16);
      const int sw=wswz(8*g+tq);
      unsigned lw0=sb0+(8*g+tq)*128+(unsigned)((((2*h)^sw)*32)+8*tp), lw1=sb0+(8*g+tq)*128+(unsigned)((((2*h+1)^sw)*32)+8*tp);
      char* ot=ws+WS_O+(rb*16+hp)*16384+r*8192+h*2048;
      unsigned it; unsigned long long rm; asm("s_cmp_lg_u32 %1, 0\n\ts_cselect_b64 %0, -1, 0" : "=s"(rm) : "s"(r) : "scc");
      asm volatile(N2_ASM
        : [it]"=&s"(it), "+{v56}"(lx0), "+{v57}"(lx1), "+{v58}"(lw0), "+{v59}"(lw1)
        : "{v62}"(l*16), "{v63}"(sb0+EX_OFF+h*3072+l*16), [r]"s"(r), [ot]"s"(ot), [ou]"s"(ot+4096), [fa]"s"(sb0+SYN+32+w*4), [fp]"s"(sb0+SYN+32+(w^2)*4),
          [vo]"s"(2048-1024*r), [rm]"s"(rm), [z]"s"(rb|r)
        : "memory", "scc", "vcc", N2_CLOB);
    }
    TS(3);
    unsigned tb;
    {
      LANEVARS
      const unsigned tag=rd1(syn+64)+1u;
      const unsigned ra=sb0+(4*g+tq)*128+8*tp; const int sw=(2*g+(tq>>1))&3;
      unsigned b0=ra+((0^sw)*32), b1=ra+((1^sw)*32), b2=ra+((2^sw)*32), b3=ra+((3^sw)*32), vo=(unsigned)(w*4096+l*16);
      tb=ORD((unsigned)l&15u)*16384u;
      asm volatile("" : "+{v160}"(b0), "+{v161}"(b1), "+{v162}"(b2), "+{v163}"(b3), "+{v164}"(vo), "+{v167}"(tb));
      WAITV(0);
      if(lds_inc(syn+16,l)==3u){ if(l<16) stflag(flags+F_O+(rb*16+l)*16+hp, tag); }
      TS(4);
      __builtin_amdgcn_s_setprio(G2P);
      unsigned c=0, iss=0, th, q0, q1, cn=0;
      asm volatile(G3_ASM
        : "={v[128:143]}"(e0), "={v[144:159]}"(e1), [c]"+s"(c), [iss]"+s"(iss), [th]"=&s"(th), [t0]"=&s"(q0), [t1]"=&s"(q1), [cn]"+&s"(cn),
          "+{v160}"(b0), "+{v161}"(b1), "+{v162}"(b2), "+{v163}"(b3), "+{v167}"(tb)
        : "{v164}"(vo), "{v165}"(sb0+SYN+20), [go]"s"(60+w*4), [ob]"s"(srd(ws+WS_O+rb*262144,262144u))
        : "memory", "scc", "vcc", G3_CLOB);
    }
    {
      LANEVARS
      char* sg=smem+EX_OFF+w*2048;
      char* op=ptr64(a1[0],a1[1])+(s0+w*32)*4096+2048+hp*128; const unsigned oo=(unsigned)((l>>3)*4096+(l&7)*16);
      u32x4 ov[4];
      #pragma unroll
      for(int t=0;t<2;t++){
        #pragma unroll
        for(int j=0;j<4;j++){
          #define E2(i) (t? e1[(i)&15] : e0[(i)&15])
          *(volatile LDS_AS u32x2*)(LDS_AS char*)(sg+fr*128+(((2*j+(g>>1))^(fr&7))*16)+(g&1)*8)=u32x2{pack2(E2(4*j),E2(4*j+1)),pack2(E2(4*j+2),E2(4*j+3))};
        }
        #pragma unroll
        for(int i=0;i<2;i++) ov[2*t+i]=*(volatile LDS_AS u32x4*)(LDS_AS char*)(sg+(i*8+(l>>3))*128+(((l&7)^(l>>3))*16));
      }
      #pragma unroll
      for(int i=0;i<4;i++) stout(op+i*32768, oo, ov[i]);
      ENDW TS(6); TOUT(7);
    }
    return;
  }

  {
    ROLE
    u32x8 a1; u32x4 a2; u32x2 a3;
    asm volatile("s_load_dwordx8 %0, %3, 0x20\n\ts_load_dwordx4 %1, %3, 0x40\n\ts_load_dwordx2 %2, %3, 0x50" : "=&s"(a1), "=&s"(a2), "=&s"(a3) : "s"(ka));
    char* cr=smem+C_OFF+2048+w*4096;
    const unsigned crs=sb0+C_OFF+2048+w*4096;
    const int sblk=bid>>3, td0=(bid&7)*128+(w&1)*64;
    const int vb=(LOCA&1)? xcd*32+(idx&16)+(idx&1)*8+((idx>>1)&7) : bid, fb=(LOCA&2)? (idx&15)*16+xcd*2+(idx>>4) : bid;
    const int d=(vb>>4)*64+(((vb>>3)&1)*4+w)*8+(vb&7);
    const int kv0=KV0+(sblk&KVS);
    static_assert(KV0+KVS+PP_NB<=33, "barrier budget");
    unsigned tagv, ctr; f32x8 RB;
    const int fd0=(fb&15)*64, fs0=(fb>>4)*128+w*32;
    u32x4 qv[4];
    f32x16 A0,A1,A2,A3;
    const int rw=sblk*64+(w>>1)*32, j=rw>>5;
    unsigned k0,k1,k2,ko,ta,se,so,ha,l16,rb0[2];
    {
      LANEVARS
      if(w==0 && l<25 && l!=16) ((unsigned*)(smem+SYN))[l]=0u;
      if(l<32) *(u32x4*)(smem+C_OFF+w*512+l*16)=u32x4{0,0,0,0};
      asm volatile("" ::: "memory");
      __builtin_amdgcn_s_setprio(CPR);
      const int dg=l&7, rg=l>>3;
      ko=(unsigned)(rg*8192+dg*16);
      const bool top=(rw==0 && rg==0);
      k0=top? ko+6144:ko; k1=top? ko+6144:ko+2048; k2=top? ko+6144:ko+4096;
      ta=sb0+EX_OFF+w*1024+(w&1)*512+dg*64; se=(l&8)? 0x03020706u : 0x05040100u; ha=crs+(32*g-4*tq+4*tp)*2; l16=l*16;
      so=(unsigned)(rg*32768+((j>>2)&3)*128+((dg&4)|((dg+j)&3))*16);
      #pragma unroll
      for(int k=0;k<2;k++){ const int q=fr-1-2*g+k; rb0[k]=sb0+C_OFF+(4+(q>>1))*256+((q>>3)&3)*64+((w+(q>>1))&3)*16+(q&1)*8; }
      asm volatile("" : "+{v152}"(k0), "+{v153}"(k1), "+{v154}"(k2), "+{v155}"(ko), "+{v156}"(ta), "+{v157}"(se), "+{v159}"(so), "+{v163}"(l16), "+{v165}"(ha), "+{v166}"(rb0[0]), "+{v167}"(rb0[1]));
    }
    TSS(3);
    ARGS0
    asm volatile("" : "+s"(a1), "+s"(a2), "+s"(a3));
    char* out=ptr64(a1[0],a1[1]); const char* qp=ptr64(a2[2],a2[3])+(fs0*DM+fd0)*2;
    {
      const char* kb=ptr64(a1[2],a1[3])+(rw*DM+td0)*2; const char* vb=ptr64(a1[4],a1[5])+(rw*DM+td0)*2;
      unsigned long long zm; asm("s_cmp_eq_u32 %1, 0\n\ts_cselect_b64 %0, 0xff, 0" : "=s"(zm) : "s"(rw) : "scc");
      unsigned t0;
      if(PGT&8){ const char* tp= w==0? kb : w==1? vb : w==2? qp : ptr64(a2[0],a2[1])+d*4096;
        asm volatile("s_mov_b32 m0, %1\n\ts_mov_b64 exec, 1\n\tglobal_load_lds_dword v163, %0\n\ts_mov_b64 exec, -1" :: "s"(tp), "s"(sb0+SINK+512), "{v163}"(l16) : "memory"); }
      TSS(5);
      asm volatile(PP_ASM
        : [t0]"=&s"(t0), "=&{v164}"(ctr), "+{v159}"(so), "=&{v160}"(tagv), "=&{v[24:31]}"(RB), "=&{v[44:47]}"(qv[0]), "=&{v[48:51]}"(qv[1]), "=&{v[52:55]}"(qv[2]), "=&{v[56:59]}"(qv[3]),
          "=&{v[64:79]}"(A0), "=&{v[80:95]}"(A1), "=&{v[96:111]}"(A2), "=&{v[112:127]}"(A3)
        : "{v152}"(k0), "{v153}"(k1), "{v154}"(k2), "{v155}"(ko), "{v156}"(ta), "{v157}"(se), "{v166}"(rb0[0]), "{v167}"(rb0[1]), "{v165}"(ha), "{v163}"(l16),
          [qb0]"s"(qp), [qb2]"s"(qp+4096), [mk]"s"(0x180u), [em]"s"(1ull|(512ull<<w)), [hd]"s"(crs),
          [kb0]"s"(kb-6144), [kb3]"s"(kb), [kb5]"s"(kb+4096), [vb0]"s"(vb), [vb2]"s"(vb+4096), [sb]"s"(ws+WS_KV+td0*4096+j*512),
          [n0]"s"(kv0), [ff]"s"(flags+F_PP+(td0>>6)*64+j), [zm]"s"(zm), [shd]"s"(sb0+EX_OFF+w*1024), [tgd]"s"(sb0+SYN+64), [shp]"s"(ptr64(a1[6],a1[7])+(bid&7)*1024), [fl]"s"(flags),
          [hp]"s"(ptr64(a2[0],a2[1])+d*4096), [dp]"s"(ptr64(a3[0],a3[1])+d*2)
        : "memory", "scc", PP_CLOB);
    }
    LANEVARS
    const unsigned tag=__builtin_amdgcn_readfirstlane(tagv)+1u;
    unsigned rem=33-PP_NB-kv0;
    barc(rem);
    const unsigned* pf=flags+F_PP+(d>>6)*64;
    const char* kvp=ws+WS_KV+((vb>>4)*8+(vb&7))*32768+((vb>>3)&1)*64+w*8192;
    const unsigned kvo=(unsigned)((l>>2)*128+(l&3)*16);
    int bidx=((l>=16)? l-16 : (l==0? 63 : l+47))*4;
    asm volatile("" : "+{v32}"(bidx));
    __builtin_amdgcn_s_setprio(3);
    while(rem>PREM) barc(rem);
    wait_flag_b<8>(pf, l*4, tag, rem CTRP);
    #pragma unroll
    for(int i=0;i<4;i++){ if(i%KTH==0 && i) barc(rem); gldsc(kvp+i*2048, kvo, crs+i*1024); }
    barc(rem); barc(rem);
    WAITV(0);
    asm volatile("s_mov_b32 m0, %2\n\ts_mov_b64 exec, 1\n\tglobal_load_lds_dword %0, %1 sc1\n\ts_mov_b64 exec, -1" :: "v"(0u), "s"(pf), "s"(sb0+SYN+100+w*4) : "memory");
    while(rem) barc(rem);
    { int sp=0; while(!__all(*(volatile LDS_AS unsigned*)(LDS_AS char*)(syn+100+(l&3)*4)==tag) && ++sp<(1<<22)) __builtin_amdgcn_s_sleep(1); }
    __builtin_amdgcn_s_setprio(CVP);
    TS(3);
    f32x16 ac0, ac1;
    {
      unsigned p0,p1,p2,p3,p4,p5,p6;
      asm volatile("s_nop 1\n\t" CV_ASM
        : "=&{v[128:143]}"(ac0), "=&{v[144:159]}"(ac1), [p0]"=&s"(p0), [p1]"=&s"(p1), [p2]"=&s"(p2), [p3]"=&s"(p3), [p4]"=&s"(p4), [p5]"=&s"(p5), [p6]"=&s"(p6)
          , "+{v[44:47]}"(qv[0]), "+{v[48:51]}"(qv[1]), "+{v[52:55]}"(qv[2]), "+{v[56:59]}"(qv[3])
        : "{v[64:79]}"(A0), "{v[80:95]}"(A1), "{v[96:111]}"(A2), "{v[112:127]}"(A3),
          "{v[24:31]}"(RB), "{v32}"(bidx), "{v33}"(sb0+SYN+80+w*4)
          , "{v43}"((l>>3)*8192+(l&7)*16), [q0]"s"(qp), [q2]"s"(qp+4096)
        : "memory", "scc", CV_CLOB);
    }
    TS(4);
    __builtin_amdgcn_s_setprio(3);
    {
      const unsigned yo=(unsigned)((256*(g&1)+16*fr+4*(g&2))*2);
      #pragma unroll
      for(int J=0;J<8;J+=2){
        #define YA(i) ((i)<16? ac0[(i)&15] : ac1[(i)&15])
        const u32x2 a={pack2(YA(4*J),YA(4*J+1)),pack2(YA(4*J+2),YA(4*J+3))}, b={pack2(YA(4*J+4),YA(4*J+5)),pack2(YA(4*J+6),YA(4*J+7))};
        st16cs(ws+WS_Y+d*4096+512*J, yo, widen(a,b));
      }
    }
    WAITV(0);
    if(lds_inc(syn+24,l)==3u){ if(l<16) stflag(flags+F_Y+(l*16+(vb>>4))*16+(vb&15), tag); }
    TS(5);
    if(w==0){
      SPIN(rd1(syn+96)==0u,2);
      wait_flag<YSLP>(flags+F_Y+fb*16+(l&15), tag);
      SPIN(rd1(syn+24)<4u,1);
      PUB(sb0+(l==0? SYN+28 : SINK+l*4), 1u);
    } else SPIN(rd1(syn+28)==0u,4);
    #pragma unroll
    for(int i=0;i<4;i++) gldsc(ws+WS_Y+(fd0+i*16)*4096+fs0*2, (l>>2)*4096+(l&3)*16, crs+i*1024);
    WAITV(0);
    {
      u32x4 y2[4];
      #pragma unroll
      for(int i=0;i<4;i++){ const char* yp=cr+((i>>1)*32+g*8+tq)*64+(i&1)*32+tp*8; y2[i]=__builtin_bit_cast(u32x4,cat44(trread(yp),trread(yp+256))); }
      asm volatile("" : "+v"(y2[0]), "+v"(y2[1]), "+v"(y2[2]), "+v"(y2[3]) :: "memory");
      #pragma unroll
      for(int i=0;i<4;i++) *(u32x4*)(cr+((i&1)*16+fr)*128+((((i>>1)*4+g)^(fr&7))*16))=y2[i];
      asm volatile("" ::: "memory");
      char* op=out+fs0*4096+fd0*2; const unsigned oo=(unsigned)((l>>3)*16384+(l&7)*16);
      #pragma unroll
      for(int i=0;i<4;i++){
        const int row=4*(l>>3)+i;
        const u32x4 y4=*(const u32x4*)(cr+row*128+(((l&7)^(row&7))*16)), q4=qv[i]; u32x4 ov;
        #pragma unroll
        for(int e=0;e<4;e++) ov[e]=pack2(bflo(q4[e])*bflo(y4[e]), bfhi(q4[e])*bfhi(y4[e]));
        stout(op+i*4096, oo, ov);
      }
    }
    ENDW TS(6); TOUT(7);
  }
}

extern "C" void kernel_launch(void* const* d_in, const int* in_sizes, int n_in, void* d_out, int out_size, void* d_ws, size_t ws_size, hipStream_t stream){
  const u16* q=(const u16*)d_in[0]; const u16* k=(const u16*)d_in[1]; const u16* v=(const u16*)d_in[2];
  const u16* shk=(const u16*)d_in[3]; const u16* hk=(const u16*)d_in[4]; const u16* sD=(const u16*)d_in[5];
  const u16* x=(const u16*)d_in[6]; const u16* wqkv=(const u16*)d_in[7]; const u16* wo=(const u16*)d_in[8];
  fused_kernel<<<256,768,0,stream>>>(x,wqkv,wo,(char*)d_ws,(u16*)d_out,k,v,shk,hk,q,sD);
}
```

```cpp
#include <hip/hip_runtime.h>
#include <hip/hip_bf16.h>
#include <stdint.h>

typedef unsigned short u16;
typedef short bf16x8 __attribute__((ext_vector_type(8)));
typedef short s16x4 __attribute__((ext_vector_type(4)));
typedef float f32x2 __attribute__((ext_vector_type(2)));
typedef float f32x4 __attribute__((ext_vector_type(4)));
typedef float f32x8 __attribute__((ext_vector_type(8)));
typedef float f32x16 __attribute__((ext_vector_type(16)));
typedef unsigned u32x4 __attribute__((ext_vector_type(4)));
typedef unsigned u32x2 __attribute__((ext_vector_type(2)));
typedef __bf16 bf16x2_t __attribute__((ext_vector_type(2)));
#define LDS_AS __attribute__((address_space(3)))

__device__ __forceinline__ float bflo(unsigned x){ return __uint_as_float(x<<16); }
__device__ __forceinline__ float bfhi(unsigned x){ return __uint_as_float(x&0xffff0000u); }
__device__ __forceinline__ unsigned pack2(float lo, float hi){ return __builtin_bit_cast(unsigned, __builtin_convertvector(f32x2{lo,hi}, bf16x2_t)); }
__device__ __forceinline__ float mul1(float a, float b){ float r; asm("v_mul_f32 %0, %1, %2" : "=v"(r) : "v"(a), "v"(b)); return r; }
__device__ __forceinline__ float fma1(float a, float b, float c){ float r; asm("v_fma_f32 %0, %1, %2, %3" : "=v"(r) : "v"(a), "v"(b), "v"(c)); return r; }

__device__ __forceinline__ void glds(const void* sbase, unsigned voff, unsigned ldsaddr){
  asm volatile("s_mov_b32 m0, %2\n\ts_nop 0\n\tglobal_load_lds_dwordx4 %0, %1" :: "v"(voff), "s"(sbase), "s"(ldsaddr) : "memory");
}
__device__ __forceinline__ void gldsc(const void* sbase, unsigned voff, unsigned ldsaddr){
  asm volatile("s_mov_b32 m0, %2\n\ts_nop 0\n\tglobal_load_lds_dwordx4 %0, %1 sc1" :: "v"(voff), "s"(sbase), "s"(ldsaddr) : "memory");
}
__device__ __forceinline__ void touch(const void* sbase, unsigned voff, unsigned ldsaddr){
  asm volatile("s_mov_b32 m0, %2\n\ts_nop 0\n\tglobal_load_lds_dword %0, %1" :: "v"(voff), "s"(sbase), "s"(ldsaddr) : "memory");
}
__device__ __forceinline__ void st16c(void* p, u32x4 v){ asm volatile("global_store_dwordx4 %0, %1, off sc1\n\ts_nop 1" :: "v"(p), "v"(v) : "memory"); }
__device__ __forceinline__ void st16cs(void* sbase, unsigned voff, u32x4 v){ asm volatile("global_store_dwordx4 %0, %1, %2 sc1\n\ts_nop 1" :: "v"(voff), "v"(v), "s"(sbase) : "memory"); }
#define GLB_AS __attribute__((address_space(1)))
__device__ __forceinline__ unsigned ldflag(const unsigned* p){ return __hip_atomic_load((const GLB_AS unsigned*)p, __ATOMIC_RELAXED, __HIP_MEMORY_SCOPE_AGENT); }
__device__ __forceinline__ void stflag(unsigned* p, unsigned v){ __hip_atomic_store((GLB_AS unsigned*)p, v, __ATOMIC_RELAXED, __HIP_MEMORY_SCOPE_AGENT); }
template<int SLP> __device__ __forceinline__ void wait_flag(const unsigned* p, unsigned tag){
  int spins=0;
  while(true){ unsigned v=ldflag(p); if(__all(v==tag)) break; __builtin_amdgcn_s_sleep(SLP); if(++spins>(1<<21)) break; }
}
__device__ __forceinline__ unsigned lds_addr(const void* p){ return (unsigned)(size_t)(const LDS_AS void*)p; }
#define WAITV(n) asm volatile("s_waitcnt vmcnt(" #n ")":::"memory")
#define WAITL() asm volatile("s_waitcnt lgkmcnt(0)":::"memory")
#define SBAR() asm volatile("s_barrier":::"memory")
#define PUB(addr,val) asm volatile("ds_write_b32 %0, %1" :: "v"(addr), "v"(val) : "memory")
__device__ __forceinline__ s16x4 trread(const char* p){ return __builtin_amdgcn_ds_read_tr16_b64_v4i16((LDS_AS s16x4*)p); }
__device__ __forceinline__ bf16x8 cat44(s16x4 a, s16x4 b){ return __builtin_shufflevector(a,b,0,1,2,3,4,5,6,7); }
__device__ __forceinline__ int wswz(int row){ return ((row>>1)&1) | (((row>>3)&1)<<1); }
__device__ __forceinline__ u32x4 widen(u32x2 a, u32x2 b){
  auto r0=__builtin_amdgcn_permlane16_swap(a[0],b[0],false,false);
  auto r1=__builtin_amdgcn_permlane16_swap(a[1],b[1],false,false);
  return u32x4{r0[0],r1[0],r0[1],r1[1]};
}

constexpr int S=2048, DM=1024;
constexpr int WS=12288, XS=18432, X_OFF=6*WS;
constexpr int CREG=4608, C_OFF=131072;
constexpr int EX_OFF=C_OFF+4*CREG;
constexpr int SYN=EX_OFF+8192;
constexpr int SINK=SYN+256;
constexpr int ALL_LDS=SINK+768;
constexpr int F_PP=1024, F_O=4096, F_Y=8192;
#define KV0 1
#define KVS 3
#define LPR 2
#define CPR 2
#define GPR 1
#define CGATE 0
#define OSLP 2
#define YSLP 8
#define TPX 16
#define TPW 2
#define TWO 0
#define CVP 1
#define G2P 3
#define OUTC 1
#define ROT 0
#define WTL 2
#define PB2 1
#define PREM 9
#define KTH 1
#define YDEF 3
#define OPOL 1
#define CODEL 276
#define MIX 0
#define LOCA 3
#define XLOC 1
#define PGT 0
#define PGO 0
#define QREM 0
#define EB 0
#define TW0 1
#define HLAST 1

#define N2_ASM \
  "v_mov_b64 v[64:65], 0\n\t" \
  "v_mov_b64 v[66:67], 0\n\t" \
  "v_mov_b64 v[68:69], 0\n\t" \
  "v_mov_b64 v[70:71], 0\n\t" \
  "v_mov_b64 v[72:73], 0\n\t" \
  "v_mov_b64 v[74:75], 0\n\t" \
  "v_mov_b64 v[76:77], 0\n\t" \
  "v_mov_b64 v[78:79], 0\n\t" \
  "v_mov_b64 v[80:81], 0\n\t" \
  "v_mov_b64 v[82:83], 0\n\t" \
  "v_mov_b64 v[84:85], 0\n\t" \
  "v_mov_b64 v[86:87], 0\n\t" \
  "v_mov_b64 v[88:89], 0\n\t" \
  "v_mov_b64 v[90:91], 0\n\t" \
  "v_mov_b64 v[92:93], 0\n\t" \
  "v_mov_b64 v[94:95], 0\n\t" \
  "v_mov_b64 v[96:97], 0\n\t" \
  "v_mov_b64 v[98:99], 0\n\t" \
  "v_mov_b64 v[100:101], 0\n\t" \
  "v_mov_b64 v[102:103], 0\n\t" \
  "v_mov_b64 v[104:105], 0\n\t" \
  "v_mov_b64 v[106:107], 0\n\t" \
  "v_mov_b64 v[108:109], 0\n\t" \
  "v_mov_b64 v[110:111], 0\n\t" \
  "v_mov_b64 v[112:113], 0\n\t" \
  "v_mov_b64 v[114:115], 0\n\t" \
  "v_mov_b64 v[116:117], 0\n\t" \
  "v_mov_b64 v[118:119], 0\n\t" \
  "v_mov_b64 v[120:121], 0\n\t" \
  "v_mov_b64 v[122:123], 0\n\t" \
  "v_mov_b64 v[124:125], 0\n\t" \
  "v_mov_b64 v[126:127], 0\n\t" \
  "v_mov_b64 v[128:129], 0\n\t" \
  "v_mov_b64 v[130:131], 0\n\t" \
  "v_mov_b64 v[132:133], 0\n\t" \
  "v_mov_b64 v[134:135], 0\n\t" \
  "v_mov_b64 v[136:137], 0\n\t" \
  "v_mov_b64 v[138:139], 0\n\t" \
  "v_mov_b64 v[140:141], 0\n\t" \
  "v_mov_b64 v[142:143], 0\n\t" \
  "v_mov_b64 v[144:145], 0\n\t" \
  "v_mov_b64 v[146:147], 0\n\t" \
  "v_mov_b64 v[148:149], 0\n\t" \
  "v_mov_b64 v[150:151], 0\n\t" \
  "v_mov_b64 v[152:153], 0\n\t" \
  "v_mov_b64 v[154:155], 0\n\t" \
  "v_mov_b64 v[156:157], 0\n\t" \
  "v_mov_b64 v[158:159], 0\n\t" \
  "v_mov_b64 v[160:161], 0\n\t" \
  "v_mov_b64 v[162:163], 0\n\t" \
  "v_mov_b64 v[164:165], 0\n\t" \
  "v_mov_b64 v[166:167], 0\n\t" \
  "v_add_u32 v60, 36864, v58\n\t" \
  "v_add_u32 v61, 36864, v59\n\t" \
  "s_mov_b32 %[it], 0\n\t" \
  "s_cmp_lg_u32 %[r], 0\n\t" \
  "s_barrier\n\t" \
  "s_cbranch_scc1 NB_%=\n\t" \
  "ds_read_b64_tr_b16 v[40:41], v60 offset:13312\n\t" \
  "ds_read_b64_tr_b16 v[42:43], v60 offset:13824\n\t" \
  "ds_read_b64_tr_b16 v[44:45], v60 offset:14336\n\t" \
  "ds_read_b64_tr_b16 v[46:47], v60 offset:14848\n\t" \
  "ds_read_b64_tr_b16 v[48:49], v61 offset:13312\n\t" \
  "ds_read_b64_tr_b16 v[50:51], v61 offset:13824\n\t" \
  "ds_read_b128 v[0:3], v56 offset:36864\n\t" \
  "ds_read_b128 v[4:7], v56 offset:38912\n\t" \
  "ds_read_b128 v[8:11], v56 offset:40960\n\t" \
  "ds_read_b128 v[12:15], v56 offset:43008\n\t" \
  "ds_read_b128 v[16:19], v56 offset:45056\n\t" \
  "s_mov_b32 s68, 0x50000\n\t" \
  "s_mov_b32 s69, 0x5550055\n\t" \
  "s_mov_b32 s70, 0xa0000\n\t" \
  "s_mov_b32 s71, 0xaaa00aa\n\t" \
  "s_mov_b32 s72, 0x150001\n\t" \
  "s_mov_b32 s73, 0x15550155\n\t" \
  "s_mov_b32 s74, 0x2a0002\n\t" \
  "s_mov_b32 s75, 0x2aaa02aa\n\t" \
  "s_mov_b32 s76, 0x55505555\n\t" \
  "s_mov_b32 s77, 0x50005500\n\t" \
  "s_mov_b32 s78, 0xaaa0aaaa\n\t" \
  "s_mov_b32 s79, 0xa000aa00\n\t" \
  "s_mov_b32 s80, 0x55405554\n\t" \
  "s_mov_b32 s81, 0x40005400\n\t" \
  "s_mov_b32 s82, 0xaa80aaa8\n\t" \
  "s_mov_b32 s83, 0x8000a800\n\t" \
  "s_mov_b32 s84, 0xaaaaaaaa\n\t" \
  "s_mov_b32 s85, 0xaaaaaaaa\n\t" \
  "s_waitcnt lgkmcnt(0)\n\t" \
  "s_branch N4a_%=\n\t" \
  "N0a_%=:\n\t" \
  "s_waitcnt lgkmcnt(4)\n\t" \
  "v_mfma_f32_16x16x32_bf16 v[160:163], v[40:43], v[0:3], v[160:163]\n\t" \
  "ds_read_b64_tr_b16 v[52:53], v59 offset:2048\n\t" \
  "v_mfma_f32_16x16x32_bf16 v[96:99], v[40:43], v[4:7], v[96:99]\n\t" \
  "ds_read_b64_tr_b16 v[54:55], v59 offset:2560\n\t" \
  "v_mfma_f32_16x16x32_bf16 v[104:107], v[40:43], v[8:11], v[104:107]\n\t" \
  "s_barrier\n\t" \
  "v_mfma_f32_16x16x32_bf16 v[112:115], v[40:43], v[12:15], v[112:115]\n\t" \
  "v_mfma_f32_16x16x32_bf16 v[120:123], v[40:43], v[16:19], v[120:123]\n\t" \
  "s_waitcnt lgkmcnt(4)\n\t" \
  "v_mfma_f32_16x16x32_bf16 v[128:131], v[4:7], v[44:47], v[128:131]\n\t" \
  "ds_read_b64_tr_b16 v[40:41], v58 offset:0\n\t" \
  "v_mfma_f32_16x16x32_bf16 v[136:139], v[8:11], v[44:47], v[136:139]\n\t" \
  "ds_read_b64_tr_b16 v[42:43], v58 offset:512\n\t" \
  "v_mfma_f32_16x16x32_bf16 v[144:147], v[12:15], v[44:47], v[144:147]\n\t" \
  "ds_read_b128 v[20:23], v57 offset:0\n\t" \
  "v_mfma_f32_16x16x32_bf16 v[152:155], v[16:19], v[44:47], v[152:155]\n\t" \
  "ds_read_b128 v[24:27], v57 offset:2048\n\t" \
  "s_waitcnt lgkmcnt(6)\n\t" \
  "v_mfma_f32_16x16x32_bf16 v[164:167], v[48:51], v[0:3], v[164:167]\n\t" \
  "ds_read_b64_tr_b16 v[44:45], v59 offset:0\n\t" \
  "v_mfma_f32_16x16x32_bf16 v[100:103], v[48:51], v[4:7], v[100:103]\n\t" \
  "ds_read_b64_tr_b16 v[46:47], v59 offset:512\n\t" \
  "v_mfma_f32_16x16x32_bf16 v[108:111], v[48:51], v[8:11], v[108:111]\n\t" \
  "ds_read_b128 v[28:31], v57 offset:4096\n\t" \
  "v_mfma_f32_16x16x32_bf16 v[116:119], v[48:51], v[12:15], v[116:119]\n\t" \
  "ds_read_b128 v[32:35], v57 offset:6144\n\t" \
  "v_mfma_f32_16x16x32_bf16 v[124:127], v[48:51], v[16:19], v[124:127]\n\t" \
  "s_waitcnt lgkmcnt(8)\n\t" \
  "v_mfma_f32_16x16x32_bf16 v[132:135], v[4:7], v[52:55], v[132:135]\n\t" \
  "ds_read_b64_tr_b16 v[48:49], v58 offset:13312\n\t" \
  "v_mfma_f32_16x16x32_bf16 v[140:143], v[8:11], v[52:55], v[140:143]\n\t" \
  "ds_read_b64_tr_b16 v[50:51], v58 offset:13824\n\t" \
  "v_mfma_f32_16x16x32_bf16 v[148:151], v[12:15], v[52:55], v[148:151]\n\t" \
  "ds_read_b128 v[36:39], v57 offset:8192\n\t" \
  "v_mfma_f32_16x16x32_bf16 v[156:159], v[16:19], v[52:55], v[156:159]\n\t" \
  "s_waitcnt lgkmcnt(9)\n\t" \
  "v_mfma_f32_16x16x32_bf16 v[64:67], v[40:43], v[4:7], v[64:67]\n\t" \
  "ds_read_b64_tr_b16 v[52:53], v58 offset:14336\n\t" \
  "v_mfma_f32_16x16x32_bf16 v[72:75], v[40:43], v[8:11], v[72:75]\n\t" \
  "ds_read_b64_tr_b16 v[54:55], v58 offset:14848\n\t" \
  "v_mfma_f32_16x16x32_bf16 v[80:83], v[40:43], v[12:15], v[80:83]\n\t" \
  "v_mfma_f32_16x16x32_bf16 v[88:91], v[40:43], v[16:19], v[88:91]\n\t" \
  "s_waitcnt lgkmcnt(7)\n\t" \
  "v_mfma_f32_16x16x32_bf16 v[68:71], v[44:47], v[4:7], v[68:71]\n\t" \
  "ds_read_b64_tr_b16 v[40:41], v59 offset:13312\n\t" \
  "v_mfma_f32_16x16x32_bf16 v[76:79], v[44:47], v[8:11], v[76:79]\n\t" \
  "ds_read_b64_tr_b16 v[42:43], v59 offset:13824\n\t" \
  "v_mfma_f32_16x16x32_bf16 v[84:87], v[44:47], v[12:15], v[84:87]\n\t" \
  "v_mfma_f32_16x16x32_bf16 v[92:95], v[44:47], v[16:19], v[92:95]\n\t" \
  "N1a_%=:\n\t" \
  "s_waitcnt lgkmcnt(4)\n\t" \
  "v_mfma_f32_16x16x32_bf16 v[160:163], v[48:51], v[20:23], v[160:163]\n\t" \
  "ds_read_b64_tr_b16 v[44:45], v59 offset:14336\n\t" \
  "v_mfma_f32_16x16x32_bf16 v[96:99], v[48:51], v[24:27], v[96:99]\n\t" \
  "ds_read_b64_tr_b16 v[46:47], v59 offset:14848\n\t" \
  "v_mfma_f32_16x16x32_bf16 v[104:107], v[48:51], v[28:31], v[104:107]\n\t" \
  "s_barrier\n\t" \
  "v_mfma_f32_16x16x32_bf16 v[112:115], v[48:51], v[32:35], v[112:115]\n\t" \
  "v_mfma_f32_16x16x32_bf16 v[120:123], v[48:51], v[36:39], v[120:123]\n\t" \
  "s_waitcnt lgkmcnt(4)\n\t" \
  "v_mfma_f32_16x16x32_bf16 v[128:131], v[24:27], v[52:55], v[128:131]\n\t" \
  "ds_read_b64_tr_b16 v[48:49], v58 offset:12288\n\t" \
  "v_mfma_f32_16x16x32_bf16 v[136:139], v[28:31], v[52:55], v[136:139]\n\t" \
  "ds_read_b64_tr_b16 v[50:51], v58 offset:12800\n\t" \
  "v_mfma_f32_16x16x32_bf16 v[144:147], v[32:35], v[52:55], v[144:147]\n\t" \
  "ds_read_b128 v[0:3], v56 offset:18432\n\t" \
  "v_mfma_f32_16x16x32_bf16 v[152:155], v[36:39], v[52:55], v[152:155]\n\t" \
  "ds_read_b128 v[4:7], v56 offset:20480\n\t" \
  "s_waitcnt lgkmcnt(6)\n\t" \
  "v_mfma_f32_16x16x32_bf16 v[164:167], v[40:43], v[20:23], v[164:167]\n\t" \
  "ds_read_b64_tr_b16 v[52:53], v59 offset:12288\n\t" \
  "v_mfma_f32_16x16x32_bf16 v[100:103], v[40:43], v[24:27], v[100:103]\n\t" \
  "ds_read_b64_tr_b16 v[54:55], v59 offset:12800\n\t" \
  "v_mfma_f32_16x16x32_bf16 v[108:111], v[40:43], v[28:31], v[108:111]\n\t" \
  "ds_read_b128 v[8:11], v56 offset:22528\n\t" \
  "v_mfma_f32_16x16x32_bf16 v[116:119], v[40:43], v[32:35], v[116:119]\n\t" \
  "ds_read_b128 v[12:15], v56 offset:24576\n\t" \
  "v_mfma_f32_16x16x32_bf16 v[124:127], v[40:43], v[36:39], v[124:127]\n\t" \
  "s_waitcnt lgkmcnt(8)\n\t" \
  "v_mfma_f32_16x16x32_bf16 v[132:135], v[24:27], v[44:47], v[132:135]\n\t" \
  "ds_read_b64_tr_b16 v[40:41], v58 offset:25600\n\t" \
  "v_mfma_f32_16x16x32_bf16 v[140:143], v[28:31], v[44:47], v[140:143]\n\t" \
  "ds_read_b64_tr_b16 v[42:43], v58 offset:26112\n\t" \
  "v_mfma_f32_16x16x32_bf16 v[148:151], v[32:35], v[44:47], v[148:151]\n\t" \
  "ds_read_b128 v[16:19], v56 offset:26624\n\t" \
  "v_mfma_f32_16x16x32_bf16 v[156:159], v[36:39], v[44:47], v[156:159]\n\t" \
  "s_waitcnt lgkmcnt(9)\n\t" \
  "v_mfma_f32_16x16x32_bf16 v[64:67], v[48:51], v[24:27], v[64:67]\n\t" \
  "ds_read_b64_tr_b16 v[44:45], v58 offset:26624\n\t" \
  "v_mfma_f32_16x16x32_bf16 v[72:75], v[48:51], v[28:31], v[72:75]\n\t" \
  "ds_read_b64_tr_b16 v[46:47], v58 offset:27136\n\t" \
  "v_mfma_f32_16x16x32_bf16 v[80:83], v[48:51], v[32:35], v[80:83]\n\t" \
  "v_mfma_f32_16x16x32_bf16 v[88:91], v[48:51], v[36:39], v[88:91]\n\t" \
  "s_waitcnt lgkmcnt(7)\n\t" \
  "v_mfma_f32_16x16x32_bf16 v[68:71], v[52:55], v[24:27], v[68:71]\n\t" \
  "ds_read_b64_tr_b16 v[48:49], v59 offset:25600\n\t" \
  "v_mfma_f32_16x16x32_bf16 v[76:79], v[52:55], v[28:31], v[76:79]\n\t" \
  "ds_read_b64_tr_b16 v[50:51], v59 offset:26112\n\t" \
  "v_mfma_f32_16x16x32_bf16 v[84:87], v[52:55], v[32:35], v[84:87]\n\t" \
  "v_mfma_f32_16x16x32_bf16 v[92:95], v[52:55], v[36:39], v[92:95]\n\t" \
  "N2a_%=:\n\t" \
  "s_waitcnt lgkmcnt(4)\n\t" \
  "v_mfma_f32_16x16x32_bf16 v[160:163], v[40:43], v[0:3], v[160:163]\n\t" \
  "ds_read_b64_tr_b16 v[52:53], v59 offset:26624\n\t" \
  "v_mfma_f32_16x16x32_bf16 v[96:99], v[40:43], v[4:7], v[96:99]\n\t" \
  "ds_read_b64_tr_b16 v[54:55], v59 offset:27136\n\t" \
  "v_mfma_f32_16x16x32_bf16 v[104:107], v[40:43], v[8:11], v[104:107]\n\t" \
  "s_barrier\n\t" \
  "v_mfma_f32_16x16x32_bf16 v[112:115], v[40:43], v[12:15], v[112:115]\n\t" \
  "v_mfma_f32_16x16x32_bf16 v[120:123], v[40:43], v[16:19], v[120:123]\n\t" \
  "s_waitcnt lgkmcnt(4)\n\t" \
  "v_mfma_f32_16x16x32_bf16 v[128:131], v[4:7], v[44:47], v[128:131]\n\t" \
  "ds_read_b64_tr_b16 v[40:41], v58 offset:24576\n\t" \
  "v_mfma_f32_16x16x32_bf16 v[136:139], v[8:11], v[44:47], v[136:139]\n\t" \
  "ds_read_b64_tr_b16 v[42:43], v58 offset:25088\n\t" \
  "v_mfma_f32_16x16x32_bf16 v[144:147], v[12:15], v[44:47], v[144:147]\n\t" \
  "ds_read_b128 v[20:23], v57 offset:18432\n\t" \
  "v_mfma_f32_16x16x32_bf16 v[152:155], v[16:19], v[44:47], v[152:155]\n\t" \
  "ds_read_b128 v[24:27], v57 offset:20480\n\t" \
  "s_waitcnt lgkmcnt(6)\n\t" \
  "v_mfma_f32_16x16x32_bf16 v[164:167], v[48:51], v[0:3], v[164:167]\n\t" \
  "ds_read_b64_tr_b16 v[44:45], v59 offset:24576\n\t" \
  "v_mfma_f32_16x16x32_bf16 v[100:103], v[48:51], v[4:7], v[100:103]\n\t" \
  "ds_read_b64_tr_b16 v[46:47], v59 offset:25088\n\t" \
  "v_mfma_f32_16x16x32_bf16 v[108:111], v[48:51], v[8:11], v[108:111]\n\t" \
  "ds_read_b128 v[28:31], v57 offset:22528\n\t" \
  "v_mfma_f32_16x16x32_bf16 v[116:119], v[48:51], v[12:15], v[116:119]\n\t" \
  "ds_read_b128 v[32:35], v57 offset:24576\n\t" \
  "v_mfma_f32_16x16x32_bf16 v[124:127], v[48:51], v[16:19], v[124:127]\n\t" \
  "s_waitcnt lgkmcnt(8)\n\t" \
  "v_mfma_f32_16x16x32_bf16 v[132:135], v[4:7], v[52:55], v[132:135]\n\t" \
  "ds_read_b64_tr_b16 v[48:49], v60 offset:1024\n\t" \
  "v_mfma_f32_16x16x32_bf16 v[140:143], v[8:11], v[52:55], v[140:143]\n\t" \
  "ds_read_b64_tr_b16 v[50:51], v60 offset:1536\n\t" \
  "v_mfma_f32_16x16x32_bf16 v[148:151], v[12:15], v[52:55], v[148:151]\n\t" \
  "ds_read_b128 v[36:39], v57 offset:26624\n\t" \
  "v_mfma_f32_16x16x32_bf16 v[156:159], v[16:19], v[52:55], v[156:159]\n\t" \
  "s_waitcnt lgkmcnt(9)\n\t" \
  "v_mfma_f32_16x16x32_bf16 v[64:67], v[40:43], v[4:7], v[64:67]\n\t" \
  "ds_read_b64_tr_b16 v[52:53], v60 offset:2048\n\t" \
  "v_mfma_f32_16x16x32_bf16 v[72:75], v[40:43], v[8:11], v[72:75]\n\t" \
  "ds_read_b64_tr_b16 v[54:55], v60 offset:2560\n\t" \
  "v_mfma_f32_16x16x32_bf16 v[80:83], v[40:43], v[12:15], v[80:83]\n\t" \
  "v_mfma_f32_16x16x32_bf16 v[88:91], v[40:43], v[16:19], v[88:91]\n\t" \
  "s_waitcnt lgkmcnt(7)\n\t" \
  "v_mfma_f32_16x16x32_bf16 v[68:71], v[44:47], v[4:7], v[68:71]\n\t" \
  "ds_read_b64_tr_b16 v[40:41], v61 offset:1024\n\t" \
  "v_mfma_f32_16x16x32_bf16 v[76:79], v[44:47], v[8:11], v[76:79]\n\t" \
  "ds_read_b64_tr_b16 v[42:43], v61 offset:1536\n\t" \
  "v_mfma_f32_16x16x32_bf16 v[84:87], v[44:47], v[12:15], v[84:87]\n\t" \
  "v_mfma_f32_16x16x32_bf16 v[92:95], v[44:47], v[16:19], v[92:95]\n\t" \
  "N3a_%=:\n\t" \
  "s_waitcnt lgkmcnt(4)\n\t" \
  "v_mfma_f32_16x16x32_bf16 v[160:163], v[48:51], v[20:23], v[160:163]\n\t" \
  "ds_read_b64_tr_b16 v[44:45], v61 offset:2048\n\t" \
  "v_mfma_f32_16x16x32_bf16 v[96:99], v[48:51], v[24:27], v[96:99]\n\t" \
  "ds_read_b64_tr_b16 v[46:47], v61 offset:2560\n\t" \
  "v_mfma_f32_16x16x32_bf16 v[104:107], v[48:51], v[28:31], v[104:107]\n\t" \
  "s_barrier\n\t" \
  "v_mfma_f32_16x16x32_bf16 v[112:115], v[48:51], v[32:35], v[112:115]\n\t" \
  "v_mfma_f32_16x16x32_bf16 v[120:123], v[48:51], v[36:39], v[120:123]\n\t" \
  "s_waitcnt lgkmcnt(4)\n\t" \
  "v_mfma_f32_16x16x32_bf16 v[128:131], v[24:27], v[52:55], v[128:131]\n\t" \
  "ds_read_b64_tr_b16 v[48:49], v60 offset:0\n\t" \
  "v_mfma_f32_16x16x32_bf16 v[136:139], v[28:31], v[52:55], v[136:139]\n\t" \
  "ds_read_b64_tr_b16 v[50:51], v60 offset:512\n\t" \
  "v_mfma_f32_16x16x32_bf16 v[144:147], v[32:35], v[52:55], v[144:147]\n\t" \
  "ds_read_b128 v[0:3], v56 offset:36864\n\t" \
  "v_mfma_f32_16x16x32_bf16 v[152:155], v[36:39], v[52:55], v[152:155]\n\t" \
  "ds_read_b128 v[4:7], v56 offset:38912\n\t" \
  "s_waitcnt lgkmcnt(6)\n\t" \
  "v_mfma_f32_16x16x32_bf16 v[164:167], v[40:43], v[20:23], v[164:167]\n\t" \
  "ds_read_b64_tr_b16 v[52:53], v61 offset:0\n\t" \
  "v_mfma_f32_16x16x32_bf16 v[100:103], v[40:43], v[24:27], v[100:103]\n\t" \
  "ds_read_b64_tr_b16 v[54:55], v61 offset:512\n\t" \
  "v_mfma_f32_16x16x32_bf16 v[108:111], v[40:43], v[28:31], v[108:111]\n\t" \
  "ds_read_b128 v[8:11], v56 offset:40960\n\t" \
  "v_mfma_f32_16x16x32_bf16 v[116:119], v[40:43], v[32:35], v[116:119]\n\t" \
  "ds_read_b128 v[12:15], v56 offset:43008\n\t" \
  "v_mfma_f32_16x16x32_bf16 v[124:127], v[40:43], v[36:39], v[124:127]\n\t" \
  "s_waitcnt lgkmcnt(8)\n\t" \
  "v_mfma_f32_16x16x32_bf16 v[132:135], v[24:27], v[44:47], v[132:135]\n\t" \
  "ds_read_b64_tr_b16 v[40:41], v60 offset:13312\n\t" \
  "v_mfma_f32_16x16x32_bf16 v[140:143], v[28:31], v[44:47], v[140:143]\n\t" \
  "ds_read_b64_tr_b16 v[42:43], v60 offset:13824\n\t" \
  "v_mfma_f32_16x16x32_bf16 v[148:151], v[32:35], v[44:47], v[148:151]\n\t" \
  "ds_read_b128 v[16:19], v56 offset:45056\n\t" \
  "v_mfma_f32_16x16x32_bf16 v[156:159], v[36:39], v[44:47], v[156:159]\n\t" \
  "s_waitcnt lgkmcnt(9)\n\t" \
  "v_mfma_f32_16x16x32_bf16 v[64:67], v[48:51], v[24:27], v[64:67]\n\t" \
  "ds_read_b64_tr_b16 v[44:45], v60 offset:14336\n\t" \
  "v_mfma_f32_16x16x32_bf16 v[72:75], v[48:51], v[28:31], v[72:75]\n\t" \
  "ds_read_b64_tr_b16 v[46:47], v60 offset:14848\n\t" \
  "v_mfma_f32_16x16x32_bf16 v[80:83], v[48:51], v[32:35], v[80:83]\n\t" \
  "v_mfma_f32_16x16x32_bf16 v[88:91], v[48:51], v[36:39], v[88:91]\n\t" \
  "s_waitcnt lgkmcnt(7)\n\t" \
  "v_mfma_f32_16x16x32_bf16 v[68:71], v[52:55], v[24:27], v[68:71]\n\t" \
  "ds_read_b64_tr_b16 v[48:49], v61 offset:13312\n\t" \
  "v_mfma_f32_16x16x32_bf16 v[76:79], v[52:55], v[28:31], v[76:79]\n\t" \
  "ds_read_b64_tr_b16 v[50:51], v61 offset:13824\n\t" \
  "v_mfma_f32_16x16x32_bf16 v[84:87], v[52:55], v[32:35], v[84:87]\n\t" \
  "v_mfma_f32_16x16x32_bf16 v[92:95], v[52:55], v[36:39], v[92:95]\n\t" \
  "N4a_%=:\n\t" \
  "s_waitcnt lgkmcnt(4)\n\t" \
  "v_mfma_f32_16x16x32_bf16 v[160:163], v[40:43], v[0:3], v[160:163]\n\t" \
  "ds_read_b64_tr_b16 v[52:53], v61 offset:14336\n\t" \
  "v_mfma_f32_16x16x32_bf16 v[96:99], v[40:43], v[4:7], v[96:99]\n\t" \
  "ds_read_b64_tr_b16 v[54:55], v61 offset:14848\n\t" \
  "v_mfma_f32_16x16x32_bf16 v[104:107], v[40:43], v[8:11], v[104:107]\n\t" \
  "s_barrier\n\t" \
  "v_mfma_f32_16x16x32_bf16 v[112:115], v[40:43], v[12:15], v[112:115]\n\t" \
  "v_mfma_f32_16x16x32_bf16 v[120:123], v[40:43], v[16:19], v[120:123]\n\t" \
  "s_waitcnt lgkmcnt(4)\n\t" \
  "v_mfma_f32_16x16x32_bf16 v[128:131], v[4:7], v[44:47], v[128:131]\n\t" \
  "ds_read_b64_tr_b16 v[40:41], v60 offset:12288\n\t" \
  "v_mfma_f32_16x16x32_bf16 v[136:139], v[8:11], v[44:47], v[136:139]\n\t" \
  "ds_read_b64_tr_b16 v[42:43], v60 offset:12800\n\t" \
  "v_mfma_f32_16x16x32_bf16 v[144:147], v[12:15], v[44:47], v[144:147]\n\t" \
  "ds_read_b128 v[20:23], v57 offset:36864\n\t" \
  "v_mfma_f32_16x16x32_bf16 v[152:155], v[16:19], v[44:47], v[152:155]\n\t" \
  "ds_read_b128 v[24:27], v57 offset:38912\n\t" \
  "s_waitcnt lgkmcnt(6)\n\t" \
  "v_mfma_f32_16x16x32_bf16 v[164:167], v[48:51], v[0:3], v[164:167]\n\t" \
  "ds_read_b64_tr_b16 v[44:45], v61 offset:12288\n\t" \
  "v_mfma_f32_16x16x32_bf16 v[100:103], v[48:51], v[4:7], v[100:103]\n\t" \
  "ds_read_b64_tr_b16 v[46:47], v61 offset:12800\n\t" \
  "v_mfma_f32_16x16x32_bf16 v[108:111], v[48:51], v[8:11], v[108:111]\n\t" \
  "ds_read_b128 v[28:31], v57 offset:40960\n\t" \
  "v_mfma_f32_16x16x32_bf16 v[116:119], v[48:51], v[12:15], v[116:119]\n\t" \
  "ds_read_b128 v[32:35], v57 offset:43008\n\t" \
  "v_mfma_f32_16x16x32_bf16 v[124:127], v[48:51], v[16:19], v[124:127]\n\t" \
  "s_waitcnt lgkmcnt(8)\n\t" \
  "v_mfma_f32_16x16x32_bf16 v[132:135], v[4:7], v[52:55], v[132:135]\n\t" \
  "ds_read_b64_tr_b16 v[48:49], v60 offset:25600\n\t" \
  "v_mfma_f32_16x16x32_bf16 v[140:143], v[8:11], v[52:55], v[140:143]\n\t" \
  "ds_read_b64_tr_b16 v[50:51], v60 offset:26112\n\t" \
  "v_mfma_f32_16x16x32_bf16 v[148:151], v[12:15], v[52:55], v[148:151]\n\t" \
  "ds_read_b128 v[36:39], v57 offset:45056\n\t" \
  "v_mfma_f32_16x16x32_bf16 v[156:159], v[16:19], v[52:55], v[156:159]\n\t" \
  "s_waitcnt lgkmcnt(9)\n\t" \
  "v_mfma_f32_16x16x32_bf16 v[64:67], v[40:43], v[4:7], v[64:67]\n\t" \
  "ds_read_b64_tr_b16 v[52:53], v60 offset:26624\n\t" \
  "v_mfma_f32_16x16x32_bf16 v[72:75], v[40:43], v[8:11], v[72:75]\n\t" \
  "ds_read_b64_tr_b16 v[54:55], v60 offset:27136\n\t" \
  "v_mfma_f32_16x16x32_bf16 v[80:83], v[40:43], v[12:15], v[80:83]\n\t" \
  "v_mfma_f32_16x16x32_bf16 v[88:91], v[40:43], v[16:19], v[88:91]\n\t" \
  "s_waitcnt lgkmcnt(7)\n\t" \
  "v_mfma_f32_16x16x32_bf16 v[68:71], v[44:47], v[4:7], v[68:71]\n\t" \
  "ds_read_b64_tr_b16 v[40:41], v61 offset:25600\n\t" \
  "v_mfma_f32_16x16x32_bf16 v[76:79], v[44:47], v[8:11], v[76:79]\n\t" \
  "ds_read_b64_tr_b16 v[42:43], v61 offset:26112\n\t" \
  "v_mfma_f32_16x16x32_bf16 v[84:87], v[44:47], v[12:15], v[84:87]\n\t" \
  "v_mfma_f32_16x16x32_bf16 v[92:95], v[44:47], v[16:19], v[92:95]\n\t" \
  "N5a_%=:\n\t" \
  "s_waitcnt lgkmcnt(4)\n\t" \
  "v_mfma_f32_16x16x32_bf16 v[160:163], v[48:51], v[20:23], v[160:163]\n\t" \
  "ds_read_b64_tr_b16 v[44:45], v61 offset:26624\n\t" \
  "v_mfma_f32_16x16x32_bf16 v[96:99], v[48:51], v[24:27], v[96:99]\n\t" \
  "ds_read_b64_tr_b16 v[46:47], v61 offset:27136\n\t" \
  "v_mfma_f32_16x16x32_bf16 v[104:107], v[48:51], v[28:31], v[104:107]\n\t" \
  "s_barrier\n\t" \
  "v_mfma_f32_16x16x32_bf16 v[112:115], v[48:51], v[32:35], v[112:115]\n\t" \
  "v_mfma_f32_16x16x32_bf16 v[120:123], v[48:51], v[36:39], v[120:123]\n\t" \
  "s_waitcnt lgkmcnt(4)\n\t" \
  "v_mfma_f32_16x16x32_bf16 v[128:131], v[24:27], v[52:55], v[128:131]\n\t" \
  "ds_read_b64_tr_b16 v[48:49], v60 offset:24576\n\t" \
  "v_mfma_f32_16x16x32_bf16 v[136:139], v[28:31], v[52:55], v[136:139]\n\t" \
  "ds_read_b64_tr_b16 v[50:51], v60 offset:25088\n\t" \
  "v_mfma_f32_16x16x32_bf16 v[144:147], v[32:35], v[52:55], v[144:147]\n\t" \
  "ds_read_b128 v[0:3], v56 offset:0\n\t" \
  "v_mfma_f32_16x16x32_bf16 v[152:155], v[36:39], v[52:55], v[152:155]\n\t" \
  "ds_read_b128 v[4:7], v56 offset:2048\n\t" \
  "s_waitcnt lgkmcnt(6)\n\t" \
  "v_mfma_f32_16x16x32_bf16 v[164:167], v[40:43], v[20:23], v[164:167]\n\t" \
  "ds_read_b64_tr_b16 v[52:53], v61 offset:24576\n\t" \
  "v_mfma_f32_16x16x32_bf16 v[100:103], v[40:43], v[24:27], v[100:103]\n\t" \
  "ds_read_b64_tr_b16 v[54:55], v61 offset:25088\n\t" \
  "v_mfma_f32_16x16x32_bf16 v[108:111], v[40:43], v[28:31], v[108:111]\n\t" \
  "ds_read_b128 v[8:11], v56 offset:4096\n\t" \
  "v_mfma_f32_16x16x32_bf16 v[116:119], v[40:43], v[32:35], v[116:119]\n\t" \
  "ds_read_b128 v[12:15], v56 offset:6144\n\t" \
  "v_mfma_f32_16x16x32_bf16 v[124:127], v[40:43], v[36:39], v[124:127]\n\t" \
  "s_waitcnt lgkmcnt(8)\n\t" \
  "v_mfma_f32_16x16x32_bf16 v[132:135], v[24:27], v[44:47], v[132:135]\n\t" \
  "ds_read_b64_tr_b16 v[40:41], v58 offset:1024\n\t" \
  "v_mfma_f32_16x16x32_bf16 v[140:143], v[28:31], v[44:47], v[140:143]\n\t" \
  "ds_read_b64_tr_b16 v[42:43], v58 offset:1536\n\t" \
  "v_mfma_f32_16x16x32_bf16 v[148:151], v[32:35], v[44:47], v[148:151]\n\t" \
  "ds_read_b128 v[16:19], v56 offset:8192\n\t" \
  "v_mfma_f32_16x16x32_bf16 v[156:159], v[36:39], v[44:47], v[156:159]\n\t" \
  "s_waitcnt lgkmcnt(9)\n\t" \
  "v_mfma_f32_16x16x32_bf16 v[64:67], v[48:51], v[24:27], v[64:67]\n\t" \
  "ds_read_b64_tr_b16 v[44:45], v58 offset:2048\n\t" \
  "v_mfma_f32_16x16x32_bf16 v[72:75], v[48:51], v[28:31], v[72:75]\n\t" \
  "ds_read_b64_tr_b16 v[46:47], v58 offset:2560\n\t" \
  "v_mfma_f32_16x16x32_bf16 v[80:83], v[48:51], v[32:35], v[80:83]\n\t" \
  "v_mfma_f32_16x16x32_bf16 v[88:91], v[48:51], v[36:39], v[88:91]\n\t" \
  "s_waitcnt lgkmcnt(7)\n\t" \
  "v_mfma_f32_16x16x32_bf16 v[68:71], v[52:55], v[24:27], v[68:71]\n\t" \
  "ds_read_b64_tr_b16 v[48:49], v59 offset:1024\n\t" \
  "v_mfma_f32_16x16x32_bf16 v[76:79], v[52:55], v[28:31], v[76:79]\n\t" \
  "ds_read_b64_tr_b16 v[50:51], v59 offset:1536\n\t" \
  "s_addk_i32 %[it], 1\n\t" \
  "v_mfma_f32_16x16x32_bf16 v[84:87], v[52:55], v[32:35], v[84:87]\n\t" \
  "s_cmp_lt_u32 %[it], 6\n\t" \
  "v_mfma_f32_16x16x32_bf16 v[92:95], v[52:55], v[36:39], v[92:95]\n\t" \
  "s_cbranch_scc1 N0a_%=\n\t" \
  "s_setprio 3\n\t" \
  "v_cvt_pk_bf16_f32 v120, v120, v121\n\t" \
  "v_cvt_pk_bf16_f32 v121, v122, v123\n\t" \
  "v_cvt_pk_bf16_f32 v122, v124, v125\n\t" \
  "v_cvt_pk_bf16_f32 v123, v126, v127\n\t" \
  "v_cvt_pk_bf16_f32 v28, v152, v153\n\t" \
  "v_cvt_pk_bf16_f32 v29, v154, v155\n\t" \
  "v_cvt_pk_bf16_f32 v38, v156, v157\n\t" \
  "v_cvt_pk_bf16_f32 v39, v158, v159\n\t" \
  "ds_write_b128 v63, v[120:123]\n\t" \
  "ds_write2_b64 v63, v[28:29], v[38:39] offset0:128 offset1:129\n\t" \
  "v_mov_b32 v57, %[fa]\n\t" \
  "v_mov_b32 v58, 1\n\t" \
  "s_mov_b64 exec, 1\n\t" \
  "ds_write_b32 v57, v58\n\t" \
  "s_mov_b64 exec, -1\n\t" \
  "v_cvt_pk_bf16_f32 v160, v160, v161\n\t" \
  "v_cvt_pk_bf16_f32 v161, v162, v163\n\t" \
  "v_cvt_pk_bf16_f32 v162, v164, v165\n\t" \
  "v_cvt_pk_bf16_f32 v163, v166, v167\n\t" \
  "s_branch NX_%=\n\t" \
  "NB_%=:\n\t" \
  "ds_read_b64_tr_b16 v[40:41], v60 offset:14336\n\t" \
  "ds_read_b64_tr_b16 v[42:43], v60 offset:14848\n\t" \
  "ds_read_b64_tr_b16 v[44:45], v60 offset:13312\n\t" \
  "ds_read_b64_tr_b16 v[46:47], v60 offset:13824\n\t" \
  "ds_read_b64_tr_b16 v[48:49], v61 offset:14336\n\t" \
  "ds_read_b64_tr_b16 v[50:51], v61 offset:14848\n\t" \
  "ds_read_b128 v[0:3], v56 offset:36864\n\t" \
  "ds_read_b128 v[4:7], v56 offset:47104\n\t" \
  "ds_read_b128 v[8:11], v56 offset:49152\n\t" \
  "ds_read_b128 v[12:15], v56 offset:51200\n\t" \
  "ds_read_b128 v[16:19], v56 offset:53248\n\t" \
  "s_mov_b32 s68, 0x50000\n\t" \
  "s_mov_b32 s69, 0x5550055\n\t" \
  "s_mov_b32 s70, 0xa0000\n\t" \
  "s_mov_b32 s71, 0xaaa00aa\n\t" \
  "s_mov_b32 s72, 0x150001\n\t" \
  "s_mov_b32 s73, 0x15550155\n\t" \
  "s_mov_b32 s74, 0x2a0002\n\t" \
  "s_mov_b32 s75, 0x2aaa02aa\n\t" \
  "s_mov_b32 s76, 0x55505555\n\t" \
  "s_mov_b32 s77, 0x50005500\n\t" \
  "s_mov_b32 s78, 0xaaa0aaaa\n\t" \
  "s_mov_b32 s79, 0xa000aa00\n\t" \
  "s_mov_b32 s80, 0x55405554\n\t" \
  "s_mov_b32 s81, 0x40005400\n\t" \
  "s_mov_b32 s82, 0xaa80aaa8\n\t" \
  "s_mov_b32 s83, 0x8000a800\n\t" \
  "s_mov_b32 s84, 0xaaaaaaaa\n\t" \
  "s_mov_b32 s85, 0xaaaaaaaa\n\t" \
  "s_waitcnt lgkmcnt(0)\n\t" \
  "s_branch N4b_%=\n\t" \
  "N0b_%=:\n\t" \
  "s_waitcnt lgkmcnt(4)\n\t" \
  "v_mfma_f32_16x16x32_bf16 v[160:163], v[0:3], v[40:43], v[160:163]\n\t" \
  "ds_read_b64_tr_b16 v[52:53], v59 offset:1024\n\t" \
  "v_mfma_f32_16x16x32_bf16 v[128:131], v[4:7], v[40:43], v[128:131]\n\t" \
  "ds_read_b64_tr_b16 v[54:55], v59 offset:1536\n\t" \
  "v_mfma_f32_16x16x32_bf16 v[136:139], v[8:11], v[40:43], v[136:139]\n\t" \
  "s_barrier\n\t" \
  "v_mfma_f32_16x16x32_bf16 v[144:147], v[12:15], v[40:43], v[144:147]\n\t" \
  "v_mfma_f32_16x16x32_bf16 v[152:155], v[16:19], v[40:43], v[152:155]\n\t" \
  "s_waitcnt lgkmcnt(4)\n\t" \
  "v_mfma_f32_16x16x32_bf16 v[96:99], v[44:47], v[4:7], v[96:99]\n\t" \
  "ds_read_b64_tr_b16 v[40:41], v58 offset:0\n\t" \
  "v_mfma_f32_16x16x32_bf16 v[104:107], v[44:47], v[8:11], v[104:107]\n\t" \
  "ds_read_b64_tr_b16 v[42:43], v58 offset:512\n\t" \
  "v_mfma_f32_16x16x32_bf16 v[112:115], v[44:47], v[12:15], v[112:115]\n\t" \
  "ds_read_b128 v[20:23], v57 offset:0\n\t" \
  "v_mfma_f32_16x16x32_bf16 v[120:123], v[44:47], v[16:19], v[120:123]\n\t" \
  "ds_read_b128 v[24:27], v57 offset:10240\n\t" \
  "s_waitcnt lgkmcnt(6)\n\t" \
  "v_mfma_f32_16x16x32_bf16 v[164:167], v[0:3], v[48:51], v[164:167]\n\t" \
  "ds_read_b64_tr_b16 v[44:45], v59 offset:0\n\t" \
  "v_mfma_f32_16x16x32_bf16 v[132:135], v[4:7], v[48:51], v[132:135]\n\t" \
  "ds_read_b64_tr_b16 v[46:47], v59 offset:512\n\t" \
  "v_mfma_f32_16x16x32_bf16 v[140:143], v[8:11], v[48:51], v[140:143]\n\t" \
  "ds_read_b128 v[28:31], v57 offset:12288\n\t" \
  "v_mfma_f32_16x16x32_bf16 v[148:151], v[12:15], v[48:51], v[148:151]\n\t" \
  "ds_read_b128 v[32:35], v57 offset:14336\n\t" \
  "v_mfma_f32_16x16x32_bf16 v[156:159], v[16:19], v[48:51], v[156:159]\n\t" \
  "s_waitcnt lgkmcnt(8)\n\t" \
  "v_mfma_f32_16x16x32_bf16 v[100:103], v[52:55], v[4:7], v[100:103]\n\t" \
  "ds_read_b64_tr_b16 v[48:49], v58 offset:14336\n\t" \
  "v_mfma_f32_16x16x32_bf16 v[108:111], v[52:55], v[8:11], v[108:111]\n\t" \
  "ds_read_b64_tr_b16 v[50:51], v58 offset:14848\n\t" \
  "v_mfma_f32_16x16x32_bf16 v[116:119], v[52:55], v[12:15], v[116:119]\n\t" \
  "ds_read_b128 v[36:39], v57 offset:16384\n\t" \
  "v_mfma_f32_16x16x32_bf16 v[124:127], v[52:55], v[16:19], v[124:127]\n\t" \
  "s_waitcnt lgkmcnt(9)\n\t" \
  "v_mfma_f32_16x16x32_bf16 v[64:67], v[40:43], v[4:7], v[64:67]\n\t" \
  "ds_read_b64_tr_b16 v[52:53], v58 offset:13312\n\t" \
  "v_mfma_f32_16x16x32_bf16 v[72:75], v[40:43], v[8:11], v[72:75]\n\t" \
  "ds_read_b64_tr_b16 v[54:55], v58 offset:13824\n\t" \
  "v_mfma_f32_16x16x32_bf16 v[80:83], v[40:43], v[12:15], v[80:83]\n\t" \
  "v_mfma_f32_16x16x32_bf16 v[88:91], v[40:43], v[16:19], v[88:91]\n\t" \
  "s_waitcnt lgkmcnt(7)\n\t" \
  "v_mfma_f32_16x16x32_bf16 v[68:71], v[44:47], v[4:7], v[68:71]\n\t" \
  "ds_read_b64_tr_b16 v[40:41], v59 offset:14336\n\t" \
  "v_mfma_f32_16x16x32_bf16 v[76:79], v[44:47], v[8:11], v[76:79]\n\t" \
  "ds_read_b64_tr_b16 v[42:43], v59 offset:14848\n\t" \
  "v_mfma_f32_16x16x32_bf16 v[84:87], v[44:47], v[12:15], v[84:87]\n\t" \
  "v_mfma_f32_16x16x32_bf16 v[92:95], v[44:47], v[16:19], v[92:95]\n\t" \
  "N1b_%=:\n\t" \
  "s_waitcnt lgkmcnt(4)\n\t" \
  "v_mfma_f32_16x16x32_bf16 v[160:163], v[20:23], v[48:51], v[160:163]\n\t" \
  "ds_read_b64_tr_b16 v[44:45], v59 offset:13312\n\t" \
  "v_mfma_f32_16x16x32_bf16 v[128:131], v[24:27], v[48:51], v[128:131]\n\t" \
  "ds_read_b64_tr_b16 v[46:47], v59 offset:13824\n\t" \
  "v_mfma_f32_16x16x32_bf16 v[136:139], v[28:31], v[48:51], v[136:139]\n\t" \
  "s_barrier\n\t" \
  "v_mfma_f32_16x16x32_bf16 v[144:147], v[32:35], v[48:51], v[144:147]\n\t" \
  "v_mfma_f32_16x16x32_bf16 v[152:155], v[36:39], v[48:51], v[152:155]\n\t" \
  "s_waitcnt lgkmcnt(4)\n\t" \
  "v_mfma_f32_16x16x32_bf16 v[96:99], v[52:55], v[24:27], v[96:99]\n\t" \
  "ds_read_b64_tr_b16 v[48:49], v58 offset:12288\n\t" \
  "v_mfma_f32_16x16x32_bf16 v[104:107], v[52:55], v[28:31], v[104:107]\n\t" \
  "ds_read_b64_tr_b16 v[50:51], v58 offset:12800\n\t" \
  "v_mfma_f32_16x16x32_bf16 v[112:115], v[52:55], v[32:35], v[112:115]\n\t" \
  "ds_read_b128 v[0:3], v56 offset:18432\n\t" \
  "v_mfma_f32_16x16x32_bf16 v[120:123], v[52:55], v[36:39], v[120:123]\n\t" \
  "ds_read_b128 v[4:7], v56 offset:28672\n\t" \
  "s_waitcnt lgkmcnt(6)\n\t" \
  "v_mfma_f32_16x16x32_bf16 v[164:167], v[20:23], v[40:43], v[164:167]\n\t" \
  "ds_read_b64_tr_b16 v[52:53], v59 offset:12288\n\t" \
  "v_mfma_f32_16x16x32_bf16 v[132:135], v[24:27], v[40:43], v[132:135]\n\t" \
  "ds_read_b64_tr_b16 v[54:55], v59 offset:12800\n\t" \
  "v_mfma_f32_16x16x32_bf16 v[140:143], v[28:31], v[40:43], v[140:143]\n\t" \
  "ds_read_b128 v[8:11], v56 offset:30720\n\t" \
  "v_mfma_f32_16x16x32_bf16 v[148:151], v[32:35], v[40:43], v[148:151]\n\t" \
  "ds_read_b128 v[12:15], v56 offset:32768\n\t" \
  "v_mfma_f32_16x16x32_bf16 v[156:159], v[36:39], v[40:43], v[156:159]\n\t" \
  "s_waitcnt lgkmcnt(8)\n\t" \
  "v_mfma_f32_16x16x32_bf16 v[100:103], v[44:47], v[24:27], v[100:103]\n\t" \
  "ds_read_b64_tr_b16 v[40:41], v58 offset:26624\n\t" \
  "v_mfma_f32_16x16x32_bf16 v[108:111], v[44:47], v[28:31], v[108:111]\n\t" \
  "ds_read_b64_tr_b16 v[42:43], v58 offset:27136\n\t" \
  "v_mfma_f32_16x16x32_bf16 v[116:119], v[44:47], v[32:35], v[116:119]\n\t" \
  "ds_read_b128 v[16:19], v56 offset:34816\n\t" \
  "v_mfma_f32_16x16x32_bf16 v[124:127], v[44:47], v[36:39], v[124:127]\n\t" \
  "s_waitcnt lgkmcnt(9)\n\t" \
  "v_mfma_f32_16x16x32_bf16 v[64:67], v[48:51], v[24:27], v[64:67]\n\t" \
  "ds_read_b64_tr_b16 v[44:45], v58 offset:25600\n\t" \
  "v_mfma_f32_16x16x32_bf16 v[72:75], v[48:51], v[28:31], v[72:75]\n\t" \
  "ds_read_b64_tr_b16 v[46:47], v58 offset:26112\n\t" \
  "v_mfma_f32_16x16x32_bf16 v[80:83], v[48:51], v[32:35], v[80:83]\n\t" \
  "v_mfma_f32_16x16x32_bf16 v[88:91], v[48:51], v[36:39], v[88:91]\n\t" \
  "s_waitcnt lgkmcnt(7)\n\t" \
  "v_mfma_f32_16x16x32_bf16 v[68:71], v[52:55], v[24:27], v[68:71]\n\t" \
  "ds_read_b64_tr_b16 v[48:49], v59 offset:26624\n\t" \
  "v_mfma_f32_16x16x32_bf16 v[76:79], v[52:55], v[28:31], v[76:79]\n\t" \
  "ds_read_b64_tr_b16 v[50:51], v59 offset:27136\n\t" \
  "v_mfma_f32_16x16x32_bf16 v[84:87], v[52:55], v[32:35], v[84:87]\n\t" \
  "v_mfma_f32_16x16x32_bf16 v[92:95], v[52:55], v[36:39], v[92:95]\n\t" \
  "N2b_%=:\n\t" \
  "s_waitcnt lgkmcnt(4)\n\t" \
  "v_mfma_f32_16x16x32_bf16 v[160:163], v[0:3], v[40:43], v[160:163]\n\t" \
  "ds_read_b64_tr_b16 v[52:53], v59 offset:25600\n\t" \
  "v_mfma_f32_16x16x32_bf16 v[128:131], v[4:7], v[40:43], v[128:131]\n\t" \
  "ds_read_b64_tr_b16 v[54:55], v59 offset:26112\n\t" \
  "v_mfma_f32_16x16x32_bf16 v[136:139], v[8:11], v[40:43], v[136:139]\n\t" \
  "s_barrier\n\t" \
  "v_mfma_f32_16x16x32_bf16 v[144:147], v[12:15], v[40:43], v[144:147]\n\t" \
  "v_mfma_f32_16x16x32_bf16 v[152:155], v[16:19], v[40:43], v[152:155]\n\t" \
  "s_waitcnt lgkmcnt(4)\n\t" \
  "v_mfma_f32_16x16x32_bf16 v[96:99], v[44:47], v[4:7], v[96:99]\n\t" \
  "ds_read_b64_tr_b16 v[40:41], v58 offset:24576\n\t" \
  "v_mfma_f32_16x16x32_bf16 v[104:107], v[44:47], v[8:11], v[104:107]\n\t" \
  "ds_read_b64_tr_b16 v[42:43], v58 offset:25088\n\t" \
  "v_mfma_f32_16x16x32_bf16 v[112:115], v[44:47], v[12:15], v[112:115]\n\t" \
  "ds_read_b128 v[20:23], v57 offset:18432\n\t" \
  "v_mfma_f32_16x16x32_bf16 v[120:123], v[44:47], v[16:19], v[120:123]\n\t" \
  "ds_read_b128 v[24:27], v57 offset:28672\n\t" \
  "s_waitcnt lgkmcnt(6)\n\t" \
  "v_mfma_f32_16x16x32_bf16 v[164:167], v[0:3], v[48:51], v[164:167]\n\t" \
  "ds_read_b64_tr_b16 v[44:45], v59 offset:24576\n\t" \
  "v_mfma_f32_16x16x32_bf16 v[132:135], v[4:7], v[48:51], v[132:135]\n\t" \
  "ds_read_b64_tr_b16 v[46:47], v59 offset:25088\n\t" \
  "v_mfma_f32_16x16x32_bf16 v[140:143], v[8:11], v[48:51], v[140:143]\n\t" \
  "ds_read_b128 v[28:31], v57 offset:30720\n\t" \
  "v_mfma_f32_16x16x32_bf16 v[148:151], v[12:15], v[48:51], v[148:151]\n\t" \
  "ds_read_b128 v[32:35], v57 offset:32768\n\t" \
  "v_mfma_f32_16x16x32_bf16 v[156:159], v[16:19], v[48:51], v[156:159]\n\t" \
  "s_waitcnt lgkmcnt(8)\n\t" \
  "v_mfma_f32_16x16x32_bf16 v[100:103], v[52:55], v[4:7], v[100:103]\n\t" \
  "ds_read_b64_tr_b16 v[48:49], v60 offset:2048\n\t" \
  "v_mfma_f32_16x16x32_bf16 v[108:111], v[52:55], v[8:11], v[108:111]\n\t" \
  "ds_read_b64_tr_b16 v[50:51], v60 offset:2560\n\t" \
  "v_mfma_f32_16x16x32_bf16 v[116:119], v[52:55], v[12:15], v[116:119]\n\t" \
  "ds_read_b128 v[36:39], v57 offset:34816\n\t" \
  "v_mfma_f32_16x16x32_bf16 v[124:127], v[52:55], v[16:19], v[124:127]\n\t" \
  "s_waitcnt lgkmcnt(9)\n\t" \
  "v_mfma_f32_16x16x32_bf16 v[64:67], v[40:43], v[4:7], v[64:67]\n\t" \
  "ds_read_b64_tr_b16 v[52:53], v60 offset:1024\n\t" \
  "v_mfma_f32_16x16x32_bf16 v[72:75], v[40:43], v[8:11], v[72:75]\n\t" \
  "ds_read_b64_tr_b16 v[54:55], v60 offset:1536\n\t" \
  "v_mfma_f32_16x16x32_bf16 v[80:83], v[40:43], v[12:15], v[80:83]\n\t" \
  "v_mfma_f32_16x16x32_bf16 v[88:91], v[40:43], v[16:19], v[88:91]\n\t" \
  "s_waitcnt lgkmcnt(7)\n\t" \
  "v_mfma_f32_16x16x32_bf16 v[68:71], v[44:47], v[4:7], v[68:71]\n\t" \
  "ds_read_b64_tr_b16 v[40:41], v61 offset:2048\n\t" \
  "v_mfma_f32_16x16x32_bf16 v[76:79], v[44:47], v[8:11], v[76:79]\n\t" \
  "ds_read_b64_tr_b16 v[42:43], v61 offset:2560\n\t" \
  "v_mfma_f32_16x16x32_bf16 v[84:87], v[44:47], v[12:15], v[84:87]\n\t" \
  "v_mfma_f32_16x16x32_bf16 v[92:95], v[44:47], v[16:19], v[92:95]\n\t" \
  "N3b_%=:\n\t" \
  "s_waitcnt lgkmcnt(4)\n\t" \
  "v_mfma_f32_16x16x32_bf16 v[160:163], v[20:23], v[48:51], v[160:163]\n\t" \
  "ds_read_b64_tr_b16 v[44:45], v61 offset:1024\n\t" \
  "v_mfma_f32_16x16x32_bf16 v[128:131], v[24:27], v[48:51], v[128:131]\n\t" \
  "ds_read_b64_tr_b16 v[46:47], v61 offset:1536\n\t" \
  "v_mfma_f32_16x16x32_bf16 v[136:139], v[28:31], v[48:51], v[136:139]\n\t" \
  "s_barrier\n\t" \
  "v_mfma_f32_16x16x32_bf16 v[144:147], v[32:35], v[48:51], v[144:147]\n\t" \
  "v_mfma_f32_16x16x32_bf16 v[152:155], v[36:39], v[48:51], v[152:155]\n\t" \
  "s_waitcnt lgkmcnt(4)\n\t" \
  "v_mfma_f32_16x16x32_bf16 v[96:99], v[52:55], v[24:27], v[96:99]\n\t" \
  "ds_read_b64_tr_b16 v[48:49], v60 offset:0\n\t" \
  "v_mfma_f32_16x16x32_bf16 v[104:107], v[52:55], v[28:31], v[104:107]\n\t" \
  "ds_read_b64_tr_b16 v[50:51], v60 offset:512\n\t" \
  "v_mfma_f32_16x16x32_bf16 v[112:115], v[52:55], v[32:35], v[112:115]\n\t" \
  "ds_read_b128 v[0:3], v56 offset:36864\n\t" \
  "v_mfma_f32_16x16x32_bf16 v[120:123], v[52:55], v[36:39], v[120:123]\n\t" \
  "ds_read_b128 v[4:7], v56 offset:47104\n\t" \
  "s_waitcnt lgkmcnt(6)\n\t" \
  "v_mfma_f32_16x16x32_bf16 v[164:167], v[20:23], v[40:43], v[164:167]\n\t" \
  "ds_read_b64_tr_b16 v[52:53], v61 offset:0\n\t" \
  "v_mfma_f32_16x16x32_bf16 v[132:135], v[24:27], v[40:43], v[132:135]\n\t" \
  "ds_read_b64_tr_b16 v[54:55], v61 offset:512\n\t" \
  "v_mfma_f32_16x16x32_bf16 v[140:143], v[28:31], v[40:43], v[140:143]\n\t" \
  "ds_read_b128 v[8:11], v56 offset:49152\n\t" \
  "v_mfma_f32_16x16x32_bf16 v[148:151], v[32:35], v[40:43], v[148:151]\n\t" \
  "ds_read_b128 v[12:15], v56 offset:51200\n\t" \
  "v_mfma_f32_16x16x32_bf16 v[156:159], v[36:39], v[40:43], v[156:159]\n\t" \
  "s_waitcnt lgkmcnt(8)\n\t" \
  "v_mfma_f32_16x16x32_bf16 v[100:103], v[44:47], v[24:27], v[100:103]\n\t" \
  "ds_read_b64_tr_b16 v[40:41], v60 offset:14336\n\t" \
  "v_mfma_f32_16x16x32_bf16 v[108:111], v[44:47], v[28:31], v[108:111]\n\t" \
  "ds_read_b64_tr_b16 v[42:43], v60 offset:14848\n\t" \
  "v_mfma_f32_16x16x32_bf16 v[116:119], v[44:47], v[32:35], v[116:119]\n\t" \
  "ds_read_b128 v[16:19], v56 offset:53248\n\t" \
  "v_mfma_f32_16x16x32_bf16 v[124:127], v[44:47], v[36:39], v[124:127]\n\t" \
  "s_waitcnt lgkmcnt(9)\n\t" \
  "v_mfma_f32_16x16x32_bf16 v[64:67], v[48:51], v[24:27], v[64:67]\n\t" \
  "ds_read_b64_tr_b16 v[44:45], v60 offset:13312\n\t" \
  "v_mfma_f32_16x16x32_bf16 v[72:75], v[48:51], v[28:31], v[72:75]\n\t" \
  "ds_read_b64_tr_b16 v[46:47], v60 offset:13824\n\t" \
  "v_mfma_f32_16x16x32_bf16 v[80:83], v[48:51], v[32:35], v[80:83]\n\t" \
  "v_mfma_f32_16x16x32_bf16 v[88:91], v[48:51], v[36:39], v[88:91]\n\t" \
  "s_waitcnt lgkmcnt(7)\n\t" \
  "v_mfma_f32_16x16x32_bf16 v[68:71], v[52:55], v[24:27], v[68:71]\n\t" \
  "ds_read_b64_tr_b16 v[48:49], v61 offset:14336\n\t" \
  "v_mfma_f32_16x16x32_bf16 v[76:79], v[52:55], v[28:31], v[76:79]\n\t" \
  "ds_read_b64_tr_b16 v[50:51], v61 offset:14848\n\t" \
  "v_mfma_f32_16x16x32_bf16 v[84:87], v[52:55], v[32:35], v[84:87]\n\t" \
  "v_mfma_f32_16x16x32_bf16 v[92:95], v[52:55], v[36:39], v[92:95]\n\t" \
  "N4b_%=:\n\t" \
  "s_waitcnt lgkmcnt(4)\n\t" \
  "v_mfma_f32_16x16x32_bf16 v[160:163], v[0:3], v[40:43], v[160:163]\n\t" \
  "ds_read_b64_tr_b16 v[52:53], v61 offset:13312\n\t" \
  "v_mfma_f32_16x16x32_bf16 v[128:131], v[4:7], v[40:43], v[128:131]\n\t" \
  "ds_read_b64_tr_b16 v[54:55], v61 offset:13824\n\t" \
  "v_mfma_f32_16x16x32_bf16 v[136:139], v[8:11], v[40:43], v[136:139]\n\t" \
  "s_barrier\n\t" \
  "v_mfma_f32_16x16x32_bf16 v[144:147], v[12:15], v[40:43], v[144:147]\n\t" \
  "v_mfma_f32_16x16x32_bf16 v[152:155], v[16:19], v[40:43], v[152:155]\n\t" \
  "s_waitcnt lgkmcnt(4)\n\t" \
  "v_mfma_f32_16x16x32_bf16 v[96:99], v[44:47], v[4:7], v[96:99]\n\t" \
  "ds_read_b64_tr_b16 v[40:41], v60 offset:12288\n\t" \
  "v_mfma_f32_16x16x32_bf16 v[104:107], v[44:47], v[8:11], v[104:107]\n\t" \
  "ds_read_b64_tr_b16 v[42:43], v60 offset:12800\n\t" \
  "v_mfma_f32_16x16x32_bf16 v[112:115], v[44:47], v[12:15], v[112:115]\n\t" \
  "ds_read_b128 v[20:23], v57 offset:36864\n\t" \
  "v_mfma_f32_16x16x32_bf16 v[120:123], v[44:47], v[16:19], v[120:123]\n\t" \
  "ds_read_b128 v[24:27], v57 offset:47104\n\t" \
  "s_waitcnt lgkmcnt(6)\n\t" \
  "v_mfma_f32_16x16x32_bf16 v[164:167], v[0:3], v[48:51], v[164:167]\n\t" \
  "ds_read_b64_tr_b16 v[44:45], v61 offset:12288\n\t" \
  "v_mfma_f32_16x16x32_bf16 v[132:135], v[4:7], v[48:51], v[132:135]\n\t" \
  "ds_read_b64_tr_b16 v[46:47], v61 offset:12800\n\t" \
  "v_mfma_f32_16x16x32_bf16 v[140:143], v[8:11], v[48:51], v[140:143]\n\t" \
  "ds_read_b128 v[28:31], v57 offset:49152\n\t" \
  "v_mfma_f32_16x16x32_bf16 v[148:151], v[12:15], v[48:51], v[148:151]\n\t" \
  "ds_read_b128 v[32:35], v57 offset:51200\n\t" \
  "v_mfma_f32_16x16x32_bf16 v[156:159], v[16:19], v[48:51], v[156:159]\n\t" \
  "s_waitcnt lgkmcnt(8)\n\t" \
  "v_mfma_f32_16x16x32_bf16 v[100:103], v[52:55], v[4:7], v[100:103]\n\t" \
  "ds_read_b64_tr_b16 v[48:49], v60 offset:26624\n\t" \
  "v_mfma_f32_16x16x32_bf16 v[108:111], v[52:55], v[8:11], v[108:111]\n\t" \
  "ds_read_b64_tr_b16 v[50:51], v60 offset:27136\n\t" \
  "v_mfma_f32_16x16x32_bf16 v[116:119], v[52:55], v[12:15], v[116:119]\n\t" \
  "ds_read_b128 v[36:39], v57 offset:53248\n\t" \
  "v_mfma_f32_16x16x32_bf16 v[124:127], v[52:55], v[16:19], v[124:127]\n\t" \
  "s_waitcnt lgkmcnt(9)\n\t" \
  "v_mfma_f32_16x16x32_bf16 v[64:67], v[40:43], v[4:7], v[64:67]\n\t" \
  "ds_read_b64_tr_b16 v[52:53], v60 offset:25600\n\t" \
  "v_mfma_f32_16x16x32_bf16 v[72:75], v[40:43], v[8:11], v[72:75]\n\t" \
  "ds_read_b64_tr_b16 v[54:55], v60 offset:26112\n\t" \
  "v_mfma_f32_16x16x32_bf16 v[80:83], v[40:43], v[12:15], v[80:83]\n\t" \
  "v_mfma_f32_16x16x32_bf16 v[88:91], v[40:43], v[16:19], v[88:91]\n\t" \
  "s_waitcnt lgkmcnt(7)\n\t" \
  "v_mfma_f32_16x16x32_bf16 v[68:71], v[44:47], v[4:7], v[68:71]\n\t" \
  "ds_read_b64_tr_b16 v[40:41], v61 offset:26624\n\t" \
  "v_mfma_f32_16x16x32_bf16 v[76:79], v[44:47], v[8:11], v[76:79]\n\t" \
  "ds_read_b64_tr_b16 v[42:43], v61 offset:27136\n\t" \
  "v_mfma_f32_16x16x32_bf16 v[84:87], v[44:47], v[12:15], v[84:87]\n\t" \
  "v_mfma_f32_16x16x32_bf16 v[92:95], v[44:47], v[16:19], v[92:95]\n\t" \
  "N5b_%=:\n\t" \
  "s_waitcnt lgkmcnt(4)\n\t" \
  "v_mfma_f32_16x16x32_bf16 v[160:163], v[20:23], v[48:51], v[160:163]\n\t" \
  "ds_read_b64_tr_b16 v[44:45], v61 offset:25600\n\t" \
  "v_mfma_f32_16x16x32_bf16 v[128:131], v[24:27], v[48:51], v[128:131]\n\t" \
  "ds_read_b64_tr_b16 v[46:47], v61 offset:26112\n\t" \
  "v_mfma_f32_16x16x32_bf16 v[136:139], v[28:31], v[48:51], v[136:139]\n\t" \
  "s_barrier\n\t" \
  "v_mfma_f32_16x16x32_bf16 v[144:147], v[32:35], v[48:51], v[144:147]\n\t" \
  "v_mfma_f32_16x16x32_bf16 v[152:155], v[36:39], v[48:51], v[152:155]\n\t" \
  "s_waitcnt lgkmcnt(4)\n\t" \
  "v_mfma_f32_16x16x32_bf16 v[96:99], v[52:55], v[24:27], v[96:99]\n\t" \
  "ds_read_b64_tr_b16 v[48:49], v60 offset:24576\n\t" \
  "v_mfma_f32_16x16x32_bf16 v[104:107], v[52:55], v[28:31], v[104:107]\n\t" \
  "ds_read_b64_tr_b16 v[50:51], v60 offset:25088\n\t" \
  "v_mfma_f32_16x16x32_bf16 v[112:115], v[52:55], v[32:35], v[112:115]\n\t" \
  "ds_read_b128 v[0:3], v56 offset:0\n\t" \
  "v_mfma_f32_16x16x32_bf16 v[120:123], v[52:55], v[36:39], v[120:123]\n\t" \
  "ds_read_b128 v[4:7], v56 offset:10240\n\t" \
  "s_waitcnt lgkmcnt(6)\n\t" \
  "v_mfma_f32_16x16x32_bf16 v[164:167], v[20:23], v[40:43], v[164:167]\n\t" \
  "ds_read_b64_tr_b16 v[52:53], v61 offset:24576\n\t" \
  "v_mfma_f32_16x16x32_bf16 v[132:135], v[24:27], v[40:43], v[132:135]\n\t" \
  "ds_read_b64_tr_b16 v[54:55], v61 offset:25088\n\t" \
  "v_mfma_f32_16x16x32_bf16 v[140:143], v[28:31], v[40:43], v[140:143]\n\t" \
  "ds_read_b128 v[8:11], v56 offset:12288\n\t" \
  "v_mfma_f32_16x16x32_bf16 v[148:151], v[32:35], v[40:43], v[148:151]\n\t" \
  "ds_read_b128 v[12:15], v56 offset:14336\n\t" \
  "v_mfma_f32_16x16x32_bf16 v[156:159], v[36:39], v[40:43], v[156:159]\n\t" \
  "s_waitcnt lgkmcnt(8)\n\t" \
  "v_mfma_f32_16x16x32_bf16 v[100:103], v[44:47], v[24:27], v[100:103]\n\t" \
  "ds_read_b64_tr_b16 v[40:41], v58 offset:2048\n\t" \
  "v_mfma_f32_16x16x32_bf16 v[108:111], v[44:47], v[28:31], v[108:111]\n\t" \
  "ds_read_b64_tr_b16 v[42:43], v58 offset:2560\n\t" \
  "v_mfma_f32_16x16x32_bf16 v[116:119], v[44:47], v[32:35], v[116:119]\n\t" \
  "ds_read_b128 v[16:19], v56 offset:16384\n\t" \
  "v_mfma_f32_16x16x32_bf16 v[124:127], v[44:47], v[36:39], v[124:127]\n\t" \
  "s_waitcnt lgkmcnt(9)\n\t" \
  "v_mfma_f32_16x16x32_bf16 v[64:67], v[48:51], v[24:27], v[64:67]\n\t" \
  "ds_read_b64_tr_b16 v[44:45], v58 offset:1024\n\t" \
  "v_mfma_f32_16x16x32_bf16 v[72:75], v[48:51], v[28:31], v[72:75]\n\t" \
  "ds_read_b64_tr_b16 v[46:47], v58 offset:1536\n\t" \
  "v_mfma_f32_16x16x32_bf16 v[80:83], v[48:51], v[32:35], v[80:83]\n\t" \
  "v_mfma_f32_16x16x32_bf16 v[88:91], v[48:51], v[36:39], v[88:91]\n\t" \
  "s_waitcnt lgkmcnt(7)\n\t" \
  "v_mfma_f32_16x16x32_bf16 v[68:71], v[52:55], v[24:27], v[68:71]\n\t" \
  "ds_read_b64_tr_b16 v[48:49], v59 offset:2048\n\t" \
  "v_mfma_f32_16x16x32_bf16 v[76:79], v[52:55], v[28:31], v[76:79]\n\t" \
  "ds_read_b64_tr_b16 v[50:51], v59 offset:2560\n\t" \
  "s_addk_i32 %[it], 1\n\t" \
  "v_mfma_f32_16x16x32_bf16 v[84:87], v[52:55], v[32:35], v[84:87]\n\t" \
  "s_cmp_lt_u32 %[it], 6\n\t" \
  "v_mfma_f32_16x16x32_bf16 v[92:95], v[52:55], v[36:39], v[92:95]\n\t" \
  "s_cbranch_scc1 N0b_%=\n\t" \
  "s_setprio 3\n\t" \
  "v_cvt_pk_bf16_f32 v160, v160, v161\n\t" \
  "v_cvt_pk_bf16_f32 v161, v162, v163\n\t" \
  "v_cvt_pk_bf16_f32 v162, v164, v165\n\t" \
  "v_cvt_pk_bf16_f32 v163, v166, v167\n\t" \
  "ds_write_b128 v63, v[160:163] offset:2048\n\t" \
  "v_mov_b32 v57, %[fa]\n\t" \
  "v_mov_b32 v58, 1\n\t" \
  "s_mov_b64 exec, 1\n\t" \
  "ds_write_b32 v57, v58\n\t" \
  "s_mov_b64 exec, -1\n\t" \
  "v_cvt_pk_bf16_f32 v120, v120, v121\n\t" \
  "v_cvt_pk_bf16_f32 v121, v122, v123\n\t" \
  "v_cvt_pk_bf16_f32 v122, v124, v125\n\t" \
  "v_cvt_pk_bf16_f32 v123, v126, v127\n\t" \
  "v_cvt_pk_bf16_f32 v28, v152, v153\n\t" \
  "v_cvt_pk_bf16_f32 v29, v154, v155\n\t" \
  "v_cvt_pk_bf16_f32 v38, v156, v157\n\t" \
  "v_cvt_pk_bf16_f32 v39, v158, v159\n\t" \
  "NX_%=:\n\t" \
  "v_cvt_pk_bf16_f32 v96, v96, v97\n\t" \
  "v_cvt_pk_bf16_f32 v97, v98, v99\n\t" \
  "v_cvt_pk_bf16_f32 v98, v100, v101\n\t" \
  "v_cvt_pk_bf16_f32 v99, v102, v103\n\t" \
  "v_cvt_pk_bf16_f32 v104, v104, v105\n\t" \
  "v_cvt_pk_bf16_f32 v105, v106, v107\n\t" \
  "v_cvt_pk_bf16_f32 v106, v108, v109\n\t" \
  "v_cvt_pk_bf16_f32 v107, v110, v111\n\t" \
  "v_mov_b32 v60, 0xff61b1e6\n\t" \
  "v_cndmask_b32_e64 v56, v60, 0, s[68:69]\n\t" \
  "v_cndmask_b32_e64 v57, v60, 0, s[70:71]\n\t" \
  "v_cndmask_b32_e64 v58, v60, 0, s[72:73]\n\t" \
  "v_cndmask_b32_e64 v59, v60, 0, s[74:75]\n\t" \
  "v_cvt_pk_bf16_f32 v72, v72, v73\n\t" \
  "v_cvt_pk_bf16_f32 v73, v74, v75\n\t" \
  "v_cvt_pk_bf16_f32 v74, v76, v77\n\t" \
  "v_cvt_pk_bf16_f32 v75, v78, v79\n\t" \
  "v_cndmask_b32_e64 v52, v60, 0, s[76:77]\n\t" \
  "v_cndmask_b32_e64 v53, v60, 0, s[78:79]\n\t" \
  "v_cndmask_b32_e64 v54, v60, 0, s[80:81]\n\t" \
  "v_cndmask_b32_e64 v55, v60, 0, s[82:83]\n\t" \
  "v_cvt_pk_bf16_f32 v24, v136, v137\n\t" \
  "v_cvt_pk_bf16_f32 v25, v138, v139\n\t" \
  "v_cvt_pk_bf16_f32 v34, v140, v141\n\t" \
  "v_cvt_pk_bf16_f32 v35, v142, v143\n\t" \
  "v_cvt_pk_bf16_f32 v26, v144, v145\n\t" \
  "v_cvt_pk_bf16_f32 v27, v146, v147\n\t" \
  "v_cvt_pk_bf16_f32 v36, v148, v149\n\t" \
  "v_cvt_pk_bf16_f32 v37, v150, v151\n\t" \
  "v_mfma_f32_16x16x32_bf16 v[136:139], v[96:99], v[72:75], v[56:59]\n\t" \
  "v_cvt_pk_bf16_f32 v112, v112, v113\n\t" \
  "v_cvt_pk_bf16_f32 v113, v114, v115\n\t" \
  "v_mfma_f32_16x16x32_bf16 v[140:143], v[104:107], v[72:75], v[52:55]\n\t" \
  "v_cvt_pk_bf16_f32 v114, v116, v117\n\t" \
  "v_cvt_pk_bf16_f32 v115, v118, v119\n\t" \
  "v_cvt_pk_bf16_f32 v80, v80, v81\n\t" \
  "v_cvt_pk_bf16_f32 v81, v82, v83\n\t" \
  "v_cvt_pk_bf16_f32 v82, v84, v85\n\t" \
  "v_cvt_pk_bf16_f32 v83, v86, v87\n\t" \
  "v_cvt_pk_bf16_f32 v22, v128, v129\n\t" \
  "v_cvt_pk_bf16_f32 v23, v130, v131\n\t" \
  "v_mfma_f32_16x16x32_bf16 v[144:147], v[104:107], v[80:83], v[56:59]\n\t" \
  "v_cvt_pk_bf16_f32 v32, v132, v133\n\t" \
  "v_cvt_pk_bf16_f32 v33, v134, v135\n\t" \
  "v_mfma_f32_16x16x32_bf16 v[148:151], v[112:115], v[80:83], v[52:55]\n\t" \
  "v_cvt_pk_bf16_f32 v88, v88, v89\n\t" \
  "v_cvt_pk_bf16_f32 v89, v90, v91\n\t" \
  "v_cvt_pk_bf16_f32 v90, v92, v93\n\t" \
  "v_cvt_pk_bf16_f32 v91, v94, v95\n\t" \
  "v_mov_b32 v61, %[fp]\n\t" \
  "v_add_u32 v60, %[vo], v63\n\t" \
  "v_mfma_f32_16x16x32_bf16 v[152:155], v[112:115], v[88:91], v[56:59]\n\t" \
  "ds_read_b32 v0, v61\n\t" \
  "ds_read_b64 v[20:21], v60\n\t" \
  "ds_read_b64 v[30:31], v60 offset:8\n\t" \
  "s_mov_b64 exec, %[rm]\n\t" \
  "ds_read_b128 v[160:163], v63\n\t" \
  "s_mov_b64 exec, -1\n\t" \
  "v_mfma_f32_16x16x32_bf16 v[156:159], v[120:123], v[88:91], v[52:55]\n\t" \
  "v_cvt_pk_bf16_f32 v64, v64, v65\n\t" \
  "v_cvt_pk_bf16_f32 v65, v66, v67\n\t" \
  "v_cvt_pk_bf16_f32 v66, v68, v69\n\t" \
  "v_cvt_pk_bf16_f32 v67, v70, v71\n\t" \
  "v_max_f32 v136, v136, v137\n\t" \
  "v_max_f32 v138, v138, v139\n\t" \
  "v_mfma_f32_16x16x32_bf16 v[132:135], v[96:99], v[64:67], v[52:55]\n\t" \
  "v_max_f32 v140, v140, v141\n\t" \
  "v_max_f32 v142, v142, v143\n\t" \
  "AW_%=:\n\t" \
  "s_waitcnt lgkmcnt(0)\n\t" \
  "v_cmp_eq_u32 vcc, 0, v0\n\t" \
  "v_max3_f32 v5, v136, v138, v140\n\t" \
  "v_max_f32 v5, v5, v142\n\t" \
  "s_cbranch_vccnz AR_%=\n\t" \
  "s_cmp_eq_u32 %[z], 0\n\t" \
  "s_cbranch_scc1 AZ_%=\n\t" \
  "AY_%=:\n\t" \
  "v_mfma_f32_16x16x32_bf16 v[128:131], v[160:163], v[64:67], v[56:59]\n\t" \
  "v_max_f32 v144, v144, v145\n\t" \
  "v_max_f32 v146, v146, v147\n\t" \
  "v_max_f32 v148, v148, v149\n\t" \
  "v_max_f32 v150, v150, v151\n\t" \
  "v_max3_f32 v6, v144, v146, v148\n\t" \
  "v_max_f32 v6, v6, v150\n\t" \
  "v_max_f32 v152, v152, v153\n\t" \
  "v_max_f32 v154, v154, v155\n\t" \
  "v_max_f32 v156, v156, v157\n\t" \
  "v_max_f32 v158, v158, v159\n\t" \
  "v_max3_f32 v7, v152, v154, v156\n\t" \
  "v_max_f32 v7, v7, v158\n\t" \
  "v_mov_b32 v8, 0xc0c0100\n\t" \
  "v_mov_b32 v9, 0x1000c0c\n\t" \
  "v_max_f32 v128, v128, v129\n\t" \
  "v_max_f32 v130, v130, v131\n\t" \
  "v_max_f32 v132, v132, v133\n\t" \
  "v_max_f32 v134, v134, v135\n\t" \
  "v_max3_f32 v4, v128, v130, v132\n\t" \
  "v_max_f32 v4, v4, v134\n\t" \
  "v_cndmask_b32_e64 v60, v8, v9, s[84:85]\n\t" \
  "v_mov_b32 v8, 0xc0c0302\n\t" \
  "v_permlane16_swap_b32 v6, v7\n\t" \
  "v_permlane16_swap_b32 v4, v5\n\t" \
  "v_max_f32 v6, v6, v7\n\t" \
  "v_max_f32 v4, v4, v5\n\t" \
  "v_mov_b32 v9, 0x3020c0c\n\t" \
  "v_cndmask_b32_e64 v61, v8, v9, s[84:85]\n\t" \
  "v_permlane32_swap_b32 v4, v6\n\t" \
  "v_max_f32 v4, v4, v6\n\t" \
  "v_mul_f32 v6, 0xbe8293ee, v4\n\t" \
  "v_mul_f32 v4, 0xbe8293ee, v4\n\t" \
  "s_nop 1\n\t" \
  "v_permlane32_swap_b32 v4, v6\n\t" \
  "v_mov_b32 v5, v4\n\t" \
  "v_mov_b32 v7, v6\n\t" \
  "s_nop 0\n\t" \
  "v_permlane16_swap_b32 v4, v5\n\t" \
  "v_permlane16_swap_b32 v6, v7\n\t" \
  "v_fmamk_f32 v128, v128, 0x3e8293ee, v4\n\t" \
  "v_fmamk_f32 v130, v130, 0x3e8293ee, v4\n\t" \
  "v_fmamk_f32 v132, v132, 0x3e8293ee, v4\n\t" \
  "v_fmamk_f32 v134, v134, 0x3e8293ee, v4\n\t" \
  "v_fmamk_f32 v136, v136, 0x3e8293ee, v5\n\t" \
  "v_fmamk_f32 v138, v138, 0x3e8293ee, v5\n\t" \
  "v_fmamk_f32 v140, v140, 0x3e8293ee, v5\n\t" \
  "v_fmamk_f32 v142, v142, 0x3e8293ee, v5\n\t" \
  "v_fmamk_f32 v144, v144, 0x3e8293ee, v6\n\t" \
  "v_fmamk_f32 v146, v146, 0x3e8293ee, v6\n\t" \
  "v_fmamk_f32 v148, v148, 0x3e8293ee, v6\n\t" \
  "v_fmamk_f32 v150, v150, 0x3e8293ee, v6\n\t" \
  "v_fmamk_f32 v152, v152, 0x3e8293ee, v7\n\t" \
  "v_fmamk_f32 v154, v154, 0x3e8293ee, v7\n\t" \
  "v_fmamk_f32 v156, v156, 0x3e8293ee, v7\n\t" \
  "v_fmamk_f32 v158, v158, 0x3e8293ee, v7\n\t" \
  "v_exp_f32 v128, v128\n\t" \
  "v_exp_f32 v130, v130\n\t" \
  "v_exp_f32 v132, v132\n\t" \
  "v_exp_f32 v134, v134\n\t" \
  "v_exp_f32 v136, v136\n\t" \
  "v_exp_f32 v138, v138\n\t" \
  "v_exp_f32 v140, v140\n\t" \
  "v_exp_f32 v142, v142\n\t" \
  "v_exp_f32 v144, v144\n\t" \
  "v_exp_f32 v146, v146\n\t" \
  "v_exp_f32 v148, v148\n\t" \
  "v_exp_f32 v150, v150\n\t" \
  "v_exp_f32 v152, v152\n\t" \
  "v_exp_f32 v154, v154\n\t" \
  "v_exp_f32 v156, v156\n\t" \
  "v_exp_f32 v158, v158\n\t" \
  "v_add_f32 v4, v128, v130\n\t" \
  "v_add_f32 v8, v132, v134\n\t" \
  "v_add_f32 v5, v136, v138\n\t" \
  "v_add_f32 v9, v140, v142\n\t" \
  "v_add_f32 v6, v144, v146\n\t" \
  "v_add_f32 v10, v148, v150\n\t" \
  "v_add_f32 v7, v152, v154\n\t" \
  "v_add_f32 v11, v156, v158\n\t" \
  "v_add_f32 v4, v4, v8\n\t" \
  "v_add_f32 v5, v5, v9\n\t" \
  "v_add_f32 v6, v6, v10\n\t" \
  "v_add_f32 v7, v7, v11\n\t" \
  "v_permlane16_swap_b32 v4, v5\n\t" \
  "s_nop 0\n\t" \
  "v_permlane16_swap_b32 v6, v7\n\t" \
  "v_add_f32 v4, v4, v5\n\t" \
  "v_add_f32 v6, v6, v7\n\t" \
  "s_nop 1\n\t" \
  "v_permlane32_swap_b32 v4, v6\n\t" \
  "v_add_f32 v4, v4, v6\n\t" \
  "v_rcp_f32 v4, v4\n\t" \
  "s_nop 0\n\t" \
  "v_mov_b32 v6, v4\n\t" \
  "s_nop 1\n\t" \
  "v_permlane32_swap_b32 v4, v6\n\t" \
  "v_mov_b32 v5, v4\n\t" \
  "v_mov_b32 v7, v6\n\t" \
  "s_nop 0\n\t" \
  "v_permlane16_swap_b32 v4, v5\n\t" \
  "v_permlane16_swap_b32 v6, v7\n\t" \
  "v_mul_f32 v128, v128, v4\n\t" \
  "v_mul_f32 v130, v130, v4\n\t" \
  "v_mul_f32 v132, v132, v4\n\t" \
  "v_mul_f32 v134, v134, v4\n\t" \
  "v_cvt_pk_bf16_f32 v128, v128, v130\n\t" \
  "v_cvt_pk_bf16_f32 v132, v132, v134\n\t" \
  "v_perm_b32 v68, 0, v128, v60\n\t" \
  "v_perm_b32 v69, 0, v128, v61\n\t" \
  "v_perm_b32 v70, 0, v132, v60\n\t" \
  "v_perm_b32 v71, 0, v132, v61\n\t" \
  "v_mul_f32 v136, v136, v5\n\t" \
  "v_mul_f32 v138, v138, v5\n\t" \
  "v_mfma_f32_16x16x32_bf16 v[128:131], v[20:23], v[68:71], 0\n\t" \
  "v_mul_f32 v140, v140, v5\n\t" \
  "v_mul_f32 v142, v142, v5\n\t" \
  "v_cvt_pk_bf16_f32 v136, v136, v138\n\t" \
  "v_mfma_f32_16x16x32_bf16 v[132:135], v[30:33], v[68:71], 0\n\t" \
  "v_cvt_pk_bf16_f32 v140, v140, v142\n\t" \
  "v_perm_b32 v76, 0, v136, v60\n\t" \
  "v_perm_b32 v77, 0, v136, v61\n\t" \
  "v_perm_b32 v78, 0, v140, v60\n\t" \
  "v_perm_b32 v79, 0, v140, v61\n\t" \
  "v_mul_f32 v144, v144, v6\n\t" \
  "v_mul_f32 v146, v146, v6\n\t" \
  "v_mfma_f32_16x16x32_bf16 v[136:139], v[22:25], v[76:79], 0\n\t" \
  "v_mul_f32 v148, v148, v6\n\t" \
  "v_mul_f32 v150, v150, v6\n\t" \
  "v_cvt_pk_bf16_f32 v144, v144, v146\n\t" \
  "v_mfma_f32_16x16x32_bf16 v[140:143], v[32:35], v[76:79], 0\n\t" \
  "v_cvt_pk_bf16_f32 v148, v148, v150\n\t" \
  "v_perm_b32 v84, 0, v144, v60\n\t" \
  "v_perm_b32 v85, 0, v144, v61\n\t" \
  "v_perm_b32 v86, 0, v148, v60\n\t" \
  "v_perm_b32 v87, 0, v148, v61\n\t" \
  "v_cvt_pk_bf16_f32 v100, v128, v129\n\t" \
  "v_cvt_pk_bf16_f32 v101, v130, v131\n\t" \
  "v_mfma_f32_16x16x32_bf16 v[144:147], v[24:27], v[84:87], 0\n\t" \
  "v_cvt_pk_bf16_f32 v102, v132, v133\n\t" \
  "v_cvt_pk_bf16_f32 v103, v134, v135\n\t" \
  "global_store_dwordx4 v62, v[100:103], %[ot] offset:0 sc1\n\t" \
  "v_mfma_f32_16x16x32_bf16 v[148:151], v[34:37], v[84:87], 0\n\t" \
  "v_mul_f32 v152, v152, v7\n\t" \
  "v_mul_f32 v154, v154, v7\n\t" \
  "v_mul_f32 v156, v156, v7\n\t" \
  "v_mul_f32 v158, v158, v7\n\t" \
  "v_cvt_pk_bf16_f32 v152, v152, v154\n\t" \
  "v_cvt_pk_bf16_f32 v156, v156, v158\n\t" \
  "v_perm_b32 v92, 0, v152, v60\n\t" \
  "v_perm_b32 v93, 0, v152, v61\n\t" \
  "v_perm_b32 v94, 0, v156, v60\n\t" \
  "v_perm_b32 v95, 0, v156, v61\n\t" \
  "v_cvt_pk_bf16_f32 v108, v136, v137\n\t" \
  "v_cvt_pk_bf16_f32 v109, v138, v139\n\t" \
  "v_mfma_f32_16x16x32_bf16 v[152:155], v[26:29], v[92:95], 0\n\t" \
  "v_cvt_pk_bf16_f32 v110, v140, v141\n\t" \
  "v_cvt_pk_bf16_f32 v111, v142, v143\n\t" \
  "global_store_dwordx4 v62, v[108:111], %[ot] offset:1024 sc1\n\t" \
  "v_mfma_f32_16x16x32_bf16 v[156:159], v[36:39], v[92:95], 0\n\t" \
  "v_cvt_pk_bf16_f32 v116, v144, v145\n\t" \
  "v_cvt_pk_bf16_f32 v117, v146, v147\n\t" \
  "v_cvt_pk_bf16_f32 v118, v148, v149\n\t" \
  "v_cvt_pk_bf16_f32 v119, v150, v151\n\t" \
  "global_store_dwordx4 v62, v[116:119], %[ou] offset:0 sc1\n\t" \
  "v_cvt_pk_bf16_f32 v124, v152, v153\n\t" \
  "v_cvt_pk_bf16_f32 v125, v154, v155\n\t" \
  "s_nop 0\n\t" \
  "v_cvt_pk_bf16_f32 v126, v156, v157\n\t" \
  "v_cvt_pk_bf16_f32 v127, v158, v159\n\t" \
  "global_store_dwordx4 v62, v[124:127], %[ou] offset:1024 sc1\n\t" \
  "s_branch AE_%=\n\t" \
  "AR_%=:\n\t" \
  "s_addk_i32 %[it], 1\n\t" \
  "s_cmp_gt_u32 %[it], 0x400000\n\t" \
  "s_cbranch_scc1 AY_%=\n\t" \
  "ds_read_b32 v0, v61\n\t" \
  "ds_read_b64 v[20:21], v60\n\t" \
  "ds_read_b64 v[30:31], v60 offset:8\n\t" \
  "s_mov_b64 exec, %[rm]\n\t" \
  "ds_read_b128 v[160:163], v63\n\t" \
  "s_mov_b64 exec, -1\n\t" \
  "s_branch AW_%=\n\t" \
  "AZ_%=:\n\t" \
  "v_mov_b32 v0, 0xff61b1e6\n\t" \
  "v_mov_b32 v1, 0xcfa8965f\n\t" \
  "s_nop 3\n\t" \
  "v_cndmask_b32_e64 v56, v0, v1, s[68:69]\n\t" \
  "v_cndmask_b32_e64 v57, v0, v1, s[70:71]\n\t" \
  "v_cndmask_b32_e64 v58, v0, v1, s[72:73]\n\t" \
  "v_cndmask_b32_e64 v59, v0, v1, s[74:75]\n\t" \
  "s_nop 1\n\t" \
  "s_branch AY_%=\n\t" \
  "AE_%=:\n\t" \
  "s_nop 1\n\t"
#define N2_CLOB "v0", "v1", "v2", "v3", "v4", "v5", "v6", "v7", "v8", "v9", "v10", "v11", "v12", "v13", "v14", "v15", "v16", "v17", "v18", "v19", "v20", "v21", "v22", "v23", "v24", "v25", "v26", "v27", "v28", "v29", "v30", "v31", "v32", "v33", "v34", "v35", "v36", "v37", "v38", "v39", "v40", "v41", "v42", "v43", "v44", "v45", "v46", "v47", "v48", "v49", "v50", "v51", "v52", "v53", "v54", "v55", "v60", "v61", "v64", "v65", "v66", "v67", "v68", "v69", "v70", "v71", "v72", "v73", "v74", "v75", "v76", "v77", "v78", "v79", "v80", "v81", "v82", "v83", "v84", "v85", "v86", "v87", "v88", "v89", "v90", "v91", "v92", "v93", "v94", "v95", "v96", "v97", "v98", "v99", "v100", "v101", "v102", "v103", "v104", "v105", "v106", "v107", "v108", "v109", "v110", "v111", "v112", "v113", "v114", "v115", "v116", "v117", "v118", "v119", "v120", "v121", "v122", "v123", "v124", "v125", "v126", "v127", "v128", "v129", "v130", "v131", "v132", "v133", "v134", "v135", "v136", "v137", "v138", "v139", "v140", "v141", "v142", "v143", "v144", "v145", "v146", "v147", "v148", "v149", "v150", "v151", "v152", "v153", "v154", "v155", "v156", "v157", "v158", "v159", "v160", "v161", "v162", "v163", "v164", "v165", "v166", "v167", "s68", "s69", "s70", "s71", "s72", "s73", "s74", "s75", "s76", "s77", "s78", "s79", "s80", "s81", "s82", "s83", "s84", "s85"

#define CV_ASM \
  "s_mov_b64 vcc, 1\n\t" \
  "ds_read_b64 v[0:1], v28 offset:0\n\t" \
  "ds_read_b64 v[2:3], v29 offset:0\n\t" \
  "ds_read_b64 v[4:5], v30 offset:1024\n\t" \
  "ds_read_b64 v[6:7], v31 offset:1024\n\t" \
  "ds_read_b64 v[8:9], v24 offset:2048\n\t" \
  "ds_read_b64 v[10:11], v25 offset:2048\n\t" \
  "ds_read_b64 v[12:13], v26 offset:3072\n\t" \
  "ds_read_b64 v[14:15], v27 offset:3072\n\t" \
  "ds_read_b64 v[16:17], v28 offset:4096\n\t" \
  "ds_read_b64 v[18:19], v29 offset:4096\n\t" \
  "ds_read_b64 v[20:21], v30 offset:5120\n\t" \
  "ds_read_b64 v[22:23], v31 offset:5120\n\t" \
  "s_waitcnt lgkmcnt(0)\n\t" \
  "v_mfma_f32_16x16x32_bf16 v[128:131], v[68:71], v[0:3], 0\n\t" \
  "v_mfma_f32_16x16x32_bf16 v[128:131], v[64:67], v[4:7], v[128:131]\n\t" \
  "v_mfma_f32_16x16x32_bf16 v[132:135], v[76:79], v[0:3], 0\n\t" \
  "v_mfma_f32_16x16x32_bf16 v[132:135], v[72:75], v[4:7], v[132:135]\n\t" \
  "v_mfma_f32_16x16x32_bf16 v[136:139], v[84:87], v[0:3], 0\n\t" \
  "v_mfma_f32_16x16x32_bf16 v[136:139], v[80:83], v[4:7], v[136:139]\n\t" \
  "v_mfma_f32_16x16x32_bf16 v[140:143], v[92:95], v[0:3], 0\n\t" \
  "v_mfma_f32_16x16x32_bf16 v[140:143], v[88:91], v[4:7], v[140:143]\n\t" \
  "v_mfma_f32_16x16x32_bf16 v[144:147], v[100:103], v[0:3], 0\n\t" \
  "v_mfma_f32_16x16x32_bf16 v[144:147], v[96:99], v[4:7], v[144:147]\n\t" \
  "ds_bpermute_b32 v34, v32, v131\n\t" \
  "v_mfma_f32_16x16x32_bf16 v[148:151], v[108:111], v[0:3], 0\n\t" \
  "v_mov_b32 v131, v130\n\t" \
  "v_mov_b32 v130, v129\n\t" \
  "v_mfma_f32_16x16x32_bf16 v[148:151], v[104:107], v[4:7], v[148:151]\n\t" \
  "v_mov_b32 v129, v128\n\t" \
  "v_mfma_f32_16x16x32_bf16 v[152:155], v[116:119], v[0:3], 0\n\t" \
  "v_mfma_f32_16x16x32_bf16 v[152:155], v[112:115], v[4:7], v[152:155]\n\t" \
  "v_mfma_f32_16x16x32_bf16 v[156:159], v[124:127], v[0:3], 0\n\t" \
  "ds_read_b64 v[0:1], v24 offset:6144\n\t" \
  "ds_read_b64 v[2:3], v25 offset:6144\n\t" \
  "v_mfma_f32_16x16x32_bf16 v[156:159], v[120:123], v[4:7], v[156:159]\n\t" \
  "ds_read_b64 v[4:5], v26 offset:7168\n\t" \
  "ds_read_b64 v[6:7], v27 offset:7168\n\t" \
  "v_mfma_f32_16x16x32_bf16 v[132:135], v[68:71], v[8:11], v[132:135]\n\t" \
  "v_mfma_f32_16x16x32_bf16 v[132:135], v[64:67], v[12:15], v[132:135]\n\t" \
  "v_mfma_f32_16x16x32_bf16 v[136:139], v[76:79], v[8:11], v[136:139]\n\t" \
  "v_mfma_f32_16x16x32_bf16 v[136:139], v[72:75], v[12:15], v[136:139]\n\t" \
  "v_mfma_f32_16x16x32_bf16 v[140:143], v[84:87], v[8:11], v[140:143]\n\t" \
  "s_waitcnt lgkmcnt(4)\n\t" \
  "v_cndmask_b32_e64 v128, v34, 0, vcc\n\t" \
  "v_mfma_f32_16x16x32_bf16 v[140:143], v[80:83], v[12:15], v[140:143]\n\t" \
  "v_mfma_f32_16x16x32_bf16 v[144:147], v[92:95], v[8:11], v[144:147]\n\t" \
  "v_mfma_f32_16x16x32_bf16 v[144:147], v[88:91], v[12:15], v[144:147]\n\t" \
  "ds_bpermute_b32 v35, v32, v135\n\t" \
  "v_mfma_f32_16x16x32_bf16 v[148:151], v[100:103], v[8:11], v[148:151]\n\t" \
  "v_mov_b32 v135, v134\n\t" \
  "v_mov_b32 v134, v133\n\t" \
  "v_mfma_f32_16x16x32_bf16 v[148:151], v[96:99], v[12:15], v[148:151]\n\t" \
  "v_mov_b32 v133, v132\n\t" \
  "v_mfma_f32_16x16x32_bf16 v[152:155], v[108:111], v[8:11], v[152:155]\n\t" \
  "v_mfma_f32_16x16x32_bf16 v[152:155], v[104:107], v[12:15], v[152:155]\n\t" \
  "v_mfma_f32_16x16x32_bf16 v[156:159], v[116:119], v[8:11], v[156:159]\n\t" \
  "ds_read_b64 v[8:9], v28 offset:8192\n\t" \
  "ds_read_b64 v[10:11], v29 offset:8192\n\t" \
  "v_mfma_f32_16x16x32_bf16 v[156:159], v[112:115], v[12:15], v[156:159]\n\t" \
  "ds_read_b64 v[12:13], v30 offset:9216\n\t" \
  "ds_read_b64 v[14:15], v31 offset:9216\n\t" \
  "v_mfma_f32_16x16x32_bf16 v[136:139], v[68:71], v[16:19], v[136:139]\n\t" \
  "v_mfma_f32_16x16x32_bf16 v[136:139], v[64:67], v[20:23], v[136:139]\n\t" \
  "v_mfma_f32_16x16x32_bf16 v[140:143], v[76:79], v[16:19], v[140:143]\n\t" \
  "v_mfma_f32_16x16x32_bf16 v[140:143], v[72:75], v[20:23], v[140:143]\n\t" \
  "v_mfma_f32_16x16x32_bf16 v[144:147], v[84:87], v[16:19], v[144:147]\n\t" \
  "s_waitcnt lgkmcnt(4)\n\t" \
  "v_cndmask_b32_e64 v132, v35, v34, vcc\n\t" \
  "v_mfma_f32_16x16x32_bf16 v[144:147], v[80:83], v[20:23], v[144:147]\n\t" \
  "v_mfma_f32_16x16x32_bf16 v[148:151], v[92:95], v[16:19], v[148:151]\n\t" \
  "v_mfma_f32_16x16x32_bf16 v[148:151], v[88:91], v[20:23], v[148:151]\n\t" \
  "ds_bpermute_b32 v36, v32, v139\n\t" \
  "v_mfma_f32_16x16x32_bf16 v[152:155], v[100:103], v[16:19], v[152:155]\n\t" \
  "v_mov_b32 v139, v138\n\t" \
  "v_mov_b32 v138, v137\n\t" \
  "v_mfma_f32_16x16x32_bf16 v[152:155], v[96:99], v[20:23], v[152:155]\n\t" \
  "v_mov_b32 v137, v136\n\t" \
  "v_mfma_f32_16x16x32_bf16 v[156:159], v[108:111], v[16:19], v[156:159]\n\t" \
  "ds_read_b64 v[16:17], v24 offset:10240\n\t" \
  "ds_read_b64 v[18:19], v25 offset:10240\n\t" \
  "v_mfma_f32_16x16x32_bf16 v[156:159], v[104:107], v[20:23], v[156:159]\n\t" \
  "ds_read_b64 v[20:21], v26 offset:11264\n\t" \
  "ds_read_b64 v[22:23], v27 offset:11264\n\t" \
  "v_mfma_f32_16x16x32_bf16 v[140:143], v[68:71], v[0:3], v[140:143]\n\t" \
  "v_mfma_f32_16x16x32_bf16 v[140:143], v[64:67], v[4:7], v[140:143]\n\t" \
  "v_mfma_f32_16x16x32_bf16 v[144:147], v[76:79], v[0:3], v[144:147]\n\t" \
  "v_mfma_f32_16x16x32_bf16 v[144:147], v[72:75], v[4:7], v[144:147]\n\t" \
  "v_mfma_f32_16x16x32_bf16 v[148:151], v[84:87], v[0:3], v[148:151]\n\t" \
  "v_mfma_f32_16x16x32_bf16 v[148:151], v[80:83], v[4:7], v[148:151]\n\t" \
  "v_mfma_f32_16x16x32_bf16 v[152:155], v[92:95], v[0:3], v[152:155]\n\t" \
  "s_waitcnt lgkmcnt(4)\n\t" \
  "v_cndmask_b32_e64 v136, v36, v35, vcc\n\t" \
  "v_mfma_f32_16x16x32_bf16 v[152:155], v[88:91], v[4:7], v[152:155]\n\t" \
  "ds_bpermute_b32 v37, v32, v143\n\t" \
  "v_mfma_f32_16x16x32_bf16 v[156:159], v[100:103], v[0:3], v[156:159]\n\t" \
  "ds_read_b64 v[0:1], v28 offset:12288\n\t" \
  "ds_read_b64 v[2:3], v29 offset:12288\n\t" \
  "v_mfma_f32_16x16x32_bf16 v[156:159], v[96:99], v[4:7], v[156:159]\n\t" \
  "ds_read_b64 v[4:5], v30 offset:13312\n\t" \
  "ds_read_b64 v[6:7], v31 offset:13312\n\t" \
  "v_mfma_f32_16x16x32_bf16 v[144:147], v[68:71], v[8:11], v[144:147]\n\t" \
  "v_mov_b32 v143, v142\n\t" \
  "v_mov_b32 v142, v141\n\t" \
  "v_mfma_f32_16x16x32_bf16 v[144:147], v[64:67], v[12:15], v[144:147]\n\t" \
  "v_mov_b32 v141, v140\n\t" \
  "v_mfma_f32_16x16x32_bf16 v[148:151], v[76:79], v[8:11], v[148:151]\n\t" \
  "v_mfma_f32_16x16x32_bf16 v[148:151], v[72:75], v[12:15], v[148:151]\n\t" \
  "v_mfma_f32_16x16x32_bf16 v[152:155], v[84:87], v[8:11], v[152:155]\n\t" \
  "v_mfma_f32_16x16x32_bf16 v[152:155], v[80:83], v[12:15], v[152:155]\n\t" \
  "v_mfma_f32_16x16x32_bf16 v[156:159], v[92:95], v[8:11], v[156:159]\n\t" \
  "ds_read_b64 v[8:9], v24 offset:14336\n\t" \
  "ds_read_b64 v[10:11], v25 offset:14336\n\t" \
  "v_mfma_f32_16x16x32_bf16 v[156:159], v[88:91], v[12:15], v[156:159]\n\t" \
  "ds_read_b64 v[12:13], v26 offset:15360\n\t" \
  "s_waitcnt lgkmcnt(8)\n\t" \
  "v_mfma_f32_16x16x32_bf16 v[148:151], v[68:71], v[16:19], v[148:151]\n\t" \
  "ds_read_b64 v[14:15], v27 offset:15360\n\t" \
  "ds_bpermute_b32 v38, v32, v147\n\t" \
  "v_mfma_f32_16x16x32_bf16 v[148:151], v[64:67], v[20:23], v[148:151]\n\t" \
  "v_mov_b32 v147, v146\n\t" \
  "v_mov_b32 v146, v145\n\t" \
  "v_mfma_f32_16x16x32_bf16 v[152:155], v[76:79], v[16:19], v[152:155]\n\t" \
  "s_waitcnt lgkmcnt(9)\n\t" \
  "v_cndmask_b32_e64 v140, v37, v36, vcc\n\t" \
  "v_mfma_f32_16x16x32_bf16 v[152:155], v[72:75], v[20:23], v[152:155]\n\t" \
  "v_mov_b32 v145, v144\n\t" \
  "v_mfma_f32_16x16x32_bf16 v[156:159], v[84:87], v[16:19], v[156:159]\n\t" \
  "ds_read_b64 v[16:17], v26 offset:0\n\t" \
  "ds_read_b64 v[18:19], v27 offset:0\n\t" \
  "v_mfma_f32_16x16x32_bf16 v[156:159], v[80:83], v[20:23], v[156:159]\n\t" \
  "ds_read_b64 v[20:21], v28 offset:1024\n\t" \
  "ds_read_b64 v[22:23], v29 offset:1024\n\t" \
  "s_waitcnt lgkmcnt(9)\n\t" \
  "v_mfma_f32_16x16x32_bf16 v[152:155], v[68:71], v[0:3], v[152:155]\n\t" \
  "ds_bpermute_b32 v39, v32, v151\n\t" \
  "v_mfma_f32_16x16x32_bf16 v[152:155], v[64:67], v[4:7], v[152:155]\n\t" \
  "v_mov_b32 v151, v150\n\t" \
  "v_mov_b32 v150, v149\n\t" \
  "v_mfma_f32_16x16x32_bf16 v[156:159], v[76:79], v[0:3], v[156:159]\n\t" \
  "ds_read_b64 v[0:1], v30 offset:2048\n\t" \
  "ds_read_b64 v[2:3], v31 offset:2048\n\t" \
  "v_mfma_f32_16x16x32_bf16 v[156:159], v[72:75], v[4:7], v[156:159]\n\t" \
  "ds_read_b64 v[4:5], v24 offset:3072\n\t" \
  "s_waitcnt lgkmcnt(11)\n\t" \
  "v_mfma_f32_16x16x32_bf16 v[156:159], v[68:71], v[8:11], v[156:159]\n\t" \
  "ds_bpermute_b32 v40, v32, v155\n\t" \
  "s_waitcnt lgkmcnt(10)\n\t" \
  "v_mfma_f32_16x16x32_bf16 v[156:159], v[64:67], v[12:15], v[156:159]\n\t" \
  "ds_read_b64 v[6:7], v25 offset:3072\n\t" \
  "s_waitcnt lgkmcnt(8)\n\t" \
  "v_mfma_f32_16x16x32_bf16 v[128:131], v[68:71], v[16:19], v[128:131]\n\t" \
  "v_cndmask_b32_e64 v144, v38, v37, vcc\n\t" \
  "s_waitcnt lgkmcnt(6)\n\t" \
  "v_mfma_f32_16x16x32_bf16 v[128:131], v[64:67], v[20:23], v[128:131]\n\t" \
  "v_mov_b32 v149, v148\n\t" \
  "v_mov_b32 v155, v154\n\t" \
  "v_mfma_f32_16x16x32_bf16 v[132:135], v[76:79], v[16:19], v[132:135]\n\t" \
  "ds_bpermute_b32 v41, v32, v159\n\t" \
  "v_mov_b32 v154, v153\n\t" \
  "v_mfma_f32_16x16x32_bf16 v[132:135], v[72:75], v[20:23], v[132:135]\n\t" \
  "v_mov_b32 v153, v152\n\t" \
  "v_mov_b32 v159, v158\n\t" \
  "v_mfma_f32_16x16x32_bf16 v[136:139], v[84:87], v[16:19], v[136:139]\n\t" \
  "v_mov_b32 v158, v157\n\t" \
  "v_mov_b32 v157, v156\n\t" \
  "v_mfma_f32_16x16x32_bf16 v[136:139], v[80:83], v[20:23], v[136:139]\n\t" \
  "s_waitcnt lgkmcnt(6)\n\t" \
  "v_cndmask_b32_e64 v148, v39, v38, vcc\n\t" \
  "v_mfma_f32_16x16x32_bf16 v[140:143], v[92:95], v[16:19], v[140:143]\n\t" \
  "global_load_dwordx4 v[44:47], v43, %[q0] offset:0\n\t" \
  "v_mfma_f32_16x16x32_bf16 v[140:143], v[88:91], v[20:23], v[140:143]\n\t" \
  "ds_read_b64 v[8:9], v26 offset:4096\n\t" \
  "ds_read_b64 v[10:11], v27 offset:4096\n\t" \
  "v_mfma_f32_16x16x32_bf16 v[144:147], v[100:103], v[16:19], v[144:147]\n\t" \
  "s_waitcnt lgkmcnt(4)\n\t" \
  "v_cndmask_b32_e64 v152, v40, v39, vcc\n\t" \
  "v_mfma_f32_16x16x32_bf16 v[144:147], v[96:99], v[20:23], v[144:147]\n\t" \
  "ds_read_b64 v[12:13], v28 offset:5120\n\t" \
  "ds_read_b64 v[14:15], v29 offset:5120\n\t" \
  "v_mfma_f32_16x16x32_bf16 v[148:151], v[108:111], v[16:19], v[148:151]\n\t" \
  "s_waitcnt lgkmcnt(4)\n\t" \
  "v_cndmask_b32_e64 v156, v41, v40, vcc\n\t" \
  "v_mfma_f32_16x16x32_bf16 v[148:151], v[104:107], v[20:23], v[148:151]\n\t" \
  "global_load_dwordx4 v[48:51], v43, %[q0] offset:2048\n\t" \
  "v_mfma_f32_16x16x32_bf16 v[152:155], v[116:119], v[16:19], v[152:155]\n\t" \
  "ds_bpermute_b32 v34, v32, v131\n\t" \
  "v_mfma_f32_16x16x32_bf16 v[152:155], v[112:115], v[20:23], v[152:155]\n\t" \
  "v_mov_b32 v131, v130\n\t" \
  "v_mov_b32 v130, v129\n\t" \
  "v_mfma_f32_16x16x32_bf16 v[156:159], v[124:127], v[16:19], v[156:159]\n\t" \
  "global_load_dwordx4 v[52:55], v43, %[q2] offset:0\n\t" \
  "v_mfma_f32_16x16x32_bf16 v[156:159], v[120:123], v[20:23], v[156:159]\n\t" \
  "ds_read_b64 v[16:17], v30 offset:6144\n\t" \
  "ds_read_b64 v[18:19], v31 offset:6144\n\t" \
  "v_mfma_f32_16x16x32_bf16 v[132:135], v[68:71], v[0:3], v[132:135]\n\t" \
  "ds_read_b64 v[20:21], v24 offset:7168\n\t" \
  "ds_read_b64 v[22:23], v25 offset:7168\n\t" \
  "v_mfma_f32_16x16x32_bf16 v[132:135], v[64:67], v[4:7], v[132:135]\n\t" \
  "v_mov_b32 v129, v128\n\t" \
  "v_mfma_f32_16x16x32_bf16 v[136:139], v[76:79], v[0:3], v[136:139]\n\t" \
  "global_load_dwordx4 v[56:59], v43, %[q2] offset:2048\n\t" \
  "v_mfma_f32_16x16x32_bf16 v[136:139], v[72:75], v[4:7], v[136:139]\n\t" \
  "v_mfma_f32_16x16x32_bf16 v[140:143], v[84:87], v[0:3], v[140:143]\n\t" \
  "v_mfma_f32_16x16x32_bf16 v[140:143], v[80:83], v[4:7], v[140:143]\n\t" \
  "v_mfma_f32_16x16x32_bf16 v[144:147], v[92:95], v[0:3], v[144:147]\n\t" \
  "v_mfma_f32_16x16x32_bf16 v[144:147], v[88:91], v[4:7], v[144:147]\n\t" \
  "ds_bpermute_b32 v35, v32, v135\n\t" \
  "v_mfma_f32_16x16x32_bf16 v[148:151], v[100:103], v[0:3], v[148:151]\n\t" \
  "v_mov_b32 v135, v134\n\t" \
  "v_mov_b32 v134, v133\n\t" \
  "v_mfma_f32_16x16x32_bf16 v[148:151], v[96:99], v[4:7], v[148:151]\n\t" \
  "s_waitcnt lgkmcnt(5)\n\t" \
  "v_cndmask_b32_e64 v128, v34, 0, vcc\n\t" \
  "v_mfma_f32_16x16x32_bf16 v[152:155], v[108:111], v[0:3], v[152:155]\n\t" \
  "v_mov_b32 v133, v132\n\t" \
  "v_mfma_f32_16x16x32_bf16 v[152:155], v[104:107], v[4:7], v[152:155]\n\t" \
  "v_mfma_f32_16x16x32_bf16 v[156:159], v[116:119], v[0:3], v[156:159]\n\t" \
  "ds_read_b64 v[0:1], v26 offset:8192\n\t" \
  "ds_read_b64 v[2:3], v27 offset:8192\n\t" \
  "v_mfma_f32_16x16x32_bf16 v[156:159], v[112:115], v[4:7], v[156:159]\n\t" \
  "ds_read_b64 v[4:5], v28 offset:9216\n\t" \
  "ds_read_b64 v[6:7], v29 offset:9216\n\t" \
  "v_mfma_f32_16x16x32_bf16 v[136:139], v[68:71], v[8:11], v[136:139]\n\t" \
  "v_mfma_f32_16x16x32_bf16 v[136:139], v[64:67], v[12:15], v[136:139]\n\t" \
  "v_mfma_f32_16x16x32_bf16 v[140:143], v[76:79], v[8:11], v[140:143]\n\t" \
  "v_mfma_f32_16x16x32_bf16 v[140:143], v[72:75], v[12:15], v[140:143]\n\t" \
  "v_mfma_f32_16x16x32_bf16 v[144:147], v[84:87], v[8:11], v[144:147]\n\t" \
  "s_waitcnt lgkmcnt(4)\n\t" \
  "v_cndmask_b32_e64 v132, v35, v34, vcc\n\t" \
  "v_mfma_f32_16x16x32_bf16 v[144:147], v[80:83], v[12:15], v[144:147]\n\t" \
  "v_mfma_f32_16x16x32_bf16 v[148:151], v[92:95], v[8:11], v[148:151]\n\t" \
  "v_mfma_f32_16x16x32_bf16 v[148:151], v[88:91], v[12:15], v[148:151]\n\t" \
  "ds_bpermute_b32 v36, v32, v139\n\t" \
  "v_mfma_f32_16x16x32_bf16 v[152:155], v[100:103], v[8:11], v[152:155]\n\t" \
  "v_mov_b32 v139, v138\n\t" \
  "v_mov_b32 v138, v137\n\t" \
  "v_mfma_f32_16x16x32_bf16 v[152:155], v[96:99], v[12:15], v[152:155]\n\t" \
  "v_mov_b32 v137, v136\n\t" \
  "v_mfma_f32_16x16x32_bf16 v[156:159], v[108:111], v[8:11], v[156:159]\n\t" \
  "ds_read_b64 v[8:9], v30 offset:10240\n\t" \
  "ds_read_b64 v[10:11], v31 offset:10240\n\t" \
  "v_mfma_f32_16x16x32_bf16 v[156:159], v[104:107], v[12:15], v[156:159]\n\t" \
  "ds_read_b64 v[12:13], v24 offset:11264\n\t" \
  "ds_read_b64 v[14:15], v25 offset:11264\n\t" \
  "v_mfma_f32_16x16x32_bf16 v[140:143], v[68:71], v[16:19], v[140:143]\n\t" \
  "v_mfma_f32_16x16x32_bf16 v[140:143], v[64:67], v[20:23], v[140:143]\n\t" \
  "v_mfma_f32_16x16x32_bf16 v[144:147], v[76:79], v[16:19], v[144:147]\n\t" \
  "v_mfma_f32_16x16x32_bf16 v[144:147], v[72:75], v[20:23], v[144:147]\n\t" \
  "v_mfma_f32_16x16x32_bf16 v[148:151], v[84:87], v[16:19], v[148:151]\n\t" \
  "v_mfma_f32_16x16x32_bf16 v[148:151], v[80:83], v[20:23], v[148:151]\n\t" \
  "v_mfma_f32_16x16x32_bf16 v[152:155], v[92:95], v[16:19], v[152:155]\n\t" \
  "s_waitcnt lgkmcnt(4)\n\t" \
  "v_cndmask_b32_e64 v136, v36, v35, vcc\n\t" \
  "v_mfma_f32_16x16x32_bf16 v[152:155], v[88:91], v[20:23], v[152:155]\n\t" \
  "ds_bpermute_b32 v37, v32, v143\n\t" \
  "v_mfma_f32_16x16x32_bf16 v[156:159], v[100:103], v[16:19], v[156:159]\n\t" \
  "ds_read_b64 v[16:17], v26 offset:12288\n\t" \
  "ds_read_b64 v[18:19], v27 offset:12288\n\t" \
  "v_mfma_f32_16x16x32_bf16 v[156:159], v[96:99], v[20:23], v[156:159]\n\t" \
  "ds_read_b64 v[20:21], v28 offset:13312\n\t" \
  "ds_read_b64 v[22:23], v29 offset:13312\n\t" \
  "v_mfma_f32_16x16x32_bf16 v[144:147], v[68:71], v[0:3], v[144:147]\n\t" \
  "v_mov_b32 v143, v142\n\t" \
  "v_mov_b32 v142, v141\n\t" \
  "v_mfma_f32_16x16x32_bf16 v[144:147], v[64:67], v[4:7], v[144:147]\n\t" \
  "v_mov_b32 v141, v140\n\t" \
  "v_mfma_f32_16x16x32_bf16 v[148:151], v[76:79], v[0:3], v[148:151]\n\t" \
  "v_mfma_f32_16x16x32_bf16 v[148:151], v[72:75], v[4:7], v[148:151]\n\t" \
  "v_mfma_f32_16x16x32_bf16 v[152:155], v[84:87], v[0:3], v[152:155]\n\t" \
  "v_mfma_f32_16x16x32_bf16 v[152:155], v[80:83], v[4:7], v[152:155]\n\t" \
  "v_mfma_f32_16x16x32_bf16 v[156:159], v[92:95], v[0:3], v[156:159]\n\t" \
  "ds_read_b64 v[0:1], v30 offset:14336\n\t" \
  "ds_read_b64 v[2:3], v31 offset:14336\n\t" \
  "v_mfma_f32_16x16x32_bf16 v[156:159], v[88:91], v[4:7], v[156:159]\n\t" \
  "ds_read_b64 v[4:5], v24 offset:15360\n\t" \
  "s_waitcnt lgkmcnt(8)\n\t" \
  "v_mfma_f32_16x16x32_bf16 v[148:151], v[68:71], v[8:11], v[148:151]\n\t" \
  "ds_read_b64 v[6:7], v25 offset:15360\n\t" \
  "ds_bpermute_b32 v38, v32, v147\n\t" \
  "v_mfma_f32_16x16x32_bf16 v[148:151], v[64:67], v[12:15], v[148:151]\n\t" \
  "v_mov_b32 v147, v146\n\t" \
  "v_mov_b32 v146, v145\n\t" \
  "v_mfma_f32_16x16x32_bf16 v[152:155], v[76:79], v[8:11], v[152:155]\n\t" \
  "s_waitcnt lgkmcnt(9)\n\t" \
  "v_cndmask_b32_e64 v140, v37, v36, vcc\n\t" \
  "v_mfma_f32_16x16x32_bf16 v[152:155], v[72:75], v[12:15], v[152:155]\n\t" \
  "v_mov_b32 v145, v144\n\t" \
  "v_mfma_f32_16x16x32_bf16 v[156:159], v[84:87], v[8:11], v[156:159]\n\t" \
  "ds_read_b64 v[8:9], v24 offset:0\n\t" \
  "ds_read_b64 v[10:11], v25 offset:0\n\t" \
  "v_mfma_f32_16x16x32_bf16 v[156:159], v[80:83], v[12:15], v[156:159]\n\t" \
  "ds_read_b64 v[12:13], v26 offset:1024\n\t" \
  "ds_read_b64 v[14:15], v27 offset:1024\n\t" \
  "s_waitcnt lgkmcnt(9)\n\t" \
  "v_mfma_f32_16x16x32_bf16 v[152:155], v[68:71], v[16:19], v[152:155]\n\t" \
  "ds_bpermute_b32 v39, v32, v151\n\t" \
  "v_mfma_f32_16x16x32_bf16 v[152:155], v[64:67], v[20:23], v[152:155]\n\t" \
  "v_mov_b32 v151, v150\n\t" \
  "v_mov_b32 v150, v149\n\t" \
  "v_mfma_f32_16x16x32_bf16 v[156:159], v[76:79], v[16:19], v[156:159]\n\t" \
  "ds_read_b64 v[16:17], v28 offset:2048\n\t" \
  "ds_read_b64 v[18:19], v29 offset:2048\n\t" \
  "v_mfma_f32_16x16x32_bf16 v[156:159], v[72:75], v[20:23], v[156:159]\n\t" \
  "ds_read_b64 v[20:21], v30 offset:3072\n\t" \
  "s_waitcnt lgkmcnt(11)\n\t" \
  "v_mfma_f32_16x16x32_bf16 v[156:159], v[68:71], v[0:3], v[156:159]\n\t" \
  "ds_bpermute_b32 v40, v32, v155\n\t" \
  "s_waitcnt lgkmcnt(10)\n\t" \
  "v_mfma_f32_16x16x32_bf16 v[156:159], v[64:67], v[4:7], v[156:159]\n\t" \
  "ds_read_b64 v[22:23], v31 offset:3072\n\t" \
  "s_waitcnt lgkmcnt(8)\n\t" \
  "v_mfma_f32_16x16x32_bf16 v[128:131], v[68:71], v[8:11], v[128:131]\n\t" \
  "v_cndmask_b32_e64 v144, v38, v37, vcc\n\t" \
  "s_waitcnt lgkmcnt(6)\n\t" \
  "v_mfma_f32_16x16x32_bf16 v[128:131], v[64:67], v[12:15], v[128:131]\n\t" \
  "v_mov_b32 v149, v148\n\t" \
  "v_mov_b32 v155, v154\n\t" \
  "v_mfma_f32_16x16x32_bf16 v[132:135], v[76:79], v[8:11], v[132:135]\n\t" \
  "ds_bpermute_b32 v41, v32, v159\n\t" \
  "v_mov_b32 v154, v153\n\t" \
  "v_mfma_f32_16x16x32_bf16 v[132:135], v[72:75], v[12:15], v[132:135]\n\t" \
  "v_mov_b32 v153, v152\n\t" \
  "v_mov_b32 v159, v158\n\t" \
  "v_mfma_f32_16x16x32_bf16 v[136:139], v[84:87], v[8:11], v[136:139]\n\t" \
  "v_mov_b32 v158, v157\n\t" \
  "v_mov_b32 v157, v156\n\t" \
  "v_mfma_f32_16x16x32_bf16 v[136:139], v[80:83], v[12:15], v[136:139]\n\t" \
  "s_waitcnt lgkmcnt(6)\n\t" \
  "v_cndmask_b32_e64 v148, v39, v38, vcc\n\t" \
  "v_mfma_f32_16x16x32_bf16 v[140:143], v[92:95], v[8:11], v[140:143]\n\t" \
  "ds_read_b64 v[0:1], v24 offset:4096\n\t" \
  "ds_read_b64 v[2:3], v25 offset:4096\n\t" \
  "v_mfma_f32_16x16x32_bf16 v[140:143], v[88:91], v[12:15], v[140:143]\n\t" \
  "ds_read_b64 v[4:5], v26 offset:5120\n\t" \
  "ds_read_b64 v[6:7], v27 offset:5120\n\t" \
  "v_mfma_f32_16x16x32_bf16 v[144:147], v[100:103], v[8:11], v[144:147]\n\t" \
  "s_waitcnt lgkmcnt(6)\n\t" \
  "v_cndmask_b32_e64 v152, v40, v39, vcc\n\t" \
  "v_mfma_f32_16x16x32_bf16 v[144:147], v[96:99], v[12:15], v[144:147]\n\t" \
  "ds_bpermute_b32 v34, v32, v131\n\t" \
  "v_mfma_f32_16x16x32_bf16 v[148:151], v[108:111], v[8:11], v[148:151]\n\t" \
  "s_waitcnt lgkmcnt(5)\n\t" \
  "v_cndmask_b32_e64 v156, v41, v40, vcc\n\t" \
  "v_mfma_f32_16x16x32_bf16 v[148:151], v[104:107], v[12:15], v[148:151]\n\t" \
  "v_mov_b32 v131, v130\n\t" \
  "v_mov_b32 v130, v129\n\t" \
  "v_mfma_f32_16x16x32_bf16 v[152:155], v[116:119], v[8:11], v[152:155]\n\t" \
  "v_mov_b32 v129, v128\n\t" \
  "v_mfma_f32_16x16x32_bf16 v[152:155], v[112:115], v[12:15], v[152:155]\n\t" \
  "v_mfma_f32_16x16x32_bf16 v[156:159], v[124:127], v[8:11], v[156:159]\n\t" \
  "ds_read_b64 v[8:9], v28 offset:6144\n\t" \
  "ds_read_b64 v[10:11], v29 offset:6144\n\t" \
  "v_mfma_f32_16x16x32_bf16 v[156:159], v[120:123], v[12:15], v[156:159]\n\t" \
  "ds_read_b64 v[12:13], v30 offset:7168\n\t" \
  "ds_read_b64 v[14:15], v31 offset:7168\n\t" \
  "v_mfma_f32_16x16x32_bf16 v[132:135], v[68:71], v[16:19], v[132:135]\n\t" \
  "v_mfma_f32_16x16x32_bf16 v[132:135], v[64:67], v[20:23], v[132:135]\n\t" \
  "v_mfma_f32_16x16x32_bf16 v[136:139], v[76:79], v[16:19], v[136:139]\n\t" \
  "v_mfma_f32_16x16x32_bf16 v[136:139], v[72:75], v[20:23], v[136:139]\n\t" \
  "v_mfma_f32_16x16x32_bf16 v[140:143], v[84:87], v[16:19], v[140:143]\n\t" \
  "s_waitcnt lgkmcnt(4)\n\t" \
  "v_cndmask_b32_e64 v128, v34, 0, vcc\n\t" \
  "v_mfma_f32_16x16x32_bf16 v[140:143], v[80:83], v[20:23], v[140:143]\n\t" \
  "v_mfma_f32_16x16x32_bf16 v[144:147], v[92:95], v[16:19], v[144:147]\n\t" \
  "v_mfma_f32_16x16x32_bf16 v[144:147], v[88:91], v[20:23], v[144:147]\n\t" \
  "ds_bpermute_b32 v35, v32, v135\n\t" \
  "v_mfma_f32_16x16x32_bf16 v[148:151], v[100:103], v[16:19], v[148:151]\n\t" \
  "v_mov_b32 v135, v134\n\t" \
  "v_mov_b32 v134, v133\n\t" \
  "v_mfma_f32_16x16x32_bf16 v[148:151], v[96:99], v[20:23], v[148:151]\n\t" \
  "v_mov_b32 v133, v132\n\t" \
  "v_mfma_f32_16x16x32_bf16 v[152:155], v[108:111], v[16:19], v[152:155]\n\t" \
  "v_mfma_f32_16x16x32_bf16 v[152:155], v[104:107], v[20:23], v[152:155]\n\t" \
  "v_mfma_f32_16x16x32_bf16 v[156:159], v[116:119], v[16:19], v[156:159]\n\t" \
  "ds_read_b64 v[16:17], v24 offset:8192\n\t" \
  "ds_read_b64 v[18:19], v25 offset:8192\n\t" \
  "v_mfma_f32_16x16x32_bf16 v[156:159], v[112:115], v[20:23], v[156:159]\n\t" \
  "ds_read_b64 v[20:21], v26 offset:9216\n\t" \
  "ds_read_b64 v[22:23], v27 offset:9216\n\t" \
  "v_mfma_f32_16x16x32_bf16 v[136:139], v[68:71], v[0:3], v[136:139]\n\t" \
  "v_mfma_f32_16x16x32_bf16 v[136:139], v[64:67], v[4:7], v[136:139]\n\t" \
  "v_mfma_f32_16x16x32_bf16 v[140:143], v[76:79], v[0:3], v[140:143]\n\t" \
  "v_mfma_f32_16x16x32_bf16 v[140:143], v[72:75], v[4:7], v[140:143]\n\t" \
  "v_mfma_f32_16x16x32_bf16 v[144:147], v[84:87], v[0:3], v[144:147]\n\t" \
  "s_waitcnt lgkmcnt(4)\n\t" \
  "v_cndmask_b32_e64 v132, v35, v34, vcc\n\t" \
  "v_mfma_f32_16x16x32_bf16 v[144:147], v[80:83], v[4:7], v[144:147]\n\t" \
  "v_mfma_f32_16x16x32_bf16 v[148:151], v[92:95], v[0:3], v[148:151]\n\t" \
  "v_mfma_f32_16x16x32_bf16 v[148:151], v[88:91], v[4:7], v[148:151]\n\t" \
  "ds_bpermute_b32 v36, v32, v139\n\t" \
  "v_mfma_f32_16x16x32_bf16 v[152:155], v[100:103], v[0:3], v[152:155]\n\t" \
  "v_mov_b32 v139, v138\n\t" \
  "v_mov_b32 v138, v137\n\t" \
  "v_mfma_f32_16x16x32_bf16 v[152:155], v[96:99], v[4:7], v[152:155]\n\t" \
  "v_mov_b32 v137, v136\n\t" \
  "v_mfma_f32_16x16x32_bf16 v[156:159], v[108:111], v[0:3], v[156:159]\n\t" \
  "ds_read_b64 v[0:1], v28 offset:10240\n\t" \
  "ds_read_b64 v[2:3], v29 offset:10240\n\t" \
  "v_mfma_f32_16x16x32_bf16 v[156:159], v[104:107], v[4:7], v[156:159]\n\t" \
  "ds_read_b64 v[4:5], v30 offset:11264\n\t" \
  "ds_read_b64 v[6:7], v31 offset:11264\n\t" \
  "v_mfma_f32_16x16x32_bf16 v[140:143], v[68:71], v[8:11], v[140:143]\n\t" \
  "v_mfma_f32_16x16x32_bf16 v[140:143], v[64:67], v[12:15], v[140:143]\n\t" \
  "v_mfma_f32_16x16x32_bf16 v[144:147], v[76:79], v[8:11], v[144:147]\n\t" \
  "v_mfma_f32_16x16x32_bf16 v[144:147], v[72:75], v[12:15], v[144:147]\n\t" \
  "v_mfma_f32_16x16x32_bf16 v[148:151], v[84:87], v[8:11], v[148:151]\n\t" \
  "v_mfma_f32_16x16x32_bf16 v[148:151], v[80:83], v[12:15], v[148:151]\n\t" \
  "v_mfma_f32_16x16x32_bf16 v[152:155], v[92:95], v[8:11], v[152:155]\n\t" \
  "s_waitcnt lgkmcnt(4)\n\t" \
  "v_cndmask_b32_e64 v136, v36, v35, vcc\n\t" \
  "v_mfma_f32_16x16x32_bf16 v[152:155], v[88:91], v[12:15], v[152:155]\n\t" \
  "ds_bpermute_b32 v37, v32, v143\n\t" \
  "v_mfma_f32_16x16x32_bf16 v[156:159], v[100:103], v[8:11], v[156:159]\n\t" \
  "ds_read_b64 v[8:9], v24 offset:12288\n\t" \
  "ds_read_b64 v[10:11], v25 offset:12288\n\t" \
  "v_mfma_f32_16x16x32_bf16 v[156:159], v[96:99], v[12:15], v[156:159]\n\t" \
  "ds_read_b64 v[12:13], v26 offset:13312\n\t" \
  "ds_read_b64 v[14:15], v27 offset:13312\n\t" \
  "v_mfma_f32_16x16x32_bf16 v[144:147], v[68:71], v[16:19], v[144:147]\n\t" \
  "v_mov_b32 v143, v142\n\t" \
  "v_mov_b32 v142, v141\n\t" \
  "v_mfma_f32_16x16x32_bf16 v[144:147], v[64:67], v[20:23], v[144:147]\n\t" \
  "v_mov_b32 v141, v140\n\t" \
  "v_mfma_f32_16x16x32_bf16 v[148:151], v[76:79], v[16:19], v[148:151]\n\t" \
  "v_mfma_f32_16x16x32_bf16 v[148:151], v[72:75], v[20:23], v[148:151]\n\t" \
  "v_mfma_f32_16x16x32_bf16 v[152:155], v[84:87], v[16:19], v[152:155]\n\t" \
  "v_mfma_f32_16x16x32_bf16 v[152:155], v[80:83], v[20:23], v[152:155]\n\t" \
  "v_mfma_f32_16x16x32_bf16 v[156:159], v[92:95], v[16:19], v[156:159]\n\t" \
  "ds_read_b64 v[16:17], v28 offset:14336\n\t" \
  "ds_read_b64 v[18:19], v29 offset:14336\n\t" \
  "v_mfma_f32_16x16x32_bf16 v[156:159], v[88:91], v[20:23], v[156:159]\n\t" \
  "ds_read_b64 v[20:21], v30 offset:15360\n\t" \
  "s_waitcnt lgkmcnt(8)\n\t" \
  "v_mfma_f32_16x16x32_bf16 v[148:151], v[68:71], v[0:3], v[148:151]\n\t" \
  "ds_read_b64 v[22:23], v31 offset:15360\n\t" \
  "ds_bpermute_b32 v38, v32, v147\n\t" \
  "v_mfma_f32_16x16x32_bf16 v[148:151], v[64:67], v[4:7], v[148:151]\n\t" \
  "v_mov_b32 v147, v146\n\t" \
  "v_mov_b32 v146, v145\n\t" \
  "v_mfma_f32_16x16x32_bf16 v[152:155], v[76:79], v[0:3], v[152:155]\n\t" \
  "s_waitcnt lgkmcnt(9)\n\t" \
  "v_cndmask_b32_e64 v140, v37, v36, vcc\n\t" \
  "v_mfma_f32_16x16x32_bf16 v[152:155], v[72:75], v[4:7], v[152:155]\n\t" \
  "v_mov_b32 v145, v144\n\t" \
  "v_mfma_f32_16x16x32_bf16 v[156:159], v[84:87], v[0:3], v[156:159]\n\t" \
  "ds_read_b64 v[0:1], v30 offset:0\n\t" \
  "ds_read_b64 v[2:3], v31 offset:0\n\t" \
  "v_mfma_f32_16x16x32_bf16 v[156:159], v[80:83], v[4:7], v[156:159]\n\t" \
  "ds_read_b64 v[4:5], v24 offset:1024\n\t" \
  "ds_read_b64 v[6:7], v25 offset:1024\n\t" \
  "s_waitcnt lgkmcnt(9)\n\t" \
  "v_mfma_f32_16x16x32_bf16 v[152:155], v[68:71], v[8:11], v[152:155]\n\t" \
  "ds_bpermute_b32 v39, v32, v151\n\t" \
  "v_mfma_f32_16x16x32_bf16 v[152:155], v[64:67], v[12:15], v[152:155]\n\t" \
  "v_mov_b32 v151, v150\n\t" \
  "v_mov_b32 v150, v149\n\t" \
  "v_mfma_f32_16x16x32_bf16 v[156:159], v[76:79], v[8:11], v[156:159]\n\t" \
  "ds_read_b64 v[8:9], v26 offset:2048\n\t" \
  "ds_read_b64 v[10:11], v27 offset:2048\n\t" \
  "v_mfma_f32_16x16x32_bf16 v[156:159], v[72:75], v[12:15], v[156:159]\n\t" \
  "ds_read_b64 v[12:13], v28 offset:3072\n\t" \
  "s_waitcnt lgkmcnt(11)\n\t" \
  "v_mfma_f32_16x16x32_bf16 v[156:159], v[68:71], v[16:19], v[156:159]\n\t" \
  "ds_bpermute_b32 v40, v32, v155\n\t" \
  "s_waitcnt lgkmcnt(10)\n\t" \
  "v_mfma_f32_16x16x32_bf16 v[156:159], v[64:67], v[20:23], v[156:159]\n\t" \
  "ds_read_b64 v[14:15], v29 offset:3072\n\t" \
  "s_waitcnt lgkmcnt(8)\n\t" \
  "v_mfma_f32_16x16x32_bf16 v[128:131], v[68:71], v[0:3], v[128:131]\n\t" \
  "v_cndmask_b32_e64 v144, v38, v37, vcc\n\t" \
  "s_waitcnt lgkmcnt(6)\n\t" \
  "v_mfma_f32_16x16x32_bf16 v[128:131], v[64:67], v[4:7], v[128:131]\n\t" \
  "v_mov_b32 v149, v148\n\t" \
  "v_mov_b32 v155, v154\n\t" \
  "v_mfma_f32_16x16x32_bf16 v[132:135], v[76:79], v[0:3], v[132:135]\n\t" \
  "ds_bpermute_b32 v41, v32, v159\n\t" \
  "v_mov_b32 v154, v153\n\t" \
  "v_mfma_f32_16x16x32_bf16 v[132:135], v[72:75], v[4:7], v[132:135]\n\t" \
  "v_mov_b32 v153, v152\n\t" \
  "v_mov_b32 v159, v158\n\t" \
  "v_mfma_f32_16x16x32_bf16 v[136:139], v[84:87], v[0:3], v[136:139]\n\t" \
  "v_mov_b32 v158, v157\n\t" \
  "v_mov_b32 v157, v156\n\t" \
  "v_mfma_f32_16x16x32_bf16 v[136:139], v[80:83], v[4:7], v[136:139]\n\t" \
  "s_waitcnt lgkmcnt(6)\n\t" \
  "v_cndmask_b32_e64 v148, v39, v38, vcc\n\t" \
  "v_mfma_f32_16x16x32_bf16 v[140:143], v[92:95], v[0:3], v[140:143]\n\t" \
  "ds_read_b64 v[16:17], v30 offset:4096\n\t" \
  "ds_read_b64 v[18:19], v31 offset:4096\n\t" \
  "v_mfma_f32_16x16x32_bf16 v[140:143], v[88:91], v[4:7], v[140:143]\n\t" \
  "ds_read_b64 v[20:21], v24 offset:5120\n\t" \
  "ds_read_b64 v[22:23], v25 offset:5120\n\t" \
  "v_mfma_f32_16x16x32_bf16 v[144:147], v[100:103], v[0:3], v[144:147]\n\t" \
  "s_waitcnt lgkmcnt(6)\n\t" \
  "v_cndmask_b32_e64 v152, v40, v39, vcc\n\t" \
  "v_mfma_f32_16x16x32_bf16 v[144:147], v[96:99], v[4:7], v[144:147]\n\t" \
  "v_mfma_f32_16x16x32_bf16 v[148:151], v[108:111], v[0:3], v[148:151]\n\t" \
  "s_waitcnt lgkmcnt(4)\n\t" \
  "v_cndmask_b32_e64 v156, v41, v40, vcc\n\t" \
  "v_mfma_f32_16x16x32_bf16 v[148:151], v[104:107], v[4:7], v[148:151]\n\t" \
  "v_mfma_f32_16x16x32_bf16 v[152:155], v[116:119], v[0:3], v[152:155]\n\t" \
  "v_mfma_f32_16x16x32_bf16 v[152:155], v[112:115], v[4:7], v[152:155]\n\t" \
  "v_mfma_f32_16x16x32_bf16 v[156:159], v[124:127], v[0:3], v[156:159]\n\t" \
  "ds_read_b64 v[0:1], v26 offset:6144\n\t" \
  "ds_read_b64 v[2:3], v27 offset:6144\n\t" \
  "v_mfma_f32_16x16x32_bf16 v[156:159], v[120:123], v[4:7], v[156:159]\n\t" \
  "ds_read_b64 v[4:5], v28 offset:7168\n\t" \
  "ds_read_b64 v[6:7], v29 offset:7168\n\t" \
  "v_mfma_f32_16x16x32_bf16 v[132:135], v[68:71], v[8:11], v[132:135]\n\t" \
  "v_mfma_f32_16x16x32_bf16 v[132:135], v[64:67], v[12:15], v[132:135]\n\t" \
  "v_mfma_f32_16x16x32_bf16 v[136:139], v[76:79], v[8:11], v[136:139]\n\t" \
  "v_mfma_f32_16x16x32_bf16 v[136:139], v[72:75], v[12:15], v[136:139]\n\t" \
  "v_mfma_f32_16x16x32_bf16 v[140:143], v[84:87], v[8:11], v[140:143]\n\t" \
  "v_mfma_f32_16x16x32_bf16 v[140:143], v[80:83], v[12:15], v[140:143]\n\t" \
  "v_mfma_f32_16x16x32_bf16 v[144:147], v[92:95], v[8:11], v[144:147]\n\t" \
  "v_mfma_f32_16x16x32_bf16 v[144:147], v[88:91], v[12:15], v[144:147]\n\t" \
  "v_mfma_f32_16x16x32_bf16 v[148:151], v[100:103], v[8:11], v[148:151]\n\t" \
  "s_mov_b64 exec, 1\n\t" \
  "ds_write_b32 v33, v33\n\t" \
  "s_mov_b64 exec, -1\n\t" \
  "v_mfma_f32_16x16x32_bf16 v[148:151], v[96:99], v[12:15], v[148:151]\n\t" \
  "v_mfma_f32_16x16x32_bf16 v[152:155], v[108:111], v[8:11], v[152:155]\n\t" \
  "v_mfma_f32_16x16x32_bf16 v[152:155], v[104:107], v[12:15], v[152:155]\n\t" \
  "v_mfma_f32_16x16x32_bf16 v[156:159], v[116:119], v[8:11], v[156:159]\n\t" \
  "ds_read_b64 v[8:9], v30 offset:8192\n\t" \
  "ds_read_b64 v[10:11], v31 offset:8192\n\t" \
  "v_mfma_f32_16x16x32_bf16 v[156:159], v[112:115], v[12:15], v[156:159]\n\t" \
  "ds_read_b64 v[12:13], v24 offset:9216\n\t" \
  "s_waitcnt lgkmcnt(4)\n\t" \
  "v_mfma_f32_16x16x32_bf16 v[136:139], v[68:71], v[16:19], v[136:139]\n\t" \
  "ds_read_b64 v[14:15], v25 offset:9216\n\t" \
  "v_mfma_f32_16x16x32_bf16 v[136:139], v[64:67], v[20:23], v[136:139]\n\t" \
  "v_mfma_f32_16x16x32_bf16 v[140:143], v[76:79], v[16:19], v[140:143]\n\t" \
  "v_mfma_f32_16x16x32_bf16 v[140:143], v[72:75], v[20:23], v[140:143]\n\t" \
  "v_mfma_f32_16x16x32_bf16 v[144:147], v[84:87], v[16:19], v[144:147]\n\t" \
  "v_mfma_f32_16x16x32_bf16 v[144:147], v[80:83], v[20:23], v[144:147]\n\t" \
  "v_mfma_f32_16x16x32_bf16 v[148:151], v[92:95], v[16:19], v[148:151]\n\t" \
  "v_mfma_f32_16x16x32_bf16 v[148:151], v[88:91], v[20:23], v[148:151]\n\t" \
  "v_mfma_f32_16x16x32_bf16 v[152:155], v[100:103], v[16:19], v[152:155]\n\t" \
  "v_mfma_f32_16x16x32_bf16 v[152:155], v[96:99], v[20:23], v[152:155]\n\t" \
  "v_mfma_f32_16x16x32_bf16 v[156:159], v[108:111], v[16:19], v[156:159]\n\t" \
  "ds_read_b64 v[16:17], v26 offset:10240\n\t" \
  "ds_read_b64 v[18:19], v27 offset:10240\n\t" \
  "v_mfma_f32_16x16x32_bf16 v[156:159], v[104:107], v[20:23], v[156:159]\n\t" \
  "ds_read_b64 v[20:21], v28 offset:11264\n\t" \
  "ds_read_b64 v[22:23], v29 offset:11264\n\t" \
  "v_mfma_f32_16x16x32_bf16 v[140:143], v[68:71], v[0:3], v[140:143]\n\t" \
  "v_mfma_f32_16x16x32_bf16 v[140:143], v[64:67], v[4:7], v[140:143]\n\t" \
  "v_mfma_f32_16x16x32_bf16 v[144:147], v[76:79], v[0:3], v[144:147]\n\t" \
  "v_mfma_f32_16x16x32_bf16 v[144:147], v[72:75], v[4:7], v[144:147]\n\t" \
  "v_mfma_f32_16x16x32_bf16 v[148:151], v[84:87], v[0:3], v[148:151]\n\t" \
  "v_mfma_f32_16x16x32_bf16 v[148:151], v[80:83], v[4:7], v[148:151]\n\t" \
  "v_mfma_f32_16x16x32_bf16 v[152:155], v[92:95], v[0:3], v[152:155]\n\t" \
  "v_mfma_f32_16x16x32_bf16 v[152:155], v[88:91], v[4:7], v[152:155]\n\t" \
  "v_mfma_f32_16x16x32_bf16 v[156:159], v[100:103], v[0:3], v[156:159]\n\t" \
  "ds_read_b64 v[0:1], v30 offset:12288\n\t" \
  "ds_read_b64 v[2:3], v31 offset:12288\n\t" \
  "v_mfma_f32_16x16x32_bf16 v[156:159], v[96:99], v[4:7], v[156:159]\n\t" \
  "ds_read_b64 v[4:5], v24 offset:13312\n\t" \
  "s_waitcnt lgkmcnt(5)\n\t" \
  "v_mfma_f32_16x16x32_bf16 v[144:147], v[68:71], v[8:11], v[144:147]\n\t" \
  "ds_read_b64 v[6:7], v25 offset:13312\n\t" \
  "v_mfma_f32_16x16x32_bf16 v[144:147], v[64:67], v[12:15], v[144:147]\n\t" \
  "v_mfma_f32_16x16x32_bf16 v[148:151], v[76:79], v[8:11], v[148:151]\n\t" \
  "v_mfma_f32_16x16x32_bf16 v[148:151], v[72:75], v[12:15], v[148:151]\n\t" \
  "v_mfma_f32_16x16x32_bf16 v[152:155], v[84:87], v[8:11], v[152:155]\n\t" \
  "v_mfma_f32_16x16x32_bf16 v[152:155], v[80:83], v[12:15], v[152:155]\n\t" \
  "v_mfma_f32_16x16x32_bf16 v[156:159], v[92:95], v[8:11], v[156:159]\n\t" \
  "ds_read_b64 v[8:9], v26 offset:14336\n\t" \
  "ds_read_b64 v[10:11], v27 offset:14336\n\t" \
  "v_mfma_f32_16x16x32_bf16 v[156:159], v[88:91], v[12:15], v[156:159]\n\t" \
  "ds_read_b64 v[12:13], v28 offset:15360\n\t" \
  "ds_read_b64 v[14:15], v29 offset:15360\n\t" \
  "v_mfma_f32_16x16x32_bf16 v[148:151], v[68:71], v[16:19], v[148:151]\n\t" \
  "s_waitcnt lgkmcnt(8)\n\t" \
  "v_mfma_f32_16x16x32_bf16 v[148:151], v[64:67], v[20:23], v[148:151]\n\t" \
  "v_mfma_f32_16x16x32_bf16 v[152:155], v[76:79], v[16:19], v[152:155]\n\t" \
  "v_mfma_f32_16x16x32_bf16 v[152:155], v[72:75], v[20:23], v[152:155]\n\t" \
  "v_mfma_f32_16x16x32_bf16 v[156:159], v[84:87], v[16:19], v[156:159]\n\t" \
  "v_mfma_f32_16x16x32_bf16 v[156:159], v[80:83], v[20:23], v[156:159]\n\t" \
  "s_waitcnt lgkmcnt(4)\n\t" \
  "v_mfma_f32_16x16x32_bf16 v[152:155], v[68:71], v[0:3], v[152:155]\n\t" \
  "v_mfma_f32_16x16x32_bf16 v[152:155], v[64:67], v[4:7], v[152:155]\n\t" \
  "v_mfma_f32_16x16x32_bf16 v[156:159], v[76:79], v[0:3], v[156:159]\n\t" \
  "v_mfma_f32_16x16x32_bf16 v[156:159], v[72:75], v[4:7], v[156:159]\n\t" \
  "s_waitcnt lgkmcnt(2)\n\t" \
  "v_mfma_f32_16x16x32_bf16 v[156:159], v[68:71], v[8:11], v[156:159]\n\t" \
  "s_waitcnt lgkmcnt(0)\n\t" \
  "v_mfma_f32_16x16x32_bf16 v[156:159], v[64:67], v[12:15], v[156:159]\n\t" \
  "s_waitcnt lgkmcnt(0)\n\t" \
  "s_nop 7\n\t"
#define CV_Q 1
#define CV_CLOB "vcc", "v0", "v1", "v2", "v3", "v4", "v5", "v6", "v7", "v8", "v9", "v10", "v11", "v12", "v13", "v14", "v15", "v16", "v17", "v18", "v19", "v20", "v21", "v22", "v23", "v34", "v35", "v36", "v37", "v38", "v39", "v40", "v41"

#define G3_ASM \
  "s_getpc_b64 s[92:93]\n\t" \
  "s_branch QC_%=\n\t" \
  "ds_read_b64_tr_b16 v[96:97], v160 offset:0\n\t" \
  "ds_read_b64_tr_b16 v[98:99], v160 offset:2048\n\t" \
  "ds_read_b64_tr_b16 v[100:101], v161 offset:0\n\t" \
  "ds_read_b64_tr_b16 v[102:103], v161 offset:2048\n\t" \
  "ds_read_b64_tr_b16 v[104:105], v162 offset:0\n\t" \
  "ds_read_b64_tr_b16 v[106:107], v162 offset:2048\n\t" \
  "ds_read_b64_tr_b16 v[108:109], v163 offset:0\n\t" \
  "ds_read_b64_tr_b16 v[110:111], v163 offset:2048\n\t" \
  "ds_read_b64_tr_b16 v[112:113], v160 offset:4096\n\t" \
  "ds_read_b64_tr_b16 v[114:115], v160 offset:6144\n\t" \
  "ds_read_b64_tr_b16 v[116:117], v161 offset:4096\n\t" \
  "ds_read_b64_tr_b16 v[118:119], v161 offset:6144\n\t" \
  "ds_read_b64_tr_b16 v[120:121], v162 offset:4096\n\t" \
  "ds_read_b64_tr_b16 v[122:123], v162 offset:6144\n\t" \
  "QH_%=:\n\t" \
  "v_add_u32 v166, %[go], v165\n\t" \
  "ds_read_b32 v166, v166\n\t" \
  "s_addk_i32 %[cn], 1\n\t" \
  "s_cmp_gt_u32 %[cn], 0x400000\n\t" \
  "s_cbranch_scc1 QI_%=\n\t" \
  "s_waitcnt lgkmcnt(0)\n\t" \
  "v_cmp_eq_u32 vcc, 0, v166\n\t" \
  "s_cbranch_vccz QI_%=\n\t" \
  "s_sleep 2\n\t" \
  "s_getpc_b64 s[92:93]\n\t" \
  "s_branch QC_%=\n\t" \
  "s_branch QH_%=\n\t" \
  "QI_%=:\n\t" \
  "s_setprio 0\n\t" \
  "ds_read_b32 v166, v165\n\t" \
  "s_waitcnt lgkmcnt(11)\n\t" \
  "v_mfma_f32_16x16x32_bf16 v[128:131], v[96:99], v[0:3], 0\n\t" \
  "ds_read_b64_tr_b16 v[124:125], v163 offset:4096\n\t" \
  "v_readlane_b32 s94, v167, 5\n\t" \
  "v_mfma_f32_16x16x32_bf16 v[144:147], v[96:99], v[4:7], 0\n\t" \
  "ds_read_b64_tr_b16 v[126:127], v163 offset:6144\n\t" \
  "s_waitcnt lgkmcnt(2)\n\t" \
  "v_cmp_lt_u32 vcc, %[th], v166\n\t" \
  "v_mfma_f32_16x16x32_bf16 v[132:135], v[100:103], v[0:3], 0\n\t" \
  "s_mov_b64 exec, vcc\n\t" \
  "buffer_load_dwordx4 v[80:83], v164, %[ob], s94 offen offset:0 sc1\n\t" \
  "s_mov_b64 exec, -1\n\t" \
  "v_mfma_f32_16x16x32_bf16 v[148:151], v[100:103], v[4:7], 0\n\t" \
  "ds_read_b64_tr_b16 v[96:97], v160 offset:8192\n\t" \
  "ds_read_b64_tr_b16 v[98:99], v160 offset:10240\n\t" \
  "v_mfma_f32_16x16x32_bf16 v[136:139], v[104:107], v[0:3], 0\n\t" \
  "s_mov_b64 exec, vcc\n\t" \
  "buffer_load_dwordx4 v[84:87], v164, %[ob], s94 offen offset:1024 sc1\n\t" \
  "s_mov_b64 exec, -1\n\t" \
  "v_mfma_f32_16x16x32_bf16 v[152:155], v[104:107], v[4:7], 0\n\t" \
  "ds_read_b64_tr_b16 v[100:101], v161 offset:8192\n\t" \
  "ds_read_b64_tr_b16 v[102:103], v161 offset:10240\n\t" \
  "v_mfma_f32_16x16x32_bf16 v[140:143], v[108:111], v[0:3], 0\n\t" \
  "s_mov_b64 exec, vcc\n\t" \
  "buffer_load_dwordx4 v[88:91], v164, %[ob], s94 offen offset:2048 sc1\n\t" \
  "s_mov_b64 exec, -1\n\t" \
  "v_mfma_f32_16x16x32_bf16 v[156:159], v[108:111], v[4:7], 0\n\t" \
  "ds_read_b64_tr_b16 v[104:105], v162 offset:8192\n\t" \
  "ds_read_b64_tr_b16 v[106:107], v162 offset:10240\n\t" \
  "v_mfma_f32_16x16x32_bf16 v[128:131], v[112:115], v[8:11], v[128:131]\n\t" \
  "s_mov_b64 exec, vcc\n\t" \
  "buffer_load_dwordx4 v[92:95], v164, %[ob], s94 offen offset:3072 sc1\n\t" \
  "s_mov_b64 exec, -1\n\t" \
  "v_mfma_f32_16x16x32_bf16 v[144:147], v[112:115], v[12:15], v[144:147]\n\t" \
  "ds_read_b64_tr_b16 v[108:109], v163 offset:8192\n\t" \
  "ds_read_b64_tr_b16 v[110:111], v163 offset:10240\n\t" \
  "v_mfma_f32_16x16x32_bf16 v[132:135], v[116:119], v[8:11], v[132:135]\n\t" \
  "ds_read_b64_tr_b16 v[112:113], v160 offset:12288\n\t" \
  "ds_read_b32 v166, v165\n\t" \
  "v_mfma_f32_16x16x32_bf16 v[148:151], v[116:119], v[12:15], v[148:151]\n\t" \
  "ds_read_b64_tr_b16 v[114:115], v160 offset:14336\n\t" \
  "s_waitcnt lgkmcnt(11)\n\t" \
  "v_mfma_f32_16x16x32_bf16 v[136:139], v[120:123], v[8:11], v[136:139]\n\t" \
  "ds_read_b64_tr_b16 v[116:117], v161 offset:12288\n\t" \
  "v_mfma_f32_16x16x32_bf16 v[152:155], v[120:123], v[12:15], v[152:155]\n\t" \
  "ds_read_b64_tr_b16 v[118:119], v161 offset:14336\n\t" \
  "s_sub_u32 %[iss], %[iss], vcc_lo\n\t" \
  "v_mfma_f32_16x16x32_bf16 v[140:143], v[124:127], v[8:11], v[140:143]\n\t" \
  "ds_read_b64_tr_b16 v[120:121], v162 offset:12288\n\t" \
  "v_mfma_f32_16x16x32_bf16 v[156:159], v[124:127], v[12:15], v[156:159]\n\t" \
  "ds_read_b64_tr_b16 v[122:123], v162 offset:14336\n\t" \
  "s_add_u32 %[th], %[th], 1\n\t" \
  "s_cbranch_vccz QF1_%=\n\t" \
  "s_waitcnt vmcnt(16)\n\t" \
  "QG1_%=:\n\t" \
  "s_waitcnt lgkmcnt(11)\n\t" \
  "v_mfma_f32_16x16x32_bf16 v[128:131], v[96:99], v[16:19], v[128:131]\n\t" \
  "ds_read_b64_tr_b16 v[124:125], v163 offset:12288\n\t" \
  "v_readlane_b32 s94, v167, 6\n\t" \
  "v_mfma_f32_16x16x32_bf16 v[144:147], v[96:99], v[20:23], v[144:147]\n\t" \
  "ds_read_b64_tr_b16 v[126:127], v163 offset:14336\n\t" \
  "s_waitcnt lgkmcnt(7)\n\t" \
  "v_cmp_lt_u32 vcc, %[th], v166\n\t" \
  "v_mfma_f32_16x16x32_bf16 v[132:135], v[100:103], v[16:19], v[132:135]\n\t" \
  "s_mov_b64 exec, vcc\n\t" \
  "buffer_load_dwordx4 v[0:3], v164, %[ob], s94 offen offset:0 sc1\n\t" \
  "s_mov_b64 exec, -1\n\t" \
  "v_mfma_f32_16x16x32_bf16 v[148:151], v[100:103], v[20:23], v[148:151]\n\t" \
  "ds_read_b64_tr_b16 v[96:97], v160 offset:16384\n\t" \
  "ds_read_b64_tr_b16 v[98:99], v160 offset:18432\n\t" \
  "v_mfma_f32_16x16x32_bf16 v[136:139], v[104:107], v[16:19], v[136:139]\n\t" \
  "s_mov_b64 exec, vcc\n\t" \
  "buffer_load_dwordx4 v[4:7], v164, %[ob], s94 offen offset:1024 sc1\n\t" \
  "s_mov_b64 exec, -1\n\t" \
  "v_mfma_f32_16x16x32_bf16 v[152:155], v[104:107], v[20:23], v[152:155]\n\t" \
  "ds_read_b64_tr_b16 v[100:101], v161 offset:16384\n\t" \
  "ds_read_b64_tr_b16 v[102:103], v161 offset:18432\n\t" \
  "v_mfma_f32_16x16x32_bf16 v[140:143], v[108:111], v[16:19], v[140:143]\n\t" \
  "s_mov_b64 exec, vcc\n\t" \
  "buffer_load_dwordx4 v[8:11], v164, %[ob], s94 offen offset:2048 sc1\n\t" \
  "s_mov_b64 exec, -1\n\t" \
  "v_mfma_f32_16x16x32_bf16 v[156:159], v[108:111], v[20:23], v[156:159]\n\t" \
  "ds_read_b64_tr_b16 v[104:105], v162 offset:16384\n\t" \
  "ds_read_b64_tr_b16 v[106:107], v162 offset:18432\n\t" \
  "s_waitcnt lgkmcnt(10)\n\t" \
  "v_mfma_f32_16x16x32_bf16 v[128:131], v[112:115], v[24:27], v[128:131]\n\t" \
  "s_mov_b64 exec, vcc\n\t" \
  "buffer_load_dwordx4 v[12:15], v164, %[ob], s94 offen offset:3072 sc1\n\t" \
  "s_mov_b64 exec, -1\n\t" \
  "v_mfma_f32_16x16x32_bf16 v[144:147], v[112:115], v[28:31], v[144:147]\n\t" \
  "ds_read_b64_tr_b16 v[108:109], v163 offset:16384\n\t" \
  "ds_read_b64_tr_b16 v[110:111], v163 offset:18432\n\t" \
  "v_mfma_f32_16x16x32_bf16 v[132:135], v[116:119], v[24:27], v[132:135]\n\t" \
  "ds_read_b64_tr_b16 v[112:113], v160 offset:20480\n\t" \
  "ds_read_b32 v166, v165\n\t" \
  "v_mfma_f32_16x16x32_bf16 v[148:151], v[116:119], v[28:31], v[148:151]\n\t" \
  "ds_read_b64_tr_b16 v[114:115], v160 offset:22528\n\t" \
  "s_waitcnt lgkmcnt(11)\n\t" \
  "v_mfma_f32_16x16x32_bf16 v[136:139], v[120:123], v[24:27], v[136:139]\n\t" \
  "ds_read_b64_tr_b16 v[116:117], v161 offset:20480\n\t" \
  "v_mfma_f32_16x16x32_bf16 v[152:155], v[120:123], v[28:31], v[152:155]\n\t" \
  "ds_read_b64_tr_b16 v[118:119], v161 offset:22528\n\t" \
  "s_sub_u32 %[iss], %[iss], vcc_lo\n\t" \
  "v_mfma_f32_16x16x32_bf16 v[140:143], v[124:127], v[24:27], v[140:143]\n\t" \
  "ds_read_b64_tr_b16 v[120:121], v162 offset:20480\n\t" \
  "v_mfma_f32_16x16x32_bf16 v[156:159], v[124:127], v[28:31], v[156:159]\n\t" \
  "ds_read_b64_tr_b16 v[122:123], v162 offset:22528\n\t" \
  "s_add_u32 %[th], %[th], 1\n\t" \
  "s_cbranch_vccz QF2_%=\n\t" \
  "s_waitcnt vmcnt(16)\n\t" \
  "QG2_%=:\n\t" \
  "s_waitcnt lgkmcnt(11)\n\t" \
  "v_mfma_f32_16x16x32_bf16 v[128:131], v[96:99], v[32:35], v[128:131]\n\t" \
  "ds_read_b64_tr_b16 v[124:125], v163 offset:20480\n\t" \
  "v_readlane_b32 s94, v167, 7\n\t" \
  "v_mfma_f32_16x16x32_bf16 v[144:147], v[96:99], v[36:39], v[144:147]\n\t" \
  "ds_read_b64_tr_b16 v[126:127], v163 offset:22528\n\t" \
  "s_waitcnt lgkmcnt(7)\n\t" \
  "v_cmp_lt_u32 vcc, %[th], v166\n\t" \
  "v_mfma_f32_16x16x32_bf16 v[132:135], v[100:103], v[32:35], v[132:135]\n\t" \
  "s_mov_b64 exec, vcc\n\t" \
  "buffer_load_dwordx4 v[16:19], v164, %[ob], s94 offen offset:0 sc1\n\t" \
  "s_mov_b64 exec, -1\n\t" \
  "v_mfma_f32_16x16x32_bf16 v[148:151], v[100:103], v[36:39], v[148:151]\n\t" \
  "ds_read_b64_tr_b16 v[96:97], v160 offset:24576\n\t" \
  "ds_read_b64_tr_b16 v[98:99], v160 offset:26624\n\t" \
  "v_mfma_f32_16x16x32_bf16 v[136:139], v[104:107], v[32:35], v[136:139]\n\t" \
  "s_mov_b64 exec, vcc\n\t" \
  "buffer_load_dwordx4 v[20:23], v164, %[ob], s94 offen offset:1024 sc1\n\t" \
  "s_mov_b64 exec, -1\n\t" \
  "v_mfma_f32_16x16x32_bf16 v[152:155], v[104:107], v[36:39], v[152:155]\n\t" \
  "ds_read_b64_tr_b16 v[100:101], v161 offset:24576\n\t" \
  "ds_read_b64_tr_b16 v[102:103], v161 offset:26624\n\t" \
  "v_mfma_f32_16x16x32_bf16 v[140:143], v[108:111], v[32:35], v[140:143]\n\t" \
  "s_mov_b64 exec, vcc\n\t" \
  "buffer_load_dwordx4 v[24:27], v164, %[ob], s94 offen offset:2048 sc1\n\t" \
  "s_mov_b64 exec, -1\n\t" \
  "v_mfma_f32_16x16x32_bf16 v[156:159], v[108:111], v[36:39], v[156:159]\n\t" \
  "ds_read_b64_tr_b16 v[104:105], v162 offset:24576\n\t" \
  "ds_read_b64_tr_b16 v[106:107], v162 offset:26624\n\t" \
  "s_waitcnt lgkmcnt(10)\n\t" \
  "v_mfma_f32_16x16x32_bf16 v[128:131], v[112:115], v[40:43], v[128:131]\n\t" \
  "s_mov_b64 exec, vcc\n\t" \
  "buffer_load_dwordx4 v[28:31], v164, %[ob], s94 offen offset:3072 sc1\n\t" \
  "s_mov_b64 exec, -1\n\t" \
  "v_mfma_f32_16x16x32_bf16 v[144:147], v[112:115], v[44:47], v[144:147]\n\t" \
  "ds_read_b64_tr_b16 v[108:109], v163 offset:24576\n\t" \
  "ds_read_b64_tr_b16 v[110:111], v163 offset:26624\n\t" \
  "v_mfma_f32_16x16x32_bf16 v[132:135], v[116:119], v[40:43], v[132:135]\n\t" \
  "ds_read_b64_tr_b16 v[112:113], v160 offset:28672\n\t" \
  "ds_read_b32 v166, v165\n\t" \
  "v_mfma_f32_16x16x32_bf16 v[148:151], v[116:119], v[44:47], v[148:151]\n\t" \
  "ds_read_b64_tr_b16 v[114:115], v160 offset:30720\n\t" \
  "s_waitcnt lgkmcnt(11)\n\t" \
  "v_mfma_f32_16x16x32_bf16 v[136:139], v[120:123], v[40:43], v[136:139]\n\t" \
  "ds_read_b64_tr_b16 v[116:117], v161 offset:28672\n\t" \
  "v_mfma_f32_16x16x32_bf16 v[152:155], v[120:123], v[44:47], v[152:155]\n\t" \
  "ds_read_b64_tr_b16 v[118:119], v161 offset:30720\n\t" \
  "s_sub_u32 %[iss], %[iss], vcc_lo\n\t" \
  "v_mfma_f32_16x16x32_bf16 v[140:143], v[124:127], v[40:43], v[140:143]\n\t" \
  "ds_read_b64_tr_b16 v[120:121], v162 offset:28672\n\t" \
  "v_mfma_f32_16x16x32_bf16 v[156:159], v[124:127], v[44:47], v[156:159]\n\t" \
  "ds_read_b64_tr_b16 v[122:123], v162 offset:30720\n\t" \
  "s_add_u32 %[th], %[th], 1\n\t" \
  "s_cbranch_vccz QF3_%=\n\t" \
  "s_waitcnt vmcnt(16)\n\t" \
  "QG3_%=:\n\t" \
  "s_waitcnt lgkmcnt(11)\n\t" \
  "v_mfma_f32_16x16x32_bf16 v[128:131], v[96:99], v[48:51], v[128:131]\n\t" \
  "ds_read_b64_tr_b16 v[124:125], v163 offset:28672\n\t" \
  "v_readlane_b32 s94, v167, 8\n\t" \
  "v_mfma_f32_16x16x32_bf16 v[144:147], v[96:99], v[52:55], v[144:147]\n\t" \
  "ds_read_b64_tr_b16 v[126:127], v163 offset:30720\n\t" \
  "s_waitcnt lgkmcnt(7)\n\t" \
  "v_cmp_lt_u32 vcc, %[th], v166\n\t" \
  "v_mfma_f32_16x16x32_bf16 v[132:135], v[100:103], v[48:51], v[132:135]\n\t" \
  "s_mov_b64 exec, vcc\n\t" \
  "buffer_load_dwordx4 v[32:35], v164, %[ob], s94 offen offset:0 sc1\n\t" \
  "s_mov_b64 exec, -1\n\t" \
  "v_mfma_f32_16x16x32_bf16 v[148:151], v[100:103], v[52:55], v[148:151]\n\t" \
  "ds_read_b64_tr_b16 v[96:97], v160 offset:32768\n\t" \
  "ds_read_b64_tr_b16 v[98:99], v160 offset:34816\n\t" \
  "v_mfma_f32_16x16x32_bf16 v[136:139], v[104:107], v[48:51], v[136:139]\n\t" \
  "s_mov_b64 exec, vcc\n\t" \
  "buffer_load_dwordx4 v[36:39], v164, %[ob], s94 offen offset:1024 sc1\n\t" \
  "s_mov_b64 exec, -1\n\t" \
  "v_mfma_f32_16x16x32_bf16 v[152:155], v[104:107], v[52:55], v[152:155]\n\t" \
  "ds_read_b64_tr_b16 v[100:101], v161 offset:32768\n\t" \
  "ds_read_b64_tr_b16 v[102:103], v161 offset:34816\n\t" \
  "v_mfma_f32_16x16x32_bf16 v[140:143], v[108:111], v[48:51], v[140:143]\n\t" \
  "s_mov_b64 exec, vcc\n\t" \
  "buffer_load_dwordx4 v[40:43], v164, %[ob], s94 offen offset:2048 sc1\n\t" \
  "s_mov_b64 exec, -1\n\t" \
  "v_mfma_f32_16x16x32_bf16 v[156:159], v[108:111], v[52:55], v[156:159]\n\t" \
  "ds_read_b64_tr_b16 v[104:105], v162 offset:32768\n\t" \
  "ds_read_b64_tr_b16 v[106:107], v162 offset:34816\n\t" \
  "s_waitcnt lgkmcnt(10)\n\t" \
  "v_mfma_f32_16x16x32_bf16 v[128:131], v[112:115], v[56:59], v[128:131]\n\t" \
  "s_mov_b64 exec, vcc\n\t" \
  "buffer_load_dwordx4 v[44:47], v164, %[ob], s94 offen offset:3072 sc1\n\t" \
  "s_mov_b64 exec, -1\n\t" \
  "v_mfma_f32_16x16x32_bf16 v[144:147], v[112:115], v[60:63], v[144:147]\n\t" \
  "ds_read_b64_tr_b16 v[108:109], v163 offset:32768\n\t" \
  "ds_read_b64_tr_b16 v[110:111], v163 offset:34816\n\t" \
  "v_mfma_f32_16x16x32_bf16 v[132:135], v[116:119], v[56:59], v[132:135]\n\t" \
  "ds_read_b64_tr_b16 v[112:113], v160 offset:36864\n\t" \
  "ds_read_b32 v166, v165\n\t" \
  "v_mfma_f32_16x16x32_bf16 v[148:151], v[116:119], v[60:63], v[148:151]\n\t" \
  "ds_read_b64_tr_b16 v[114:115], v160 offset:38912\n\t" \
  "s_waitcnt lgkmcnt(11)\n\t" \
  "v_mfma_f32_16x16x32_bf16 v[136:139], v[120:123], v[56:59], v[136:139]\n\t" \
  "ds_read_b64_tr_b16 v[116:117], v161 offset:36864\n\t" \
  "v_mfma_f32_16x16x32_bf16 v[152:155], v[120:123], v[60:63], v[152:155]\n\t" \
  "ds_read_b64_tr_b16 v[118:119], v161 offset:38912\n\t" \
  "s_sub_u32 %[iss], %[iss], vcc_lo\n\t" \
  "v_mfma_f32_16x16x32_bf16 v[140:143], v[124:127], v[56:59], v[140:143]\n\t" \
  "ds_read_b64_tr_b16 v[120:121], v162 offset:36864\n\t" \
  "v_mfma_f32_16x16x32_bf16 v[156:159], v[124:127], v[60:63], v[156:159]\n\t" \
  "ds_read_b64_tr_b16 v[122:123], v162 offset:38912\n\t" \
  "s_add_u32 %[th], %[th], 1\n\t" \
  "s_cbranch_vccz QF4_%=\n\t" \
  "s_waitcnt vmcnt(16)\n\t" \
  "QG4_%=:\n\t" \
  "s_waitcnt lgkmcnt(11)\n\t" \
  "v_mfma_f32_16x16x32_bf16 v[128:131], v[96:99], v[64:67], v[128:131]\n\t" \
  "ds_read_b64_tr_b16 v[124:125], v163 offset:36864\n\t" \
  "v_readlane_b32 s94, v167, 9\n\t" \
  "v_mfma_f32_16x16x32_bf16 v[144:147], v[96:99], v[68:71], v[144:147]\n\t" \
  "ds_read_b64_tr_b16 v[126:127], v163 offset:38912\n\t" \
  "s_waitcnt lgkmcnt(7)\n\t" \
  "v_cmp_lt_u32 vcc, %[th], v166\n\t" \
  "v_mfma_f32_16x16x32_bf16 v[132:135], v[100:103], v[64:67], v[132:135]\n\t" \
  "s_mov_b64 exec, vcc\n\t" \
  "buffer_load_dwordx4 v[48:51], v164, %[ob], s94 offen offset:0 sc1\n\t" \
  "s_mov_b64 exec, -1\n\t" \
  "v_mfma_f32_16x16x32_bf16 v[148:151], v[100:103], v[68:71], v[148:151]\n\t" \
  "ds_read_b64_tr_b16 v[96:97], v160 offset:40960\n\t" \
  "ds_read_b64_tr_b16 v[98:99], v160 offset:43008\n\t" \
  "v_mfma_f32_16x16x32_bf16 v[136:139], v[104:107], v[64:67], v[136:139]\n\t" \
  "s_mov_b64 exec, vcc\n\t" \
  "buffer_load_dwordx4 v[52:55], v164, %[ob], s94 offen offset:1024 sc1\n\t" \
  "s_mov_b64 exec, -1\n\t" \
  "v_mfma_f32_16x16x32_bf16 v[152:155], v[104:107], v[68:71], v[152:155]\n\t" \
  "ds_read_b64_tr_b16 v[100:101], v161 offset:40960\n\t" \
  "ds_read_b64_tr_b16 v[102:103], v161 offset:43008\n\t" \
  "v_mfma_f32_16x16x32_bf16 v[140:143], v[108:111], v[64:67], v[140:143]\n\t" \
  "s_mov_b64 exec, vcc\n\t" \
  "buffer_load_dwordx4 v[56:59], v164, %[ob], s94 offen offset:2048 sc1\n\t" \
  "s_mov_b64 exec, -1\n\t" \
  "v_mfma_f32_16x16x32_bf16 v[156:159], v[108:111], v[68:71], v[156:159]\n\t" \
  "ds_read_b64_tr_b16 v[104:105], v162 offset:40960\n\t" \
  "ds_read_b64_tr_b16 v[106:107], v162 offset:43008\n\t" \
  "s_waitcnt lgkmcnt(10)\n\t" \
  "v_mfma_f32_16x16x32_bf16 v[128:131], v[112:115], v[72:75], v[128:131]\n\t" \
  "s_mov_b64 exec, vcc\n\t" \
  "buffer_load_dwordx4 v[60:63], v164, %[ob], s94 offen offset:3072 sc1\n\t" \
  "s_mov_b64 exec, -1\n\t" \
  "v_mfma_f32_16x16x32_bf16 v[144:147], v[112:115], v[76:79], v[144:147]\n\t" \
  "ds_read_b64_tr_b16 v[108:109], v163 offset:40960\n\t" \
  "ds_read_b64_tr_b16 v[110:111], v163 offset:43008\n\t" \
  "v_mfma_f32_16x16x32_bf16 v[132:135], v[116:119], v[72:75], v[132:135]\n\t" \
  "ds_read_b64_tr_b16 v[112:113], v160 offset:45056\n\t" \
  "ds_read_b32 v166, v165\n\t" \
  "v_mfma_f32_16x16x32_bf16 v[148:151], v[116:119], v[76:79], v[148:151]\n\t" \
  "ds_read_b64_tr_b16 v[114:115], v160 offset:47104\n\t" \
  "s_waitcnt lgkmcnt(11)\n\t" \
  "v_mfma_f32_16x16x32_bf16 v[136:139], v[120:123], v[72:75], v[136:139]\n\t" \
  "ds_read_b64_tr_b16 v[116:117], v161 offset:45056\n\t" \
  "v_mfma_f32_16x16x32_bf16 v[152:155], v[120:123], v[76:79], v[152:155]\n\t" \
  "ds_read_b64_tr_b16 v[118:119], v161 offset:47104\n\t" \
  "s_sub_u32 %[iss], %[iss], vcc_lo\n\t" \
  "v_mfma_f32_16x16x32_bf16 v[140:143], v[124:127], v[72:75], v[140:143]\n\t" \
  "ds_read_b64_tr_b16 v[120:121], v162 offset:45056\n\t" \
  "v_mfma_f32_16x16x32_bf16 v[156:159], v[124:127], v[76:79], v[156:159]\n\t" \
  "ds_read_b64_tr_b16 v[122:123], v162 offset:47104\n\t" \
  "s_add_u32 %[th], %[th], 1\n\t" \
  "s_cbranch_vccz QF5_%=\n\t" \
  "s_waitcnt vmcnt(16)\n\t" \
  "QG5_%=:\n\t" \
  "s_waitcnt lgkmcnt(11)\n\t" \
  "v_mfma_f32_16x16x32_bf16 v[128:131], v[96:99], v[80:83], v[128:131]\n\t" \
  "ds_read_b64_tr_b16 v[124:125], v163 offset:45056\n\t" \
  "v_readlane_b32 s94, v167, 10\n\t" \
  "v_mfma_f32_16x16x32_bf16 v[144:147], v[96:99], v[84:87], v[144:147]\n\t" \
  "ds_read_b64_tr_b16 v[126:127], v163 offset:47104\n\t" \
  "s_waitcnt lgkmcnt(7)\n\t" \
  "v_cmp_lt_u32 vcc, %[th], v166\n\t" \
  "v_mfma_f32_16x16x32_bf16 v[132:135], v[100:103], v[80:83], v[132:135]\n\t" \
  "s_mov_b64 exec, vcc\n\t" \
  "buffer_load_dwordx4 v[64:67], v164, %[ob], s94 offen offset:0 sc1\n\t" \
  "s_mov_b64 exec, -1\n\t" \
  "v_mfma_f32_16x16x32_bf16 v[148:151], v[100:103], v[84:87], v[148:151]\n\t" \
  "ds_read_b64_tr_b16 v[96:97], v160 offset:49152\n\t" \
  "ds_read_b64_tr_b16 v[98:99], v160 offset:51200\n\t" \
  "v_mfma_f32_16x16x32_bf16 v[136:139], v[104:107], v[80:83], v[136:139]\n\t" \
  "s_mov_b64 exec, vcc\n\t" \
  "buffer_load_dwordx4 v[68:71], v164, %[ob], s94 offen offset:1024 sc1\n\t" \
  "s_mov_b64 exec, -1\n\t" \
  "v_mfma_f32_16x16x32_bf16 v[152:155], v[104:107], v[84:87], v[152:155]\n\t" \
  "ds_read_b64_tr_b16 v[100:101], v161 offset:49152\n\t" \
  "ds_read_b64_tr_b16 v[102:103], v161 offset:51200\n\t" \
  "v_mfma_f32_16x16x32_bf16 v[140:143], v[108:111], v[80:83], v[140:143]\n\t" \
  "s_mov_b64 exec, vcc\n\t" \
  "buffer_load_dwordx4 v[72:75], v164, %[ob], s94 offen offset:2048 sc1\n\t" \
  "s_mov_b64 exec, -1\n\t" \
  "v_mfma_f32_16x16x32_bf16 v[156:159], v[108:111], v[84:87], v[156:159]\n\t" \
  "ds_read_b64_tr_b16 v[104:105], v162 offset:49152\n\t" \
  "ds_read_b64_tr_b16 v[106:107], v162 offset:51200\n\t" \
  "s_waitcnt lgkmcnt(10)\n\t" \
  "v_mfma_f32_16x16x32_bf16 v[128:131], v[112:115], v[88:91], v[128:131]\n\t" \
  "s_mov_b64 exec, vcc\n\t" \
  "buffer_load_dwordx4 v[76:79], v164, %[ob], s94 offen offset:3072 sc1\n\t" \
  "s_mov_b64 exec, -1\n\t" \
  "v_mfma_f32_16x16x32_bf16 v[144:147], v[112:115], v[92:95], v[144:147]\n\t" \
  "ds_read_b64_tr_b16 v[108:109], v163 offset:49152\n\t" \
  "ds_read_b64_tr_b16 v[110:111], v163 offset:51200\n\t" \
  "v_mfma_f32_16x16x32_bf16 v[132:135], v[116:119], v[88:91], v[132:135]\n\t" \
  "ds_read_b64_tr_b16 v[112:113], v160 offset:53248\n\t" \
  "ds_read_b32 v166, v165\n\t" \
  "v_mfma_f32_16x16x32_bf16 v[148:151], v[116:119], v[92:95], v[148:151]\n\t" \
  "ds_read_b64_tr_b16 v[114:115], v160 offset:55296\n\t" \
  "s_waitcnt lgkmcnt(11)\n\t" \
  "v_mfma_f32_16x16x32_bf16 v[136:139], v[120:123], v[88:91], v[136:139]\n\t" \
  "ds_read_b64_tr_b16 v[116:117], v161 offset:53248\n\t" \
  "v_mfma_f32_16x16x32_bf16 v[152:155], v[120:123], v[92:95], v[152:155]\n\t" \
  "ds_read_b64_tr_b16 v[118:119], v161 offset:55296\n\t" \
  "s_sub_u32 %[iss], %[iss], vcc_lo\n\t" \
  "v_mfma_f32_16x16x32_bf16 v[140:143], v[124:127], v[88:91], v[140:143]\n\t" \
  "ds_read_b64_tr_b16 v[120:121], v162 offset:53248\n\t" \
  "v_mfma_f32_16x16x32_bf16 v[156:159], v[124:127], v[92:95], v[156:159]\n\t" \
  "ds_read_b64_tr_b16 v[122:123], v162 offset:55296\n\t" \
  "s_add_u32 %[th], %[th], 1\n\t" \
  "s_cbranch_vccz QF6_%=\n\t" \
  "s_waitcnt vmcnt(16)\n\t" \
  "QG6_%=:\n\t" \
  "s_waitcnt lgkmcnt(11)\n\t" \
  "v_mfma_f32_16x16x32_bf16 v[128:131], v[96:99], v[0:3], v[128:131]\n\t" \
  "ds_read_b64_tr_b16 v[124:125], v163 offset:53248\n\t" \
  "v_readlane_b32 s94, v167, 11\n\t" \
  "v_mfma_f32_16x16x32_bf16 v[144:147], v[96:99], v[4:7], v[144:147]\n\t" \
  "ds_read_b64_tr_b16 v[126:127], v163 offset:55296\n\t" \
  "s_waitcnt lgkmcnt(7)\n\t" \
  "v_cmp_lt_u32 vcc, %[th], v166\n\t" \
  "v_mfma_f32_16x16x32_bf16 v[132:135], v[100:103], v[0:3], v[132:135]\n\t" \
  "s_mov_b64 exec, vcc\n\t" \
  "buffer_load_dwordx4 v[80:83], v164, %[ob], s94 offen offset:0 sc1\n\t" \
  "s_mov_b64 exec, -1\n\t" \
  "v_mfma_f32_16x16x32_bf16 v[148:151], v[100:103], v[4:7], v[148:151]\n\t" \
  "ds_read_b64_tr_b16 v[96:97], v160 offset:57344\n\t" \
  "ds_read_b64_tr_b16 v[98:99], v160 offset:59392\n\t" \
  "v_mfma_f32_16x16x32_bf16 v[136:139], v[104:107], v[0:3], v[136:139]\n\t" \
  "s_mov_b64 exec, vcc\n\t" \
  "buffer_load_dwordx4 v[84:87], v164, %[ob], s94 offen offset:1024 sc1\n\t" \
  "s_mov_b64 exec, -1\n\t" \
  "v_mfma_f32_16x16x32_bf16 v[152:155], v[104:107], v[4:7], v[152:155]\n\t" \
  "ds_read_b64_tr_b16 v[100:101], v161 offset:57344\n\t" \
  "ds_read_b64_tr_b16 v[102:103], v161 offset:59392\n\t" \
  "v_mfma_f32_16x16x32_bf16 v[140:143], v[108:111], v[0:3], v[140:143]\n\t" \
  "s_mov_b64 exec, vcc\n\t" \
  "buffer_load_dwordx4 v[88:91], v164, %[ob], s94 offen offset:2048 sc1\n\t" \
  "s_mov_b64 exec, -1\n\t" \
  "v_mfma_f32_16x16x32_bf16 v[156:159], v[108:111], v[4:7], v[156:159]\n\t" \
  "ds_read_b64_tr_b16 v[104:105], v162 offset:57344\n\t" \
  "ds_read_b64_tr_b16 v[106:107], v162 offset:59392\n\t" \
  "s_waitcnt lgkmcnt(10)\n\t" \
  "v_mfma_f32_16x16x32_bf16 v[128:131], v[112:115], v[8:11], v[128:131]\n\t" \
  "s_mov_b64 exec, vcc\n\t" \
  "buffer_load_dwordx4 v[92:95], v164, %[ob], s94 offen offset:3072 sc1\n\t" \
  "s_mov_b64 exec, -1\n\t" \
  "v_mfma_f32_16x16x32_bf16 v[144:147], v[112:115], v[12:15], v[144:147]\n\t" \
  "ds_read_b64_tr_b16 v[108:109], v163 offset:57344\n\t" \
  "ds_read_b64_tr_b16 v[110:111], v163 offset:59392\n\t" \
  "v_mfma_f32_16x16x32_bf16 v[132:135], v[116:119], v[8:11], v[132:135]\n\t" \
  "ds_read_b64_tr_b16 v[112:113], v160 offset:61440\n\t" \
  "ds_read_b32 v166, v165\n\t" \
  "v_mfma_f32_16x16x32_bf16 v[148:151], v[116:119], v[12:15], v[148:151]\n\t" \
  "ds_read_b64_tr_b16 v[114:115], v160 offset:63488\n\t" \
  "s_waitcnt lgkmcnt(11)\n\t" \
  "v_mfma_f32_16x16x32_bf16 v[136:139], v[120:123], v[8:11], v[136:139]\n\t" \
  "ds_read_b64_tr_b16 v[116:117], v161 offset:61440\n\t" \
  "v_mfma_f32_16x16x32_bf16 v[152:155], v[120:123], v[12:15], v[152:155]\n\t" \
  "ds_read_b64_tr_b16 v[118:119], v161 offset:63488\n\t" \
  "s_sub_u32 %[iss], %[iss], vcc_lo\n\t" \
  "v_mfma_f32_16x16x32_bf16 v[140:143], v[124:127], v[8:11], v[140:143]\n\t" \
  "ds_read_b64_tr_b16 v[120:121], v162 offset:61440\n\t" \
  "s_mov_b64 exec, 1\n\t" \
  "ds_write_b32 v165, v165 offset:76\n\t" \
  "s_mov_b64 exec, -1\n\t" \
  "v_mfma_f32_16x16x32_bf16 v[156:159], v[124:127], v[12:15], v[156:159]\n\t" \
  "ds_read_b64_tr_b16 v[122:123], v162 offset:63488\n\t" \
  "s_add_u32 %[th], %[th], 1\n\t" \
  "s_cbranch_vccz QF7_%=\n\t" \
  "s_waitcnt vmcnt(16)\n\t" \
  "QG7_%=:\n\t" \
  "s_waitcnt lgkmcnt(12)\n\t" \
  "v_mfma_f32_16x16x32_bf16 v[128:131], v[96:99], v[16:19], v[128:131]\n\t" \
  "ds_read_b64_tr_b16 v[124:125], v163 offset:61440\n\t" \
  "v_readlane_b32 s94, v167, 12\n\t" \
  "v_mfma_f32_16x16x32_bf16 v[144:147], v[96:99], v[20:23], v[144:147]\n\t" \
  "ds_read_b64_tr_b16 v[126:127], v163 offset:63488\n\t" \
  "s_waitcnt lgkmcnt(8)\n\t" \
  "v_cmp_lt_u32 vcc, %[th], v166\n\t" \
  "v_mfma_f32_16x16x32_bf16 v[132:135], v[100:103], v[16:19], v[132:135]\n\t" \
  "v_add_u32 v160, 0x10000, v160\n\t" \
  "s_mov_b64 exec, vcc\n\t" \
  "buffer_load_dwordx4 v[0:3], v164, %[ob], s94 offen offset:0 sc1\n\t" \
  "s_mov_b64 exec, -1\n\t" \
  "v_mfma_f32_16x16x32_bf16 v[148:151], v[100:103], v[20:23], v[148:151]\n\t" \
  "ds_read_b64_tr_b16 v[96:97], v160 offset:0\n\t" \
  "ds_read_b64_tr_b16 v[98:99], v160 offset:2048\n\t" \
  "v_mfma_f32_16x16x32_bf16 v[136:139], v[104:107], v[16:19], v[136:139]\n\t" \
  "v_add_u32 v161, 0x10000, v161\n\t" \
  "s_mov_b64 exec, vcc\n\t" \
  "buffer_load_dwordx4 v[4:7], v164, %[ob], s94 offen offset:1024 sc1\n\t" \
  "s_mov_b64 exec, -1\n\t" \
  "v_mfma_f32_16x16x32_bf16 v[152:155], v[104:107], v[20:23], v[152:155]\n\t" \
  "ds_read_b64_tr_b16 v[100:101], v161 offset:0\n\t" \
  "ds_read_b64_tr_b16 v[102:103], v161 offset:2048\n\t" \
  "v_mfma_f32_16x16x32_bf16 v[140:143], v[108:111], v[16:19], v[140:143]\n\t" \
  "v_add_u32 v162, 0x10000, v162\n\t" \
  "s_mov_b64 exec, vcc\n\t" \
  "buffer_load_dwordx4 v[8:11], v164, %[ob], s94 offen offset:2048 sc1\n\t" \
  "s_mov_b64 exec, -1\n\t" \
  "v_mfma_f32_16x16x32_bf16 v[156:159], v[108:111], v[20:23], v[156:159]\n\t" \
  "ds_read_b64_tr_b16 v[104:105], v162 offset:0\n\t" \
  "ds_read_b64_tr_b16 v[106:107], v162 offset:2048\n\t" \
  "s_waitcnt lgkmcnt(11)\n\t" \
  "v_mfma_f32_16x16x32_bf16 v[128:131], v[112:115], v[24:27], v[128:131]\n\t" \
  "v_add_u32 v163, 0x10000, v163\n\t" \
  "s_mov_b64 exec, vcc\n\t" \
  "buffer_load_dwordx4 v[12:15], v164, %[ob], s94 offen offset:3072 sc1\n\t" \
  "s_mov_b64 exec, -1\n\t" \
  "v_mfma_f32_16x16x32_bf16 v[144:147], v[112:115], v[28:31], v[144:147]\n\t" \
  "ds_read_b64_tr_b16 v[108:109], v163 offset:0\n\t" \
  "ds_read_b64_tr_b16 v[110:111], v163 offset:2048\n\t" \
  "v_mfma_f32_16x16x32_bf16 v[132:135], v[116:119], v[24:27], v[132:135]\n\t" \
  "ds_read_b64_tr_b16 v[112:113], v160 offset:4096\n\t" \
  "ds_read_b32 v166, v165\n\t" \
  "v_mfma_f32_16x16x32_bf16 v[148:151], v[116:119], v[28:31], v[148:151]\n\t" \
  "ds_read_b64_tr_b16 v[114:115], v160 offset:6144\n\t" \
  "s_waitcnt lgkmcnt(11)\n\t" \
  "v_mfma_f32_16x16x32_bf16 v[136:139], v[120:123], v[24:27], v[136:139]\n\t" \
  "ds_read_b64_tr_b16 v[116:117], v161 offset:4096\n\t" \
  "v_mfma_f32_16x16x32_bf16 v[152:155], v[120:123], v[28:31], v[152:155]\n\t" \
  "ds_read_b64_tr_b16 v[118:119], v161 offset:6144\n\t" \
  "s_sub_u32 %[iss], %[iss], vcc_lo\n\t" \
  "v_mfma_f32_16x16x32_bf16 v[140:143], v[124:127], v[24:27], v[140:143]\n\t" \
  "ds_read_b64_tr_b16 v[120:121], v162 offset:4096\n\t" \
  "v_mfma_f32_16x16x32_bf16 v[156:159], v[124:127], v[28:31], v[156:159]\n\t" \
  "ds_read_b64_tr_b16 v[122:123], v162 offset:6144\n\t" \
  "s_add_u32 %[th], %[th], 1\n\t" \
  "s_cbranch_vccz QF8_%=\n\t" \
  "s_waitcnt vmcnt(16)\n\t" \
  "QG8_%=:\n\t" \
  "s_waitcnt lgkmcnt(11)\n\t" \
  "v_mfma_f32_16x16x32_bf16 v[128:131], v[96:99], v[32:35], v[128:131]\n\t" \
  "ds_read_b64_tr_b16 v[124:125], v163 offset:4096\n\t" \
  "v_readlane_b32 s94, v167, 13\n\t" \
  "v_mfma_f32_16x16x32_bf16 v[144:147], v[96:99], v[36:39], v[144:147]\n\t" \
  "ds_read_b64_tr_b16 v[126:127], v163 offset:6144\n\t" \
  "s_waitcnt lgkmcnt(7)\n\t" \
  "v_cmp_lt_u32 vcc, %[th], v166\n\t" \
  "v_mfma_f32_16x16x32_bf16 v[132:135], v[100:103], v[32:35], v[132:135]\n\t" \
  "s_mov_b64 exec, vcc\n\t" \
  "buffer_load_dwordx4 v[16:19], v164, %[ob], s94 offen offset:0 sc1\n\t" \
  "s_mov_b64 exec, -1\n\t" \
  "v_mfma_f32_16x16x32_bf16 v[148:151], v[100:103], v[36:39], v[148:151]\n\t" \
  "ds_read_b64_tr_b16 v[96:97], v160 offset:8192\n\t" \
  "ds_read_b64_tr_b16 v[98:99], v160 offset:10240\n\t" \
  "v_mfma_f32_16x16x32_bf16 v[136:139], v[104:107], v[32:35], v[136:139]\n\t" \
  "s_mov_b64 exec, vcc\n\t" \
  "buffer_load_dwordx4 v[20:23], v164, %[ob], s94 offen offset:1024 sc1\n\t" \
  "s_mov_b64 exec, -1\n\t" \
  "v_mfma_f32_16x16x32_bf16 v[152:155], v[104:107], v[36:39], v[152:155]\n\t" \
  "ds_read_b64_tr_b16 v[100:101], v161 offset:8192\n\t" \
  "ds_read_b64_tr_b16 v[102:103], v161 offset:10240\n\t" \
  "v_mfma_f32_16x16x32_bf16 v[140:143], v[108:111], v[32:35], v[140:143]\n\t" \
  "s_mov_b64 exec, vcc\n\t" \
  "buffer_load_dwordx4 v[24:27], v164, %[ob], s94 offen offset:2048 sc1\n\t" \
  "s_mov_b64 exec, -1\n\t" \
  "v_mfma_f32_16x16x32_bf16 v[156:159], v[108:111], v[36:39], v[156:159]\n\t" \
  "ds_read_b64_tr_b16 v[104:105], v162 offset:8192\n\t" \
  "ds_read_b64_tr_b16 v[106:107], v162 offset:10240\n\t" \
  "s_waitcnt lgkmcnt(10)\n\t" \
  "v_mfma_f32_16x16x32_bf16 v[128:131], v[112:115], v[40:43], v[128:131]\n\t" \
  "s_mov_b64 exec, vcc\n\t" \
  "buffer_load_dwordx4 v[28:31], v164, %[ob], s94 offen offset:3072 sc1\n\t" \
  "s_mov_b64 exec, -1\n\t" \
  "v_mfma_f32_16x16x32_bf16 v[144:147], v[112:115], v[44:47], v[144:147]\n\t" \
  "ds_read_b64_tr_b16 v[108:109], v163 offset:8192\n\t" \
  "ds_read_b64_tr_b16 v[110:111], v163 offset:10240\n\t" \
  "v_mfma_f32_16x16x32_bf16 v[132:135], v[116:119], v[40:43], v[132:135]\n\t" \
  "ds_read_b64_tr_b16 v[112:113], v160 offset:12288\n\t" \
  "ds_read_b32 v166, v165\n\t" \
  "v_mfma_f32_16x16x32_bf16 v[148:151], v[116:119], v[44:47], v[148:151]\n\t" \
  "ds_read_b64_tr_b16 v[114:115], v160 offset:14336\n\t" \
  "s_waitcnt lgkmcnt(11)\n\t" \
  "v_mfma_f32_16x16x32_bf16 v[136:139], v[120:123], v[40:43], v[136:139]\n\t" \
  "ds_read_b64_tr_b16 v[116:117], v161 offset:12288\n\t" \
  "v_mfma_f32_16x16x32_bf16 v[152:155], v[120:123], v[44:47], v[152:155]\n\t" \
  "ds_read_b64_tr_b16 v[118:119], v161 offset:14336\n\t" \
  "s_sub_u32 %[iss], %[iss], vcc_lo\n\t" \
  "v_mfma_f32_16x16x32_bf16 v[140:143], v[124:127], v[40:43], v[140:143]\n\t" \
  "ds_read_b64_tr_b16 v[120:121], v162 offset:12288\n\t" \
  "v_mfma_f32_16x16x32_bf16 v[156:159], v[124:127], v[44:47], v[156:159]\n\t" \
  "ds_read_b64_tr_b16 v[122:123], v162 offset:14336\n\t" \
  "s_add_u32 %[th], %[th], 1\n\t" \
  "s_cbranch_vccz QF9_%=\n\t" \
  "s_waitcnt vmcnt(16)\n\t" \
  "QG9_%=:\n\t" \
  "s_waitcnt lgkmcnt(11)\n\t" \
  "v_mfma_f32_16x16x32_bf16 v[128:131], v[96:99], v[48:51], v[128:131]\n\t" \
  "ds_read_b64_tr_b16 v[124:125], v163 offset:12288\n\t" \
  "v_readlane_b32 s94, v167, 14\n\t" \
  "v_mfma_f32_16x16x32_bf16 v[144:147], v[96:99], v[52:55], v[144:147]\n\t" \
  "ds_read_b64_tr_b16 v[126:127], v163 offset:14336\n\t" \
  "s_waitcnt lgkmcnt(7)\n\t" \
  "v_cmp_lt_u32 vcc, %[th], v166\n\t" \
  "v_mfma_f32_16x16x32_bf16 v[132:135], v[100:103], v[48:51], v[132:135]\n\t" \
  "s_mov_b64 exec, vcc\n\t" \
  "buffer_load_dwordx4 v[32:35], v164, %[ob], s94 offen offset:0 sc1\n\t" \
  "s_mov_b64 exec, -1\n\t" \
  "v_mfma_f32_16x16x32_bf16 v[148:151], v[100:103], v[52:55], v[148:151]\n\t" \
  "ds_read_b64_tr_b16 v[96:97], v160 offset:16384\n\t" \
  "ds_read_b64_tr_b16 v[98:99], v160 offset:18432\n\t" \
  "v_mfma_f32_16x16x32_bf16 v[136:139], v[104:107], v[48:51], v[136:139]\n\t" \
  "s_mov_b64 exec, vcc\n\t" \
  "buffer_load_dwordx4 v[36:39], v164, %[ob], s94 offen offset:1024 sc1\n\t" \
  "s_mov_b64 exec, -1\n\t" \
  "v_mfma_f32_16x16x32_bf16 v[152:155], v[104:107], v[52:55], v[152:155]\n\t" \
  "ds_read_b64_tr_b16 v[100:101], v161 offset:16384\n\t" \
  "ds_read_b64_tr_b16 v[102:103], v161 offset:18432\n\t" \
  "v_mfma_f32_16x16x32_bf16 v[140:143], v[108:111], v[48:51], v[140:143]\n\t" \
  "s_mov_b64 exec, vcc\n\t" \
  "buffer_load_dwordx4 v[40:43], v164, %[ob], s94 offen offset:2048 sc1\n\t" \
  "s_mov_b64 exec, -1\n\t" \
  "v_mfma_f32_16x16x32_bf16 v[156:159], v[108:111], v[52:55], v[156:159]\n\t" \
  "ds_read_b64_tr_b16 v[104:105], v162 offset:16384\n\t" \
  "ds_read_b64_tr_b16 v[106:107], v162 offset:18432\n\t" \
  "s_waitcnt lgkmcnt(10)\n\t" \
  "v_mfma_f32_16x16x32_bf16 v[128:131], v[112:115], v[56:59], v[128:131]\n\t" \
  "s_mov_b64 exec, vcc\n\t" \
  "buffer_load_dwordx4 v[44:47], v164, %[ob], s94 offen offset:3072 sc1\n\t" \
  "s_mov_b64 exec, -1\n\t" \
  "v_mfma_f32_16x16x32_bf16 v[144:147], v[112:115], v[60:63], v[144:147]\n\t" \
  "ds_read_b64_tr_b16 v[108:109], v163 offset:16384\n\t" \
  "ds_read_b64_tr_b16 v[110:111], v163 offset:18432\n\t" \
  "v_mfma_f32_16x16x32_bf16 v[132:135], v[116:119], v[56:59], v[132:135]\n\t" \
  "ds_read_b64_tr_b16 v[112:113], v160 offset:20480\n\t" \
  "ds_read_b32 v166, v165\n\t" \
  "v_mfma_f32_16x16x32_bf16 v[148:151], v[116:119], v[60:63], v[148:151]\n\t" \
  "ds_read_b64_tr_b16 v[114:115], v160 offset:22528\n\t" \
  "s_waitcnt lgkmcnt(11)\n\t" \
  "v_mfma_f32_16x16x32_bf16 v[136:139], v[120:123], v[56:59], v[136:139]\n\t" \
  "ds_read_b64_tr_b16 v[116:117], v161 offset:20480\n\t" \
  "v_mfma_f32_16x16x32_bf16 v[152:155], v[120:123], v[60:63], v[152:155]\n\t" \
  "ds_read_b64_tr_b16 v[118:119], v161 offset:22528\n\t" \
  "s_sub_u32 %[iss], %[iss], vcc_lo\n\t" \
  "v_mfma_f32_16x16x32_bf16 v[140:143], v[124:127], v[56:59], v[140:143]\n\t" \
  "ds_read_b64_tr_b16 v[120:121], v162 offset:20480\n\t" \
  "v_mfma_f32_16x16x32_bf16 v[156:159], v[124:127], v[60:63], v[156:159]\n\t" \
  "ds_read_b64_tr_b16 v[122:123], v162 offset:22528\n\t" \
  "s_add_u32 %[th], %[th], 1\n\t" \
  "s_cbranch_vccz QF10_%=\n\t" \
  "s_waitcnt vmcnt(16)\n\t" \
  "QG10_%=:\n\t" \
  "s_waitcnt lgkmcnt(11)\n\t" \
  "v_mfma_f32_16x16x32_bf16 v[128:131], v[96:99], v[64:67], v[128:131]\n\t" \
  "ds_read_b64_tr_b16 v[124:125], v163 offset:20480\n\t" \
  "v_readlane_b32 s94, v167, 15\n\t" \
  "v_mfma_f32_16x16x32_bf16 v[144:147], v[96:99], v[68:71], v[144:147]\n\t" \
  "ds_read_b64_tr_b16 v[126:127], v163 offset:22528\n\t" \
  "s_waitcnt lgkmcnt(7)\n\t" \
  "v_cmp_lt_u32 vcc, %[th], v166\n\t" \
  "v_mfma_f32_16x16x32_bf16 v[132:135], v[100:103], v[64:67], v[132:135]\n\t" \
  "s_mov_b64 exec, vcc\n\t" \
  "buffer_load_dwordx4 v[48:51], v164, %[ob], s94 offen offset:0 sc1\n\t" \
  "s_mov_b64 exec, -1\n\t" \
  "v_mfma_f32_16x16x32_bf16 v[148:151], v[100:103], v[68:71], v[148:151]\n\t" \
  "ds_read_b64_tr_b16 v[96:97], v160 offset:24576\n\t" \
  "ds_read_b64_tr_b16 v[98:99], v160 offset:26624\n\t" \
  "v_mfma_f32_16x16x32_bf16 v[136:139], v[104:107], v[64:67], v[136:139]\n\t" \
  "s_mov_b64 exec, vcc\n\t" \
  "buffer_load_dwordx4 v[52:55], v164, %[ob], s94 offen offset:1024 sc1\n\t" \
  "s_mov_b64 exec, -1\n\t" \
  "v_mfma_f32_16x16x32_bf16 v[152:155], v[104:107], v[68:71], v[152:155]\n\t" \
  "ds_read_b64_tr_b16 v[100:101], v161 offset:24576\n\t" \
  "ds_read_b64_tr_b16 v[102:103], v161 offset:26624\n\t" \
  "v_mfma_f32_16x16x32_bf16 v[140:143], v[108:111], v[64:67], v[140:143]\n\t" \
  "s_mov_b64 exec, vcc\n\t" \
  "buffer_load_dwordx4 v[56:59], v164, %[ob], s94 offen offset:2048 sc1\n\t" \
  "s_mov_b64 exec, -1\n\t" \
  "v_mfma_f32_16x16x32_bf16 v[156:159], v[108:111], v[68:71], v[156:159]\n\t" \
  "ds_read_b64_tr_b16 v[104:105], v162 offset:24576\n\t" \
  "ds_read_b64_tr_b16 v[106:107], v162 offset:26624\n\t" \
  "s_waitcnt lgkmcnt(10)\n\t" \
  "v_mfma_f32_16x16x32_bf16 v[128:131], v[112:115], v[72:75], v[128:131]\n\t" \
  "s_mov_b64 exec, vcc\n\t" \
  "buffer_load_dwordx4 v[60:63], v164, %[ob], s94 offen offset:3072 sc1\n\t" \
  "s_mov_b64 exec, -1\n\t" \
  "v_mfma_f32_16x16x32_bf16 v[144:147], v[112:115], v[76:79], v[144:147]\n\t" \
  "ds_read_b64_tr_b16 v[108:109], v163 offset:24576\n\t" \
  "ds_read_b64_tr_b16 v[110:111], v163 offset:26624\n\t" \
  "v_mfma_f32_16x16x32_bf16 v[132:135], v[116:119], v[72:75], v[132:135]\n\t" \
  "ds_read_b64_tr_b16 v[112:113], v160 offset:28672\n\t" \
  "v_mfma_f32_16x16x32_bf16 v[148:151], v[116:119], v[76:79], v[148:151]\n\t" \
  "ds_read_b64_tr_b16 v[114:115], v160 offset:30720\n\t" \
  "s_waitcnt lgkmcnt(10)\n\t" \
  "v_mfma_f32_16x16x32_bf16 v[136:139], v[120:123], v[72:75], v[136:139]\n\t" \
  "ds_read_b64_tr_b16 v[116:117], v161 offset:28672\n\t" \
  "v_mfma_f32_16x16x32_bf16 v[152:155], v[120:123], v[76:79], v[152:155]\n\t" \
  "ds_read_b64_tr_b16 v[118:119], v161 offset:30720\n\t" \
  "s_sub_u32 %[iss], %[iss], vcc_lo\n\t" \
  "v_mfma_f32_16x16x32_bf16 v[140:143], v[124:127], v[72:75], v[140:143]\n\t" \
  "ds_read_b64_tr_b16 v[120:121], v162 offset:28672\n\t" \
  "v_mfma_f32_16x16x32_bf16 v[156:159], v[124:127], v[76:79], v[156:159]\n\t" \
  "ds_read_b64_tr_b16 v[122:123], v162 offset:30720\n\t" \
  "s_add_u32 %[th], %[th], 1\n\t" \
  "s_cbranch_vccz QF11_%=\n\t" \
  "s_waitcnt vmcnt(16)\n\t" \
  "QG11_%=:\n\t" \
  "s_waitcnt lgkmcnt(10)\n\t" \
  "v_mfma_f32_16x16x32_bf16 v[128:131], v[96:99], v[80:83], v[128:131]\n\t" \
  "ds_read_b64_tr_b16 v[124:125], v163 offset:28672\n\t" \
  "v_mfma_f32_16x16x32_bf16 v[144:147], v[96:99], v[84:87], v[144:147]\n\t" \
  "ds_read_b64_tr_b16 v[126:127], v163 offset:30720\n\t" \
  "v_mfma_f32_16x16x32_bf16 v[132:135], v[100:103], v[80:83], v[132:135]\n\t" \
  "ds_read_b64_tr_b16 v[96:97], v160 offset:32768\n\t" \
  "v_mfma_f32_16x16x32_bf16 v[148:151], v[100:103], v[84:87], v[148:151]\n\t" \
  "ds_read_b64_tr_b16 v[98:99], v160 offset:34816\n\t" \
  "s_waitcnt lgkmcnt(10)\n\t" \
  "v_mfma_f32_16x16x32_bf16 v[136:139], v[104:107], v[80:83], v[136:139]\n\t" \
  "ds_read_b64_tr_b16 v[100:101], v161 offset:32768\n\t" \
  "v_mfma_f32_16x16x32_bf16 v[152:155], v[104:107], v[84:87], v[152:155]\n\t" \
  "ds_read_b64_tr_b16 v[102:103], v161 offset:34816\n\t" \
  "v_mfma_f32_16x16x32_bf16 v[140:143], v[108:111], v[80:83], v[140:143]\n\t" \
  "ds_read_b64_tr_b16 v[104:105], v162 offset:32768\n\t" \
  "v_mfma_f32_16x16x32_bf16 v[156:159], v[108:111], v[84:87], v[156:159]\n\t" \
  "ds_read_b64_tr_b16 v[106:107], v162 offset:34816\n\t" \
  "s_waitcnt lgkmcnt(10)\n\t" \
  "v_mfma_f32_16x16x32_bf16 v[128:131], v[112:115], v[88:91], v[128:131]\n\t" \
  "ds_read_b64_tr_b16 v[108:109], v163 offset:32768\n\t" \
  "v_mfma_f32_16x16x32_bf16 v[144:147], v[112:115], v[92:95], v[144:147]\n\t" \
  "ds_read_b64_tr_b16 v[110:111], v163 offset:34816\n\t" \
  "v_mfma_f32_16x16x32_bf16 v[132:135], v[116:119], v[88:91], v[132:135]\n\t" \
  "ds_read_b64_tr_b16 v[112:113], v160 offset:36864\n\t" \
  "v_mfma_f32_16x16x32_bf16 v[148:151], v[116:119], v[92:95], v[148:151]\n\t" \
  "ds_read_b64_tr_b16 v[114:115], v160 offset:38912\n\t" \
  "s_waitcnt lgkmcnt(10)\n\t" \
  "v_mfma_f32_16x16x32_bf16 v[136:139], v[120:123], v[88:91], v[136:139]\n\t" \
  "ds_read_b64_tr_b16 v[116:117], v161 offset:36864\n\t" \
  "v_mfma_f32_16x16x32_bf16 v[152:155], v[120:123], v[92:95], v[152:155]\n\t" \
  "ds_read_b64_tr_b16 v[118:119], v161 offset:38912\n\t" \
  "v_mfma_f32_16x16x32_bf16 v[140:143], v[124:127], v[88:91], v[140:143]\n\t" \
  "ds_read_b64_tr_b16 v[120:121], v162 offset:36864\n\t" \
  "v_mfma_f32_16x16x32_bf16 v[156:159], v[124:127], v[92:95], v[156:159]\n\t" \
  "ds_read_b64_tr_b16 v[122:123], v162 offset:38912\n\t" \
  "s_cbranch_vccz QF12_%=\n\t" \
  "s_waitcnt vmcnt(12)\n\t" \
  "QG12_%=:\n\t" \
  "s_waitcnt lgkmcnt(10)\n\t" \
  "v_mfma_f32_16x16x32_bf16 v[128:131], v[96:99], v[0:3], v[128:131]\n\t" \
  "ds_read_b64_tr_b16 v[124:125], v163 offset:36864\n\t" \
  "v_mfma_f32_16x16x32_bf16 v[144:147], v[96:99], v[4:7], v[144:147]\n\t" \
  "ds_read_b64_tr_b16 v[126:127], v163 offset:38912\n\t" \
  "v_mfma_f32_16x16x32_bf16 v[132:135], v[100:103], v[0:3], v[132:135]\n\t" \
  "ds_read_b64_tr_b16 v[96:97], v160 offset:40960\n\t" \
  "v_mfma_f32_16x16x32_bf16 v[148:151], v[100:103], v[4:7], v[148:151]\n\t" \
  "ds_read_b64_tr_b16 v[98:99], v160 offset:43008\n\t" \
  "s_waitcnt lgkmcnt(10)\n\t" \
  "v_mfma_f32_16x16x32_bf16 v[136:139], v[104:107], v[0:3], v[136:139]\n\t" \
  "ds_read_b64_tr_b16 v[100:101], v161 offset:40960\n\t" \
  "v_mfma_f32_16x16x32_bf16 v[152:155], v[104:107], v[4:7], v[152:155]\n\t" \
  "ds_read_b64_tr_b16 v[102:103], v161 offset:43008\n\t" \
  "v_mfma_f32_16x16x32_bf16 v[140:143], v[108:111], v[0:3], v[140:143]\n\t" \
  "ds_read_b64_tr_b16 v[104:105], v162 offset:40960\n\t" \
  "v_mfma_f32_16x16x32_bf16 v[156:159], v[108:111], v[4:7], v[156:159]\n\t" \
  "ds_read_b64_tr_b16 v[106:107], v162 offset:43008\n\t" \
  "s_waitcnt lgkmcnt(10)\n\t" \
  "v_mfma_f32_16x16x32_bf16 v[128:131], v[112:115], v[8:11], v[128:131]\n\t" \
  "ds_read_b64_tr_b16 v[108:109], v163 offset:40960\n\t" \
  "v_mfma_f32_16x16x32_bf16 v[144:147], v[112:115], v[12:15], v[144:147]\n\t" \
  "ds_read_b64_tr_b16 v[110:111], v163 offset:43008\n\t" \
  "v_mfma_f32_16x16x32_bf16 v[132:135], v[116:119], v[8:11], v[132:135]\n\t" \
  "ds_read_b64_tr_b16 v[112:113], v160 offset:45056\n\t" \
  "v_mfma_f32_16x16x32_bf16 v[148:151], v[116:119], v[12:15], v[148:151]\n\t" \
  "ds_read_b64_tr_b16 v[114:115], v160 offset:47104\n\t" \
  "s_waitcnt lgkmcnt(10)\n\t" \
  "v_mfma_f32_16x16x32_bf16 v[136:139], v[120:123], v[8:11], v[136:139]\n\t" \
  "ds_read_b64_tr_b16 v[116:117], v161 offset:45056\n\t" \
  "v_mfma_f32_16x16x32_bf16 v[152:155], v[120:123], v[12:15], v[152:155]\n\t" \
  "ds_read_b64_tr_b16 v[118:119], v161 offset:47104\n\t" \
  "v_mfma_f32_16x16x32_bf16 v[140:143], v[124:127], v[8:11], v[140:143]\n\t" \
  "ds_read_b64_tr_b16 v[120:121], v162 offset:45056\n\t" \
  "v_mfma_f32_16x16x32_bf16 v[156:159], v[124:127], v[12:15], v[156:159]\n\t" \
  "ds_read_b64_tr_b16 v[122:123], v162 offset:47104\n\t" \
  "s_cbranch_vccz QF13_%=\n\t" \
  "s_waitcnt vmcnt(8)\n\t" \
  "QG13_%=:\n\t" \
  "s_waitcnt lgkmcnt(10)\n\t" \
  "v_mfma_f32_16x16x32_bf16 v[128:131], v[96:99], v[16:19], v[128:131]\n\t" \
  "ds_read_b64_tr_b16 v[124:125], v163 offset:45056\n\t" \
  "v_mfma_f32_16x16x32_bf16 v[144:147], v[96:99], v[20:23], v[144:147]\n\t" \
  "ds_read_b64_tr_b16 v[126:127], v163 offset:47104\n\t" \
  "v_mfma_f32_16x16x32_bf16 v[132:135], v[100:103], v[16:19], v[132:135]\n\t" \
  "ds_read_b64_tr_b16 v[96:97], v160 offset:49152\n\t" \
  "v_mfma_f32_16x16x32_bf16 v[148:151], v[100:103], v[20:23], v[148:151]\n\t" \
  "ds_read_b64_tr_b16 v[98:99], v160 offset:51200\n\t" \
  "s_waitcnt lgkmcnt(10)\n\t" \
  "v_mfma_f32_16x16x32_bf16 v[136:139], v[104:107], v[16:19], v[136:139]\n\t" \
  "ds_read_b64_tr_b16 v[100:101], v161 offset:49152\n\t" \
  "v_mfma_f32_16x16x32_bf16 v[152:155], v[104:107], v[20:23], v[152:155]\n\t" \
  "ds_read_b64_tr_b16 v[102:103], v161 offset:51200\n\t" \
  "v_mfma_f32_16x16x32_bf16 v[140:143], v[108:111], v[16:19], v[140:143]\n\t" \
  "ds_read_b64_tr_b16 v[104:105], v162 offset:49152\n\t" \
  "v_mfma_f32_16x16x32_bf16 v[156:159], v[108:111], v[20:23], v[156:159]\n\t" \
  "ds_read_b64_tr_b16 v[106:107], v162 offset:51200\n\t" \
  "s_waitcnt lgkmcnt(10)\n\t" \
  "v_mfma_f32_16x16x32_bf16 v[128:131], v[112:115], v[24:27], v[128:131]\n\t" \
  "ds_read_b64_tr_b16 v[108:109], v163 offset:49152\n\t" \
  "v_mfma_f32_16x16x32_bf16 v[144:147], v[112:115], v[28:31], v[144:147]\n\t" \
  "ds_read_b64_tr_b16 v[110:111], v163 offset:51200\n\t" \
  "v_mfma_f32_16x16x32_bf16 v[132:135], v[116:119], v[24:27], v[132:135]\n\t" \
  "ds_read_b64_tr_b16 v[112:113], v160 offset:53248\n\t" \
  "v_mfma_f32_16x16x32_bf16 v[148:151], v[116:119], v[28:31], v[148:151]\n\t" \
  "ds_read_b64_tr_b16 v[114:115], v160 offset:55296\n\t" \
  "s_waitcnt lgkmcnt(10)\n\t" \
  "v_mfma_f32_16x16x32_bf16 v[136:139], v[120:123], v[24:27], v[136:139]\n\t" \
  "ds_read_b64_tr_b16 v[116:117], v161 offset:53248\n\t" \
  "v_mfma_f32_16x16x32_bf16 v[152:155], v[120:123], v[28:31], v[152:155]\n\t" \
  "ds_read_b64_tr_b16 v[118:119], v161 offset:55296\n\t" \
  "v_mfma_f32_16x16x32_bf16 v[140:143], v[124:127], v[24:27], v[140:143]\n\t" \
  "ds_read_b64_tr_b16 v[120:121], v162 offset:53248\n\t" \
  "v_mfma_f32_16x16x32_bf16 v[156:159], v[124:127], v[28:31], v[156:159]\n\t" \
  "ds_read_b64_tr_b16 v[122:123], v162 offset:55296\n\t" \
  "s_cbranch_vccz QF14_%=\n\t" \
  "s_waitcnt vmcnt(4)\n\t" \
  "QG14_%=:\n\t" \
  "s_waitcnt lgkmcnt(10)\n\t" \
  "v_mfma_f32_16x16x32_bf16 v[128:131], v[96:99], v[32:35], v[128:131]\n\t" \
  "ds_read_b64_tr_b16 v[124:125], v163 offset:53248\n\t" \
  "v_mfma_f32_16x16x32_bf16 v[144:147], v[96:99], v[36:39], v[144:147]\n\t" \
  "ds_read_b64_tr_b16 v[126:127], v163 offset:55296\n\t" \
  "v_mfma_f32_16x16x32_bf16 v[132:135], v[100:103], v[32:35], v[132:135]\n\t" \
  "ds_read_b64_tr_b16 v[96:97], v160 offset:57344\n\t" \
  "v_mfma_f32_16x16x32_bf16 v[148:151], v[100:103], v[36:39], v[148:151]\n\t" \
  "ds_read_b64_tr_b16 v[98:99], v160 offset:59392\n\t" \
  "s_waitcnt lgkmcnt(10)\n\t" \
  "v_mfma_f32_16x16x32_bf16 v[136:139], v[104:107], v[32:35], v[136:139]\n\t" \
  "ds_read_b64_tr_b16 v[100:101], v161 offset:57344\n\t" \
  "v_mfma_f32_16x16x32_bf16 v[152:155], v[104:107], v[36:39], v[152:155]\n\t" \
  "ds_read_b64_tr_b16 v[102:103], v161 offset:59392\n\t" \
  "v_mfma_f32_16x16x32_bf16 v[140:143], v[108:111], v[32:35], v[140:143]\n\t" \
  "ds_read_b64_tr_b16 v[104:105], v162 offset:57344\n\t" \
  "v_mfma_f32_16x16x32_bf16 v[156:159], v[108:111], v[36:39], v[156:159]\n\t" \
  "ds_read_b64_tr_b16 v[106:107], v162 offset:59392\n\t" \
  "s_waitcnt lgkmcnt(10)\n\t" \
  "v_mfma_f32_16x16x32_bf16 v[128:131], v[112:115], v[40:43], v[128:131]\n\t" \
  "ds_read_b64_tr_b16 v[108:109], v163 offset:57344\n\t" \
  "v_mfma_f32_16x16x32_bf16 v[144:147], v[112:115], v[44:47], v[144:147]\n\t" \
  "ds_read_b64_tr_b16 v[110:111], v163 offset:59392\n\t" \
  "v_mfma_f32_16x16x32_bf16 v[132:135], v[116:119], v[40:43], v[132:135]\n\t" \
  "ds_read_b64_tr_b16 v[112:113], v160 offset:61440\n\t" \
  "v_mfma_f32_16x16x32_bf16 v[148:151], v[116:119], v[44:47], v[148:151]\n\t" \
  "ds_read_b64_tr_b16 v[114:115], v160 offset:63488\n\t" \
  "s_waitcnt lgkmcnt(10)\n\t" \
  "v_mfma_f32_16x16x32_bf16 v[136:139], v[120:123], v[40:43], v[136:139]\n\t" \
  "ds_read_b64_tr_b16 v[116:117], v161 offset:61440\n\t" \
  "v_mfma_f32_16x16x32_bf16 v[152:155], v[120:123], v[44:47], v[152:155]\n\t" \
  "ds_read_b64_tr_b16 v[118:119], v161 offset:63488\n\t" \
  "v_mfma_f32_16x16x32_bf16 v[140:143], v[124:127], v[40:43], v[140:143]\n\t" \
  "ds_read_b64_tr_b16 v[120:121], v162 offset:61440\n\t" \
  "v_mfma_f32_16x16x32_bf16 v[156:159], v[124:127], v[44:47], v[156:159]\n\t" \
  "ds_read_b64_tr_b16 v[122:123], v162 offset:63488\n\t" \
  "s_cbranch_vccz QF15_%=\n\t" \
  "s_waitcnt vmcnt(0)\n\t" \
  "QG15_%=:\n\t" \
  "s_waitcnt lgkmcnt(10)\n\t" \
  "v_mfma_f32_16x16x32_bf16 v[128:131], v[96:99], v[48:51], v[128:131]\n\t" \
  "ds_read_b64_tr_b16 v[124:125], v163 offset:61440\n\t" \
  "v_mfma_f32_16x16x32_bf16 v[144:147], v[96:99], v[52:55], v[144:147]\n\t" \
  "ds_read_b64_tr_b16 v[126:127], v163 offset:63488\n\t" \
  "v_mfma_f32_16x16x32_bf16 v[132:135], v[100:103], v[48:51], v[132:135]\n\t" \
  "v_mfma_f32_16x16x32_bf16 v[148:151], v[100:103], v[52:55], v[148:151]\n\t" \
  "s_waitcnt lgkmcnt(8)\n\t" \
  "v_mfma_f32_16x16x32_bf16 v[136:139], v[104:107], v[48:51], v[136:139]\n\t" \
  "v_mfma_f32_16x16x32_bf16 v[152:155], v[104:107], v[52:55], v[152:155]\n\t" \
  "v_mfma_f32_16x16x32_bf16 v[140:143], v[108:111], v[48:51], v[140:143]\n\t" \
  "v_mfma_f32_16x16x32_bf16 v[156:159], v[108:111], v[52:55], v[156:159]\n\t" \
  "s_waitcnt lgkmcnt(4)\n\t" \
  "v_mfma_f32_16x16x32_bf16 v[128:131], v[112:115], v[56:59], v[128:131]\n\t" \
  "v_mfma_f32_16x16x32_bf16 v[144:147], v[112:115], v[60:63], v[144:147]\n\t" \
  "v_mfma_f32_16x16x32_bf16 v[132:135], v[116:119], v[56:59], v[132:135]\n\t" \
  "v_mfma_f32_16x16x32_bf16 v[148:151], v[116:119], v[60:63], v[148:151]\n\t" \
  "s_waitcnt lgkmcnt(0)\n\t" \
  "v_mfma_f32_16x16x32_bf16 v[136:139], v[120:123], v[56:59], v[136:139]\n\t" \
  "v_mfma_f32_16x16x32_bf16 v[152:155], v[120:123], v[60:63], v[152:155]\n\t" \
  "v_mfma_f32_16x16x32_bf16 v[140:143], v[124:127], v[56:59], v[140:143]\n\t" \
  "v_mfma_f32_16x16x32_bf16 v[156:159], v[124:127], v[60:63], v[156:159]\n\t" \
  "s_branch QX_%=\n\t" \
  "QF1_%=:\n\t" \
  "s_mov_b32 %[c], 1\n\t" \
  "s_getpc_b64 s[92:93]\n\t" \
  "s_branch QC_%=\n\t" \
  "s_branch QG1_%=\n\t" \
  "QF2_%=:\n\t" \
  "s_mov_b32 %[c], 2\n\t" \
  "s_getpc_b64 s[92:93]\n\t" \
  "s_branch QC_%=\n\t" \
  "s_branch QG2_%=\n\t" \
  "QF3_%=:\n\t" \
  "s_mov_b32 %[c], 3\n\t" \
  "s_getpc_b64 s[92:93]\n\t" \
  "s_branch QC_%=\n\t" \
  "s_branch QG3_%=\n\t" \
  "QF4_%=:\n\t" \
  "s_mov_b32 %[c], 4\n\t" \
  "s_getpc_b64 s[92:93]\n\t" \
  "s_branch QC_%=\n\t" \
  "s_branch QG4_%=\n\t" \
  "QF5_%=:\n\t" \
  "s_mov_b32 %[c], 5\n\t" \
  "s_getpc_b64 s[92:93]\n\t" \
  "s_branch QC_%=\n\t" \
  "s_branch QG5_%=\n\t" \
  "QF6_%=:\n\t" \
  "s_mov_b32 %[c], 6\n\t" \
  "s_getpc_b64 s[92:93]\n\t" \
  "s_branch QC_%=\n\t" \
  "s_branch QG6_%=\n\t" \
  "QF7_%=:\n\t" \
  "s_mov_b32 %[c], 7\n\t" \
  "s_getpc_b64 s[92:93]\n\t" \
  "s_branch QC_%=\n\t" \
  "s_branch QG7_%=\n\t" \
  "QF8_%=:\n\t" \
  "s_mov_b32 %[c], 8\n\t" \
  "s_getpc_b64 s[92:93]\n\t" \
  "s_branch QC_%=\n\t" \
  "s_branch QG8_%=\n\t" \
  "QF9_%=:\n\t" \
  "s_mov_b32 %[c], 9\n\t" \
  "s_getpc_b64 s[92:93]\n\t" \
  "s_branch QC_%=\n\t" \
  "s_branch QG9_%=\n\t" \
  "QF10_%=:\n\t" \
  "s_mov_b32 %[c], 10\n\t" \
  "s_getpc_b64 s[92:93]\n\t" \
  "s_branch QC_%=\n\t" \
  "s_branch QG10_%=\n\t" \
  "QF11_%=:\n\t" \
  "s_mov_b32 %[c], 11\n\t" \
  "s_getpc_b64 s[92:93]\n\t" \
  "s_branch QC_%=\n\t" \
  "s_branch QG11_%=\n\t" \
  "QF12_%=:\n\t" \
  "s_mov_b32 %[c], 12\n\t" \
  "s_getpc_b64 s[92:93]\n\t" \
  "s_branch QC_%=\n\t" \
  "s_branch QG12_%=\n\t" \
  "QF13_%=:\n\t" \
  "s_mov_b32 %[c], 13\n\t" \
  "s_getpc_b64 s[92:93]\n\t" \
  "s_branch QC_%=\n\t" \
  "s_branch QG13_%=\n\t" \
  "QF14_%=:\n\t" \
  "s_mov_b32 %[c], 14\n\t" \
  "s_getpc_b64 s[92:93]\n\t" \
  "s_branch QC_%=\n\t" \
  "s_branch QG14_%=\n\t" \
  "QF15_%=:\n\t" \
  "s_mov_b32 %[c], 15\n\t" \
  "s_getpc_b64 s[92:93]\n\t" \
  "s_branch QC_%=\n\t" \
  "s_branch QG15_%=\n\t" \
  "QC_%=:\n\t" \
  "s_add_u32 s92, s92, 4\n\t" \
  "s_addc_u32 s93, s93, 0\n\t" \
  "QU_%=:\n\t" \
  "ds_read_b32 v166, v165\n\t" \
  "s_add_u32 %[t1], %[c], 5\n\t" \
  "s_waitcnt lgkmcnt(0)\n\t" \
  "v_readfirstlane_b32 %[t0], v166\n\t" \
  "s_nop 0\n\t" \
  "s_min_u32 %[t1], %[t1], %[t0]\n\t" \
  "QL_%=:\n\t" \
  "s_cmp_ge_u32 %[iss], %[t1]\n\t" \
  "s_cbranch_scc1 QD_%=\n\t" \
  "s_mov_b32 %[t0], %[iss]\n\t" \
  "s_sub_u32 %[th], %[t0], 6\n\t" \
  "s_cselect_b32 %[t0], %[t0], %[th]\n\t" \
  "s_sub_u32 %[th], %[t0], 6\n\t" \
  "s_cselect_b32 %[t0], %[t0], %[th]\n\t" \
  "s_cmp_eq_u32 %[t0], 1\n\t" \
  "s_cbranch_scc1 QS1_%=\n\t" \
  "s_cmp_eq_u32 %[t0], 2\n\t" \
  "s_cbranch_scc1 QS2_%=\n\t" \
  "s_cmp_eq_u32 %[t0], 3\n\t" \
  "s_cbranch_scc1 QS3_%=\n\t" \
  "s_cmp_eq_u32 %[t0], 4\n\t" \
  "s_cbranch_scc1 QS4_%=\n\t" \
  "s_cmp_eq_u32 %[t0], 5\n\t" \
  "s_cbranch_scc1 QS5_%=\n\t" \
  "QS0_%=:\n\t" \
  "v_readlane_b32 s94, v167, %[iss]\n\t" \
  "s_nop 4\n\t" \
  "buffer_load_dwordx4 v[0:3], v164, %[ob], s94 offen offset:0 sc1\n\t" \
  "buffer_load_dwordx4 v[4:7], v164, %[ob], s94 offen offset:1024 sc1\n\t" \
  "buffer_load_dwordx4 v[8:11], v164, %[ob], s94 offen offset:2048 sc1\n\t" \
  "buffer_load_dwordx4 v[12:15], v164, %[ob], s94 offen offset:3072 sc1\n\t" \
  "s_add_u32 %[iss], %[iss], 1\n\t" \
  "s_cmp_ge_u32 %[iss], %[t1]\n\t" \
  "s_cbranch_scc1 QD_%=\n\t" \
  "QS1_%=:\n\t" \
  "v_readlane_b32 s94, v167, %[iss]\n\t" \
  "s_nop 4\n\t" \
  "buffer_load_dwordx4 v[16:19], v164, %[ob], s94 offen offset:0 sc1\n\t" \
  "buffer_load_dwordx4 v[20:23], v164, %[ob], s94 offen offset:1024 sc1\n\t" \
  "buffer_load_dwordx4 v[24:27], v164, %[ob], s94 offen offset:2048 sc1\n\t" \
  "buffer_load_dwordx4 v[28:31], v164, %[ob], s94 offen offset:3072 sc1\n\t" \
  "s_add_u32 %[iss], %[iss], 1\n\t" \
  "s_cmp_ge_u32 %[iss], %[t1]\n\t" \
  "s_cbranch_scc1 QD_%=\n\t" \
  "QS2_%=:\n\t" \
  "v_readlane_b32 s94, v167, %[iss]\n\t" \
  "s_nop 4\n\t" \
  "buffer_load_dwordx4 v[32:35], v164, %[ob], s94 offen offset:0 sc1\n\t" \
  "buffer_load_dwordx4 v[36:39], v164, %[ob], s94 offen offset:1024 sc1\n\t" \
  "buffer_load_dwordx4 v[40:43], v164, %[ob], s94 offen offset:2048 sc1\n\t" \
  "buffer_load_dwordx4 v[44:47], v164, %[ob], s94 offen offset:3072 sc1\n\t" \
  "s_add_u32 %[iss], %[iss], 1\n\t" \
  "s_cmp_ge_u32 %[iss], %[t1]\n\t" \
  "s_cbranch_scc1 QD_%=\n\t" \
  "QS3_%=:\n\t" \
  "v_readlane_b32 s94, v167, %[iss]\n\t" \
  "s_nop 4\n\t" \
  "buffer_load_dwordx4 v[48:51], v164, %[ob], s94 offen offset:0 sc1\n\t" \
  "buffer_load_dwordx4 v[52:55], v164, %[ob], s94 offen offset:1024 sc1\n\t" \
  "buffer_load_dwordx4 v[56:59], v164, %[ob], s94 offen offset:2048 sc1\n\t" \
  "buffer_load_dwordx4 v[60:63], v164, %[ob], s94 offen offset:3072 sc1\n\t" \
  "s_add_u32 %[iss], %[iss], 1\n\t" \
  "s_cmp_ge_u32 %[iss], %[t1]\n\t" \
  "s_cbranch_scc1 QD_%=\n\t" \
  "QS4_%=:\n\t" \
  "v_readlane_b32 s94, v167, %[iss]\n\t" \
  "s_nop 4\n\t" \
  "buffer_load_dwordx4 v[64:67], v164, %[ob], s94 offen offset:0 sc1\n\t" \
  "buffer_load_dwordx4 v[68:71], v164, %[ob], s94 offen offset:1024 sc1\n\t" \
  "buffer_load_dwordx4 v[72:75], v164, %[ob], s94 offen offset:2048 sc1\n\t" \
  "buffer_load_dwordx4 v[76:79], v164, %[ob], s94 offen offset:3072 sc1\n\t" \
  "s_add_u32 %[iss], %[iss], 1\n\t" \
  "s_cmp_ge_u32 %[iss], %[t1]\n\t" \
  "s_cbranch_scc1 QD_%=\n\t" \
  "QS5_%=:\n\t" \
  "v_readlane_b32 s94, v167, %[iss]\n\t" \
  "s_nop 4\n\t" \
  "buffer_load_dwordx4 v[80:83], v164, %[ob], s94 offen offset:0 sc1\n\t" \
  "buffer_load_dwordx4 v[84:87], v164, %[ob], s94 offen offset:1024 sc1\n\t" \
  "buffer_load_dwordx4 v[88:91], v164, %[ob], s94 offen offset:2048 sc1\n\t" \
  "buffer_load_dwordx4 v[92:95], v164, %[ob], s94 offen offset:3072 sc1\n\t" \
  "s_add_u32 %[iss], %[iss], 1\n\t" \
  "s_cmp_ge_u32 %[iss], %[t1]\n\t" \
  "s_cbranch_scc0 QS0_%=\n\t" \
  "QD_%=:\n\t" \
  "s_cmp_gt_u32 %[iss], %[c]\n\t" \
  "s_cbranch_scc1 QW_%=\n\t" \
  "s_addk_i32 %[cn], 1\n\t" \
  "s_cmp_gt_u32 %[cn], 0x400000\n\t" \
  "s_cbranch_scc1 QT_%=\n\t" \
  "s_sleep 1\n\t" \
  "s_branch QU_%=\n\t" \
  "QT_%=:\n\t" \
  "s_add_u32 %[t1], %[c], 1\n\t" \
  "s_branch QL_%=\n\t" \
  "QW_%=:\n\t" \
  "s_cmp_eq_u32 %[iss], 16\n\t" \
  "s_cselect_b64 vcc, -1, 0\n\t" \
  "s_sub_u32 %[t0], %[iss], %[c]\n\t" \
  "s_cmp_eq_u32 %[t0], 5\n\t" \
  "s_cselect_b32 %[th], %[iss], 99\n\t" \
  "s_waitcnt vmcnt(16)\n\t" \
  "s_cmp_ge_u32 %[t0], 5\n\t" \
  "s_cbranch_scc1 QR_%=\n\t" \
  "s_waitcnt vmcnt(12)\n\t" \
  "s_cmp_ge_u32 %[t0], 4\n\t" \
  "s_cbranch_scc1 QR_%=\n\t" \
  "s_waitcnt vmcnt(8)\n\t" \
  "s_cmp_ge_u32 %[t0], 3\n\t" \
  "s_cbranch_scc1 QR_%=\n\t" \
  "s_waitcnt vmcnt(4)\n\t" \
  "s_cmp_ge_u32 %[t0], 2\n\t" \
  "s_cbranch_scc1 QR_%=\n\t" \
  "s_waitcnt vmcnt(0)\n\t" \
  "QR_%=:\n\t" \
  "s_setpc_b64 s[92:93]\n\t" \
  "QX_%=:\n\t" \
  "s_nop 7\n\t"
#define G3_CLOB "v0", "v1", "v2", "v3", "v4", "v5", "v6", "v7", "v8", "v9", "v10", "v11", "v12", "v13", "v14", "v15", "v16", "v17", "v18", "v19", "v20", "v21", "v22", "v23", "v24", "v25", "v26", "v27", "v28", "v29", "v30", "v31", "v32", "v33", "v34", "v35", "v36", "v37", "v38", "v39", "v40", "v41", "v42", "v43", "v44", "v45", "v46", "v47", "v48", "v49", "v50", "v51", "v52", "v53", "v54", "v55", "v56", "v57", "v58", "v59", "v60", "v61", "v62", "v63", "v64", "v65", "v66", "v67", "v68", "v69", "v70", "v71", "v72", "v73", "v74", "v75", "v76", "v77", "v78", "v79", "v80", "v81", "v82", "v83", "v84", "v85", "v86", "v87", "v88", "v89", "v90", "v91", "v92", "v93", "v94", "v95", "v96", "v97", "v98", "v99", "v100", "v101", "v102", "v103", "v104", "v105", "v106", "v107", "v108", "v109", "v110", "v111", "v112", "v113", "v114", "v115", "v116", "v117", "v118", "v119", "v120", "v121", "v122", "v123", "v124", "v125", "v126", "v127", "v166", "s88", "s89", "s92", "s93", "s94"

#define PP_ASM \
  "v_mov_b32 v60, 0\n\t" \
  "s_mov_b32 %[t0], %[n0]\n\t" \
  "s_cmp_eq_u32 %[t0], 0\n\t" \
  "s_cbranch_scc1 PAx_%=\n\t" \
  "PA_%=:\n\t" \
  "s_barrier\n\t" \
  "s_sub_u32 %[t0], %[t0], 1\n\t" \
  "s_cmp_lg_u32 %[t0], 0\n\t" \
  "s_cbranch_scc1 PA_%=\n\t" \
  "PAx_%=:\n\t" \
  "s_mov_b32 m0, %[shd]\n\t" \
  "global_load_dword v160, v60, %[fl] sc1\n\t" \
  "global_load_lds_dwordx4 v163, %[shp]\n\t" \
  "s_mov_b32 m0, %[tgd]\n\t" \
  "s_mov_b64 exec, %[em]\n\t" \
  "global_load_lds_dword v60, %[fl] sc1\n\t" \
  "s_mov_b64 exec, -1\n\t" \
  "global_load_ushort v161, v60, %[hp]\n\t" \
  "global_load_ushort v162, v60, %[dp]\n\t" \
  "global_load_dwordx4 v[0:3], v152, %[kb0]\n\t" \
  "global_load_dwordx4 v[4:7], v153, %[kb0]\n\t" \
  "global_load_dwordx4 v[8:11], v154, %[kb0]\n\t" \
  "global_load_dwordx4 v[12:15], v155, %[kb3] offset:0\n\t" \
  "global_load_dwordx4 v[28:31], v155, %[vb0] offset:0\n\t" \
  "s_setprio 2\n\t" \
  "s_barrier\n\t" \
  "s_barrier\n\t" \
  "s_waitcnt vmcnt(5)\n\t" \
  "ds_read_b128 v[128:131], v156 offset:0\n\t" \
  "ds_read_b128 v[132:135], v156 offset:16\n\t" \
  "ds_read_b128 v[136:139], v156 offset:32\n\t" \
  "ds_read_b128 v[140:143], v156 offset:48\n\t" \
  "s_waitcnt lgkmcnt(0)\n\t" \
  "v_lshlrev_b32 v64, 16, v128\n\t" \
  "v_and_b32 v65, 0xffff0000, v128\n\t" \
  "v_lshlrev_b32 v66, 16, v129\n\t" \
  "v_and_b32 v67, 0xffff0000, v129\n\t" \
  "v_lshlrev_b32 v68, 16, v130\n\t" \
  "v_and_b32 v69, 0xffff0000, v130\n\t" \
  "v_lshlrev_b32 v70, 16, v131\n\t" \
  "v_and_b32 v71, 0xffff0000, v131\n\t" \
  "v_lshlrev_b32 v72, 16, v132\n\t" \
  "v_and_b32 v73, 0xffff0000, v132\n\t" \
  "v_lshlrev_b32 v74, 16, v133\n\t" \
  "v_and_b32 v75, 0xffff0000, v133\n\t" \
  "v_lshlrev_b32 v76, 16, v134\n\t" \
  "v_and_b32 v77, 0xffff0000, v134\n\t" \
  "v_lshlrev_b32 v78, 16, v135\n\t" \
  "v_and_b32 v79, 0xffff0000, v135\n\t" \
  "v_lshlrev_b32 v80, 16, v136\n\t" \
  "v_and_b32 v81, 0xffff0000, v136\n\t" \
  "v_lshlrev_b32 v82, 16, v137\n\t" \
  "v_and_b32 v83, 0xffff0000, v137\n\t" \
  "v_lshlrev_b32 v84, 16, v138\n\t" \
  "v_and_b32 v85, 0xffff0000, v138\n\t" \
  "v_lshlrev_b32 v86, 16, v139\n\t" \
  "v_and_b32 v87, 0xffff0000, v139\n\t" \
  "v_lshlrev_b32 v88, 16, v140\n\t" \
  "v_and_b32 v89, 0xffff0000, v140\n\t" \
  "v_lshlrev_b32 v90, 16, v141\n\t" \
  "v_and_b32 v91, 0xffff0000, v141\n\t" \
  "v_lshlrev_b32 v92, 16, v142\n\t" \
  "v_and_b32 v93, 0xffff0000, v142\n\t" \
  "v_lshlrev_b32 v94, 16, v143\n\t" \
  "v_and_b32 v95, 0xffff0000, v143\n\t" \
  "s_waitcnt vmcnt(4)\n\t" \
  "s_mov_b64 exec, %[zm]\n\t" \
  "v_mov_b32 v0, 0\n\t" \
  "v_mov_b32 v1, 0\n\t" \
  "v_mov_b32 v2, 0\n\t" \
  "v_mov_b32 v3, 0\n\t" \
  "s_mov_b64 exec, -1\n\t" \
  "v_lshlrev_b32 v128, 16, v0\n\t" \
  "v_and_b32 v129, 0xffff0000, v0\n\t" \
  "s_setprio 3\n\t" \
  "s_barrier\n\t" \
  "global_load_dwordx4 v[16:19], v155, %[kb3] offset:2048\n\t" \
  "global_load_dwordx4 v[32:35], v155, %[vb0] offset:2048\n\t" \
  "global_load_dwordx4 v[20:23], v155, %[kb5] offset:0\n\t" \
  "s_setprio 2\n\t" \
  "v_lshlrev_b32 v130, 16, v1\n\t" \
  "v_and_b32 v131, 0xffff0000, v1\n\t" \
  "v_lshlrev_b32 v132, 16, v2\n\t" \
  "v_and_b32 v133, 0xffff0000, v2\n\t" \
  "v_lshlrev_b32 v134, 16, v3\n\t" \
  "v_and_b32 v135, 0xffff0000, v3\n\t" \
  "v_mul_f32 v96, v128, v67\n\t" \
  "v_mul_f32 v97, v129, v71\n\t" \
  "v_mul_f32 v98, v130, v75\n\t" \
  "v_mul_f32 v99, v131, v79\n\t" \
  "v_mul_f32 v100, v132, v83\n\t" \
  "v_mul_f32 v101, v133, v87\n\t" \
  "v_mul_f32 v102, v134, v91\n\t" \
  "v_mul_f32 v103, v135, v95\n\t" \
  "s_waitcnt vmcnt(6)\n\t" \
  "s_mov_b64 exec, %[zm]\n\t" \
  "v_mov_b32 v4, 0\n\t" \
  "v_mov_b32 v5, 0\n\t" \
  "v_mov_b32 v6, 0\n\t" \
  "v_mov_b32 v7, 0\n\t" \
  "s_mov_b64 exec, -1\n\t" \
  "v_lshlrev_b32 v128, 16, v4\n\t" \
  "v_and_b32 v129, 0xffff0000, v4\n\t" \
  "v_lshlrev_b32 v130, 16, v5\n\t" \
  "v_and_b32 v131, 0xffff0000, v5\n\t" \
  "v_lshlrev_b32 v132, 16, v6\n\t" \
  "v_and_b32 v133, 0xffff0000, v6\n\t" \
  "v_lshlrev_b32 v134, 16, v7\n\t" \
  "v_and_b32 v135, 0xffff0000, v7\n\t" \
  "v_fmac_f32 v96, v128, v66\n\t" \
  "v_fmac_f32 v97, v129, v70\n\t" \
  "v_fmac_f32 v98, v130, v74\n\t" \
  "v_fmac_f32 v99, v131, v78\n\t" \
  "v_fmac_f32 v100, v132, v82\n\t" \
  "v_fmac_f32 v101, v133, v86\n\t" \
  "v_fmac_f32 v102, v134, v90\n\t" \
  "v_fmac_f32 v103, v135, v94\n\t" \
  "v_mul_f32 v104, v128, v67\n\t" \
  "v_mul_f32 v105, v129, v71\n\t" \
  "v_mul_f32 v106, v130, v75\n\t" \
  "v_mul_f32 v107, v131, v79\n\t" \
  "v_mul_f32 v108, v132, v83\n\t" \
  "v_mul_f32 v109, v133, v87\n\t" \
  "v_mul_f32 v110, v134, v91\n\t" \
  "v_mul_f32 v111, v135, v95\n\t" \
  "s_waitcnt vmcnt(5)\n\t" \
  "s_mov_b64 exec, %[zm]\n\t" \
  "v_mov_b32 v8, 0\n\t" \
  "v_mov_b32 v9, 0\n\t" \
  "v_mov_b32 v10, 0\n\t" \
  "v_mov_b32 v11, 0\n\t" \
  "s_mov_b64 exec, -1\n\t" \
  "s_barrier\n\t" \
  "v_lshlrev_b32 v128, 16, v8\n\t" \
  "v_and_b32 v129, 0xffff0000, v8\n\t" \
  "v_lshlrev_b32 v130, 16, v9\n\t" \
  "v_and_b32 v131, 0xffff0000, v9\n\t" \
  "v_lshlrev_b32 v132, 16, v10\n\t" \
  "v_and_b32 v133, 0xffff0000, v10\n\t" \
  "v_lshlrev_b32 v134, 16, v11\n\t" \
  "v_and_b32 v135, 0xffff0000, v11\n\t" \
  "v_fmac_f32 v96, v128, v65\n\t" \
  "v_fmac_f32 v97, v129, v69\n\t" \
  "v_fmac_f32 v98, v130, v73\n\t" \
  "v_fmac_f32 v99, v131, v77\n\t" \
  "v_fmac_f32 v100, v132, v81\n\t" \
  "v_fmac_f32 v101, v133, v85\n\t" \
  "v_fmac_f32 v102, v134, v89\n\t" \
  "v_fmac_f32 v103, v135, v93\n\t" \
  "v_fmac_f32 v104, v128, v66\n\t" \
  "v_fmac_f32 v105, v129, v70\n\t" \
  "v_fmac_f32 v106, v130, v74\n\t" \
  "v_fmac_f32 v107, v131, v78\n\t" \
  "v_fmac_f32 v108, v132, v82\n\t" \
  "v_fmac_f32 v109, v133, v86\n\t" \
  "v_fmac_f32 v110, v134, v90\n\t" \
  "v_fmac_f32 v111, v135, v94\n\t" \
  "v_mul_f32 v112, v128, v67\n\t" \
  "v_mul_f32 v113, v129, v71\n\t" \
  "v_mul_f32 v114, v130, v75\n\t" \
  "v_mul_f32 v115, v131, v79\n\t" \
  "v_mul_f32 v116, v132, v83\n\t" \
  "v_mul_f32 v117, v133, v87\n\t" \
  "v_mul_f32 v118, v134, v91\n\t" \
  "v_mul_f32 v119, v135, v95\n\t" \
  "s_waitcnt vmcnt(4)\n\t" \
  "v_lshlrev_b32 v128, 16, v12\n\t" \
  "v_and_b32 v129, 0xffff0000, v12\n\t" \
  "v_lshlrev_b32 v130, 16, v13\n\t" \
  "v_and_b32 v131, 0xffff0000, v13\n\t" \
  "v_lshlrev_b32 v132, 16, v14\n\t" \
  "v_and_b32 v133, 0xffff0000, v14\n\t" \
  "v_lshlrev_b32 v134, 16, v15\n\t" \
  "v_and_b32 v135, 0xffff0000, v15\n\t" \
  "s_setprio 3\n\t" \
  "s_barrier\n\t" \
  "global_load_dwordx4 v[36:39], v155, %[vb2] offset:0\n\t" \
  "global_load_dwordx4 v[24:27], v155, %[kb5] offset:2048\n\t" \
  "global_load_dwordx4 v[40:43], v155, %[vb2] offset:2048\n\t" \
  "s_setprio 2\n\t" \
  "v_fmac_f32 v96, v128, v64\n\t" \
  "v_fmac_f32 v97, v129, v68\n\t" \
  "v_fmac_f32 v98, v130, v72\n\t" \
  "v_fmac_f32 v99, v131, v76\n\t" \
  "v_fmac_f32 v100, v132, v80\n\t" \
  "v_fmac_f32 v101, v133, v84\n\t" \
  "v_fmac_f32 v102, v134, v88\n\t" \
  "v_fmac_f32 v103, v135, v92\n\t" \
  "v_fmac_f32 v104, v128, v65\n\t" \
  "v_fmac_f32 v105, v129, v69\n\t" \
  "v_fmac_f32 v106, v130, v73\n\t" \
  "v_fmac_f32 v107, v131, v77\n\t" \
  "v_fmac_f32 v108, v132, v81\n\t" \
  "v_fmac_f32 v109, v133, v85\n\t" \
  "v_fmac_f32 v110, v134, v89\n\t" \
  "v_fmac_f32 v111, v135, v93\n\t" \
  "v_fmac_f32 v112, v128, v66\n\t" \
  "v_fmac_f32 v113, v129, v70\n\t" \
  "v_fmac_f32 v114, v130, v74\n\t" \
  "v_fmac_f32 v115, v131, v78\n\t" \
  "v_fmac_f32 v116, v132, v82\n\t" \
  "v_fmac_f32 v117, v133, v86\n\t" \
  "v_fmac_f32 v118, v134, v90\n\t" \
  "v_fmac_f32 v119, v135, v94\n\t" \
  "v_mul_f32 v120, v128, v67\n\t" \
  "v_mul_f32 v121, v129, v71\n\t" \
  "v_mul_f32 v122, v130, v75\n\t" \
  "v_mul_f32 v123, v131, v79\n\t" \
  "v_mul_f32 v124, v132, v83\n\t" \
  "v_mul_f32 v125, v133, v87\n\t" \
  "v_mul_f32 v126, v134, v91\n\t" \
  "v_mul_f32 v127, v135, v95\n\t" \
  "s_waitcnt vmcnt(6)\n\t" \
  "v_lshlrev_b32 v128, 16, v28\n\t" \
  "v_and_b32 v129, 0xffff0000, v28\n\t" \
  "v_lshlrev_b32 v130, 16, v29\n\t" \
  "v_and_b32 v131, 0xffff0000, v29\n\t" \
  "v_lshlrev_b32 v132, 16, v30\n\t" \
  "v_and_b32 v133, 0xffff0000, v30\n\t" \
  "v_lshlrev_b32 v134, 16, v31\n\t" \
  "v_and_b32 v135, 0xffff0000, v31\n\t" \
  "s_barrier\n\t" \
  "v_mul_f32 v96, v96, v128\n\t" \
  "v_mul_f32 v97, v97, v129\n\t" \
  "v_mul_f32 v98, v98, v130\n\t" \
  "v_mul_f32 v99, v99, v131\n\t" \
  "v_mul_f32 v100, v100, v132\n\t" \
  "v_mul_f32 v101, v101, v133\n\t" \
  "v_mul_f32 v102, v102, v134\n\t" \
  "v_mul_f32 v103, v103, v135\n\t" \
  "v_cvt_pk_bf16_f32 v136, v96, v97\n\t" \
  "v_cvt_pk_bf16_f32 v137, v98, v99\n\t" \
  "v_cvt_pk_bf16_f32 v138, v100, v101\n\t" \
  "v_cvt_pk_bf16_f32 v139, v102, v103\n\t" \
  "v_mov_b32_dpp v128, v136 row_ror:8 row_mask:0xf bank_mask:0xf\n\t" \
  "v_mov_b32_dpp v129, v137 row_ror:8 row_mask:0xf bank_mask:0xf\n\t" \
  "v_mov_b32_dpp v130, v138 row_ror:8 row_mask:0xf bank_mask:0xf\n\t" \
  "v_mov_b32_dpp v131, v139 row_ror:8 row_mask:0xf bank_mask:0xf\n\t" \
  "v_perm_b32 v136, v128, v136, v157\n\t" \
  "v_perm_b32 v137, v129, v137, v157\n\t" \
  "v_perm_b32 v138, v130, v138, v157\n\t" \
  "v_perm_b32 v139, v131, v139, v157\n\t" \
  "v_permlane16_swap_b32 v136, v137\n\t" \
  "s_nop 0\n\t" \
  "v_permlane16_swap_b32 v138, v139\n\t" \
  "s_nop 1\n\t" \
  "v_permlane32_swap_b32 v136, v138\n\t" \
  "v_permlane32_swap_b32 v137, v139\n\t" \
  "s_waitcnt vmcnt(5)\n\t" \
  "v_lshlrev_b32 v128, 16, v16\n\t" \
  "v_and_b32 v129, 0xffff0000, v16\n\t" \
  "v_lshlrev_b32 v130, 16, v17\n\t" \
  "v_and_b32 v131, 0xffff0000, v17\n\t" \
  "v_lshlrev_b32 v132, 16, v18\n\t" \
  "v_and_b32 v133, 0xffff0000, v18\n\t" \
  "v_lshlrev_b32 v134, 16, v19\n\t" \
  "v_and_b32 v135, 0xffff0000, v19\n\t" \
  "v_fmac_f32 v104, v128, v64\n\t" \
  "v_fmac_f32 v105, v129, v68\n\t" \
  "v_fmac_f32 v106, v130, v72\n\t" \
  "v_fmac_f32 v107, v131, v76\n\t" \
  "v_fmac_f32 v108, v132, v80\n\t" \
  "v_fmac_f32 v109, v133, v84\n\t" \
  "s_setprio 3\n\t" \
  "s_barrier\n\t" \
  "global_store_dwordx4 v159, v[136:139], %[sb] sc1\n\t" \
  "v_add_u32 v158, 0x80, v159\n\t" \
  "v_bfi_b32 v159, %[mk], v158, v159\n\t" \
  "s_setprio 2\n\t" \
  "v_fmac_f32 v110, v134, v88\n\t" \
  "v_fmac_f32 v111, v135, v92\n\t" \
  "v_fmac_f32 v112, v128, v65\n\t" \
  "v_fmac_f32 v113, v129, v69\n\t" \
  "v_fmac_f32 v114, v130, v73\n\t" \
  "v_fmac_f32 v115, v131, v77\n\t" \
  "v_fmac_f32 v116, v132, v81\n\t" \
  "v_fmac_f32 v117, v133, v85\n\t" \
  "v_fmac_f32 v118, v134, v89\n\t" \
  "v_fmac_f32 v119, v135, v93\n\t" \
  "v_fmac_f32 v120, v128, v66\n\t" \
  "v_fmac_f32 v121, v129, v70\n\t" \
  "v_fmac_f32 v122, v130, v74\n\t" \
  "v_fmac_f32 v123, v131, v78\n\t" \
  "v_fmac_f32 v124, v132, v82\n\t" \
  "v_fmac_f32 v125, v133, v86\n\t" \
  "v_fmac_f32 v126, v134, v90\n\t" \
  "v_fmac_f32 v127, v135, v94\n\t" \
  "s_waitcnt vmcnt(5)\n\t" \
  "v_lshlrev_b32 v128, 16, v32\n\t" \
  "v_and_b32 v129, 0xffff0000, v32\n\t" \
  "v_lshlrev_b32 v130, 16, v33\n\t" \
  "v_and_b32 v131, 0xffff0000, v33\n\t" \
  "v_lshlrev_b32 v132, 16, v34\n\t" \
  "v_and_b32 v133, 0xffff0000, v34\n\t" \
  "v_lshlrev_b32 v134, 16, v35\n\t" \
  "v_and_b32 v135, 0xffff0000, v35\n\t" \
  "v_mul_f32 v104, v104, v128\n\t" \
  "v_mul_f32 v105, v105, v129\n\t" \
  "v_mul_f32 v106, v106, v130\n\t" \
  "v_mul_f32 v107, v107, v131\n\t" \
  "v_mul_f32 v108, v108, v132\n\t" \
  "v_mul_f32 v109, v109, v133\n\t" \
  "v_mul_f32 v110, v110, v134\n\t" \
  "v_mul_f32 v111, v111, v135\n\t" \
  "v_cvt_pk_bf16_f32 v140, v104, v105\n\t" \
  "v_cvt_pk_bf16_f32 v141, v106, v107\n\t" \
  "v_cvt_pk_bf16_f32 v142, v108, v109\n\t" \
  "v_cvt_pk_bf16_f32 v143, v110, v111\n\t" \
  "v_mov_b32_dpp v128, v140 row_ror:8 row_mask:0xf bank_mask:0xf\n\t" \
  "v_mov_b32_dpp v129, v141 row_ror:8 row_mask:0xf bank_mask:0xf\n\t" \
  "s_barrier\n\t" \
  "v_mov_b32_dpp v130, v142 row_ror:8 row_mask:0xf bank_mask:0xf\n\t" \
  "v_mov_b32_dpp v131, v143 row_ror:8 row_mask:0xf bank_mask:0xf\n\t" \
  "v_perm_b32 v140, v128, v140, v157\n\t" \
  "v_perm_b32 v141, v129, v141, v157\n\t" \
  "v_perm_b32 v142, v130, v142, v157\n\t" \
  "v_perm_b32 v143, v131, v143, v157\n\t" \
  "v_permlane16_swap_b32 v140, v141\n\t" \
  "s_nop 0\n\t" \
  "v_permlane16_swap_b32 v142, v143\n\t" \
  "s_nop 1\n\t" \
  "v_permlane32_swap_b32 v140, v142\n\t" \
  "v_permlane32_swap_b32 v141, v143\n\t" \
  "s_waitcnt vmcnt(4)\n\t" \
  "v_lshlrev_b32 v128, 16, v20\n\t" \
  "v_and_b32 v129, 0xffff0000, v20\n\t" \
  "v_lshlrev_b32 v130, 16, v21\n\t" \
  "v_and_b32 v131, 0xffff0000, v21\n\t" \
  "v_lshlrev_b32 v132, 16, v22\n\t" \
  "v_and_b32 v133, 0xffff0000, v22\n\t" \
  "v_lshlrev_b32 v134, 16, v23\n\t" \
  "v_and_b32 v135, 0xffff0000, v23\n\t" \
  "v_fmac_f32 v112, v128, v64\n\t" \
  "v_fmac_f32 v113, v129, v68\n\t" \
  "v_fmac_f32 v114, v130, v72\n\t" \
  "v_fmac_f32 v115, v131, v76\n\t" \
  "v_fmac_f32 v116, v132, v80\n\t" \
  "v_fmac_f32 v117, v133, v84\n\t" \
  "v_fmac_f32 v118, v134, v88\n\t" \
  "v_fmac_f32 v119, v135, v92\n\t" \
  "v_fmac_f32 v120, v128, v65\n\t" \
  "v_fmac_f32 v121, v129, v69\n\t" \
  "v_fmac_f32 v122, v130, v73\n\t" \
  "v_fmac_f32 v123, v131, v77\n\t" \
  "v_fmac_f32 v124, v132, v81\n\t" \
  "v_fmac_f32 v125, v133, v85\n\t" \
  "v_fmac_f32 v126, v134, v89\n\t" \
  "v_fmac_f32 v127, v135, v93\n\t" \
  "s_waitcnt vmcnt(3)\n\t" \
  "v_lshlrev_b32 v128, 16, v36\n\t" \
  "v_and_b32 v129, 0xffff0000, v36\n\t" \
  "v_lshlrev_b32 v130, 16, v37\n\t" \
  "v_and_b32 v131, 0xffff0000, v37\n\t" \
  "s_setprio 3\n\t" \
  "s_barrier\n\t" \
  "global_store_dwordx4 v159, v[140:143], %[sb] sc1\n\t" \
  "v_add_u32 v158, 0x80, v159\n\t" \
  "v_bfi_b32 v159, %[mk], v158, v159\n\t" \
  "s_mov_b32 m0, %[hd]\n\t" \
  "s_nop 0\n\t" \
  "global_load_lds_dwordx4 v163, %[hp] offset:0\n\t" \
  "global_load_lds_dwordx4 v163, %[hp] offset:1024\n\t" \
  "global_load_lds_dwordx4 v163, %[hp] offset:2048\n\t" \
  "global_load_lds_dwordx4 v163, %[hp] offset:3072\n\t" \
  "s_setprio 2\n\t" \
  "v_lshlrev_b32 v132, 16, v38\n\t" \
  "v_and_b32 v133, 0xffff0000, v38\n\t" \
  "v_lshlrev_b32 v134, 16, v39\n\t" \
  "v_and_b32 v135, 0xffff0000, v39\n\t" \
  "v_mul_f32 v112, v112, v128\n\t" \
  "v_mul_f32 v113, v113, v129\n\t" \
  "v_mul_f32 v114, v114, v130\n\t" \
  "v_mul_f32 v115, v115, v131\n\t" \
  "v_mul_f32 v116, v116, v132\n\t" \
  "v_mul_f32 v117, v117, v133\n\t" \
  "v_mul_f32 v118, v118, v134\n\t" \
  "v_mul_f32 v119, v119, v135\n\t" \
  "v_cvt_pk_bf16_f32 v144, v112, v113\n\t" \
  "v_cvt_pk_bf16_f32 v145, v114, v115\n\t" \
  "v_cvt_pk_bf16_f32 v146, v116, v117\n\t" \
  "v_cvt_pk_bf16_f32 v147, v118, v119\n\t" \
  "v_mov_b32_dpp v128, v144 row_ror:8 row_mask:0xf bank_mask:0xf\n\t" \
  "v_mov_b32_dpp v129, v145 row_ror:8 row_mask:0xf bank_mask:0xf\n\t" \
  "v_mov_b32_dpp v130, v146 row_ror:8 row_mask:0xf bank_mask:0xf\n\t" \
  "v_mov_b32_dpp v131, v147 row_ror:8 row_mask:0xf bank_mask:0xf\n\t" \
  "v_perm_b32 v144, v128, v144, v157\n\t" \
  "v_perm_b32 v145, v129, v145, v157\n\t" \
  "v_perm_b32 v146, v130, v146, v157\n\t" \
  "v_perm_b32 v147, v131, v147, v157\n\t" \
  "v_permlane16_swap_b32 v144, v145\n\t" \
  "s_nop 0\n\t" \
  "v_permlane16_swap_b32 v146, v147\n\t" \
  "s_nop 1\n\t" \
  "v_permlane32_swap_b32 v144, v146\n\t" \
  "v_permlane32_swap_b32 v145, v147\n\t" \
  "s_waitcnt vmcnt(7)\n\t" \
  "v_lshlrev_b32 v128, 16, v24\n\t" \
  "v_and_b32 v129, 0xffff0000, v24\n\t" \
  "v_lshlrev_b32 v130, 16, v25\n\t" \
  "v_and_b32 v131, 0xffff0000, v25\n\t" \
  "v_lshlrev_b32 v132, 16, v26\n\t" \
  "v_and_b32 v133, 0xffff0000, v26\n\t" \
  "v_lshlrev_b32 v134, 16, v27\n\t" \
  "v_and_b32 v135, 0xffff0000, v27\n\t" \
  "v_fmac_f32 v120, v128, v64\n\t" \
  "v_fmac_f32 v121, v129, v68\n\t" \
  "s_setprio 3\n\t" \
  "s_barrier\n\t" \
  "global_store_dwordx4 v159, v[144:147], %[sb] sc1\n\t" \
  "v_add_u32 v158, 0x80, v159\n\t" \
  "v_bfi_b32 v159, %[mk], v158, v159\n\t" \
  "s_setprio 2\n\t" \
  "v_fmac_f32 v122, v130, v72\n\t" \
  "v_fmac_f32 v123, v131, v76\n\t" \
  "v_fmac_f32 v124, v132, v80\n\t" \
  "v_fmac_f32 v125, v133, v84\n\t" \
  "v_fmac_f32 v126, v134, v88\n\t" \
  "v_fmac_f32 v127, v135, v92\n\t" \
  "s_waitcnt vmcnt(7)\n\t" \
  "v_lshlrev_b32 v128, 16, v40\n\t" \
  "v_and_b32 v129, 0xffff0000, v40\n\t" \
  "v_lshlrev_b32 v130, 16, v41\n\t" \
  "v_and_b32 v131, 0xffff0000, v41\n\t" \
  "v_lshlrev_b32 v132, 16, v42\n\t" \
  "v_and_b32 v133, 0xffff0000, v42\n\t" \
  "v_lshlrev_b32 v134, 16, v43\n\t" \
  "v_and_b32 v135, 0xffff0000, v43\n\t" \
  "v_mul_f32 v120, v120, v128\n\t" \
  "v_mul_f32 v121, v121, v129\n\t" \
  "v_mul_f32 v122, v122, v130\n\t" \
  "v_mul_f32 v123, v123, v131\n\t" \
  "v_mul_f32 v124, v124, v132\n\t" \
  "v_mul_f32 v125, v125, v133\n\t" \
  "v_mul_f32 v126, v126, v134\n\t" \
  "v_mul_f32 v127, v127, v135\n\t" \
  "v_cvt_pk_bf16_f32 v148, v120, v121\n\t" \
  "v_cvt_pk_bf16_f32 v149, v122, v123\n\t" \
  "v_cvt_pk_bf16_f32 v150, v124, v125\n\t" \
  "v_cvt_pk_bf16_f32 v151, v126, v127\n\t" \
  "v_mov_b32_dpp v128, v148 row_ror:8 row_mask:0xf bank_mask:0xf\n\t" \
  "v_mov_b32_dpp v129, v149 row_ror:8 row_mask:0xf bank_mask:0xf\n\t" \
  "v_mov_b32_dpp v130, v150 row_ror:8 row_mask:0xf bank_mask:0xf\n\t" \
  "v_mov_b32_dpp v131, v151 row_ror:8 row_mask:0xf bank_mask:0xf\n\t" \
  "v_perm_b32 v148, v128, v148, v157\n\t" \
  "v_perm_b32 v149, v129, v149, v157\n\t" \
  "v_perm_b32 v150, v130, v150, v157\n\t" \
  "v_perm_b32 v151, v131, v151, v157\n\t" \
  "v_permlane16_swap_b32 v148, v149\n\t" \
  "s_nop 0\n\t" \
  "v_permlane16_swap_b32 v150, v151\n\t" \
  "s_nop 1\n\t" \
  "v_permlane32_swap_b32 v148, v150\n\t" \
  "v_permlane32_swap_b32 v149, v151\n\t" \
  "s_setprio 3\n\t" \
  "s_barrier\n\t" \
  "global_store_dwordx4 v159, v[148:151], %[sb] sc1\n\t" \
  "s_setprio 2\n\t" \
  "s_waitcnt vmcnt(2)\n\t" \
  "ds_read_b64_tr_b16 v[64:65], v165 offset:32\n\t" \
  "ds_read_b64_tr_b16 v[66:67], v165 offset:0\n\t" \
  "ds_read_b64_tr_b16 v[68:69], v165 offset:288\n\t" \
  "ds_read_b64_tr_b16 v[70:71], v165 offset:256\n\t" \
  "ds_read_b64_tr_b16 v[72:73], v165 offset:544\n\t" \
  "ds_read_b64_tr_b16 v[74:75], v165 offset:512\n\t" \
  "ds_read_b64_tr_b16 v[76:77], v165 offset:800\n\t" \
  "ds_read_b64_tr_b16 v[78:79], v165 offset:768\n\t" \
  "v_mov_b32 v24, v166\n\t" \
  "v_mov_b32 v25, v167\n\t" \
  "v_mov_b32 v63, 0xc0\n\t" \
  "v_add_u32 v158, 64, v24\n\t" \
  "v_bfi_b32 v26, v63, v158, v24\n\t" \
  "v_add_u32 v158, 64, v25\n\t" \
  "v_bfi_b32 v27, v63, v158, v25\n\t" \
  "v_add_u32 v158, 64, v26\n\t" \
  "v_bfi_b32 v28, v63, v158, v26\n\t" \
  "v_add_u32 v158, 64, v27\n\t" \
  "v_bfi_b32 v29, v63, v158, v27\n\t" \
  "v_add_u32 v158, 64, v28\n\t" \
  "v_bfi_b32 v30, v63, v158, v28\n\t" \
  "v_add_u32 v158, 64, v29\n\t" \
  "v_bfi_b32 v31, v63, v158, v29\n\t" \
  "s_barrier\n\t" \
  "ds_read_b64_tr_b16 v[80:81], v165 offset:1056\n\t" \
  "ds_read_b64_tr_b16 v[82:83], v165 offset:1024\n\t" \
  "ds_read_b64_tr_b16 v[84:85], v165 offset:1312\n\t" \
  "ds_read_b64_tr_b16 v[86:87], v165 offset:1280\n\t" \
  "ds_read_b64_tr_b16 v[88:89], v165 offset:1568\n\t" \
  "ds_read_b64_tr_b16 v[90:91], v165 offset:1536\n\t" \
  "ds_read_b64_tr_b16 v[92:93], v165 offset:1824\n\t" \
  "ds_read_b64_tr_b16 v[94:95], v165 offset:1792\n\t" \
  "s_setprio 3\n\t" \
  "s_barrier\n\t" \
  "s_waitcnt vmcnt(0)\n\t" \
  "v_add_u32 v61, 1, v160\n\t" \
  "s_mov_b64 exec, 1\n\t" \
  "global_store_dword v60, v61, %[ff] sc1\n\t" \
  "s_mov_b64 exec, -1\n\t" \
  "s_setprio 2\n\t" \
  "ds_read_b64_tr_b16 v[96:97], v165 offset:2080\n\t" \
  "ds_read_b64_tr_b16 v[98:99], v165 offset:2048\n\t" \
  "ds_read_b64_tr_b16 v[100:101], v165 offset:2336\n\t" \
  "ds_read_b64_tr_b16 v[102:103], v165 offset:2304\n\t" \
  "ds_read_b64_tr_b16 v[104:105], v165 offset:2592\n\t" \
  "ds_read_b64_tr_b16 v[106:107], v165 offset:2560\n\t" \
  "ds_read_b64_tr_b16 v[108:109], v165 offset:2848\n\t" \
  "ds_read_b64_tr_b16 v[110:111], v165 offset:2816\n\t" \
  "s_barrier\n\t" \
  "ds_read_b64_tr_b16 v[112:113], v165 offset:3104\n\t" \
  "ds_read_b64_tr_b16 v[114:115], v165 offset:3072\n\t" \
  "ds_read_b64_tr_b16 v[116:117], v165 offset:3360\n\t" \
  "ds_read_b64_tr_b16 v[118:119], v165 offset:3328\n\t" \
  "ds_read_b64_tr_b16 v[120:121], v165 offset:3616\n\t" \
  "ds_read_b64_tr_b16 v[122:123], v165 offset:3584\n\t" \
  "ds_read_b64_tr_b16 v[124:125], v165 offset:3872\n\t" \
  "ds_read_b64_tr_b16 v[126:127], v165 offset:3840\n\t" \
  "s_barrier\n\t" \
  "s_waitcnt lgkmcnt(0)\n\t" \
  "v_lshlrev_b32 v61, 16, v161\n\t" \
  "v_lshlrev_b32 v62, 16, v162\n\t" \
  "v_add_f32 v61, v61, v62\n\t" \
  "v_mov_b32 v62, 0xffff\n\t" \
  "v_cvt_pk_bf16_f32 v61, v61, v61\n\t" \
  "s_mov_b64 exec, 1\n\t" \
  "v_bfi_b32 v66, v62, v61, v66\n\t" \
  "s_mov_b64 exec, 15\n\t" \
  "v_and_b32 v66, v62, v66\n\t" \
  "s_mov_b64 exec, 16\n\t" \
  "v_bfi_b32 v66, v62, v66, v61\n\t" \
  "s_mov_b64 exec, 0xff\n\t" \
  "v_mov_b32 v67, 0\n\t" \
  "s_mov_b64 exec, 0x100\n\t" \
  "v_mov_b32 v67, v61\n\t" \
  "s_mov_b64 exec, 0xf00\n\t" \
  "v_and_b32 v67, v62, v67\n\t" \
  "s_mov_b64 exec, 0x1000\n\t" \
  "v_bfi_b32 v67, v62, v67, v61\n\t" \
  "s_mov_b64 exec, -1\n\t" \
  "s_waitcnt lgkmcnt(0)\n\t"
#define PP_NB 15
#define PP_Q 0
#define PP_CLOB "v0", "v1", "v2", "v3", "v4", "v5", "v6", "v7", "v8", "v9", "v10", "v11", "v12", "v13", "v14", "v15", "v16", "v17", "v18", "v19", "v20", "v21", "v22", "v23", "v32", "v33", "v34", "v35", "v36", "v37", "v38", "v39", "v40", "v41", "v42", "v43", "v60", "v61", "v62", "v63", "v128", "v129", "v130", "v131", "v132", "v133", "v134", "v135", "v136", "v137", "v138", "v139", "v140", "v141", "v142", "v143", "v144", "v145", "v146", "v147", "v148", "v149", "v150", "v151", "v158", "v161", "v162"

#define LD_ASM1 \
  "s_add_u32 m0, s39, 49152\n\t" \
  "s_mov_b32 s68, 0x0\n\t" \
  "buffer_load_dwordx4 v5, s[44:47], s68 offen offset:0 lds\n\t" \
  "buffer_load_dwordx4 v6, s[44:47], s68 offen offset:1024 lds\n\t" \
  "buffer_load_dwordx4 v7, s[44:47], s68 offen offset:2048 lds\n\t" \
  "s_add_u32 m0, s38, 36864\n\t" \
  "s_mov_b32 s68, 0x0\n\t" \
  "buffer_load_dwordx4 v0, s[40:43], s68 offen offset:0 lds\n\t" \
  "buffer_load_dwordx4 v1, s[40:43], s68 offen offset:1024 lds\n\t" \
  "s_mov_b64 exec, 0xffff\n\t" \
  "buffer_load_dwordx4 v4, s[40:43], s68 offen offset:2048 lds\n\t" \
  "s_mov_b64 exec, -1\n\t" \
  "s_add_u32 m0, s38, 46080\n\t" \
  "s_mov_b32 s68, 0x24000\n\t" \
  "buffer_load_dwordx4 v2, s[40:43], s68 offen offset:0 lds\n\t" \
  "buffer_load_dwordx4 v3, s[40:43], s68 offen offset:1024 lds\n\t" \
  "s_mov_b64 exec, 0xffff\n\t" \
  "buffer_load_dwordx4 v4, s[40:43], s68 offen offset:2048 lds\n\t" \
  "s_mov_b64 exec, -1\n\t" \
  "s_mov_b32 m0, %[sk]\n\t" \
  "s_mov_b32 exec_hi, 0\n\t" \
  "buffer_load_dword v9, s[40:43], 0 offen lds\n\t" \
  "s_mov_b32 exec_lo, %[wm]\n\t" \
  "buffer_load_dword v10, s[44:47], 0 offen lds\n\t" \
  "s_mov_b64 exec, -1\n\t" \
  "s_add_u32 m0, s39, 61440\n\t" \
  "s_mov_b32 s68, 0x30000\n\t" \
  "buffer_load_dwordx4 v5, s[44:47], s68 offen offset:0 lds\n\t" \
  "buffer_load_dwordx4 v6, s[44:47], s68 offen offset:1024 lds\n\t" \
  "buffer_load_dwordx4 v7, s[44:47], s68 offen offset:2048 lds\n\t"
#define LD_ASM2 \
  "s_mov_b32 s85, 0x180000\n\t" \
  "s_load_dword s84, %[pw], s85\n\t" \
  "s_mov_b32 s85, 0x180800\n\t" \
  "s_load_dword s84, %[pw], s85\n\t" \
  "s_mov_b32 s85, 0x181000\n\t" \
  "s_load_dword s84, %[pw], s85\n\t" \
  "s_mov_b32 s85, 0x1b0000\n\t" \
  "s_load_dword s84, %[pw], s85\n\t" \
  "s_mov_b32 s85, 0x1b0800\n\t" \
  "s_load_dword s84, %[pw], s85\n\t" \
  "s_mov_b32 s85, 0x1b1000\n\t" \
  "s_load_dword s84, %[pw], s85\n\t" \
  "s_mov_b32 s85, 0x1e0000\n\t" \
  "s_load_dword s84, %[pw], s85\n\t" \
  "s_mov_b32 s85, 0x1e0800\n\t" \
  "s_load_dword s84, %[pw], s85\n\t" \
  "s_mov_b32 m0, %[tsk]\n\t" \
  "s_mov_b32 s85, 0x0\n\t" \
  "s_mov_b64 exec, 63\n\t" \
  "buffer_load_dword v16, %[sh], s85 offen lds\n\t" \
  "s_mov_b64 exec, -1\n\t" \
  "s_mov_b32 s68, 0x80\n\t" \
  "s_mov_b32 s69, 0x24080\n\t" \
  "s_mov_b32 s70, 0x60000\n\t" \
  "s_add_u32 m0, s38, 0\n\t" \
  "s_waitcnt vmcnt(6)\n\t" \
  "s_barrier\n\t" \
  "buffer_load_dwordx4 v0, s[40:43], s68 offen offset:0 lds\n\t" \
  "s_mov_b32 s85, 0x1e1000\n\t" \
  "s_load_dword s84, %[pw], s85\n\t" \
  "buffer_load_dwordx4 v1, s[40:43], s68 offen offset:1024 lds\n\t" \
  "s_mov_b32 s85, 0x210000\n\t" \
  "s_load_dword s84, %[pw], s85\n\t" \
  "s_mov_b64 exec, 0xffff\n\t" \
  "buffer_load_dwordx4 v4, s[40:43], s68 offen offset:2048 lds\n\t" \
  "s_add_u32 m0, s38, 9216\n\t" \
  "s_mov_b64 exec, -1\n\t" \
  "buffer_load_dwordx4 v2, s[40:43], s69 offen offset:0 lds\n\t" \
  "s_mov_b32 s85, 0x210800\n\t" \
  "s_load_dword s84, %[pw], s85\n\t" \
  "buffer_load_dwordx4 v3, s[40:43], s69 offen offset:1024 lds\n\t" \
  "s_mov_b64 exec, 0xffff\n\t" \
  "buffer_load_dwordx4 v4, s[40:43], s69 offen offset:2048 lds\n\t" \
  "s_add_u32 m0, s39, 0\n\t" \
  "s_mov_b64 exec, -1\n\t" \
  "buffer_load_dwordx4 v5, s[44:47], s70 offen offset:0 lds\n\t" \
  "buffer_load_dwordx4 v6, s[44:47], s70 offen offset:1024 lds\n\t" \
  "buffer_load_dwordx4 v7, s[44:47], s70 offen offset:2048 lds\n\t" \
  "s_mov_b32 m0, %[tsk]\n\t" \
  "s_mov_b32 s85, 0x180000\n\t" \
  "s_mov_b64 exec, 63\n\t" \
  "buffer_load_dword v16, %[sh], s85 offen lds\n\t" \
  "s_mov_b64 exec, -1\n\t" \
  "s_mov_b32 s68, 0x90000\n\t" \
  "s_mov_b32 s69, 0x100\n\t" \
  "s_mov_b32 s70, 0x24100\n\t" \
  "s_add_u32 m0, s39, 12288\n\t" \
  "s_waitcnt vmcnt(11)\n\t" \
  "s_barrier\n\t" \
  "buffer_load_dwordx4 v5, s[44:47], s68 offen offset:0 lds\n\t" \
  "s_mov_b32 s85, 0x211000\n\t" \
  "s_load_dword s84, %[pw], s85\n\t" \
  "buffer_load_dwordx4 v6, s[44:47], s68 offen offset:1024 lds\n\t" \
  "s_mov_b32 s85, 0x240000\n\t" \
  "s_load_dword s84, %[pw], s85\n\t" \
  "buffer_load_dwordx4 v7, s[44:47], s68 offen offset:2048 lds\n\t" \
  "s_mov_b32 s85, 0x240800\n\t" \
  "s_load_dword s84, %[pw], s85\n\t" \
  "s_add_u32 m0, s38, 18432\n\t" \
  "s_nop 0\n\t" \
  "buffer_load_dwordx4 v0, s[40:43], s69 offen offset:0 lds\n\t" \
  "buffer_load_dwordx4 v1, s[40:43], s69 offen offset:1024 lds\n\t" \
  "s_mov_b64 exec, 0xffff\n\t" \
  "buffer_load_dwordx4 v4, s[40:43], s69 offen offset:2048 lds\n\t" \
  "s_add_u32 m0, s38, 27648\n\t" \
  "s_mov_b64 exec, -1\n\t" \
  "buffer_load_dwordx4 v2, s[40:43], s70 offen offset:0 lds\n\t" \
  "buffer_load_dwordx4 v3, s[40:43], s70 offen offset:1024 lds\n\t" \
  "s_mov_b64 exec, 0xffff\n\t" \
  "buffer_load_dwordx4 v4, s[40:43], s70 offen offset:2048 lds\n\t" \
  "s_mov_b64 exec, -1\n\t" \
  "s_mov_b32 s68, 0xc0000\n\t" \
  "s_mov_b32 s69, 0xf0000\n\t" \
  "s_mov_b32 s70, 0x180\n\t" \
  "s_add_u32 m0, s39, 24576\n\t" \
  "s_waitcnt vmcnt(10)\n\t" \
  "s_barrier\n\t" \
  "buffer_load_dwordx4 v5, s[44:47], s68 offen offset:0 lds\n\t" \
  "s_mov_b32 s85, 0x241000\n\t" \
  "s_load_dword s84, %[pw], s85\n\t" \
  "buffer_load_dwordx4 v6, s[44:47], s68 offen offset:1024 lds\n\t" \
  "s_mov_b32 s85, 0x270000\n\t" \
  "s_load_dword s84, %[pw], s85\n\t" \
  "buffer_load_dwordx4 v7, s[44:47], s68 offen offset:2048 lds\n\t" \
  "s_mov_b32 s85, 0x270800\n\t" \
  "s_load_dword s84, %[pw], s85\n\t" \
  "s_add_u32 m0, s39, 36864\n\t" \
  "s_nop 0\n\t" \
  "buffer_load_dwordx4 v5, s[44:47], s69 offen offset:0 lds\n\t" \
  "buffer_load_dwordx4 v6, s[44:47], s69 offen offset:1024 lds\n\t" \
  "buffer_load_dwordx4 v7, s[44:47], s69 offen offset:2048 lds\n\t" \
  "s_add_u32 m0, s38, 36864\n\t" \
  "s_nop 0\n\t" \
  "buffer_load_dwordx4 v0, s[40:43], s70 offen offset:0 lds\n\t" \
  "buffer_load_dwordx4 v1, s[40:43], s70 offen offset:1024 lds\n\t" \
  "s_mov_b64 exec, 0xffff\n\t" \
  "buffer_load_dwordx4 v4, s[40:43], s70 offen offset:2048 lds\n\t" \
  "s_mov_b64 exec, -1\n\t" \
  "s_mov_b32 s68, 0x120000\n\t" \
  "s_mov_b32 s69, 0x24180\n\t" \
  "s_mov_b32 s70, 0x150000\n\t" \
  "s_add_u32 m0, s39, 49152\n\t" \
  "s_waitcnt vmcnt(15)\n\t" \
  "s_barrier\n\t" \
  "buffer_load_dwordx4 v5, s[44:47], s68 offen offset:0 lds\n\t" \
  "s_mov_b32 s85, 0x271000\n\t" \
  "s_load_dword s84, %[pw], s85\n\t" \
  "buffer_load_dwordx4 v6, s[44:47], s68 offen offset:1024 lds\n\t" \
  "s_mov_b32 s85, 0x2a0000\n\t" \
  "s_load_dword s84, %[pw], s85\n\t" \
  "buffer_load_dwordx4 v7, s[44:47], s68 offen offset:2048 lds\n\t" \
  "s_mov_b32 s85, 0x2a0800\n\t" \
  "s_load_dword s84, %[pw], s85\n\t" \
  "s_add_u32 m0, s38, 46080\n\t" \
  "s_nop 0\n\t" \
  "buffer_load_dwordx4 v2, s[40:43], s69 offen offset:0 lds\n\t" \
  "buffer_load_dwordx4 v3, s[40:43], s69 offen offset:1024 lds\n\t" \
  "s_mov_b64 exec, 0xffff\n\t" \
  "buffer_load_dwordx4 v4, s[40:43], s69 offen offset:2048 lds\n\t" \
  "s_add_u32 m0, s39, 61440\n\t" \
  "s_mov_b64 exec, -1\n\t" \
  "buffer_load_dwordx4 v5, s[44:47], s70 offen offset:0 lds\n\t" \
  "buffer_load_dwordx4 v6, s[44:47], s70 offen offset:1024 lds\n\t" \
  "buffer_load_dwordx4 v7, s[44:47], s70 offen offset:2048 lds\n\t" \
  "s_mov_b32 s68, 0x200\n\t" \
  "s_mov_b32 s69, 0x180000\n\t" \
  "s_mov_b32 s70, 0x24200\n\t" \
  "s_add_u32 m0, s38, 0\n\t" \
  "s_waitcnt vmcnt(15)\n\t" \
  "s_barrier\n\t" \
  "buffer_load_dwordx4 v0, s[40:43], s68 offen offset:0 lds\n\t" \
  "s_mov_b32 s85, 0x2a1000\n\t" \
  "s_load_dword s84, %[pw], s85\n\t" \
  "buffer_load_dwordx4 v1, s[40:43], s68 offen offset:1024 lds\n\t" \
  "s_mov_b32 s85, 0x2d0000\n\t" \
  "s_load_dword s84, %[pw], s85\n\t" \
  "s_mov_b64 exec, 0xffff\n\t" \
  "buffer_load_dwordx4 v4, s[40:43], s68 offen offset:2048 lds\n\t" \
  "s_add_u32 m0, s39, 0\n\t" \
  "s_mov_b64 exec, -1\n\t" \
  "buffer_load_dwordx4 v5, s[44:47], s69 offen offset:0 lds\n\t" \
  "s_mov_b32 s85, 0x2d0800\n\t" \
  "s_load_dword s84, %[pw], s85\n\t" \
  "buffer_load_dwordx4 v6, s[44:47], s69 offen offset:1024 lds\n\t" \
  "buffer_load_dwordx4 v7, s[44:47], s69 offen offset:2048 lds\n\t" \
  "s_add_u32 m0, s38, 9216\n\t" \
  "s_nop 0\n\t" \
  "buffer_load_dwordx4 v2, s[40:43], s70 offen offset:0 lds\n\t" \
  "buffer_load_dwordx4 v3, s[40:43], s70 offen offset:1024 lds\n\t" \
  "s_mov_b64 exec, 0xffff\n\t" \
  "buffer_load_dwordx4 v4, s[40:43], s70 offen offset:2048 lds\n\t" \
  "s_mov_b64 exec, -1\n\t" \
  "s_sleep 2\n\t" \
  "s_mov_b32 s68, 0x1b0000\n\t" \
  "s_add_u32 m0, s39, 12288\n\t" \
  "s_waitcnt vmcnt(21)\n\t" \
  "s_barrier\n\t" \
  "buffer_load_dwordx4 v5, s[44:47], s68 offen offset:0 lds\n\t" \
  "s_mov_b32 s85, 0x2d1000\n\t" \
  "s_load_dword s84, %[pw], s85\n\t" \
  "buffer_load_dwordx4 v6, s[44:47], s68 offen offset:1024 lds\n\t" \
  "s_mov_b32 s85, 0x300000\n\t" \
  "s_load_dword s84, %[pw], s85\n\t" \
  "buffer_load_dwordx4 v7, s[44:47], s68 offen offset:2048 lds\n\t" \
  "s_mov_b32 s85, 0x300800\n\t" \
  "s_load_dword s84, %[pw], s85\n\t" \
  "s_sleep 2\n\t" \
  "s_mov_b32 s68, 0x280\n\t" \
  "s_mov_b32 s69, 0x1e0000\n\t" \
  "s_mov_b32 s70, 0x24280\n\t" \
  "s_add_u32 m0, s38, 18432\n\t" \
  "s_waitcnt vmcnt(15)\n\t" \
  "s_barrier\n\t" \
  "buffer_load_dwordx4 v0, s[40:43], s68 offen offset:0 lds\n\t" \
  "s_mov_b32 s85, 0x301000\n\t" \
  "s_load_dword s84, %[pw], s85\n\t" \
  "buffer_load_dwordx4 v1, s[40:43], s68 offen offset:1024 lds\n\t" \
  "s_mov_b32 s85, 0x330000\n\t" \
  "s_load_dword s84, %[pw], s85\n\t" \
  "s_mov_b64 exec, 0xffff\n\t" \
  "buffer_load_dwordx4 v4, s[40:43], s68 offen offset:2048 lds\n\t" \
  "s_add_u32 m0, s39, 24576\n\t" \
  "s_mov_b64 exec, -1\n\t" \
  "buffer_load_dwordx4 v5, s[44:47], s69 offen offset:0 lds\n\t" \
  "s_mov_b32 s85, 0x330800\n\t" \
  "s_load_dword s84, %[pw], s85\n\t" \
  "buffer_load_dwordx4 v6, s[44:47], s69 offen offset:1024 lds\n\t" \
  "buffer_load_dwordx4 v7, s[44:47], s69 offen offset:2048 lds\n\t" \
  "s_add_u32 m0, s38, 27648\n\t" \
  "s_nop 0\n\t" \
  "buffer_load_dwordx4 v2, s[40:43], s70 offen offset:0 lds\n\t" \
  "buffer_load_dwordx4 v3, s[40:43], s70 offen offset:1024 lds\n\t" \
  "s_mov_b64 exec, 0xffff\n\t" \
  "buffer_load_dwordx4 v4, s[40:43], s70 offen offset:2048 lds\n\t" \
  "s_mov_b64 exec, -1\n\t" \
  "s_sleep 2\n\t" \
  "s_mov_b32 s68, 0x210000\n\t" \
  "s_add_u32 m0, s39, 36864\n\t" \
  "s_waitcnt vmcnt(21)\n\t" \
  "s_barrier\n\t" \
  "buffer_load_dwordx4 v5, s[44:47], s68 offen offset:0 lds\n\t" \
  "s_mov_b32 s85, 0x331000\n\t" \
  "s_load_dword s84, %[pw], s85\n\t" \
  "buffer_load_dwordx4 v6, s[44:47], s68 offen offset:1024 lds\n\t" \
  "s_mov_b32 s85, 0x360000\n\t" \
  "s_load_dword s84, %[pw], s85\n\t" \
  "buffer_load_dwordx4 v7, s[44:47], s68 offen offset:2048 lds\n\t" \
  "s_mov_b32 s85, 0x360800\n\t" \
  "s_load_dword s84, %[pw], s85\n\t" \
  "s_sleep 2\n\t" \
  "s_mov_b32 s68, 0x300\n\t" \
  "s_mov_b32 s69, 0x240000\n\t" \
  "s_add_u32 m0, s38, 36864\n\t" \
  "s_waitcnt vmcnt(15)\n\t" \
  "s_barrier\n\t" \
  "buffer_load_dwordx4 v0, s[40:43], s68 offen offset:0 lds\n\t" \
  "s_mov_b32 s85, 0x361000\n\t" \
  "s_load_dword s84, %[pw], s85\n\t" \
  "buffer_load_dwordx4 v1, s[40:43], s68 offen offset:1024 lds\n\t" \
  "s_mov_b32 s85, 0x390000\n\t" \
  "s_load_dword s84, %[pw], s85\n\t" \
  "s_mov_b64 exec, 0xffff\n\t" \
  "buffer_load_dwordx4 v4, s[40:43], s68 offen offset:2048 lds\n\t" \
  "s_add_u32 m0, s39, 49152\n\t" \
  "s_mov_b64 exec, -1\n\t" \
  "buffer_load_dwordx4 v5, s[44:47], s69 offen offset:0 lds\n\t" \
  "s_mov_b32 s85, 0x390800\n\t" \
  "s_load_dword s84, %[pw], s85\n\t" \
  "buffer_load_dwordx4 v6, s[44:47], s69 offen offset:1024 lds\n\t" \
  "buffer_load_dwordx4 v7, s[44:47], s69 offen offset:2048 lds\n\t" \
  "s_mov_b32 m0, %[tsk]\n\t" \
  "s_mov_b32 s85, 0x300000\n\t" \
  "s_mov_b64 exec, 63\n\t" \
  "buffer_load_dword v16, %[sh], s85 offen lds\n\t" \
  "s_mov_b64 exec, -1\n\t" \
  "s_sleep 2\n\t" \
  "s_mov_b32 s68, 0x24300\n\t" \
  "s_mov_b32 s69, 0x270000\n\t" \
  "s_add_u32 m0, s38, 46080\n\t" \
  "s_waitcnt vmcnt(19)\n\t" \
  "s_barrier\n\t" \
  "buffer_load_dwordx4 v2, s[40:43], s68 offen offset:0 lds\n\t" \
  "s_mov_b32 s85, 0x391000\n\t" \
  "s_load_dword s84, %[pw], s85\n\t" \
  "buffer_load_dwordx4 v3, s[40:43], s68 offen offset:1024 lds\n\t" \
  "s_mov_b32 s85, 0x3c0000\n\t" \
  "s_load_dword s84, %[pw], s85\n\t" \
  "s_mov_b64 exec, 0xffff\n\t" \
  "buffer_load_dwordx4 v4, s[40:43], s68 offen offset:2048 lds\n\t" \
  "s_add_u32 m0, s39, 61440\n\t" \
  "s_mov_b64 exec, -1\n\t" \
  "buffer_load_dwordx4 v5, s[44:47], s69 offen offset:0 lds\n\t" \
  "s_mov_b32 s85, 0x3c0800\n\t" \
  "s_load_dword s84, %[pw], s85\n\t" \
  "buffer_load_dwordx4 v6, s[44:47], s69 offen offset:1024 lds\n\t" \
  "buffer_load_dwordx4 v7, s[44:47], s69 offen offset:2048 lds\n\t" \
  "s_sleep 2\n\t" \
  "s_mov_b32 s68, 0x380\n\t" \
  "s_mov_b32 s69, 0x2a0000\n\t" \
  "s_add_u32 m0, s38, 0\n\t" \
  "s_waitcnt vmcnt(16)\n\t" \
  "s_barrier\n\t" \
  "buffer_load_dwordx4 v0, s[40:43], s68 offen offset:0 lds\n\t" \
  "s_mov_b32 s85, 0x3c1000\n\t" \
  "s_load_dword s84, %[pw], s85\n\t" \
  "buffer_load_dwordx4 v1, s[40:43], s68 offen offset:1024 lds\n\t" \
  "s_mov_b32 s85, 0x3f0000\n\t" \
  "s_load_dword s84, %[pw], s85\n\t" \
  "s_mov_b64 exec, 0xffff\n\t" \
  "buffer_load_dwordx4 v4, s[40:43], s68 offen offset:2048 lds\n\t" \
  "s_add_u32 m0, s39, 0\n\t" \
  "s_mov_b64 exec, -1\n\t" \
  "buffer_load_dwordx4 v5, s[44:47], s69 offen offset:0 lds\n\t" \
  "s_mov_b32 s85, 0x3f0800\n\t" \
  "s_load_dword s84, %[pw], s85\n\t" \
  "buffer_load_dwordx4 v6, s[44:47], s69 offen offset:1024 lds\n\t" \
  "buffer_load_dwordx4 v7, s[44:47], s69 offen offset:2048 lds\n\t" \
  "s_mov_b32 s68, 0x24380\n\t" \
  "s_mov_b32 s69, 0x2d0000\n\t" \
  "s_add_u32 m0, s38, 9216\n\t" \
  "s_waitcnt vmcnt(19)\n\t" \
  "s_barrier\n\t" \
  "buffer_load_dwordx4 v2, s[40:43], s68 offen offset:0 lds\n\t" \
  "s_mov_b32 s85, 0x3f1000\n\t" \
  "s_load_dword s84, %[pw], s85\n\t" \
  "buffer_load_dwordx4 v3, s[40:43], s68 offen offset:1024 lds\n\t" \
  "s_mov_b32 s85, 0x420000\n\t" \
  "s_load_dword s84, %[pw], s85\n\t" \
  "s_mov_b64 exec, 0xffff\n\t" \
  "buffer_load_dwordx4 v4, s[40:43], s68 offen offset:2048 lds\n\t" \
  "s_add_u32 m0, s39, 12288\n\t" \
  "s_mov_b64 exec, -1\n\t" \
  "buffer_load_dwordx4 v5, s[44:47], s69 offen offset:0 lds\n\t" \
  "s_mov_b32 s85, 0x420800\n\t" \
  "s_load_dword s84, %[pw], s85\n\t" \
  "buffer_load_dwordx4 v6, s[44:47], s69 offen offset:1024 lds\n\t" \
  "buffer_load_dwordx4 v7, s[44:47], s69 offen offset:2048 lds\n\t" \
  "s_mov_b32 s68, 0x400\n\t" \
  "s_mov_b32 s69, 0x300000\n\t" \
  "s_add_u32 m0, s38, 18432\n\t" \
  "s_waitcnt vmcnt(15)\n\t" \
  "s_barrier\n\t" \
  "buffer_load_dwordx4 v0, s[40:43], s68 offen offset:0 lds\n\t" \
  "s_mov_b32 s85, 0x421000\n\t" \
  "s_load_dword s84, %[pw], s85\n\t" \
  "buffer_load_dwordx4 v1, s[40:43], s68 offen offset:1024 lds\n\t" \
  "s_mov_b32 s85, 0x450000\n\t" \
  "s_load_dword s84, %[pw], s85\n\t" \
  "s_mov_b64 exec, 0xffff\n\t" \
  "buffer_load_dwordx4 v4, s[40:43], s68 offen offset:2048 lds\n\t" \
  "s_add_u32 m0, s39, 24576\n\t" \
  "s_mov_b64 exec, -1\n\t" \
  "buffer_load_dwordx4 v5, s[44:47], s69 offen offset:0 lds\n\t" \
  "s_mov_b32 s85, 0x450800\n\t" \
  "s_load_dword s84, %[pw], s85\n\t" \
  "buffer_load_dwordx4 v6, s[44:47], s69 offen offset:1024 lds\n\t" \
  "buffer_load_dwordx4 v7, s[44:47], s69 offen offset:2048 lds\n\t" \
  "s_mov_b32 s68, 0x24400\n\t" \
  "s_mov_b32 s69, 0x330000\n\t" \
  "s_add_u32 m0, s38, 27648\n\t" \
  "s_waitcnt vmcnt(18)\n\t" \
  "s_barrier\n\t" \
  "buffer_load_dwordx4 v2, s[40:43], s68 offen offset:0 lds\n\t" \
  "s_mov_b32 s85, 0x451000\n\t" \
  "s_load_dword s84, %[pw], s85\n\t" \
  "buffer_load_dwordx4 v3, s[40:43], s68 offen offset:1024 lds\n\t" \
  "s_mov_b32 s85, 0x480000\n\t" \
  "s_load_dword s84, %[pw], s85\n\t" \
  "s_mov_b64 exec, 0xffff\n\t" \
  "buffer_load_dwordx4 v4, s[40:43], s68 offen offset:2048 lds\n\t" \
  "s_add_u32 m0, s39, 36864\n\t" \
  "s_mov_b64 exec, -1\n\t" \
  "buffer_load_dwordx4 v5, s[44:47], s69 offen offset:0 lds\n\t" \
  "s_mov_b32 s85, 0x480800\n\t" \
  "s_load_dword s84, %[pw], s85\n\t" \
  "buffer_load_dwordx4 v6, s[44:47], s69 offen offset:1024 lds\n\t" \
  "buffer_load_dwordx4 v7, s[44:47], s69 offen offset:2048 lds\n\t" \
  "s_mov_b32 s68, 0x480\n\t" \
  "s_mov_b32 s69, 0x360000\n\t" \
  "s_add_u32 m0, s38, 36864\n\t" \
  "s_waitcnt vmcnt(15)\n\t" \
  "s_barrier\n\t" \
  "buffer_load_dwordx4 v0, s[40:43], s68 offen offset:0 lds\n\t" \
  "s_mov_b32 s85, 0x481000\n\t" \
  "s_load_dword s84, %[pw], s85\n\t" \
  "buffer_load_dwordx4 v1, s[40:43], s68 offen offset:1024 lds\n\t" \
  "s_mov_b32 s85, 0x4b0000\n\t" \
  "s_load_dword s84, %[pw], s85\n\t" \
  "s_mov_b64 exec, 0xffff\n\t" \
  "buffer_load_dwordx4 v4, s[40:43], s68 offen offset:2048 lds\n\t" \
  "s_add_u32 m0, s39, 49152\n\t" \
  "s_mov_b64 exec, -1\n\t" \
  "buffer_load_dwordx4 v5, s[44:47], s69 offen offset:0 lds\n\t" \
  "s_mov_b32 s85, 0x4b0800\n\t" \
  "s_load_dword s84, %[pw], s85\n\t" \
  "buffer_load_dwordx4 v6, s[44:47], s69 offen offset:1024 lds\n\t" \
  "buffer_load_dwordx4 v7, s[44:47], s69 offen offset:2048 lds\n\t" \
  "s_mov_b32 s68, 0x24480\n\t" \
  "s_mov_b32 s69, 0x390000\n\t" \
  "s_add_u32 m0, s38, 46080\n\t" \
  "s_waitcnt vmcnt(18)\n\t" \
  "s_barrier\n\t" \
  "buffer_load_dwordx4 v2, s[40:43], s68 offen offset:0 lds\n\t" \
  "s_mov_b32 s85, 0x4b1000\n\t" \
  "s_load_dword s84, %[pw], s85\n\t" \
  "buffer_load_dwordx4 v3, s[40:43], s68 offen offset:1024 lds\n\t" \
  "s_mov_b32 s85, 0x4e0000\n\t" \
  "s_load_dword s84, %[pw], s85\n\t" \
  "s_mov_b64 exec, 0xffff\n\t" \
  "buffer_load_dwordx4 v4, s[40:43], s68 offen offset:2048 lds\n\t" \
  "s_add_u32 m0, s39, 61440\n\t" \
  "s_mov_b64 exec, -1\n\t" \
  "buffer_load_dwordx4 v5, s[44:47], s69 offen offset:0 lds\n\t" \
  "s_mov_b32 s85, 0x4e0800\n\t" \
  "s_load_dword s84, %[pw], s85\n\t" \
  "buffer_load_dwordx4 v6, s[44:47], s69 offen offset:1024 lds\n\t" \
  "buffer_load_dwordx4 v7, s[44:47], s69 offen offset:2048 lds\n\t" \
  "s_mov_b32 s68, 0x500\n\t" \
  "s_mov_b32 s69, 0x3c0000\n\t" \
  "s_add_u32 m0, s38, 0\n\t" \
  "s_waitcnt vmcnt(15)\n\t" \
  "s_barrier\n\t" \
  "buffer_load_dwordx4 v0, s[40:43], s68 offen offset:0 lds\n\t" \
  "s_mov_b32 s85, 0x4e1000\n\t" \
  "s_load_dword s84, %[pw], s85\n\t" \
  "buffer_load_dwordx4 v1, s[40:43], s68 offen offset:1024 lds\n\t" \
  "s_mov_b32 s85, 0x510000\n\t" \
  "s_load_dword s84, %[pw], s85\n\t" \
  "s_mov_b64 exec, 0xffff\n\t" \
  "buffer_load_dwordx4 v4, s[40:43], s68 offen offset:2048 lds\n\t" \
  "s_add_u32 m0, s39, 0\n\t" \
  "s_mov_b64 exec, -1\n\t" \
  "buffer_load_dwordx4 v5, s[44:47], s69 offen offset:0 lds\n\t" \
  "s_mov_b32 s85, 0x510800\n\t" \
  "s_load_dword s84, %[pw], s85\n\t" \
  "buffer_load_dwordx4 v6, s[44:47], s69 offen offset:1024 lds\n\t" \
  "buffer_load_dwordx4 v7, s[44:47], s69 offen offset:2048 lds\n\t" \
  "s_mov_b32 m0, %[tsk]\n\t" \
  "s_mov_b32 s85, 0x480000\n\t" \
  "s_mov_b64 exec, 63\n\t" \
  "buffer_load_dword v16, %[sh], s85 offen lds\n\t" \
  "s_mov_b64 exec, -1\n\t" \
  "s_mov_b32 s68, 0x24500\n\t" \
  "s_mov_b32 s69, 0x3f0000\n\t" \
  "s_add_u32 m0, s38, 9216\n\t" \
  "s_waitcnt vmcnt(19)\n\t" \
  "s_barrier\n\t" \
  "buffer_load_dwordx4 v2, s[40:43], s68 offen offset:0 lds\n\t" \
  "s_mov_b32 s85, 0x511000\n\t" \
  "s_load_dword s84, %[pw], s85\n\t" \
  "buffer_load_dwordx4 v3, s[40:43], s68 offen offset:1024 lds\n\t" \
  "s_mov_b32 s85, 0x540000\n\t" \
  "s_load_dword s84, %[pw], s85\n\t" \
  "s_mov_b64 exec, 0xffff\n\t" \
  "buffer_load_dwordx4 v4, s[40:43], s68 offen offset:2048 lds\n\t" \
  "s_add_u32 m0, s39, 12288\n\t" \
  "s_mov_b64 exec, -1\n\t" \
  "buffer_load_dwordx4 v5, s[44:47], s69 offen offset:0 lds\n\t" \
  "s_mov_b32 s85, 0x540800\n\t" \
  "s_load_dword s84, %[pw], s85\n\t" \
  "buffer_load_dwordx4 v6, s[44:47], s69 offen offset:1024 lds\n\t" \
  "buffer_load_dwordx4 v7, s[44:47], s69 offen offset:2048 lds\n\t" \
  "s_mov_b32 m0, %[tsk]\n\t" \
  "s_mov_b64 exec, %[tm]\n\t" \
  "global_load_lds_dword v12, %[tp]\n\t" \
  "s_mov_b64 exec, -1\n\t" \
  "s_mov_b32 s68, 0x580\n\t" \
  "s_mov_b32 s69, 0x420000\n\t" \
  "s_add_u32 m0, s38, 18432\n\t" \
  "s_waitcnt vmcnt(16)\n\t" \
  "s_barrier\n\t" \
  "buffer_load_dwordx4 v0, s[40:43], s68 offen offset:0 lds\n\t" \
  "s_mov_b32 s85, 0x541000\n\t" \
  "s_load_dword s84, %[pw], s85\n\t" \
  "buffer_load_dwordx4 v1, s[40:43], s68 offen offset:1024 lds\n\t" \
  "s_mov_b32 s85, 0x570000\n\t" \
  "s_load_dword s84, %[pw], s85\n\t" \
  "s_mov_b64 exec, 0xffff\n\t" \
  "buffer_load_dwordx4 v4, s[40:43], s68 offen offset:2048 lds\n\t" \
  "s_add_u32 m0, s39, 24576\n\t" \
  "s_mov_b64 exec, -1\n\t" \
  "buffer_load_dwordx4 v5, s[44:47], s69 offen offset:0 lds\n\t" \
  "s_mov_b32 s85, 0x570800\n\t" \
  "s_load_dword s84, %[pw], s85\n\t" \
  "buffer_load_dwordx4 v6, s[44:47], s69 offen offset:1024 lds\n\t" \
  "buffer_load_dwordx4 v7, s[44:47], s69 offen offset:2048 lds\n\t" \
  "s_mov_b32 s68, 0x24580\n\t" \
  "s_mov_b32 s69, 0x450000\n\t" \
  "s_add_u32 m0, s38, 27648\n\t" \
  "s_waitcnt vmcnt(19)\n\t" \
  "s_barrier\n\t" \
  "buffer_load_dwordx4 v2, s[40:43], s68 offen offset:0 lds\n\t" \
  "s_mov_b32 s85, 0x571000\n\t" \
  "s_load_dword s84, %[pw], s85\n\t" \
  "buffer_load_dwordx4 v3, s[40:43], s68 offen offset:1024 lds\n\t" \
  "s_mov_b32 s85, 0x5a0000\n\t" \
  "s_load_dword s84, %[pw], s85\n\t" \
  "s_mov_b64 exec, 0xffff\n\t" \
  "buffer_load_dwordx4 v4, s[40:43], s68 offen offset:2048 lds\n\t" \
  "s_add_u32 m0, s39, 36864\n\t" \
  "s_mov_b64 exec, -1\n\t" \
  "buffer_load_dwordx4 v5, s[44:47], s69 offen offset:0 lds\n\t" \
  "s_mov_b32 s85, 0x5a0800\n\t" \
  "s_load_dword s84, %[pw], s85\n\t" \
  "buffer_load_dwordx4 v6, s[44:47], s69 offen offset:1024 lds\n\t" \
  "buffer_load_dwordx4 v7, s[44:47], s69 offen offset:2048 lds\n\t" \
  "s_mov_b32 s68, 0x600\n\t" \
  "s_mov_b32 s69, 0x480000\n\t" \
  "s_add_u32 m0, s38, 36864\n\t" \
  "s_waitcnt vmcnt(15)\n\t" \
  "s_barrier\n\t" \
  "buffer_load_dwordx4 v0, s[40:43], s68 offen offset:0 lds\n\t" \
  "s_mov_b32 s85, 0x5a1000\n\t" \
  "s_load_dword s84, %[pw], s85\n\t" \
  "buffer_load_dwordx4 v1, s[40:43], s68 offen offset:1024 lds\n\t" \
  "s_mov_b32 s85, 0x5d0000\n\t" \
  "s_load_dword s84, %[pw], s85\n\t" \
  "s_mov_b64 exec, 0xffff\n\t" \
  "buffer_load_dwordx4 v4, s[40:43], s68 offen offset:2048 lds\n\t" \
  "s_add_u32 m0, s39, 49152\n\t" \
  "s_mov_b64 exec, -1\n\t" \
  "buffer_load_dwordx4 v5, s[44:47], s69 offen offset:0 lds\n\t" \
  "s_mov_b32 s85, 0x5d0800\n\t" \
  "s_load_dword s84, %[pw], s85\n\t" \
  "buffer_load_dwordx4 v6, s[44:47], s69 offen offset:1024 lds\n\t" \
  "buffer_load_dwordx4 v7, s[44:47], s69 offen offset:2048 lds\n\t" \
  "s_mov_b32 s68, 0x24600\n\t" \
  "s_mov_b32 s69, 0x4b0000\n\t" \
  "s_add_u32 m0, s38, 46080\n\t" \
  "s_waitcnt vmcnt(18)\n\t" \
  "s_barrier\n\t" \
  "buffer_load_dwordx4 v2, s[40:43], s68 offen offset:0 lds\n\t" \
  "s_mov_b32 s85, 0x5d1000\n\t" \
  "s_load_dword s84, %[pw], s85\n\t" \
  "buffer_load_dwordx4 v3, s[40:43], s68 offen offset:1024 lds\n\t" \
  "s_load_dword s84, %[po], 0x0\n\t" \
  "s_mov_b64 exec, 0xffff\n\t" \
  "buffer_load_dwordx4 v4, s[40:43], s68 offen offset:2048 lds\n\t" \
  "s_add_u32 m0, s39, 61440\n\t" \
  "s_mov_b64 exec, -1\n\t" \
  "buffer_load_dwordx4 v5, s[44:47], s69 offen offset:0 lds\n\t" \
  "s_load_dword s84, %[po], 0x40\n\t" \
  "buffer_load_dwordx4 v6, s[44:47], s69 offen offset:1024 lds\n\t" \
  "buffer_load_dwordx4 v7, s[44:47], s69 offen offset:2048 lds\n\t" \
  "s_mov_b32 s68, 0x680\n\t" \
  "s_mov_b32 s69, 0x4e0000\n\t" \
  "s_add_u32 m0, s38, 0\n\t" \
  "s_waitcnt vmcnt(15)\n\t" \
  "s_barrier\n\t" \
  "buffer_load_dwordx4 v0, s[40:43], s68 offen offset:0 lds\n\t" \
  "s_load_dword s84, %[po], 0x80\n\t" \
  "buffer_load_dwordx4 v1, s[40:43], s68 offen offset:1024 lds\n\t" \
  "s_load_dword s84, %[po], 0xc0\n\t" \
  "s_mov_b64 exec, 0xffff\n\t" \
  "buffer_load_dwordx4 v4, s[40:43], s68 offen offset:2048 lds\n\t" \
  "s_add_u32 m0, s39, 0\n\t" \
  "s_mov_b64 exec, -1\n\t" \
  "buffer_load_dwordx4 v5, s[44:47], s69 offen offset:0 lds\n\t" \
  "s_load_dword s84, %[po], 0x100\n\t" \
  "buffer_load_dwordx4 v6, s[44:47], s69 offen offset:1024 lds\n\t" \
  "buffer_load_dwordx4 v7, s[44:47], s69 offen offset:2048 lds\n\t" \
  "s_mov_b32 s68, 0x24680\n\t" \
  "s_mov_b32 s69, 0x510000\n\t" \
  "s_add_u32 m0, s38, 9216\n\t" \
  "s_waitcnt vmcnt(18)\n\t" \
  "s_barrier\n\t" \
  "buffer_load_dwordx4 v2, s[40:43], s68 offen offset:0 lds\n\t" \
  "s_load_dword s84, %[po], 0x140\n\t" \
  "buffer_load_dwordx4 v3, s[40:43], s68 offen offset:1024 lds\n\t" \
  "s_load_dword s84, %[po], 0x180\n\t" \
  "s_mov_b64 exec, 0xffff\n\t" \
  "buffer_load_dwordx4 v4, s[40:43], s68 offen offset:2048 lds\n\t" \
  "s_add_u32 m0, s39, 12288\n\t" \
  "s_mov_b64 exec, -1\n\t" \
  "buffer_load_dwordx4 v5, s[44:47], s69 offen offset:0 lds\n\t" \
  "s_load_dword s84, %[po], 0x1c0\n\t" \
  "buffer_load_dwordx4 v6, s[44:47], s69 offen offset:1024 lds\n\t" \
  "buffer_load_dwordx4 v7, s[44:47], s69 offen offset:2048 lds\n\t" \
  "s_mov_b32 s68, 0x700\n\t" \
  "s_mov_b32 s69, 0x540000\n\t" \
  "s_add_u32 m0, s38, 18432\n\t" \
  "s_waitcnt vmcnt(15)\n\t" \
  "s_barrier\n\t" \
  "buffer_load_dwordx4 v0, s[40:43], s68 offen offset:0 lds\n\t" \
  "s_load_dword s84, %[po], 0x200\n\t" \
  "buffer_load_dwordx4 v1, s[40:43], s68 offen offset:1024 lds\n\t" \
  "s_load_dword s84, %[po], 0x240\n\t" \
  "s_mov_b64 exec, 0xffff\n\t" \
  "buffer_load_dwordx4 v4, s[40:43], s68 offen offset:2048 lds\n\t" \
  "s_add_u32 m0, s39, 24576\n\t" \
  "s_mov_b64 exec, -1\n\t" \
  "buffer_load_dwordx4 v5, s[44:47], s69 offen offset:0 lds\n\t" \
  "s_load_dword s84, %[po], 0x280\n\t" \
  "buffer_load_dwordx4 v6, s[44:47], s69 offen offset:1024 lds\n\t" \
  "buffer_load_dwordx4 v7, s[44:47], s69 offen offset:2048 lds\n\t" \
  "s_mov_b32 s68, 0x24700\n\t" \
  "s_mov_b32 s69, 0x570000\n\t" \
  "s_add_u32 m0, s38, 27648\n\t" \
  "s_waitcnt vmcnt(18)\n\t" \
  "s_barrier\n\t" \
  "buffer_load_dwordx4 v2, s[40:43], s68 offen offset:0 lds\n\t" \
  "s_load_dword s84, %[po], 0x2c0\n\t" \
  "buffer_load_dwordx4 v3, s[40:43], s68 offen offset:1024 lds\n\t" \
  "s_load_dword s84, %[po], 0x300\n\t" \
  "s_mov_b64 exec, 0xffff\n\t" \
  "buffer_load_dwordx4 v4, s[40:43], s68 offen offset:2048 lds\n\t" \
  "s_add_u32 m0, s39, 36864\n\t" \
  "s_mov_b64 exec, -1\n\t" \
  "buffer_load_dwordx4 v5, s[44:47], s69 offen offset:0 lds\n\t" \
  "s_load_dword s84, %[po], 0x340\n\t" \
  "buffer_load_dwordx4 v6, s[44:47], s69 offen offset:1024 lds\n\t" \
  "buffer_load_dwordx4 v7, s[44:47], s69 offen offset:2048 lds\n\t" \
  "s_mov_b32 s68, 0x780\n\t" \
  "s_mov_b32 s69, 0x5a0000\n\t" \
  "s_add_u32 m0, s38, 36864\n\t" \
  "s_waitcnt vmcnt(15)\n\t" \
  "s_barrier\n\t" \
  "buffer_load_dwordx4 v0, s[40:43], s68 offen offset:0 lds\n\t" \
  "s_load_dword s84, %[po], 0x380\n\t" \
  "buffer_load_dwordx4 v1, s[40:43], s68 offen offset:1024 lds\n\t" \
  "s_load_dword s84, %[po], 0x3c0\n\t" \
  "s_mov_b64 exec, 0xffff\n\t" \
  "buffer_load_dwordx4 v4, s[40:43], s68 offen offset:2048 lds\n\t" \
  "s_add_u32 m0, s39, 49152\n\t" \
  "s_mov_b64 exec, -1\n\t" \
  "buffer_load_dwordx4 v5, s[44:47], s69 offen offset:0 lds\n\t" \
  "s_load_dword s84, %[po], 0x400\n\t" \
  "buffer_load_dwordx4 v6, s[44:47], s69 offen offset:1024 lds\n\t" \
  "buffer_load_dwordx4 v7, s[44:47], s69 offen offset:2048 lds\n\t" \
  "s_mov_b32 s68, 0x24780\n\t" \
  "s_mov_b32 s69, 0x5d0000\n\t" \
  "s_add_u32 m0, s38, 46080\n\t" \
  "s_waitcnt vmcnt(18)\n\t" \
  "s_barrier\n\t" \
  "buffer_load_dwordx4 v2, s[40:43], s68 offen offset:0 lds\n\t" \
  "s_load_dword s84, %[po], 0x440\n\t" \
  "buffer_load_dwordx4 v3, s[40:43], s68 offen offset:1024 lds\n\t" \
  "s_load_dword s84, %[po], 0x480\n\t" \
  "s_mov_b64 exec, 0xffff\n\t" \
  "buffer_load_dwordx4 v4, s[40:43], s68 offen offset:2048 lds\n\t" \
  "s_add_u32 m0, s39, 61440\n\t" \
  "s_mov_b64 exec, -1\n\t" \
  "buffer_load_dwordx4 v5, s[44:47], s69 offen offset:0 lds\n\t" \
  "s_load_dword s84, %[po], 0x4c0\n\t" \
  "buffer_load_dwordx4 v6, s[44:47], s69 offen offset:1024 lds\n\t" \
  "buffer_load_dwordx4 v7, s[44:47], s69 offen offset:2048 lds\n\t" \
  "v_readlane_b32 s68, v9, 0\n\t" \
  "v_readlane_b32 s69, v9, 1\n\t" \
  "v_readlane_b32 s70, v9, 2\n\t" \
  "v_readlane_b32 s71, v9, 18\n\t" \
  "v_readlane_b32 s72, v9, 19\n\t" \
  "s_add_u32 m0, %[ob], 0\n\t" \
  "s_waitcnt vmcnt(15)\n\t" \
  "s_barrier\n\t" \
  "s_nop 2\n\t" \
  "buffer_load_dwordx4 v8, %[so], s68 offen lds\n\t" \
  "s_load_dword s84, %[po], 0x500\n\t" \
  "s_add_u32 m0, %[ob], 4096\n\t" \
  "s_nop 0\n\t" \
  "buffer_load_dwordx4 v8, %[so], s69 offen lds\n\t" \
  "s_load_dword s84, %[po], 0x540\n\t" \
  "s_add_u32 m0, %[ob], 8192\n\t" \
  "s_nop 0\n\t" \
  "buffer_load_dwordx4 v8, %[so], s70 offen lds\n\t" \
  "s_load_dword s84, %[po], 0x580\n\t" \
  "s_add_u32 m0, %[ob], 73728\n\t" \
  "s_nop 0\n\t" \
  "buffer_load_dwordx4 v8, %[so], s71 offen lds\n\t" \
  "s_add_u32 m0, %[ob], 77824\n\t" \
  "s_nop 0\n\t" \
  "buffer_load_dwordx4 v8, %[so], s72 offen lds\n\t" \
  "v_readlane_b32 s68, v9, 3\n\t" \
  "v_readlane_b32 s69, v9, 4\n\t" \
  "v_readlane_b32 s70, v9, 5\n\t" \
  "v_readlane_b32 s71, v9, 20\n\t" \
  "v_readlane_b32 s72, v9, 21\n\t" \
  "s_add_u32 m0, %[ob], 12288\n\t" \
  "s_waitcnt vmcnt(17)\n\t" \
  "s_barrier\n\t" \
  "s_nop 2\n\t" \
  "buffer_load_dwordx4 v8, %[so], s68 offen lds\n\t" \
  "s_load_dword s84, %[po], 0x5c0\n\t" \
  "s_add_u32 m0, %[ob], 16384\n\t" \
  "s_nop 0\n\t" \
  "buffer_load_dwordx4 v8, %[so], s69 offen lds\n\t" \
  "s_load_dword s84, %[po], 0x600\n\t" \
  "s_add_u32 m0, %[ob], 20480\n\t" \
  "s_nop 0\n\t" \
  "buffer_load_dwordx4 v8, %[so], s70 offen lds\n\t" \
  "s_load_dword s84, %[po], 0x640\n\t" \
  "s_add_u32 m0, %[ob], 81920\n\t" \
  "s_nop 0\n\t" \
  "buffer_load_dwordx4 v8, %[so], s71 offen lds\n\t" \
  "s_add_u32 m0, %[ob], 86016\n\t" \
  "s_nop 0\n\t" \
  "buffer_load_dwordx4 v8, %[so], s72 offen lds\n\t" \
  "v_readlane_b32 s68, v9, 6\n\t" \
  "v_readlane_b32 s69, v9, 7\n\t" \
  "v_readlane_b32 s70, v9, 8\n\t" \
  "v_readlane_b32 s71, v9, 22\n\t" \
  "v_readlane_b32 s72, v9, 23\n\t" \
  "v_readlane_b32 s73, v9, 24\n\t" \
  "s_add_u32 m0, %[ob], 24576\n\t" \
  "s_waitcnt vmcnt(13)\n\t" \
  "s_barrier\n\t" \
  "s_nop 2\n\t" \
  "buffer_load_dwordx4 v8, %[so], s68 offen lds\n\t" \
  "s_load_dword s84, %[po], 0x680\n\t" \
  "s_add_u32 m0, %[ob], 28672\n\t" \
  "s_nop 0\n\t" \
  "buffer_load_dwordx4 v8, %[so], s69 offen lds\n\t" \
  "s_load_dword s84, %[po], 0x6c0\n\t" \
  "s_add_u32 m0, %[ob], 32768\n\t" \
  "s_nop 0\n\t" \
  "buffer_load_dwordx4 v8, %[so], s70 offen lds\n\t" \
  "s_load_dword s84, %[po], 0x700\n\t" \
  "s_add_u32 m0, %[ob], 90112\n\t" \
  "s_nop 0\n\t" \
  "buffer_load_dwordx4 v8, %[so], s71 offen lds\n\t" \
  "s_add_u32 m0, %[ob], 94208\n\t" \
  "s_nop 0\n\t" \
  "buffer_load_dwordx4 v8, %[so], s72 offen lds\n\t" \
  "s_add_u32 m0, %[ob], 98304\n\t" \
  "s_nop 0\n\t" \
  "buffer_load_dwordx4 v8, %[so], s73 offen lds\n\t" \
  "v_readlane_b32 s68, v9, 9\n\t" \
  "v_readlane_b32 s69, v9, 10\n\t" \
  "v_readlane_b32 s70, v9, 11\n\t" \
  "v_readlane_b32 s71, v9, 25\n\t" \
  "v_readlane_b32 s72, v9, 26\n\t" \
  "s_add_u32 m0, %[ob], 36864\n\t" \
  "s_waitcnt vmcnt(16)\n\t" \
  "s_barrier\n\t" \
  "s_nop 2\n\t" \
  "buffer_load_dwordx4 v8, %[so], s68 offen lds\n\t" \
  "s_load_dword s84, %[po], 0x740\n\t" \
  "s_add_u32 m0, %[ob], 40960\n\t" \
  "s_nop 0\n\t" \
  "buffer_load_dwordx4 v8, %[so], s69 offen lds\n\t" \
  "s_load_dword s84, %[po], 0x780\n\t" \
  "s_add_u32 m0, %[ob], 45056\n\t" \
  "s_nop 0\n\t" \
  "buffer_load_dwordx4 v8, %[so], s70 offen lds\n\t" \
  "s_load_dword s84, %[po], 0x7c0\n\t" \
  "s_add_u32 m0, %[ob], 102400\n\t" \
  "s_nop 0\n\t" \
  "buffer_load_dwordx4 v8, %[so], s71 offen lds\n\t" \
  "s_add_u32 m0, %[ob], 106496\n\t" \
  "s_nop 0\n\t" \
  "buffer_load_dwordx4 v8, %[so], s72 offen lds\n\t" \
  "v_readlane_b32 s68, v9, 12\n\t" \
  "v_readlane_b32 s69, v9, 13\n\t" \
  "v_readlane_b32 s70, v9, 14\n\t" \
  "v_readlane_b32 s71, v9, 27\n\t" \
  "v_readlane_b32 s72, v9, 28\n\t" \
  "v_readlane_b32 s73, v9, 29\n\t" \
  "s_add_u32 m0, %[ob], 49152\n\t" \
  "s_nop 3\n\t" \
  "s_barrier\n\t" \
  "s_nop 2\n\t" \
  "buffer_load_dwordx4 v8, %[so], s68 offen lds\n\t" \
  "s_add_u32 m0, %[ob], 53248\n\t" \
  "s_nop 0\n\t" \
  "buffer_load_dwordx4 v8, %[so], s69 offen lds\n\t" \
  "s_add_u32 m0, %[ob], 57344\n\t" \
  "s_nop 0\n\t" \
  "buffer_load_dwordx4 v8, %[so], s70 offen lds\n\t" \
  "s_add_u32 m0, %[ob], 110592\n\t" \
  "s_nop 0\n\t" \
  "buffer_load_dwordx4 v8, %[so], s71 offen lds\n\t" \
  "s_add_u32 m0, %[ob], 114688\n\t" \
  "s_nop 0\n\t" \
  "buffer_load_dwordx4 v8, %[so], s72 offen lds\n\t" \
  "s_add_u32 m0, %[ob], 118784\n\t" \
  "s_nop 0\n\t" \
  "buffer_load_dwordx4 v8, %[so], s73 offen lds\n\t" \
  "v_readlane_b32 s68, v9, 15\n\t" \
  "v_readlane_b32 s69, v9, 16\n\t" \
  "v_readlane_b32 s70, v9, 17\n\t" \
  "v_readlane_b32 s71, v9, 30\n\t" \
  "v_readlane_b32 s72, v9, 31\n\t" \
  "s_add_u32 m0, %[ob], 61440\n\t" \
  "LS_%=:\n\t" \
  "ds_read_b32 v11, v10\n\t" \
  "s_addk_i32 %[cn], 1\n\t" \
  "s_cmp_gt_u32 %[cn], 0x400000\n\t" \
  "s_cbranch_scc1 LX_%=\n\t" \
  "s_waitcnt lgkmcnt(0)\n\t" \
  "v_cmp_eq_u32 vcc, 0, v11\n\t" \
  "s_cbranch_vccz LX_%=\n\t" \
  "s_sleep 1\n\t" \
  "s_branch LS_%=\n\t" \
  "LX_%=:\n\t" \
  "s_nop 2\n\t" \
  "buffer_load_dwordx4 v8, %[so], s68 offen lds\n\t" \
  "s_add_u32 m0, %[ob], 65536\n\t" \
  "s_nop 0\n\t" \
  "buffer_load_dwordx4 v8, %[so], s69 offen lds\n\t" \
  "s_add_u32 m0, %[ob], 69632\n\t" \
  "s_nop 0\n\t" \
  "buffer_load_dwordx4 v8, %[so], s70 offen lds\n\t" \
  "s_add_u32 m0, %[ob], 122880\n\t" \
  "s_nop 0\n\t" \
  "buffer_load_dwordx4 v8, %[so], s71 offen lds\n\t" \
  "s_add_u32 m0, %[ob], 126976\n\t" \
  "s_nop 0\n\t" \
  "buffer_load_dwordx4 v8, %[so], s72 offen lds\n\t" \
  "s_waitcnt vmcnt(0) lgkmcnt(0)\n\t"
#define LD_HT 8
#define LD_TP 2
#define LD_CLOB "v11", "s68", "s69", "s70", "s71", "s72", "s73", "s74", "s75", "s76", "s77", "s78", "s79", "s80", "s81", "s82", "s83", "s84", "s85"

#define ENDW
#define TS(i)
#define TSS(i)
#define TOUT(n)

#define SPIN(cond,slp) do{ int sp_=0; while(cond){ __builtin_amdgcn_s_sleep(slp); if(++sp_>(1<<22)) break; } }while(0)
__device__ __forceinline__ unsigned rd1(const char* p){ return __builtin_amdgcn_readfirstlane(*(volatile LDS_AS unsigned*)(LDS_AS char*)p); }
__device__ __forceinline__ unsigned lds_inc(const char* p, int l){ unsigned r=0; if(l==0) r=__hip_atomic_fetch_add((LDS_AS unsigned*)(LDS_AS char*)p, 1u, __ATOMIC_RELAXED, __HIP_MEMORY_SCOPE_WORKGROUP); return __builtin_amdgcn_readfirstlane(r); }
__device__ __forceinline__ void lds_add(unsigned adr){ asm volatile("s_mov_b64 exec, 1\n\tds_add_u32 %0, %1\n\ts_mov_b64 exec, -1" :: "v"(adr), "v"(1u) : "memory"); }
__device__ __forceinline__ void bars(int n){
  asm volatile("BL_%=:\n\ts_barrier\n\ts_sub_u32 %0, %0, 1\n\ts_cmp_lg_u32 %0, 0\n\ts_cbranch_scc1 BL_%=" : "+s"(n) :: "memory", "scc");
}
#define barc(rem) barc_(rem)
#define CTRA
#define CTRP
__device__ __forceinline__ void barc_(unsigned& rem){
  asm volatile("s_cmp_eq_u32 %0, 0\n\ts_cbranch_scc1 BC_%=\n\ts_barrier\n\ts_sub_u32 %0, %0, 1\n\tBC_%=:" : "+s"(rem) :: "memory", "scc");
}
template<int SLP> __device__ __forceinline__ void wait_flag_b(const unsigned* p, unsigned voff, unsigned tag, unsigned& rem CTRA){
  int spins=0;
  while(true){
    unsigned v; asm volatile("global_load_dword %0, %1, %2 sc1" : "=v"(v) : "v"(voff), "s"(p) : "memory");
    barc(rem); if(PB2) barc(rem);
    asm volatile("s_waitcnt vmcnt(0)" : "+v"(v) :: "memory");
    if(__all(v==tag) || ++spins>(1<<21)) break;
    if(rem==0) __builtin_amdgcn_s_sleep(SLP);
  }
}
__device__ __forceinline__ void stout(void* sbase, unsigned voff, u32x4 v){
  st16cs(sbase,voff,v);
}

typedef unsigned u32x8 __attribute__((ext_vector_type(8)));
typedef unsigned u32x16 __attribute__((ext_vector_type(16)));
__device__ __forceinline__ char* ptr64(unsigned lo, unsigned hi){ return (char*)(((unsigned long long)hi<<32)|lo); }
__device__ __forceinline__ u32x4 srd(const char* p, unsigned bytes){ return u32x4{(unsigned)(size_t)p,(unsigned)((size_t)p>>32)&0xffffu,bytes,0x20000u}; }
constexpr size_t WS_O=0, WS_KV=(size_t)S*DM*2, WS_Y=(size_t)S*DM*4, WS_FL=(size_t)S*DM*12;

__global__ __launch_bounds__(768,3) void fused_kernel(const u16* x_, const u16* wqkv_, const u16* wo_, char* ws_, u16* out_,
    const u16* kin_, const u16* vin_, const u16* shk_, const u16* hk_, const u16* qin_, const u16* sD_)
{
  __shared__ __attribute__((aligned(128))) char smem[ALL_LDS];
  TS(0);
  int wv;
  asm volatile(
      "s_load_dwordx16 s[48:63], %[ka], 0x0\n\ts_load_dwordx4 s[64:67], %[ka], 0x40\n\ts_load_dwordx2 s[78:79], %[ka], 0x50\n\ts_getpc_b64 s[72:73]\n\tv_readfirstlane_b32 s75, v0\n\ts_lshr_b32 s74, %[b], 3\n\ts_mul_i32 s74, s74, 12\n\ts_lshr_b32 s75, s75, 6\n\t"
      "s_add_u32 s74, s74, s75\n\ts_min_u32 s74, s74, %[cl]\n\ts_lshl_b32 s74, s74, 7\n\ts_mov_b32 m0, %[sk]\n\tv_mov_b32 v1, s74\n\ts_mov_b64 exec, 1\n\tglobal_load_lds_dword v1, s[72:73]\n\ts_mov_b64 exec, -1\n\t"
      "s_mov_b32 s76, %[b]\n\ts_cmp_lt_u32 s75, 4\n\ts_cbranch_scc1 FK_G\n\ts_cmp_lt_u32 s75, 8\n\ts_cbranch_scc0 FK_C\n\t"
      "v_and_b32 v9, 7, v0\n\tv_and_b32 v10, 63, v0\n\ts_lshl_b32 s74, %[b], 19\n\ts_lshl_b32 s73, %[b], 11\n\ts_or_b32 s74, s74, s73\n\ts_and_b32 s74, s74, 0x3fc000\n\ts_lshl_b32 s73, s75, 9\n\t"
      "s_add_u32 s74, s74, s73\n\ts_add_u32 s74, s74, 0x7800\n\tv_lshl_add_u32 v9, v9, 11, s74\n\tv_and_b32 v1, 24, v0\n\ts_lshl2_add_u32 s74, %[b], s75\n\ts_mul_i32 s74, s74, %[nw]\n\tv_lshl_add_u32 v9, v1, 4, v9\n\t"
      "s_add_u32 s74, s74, %[w0]\n\tv_add_lshl_u32 v10, v10, s74, 7\n\t"
      "s_setprio %[lp]\n\ts_and_b32 s74, s75, 3\n\tv_lshrrev_b32 v1, 3, v0\n\tv_lshrrev_b32 v3, 4, v0\n\ts_and_b32 s72, %[b], 1\n\ts_and_b32 s73, %[b], 0xe0\n\ts_lshl_b32 s72, s72, 10\n\ts_lshl2_add_u32 s72, s73, s72\n\t"
      "v_and_b32 v4, 2, v1\n\tv_and_or_b32 v3, v3, 4, v4\n\tv_and_b32 v4, 31, v1\n\tv_xor_b32 v3, v3, v0\n\tv_mul_u32_u24 v4, 0x1800, v4\n\tv_and_b32 v3, 7, v3\n\tv_lshl_add_u32 v3, v3, 4, s72\n\t"
      "s_mul_i32 s39, s74, 0xc00\n\ts_mul_i32 s38, s74, 0x900\n\ts_mul_i32 s74, s74, 18\n\tv_add_u32 v5, v3, v4\n\tv_and_b32 v1, 7, v1\n\tv_add_u32 v6, 0x400, v5\n\tv_add_u32 v1, s74, v1\n\tv_add_u32 v7, 0x800, v5\n\t"
      "s_and_b32 s72, %[b], 6\n\ts_and_b32 s73, %[b], 24\n\ts_lshl4_add_u32 s72, s72, s73\n\tv_xor_b32 v3, v1, v0\n\ts_lshl_b32 s72, s72, 15\n\tv_and_b32 v3, 7, v3\n\tv_lshl_add_u32 v1, v1, 11, s72\n\t"
      "s_mov_b32 s47, 0x20000\n\ts_mov_b32 s46, 0x600000\n\tv_lshl_add_u32 v2, v3, 4, v1\n\ts_mov_b32 s43, s47\n\ts_mov_b32 s42, 0x408000\n\ts_movk_i32 s73, 0x3c00\n\t"
      "v_add_u32 v3, s73, v2\n\ts_add_u32 s38, s38, %[xf]\n\tv_max_u32 v0, 0x8000, v2\n\tv_add_u32 v4, s73, v3\n\tv_max_u32 v1, 0x8000, v3\n\t"
      "s_waitcnt lgkmcnt(0)\n\ts_mov_b64 s[44:45], s[50:51]\n\ts_sub_u32 s40, s48, 0x8000\n\ts_subb_u32 s41, s49, 0\n\t"
      LD_ASM1
      "s_branch FK_L"
      : "=s"(wv)
      : "{v0}"(threadIdx.x), [ka]"s"(__builtin_amdgcn_kernarg_segment_ptr()), [b]"s"(blockIdx.x), [cl]"n"(CODEL-1), [sk]"n"(SINK+256), [lp]"n"(LPR), [xf]"n"(X_OFF), [nw]"n"(3*TPW), [w0]"n"(TW0*3072-12*TPW), [wm]"n"((1<<(3*TPW))-1)
      : "memory", "scc", "s48", "s49", "s50", "s51", "s52", "s53", "s54", "s55", "s72", "s73", "s74", "s75", "s76", "s78", "s79", "v1", "v2", "v3", "v4", "v5", "v6", "v7", "v9", "v10",
        "s40", "s41", "s42", "s43", "s44", "s45", "s46", "s47", "s38", "s39", "s56", "s57", "s58", "s59", "s60", "s61", "s62", "s63", "s64", "s65", "s66", "s67", "s68");
  #define ENTER(lbl) int w8, bid; u32x8 a0, a1; u32x4 a2; u32x2 a3; asm volatile(lbl ":" : "={s75}"(w8), "={s76}"(bid), "={s[48:55]}"(a0), "={s[56:63]}"(a1), "={s[64:67]}"(a2), "={s[78:79]}"(a3) :: "memory");
  #define ARGW asm volatile("s_waitcnt lgkmcnt(0)" : "+{s[48:55]}"(a0), "+{s[56:63]}"(a1), "+{s[64:67]}"(a2), "+{s[78:79]}"(a3));
  #define ROLE const int w=w8&3, xcd=bid&7, idx=bid>>3, rb=(xcd>>1)*4+(idx&3), hp=(xcd&1)*8+(idx>>2), s0=rb*128; (void)s0; (void)hp;
  #define LANEVARS int l; asm volatile("v_mbcnt_lo_u32_b32 %0, -1, 0\n\tv_mbcnt_hi_u32_b32 %0, -1, %0" : "=v"(l)); const int g=l>>4, fr=l&15, tq=(l&15)>>2, tp=l&3; (void)g; (void)fr; (void)tq; (void)tp;
  #define ARGS0 const char* x=ptr64(a0[0],a0[1]); const char* wqkv=ptr64(a0[2],a0[3]); const char* wo=ptr64(a0[4],a0[5]); char* ws=ptr64(a0[6],a0[7]); \
    unsigned* flags=(unsigned*)(ws+WS_FL); const int hot0=(3-(int)(a0[2]>>7))&7; (void)x; (void)wqkv; (void)wo; (void)flags; (void)hot0;
  #define ORA ((unsigned)(hp-hot0-1)&7u)
  #define ORD(c) ((((ORA==7u? ((c)==0u||(c)==15u? 7u : ((c)-1u)>>1) : ((c)<14u? (ORA+((c)>>1))%7u : 7u))+hot0+1)&7u)+8u*(((hp>>3)^(c))&1u))
  const unsigned sb0=__builtin_amdgcn_readfirstlane(lds_addr(smem));
  const char* syn=smem+SYN;

  if(__builtin_expect(wv==1,1)){
    ENTER("FK_L")
    u32x4 sx, sw; unsigned xl, wl, xoA, xoB, xo, xo1, xo2, wof[3];
    asm volatile("; descriptors and LDS bases" : "={s[40:43]}"(sx), "={s[44:47]}"(sw), "={s38}"(xl), "={s39}"(wl));
    asm volatile("; lane offsets" : "={v0}"(xoA), "={v1}"(xoB), "={v2}"(xo), "={v3}"(xo1), "={v4}"(xo2), "={v5}"(wof[0]), "={v6}"(wof[1]), "={v7}"(wof[2]));
    ROLE
    LANEVARS
    unsigned ltr=0, ltr2=0, ltr3=0; (void)ltr; (void)ltr2; (void)ltr3;
    TSS(2); TSS(3);
    ARGS0
    const unsigned wd=hp*128;
    {
      const unsigned wof2=(unsigned)(w*16384+(l>>3)*2048+((((l&7)>>1)^((l>>4)&3))*32)+(l&1)*16);
      const unsigned to=ORD((unsigned)(l>>1)&15u);
      const unsigned tab=to*131072u+(l&1)*65536;
      unsigned long long tm; asm("s_cmp_eq_u32 %1, 0\n\ts_cselect_b64 %0, %2, 0" : "=s"(tm) : "s"(w), "s"(WTL==2? 0xffffffffull : 0ull) : "scc");
      unsigned cn=0;
      const unsigned hn=(unsigned)(w*(3*LD_HT/4)+l), hto=(hn/6u)*393216u+(hn%6u)*2048u;
      asm volatile(LD_ASM2
        : [cn]"+&s"(cn)
        : "{v0}"(xoA), "{v1}"(xoB), "{v2}"(xo), "{v3}"(xo1), "{v4}"(xo2), "{v5}"(wof[0]), "{v6}"(wof[1]), "{v7}"(wof[2]), "{v8}"(wof2), "{v9}"(tab),
          "{v10}"(sb0+SYN+32+(l&3)*4), "{v12}"((l&31)*2048),
          "{v16}"(hto), [sh]"s"(srd(wqkv+((xcd&1)*8+hot0)*128+idx*12288,6291456u-2048u-idx*12288u+128u)),
          "{s[40:43]}"(sx), "{s[44:47]}"(sw), [so]"s"(srd(wo+wd,2097152u-wd)), "{s38}"(xl), "{s39}"(wl), [ob]"s"(sb0+w*1024),
          [pw]"s"(wqkv+idx*6144+(xcd&1)*1024+(xcd>>1)*256+w*64), [po]"s"(wo+(bid*4+w)*2048),
          [tsk]"s"(sb0+SINK+256), [tm]"s"(tm), [tp]"s"(wo+idx*65536+(xcd&1)*1024+((3-(int)(a0[4]>>7))&7)*128)
        : "memory", "scc", "vcc", LD_CLOB);
    }
    const unsigned tag=rd1(syn+64)+1u;
    lds_add(sb0+SYN+12);
    TS(2);
    if(w==3){
      const unsigned* fp=flags+F_O+(rb*16+hp)*16; const unsigned fo=ORD((unsigned)l&15u)*4u;
      const unsigned madr=sb0+(l==0? SYN+20 : SINK+l*4);
      SPIN(rd1(syn+12)<4u || rd1(syn+16)<4u,1);
      PUB(madr,1u);
      unsigned rdy=1; int spins=0;
      while(rdy!=0xFFFFu){
        unsigned v;
        asm volatile("s_mov_b64 exec, 0xffff\n\tglobal_atomic_or %0, %1, %3, %2 sc0 sc1\n\ts_mov_b64 exec, -1\n\ts_waitcnt vmcnt(0)" : "=&v"(v) : "v"(fo), "s"(fp), "v"(0u) : "memory");
        unsigned r=((unsigned)__ballot(v==tag)&0xFFFFu)|1u;
        if(++spins>(1<<21)) r=0xFFFFu;
        if(r!=rdy){ PUB(madr,(unsigned)__builtin_ctz(~r)); rdy=r; }
        else __builtin_amdgcn_s_sleep(OSLP);
      }
      TS(3);
      if(bid==0){
        int sp=0;
        while(true){
          u32x4 v; asm volatile("global_load_dwordx4 %0, %1, %2 sc1\n\ts_waitcnt vmcnt(0)" : "=&v"(v) : "v"(l*16), "s"(flags+F_Y) : "memory");
          if(__all(v[0]==tag && v[1]==tag && v[2]==tag && v[3]==tag) || ++sp>(1<<20)) break;
          __builtin_amdgcn_s_sleep(12);
        }
        if(l==0) stflag(flags, tag);
      }
    } else { TS(3); }
    TOUT(4);
    __builtin_amdgcn_endpgm();
  }

  if(wv==0){
    ENTER("FK_G")
    ROLE
    __builtin_amdgcn_s_setprio(GPR);
    const int h=w&1, r=w>>1;
    f32x16 e0, e1;
    LANEVARS
    TSS(3);
    ARGW
    ARGS0
    if(PGT&1){ if(w==1 && l<7) touch(ws, l<6? l*2097152+PGO : (unsigned)WS_FL+2048, sb0+SINK+512); }
    if(PGT&2){ if(w==2 && l<4) touch(ptr64(a1[0],a1[1]), l*2097152+PGO, sb0+SINK+512); }
    if(PGT&4){ if(w==3 && l<1) touch(wo, PGO, sb0+SINK+512); if(w==0 && l<2) touch(wqkv, l*2097152+2097152, sb0+SINK+512); }
    TSS(4);
    {
      unsigned lx0=sb0+X_OFF+fr*128+((g^(fr&7))*16), lx1=sb0+X_OFF+fr*128+(((4+g)^(fr&7))*16);
      const int sw=wswz(8*g+tq);
      unsigned lw0=sb0+g*3072+tq*128+(unsigned)((((2*h)^sw)*32)+8*tp), lw1=sb0+g*3072+tq*128+(unsigned)((((2*h+1)^sw)*32)+8*tp);
      char* ot=ws+WS_O+(rb*16+hp)*16384+r*8192+h*2048;
      unsigned it; unsigned long long rm; asm("s_cmp_lg_u32 %1, 0\n\ts_cselect_b64 %0, -1, 0" : "=s"(rm) : "s"(r) : "scc");
      asm volatile(N2_ASM
        : [it]"=&s"(it), "+{v56}"(lx0), "+{v57}"(lx1), "+{v58}"(lw0), "+{v59}"(lw1)
        : "{v62}"(l*16), "{v63}"(sb0+EX_OFF+h*3072+l*16), [r]"s"(r), [ot]"s"(ot), [ou]"s"(ot+4096), [fa]"s"(sb0+SYN+32+w*4), [fp]"s"(sb0+SYN+32+(w^2)*4),
          [vo]"s"(2048-1024*r), [rm]"s"(rm), [z]"s"(rb|r)
        : "memory", "scc", "vcc", N2_CLOB);
    }
    TS(3);
    unsigned tb;
    {
      LANEVARS
      const unsigned tag=rd1(syn+64)+1u;
      const unsigned ra=sb0+(4*g+tq)*128+8*tp; const int sw=(2*g+(tq>>1))&3;
      unsigned b0=ra+((0^sw)*32), b1=ra+((1^sw)*32), b2=ra+((2^sw)*32), b3=ra+((3^sw)*32), vo=(unsigned)(w*4096+l*16);
      tb=ORD((unsigned)l&15u)*16384u;
      asm volatile("" : "+{v160}"(b0), "+{v161}"(b1), "+{v162}"(b2), "+{v163}"(b3), "+{v164}"(vo), "+{v167}"(tb));
      WAITV(0);
      if(lds_inc(syn+16,l)==3u){ if(l<16) stflag(flags+F_O+(rb*16+l)*16+hp, tag); }
      TS(4);
      __builtin_amdgcn_s_setprio(G2P);
      unsigned c=0, iss=0, th, q0, q1, cn=0;
      asm volatile(G3_ASM
        : "={v[128:143]}"(e0), "={v[144:159]}"(e1), [c]"+s"(c), [iss]"+s"(iss), [th]"=&s"(th), [t0]"=&s"(q0), [t1]"=&s"(q1), [cn]"+&s"(cn),
          "+{v160}"(b0), "+{v161}"(b1), "+{v162}"(b2), "+{v163}"(b3), "+{v167}"(tb)
        : "{v164}"(vo), "{v165}"(sb0+SYN+20), [go]"s"(60+w*4), [ob]"s"(srd(ws+WS_O+rb*262144,262144u))
        : "memory", "scc", "vcc", G3_CLOB);
    }
    {
      LANEVARS
      char* sg=smem+EX_OFF+w*2048;
      char* op=ptr64(a1[0],a1[1])+(s0+w*32)*4096+2048+hp*128; const unsigned oo=(unsigned)((l>>3)*4096+(l&7)*16);
      u32x4 ov[4];
      #pragma unroll
      for(int t=0;t<2;t++){
        #pragma unroll
        for(int j=0;j<4;j++){
          #define E2(i) (t? e1[(i)&15] : e0[(i)&15])
          *(volatile LDS_AS u32x2*)(LDS_AS char*)(sg+fr*128+(((2*j+(g>>1))^(fr&7))*16)+(g&1)*8)=u32x2{pack2(E2(4*j),E2(4*j+1)),pack2(E2(4*j+2),E2(4*j+3))};
        }
        #pragma unroll
        for(int i=0;i<2;i++) ov[2*t+i]=*(volatile LDS_AS u32x4*)(LDS_AS char*)(sg+(i*8+(l>>3))*128+(((l&7)^(l>>3))*16));
      }
      #pragma unroll
      for(int i=0;i<4;i++) stout(op+i*32768, oo, ov[i]);
      ENDW TS(6); TOUT(7);
    }
    __builtin_amdgcn_endpgm();
  }

  {
    ENTER("FK_C")
    ROLE
    char* cr=smem+C_OFF+2048+w*4096;
    const unsigned crs=sb0+C_OFF+2048+w*4096;
    const int sblk=bid>>3, td0=(bid&7)*128+(w&1)*64;
    const int vb=(LOCA&1)? xcd*32+(idx&16)+(idx&1)*8+((idx>>1)&7) : bid, fb=(LOCA&2)? (idx&15)*16+xcd*2+(idx>>4) : bid;
    const int d=(vb>>4)*64+(((vb>>3)&1)*4+w)*8+(vb&7);
    const int kv0=KV0+(sblk&KVS);
    static_assert(KV0+KVS+PP_NB<=33 && KV0>=EB, "barrier budget");
    unsigned tagv, ctr; f32x8 RB;
    const int fd0=(fb&15)*64, fs0=(fb>>4)*128+w*32;
    u32x4 qv[4];
    f32x16 A0,A1,A2,A3;
    const int rw=sblk*64+(w>>1)*32, j=rw>>5;
    unsigned k0,k1,k2,ko,ta,se,so,ha,l16,rb0[2];
    {
      LANEVARS
      if(w==0 && l<25 && l!=16) ((unsigned*)(smem+SYN))[l]=0u;
      if(l<32) *(u32x4*)(smem+C_OFF+w*512+l*16)=u32x4{0,0,0,0};
      asm volatile("" ::: "memory");
      __builtin_amdgcn_s_setprio(CPR);
      const int dg=l&7, rg=l>>3;
      ko=(unsigned)(rg*8192+dg*16);
      const bool top=(rw==0 && rg==0);
      k0=top? ko+6144:ko; k1=top? ko+6144:ko+2048; k2=top? ko+6144:ko+4096;
      ta=sb0+EX_OFF+w*1024+(w&1)*512+dg*64; se=(l&8)? 0x03020706u : 0x05040100u; ha=crs+(32*g-4*tq+4*tp)*2; l16=l*16;
      so=(unsigned)(rg*32768+((j>>2)&3)*128+((dg&4)|((dg+j)&3))*16);
      #pragma unroll
      for(int k=0;k<2;k++){ const int q=fr-1-2*g+k; rb0[k]=sb0+C_OFF+(4+(q>>1))*256+((q>>3)&3)*64+((w+(q>>1))&3)*16+(q&1)*8; }
      asm volatile("" : "+{v152}"(k0), "+{v153}"(k1), "+{v154}"(k2), "+{v155}"(ko), "+{v156}"(ta), "+{v157}"(se), "+{v159}"(so), "+{v163}"(l16), "+{v165}"(ha), "+{v166}"(rb0[0]), "+{v167}"(rb0[1]));
    }
    TSS(3);
    ARGW
    if(EB==1) asm volatile("s_barrier" : "+s"(a1), "+s"(a2), "+s"(a3), "+s"(a0) :: "memory");
    if(EB==2) asm volatile("s_barrier\n\ts_barrier" : "+s"(a1), "+s"(a2), "+s"(a3), "+s"(a0) :: "memory");
    ARGS0
    char* out=ptr64(a1[0],a1[1]); const char* qp=ptr64(a2[2],a2[3])+(fs0*DM+fd0)*2;
    {
      const char* kb=ptr64(a1[2],a1[3])+(rw*DM+td0)*2; const char* vb=ptr64(a1[4],a1[5])+(rw*DM+td0)*2;
      unsigned long long zm; asm("s_cmp_eq_u32 %1, 0\n\ts_cselect_b64 %0, 0xff, 0" : "=s"(zm) : "s"(rw) : "scc");
      unsigned t0;
      if(PGT&8){ const char* tp= w==0? kb : w==1? vb : w==2? qp : ptr64(a2[0],a2[1])+d*4096;
        asm volatile("s_mov_b32 m0, %1\n\ts_mov_b64 exec, 1\n\tglobal_load_lds_dword v163, %0\n\ts_mov_b64 exec, -1" :: "s"(tp), "s"(sb0+SINK+512), "{v163}"(l16) : "memory"); }
      TSS(5);
      asm volatile(PP_ASM
        : [t0]"=&s"(t0), "=&{v164}"(ctr), "+{v159}"(so), "=&{v160}"(tagv), "=&{v[24:31]}"(RB), "=&{v[44:47]}"(qv[0]), "=&{v[48:51]}"(qv[1]), "=&{v[52:55]}"(qv[2]), "=&{v[56:59]}"(qv[3]),
          "=&{v[64:79]}"(A0), "=&{v[80:95]}"(A1), "=&{v[96:111]}"(A2), "=&{v[112:127]}"(A3)
        : "{v152}"(k0), "{v153}"(k1), "{v154}"(k2), "{v155}"(ko), "{v156}"(ta), "{v157}"(se), "{v166}"(rb0[0]), "{v167}"(rb0[1]), "{v165}"(ha), "{v163}"(l16),
          [qb0]"s"(qp), [qb2]"s"(qp+4096), [mk]"s"(0x180u), [em]"s"(1ull|(512ull<<w)), [hd]"s"(crs),
          [kb0]"s"(kb-6144), [kb3]"s"(kb), [kb5]"s"(kb+4096), [vb0]"s"(vb), [vb2]"s"(vb+4096), [sb]"s"(ws+WS_KV+td0*4096+j*512),
          [n0]"s"(kv0-EB), [ff]"s"(flags+F_PP+(td0>>6)*64+j), [zm]"s"(zm), [shd]"s"(sb0+EX_OFF+w*1024), [tgd]"s"(sb0+SYN+64), [shp]"s"(ptr64(a1[6],a1[7])+(bid&7)*1024), [fl]"s"(flags),
          [hp]"s"(ptr64(a2[0],a2[1])+d*4096), [dp]"s"(ptr64(a3[0],a3[1])+d*2)
        : "memory", "scc", PP_CLOB);
    }
    LANEVARS
    const unsigned tag=__builtin_amdgcn_readfirstlane(tagv)+1u;
    unsigned rem=33-PP_NB-kv0;
    barc(rem);
    const unsigned* pf=flags+F_PP+(d>>6)*64;
    const char* kvp=ws+WS_KV+((vb>>4)*8+(vb&7))*32768+((vb>>3)&1)*64+w*8192;
    const unsigned kvo=(unsigned)((l>>2)*128+(l&3)*16);
    int bidx=((l>=16)? l-16 : (l==0? 63 : l+47))*4;
    asm volatile("" : "+{v32}"(bidx));
    __builtin_amdgcn_s_setprio(3);
    while(rem>PREM) barc(rem);
    wait_flag_b<8>(pf, l*4, tag, rem CTRP);
    #pragma unroll
    for(int i=0;i<4;i++){ if(i%KTH==0 && i) barc(rem); gldsc(kvp+i*2048, kvo, crs+i*1024); }
    barc(rem); barc(rem);
    WAITV(0);
    asm volatile("s_mov_b32 m0, %2\n\ts_mov_b64 exec, 1\n\tglobal_load_lds_dword %0, %1 sc1\n\ts_mov_b64 exec, -1" :: "v"(0u), "s"(pf), "s"(sb0+SYN+100+w*4) : "memory");
    while(rem) barc(rem);
    { int sp=0; while(!__all(*(volatile LDS_AS unsigned*)(LDS_AS char*)(syn+100+(l&3)*4)==tag) && ++sp<(1<<22)) __builtin_amdgcn_s_sleep(1); }
    __builtin_amdgcn_s_setprio(CVP);
    TS(3);
    f32x16 ac0, ac1;
    {
      unsigned p0,p1,p2,p3,p4,p5,p6;
      asm volatile("s_nop 1\n\t" CV_ASM
        : "=&{v[128:143]}"(ac0), "=&{v[144:159]}"(ac1), [p0]"=&s"(p0), [p1]"=&s"(p1), [p2]"=&s"(p2), [p3]"=&s"(p3), [p4]"=&s"(p4), [p5]"=&s"(p5), [p6]"=&s"(p6)
          , "+{v[44:47]}"(qv[0]), "+{v[48:51]}"(qv[1]), "+{v[52:55]}"(qv[2]), "+{v[56:59]}"(qv[3])
        : "{v[64:79]}"(A0), "{v[80:95]}"(A1), "{v[96:111]}"(A2), "{v[112:127]}"(A3),
          "{v[24:31]}"(RB), "{v32}"(bidx), "{v33}"(sb0+SYN+80+w*4)
          , "{v43}"((l>>3)*8192+(l&7)*16), [q0]"s"(qp), [q2]"s"(qp+4096)
        : "memory", "scc", CV_CLOB);
    }
    TS(4);
    __builtin_amdgcn_s_setprio(3);
    {
      const unsigned yo=(unsigned)((256*(g&1)+16*fr+4*(g&2))*2);
      #pragma unroll
      for(int J=0;J<8;J+=2){
        #define YA(i) ((i)<16? ac0[(i)&15] : ac1[(i)&15])
        const u32x2 a={pack2(YA(4*J),YA(4*J+1)),pack2(YA(4*J+2),YA(4*J+3))}, b={pack2(YA(4*J+4),YA(4*J+5)),pack2(YA(4*J+6),YA(4*J+7))};
        st16cs(ws+WS_Y+d*4096+512*J, yo, widen(a,b));
      }
    }
    WAITV(0);
    if(lds_inc(syn+24,l)==3u){ if(l<16) stflag(flags+F_Y+(l*16+(vb>>4))*16+(vb&15), tag); }
    TS(5);
    if(w==0){
      SPIN(rd1(syn+96)==0u,2);
      wait_flag<YSLP>(flags+F_Y+fb*16+(l&15), tag);
      SPIN(rd1(syn+24)<4u,1);
      PUB(sb0+(l==0? SYN+28 : SINK+l*4), 1u);
    } else SPIN(rd1(syn+28)==0u,4);
    #pragma unroll
    for(int i=0;i<4;i++) gldsc(ws+WS_Y+(fd0+i*16)*4096+fs0*2, (l>>2)*4096+(l&3)*16, crs+i*1024);
    WAITV(0);
    {
      u32x4 y2[4];
      #pragma unroll
      for(int i=0;i<4;i++){ const char* yp=cr+((i>>1)*32+g*8+tq)*64+(i&1)*32+tp*8; y2[i]=__builtin_bit_cast(u32x4,cat44(trread(yp),trread(yp+256))); }
      asm volatile("" : "+v"(y2[0]), "+v"(y2[1]), "+v"(y2[2]), "+v"(y2[3]) :: "memory");
      #pragma unroll
      for(int i=0;i<4;i++) *(u32x4*)(cr+((i&1)*16+fr)*128+((((i>>1)*4+g)^(fr&7))*16))=y2[i];
      asm volatile("" ::: "memory");
      char* op=out+fs0*4096+fd0*2; const unsigned oo=(unsigned)((l>>3)*16384+(l&7)*16);
      #pragma unroll
      for(int i=0;i<4;i++){
        const int row=4*(l>>3)+i;
        const u32x4 y4=*(const u32x4*)(cr+row*128+(((l&7)^(row&7))*16)), q4=qv[i]; u32x4 ov;
        #pragma unroll
        for(int e=0;e<4;e++) ov[e]=pack2(bflo(q4[e])*bflo(y4[e]), bfhi(q4[e])*bfhi(y4[e]));
        stout(op+i*4096, oo, ov);
      }
    }
    ENDW TS(6); TOUT(7);
    __builtin_amdgcn_endpgm();
  }
}

extern "C" void kernel_launch(void* const* d_in, const int* in_sizes, int n_in, void* d_out, int out_size, void* d_ws, size_t ws_size, hipStream_t stream){
  const u16* q=(const u16*)d_in[0]; const u16* k=(const u16*)d_in[1]; const u16* v=(const u16*)d_in[2];
  const u16* shk=(const u16*)d_in[3]; const u16* hk=(const u16*)d_in[4]; const u16* sD=(const u16*)d_in[5];
  const u16* x=(const u16*)d_in[6]; const u16* wqkv=(const u16*)d_in[7]; const u16* wo=(const u16*)d_in[8];
  fused_kernel<<<256,768,0,stream>>>(x,wqkv,wo,(char*)d_ws,(u16*)d_out,k,v,shk,hk,q,sD);
}
```

```cpp
#include <hip/hip_runtime.h>
#include <hip/hip_bf16.h>
#include <stdint.h>

typedef unsigned short u16;
typedef short bf16x8 __attribute__((ext_vector_type(8)));
typedef short s16x4 __attribute__((ext_vector_type(4)));
typedef float f32x2 __attribute__((ext_vector_type(2)));
typedef float f32x4 __attribute__((ext_vector_type(4)));
typedef float f32x8 __attribute__((ext_vector_type(8)));
typedef float f32x16 __attribute__((ext_vector_type(16)));
typedef unsigned u32x4 __attribute__((ext_vector_type(4)));
typedef unsigned u32x2 __attribute__((ext_vector_type(2)));
typedef __bf16 bf16x2_t __attribute__((ext_vector_type(2)));
#define LDS_AS __attribute__((address_space(3)))

__device__ __forceinline__ float bflo(unsigned x){ return __uint_as_float(x<<16); }
__device__ __forceinline__ float bfhi(unsigned x){ return __uint_as_float(x&0xffff0000u); }
__device__ __forceinline__ unsigned pack2(float lo, float hi){ return __builtin_bit_cast(unsigned, __builtin_convertvector(f32x2{lo,hi}, bf16x2_t)); }
__device__ __forceinline__ float mul1(float a, float b){ float r; asm("v_mul_f32 %0, %1, %2" : "=v"(r) : "v"(a), "v"(b)); return r; }
__device__ __forceinline__ float fma1(float a, float b, float c){ float r; asm("v_fma_f32 %0, %1, %2, %3" : "=v"(r) : "v"(a), "v"(b), "v"(c)); return r; }

__device__ __forceinline__ void glds(const void* sbase, unsigned voff, unsigned ldsaddr){
  asm volatile("s_mov_b32 m0, %2\n\ts_nop 0\n\tglobal_load_lds_dwordx4 %0, %1" :: "v"(voff), "s"(sbase), "s"(ldsaddr) : "memory");
}
__device__ __forceinline__ void gldsc(const void* sbase, unsigned voff, unsigned ldsaddr){
  asm volatile("s_mov_b32 m0, %2\n\ts_nop 0\n\tglobal_load_lds_dwordx4 %0, %1 sc1" :: "v"(voff), "s"(sbase), "s"(ldsaddr) : "memory");
}
__device__ __forceinline__ void touch(const void* sbase, unsigned voff, unsigned ldsaddr){
  asm volatile("s_mov_b32 m0, %2\n\ts_nop 0\n\tglobal_load_lds_dword %0, %1" :: "v"(voff), "s"(sbase), "s"(ldsaddr) : "memory");
}
__device__ __forceinline__ void st16c(void* p, u32x4 v){ asm volatile("global_store_dwordx4 %0, %1, off sc1\n\ts_nop 1" :: "v"(p), "v"(v) : "memory"); }
__device__ __forceinline__ void st16cs(void* sbase, unsigned voff, u32x4 v){ asm volatile("global_store_dwordx4 %0, %1, %2 sc1\n\ts_nop 1" :: "v"(voff), "v"(v), "s"(sbase) : "memory"); }
#define GLB_AS __attribute__((address_space(1)))
__device__ __forceinline__ unsigned ldflag(const unsigned* p){ return __hip_atomic_load((const GLB_AS unsigned*)p, __ATOMIC_RELAXED, __HIP_MEMORY_SCOPE_AGENT); }
__device__ __forceinline__ void stflag(unsigned* p, unsigned v){ __hip_atomic_store((GLB_AS unsigned*)p, v, __ATOMIC_RELAXED, __HIP_MEMORY_SCOPE_AGENT); }
template<int SLP> __device__ __forceinline__ void wait_flag(const unsigned* p, unsigned tag){
  int spins=0;
  while(true){ unsigned v=ldflag(p); if(__all(v==tag)) break; __builtin_amdgcn_s_sleep(SLP); if(++spins>(1<<21)) break; }
}
__device__ __forceinline__ unsigned lds_addr(const void* p){ return (unsigned)(size_t)(const LDS_AS void*)p; }
#define WAITV(n) asm volatile("s_waitcnt vmcnt(" #n ")":::"memory")
#define WAITL() asm volatile("s_waitcnt lgkmcnt(0)":::"memory")
#define SBAR() asm volatile("s_barrier":::"memory")
#define PUBM(addr,val) asm volatile("ds_max_u32 %0, %1" :: "v"(addr), "v"(val) : "memory")
#define PUB(addr,val) asm volatile("ds_write_b32 %0, %1" :: "v"(addr), "v"(val) : "memory")
__device__ __forceinline__ s16x4 trread(const char* p){ return __builtin_amdgcn_ds_read_tr16_b64_v4i16((LDS_AS s16x4*)p); }
__device__ __forceinline__ bf16x8 cat44(s16x4 a, s16x4 b){ return __builtin_shufflevector(a,b,0,1,2,3,4,5,6,7); }
__device__ __forceinline__ int wswz(int row){ return ((row>>1)&1) | (((row>>3)&1)<<1); }
__device__ __forceinline__ u32x4 widen(u32x2 a, u32x2 b){
  auto r0=__builtin_amdgcn_permlane16_swap(a[0],b[0],false,false);
  auto r1=__builtin_amdgcn_permlane16_swap(a[1],b[1],false,false);
  return u32x4{r0[0],r1[0],r0[1],r1[1]};
}

constexpr int S=2048, DM=1024;
constexpr int WS=12288, XS=18432, X_OFF=6*WS;
constexpr int CREG=4608, C_OFF=131072;
constexpr int EX_OFF=C_OFF+4*CREG;
constexpr int SYN=EX_OFF+8192;
constexpr int SINK=SYN+256;
constexpr int ALL_LDS=SINK+768;
constexpr int F_PP=1024, F_O=4096, F_Y=8192;
#define KV0 1
#define KVS 3
#define LPR 2
#define CPR 2
#define GPR 1
#define CGATE 0
#define OSLP 2
#define YSLP 8
#define TPX 16
#define TPW 2
#define TWO 0
#define CVP 1
#define G2P 3
#define OUTC 1
#define ROT 0
#define WTL 2
#define PB2 1
#define PREM 9
#define KTH 1
#define YDEF 3
#define OPOL 1
#define CODEL 272
#define MIX 0
#define LOCA 3
#define XLOC 1
#define PGT 0
#define PGO 0
#define QREM 0
#define EB 0
#define TW0 1
#define ORDM 1
#define NF 7u
#define NPOL 1
#define PD0 0
#define PD1 4
#define HH 0
#define HW 3
#define HKV 4
#define YPOL 1
#define YS2 2
#define HLAST 1

#define N2_ASM \
  "v_mov_b64 v[64:65], 0\n\t" \
  "v_mov_b64 v[66:67], 0\n\t" \
  "v_mov_b64 v[68:69], 0\n\t" \
  "v_mov_b64 v[70:71], 0\n\t" \
  "v_mov_b64 v[72:73], 0\n\t" \
  "v_mov_b64 v[74:75], 0\n\t" \
  "v_mov_b64 v[76:77], 0\n\t" \
  "v_mov_b64 v[78:79], 0\n\t" \
  "v_mov_b64 v[80:81], 0\n\t" \
  "v_mov_b64 v[82:83], 0\n\t" \
  "v_mov_b64 v[84:85], 0\n\t" \
  "v_mov_b64 v[86:87], 0\n\t" \
  "v_mov_b64 v[88:89], 0\n\t" \
  "v_mov_b64 v[90:91], 0\n\t" \
  "v_mov_b64 v[92:93], 0\n\t" \
  "v_mov_b64 v[94:95], 0\n\t" \
  "v_mov_b64 v[96:97], 0\n\t" \
  "v_mov_b64 v[98:99], 0\n\t" \
  "v_mov_b64 v[100:101], 0\n\t" \
  "v_mov_b64 v[102:103], 0\n\t" \
  "v_mov_b64 v[104:105], 0\n\t" \
  "v_mov_b64 v[106:107], 0\n\t" \
  "v_mov_b64 v[108:109], 0\n\t" \
  "v_mov_b64 v[110:111], 0\n\t" \
  "v_mov_b64 v[112:113], 0\n\t" \
  "v_mov_b64 v[114:115], 0\n\t" \
  "v_mov_b64 v[116:117], 0\n\t" \
  "v_mov_b64 v[118:119], 0\n\t" \
  "v_mov_b64 v[120:121], 0\n\t" \
  "v_mov_b64 v[122:123], 0\n\t" \
  "v_mov_b64 v[124:125], 0\n\t" \
  "v_mov_b64 v[126:127], 0\n\t" \
  "v_mov_b64 v[128:129], 0\n\t" \
  "v_mov_b64 v[130:131], 0\n\t" \
  "v_mov_b64 v[132:133], 0\n\t" \
  "v_mov_b64 v[134:135], 0\n\t" \
  "v_mov_b64 v[136:137], 0\n\t" \
  "v_mov_b64 v[138:139], 0\n\t" \
  "v_mov_b64 v[140:141], 0\n\t" \
  "v_mov_b64 v[142:143], 0\n\t" \
  "v_mov_b64 v[144:145], 0\n\t" \
  "v_mov_b64 v[146:147], 0\n\t" \
  "v_mov_b64 v[148:149], 0\n\t" \
  "v_mov_b64 v[150:151], 0\n\t" \
  "v_mov_b64 v[152:153], 0\n\t" \
  "v_mov_b64 v[154:155], 0\n\t" \
  "v_mov_b64 v[156:157], 0\n\t" \
  "v_mov_b64 v[158:159], 0\n\t" \
  "v_mov_b64 v[160:161], 0\n\t" \
  "v_mov_b64 v[162:163], 0\n\t" \
  "v_mov_b64 v[164:165], 0\n\t" \
  "v_mov_b64 v[166:167], 0\n\t" \
  "v_add_u32 v60, 36864, v58\n\t" \
  "v_add_u32 v61, 36864, v59\n\t" \
  "s_mov_b32 %[it], 0\n\t" \
  "s_cmp_lg_u32 %[r], 0\n\t" \
  "s_barrier\n\t" \
  "s_cbranch_scc1 NB_%=\n\t" \
  "ds_read_b64_tr_b16 v[40:41], v60 offset:13312\n\t" \
  "ds_read_b64_tr_b16 v[42:43], v60 offset:13824\n\t" \
  "ds_read_b64_tr_b16 v[44:45], v60 offset:14336\n\t" \
  "ds_read_b64_tr_b16 v[46:47], v60 offset:14848\n\t" \
  "ds_read_b64_tr_b16 v[48:49], v61 offset:13312\n\t" \
  "ds_read_b64_tr_b16 v[50:51], v61 offset:13824\n\t" \
  "ds_read_b128 v[0:3], v56 offset:36864\n\t" \
  "ds_read_b128 v[4:7], v56 offset:38912\n\t" \
  "ds_read_b128 v[8:11], v56 offset:40960\n\t" \
  "ds_read_b128 v[12:15], v56 offset:43008\n\t" \
  "ds_read_b128 v[16:19], v56 offset:45056\n\t" \
  "s_mov_b32 s68, 0x50000\n\t" \
  "s_mov_b32 s69, 0x5550055\n\t" \
  "s_mov_b32 s70, 0xa0000\n\t" \
  "s_mov_b32 s71, 0xaaa00aa\n\t" \
  "s_mov_b32 s72, 0x150001\n\t" \
  "s_mov_b32 s73, 0x15550155\n\t" \
  "s_mov_b32 s74, 0x2a0002\n\t" \
  "s_mov_b32 s75, 0x2aaa02aa\n\t" \
  "s_mov_b32 s76, 0x55505555\n\t" \
  "s_mov_b32 s77, 0x50005500\n\t" \
  "s_mov_b32 s78, 0xaaa0aaaa\n\t" \
  "s_mov_b32 s79, 0xa000aa00\n\t" \
  "s_mov_b32 s80, 0x55405554\n\t" \
  "s_mov_b32 s81, 0x40005400\n\t" \
  "s_mov_b32 s82, 0xaa80aaa8\n\t" \
  "s_mov_b32 s83, 0x8000a800\n\t" \
  "s_mov_b32 s84, 0xaaaaaaaa\n\t" \
  "s_mov_b32 s85, 0xaaaaaaaa\n\t" \
  "s_waitcnt lgkmcnt(0)\n\t" \
  "s_branch N4a_%=\n\t" \
  "N0a_%=:\n\t" \
  "s_waitcnt lgkmcnt(4)\n\t" \
  "v_mfma_f32_16x16x32_bf16 v[160:163], v[40:43], v[0:3], v[160:163]\n\t" \
  "ds_read_b64_tr_b16 v[52:53], v59 offset:2048\n\t" \
  "v_mfma_f32_16x16x32_bf16 v[96:99], v[40:43], v[4:7], v[96:99]\n\t" \
  "ds_read_b64_tr_b16 v[54:55], v59 offset:2560\n\t" \
  "v_mfma_f32_16x16x32_bf16 v[104:107], v[40:43], v[8:11], v[104:107]\n\t" \
  "s_barrier\n\t" \
  "v_mfma_f32_16x16x32_bf16 v[112:115], v[40:43], v[12:15], v[112:115]\n\t" \
  "v_mfma_f32_16x16x32_bf16 v[120:123], v[40:43], v[16:19], v[120:123]\n\t" \
  "s_waitcnt lgkmcnt(4)\n\t" \
  "v_mfma_f32_16x16x32_bf16 v[128:131], v[4:7], v[44:47], v[128:131]\n\t" \
  "ds_read_b64_tr_b16 v[40:41], v58 offset:0\n\t" \
  "v_mfma_f32_16x16x32_bf16 v[136:139], v[8:11], v[44:47], v[136:139]\n\t" \
  "ds_read_b64_tr_b16 v[42:43], v58 offset:512\n\t" \
  "v_mfma_f32_16x16x32_bf16 v[144:147], v[12:15], v[44:47], v[144:147]\n\t" \
  "ds_read_b128 v[20:23], v57 offset:0\n\t" \
  "v_mfma_f32_16x16x32_bf16 v[152:155], v[16:19], v[44:47], v[152:155]\n\t" \
  "ds_read_b128 v[24:27], v57 offset:2048\n\t" \
  "s_waitcnt lgkmcnt(6)\n\t" \
  "v_mfma_f32_16x16x32_bf16 v[164:167], v[48:51], v[0:3], v[164:167]\n\t" \
  "ds_read_b64_tr_b16 v[44:45], v59 offset:0\n\t" \
  "v_mfma_f32_16x16x32_bf16 v[100:103], v[48:51], v[4:7], v[100:103]\n\t" \
  "ds_read_b64_tr_b16 v[46:47], v59 offset:512\n\t" \
  "v_mfma_f32_16x16x32_bf16 v[108:111], v[48:51], v[8:11], v[108:111]\n\t" \
  "ds_read_b128 v[28:31], v57 offset:4096\n\t" \
  "v_mfma_f32_16x16x32_bf16 v[116:119], v[48:51], v[12:15], v[116:119]\n\t" \
  "ds_read_b128 v[32:35], v57 offset:6144\n\t" \
  "v_mfma_f32_16x16x32_bf16 v[124:127], v[48:51], v[16:19], v[124:127]\n\t" \
  "s_waitcnt lgkmcnt(8)\n\t" \
  "v_mfma_f32_16x16x32_bf16 v[132:135], v[4:7], v[52:55], v[132:135]\n\t" \
  "ds_read_b64_tr_b16 v[48:49], v58 offset:13312\n\t" \
  "v_mfma_f32_16x16x32_bf16 v[140:143], v[8:11], v[52:55], v[140:143]\n\t" \
  "ds_read_b64_tr_b16 v[50:51], v58 offset:13824\n\t" \
  "v_mfma_f32_16x16x32_bf16 v[148:151], v[12:15], v[52:55], v[148:151]\n\t" \
  "ds_read_b128 v[36:39], v57 offset:8192\n\t" \
  "v_mfma_f32_16x16x32_bf16 v[156:159], v[16:19], v[52:55], v[156:159]\n\t" \
  "s_waitcnt lgkmcnt(9)\n\t" \
  "v_mfma_f32_16x16x32_bf16 v[64:67], v[40:43], v[4:7], v[64:67]\n\t" \
  "ds_read_b64_tr_b16 v[52:53], v58 offset:14336\n\t" \
  "v_mfma_f32_16x16x32_bf16 v[72:75], v[40:43], v[8:11], v[72:75]\n\t" \
  "ds_read_b64_tr_b16 v[54:55], v58 offset:14848\n\t" \
  "v_mfma_f32_16x16x32_bf16 v[80:83], v[40:43], v[12:15], v[80:83]\n\t" \
  "v_mfma_f32_16x16x32_bf16 v[88:91], v[40:43], v[16:19], v[88:91]\n\t" \
  "s_waitcnt lgkmcnt(7)\n\t" \
  "v_mfma_f32_16x16x32_bf16 v[68:71], v[44:47], v[4:7], v[68:71]\n\t" \
  "ds_read_b64_tr_b16 v[40:41], v59 offset:13312\n\t" \
  "v_mfma_f32_16x16x32_bf16 v[76:79], v[44:47], v[8:11], v[76:79]\n\t" \
  "ds_read_b64_tr_b16 v[42:43], v59 offset:13824\n\t" \
  "v_mfma_f32_16x16x32_bf16 v[84:87], v[44:47], v[12:15], v[84:87]\n\t" \
  "v_mfma_f32_16x16x32_bf16 v[92:95], v[44:47], v[16:19], v[92:95]\n\t" \
  "N1a_%=:\n\t" \
  "s_waitcnt lgkmcnt(4)\n\t" \
  "v_mfma_f32_16x16x32_bf16 v[160:163], v[48:51], v[20:23], v[160:163]\n\t" \
  "ds_read_b64_tr_b16 v[44:45], v59 offset:14336\n\t" \
  "v_mfma_f32_16x16x32_bf16 v[96:99], v[48:51], v[24:27], v[96:99]\n\t" \
  "ds_read_b64_tr_b16 v[46:47], v59 offset:14848\n\t" \
  "v_mfma_f32_16x16x32_bf16 v[104:107], v[48:51], v[28:31], v[104:107]\n\t" \
  "s_barrier\n\t" \
  "v_mfma_f32_16x16x32_bf16 v[112:115], v[48:51], v[32:35], v[112:115]\n\t" \
  "v_mfma_f32_16x16x32_bf16 v[120:123], v[48:51], v[36:39], v[120:123]\n\t" \
  "s_waitcnt lgkmcnt(4)\n\t" \
  "v_mfma_f32_16x16x32_bf16 v[128:131], v[24:27], v[52:55], v[128:131]\n\t" \
  "ds_read_b64_tr_b16 v[48:49], v58 offset:12288\n\t" \
  "v_mfma_f32_16x16x32_bf16 v[136:139], v[28:31], v[52:55], v[136:139]\n\t" \
  "ds_read_b64_tr_b16 v[50:51], v58 offset:12800\n\t" \
  "v_mfma_f32_16x16x32_bf16 v[144:147], v[32:35], v[52:55], v[144:147]\n\t" \
  "ds_read_b128 v[0:3], v56 offset:18432\n\t" \
  "v_mfma_f32_16x16x32_bf16 v[152:155], v[36:39], v[52:55], v[152:155]\n\t" \
  "ds_read_b128 v[4:7], v56 offset:20480\n\t" \
  "s_waitcnt lgkmcnt(6)\n\t" \
  "v_mfma_f32_16x16x32_bf16 v[164:167], v[40:43], v[20:23], v[164:167]\n\t" \
  "ds_read_b64_tr_b16 v[52:53], v59 offset:12288\n\t" \
  "v_mfma_f32_16x16x32_bf16 v[100:103], v[40:43], v[24:27], v[100:103]\n\t" \
  "ds_read_b64_tr_b16 v[54:55], v59 offset:12800\n\t" \
  "v_mfma_f32_16x16x32_bf16 v[108:111], v[40:43], v[28:31], v[108:111]\n\t" \
  "ds_read_b128 v[8:11], v56 offset:22528\n\t" \
  "v_mfma_f32_16x16x32_bf16 v[116:119], v[40:43], v[32:35], v[116:119]\n\t" \
  "ds_read_b128 v[12:15], v56 offset:24576\n\t" \
  "v_mfma_f32_16x16x32_bf16 v[124:127], v[40:43], v[36:39], v[124:127]\n\t" \
  "s_waitcnt lgkmcnt(8)\n\t" \
  "v_mfma_f32_16x16x32_bf16 v[132:135], v[24:27], v[44:47], v[132:135]\n\t" \
  "ds_read_b64_tr_b16 v[40:41], v58 offset:25600\n\t" \
  "v_mfma_f32_16x16x32_bf16 v[140:143], v[28:31], v[44:47], v[140:143]\n\t" \
  "ds_read_b64_tr_b16 v[42:43], v58 offset:26112\n\t" \
  "v_mfma_f32_16x16x32_bf16 v[148:151], v[32:35], v[44:47], v[148:151]\n\t" \
  "ds_read_b128 v[16:19], v56 offset:26624\n\t" \
  "v_mfma_f32_16x16x32_bf16 v[156:159], v[36:39], v[44:47], v[156:159]\n\t" \
  "s_waitcnt lgkmcnt(9)\n\t" \
  "v_mfma_f32_16x16x32_bf16 v[64:67], v[48:51], v[24:27], v[64:67]\n\t" \
  "ds_read_b64_tr_b16 v[44:45], v58 offset:26624\n\t" \
  "v_mfma_f32_16x16x32_bf16 v[72:75], v[48:51], v[28:31], v[72:75]\n\t" \
  "ds_read_b64_tr_b16 v[46:47], v58 offset:27136\n\t" \
  "v_mfma_f32_16x16x32_bf16 v[80:83], v[48:51], v[32:35], v[80:83]\n\t" \
  "v_mfma_f32_16x16x32_bf16 v[88:91], v[48:51], v[36:39], v[88:91]\n\t" \
  "s_waitcnt lgkmcnt(7)\n\t" \
  "v_mfma_f32_16x16x32_bf16 v[68:71], v[52:55], v[24:27], v[68:71]\n\t" \
  "ds_read_b64_tr_b16 v[48:49], v59 offset:25600\n\t" \
  "v_mfma_f32_16x16x32_bf16 v[76:79], v[52:55], v[28:31], v[76:79]\n\t" \
  "ds_read_b64_tr_b16 v[50:51], v59 offset:26112\n\t" \
  "v_mfma_f32_16x16x32_bf16 v[84:87], v[52:55], v[32:35], v[84:87]\n\t" \
  "v_mfma_f32_16x16x32_bf16 v[92:95], v[52:55], v[36:39], v[92:95]\n\t" \
  "N2a_%=:\n\t" \
  "s_waitcnt lgkmcnt(4)\n\t" \
  "v_mfma_f32_16x16x32_bf16 v[160:163], v[40:43], v[0:3], v[160:163]\n\t" \
  "ds_read_b64_tr_b16 v[52:53], v59 offset:26624\n\t" \
  "v_mfma_f32_16x16x32_bf16 v[96:99], v[40:43], v[4:7], v[96:99]\n\t" \
  "ds_read_b64_tr_b16 v[54:55], v59 offset:27136\n\t" \
  "v_mfma_f32_16x16x32_bf16 v[104:107], v[40:43], v[8:11], v[104:107]\n\t" \
  "s_barrier\n\t" \
  "v_mfma_f32_16x16x32_bf16 v[112:115], v[40:43], v[12:15], v[112:115]\n\t" \
  "v_mfma_f32_16x16x32_bf16 v[120:123], v[40:43], v[16:19], v[120:123]\n\t" \
  "s_waitcnt lgkmcnt(4)\n\t" \
  "v_mfma_f32_16x16x32_bf16 v[128:131], v[4:7], v[44:47], v[128:131]\n\t" \
  "ds_read_b64_tr_b16 v[40:41], v58 offset:24576\n\t" \
  "v_mfma_f32_16x16x32_bf16 v[136:139], v[8:11], v[44:47], v[136:139]\n\t" \
  "ds_read_b64_tr_b16 v[42:43], v58 offset:25088\n\t" \
  "v_mfma_f32_16x16x32_bf16 v[144:147], v[12:15], v[44:47], v[144:147]\n\t" \
  "ds_read_b128 v[20:23], v57 offset:18432\n\t" \
  "v_mfma_f32_16x16x32_bf16 v[152:155], v[16:19], v[44:47], v[152:155]\n\t" \
  "ds_read_b128 v[24:27], v57 offset:20480\n\t" \
  "s_waitcnt lgkmcnt(6)\n\t" \
  "v_mfma_f32_16x16x32_bf16 v[164:167], v[48:51], v[0:3], v[164:167]\n\t" \
  "ds_read_b64_tr_b16 v[44:45], v59 offset:24576\n\t" \
  "v_mfma_f32_16x16x32_bf16 v[100:103], v[48:51], v[4:7], v[100:103]\n\t" \
  "ds_read_b64_tr_b16 v[46:47], v59 offset:25088\n\t" \
  "v_mfma_f32_16x16x32_bf16 v[108:111], v[48:51], v[8:11], v[108:111]\n\t" \
  "ds_read_b128 v[28:31], v57 offset:22528\n\t" \
  "v_mfma_f32_16x16x32_bf16 v[116:119], v[48:51], v[12:15], v[116:119]\n\t" \
  "ds_read_b128 v[32:35], v57 offset:24576\n\t" \
  "v_mfma_f32_16x16x32_bf16 v[124:127], v[48:51], v[16:19], v[124:127]\n\t" \
  "s_waitcnt lgkmcnt(8)\n\t" \
  "v_mfma_f32_16x16x32_bf16 v[132:135], v[4:7], v[52:55], v[132:135]\n\t" \
  "ds_read_b64_tr_b16 v[48:49], v60 offset:1024\n\t" \
  "v_mfma_f32_16x16x32_bf16 v[140:143], v[8:11], v[52:55], v[140:143]\n\t" \
  "ds_read_b64_tr_b16 v[50:51], v60 offset:1536\n\t" \
  "v_mfma_f32_16x16x32_bf16 v[148:151], v[12:15], v[52:55], v[148:151]\n\t" \
  "ds_read_b128 v[36:39], v57 offset:26624\n\t" \
  "v_mfma_f32_16x16x32_bf16 v[156:159], v[16:19], v[52:55], v[156:159]\n\t" \
  "s_waitcnt lgkmcnt(9)\n\t" \
  "v_mfma_f32_16x16x32_bf16 v[64:67], v[40:43], v[4:7], v[64:67]\n\t" \
  "ds_read_b64_tr_b16 v[52:53], v60 offset:2048\n\t" \
  "v_mfma_f32_16x16x32_bf16 v[72:75], v[40:43], v[8:11], v[72:75]\n\t" \
  "ds_read_b64_tr_b16 v[54:55], v60 offset:2560\n\t" \
  "v_mfma_f32_16x16x32_bf16 v[80:83], v[40:43], v[12:15], v[80:83]\n\t" \
  "v_mfma_f32_16x16x32_bf16 v[88:91], v[40:43], v[16:19], v[88:91]\n\t" \
  "s_waitcnt lgkmcnt(7)\n\t" \
  "v_mfma_f32_16x16x32_bf16 v[68:71], v[44:47], v[4:7], v[68:71]\n\t" \
  "ds_read_b64_tr_b16 v[40:41], v61 offset:1024\n\t" \
  "v_mfma_f32_16x16x32_bf16 v[76:79], v[44:47], v[8:11], v[76:79]\n\t" \
  "ds_read_b64_tr_b16 v[42:43], v61 offset:1536\n\t" \
  "v_mfma_f32_16x16x32_bf16 v[84:87], v[44:47], v[12:15], v[84:87]\n\t" \
  "v_mfma_f32_16x16x32_bf16 v[92:95], v[44:47], v[16:19], v[92:95]\n\t" \
  "N3a_%=:\n\t" \
  "s_waitcnt lgkmcnt(4)\n\t" \
  "v_mfma_f32_16x16x32_bf16 v[160:163], v[48:51], v[20:23], v[160:163]\n\t" \
  "ds_read_b64_tr_b16 v[44:45], v61 offset:2048\n\t" \
  "v_mfma_f32_16x16x32_bf16 v[96:99], v[48:51], v[24:27], v[96:99]\n\t" \
  "ds_read_b64_tr_b16 v[46:47], v61 offset:2560\n\t" \
  "v_mfma_f32_16x16x32_bf16 v[104:107], v[48:51], v[28:31], v[104:107]\n\t" \
  "s_barrier\n\t" \
  "v_mfma_f32_16x16x32_bf16 v[112:115], v[48:51], v[32:35], v[112:115]\n\t" \
  "v_mfma_f32_16x16x32_bf16 v[120:123], v[48:51], v[36:39], v[120:123]\n\t" \
  "s_waitcnt lgkmcnt(4)\n\t" \
  "v_mfma_f32_16x16x32_bf16 v[128:131], v[24:27], v[52:55], v[128:131]\n\t" \
  "ds_read_b64_tr_b16 v[48:49], v60 offset:0\n\t" \
  "v_mfma_f32_16x16x32_bf16 v[136:139], v[28:31], v[52:55], v[136:139]\n\t" \
  "ds_read_b64_tr_b16 v[50:51], v60 offset:512\n\t" \
  "v_mfma_f32_16x16x32_bf16 v[144:147], v[32:35], v[52:55], v[144:147]\n\t" \
  "ds_read_b128 v[0:3], v56 offset:36864\n\t" \
  "v_mfma_f32_16x16x32_bf16 v[152:155], v[36:39], v[52:55], v[152:155]\n\t" \
  "ds_read_b128 v[4:7], v56 offset:38912\n\t" \
  "s_waitcnt lgkmcnt(6)\n\t" \
  "v_mfma_f32_16x16x32_bf16 v[164:167], v[40:43], v[20:23], v[164:167]\n\t" \
  "ds_read_b64_tr_b16 v[52:53], v61 offset:0\n\t" \
  "v_mfma_f32_16x16x32_bf16 v[100:103], v[40:43], v[24:27], v[100:103]\n\t" \
  "ds_read_b64_tr_b16 v[54:55], v61 offset:512\n\t" \
  "v_mfma_f32_16x16x32_bf16 v[108:111], v[40:43], v[28:31], v[108:111]\n\t" \
  "ds_read_b128 v[8:11], v56 offset:40960\n\t" \
  "v_mfma_f32_16x16x32_bf16 v[116:119], v[40:43], v[32:35], v[116:119]\n\t" \
  "ds_read_b128 v[12:15], v56 offset:43008\n\t" \
  "v_mfma_f32_16x16x32_bf16 v[124:127], v[40:43], v[36:39], v[124:127]\n\t" \
  "s_waitcnt lgkmcnt(8)\n\t" \
  "v_mfma_f32_16x16x32_bf16 v[132:135], v[24:27], v[44:47], v[132:135]\n\t" \
  "ds_read_b64_tr_b16 v[40:41], v60 offset:13312\n\t" \
  "v_mfma_f32_16x16x32_bf16 v[140:143], v[28:31], v[44:47], v[140:143]\n\t" \
  "ds_read_b64_tr_b16 v[42:43], v60 offset:13824\n\t" \
  "v_mfma_f32_16x16x32_bf16 v[148:151], v[32:35], v[44:47], v[148:151]\n\t" \
  "ds_read_b128 v[16:19], v56 offset:45056\n\t" \
  "v_mfma_f32_16x16x32_bf16 v[156:159], v[36:39], v[44:47], v[156:159]\n\t" \
  "s_waitcnt lgkmcnt(9)\n\t" \
  "v_mfma_f32_16x16x32_bf16 v[64:67], v[48:51], v[24:27], v[64:67]\n\t" \
  "ds_read_b64_tr_b16 v[44:45], v60 offset:14336\n\t" \
  "v_mfma_f32_16x16x32_bf16 v[72:75], v[48:51], v[28:31], v[72:75]\n\t" \
  "ds_read_b64_tr_b16 v[46:47], v60 offset:14848\n\t" \
  "v_mfma_f32_16x16x32_bf16 v[80:83], v[48:51], v[32:35], v[80:83]\n\t" \
  "v_mfma_f32_16x16x32_bf16 v[88:91], v[48:51], v[36:39], v[88:91]\n\t" \
  "s_waitcnt lgkmcnt(7)\n\t" \
  "v_mfma_f32_16x16x32_bf16 v[68:71], v[52:55], v[24:27], v[68:71]\n\t" \
  "ds_read_b64_tr_b16 v[48:49], v61 offset:13312\n\t" \
  "v_mfma_f32_16x16x32_bf16 v[76:79], v[52:55], v[28:31], v[76:79]\n\t" \
  "ds_read_b64_tr_b16 v[50:51], v61 offset:13824\n\t" \
  "v_mfma_f32_16x16x32_bf16 v[84:87], v[52:55], v[32:35], v[84:87]\n\t" \
  "v_mfma_f32_16x16x32_bf16 v[92:95], v[52:55], v[36:39], v[92:95]\n\t" \
  "N4a_%=:\n\t" \
  "s_waitcnt lgkmcnt(4)\n\t" \
  "v_mfma_f32_16x16x32_bf16 v[160:163], v[40:43], v[0:3], v[160:163]\n\t" \
  "ds_read_b64_tr_b16 v[52:53], v61 offset:14336\n\t" \
  "v_mfma_f32_16x16x32_bf16 v[96:99], v[40:43], v[4:7], v[96:99]\n\t" \
  "ds_read_b64_tr_b16 v[54:55], v61 offset:14848\n\t" \
  "v_mfma_f32_16x16x32_bf16 v[104:107], v[40:43], v[8:11], v[104:107]\n\t" \
  "s_barrier\n\t" \
  "v_mfma_f32_16x16x32_bf16 v[112:115], v[40:43], v[12:15], v[112:115]\n\t" \
  "v_mfma_f32_16x16x32_bf16 v[120:123], v[40:43], v[16:19], v[120:123]\n\t" \
  "s_waitcnt lgkmcnt(4)\n\t" \
  "v_mfma_f32_16x16x32_bf16 v[128:131], v[4:7], v[44:47], v[128:131]\n\t" \
  "ds_read_b64_tr_b16 v[40:41], v60 offset:12288\n\t" \
  "v_mfma_f32_16x16x32_bf16 v[136:139], v[8:11], v[44:47], v[136:139]\n\t" \
  "ds_read_b64_tr_b16 v[42:43], v60 offset:12800\n\t" \
  "v_mfma_f32_16x16x32_bf16 v[144:147], v[12:15], v[44:47], v[144:147]\n\t" \
  "ds_read_b128 v[20:23], v57 offset:36864\n\t" \
  "v_mfma_f32_16x16x32_bf16 v[152:155], v[16:19], v[44:47], v[152:155]\n\t" \
  "ds_read_b128 v[24:27], v57 offset:38912\n\t" \
  "s_waitcnt lgkmcnt(6)\n\t" \
  "v_mfma_f32_16x16x32_bf16 v[164:167], v[48:51], v[0:3], v[164:167]\n\t" \
  "ds_read_b64_tr_b16 v[44:45], v61 offset:12288\n\t" \
  "v_mfma_f32_16x16x32_bf16 v[100:103], v[48:51], v[4:7], v[100:103]\n\t" \
  "ds_read_b64_tr_b16 v[46:47], v61 offset:12800\n\t" \
  "v_mfma_f32_16x16x32_bf16 v[108:111], v[48:51], v[8:11], v[108:111]\n\t" \
  "ds_read_b128 v[28:31], v57 offset:40960\n\t" \
  "v_mfma_f32_16x16x32_bf16 v[116:119], v[48:51], v[12:15], v[116:119]\n\t" \
  "ds_read_b128 v[32:35], v57 offset:43008\n\t" \
  "v_mfma_f32_16x16x32_bf16 v[124:127], v[48:51], v[16:19], v[124:127]\n\t" \
  "s_waitcnt lgkmcnt(8)\n\t" \
  "v_mfma_f32_16x16x32_bf16 v[132:135], v[4:7], v[52:55], v[132:135]\n\t" \
  "ds_read_b64_tr_b16 v[48:49], v60 offset:25600\n\t" \
  "v_mfma_f32_16x16x32_bf16 v[140:143], v[8:11], v[52:55], v[140:143]\n\t" \
  "ds_read_b64_tr_b16 v[50:51], v60 offset:26112\n\t" \
  "v_mfma_f32_16x16x32_bf16 v[148:151], v[12:15], v[52:55], v[148:151]\n\t" \
  "ds_read_b128 v[36:39], v57 offset:45056\n\t" \
  "v_mfma_f32_16x16x32_bf16 v[156:159], v[16:19], v[52:55], v[156:159]\n\t" \
  "s_waitcnt lgkmcnt(9)\n\t" \
  "v_mfma_f32_16x16x32_bf16 v[64:67], v[40:43], v[4:7], v[64:67]\n\t" \
  "ds_read_b64_tr_b16 v[52:53], v60 offset:26624\n\t" \
  "v_mfma_f32_16x16x32_bf16 v[72:75], v[40:43], v[8:11], v[72:75]\n\t" \
  "ds_read_b64_tr_b16 v[54:55], v60 offset:27136\n\t" \
  "v_mfma_f32_16x16x32_bf16 v[80:83], v[40:43], v[12:15], v[80:83]\n\t" \
  "v_mfma_f32_16x16x32_bf16 v[88:91], v[40:43], v[16:19], v[88:91]\n\t" \
  "s_waitcnt lgkmcnt(7)\n\t" \
  "v_mfma_f32_16x16x32_bf16 v[68:71], v[44:47], v[4:7], v[68:71]\n\t" \
  "ds_read_b64_tr_b16 v[40:41], v61 offset:25600\n\t" \
  "v_mfma_f32_16x16x32_bf16 v[76:79], v[44:47], v[8:11], v[76:79]\n\t" \
  "ds_read_b64_tr_b16 v[42:43], v61 offset:26112\n\t" \
  "v_mfma_f32_16x16x32_bf16 v[84:87], v[44:47], v[12:15], v[84:87]\n\t" \
  "v_mfma_f32_16x16x32_bf16 v[92:95], v[44:47], v[16:19], v[92:95]\n\t" \
  "N5a_%=:\n\t" \
  "s_waitcnt lgkmcnt(4)\n\t" \
  "v_mfma_f32_16x16x32_bf16 v[160:163], v[48:51], v[20:23], v[160:163]\n\t" \
  "ds_read_b64_tr_b16 v[44:45], v61 offset:26624\n\t" \
  "v_mfma_f32_16x16x32_bf16 v[96:99], v[48:51], v[24:27], v[96:99]\n\t" \
  "ds_read_b64_tr_b16 v[46:47], v61 offset:27136\n\t" \
  "v_mfma_f32_16x16x32_bf16 v[104:107], v[48:51], v[28:31], v[104:107]\n\t" \
  "s_barrier\n\t" \
  "v_mfma_f32_16x16x32_bf16 v[112:115], v[48:51], v[32:35], v[112:115]\n\t" \
  "v_mfma_f32_16x16x32_bf16 v[120:123], v[48:51], v[36:39], v[120:123]\n\t" \
  "s_waitcnt lgkmcnt(4)\n\t" \
  "v_mfma_f32_16x16x32_bf16 v[128:131], v[24:27], v[52:55], v[128:131]\n\t" \
  "ds_read_b64_tr_b16 v[48:49], v60 offset:24576\n\t" \
  "v_mfma_f32_16x16x32_bf16 v[136:139], v[28:31], v[52:55], v[136:139]\n\t" \
  "ds_read_b64_tr_b16 v[50:51], v60 offset:25088\n\t" \
  "v_mfma_f32_16x16x32_bf16 v[144:147], v[32:35], v[52:55], v[144:147]\n\t" \
  "ds_read_b128 v[0:3], v56 offset:0\n\t" \
  "v_mfma_f32_16x16x32_bf16 v[152:155], v[36:39], v[52:55], v[152:155]\n\t" \
  "ds_read_b128 v[4:7], v56 offset:2048\n\t" \
  "s_waitcnt lgkmcnt(6)\n\t" \
  "v_mfma_f32_16x16x32_bf16 v[164:167], v[40:43], v[20:23], v[164:167]\n\t" \
  "ds_read_b64_tr_b16 v[52:53], v61 offset:24576\n\t" \
  "v_mfma_f32_16x16x32_bf16 v[100:103], v[40:43], v[24:27], v[100:103]\n\t" \
  "ds_read_b64_tr_b16 v[54:55], v61 offset:25088\n\t" \
  "v_mfma_f32_16x16x32_bf16 v[108:111], v[40:43], v[28:31], v[108:111]\n\t" \
  "ds_read_b128 v[8:11], v56 offset:4096\n\t" \
  "v_mfma_f32_16x16x32_bf16 v[116:119], v[40:43], v[32:35], v[116:119]\n\t" \
  "ds_read_b128 v[12:15], v56 offset:6144\n\t" \
  "v_mfma_f32_16x16x32_bf16 v[124:127], v[40:43], v[36:39], v[124:127]\n\t" \
  "s_waitcnt lgkmcnt(8)\n\t" \
  "v_mfma_f32_16x16x32_bf16 v[132:135], v[24:27], v[44:47], v[132:135]\n\t" \
  "ds_read_b64_tr_b16 v[40:41], v58 offset:1024\n\t" \
  "v_mfma_f32_16x16x32_bf16 v[140:143], v[28:31], v[44:47], v[140:143]\n\t" \
  "ds_read_b64_tr_b16 v[42:43], v58 offset:1536\n\t" \
  "v_mfma_f32_16x16x32_bf16 v[148:151], v[32:35], v[44:47], v[148:151]\n\t" \
  "ds_read_b128 v[16:19], v56 offset:8192\n\t" \
  "v_mfma_f32_16x16x32_bf16 v[156:159], v[36:39], v[44:47], v[156:159]\n\t" \
  "s_waitcnt lgkmcnt(9)\n\t" \
  "v_mfma_f32_16x16x32_bf16 v[64:67], v[48:51], v[24:27], v[64:67]\n\t" \
  "ds_read_b64_tr_b16 v[44:45], v58 offset:2048\n\t" \
  "v_mfma_f32_16x16x32_bf16 v[72:75], v[48:51], v[28:31], v[72:75]\n\t" \
  "ds_read_b64_tr_b16 v[46:47], v58 offset:2560\n\t" \
  "v_mfma_f32_16x16x32_bf16 v[80:83], v[48:51], v[32:35], v[80:83]\n\t" \
  "v_mfma_f32_16x16x32_bf16 v[88:91], v[48:51], v[36:39], v[88:91]\n\t" \
  "s_waitcnt lgkmcnt(7)\n\t" \
  "v_mfma_f32_16x16x32_bf16 v[68:71], v[52:55], v[24:27], v[68:71]\n\t" \
  "ds_read_b64_tr_b16 v[48:49], v59 offset:1024\n\t" \
  "v_mfma_f32_16x16x32_bf16 v[76:79], v[52:55], v[28:31], v[76:79]\n\t" \
  "ds_read_b64_tr_b16 v[50:51], v59 offset:1536\n\t" \
  "s_addk_i32 %[it], 1\n\t" \
  "v_mfma_f32_16x16x32_bf16 v[84:87], v[52:55], v[32:35], v[84:87]\n\t" \
  "s_cmp_lt_u32 %[it], 6\n\t" \
  "v_mfma_f32_16x16x32_bf16 v[92:95], v[52:55], v[36:39], v[92:95]\n\t" \
  "s_cbranch_scc1 N0a_%=\n\t" \
  "s_setprio 3\n\t" \
  "v_cvt_pk_bf16_f32 v120, v120, v121\n\t" \
  "v_cvt_pk_bf16_f32 v121, v122, v123\n\t" \
  "v_cvt_pk_bf16_f32 v122, v124, v125\n\t" \
  "v_cvt_pk_bf16_f32 v123, v126, v127\n\t" \
  "v_cvt_pk_bf16_f32 v28, v152, v153\n\t" \
  "v_cvt_pk_bf16_f32 v29, v154, v155\n\t" \
  "v_cvt_pk_bf16_f32 v38, v156, v157\n\t" \
  "v_cvt_pk_bf16_f32 v39, v158, v159\n\t" \
  "ds_write_b128 v63, v[120:123]\n\t" \
  "ds_write2_b64 v63, v[28:29], v[38:39] offset0:128 offset1:129\n\t" \
  "v_mov_b32 v57, %[fa]\n\t" \
  "v_mov_b32 v58, 1\n\t" \
  "s_mov_b64 exec, 1\n\t" \
  "ds_write_b32 v57, v58\n\t" \
  "s_mov_b64 exec, -1\n\t" \
  "v_cvt_pk_bf16_f32 v160, v160, v161\n\t" \
  "v_cvt_pk_bf16_f32 v161, v162, v163\n\t" \
  "v_cvt_pk_bf16_f32 v162, v164, v165\n\t" \
  "v_cvt_pk_bf16_f32 v163, v166, v167\n\t" \
  "s_branch NX_%=\n\t" \
  "NB_%=:\n\t" \
  "ds_read_b64_tr_b16 v[40:41], v60 offset:14336\n\t" \
  "ds_read_b64_tr_b16 v[42:43], v60 offset:14848\n\t" \
  "ds_read_b64_tr_b16 v[44:45], v60 offset:13312\n\t" \
  "ds_read_b64_tr_b16 v[46:47], v60 offset:13824\n\t" \
  "ds_read_b64_tr_b16 v[48:49], v61 offset:14336\n\t" \
  "ds_read_b64_tr_b16 v[50:51], v61 offset:14848\n\t" \
  "ds_read_b128 v[0:3], v56 offset:36864\n\t" \
  "ds_read_b128 v[4:7], v56 offset:47104\n\t" \
  "ds_read_b128 v[8:11], v56 offset:49152\n\t" \
  "ds_read_b128 v[12:15], v56 offset:51200\n\t" \
  "ds_read_b128 v[16:19], v56 offset:53248\n\t" \
  "s_mov_b32 s68, 0x50000\n\t" \
  "s_mov_b32 s69, 0x5550055\n\t" \
  "s_mov_b32 s70, 0xa0000\n\t" \
  "s_mov_b32 s71, 0xaaa00aa\n\t" \
  "s_mov_b32 s72, 0x150001\n\t" \
  "s_mov_b32 s73, 0x15550155\n\t" \
  "s_mov_b32 s74, 0x2a0002\n\t" \
  "s_mov_b32 s75, 0x2aaa02aa\n\t" \
  "s_mov_b32 s76, 0x55505555\n\t" \
  "s_mov_b32 s77, 0x50005500\n\t" \
  "s_mov_b32 s78, 0xaaa0aaaa\n\t" \
  "s_mov_b32 s79, 0xa000aa00\n\t" \
  "s_mov_b32 s80, 0x55405554\n\t" \
  "s_mov_b32 s81, 0x40005400\n\t" \
  "s_mov_b32 s82, 0xaa80aaa8\n\t" \
  "s_mov_b32 s83, 0x8000a800\n\t" \
  "s_mov_b32 s84, 0xaaaaaaaa\n\t" \
  "s_mov_b32 s85, 0xaaaaaaaa\n\t" \
  "s_waitcnt lgkmcnt(0)\n\t" \
  "s_branch N4b_%=\n\t" \
  "N0b_%=:\n\t" \
  "s_waitcnt lgkmcnt(4)\n\t" \
  "v_mfma_f32_16x16x32_bf16 v[160:163], v[0:3], v[40:43], v[160:163]\n\t" \
  "ds_read_b64_tr_b16 v[52:53], v59 offset:1024\n\t" \
  "v_mfma_f32_16x16x32_bf16 v[128:131], v[4:7], v[40:43], v[128:131]\n\t" \
  "ds_read_b64_tr_b16 v[54:55], v59 offset:1536\n\t" \
  "v_mfma_f32_16x16x32_bf16 v[136:139], v[8:11], v[40:43], v[136:139]\n\t" \
  "s_barrier\n\t" \
  "v_mfma_f32_16x16x32_bf16 v[144:147], v[12:15], v[40:43], v[144:147]\n\t" \
  "v_mfma_f32_16x16x32_bf16 v[152:155], v[16:19], v[40:43], v[152:155]\n\t" \
  "s_waitcnt lgkmcnt(4)\n\t" \
  "v_mfma_f32_16x16x32_bf16 v[96:99], v[44:47], v[4:7], v[96:99]\n\t" \
  "ds_read_b64_tr_b16 v[40:41], v58 offset:0\n\t" \
  "v_mfma_f32_16x16x32_bf16 v[104:107], v[44:47], v[8:11], v[104:107]\n\t" \
  "ds_read_b64_tr_b16 v[42:43], v58 offset:512\n\t" \
  "v_mfma_f32_16x16x32_bf16 v[112:115], v[44:47], v[12:15], v[112:115]\n\t" \
  "ds_read_b128 v[20:23], v57 offset:0\n\t" \
  "v_mfma_f32_16x16x32_bf16 v[120:123], v[44:47], v[16:19], v[120:123]\n\t" \
  "ds_read_b128 v[24:27], v57 offset:10240\n\t" \
  "s_waitcnt lgkmcnt(6)\n\t" \
  "v_mfma_f32_16x16x32_bf16 v[164:167], v[0:3], v[48:51], v[164:167]\n\t" \
  "ds_read_b64_tr_b16 v[44:45], v59 offset:0\n\t" \
  "v_mfma_f32_16x16x32_bf16 v[132:135], v[4:7], v[48:51], v[132:135]\n\t" \
  "ds_read_b64_tr_b16 v[46:47], v59 offset:512\n\t" \
  "v_mfma_f32_16x16x32_bf16 v[140:143], v[8:11], v[48:51], v[140:143]\n\t" \
  "ds_read_b128 v[28:31], v57 offset:12288\n\t" \
  "v_mfma_f32_16x16x32_bf16 v[148:151], v[12:15], v[48:51], v[148:151]\n\t" \
  "ds_read_b128 v[32:35], v57 offset:14336\n\t" \
  "v_mfma_f32_16x16x32_bf16 v[156:159], v[16:19], v[48:51], v[156:159]\n\t" \
  "s_waitcnt lgkmcnt(8)\n\t" \
  "v_mfma_f32_16x16x32_bf16 v[100:103], v[52:55], v[4:7], v[100:103]\n\t" \
  "ds_read_b64_tr_b16 v[48:49], v58 offset:14336\n\t" \
  "v_mfma_f32_16x16x32_bf16 v[108:111], v[52:55], v[8:11], v[108:111]\n\t" \
  "ds_read_b64_tr_b16 v[50:51], v58 offset:14848\n\t" \
  "v_mfma_f32_16x16x32_bf16 v[116:119], v[52:55], v[12:15], v[116:119]\n\t" \
  "ds_read_b128 v[36:39], v57 offset:16384\n\t" \
  "v_mfma_f32_16x16x32_bf16 v[124:127], v[52:55], v[16:19], v[124:127]\n\t" \
  "s_waitcnt lgkmcnt(9)\n\t" \
  "v_mfma_f32_16x16x32_bf16 v[64:67], v[40:43], v[4:7], v[64:67]\n\t" \
  "ds_read_b64_tr_b16 v[52:53], v58 offset:13312\n\t" \
  "v_mfma_f32_16x16x32_bf16 v[72:75], v[40:43], v[8:11], v[72:75]\n\t" \
  "ds_read_b64_tr_b16 v[54:55], v58 offset:13824\n\t" \
  "v_mfma_f32_16x16x32_bf16 v[80:83], v[40:43], v[12:15], v[80:83]\n\t" \
  "v_mfma_f32_16x16x32_bf16 v[88:91], v[40:43], v[16:19], v[88:91]\n\t" \
  "s_waitcnt lgkmcnt(7)\n\t" \
  "v_mfma_f32_16x16x32_bf16 v[68:71], v[44:47], v[4:7], v[68:71]\n\t" \
  "ds_read_b64_tr_b16 v[40:41], v59 offset:14336\n\t" \
  "v_mfma_f32_16x16x32_bf16 v[76:79], v[44:47], v[8:11], v[76:79]\n\t" \
  "ds_read_b64_tr_b16 v[42:43], v59 offset:14848\n\t" \
  "v_mfma_f32_16x16x32_bf16 v[84:87], v[44:47], v[12:15], v[84:87]\n\t" \
  "v_mfma_f32_16x16x32_bf16 v[92:95], v[44:47], v[16:19], v[92:95]\n\t" \
  "N1b_%=:\n\t" \
  "s_waitcnt lgkmcnt(4)\n\t" \
  "v_mfma_f32_16x16x32_bf16 v[160:163], v[20:23], v[48:51], v[160:163]\n\t" \
  "ds_read_b64_tr_b16 v[44:45], v59 offset:13312\n\t" \
  "v_mfma_f32_16x16x32_bf16 v[128:131], v[24:27], v[48:51], v[128:131]\n\t" \
  "ds_read_b64_tr_b16 v[46:47], v59 offset:13824\n\t" \
  "v_mfma_f32_16x16x32_bf16 v[136:139], v[28:31], v[48:51], v[136:139]\n\t" \
  "s_barrier\n\t" \
  "v_mfma_f32_16x16x32_bf16 v[144:147], v[32:35], v[48:51], v[144:147]\n\t" \
  "v_mfma_f32_16x16x32_bf16 v[152:155], v[36:39], v[48:51], v[152:155]\n\t" \
  "s_waitcnt lgkmcnt(4)\n\t" \
  "v_mfma_f32_16x16x32_bf16 v[96:99], v[52:55], v[24:27], v[96:99]\n\t" \
  "ds_read_b64_tr_b16 v[48:49], v58 offset:12288\n\t" \
  "v_mfma_f32_16x16x32_bf16 v[104:107], v[52:55], v[28:31], v[104:107]\n\t" \
  "ds_read_b64_tr_b16 v[50:51], v58 offset:12800\n\t" \
  "v_mfma_f32_16x16x32_bf16 v[112:115], v[52:55], v[32:35], v[112:115]\n\t" \
  "ds_read_b128 v[0:3], v56 offset:18432\n\t" \
  "v_mfma_f32_16x16x32_bf16 v[120:123], v[52:55], v[36:39], v[120:123]\n\t" \
  "ds_read_b128 v[4:7], v56 offset:28672\n\t" \
  "s_waitcnt lgkmcnt(6)\n\t" \
  "v_mfma_f32_16x16x32_bf16 v[164:167], v[20:23], v[40:43], v[164:167]\n\t" \
  "ds_read_b64_tr_b16 v[52:53], v59 offset:12288\n\t" \
  "v_mfma_f32_16x16x32_bf16 v[132:135], v[24:27], v[40:43], v[132:135]\n\t" \
  "ds_read_b64_tr_b16 v[54:55], v59 offset:12800\n\t" \
  "v_mfma_f32_16x16x32_bf16 v[140:143], v[28:31], v[40:43], v[140:143]\n\t" \
  "ds_read_b128 v[8:11], v56 offset:30720\n\t" \
  "v_mfma_f32_16x16x32_bf16 v[148:151], v[32:35], v[40:43], v[148:151]\n\t" \
  "ds_read_b128 v[12:15], v56 offset:32768\n\t" \
  "v_mfma_f32_16x16x32_bf16 v[156:159], v[36:39], v[40:43], v[156:159]\n\t" \
  "s_waitcnt lgkmcnt(8)\n\t" \
  "v_mfma_f32_16x16x32_bf16 v[100:103], v[44:47], v[24:27], v[100:103]\n\t" \
  "ds_read_b64_tr_b16 v[40:41], v58 offset:26624\n\t" \
  "v_mfma_f32_16x16x32_bf16 v[108:111], v[44:47], v[28:31], v[108:111]\n\t" \
  "ds_read_b64_tr_b16 v[42:43], v58 offset:27136\n\t" \
  "v_mfma_f32_16x16x32_bf16 v[116:119], v[44:47], v[32:35], v[116:119]\n\t" \
  "ds_read_b128 v[16:19], v56 offset:34816\n\t" \
  "v_mfma_f32_16x16x32_bf16 v[124:127], v[44:47], v[36:39], v[124:127]\n\t" \
  "s_waitcnt lgkmcnt(9)\n\t" \
  "v_mfma_f32_16x16x32_bf16 v[64:67], v[48:51], v[24:27], v[64:67]\n\t" \
  "ds_read_b64_tr_b16 v[44:45], v58 offset:25600\n\t" \
  "v_mfma_f32_16x16x32_bf16 v[72:75], v[48:51], v[28:31], v[72:75]\n\t" \
  "ds_read_b64_tr_b16 v[46:47], v58 offset:26112\n\t" \
  "v_mfma_f32_16x16x32_bf16 v[80:83], v[48:51], v[32:35], v[80:83]\n\t" \
  "v_mfma_f32_16x16x32_bf16 v[88:91], v[48:51], v[36:39], v[88:91]\n\t" \
  "s_waitcnt lgkmcnt(7)\n\t" \
  "v_mfma_f32_16x16x32_bf16 v[68:71], v[52:55], v[24:27], v[68:71]\n\t" \
  "ds_read_b64_tr_b16 v[48:49], v59 offset:26624\n\t" \
  "v_mfma_f32_16x16x32_bf16 v[76:79], v[52:55], v[28:31], v[76:79]\n\t" \
  "ds_read_b64_tr_b16 v[50:51], v59 offset:27136\n\t" \
  "v_mfma_f32_16x16x32_bf16 v[84:87], v[52:55], v[32:35], v[84:87]\n\t" \
  "v_mfma_f32_16x16x32_bf16 v[92:95], v[52:55], v[36:39], v[92:95]\n\t" \
  "N2b_%=:\n\t" \
  "s_waitcnt lgkmcnt(4)\n\t" \
  "v_mfma_f32_16x16x32_bf16 v[160:163], v[0:3], v[40:43], v[160:163]\n\t" \
  "ds_read_b64_tr_b16 v[52:53], v59 offset:25600\n\t" \
  "v_mfma_f32_16x16x32_bf16 v[128:131], v[4:7], v[40:43], v[128:131]\n\t" \
  "ds_read_b64_tr_b16 v[54:55], v59 offset:26112\n\t" \
  "v_mfma_f32_16x16x32_bf16 v[136:139], v[8:11], v[40:43], v[136:139]\n\t" \
  "s_barrier\n\t" \
  "v_mfma_f32_16x16x32_bf16 v[144:147], v[12:15], v[40:43], v[144:147]\n\t" \
  "v_mfma_f32_16x16x32_bf16 v[152:155], v[16:19], v[40:43], v[152:155]\n\t" \
  "s_waitcnt lgkmcnt(4)\n\t" \
  "v_mfma_f32_16x16x32_bf16 v[96:99], v[44:47], v[4:7], v[96:99]\n\t" \
  "ds_read_b64_tr_b16 v[40:41], v58 offset:24576\n\t" \
  "v_mfma_f32_16x16x32_bf16 v[104:107], v[44:47], v[8:11], v[104:107]\n\t" \
  "ds_read_b64_tr_b16 v[42:43], v58 offset:25088\n\t" \
  "v_mfma_f32_16x16x32_bf16 v[112:115], v[44:47], v[12:15], v[112:115]\n\t" \
  "ds_read_b128 v[20:23], v57 offset:18432\n\t" \
  "v_mfma_f32_16x16x32_bf16 v[120:123], v[44:47], v[16:19], v[120:123]\n\t" \
  "ds_read_b128 v[24:27], v57 offset:28672\n\t" \
  "s_waitcnt lgkmcnt(6)\n\t" \
  "v_mfma_f32_16x16x32_bf16 v[164:167], v[0:3], v[48:51], v[164:167]\n\t" \
  "ds_read_b64_tr_b16 v[44:45], v59 offset:24576\n\t" \
  "v_mfma_f32_16x16x32_bf16 v[132:135], v[4:7], v[48:51], v[132:135]\n\t" \
  "ds_read_b64_tr_b16 v[46:47], v59 offset:25088\n\t" \
  "v_mfma_f32_16x16x32_bf16 v[140:143], v[8:11], v[48:51], v[140:143]\n\t" \
  "ds_read_b128 v[28:31], v57 offset:30720\n\t" \
  "v_mfma_f32_16x16x32_bf16 v[148:151], v[12:15], v[48:51], v[148:151]\n\t" \
  "ds_read_b128 v[32:35], v57 offset:32768\n\t" \
  "v_mfma_f32_16x16x32_bf16 v[156:159], v[16:19], v[48:51], v[156:159]\n\t" \
  "s_waitcnt lgkmcnt(8)\n\t" \
  "v_mfma_f32_16x16x32_bf16 v[100:103], v[52:55], v[4:7], v[100:103]\n\t" \
  "ds_read_b64_tr_b16 v[48:49], v60 offset:2048\n\t" \
  "v_mfma_f32_16x16x32_bf16 v[108:111], v[52:55], v[8:11], v[108:111]\n\t" \
  "ds_read_b64_tr_b16 v[50:51], v60 offset:2560\n\t" \
  "v_mfma_f32_16x16x32_bf16 v[116:119], v[52:55], v[12:15], v[116:119]\n\t" \
  "ds_read_b128 v[36:39], v57 offset:34816\n\t" \
  "v_mfma_f32_16x16x32_bf16 v[124:127], v[52:55], v[16:19], v[124:127]\n\t" \
  "s_waitcnt lgkmcnt(9)\n\t" \
  "v_mfma_f32_16x16x32_bf16 v[64:67], v[40:43], v[4:7], v[64:67]\n\t" \
  "ds_read_b64_tr_b16 v[52:53], v60 offset:1024\n\t" \
  "v_mfma_f32_16x16x32_bf16 v[72:75], v[40:43], v[8:11], v[72:75]\n\t" \
  "ds_read_b64_tr_b16 v[54:55], v60 offset:1536\n\t" \
  "v_mfma_f32_16x16x32_bf16 v[80:83], v[40:43], v[12:15], v[80:83]\n\t" \
  "v_mfma_f32_16x16x32_bf16 v[88:91], v[40:43], v[16:19], v[88:91]\n\t" \
  "s_waitcnt lgkmcnt(7)\n\t" \
  "v_mfma_f32_16x16x32_bf16 v[68:71], v[44:47], v[4:7], v[68:71]\n\t" \
  "ds_read_b64_tr_b16 v[40:41], v61 offset:2048\n\t" \
  "v_mfma_f32_16x16x32_bf16 v[76:79], v[44:47], v[8:11], v[76:79]\n\t" \
  "ds_read_b64_tr_b16 v[42:43], v61 offset:2560\n\t" \
  "v_mfma_f32_16x16x32_bf16 v[84:87], v[44:47], v[12:15], v[84:87]\n\t" \
  "v_mfma_f32_16x16x32_bf16 v[92:95], v[44:47], v[16:19], v[92:95]\n\t" \
  "N3b_%=:\n\t" \
  "s_waitcnt lgkmcnt(4)\n\t" \
  "v_mfma_f32_16x16x32_bf16 v[160:163], v[20:23], v[48:51], v[160:163]\n\t" \
  "ds_read_b64_tr_b16 v[44:45], v61 offset:1024\n\t" \
  "v_mfma_f32_16x16x32_bf16 v[128:131], v[24:27], v[48:51], v[128:131]\n\t" \
  "ds_read_b64_tr_b16 v[46:47], v61 offset:1536\n\t" \
  "v_mfma_f32_16x16x32_bf16 v[136:139], v[28:31], v[48:51], v[136:139]\n\t" \
  "s_barrier\n\t" \
  "v_mfma_f32_16x16x32_bf16 v[144:147], v[32:35], v[48:51], v[144:147]\n\t" \
  "v_mfma_f32_16x16x32_bf16 v[152:155], v[36:39], v[48:51], v[152:155]\n\t" \
  "s_waitcnt lgkmcnt(4)\n\t" \
  "v_mfma_f32_16x16x32_bf16 v[96:99], v[52:55], v[24:27], v[96:99]\n\t" \
  "ds_read_b64_tr_b16 v[48:49], v60 offset:0\n\t" \
  "v_mfma_f32_16x16x32_bf16 v[104:107], v[52:55], v[28:31], v[104:107]\n\t" \
  "ds_read_b64_tr_b16 v[50:51], v60 offset:512\n\t" \
  "v_mfma_f32_16x16x32_bf16 v[112:115], v[52:55], v[32:35], v[112:115]\n\t" \
  "ds_read_b128 v[0:3], v56 offset:36864\n\t" \
  "v_mfma_f32_16x16x32_bf16 v[120:123], v[52:55], v[36:39], v[120:123]\n\t" \
  "ds_read_b128 v[4:7], v56 offset:47104\n\t" \
  "s_waitcnt lgkmcnt(6)\n\t" \
  "v_mfma_f32_16x16x32_bf16 v[164:167], v[20:23], v[40:43], v[164:167]\n\t" \
  "ds_read_b64_tr_b16 v[52:53], v61 offset:0\n\t" \
  "v_mfma_f32_16x16x32_bf16 v[132:135], v[24:27], v[40:43], v[132:135]\n\t" \
  "ds_read_b64_tr_b16 v[54:55], v61 offset:512\n\t" \
  "v_mfma_f32_16x16x32_bf16 v[140:143], v[28:31], v[40:43], v[140:143]\n\t" \
  "ds_read_b128 v[8:11], v56 offset:49152\n\t" \
  "v_mfma_f32_16x16x32_bf16 v[148:151], v[32:35], v[40:43], v[148:151]\n\t" \
  "ds_read_b128 v[12:15], v56 offset:51200\n\t" \
  "v_mfma_f32_16x16x32_bf16 v[156:159], v[36:39], v[40:43], v[156:159]\n\t" \
  "s_waitcnt lgkmcnt(8)\n\t" \
  "v_mfma_f32_16x16x32_bf16 v[100:103], v[44:47], v[24:27], v[100:103]\n\t" \
  "ds_read_b64_tr_b16 v[40:41], v60 offset:14336\n\t" \
  "v_mfma_f32_16x16x32_bf16 v[108:111], v[44:47], v[28:31], v[108:111]\n\t" \
  "ds_read_b64_tr_b16 v[42:43], v60 offset:14848\n\t" \
  "v_mfma_f32_16x16x32_bf16 v[116:119], v[44:47], v[32:35], v[116:119]\n\t" \
  "ds_read_b128 v[16:19], v56 offset:53248\n\t" \
  "v_mfma_f32_16x16x32_bf16 v[124:127], v[44:47], v[36:39], v[124:127]\n\t" \
  "s_waitcnt lgkmcnt(9)\n\t" \
  "v_mfma_f32_16x16x32_bf16 v[64:67], v[48:51], v[24:27], v[64:67]\n\t" \
  "ds_read_b64_tr_b16 v[44:45], v60 offset:13312\n\t" \
  "v_mfma_f32_16x16x32_bf16 v[72:75], v[48:51], v[28:31], v[72:75]\n\t" \
  "ds_read_b64_tr_b16 v[46:47], v60 offset:13824\n\t" \
  "v_mfma_f32_16x16x32_bf16 v[80:83], v[48:51], v[32:35], v[80:83]\n\t" \
  "v_mfma_f32_16x16x32_bf16 v[88:91], v[48:51], v[36:39], v[88:91]\n\t" \
  "s_waitcnt lgkmcnt(7)\n\t" \
  "v_mfma_f32_16x16x32_bf16 v[68:71], v[52:55], v[24:27], v[68:71]\n\t" \
  "ds_read_b64_tr_b16 v[48:49], v61 offset:14336\n\t" \
  "v_mfma_f32_16x16x32_bf16 v[76:79], v[52:55], v[28:31], v[76:79]\n\t" \
  "ds_read_b64_tr_b16 v[50:51], v61 offset:14848\n\t" \
  "v_mfma_f32_16x16x32_bf16 v[84:87], v[52:55], v[32:35], v[84:87]\n\t" \
  "v_mfma_f32_16x16x32_bf16 v[92:95], v[52:55], v[36:39], v[92:95]\n\t" \
  "N4b_%=:\n\t" \
  "s_waitcnt lgkmcnt(4)\n\t" \
  "v_mfma_f32_16x16x32_bf16 v[160:163], v[0:3], v[40:43], v[160:163]\n\t" \
  "ds_read_b64_tr_b16 v[52:53], v61 offset:13312\n\t" \
  "v_mfma_f32_16x16x32_bf16 v[128:131], v[4:7], v[40:43], v[128:131]\n\t" \
  "ds_read_b64_tr_b16 v[54:55], v61 offset:13824\n\t" \
  "v_mfma_f32_16x16x32_bf16 v[136:139], v[8:11], v[40:43], v[136:139]\n\t" \
  "s_barrier\n\t" \
  "v_mfma_f32_16x16x32_bf16 v[144:147], v[12:15], v[40:43], v[144:147]\n\t" \
  "v_mfma_f32_16x16x32_bf16 v[152:155], v[16:19], v[40:43], v[152:155]\n\t" \
  "s_waitcnt lgkmcnt(4)\n\t" \
  "v_mfma_f32_16x16x32_bf16 v[96:99], v[44:47], v[4:7], v[96:99]\n\t" \
  "ds_read_b64_tr_b16 v[40:41], v60 offset:12288\n\t" \
  "v_mfma_f32_16x16x32_bf16 v[104:107], v[44:47], v[8:11], v[104:107]\n\t" \
  "ds_read_b64_tr_b16 v[42:43], v60 offset:12800\n\t" \
  "v_mfma_f32_16x16x32_bf16 v[112:115], v[44:47], v[12:15], v[112:115]\n\t" \
  "ds_read_b128 v[20:23], v57 offset:36864\n\t" \
  "v_mfma_f32_16x16x32_bf16 v[120:123], v[44:47], v[16:19], v[120:123]\n\t" \
  "ds_read_b128 v[24:27], v57 offset:47104\n\t" \
  "s_waitcnt lgkmcnt(6)\n\t" \
  "v_mfma_f32_16x16x32_bf16 v[164:167], v[0:3], v[48:51], v[164:167]\n\t" \
  "ds_read_b64_tr_b16 v[44:45], v61 offset:12288\n\t" \
  "v_mfma_f32_16x16x32_bf16 v[132:135], v[4:7], v[48:51], v[132:135]\n\t" \
  "ds_read_b64_tr_b16 v[46:47], v61 offset:12800\n\t" \
  "v_mfma_f32_16x16x32_bf16 v[140:143], v[8:11], v[48:51], v[140:143]\n\t" \
  "ds_read_b128 v[28:31], v57 offset:49152\n\t" \
  "v_mfma_f32_16x16x32_bf16 v[148:151], v[12:15], v[48:51], v[148:151]\n\t" \
  "ds_read_b128 v[32:35], v57 offset:51200\n\t" \
  "v_mfma_f32_16x16x32_bf16 v[156:159], v[16:19], v[48:51], v[156:159]\n\t" \
  "s_waitcnt lgkmcnt(8)\n\t" \
  "v_mfma_f32_16x16x32_bf16 v[100:103], v[52:55], v[4:7], v[100:103]\n\t" \
  "ds_read_b64_tr_b16 v[48:49], v60 offset:26624\n\t" \
  "v_mfma_f32_16x16x32_bf16 v[108:111], v[52:55], v[8:11], v[108:111]\n\t" \
  "ds_read_b64_tr_b16 v[50:51], v60 offset:27136\n\t" \
  "v_mfma_f32_16x16x32_bf16 v[116:119], v[52:55], v[12:15], v[116:119]\n\t" \
  "ds_read_b128 v[36:39], v57 offset:53248\n\t" \
  "v_mfma_f32_16x16x32_bf16 v[124:127], v[52:55], v[16:19], v[124:127]\n\t" \
  "s_waitcnt lgkmcnt(9)\n\t" \
  "v_mfma_f32_16x16x32_bf16 v[64:67], v[40:43], v[4:7], v[64:67]\n\t" \
  "ds_read_b64_tr_b16 v[52:53], v60 offset:25600\n\t" \
  "v_mfma_f32_16x16x32_bf16 v[72:75], v[40:43], v[8:11], v[72:75]\n\t" \
  "ds_read_b64_tr_b16 v[54:55], v60 offset:26112\n\t" \
  "v_mfma_f32_16x16x32_bf16 v[80:83], v[40:43], v[12:15], v[80:83]\n\t" \
  "v_mfma_f32_16x16x32_bf16 v[88:91], v[40:43], v[16:19], v[88:91]\n\t" \
  "s_waitcnt lgkmcnt(7)\n\t" \
  "v_mfma_f32_16x16x32_bf16 v[68:71], v[44:47], v[4:7], v[68:71]\n\t" \
  "ds_read_b64_tr_b16 v[40:41], v61 offset:26624\n\t" \
  "v_mfma_f32_16x16x32_bf16 v[76:79], v[44:47], v[8:11], v[76:79]\n\t" \
  "ds_read_b64_tr_b16 v[42:43], v61 offset:27136\n\t" \
  "v_mfma_f32_16x16x32_bf16 v[84:87], v[44:47], v[12:15], v[84:87]\n\t" \
  "v_mfma_f32_16x16x32_bf16 v[92:95], v[44:47], v[16:19], v[92:95]\n\t" \
  "N5b_%=:\n\t" \
  "s_waitcnt lgkmcnt(4)\n\t" \
  "v_mfma_f32_16x16x32_bf16 v[160:163], v[20:23], v[48:51], v[160:163]\n\t" \
  "ds_read_b64_tr_b16 v[44:45], v61 offset:25600\n\t" \
  "v_mfma_f32_16x16x32_bf16 v[128:131], v[24:27], v[48:51], v[128:131]\n\t" \
  "ds_read_b64_tr_b16 v[46:47], v61 offset:26112\n\t" \
  "v_mfma_f32_16x16x32_bf16 v[136:139], v[28:31], v[48:51], v[136:139]\n\t" \
  "s_barrier\n\t" \
  "v_mfma_f32_16x16x32_bf16 v[144:147], v[32:35], v[48:51], v[144:147]\n\t" \
  "v_mfma_f32_16x16x32_bf16 v[152:155], v[36:39], v[48:51], v[152:155]\n\t" \
  "s_waitcnt lgkmcnt(4)\n\t" \
  "v_mfma_f32_16x16x32_bf16 v[96:99], v[52:55], v[24:27], v[96:99]\n\t" \
  "ds_read_b64_tr_b16 v[48:49], v60 offset:24576\n\t" \
  "v_mfma_f32_16x16x32_bf16 v[104:107], v[52:55], v[28:31], v[104:107]\n\t" \
  "ds_read_b64_tr_b16 v[50:51], v60 offset:25088\n\t" \
  "v_mfma_f32_16x16x32_bf16 v[112:115], v[52:55], v[32:35], v[112:115]\n\t" \
  "ds_read_b128 v[0:3], v56 offset:0\n\t" \
  "v_mfma_f32_16x16x32_bf16 v[120:123], v[52:55], v[36:39], v[120:123]\n\t" \
  "ds_read_b128 v[4:7], v56 offset:10240\n\t" \
  "s_waitcnt lgkmcnt(6)\n\t" \
  "v_mfma_f32_16x16x32_bf16 v[164:167], v[20:23], v[40:43], v[164:167]\n\t" \
  "ds_read_b64_tr_b16 v[52:53], v61 offset:24576\n\t" \
  "v_mfma_f32_16x16x32_bf16 v[132:135], v[24:27], v[40:43], v[132:135]\n\t" \
  "ds_read_b64_tr_b16 v[54:55], v61 offset:25088\n\t" \
  "v_mfma_f32_16x16x32_bf16 v[140:143], v[28:31], v[40:43], v[140:143]\n\t" \
  "ds_read_b128 v[8:11], v56 offset:12288\n\t" \
  "v_mfma_f32_16x16x32_bf16 v[148:151], v[32:35], v[40:43], v[148:151]\n\t" \
  "ds_read_b128 v[12:15], v56 offset:14336\n\t" \
  "v_mfma_f32_16x16x32_bf16 v[156:159], v[36:39], v[40:43], v[156:159]\n\t" \
  "s_waitcnt lgkmcnt(8)\n\t" \
  "v_mfma_f32_16x16x32_bf16 v[100:103], v[44:47], v[24:27], v[100:103]\n\t" \
  "ds_read_b64_tr_b16 v[40:41], v58 offset:2048\n\t" \
  "v_mfma_f32_16x16x32_bf16 v[108:111], v[44:47], v[28:31], v[108:111]\n\t" \
  "ds_read_b64_tr_b16 v[42:43], v58 offset:2560\n\t" \
  "v_mfma_f32_16x16x32_bf16 v[116:119], v[44:47], v[32:35], v[116:119]\n\t" \
  "ds_read_b128 v[16:19], v56 offset:16384\n\t" \
  "v_mfma_f32_16x16x32_bf16 v[124:127], v[44:47], v[36:39], v[124:127]\n\t" \
  "s_waitcnt lgkmcnt(9)\n\t" \
  "v_mfma_f32_16x16x32_bf16 v[64:67], v[48:51], v[24:27], v[64:67]\n\t" \
  "ds_read_b64_tr_b16 v[44:45], v58 offset:1024\n\t" \
  "v_mfma_f32_16x16x32_bf16 v[72:75], v[48:51], v[28:31], v[72:75]\n\t" \
  "ds_read_b64_tr_b16 v[46:47], v58 offset:1536\n\t" \
  "v_mfma_f32_16x16x32_bf16 v[80:83], v[48:51], v[32:35], v[80:83]\n\t" \
  "v_mfma_f32_16x16x32_bf16 v[88:91], v[48:51], v[36:39], v[88:91]\n\t" \
  "s_waitcnt lgkmcnt(7)\n\t" \
  "v_mfma_f32_16x16x32_bf16 v[68:71], v[52:55], v[24:27], v[68:71]\n\t" \
  "ds_read_b64_tr_b16 v[48:49], v59 offset:2048\n\t" \
  "v_mfma_f32_16x16x32_bf16 v[76:79], v[52:55], v[28:31], v[76:79]\n\t" \
  "ds_read_b64_tr_b16 v[50:51], v59 offset:2560\n\t" \
  "s_addk_i32 %[it], 1\n\t" \
  "v_mfma_f32_16x16x32_bf16 v[84:87], v[52:55], v[32:35], v[84:87]\n\t" \
  "s_cmp_lt_u32 %[it], 6\n\t" \
  "v_mfma_f32_16x16x32_bf16 v[92:95], v[52:55], v[36:39], v[92:95]\n\t" \
  "s_cbranch_scc1 N0b_%=\n\t" \
  "s_setprio 3\n\t" \
  "v_cvt_pk_bf16_f32 v160, v160, v161\n\t" \
  "v_cvt_pk_bf16_f32 v161, v162, v163\n\t" \
  "v_cvt_pk_bf16_f32 v162, v164, v165\n\t" \
  "v_cvt_pk_bf16_f32 v163, v166, v167\n\t" \
  "ds_write_b128 v63, v[160:163] offset:2048\n\t" \
  "v_mov_b32 v57, %[fa]\n\t" \
  "v_mov_b32 v58, 1\n\t" \
  "s_mov_b64 exec, 1\n\t" \
  "ds_write_b32 v57, v58\n\t" \
  "s_mov_b64 exec, -1\n\t" \
  "v_cvt_pk_bf16_f32 v120, v120, v121\n\t" \
  "v_cvt_pk_bf16_f32 v121, v122, v123\n\t" \
  "v_cvt_pk_bf16_f32 v122, v124, v125\n\t" \
  "v_cvt_pk_bf16_f32 v123, v126, v127\n\t" \
  "v_cvt_pk_bf16_f32 v28, v152, v153\n\t" \
  "v_cvt_pk_bf16_f32 v29, v154, v155\n\t" \
  "v_cvt_pk_bf16_f32 v38, v156, v157\n\t" \
  "v_cvt_pk_bf16_f32 v39, v158, v159\n\t" \
  "NX_%=:\n\t" \
  "v_cvt_pk_bf16_f32 v96, v96, v97\n\t" \
  "v_cvt_pk_bf16_f32 v97, v98, v99\n\t" \
  "v_cvt_pk_bf16_f32 v98, v100, v101\n\t" \
  "v_cvt_pk_bf16_f32 v99, v102, v103\n\t" \
  "v_cvt_pk_bf16_f32 v104, v104, v105\n\t" \
  "v_cvt_pk_bf16_f32 v105, v106, v107\n\t" \
  "v_cvt_pk_bf16_f32 v106, v108, v109\n\t" \
  "v_cvt_pk_bf16_f32 v107, v110, v111\n\t" \
  "v_mov_b32 v60, 0xff61b1e6\n\t" \
  "v_cndmask_b32_e64 v56, v60, 0, s[68:69]\n\t" \
  "v_cndmask_b32_e64 v57, v60, 0, s[70:71]\n\t" \
  "v_cndmask_b32_e64 v58, v60, 0, s[72:73]\n\t" \
  "v_cndmask_b32_e64 v59, v60, 0, s[74:75]\n\t" \
  "v_cvt_pk_bf16_f32 v72, v72, v73\n\t" \
  "v_cvt_pk_bf16_f32 v73, v74, v75\n\t" \
  "v_cvt_pk_bf16_f32 v74, v76, v77\n\t" \
  "v_cvt_pk_bf16_f32 v75, v78, v79\n\t" \
  "v_cndmask_b32_e64 v52, v60, 0, s[76:77]\n\t" \
  "v_cndmask_b32_e64 v53, v60, 0, s[78:79]\n\t" \
  "v_cndmask_b32_e64 v54, v60, 0, s[80:81]\n\t" \
  "v_cndmask_b32_e64 v55, v60, 0, s[82:83]\n\t" \
  "v_cvt_pk_bf16_f32 v24, v136, v137\n\t" \
  "v_cvt_pk_bf16_f32 v25, v138, v139\n\t" \
  "v_cvt_pk_bf16_f32 v34, v140, v141\n\t" \
  "v_cvt_pk_bf16_f32 v35, v142, v143\n\t" \
  "v_cvt_pk_bf16_f32 v26, v144, v145\n\t" \
  "v_cvt_pk_bf16_f32 v27, v146, v147\n\t" \
  "v_cvt_pk_bf16_f32 v36, v148, v149\n\t" \
  "v_cvt_pk_bf16_f32 v37, v150, v151\n\t" \
  "v_mfma_f32_16x16x32_bf16 v[136:139], v[96:99], v[72:75], v[56:59]\n\t" \
  "v_cvt_pk_bf16_f32 v112, v112, v113\n\t" \
  "v_cvt_pk_bf16_f32 v113, v114, v115\n\t" \
  "v_mfma_f32_16x16x32_bf16 v[140:143], v[104:107], v[72:75], v[52:55]\n\t" \
  "v_cvt_pk_bf16_f32 v114, v116, v117\n\t" \
  "v_cvt_pk_bf16_f32 v115, v118, v119\n\t" \
  "v_cvt_pk_bf16_f32 v80, v80, v81\n\t" \
  "v_cvt_pk_bf16_f32 v81, v82, v83\n\t" \
  "v_cvt_pk_bf16_f32 v82, v84, v85\n\t" \
  "v_cvt_pk_bf16_f32 v83, v86, v87\n\t" \
  "v_cvt_pk_bf16_f32 v22, v128, v129\n\t" \
  "v_cvt_pk_bf16_f32 v23, v130, v131\n\t" \
  "v_mfma_f32_16x16x32_bf16 v[144:147], v[104:107], v[80:83], v[56:59]\n\t" \
  "v_cvt_pk_bf16_f32 v32, v132, v133\n\t" \
  "v_cvt_pk_bf16_f32 v33, v134, v135\n\t" \
  "v_mfma_f32_16x16x32_bf16 v[148:151], v[112:115], v[80:83], v[52:55]\n\t" \
  "v_cvt_pk_bf16_f32 v88, v88, v89\n\t" \
  "v_cvt_pk_bf16_f32 v89, v90, v91\n\t" \
  "v_cvt_pk_bf16_f32 v90, v92, v93\n\t" \
  "v_cvt_pk_bf16_f32 v91, v94, v95\n\t" \
  "v_mov_b32 v61, %[fp]\n\t" \
  "v_add_u32 v60, %[vo], v63\n\t" \
  "v_mfma_f32_16x16x32_bf16 v[152:155], v[112:115], v[88:91], v[56:59]\n\t" \
  "ds_read_b32 v0, v61\n\t" \
  "ds_read_b64 v[20:21], v60\n\t" \
  "ds_read_b64 v[30:31], v60 offset:8\n\t" \
  "s_mov_b64 exec, %[rm]\n\t" \
  "ds_read_b128 v[160:163], v63\n\t" \
  "s_mov_b64 exec, -1\n\t" \
  "v_mfma_f32_16x16x32_bf16 v[156:159], v[120:123], v[88:91], v[52:55]\n\t" \
  "v_cvt_pk_bf16_f32 v64, v64, v65\n\t" \
  "v_cvt_pk_bf16_f32 v65, v66, v67\n\t" \
  "v_cvt_pk_bf16_f32 v66, v68, v69\n\t" \
  "v_cvt_pk_bf16_f32 v67, v70, v71\n\t" \
  "v_max_f32 v136, v136, v137\n\t" \
  "v_max_f32 v138, v138, v139\n\t" \
  "v_mfma_f32_16x16x32_bf16 v[132:135], v[96:99], v[64:67], v[52:55]\n\t" \
  "v_max_f32 v140, v140, v141\n\t" \
  "v_max_f32 v142, v142, v143\n\t" \
  "AW_%=:\n\t" \
  "s_waitcnt lgkmcnt(0)\n\t" \
  "v_cmp_eq_u32 vcc, 0, v0\n\t" \
  "v_max3_f32 v5, v136, v138, v140\n\t" \
  "v_max_f32 v5, v5, v142\n\t" \
  "s_cbranch_vccnz AR_%=\n\t" \
  "s_cmp_eq_u32 %[z], 0\n\t" \
  "s_cbranch_scc1 AZ_%=\n\t" \
  "AY_%=:\n\t" \
  "v_mfma_f32_16x16x32_bf16 v[128:131], v[160:163], v[64:67], v[56:59]\n\t" \
  "v_max_f32 v144, v144, v145\n\t" \
  "v_max_f32 v146, v146, v147\n\t" \
  "v_max_f32 v148, v148, v149\n\t" \
  "v_max_f32 v150, v150, v151\n\t" \
  "v_max3_f32 v6, v144, v146, v148\n\t" \
  "v_max_f32 v6, v6, v150\n\t" \
  "v_max_f32 v152, v152, v153\n\t" \
  "v_max_f32 v154, v154, v155\n\t" \
  "v_max_f32 v156, v156, v157\n\t" \
  "v_max_f32 v158, v158, v159\n\t" \
  "v_max3_f32 v7, v152, v154, v156\n\t" \
  "v_max_f32 v7, v7, v158\n\t" \
  "v_mov_b32 v8, 0xc0c0100\n\t" \
  "v_mov_b32 v9, 0x1000c0c\n\t" \
  "v_max_f32 v128, v128, v129\n\t" \
  "v_max_f32 v130, v130, v131\n\t" \
  "v_max_f32 v132, v132, v133\n\t" \
  "v_max_f32 v134, v134, v135\n\t" \
  "v_max3_f32 v4, v128, v130, v132\n\t" \
  "v_max_f32 v4, v4, v134\n\t" \
  "v_cndmask_b32_e64 v60, v8, v9, s[84:85]\n\t" \
  "v_mov_b32 v8, 0xc0c0302\n\t" \
  "v_permlane16_swap_b32 v6, v7\n\t" \
  "v_permlane16_swap_b32 v4, v5\n\t" \
  "v_max_f32 v6, v6, v7\n\t" \
  "v_max_f32 v4, v4, v5\n\t" \
  "v_mov_b32 v9, 0x3020c0c\n\t" \
  "v_cndmask_b32_e64 v61, v8, v9, s[84:85]\n\t" \
  "v_permlane32_swap_b32 v4, v6\n\t" \
  "v_max_f32 v4, v4, v6\n\t" \
  "v_mul_f32 v6, 0xbe8293ee, v4\n\t" \
  "v_mul_f32 v4, 0xbe8293ee, v4\n\t" \
  "s_nop 1\n\t" \
  "v_permlane32_swap_b32 v4, v6\n\t" \
  "v_mov_b32 v5, v4\n\t" \
  "v_mov_b32 v7, v6\n\t" \
  "s_nop 0\n\t" \
  "v_permlane16_swap_b32 v4, v5\n\t" \
  "v_permlane16_swap_b32 v6, v7\n\t" \
  "v_fmamk_f32 v128, v128, 0x3e8293ee, v4\n\t" \
  "v_fmamk_f32 v130, v130, 0x3e8293ee, v4\n\t" \
  "v_fmamk_f32 v132, v132, 0x3e8293ee, v4\n\t" \
  "v_fmamk_f32 v134, v134, 0x3e8293ee, v4\n\t" \
  "v_fmamk_f32 v136, v136, 0x3e8293ee, v5\n\t" \
  "v_fmamk_f32 v138, v138, 0x3e8293ee, v5\n\t" \
  "v_fmamk_f32 v140, v140, 0x3e8293ee, v5\n\t" \
  "v_fmamk_f32 v142, v142, 0x3e8293ee, v5\n\t" \
  "v_fmamk_f32 v144, v144, 0x3e8293ee, v6\n\t" \
  "v_fmamk_f32 v146, v146, 0x3e8293ee, v6\n\t" \
  "v_fmamk_f32 v148, v148, 0x3e8293ee, v6\n\t" \
  "v_fmamk_f32 v150, v150, 0x3e8293ee, v6\n\t" \
  "v_fmamk_f32 v152, v152, 0x3e8293ee, v7\n\t" \
  "v_fmamk_f32 v154, v154, 0x3e8293ee, v7\n\t" \
  "v_fmamk_f32 v156, v156, 0x3e8293ee, v7\n\t" \
  "v_fmamk_f32 v158, v158, 0x3e8293ee, v7\n\t" \
  "v_exp_f32 v128, v128\n\t" \
  "v_exp_f32 v130, v130\n\t" \
  "v_exp_f32 v132, v132\n\t" \
  "v_exp_f32 v134, v134\n\t" \
  "v_exp_f32 v136, v136\n\t" \
  "v_exp_f32 v138, v138\n\t" \
  "v_exp_f32 v140, v140\n\t" \
  "v_exp_f32 v142, v142\n\t" \
  "v_exp_f32 v144, v144\n\t" \
  "v_exp_f32 v146, v146\n\t" \
  "v_exp_f32 v148, v148\n\t" \
  "v_exp_f32 v150, v150\n\t" \
  "v_exp_f32 v152, v152\n\t" \
  "v_exp_f32 v154, v154\n\t" \
  "v_exp_f32 v156, v156\n\t" \
  "v_exp_f32 v158, v158\n\t" \
  "v_add_f32 v4, v128, v130\n\t" \
  "v_add_f32 v8, v132, v134\n\t" \
  "v_add_f32 v5, v136, v138\n\t" \
  "v_add_f32 v9, v140, v142\n\t" \
  "v_add_f32 v6, v144, v146\n\t" \
  "v_add_f32 v10, v148, v150\n\t" \
  "v_add_f32 v7, v152, v154\n\t" \
  "v_add_f32 v11, v156, v158\n\t" \
  "v_add_f32 v4, v4, v8\n\t" \
  "v_add_f32 v5, v5, v9\n\t" \
  "v_add_f32 v6, v6, v10\n\t" \
  "v_add_f32 v7, v7, v11\n\t" \
  "v_permlane16_swap_b32 v4, v5\n\t" \
  "s_nop 0\n\t" \
  "v_permlane16_swap_b32 v6, v7\n\t" \
  "v_add_f32 v4, v4, v5\n\t" \
  "v_add_f32 v6, v6, v7\n\t" \
  "s_nop 1\n\t" \
  "v_permlane32_swap_b32 v4, v6\n\t" \
  "v_add_f32 v4, v4, v6\n\t" \
  "v_rcp_f32 v4, v4\n\t" \
  "s_nop 0\n\t" \
  "v_mov_b32 v6, v4\n\t" \
  "s_nop 1\n\t" \
  "v_permlane32_swap_b32 v4, v6\n\t" \
  "v_mov_b32 v5, v4\n\t" \
  "v_mov_b32 v7, v6\n\t" \
  "s_nop 0\n\t" \
  "v_permlane16_swap_b32 v4, v5\n\t" \
  "v_permlane16_swap_b32 v6, v7\n\t" \
  "v_mul_f32 v128, v128, v4\n\t" \
  "v_mul_f32 v130, v130, v4\n\t" \
  "v_mul_f32 v132, v132, v4\n\t" \
  "v_mul_f32 v134, v134, v4\n\t" \
  "v_cvt_pk_bf16_f32 v128, v128, v130\n\t" \
  "v_cvt_pk_bf16_f32 v132, v132, v134\n\t" \
  "v_perm_b32 v68, 0, v128, v60\n\t" \
  "v_perm_b32 v69, 0, v128, v61\n\t" \
  "v_perm_b32 v70, 0, v132, v60\n\t" \
  "v_perm_b32 v71, 0, v132, v61\n\t" \
  "v_mul_f32 v136, v136, v5\n\t" \
  "v_mul_f32 v138, v138, v5\n\t" \
  "v_mfma_f32_16x16x32_bf16 v[128:131], v[20:23], v[68:71], 0\n\t" \
  "v_mul_f32 v140, v140, v5\n\t" \
  "v_mul_f32 v142, v142, v5\n\t" \
  "v_cvt_pk_bf16_f32 v136, v136, v138\n\t" \
  "v_mfma_f32_16x16x32_bf16 v[132:135], v[30:33], v[68:71], 0\n\t" \
  "v_cvt_pk_bf16_f32 v140, v140, v142\n\t" \
  "v_perm_b32 v76, 0, v136, v60\n\t" \
  "v_perm_b32 v77, 0, v136, v61\n\t" \
  "v_perm_b32 v78, 0, v140, v60\n\t" \
  "v_perm_b32 v79, 0, v140, v61\n\t" \
  "v_mul_f32 v144, v144, v6\n\t" \
  "v_mul_f32 v146, v146, v6\n\t" \
  "v_mfma_f32_16x16x32_bf16 v[136:139], v[22:25], v[76:79], 0\n\t" \
  "v_mul_f32 v148, v148, v6\n\t" \
  "v_mul_f32 v150, v150, v6\n\t" \
  "v_cvt_pk_bf16_f32 v144, v144, v146\n\t" \
  "v_mfma_f32_16x16x32_bf16 v[140:143], v[32:35], v[76:79], 0\n\t" \
  "v_cvt_pk_bf16_f32 v148, v148, v150\n\t" \
  "v_perm_b32 v84, 0, v144, v60\n\t" \
  "v_perm_b32 v85, 0, v144, v61\n\t" \
  "v_perm_b32 v86, 0, v148, v60\n\t" \
  "v_perm_b32 v87, 0, v148, v61\n\t" \
  "v_cvt_pk_bf16_f32 v100, v128, v129\n\t" \
  "v_cvt_pk_bf16_f32 v101, v130, v131\n\t" \
  "v_mfma_f32_16x16x32_bf16 v[144:147], v[24:27], v[84:87], 0\n\t" \
  "v_cvt_pk_bf16_f32 v102, v132, v133\n\t" \
  "v_cvt_pk_bf16_f32 v103, v134, v135\n\t" \
  "global_store_dwordx4 v62, v[100:103], %[ot] offset:0 sc1\n\t" \
  "v_mfma_f32_16x16x32_bf16 v[148:151], v[34:37], v[84:87], 0\n\t" \
  "v_mul_f32 v152, v152, v7\n\t" \
  "v_mul_f32 v154, v154, v7\n\t" \
  "v_mul_f32 v156, v156, v7\n\t" \
  "v_mul_f32 v158, v158, v7\n\t" \
  "v_cvt_pk_bf16_f32 v152, v152, v154\n\t" \
  "v_cvt_pk_bf16_f32 v156, v156, v158\n\t" \
  "v_perm_b32 v92, 0, v152, v60\n\t" \
  "v_perm_b32 v93, 0, v152, v61\n\t" \
  "v_perm_b32 v94, 0, v156, v60\n\t" \
  "v_perm_b32 v95, 0, v156, v61\n\t" \
  "v_cvt_pk_bf16_f32 v108, v136, v137\n\t" \
  "v_cvt_pk_bf16_f32 v109, v138, v139\n\t" \
  "v_mfma_f32_16x16x32_bf16 v[152:155], v[26:29], v[92:95], 0\n\t" \
  "v_cvt_pk_bf16_f32 v110, v140, v141\n\t" \
  "v_cvt_pk_bf16_f32 v111, v142, v143\n\t" \
  "global_store_dwordx4 v62, v[108:111], %[ot] offset:1024 sc1\n\t" \
  "v_mfma_f32_16x16x32_bf16 v[156:159], v[36:39], v[92:95], 0\n\t" \
  "v_cvt_pk_bf16_f32 v116, v144, v145\n\t" \
  "v_cvt_pk_bf16_f32 v117, v146, v147\n\t" \
  "v_cvt_pk_bf16_f32 v118, v148, v149\n\t" \
  "v_cvt_pk_bf16_f32 v119, v150, v151\n\t" \
  "global_store_dwordx4 v62, v[116:119], %[ou] offset:0 sc1\n\t" \
  "v_cvt_pk_bf16_f32 v124, v152, v153\n\t" \
  "v_cvt_pk_bf16_f32 v125, v154, v155\n\t" \
  "s_nop 0\n\t" \
  "v_cvt_pk_bf16_f32 v126, v156, v157\n\t" \
  "v_cvt_pk_bf16_f32 v127, v158, v159\n\t" \
  "global_store_dwordx4 v62, v[124:127], %[ou] offset:1024 sc1\n\t" \
  "s_branch AE_%=\n\t" \
  "AR_%=:\n\t" \
  "s_addk_i32 %[it], 1\n\t" \
  "s_cmp_gt_u32 %[it], 0x400000\n\t" \
  "s_cbranch_scc1 AY_%=\n\t" \
  "ds_read_b32 v0, v61\n\t" \
  "ds_read_b64 v[20:21], v60\n\t" \
  "ds_read_b64 v[30:31], v60 offset:8\n\t" \
  "s_mov_b64 exec, %[rm]\n\t" \
  "ds_read_b128 v[160:163], v63\n\t" \
  "s_mov_b64 exec, -1\n\t" \
  "s_branch AW_%=\n\t" \
  "AZ_%=:\n\t" \
  "v_mov_b32 v0, 0xff61b1e6\n\t" \
  "v_mov_b32 v1, 0xcfa8965f\n\t" \
  "s_nop 3\n\t" \
  "v_cndmask_b32_e64 v56, v0, v1, s[68:69]\n\t" \
  "v_cndmask_b32_e64 v57, v0, v1, s[70:71]\n\t" \
  "v_cndmask_b32_e64 v58, v0, v1, s[72:73]\n\t" \
  "v_cndmask_b32_e64 v59, v0, v1, s[74:75]\n\t" \
  "s_nop 1\n\t" \
  "s_branch AY_%=\n\t" \
  "AE_%=:\n\t" \
  "s_nop 1\n\t"
#define N2_CLOB "v0", "v1", "v2", "v3", "v4", "v5", "v6", "v7", "v8", "v9", "v10", "v11", "v12", "v13", "v14", "v15", "v16", "v17", "v18", "v19", "v20", "v21", "v22", "v23", "v24", "v25", "v26", "v27", "v28", "v29", "v30", "v31", "v32", "v33", "v34", "v35", "v36", "v37", "v38", "v39", "v40", "v41", "v42", "v43", "v44", "v45", "v46", "v47", "v48", "v49", "v50", "v51", "v52", "v53", "v54", "v55", "v60", "v61", "v64", "v65", "v66", "v67", "v68", "v69", "v70", "v71", "v72", "v73", "v74", "v75", "v76", "v77", "v78", "v79", "v80", "v81", "v82", "v83", "v84", "v85", "v86", "v87", "v88", "v89", "v90", "v91", "v92", "v93", "v94", "v95", "v96", "v97", "v98", "v99", "v100", "v101", "v102", "v103", "v104", "v105", "v106", "v107", "v108", "v109", "v110", "v111", "v112", "v113", "v114", "v115", "v116", "v117", "v118", "v119", "v120", "v121", "v122", "v123", "v124", "v125", "v126", "v127", "v128", "v129", "v130", "v131", "v132", "v133", "v134", "v135", "v136", "v137", "v138", "v139", "v140", "v141", "v142", "v143", "v144", "v145", "v146", "v147", "v148", "v149", "v150", "v151", "v152", "v153", "v154", "v155", "v156", "v157", "v158", "v159", "v160", "v161", "v162", "v163", "v164", "v165", "v166", "v167", "s68", "s69", "s70", "s71", "s72", "s73", "s74", "s75", "s76", "s77", "s78", "s79", "s80", "s81", "s82", "s83", "s84", "s85"

#define CV_Y 1
#define CV_ASM \
  "s_mov_b64 vcc, 1\n\t" \
  "ds_read_b64 v[0:1], v28 offset:0\n\t" \
  "ds_read_b64 v[2:3], v29 offset:0\n\t" \
  "ds_read_b64 v[4:5], v30 offset:1024\n\t" \
  "ds_read_b64 v[6:7], v31 offset:1024\n\t" \
  "ds_read_b64 v[8:9], v24 offset:2048\n\t" \
  "ds_read_b64 v[10:11], v25 offset:2048\n\t" \
  "ds_read_b64 v[12:13], v26 offset:3072\n\t" \
  "ds_read_b64 v[14:15], v27 offset:3072\n\t" \
  "ds_read_b64 v[16:17], v28 offset:4096\n\t" \
  "ds_read_b64 v[18:19], v29 offset:4096\n\t" \
  "ds_read_b64 v[20:21], v30 offset:5120\n\t" \
  "ds_read_b64 v[22:23], v31 offset:5120\n\t" \
  "s_waitcnt lgkmcnt(0)\n\t" \
  "v_mfma_f32_16x16x32_bf16 v[128:131], v[68:71], v[0:3], 0\n\t" \
  "v_mfma_f32_16x16x32_bf16 v[128:131], v[64:67], v[4:7], v[128:131]\n\t" \
  "v_mfma_f32_16x16x32_bf16 v[132:135], v[76:79], v[0:3], 0\n\t" \
  "v_mfma_f32_16x16x32_bf16 v[132:135], v[72:75], v[4:7], v[132:135]\n\t" \
  "v_mfma_f32_16x16x32_bf16 v[136:139], v[84:87], v[0:3], 0\n\t" \
  "v_mfma_f32_16x16x32_bf16 v[136:139], v[80:83], v[4:7], v[136:139]\n\t" \
  "v_mfma_f32_16x16x32_bf16 v[140:143], v[92:95], v[0:3], 0\n\t" \
  "v_mfma_f32_16x16x32_bf16 v[140:143], v[88:91], v[4:7], v[140:143]\n\t" \
  "v_mfma_f32_16x16x32_bf16 v[144:147], v[100:103], v[0:3], 0\n\t" \
  "v_mfma_f32_16x16x32_bf16 v[144:147], v[96:99], v[4:7], v[144:147]\n\t" \
  "ds_bpermute_b32 v34, v32, v131\n\t" \
  "v_mfma_f32_16x16x32_bf16 v[148:151], v[108:111], v[0:3], 0\n\t" \
  "v_mov_b32 v131, v130\n\t" \
  "v_mov_b32 v130, v129\n\t" \
  "v_mfma_f32_16x16x32_bf16 v[148:151], v[104:107], v[4:7], v[148:151]\n\t" \
  "v_mov_b32 v129, v128\n\t" \
  "v_mfma_f32_16x16x32_bf16 v[152:155], v[116:119], v[0:3], 0\n\t" \
  "v_mfma_f32_16x16x32_bf16 v[152:155], v[112:115], v[4:7], v[152:155]\n\t" \
  "v_mfma_f32_16x16x32_bf16 v[156:159], v[124:127], v[0:3], 0\n\t" \
  "ds_read_b64 v[0:1], v24 offset:6144\n\t" \
  "ds_read_b64 v[2:3], v25 offset:6144\n\t" \
  "v_mfma_f32_16x16x32_bf16 v[156:159], v[120:123], v[4:7], v[156:159]\n\t" \
  "ds_read_b64 v[4:5], v26 offset:7168\n\t" \
  "ds_read_b64 v[6:7], v27 offset:7168\n\t" \
  "v_mfma_f32_16x16x32_bf16 v[132:135], v[68:71], v[8:11], v[132:135]\n\t" \
  "v_mfma_f32_16x16x32_bf16 v[132:135], v[64:67], v[12:15], v[132:135]\n\t" \
  "v_mfma_f32_16x16x32_bf16 v[136:139], v[76:79], v[8:11], v[136:139]\n\t" \
  "v_mfma_f32_16x16x32_bf16 v[136:139], v[72:75], v[12:15], v[136:139]\n\t" \
  "v_mfma_f32_16x16x32_bf16 v[140:143], v[84:87], v[8:11], v[140:143]\n\t" \
  "s_waitcnt lgkmcnt(4)\n\t" \
  "v_cndmask_b32_e64 v128, v34, 0, vcc\n\t" \
  "v_mfma_f32_16x16x32_bf16 v[140:143], v[80:83], v[12:15], v[140:143]\n\t" \
  "v_mfma_f32_16x16x32_bf16 v[144:147], v[92:95], v[8:11], v[144:147]\n\t" \
  "v_mfma_f32_16x16x32_bf16 v[144:147], v[88:91], v[12:15], v[144:147]\n\t" \
  "ds_bpermute_b32 v35, v32, v135\n\t" \
  "v_mfma_f32_16x16x32_bf16 v[148:151], v[100:103], v[8:11], v[148:151]\n\t" \
  "v_mov_b32 v135, v134\n\t" \
  "v_mov_b32 v134, v133\n\t" \
  "v_mfma_f32_16x16x32_bf16 v[148:151], v[96:99], v[12:15], v[148:151]\n\t" \
  "v_mov_b32 v133, v132\n\t" \
  "v_mfma_f32_16x16x32_bf16 v[152:155], v[108:111], v[8:11], v[152:155]\n\t" \
  "v_mfma_f32_16x16x32_bf16 v[152:155], v[104:107], v[12:15], v[152:155]\n\t" \
  "v_mfma_f32_16x16x32_bf16 v[156:159], v[116:119], v[8:11], v[156:159]\n\t" \
  "ds_read_b64 v[8:9], v28 offset:8192\n\t" \
  "ds_read_b64 v[10:11], v29 offset:8192\n\t" \
  "v_mfma_f32_16x16x32_bf16 v[156:159], v[112:115], v[12:15], v[156:159]\n\t" \
  "ds_read_b64 v[12:13], v30 offset:9216\n\t" \
  "ds_read_b64 v[14:15], v31 offset:9216\n\t" \
  "v_mfma_f32_16x16x32_bf16 v[136:139], v[68:71], v[16:19], v[136:139]\n\t" \
  "v_mfma_f32_16x16x32_bf16 v[136:139], v[64:67], v[20:23], v[136:139]\n\t" \
  "v_mfma_f32_16x16x32_bf16 v[140:143], v[76:79], v[16:19], v[140:143]\n\t" \
  "v_mfma_f32_16x16x32_bf16 v[140:143], v[72:75], v[20:23], v[140:143]\n\t" \
  "v_mfma_f32_16x16x32_bf16 v[144:147], v[84:87], v[16:19], v[144:147]\n\t" \
  "s_waitcnt lgkmcnt(4)\n\t" \
  "v_cndmask_b32_e64 v132, v35, v34, vcc\n\t" \
  "v_mfma_f32_16x16x32_bf16 v[144:147], v[80:83], v[20:23], v[144:147]\n\t" \
  "v_mfma_f32_16x16x32_bf16 v[148:151], v[92:95], v[16:19], v[148:151]\n\t" \
  "v_mfma_f32_16x16x32_bf16 v[148:151], v[88:91], v[20:23], v[148:151]\n\t" \
  "ds_bpermute_b32 v36, v32, v139\n\t" \
  "v_mfma_f32_16x16x32_bf16 v[152:155], v[100:103], v[16:19], v[152:155]\n\t" \
  "v_mov_b32 v139, v138\n\t" \
  "v_mov_b32 v138, v137\n\t" \
  "v_mfma_f32_16x16x32_bf16 v[152:155], v[96:99], v[20:23], v[152:155]\n\t" \
  "v_mov_b32 v137, v136\n\t" \
  "v_mfma_f32_16x16x32_bf16 v[156:159], v[108:111], v[16:19], v[156:159]\n\t" \
  "ds_read_b64 v[16:17], v24 offset:10240\n\t" \
  "ds_read_b64 v[18:19], v25 offset:10240\n\t" \
  "v_mfma_f32_16x16x32_bf16 v[156:159], v[104:107], v[20:23], v[156:159]\n\t" \
  "ds_read_b64 v[20:21], v26 offset:11264\n\t" \
  "ds_read_b64 v[22:23], v27 offset:11264\n\t" \
  "v_mfma_f32_16x16x32_bf16 v[140:143], v[68:71], v[0:3], v[140:143]\n\t" \
  "v_mfma_f32_16x16x32_bf16 v[140:143], v[64:67], v[4:7], v[140:143]\n\t" \
  "v_mfma_f32_16x16x32_bf16 v[144:147], v[76:79], v[0:3], v[144:147]\n\t" \
  "v_mfma_f32_16x16x32_bf16 v[144:147], v[72:75], v[4:7], v[144:147]\n\t" \
  "v_mfma_f32_16x16x32_bf16 v[148:151], v[84:87], v[0:3], v[148:151]\n\t" \
  "v_mfma_f32_16x16x32_bf16 v[148:151], v[80:83], v[4:7], v[148:151]\n\t" \
  "v_mfma_f32_16x16x32_bf16 v[152:155], v[92:95], v[0:3], v[152:155]\n\t" \
  "s_waitcnt lgkmcnt(4)\n\t" \
  "v_cndmask_b32_e64 v136, v36, v35, vcc\n\t" \
  "v_mfma_f32_16x16x32_bf16 v[152:155], v[88:91], v[4:7], v[152:155]\n\t" \
  "ds_bpermute_b32 v37, v32, v143\n\t" \
  "v_mfma_f32_16x16x32_bf16 v[156:159], v[100:103], v[0:3], v[156:159]\n\t" \
  "ds_read_b64 v[0:1], v28 offset:12288\n\t" \
  "ds_read_b64 v[2:3], v29 offset:12288\n\t" \
  "v_mfma_f32_16x16x32_bf16 v[156:159], v[96:99], v[4:7], v[156:159]\n\t" \
  "ds_read_b64 v[4:5], v30 offset:13312\n\t" \
  "ds_read_b64 v[6:7], v31 offset:13312\n\t" \
  "v_mfma_f32_16x16x32_bf16 v[144:147], v[68:71], v[8:11], v[144:147]\n\t" \
  "v_mov_b32 v143, v142\n\t" \
  "v_mov_b32 v142, v141\n\t" \
  "v_mfma_f32_16x16x32_bf16 v[144:147], v[64:67], v[12:15], v[144:147]\n\t" \
  "v_mov_b32 v141, v140\n\t" \
  "v_mfma_f32_16x16x32_bf16 v[148:151], v[76:79], v[8:11], v[148:151]\n\t" \
  "v_mfma_f32_16x16x32_bf16 v[148:151], v[72:75], v[12:15], v[148:151]\n\t" \
  "v_mfma_f32_16x16x32_bf16 v[152:155], v[84:87], v[8:11], v[152:155]\n\t" \
  "v_mfma_f32_16x16x32_bf16 v[152:155], v[80:83], v[12:15], v[152:155]\n\t" \
  "v_mfma_f32_16x16x32_bf16 v[156:159], v[92:95], v[8:11], v[156:159]\n\t" \
  "ds_read_b64 v[8:9], v24 offset:14336\n\t" \
  "ds_read_b64 v[10:11], v25 offset:14336\n\t" \
  "v_mfma_f32_16x16x32_bf16 v[156:159], v[88:91], v[12:15], v[156:159]\n\t" \
  "ds_read_b64 v[12:13], v26 offset:15360\n\t" \
  "s_waitcnt lgkmcnt(8)\n\t" \
  "v_mfma_f32_16x16x32_bf16 v[148:151], v[68:71], v[16:19], v[148:151]\n\t" \
  "ds_read_b64 v[14:15], v27 offset:15360\n\t" \
  "ds_bpermute_b32 v38, v32, v147\n\t" \
  "v_mfma_f32_16x16x32_bf16 v[148:151], v[64:67], v[20:23], v[148:151]\n\t" \
  "v_mov_b32 v147, v146\n\t" \
  "v_mov_b32 v146, v145\n\t" \
  "v_mfma_f32_16x16x32_bf16 v[152:155], v[76:79], v[16:19], v[152:155]\n\t" \
  "s_waitcnt lgkmcnt(9)\n\t" \
  "v_cndmask_b32_e64 v140, v37, v36, vcc\n\t" \
  "v_mfma_f32_16x16x32_bf16 v[152:155], v[72:75], v[20:23], v[152:155]\n\t" \
  "v_mov_b32 v145, v144\n\t" \
  "v_mfma_f32_16x16x32_bf16 v[156:159], v[84:87], v[16:19], v[156:159]\n\t" \
  "ds_read_b64 v[16:17], v26 offset:0\n\t" \
  "ds_read_b64 v[18:19], v27 offset:0\n\t" \
  "v_mfma_f32_16x16x32_bf16 v[156:159], v[80:83], v[20:23], v[156:159]\n\t" \
  "ds_read_b64 v[20:21], v28 offset:1024\n\t" \
  "ds_read_b64 v[22:23], v29 offset:1024\n\t" \
  "s_waitcnt lgkmcnt(9)\n\t" \
  "v_mfma_f32_16x16x32_bf16 v[152:155], v[68:71], v[0:3], v[152:155]\n\t" \
  "ds_bpermute_b32 v39, v32, v151\n\t" \
  "v_mfma_f32_16x16x32_bf16 v[152:155], v[64:67], v[4:7], v[152:155]\n\t" \
  "v_mov_b32 v151, v150\n\t" \
  "v_mov_b32 v150, v149\n\t" \
  "v_mfma_f32_16x16x32_bf16 v[156:159], v[76:79], v[0:3], v[156:159]\n\t" \
  "ds_read_b64 v[0:1], v30 offset:2048\n\t" \
  "ds_read_b64 v[2:3], v31 offset:2048\n\t" \
  "v_mfma_f32_16x16x32_bf16 v[156:159], v[72:75], v[4:7], v[156:159]\n\t" \
  "ds_read_b64 v[4:5], v24 offset:3072\n\t" \
  "s_waitcnt lgkmcnt(11)\n\t" \
  "v_mfma_f32_16x16x32_bf16 v[156:159], v[68:71], v[8:11], v[156:159]\n\t" \
  "ds_bpermute_b32 v40, v32, v155\n\t" \
  "s_waitcnt lgkmcnt(10)\n\t" \
  "v_mfma_f32_16x16x32_bf16 v[156:159], v[64:67], v[12:15], v[156:159]\n\t" \
  "ds_read_b64 v[6:7], v25 offset:3072\n\t" \
  "s_waitcnt lgkmcnt(8)\n\t" \
  "v_mfma_f32_16x16x32_bf16 v[128:131], v[68:71], v[16:19], v[128:131]\n\t" \
  "v_cndmask_b32_e64 v144, v38, v37, vcc\n\t" \
  "s_waitcnt lgkmcnt(6)\n\t" \
  "v_mfma_f32_16x16x32_bf16 v[128:131], v[64:67], v[20:23], v[128:131]\n\t" \
  "v_mov_b32 v149, v148\n\t" \
  "v_mov_b32 v155, v154\n\t" \
  "v_mfma_f32_16x16x32_bf16 v[132:135], v[76:79], v[16:19], v[132:135]\n\t" \
  "ds_bpermute_b32 v41, v32, v159\n\t" \
  "v_mov_b32 v154, v153\n\t" \
  "v_mfma_f32_16x16x32_bf16 v[132:135], v[72:75], v[20:23], v[132:135]\n\t" \
  "v_mov_b32 v153, v152\n\t" \
  "v_mov_b32 v159, v158\n\t" \
  "v_mfma_f32_16x16x32_bf16 v[136:139], v[84:87], v[16:19], v[136:139]\n\t" \
  "v_mov_b32 v158, v157\n\t" \
  "v_mov_b32 v157, v156\n\t" \
  "v_mfma_f32_16x16x32_bf16 v[136:139], v[80:83], v[20:23], v[136:139]\n\t" \
  "s_waitcnt lgkmcnt(6)\n\t" \
  "v_cndmask_b32_e64 v148, v39, v38, vcc\n\t" \
  "v_mfma_f32_16x16x32_bf16 v[140:143], v[92:95], v[16:19], v[140:143]\n\t" \
  "global_load_dwordx4 v[44:47], v43, %[q0] offset:0\n\t" \
  "v_mfma_f32_16x16x32_bf16 v[140:143], v[88:91], v[20:23], v[140:143]\n\t" \
  "ds_read_b64 v[8:9], v26 offset:4096\n\t" \
  "ds_read_b64 v[10:11], v27 offset:4096\n\t" \
  "v_mfma_f32_16x16x32_bf16 v[144:147], v[100:103], v[16:19], v[144:147]\n\t" \
  "s_waitcnt lgkmcnt(4)\n\t" \
  "v_cndmask_b32_e64 v152, v40, v39, vcc\n\t" \
  "v_mfma_f32_16x16x32_bf16 v[144:147], v[96:99], v[20:23], v[144:147]\n\t" \
  "ds_read_b64 v[12:13], v28 offset:5120\n\t" \
  "ds_read_b64 v[14:15], v29 offset:5120\n\t" \
  "v_mfma_f32_16x16x32_bf16 v[148:151], v[108:111], v[16:19], v[148:151]\n\t" \
  "s_waitcnt lgkmcnt(4)\n\t" \
  "v_cndmask_b32_e64 v156, v41, v40, vcc\n\t" \
  "v_mfma_f32_16x16x32_bf16 v[148:151], v[104:107], v[20:23], v[148:151]\n\t" \
  "global_load_dwordx4 v[48:51], v43, %[q0] offset:2048\n\t" \
  "v_mfma_f32_16x16x32_bf16 v[152:155], v[116:119], v[16:19], v[152:155]\n\t" \
  "ds_bpermute_b32 v34, v32, v131\n\t" \
  "v_mfma_f32_16x16x32_bf16 v[152:155], v[112:115], v[20:23], v[152:155]\n\t" \
  "v_mov_b32 v131, v130\n\t" \
  "v_mov_b32 v130, v129\n\t" \
  "v_mfma_f32_16x16x32_bf16 v[156:159], v[124:127], v[16:19], v[156:159]\n\t" \
  "global_load_dwordx4 v[52:55], v43, %[q2] offset:0\n\t" \
  "v_mfma_f32_16x16x32_bf16 v[156:159], v[120:123], v[20:23], v[156:159]\n\t" \
  "ds_read_b64 v[16:17], v30 offset:6144\n\t" \
  "ds_read_b64 v[18:19], v31 offset:6144\n\t" \
  "v_mfma_f32_16x16x32_bf16 v[132:135], v[68:71], v[0:3], v[132:135]\n\t" \
  "ds_read_b64 v[20:21], v24 offset:7168\n\t" \
  "ds_read_b64 v[22:23], v25 offset:7168\n\t" \
  "v_mfma_f32_16x16x32_bf16 v[132:135], v[64:67], v[4:7], v[132:135]\n\t" \
  "v_mov_b32 v129, v128\n\t" \
  "v_mfma_f32_16x16x32_bf16 v[136:139], v[76:79], v[0:3], v[136:139]\n\t" \
  "global_load_dwordx4 v[56:59], v43, %[q2] offset:2048\n\t" \
  "v_mfma_f32_16x16x32_bf16 v[136:139], v[72:75], v[4:7], v[136:139]\n\t" \
  "v_mfma_f32_16x16x32_bf16 v[140:143], v[84:87], v[0:3], v[140:143]\n\t" \
  "v_mfma_f32_16x16x32_bf16 v[140:143], v[80:83], v[4:7], v[140:143]\n\t" \
  "v_mfma_f32_16x16x32_bf16 v[144:147], v[92:95], v[0:3], v[144:147]\n\t" \
  "v_mfma_f32_16x16x32_bf16 v[144:147], v[88:91], v[4:7], v[144:147]\n\t" \
  "ds_bpermute_b32 v35, v32, v135\n\t" \
  "v_mfma_f32_16x16x32_bf16 v[148:151], v[100:103], v[0:3], v[148:151]\n\t" \
  "v_mov_b32 v135, v134\n\t" \
  "v_mov_b32 v134, v133\n\t" \
  "v_mfma_f32_16x16x32_bf16 v[148:151], v[96:99], v[4:7], v[148:151]\n\t" \
  "s_waitcnt lgkmcnt(5)\n\t" \
  "v_cndmask_b32_e64 v128, v34, 0, vcc\n\t" \
  "v_mfma_f32_16x16x32_bf16 v[152:155], v[108:111], v[0:3], v[152:155]\n\t" \
  "v_mov_b32 v133, v132\n\t" \
  "v_mfma_f32_16x16x32_bf16 v[152:155], v[104:107], v[4:7], v[152:155]\n\t" \
  "v_mfma_f32_16x16x32_bf16 v[156:159], v[116:119], v[0:3], v[156:159]\n\t" \
  "ds_read_b64 v[0:1], v26 offset:8192\n\t" \
  "ds_read_b64 v[2:3], v27 offset:8192\n\t" \
  "v_mfma_f32_16x16x32_bf16 v[156:159], v[112:115], v[4:7], v[156:159]\n\t" \
  "ds_read_b64 v[4:5], v28 offset:9216\n\t" \
  "ds_read_b64 v[6:7], v29 offset:9216\n\t" \
  "v_mfma_f32_16x16x32_bf16 v[136:139], v[68:71], v[8:11], v[136:139]\n\t" \
  "v_mfma_f32_16x16x32_bf16 v[136:139], v[64:67], v[12:15], v[136:139]\n\t" \
  "v_mfma_f32_16x16x32_bf16 v[140:143], v[76:79], v[8:11], v[140:143]\n\t" \
  "v_mfma_f32_16x16x32_bf16 v[140:143], v[72:75], v[12:15], v[140:143]\n\t" \
  "v_mfma_f32_16x16x32_bf16 v[144:147], v[84:87], v[8:11], v[144:147]\n\t" \
  "s_waitcnt lgkmcnt(4)\n\t" \
  "v_cndmask_b32_e64 v132, v35, v34, vcc\n\t" \
  "v_mfma_f32_16x16x32_bf16 v[144:147], v[80:83], v[12:15], v[144:147]\n\t" \
  "v_mfma_f32_16x16x32_bf16 v[148:151], v[92:95], v[8:11], v[148:151]\n\t" \
  "v_mfma_f32_16x16x32_bf16 v[148:151], v[88:91], v[12:15], v[148:151]\n\t" \
  "ds_bpermute_b32 v36, v32, v139\n\t" \
  "v_mfma_f32_16x16x32_bf16 v[152:155], v[100:103], v[8:11], v[152:155]\n\t" \
  "v_mov_b32 v139, v138\n\t" \
  "v_mov_b32 v138, v137\n\t" \
  "v_mfma_f32_16x16x32_bf16 v[152:155], v[96:99], v[12:15], v[152:155]\n\t" \
  "v_mov_b32 v137, v136\n\t" \
  "v_mfma_f32_16x16x32_bf16 v[156:159], v[108:111], v[8:11], v[156:159]\n\t" \
  "ds_read_b64 v[8:9], v30 offset:10240\n\t" \
  "ds_read_b64 v[10:11], v31 offset:10240\n\t" \
  "v_mfma_f32_16x16x32_bf16 v[156:159], v[104:107], v[12:15], v[156:159]\n\t" \
  "ds_read_b64 v[12:13], v24 offset:11264\n\t" \
  "ds_read_b64 v[14:15], v25 offset:11264\n\t" \
  "v_mfma_f32_16x16x32_bf16 v[140:143], v[68:71], v[16:19], v[140:143]\n\t" \
  "v_mfma_f32_16x16x32_bf16 v[140:143], v[64:67], v[20:23], v[140:143]\n\t" \
  "v_mfma_f32_16x16x32_bf16 v[144:147], v[76:79], v[16:19], v[144:147]\n\t" \
  "v_mfma_f32_16x16x32_bf16 v[144:147], v[72:75], v[20:23], v[144:147]\n\t" \
  "v_mfma_f32_16x16x32_bf16 v[148:151], v[84:87], v[16:19], v[148:151]\n\t" \
  "v_mfma_f32_16x16x32_bf16 v[148:151], v[80:83], v[20:23], v[148:151]\n\t" \
  "v_mfma_f32_16x16x32_bf16 v[152:155], v[92:95], v[16:19], v[152:155]\n\t" \
  "s_waitcnt lgkmcnt(4)\n\t" \
  "v_cndmask_b32_e64 v136, v36, v35, vcc\n\t" \
  "v_mfma_f32_16x16x32_bf16 v[152:155], v[88:91], v[20:23], v[152:155]\n\t" \
  "ds_bpermute_b32 v37, v32, v143\n\t" \
  "v_mfma_f32_16x16x32_bf16 v[156:159], v[100:103], v[16:19], v[156:159]\n\t" \
  "ds_read_b64 v[16:17], v26 offset:12288\n\t" \
  "ds_read_b64 v[18:19], v27 offset:12288\n\t" \
  "v_mfma_f32_16x16x32_bf16 v[156:159], v[96:99], v[20:23], v[156:159]\n\t" \
  "ds_read_b64 v[20:21], v28 offset:13312\n\t" \
  "ds_read_b64 v[22:23], v29 offset:13312\n\t" \
  "v_mfma_f32_16x16x32_bf16 v[144:147], v[68:71], v[0:3], v[144:147]\n\t" \
  "v_mov_b32 v143, v142\n\t" \
  "v_mov_b32 v142, v141\n\t" \
  "v_mfma_f32_16x16x32_bf16 v[144:147], v[64:67], v[4:7], v[144:147]\n\t" \
  "v_mov_b32 v141, v140\n\t" \
  "v_mfma_f32_16x16x32_bf16 v[148:151], v[76:79], v[0:3], v[148:151]\n\t" \
  "v_mfma_f32_16x16x32_bf16 v[148:151], v[72:75], v[4:7], v[148:151]\n\t" \
  "v_mfma_f32_16x16x32_bf16 v[152:155], v[84:87], v[0:3], v[152:155]\n\t" \
  "v_mfma_f32_16x16x32_bf16 v[152:155], v[80:83], v[4:7], v[152:155]\n\t" \
  "v_mfma_f32_16x16x32_bf16 v[156:159], v[92:95], v[0:3], v[156:159]\n\t" \
  "ds_read_b64 v[0:1], v30 offset:14336\n\t" \
  "ds_read_b64 v[2:3], v31 offset:14336\n\t" \
  "v_mfma_f32_16x16x32_bf16 v[156:159], v[88:91], v[4:7], v[156:159]\n\t" \
  "ds_read_b64 v[4:5], v24 offset:15360\n\t" \
  "s_waitcnt lgkmcnt(8)\n\t" \
  "v_mfma_f32_16x16x32_bf16 v[148:151], v[68:71], v[8:11], v[148:151]\n\t" \
  "ds_read_b64 v[6:7], v25 offset:15360\n\t" \
  "ds_bpermute_b32 v38, v32, v147\n\t" \
  "v_mfma_f32_16x16x32_bf16 v[148:151], v[64:67], v[12:15], v[148:151]\n\t" \
  "v_mov_b32 v147, v146\n\t" \
  "v_mov_b32 v146, v145\n\t" \
  "v_mfma_f32_16x16x32_bf16 v[152:155], v[76:79], v[8:11], v[152:155]\n\t" \
  "s_waitcnt lgkmcnt(9)\n\t" \
  "v_cndmask_b32_e64 v140, v37, v36, vcc\n\t" \
  "v_mfma_f32_16x16x32_bf16 v[152:155], v[72:75], v[12:15], v[152:155]\n\t" \
  "v_mov_b32 v145, v144\n\t" \
  "v_mfma_f32_16x16x32_bf16 v[156:159], v[84:87], v[8:11], v[156:159]\n\t" \
  "ds_read_b64 v[8:9], v24 offset:0\n\t" \
  "ds_read_b64 v[10:11], v25 offset:0\n\t" \
  "v_mfma_f32_16x16x32_bf16 v[156:159], v[80:83], v[12:15], v[156:159]\n\t" \
  "ds_read_b64 v[12:13], v26 offset:1024\n\t" \
  "ds_read_b64 v[14:15], v27 offset:1024\n\t" \
  "s_waitcnt lgkmcnt(9)\n\t" \
  "v_mfma_f32_16x16x32_bf16 v[152:155], v[68:71], v[16:19], v[152:155]\n\t" \
  "ds_bpermute_b32 v39, v32, v151\n\t" \
  "v_mfma_f32_16x16x32_bf16 v[152:155], v[64:67], v[20:23], v[152:155]\n\t" \
  "v_mov_b32 v151, v150\n\t" \
  "v_mov_b32 v150, v149\n\t" \
  "v_mfma_f32_16x16x32_bf16 v[156:159], v[76:79], v[16:19], v[156:159]\n\t" \
  "ds_read_b64 v[16:17], v28 offset:2048\n\t" \
  "ds_read_b64 v[18:19], v29 offset:2048\n\t" \
  "v_mfma_f32_16x16x32_bf16 v[156:159], v[72:75], v[20:23], v[156:159]\n\t" \
  "ds_read_b64 v[20:21], v30 offset:3072\n\t" \
  "s_waitcnt lgkmcnt(11)\n\t" \
  "v_mfma_f32_16x16x32_bf16 v[156:159], v[68:71], v[0:3], v[156:159]\n\t" \
  "ds_bpermute_b32 v40, v32, v155\n\t" \
  "s_waitcnt lgkmcnt(10)\n\t" \
  "v_mfma_f32_16x16x32_bf16 v[156:159], v[64:67], v[4:7], v[156:159]\n\t" \
  "ds_read_b64 v[22:23], v31 offset:3072\n\t" \
  "s_waitcnt lgkmcnt(8)\n\t" \
  "v_mfma_f32_16x16x32_bf16 v[128:131], v[68:71], v[8:11], v[128:131]\n\t" \
  "v_cndmask_b32_e64 v144, v38, v37, vcc\n\t" \
  "s_waitcnt lgkmcnt(6)\n\t" \
  "v_mfma_f32_16x16x32_bf16 v[128:131], v[64:67], v[12:15], v[128:131]\n\t" \
  "v_mov_b32 v149, v148\n\t" \
  "v_mov_b32 v155, v154\n\t" \
  "v_mfma_f32_16x16x32_bf16 v[132:135], v[76:79], v[8:11], v[132:135]\n\t" \
  "ds_bpermute_b32 v41, v32, v159\n\t" \
  "v_mov_b32 v154, v153\n\t" \
  "v_mfma_f32_16x16x32_bf16 v[132:135], v[72:75], v[12:15], v[132:135]\n\t" \
  "v_mov_b32 v153, v152\n\t" \
  "v_mov_b32 v159, v158\n\t" \
  "v_mfma_f32_16x16x32_bf16 v[136:139], v[84:87], v[8:11], v[136:139]\n\t" \
  "v_mov_b32 v158, v157\n\t" \
  "v_mov_b32 v157, v156\n\t" \
  "v_mfma_f32_16x16x32_bf16 v[136:139], v[80:83], v[12:15], v[136:139]\n\t" \
  "s_waitcnt lgkmcnt(6)\n\t" \
  "v_cndmask_b32_e64 v148, v39, v38, vcc\n\t" \
  "v_mfma_f32_16x16x32_bf16 v[140:143], v[92:95], v[8:11], v[140:143]\n\t" \
  "ds_read_b64 v[0:1], v24 offset:4096\n\t" \
  "ds_read_b64 v[2:3], v25 offset:4096\n\t" \
  "v_mfma_f32_16x16x32_bf16 v[140:143], v[88:91], v[12:15], v[140:143]\n\t" \
  "ds_read_b64 v[4:5], v26 offset:5120\n\t" \
  "ds_read_b64 v[6:7], v27 offset:5120\n\t" \
  "v_mfma_f32_16x16x32_bf16 v[144:147], v[100:103], v[8:11], v[144:147]\n\t" \
  "s_waitcnt lgkmcnt(6)\n\t" \
  "v_cndmask_b32_e64 v152, v40, v39, vcc\n\t" \
  "v_mfma_f32_16x16x32_bf16 v[144:147], v[96:99], v[12:15], v[144:147]\n\t" \
  "ds_bpermute_b32 v34, v32, v131\n\t" \
  "v_mfma_f32_16x16x32_bf16 v[148:151], v[108:111], v[8:11], v[148:151]\n\t" \
  "s_waitcnt lgkmcnt(5)\n\t" \
  "v_cndmask_b32_e64 v156, v41, v40, vcc\n\t" \
  "v_mfma_f32_16x16x32_bf16 v[148:151], v[104:107], v[12:15], v[148:151]\n\t" \
  "v_mov_b32 v131, v130\n\t" \
  "v_mov_b32 v130, v129\n\t" \
  "v_mfma_f32_16x16x32_bf16 v[152:155], v[116:119], v[8:11], v[152:155]\n\t" \
  "v_mov_b32 v129, v128\n\t" \
  "v_mfma_f32_16x16x32_bf16 v[152:155], v[112:115], v[12:15], v[152:155]\n\t" \
  "v_mfma_f32_16x16x32_bf16 v[156:159], v[124:127], v[8:11], v[156:159]\n\t" \
  "ds_read_b64 v[8:9], v28 offset:6144\n\t" \
  "ds_read_b64 v[10:11], v29 offset:6144\n\t" \
  "v_mfma_f32_16x16x32_bf16 v[156:159], v[120:123], v[12:15], v[156:159]\n\t" \
  "ds_read_b64 v[12:13], v30 offset:7168\n\t" \
  "ds_read_b64 v[14:15], v31 offset:7168\n\t" \
  "v_mfma_f32_16x16x32_bf16 v[132:135], v[68:71], v[16:19], v[132:135]\n\t" \
  "v_mfma_f32_16x16x32_bf16 v[132:135], v[64:67], v[20:23], v[132:135]\n\t" \
  "v_mfma_f32_16x16x32_bf16 v[136:139], v[76:79], v[16:19], v[136:139]\n\t" \
  "v_mfma_f32_16x16x32_bf16 v[136:139], v[72:75], v[20:23], v[136:139]\n\t" \
  "v_mfma_f32_16x16x32_bf16 v[140:143], v[84:87], v[16:19], v[140:143]\n\t" \
  "s_waitcnt lgkmcnt(4)\n\t" \
  "v_cndmask_b32_e64 v128, v34, 0, vcc\n\t" \
  "v_mfma_f32_16x16x32_bf16 v[140:143], v[80:83], v[20:23], v[140:143]\n\t" \
  "v_mfma_f32_16x16x32_bf16 v[144:147], v[92:95], v[16:19], v[144:147]\n\t" \
  "v_mfma_f32_16x16x32_bf16 v[144:147], v[88:91], v[20:23], v[144:147]\n\t" \
  "ds_bpermute_b32 v35, v32, v135\n\t" \
  "v_mfma_f32_16x16x32_bf16 v[148:151], v[100:103], v[16:19], v[148:151]\n\t" \
  "v_mov_b32 v135, v134\n\t" \
  "v_mov_b32 v134, v133\n\t" \
  "v_mfma_f32_16x16x32_bf16 v[148:151], v[96:99], v[20:23], v[148:151]\n\t" \
  "v_mov_b32 v133, v132\n\t" \
  "v_mfma_f32_16x16x32_bf16 v[152:155], v[108:111], v[16:19], v[152:155]\n\t" \
  "v_mfma_f32_16x16x32_bf16 v[152:155], v[104:107], v[20:23], v[152:155]\n\t" \
  "v_mfma_f32_16x16x32_bf16 v[156:159], v[116:119], v[16:19], v[156:159]\n\t" \
  "ds_read_b64 v[16:17], v24 offset:8192\n\t" \
  "ds_read_b64 v[18:19], v25 offset:8192\n\t" \
  "v_mfma_f32_16x16x32_bf16 v[156:159], v[112:115], v[20:23], v[156:159]\n\t" \
  "ds_read_b64 v[20:21], v26 offset:9216\n\t" \
  "ds_read_b64 v[22:23], v27 offset:9216\n\t" \
  "v_mfma_f32_16x16x32_bf16 v[136:139], v[68:71], v[0:3], v[136:139]\n\t" \
  "v_mfma_f32_16x16x32_bf16 v[136:139], v[64:67], v[4:7], v[136:139]\n\t" \
  "v_mfma_f32_16x16x32_bf16 v[140:143], v[76:79], v[0:3], v[140:143]\n\t" \
  "v_mfma_f32_16x16x32_bf16 v[140:143], v[72:75], v[4:7], v[140:143]\n\t" \
  "v_mfma_f32_16x16x32_bf16 v[144:147], v[84:87], v[0:3], v[144:147]\n\t" \
  "s_waitcnt lgkmcnt(4)\n\t" \
  "v_cndmask_b32_e64 v132, v35, v34, vcc\n\t" \
  "v_mfma_f32_16x16x32_bf16 v[144:147], v[80:83], v[4:7], v[144:147]\n\t" \
  "v_mfma_f32_16x16x32_bf16 v[148:151], v[92:95], v[0:3], v[148:151]\n\t" \
  "v_mfma_f32_16x16x32_bf16 v[148:151], v[88:91], v[4:7], v[148:151]\n\t" \
  "ds_bpermute_b32 v36, v32, v139\n\t" \
  "v_mfma_f32_16x16x32_bf16 v[152:155], v[100:103], v[0:3], v[152:155]\n\t" \
  "v_mov_b32 v139, v138\n\t" \
  "v_mov_b32 v138, v137\n\t" \
  "v_mfma_f32_16x16x32_bf16 v[152:155], v[96:99], v[4:7], v[152:155]\n\t" \
  "v_mov_b32 v137, v136\n\t" \
  "v_mfma_f32_16x16x32_bf16 v[156:159], v[108:111], v[0:3], v[156:159]\n\t" \
  "ds_read_b64 v[0:1], v28 offset:10240\n\t" \
  "ds_read_b64 v[2:3], v29 offset:10240\n\t" \
  "v_mfma_f32_16x16x32_bf16 v[156:159], v[104:107], v[4:7], v[156:159]\n\t" \
  "ds_read_b64 v[4:5], v30 offset:11264\n\t" \
  "ds_read_b64 v[6:7], v31 offset:11264\n\t" \
  "v_mfma_f32_16x16x32_bf16 v[140:143], v[68:71], v[8:11], v[140:143]\n\t" \
  "v_mfma_f32_16x16x32_bf16 v[140:143], v[64:67], v[12:15], v[140:143]\n\t" \
  "v_mfma_f32_16x16x32_bf16 v[144:147], v[76:79], v[8:11], v[144:147]\n\t" \
  "v_mfma_f32_16x16x32_bf16 v[144:147], v[72:75], v[12:15], v[144:147]\n\t" \
  "v_mfma_f32_16x16x32_bf16 v[148:151], v[84:87], v[8:11], v[148:151]\n\t" \
  "v_mfma_f32_16x16x32_bf16 v[148:151], v[80:83], v[12:15], v[148:151]\n\t" \
  "v_mfma_f32_16x16x32_bf16 v[152:155], v[92:95], v[8:11], v[152:155]\n\t" \
  "s_waitcnt lgkmcnt(4)\n\t" \
  "v_cndmask_b32_e64 v136, v36, v35, vcc\n\t" \
  "v_mfma_f32_16x16x32_bf16 v[152:155], v[88:91], v[12:15], v[152:155]\n\t" \
  "ds_bpermute_b32 v37, v32, v143\n\t" \
  "v_mfma_f32_16x16x32_bf16 v[156:159], v[100:103], v[8:11], v[156:159]\n\t" \
  "ds_read_b64 v[8:9], v24 offset:12288\n\t" \
  "ds_read_b64 v[10:11], v25 offset:12288\n\t" \
  "v_mfma_f32_16x16x32_bf16 v[156:159], v[96:99], v[12:15], v[156:159]\n\t" \
  "ds_read_b64 v[12:13], v26 offset:13312\n\t" \
  "ds_read_b64 v[14:15], v27 offset:13312\n\t" \
  "v_mfma_f32_16x16x32_bf16 v[144:147], v[68:71], v[16:19], v[144:147]\n\t" \
  "v_mov_b32 v143, v142\n\t" \
  "v_mov_b32 v142, v141\n\t" \
  "v_mfma_f32_16x16x32_bf16 v[144:147], v[64:67], v[20:23], v[144:147]\n\t" \
  "v_mov_b32 v141, v140\n\t" \
  "v_mfma_f32_16x16x32_bf16 v[148:151], v[76:79], v[16:19], v[148:151]\n\t" \
  "v_mfma_f32_16x16x32_bf16 v[148:151], v[72:75], v[20:23], v[148:151]\n\t" \
  "v_mfma_f32_16x16x32_bf16 v[152:155], v[84:87], v[16:19], v[152:155]\n\t" \
  "v_mfma_f32_16x16x32_bf16 v[152:155], v[80:83], v[20:23], v[152:155]\n\t" \
  "v_mfma_f32_16x16x32_bf16 v[156:159], v[92:95], v[16:19], v[156:159]\n\t" \
  "ds_read_b64 v[16:17], v28 offset:14336\n\t" \
  "ds_read_b64 v[18:19], v29 offset:14336\n\t" \
  "v_mfma_f32_16x16x32_bf16 v[156:159], v[88:91], v[20:23], v[156:159]\n\t" \
  "ds_read_b64 v[20:21], v30 offset:15360\n\t" \
  "s_waitcnt lgkmcnt(8)\n\t" \
  "v_mfma_f32_16x16x32_bf16 v[148:151], v[68:71], v[0:3], v[148:151]\n\t" \
  "ds_read_b64 v[22:23], v31 offset:15360\n\t" \
  "ds_bpermute_b32 v38, v32, v147\n\t" \
  "v_mfma_f32_16x16x32_bf16 v[148:151], v[64:67], v[4:7], v[148:151]\n\t" \
  "v_mov_b32 v147, v146\n\t" \
  "v_mov_b32 v146, v145\n\t" \
  "v_mfma_f32_16x16x32_bf16 v[152:155], v[76:79], v[0:3], v[152:155]\n\t" \
  "s_waitcnt lgkmcnt(9)\n\t" \
  "v_cndmask_b32_e64 v140, v37, v36, vcc\n\t" \
  "v_mfma_f32_16x16x32_bf16 v[152:155], v[72:75], v[4:7], v[152:155]\n\t" \
  "v_mov_b32 v145, v144\n\t" \
  "v_mfma_f32_16x16x32_bf16 v[156:159], v[84:87], v[0:3], v[156:159]\n\t" \
  "ds_read_b64 v[0:1], v30 offset:0\n\t" \
  "ds_read_b64 v[2:3], v31 offset:0\n\t" \
  "v_mfma_f32_16x16x32_bf16 v[156:159], v[80:83], v[4:7], v[156:159]\n\t" \
  "ds_read_b64 v[4:5], v24 offset:1024\n\t" \
  "ds_read_b64 v[6:7], v25 offset:1024\n\t" \
  "s_waitcnt lgkmcnt(9)\n\t" \
  "v_mfma_f32_16x16x32_bf16 v[152:155], v[68:71], v[8:11], v[152:155]\n\t" \
  "ds_bpermute_b32 v39, v32, v151\n\t" \
  "v_mfma_f32_16x16x32_bf16 v[152:155], v[64:67], v[12:15], v[152:155]\n\t" \
  "v_mov_b32 v151, v150\n\t" \
  "v_mov_b32 v150, v149\n\t" \
  "v_mfma_f32_16x16x32_bf16 v[156:159], v[76:79], v[8:11], v[156:159]\n\t" \
  "ds_read_b64 v[8:9], v26 offset:2048\n\t" \
  "ds_read_b64 v[10:11], v27 offset:2048\n\t" \
  "v_mfma_f32_16x16x32_bf16 v[156:159], v[72:75], v[12:15], v[156:159]\n\t" \
  "ds_read_b64 v[12:13], v28 offset:3072\n\t" \
  "s_waitcnt lgkmcnt(11)\n\t" \
  "v_mfma_f32_16x16x32_bf16 v[156:159], v[68:71], v[16:19], v[156:159]\n\t" \
  "ds_bpermute_b32 v40, v32, v155\n\t" \
  "s_waitcnt lgkmcnt(10)\n\t" \
  "v_mfma_f32_16x16x32_bf16 v[156:159], v[64:67], v[20:23], v[156:159]\n\t" \
  "ds_read_b64 v[14:15], v29 offset:3072\n\t" \
  "s_waitcnt lgkmcnt(8)\n\t" \
  "v_mfma_f32_16x16x32_bf16 v[128:131], v[68:71], v[0:3], v[128:131]\n\t" \
  "v_cndmask_b32_e64 v144, v38, v37, vcc\n\t" \
  "s_waitcnt lgkmcnt(6)\n\t" \
  "v_mfma_f32_16x16x32_bf16 v[128:131], v[64:67], v[4:7], v[128:131]\n\t" \
  "v_mov_b32 v149, v148\n\t" \
  "v_mov_b32 v155, v154\n\t" \
  "v_mfma_f32_16x16x32_bf16 v[132:135], v[76:79], v[0:3], v[132:135]\n\t" \
  "ds_bpermute_b32 v41, v32, v159\n\t" \
  "v_mov_b32 v154, v153\n\t" \
  "v_mfma_f32_16x16x32_bf16 v[132:135], v[72:75], v[4:7], v[132:135]\n\t" \
  "v_mov_b32 v153, v152\n\t" \
  "v_mov_b32 v159, v158\n\t" \
  "v_mfma_f32_16x16x32_bf16 v[136:139], v[84:87], v[0:3], v[136:139]\n\t" \
  "v_mov_b32 v158, v157\n\t" \
  "v_mov_b32 v157, v156\n\t" \
  "v_mfma_f32_16x16x32_bf16 v[136:139], v[80:83], v[4:7], v[136:139]\n\t" \
  "s_waitcnt lgkmcnt(6)\n\t" \
  "v_cndmask_b32_e64 v148, v39, v38, vcc\n\t" \
  "v_mfma_f32_16x16x32_bf16 v[140:143], v[92:95], v[0:3], v[140:143]\n\t" \
  "ds_read_b64 v[16:17], v30 offset:4096\n\t" \
  "ds_read_b64 v[18:19], v31 offset:4096\n\t" \
  "v_mfma_f32_16x16x32_bf16 v[140:143], v[88:91], v[4:7], v[140:143]\n\t" \
  "ds_read_b64 v[20:21], v24 offset:5120\n\t" \
  "ds_read_b64 v[22:23], v25 offset:5120\n\t" \
  "v_mfma_f32_16x16x32_bf16 v[144:147], v[100:103], v[0:3], v[144:147]\n\t" \
  "s_waitcnt lgkmcnt(6)\n\t" \
  "v_cndmask_b32_e64 v152, v40, v39, vcc\n\t" \
  "v_mfma_f32_16x16x32_bf16 v[144:147], v[96:99], v[4:7], v[144:147]\n\t" \
  "s_mov_b64 exec, 1\n\t" \
  "ds_write_b32 v33, v33\n\t" \
  "s_mov_b64 exec, -1\n\t" \
  "v_mfma_f32_16x16x32_bf16 v[148:151], v[108:111], v[0:3], v[148:151]\n\t" \
  "s_waitcnt lgkmcnt(5)\n\t" \
  "v_cndmask_b32_e64 v156, v41, v40, vcc\n\t" \
  "v_mfma_f32_16x16x32_bf16 v[148:151], v[104:107], v[4:7], v[148:151]\n\t" \
  "v_cvt_pk_bf16_f32 v34, v128, v129\n\t" \
  "v_cvt_pk_bf16_f32 v35, v130, v131\n\t" \
  "v_mfma_f32_16x16x32_bf16 v[152:155], v[116:119], v[0:3], v[152:155]\n\t" \
  "v_mfma_f32_16x16x32_bf16 v[152:155], v[112:115], v[4:7], v[152:155]\n\t" \
  "v_mfma_f32_16x16x32_bf16 v[156:159], v[124:127], v[0:3], v[156:159]\n\t" \
  "ds_read_b64 v[0:1], v26 offset:6144\n\t" \
  "ds_read_b64 v[2:3], v27 offset:6144\n\t" \
  "v_mfma_f32_16x16x32_bf16 v[156:159], v[120:123], v[4:7], v[156:159]\n\t" \
  "ds_read_b64 v[4:5], v28 offset:7168\n\t" \
  "ds_read_b64 v[6:7], v29 offset:7168\n\t" \
  "v_mfma_f32_16x16x32_bf16 v[132:135], v[68:71], v[8:11], v[132:135]\n\t" \
  "v_mfma_f32_16x16x32_bf16 v[132:135], v[64:67], v[12:15], v[132:135]\n\t" \
  "v_mfma_f32_16x16x32_bf16 v[136:139], v[76:79], v[8:11], v[136:139]\n\t" \
  "v_mfma_f32_16x16x32_bf16 v[136:139], v[72:75], v[12:15], v[136:139]\n\t" \
  "v_mfma_f32_16x16x32_bf16 v[140:143], v[84:87], v[8:11], v[140:143]\n\t" \
  "v_mfma_f32_16x16x32_bf16 v[140:143], v[80:83], v[12:15], v[140:143]\n\t" \
  "v_mfma_f32_16x16x32_bf16 v[144:147], v[92:95], v[8:11], v[144:147]\n\t" \
  "v_mfma_f32_16x16x32_bf16 v[144:147], v[88:91], v[12:15], v[144:147]\n\t" \
  "v_mfma_f32_16x16x32_bf16 v[148:151], v[100:103], v[8:11], v[148:151]\n\t" \
  "v_mfma_f32_16x16x32_bf16 v[148:151], v[96:99], v[12:15], v[148:151]\n\t" \
  "v_cvt_pk_bf16_f32 v36, v132, v133\n\t" \
  "v_cvt_pk_bf16_f32 v37, v134, v135\n\t" \
  "v_mfma_f32_16x16x32_bf16 v[152:155], v[108:111], v[8:11], v[152:155]\n\t" \
  "v_mfma_f32_16x16x32_bf16 v[152:155], v[104:107], v[12:15], v[152:155]\n\t" \
  "v_permlane16_swap_b32 v34, v36\n\t" \
  "v_permlane16_swap_b32 v35, v37\n\t" \
  "v_mfma_f32_16x16x32_bf16 v[156:159], v[116:119], v[8:11], v[156:159]\n\t" \
  "global_store_dwordx4 v42, v[34:37], %[yb] offset:0 sc1\n\t" \
  "v_mfma_f32_16x16x32_bf16 v[156:159], v[112:115], v[12:15], v[156:159]\n\t" \
  "ds_read_b64 v[8:9], v30 offset:8192\n\t" \
  "s_waitcnt lgkmcnt(1)\n\t" \
  "v_mfma_f32_16x16x32_bf16 v[136:139], v[68:71], v[16:19], v[136:139]\n\t" \
  "ds_read_b64 v[10:11], v31 offset:8192\n\t" \
  "ds_read_b64 v[12:13], v24 offset:9216\n\t" \
  "v_mfma_f32_16x16x32_bf16 v[136:139], v[64:67], v[20:23], v[136:139]\n\t" \
  "ds_read_b64 v[14:15], v25 offset:9216\n\t" \
  "v_mfma_f32_16x16x32_bf16 v[140:143], v[76:79], v[16:19], v[140:143]\n\t" \
  "v_mfma_f32_16x16x32_bf16 v[140:143], v[72:75], v[20:23], v[140:143]\n\t" \
  "v_mfma_f32_16x16x32_bf16 v[144:147], v[84:87], v[16:19], v[144:147]\n\t" \
  "v_mfma_f32_16x16x32_bf16 v[144:147], v[80:83], v[20:23], v[144:147]\n\t" \
  "v_mfma_f32_16x16x32_bf16 v[148:151], v[92:95], v[16:19], v[148:151]\n\t" \
  "v_mfma_f32_16x16x32_bf16 v[148:151], v[88:91], v[20:23], v[148:151]\n\t" \
  "v_mfma_f32_16x16x32_bf16 v[152:155], v[100:103], v[16:19], v[152:155]\n\t" \
  "v_cvt_pk_bf16_f32 v38, v136, v137\n\t" \
  "v_cvt_pk_bf16_f32 v39, v138, v139\n\t" \
  "v_mfma_f32_16x16x32_bf16 v[152:155], v[96:99], v[20:23], v[152:155]\n\t" \
  "v_mfma_f32_16x16x32_bf16 v[156:159], v[108:111], v[16:19], v[156:159]\n\t" \
  "ds_read_b64 v[16:17], v26 offset:10240\n\t" \
  "ds_read_b64 v[18:19], v27 offset:10240\n\t" \
  "v_mfma_f32_16x16x32_bf16 v[156:159], v[104:107], v[20:23], v[156:159]\n\t" \
  "ds_read_b64 v[20:21], v28 offset:11264\n\t" \
  "ds_read_b64 v[22:23], v29 offset:11264\n\t" \
  "v_mfma_f32_16x16x32_bf16 v[140:143], v[68:71], v[0:3], v[140:143]\n\t" \
  "v_mfma_f32_16x16x32_bf16 v[140:143], v[64:67], v[4:7], v[140:143]\n\t" \
  "v_mfma_f32_16x16x32_bf16 v[144:147], v[76:79], v[0:3], v[144:147]\n\t" \
  "v_mfma_f32_16x16x32_bf16 v[144:147], v[72:75], v[4:7], v[144:147]\n\t" \
  "v_mfma_f32_16x16x32_bf16 v[148:151], v[84:87], v[0:3], v[148:151]\n\t" \
  "v_mfma_f32_16x16x32_bf16 v[148:151], v[80:83], v[4:7], v[148:151]\n\t" \
  "v_mfma_f32_16x16x32_bf16 v[152:155], v[92:95], v[0:3], v[152:155]\n\t" \
  "v_mfma_f32_16x16x32_bf16 v[152:155], v[88:91], v[4:7], v[152:155]\n\t" \
  "v_mfma_f32_16x16x32_bf16 v[156:159], v[100:103], v[0:3], v[156:159]\n\t" \
  "ds_read_b64 v[0:1], v30 offset:12288\n\t" \
  "v_cvt_pk_bf16_f32 v40, v140, v141\n\t" \
  "v_mfma_f32_16x16x32_bf16 v[156:159], v[96:99], v[4:7], v[156:159]\n\t" \
  "v_cvt_pk_bf16_f32 v41, v142, v143\n\t" \
  "s_waitcnt lgkmcnt(3)\n\t" \
  "v_mfma_f32_16x16x32_bf16 v[144:147], v[68:71], v[8:11], v[144:147]\n\t" \
  "ds_read_b64 v[2:3], v31 offset:12288\n\t" \
  "ds_read_b64 v[4:5], v24 offset:13312\n\t" \
  "v_mfma_f32_16x16x32_bf16 v[144:147], v[64:67], v[12:15], v[144:147]\n\t" \
  "v_permlane16_swap_b32 v38, v40\n\t" \
  "v_permlane16_swap_b32 v39, v41\n\t" \
  "v_mfma_f32_16x16x32_bf16 v[148:151], v[76:79], v[8:11], v[148:151]\n\t" \
  "ds_read_b64 v[6:7], v25 offset:13312\n\t" \
  "v_mfma_f32_16x16x32_bf16 v[148:151], v[72:75], v[12:15], v[148:151]\n\t" \
  "global_store_dwordx4 v42, v[38:41], %[yb] offset:1024 sc1\n\t" \
  "v_mfma_f32_16x16x32_bf16 v[152:155], v[84:87], v[8:11], v[152:155]\n\t" \
  "v_mfma_f32_16x16x32_bf16 v[152:155], v[80:83], v[12:15], v[152:155]\n\t" \
  "v_cvt_pk_bf16_f32 v34, v144, v145\n\t" \
  "v_cvt_pk_bf16_f32 v35, v146, v147\n\t" \
  "v_mfma_f32_16x16x32_bf16 v[156:159], v[92:95], v[8:11], v[156:159]\n\t" \
  "ds_read_b64 v[8:9], v26 offset:14336\n\t" \
  "ds_read_b64 v[10:11], v27 offset:14336\n\t" \
  "v_mfma_f32_16x16x32_bf16 v[156:159], v[88:91], v[12:15], v[156:159]\n\t" \
  "ds_read_b64 v[12:13], v28 offset:15360\n\t" \
  "ds_read_b64 v[14:15], v29 offset:15360\n\t" \
  "v_mfma_f32_16x16x32_bf16 v[148:151], v[68:71], v[16:19], v[148:151]\n\t" \
  "s_waitcnt lgkmcnt(8)\n\t" \
  "v_mfma_f32_16x16x32_bf16 v[148:151], v[64:67], v[20:23], v[148:151]\n\t" \
  "v_mfma_f32_16x16x32_bf16 v[152:155], v[76:79], v[16:19], v[152:155]\n\t" \
  "v_mfma_f32_16x16x32_bf16 v[152:155], v[72:75], v[20:23], v[152:155]\n\t" \
  "v_mfma_f32_16x16x32_bf16 v[156:159], v[84:87], v[16:19], v[156:159]\n\t" \
  "v_mfma_f32_16x16x32_bf16 v[156:159], v[80:83], v[20:23], v[156:159]\n\t" \
  "s_waitcnt lgkmcnt(4)\n\t" \
  "v_mfma_f32_16x16x32_bf16 v[152:155], v[68:71], v[0:3], v[152:155]\n\t" \
  "v_mfma_f32_16x16x32_bf16 v[152:155], v[64:67], v[4:7], v[152:155]\n\t" \
  "v_mfma_f32_16x16x32_bf16 v[156:159], v[76:79], v[0:3], v[156:159]\n\t" \
  "v_cvt_pk_bf16_f32 v36, v148, v149\n\t" \
  "v_cvt_pk_bf16_f32 v37, v150, v151\n\t" \
  "v_mfma_f32_16x16x32_bf16 v[156:159], v[72:75], v[4:7], v[156:159]\n\t" \
  "s_waitcnt lgkmcnt(2)\n\t" \
  "v_mfma_f32_16x16x32_bf16 v[156:159], v[68:71], v[8:11], v[156:159]\n\t" \
  "v_permlane16_swap_b32 v34, v36\n\t" \
  "s_waitcnt lgkmcnt(0)\n\t" \
  "v_mfma_f32_16x16x32_bf16 v[156:159], v[64:67], v[12:15], v[156:159]\n\t" \
  "v_permlane16_swap_b32 v35, v37\n\t" \
  "global_store_dwordx4 v42, v[34:37], %[yb] offset:2048 sc1\n\t" \
  "s_waitcnt lgkmcnt(0)\n\t" \
  "s_nop 4\n\t" \
  "v_cvt_pk_bf16_f32 v38, v152, v153\n\t" \
  "v_cvt_pk_bf16_f32 v39, v154, v155\n\t" \
  "v_cvt_pk_bf16_f32 v40, v156, v157\n\t" \
  "v_cvt_pk_bf16_f32 v41, v158, v159\n\t" \
  "s_nop 1\n\t" \
  "v_permlane16_swap_b32 v38, v40\n\t" \
  "v_permlane16_swap_b32 v39, v41\n\t" \
  "global_store_dwordx4 v42, v[38:41], %[yb] offset:3072 sc1\n\t" \
  "s_nop 1\n\t"
#define CV_Q 1
#define CV_CLOB "vcc", "v0", "v1", "v2", "v3", "v4", "v5", "v6", "v7", "v8", "v9", "v10", "v11", "v12", "v13", "v14", "v15", "v16", "v17", "v18", "v19", "v20", "v21", "v22", "v23", "v34", "v35", "v36", "v37", "v38", "v39", "v40", "v41"

#define G3_ASM \
  "s_mov_b32 %[c], -1\n\t" \
  "s_getpc_b64 s[92:93]\n\t" \
  "s_branch QC_%=\n\t" \
  "QH_%=:\n\t" \
  "v_add_u32 v166, %[go], v165\n\t" \
  "ds_read_b32 v166, v166\n\t" \
  "s_addk_i32 %[cn], 1\n\t" \
  "s_cmp_gt_u32 %[cn], 0x400000\n\t" \
  "s_cbranch_scc1 QI_%=\n\t" \
  "s_waitcnt lgkmcnt(0)\n\t" \
  "v_cmp_eq_u32 vcc, 0, v166\n\t" \
  "s_cbranch_vccz QI_%=\n\t" \
  "s_sleep 1\n\t" \
  "s_getpc_b64 s[92:93]\n\t" \
  "s_branch QC_%=\n\t" \
  "s_branch QH_%=\n\t" \
  "QI_%=:\n\t" \
  "s_mov_b32 %[c], 0\n\t" \
  "s_getpc_b64 s[92:93]\n\t" \
  "s_branch QC_%=\n\t" \
  "s_setprio 0\n\t" \
  "ds_read_b32 v166, v165\n\t" \
  "ds_read_b64_tr_b16 v[96:97], v160 offset:0\n\t" \
  "ds_read_b64_tr_b16 v[98:99], v160 offset:2048\n\t" \
  "ds_read_b64_tr_b16 v[100:101], v161 offset:0\n\t" \
  "ds_read_b64_tr_b16 v[102:103], v161 offset:2048\n\t" \
  "ds_read_b64_tr_b16 v[104:105], v162 offset:0\n\t" \
  "ds_read_b64_tr_b16 v[106:107], v162 offset:2048\n\t" \
  "ds_read_b64_tr_b16 v[108:109], v163 offset:0\n\t" \
  "ds_read_b64_tr_b16 v[110:111], v163 offset:2048\n\t" \
  "ds_read_b64_tr_b16 v[112:113], v160 offset:4096\n\t" \
  "ds_read_b64_tr_b16 v[114:115], v160 offset:6144\n\t" \
  "ds_read_b64_tr_b16 v[116:117], v161 offset:4096\n\t" \
  "ds_read_b64_tr_b16 v[118:119], v161 offset:6144\n\t" \
  "ds_read_b64_tr_b16 v[120:121], v162 offset:4096\n\t" \
  "ds_read_b64_tr_b16 v[122:123], v162 offset:6144\n\t" \
  "s_waitcnt lgkmcnt(10)\n\t" \
  "v_mfma_f32_16x16x32_bf16 v[128:131], v[96:99], v[0:3], 0\n\t" \
  "ds_read_b64_tr_b16 v[124:125], v163 offset:4096\n\t" \
  "v_readlane_b32 s94, v167, 4\n\t" \
  "v_mfma_f32_16x16x32_bf16 v[144:147], v[96:99], v[4:7], 0\n\t" \
  "ds_read_b64_tr_b16 v[126:127], v163 offset:6144\n\t" \
  "v_cmp_lt_u32 vcc, %[th], v166\n\t" \
  "v_mfma_f32_16x16x32_bf16 v[132:135], v[100:103], v[0:3], 0\n\t" \
  "s_mov_b64 exec, vcc\n\t" \
  "buffer_load_dwordx4 v[64:67], v164, %[ob], s94 offen offset:0 sc1\n\t" \
  "s_mov_b64 exec, -1\n\t" \
  "v_mfma_f32_16x16x32_bf16 v[148:151], v[100:103], v[4:7], 0\n\t" \
  "ds_read_b64_tr_b16 v[96:97], v160 offset:8192\n\t" \
  "ds_read_b64_tr_b16 v[98:99], v160 offset:10240\n\t" \
  "s_waitcnt lgkmcnt(10)\n\t" \
  "v_mfma_f32_16x16x32_bf16 v[136:139], v[104:107], v[0:3], 0\n\t" \
  "s_mov_b64 exec, vcc\n\t" \
  "buffer_load_dwordx4 v[68:71], v164, %[ob], s94 offen offset:1024 sc1\n\t" \
  "s_mov_b64 exec, -1\n\t" \
  "v_mfma_f32_16x16x32_bf16 v[152:155], v[104:107], v[4:7], 0\n\t" \
  "ds_read_b64_tr_b16 v[100:101], v161 offset:8192\n\t" \
  "ds_read_b64_tr_b16 v[102:103], v161 offset:10240\n\t" \
  "v_mfma_f32_16x16x32_bf16 v[140:143], v[108:111], v[0:3], 0\n\t" \
  "s_mov_b64 exec, vcc\n\t" \
  "buffer_load_dwordx4 v[72:75], v164, %[ob], s94 offen offset:2048 sc1\n\t" \
  "s_mov_b64 exec, -1\n\t" \
  "v_mfma_f32_16x16x32_bf16 v[156:159], v[108:111], v[4:7], 0\n\t" \
  "ds_read_b64_tr_b16 v[104:105], v162 offset:8192\n\t" \
  "ds_read_b64_tr_b16 v[106:107], v162 offset:10240\n\t" \
  "s_waitcnt lgkmcnt(10)\n\t" \
  "v_mfma_f32_16x16x32_bf16 v[128:131], v[112:115], v[8:11], v[128:131]\n\t" \
  "s_mov_b64 exec, vcc\n\t" \
  "buffer_load_dwordx4 v[76:79], v164, %[ob], s94 offen offset:3072 sc1\n\t" \
  "s_mov_b64 exec, -1\n\t" \
  "v_mfma_f32_16x16x32_bf16 v[144:147], v[112:115], v[12:15], v[144:147]\n\t" \
  "ds_read_b64_tr_b16 v[108:109], v163 offset:8192\n\t" \
  "ds_read_b64_tr_b16 v[110:111], v163 offset:10240\n\t" \
  "v_mfma_f32_16x16x32_bf16 v[132:135], v[116:119], v[8:11], v[132:135]\n\t" \
  "ds_read_b64_tr_b16 v[112:113], v160 offset:12288\n\t" \
  "ds_read_b32 v166, v165\n\t" \
  "v_mfma_f32_16x16x32_bf16 v[148:151], v[116:119], v[12:15], v[148:151]\n\t" \
  "ds_read_b64_tr_b16 v[114:115], v160 offset:14336\n\t" \
  "s_waitcnt lgkmcnt(11)\n\t" \
  "v_mfma_f32_16x16x32_bf16 v[136:139], v[120:123], v[8:11], v[136:139]\n\t" \
  "ds_read_b64_tr_b16 v[116:117], v161 offset:12288\n\t" \
  "v_mfma_f32_16x16x32_bf16 v[152:155], v[120:123], v[12:15], v[152:155]\n\t" \
  "ds_read_b64_tr_b16 v[118:119], v161 offset:14336\n\t" \
  "s_sub_u32 %[iss], %[iss], vcc_lo\n\t" \
  "v_mfma_f32_16x16x32_bf16 v[140:143], v[124:127], v[8:11], v[140:143]\n\t" \
  "ds_read_b64_tr_b16 v[120:121], v162 offset:12288\n\t" \
  "v_mfma_f32_16x16x32_bf16 v[156:159], v[124:127], v[12:15], v[156:159]\n\t" \
  "ds_read_b64_tr_b16 v[122:123], v162 offset:14336\n\t" \
  "s_add_u32 %[th], %[th], 1\n\t" \
  "s_cbranch_vccz QF1_%=\n\t" \
  "s_waitcnt vmcnt(12)\n\t" \
  "QG1_%=:\n\t" \
  "s_waitcnt lgkmcnt(11)\n\t" \
  "v_mfma_f32_16x16x32_bf16 v[128:131], v[96:99], v[16:19], v[128:131]\n\t" \
  "ds_read_b64_tr_b16 v[124:125], v163 offset:12288\n\t" \
  "v_readlane_b32 s94, v167, 5\n\t" \
  "v_mfma_f32_16x16x32_bf16 v[144:147], v[96:99], v[20:23], v[144:147]\n\t" \
  "ds_read_b64_tr_b16 v[126:127], v163 offset:14336\n\t" \
  "s_waitcnt lgkmcnt(7)\n\t" \
  "v_cmp_lt_u32 vcc, %[th], v166\n\t" \
  "v_mfma_f32_16x16x32_bf16 v[132:135], v[100:103], v[16:19], v[132:135]\n\t" \
  "s_mov_b64 exec, vcc\n\t" \
  "buffer_load_dwordx4 v[0:3], v164, %[ob], s94 offen offset:0 sc1\n\t" \
  "s_mov_b64 exec, -1\n\t" \
  "v_mfma_f32_16x16x32_bf16 v[148:151], v[100:103], v[20:23], v[148:151]\n\t" \
  "ds_read_b64_tr_b16 v[96:97], v160 offset:16384\n\t" \
  "ds_read_b64_tr_b16 v[98:99], v160 offset:18432\n\t" \
  "v_mfma_f32_16x16x32_bf16 v[136:139], v[104:107], v[16:19], v[136:139]\n\t" \
  "s_mov_b64 exec, vcc\n\t" \
  "buffer_load_dwordx4 v[4:7], v164, %[ob], s94 offen offset:1024 sc1\n\t" \
  "s_mov_b64 exec, -1\n\t" \
  "v_mfma_f32_16x16x32_bf16 v[152:155], v[104:107], v[20:23], v[152:155]\n\t" \
  "ds_read_b64_tr_b16 v[100:101], v161 offset:16384\n\t" \
  "ds_read_b64_tr_b16 v[102:103], v161 offset:18432\n\t" \
  "v_mfma_f32_16x16x32_bf16 v[140:143], v[108:111], v[16:19], v[140:143]\n\t" \
  "s_mov_b64 exec, vcc\n\t" \
  "buffer_load_dwordx4 v[8:11], v164, %[ob], s94 offen offset:2048 sc1\n\t" \
  "s_mov_b64 exec, -1\n\t" \
  "v_mfma_f32_16x16x32_bf16 v[156:159], v[108:111], v[20:23], v[156:159]\n\t" \
  "ds_read_b64_tr_b16 v[104:105], v162 offset:16384\n\t" \
  "ds_read_b64_tr_b16 v[106:107], v162 offset:18432\n\t" \
  "s_waitcnt lgkmcnt(10)\n\t" \
  "v_mfma_f32_16x16x32_bf16 v[128:131], v[112:115], v[24:27], v[128:131]\n\t" \
  "s_mov_b64 exec, vcc\n\t" \
  "buffer_load_dwordx4 v[12:15], v164, %[ob], s94 offen offset:3072 sc1\n\t" \
  "s_mov_b64 exec, -1\n\t" \
  "v_mfma_f32_16x16x32_bf16 v[144:147], v[112:115], v[28:31], v[144:147]\n\t" \
  "ds_read_b64_tr_b16 v[108:109], v163 offset:16384\n\t" \
  "ds_read_b64_tr_b16 v[110:111], v163 offset:18432\n\t" \
  "v_mfma_f32_16x16x32_bf16 v[132:135], v[116:119], v[24:27], v[132:135]\n\t" \
  "ds_read_b64_tr_b16 v[112:113], v160 offset:20480\n\t" \
  "ds_read_b32 v166, v165\n\t" \
  "v_mfma_f32_16x16x32_bf16 v[148:151], v[116:119], v[28:31], v[148:151]\n\t" \
  "ds_read_b64_tr_b16 v[114:115], v160 offset:22528\n\t" \
  "s_waitcnt lgkmcnt(11)\n\t" \
  "v_mfma_f32_16x16x32_bf16 v[136:139], v[120:123], v[24:27], v[136:139]\n\t" \
  "ds_read_b64_tr_b16 v[116:117], v161 offset:20480\n\t" \
  "v_mfma_f32_16x16x32_bf16 v[152:155], v[120:123], v[28:31], v[152:155]\n\t" \
  "ds_read_b64_tr_b16 v[118:119], v161 offset:22528\n\t" \
  "s_sub_u32 %[iss], %[iss], vcc_lo\n\t" \
  "v_mfma_f32_16x16x32_bf16 v[140:143], v[124:127], v[24:27], v[140:143]\n\t" \
  "ds_read_b64_tr_b16 v[120:121], v162 offset:20480\n\t" \
  "v_mfma_f32_16x16x32_bf16 v[156:159], v[124:127], v[28:31], v[156:159]\n\t" \
  "ds_read_b64_tr_b16 v[122:123], v162 offset:22528\n\t" \
  "s_add_u32 %[th], %[th], 1\n\t" \
  "s_cbranch_vccz QF2_%=\n\t" \
  "s_waitcnt vmcnt(12)\n\t" \
  "QG2_%=:\n\t" \
  "s_waitcnt lgkmcnt(11)\n\t" \
  "v_mfma_f32_16x16x32_bf16 v[128:131], v[96:99], v[32:35], v[128:131]\n\t" \
  "ds_read_b64_tr_b16 v[124:125], v163 offset:20480\n\t" \
  "v_readlane_b32 s94, v167, 6\n\t" \
  "v_mfma_f32_16x16x32_bf16 v[144:147], v[96:99], v[36:39], v[144:147]\n\t" \
  "ds_read_b64_tr_b16 v[126:127], v163 offset:22528\n\t" \
  "s_waitcnt lgkmcnt(7)\n\t" \
  "v_cmp_lt_u32 vcc, %[th], v166\n\t" \
  "v_mfma_f32_16x16x32_bf16 v[132:135], v[100:103], v[32:35], v[132:135]\n\t" \
  "s_mov_b64 exec, vcc\n\t" \
  "buffer_load_dwordx4 v[16:19], v164, %[ob], s94 offen offset:0 sc1\n\t" \
  "s_mov_b64 exec, -1\n\t" \
  "v_mfma_f32_16x16x32_bf16 v[148:151], v[100:103], v[36:39], v[148:151]\n\t" \
  "ds_read_b64_tr_b16 v[96:97], v160 offset:24576\n\t" \
  "ds_read_b64_tr_b16 v[98:99], v160 offset:26624\n\t" \
  "v_mfma_f32_16x16x32_bf16 v[136:139], v[104:107], v[32:35], v[136:139]\n\t" \
  "s_mov_b64 exec, vcc\n\t" \
  "buffer_load_dwordx4 v[20:23], v164, %[ob], s94 offen offset:1024 sc1\n\t" \
  "s_mov_b64 exec, -1\n\t" \
  "v_mfma_f32_16x16x32_bf16 v[152:155], v[104:107], v[36:39], v[152:155]\n\t" \
  "ds_read_b64_tr_b16 v[100:101], v161 offset:24576\n\t" \
  "ds_read_b64_tr_b16 v[102:103], v161 offset:26624\n\t" \
  "v_mfma_f32_16x16x32_bf16 v[140:143], v[108:111], v[32:35], v[140:143]\n\t" \
  "s_mov_b64 exec, vcc\n\t" \
  "buffer_load_dwordx4 v[24:27], v164, %[ob], s94 offen offset:2048 sc1\n\t" \
  "s_mov_b64 exec, -1\n\t" \
  "v_mfma_f32_16x16x32_bf16 v[156:159], v[108:111], v[36:39], v[156:159]\n\t" \
  "ds_read_b64_tr_b16 v[104:105], v162 offset:24576\n\t" \
  "ds_read_b64_tr_b16 v[106:107], v162 offset:26624\n\t" \
  "s_waitcnt lgkmcnt(10)\n\t" \
  "v_mfma_f32_16x16x32_bf16 v[128:131], v[112:115], v[40:43], v[128:131]\n\t" \
  "s_mov_b64 exec, vcc\n\t" \
  "buffer_load_dwordx4 v[28:31], v164, %[ob], s94 offen offset:3072 sc1\n\t" \
  "s_mov_b64 exec, -1\n\t" \
  "v_mfma_f32_16x16x32_bf16 v[144:147], v[112:115], v[44:47], v[144:147]\n\t" \
  "ds_read_b64_tr_b16 v[108:109], v163 offset:24576\n\t" \
  "ds_read_b64_tr_b16 v[110:111], v163 offset:26624\n\t" \
  "v_mfma_f32_16x16x32_bf16 v[132:135], v[116:119], v[40:43], v[132:135]\n\t" \
  "ds_read_b64_tr_b16 v[112:113], v160 offset:28672\n\t" \
  "ds_read_b32 v166, v165\n\t" \
  "v_mfma_f32_16x16x32_bf16 v[148:151], v[116:119], v[44:47], v[148:151]\n\t" \
  "ds_read_b64_tr_b16 v[114:115], v160 offset:30720\n\t" \
  "s_waitcnt lgkmcnt(11)\n\t" \
  "v_mfma_f32_16x16x32_bf16 v[136:139], v[120:123], v[40:43], v[136:139]\n\t" \
  "ds_read_b64_tr_b16 v[116:117], v161 offset:28672\n\t" \
  "v_mfma_f32_16x16x32_bf16 v[152:155], v[120:123], v[44:47], v[152:155]\n\t" \
  "ds_read_b64_tr_b16 v[118:119], v161 offset:30720\n\t" \
  "s_sub_u32 %[iss], %[iss], vcc_lo\n\t" \
  "v_mfma_f32_16x16x32_bf16 v[140:143], v[124:127], v[40:43], v[140:143]\n\t" \
  "ds_read_b64_tr_b16 v[120:121], v162 offset:28672\n\t" \
  "v_mfma_f32_16x16x32_bf16 v[156:159], v[124:127], v[44:47], v[156:159]\n\t" \
  "ds_read_b64_tr_b16 v[122:123], v162 offset:30720\n\t" \
  "s_add_u32 %[th], %[th], 1\n\t" \
  "s_cbranch_vccz QF3_%=\n\t" \
  "s_waitcnt vmcnt(12)\n\t" \
  "QG3_%=:\n\t" \
  "s_waitcnt lgkmcnt(11)\n\t" \
  "v_mfma_f32_16x16x32_bf16 v[128:131], v[96:99], v[48:51], v[128:131]\n\t" \
  "ds_read_b64_tr_b16 v[124:125], v163 offset:28672\n\t" \
  "v_readlane_b32 s94, v167, 7\n\t" \
  "v_mfma_f32_16x16x32_bf16 v[144:147], v[96:99], v[52:55], v[144:147]\n\t" \
  "ds_read_b64_tr_b16 v[126:127], v163 offset:30720\n\t" \
  "s_waitcnt lgkmcnt(7)\n\t" \
  "v_cmp_lt_u32 vcc, %[th], v166\n\t" \
  "v_mfma_f32_16x16x32_bf16 v[132:135], v[100:103], v[48:51], v[132:135]\n\t" \
  "s_mov_b64 exec, vcc\n\t" \
  "buffer_load_dwordx4 v[32:35], v164, %[ob], s94 offen offset:0 sc1\n\t" \
  "s_mov_b64 exec, -1\n\t" \
  "v_mfma_f32_16x16x32_bf16 v[148:151], v[100:103], v[52:55], v[148:151]\n\t" \
  "ds_read_b64_tr_b16 v[96:97], v160 offset:32768\n\t" \
  "ds_read_b64_tr_b16 v[98:99], v160 offset:34816\n\t" \
  "v_mfma_f32_16x16x32_bf16 v[136:139], v[104:107], v[48:51], v[136:139]\n\t" \
  "s_mov_b64 exec, vcc\n\t" \
  "buffer_load_dwordx4 v[36:39], v164, %[ob], s94 offen offset:1024 sc1\n\t" \
  "s_mov_b64 exec, -1\n\t" \
  "v_mfma_f32_16x16x32_bf16 v[152:155], v[104:107], v[52:55], v[152:155]\n\t" \
  "ds_read_b64_tr_b16 v[100:101], v161 offset:32768\n\t" \
  "ds_read_b64_tr_b16 v[102:103], v161 offset:34816\n\t" \
  "v_mfma_f32_16x16x32_bf16 v[140:143], v[108:111], v[48:51], v[140:143]\n\t" \
  "s_mov_b64 exec, vcc\n\t" \
  "buffer_load_dwordx4 v[40:43], v164, %[ob], s94 offen offset:2048 sc1\n\t" \
  "s_mov_b64 exec, -1\n\t" \
  "v_mfma_f32_16x16x32_bf16 v[156:159], v[108:111], v[52:55], v[156:159]\n\t" \
  "ds_read_b64_tr_b16 v[104:105], v162 offset:32768\n\t" \
  "ds_read_b64_tr_b16 v[106:107], v162 offset:34816\n\t" \
  "s_waitcnt lgkmcnt(10)\n\t" \
  "v_mfma_f32_16x16x32_bf16 v[128:131], v[112:115], v[56:59], v[128:131]\n\t" \
  "s_mov_b64 exec, vcc\n\t" \
  "buffer_load_dwordx4 v[44:47], v164, %[ob], s94 offen offset:3072 sc1\n\t" \
  "s_mov_b64 exec, -1\n\t" \
  "v_mfma_f32_16x16x32_bf16 v[144:147], v[112:115], v[60:63], v[144:147]\n\t" \
  "ds_read_b64_tr_b16 v[108:109], v163 offset:32768\n\t" \
  "ds_read_b64_tr_b16 v[110:111], v163 offset:34816\n\t" \
  "v_mfma_f32_16x16x32_bf16 v[132:135], v[116:119], v[56:59], v[132:135]\n\t" \
  "ds_read_b64_tr_b16 v[112:113], v160 offset:36864\n\t" \
  "ds_read_b32 v166, v165\n\t" \
  "v_mfma_f32_16x16x32_bf16 v[148:151], v[116:119], v[60:63], v[148:151]\n\t" \
  "ds_read_b64_tr_b16 v[114:115], v160 offset:38912\n\t" \
  "s_waitcnt lgkmcnt(11)\n\t" \
  "v_mfma_f32_16x16x32_bf16 v[136:139], v[120:123], v[56:59], v[136:139]\n\t" \
  "ds_read_b64_tr_b16 v[116:117], v161 offset:36864\n\t" \
  "v_mfma_f32_16x16x32_bf16 v[152:155], v[120:123], v[60:63], v[152:155]\n\t" \
  "ds_read_b64_tr_b16 v[118:119], v161 offset:38912\n\t" \
  "s_sub_u32 %[iss], %[iss], vcc_lo\n\t" \
  "v_mfma_f32_16x16x32_bf16 v[140:143], v[124:127], v[56:59], v[140:143]\n\t" \
  "ds_read_b64_tr_b16 v[120:121], v162 offset:36864\n\t" \
  "v_mfma_f32_16x16x32_bf16 v[156:159], v[124:127], v[60:63], v[156:159]\n\t" \
  "ds_read_b64_tr_b16 v[122:123], v162 offset:38912\n\t" \
  "s_add_u32 %[th], %[th], 1\n\t" \
  "s_cbranch_vccz QF4_%=\n\t" \
  "s_waitcnt vmcnt(12)\n\t" \
  "QG4_%=:\n\t" \
  "s_waitcnt lgkmcnt(11)\n\t" \
  "v_mfma_f32_16x16x32_bf16 v[128:131], v[96:99], v[64:67], v[128:131]\n\t" \
  "ds_read_b64_tr_b16 v[124:125], v163 offset:36864\n\t" \
  "v_readlane_b32 s94, v167, 8\n\t" \
  "v_mfma_f32_16x16x32_bf16 v[144:147], v[96:99], v[68:71], v[144:147]\n\t" \
  "ds_read_b64_tr_b16 v[126:127], v163 offset:38912\n\t" \
  "s_waitcnt lgkmcnt(7)\n\t" \
  "v_cmp_lt_u32 vcc, %[th], v166\n\t" \
  "v_mfma_f32_16x16x32_bf16 v[132:135], v[100:103], v[64:67], v[132:135]\n\t" \
  "s_mov_b64 exec, vcc\n\t" \
  "buffer_load_dwordx4 v[48:51], v164, %[ob], s94 offen offset:0 sc1\n\t" \
  "s_mov_b64 exec, -1\n\t" \
  "v_mfma_f32_16x16x32_bf16 v[148:151], v[100:103], v[68:71], v[148:151]\n\t" \
  "ds_read_b64_tr_b16 v[96:97], v160 offset:40960\n\t" \
  "ds_read_b64_tr_b16 v[98:99], v160 offset:43008\n\t" \
  "v_mfma_f32_16x16x32_bf16 v[136:139], v[104:107], v[64:67], v[136:139]\n\t" \
  "s_mov_b64 exec, vcc\n\t" \
  "buffer_load_dwordx4 v[52:55], v164, %[ob], s94 offen offset:1024 sc1\n\t" \
  "s_mov_b64 exec, -1\n\t" \
  "v_mfma_f32_16x16x32_bf16 v[152:155], v[104:107], v[68:71], v[152:155]\n\t" \
  "ds_read_b64_tr_b16 v[100:101], v161 offset:40960\n\t" \
  "ds_read_b64_tr_b16 v[102:103], v161 offset:43008\n\t" \
  "v_mfma_f32_16x16x32_bf16 v[140:143], v[108:111], v[64:67], v[140:143]\n\t" \
  "s_mov_b64 exec, vcc\n\t" \
  "buffer_load_dwordx4 v[56:59], v164, %[ob], s94 offen offset:2048 sc1\n\t" \
  "s_mov_b64 exec, -1\n\t" \
  "v_mfma_f32_16x16x32_bf16 v[156:159], v[108:111], v[68:71], v[156:159]\n\t" \
  "ds_read_b64_tr_b16 v[104:105], v162 offset:40960\n\t" \
  "ds_read_b64_tr_b16 v[106:107], v162 offset:43008\n\t" \
  "s_waitcnt lgkmcnt(10)\n\t" \
  "v_mfma_f32_16x16x32_bf16 v[128:131], v[112:115], v[72:75], v[128:131]\n\t" \
  "s_mov_b64 exec, vcc\n\t" \
  "buffer_load_dwordx4 v[60:63], v164, %[ob], s94 offen offset:3072 sc1\n\t" \
  "s_mov_b64 exec, -1\n\t" \
  "v_mfma_f32_16x16x32_bf16 v[144:147], v[112:115], v[76:79], v[144:147]\n\t" \
  "ds_read_b64_tr_b16 v[108:109], v163 offset:40960\n\t" \
  "ds_read_b64_tr_b16 v[110:111], v163 offset:43008\n\t" \
  "v_mfma_f32_16x16x32_bf16 v[132:135], v[116:119], v[72:75], v[132:135]\n\t" \
  "ds_read_b64_tr_b16 v[112:113], v160 offset:45056\n\t" \
  "ds_read_b32 v166, v165\n\t" \
  "v_mfma_f32_16x16x32_bf16 v[148:151], v[116:119], v[76:79], v[148:151]\n\t" \
  "ds_read_b64_tr_b16 v[114:115], v160 offset:47104\n\t" \
  "s_waitcnt lgkmcnt(11)\n\t" \
  "v_mfma_f32_16x16x32_bf16 v[136:139], v[120:123], v[72:75], v[136:139]\n\t" \
  "ds_read_b64_tr_b16 v[116:117], v161 offset:45056\n\t" \
  "v_mfma_f32_16x16x32_bf16 v[152:155], v[120:123], v[76:79], v[152:155]\n\t" \
  "ds_read_b64_tr_b16 v[118:119], v161 offset:47104\n\t" \
  "s_sub_u32 %[iss], %[iss], vcc_lo\n\t" \
  "v_mfma_f32_16x16x32_bf16 v[140:143], v[124:127], v[72:75], v[140:143]\n\t" \
  "ds_read_b64_tr_b16 v[120:121], v162 offset:45056\n\t" \
  "v_mfma_f32_16x16x32_bf16 v[156:159], v[124:127], v[76:79], v[156:159]\n\t" \
  "ds_read_b64_tr_b16 v[122:123], v162 offset:47104\n\t" \
  "s_add_u32 %[th], %[th], 1\n\t" \
  "s_cbranch_vccz QF5_%=\n\t" \
  "s_waitcnt vmcnt(12)\n\t" \
  "QG5_%=:\n\t" \
  "s_waitcnt lgkmcnt(11)\n\t" \
  "v_mfma_f32_16x16x32_bf16 v[128:131], v[96:99], v[0:3], v[128:131]\n\t" \
  "ds_read_b64_tr_b16 v[124:125], v163 offset:45056\n\t" \
  "v_readlane_b32 s94, v167, 9\n\t" \
  "v_mfma_f32_16x16x32_bf16 v[144:147], v[96:99], v[4:7], v[144:147]\n\t" \
  "ds_read_b64_tr_b16 v[126:127], v163 offset:47104\n\t" \
  "s_waitcnt lgkmcnt(7)\n\t" \
  "v_cmp_lt_u32 vcc, %[th], v166\n\t" \
  "v_mfma_f32_16x16x32_bf16 v[132:135], v[100:103], v[0:3], v[132:135]\n\t" \
  "s_mov_b64 exec, vcc\n\t" \
  "buffer_load_dwordx4 v[64:67], v164, %[ob], s94 offen offset:0 sc1\n\t" \
  "s_mov_b64 exec, -1\n\t" \
  "v_mfma_f32_16x16x32_bf16 v[148:151], v[100:103], v[4:7], v[148:151]\n\t" \
  "ds_read_b64_tr_b16 v[96:97], v160 offset:49152\n\t" \
  "ds_read_b64_tr_b16 v[98:99], v160 offset:51200\n\t" \
  "v_mfma_f32_16x16x32_bf16 v[136:139], v[104:107], v[0:3], v[136:139]\n\t" \
  "s_mov_b64 exec, vcc\n\t" \
  "buffer_load_dwordx4 v[68:71], v164, %[ob], s94 offen offset:1024 sc1\n\t" \
  "s_mov_b64 exec, -1\n\t" \
  "v_mfma_f32_16x16x32_bf16 v[152:155], v[104:107], v[4:7], v[152:155]\n\t" \
  "ds_read_b64_tr_b16 v[100:101], v161 offset:49152\n\t" \
  "ds_read_b64_tr_b16 v[102:103], v161 offset:51200\n\t" \
  "v_mfma_f32_16x16x32_bf16 v[140:143], v[108:111], v[0:3], v[140:143]\n\t" \
  "s_mov_b64 exec, vcc\n\t" \
  "buffer_load_dwordx4 v[72:75], v164, %[ob], s94 offen offset:2048 sc1\n\t" \
  "s_mov_b64 exec, -1\n\t" \
  "v_mfma_f32_16x16x32_bf16 v[156:159], v[108:111], v[4:7], v[156:159]\n\t" \
  "ds_read_b64_tr_b16 v[104:105], v162 offset:49152\n\t" \
  "ds_read_b64_tr_b16 v[106:107], v162 offset:51200\n\t" \
  "s_waitcnt lgkmcnt(10)\n\t" \
  "v_mfma_f32_16x16x32_bf16 v[128:131], v[112:115], v[8:11], v[128:131]\n\t" \
  "s_mov_b64 exec, vcc\n\t" \
  "buffer_load_dwordx4 v[76:79], v164, %[ob], s94 offen offset:3072 sc1\n\t" \
  "s_mov_b64 exec, -1\n\t" \
  "v_mfma_f32_16x16x32_bf16 v[144:147], v[112:115], v[12:15], v[144:147]\n\t" \
  "ds_read_b64_tr_b16 v[108:109], v163 offset:49152\n\t" \
  "ds_read_b64_tr_b16 v[110:111], v163 offset:51200\n\t" \
  "v_mfma_f32_16x16x32_bf16 v[132:135], v[116:119], v[8:11], v[132:135]\n\t" \
  "ds_read_b64_tr_b16 v[112:113], v160 offset:53248\n\t" \
  "ds_read_b32 v166, v165\n\t" \
  "v_mfma_f32_16x16x32_bf16 v[148:151], v[116:119], v[12:15], v[148:151]\n\t" \
  "ds_read_b64_tr_b16 v[114:115], v160 offset:55296\n\t" \
  "s_waitcnt lgkmcnt(11)\n\t" \
  "v_mfma_f32_16x16x32_bf16 v[136:139], v[120:123], v[8:11], v[136:139]\n\t" \
  "ds_read_b64_tr_b16 v[116:117], v161 offset:53248\n\t" \
  "v_mfma_f32_16x16x32_bf16 v[152:155], v[120:123], v[12:15], v[152:155]\n\t" \
  "ds_read_b64_tr_b16 v[118:119], v161 offset:55296\n\t" \
  "s_sub_u32 %[iss], %[iss], vcc_lo\n\t" \
  "v_mfma_f32_16x16x32_bf16 v[140:143], v[124:127], v[8:11], v[140:143]\n\t" \
  "ds_read_b64_tr_b16 v[120:121], v162 offset:53248\n\t" \
  "v_mfma_f32_16x16x32_bf16 v[156:159], v[124:127], v[12:15], v[156:159]\n\t" \
  "ds_read_b64_tr_b16 v[122:123], v162 offset:55296\n\t" \
  "s_add_u32 %[th], %[th], 1\n\t" \
  "s_cbranch_vccz QF6_%=\n\t" \
  "s_waitcnt vmcnt(12)\n\t" \
  "QG6_%=:\n\t" \
  "s_waitcnt lgkmcnt(11)\n\t" \
  "v_mfma_f32_16x16x32_bf16 v[128:131], v[96:99], v[16:19], v[128:131]\n\t" \
  "ds_read_b64_tr_b16 v[124:125], v163 offset:53248\n\t" \
  "v_readlane_b32 s94, v167, 10\n\t" \
  "v_mfma_f32_16x16x32_bf16 v[144:147], v[96:99], v[20:23], v[144:147]\n\t" \
  "ds_read_b64_tr_b16 v[126:127], v163 offset:55296\n\t" \
  "s_waitcnt lgkmcnt(7)\n\t" \
  "v_cmp_lt_u32 vcc, %[th], v166\n\t" \
  "v_mfma_f32_16x16x32_bf16 v[132:135], v[100:103], v[16:19], v[132:135]\n\t" \
  "s_mov_b64 exec, vcc\n\t" \
  "buffer_load_dwordx4 v[0:3], v164, %[ob], s94 offen offset:0 sc1\n\t" \
  "s_mov_b64 exec, -1\n\t" \
  "v_mfma_f32_16x16x32_bf16 v[148:151], v[100:103], v[20:23], v[148:151]\n\t" \
  "ds_read_b64_tr_b16 v[96:97], v160 offset:57344\n\t" \
  "ds_read_b64_tr_b16 v[98:99], v160 offset:59392\n\t" \
  "v_mfma_f32_16x16x32_bf16 v[136:139], v[104:107], v[16:19], v[136:139]\n\t" \
  "s_mov_b64 exec, vcc\n\t" \
  "buffer_load_dwordx4 v[4:7], v164, %[ob], s94 offen offset:1024 sc1\n\t" \
  "s_mov_b64 exec, -1\n\t" \
  "v_mfma_f32_16x16x32_bf16 v[152:155], v[104:107], v[20:23], v[152:155]\n\t" \
  "ds_read_b64_tr_b16 v[100:101], v161 offset:57344\n\t" \
  "ds_read_b64_tr_b16 v[102:103], v161 offset:59392\n\t" \
  "v_mfma_f32_16x16x32_bf16 v[140:143], v[108:111], v[16:19], v[140:143]\n\t" \
  "s_mov_b64 exec, vcc\n\t" \
  "buffer_load_dwordx4 v[8:11], v164, %[ob], s94 offen offset:2048 sc1\n\t" \
  "s_mov_b64 exec, -1\n\t" \
  "v_mfma_f32_16x16x32_bf16 v[156:159], v[108:111], v[20:23], v[156:159]\n\t" \
  "ds_read_b64_tr_b16 v[104:105], v162 offset:57344\n\t" \
  "ds_read_b64_tr_b16 v[106:107], v162 offset:59392\n\t" \
  "s_waitcnt lgkmcnt(10)\n\t" \
  "v_mfma_f32_16x16x32_bf16 v[128:131], v[112:115], v[24:27], v[128:131]\n\t" \
  "s_mov_b64 exec, vcc\n\t" \
  "buffer_load_dwordx4 v[12:15], v164, %[ob], s94 offen offset:3072 sc1\n\t" \
  "s_mov_b64 exec, -1\n\t" \
  "v_mfma_f32_16x16x32_bf16 v[144:147], v[112:115], v[28:31], v[144:147]\n\t" \
  "ds_read_b64_tr_b16 v[108:109], v163 offset:57344\n\t" \
  "ds_read_b64_tr_b16 v[110:111], v163 offset:59392\n\t" \
  "v_mfma_f32_16x16x32_bf16 v[132:135], v[116:119], v[24:27], v[132:135]\n\t" \
  "ds_read_b64_tr_b16 v[112:113], v160 offset:61440\n\t" \
  "ds_read_b32 v166, v165\n\t" \
  "v_mfma_f32_16x16x32_bf16 v[148:151], v[116:119], v[28:31], v[148:151]\n\t" \
  "ds_read_b64_tr_b16 v[114:115], v160 offset:63488\n\t" \
  "s_waitcnt lgkmcnt(11)\n\t" \
  "v_mfma_f32_16x16x32_bf16 v[136:139], v[120:123], v[24:27], v[136:139]\n\t" \
  "ds_read_b64_tr_b16 v[116:117], v161 offset:61440\n\t" \
  "v_mfma_f32_16x16x32_bf16 v[152:155], v[120:123], v[28:31], v[152:155]\n\t" \
  "ds_read_b64_tr_b16 v[118:119], v161 offset:63488\n\t" \
  "s_sub_u32 %[iss], %[iss], vcc_lo\n\t" \
  "v_mfma_f32_16x16x32_bf16 v[140:143], v[124:127], v[24:27], v[140:143]\n\t" \
  "ds_read_b64_tr_b16 v[120:121], v162 offset:61440\n\t" \
  "s_mov_b64 exec, 1\n\t" \
  "ds_write_b32 v165, v165 offset:76\n\t" \
  "s_mov_b64 exec, -1\n\t" \
  "v_mfma_f32_16x16x32_bf16 v[156:159], v[124:127], v[28:31], v[156:159]\n\t" \
  "ds_read_b64_tr_b16 v[122:123], v162 offset:63488\n\t" \
  "s_add_u32 %[th], %[th], 1\n\t" \
  "s_cbranch_vccz QF7_%=\n\t" \
  "s_waitcnt vmcnt(12)\n\t" \
  "QG7_%=:\n\t" \
  "s_waitcnt lgkmcnt(12)\n\t" \
  "v_mfma_f32_16x16x32_bf16 v[128:131], v[96:99], v[32:35], v[128:131]\n\t" \
  "ds_read_b64_tr_b16 v[124:125], v163 offset:61440\n\t" \
  "v_readlane_b32 s94, v167, 11\n\t" \
  "v_mfma_f32_16x16x32_bf16 v[144:147], v[96:99], v[36:39], v[144:147]\n\t" \
  "ds_read_b64_tr_b16 v[126:127], v163 offset:63488\n\t" \
  "s_waitcnt lgkmcnt(8)\n\t" \
  "v_cmp_lt_u32 vcc, %[th], v166\n\t" \
  "v_mfma_f32_16x16x32_bf16 v[132:135], v[100:103], v[32:35], v[132:135]\n\t" \
  "v_add_u32 v160, 0x10000, v160\n\t" \
  "s_mov_b64 exec, vcc\n\t" \
  "buffer_load_dwordx4 v[16:19], v164, %[ob], s94 offen offset:0 sc1\n\t" \
  "s_mov_b64 exec, -1\n\t" \
  "v_mfma_f32_16x16x32_bf16 v[148:151], v[100:103], v[36:39], v[148:151]\n\t" \
  "ds_read_b64_tr_b16 v[96:97], v160 offset:0\n\t" \
  "ds_read_b64_tr_b16 v[98:99], v160 offset:2048\n\t" \
  "v_mfma_f32_16x16x32_bf16 v[136:139], v[104:107], v[32:35], v[136:139]\n\t" \
  "v_add_u32 v161, 0x10000, v161\n\t" \
  "s_mov_b64 exec, vcc\n\t" \
  "buffer_load_dwordx4 v[20:23], v164, %[ob], s94 offen offset:1024 sc1\n\t" \
  "s_mov_b64 exec, -1\n\t" \
  "v_mfma_f32_16x16x32_bf16 v[152:155], v[104:107], v[36:39], v[152:155]\n\t" \
  "ds_read_b64_tr_b16 v[100:101], v161 offset:0\n\t" \
  "ds_read_b64_tr_b16 v[102:103], v161 offset:2048\n\t" \
  "v_mfma_f32_16x16x32_bf16 v[140:143], v[108:111], v[32:35], v[140:143]\n\t" \
  "v_add_u32 v162, 0x10000, v162\n\t" \
  "s_mov_b64 exec, vcc\n\t" \
  "buffer_load_dwordx4 v[24:27], v164, %[ob], s94 offen offset:2048 sc1\n\t" \
  "s_mov_b64 exec, -1\n\t" \
  "v_mfma_f32_16x16x32_bf16 v[156:159], v[108:111], v[36:39], v[156:159]\n\t" \
  "ds_read_b64_tr_b16 v[104:105], v162 offset:0\n\t" \
  "ds_read_b64_tr_b16 v[106:107], v162 offset:2048\n\t" \
  "s_waitcnt lgkmcnt(11)\n\t" \
  "v_mfma_f32_16x16x32_bf16 v[128:131], v[112:115], v[40:43], v[128:131]\n\t" \
  "v_add_u32 v163, 0x10000, v163\n\t" \
  "s_mov_b64 exec, vcc\n\t" \
  "buffer_load_dwordx4 v[28:31], v164, %[ob], s94 offen offset:3072 sc1\n\t" \
  "s_mov_b64 exec, -1\n\t" \
  "v_mfma_f32_16x16x32_bf16 v[144:147], v[112:115], v[44:47], v[144:147]\n\t" \
  "ds_read_b64_tr_b16 v[108:109], v163 offset:0\n\t" \
  "ds_read_b64_tr_b16 v[110:111], v163 offset:2048\n\t" \
  "v_mfma_f32_16x16x32_bf16 v[132:135], v[116:119], v[40:43], v[132:135]\n\t" \
  "ds_read_b64_tr_b16 v[112:113], v160 offset:4096\n\t" \
  "ds_read_b32 v166, v165\n\t" \
  "v_mfma_f32_16x16x32_bf16 v[148:151], v[116:119], v[44:47], v[148:151]\n\t" \
  "ds_read_b64_tr_b16 v[114:115], v160 offset:6144\n\t" \
  "s_waitcnt lgkmcnt(11)\n\t" \
  "v_mfma_f32_16x16x32_bf16 v[136:139], v[120:123], v[40:43], v[136:139]\n\t" \
  "ds_read_b64_tr_b16 v[116:117], v161 offset:4096\n\t" \
  "v_mfma_f32_16x16x32_bf16 v[152:155], v[120:123], v[44:47], v[152:155]\n\t" \
  "ds_read_b64_tr_b16 v[118:119], v161 offset:6144\n\t" \
  "s_sub_u32 %[iss], %[iss], vcc_lo\n\t" \
  "v_mfma_f32_16x16x32_bf16 v[140:143], v[124:127], v[40:43], v[140:143]\n\t" \
  "ds_read_b64_tr_b16 v[120:121], v162 offset:4096\n\t" \
  "v_mfma_f32_16x16x32_bf16 v[156:159], v[124:127], v[44:47], v[156:159]\n\t" \
  "ds_read_b64_tr_b16 v[122:123], v162 offset:6144\n\t" \
  "s_add_u32 %[th], %[th], 1\n\t" \
  "s_cbranch_vccz QF8_%=\n\t" \
  "s_waitcnt vmcnt(12)\n\t" \
  "QG8_%=:\n\t" \
  "s_waitcnt lgkmcnt(11)\n\t" \
  "v_mfma_f32_16x16x32_bf16 v[128:131], v[96:99], v[48:51], v[128:131]\n\t" \
  "ds_read_b64_tr_b16 v[124:125], v163 offset:4096\n\t" \
  "v_readlane_b32 s94, v167, 12\n\t" \
  "v_mfma_f32_16x16x32_bf16 v[144:147], v[96:99], v[52:55], v[144:147]\n\t" \
  "ds_read_b64_tr_b16 v[126:127], v163 offset:6144\n\t" \
  "s_waitcnt lgkmcnt(7)\n\t" \
  "v_cmp_lt_u32 vcc, %[th], v166\n\t" \
  "v_mfma_f32_16x16x32_bf16 v[132:135], v[100:103], v[48:51], v[132:135]\n\t" \
  "s_mov_b64 exec, vcc\n\t" \
  "buffer_load_dwordx4 v[32:35], v164, %[ob], s94 offen offset:0 sc1\n\t" \
  "s_mov_b64 exec, -1\n\t" \
  "v_mfma_f32_16x16x32_bf16 v[148:151], v[100:103], v[52:55], v[148:151]\n\t" \
  "ds_read_b64_tr_b16 v[96:97], v160 offset:8192\n\t" \
  "ds_read_b64_tr_b16 v[98:99], v160 offset:10240\n\t" \
  "v_mfma_f32_16x16x32_bf16 v[136:139], v[104:107], v[48:51], v[136:139]\n\t" \
  "s_mov_b64 exec, vcc\n\t" \
  "buffer_load_dwordx4 v[36:39], v164, %[ob], s94 offen offset:1024 sc1\n\t" \
  "s_mov_b64 exec, -1\n\t" \
  "v_mfma_f32_16x16x32_bf16 v[152:155], v[104:107], v[52:55], v[152:155]\n\t" \
  "ds_read_b64_tr_b16 v[100:101], v161 offset:8192\n\t" \
  "ds_read_b64_tr_b16 v[102:103], v161 offset:10240\n\t" \
  "v_mfma_f32_16x16x32_bf16 v[140:143], v[108:111], v[48:51], v[140:143]\n\t" \
  "s_mov_b64 exec, vcc\n\t" \
  "buffer_load_dwordx4 v[40:43], v164, %[ob], s94 offen offset:2048 sc1\n\t" \
  "s_mov_b64 exec, -1\n\t" \
  "v_mfma_f32_16x16x32_bf16 v[156:159], v[108:111], v[52:55], v[156:159]\n\t" \
  "ds_read_b64_tr_b16 v[104:105], v162 offset:8192\n\t" \
  "ds_read_b64_tr_b16 v[106:107], v162 offset:10240\n\t" \
  "s_waitcnt lgkmcnt(10)\n\t" \
  "v_mfma_f32_16x16x32_bf16 v[128:131], v[112:115], v[56:59], v[128:131]\n\t" \
  "s_mov_b64 exec, vcc\n\t" \
  "buffer_load_dwordx4 v[44:47], v164, %[ob], s94 offen offset:3072 sc1\n\t" \
  "s_mov_b64 exec, -1\n\t" \
  "v_mfma_f32_16x16x32_bf16 v[144:147], v[112:115], v[60:63], v[144:147]\n\t" \
  "ds_read_b64_tr_b16 v[108:109], v163 offset:8192\n\t" \
  "ds_read_b64_tr_b16 v[110:111], v163 offset:10240\n\t" \
  "v_mfma_f32_16x16x32_bf16 v[132:135], v[116:119], v[56:59], v[132:135]\n\t" \
  "ds_read_b64_tr_b16 v[112:113], v160 offset:12288\n\t" \
  "ds_read_b32 v166, v165\n\t" \
  "v_mfma_f32_16x16x32_bf16 v[148:151], v[116:119], v[60:63], v[148:151]\n\t" \
  "ds_read_b64_tr_b16 v[114:115], v160 offset:14336\n\t" \
  "s_waitcnt lgkmcnt(11)\n\t" \
  "v_mfma_f32_16x16x32_bf16 v[136:139], v[120:123], v[56:59], v[136:139]\n\t" \
  "ds_read_b64_tr_b16 v[116:117], v161 offset:12288\n\t" \
  "v_mfma_f32_16x16x32_bf16 v[152:155], v[120:123], v[60:63], v[152:155]\n\t" \
  "ds_read_b64_tr_b16 v[118:119], v161 offset:14336\n\t" \
  "s_sub_u32 %[iss], %[iss], vcc_lo\n\t" \
  "v_mfma_f32_16x16x32_bf16 v[140:143], v[124:127], v[56:59], v[140:143]\n\t" \
  "ds_read_b64_tr_b16 v[120:121], v162 offset:12288\n\t" \
  "v_mfma_f32_16x16x32_bf16 v[156:159], v[124:127], v[60:63], v[156:159]\n\t" \
  "ds_read_b64_tr_b16 v[122:123], v162 offset:14336\n\t" \
  "s_add_u32 %[th], %[th], 1\n\t" \
  "s_cbranch_vccz QF9_%=\n\t" \
  "s_waitcnt vmcnt(12)\n\t" \
  "QG9_%=:\n\t" \
  "s_waitcnt lgkmcnt(11)\n\t" \
  "v_mfma_f32_16x16x32_bf16 v[128:131], v[96:99], v[64:67], v[128:131]\n\t" \
  "ds_read_b64_tr_b16 v[124:125], v163 offset:12288\n\t" \
  "v_readlane_b32 s94, v167, 13\n\t" \
  "v_mfma_f32_16x16x32_bf16 v[144:147], v[96:99], v[68:71], v[144:147]\n\t" \
  "ds_read_b64_tr_b16 v[126:127], v163 offset:14336\n\t" \
  "s_waitcnt lgkmcnt(7)\n\t" \
  "v_cmp_lt_u32 vcc, %[th], v166\n\t" \
  "v_mfma_f32_16x16x32_bf16 v[132:135], v[100:103], v[64:67], v[132:135]\n\t" \
  "s_mov_b64 exec, vcc\n\t" \
  "buffer_load_dwordx4 v[48:51], v164, %[ob], s94 offen offset:0 sc1\n\t" \
  "s_mov_b64 exec, -1\n\t" \
  "v_mfma_f32_16x16x32_bf16 v[148:151], v[100:103], v[68:71], v[148:151]\n\t" \
  "ds_read_b64_tr_b16 v[96:97], v160 offset:16384\n\t" \
  "ds_read_b64_tr_b16 v[98:99], v160 offset:18432\n\t" \
  "v_mfma_f32_16x16x32_bf16 v[136:139], v[104:107], v[64:67], v[136:139]\n\t" \
  "s_mov_b64 exec, vcc\n\t" \
  "buffer_load_dwordx4 v[52:55], v164, %[ob], s94 offen offset:1024 sc1\n\t" \
  "s_mov_b64 exec, -1\n\t" \
  "v_mfma_f32_16x16x32_bf16 v[152:155], v[104:107], v[68:71], v[152:155]\n\t" \
  "ds_read_b64_tr_b16 v[100:101], v161 offset:16384\n\t" \
  "ds_read_b64_tr_b16 v[102:103], v161 offset:18432\n\t" \
  "v_mfma_f32_16x16x32_bf16 v[140:143], v[108:111], v[64:67], v[140:143]\n\t" \
  "s_mov_b64 exec, vcc\n\t" \
  "buffer_load_dwordx4 v[56:59], v164, %[ob], s94 offen offset:2048 sc1\n\t" \
  "s_mov_b64 exec, -1\n\t" \
  "v_mfma_f32_16x16x32_bf16 v[156:159], v[108:111], v[68:71], v[156:159]\n\t" \
  "ds_read_b64_tr_b16 v[104:105], v162 offset:16384\n\t" \
  "ds_read_b64_tr_b16 v[106:107], v162 offset:18432\n\t" \
  "s_waitcnt lgkmcnt(10)\n\t" \
  "v_mfma_f32_16x16x32_bf16 v[128:131], v[112:115], v[72:75], v[128:131]\n\t" \
  "s_mov_b64 exec, vcc\n\t" \
  "buffer_load_dwordx4 v[60:63], v164, %[ob], s94 offen offset:3072 sc1\n\t" \
  "s_mov_b64 exec, -1\n\t" \
  "v_mfma_f32_16x16x32_bf16 v[144:147], v[112:115], v[76:79], v[144:147]\n\t" \
  "ds_read_b64_tr_b16 v[108:109], v163 offset:16384\n\t" \
  "ds_read_b64_tr_b16 v[110:111], v163 offset:18432\n\t" \
  "v_mfma_f32_16x16x32_bf16 v[132:135], v[116:119], v[72:75], v[132:135]\n\t" \
  "ds_read_b64_tr_b16 v[112:113], v160 offset:20480\n\t" \
  "ds_read_b32 v166, v165\n\t" \
  "v_mfma_f32_16x16x32_bf16 v[148:151], v[116:119], v[76:79], v[148:151]\n\t" \
  "ds_read_b64_tr_b16 v[114:115], v160 offset:22528\n\t" \
  "s_waitcnt lgkmcnt(11)\n\t" \
  "v_mfma_f32_16x16x32_bf16 v[136:139], v[120:123], v[72:75], v[136:139]\n\t" \
  "ds_read_b64_tr_b16 v[116:117], v161 offset:20480\n\t" \
  "v_mfma_f32_16x16x32_bf16 v[152:155], v[120:123], v[76:79], v[152:155]\n\t" \
  "ds_read_b64_tr_b16 v[118:119], v161 offset:22528\n\t" \
  "s_sub_u32 %[iss], %[iss], vcc_lo\n\t" \
  "v_mfma_f32_16x16x32_bf16 v[140:143], v[124:127], v[72:75], v[140:143]\n\t" \
  "ds_read_b64_tr_b16 v[120:121], v162 offset:20480\n\t" \
  "v_mfma_f32_16x16x32_bf16 v[156:159], v[124:127], v[76:79], v[156:159]\n\t" \
  "ds_read_b64_tr_b16 v[122:123], v162 offset:22528\n\t" \
  "s_add_u32 %[th], %[th], 1\n\t" \
  "s_cbranch_vccz QF10_%=\n\t" \
  "s_waitcnt vmcnt(12)\n\t" \
  "QG10_%=:\n\t" \
  "s_waitcnt lgkmcnt(11)\n\t" \
  "v_mfma_f32_16x16x32_bf16 v[128:131], v[96:99], v[0:3], v[128:131]\n\t" \
  "ds_read_b64_tr_b16 v[124:125], v163 offset:20480\n\t" \
  "v_readlane_b32 s94, v167, 14\n\t" \
  "v_mfma_f32_16x16x32_bf16 v[144:147], v[96:99], v[4:7], v[144:147]\n\t" \
  "ds_read_b64_tr_b16 v[126:127], v163 offset:22528\n\t" \
  "s_waitcnt lgkmcnt(7)\n\t" \
  "v_cmp_lt_u32 vcc, %[th], v166\n\t" \
  "v_mfma_f32_16x16x32_bf16 v[132:135], v[100:103], v[0:3], v[132:135]\n\t" \
  "s_mov_b64 exec, vcc\n\t" \
  "buffer_load_dwordx4 v[64:67], v164, %[ob], s94 offen offset:0 sc1\n\t" \
  "s_mov_b64 exec, -1\n\t" \
  "v_mfma_f32_16x16x32_bf16 v[148:151], v[100:103], v[4:7], v[148:151]\n\t" \
  "ds_read_b64_tr_b16 v[96:97], v160 offset:24576\n\t" \
  "ds_read_b64_tr_b16 v[98:99], v160 offset:26624\n\t" \
  "v_mfma_f32_16x16x32_bf16 v[136:139], v[104:107], v[0:3], v[136:139]\n\t" \
  "s_mov_b64 exec, vcc\n\t" \
  "buffer_load_dwordx4 v[68:71], v164, %[ob], s94 offen offset:1024 sc1\n\t" \
  "s_mov_b64 exec, -1\n\t" \
  "v_mfma_f32_16x16x32_bf16 v[152:155], v[104:107], v[4:7], v[152:155]\n\t" \
  "ds_read_b64_tr_b16 v[100:101], v161 offset:24576\n\t" \
  "ds_read_b64_tr_b16 v[102:103], v161 offset:26624\n\t" \
  "v_mfma_f32_16x16x32_bf16 v[140:143], v[108:111], v[0:3], v[140:143]\n\t" \
  "s_mov_b64 exec, vcc\n\t" \
  "buffer_load_dwordx4 v[72:75], v164, %[ob], s94 offen offset:2048 sc1\n\t" \
  "s_mov_b64 exec, -1\n\t" \
  "v_mfma_f32_16x16x32_bf16 v[156:159], v[108:111], v[4:7], v[156:159]\n\t" \
  "ds_read_b64_tr_b16 v[104:105], v162 offset:24576\n\t" \
  "ds_read_b64_tr_b16 v[106:107], v162 offset:26624\n\t" \
  "s_waitcnt lgkmcnt(10)\n\t" \
  "v_mfma_f32_16x16x32_bf16 v[128:131], v[112:115], v[8:11], v[128:131]\n\t" \
  "s_mov_b64 exec, vcc\n\t" \
  "buffer_load_dwordx4 v[76:79], v164, %[ob], s94 offen offset:3072 sc1\n\t" \
  "s_mov_b64 exec, -1\n\t" \
  "v_mfma_f32_16x16x32_bf16 v[144:147], v[112:115], v[12:15], v[144:147]\n\t" \
  "ds_read_b64_tr_b16 v[108:109], v163 offset:24576\n\t" \
  "ds_read_b64_tr_b16 v[110:111], v163 offset:26624\n\t" \
  "v_mfma_f32_16x16x32_bf16 v[132:135], v[116:119], v[8:11], v[132:135]\n\t" \
  "ds_read_b64_tr_b16 v[112:113], v160 offset:28672\n\t" \
  "ds_read_b32 v166, v165\n\t" \
  "v_mfma_f32_16x16x32_bf16 v[148:151], v[116:119], v[12:15], v[148:151]\n\t" \
  "ds_read_b64_tr_b16 v[114:115], v160 offset:30720\n\t" \
  "s_waitcnt lgkmcnt(11)\n\t" \
  "v_mfma_f32_16x16x32_bf16 v[136:139], v[120:123], v[8:11], v[136:139]\n\t" \
  "ds_read_b64_tr_b16 v[116:117], v161 offset:28672\n\t" \
  "v_mfma_f32_16x16x32_bf16 v[152:155], v[120:123], v[12:15], v[152:155]\n\t" \
  "ds_read_b64_tr_b16 v[118:119], v161 offset:30720\n\t" \
  "s_sub_u32 %[iss], %[iss], vcc_lo\n\t" \
  "v_mfma_f32_16x16x32_bf16 v[140:143], v[124:127], v[8:11], v[140:143]\n\t" \
  "ds_read_b64_tr_b16 v[120:121], v162 offset:28672\n\t" \
  "v_mfma_f32_16x16x32_bf16 v[156:159], v[124:127], v[12:15], v[156:159]\n\t" \
  "ds_read_b64_tr_b16 v[122:123], v162 offset:30720\n\t" \
  "s_add_u32 %[th], %[th], 1\n\t" \
  "s_cbranch_vccz QF11_%=\n\t" \
  "s_waitcnt vmcnt(12)\n\t" \
  "QG11_%=:\n\t" \
  "s_waitcnt lgkmcnt(11)\n\t" \
  "v_mfma_f32_16x16x32_bf16 v[128:131], v[96:99], v[16:19], v[128:131]\n\t" \
  "ds_read_b64_tr_b16 v[124:125], v163 offset:28672\n\t" \
  "v_readlane_b32 s94, v167, 15\n\t" \
  "v_mfma_f32_16x16x32_bf16 v[144:147], v[96:99], v[20:23], v[144:147]\n\t" \
  "ds_read_b64_tr_b16 v[126:127], v163 offset:30720\n\t" \
  "s_waitcnt lgkmcnt(7)\n\t" \
  "v_cmp_lt_u32 vcc, %[th], v166\n\t" \
  "v_mfma_f32_16x16x32_bf16 v[132:135], v[100:103], v[16:19], v[132:135]\n\t" \
  "s_mov_b64 exec, vcc\n\t" \
  "buffer_load_dwordx4 v[0:3], v164, %[ob], s94 offen offset:0 sc1\n\t" \
  "s_mov_b64 exec, -1\n\t" \
  "v_mfma_f32_16x16x32_bf16 v[148:151], v[100:103], v[20:23], v[148:151]\n\t" \
  "ds_read_b64_tr_b16 v[96:97], v160 offset:32768\n\t" \
  "ds_read_b64_tr_b16 v[98:99], v160 offset:34816\n\t" \
  "v_mfma_f32_16x16x32_bf16 v[136:139], v[104:107], v[16:19], v[136:139]\n\t" \
  "s_mov_b64 exec, vcc\n\t" \
  "buffer_load_dwordx4 v[4:7], v164, %[ob], s94 offen offset:1024 sc1\n\t" \
  "s_mov_b64 exec, -1\n\t" \
  "v_mfma_f32_16x16x32_bf16 v[152:155], v[104:107], v[20:23], v[152:155]\n\t" \
  "ds_read_b64_tr_b16 v[100:101], v161 offset:32768\n\t" \
  "ds_read_b64_tr_b16 v[102:103], v161 offset:34816\n\t" \
  "v_mfma_f32_16x16x32_bf16 v[140:143], v[108:111], v[16:19], v[140:143]\n\t" \
  "s_mov_b64 exec, vcc\n\t" \
  "buffer_load_dwordx4 v[8:11], v164, %[ob], s94 offen offset:2048 sc1\n\t" \
  "s_mov_b64 exec, -1\n\t" \
  "v_mfma_f32_16x16x32_bf16 v[156:159], v[108:111], v[20:23], v[156:159]\n\t" \
  "ds_read_b64_tr_b16 v[104:105], v162 offset:32768\n\t" \
  "ds_read_b64_tr_b16 v[106:107], v162 offset:34816\n\t" \
  "s_waitcnt lgkmcnt(10)\n\t" \
  "v_mfma_f32_16x16x32_bf16 v[128:131], v[112:115], v[24:27], v[128:131]\n\t" \
  "s_mov_b64 exec, vcc\n\t" \
  "buffer_load_dwordx4 v[12:15], v164, %[ob], s94 offen offset:3072 sc1\n\t" \
  "s_mov_b64 exec, -1\n\t" \
  "v_mfma_f32_16x16x32_bf16 v[144:147], v[112:115], v[28:31], v[144:147]\n\t" \
  "ds_read_b64_tr_b16 v[108:109], v163 offset:32768\n\t" \
  "ds_read_b64_tr_b16 v[110:111], v163 offset:34816\n\t" \
  "v_mfma_f32_16x16x32_bf16 v[132:135], v[116:119], v[24:27], v[132:135]\n\t" \
  "ds_read_b64_tr_b16 v[112:113], v160 offset:36864\n\t" \
  "v_mfma_f32_16x16x32_bf16 v[148:151], v[116:119], v[28:31], v[148:151]\n\t" \
  "ds_read_b64_tr_b16 v[114:115], v160 offset:38912\n\t" \
  "s_waitcnt lgkmcnt(10)\n\t" \
  "v_mfma_f32_16x16x32_bf16 v[136:139], v[120:123], v[24:27], v[136:139]\n\t" \
  "ds_read_b64_tr_b16 v[116:117], v161 offset:36864\n\t" \
  "v_mfma_f32_16x16x32_bf16 v[152:155], v[120:123], v[28:31], v[152:155]\n\t" \
  "ds_read_b64_tr_b16 v[118:119], v161 offset:38912\n\t" \
  "s_sub_u32 %[iss], %[iss], vcc_lo\n\t" \
  "v_mfma_f32_16x16x32_bf16 v[140:143], v[124:127], v[24:27], v[140:143]\n\t" \
  "ds_read_b64_tr_b16 v[120:121], v162 offset:36864\n\t" \
  "v_mfma_f32_16x16x32_bf16 v[156:159], v[124:127], v[28:31], v[156:159]\n\t" \
  "ds_read_b64_tr_b16 v[122:123], v162 offset:38912\n\t" \
  "s_add_u32 %[th], %[th], 1\n\t" \
  "s_cbranch_vccz QF12_%=\n\t" \
  "s_waitcnt vmcnt(12)\n\t" \
  "QG12_%=:\n\t" \
  "s_waitcnt lgkmcnt(10)\n\t" \
  "v_mfma_f32_16x16x32_bf16 v[128:131], v[96:99], v[32:35], v[128:131]\n\t" \
  "ds_read_b64_tr_b16 v[124:125], v163 offset:36864\n\t" \
  "v_mfma_f32_16x16x32_bf16 v[144:147], v[96:99], v[36:39], v[144:147]\n\t" \
  "ds_read_b64_tr_b16 v[126:127], v163 offset:38912\n\t" \
  "v_mfma_f32_16x16x32_bf16 v[132:135], v[100:103], v[32:35], v[132:135]\n\t" \
  "ds_read_b64_tr_b16 v[96:97], v160 offset:40960\n\t" \
  "v_mfma_f32_16x16x32_bf16 v[148:151], v[100:103], v[36:39], v[148:151]\n\t" \
  "ds_read_b64_tr_b16 v[98:99], v160 offset:43008\n\t" \
  "s_waitcnt lgkmcnt(10)\n\t" \
  "v_mfma_f32_16x16x32_bf16 v[136:139], v[104:107], v[32:35], v[136:139]\n\t" \
  "ds_read_b64_tr_b16 v[100:101], v161 offset:40960\n\t" \
  "v_mfma_f32_16x16x32_bf16 v[152:155], v[104:107], v[36:39], v[152:155]\n\t" \
  "ds_read_b64_tr_b16 v[102:103], v161 offset:43008\n\t" \
  "v_mfma_f32_16x16x32_bf16 v[140:143], v[108:111], v[32:35], v[140:143]\n\t" \
  "ds_read_b64_tr_b16 v[104:105], v162 offset:40960\n\t" \
  "v_mfma_f32_16x16x32_bf16 v[156:159], v[108:111], v[36:39], v[156:159]\n\t" \
  "ds_read_b64_tr_b16 v[106:107], v162 offset:43008\n\t" \
  "s_waitcnt lgkmcnt(10)\n\t" \
  "v_mfma_f32_16x16x32_bf16 v[128:131], v[112:115], v[40:43], v[128:131]\n\t" \
  "ds_read_b64_tr_b16 v[108:109], v163 offset:40960\n\t" \
  "v_mfma_f32_16x16x32_bf16 v[144:147], v[112:115], v[44:47], v[144:147]\n\t" \
  "ds_read_b64_tr_b16 v[110:111], v163 offset:43008\n\t" \
  "v_mfma_f32_16x16x32_bf16 v[132:135], v[116:119], v[40:43], v[132:135]\n\t" \
  "ds_read_b64_tr_b16 v[112:113], v160 offset:45056\n\t" \
  "v_mfma_f32_16x16x32_bf16 v[148:151], v[116:119], v[44:47], v[148:151]\n\t" \
  "ds_read_b64_tr_b16 v[114:115], v160 offset:47104\n\t" \
  "s_waitcnt lgkmcnt(10)\n\t" \
  "v_mfma_f32_16x16x32_bf16 v[136:139], v[120:123], v[40:43], v[136:139]\n\t" \
  "ds_read_b64_tr_b16 v[116:117], v161 offset:45056\n\t" \
  "v_mfma_f32_16x16x32_bf16 v[152:155], v[120:123], v[44:47], v[152:155]\n\t" \
  "ds_read_b64_tr_b16 v[118:119], v161 offset:47104\n\t" \
  "v_mfma_f32_16x16x32_bf16 v[140:143], v[124:127], v[40:43], v[140:143]\n\t" \
  "ds_read_b64_tr_b16 v[120:121], v162 offset:45056\n\t" \
  "v_mfma_f32_16x16x32_bf16 v[156:159], v[124:127], v[44:47], v[156:159]\n\t" \
  "ds_read_b64_tr_b16 v[122:123], v162 offset:47104\n\t" \
  "s_cbranch_vccz QF13_%=\n\t" \
  "s_waitcnt vmcnt(8)\n\t" \
  "QG13_%=:\n\t" \
  "s_waitcnt lgkmcnt(10)\n\t" \
  "v_mfma_f32_16x16x32_bf16 v[128:131], v[96:99], v[48:51], v[128:131]\n\t" \
  "ds_read_b64_tr_b16 v[124:125], v163 offset:45056\n\t" \
  "v_mfma_f32_16x16x32_bf16 v[144:147], v[96:99], v[52:55], v[144:147]\n\t" \
  "ds_read_b64_tr_b16 v[126:127], v163 offset:47104\n\t" \
  "v_mfma_f32_16x16x32_bf16 v[132:135], v[100:103], v[48:51], v[132:135]\n\t" \
  "ds_read_b64_tr_b16 v[96:97], v160 offset:49152\n\t" \
  "v_mfma_f32_16x16x32_bf16 v[148:151], v[100:103], v[52:55], v[148:151]\n\t" \
  "ds_read_b64_tr_b16 v[98:99], v160 offset:51200\n\t" \
  "s_waitcnt lgkmcnt(10)\n\t" \
  "v_mfma_f32_16x16x32_bf16 v[136:139], v[104:107], v[48:51], v[136:139]\n\t" \
  "ds_read_b64_tr_b16 v[100:101], v161 offset:49152\n\t" \
  "v_mfma_f32_16x16x32_bf16 v[152:155], v[104:107], v[52:55], v[152:155]\n\t" \
  "ds_read_b64_tr_b16 v[102:103], v161 offset:51200\n\t" \
  "v_mfma_f32_16x16x32_bf16 v[140:143], v[108:111], v[48:51], v[140:143]\n\t" \
  "ds_read_b64_tr_b16 v[104:105], v162 offset:49152\n\t" \
  "v_mfma_f32_16x16x32_bf16 v[156:159], v[108:111], v[52:55], v[156:159]\n\t" \
  "ds_read_b64_tr_b16 v[106:107], v162 offset:51200\n\t" \
  "s_waitcnt lgkmcnt(10)\n\t" \
  "v_mfma_f32_16x16x32_bf16 v[128:131], v[112:115], v[56:59], v[128:131]\n\t" \
  "ds_read_b64_tr_b16 v[108:109], v163 offset:49152\n\t" \
  "v_mfma_f32_16x16x32_bf16 v[144:147], v[112:115], v[60:63], v[144:147]\n\t" \
  "ds_read_b64_tr_b16 v[110:111], v163 offset:51200\n\t" \
  "v_mfma_f32_16x16x32_bf16 v[132:135], v[116:119], v[56:59], v[132:135]\n\t" \
  "ds_read_b64_tr_b16 v[112:113], v160 offset:53248\n\t" \
  "v_mfma_f32_16x16x32_bf16 v[148:151], v[116:119], v[60:63], v[148:151]\n\t" \
  "ds_read_b64_tr_b16 v[114:115], v160 offset:55296\n\t" \
  "s_waitcnt lgkmcnt(10)\n\t" \
  "v_mfma_f32_16x16x32_bf16 v[136:139], v[120:123], v[56:59], v[136:139]\n\t" \
  "ds_read_b64_tr_b16 v[116:117], v161 offset:53248\n\t" \
  "v_mfma_f32_16x16x32_bf16 v[152:155], v[120:123], v[60:63], v[152:155]\n\t" \
  "ds_read_b64_tr_b16 v[118:119], v161 offset:55296\n\t" \
  "v_mfma_f32_16x16x32_bf16 v[140:143], v[124:127], v[56:59], v[140:143]\n\t" \
  "ds_read_b64_tr_b16 v[120:121], v162 offset:53248\n\t" \
  "v_mfma_f32_16x16x32_bf16 v[156:159], v[124:127], v[60:63], v[156:159]\n\t" \
  "ds_read_b64_tr_b16 v[122:123], v162 offset:55296\n\t" \
  "s_cbranch_vccz QF14_%=\n\t" \
  "s_waitcnt vmcnt(4)\n\t" \
  "QG14_%=:\n\t" \
  "s_waitcnt lgkmcnt(10)\n\t" \
  "v_mfma_f32_16x16x32_bf16 v[128:131], v[96:99], v[64:67], v[128:131]\n\t" \
  "ds_read_b64_tr_b16 v[124:125], v163 offset:53248\n\t" \
  "v_mfma_f32_16x16x32_bf16 v[144:147], v[96:99], v[68:71], v[144:147]\n\t" \
  "ds_read_b64_tr_b16 v[126:127], v163 offset:55296\n\t" \
  "v_mfma_f32_16x16x32_bf16 v[132:135], v[100:103], v[64:67], v[132:135]\n\t" \
  "ds_read_b64_tr_b16 v[96:97], v160 offset:57344\n\t" \
  "v_mfma_f32_16x16x32_bf16 v[148:151], v[100:103], v[68:71], v[148:151]\n\t" \
  "ds_read_b64_tr_b16 v[98:99], v160 offset:59392\n\t" \
  "s_waitcnt lgkmcnt(10)\n\t" \
  "v_mfma_f32_16x16x32_bf16 v[136:139], v[104:107], v[64:67], v[136:139]\n\t" \
  "ds_read_b64_tr_b16 v[100:101], v161 offset:57344\n\t" \
  "v_mfma_f32_16x16x32_bf16 v[152:155], v[104:107], v[68:71], v[152:155]\n\t" \
  "ds_read_b64_tr_b16 v[102:103], v161 offset:59392\n\t" \
  "v_mfma_f32_16x16x32_bf16 v[140:143], v[108:111], v[64:67], v[140:143]\n\t" \
  "ds_read_b64_tr_b16 v[104:105], v162 offset:57344\n\t" \
  "v_mfma_f32_16x16x32_bf16 v[156:159], v[108:111], v[68:71], v[156:159]\n\t" \
  "ds_read_b64_tr_b16 v[106:107], v162 offset:59392\n\t" \
  "s_waitcnt lgkmcnt(10)\n\t" \
  "v_mfma_f32_16x16x32_bf16 v[128:131], v[112:115], v[72:75], v[128:131]\n\t" \
  "ds_read_b64_tr_b16 v[108:109], v163 offset:57344\n\t" \
  "v_mfma_f32_16x16x32_bf16 v[144:147], v[112:115], v[76:79], v[144:147]\n\t" \
  "ds_read_b64_tr_b16 v[110:111], v163 offset:59392\n\t" \
  "v_mfma_f32_16x16x32_bf16 v[132:135], v[116:119], v[72:75], v[132:135]\n\t" \
  "ds_read_b64_tr_b16 v[112:113], v160 offset:61440\n\t" \
  "v_mfma_f32_16x16x32_bf16 v[148:151], v[116:119], v[76:79], v[148:151]\n\t" \
  "ds_read_b64_tr_b16 v[114:115], v160 offset:63488\n\t" \
  "s_waitcnt lgkmcnt(10)\n\t" \
  "v_mfma_f32_16x16x32_bf16 v[136:139], v[120:123], v[72:75], v[136:139]\n\t" \
  "ds_read_b64_tr_b16 v[116:117], v161 offset:61440\n\t" \
  "v_mfma_f32_16x16x32_bf16 v[152:155], v[120:123], v[76:79], v[152:155]\n\t" \
  "ds_read_b64_tr_b16 v[118:119], v161 offset:63488\n\t" \
  "v_mfma_f32_16x16x32_bf16 v[140:143], v[124:127], v[72:75], v[140:143]\n\t" \
  "ds_read_b64_tr_b16 v[120:121], v162 offset:61440\n\t" \
  "v_mfma_f32_16x16x32_bf16 v[156:159], v[124:127], v[76:79], v[156:159]\n\t" \
  "ds_read_b64_tr_b16 v[122:123], v162 offset:63488\n\t" \
  "s_cbranch_vccz QF15_%=\n\t" \
  "s_waitcnt vmcnt(0)\n\t" \
  "QG15_%=:\n\t" \
  "s_waitcnt lgkmcnt(10)\n\t" \
  "v_mfma_f32_16x16x32_bf16 v[128:131], v[96:99], v[0:3], v[128:131]\n\t" \
  "ds_read_b64_tr_b16 v[124:125], v163 offset:61440\n\t" \
  "v_mfma_f32_16x16x32_bf16 v[144:147], v[96:99], v[4:7], v[144:147]\n\t" \
  "ds_read_b64_tr_b16 v[126:127], v163 offset:63488\n\t" \
  "v_mfma_f32_16x16x32_bf16 v[132:135], v[100:103], v[0:3], v[132:135]\n\t" \
  "v_mfma_f32_16x16x32_bf16 v[148:151], v[100:103], v[4:7], v[148:151]\n\t" \
  "s_waitcnt lgkmcnt(8)\n\t" \
  "v_mfma_f32_16x16x32_bf16 v[136:139], v[104:107], v[0:3], v[136:139]\n\t" \
  "v_mfma_f32_16x16x32_bf16 v[152:155], v[104:107], v[4:7], v[152:155]\n\t" \
  "v_mfma_f32_16x16x32_bf16 v[140:143], v[108:111], v[0:3], v[140:143]\n\t" \
  "v_mfma_f32_16x16x32_bf16 v[156:159], v[108:111], v[4:7], v[156:159]\n\t" \
  "s_waitcnt lgkmcnt(4)\n\t" \
  "v_mfma_f32_16x16x32_bf16 v[128:131], v[112:115], v[8:11], v[128:131]\n\t" \
  "v_mfma_f32_16x16x32_bf16 v[144:147], v[112:115], v[12:15], v[144:147]\n\t" \
  "v_mfma_f32_16x16x32_bf16 v[132:135], v[116:119], v[8:11], v[132:135]\n\t" \
  "v_mfma_f32_16x16x32_bf16 v[148:151], v[116:119], v[12:15], v[148:151]\n\t" \
  "s_waitcnt lgkmcnt(0)\n\t" \
  "v_mfma_f32_16x16x32_bf16 v[136:139], v[120:123], v[8:11], v[136:139]\n\t" \
  "v_mfma_f32_16x16x32_bf16 v[152:155], v[120:123], v[12:15], v[152:155]\n\t" \
  "v_mfma_f32_16x16x32_bf16 v[140:143], v[124:127], v[8:11], v[140:143]\n\t" \
  "v_mfma_f32_16x16x32_bf16 v[156:159], v[124:127], v[12:15], v[156:159]\n\t" \
  "s_branch QX_%=\n\t" \
  "QF1_%=:\n\t" \
  "s_mov_b32 %[c], 1\n\t" \
  "s_getpc_b64 s[92:93]\n\t" \
  "s_branch QC_%=\n\t" \
  "s_branch QG1_%=\n\t" \
  "QF2_%=:\n\t" \
  "s_mov_b32 %[c], 2\n\t" \
  "s_getpc_b64 s[92:93]\n\t" \
  "s_branch QC_%=\n\t" \
  "s_branch QG2_%=\n\t" \
  "QF3_%=:\n\t" \
  "s_mov_b32 %[c], 3\n\t" \
  "s_getpc_b64 s[92:93]\n\t" \
  "s_branch QC_%=\n\t" \
  "s_branch QG3_%=\n\t" \
  "QF4_%=:\n\t" \
  "s_mov_b32 %[c], 4\n\t" \
  "s_getpc_b64 s[92:93]\n\t" \
  "s_branch QC_%=\n\t" \
  "s_branch QG4_%=\n\t" \
  "QF5_%=:\n\t" \
  "s_mov_b32 %[c], 5\n\t" \
  "s_getpc_b64 s[92:93]\n\t" \
  "s_branch QC_%=\n\t" \
  "s_branch QG5_%=\n\t" \
  "QF6_%=:\n\t" \
  "s_mov_b32 %[c], 6\n\t" \
  "s_getpc_b64 s[92:93]\n\t" \
  "s_branch QC_%=\n\t" \
  "s_branch QG6_%=\n\t" \
  "QF7_%=:\n\t" \
  "s_mov_b32 %[c], 7\n\t" \
  "s_getpc_b64 s[92:93]\n\t" \
  "s_branch QC_%=\n\t" \
  "s_branch QG7_%=\n\t" \
  "QF8_%=:\n\t" \
  "s_mov_b32 %[c], 8\n\t" \
  "s_getpc_b64 s[92:93]\n\t" \
  "s_branch QC_%=\n\t" \
  "s_branch QG8_%=\n\t" \
  "QF9_%=:\n\t" \
  "s_mov_b32 %[c], 9\n\t" \
  "s_getpc_b64 s[92:93]\n\t" \
  "s_branch QC_%=\n\t" \
  "s_branch QG9_%=\n\t" \
  "QF10_%=:\n\t" \
  "s_mov_b32 %[c], 10\n\t" \
  "s_getpc_b64 s[92:93]\n\t" \
  "s_branch QC_%=\n\t" \
  "s_branch QG10_%=\n\t" \
  "QF11_%=:\n\t" \
  "s_mov_b32 %[c], 11\n\t" \
  "s_getpc_b64 s[92:93]\n\t" \
  "s_branch QC_%=\n\t" \
  "s_branch QG11_%=\n\t" \
  "QF12_%=:\n\t" \
  "s_mov_b32 %[c], 12\n\t" \
  "s_getpc_b64 s[92:93]\n\t" \
  "s_branch QC_%=\n\t" \
  "s_branch QG12_%=\n\t" \
  "QF13_%=:\n\t" \
  "s_mov_b32 %[c], 13\n\t" \
  "s_getpc_b64 s[92:93]\n\t" \
  "s_branch QC_%=\n\t" \
  "s_branch QG13_%=\n\t" \
  "QF14_%=:\n\t" \
  "s_mov_b32 %[c], 14\n\t" \
  "s_getpc_b64 s[92:93]\n\t" \
  "s_branch QC_%=\n\t" \
  "s_branch QG14_%=\n\t" \
  "QF15_%=:\n\t" \
  "s_mov_b32 %[c], 15\n\t" \
  "s_getpc_b64 s[92:93]\n\t" \
  "s_branch QC_%=\n\t" \
  "s_branch QG15_%=\n\t" \
  "QC_%=:\n\t" \
  "s_add_u32 s92, s92, 4\n\t" \
  "s_addc_u32 s93, s93, 0\n\t" \
  "QU_%=:\n\t" \
  "ds_read_b32 v166, v165\n\t" \
  "s_max_i32 %[t1], %[c], 0\n\t" \
  "s_add_u32 %[t1], %[t1], 4\n\t" \
  "s_waitcnt lgkmcnt(0)\n\t" \
  "v_readfirstlane_b32 %[t0], v166\n\t" \
  "s_nop 0\n\t" \
  "s_min_u32 %[t1], %[t1], %[t0]\n\t" \
  "QL_%=:\n\t" \
  "s_cmp_ge_u32 %[iss], %[t1]\n\t" \
  "s_cbranch_scc1 QD_%=\n\t" \
  "s_mov_b32 %[t0], %[iss]\n\t" \
  "s_sub_u32 %[th], %[t0], 5\n\t" \
  "s_cselect_b32 %[t0], %[t0], %[th]\n\t" \
  "s_sub_u32 %[th], %[t0], 5\n\t" \
  "s_cselect_b32 %[t0], %[t0], %[th]\n\t" \
  "s_sub_u32 %[th], %[t0], 5\n\t" \
  "s_cselect_b32 %[t0], %[t0], %[th]\n\t" \
  "s_cmp_eq_u32 %[t0], 1\n\t" \
  "s_cbranch_scc1 QS1_%=\n\t" \
  "s_cmp_eq_u32 %[t0], 2\n\t" \
  "s_cbranch_scc1 QS2_%=\n\t" \
  "s_cmp_eq_u32 %[t0], 3\n\t" \
  "s_cbranch_scc1 QS3_%=\n\t" \
  "s_cmp_eq_u32 %[t0], 4\n\t" \
  "s_cbranch_scc1 QS4_%=\n\t" \
  "QS0_%=:\n\t" \
  "v_readlane_b32 s94, v167, %[iss]\n\t" \
  "s_nop 4\n\t" \
  "buffer_load_dwordx4 v[0:3], v164, %[ob], s94 offen offset:0 sc1\n\t" \
  "buffer_load_dwordx4 v[4:7], v164, %[ob], s94 offen offset:1024 sc1\n\t" \
  "buffer_load_dwordx4 v[8:11], v164, %[ob], s94 offen offset:2048 sc1\n\t" \
  "buffer_load_dwordx4 v[12:15], v164, %[ob], s94 offen offset:3072 sc1\n\t" \
  "s_add_u32 %[iss], %[iss], 1\n\t" \
  "s_cmp_ge_u32 %[iss], %[t1]\n\t" \
  "s_cbranch_scc1 QD_%=\n\t" \
  "QS1_%=:\n\t" \
  "v_readlane_b32 s94, v167, %[iss]\n\t" \
  "s_nop 4\n\t" \
  "buffer_load_dwordx4 v[16:19], v164, %[ob], s94 offen offset:0 sc1\n\t" \
  "buffer_load_dwordx4 v[20:23], v164, %[ob], s94 offen offset:1024 sc1\n\t" \
  "buffer_load_dwordx4 v[24:27], v164, %[ob], s94 offen offset:2048 sc1\n\t" \
  "buffer_load_dwordx4 v[28:31], v164, %[ob], s94 offen offset:3072 sc1\n\t" \
  "s_add_u32 %[iss], %[iss], 1\n\t" \
  "s_cmp_ge_u32 %[iss], %[t1]\n\t" \
  "s_cbranch_scc1 QD_%=\n\t" \
  "QS2_%=:\n\t" \
  "v_readlane_b32 s94, v167, %[iss]\n\t" \
  "s_nop 4\n\t" \
  "buffer_load_dwordx4 v[32:35], v164, %[ob], s94 offen offset:0 sc1\n\t" \
  "buffer_load_dwordx4 v[36:39], v164, %[ob], s94 offen offset:1024 sc1\n\t" \
  "buffer_load_dwordx4 v[40:43], v164, %[ob], s94 offen offset:2048 sc1\n\t" \
  "buffer_load_dwordx4 v[44:47], v164, %[ob], s94 offen offset:3072 sc1\n\t" \
  "s_add_u32 %[iss], %[iss], 1\n\t" \
  "s_cmp_ge_u32 %[iss], %[t1]\n\t" \
  "s_cbranch_scc1 QD_%=\n\t" \
  "QS3_%=:\n\t" \
  "v_readlane_b32 s94, v167, %[iss]\n\t" \
  "s_nop 4\n\t" \
  "buffer_load_dwordx4 v[48:51], v164, %[ob], s94 offen offset:0 sc1\n\t" \
  "buffer_load_dwordx4 v[52:55], v164, %[ob], s94 offen offset:1024 sc1\n\t" \
  "buffer_load_dwordx4 v[56:59], v164, %[ob], s94 offen offset:2048 sc1\n\t" \
  "buffer_load_dwordx4 v[60:63], v164, %[ob], s94 offen offset:3072 sc1\n\t" \
  "s_add_u32 %[iss], %[iss], 1\n\t" \
  "s_cmp_ge_u32 %[iss], %[t1]\n\t" \
  "s_cbranch_scc1 QD_%=\n\t" \
  "QS4_%=:\n\t" \
  "v_readlane_b32 s94, v167, %[iss]\n\t" \
  "s_nop 4\n\t" \
  "buffer_load_dwordx4 v[64:67], v164, %[ob], s94 offen offset:0 sc1\n\t" \
  "buffer_load_dwordx4 v[68:71], v164, %[ob], s94 offen offset:1024 sc1\n\t" \
  "buffer_load_dwordx4 v[72:75], v164, %[ob], s94 offen offset:2048 sc1\n\t" \
  "buffer_load_dwordx4 v[76:79], v164, %[ob], s94 offen offset:3072 sc1\n\t" \
  "s_add_u32 %[iss], %[iss], 1\n\t" \
  "s_cmp_ge_u32 %[iss], %[t1]\n\t" \
  "s_cbranch_scc0 QS0_%=\n\t" \
  "QD_%=:\n\t" \
  "s_cmp_lt_i32 %[c], 0\n\t" \
  "s_cbranch_scc1 QR_%=\n\t" \
  "s_cmp_gt_u32 %[iss], %[c]\n\t" \
  "s_cbranch_scc1 QW_%=\n\t" \
  "s_addk_i32 %[cn], 1\n\t" \
  "s_cmp_gt_u32 %[cn], 0x400000\n\t" \
  "s_cbranch_scc1 QT_%=\n\t" \
  "s_sleep 1\n\t" \
  "s_branch QU_%=\n\t" \
  "QT_%=:\n\t" \
  "s_add_u32 %[t1], %[c], 1\n\t" \
  "s_branch QL_%=\n\t" \
  "QW_%=:\n\t" \
  "s_cmp_eq_u32 %[iss], 16\n\t" \
  "s_cselect_b64 vcc, -1, 0\n\t" \
  "s_sub_u32 %[t0], %[iss], %[c]\n\t" \
  "s_cmp_eq_u32 %[t0], 4\n\t" \
  "s_cselect_b32 %[th], %[iss], 99\n\t" \
  "s_waitcnt vmcnt(12)\n\t" \
  "s_cmp_ge_u32 %[t0], 4\n\t" \
  "s_cbranch_scc1 QR_%=\n\t" \
  "s_waitcnt vmcnt(8)\n\t" \
  "s_cmp_ge_u32 %[t0], 3\n\t" \
  "s_cbranch_scc1 QR_%=\n\t" \
  "s_waitcnt vmcnt(4)\n\t" \
  "s_cmp_ge_u32 %[t0], 2\n\t" \
  "s_cbranch_scc1 QR_%=\n\t" \
  "s_waitcnt vmcnt(0)\n\t" \
  "QR_%=:\n\t" \
  "s_setpc_b64 s[92:93]\n\t" \
  "QX_%=:\n\t" \
  "s_nop 7\n\t"
#define G3_CLOB "v0", "v1", "v2", "v3", "v4", "v5", "v6", "v7", "v8", "v9", "v10", "v11", "v12", "v13", "v14", "v15", "v16", "v17", "v18", "v19", "v20", "v21", "v22", "v23", "v24", "v25", "v26", "v27", "v28", "v29", "v30", "v31", "v32", "v33", "v34", "v35", "v36", "v37", "v38", "v39", "v40", "v41", "v42", "v43", "v44", "v45", "v46", "v47", "v48", "v49", "v50", "v51", "v52", "v53", "v54", "v55", "v56", "v57", "v58", "v59", "v60", "v61", "v62", "v63", "v64", "v65", "v66", "v67", "v68", "v69", "v70", "v71", "v72", "v73", "v74", "v75", "v76", "v77", "v78", "v79", "v80", "v81", "v82", "v83", "v84", "v85", "v86", "v87", "v88", "v89", "v90", "v91", "v92", "v93", "v94", "v95", "v96", "v97", "v98", "v99", "v100", "v101", "v102", "v103", "v104", "v105", "v106", "v107", "v108", "v109", "v110", "v111", "v112", "v113", "v114", "v115", "v116", "v117", "v118", "v119", "v120", "v121", "v122", "v123", "v124", "v125", "v126", "v127", "v166", "s88", "s89", "s92", "s93", "s94"

#define PP_ASM \
  "v_mov_b32 v60, 0\n\t" \
  "s_mov_b32 %[t0], %[n0]\n\t" \
  "s_cmp_eq_u32 %[t0], 0\n\t" \
  "s_cbranch_scc1 PAx_%=\n\t" \
  "PA_%=:\n\t" \
  "s_barrier\n\t" \
  "s_sub_u32 %[t0], %[t0], 1\n\t" \
  "s_cmp_lg_u32 %[t0], 0\n\t" \
  "s_cbranch_scc1 PA_%=\n\t" \
  "PAx_%=:\n\t" \
  "s_mov_b32 m0, %[shd]\n\t" \
  "global_load_dword v160, v60, %[fl] sc1\n\t" \
  "global_load_lds_dwordx4 v163, %[shp]\n\t" \
  "s_mov_b32 m0, %[tgd]\n\t" \
  "s_mov_b64 exec, %[em]\n\t" \
  "global_load_lds_dword v60, %[fl] sc1\n\t" \
  "s_mov_b64 exec, -1\n\t" \
  "global_load_ushort v161, v60, %[hp]\n\t" \
  "global_load_ushort v162, v60, %[dp]\n\t" \
  "global_load_dwordx4 v[0:3], v152, %[kb0]\n\t" \
  "global_load_dwordx4 v[4:7], v153, %[kb0]\n\t" \
  "global_load_dwordx4 v[8:11], v154, %[kb0]\n\t" \
  "global_load_dwordx4 v[12:15], v155, %[kb3] offset:0\n\t" \
  "global_load_dwordx4 v[28:31], v155, %[vb0] offset:0\n\t" \
  "s_setprio 2\n\t" \
  "s_barrier\n\t" \
  "s_barrier\n\t" \
  "s_waitcnt vmcnt(5)\n\t" \
  "ds_read_b128 v[128:131], v156 offset:0\n\t" \
  "ds_read_b128 v[132:135], v156 offset:16\n\t" \
  "ds_read_b128 v[136:139], v156 offset:32\n\t" \
  "ds_read_b128 v[140:143], v156 offset:48\n\t" \
  "s_waitcnt lgkmcnt(0)\n\t" \
  "v_lshlrev_b32 v64, 16, v128\n\t" \
  "v_and_b32 v65, 0xffff0000, v128\n\t" \
  "v_lshlrev_b32 v66, 16, v129\n\t" \
  "v_and_b32 v67, 0xffff0000, v129\n\t" \
  "v_lshlrev_b32 v68, 16, v130\n\t" \
  "v_and_b32 v69, 0xffff0000, v130\n\t" \
  "v_lshlrev_b32 v70, 16, v131\n\t" \
  "v_and_b32 v71, 0xffff0000, v131\n\t" \
  "v_lshlrev_b32 v72, 16, v132\n\t" \
  "v_and_b32 v73, 0xffff0000, v132\n\t" \
  "v_lshlrev_b32 v74, 16, v133\n\t" \
  "v_and_b32 v75, 0xffff0000, v133\n\t" \
  "v_lshlrev_b32 v76, 16, v134\n\t" \
  "v_and_b32 v77, 0xffff0000, v134\n\t" \
  "v_lshlrev_b32 v78, 16, v135\n\t" \
  "v_and_b32 v79, 0xffff0000, v135\n\t" \
  "v_lshlrev_b32 v80, 16, v136\n\t" \
  "v_and_b32 v81, 0xffff0000, v136\n\t" \
  "v_lshlrev_b32 v82, 16, v137\n\t" \
  "v_and_b32 v83, 0xffff0000, v137\n\t" \
  "v_lshlrev_b32 v84, 16, v138\n\t" \
  "v_and_b32 v85, 0xffff0000, v138\n\t" \
  "v_lshlrev_b32 v86, 16, v139\n\t" \
  "v_and_b32 v87, 0xffff0000, v139\n\t" \
  "v_lshlrev_b32 v88, 16, v140\n\t" \
  "v_and_b32 v89, 0xffff0000, v140\n\t" \
  "v_lshlrev_b32 v90, 16, v141\n\t" \
  "v_and_b32 v91, 0xffff0000, v141\n\t" \
  "v_lshlrev_b32 v92, 16, v142\n\t" \
  "v_and_b32 v93, 0xffff0000, v142\n\t" \
  "v_lshlrev_b32 v94, 16, v143\n\t" \
  "v_and_b32 v95, 0xffff0000, v143\n\t" \
  "s_waitcnt vmcnt(4)\n\t" \
  "s_mov_b64 exec, %[zm]\n\t" \
  "v_mov_b32 v0, 0\n\t" \
  "v_mov_b32 v1, 0\n\t" \
  "v_mov_b32 v2, 0\n\t" \
  "v_mov_b32 v3, 0\n\t" \
  "s_mov_b64 exec, -1\n\t" \
  "v_lshlrev_b32 v128, 16, v0\n\t" \
  "v_and_b32 v129, 0xffff0000, v0\n\t" \
  "s_setprio 3\n\t" \
  "s_barrier\n\t" \
  "global_load_dwordx4 v[16:19], v155, %[kb3] offset:2048\n\t" \
  "global_load_dwordx4 v[32:35], v155, %[vb0] offset:2048\n\t" \
  "global_load_dwordx4 v[20:23], v155, %[kb5] offset:0\n\t" \
  "s_setprio 2\n\t" \
  "v_lshlrev_b32 v130, 16, v1\n\t" \
  "v_and_b32 v131, 0xffff0000, v1\n\t" \
  "v_lshlrev_b32 v132, 16, v2\n\t" \
  "v_and_b32 v133, 0xffff0000, v2\n\t" \
  "v_lshlrev_b32 v134, 16, v3\n\t" \
  "v_and_b32 v135, 0xffff0000, v3\n\t" \
  "v_mul_f32 v96, v128, v67\n\t" \
  "v_mul_f32 v97, v129, v71\n\t" \
  "v_mul_f32 v98, v130, v75\n\t" \
  "v_mul_f32 v99, v131, v79\n\t" \
  "v_mul_f32 v100, v132, v83\n\t" \
  "v_mul_f32 v101, v133, v87\n\t" \
  "v_mul_f32 v102, v134, v91\n\t" \
  "v_mul_f32 v103, v135, v95\n\t" \
  "s_waitcnt vmcnt(6)\n\t" \
  "s_mov_b64 exec, %[zm]\n\t" \
  "v_mov_b32 v4, 0\n\t" \
  "v_mov_b32 v5, 0\n\t" \
  "v_mov_b32 v6, 0\n\t" \
  "v_mov_b32 v7, 0\n\t" \
  "s_mov_b64 exec, -1\n\t" \
  "v_lshlrev_b32 v128, 16, v4\n\t" \
  "v_and_b32 v129, 0xffff0000, v4\n\t" \
  "v_lshlrev_b32 v130, 16, v5\n\t" \
  "v_and_b32 v131, 0xffff0000, v5\n\t" \
  "v_lshlrev_b32 v132, 16, v6\n\t" \
  "v_and_b32 v133, 0xffff0000, v6\n\t" \
  "v_lshlrev_b32 v134, 16, v7\n\t" \
  "v_and_b32 v135, 0xffff0000, v7\n\t" \
  "v_fmac_f32 v96, v128, v66\n\t" \
  "v_fmac_f32 v97, v129, v70\n\t" \
  "v_fmac_f32 v98, v130, v74\n\t" \
  "v_fmac_f32 v99, v131, v78\n\t" \
  "v_fmac_f32 v100, v132, v82\n\t" \
  "v_fmac_f32 v101, v133, v86\n\t" \
  "v_fmac_f32 v102, v134, v90\n\t" \
  "v_fmac_f32 v103, v135, v94\n\t" \
  "v_mul_f32 v104, v128, v67\n\t" \
  "v_mul_f32 v105, v129, v71\n\t" \
  "v_mul_f32 v106, v130, v75\n\t" \
  "v_mul_f32 v107, v131, v79\n\t" \
  "v_mul_f32 v108, v132, v83\n\t" \
  "v_mul_f32 v109, v133, v87\n\t" \
  "v_mul_f32 v110, v134, v91\n\t" \
  "v_mul_f32 v111, v135, v95\n\t" \
  "s_waitcnt vmcnt(5)\n\t" \
  "s_mov_b64 exec, %[zm]\n\t" \
  "v_mov_b32 v8, 0\n\t" \
  "v_mov_b32 v9, 0\n\t" \
  "v_mov_b32 v10, 0\n\t" \
  "v_mov_b32 v11, 0\n\t" \
  "s_mov_b64 exec, -1\n\t" \
  "s_barrier\n\t" \
  "v_lshlrev_b32 v128, 16, v8\n\t" \
  "v_and_b32 v129, 0xffff0000, v8\n\t" \
  "v_lshlrev_b32 v130, 16, v9\n\t" \
  "v_and_b32 v131, 0xffff0000, v9\n\t" \
  "v_lshlrev_b32 v132, 16, v10\n\t" \
  "v_and_b32 v133, 0xffff0000, v10\n\t" \
  "v_lshlrev_b32 v134, 16, v11\n\t" \
  "v_and_b32 v135, 0xffff0000, v11\n\t" \
  "v_fmac_f32 v96, v128, v65\n\t" \
  "v_fmac_f32 v97, v129, v69\n\t" \
  "v_fmac_f32 v98, v130, v73\n\t" \
  "v_fmac_f32 v99, v131, v77\n\t" \
  "v_fmac_f32 v100, v132, v81\n\t" \
  "v_fmac_f32 v101, v133, v85\n\t" \
  "v_fmac_f32 v102, v134, v89\n\t" \
  "v_fmac_f32 v103, v135, v93\n\t" \
  "v_fmac_f32 v104, v128, v66\n\t" \
  "v_fmac_f32 v105, v129, v70\n\t" \
  "v_fmac_f32 v106, v130, v74\n\t" \
  "v_fmac_f32 v107, v131, v78\n\t" \
  "v_fmac_f32 v108, v132, v82\n\t" \
  "v_fmac_f32 v109, v133, v86\n\t" \
  "v_fmac_f32 v110, v134, v90\n\t" \
  "v_fmac_f32 v111, v135, v94\n\t" \
  "v_mul_f32 v112, v128, v67\n\t" \
  "v_mul_f32 v113, v129, v71\n\t" \
  "v_mul_f32 v114, v130, v75\n\t" \
  "v_mul_f32 v115, v131, v79\n\t" \
  "v_mul_f32 v116, v132, v83\n\t" \
  "v_mul_f32 v117, v133, v87\n\t" \
  "v_mul_f32 v118, v134, v91\n\t" \
  "v_mul_f32 v119, v135, v95\n\t" \
  "s_waitcnt vmcnt(4)\n\t" \
  "v_lshlrev_b32 v128, 16, v12\n\t" \
  "v_and_b32 v129, 0xffff0000, v12\n\t" \
  "v_lshlrev_b32 v130, 16, v13\n\t" \
  "v_and_b32 v131, 0xffff0000, v13\n\t" \
  "v_lshlrev_b32 v132, 16, v14\n\t" \
  "v_and_b32 v133, 0xffff0000, v14\n\t" \
  "v_lshlrev_b32 v134, 16, v15\n\t" \
  "v_and_b32 v135, 0xffff0000, v15\n\t" \
  "s_setprio 3\n\t" \
  "s_barrier\n\t" \
  "global_load_dwordx4 v[36:39], v155, %[vb2] offset:0\n\t" \
  "global_load_dwordx4 v[24:27], v155, %[kb5] offset:2048\n\t" \
  "global_load_dwordx4 v[40:43], v155, %[vb2] offset:2048\n\t" \
  "s_setprio 2\n\t" \
  "v_fmac_f32 v96, v128, v64\n\t" \
  "v_fmac_f32 v97, v129, v68\n\t" \
  "v_fmac_f32 v98, v130, v72\n\t" \
  "v_fmac_f32 v99, v131, v76\n\t" \
  "v_fmac_f32 v100, v132, v80\n\t" \
  "v_fmac_f32 v101, v133, v84\n\t" \
  "v_fmac_f32 v102, v134, v88\n\t" \
  "v_fmac_f32 v103, v135, v92\n\t" \
  "v_fmac_f32 v104, v128, v65\n\t" \
  "v_fmac_f32 v105, v129, v69\n\t" \
  "v_fmac_f32 v106, v130, v73\n\t" \
  "v_fmac_f32 v107, v131, v77\n\t" \
  "v_fmac_f32 v108, v132, v81\n\t" \
  "v_fmac_f32 v109, v133, v85\n\t" \
  "v_fmac_f32 v110, v134, v89\n\t" \
  "v_fmac_f32 v111, v135, v93\n\t" \
  "v_fmac_f32 v112, v128, v66\n\t" \
  "v_fmac_f32 v113, v129, v70\n\t" \
  "v_fmac_f32 v114, v130, v74\n\t" \
  "v_fmac_f32 v115, v131, v78\n\t" \
  "v_fmac_f32 v116, v132, v82\n\t" \
  "v_fmac_f32 v117, v133, v86\n\t" \
  "v_fmac_f32 v118, v134, v90\n\t" \
  "v_fmac_f32 v119, v135, v94\n\t" \
  "v_mul_f32 v120, v128, v67\n\t" \
  "v_mul_f32 v121, v129, v71\n\t" \
  "v_mul_f32 v122, v130, v75\n\t" \
  "v_mul_f32 v123, v131, v79\n\t" \
  "v_mul_f32 v124, v132, v83\n\t" \
  "v_mul_f32 v125, v133, v87\n\t" \
  "v_mul_f32 v126, v134, v91\n\t" \
  "v_mul_f32 v127, v135, v95\n\t" \
  "s_waitcnt vmcnt(6)\n\t" \
  "v_lshlrev_b32 v128, 16, v28\n\t" \
  "v_and_b32 v129, 0xffff0000, v28\n\t" \
  "v_lshlrev_b32 v130, 16, v29\n\t" \
  "v_and_b32 v131, 0xffff0000, v29\n\t" \
  "v_lshlrev_b32 v132, 16, v30\n\t" \
  "v_and_b32 v133, 0xffff0000, v30\n\t" \
  "v_lshlrev_b32 v134, 16, v31\n\t" \
  "v_and_b32 v135, 0xffff0000, v31\n\t" \
  "s_barrier\n\t" \
  "v_mul_f32 v96, v96, v128\n\t" \
  "v_mul_f32 v97, v97, v129\n\t" \
  "v_mul_f32 v98, v98, v130\n\t" \
  "v_mul_f32 v99, v99, v131\n\t" \
  "v_mul_f32 v100, v100, v132\n\t" \
  "v_mul_f32 v101, v101, v133\n\t" \
  "v_mul_f32 v102, v102, v134\n\t" \
  "v_mul_f32 v103, v103, v135\n\t" \
  "v_cvt_pk_bf16_f32 v136, v96, v97\n\t" \
  "v_cvt_pk_bf16_f32 v137, v98, v99\n\t" \
  "v_cvt_pk_bf16_f32 v138, v100, v101\n\t" \
  "v_cvt_pk_bf16_f32 v139, v102, v103\n\t" \
  "v_mov_b32_dpp v128, v136 row_ror:8 row_mask:0xf bank_mask:0xf\n\t" \
  "v_mov_b32_dpp v129, v137 row_ror:8 row_mask:0xf bank_mask:0xf\n\t" \
  "v_mov_b32_dpp v130, v138 row_ror:8 row_mask:0xf bank_mask:0xf\n\t" \
  "v_mov_b32_dpp v131, v139 row_ror:8 row_mask:0xf bank_mask:0xf\n\t" \
  "v_perm_b32 v136, v128, v136, v157\n\t" \
  "v_perm_b32 v137, v129, v137, v157\n\t" \
  "v_perm_b32 v138, v130, v138, v157\n\t" \
  "v_perm_b32 v139, v131, v139, v157\n\t" \
  "v_permlane16_swap_b32 v136, v137\n\t" \
  "s_nop 0\n\t" \
  "v_permlane16_swap_b32 v138, v139\n\t" \
  "s_nop 1\n\t" \
  "v_permlane32_swap_b32 v136, v138\n\t" \
  "v_permlane32_swap_b32 v137, v139\n\t" \
  "s_waitcnt vmcnt(5)\n\t" \
  "v_lshlrev_b32 v128, 16, v16\n\t" \
  "v_and_b32 v129, 0xffff0000, v16\n\t" \
  "v_lshlrev_b32 v130, 16, v17\n\t" \
  "v_and_b32 v131, 0xffff0000, v17\n\t" \
  "v_lshlrev_b32 v132, 16, v18\n\t" \
  "v_and_b32 v133, 0xffff0000, v18\n\t" \
  "v_lshlrev_b32 v134, 16, v19\n\t" \
  "v_and_b32 v135, 0xffff0000, v19\n\t" \
  "v_fmac_f32 v104, v128, v64\n\t" \
  "v_fmac_f32 v105, v129, v68\n\t" \
  "v_fmac_f32 v106, v130, v72\n\t" \
  "v_fmac_f32 v107, v131, v76\n\t" \
  "v_fmac_f32 v108, v132, v80\n\t" \
  "v_fmac_f32 v109, v133, v84\n\t" \
  "s_setprio 3\n\t" \
  "s_barrier\n\t" \
  "global_store_dwordx4 v159, v[136:139], %[sb] sc1\n\t" \
  "v_add_u32 v158, 0x80, v159\n\t" \
  "v_bfi_b32 v159, %[mk], v158, v159\n\t" \
  "s_setprio 2\n\t" \
  "v_fmac_f32 v110, v134, v88\n\t" \
  "v_fmac_f32 v111, v135, v92\n\t" \
  "v_fmac_f32 v112, v128, v65\n\t" \
  "v_fmac_f32 v113, v129, v69\n\t" \
  "v_fmac_f32 v114, v130, v73\n\t" \
  "v_fmac_f32 v115, v131, v77\n\t" \
  "v_fmac_f32 v116, v132, v81\n\t" \
  "v_fmac_f32 v117, v133, v85\n\t" \
  "v_fmac_f32 v118, v134, v89\n\t" \
  "v_fmac_f32 v119, v135, v93\n\t" \
  "v_fmac_f32 v120, v128, v66\n\t" \
  "v_fmac_f32 v121, v129, v70\n\t" \
  "v_fmac_f32 v122, v130, v74\n\t" \
  "v_fmac_f32 v123, v131, v78\n\t" \
  "v_fmac_f32 v124, v132, v82\n\t" \
  "v_fmac_f32 v125, v133, v86\n\t" \
  "v_fmac_f32 v126, v134, v90\n\t" \
  "v_fmac_f32 v127, v135, v94\n\t" \
  "s_waitcnt vmcnt(5)\n\t" \
  "v_lshlrev_b32 v128, 16, v32\n\t" \
  "v_and_b32 v129, 0xffff0000, v32\n\t" \
  "v_lshlrev_b32 v130, 16, v33\n\t" \
  "v_and_b32 v131, 0xffff0000, v33\n\t" \
  "v_lshlrev_b32 v132, 16, v34\n\t" \
  "v_and_b32 v133, 0xffff0000, v34\n\t" \
  "v_lshlrev_b32 v134, 16, v35\n\t" \
  "v_and_b32 v135, 0xffff0000, v35\n\t" \
  "v_mul_f32 v104, v104, v128\n\t" \
  "v_mul_f32 v105, v105, v129\n\t" \
  "v_mul_f32 v106, v106, v130\n\t" \
  "v_mul_f32 v107, v107, v131\n\t" \
  "v_mul_f32 v108, v108, v132\n\t" \
  "v_mul_f32 v109, v109, v133\n\t" \
  "v_mul_f32 v110, v110, v134\n\t" \
  "v_mul_f32 v111, v111, v135\n\t" \
  "v_cvt_pk_bf16_f32 v140, v104, v105\n\t" \
  "v_cvt_pk_bf16_f32 v141, v106, v107\n\t" \
  "v_cvt_pk_bf16_f32 v142, v108, v109\n\t" \
  "v_cvt_pk_bf16_f32 v143, v110, v111\n\t" \
  "v_mov_b32_dpp v128, v140 row_ror:8 row_mask:0xf bank_mask:0xf\n\t" \
  "v_mov_b32_dpp v129, v141 row_ror:8 row_mask:0xf bank_mask:0xf\n\t" \
  "s_barrier\n\t" \
  "v_mov_b32_dpp v130, v142 row_ror:8 row_mask:0xf bank_mask:0xf\n\t" \
  "v_mov_b32_dpp v131, v143 row_ror:8 row_mask:0xf bank_mask:0xf\n\t" \
  "v_perm_b32 v140, v128, v140, v157\n\t" \
  "v_perm_b32 v141, v129, v141, v157\n\t" \
  "v_perm_b32 v142, v130, v142, v157\n\t" \
  "v_perm_b32 v143, v131, v143, v157\n\t" \
  "v_permlane16_swap_b32 v140, v141\n\t" \
  "s_nop 0\n\t" \
  "v_permlane16_swap_b32 v142, v143\n\t" \
  "s_nop 1\n\t" \
  "v_permlane32_swap_b32 v140, v142\n\t" \
  "v_permlane32_swap_b32 v141, v143\n\t" \
  "s_waitcnt vmcnt(4)\n\t" \
  "v_lshlrev_b32 v128, 16, v20\n\t" \
  "v_and_b32 v129, 0xffff0000, v20\n\t" \
  "v_lshlrev_b32 v130, 16, v21\n\t" \
  "v_and_b32 v131, 0xffff0000, v21\n\t" \
  "v_lshlrev_b32 v132, 16, v22\n\t" \
  "v_and_b32 v133, 0xffff0000, v22\n\t" \
  "v_lshlrev_b32 v134, 16, v23\n\t" \
  "v_and_b32 v135, 0xffff0000, v23\n\t" \
  "v_fmac_f32 v112, v128, v64\n\t" \
  "v_fmac_f32 v113, v129, v68\n\t" \
  "v_fmac_f32 v114, v130, v72\n\t" \
  "v_fmac_f32 v115, v131, v76\n\t" \
  "v_fmac_f32 v116, v132, v80\n\t" \
  "v_fmac_f32 v117, v133, v84\n\t" \
  "v_fmac_f32 v118, v134, v88\n\t" \
  "v_fmac_f32 v119, v135, v92\n\t" \
  "v_fmac_f32 v120, v128, v65\n\t" \
  "v_fmac_f32 v121, v129, v69\n\t" \
  "v_fmac_f32 v122, v130, v73\n\t" \
  "v_fmac_f32 v123, v131, v77\n\t" \
  "v_fmac_f32 v124, v132, v81\n\t" \
  "v_fmac_f32 v125, v133, v85\n\t" \
  "v_fmac_f32 v126, v134, v89\n\t" \
  "v_fmac_f32 v127, v135, v93\n\t" \
  "s_waitcnt vmcnt(3)\n\t" \
  "v_lshlrev_b32 v128, 16, v36\n\t" \
  "v_and_b32 v129, 0xffff0000, v36\n\t" \
  "v_lshlrev_b32 v130, 16, v37\n\t" \
  "v_and_b32 v131, 0xffff0000, v37\n\t" \
  "s_setprio 3\n\t" \
  "s_barrier\n\t" \
  "global_store_dwordx4 v159, v[140:143], %[sb] sc1\n\t" \
  "v_add_u32 v158, 0x80, v159\n\t" \
  "v_bfi_b32 v159, %[mk], v158, v159\n\t" \
  "s_mov_b32 m0, %[hd]\n\t" \
  "s_nop 0\n\t" \
  "global_load_lds_dwordx4 v163, %[hp] offset:0\n\t" \
  "global_load_lds_dwordx4 v163, %[hp] offset:1024\n\t" \
  "global_load_lds_dwordx4 v163, %[hp] offset:2048\n\t" \
  "global_load_lds_dwordx4 v163, %[hp] offset:3072\n\t" \
  "s_setprio 2\n\t" \
  "v_lshlrev_b32 v132, 16, v38\n\t" \
  "v_and_b32 v133, 0xffff0000, v38\n\t" \
  "v_lshlrev_b32 v134, 16, v39\n\t" \
  "v_and_b32 v135, 0xffff0000, v39\n\t" \
  "v_mul_f32 v112, v112, v128\n\t" \
  "v_mul_f32 v113, v113, v129\n\t" \
  "v_mul_f32 v114, v114, v130\n\t" \
  "v_mul_f32 v115, v115, v131\n\t" \
  "v_mul_f32 v116, v116, v132\n\t" \
  "v_mul_f32 v117, v117, v133\n\t" \
  "v_mul_f32 v118, v118, v134\n\t" \
  "v_mul_f32 v119, v119, v135\n\t" \
  "v_cvt_pk_bf16_f32 v144, v112, v113\n\t" \
  "v_cvt_pk_bf16_f32 v145, v114, v115\n\t" \
  "v_cvt_pk_bf16_f32 v146, v116, v117\n\t" \
  "v_cvt_pk_bf16_f32 v147, v118, v119\n\t" \
  "v_mov_b32_dpp v128, v144 row_ror:8 row_mask:0xf bank_mask:0xf\n\t" \
  "v_mov_b32_dpp v129, v145 row_ror:8 row_mask:0xf bank_mask:0xf\n\t" \
  "v_mov_b32_dpp v130, v146 row_ror:8 row_mask:0xf bank_mask:0xf\n\t" \
  "v_mov_b32_dpp v131, v147 row_ror:8 row_mask:0xf bank_mask:0xf\n\t" \
  "v_perm_b32 v144, v128, v144, v157\n\t" \
  "v_perm_b32 v145, v129, v145, v157\n\t" \
  "v_perm_b32 v146, v130, v146, v157\n\t" \
  "v_perm_b32 v147, v131, v147, v157\n\t" \
  "v_permlane16_swap_b32 v144, v145\n\t" \
  "s_nop 0\n\t" \
  "v_permlane16_swap_b32 v146, v147\n\t" \
  "s_nop 1\n\t" \
  "v_permlane32_swap_b32 v144, v146\n\t" \
  "v_permlane32_swap_b32 v145, v147\n\t" \
  "s_waitcnt vmcnt(7)\n\t" \
  "v_lshlrev_b32 v128, 16, v24\n\t" \
  "v_and_b32 v129, 0xffff0000, v24\n\t" \
  "v_lshlrev_b32 v130, 16, v25\n\t" \
  "v_and_b32 v131, 0xffff0000, v25\n\t" \
  "v_lshlrev_b32 v132, 16, v26\n\t" \
  "v_and_b32 v133, 0xffff0000, v26\n\t" \
  "v_lshlrev_b32 v134, 16, v27\n\t" \
  "v_and_b32 v135, 0xffff0000, v27\n\t" \
  "v_fmac_f32 v120, v128, v64\n\t" \
  "v_fmac_f32 v121, v129, v68\n\t" \
  "s_setprio 3\n\t" \
  "s_barrier\n\t" \
  "global_store_dwordx4 v159, v[144:147], %[sb] sc1\n\t" \
  "v_add_u32 v158, 0x80, v159\n\t" \
  "v_bfi_b32 v159, %[mk], v158, v159\n\t" \
  "s_setprio 2\n\t" \
  "v_fmac_f32 v122, v130, v72\n\t" \
  "v_fmac_f32 v123, v131, v76\n\t" \
  "v_fmac_f32 v124, v132, v80\n\t" \
  "v_fmac_f32 v125, v133, v84\n\t" \
  "v_fmac_f32 v126, v134, v88\n\t" \
  "v_fmac_f32 v127, v135, v92\n\t" \
  "s_waitcnt vmcnt(7)\n\t" \
  "v_lshlrev_b32 v128, 16, v40\n\t" \
  "v_and_b32 v129, 0xffff0000, v40\n\t" \
  "v_lshlrev_b32 v130, 16, v41\n\t" \
  "v_and_b32 v131, 0xffff0000, v41\n\t" \
  "v_lshlrev_b32 v132, 16, v42\n\t" \
  "v_and_b32 v133, 0xffff0000, v42\n\t" \
  "v_lshlrev_b32 v134, 16, v43\n\t" \
  "v_and_b32 v135, 0xffff0000, v43\n\t" \
  "v_mul_f32 v120, v120, v128\n\t" \
  "v_mul_f32 v121, v121, v129\n\t" \
  "v_mul_f32 v122, v122, v130\n\t" \
  "v_mul_f32 v123, v123, v131\n\t" \
  "v_mul_f32 v124, v124, v132\n\t" \
  "v_mul_f32 v125, v125, v133\n\t" \
  "v_mul_f32 v126, v126, v134\n\t" \
  "v_mul_f32 v127, v127, v135\n\t" \
  "v_cvt_pk_bf16_f32 v148, v120, v121\n\t" \
  "v_cvt_pk_bf16_f32 v149, v122, v123\n\t" \
  "v_cvt_pk_bf16_f32 v150, v124, v125\n\t" \
  "v_cvt_pk_bf16_f32 v151, v126, v127\n\t" \
  "v_mov_b32_dpp v128, v148 row_ror:8 row_mask:0xf bank_mask:0xf\n\t" \
  "v_mov_b32_dpp v129, v149 row_ror:8 row_mask:0xf bank_mask:0xf\n\t" \
  "v_mov_b32_dpp v130, v150 row_ror:8 row_mask:0xf bank_mask:0xf\n\t" \
  "v_mov_b32_dpp v131, v151 row_ror:8 row_mask:0xf bank_mask:0xf\n\t" \
  "v_perm_b32 v148, v128, v148, v157\n\t" \
  "v_perm_b32 v149, v129, v149, v157\n\t" \
  "v_perm_b32 v150, v130, v150, v157\n\t" \
  "v_perm_b32 v151, v131, v151, v157\n\t" \
  "v_permlane16_swap_b32 v148, v149\n\t" \
  "s_nop 0\n\t" \
  "v_permlane16_swap_b32 v150, v151\n\t" \
  "s_nop 1\n\t" \
  "v_permlane32_swap_b32 v148, v150\n\t" \
  "v_permlane32_swap_b32 v149, v151\n\t" \
  "s_setprio 3\n\t" \
  "s_barrier\n\t" \
  "global_store_dwordx4 v159, v[148:151], %[sb] sc1\n\t" \
  "s_setprio 2\n\t" \
  "s_waitcnt vmcnt(2)\n\t" \
  "ds_read_b64_tr_b16 v[64:65], v165 offset:32\n\t" \
  "ds_read_b64_tr_b16 v[66:67], v165 offset:0\n\t" \
  "ds_read_b64_tr_b16 v[68:69], v165 offset:288\n\t" \
  "ds_read_b64_tr_b16 v[70:71], v165 offset:256\n\t" \
  "ds_read_b64_tr_b16 v[72:73], v165 offset:544\n\t" \
  "ds_read_b64_tr_b16 v[74:75], v165 offset:512\n\t" \
  "ds_read_b64_tr_b16 v[76:77], v165 offset:800\n\t" \
  "ds_read_b64_tr_b16 v[78:79], v165 offset:768\n\t" \
  "v_mov_b32 v24, v166\n\t" \
  "v_mov_b32 v25, v167\n\t" \
  "v_mov_b32 v63, 0xc0\n\t" \
  "v_add_u32 v158, 64, v24\n\t" \
  "v_bfi_b32 v26, v63, v158, v24\n\t" \
  "v_add_u32 v158, 64, v25\n\t" \
  "v_bfi_b32 v27, v63, v158, v25\n\t" \
  "v_add_u32 v158, 64, v26\n\t" \
  "v_bfi_b32 v28, v63, v158, v26\n\t" \
  "v_add_u32 v158, 64, v27\n\t" \
  "v_bfi_b32 v29, v63, v158, v27\n\t" \
  "v_add_u32 v158, 64, v28\n\t" \
  "v_bfi_b32 v30, v63, v158, v28\n\t" \
  "v_add_u32 v158, 64, v29\n\t" \
  "v_bfi_b32 v31, v63, v158, v29\n\t" \
  "s_barrier\n\t" \
  "ds_read_b64_tr_b16 v[80:81], v165 offset:1056\n\t" \
  "ds_read_b64_tr_b16 v[82:83], v165 offset:1024\n\t" \
  "ds_read_b64_tr_b16 v[84:85], v165 offset:1312\n\t" \
  "ds_read_b64_tr_b16 v[86:87], v165 offset:1280\n\t" \
  "ds_read_b64_tr_b16 v[88:89], v165 offset:1568\n\t" \
  "ds_read_b64_tr_b16 v[90:91], v165 offset:1536\n\t" \
  "ds_read_b64_tr_b16 v[92:93], v165 offset:1824\n\t" \
  "ds_read_b64_tr_b16 v[94:95], v165 offset:1792\n\t" \
  "s_setprio 3\n\t" \
  "s_barrier\n\t" \
  "s_waitcnt vmcnt(0)\n\t" \
  "v_add_u32 v61, 1, v160\n\t" \
  "s_mov_b64 exec, 1\n\t" \
  "global_store_dword v60, v61, %[ff] sc1\n\t" \
  "s_mov_b64 exec, -1\n\t" \
  "s_setprio 2\n\t" \
  "ds_read_b64_tr_b16 v[96:97], v165 offset:2080\n\t" \
  "ds_read_b64_tr_b16 v[98:99], v165 offset:2048\n\t" \
  "ds_read_b64_tr_b16 v[100:101], v165 offset:2336\n\t" \
  "ds_read_b64_tr_b16 v[102:103], v165 offset:2304\n\t" \
  "ds_read_b64_tr_b16 v[104:105], v165 offset:2592\n\t" \
  "ds_read_b64_tr_b16 v[106:107], v165 offset:2560\n\t" \
  "ds_read_b64_tr_b16 v[108:109], v165 offset:2848\n\t" \
  "ds_read_b64_tr_b16 v[110:111], v165 offset:2816\n\t" \
  "s_barrier\n\t" \
  "ds_read_b64_tr_b16 v[112:113], v165 offset:3104\n\t" \
  "ds_read_b64_tr_b16 v[114:115], v165 offset:3072\n\t" \
  "ds_read_b64_tr_b16 v[116:117], v165 offset:3360\n\t" \
  "ds_read_b64_tr_b16 v[118:119], v165 offset:3328\n\t" \
  "ds_read_b64_tr_b16 v[120:121], v165 offset:3616\n\t" \
  "ds_read_b64_tr_b16 v[122:123], v165 offset:3584\n\t" \
  "ds_read_b64_tr_b16 v[124:125], v165 offset:3872\n\t" \
  "ds_read_b64_tr_b16 v[126:127], v165 offset:3840\n\t" \
  "s_barrier\n\t" \
  "s_waitcnt lgkmcnt(0)\n\t" \
  "v_lshlrev_b32 v61, 16, v161\n\t" \
  "v_lshlrev_b32 v62, 16, v162\n\t" \
  "v_add_f32 v61, v61, v62\n\t" \
  "v_mov_b32 v62, 0xffff\n\t" \
  "v_cvt_pk_bf16_f32 v61, v61, v61\n\t" \
  "s_mov_b64 exec, 1\n\t" \
  "v_bfi_b32 v66, v62, v61, v66\n\t" \
  "s_mov_b64 exec, 15\n\t" \
  "v_and_b32 v66, v62, v66\n\t" \
  "s_mov_b64 exec, 16\n\t" \
  "v_bfi_b32 v66, v62, v66, v61\n\t" \
  "s_mov_b64 exec, 0xff\n\t" \
  "v_mov_b32 v67, 0\n\t" \
  "s_mov_b64 exec, 0x100\n\t" \
  "v_mov_b32 v67, v61\n\t" \
  "s_mov_b64 exec, 0xf00\n\t" \
  "v_and_b32 v67, v62, v67\n\t" \
  "s_mov_b64 exec, 0x1000\n\t" \
  "v_bfi_b32 v67, v62, v67, v61\n\t" \
  "s_mov_b64 exec, -1\n\t" \
  "s_waitcnt lgkmcnt(0)\n\t"
#define PP_NB 15
#define PP_Q 0
#define PP_CLOB "v0", "v1", "v2", "v3", "v4", "v5", "v6", "v7", "v8", "v9", "v10", "v11", "v12", "v13", "v14", "v15", "v16", "v17", "v18", "v19", "v20", "v21", "v22", "v23", "v32", "v33", "v34", "v35", "v36", "v37", "v38", "v39", "v40", "v41", "v42", "v43", "v60", "v61", "v62", "v63", "v128", "v129", "v130", "v131", "v132", "v133", "v134", "v135", "v136", "v137", "v138", "v139", "v140", "v141", "v142", "v143", "v144", "v145", "v146", "v147", "v148", "v149", "v150", "v151", "v158", "v161", "v162"

#define LD_ASM1 \
  "s_add_u32 m0, s39, 49152\n\t" \
  "s_mov_b32 s68, 0x0\n\t" \
  "buffer_load_dwordx4 v5, s[44:47], s68 offen offset:0 lds\n\t" \
  "buffer_load_dwordx4 v6, s[44:47], s68 offen offset:1024 lds\n\t" \
  "buffer_load_dwordx4 v7, s[44:47], s68 offen offset:2048 lds\n\t" \
  "s_add_u32 m0, s38, 36864\n\t" \
  "s_mov_b32 s68, 0x0\n\t" \
  "buffer_load_dwordx4 v0, s[40:43], s68 offen offset:0 lds\n\t" \
  "buffer_load_dwordx4 v1, s[40:43], s68 offen offset:1024 lds\n\t" \
  "s_mov_b64 exec, 0xffff\n\t" \
  "buffer_load_dwordx4 v4, s[40:43], s68 offen offset:2048 lds\n\t" \
  "s_mov_b64 exec, -1\n\t" \
  "s_add_u32 m0, s38, 46080\n\t" \
  "s_mov_b32 s68, 0x24000\n\t" \
  "buffer_load_dwordx4 v2, s[40:43], s68 offen offset:0 lds\n\t" \
  "buffer_load_dwordx4 v3, s[40:43], s68 offen offset:1024 lds\n\t" \
  "s_mov_b64 exec, 0xffff\n\t" \
  "buffer_load_dwordx4 v4, s[40:43], s68 offen offset:2048 lds\n\t" \
  "s_mov_b64 exec, -1\n\t" \
  "s_mov_b32 m0, %[sk]\n\t" \
  "s_mov_b32 exec_hi, 0\n\t" \
  "buffer_load_dword v9, s[40:43], 0 offen lds\n\t" \
  "s_mov_b32 exec_lo, %[wm]\n\t" \
  "buffer_load_dword v10, s[44:47], 0 offen lds\n\t" \
  "s_mov_b64 exec, -1\n\t" \
  "s_add_u32 m0, s39, 61440\n\t" \
  "s_mov_b32 s68, 0x30000\n\t" \
  "buffer_load_dwordx4 v5, s[44:47], s68 offen offset:0 lds\n\t" \
  "buffer_load_dwordx4 v6, s[44:47], s68 offen offset:1024 lds\n\t" \
  "buffer_load_dwordx4 v7, s[44:47], s68 offen offset:2048 lds\n\t"
#define LD_ASM2 \
  "s_mov_b32 s85, 0x180000\n\t" \
  "s_load_dword s84, %[pw], s85\n\t" \
  "s_mov_b32 s85, 0x180800\n\t" \
  "s_load_dword s84, %[pw], s85\n\t" \
  "s_mov_b32 s85, 0x181000\n\t" \
  "s_load_dword s84, %[pw], s85\n\t" \
  "s_mov_b32 s85, 0x1b0000\n\t" \
  "s_load_dword s84, %[pw], s85\n\t" \
  "s_mov_b32 s85, 0x1b0800\n\t" \
  "s_load_dword s84, %[pw], s85\n\t" \
  "s_mov_b32 s85, 0x1b1000\n\t" \
  "s_load_dword s84, %[pw], s85\n\t" \
  "s_mov_b32 s85, 0x1e0000\n\t" \
  "s_load_dword s84, %[pw], s85\n\t" \
  "s_mov_b32 s85, 0x1e0800\n\t" \
  "s_load_dword s84, %[pw], s85\n\t" \
  "s_mov_b32 m0, %[tsk]\n\t" \
  "s_mov_b32 s85, 0x0\n\t" \
  "s_mov_b64 exec, 63\n\t" \
  "buffer_load_dword v16, %[sh], s85 offen lds\n\t" \
  "s_mov_b64 exec, -1\n\t" \
  "s_mov_b32 s68, 0x80\n\t" \
  "s_mov_b32 s69, 0x24080\n\t" \
  "s_mov_b32 s70, 0x60000\n\t" \
  "s_add_u32 m0, s38, 0\n\t" \
  "s_waitcnt vmcnt(6)\n\t" \
  "s_barrier\n\t" \
  "buffer_load_dwordx4 v0, s[40:43], s68 offen offset:0 lds\n\t" \
  "s_mov_b32 s85, 0x1e1000\n\t" \
  "s_load_dword s84, %[pw], s85\n\t" \
  "buffer_load_dwordx4 v1, s[40:43], s68 offen offset:1024 lds\n\t" \
  "s_mov_b32 s85, 0x210000\n\t" \
  "s_load_dword s84, %[pw], s85\n\t" \
  "s_mov_b64 exec, 0xffff\n\t" \
  "buffer_load_dwordx4 v4, s[40:43], s68 offen offset:2048 lds\n\t" \
  "s_add_u32 m0, s38, 9216\n\t" \
  "s_mov_b64 exec, -1\n\t" \
  "buffer_load_dwordx4 v2, s[40:43], s69 offen offset:0 lds\n\t" \
  "s_mov_b32 s85, 0x210800\n\t" \
  "s_load_dword s84, %[pw], s85\n\t" \
  "buffer_load_dwordx4 v3, s[40:43], s69 offen offset:1024 lds\n\t" \
  "s_mov_b64 exec, 0xffff\n\t" \
  "buffer_load_dwordx4 v4, s[40:43], s69 offen offset:2048 lds\n\t" \
  "s_add_u32 m0, s39, 0\n\t" \
  "s_mov_b64 exec, -1\n\t" \
  "buffer_load_dwordx4 v5, s[44:47], s70 offen offset:0 lds\n\t" \
  "buffer_load_dwordx4 v6, s[44:47], s70 offen offset:1024 lds\n\t" \
  "buffer_load_dwordx4 v7, s[44:47], s70 offen offset:2048 lds\n\t" \
  "s_mov_b32 m0, %[tsk]\n\t" \
  "s_mov_b32 s85, 0x180000\n\t" \
  "s_mov_b64 exec, 63\n\t" \
  "buffer_load_dword v16, %[sh], s85 offen lds\n\t" \
  "s_mov_b64 exec, -1\n\t" \
  "s_mov_b32 s68, 0x90000\n\t" \
  "s_mov_b32 s69, 0x100\n\t" \
  "s_mov_b32 s70, 0x24100\n\t" \
  "s_add_u32 m0, s39, 12288\n\t" \
  "s_waitcnt vmcnt(11)\n\t" \
  "s_barrier\n\t" \
  "buffer_load_dwordx4 v5, s[44:47], s68 offen offset:0 lds\n\t" \
  "s_mov_b32 s85, 0x211000\n\t" \
  "s_load_dword s84, %[pw], s85\n\t" \
  "buffer_load_dwordx4 v6, s[44:47], s68 offen offset:1024 lds\n\t" \
  "s_mov_b32 s85, 0x240000\n\t" \
  "s_load_dword s84, %[pw], s85\n\t" \
  "buffer_load_dwordx4 v7, s[44:47], s68 offen offset:2048 lds\n\t" \
  "s_mov_b32 s85, 0x240800\n\t" \
  "s_load_dword s84, %[pw], s85\n\t" \
  "s_add_u32 m0, s38, 18432\n\t" \
  "s_nop 0\n\t" \
  "buffer_load_dwordx4 v0, s[40:43], s69 offen offset:0 lds\n\t" \
  "buffer_load_dwordx4 v1, s[40:43], s69 offen offset:1024 lds\n\t" \
  "s_mov_b64 exec, 0xffff\n\t" \
  "buffer_load_dwordx4 v4, s[40:43], s69 offen offset:2048 lds\n\t" \
  "s_add_u32 m0, s38, 27648\n\t" \
  "s_mov_b64 exec, -1\n\t" \
  "buffer_load_dwordx4 v2, s[40:43], s70 offen offset:0 lds\n\t" \
  "buffer_load_dwordx4 v3, s[40:43], s70 offen offset:1024 lds\n\t" \
  "s_mov_b64 exec, 0xffff\n\t" \
  "buffer_load_dwordx4 v4, s[40:43], s70 offen offset:2048 lds\n\t" \
  "s_mov_b64 exec, -1\n\t" \
  "s_mov_b32 s68, 0xc0000\n\t" \
  "s_mov_b32 s69, 0xf0000\n\t" \
  "s_mov_b32 s70, 0x180\n\t" \
  "s_add_u32 m0, s39, 24576\n\t" \
  "s_waitcnt vmcnt(10)\n\t" \
  "s_barrier\n\t" \
  "buffer_load_dwordx4 v5, s[44:47], s68 offen offset:0 lds\n\t" \
  "s_mov_b32 s85, 0x241000\n\t" \
  "s_load_dword s84, %[pw], s85\n\t" \
  "buffer_load_dwordx4 v6, s[44:47], s68 offen offset:1024 lds\n\t" \
  "s_mov_b32 s85, 0x270000\n\t" \
  "s_load_dword s84, %[pw], s85\n\t" \
  "buffer_load_dwordx4 v7, s[44:47], s68 offen offset:2048 lds\n\t" \
  "s_mov_b32 s85, 0x270800\n\t" \
  "s_load_dword s84, %[pw], s85\n\t" \
  "s_add_u32 m0, s39, 36864\n\t" \
  "s_nop 0\n\t" \
  "buffer_load_dwordx4 v5, s[44:47], s69 offen offset:0 lds\n\t" \
  "buffer_load_dwordx4 v6, s[44:47], s69 offen offset:1024 lds\n\t" \
  "buffer_load_dwordx4 v7, s[44:47], s69 offen offset:2048 lds\n\t" \
  "s_add_u32 m0, s38, 36864\n\t" \
  "s_nop 0\n\t" \
  "buffer_load_dwordx4 v0, s[40:43], s70 offen offset:0 lds\n\t" \
  "buffer_load_dwordx4 v1, s[40:43], s70 offen offset:1024 lds\n\t" \
  "s_mov_b64 exec, 0xffff\n\t" \
  "buffer_load_dwordx4 v4, s[40:43], s70 offen offset:2048 lds\n\t" \
  "s_mov_b64 exec, -1\n\t" \
  "s_mov_b32 s68, 0x120000\n\t" \
  "s_mov_b32 s69, 0x24180\n\t" \
  "s_mov_b32 s70, 0x150000\n\t" \
  "s_add_u32 m0, s39, 49152\n\t" \
  "s_waitcnt vmcnt(15)\n\t" \
  "s_barrier\n\t" \
  "buffer_load_dwordx4 v5, s[44:47], s68 offen offset:0 lds\n\t" \
  "s_mov_b32 s85, 0x271000\n\t" \
  "s_load_dword s84, %[pw], s85\n\t" \
  "buffer_load_dwordx4 v6, s[44:47], s68 offen offset:1024 lds\n\t" \
  "s_mov_b32 s85, 0x2a0000\n\t" \
  "s_load_dword s84, %[pw], s85\n\t" \
  "buffer_load_dwordx4 v7, s[44:47], s68 offen offset:2048 lds\n\t" \
  "s_mov_b32 s85, 0x2a0800\n\t" \
  "s_load_dword s84, %[pw], s85\n\t" \
  "s_add_u32 m0, s38, 46080\n\t" \
  "s_nop 0\n\t" \
  "buffer_load_dwordx4 v2, s[40:43], s69 offen offset:0 lds\n\t" \
  "buffer_load_dwordx4 v3, s[40:43], s69 offen offset:1024 lds\n\t" \
  "s_mov_b64 exec, 0xffff\n\t" \
  "buffer_load_dwordx4 v4, s[40:43], s69 offen offset:2048 lds\n\t" \
  "s_add_u32 m0, s39, 61440\n\t" \
  "s_mov_b64 exec, -1\n\t" \
  "buffer_load_dwordx4 v5, s[44:47], s70 offen offset:0 lds\n\t" \
  "buffer_load_dwordx4 v6, s[44:47], s70 offen offset:1024 lds\n\t" \
  "buffer_load_dwordx4 v7, s[44:47], s70 offen offset:2048 lds\n\t" \
  "s_mov_b32 s68, 0x200\n\t" \
  "s_mov_b32 s69, 0x180000\n\t" \
  "s_mov_b32 s70, 0x24200\n\t" \
  "s_add_u32 m0, s38, 0\n\t" \
  "s_waitcnt vmcnt(15)\n\t" \
  "s_barrier\n\t" \
  "buffer_load_dwordx4 v0, s[40:43], s68 offen offset:0 lds\n\t" \
  "s_mov_b32 s85, 0x2a1000\n\t" \
  "s_load_dword s84, %[pw], s85\n\t" \
  "buffer_load_dwordx4 v1, s[40:43], s68 offen offset:1024 lds\n\t" \
  "s_mov_b32 s85, 0x2d0000\n\t" \
  "s_load_dword s84, %[pw], s85\n\t" \
  "s_mov_b64 exec, 0xffff\n\t" \
  "buffer_load_dwordx4 v4, s[40:43], s68 offen offset:2048 lds\n\t" \
  "s_add_u32 m0, s39, 0\n\t" \
  "s_mov_b64 exec, -1\n\t" \
  "buffer_load_dwordx4 v5, s[44:47], s69 offen offset:0 lds\n\t" \
  "s_mov_b32 s85, 0x2d0800\n\t" \
  "s_load_dword s84, %[pw], s85\n\t" \
  "buffer_load_dwordx4 v6, s[44:47], s69 offen offset:1024 lds\n\t" \
  "buffer_load_dwordx4 v7, s[44:47], s69 offen offset:2048 lds\n\t" \
  "s_add_u32 m0, s38, 9216\n\t" \
  "s_nop 0\n\t" \
  "buffer_load_dwordx4 v2, s[40:43], s70 offen offset:0 lds\n\t" \
  "buffer_load_dwordx4 v3, s[40:43], s70 offen offset:1024 lds\n\t" \
  "s_mov_b64 exec, 0xffff\n\t" \
  "buffer_load_dwordx4 v4, s[40:43], s70 offen offset:2048 lds\n\t" \
  "s_mov_b64 exec, -1\n\t" \
  "s_sleep 2\n\t" \
  "s_mov_b32 s68, 0x1b0000\n\t" \
  "s_add_u32 m0, s39, 12288\n\t" \
  "s_waitcnt vmcnt(21)\n\t" \
  "s_barrier\n\t" \
  "buffer_load_dwordx4 v5, s[44:47], s68 offen offset:0 lds\n\t" \
  "s_mov_b32 s85, 0x2d1000\n\t" \
  "s_load_dword s84, %[pw], s85\n\t" \
  "buffer_load_dwordx4 v6, s[44:47], s68 offen offset:1024 lds\n\t" \
  "s_mov_b32 s85, 0x300000\n\t" \
  "s_load_dword s84, %[pw], s85\n\t" \
  "buffer_load_dwordx4 v7, s[44:47], s68 offen offset:2048 lds\n\t" \
  "s_mov_b32 s85, 0x300800\n\t" \
  "s_load_dword s84, %[pw], s85\n\t" \
  "s_sleep 2\n\t" \
  "s_mov_b32 s68, 0x280\n\t" \
  "s_mov_b32 s69, 0x1e0000\n\t" \
  "s_mov_b32 s70, 0x24280\n\t" \
  "s_add_u32 m0, s38, 18432\n\t" \
  "s_waitcnt vmcnt(15)\n\t" \
  "s_barrier\n\t" \
  "buffer_load_dwordx4 v0, s[40:43], s68 offen offset:0 lds\n\t" \
  "s_mov_b32 s85, 0x301000\n\t" \
  "s_load_dword s84, %[pw], s85\n\t" \
  "buffer_load_dwordx4 v1, s[40:43], s68 offen offset:1024 lds\n\t" \
  "s_mov_b32 s85, 0x330000\n\t" \
  "s_load_dword s84, %[pw], s85\n\t" \
  "s_mov_b64 exec, 0xffff\n\t" \
  "buffer_load_dwordx4 v4, s[40:43], s68 offen offset:2048 lds\n\t" \
  "s_add_u32 m0, s39, 24576\n\t" \
  "s_mov_b64 exec, -1\n\t" \
  "buffer_load_dwordx4 v5, s[44:47], s69 offen offset:0 lds\n\t" \
  "s_mov_b32 s85, 0x330800\n\t" \
  "s_load_dword s84, %[pw], s85\n\t" \
  "buffer_load_dwordx4 v6, s[44:47], s69 offen offset:1024 lds\n\t" \
  "buffer_load_dwordx4 v7, s[44:47], s69 offen offset:2048 lds\n\t" \
  "s_add_u32 m0, s38, 27648\n\t" \
  "s_nop 0\n\t" \
  "buffer_load_dwordx4 v2, s[40:43], s70 offen offset:0 lds\n\t" \
  "buffer_load_dwordx4 v3, s[40:43], s70 offen offset:1024 lds\n\t" \
  "s_mov_b64 exec, 0xffff\n\t" \
  "buffer_load_dwordx4 v4, s[40:43], s70 offen offset:2048 lds\n\t" \
  "s_mov_b64 exec, -1\n\t" \
  "s_sleep 2\n\t" \
  "s_mov_b32 s68, 0x210000\n\t" \
  "s_add_u32 m0, s39, 36864\n\t" \
  "s_waitcnt vmcnt(21)\n\t" \
  "s_barrier\n\t" \
  "buffer_load_dwordx4 v5, s[44:47], s68 offen offset:0 lds\n\t" \
  "s_mov_b32 s85, 0x331000\n\t" \
  "s_load_dword s84, %[pw], s85\n\t" \
  "buffer_load_dwordx4 v6, s[44:47], s68 offen offset:1024 lds\n\t" \
  "s_mov_b32 s85, 0x360000\n\t" \
  "s_load_dword s84, %[pw], s85\n\t" \
  "buffer_load_dwordx4 v7, s[44:47], s68 offen offset:2048 lds\n\t" \
  "s_mov_b32 s85, 0x360800\n\t" \
  "s_load_dword s84, %[pw], s85\n\t" \
  "s_sleep 2\n\t" \
  "s_mov_b32 s68, 0x300\n\t" \
  "s_mov_b32 s69, 0x240000\n\t" \
  "s_add_u32 m0, s38, 36864\n\t" \
  "s_waitcnt vmcnt(15)\n\t" \
  "s_barrier\n\t" \
  "buffer_load_dwordx4 v0, s[40:43], s68 offen offset:0 lds\n\t" \
  "s_mov_b32 s85, 0x361000\n\t" \
  "s_load_dword s84, %[pw], s85\n\t" \
  "buffer_load_dwordx4 v1, s[40:43], s68 offen offset:1024 lds\n\t" \
  "s_mov_b32 s85, 0x390000\n\t" \
  "s_load_dword s84, %[pw], s85\n\t" \
  "s_mov_b64 exec, 0xffff\n\t" \
  "buffer_load_dwordx4 v4, s[40:43], s68 offen offset:2048 lds\n\t" \
  "s_add_u32 m0, s39, 49152\n\t" \
  "s_mov_b64 exec, -1\n\t" \
  "buffer_load_dwordx4 v5, s[44:47], s69 offen offset:0 lds\n\t" \
  "s_mov_b32 s85, 0x390800\n\t" \
  "s_load_dword s84, %[pw], s85\n\t" \
  "buffer_load_dwordx4 v6, s[44:47], s69 offen offset:1024 lds\n\t" \
  "buffer_load_dwordx4 v7, s[44:47], s69 offen offset:2048 lds\n\t" \
  "s_mov_b32 m0, %[tsk]\n\t" \
  "s_mov_b32 s85, 0x300000\n\t" \
  "s_mov_b64 exec, 63\n\t" \
  "buffer_load_dword v16, %[sh], s85 offen lds\n\t" \
  "s_mov_b64 exec, -1\n\t" \
  "s_sleep 2\n\t" \
  "s_mov_b32 s68, 0x24300\n\t" \
  "s_mov_b32 s69, 0x270000\n\t" \
  "s_add_u32 m0, s38, 46080\n\t" \
  "s_waitcnt vmcnt(19)\n\t" \
  "s_barrier\n\t" \
  "buffer_load_dwordx4 v2, s[40:43], s68 offen offset:0 lds\n\t" \
  "s_mov_b32 s85, 0x391000\n\t" \
  "s_load_dword s84, %[pw], s85\n\t" \
  "buffer_load_dwordx4 v3, s[40:43], s68 offen offset:1024 lds\n\t" \
  "s_mov_b32 s85, 0x3c0000\n\t" \
  "s_load_dword s84, %[pw], s85\n\t" \
  "s_mov_b64 exec, 0xffff\n\t" \
  "buffer_load_dwordx4 v4, s[40:43], s68 offen offset:2048 lds\n\t" \
  "s_add_u32 m0, s39, 61440\n\t" \
  "s_mov_b64 exec, -1\n\t" \
  "buffer_load_dwordx4 v5, s[44:47], s69 offen offset:0 lds\n\t" \
  "s_mov_b32 s85, 0x3c0800\n\t" \
  "s_load_dword s84, %[pw], s85\n\t" \
  "buffer_load_dwordx4 v6, s[44:47], s69 offen offset:1024 lds\n\t" \
  "buffer_load_dwordx4 v7, s[44:47], s69 offen offset:2048 lds\n\t" \
  "s_sleep 2\n\t" \
  "s_mov_b32 s68, 0x380\n\t" \
  "s_mov_b32 s69, 0x2a0000\n\t" \
  "s_add_u32 m0, s38, 0\n\t" \
  "s_waitcnt vmcnt(16)\n\t" \
  "s_barrier\n\t" \
  "buffer_load_dwordx4 v0, s[40:43], s68 offen offset:0 lds\n\t" \
  "s_mov_b32 s85, 0x3c1000\n\t" \
  "s_load_dword s84, %[pw], s85\n\t" \
  "buffer_load_dwordx4 v1, s[40:43], s68 offen offset:1024 lds\n\t" \
  "s_mov_b32 s85, 0x3f0000\n\t" \
  "s_load_dword s84, %[pw], s85\n\t" \
  "s_mov_b64 exec, 0xffff\n\t" \
  "buffer_load_dwordx4 v4, s[40:43], s68 offen offset:2048 lds\n\t" \
  "s_add_u32 m0, s39, 0\n\t" \
  "s_mov_b64 exec, -1\n\t" \
  "buffer_load_dwordx4 v5, s[44:47], s69 offen offset:0 lds\n\t" \
  "s_mov_b32 s85, 0x3f0800\n\t" \
  "s_load_dword s84, %[pw], s85\n\t" \
  "buffer_load_dwordx4 v6, s[44:47], s69 offen offset:1024 lds\n\t" \
  "buffer_load_dwordx4 v7, s[44:47], s69 offen offset:2048 lds\n\t" \
  "s_mov_b32 s68, 0x24380\n\t" \
  "s_mov_b32 s69, 0x2d0000\n\t" \
  "s_add_u32 m0, s38, 9216\n\t" \
  "s_waitcnt vmcnt(19)\n\t" \
  "s_barrier\n\t" \
  "buffer_load_dwordx4 v2, s[40:43], s68 offen offset:0 lds\n\t" \
  "s_mov_b32 s85, 0x3f1000\n\t" \
  "s_load_dword s84, %[pw], s85\n\t" \
  "buffer_load_dwordx4 v3, s[40:43], s68 offen offset:1024 lds\n\t" \
  "s_mov_b32 s85, 0x420000\n\t" \
  "s_load_dword s84, %[pw], s85\n\t" \
  "s_mov_b64 exec, 0xffff\n\t" \
  "buffer_load_dwordx4 v4, s[40:43], s68 offen offset:2048 lds\n\t" \
  "s_add_u32 m0, s39, 12288\n\t" \
  "s_mov_b64 exec, -1\n\t" \
  "buffer_load_dwordx4 v5, s[44:47], s69 offen offset:0 lds\n\t" \
  "s_mov_b32 s85, 0x420800\n\t" \
  "s_load_dword s84, %[pw], s85\n\t" \
  "buffer_load_dwordx4 v6, s[44:47], s69 offen offset:1024 lds\n\t" \
  "buffer_load_dwordx4 v7, s[44:47], s69 offen offset:2048 lds\n\t" \
  "s_mov_b32 s68, 0x400\n\t" \
  "s_mov_b32 s69, 0x300000\n\t" \
  "s_add_u32 m0, s38, 18432\n\t" \
  "s_waitcnt vmcnt(15)\n\t" \
  "s_barrier\n\t" \
  "buffer_load_dwordx4 v0, s[40:43], s68 offen offset:0 lds\n\t" \
  "s_mov_b32 s85, 0x421000\n\t" \
  "s_load_dword s84, %[pw], s85\n\t" \
  "buffer_load_dwordx4 v1, s[40:43], s68 offen offset:1024 lds\n\t" \
  "s_mov_b32 s85, 0x450000\n\t" \
  "s_load_dword s84, %[pw], s85\n\t" \
  "s_mov_b64 exec, 0xffff\n\t" \
  "buffer_load_dwordx4 v4, s[40:43], s68 offen offset:2048 lds\n\t" \
  "s_add_u32 m0, s39, 24576\n\t" \
  "s_mov_b64 exec, -1\n\t" \
  "buffer_load_dwordx4 v5, s[44:47], s69 offen offset:0 lds\n\t" \
  "s_mov_b32 s85, 0x450800\n\t" \
  "s_load_dword s84, %[pw], s85\n\t" \
  "buffer_load_dwordx4 v6, s[44:47], s69 offen offset:1024 lds\n\t" \
  "buffer_load_dwordx4 v7, s[44:47], s69 offen offset:2048 lds\n\t" \
  "s_mov_b32 s68, 0x24400\n\t" \
  "s_mov_b32 s69, 0x330000\n\t" \
  "s_add_u32 m0, s38, 27648\n\t" \
  "s_waitcnt vmcnt(18)\n\t" \
  "s_barrier\n\t" \
  "buffer_load_dwordx4 v2, s[40:43], s68 offen offset:0 lds\n\t" \
  "s_mov_b32 s85, 0x451000\n\t" \
  "s_load_dword s84, %[pw], s85\n\t" \
  "buffer_load_dwordx4 v3, s[40:43], s68 offen offset:1024 lds\n\t" \
  "s_mov_b32 s85, 0x480000\n\t" \
  "s_load_dword s84, %[pw], s85\n\t" \
  "s_mov_b64 exec, 0xffff\n\t" \
  "buffer_load_dwordx4 v4, s[40:43], s68 offen offset:2048 lds\n\t" \
  "s_add_u32 m0, s39, 36864\n\t" \
  "s_mov_b64 exec, -1\n\t" \
  "buffer_load_dwordx4 v5, s[44:47], s69 offen offset:0 lds\n\t" \
  "s_mov_b32 s85, 0x480800\n\t" \
  "s_load_dword s84, %[pw], s85\n\t" \
  "buffer_load_dwordx4 v6, s[44:47], s69 offen offset:1024 lds\n\t" \
  "buffer_load_dwordx4 v7, s[44:47], s69 offen offset:2048 lds\n\t" \
  "s_mov_b32 s68, 0x480\n\t" \
  "s_mov_b32 s69, 0x360000\n\t" \
  "s_add_u32 m0, s38, 36864\n\t" \
  "s_waitcnt vmcnt(15)\n\t" \
  "s_barrier\n\t" \
  "buffer_load_dwordx4 v0, s[40:43], s68 offen offset:0 lds\n\t" \
  "s_mov_b32 s85, 0x481000\n\t" \
  "s_load_dword s84, %[pw], s85\n\t" \
  "buffer_load_dwordx4 v1, s[40:43], s68 offen offset:1024 lds\n\t" \
  "s_mov_b32 s85, 0x4b0000\n\t" \
  "s_load_dword s84, %[pw], s85\n\t" \
  "s_mov_b64 exec, 0xffff\n\t" \
  "buffer_load_dwordx4 v4, s[40:43], s68 offen offset:2048 lds\n\t" \
  "s_add_u32 m0, s39, 49152\n\t" \
  "s_mov_b64 exec, -1\n\t" \
  "buffer_load_dwordx4 v5, s[44:47], s69 offen offset:0 lds\n\t" \
  "s_mov_b32 s85, 0x4b0800\n\t" \
  "s_load_dword s84, %[pw], s85\n\t" \
  "buffer_load_dwordx4 v6, s[44:47], s69 offen offset:1024 lds\n\t" \
  "buffer_load_dwordx4 v7, s[44:47], s69 offen offset:2048 lds\n\t" \
  "s_mov_b32 s68, 0x24480\n\t" \
  "s_mov_b32 s69, 0x390000\n\t" \
  "s_add_u32 m0, s38, 46080\n\t" \
  "s_waitcnt vmcnt(18)\n\t" \
  "s_barrier\n\t" \
  "buffer_load_dwordx4 v2, s[40:43], s68 offen offset:0 lds\n\t" \
  "s_mov_b32 s85, 0x4b1000\n\t" \
  "s_load_dword s84, %[pw], s85\n\t" \
  "buffer_load_dwordx4 v3, s[40:43], s68 offen offset:1024 lds\n\t" \
  "s_mov_b32 s85, 0x4e0000\n\t" \
  "s_load_dword s84, %[pw], s85\n\t" \
  "s_mov_b64 exec, 0xffff\n\t" \
  "buffer_load_dwordx4 v4, s[40:43], s68 offen offset:2048 lds\n\t" \
  "s_add_u32 m0, s39, 61440\n\t" \
  "s_mov_b64 exec, -1\n\t" \
  "buffer_load_dwordx4 v5, s[44:47], s69 offen offset:0 lds\n\t" \
  "s_mov_b32 s85, 0x4e0800\n\t" \
  "s_load_dword s84, %[pw], s85\n\t" \
  "buffer_load_dwordx4 v6, s[44:47], s69 offen offset:1024 lds\n\t" \
  "buffer_load_dwordx4 v7, s[44:47], s69 offen offset:2048 lds\n\t" \
  "s_mov_b32 s68, 0x500\n\t" \
  "s_mov_b32 s69, 0x3c0000\n\t" \
  "s_add_u32 m0, s38, 0\n\t" \
  "s_waitcnt vmcnt(15)\n\t" \
  "s_barrier\n\t" \
  "buffer_load_dwordx4 v0, s[40:43], s68 offen offset:0 lds\n\t" \
  "s_mov_b32 s85, 0x4e1000\n\t" \
  "s_load_dword s84, %[pw], s85\n\t" \
  "buffer_load_dwordx4 v1, s[40:43], s68 offen offset:1024 lds\n\t" \
  "s_mov_b32 s85, 0x510000\n\t" \
  "s_load_dword s84, %[pw], s85\n\t" \
  "s_mov_b64 exec, 0xffff\n\t" \
  "buffer_load_dwordx4 v4, s[40:43], s68 offen offset:2048 lds\n\t" \
  "s_add_u32 m0, s39, 0\n\t" \
  "s_mov_b64 exec, -1\n\t" \
  "buffer_load_dwordx4 v5, s[44:47], s69 offen offset:0 lds\n\t" \
  "s_mov_b32 s85, 0x510800\n\t" \
  "s_load_dword s84, %[pw], s85\n\t" \
  "buffer_load_dwordx4 v6, s[44:47], s69 offen offset:1024 lds\n\t" \
  "buffer_load_dwordx4 v7, s[44:47], s69 offen offset:2048 lds\n\t" \
  "s_mov_b32 m0, %[tsk]\n\t" \
  "s_mov_b32 s85, 0x480000\n\t" \
  "s_mov_b64 exec, 63\n\t" \
  "buffer_load_dword v16, %[sh], s85 offen lds\n\t" \
  "s_mov_b64 exec, -1\n\t" \
  "s_mov_b32 s68, 0x24500\n\t" \
  "s_mov_b32 s69, 0x3f0000\n\t" \
  "s_add_u32 m0, s38, 9216\n\t" \
  "s_waitcnt vmcnt(19)\n\t" \
  "s_barrier\n\t" \
  "buffer_load_dwordx4 v2, s[40:43], s68 offen offset:0 lds\n\t" \
  "s_mov_b32 s85, 0x511000\n\t" \
  "s_load_dword s84, %[pw], s85\n\t" \
  "buffer_load_dwordx4 v3, s[40:43], s68 offen offset:1024 lds\n\t" \
  "s_mov_b32 s85, 0x540000\n\t" \
  "s_load_dword s84, %[pw], s85\n\t" \
  "s_mov_b64 exec, 0xffff\n\t" \
  "buffer_load_dwordx4 v4, s[40:43], s68 offen offset:2048 lds\n\t" \
  "s_add_u32 m0, s39, 12288\n\t" \
  "s_mov_b64 exec, -1\n\t" \
  "buffer_load_dwordx4 v5, s[44:47], s69 offen offset:0 lds\n\t" \
  "s_mov_b32 s85, 0x540800\n\t" \
  "s_load_dword s84, %[pw], s85\n\t" \
  "buffer_load_dwordx4 v6, s[44:47], s69 offen offset:1024 lds\n\t" \
  "buffer_load_dwordx4 v7, s[44:47], s69 offen offset:2048 lds\n\t" \
  "s_mov_b32 m0, %[tsk]\n\t" \
  "s_mov_b64 exec, %[tm]\n\t" \
  "global_load_lds_dword v12, %[tp]\n\t" \
  "s_mov_b64 exec, -1\n\t" \
  "s_mov_b32 s68, 0x580\n\t" \
  "s_mov_b32 s69, 0x420000\n\t" \
  "s_add_u32 m0, s38, 18432\n\t" \
  "s_waitcnt vmcnt(16)\n\t" \
  "s_barrier\n\t" \
  "buffer_load_dwordx4 v0, s[40:43], s68 offen offset:0 lds\n\t" \
  "s_mov_b32 s85, 0x541000\n\t" \
  "s_load_dword s84, %[pw], s85\n\t" \
  "buffer_load_dwordx4 v1, s[40:43], s68 offen offset:1024 lds\n\t" \
  "s_mov_b32 s85, 0x570000\n\t" \
  "s_load_dword s84, %[pw], s85\n\t" \
  "s_mov_b64 exec, 0xffff\n\t" \
  "buffer_load_dwordx4 v4, s[40:43], s68 offen offset:2048 lds\n\t" \
  "s_add_u32 m0, s39, 24576\n\t" \
  "s_mov_b64 exec, -1\n\t" \
  "buffer_load_dwordx4 v5, s[44:47], s69 offen offset:0 lds\n\t" \
  "s_mov_b32 s85, 0x570800\n\t" \
  "s_load_dword s84, %[pw], s85\n\t" \
  "buffer_load_dwordx4 v6, s[44:47], s69 offen offset:1024 lds\n\t" \
  "buffer_load_dwordx4 v7, s[44:47], s69 offen offset:2048 lds\n\t" \
  "s_mov_b32 s68, 0x24580\n\t" \
  "s_mov_b32 s69, 0x450000\n\t" \
  "s_add_u32 m0, s38, 27648\n\t" \
  "s_waitcnt vmcnt(19)\n\t" \
  "s_barrier\n\t" \
  "buffer_load_dwordx4 v2, s[40:43], s68 offen offset:0 lds\n\t" \
  "s_mov_b32 s85, 0x571000\n\t" \
  "s_load_dword s84, %[pw], s85\n\t" \
  "buffer_load_dwordx4 v3, s[40:43], s68 offen offset:1024 lds\n\t" \
  "s_mov_b32 s85, 0x5a0000\n\t" \
  "s_load_dword s84, %[pw], s85\n\t" \
  "s_mov_b64 exec, 0xffff\n\t" \
  "buffer_load_dwordx4 v4, s[40:43], s68 offen offset:2048 lds\n\t" \
  "s_add_u32 m0, s39, 36864\n\t" \
  "s_mov_b64 exec, -1\n\t" \
  "buffer_load_dwordx4 v5, s[44:47], s69 offen offset:0 lds\n\t" \
  "s_mov_b32 s85, 0x5a0800\n\t" \
  "s_load_dword s84, %[pw], s85\n\t" \
  "buffer_load_dwordx4 v6, s[44:47], s69 offen offset:1024 lds\n\t" \
  "buffer_load_dwordx4 v7, s[44:47], s69 offen offset:2048 lds\n\t" \
  "s_mov_b32 s68, 0x600\n\t" \
  "s_mov_b32 s69, 0x480000\n\t" \
  "s_add_u32 m0, s38, 36864\n\t" \
  "s_waitcnt vmcnt(15)\n\t" \
  "s_barrier\n\t" \
  "buffer_load_dwordx4 v0, s[40:43], s68 offen offset:0 lds\n\t" \
  "s_mov_b32 s85, 0x5a1000\n\t" \
  "s_load_dword s84, %[pw], s85\n\t" \
  "buffer_load_dwordx4 v1, s[40:43], s68 offen offset:1024 lds\n\t" \
  "s_mov_b32 s85, 0x5d0000\n\t" \
  "s_load_dword s84, %[pw], s85\n\t" \
  "s_mov_b64 exec, 0xffff\n\t" \
  "buffer_load_dwordx4 v4, s[40:43], s68 offen offset:2048 lds\n\t" \
  "s_add_u32 m0, s39, 49152\n\t" \
  "s_mov_b64 exec, -1\n\t" \
  "buffer_load_dwordx4 v5, s[44:47], s69 offen offset:0 lds\n\t" \
  "s_mov_b32 s85, 0x5d0800\n\t" \
  "s_load_dword s84, %[pw], s85\n\t" \
  "buffer_load_dwordx4 v6, s[44:47], s69 offen offset:1024 lds\n\t" \
  "buffer_load_dwordx4 v7, s[44:47], s69 offen offset:2048 lds\n\t" \
  "s_mov_b32 s68, 0x24600\n\t" \
  "s_mov_b32 s69, 0x4b0000\n\t" \
  "s_add_u32 m0, s38, 46080\n\t" \
  "s_waitcnt vmcnt(18)\n\t" \
  "s_barrier\n\t" \
  "buffer_load_dwordx4 v2, s[40:43], s68 offen offset:0 lds\n\t" \
  "s_mov_b32 s85, 0x5d1000\n\t" \
  "s_load_dword s84, %[pw], s85\n\t" \
  "buffer_load_dwordx4 v3, s[40:43], s68 offen offset:1024 lds\n\t" \
  "s_load_dword s84, %[po], 0x0\n\t" \
  "s_mov_b64 exec, 0xffff\n\t" \
  "buffer_load_dwordx4 v4, s[40:43], s68 offen offset:2048 lds\n\t" \
  "s_add_u32 m0, s39, 61440\n\t" \
  "s_mov_b64 exec, -1\n\t" \
  "buffer_load_dwordx4 v5, s[44:47], s69 offen offset:0 lds\n\t" \
  "s_load_dword s84, %[po], 0x40\n\t" \
  "buffer_load_dwordx4 v6, s[44:47], s69 offen offset:1024 lds\n\t" \
  "buffer_load_dwordx4 v7, s[44:47], s69 offen offset:2048 lds\n\t" \
  "s_mov_b32 s68, 0x680\n\t" \
  "s_mov_b32 s69, 0x4e0000\n\t" \
  "s_add_u32 m0, s38, 0\n\t" \
  "s_waitcnt vmcnt(15)\n\t" \
  "s_barrier\n\t" \
  "buffer_load_dwordx4 v0, s[40:43], s68 offen offset:0 lds\n\t" \
  "s_load_dword s84, %[po], 0x80\n\t" \
  "buffer_load_dwordx4 v1, s[40:43], s68 offen offset:1024 lds\n\t" \
  "s_load_dword s84, %[po], 0xc0\n\t" \
  "s_mov_b64 exec, 0xffff\n\t" \
  "buffer_load_dwordx4 v4, s[40:43], s68 offen offset:2048 lds\n\t" \
  "s_add_u32 m0, s39, 0\n\t" \
  "s_mov_b64 exec, -1\n\t" \
  "buffer_load_dwordx4 v5, s[44:47], s69 offen offset:0 lds\n\t" \
  "s_load_dword s84, %[po], 0x100\n\t" \
  "buffer_load_dwordx4 v6, s[44:47], s69 offen offset:1024 lds\n\t" \
  "buffer_load_dwordx4 v7, s[44:47], s69 offen offset:2048 lds\n\t" \
  "s_mov_b32 s68, 0x24680\n\t" \
  "s_mov_b32 s69, 0x510000\n\t" \
  "s_add_u32 m0, s38, 9216\n\t" \
  "s_waitcnt vmcnt(18)\n\t" \
  "s_barrier\n\t" \
  "buffer_load_dwordx4 v2, s[40:43], s68 offen offset:0 lds\n\t" \
  "s_load_dword s84, %[po], 0x140\n\t" \
  "buffer_load_dwordx4 v3, s[40:43], s68 offen offset:1024 lds\n\t" \
  "s_load_dword s84, %[po], 0x180\n\t" \
  "s_mov_b64 exec, 0xffff\n\t" \
  "buffer_load_dwordx4 v4, s[40:43], s68 offen offset:2048 lds\n\t" \
  "s_add_u32 m0, s39, 12288\n\t" \
  "s_mov_b64 exec, -1\n\t" \
  "buffer_load_dwordx4 v5, s[44:47], s69 offen offset:0 lds\n\t" \
  "s_load_dword s84, %[po], 0x1c0\n\t" \
  "buffer_load_dwordx4 v6, s[44:47], s69 offen offset:1024 lds\n\t" \
  "buffer_load_dwordx4 v7, s[44:47], s69 offen offset:2048 lds\n\t" \
  "s_mov_b32 s68, 0x700\n\t" \
  "s_mov_b32 s69, 0x540000\n\t" \
  "s_add_u32 m0, s38, 18432\n\t" \
  "s_waitcnt vmcnt(15)\n\t" \
  "s_barrier\n\t" \
  "buffer_load_dwordx4 v0, s[40:43], s68 offen offset:0 lds\n\t" \
  "s_load_dword s84, %[po], 0x200\n\t" \
  "buffer_load_dwordx4 v1, s[40:43], s68 offen offset:1024 lds\n\t" \
  "s_load_dword s84, %[po], 0x240\n\t" \
  "s_mov_b64 exec, 0xffff\n\t" \
  "buffer_load_dwordx4 v4, s[40:43], s68 offen offset:2048 lds\n\t" \
  "s_add_u32 m0, s39, 24576\n\t" \
  "s_mov_b64 exec, -1\n\t" \
  "buffer_load_dwordx4 v5, s[44:47], s69 offen offset:0 lds\n\t" \
  "s_load_dword s84, %[po], 0x280\n\t" \
  "buffer_load_dwordx4 v6, s[44:47], s69 offen offset:1024 lds\n\t" \
  "buffer_load_dwordx4 v7, s[44:47], s69 offen offset:2048 lds\n\t" \
  "s_mov_b32 s68, 0x24700\n\t" \
  "s_mov_b32 s69, 0x570000\n\t" \
  "s_add_u32 m0, s38, 27648\n\t" \
  "s_waitcnt vmcnt(18)\n\t" \
  "s_barrier\n\t" \
  "buffer_load_dwordx4 v2, s[40:43], s68 offen offset:0 lds\n\t" \
  "s_load_dword s84, %[po], 0x2c0\n\t" \
  "buffer_load_dwordx4 v3, s[40:43], s68 offen offset:1024 lds\n\t" \
  "s_load_dword s84, %[po], 0x300\n\t" \
  "s_mov_b64 exec, 0xffff\n\t" \
  "buffer_load_dwordx4 v4, s[40:43], s68 offen offset:2048 lds\n\t" \
  "s_add_u32 m0, s39, 36864\n\t" \
  "s_mov_b64 exec, -1\n\t" \
  "buffer_load_dwordx4 v5, s[44:47], s69 offen offset:0 lds\n\t" \
  "s_load_dword s84, %[po], 0x340\n\t" \
  "buffer_load_dwordx4 v6, s[44:47], s69 offen offset:1024 lds\n\t" \
  "buffer_load_dwordx4 v7, s[44:47], s69 offen offset:2048 lds\n\t" \
  "s_mov_b32 s68, 0x780\n\t" \
  "s_mov_b32 s69, 0x5a0000\n\t" \
  "s_add_u32 m0, s38, 36864\n\t" \
  "s_waitcnt vmcnt(15)\n\t" \
  "s_barrier\n\t" \
  "buffer_load_dwordx4 v0, s[40:43], s68 offen offset:0 lds\n\t" \
  "s_load_dword s84, %[po], 0x380\n\t" \
  "buffer_load_dwordx4 v1, s[40:43], s68 offen offset:1024 lds\n\t" \
  "s_load_dword s84, %[po], 0x3c0\n\t" \
  "s_mov_b64 exec, 0xffff\n\t" \
  "buffer_load_dwordx4 v4, s[40:43], s68 offen offset:2048 lds\n\t" \
  "s_add_u32 m0, s39, 49152\n\t" \
  "s_mov_b64 exec, -1\n\t" \
  "buffer_load_dwordx4 v5, s[44:47], s69 offen offset:0 lds\n\t" \
  "s_load_dword s84, %[po], 0x400\n\t" \
  "buffer_load_dwordx4 v6, s[44:47], s69 offen offset:1024 lds\n\t" \
  "buffer_load_dwordx4 v7, s[44:47], s69 offen offset:2048 lds\n\t" \
  "s_mov_b32 s68, 0x24780\n\t" \
  "s_mov_b32 s69, 0x5d0000\n\t" \
  "s_add_u32 m0, s38, 46080\n\t" \
  "s_waitcnt vmcnt(18)\n\t" \
  "s_barrier\n\t" \
  "buffer_load_dwordx4 v2, s[40:43], s68 offen offset:0 lds\n\t" \
  "s_load_dword s84, %[po], 0x440\n\t" \
  "buffer_load_dwordx4 v3, s[40:43], s68 offen offset:1024 lds\n\t" \
  "s_load_dword s84, %[po], 0x480\n\t" \
  "s_mov_b64 exec, 0xffff\n\t" \
  "buffer_load_dwordx4 v4, s[40:43], s68 offen offset:2048 lds\n\t" \
  "s_add_u32 m0, s39, 61440\n\t" \
  "s_mov_b64 exec, -1\n\t" \
  "buffer_load_dwordx4 v5, s[44:47], s69 offen offset:0 lds\n\t" \
  "s_load_dword s84, %[po], 0x4c0\n\t" \
  "buffer_load_dwordx4 v6, s[44:47], s69 offen offset:1024 lds\n\t" \
  "buffer_load_dwordx4 v7, s[44:47], s69 offen offset:2048 lds\n\t" \
  "v_readlane_b32 s68, v9, 0\n\t" \
  "v_readlane_b32 s69, v9, 1\n\t" \
  "v_readlane_b32 s70, v9, 2\n\t" \
  "v_readlane_b32 s71, v9, 18\n\t" \
  "v_readlane_b32 s72, v9, 19\n\t" \
  "s_add_u32 m0, %[ob], 0\n\t" \
  "s_waitcnt vmcnt(15)\n\t" \
  "s_barrier\n\t" \
  "s_nop 2\n\t" \
  "buffer_load_dwordx4 v8, %[so], s68 offen lds\n\t" \
  "s_load_dword s84, %[po], 0x500\n\t" \
  "s_add_u32 m0, %[ob], 4096\n\t" \
  "s_nop 0\n\t" \
  "buffer_load_dwordx4 v8, %[so], s69 offen lds\n\t" \
  "s_load_dword s84, %[po], 0x540\n\t" \
  "s_add_u32 m0, %[ob], 8192\n\t" \
  "s_nop 0\n\t" \
  "buffer_load_dwordx4 v8, %[so], s70 offen lds\n\t" \
  "s_load_dword s84, %[po], 0x580\n\t" \
  "s_add_u32 m0, %[ob], 73728\n\t" \
  "s_nop 0\n\t" \
  "buffer_load_dwordx4 v8, %[so], s71 offen lds\n\t" \
  "s_add_u32 m0, %[ob], 77824\n\t" \
  "s_nop 0\n\t" \
  "buffer_load_dwordx4 v8, %[so], s72 offen lds\n\t" \
  "v_readlane_b32 s68, v9, 3\n\t" \
  "v_readlane_b32 s69, v9, 4\n\t" \
  "v_readlane_b32 s70, v9, 5\n\t" \
  "v_readlane_b32 s71, v9, 20\n\t" \
  "v_readlane_b32 s72, v9, 21\n\t" \
  "s_add_u32 m0, %[ob], 12288\n\t" \
  "s_waitcnt vmcnt(17)\n\t" \
  "s_barrier\n\t" \
  "s_nop 2\n\t" \
  "buffer_load_dwordx4 v8, %[so], s68 offen lds\n\t" \
  "s_load_dword s84, %[po], 0x5c0\n\t" \
  "s_add_u32 m0, %[ob], 16384\n\t" \
  "s_nop 0\n\t" \
  "buffer_load_dwordx4 v8, %[so], s69 offen lds\n\t" \
  "s_load_dword s84, %[po], 0x600\n\t" \
  "s_add_u32 m0, %[ob], 20480\n\t" \
  "s_nop 0\n\t" \
  "buffer_load_dwordx4 v8, %[so], s70 offen lds\n\t" \
  "s_load_dword s84, %[po], 0x640\n\t" \
  "s_add_u32 m0, %[ob], 81920\n\t" \
  "s_nop 0\n\t" \
  "buffer_load_dwordx4 v8, %[so], s71 offen lds\n\t" \
  "s_add_u32 m0, %[ob], 86016\n\t" \
  "s_nop 0\n\t" \
  "buffer_load_dwordx4 v8, %[so], s72 offen lds\n\t" \
  "v_readlane_b32 s68, v9, 6\n\t" \
  "v_readlane_b32 s69, v9, 7\n\t" \
  "v_readlane_b32 s70, v9, 8\n\t" \
  "v_readlane_b32 s71, v9, 22\n\t" \
  "v_readlane_b32 s72, v9, 23\n\t" \
  "v_readlane_b32 s73, v9, 24\n\t" \
  "s_add_u32 m0, %[ob], 24576\n\t" \
  "s_waitcnt vmcnt(13)\n\t" \
  "s_barrier\n\t" \
  "s_nop 2\n\t" \
  "buffer_load_dwordx4 v8, %[so], s68 offen lds\n\t" \
  "s_load_dword s84, %[po], 0x680\n\t" \
  "s_add_u32 m0, %[ob], 28672\n\t" \
  "s_nop 0\n\t" \
  "buffer_load_dwordx4 v8, %[so], s69 offen lds\n\t" \
  "s_load_dword s84, %[po], 0x6c0\n\t" \
  "s_add_u32 m0, %[ob], 32768\n\t" \
  "s_nop 0\n\t" \
  "buffer_load_dwordx4 v8, %[so], s70 offen lds\n\t" \
  "s_load_dword s84, %[po], 0x700\n\t" \
  "s_add_u32 m0, %[ob], 90112\n\t" \
  "s_nop 0\n\t" \
  "buffer_load_dwordx4 v8, %[so], s71 offen lds\n\t" \
  "s_add_u32 m0, %[ob], 94208\n\t" \
  "s_nop 0\n\t" \
  "buffer_load_dwordx4 v8, %[so], s72 offen lds\n\t" \
  "s_add_u32 m0, %[ob], 98304\n\t" \
  "s_nop 0\n\t" \
  "buffer_load_dwordx4 v8, %[so], s73 offen lds\n\t" \
  "v_readlane_b32 s68, v9, 9\n\t" \
  "v_readlane_b32 s69, v9, 10\n\t" \
  "v_readlane_b32 s70, v9, 11\n\t" \
  "v_readlane_b32 s71, v9, 25\n\t" \
  "v_readlane_b32 s72, v9, 26\n\t" \
  "s_add_u32 m0, %[ob], 36864\n\t" \
  "s_waitcnt vmcnt(16)\n\t" \
  "s_barrier\n\t" \
  "s_nop 2\n\t" \
  "buffer_load_dwordx4 v8, %[so], s68 offen lds\n\t" \
  "s_load_dword s84, %[po], 0x740\n\t" \
  "s_add_u32 m0, %[ob], 40960\n\t" \
  "s_nop 0\n\t" \
  "buffer_load_dwordx4 v8, %[so], s69 offen lds\n\t" \
  "s_load_dword s84, %[po], 0x780\n\t" \
  "s_add_u32 m0, %[ob], 45056\n\t" \
  "s_nop 0\n\t" \
  "buffer_load_dwordx4 v8, %[so], s70 offen lds\n\t" \
  "s_load_dword s84, %[po], 0x7c0\n\t" \
  "s_add_u32 m0, %[ob], 102400\n\t" \
  "s_nop 0\n\t" \
  "buffer_load_dwordx4 v8, %[so], s71 offen lds\n\t" \
  "s_add_u32 m0, %[ob], 106496\n\t" \
  "s_nop 0\n\t" \
  "buffer_load_dwordx4 v8, %[so], s72 offen lds\n\t" \
  "v_readlane_b32 s68, v9, 12\n\t" \
  "v_readlane_b32 s69, v9, 13\n\t" \
  "v_readlane_b32 s70, v9, 14\n\t" \
  "v_readlane_b32 s71, v9, 27\n\t" \
  "v_readlane_b32 s72, v9, 28\n\t" \
  "v_readlane_b32 s73, v9, 29\n\t" \
  "s_add_u32 m0, %[ob], 49152\n\t" \
  "s_nop 3\n\t" \
  "s_barrier\n\t" \
  "s_nop 2\n\t" \
  "buffer_load_dwordx4 v8, %[so], s68 offen lds\n\t" \
  "s_add_u32 m0, %[ob], 53248\n\t" \
  "s_nop 0\n\t" \
  "buffer_load_dwordx4 v8, %[so], s69 offen lds\n\t" \
  "s_add_u32 m0, %[ob], 57344\n\t" \
  "s_nop 0\n\t" \
  "buffer_load_dwordx4 v8, %[so], s70 offen lds\n\t" \
  "s_add_u32 m0, %[ob], 110592\n\t" \
  "s_nop 0\n\t" \
  "buffer_load_dwordx4 v8, %[so], s71 offen lds\n\t" \
  "s_add_u32 m0, %[ob], 114688\n\t" \
  "s_nop 0\n\t" \
  "buffer_load_dwordx4 v8, %[so], s72 offen lds\n\t" \
  "s_add_u32 m0, %[ob], 118784\n\t" \
  "s_nop 0\n\t" \
  "buffer_load_dwordx4 v8, %[so], s73 offen lds\n\t" \
  "v_readlane_b32 s68, v9, 15\n\t" \
  "v_readlane_b32 s69, v9, 16\n\t" \
  "v_readlane_b32 s70, v9, 17\n\t" \
  "v_readlane_b32 s71, v9, 30\n\t" \
  "v_readlane_b32 s72, v9, 31\n\t" \
  "s_add_u32 m0, %[ob], 61440\n\t" \
  "LS_%=:\n\t" \
  "ds_read_b32 v11, v10\n\t" \
  "s_addk_i32 %[cn], 1\n\t" \
  "s_cmp_gt_u32 %[cn], 0x400000\n\t" \
  "s_cbranch_scc1 LX_%=\n\t" \
  "s_waitcnt lgkmcnt(0)\n\t" \
  "v_cmp_eq_u32 vcc, 0, v11\n\t" \
  "s_cbranch_vccz LX_%=\n\t" \
  "s_sleep 1\n\t" \
  "s_branch LS_%=\n\t" \
  "LX_%=:\n\t" \
  "s_nop 2\n\t" \
  "buffer_load_dwordx4 v8, %[so], s68 offen lds\n\t" \
  "s_add_u32 m0, %[ob], 65536\n\t" \
  "s_nop 0\n\t" \
  "buffer_load_dwordx4 v8, %[so], s69 offen lds\n\t" \
  "s_add_u32 m0, %[ob], 69632\n\t" \
  "s_nop 0\n\t" \
  "buffer_load_dwordx4 v8, %[so], s70 offen lds\n\t" \
  "s_add_u32 m0, %[ob], 122880\n\t" \
  "s_nop 0\n\t" \
  "buffer_load_dwordx4 v8, %[so], s71 offen lds\n\t" \
  "s_add_u32 m0, %[ob], 126976\n\t" \
  "s_nop 0\n\t" \
  "buffer_load_dwordx4 v8, %[so], s72 offen lds\n\t" \
  "s_waitcnt vmcnt(0) lgkmcnt(0)\n\t"
#define LD_HT 8
#define LD_TP 2
#define LD_CLOB "v11", "s68", "s69", "s70", "s71", "s72", "s73", "s74", "s75", "s76", "s77", "s78", "s79", "s80", "s81", "s82", "s83", "s84", "s85"

#define ENDW
#define TS(i)
#define TSS(i)
#define TOUT(n)

#define SPIN(cond,slp) do{ int sp_=0; while(cond){ __builtin_amdgcn_s_sleep(slp); if(++sp_>(1<<22)) break; } }while(0)
__device__ __forceinline__ unsigned rd1(const char* p){ return __builtin_amdgcn_readfirstlane(*(volatile LDS_AS unsigned*)(LDS_AS char*)p); }
__device__ __forceinline__ unsigned lds_inc(const char* p, int l){ unsigned r=0; if(l==0) r=__hip_atomic_fetch_add((LDS_AS unsigned*)(LDS_AS char*)p, 1u, __ATOMIC_RELAXED, __HIP_MEMORY_SCOPE_WORKGROUP); return __builtin_amdgcn_readfirstlane(r); }
__device__ __forceinline__ void lds_add(unsigned adr){ asm volatile("s_mov_b64 exec, 1\n\tds_add_u32 %0, %1\n\ts_mov_b64 exec, -1" :: "v"(adr), "v"(1u) : "memory"); }
__device__ __forceinline__ void publish(unsigned cnt, const unsigned* base, unsigned voff, unsigned tag){
  unsigned t;
  asm volatile("s_waitcnt vmcnt(0)\n\ts_mov_b64 exec, 1\n\tds_add_rtn_u32 %0, %1, %2\n\ts_waitcnt lgkmcnt(0)\n\tv_cmp_eq_u32 vcc, 3, %0\n\ts_cbranch_vccz PB_%=\n\t"
    "s_mov_b64 exec, 0xffff\n\tglobal_store_dword %3, %4, %5 sc1\n\tPB_%=:\n\ts_mov_b64 exec, -1"
    : "=&v"(t) : "v"(cnt), "v"(1u), "v"(voff), "v"(tag), "s"(base) : "memory", "vcc");
}
__device__ __forceinline__ void bars(int n){
  asm volatile("BL_%=:\n\ts_barrier\n\ts_sub_u32 %0, %0, 1\n\ts_cmp_lg_u32 %0, 0\n\ts_cbranch_scc1 BL_%=" : "+s"(n) :: "memory", "scc");
}
#define barc(rem) barc_(rem)
#define CTRA
#define CTRP
__device__ __forceinline__ void barc_(unsigned& rem){
  asm volatile("s_cmp_eq_u32 %0, 0\n\ts_cbranch_scc1 BC_%=\n\ts_barrier\n\ts_sub_u32 %0, %0, 1\n\tBC_%=:" : "+s"(rem) :: "memory", "scc");
}
template<int SLP> __device__ __forceinline__ void wait_flag_b(const unsigned* p, unsigned voff, unsigned tag, unsigned& rem CTRA){
  int spins=0;
  while(true){
    unsigned v; asm volatile("global_load_dword %0, %1, %2 sc1" : "=v"(v) : "v"(voff), "s"(p) : "memory");
    barc(rem); if(PB2) barc(rem);
    asm volatile("s_waitcnt vmcnt(0)" : "+v"(v) :: "memory");
    if(__all(v==tag) || ++spins>(1<<21)) break;
    if(rem==0) __builtin_amdgcn_s_sleep(SLP);
  }
}
__device__ __forceinline__ void stout(void* sbase, unsigned voff, u32x4 v){
  st16cs(sbase,voff,v);
}

typedef unsigned u32x8 __attribute__((ext_vector_type(8)));
typedef unsigned u32x16 __attribute__((ext_vector_type(16)));
__device__ __forceinline__ char* ptr64(unsigned lo, unsigned hi){ return (char*)(((unsigned long long)hi<<32)|lo); }
__device__ __forceinline__ u32x4 srd(const char* p, unsigned bytes){ return u32x4{(unsigned)(size_t)p,(unsigned)((size_t)p>>32)&0xffffu,bytes,0x20000u}; }
constexpr size_t WS_O=0, WS_KV=(size_t)S*DM*2, WS_Y=(size_t)S*DM*4, WS_FL=(size_t)S*DM*12;

__global__ __launch_bounds__(768,3) void fused_kernel(const u16* x_, const u16* wqkv_, const u16* wo_, char* ws_, u16* out_,
    const u16* kin_, const u16* vin_, const u16* shk_, const u16* hk_, const u16* qin_, const u16* sD_)
{
  __shared__ __attribute__((aligned(128))) char smem[ALL_LDS];
  TS(0);
  int wv;
  asm volatile(
      "s_load_dwordx16 s[48:63], %[ka], 0x0\n\ts_load_dwordx4 s[64:67], %[ka], 0x40\n\ts_load_dwordx2 s[78:79], %[ka], 0x50\n\ts_getpc_b64 s[72:73]\n\tv_readfirstlane_b32 s75, v0\n\ts_lshr_b32 s74, %[b], 3\n\ts_mul_i32 s74, s74, 12\n\ts_lshr_b32 s75, s75, 6\n\t"
      "s_add_u32 s74, s74, s75\n\ts_min_u32 s74, s74, %[cl]\n\ts_lshl_b32 s74, s74, 7\n\ts_mov_b32 m0, %[sk]\n\tv_mov_b32 v1, s74\n\ts_mov_b64 exec, 1\n\tglobal_load_lds_dword v1, s[72:73]\n\ts_mov_b64 exec, -1\n\t"
      "s_mov_b32 s76, %[b]\n\ts_cmp_lt_u32 s75, 4\n\ts_cbranch_scc1 FK_G\n\ts_cmp_lt_u32 s75, 8\n\ts_cbranch_scc0 FK_C\n\t"
      "v_and_b32 v9, 7, v0\n\tv_and_b32 v10, 63, v0\n\ts_lshl_b32 s74, %[b], 19\n\ts_lshl_b32 s73, %[b], 11\n\ts_or_b32 s74, s74, s73\n\ts_and_b32 s74, s74, 0x3fc000\n\ts_lshl_b32 s73, s75, 9\n\t"
      "s_add_u32 s74, s74, s73\n\ts_add_u32 s74, s74, 0x7800\n\tv_lshl_add_u32 v9, v9, 11, s74\n\tv_and_b32 v1, 24, v0\n\ts_lshl2_add_u32 s74, %[b], s75\n\ts_mul_i32 s74, s74, %[nw]\n\tv_lshl_add_u32 v9, v1, 4, v9\n\t"
      "s_add_u32 s74, s74, %[w0]\n\tv_add_lshl_u32 v10, v10, s74, 7\n\t"
      "s_setprio %[lp]\n\ts_and_b32 s74, s75, 3\n\tv_lshrrev_b32 v1, 3, v0\n\tv_lshrrev_b32 v3, 4, v0\n\ts_and_b32 s72, %[b], 1\n\ts_and_b32 s73, %[b], 0xe0\n\ts_lshl_b32 s72, s72, 10\n\ts_lshl2_add_u32 s72, s73, s72\n\t"
      "v_and_b32 v4, 2, v1\n\tv_and_or_b32 v3, v3, 4, v4\n\tv_and_b32 v4, 31, v1\n\tv_xor_b32 v3, v3, v0\n\tv_mul_u32_u24 v4, 0x1800, v4\n\tv_and_b32 v3, 7, v3\n\tv_lshl_add_u32 v3, v3, 4, s72\n\t"
      "s_mul_i32 s39, s74, 0xc00\n\ts_mul_i32 s38, s74, 0x900\n\ts_mul_i32 s74, s74, 18\n\tv_add_u32 v5, v3, v4\n\tv_and_b32 v1, 7, v1\n\tv_add_u32 v6, 0x400, v5\n\tv_add_u32 v1, s74, v1\n\tv_add_u32 v7, 0x800, v5\n\t"
      "s_and_b32 s72, %[b], 6\n\ts_and_b32 s73, %[b], 24\n\ts_lshl4_add_u32 s72, s72, s73\n\tv_xor_b32 v3, v1, v0\n\ts_lshl_b32 s72, s72, 15\n\tv_and_b32 v3, 7, v3\n\tv_lshl_add_u32 v1, v1, 11, s72\n\t"
      "s_mov_b32 s47, 0x20000\n\ts_mov_b32 s46, 0x600000\n\tv_lshl_add_u32 v2, v3, 4, v1\n\ts_mov_b32 s43, s47\n\ts_mov_b32 s42, 0x408000\n\ts_movk_i32 s73, 0x3c00\n\t"
      "v_add_u32 v3, s73, v2\n\ts_add_u32 s38, s38, %[xf]\n\tv_max_u32 v0, 0x8000, v2\n\tv_add_u32 v4, s73, v3\n\tv_max_u32 v1, 0x8000, v3\n\t"
      "s_waitcnt lgkmcnt(0)\n\ts_mov_b64 s[44:45], s[50:51]\n\ts_sub_u32 s40, s48, 0x8000\n\ts_subb_u32 s41, s49, 0\n\t"
      LD_ASM1
      "s_branch FK_L"
      : "=s"(wv)
      : "{v0}"(threadIdx.x), [ka]"s"(__builtin_amdgcn_kernarg_segment_ptr()), [b]"s"(blockIdx.x), [cl]"n"(CODEL-1), [sk]"n"(SINK+256), [lp]"n"(LPR), [xf]"n"(X_OFF), [nw]"n"(3*TPW), [w0]"n"(TW0*3072-12*TPW), [wm]"n"((1<<(3*TPW))-1)
      : "memory", "scc", "s48", "s49", "s50", "s51", "s52", "s53", "s54", "s55", "s72", "s73", "s74", "s75", "s76", "s78", "s79", "v1", "v2", "v3", "v4", "v5", "v6", "v7", "v9", "v10",
        "s40", "s41", "s42", "s43", "s44", "s45", "s46", "s47", "s38", "s39", "s56", "s57", "s58", "s59", "s60", "s61", "s62", "s63", "s64", "s65", "s66", "s67", "s68");
  #define ENTER(lbl) int w8, bid; u32x8 a0, a1; u32x4 a2; u32x2 a3; asm volatile(lbl ":" : "={s75}"(w8), "={s76}"(bid), "={s[48:55]}"(a0), "={s[56:63]}"(a1), "={s[64:67]}"(a2), "={s[78:79]}"(a3) :: "memory");
  #define ARGW asm volatile("s_waitcnt lgkmcnt(0)" : "+{s[48:55]}"(a0), "+{s[56:63]}"(a1), "+{s[64:67]}"(a2), "+{s[78:79]}"(a3));
  #define ROLE const int w=w8&3, xcd=bid&7, idx=bid>>3, rb=(xcd>>1)*4+(idx&3), hp=(xcd&1)*8+(idx>>2), s0=rb*128; (void)s0; (void)hp;
  #define LANEVARS int l; asm volatile("v_mbcnt_lo_u32_b32 %0, -1, 0\n\tv_mbcnt_hi_u32_b32 %0, -1, %0" : "=v"(l)); const int g=l>>4, fr=l&15, tq=(l&15)>>2, tp=l&3; (void)g; (void)fr; (void)tq; (void)tp;
  #define ARGS0 const char* x=ptr64(a0[0],a0[1]); const char* wqkv=ptr64(a0[2],a0[3]); const char* wo=ptr64(a0[4],a0[5]); char* ws=ptr64(a0[6],a0[7]); \
    unsigned* flags=(unsigned*)(ws+WS_FL); const int hot0=(3-(int)(a0[2]>>7))&7; (void)x; (void)wqkv; (void)wo; (void)flags; (void)hot0;
  #define ORA ((unsigned)(hp-hot0-1)&7u)
  #define ORE(c) (ORA==7u? (c)-1u : (c))
  #define ORO(c) (ORE(c)>=NF && ORE(c)<NF+7u)
  #define ORD(c) (((((ORE(c)<14u? ((ORA==7u? 0u:ORA)+(ORO(c)? ORE(c)-NF : (ORE(c)<NF? ORE(c) : ORE(c)-7u)))%7u : 7u)+hot0+1)&7u))+8u*(((hp>>3)^(ORO(c) || (c)==15u))&1u))
  const unsigned sb0=__builtin_amdgcn_readfirstlane(lds_addr(smem));
  const char* syn=smem+SYN;

  if(__builtin_expect(wv==1,1)){
    ENTER("FK_L")
    u32x4 sx, sw; unsigned xl, wl, xoA, xoB, xo, xo1, xo2, wof[3];
    asm volatile("; descriptors and LDS bases" : "={s[40:43]}"(sx), "={s[44:47]}"(sw), "={s38}"(xl), "={s39}"(wl));
    asm volatile("; lane offsets" : "={v0}"(xoA), "={v1}"(xoB), "={v2}"(xo), "={v3}"(xo1), "={v4}"(xo2), "={v5}"(wof[0]), "={v6}"(wof[1]), "={v7}"(wof[2]));
    ROLE
    LANEVARS
    unsigned ltr=0, ltr2=0, ltr3=0; (void)ltr; (void)ltr2; (void)ltr3;
    TSS(2); TSS(3);
    ARGS0
    const unsigned wd=hp*128;
    {
      const unsigned wof2=(unsigned)(w*16384+(l>>3)*2048+((((l&7)>>1)^((l>>4)&3))*32)+(l&1)*16);
      const unsigned to=ORD((unsigned)(l>>1)&15u);
      const unsigned tab=to*131072u+(l&1)*65536;
      unsigned long long tm; asm("s_cmp_eq_u32 %1, 0\n\ts_cselect_b64 %0, %2, 0" : "=s"(tm) : "s"(w), "s"(WTL==2? 0xffffffffull : 0ull) : "scc");
      unsigned cn=0;
      const unsigned hn=(unsigned)(w*(3*LD_HT/4)+l), hto=(hn/6u)*393216u+(hn%6u)*2048u;
      asm volatile(LD_ASM2
        : [cn]"+&s"(cn)
        : "{v0}"(xoA), "{v1}"(xoB), "{v2}"(xo), "{v3}"(xo1), "{v4}"(xo2), "{v5}"(wof[0]), "{v6}"(wof[1]), "{v7}"(wof[2]), "{v8}"(wof2), "{v9}"(tab),
          "{v10}"(sb0+SYN+32+(l&3)*4), "{v12}"((l&31)*2048),
          "{v16}"(hto), [sh]"s"(srd(wqkv+((xcd&1)*8+hot0)*128+idx*12288,6291456u-2048u-idx*12288u+128u)),
          "{s[40:43]}"(sx), "{s[44:47]}"(sw), [so]"s"(srd(wo+wd,2097152u-wd)), "{s38}"(xl), "{s39}"(wl), [ob]"s"(sb0+w*1024),
          [pw]"s"(wqkv+idx*6144+(xcd&1)*1024+(xcd>>1)*256+w*64), [po]"s"(wo+(bid*4+w)*2048),
          [tsk]"s"(sb0+SINK+256), [tm]"s"(tm), [tp]"s"(wo+idx*65536+(xcd&1)*1024+((3-(int)(a0[4]>>7))&7)*128)
        : "memory", "scc", "vcc", LD_CLOB);
    }
    const unsigned tag=rd1(syn+64)+1u;
    lds_add(sb0+SYN+12);
    TS(2);
    if(w>=4-NPOL){
      const unsigned* fp=flags+F_O+(rb*16+hp)*16; const unsigned fo=ORD((unsigned)l&15u)*4u;
      const unsigned madr=sb0+(l==0? SYN+20 : SINK+l*4);
      SPIN(rd1(syn+12)<4u || rd1(syn+16)<4u,1);
      if(w==3) PUBM(madr,1u);
      if(PD0) __builtin_amdgcn_s_sleep(PD0);
      if(w==2) __builtin_amdgcn_s_sleep(PD1); if(w==1) __builtin_amdgcn_s_sleep(2*PD1); if(w==0) __builtin_amdgcn_s_sleep(3*PD1);
      unsigned rdy=1; int spins=0;
      while(rdy!=0xFFFFu){
        unsigned v;
        asm volatile("s_mov_b64 exec, 0xffff\n\tglobal_atomic_or %0, %1, %3, %2 sc0 sc1\n\ts_mov_b64 exec, -1\n\ts_waitcnt vmcnt(0)" : "=&v"(v) : "v"(fo), "s"(fp), "v"(0u) : "memory");
        unsigned r=((unsigned)__ballot(v==tag)&0xFFFFu)|1u;
        if(++spins>(1<<21)) r=0xFFFFu;
        if(r!=rdy){ PUBM(madr,(unsigned)__builtin_ctz(~r)); rdy=r; if(w<3 && (r&0x3FFFu)==0x3FFFu) break; }
        else __builtin_amdgcn_s_sleep(OSLP);
      }
      TS(3);
      if(bid==0 && w==3){
        int sp=0;
        while(true){
          u32x4 v; asm volatile("global_load_dwordx4 %0, %1, %2 sc1\n\ts_waitcnt vmcnt(0)" : "=&v"(v) : "v"(l*16), "s"(flags+F_Y) : "memory");
          if(__all(v[0]==tag && v[1]==tag && v[2]==tag && v[3]==tag) || ++sp>(1<<20)) break;
          __builtin_amdgcn_s_sleep(12);
        }
        if(l==0) stflag(flags, tag);
      }
    } else { TS(3); }
    TOUT(4);
    __builtin_amdgcn_endpgm();
  }

  if(wv==0){
    ENTER("FK_G")
    ROLE
    __builtin_amdgcn_s_setprio(GPR);
    const int h=w&1, r=w>>1;
    f32x16 e0, e1;
    LANEVARS
    TSS(3);
    ARGW
    ARGS0
    if(PGT&1){ if(w==1 && l<7) touch(ws, l<6? l*2097152+PGO : (unsigned)WS_FL+2048, sb0+SINK+512); }
    if(PGT&2){ if(w==2 && l<4) touch(ptr64(a1[0],a1[1]), l*2097152+PGO, sb0+SINK+512); }
    if(PGT&4){ if(w==3 && l<1) touch(wo, PGO, sb0+SINK+512); if(w==0 && l<2) touch(wqkv, l*2097152+2097152, sb0+SINK+512); }
    TSS(4);
    {
      unsigned lx0=sb0+X_OFF+fr*128+((g^(fr&7))*16), lx1=sb0+X_OFF+fr*128+(((4+g)^(fr&7))*16);
      const int sw=wswz(8*g+tq);
      unsigned lw0=sb0+g*3072+tq*128+(unsigned)((((2*h)^sw)*32)+8*tp), lw1=sb0+g*3072+tq*128+(unsigned)((((2*h+1)^sw)*32)+8*tp);
      char* ot=ws+WS_O+(rb*16+hp)*16384+r*8192+h*2048;
      unsigned it; unsigned long long rm; asm("s_cmp_lg_u32 %1, 0\n\ts_cselect_b64 %0, -1, 0" : "=s"(rm) : "s"(r) : "scc");
      asm volatile(N2_ASM
        : [it]"=&s"(it), "+{v56}"(lx0), "+{v57}"(lx1), "+{v58}"(lw0), "+{v59}"(lw1)
        : "{v62}"(l*16), "{v63}"(sb0+EX_OFF+h*3072+l*16), [r]"s"(r), [ot]"s"(ot), [ou]"s"(ot+4096), [fa]"s"(sb0+SYN+32+w*4), [fp]"s"(sb0+SYN+32+(w^2)*4),
          [vo]"s"(2048-1024*r), [rm]"s"(rm), [z]"s"(rb|r)
        : "memory", "scc", "vcc", N2_CLOB);
    }
    TS(3);
    unsigned tb;
    {
      LANEVARS
      const unsigned tag=rd1(syn+64)+1u;
      const unsigned ra=sb0+(4*g+tq)*128+8*tp; const int sw=(2*g+(tq>>1))&3;
      unsigned b0=ra+((0^sw)*32), b1=ra+((1^sw)*32), b2=ra+((2^sw)*32), b3=ra+((3^sw)*32), vo=(unsigned)(w*4096+l*16);
      tb=ORD((unsigned)l&15u)*16384u;
      asm volatile("" : "+{v160}"(b0), "+{v161}"(b1), "+{v162}"(b2), "+{v163}"(b3), "+{v164}"(vo), "+{v167}"(tb));
      publish(sb0+SYN+16, flags+F_O+rb*256+hp, l*64, tag);
      TS(4);
      __builtin_amdgcn_s_setprio(G2P);
      unsigned c=0, iss=0, th, q0, q1, cn=0;
      asm volatile(G3_ASM
        : "={v[128:143]}"(e0), "={v[144:159]}"(e1), [c]"+s"(c), [iss]"+s"(iss), [th]"=&s"(th), [t0]"=&s"(q0), [t1]"=&s"(q1), [cn]"+&s"(cn),
          "+{v160}"(b0), "+{v161}"(b1), "+{v162}"(b2), "+{v163}"(b3), "+{v167}"(tb)
        : "{v164}"(vo), "{v165}"(sb0+SYN+20), [go]"s"(60+w*4), [ob]"s"(srd(ws+WS_O+rb*262144,262144u))
        : "memory", "scc", "vcc", G3_CLOB);
    }
    {
      LANEVARS
      char* sg=smem+EX_OFF+w*2048;
      char* op=ptr64(a1[0],a1[1])+(s0+w*32)*4096+2048+hp*128; const unsigned oo=(unsigned)((l>>3)*4096+(l&7)*16);
      u32x4 ov[4];
      #pragma unroll
      for(int t=0;t<2;t++){
        #pragma unroll
        for(int j=0;j<4;j++){
          #define E2(i) (t? e1[(i)&15] : e0[(i)&15])
          *(volatile LDS_AS u32x2*)(LDS_AS char*)(sg+fr*128+(((2*j+(g>>1))^(fr&7))*16)+(g&1)*8)=u32x2{pack2(E2(4*j),E2(4*j+1)),pack2(E2(4*j+2),E2(4*j+3))};
        }
        #pragma unroll
        for(int i=0;i<2;i++) ov[2*t+i]=*(volatile LDS_AS u32x4*)(LDS_AS char*)(sg+(i*8+(l>>3))*128+(((l&7)^(l>>3))*16));
      }
      #pragma unroll
      for(int i=0;i<4;i++) stout(op+i*32768, oo, ov[i]);
      ENDW TS(6); TOUT(7);
    }
    __builtin_amdgcn_endpgm();
  }

  {
    ENTER("FK_C")
    ROLE
    char* cr=smem+C_OFF+2048+w*4096;
    const unsigned crs=sb0+C_OFF+2048+w*4096;
    const int sblk=bid>>3, td0=(bid&7)*128+(w&1)*64;
    const int vb=(LOCA&1)? xcd*32+(idx&16)+(idx&1)*8+((idx>>1)&7) : bid, fb=(LOCA&2)? (idx&15)*16+xcd*2+(idx>>4) : bid;
    const int d=(vb>>4)*64+(((vb>>3)&1)*4+w)*8+(vb&7);
    int kv0=KV0+(sblk&KVS);
    static_assert(KV0+KVS+PP_NB<=33 && KV0>=EB, "barrier budget");
    unsigned tagv, ctr; f32x8 RB;
    const int fd0=(fb&15)*64, fs0=(fb>>4)*128+w*32;
    u32x4 qv[4];
    f32x16 A0,A1,A2,A3;
    const int rw=sblk*64+(w>>1)*32, j=rw>>5;
    unsigned k0,k1,k2,ko,ta,se,so,ha,l16,rb0[2];
    {
      LANEVARS
      if(w==0 && l<25 && l!=16) ((unsigned*)(smem+SYN))[l]=0u;
      if(l<32) *(u32x4*)(smem+C_OFF+w*512+l*16)=u32x4{0,0,0,0};
      asm volatile("" ::: "memory");
      __builtin_amdgcn_s_setprio(CPR);
      const int dg=l&7, rg=l>>3;
      ko=(unsigned)(rg*8192+dg*16);
      const bool top=(rw==0 && rg==0);
      k0=top? ko+6144:ko; k1=top? ko+6144:ko+2048; k2=top? ko+6144:ko+4096;
      ta=sb0+EX_OFF+w*1024+(w&1)*512+dg*64; se=(l&8)? 0x03020706u : 0x05040100u; ha=crs+(32*g-4*tq+4*tp)*2; l16=l*16;
      so=(unsigned)(rg*32768+((j>>2)&3)*128+((dg&4)|((dg+j)&3))*16);
      #pragma unroll
      for(int k=0;k<2;k++){ const int q=fr-1-2*g+k; rb0[k]=sb0+C_OFF+(4+(q>>1))*256+((q>>3)&3)*64+((w+(q>>1))&3)*16+(q&1)*8; }
      asm volatile("" : "+{v152}"(k0), "+{v153}"(k1), "+{v154}"(k2), "+{v155}"(ko), "+{v156}"(ta), "+{v157}"(se), "+{v159}"(so), "+{v163}"(l16), "+{v165}"(ha), "+{v166}"(rb0[0]), "+{v167}"(rb0[1]));
    }
    TSS(3);
    ARGW
    if(EB==1) asm volatile("s_barrier" : "+s"(a1), "+s"(a2), "+s"(a3), "+s"(a0) :: "memory");
    if(EB==2) asm volatile("s_barrier\n\ts_barrier" : "+s"(a1), "+s"(a2), "+s"(a3), "+s"(a0) :: "memory");
    ARGS0
    char* out=ptr64(a1[0],a1[1]); const char* qp=ptr64(a2[2],a2[3])+(fs0*DM+fd0)*2;
    {
      const char* kb=ptr64(a1[2],a1[3])+(rw*DM+td0)*2; const char* vb=ptr64(a1[4],a1[5])+(rw*DM+td0)*2;
      unsigned long long zm; asm("s_cmp_eq_u32 %1, 0\n\ts_cselect_b64 %0, 0xff, 0" : "=s"(zm) : "s"(rw) : "scc");
      unsigned t0;
      if(PGT&8){ const char* tp= w==0? kb : w==1? vb : w==2? qp : ptr64(a2[0],a2[1])+d*4096;
        asm volatile("s_mov_b32 m0, %1\n\ts_mov_b64 exec, 1\n\tglobal_load_lds_dword v163, %0\n\ts_mov_b64 exec, -1" :: "s"(tp), "s"(sb0+SINK+512), "{v163}"(l16) : "memory"); }
      TSS(5);
      asm volatile(PP_ASM
        : [t0]"=&s"(t0), "=&{v164}"(ctr), "+{v159}"(so), "=&{v160}"(tagv), "=&{v[24:31]}"(RB), "=&{v[44:47]}"(qv[0]), "=&{v[48:51]}"(qv[1]), "=&{v[52:55]}"(qv[2]), "=&{v[56:59]}"(qv[3]),
          "=&{v[64:79]}"(A0), "=&{v[80:95]}"(A1), "=&{v[96:111]}"(A2), "=&{v[112:127]}"(A3)
        : "{v152}"(k0), "{v153}"(k1), "{v154}"(k2), "{v155}"(ko), "{v156}"(ta), "{v157}"(se), "{v166}"(rb0[0]), "{v167}"(rb0[1]), "{v165}"(ha), "{v163}"(l16),
          [qb0]"s"(qp), [qb2]"s"(qp+4096), [mk]"s"(0x180u), [em]"s"(1ull|(512ull<<w)), [hd]"s"(crs),
          [kb0]"s"(kb-6144), [kb3]"s"(kb), [kb5]"s"(kb+4096), [vb0]"s"(vb), [vb2]"s"(vb+4096), [sb]"s"(ws+WS_KV+td0*4096+j*512),
          [n0]"s"(kv0-EB-(HH==2)), [ff]"s"(flags+F_PP+(td0>>6)*64+j), [zm]"s"(zm), [shd]"s"(sb0+EX_OFF+w*1024), [tgd]"s"(sb0+SYN+64), [shp]"s"(ptr64(a1[6],a1[7])+(bid&7)*1024), [fl]"s"(flags),
          [hp]"s"(ptr64(a2[0],a2[1])+d*4096), [dp]"s"(ptr64(a3[0],a3[1])+d*2)
        : "memory", "scc", PP_CLOB);
    }
    LANEVARS
    const unsigned tag=__builtin_amdgcn_readfirstlane(tagv)+1u;
    unsigned rem=33-PP_NB-kv0;
    barc(rem);
    const unsigned* pf=flags+F_PP+(d>>6)*64;
    const char* kvp=ws+WS_KV+((vb>>4)*8+(vb&7))*32768+((vb>>3)&1)*64+w*8192;
    const unsigned kvo=(unsigned)((l>>2)*128+(l&3)*16);
    int bidx=((l>=16)? l-16 : (l==0? 63 : l+47))*4;
    asm volatile("" : "+{v32}"(bidx));
    __builtin_amdgcn_s_setprio(3);
    while(rem>PREM) barc(rem);
    wait_flag_b<8>(pf, l*4, tag, rem CTRP);
    #pragma unroll
    for(int i=0;i<4;i++){ if(i%KTH==0 && i) barc(rem); gldsc(kvp+i*2048, kvo, crs+i*1024); }
    barc(rem); barc(rem);
    WAITV(0);
    asm volatile("s_mov_b32 m0, %2\n\ts_mov_b64 exec, 1\n\tglobal_load_lds_dword %0, %1 sc1\n\ts_mov_b64 exec, -1" :: "v"(0u), "s"(pf), "s"(sb0+SYN+100+w*4) : "memory");
    while(rem) barc(rem);
    { int sp=0; while(!__all(*(volatile LDS_AS unsigned*)(LDS_AS char*)(syn+100+(l&3)*4)==tag) && ++sp<(1<<22)) __builtin_amdgcn_s_sleep(1); }
    __builtin_amdgcn_s_setprio(CVP);
    TS(3);
    f32x16 ac0, ac1;
    {
      asm volatile("s_nop 1\n\t" CV_ASM
        : "=&{v[128:143]}"(ac0), "=&{v[144:159]}"(ac1)
          , "+{v[44:47]}"(qv[0]), "+{v[48:51]}"(qv[1]), "+{v[52:55]}"(qv[2]), "+{v[56:59]}"(qv[3])
        : "{v[64:79]}"(A0), "{v[80:95]}"(A1), "{v[96:111]}"(A2), "{v[112:127]}"(A3),
          "{v[24:31]}"(RB), "{v32}"(bidx), "{v33}"(sb0+SYN+80+w*4)
          , "{v42}"((256*(g&1)+16*fr+4*(g&2))*2), [yb]"s"(ws+WS_Y+d*4096)
          , "{v43}"((l>>3)*8192+(l&7)*16), [q0]"s"(qp), [q2]"s"(qp+4096)
        : "memory", "scc", CV_CLOB);
    }
    TS(4);
    __builtin_amdgcn_s_setprio(3);
    publish(sb0+SYN+24, flags+F_Y+vb, l*1024, tag);
    asm volatile("" : "+v"(qv[0]), "+v"(qv[1]), "+v"(qv[2]), "+v"(qv[3]));
    TS(5);
    float qf[32];
    #pragma unroll
    for(int i=0;i<16;i++){ qf[2*i]=bflo(qv[i>>2][i&3]); qf[2*i+1]=bfhi(qv[i>>2][i&3]); }
    unsigned ya=crs+(g*8+tq)*64+tp*8, wa0=crs+fr*128+((g^(fr&7))*16), wa1=crs+fr*128+(((4+g)^(fr&7))*16), ra[4], oo=(unsigned)((l>>3)*16384+(l&7)*16), yo2=(l>>2)*4096+(l&3)*16;
    #pragma unroll
    for(int i=0;i<4;i++){ const int row=4*(l>>3)+i; ra[i]=crs+row*128+(((l&7)^(row&7))*16); }
    char* op=out+fs0*4096+fd0*2; char* op2=op+8192; const char* yg=ws+WS_Y+fd0*4096+fs0*2;
    #pragma unroll
    for(int i=0;i<32;i++) asm volatile("" : "+v"(qf[i]));
    asm volatile("" : "+v"(ya), "+v"(wa0), "+v"(wa1), "+v"(ra[0]), "+v"(ra[1]), "+v"(ra[2]), "+v"(ra[3]), "+v"(oo), "+v"(yo2));
    asm volatile("" : "+s"(op), "+s"(op2), "+s"(yg));
    if(w==0){
      SPIN(rd1(syn+24)<4u,1);
      SPIN(rd1(syn+96)==0u,2);
      { int sp=0;
        while(true){
          unsigned v=tag; asm volatile("s_mov_b64 exec, 0xffff\n\tglobal_atomic_or %0, %1, %3, %2 sc0 sc1\n\ts_mov_b64 exec, -1\n\ts_waitcnt vmcnt(0)" : "+&v"(v) : "v"(l*4), "s"(flags+F_Y+fb*16), "v"(0u) : "memory");
          if(__all(v==tag) || ++sp>(1<<21)) break;
          __builtin_amdgcn_s_sleep(YSLP);
        } }
      PUB(sb0+(l==0? SYN+28 : SINK+l*4), 1u);
    } else SPIN(rd1(syn+28)==0u,YS2);
    #pragma unroll
    for(int i=0;i<4;i++) gldsc(yg+i*65536, yo2, crs+i*1024);
    {
      u32x4 y4[4];
      asm volatile("s_waitcnt vmcnt(0)\n\t"
        "ds_read_b64_tr_b16 v[0:1], %4\n\tds_read_b64_tr_b16 v[2:3], %4 offset:256\n\tds_read_b64_tr_b16 v[4:5], %4 offset:32\n\tds_read_b64_tr_b16 v[6:7], %4 offset:288\n\t"
        "ds_read_b64_tr_b16 v[8:9], %4 offset:2048\n\tds_read_b64_tr_b16 v[10:11], %4 offset:2304\n\tds_read_b64_tr_b16 v[12:13], %4 offset:2080\n\tds_read_b64_tr_b16 v[14:15], %4 offset:2336\n\t"
        "s_waitcnt lgkmcnt(6)\n\tds_write_b128 %5, v[0:3]\n\ts_waitcnt lgkmcnt(5)\n\tds_write_b128 %5, v[4:7] offset:2048\n\t"
        "s_waitcnt lgkmcnt(4)\n\tds_write_b128 %6, v[8:11]\n\ts_waitcnt lgkmcnt(3)\n\tds_write_b128 %6, v[12:15] offset:2048\n\t"
        "ds_read_b128 %0, %7\n\tds_read_b128 %1, %8\n\tds_read_b128 %2, %9\n\tds_read_b128 %3, %10\n\ts_waitcnt lgkmcnt(0)"
        : "=&v"(y4[0]), "=&v"(y4[1]), "=&v"(y4[2]), "=&v"(y4[3])
        : "v"(ya), "v"(wa0), "v"(wa1), "v"(ra[0]), "v"(ra[1]), "v"(ra[2]), "v"(ra[3])
        : "memory", "v0", "v1", "v2", "v3", "v4", "v5", "v6", "v7", "v8", "v9", "v10", "v11", "v12", "v13", "v14", "v15");
      #pragma unroll
      for(int i=0;i<4;i++){
        u32x4 ov;
        #pragma unroll
        for(int e=0;e<4;e++) ov[e]=pack2(mul1(qf[8*i+2*e],bflo(y4[i][e])), mul1(qf[8*i+2*e+1],bfhi(y4[i][e])));
        stout((i&2? op2:op)+(i&1)*4096, oo, ov);
      }
    }
    ENDW TS(6); TOUT(7);
    __builtin_amdgcn_endpgm();
  }
}

extern "C" void kernel_launch(void* const* d_in, const int* in_sizes, int n_in, void* d_out, int out_size, void* d_ws, size_t ws_size, hipStream_t stream){
  const u16* q=(const u16*)d_in[0]; const u16* k=(const u16*)d_in[1]; const u16* v=(const u16*)d_in[2];
  const u16* shk=(const u16*)d_in[3]; const u16* hk=(const u16*)d_in[4]; const u16* sD=(const u16*)d_in[5];
  const u16* x=(const u16*)d_in[6]; const u16* wqkv=(const u16*)d_in[7]; const u16* wo=(const u16*)d_in[8];
  fused_kernel<<<256,768,0,stream>>>(x,wqkv,wo,(char*)d_ws,(u16*)d_out,k,v,shk,hk,q,sD);
}
```

```cpp
#define STAMP
#include <hip/hip_runtime.h>
#include <hip/hip_bf16.h>
#include <stdint.h>

typedef unsigned short u16;
typedef short bf16x8 __attribute__((ext_vector_type(8)));
typedef short s16x4 __attribute__((ext_vector_type(4)));
typedef float f32x2 __attribute__((ext_vector_type(2)));
typedef float f32x4 __attribute__((ext_vector_type(4)));
typedef float f32x8 __attribute__((ext_vector_type(8)));
typedef float f32x16 __attribute__((ext_vector_type(16)));
typedef unsigned u32x4 __attribute__((ext_vector_type(4)));
typedef unsigned u32x2 __attribute__((ext_vector_type(2)));
typedef __bf16 bf16x2_t __attribute__((ext_vector_type(2)));
#define LDS_AS __attribute__((address_space(3)))

__device__ __forceinline__ float bflo(unsigned x){ return __uint_as_float(x<<16); }
__device__ __forceinline__ float bfhi(unsigned x){ return __uint_as_float(x&0xffff0000u); }
__device__ __forceinline__ unsigned pack2(float lo, float hi){ return __builtin_bit_cast(unsigned, __builtin_convertvector(f32x2{lo,hi}, bf16x2_t)); }
__device__ __forceinline__ float mul1(float a, float b){ float r; asm("v_mul_f32 %0, %1, %2" : "=v"(r) : "v"(a), "v"(b)); return r; }
__device__ __forceinline__ float fma1(float a, float b, float c){ float r; asm("v_fma_f32 %0, %1, %2, %3" : "=v"(r) : "v"(a), "v"(b), "v"(c)); return r; }

__device__ __forceinline__ void glds(const void* sbase, unsigned voff, unsigned ldsaddr){
  asm volatile("s_mov_b32 m0, %2\n\ts_nop 0\n\tglobal_load_lds_dwordx4 %0, %1" :: "v"(voff), "s"(sbase), "s"(ldsaddr) : "memory");
}
__device__ __forceinline__ void gldsc(const void* sbase, unsigned voff, unsigned ldsaddr){
  asm volatile("s_mov_b32 m0, %2\n\ts_nop 0\n\tglobal_load_lds_dwordx4 %0, %1 sc1" :: "v"(voff), "s"(sbase), "s"(ldsaddr) : "memory");
}
__device__ __forceinline__ void touch(const void* sbase, unsigned voff, unsigned ldsaddr){
  asm volatile("s_mov_b32 m0, %2\n\ts_nop 0\n\tglobal_load_lds_dword %0, %1" :: "v"(voff), "s"(sbase), "s"(ldsaddr) : "memory");
}
__device__ __forceinline__ void st16c(void* p, u32x4 v){ asm volatile("global_store_dwordx4 %0, %1, off sc1\n\ts_nop 1" :: "v"(p), "v"(v) : "memory"); }
__device__ __forceinline__ void st16cs(void* sbase, unsigned voff, u32x4 v){ asm volatile("global_store_dwordx4 %0, %1, %2 sc1\n\ts_nop 1" :: "v"(voff), "v"(v), "s"(sbase) : "memory"); }
#define GLB_AS __attribute__((address_space(1)))
__device__ __forceinline__ unsigned ldflag(const unsigned* p){ return __hip_atomic_load((const GLB_AS unsigned*)p, __ATOMIC_RELAXED, __HIP_MEMORY_SCOPE_AGENT); }
__device__ __forceinline__ void stflag(unsigned* p, unsigned v){ __hip_atomic_store((GLB_AS unsigned*)p, v, __ATOMIC_RELAXED, __HIP_MEMORY_SCOPE_AGENT); }
template<int SLP> __device__ __forceinline__ void wait_flag(const unsigned* p, unsigned tag){
  int spins=0;
  while(true){ unsigned v=ldflag(p); if(__all(v==tag)) break; __builtin_amdgcn_s_sleep(SLP); if(++spins>(1<<21)) break; }
}
__device__ __forceinline__ unsigned lds_addr(const void* p){ return (unsigned)(size_t)(const LDS_AS void*)p; }
#define WAITV(n) asm volatile("s_waitcnt vmcnt(" #n ")":::"memory")
#define WAITL() asm volatile("s_waitcnt lgkmcnt(0)":::"memory")
#define SBAR() asm volatile("s_barrier":::"memory")
#define PUBM(addr,val) asm volatile("ds_max_u32 %0, %1" :: "v"(addr), "v"(val) : "memory")
#define PUB(addr,val) asm volatile("ds_write_b32 %0, %1" :: "v"(addr), "v"(val) : "memory")
__device__ __forceinline__ s16x4 trread(const char* p){ return __builtin_amdgcn_ds_read_tr16_b64_v4i16((LDS_AS s16x4*)p); }
__device__ __forceinline__ bf16x8 cat44(s16x4 a, s16x4 b){ return __builtin_shufflevector(a,b,0,1,2,3,4,5,6,7); }
__device__ __forceinline__ int wswz(int row){ return ((row>>1)&1) | (((row>>3)&1)<<1); }
__device__ __forceinline__ u32x4 widen(u32x2 a, u32x2 b){
  auto r0=__builtin_amdgcn_permlane16_swap(a[0],b[0],false,false);
  auto r1=__builtin_amdgcn_permlane16_swap(a[1],b[1],false,false);
  return u32x4{r0[0],r1[0],r0[1],r1[1]};
}

constexpr int S=2048, DM=1024;
constexpr int WS=12288, XS=18432, X_OFF=6*WS;
constexpr int CREG=4608, C_OFF=131072;
constexpr int EX_OFF=C_OFF+4*CREG;
constexpr int SYN=EX_OFF+8192;
constexpr int SINK=SYN+256;
constexpr int ALL_LDS=SINK+768;
constexpr int F_PP=1024, F_O=4096, F_Y=8192;
#define KV0 1
#define KVS 3
#define LPR 2
#define CPR 2
#define GPR 1
#define CGATE 0
#define OSLP 2
#define YSLP 8
#define TPX 16
#define TPW 2
#define TWO 0
#define CVP 1
#define G2P 3
#define OUTC 1
#define ROT 0
#define WTL 2
#define PB2 1
#define PREM 9
#define KTH 1
#define YDEF 3
#define OPOL 1
#define CODEL 272
#define MIX 0
#define LOCA 3
#define XLOC 1
#define PGT 0
#define PGO 0
#define QREM 0
#define EB 0
#define TW0 1
#define ORDM 1
#define NF 7u
#define NPOL 1
#define PD0 0
#define PD1 4
#define HH 0
#define HW 3
#define HKV 4
#define YPOL 1
#define YS2 2
#define HLAST 1

#define N2_ASM \
  "v_mov_b64 v[64:65], 0\n\t" \
  "v_mov_b64 v[66:67], 0\n\t" \
  "v_mov_b64 v[68:69], 0\n\t" \
  "v_mov_b64 v[70:71], 0\n\t" \
  "v_mov_b64 v[72:73], 0\n\t" \
  "v_mov_b64 v[74:75], 0\n\t" \
  "v_mov_b64 v[76:77], 0\n\t" \
  "v_mov_b64 v[78:79], 0\n\t" \
  "v_mov_b64 v[80:81], 0\n\t" \
  "v_mov_b64 v[82:83], 0\n\t" \
  "v_mov_b64 v[84:85], 0\n\t" \
  "v_mov_b64 v[86:87], 0\n\t" \
  "v_mov_b64 v[88:89], 0\n\t" \
  "v_mov_b64 v[90:91], 0\n\t" \
  "v_mov_b64 v[92:93], 0\n\t" \
  "v_mov_b64 v[94:95], 0\n\t" \
  "v_mov_b64 v[96:97], 0\n\t" \
  "v_mov_b64 v[98:99], 0\n\t" \
  "v_mov_b64 v[100:101], 0\n\t" \
  "v_mov_b64 v[102:103], 0\n\t" \
  "v_mov_b64 v[104:105], 0\n\t" \
  "v_mov_b64 v[106:107], 0\n\t" \
  "v_mov_b64 v[108:109], 0\n\t" \
  "v_mov_b64 v[110:111], 0\n\t" \
  "v_mov_b64 v[112:113], 0\n\t" \
  "v_mov_b64 v[114:115], 0\n\t" \
  "v_mov_b64 v[116:117], 0\n\t" \
  "v_mov_b64 v[118:119], 0\n\t" \
  "v_mov_b64 v[120:121], 0\n\t" \
  "v_mov_b64 v[122:123], 0\n\t" \
  "v_mov_b64 v[124:125], 0\n\t" \
  "v_mov_b64 v[126:127], 0\n\t" \
  "v_mov_b64 v[128:129], 0\n\t" \
  "v_mov_b64 v[130:131], 0\n\t" \
  "v_mov_b64 v[132:133], 0\n\t" \
  "v_mov_b64 v[134:135], 0\n\t" \
  "v_mov_b64 v[136:137], 0\n\t" \
  "v_mov_b64 v[138:139], 0\n\t" \
  "v_mov_b64 v[140:141], 0\n\t" \
  "v_mov_b64 v[142:143], 0\n\t" \
  "v_mov_b64 v[144:145], 0\n\t" \
  "v_mov_b64 v[146:147], 0\n\t" \
  "v_mov_b64 v[148:149], 0\n\t" \
  "v_mov_b64 v[150:151], 0\n\t" \
  "v_mov_b64 v[152:153], 0\n\t" \
  "v_mov_b64 v[154:155], 0\n\t" \
  "v_mov_b64 v[156:157], 0\n\t" \
  "v_mov_b64 v[158:159], 0\n\t" \
  "v_mov_b64 v[160:161], 0\n\t" \
  "v_mov_b64 v[162:163], 0\n\t" \
  "v_mov_b64 v[164:165], 0\n\t" \
  "v_mov_b64 v[166:167], 0\n\t" \
  "v_add_u32 v60, 36864, v58\n\t" \
  "v_add_u32 v61, 36864, v59\n\t" \
  "s_mov_b32 %[it], 0\n\t" \
  "s_cmp_lg_u32 %[r], 0\n\t" \
  "s_barrier\n\t" \
  "s_memrealtime %[st1]\n\t" \
  "s_waitcnt lgkmcnt(0)\n\t" \
  "s_cbranch_scc1 NB_%=\n\t" \
  "ds_read_b64_tr_b16 v[40:41], v60 offset:13312\n\t" \
  "ds_read_b64_tr_b16 v[42:43], v60 offset:13824\n\t" \
  "ds_read_b64_tr_b16 v[44:45], v60 offset:14336\n\t" \
  "ds_read_b64_tr_b16 v[46:47], v60 offset:14848\n\t" \
  "ds_read_b64_tr_b16 v[48:49], v61 offset:13312\n\t" \
  "ds_read_b64_tr_b16 v[50:51], v61 offset:13824\n\t" \
  "ds_read_b128 v[0:3], v56 offset:36864\n\t" \
  "ds_read_b128 v[4:7], v56 offset:38912\n\t" \
  "ds_read_b128 v[8:11], v56 offset:40960\n\t" \
  "ds_read_b128 v[12:15], v56 offset:43008\n\t" \
  "ds_read_b128 v[16:19], v56 offset:45056\n\t" \
  "s_mov_b32 s68, 0x50000\n\t" \
  "s_mov_b32 s69, 0x5550055\n\t" \
  "s_mov_b32 s70, 0xa0000\n\t" \
  "s_mov_b32 s71, 0xaaa00aa\n\t" \
  "s_mov_b32 s72, 0x150001\n\t" \
  "s_mov_b32 s73, 0x15550155\n\t" \
  "s_mov_b32 s74, 0x2a0002\n\t" \
  "s_mov_b32 s75, 0x2aaa02aa\n\t" \
  "s_mov_b32 s76, 0x55505555\n\t" \
  "s_mov_b32 s77, 0x50005500\n\t" \
  "s_mov_b32 s78, 0xaaa0aaaa\n\t" \
  "s_mov_b32 s79, 0xa000aa00\n\t" \
  "s_mov_b32 s80, 0x55405554\n\t" \
  "s_mov_b32 s81, 0x40005400\n\t" \
  "s_mov_b32 s82, 0xaa80aaa8\n\t" \
  "s_mov_b32 s83, 0x8000a800\n\t" \
  "s_mov_b32 s84, 0xaaaaaaaa\n\t" \
  "s_mov_b32 s85, 0xaaaaaaaa\n\t" \
  "s_waitcnt lgkmcnt(0)\n\t" \
  "s_branch N4a_%=\n\t" \
  "N0a_%=:\n\t" \
  "s_waitcnt lgkmcnt(4)\n\t" \
  "v_mfma_f32_16x16x32_bf16 v[160:163], v[40:43], v[0:3], v[160:163]\n\t" \
  "ds_read_b64_tr_b16 v[52:53], v59 offset:2048\n\t" \
  "v_mfma_f32_16x16x32_bf16 v[96:99], v[40:43], v[4:7], v[96:99]\n\t" \
  "ds_read_b64_tr_b16 v[54:55], v59 offset:2560\n\t" \
  "v_mfma_f32_16x16x32_bf16 v[104:107], v[40:43], v[8:11], v[104:107]\n\t" \
  "s_barrier\n\t" \
  "v_mfma_f32_16x16x32_bf16 v[112:115], v[40:43], v[12:15], v[112:115]\n\t" \
  "v_mfma_f32_16x16x32_bf16 v[120:123], v[40:43], v[16:19], v[120:123]\n\t" \
  "s_waitcnt lgkmcnt(4)\n\t" \
  "v_mfma_f32_16x16x32_bf16 v[128:131], v[4:7], v[44:47], v[128:131]\n\t" \
  "ds_read_b64_tr_b16 v[40:41], v58 offset:0\n\t" \
  "v_mfma_f32_16x16x32_bf16 v[136:139], v[8:11], v[44:47], v[136:139]\n\t" \
  "ds_read_b64_tr_b16 v[42:43], v58 offset:512\n\t" \
  "v_mfma_f32_16x16x32_bf16 v[144:147], v[12:15], v[44:47], v[144:147]\n\t" \
  "ds_read_b128 v[20:23], v57 offset:0\n\t" \
  "v_mfma_f32_16x16x32_bf16 v[152:155], v[16:19], v[44:47], v[152:155]\n\t" \
  "ds_read_b128 v[24:27], v57 offset:2048\n\t" \
  "s_waitcnt lgkmcnt(6)\n\t" \
  "v_mfma_f32_16x16x32_bf16 v[164:167], v[48:51], v[0:3], v[164:167]\n\t" \
  "ds_read_b64_tr_b16 v[44:45], v59 offset:0\n\t" \
  "v_mfma_f32_16x16x32_bf16 v[100:103], v[48:51], v[4:7], v[100:103]\n\t" \
  "ds_read_b64_tr_b16 v[46:47], v59 offset:512\n\t" \
  "v_mfma_f32_16x16x32_bf16 v[108:111], v[48:51], v[8:11], v[108:111]\n\t" \
  "ds_read_b128 v[28:31], v57 offset:4096\n\t" \
  "v_mfma_f32_16x16x32_bf16 v[116:119], v[48:51], v[12:15], v[116:119]\n\t" \
  "ds_read_b128 v[32:35], v57 offset:6144\n\t" \
  "v_mfma_f32_16x16x32_bf16 v[124:127], v[48:51], v[16:19], v[124:127]\n\t" \
  "s_waitcnt lgkmcnt(8)\n\t" \
  "v_mfma_f32_16x16x32_bf16 v[132:135], v[4:7], v[52:55], v[132:135]\n\t" \
  "ds_read_b64_tr_b16 v[48:49], v58 offset:13312\n\t" \
  "v_mfma_f32_16x16x32_bf16 v[140:143], v[8:11], v[52:55], v[140:143]\n\t" \
  "ds_read_b64_tr_b16 v[50:51], v58 offset:13824\n\t" \
  "v_mfma_f32_16x16x32_bf16 v[148:151], v[12:15], v[52:55], v[148:151]\n\t" \
  "ds_read_b128 v[36:39], v57 offset:8192\n\t" \
  "v_mfma_f32_16x16x32_bf16 v[156:159], v[16:19], v[52:55], v[156:159]\n\t" \
  "s_waitcnt lgkmcnt(9)\n\t" \
  "v_mfma_f32_16x16x32_bf16 v[64:67], v[40:43], v[4:7], v[64:67]\n\t" \
  "ds_read_b64_tr_b16 v[52:53], v58 offset:14336\n\t" \
  "v_mfma_f32_16x16x32_bf16 v[72:75], v[40:43], v[8:11], v[72:75]\n\t" \
  "ds_read_b64_tr_b16 v[54:55], v58 offset:14848\n\t" \
  "v_mfma_f32_16x16x32_bf16 v[80:83], v[40:43], v[12:15], v[80:83]\n\t" \
  "v_mfma_f32_16x16x32_bf16 v[88:91], v[40:43], v[16:19], v[88:91]\n\t" \
  "s_waitcnt lgkmcnt(7)\n\t" \
  "v_mfma_f32_16x16x32_bf16 v[68:71], v[44:47], v[4:7], v[68:71]\n\t" \
  "ds_read_b64_tr_b16 v[40:41], v59 offset:13312\n\t" \
  "v_mfma_f32_16x16x32_bf16 v[76:79], v[44:47], v[8:11], v[76:79]\n\t" \
  "ds_read_b64_tr_b16 v[42:43], v59 offset:13824\n\t" \
  "v_mfma_f32_16x16x32_bf16 v[84:87], v[44:47], v[12:15], v[84:87]\n\t" \
  "v_mfma_f32_16x16x32_bf16 v[92:95], v[44:47], v[16:19], v[92:95]\n\t" \
  "N1a_%=:\n\t" \
  "s_waitcnt lgkmcnt(4)\n\t" \
  "v_mfma_f32_16x16x32_bf16 v[160:163], v[48:51], v[20:23], v[160:163]\n\t" \
  "ds_read_b64_tr_b16 v[44:45], v59 offset:14336\n\t" \
  "v_mfma_f32_16x16x32_bf16 v[96:99], v[48:51], v[24:27], v[96:99]\n\t" \
  "ds_read_b64_tr_b16 v[46:47], v59 offset:14848\n\t" \
  "v_mfma_f32_16x16x32_bf16 v[104:107], v[48:51], v[28:31], v[104:107]\n\t" \
  "s_barrier\n\t" \
  "v_mfma_f32_16x16x32_bf16 v[112:115], v[48:51], v[32:35], v[112:115]\n\t" \
  "v_mfma_f32_16x16x32_bf16 v[120:123], v[48:51], v[36:39], v[120:123]\n\t" \
  "s_waitcnt lgkmcnt(4)\n\t" \
  "v_mfma_f32_16x16x32_bf16 v[128:131], v[24:27], v[52:55], v[128:131]\n\t" \
  "ds_read_b64_tr_b16 v[48:49], v58 offset:12288\n\t" \
  "v_mfma_f32_16x16x32_bf16 v[136:139], v[28:31], v[52:55], v[136:139]\n\t" \
  "ds_read_b64_tr_b16 v[50:51], v58 offset:12800\n\t" \
  "v_mfma_f32_16x16x32_bf16 v[144:147], v[32:35], v[52:55], v[144:147]\n\t" \
  "ds_read_b128 v[0:3], v56 offset:18432\n\t" \
  "v_mfma_f32_16x16x32_bf16 v[152:155], v[36:39], v[52:55], v[152:155]\n\t" \
  "ds_read_b128 v[4:7], v56 offset:20480\n\t" \
  "s_waitcnt lgkmcnt(6)\n\t" \
  "v_mfma_f32_16x16x32_bf16 v[164:167], v[40:43], v[20:23], v[164:167]\n\t" \
  "ds_read_b64_tr_b16 v[52:53], v59 offset:12288\n\t" \
  "v_mfma_f32_16x16x32_bf16 v[100:103], v[40:43], v[24:27], v[100:103]\n\t" \
  "ds_read_b64_tr_b16 v[54:55], v59 offset:12800\n\t" \
  "v_mfma_f32_16x16x32_bf16 v[108:111], v[40:43], v[28:31], v[108:111]\n\t" \
  "ds_read_b128 v[8:11], v56 offset:22528\n\t" \
  "v_mfma_f32_16x16x32_bf16 v[116:119], v[40:43], v[32:35], v[116:119]\n\t" \
  "ds_read_b128 v[12:15], v56 offset:24576\n\t" \
  "v_mfma_f32_16x16x32_bf16 v[124:127], v[40:43], v[36:39], v[124:127]\n\t" \
  "s_waitcnt lgkmcnt(8)\n\t" \
  "v_mfma_f32_16x16x32_bf16 v[132:135], v[24:27], v[44:47], v[132:135]\n\t" \
  "ds_read_b64_tr_b16 v[40:41], v58 offset:25600\n\t" \
  "v_mfma_f32_16x16x32_bf16 v[140:143], v[28:31], v[44:47], v[140:143]\n\t" \
  "ds_read_b64_tr_b16 v[42:43], v58 offset:26112\n\t" \
  "v_mfma_f32_16x16x32_bf16 v[148:151], v[32:35], v[44:47], v[148:151]\n\t" \
  "ds_read_b128 v[16:19], v56 offset:26624\n\t" \
  "v_mfma_f32_16x16x32_bf16 v[156:159], v[36:39], v[44:47], v[156:159]\n\t" \
  "s_waitcnt lgkmcnt(9)\n\t" \
  "v_mfma_f32_16x16x32_bf16 v[64:67], v[48:51], v[24:27], v[64:67]\n\t" \
  "ds_read_b64_tr_b16 v[44:45], v58 offset:26624\n\t" \
  "v_mfma_f32_16x16x32_bf16 v[72:75], v[48:51], v[28:31], v[72:75]\n\t" \
  "ds_read_b64_tr_b16 v[46:47], v58 offset:27136\n\t" \
  "v_mfma_f32_16x16x32_bf16 v[80:83], v[48:51], v[32:35], v[80:83]\n\t" \
  "v_mfma_f32_16x16x32_bf16 v[88:91], v[48:51], v[36:39], v[88:91]\n\t" \
  "s_waitcnt lgkmcnt(7)\n\t" \
  "v_mfma_f32_16x16x32_bf16 v[68:71], v[52:55], v[24:27], v[68:71]\n\t" \
  "ds_read_b64_tr_b16 v[48:49], v59 offset:25600\n\t" \
  "v_mfma_f32_16x16x32_bf16 v[76:79], v[52:55], v[28:31], v[76:79]\n\t" \
  "ds_read_b64_tr_b16 v[50:51], v59 offset:26112\n\t" \
  "v_mfma_f32_16x16x32_bf16 v[84:87], v[52:55], v[32:35], v[84:87]\n\t" \
  "v_mfma_f32_16x16x32_bf16 v[92:95], v[52:55], v[36:39], v[92:95]\n\t" \
  "N2a_%=:\n\t" \
  "s_waitcnt lgkmcnt(4)\n\t" \
  "v_mfma_f32_16x16x32_bf16 v[160:163], v[40:43], v[0:3], v[160:163]\n\t" \
  "ds_read_b64_tr_b16 v[52:53], v59 offset:26624\n\t" \
  "v_mfma_f32_16x16x32_bf16 v[96:99], v[40:43], v[4:7], v[96:99]\n\t" \
  "ds_read_b64_tr_b16 v[54:55], v59 offset:27136\n\t" \
  "v_mfma_f32_16x16x32_bf16 v[104:107], v[40:43], v[8:11], v[104:107]\n\t" \
  "s_barrier\n\t" \
  "v_mfma_f32_16x16x32_bf16 v[112:115], v[40:43], v[12:15], v[112:115]\n\t" \
  "v_mfma_f32_16x16x32_bf16 v[120:123], v[40:43], v[16:19], v[120:123]\n\t" \
  "s_waitcnt lgkmcnt(4)\n\t" \
  "v_mfma_f32_16x16x32_bf16 v[128:131], v[4:7], v[44:47], v[128:131]\n\t" \
  "ds_read_b64_tr_b16 v[40:41], v58 offset:24576\n\t" \
  "v_mfma_f32_16x16x32_bf16 v[136:139], v[8:11], v[44:47], v[136:139]\n\t" \
  "ds_read_b64_tr_b16 v[42:43], v58 offset:25088\n\t" \
  "v_mfma_f32_16x16x32_bf16 v[144:147], v[12:15], v[44:47], v[144:147]\n\t" \
  "ds_read_b128 v[20:23], v57 offset:18432\n\t" \
  "v_mfma_f32_16x16x32_bf16 v[152:155], v[16:19], v[44:47], v[152:155]\n\t" \
  "ds_read_b128 v[24:27], v57 offset:20480\n\t" \
  "s_waitcnt lgkmcnt(6)\n\t" \
  "v_mfma_f32_16x16x32_bf16 v[164:167], v[48:51], v[0:3], v[164:167]\n\t" \
  "ds_read_b64_tr_b16 v[44:45], v59 offset:24576\n\t" \
  "v_mfma_f32_16x16x32_bf16 v[100:103], v[48:51], v[4:7], v[100:103]\n\t" \
  "ds_read_b64_tr_b16 v[46:47], v59 offset:25088\n\t" \
  "v_mfma_f32_16x16x32_bf16 v[108:111], v[48:51], v[8:11], v[108:111]\n\t" \
  "ds_read_b128 v[28:31], v57 offset:22528\n\t" \
  "v_mfma_f32_16x16x32_bf16 v[116:119], v[48:51], v[12:15], v[116:119]\n\t" \
  "ds_read_b128 v[32:35], v57 offset:24576\n\t" \
  "v_mfma_f32_16x16x32_bf16 v[124:127], v[48:51], v[16:19], v[124:127]\n\t" \
  "s_waitcnt lgkmcnt(8)\n\t" \
  "v_mfma_f32_16x16x32_bf16 v[132:135], v[4:7], v[52:55], v[132:135]\n\t" \
  "ds_read_b64_tr_b16 v[48:49], v60 offset:1024\n\t" \
  "v_mfma_f32_16x16x32_bf16 v[140:143], v[8:11], v[52:55], v[140:143]\n\t" \
  "ds_read_b64_tr_b16 v[50:51], v60 offset:1536\n\t" \
  "v_mfma_f32_16x16x32_bf16 v[148:151], v[12:15], v[52:55], v[148:151]\n\t" \
  "ds_read_b128 v[36:39], v57 offset:26624\n\t" \
  "v_mfma_f32_16x16x32_bf16 v[156:159], v[16:19], v[52:55], v[156:159]\n\t" \
  "s_waitcnt lgkmcnt(9)\n\t" \
  "v_mfma_f32_16x16x32_bf16 v[64:67], v[40:43], v[4:7], v[64:67]\n\t" \
  "ds_read_b64_tr_b16 v[52:53], v60 offset:2048\n\t" \
  "v_mfma_f32_16x16x32_bf16 v[72:75], v[40:43], v[8:11], v[72:75]\n\t" \
  "ds_read_b64_tr_b16 v[54:55], v60 offset:2560\n\t" \
  "v_mfma_f32_16x16x32_bf16 v[80:83], v[40:43], v[12:15], v[80:83]\n\t" \
  "v_mfma_f32_16x16x32_bf16 v[88:91], v[40:43], v[16:19], v[88:91]\n\t" \
  "s_waitcnt lgkmcnt(7)\n\t" \
  "v_mfma_f32_16x16x32_bf16 v[68:71], v[44:47], v[4:7], v[68:71]\n\t" \
  "ds_read_b64_tr_b16 v[40:41], v61 offset:1024\n\t" \
  "v_mfma_f32_16x16x32_bf16 v[76:79], v[44:47], v[8:11], v[76:79]\n\t" \
  "ds_read_b64_tr_b16 v[42:43], v61 offset:1536\n\t" \
  "v_mfma_f32_16x16x32_bf16 v[84:87], v[44:47], v[12:15], v[84:87]\n\t" \
  "v_mfma_f32_16x16x32_bf16 v[92:95], v[44:47], v[16:19], v[92:95]\n\t" \
  "N3a_%=:\n\t" \
  "s_waitcnt lgkmcnt(4)\n\t" \
  "v_mfma_f32_16x16x32_bf16 v[160:163], v[48:51], v[20:23], v[160:163]\n\t" \
  "ds_read_b64_tr_b16 v[44:45], v61 offset:2048\n\t" \
  "v_mfma_f32_16x16x32_bf16 v[96:99], v[48:51], v[24:27], v[96:99]\n\t" \
  "ds_read_b64_tr_b16 v[46:47], v61 offset:2560\n\t" \
  "v_mfma_f32_16x16x32_bf16 v[104:107], v[48:51], v[28:31], v[104:107]\n\t" \
  "s_barrier\n\t" \
  "v_mfma_f32_16x16x32_bf16 v[112:115], v[48:51], v[32:35], v[112:115]\n\t" \
  "v_mfma_f32_16x16x32_bf16 v[120:123], v[48:51], v[36:39], v[120:123]\n\t" \
  "s_waitcnt lgkmcnt(4)\n\t" \
  "v_mfma_f32_16x16x32_bf16 v[128:131], v[24:27], v[52:55], v[128:131]\n\t" \
  "ds_read_b64_tr_b16 v[48:49], v60 offset:0\n\t" \
  "v_mfma_f32_16x16x32_bf16 v[136:139], v[28:31], v[52:55], v[136:139]\n\t" \
  "ds_read_b64_tr_b16 v[50:51], v60 offset:512\n\t" \
  "v_mfma_f32_16x16x32_bf16 v[144:147], v[32:35], v[52:55], v[144:147]\n\t" \
  "ds_read_b128 v[0:3], v56 offset:36864\n\t" \
  "v_mfma_f32_16x16x32_bf16 v[152:155], v[36:39], v[52:55], v[152:155]\n\t" \
  "ds_read_b128 v[4:7], v56 offset:38912\n\t" \
  "s_waitcnt lgkmcnt(6)\n\t" \
  "v_mfma_f32_16x16x32_bf16 v[164:167], v[40:43], v[20:23], v[164:167]\n\t" \
  "ds_read_b64_tr_b16 v[52:53], v61 offset:0\n\t" \
  "v_mfma_f32_16x16x32_bf16 v[100:103], v[40:43], v[24:27], v[100:103]\n\t" \
  "ds_read_b64_tr_b16 v[54:55], v61 offset:512\n\t" \
  "v_mfma_f32_16x16x32_bf16 v[108:111], v[40:43], v[28:31], v[108:111]\n\t" \
  "ds_read_b128 v[8:11], v56 offset:40960\n\t" \
  "v_mfma_f32_16x16x32_bf16 v[116:119], v[40:43], v[32:35], v[116:119]\n\t" \
  "ds_read_b128 v[12:15], v56 offset:43008\n\t" \
  "v_mfma_f32_16x16x32_bf16 v[124:127], v[40:43], v[36:39], v[124:127]\n\t" \
  "s_waitcnt lgkmcnt(8)\n\t" \
  "v_mfma_f32_16x16x32_bf16 v[132:135], v[24:27], v[44:47], v[132:135]\n\t" \
  "ds_read_b64_tr_b16 v[40:41], v60 offset:13312\n\t" \
  "v_mfma_f32_16x16x32_bf16 v[140:143], v[28:31], v[44:47], v[140:143]\n\t" \
  "ds_read_b64_tr_b16 v[42:43], v60 offset:13824\n\t" \
  "v_mfma_f32_16x16x32_bf16 v[148:151], v[32:35], v[44:47], v[148:151]\n\t" \
  "ds_read_b128 v[16:19], v56 offset:45056\n\t" \
  "v_mfma_f32_16x16x32_bf16 v[156:159], v[36:39], v[44:47], v[156:159]\n\t" \
  "s_waitcnt lgkmcnt(9)\n\t" \
  "v_mfma_f32_16x16x32_bf16 v[64:67], v[48:51], v[24:27], v[64:67]\n\t" \
  "ds_read_b64_tr_b16 v[44:45], v60 offset:14336\n\t" \
  "v_mfma_f32_16x16x32_bf16 v[72:75], v[48:51], v[28:31], v[72:75]\n\t" \
  "ds_read_b64_tr_b16 v[46:47], v60 offset:14848\n\t" \
  "v_mfma_f32_16x16x32_bf16 v[80:83], v[48:51], v[32:35], v[80:83]\n\t" \
  "v_mfma_f32_16x16x32_bf16 v[88:91], v[48:51], v[36:39], v[88:91]\n\t" \
  "s_waitcnt lgkmcnt(7)\n\t" \
  "v_mfma_f32_16x16x32_bf16 v[68:71], v[52:55], v[24:27], v[68:71]\n\t" \
  "ds_read_b64_tr_b16 v[48:49], v61 offset:13312\n\t" \
  "v_mfma_f32_16x16x32_bf16 v[76:79], v[52:55], v[28:31], v[76:79]\n\t" \
  "ds_read_b64_tr_b16 v[50:51], v61 offset:13824\n\t" \
  "v_mfma_f32_16x16x32_bf16 v[84:87], v[52:55], v[32:35], v[84:87]\n\t" \
  "v_mfma_f32_16x16x32_bf16 v[92:95], v[52:55], v[36:39], v[92:95]\n\t" \
  "N4a_%=:\n\t" \
  "s_waitcnt lgkmcnt(4)\n\t" \
  "v_mfma_f32_16x16x32_bf16 v[160:163], v[40:43], v[0:3], v[160:163]\n\t" \
  "ds_read_b64_tr_b16 v[52:53], v61 offset:14336\n\t" \
  "v_mfma_f32_16x16x32_bf16 v[96:99], v[40:43], v[4:7], v[96:99]\n\t" \
  "ds_read_b64_tr_b16 v[54:55], v61 offset:14848\n\t" \
  "v_mfma_f32_16x16x32_bf16 v[104:107], v[40:43], v[8:11], v[104:107]\n\t" \
  "s_barrier\n\t" \
  "v_mfma_f32_16x16x32_bf16 v[112:115], v[40:43], v[12:15], v[112:115]\n\t" \
  "v_mfma_f32_16x16x32_bf16 v[120:123], v[40:43], v[16:19], v[120:123]\n\t" \
  "s_waitcnt lgkmcnt(4)\n\t" \
  "v_mfma_f32_16x16x32_bf16 v[128:131], v[4:7], v[44:47], v[128:131]\n\t" \
  "ds_read_b64_tr_b16 v[40:41], v60 offset:12288\n\t" \
  "v_mfma_f32_16x16x32_bf16 v[136:139], v[8:11], v[44:47], v[136:139]\n\t" \
  "ds_read_b64_tr_b16 v[42:43], v60 offset:12800\n\t" \
  "v_mfma_f32_16x16x32_bf16 v[144:147], v[12:15], v[44:47], v[144:147]\n\t" \
  "ds_read_b128 v[20:23], v57 offset:36864\n\t" \
  "v_mfma_f32_16x16x32_bf16 v[152:155], v[16:19], v[44:47], v[152:155]\n\t" \
  "ds_read_b128 v[24:27], v57 offset:38912\n\t" \
  "s_waitcnt lgkmcnt(6)\n\t" \
  "v_mfma_f32_16x16x32_bf16 v[164:167], v[48:51], v[0:3], v[164:167]\n\t" \
  "ds_read_b64_tr_b16 v[44:45], v61 offset:12288\n\t" \
  "v_mfma_f32_16x16x32_bf16 v[100:103], v[48:51], v[4:7], v[100:103]\n\t" \
  "ds_read_b64_tr_b16 v[46:47], v61 offset:12800\n\t" \
  "v_mfma_f32_16x16x32_bf16 v[108:111], v[48:51], v[8:11], v[108:111]\n\t" \
  "ds_read_b128 v[28:31], v57 offset:40960\n\t" \
  "v_mfma_f32_16x16x32_bf16 v[116:119], v[48:51], v[12:15], v[116:119]\n\t" \
  "ds_read_b128 v[32:35], v57 offset:43008\n\t" \
  "v_mfma_f32_16x16x32_bf16 v[124:127], v[48:51], v[16:19], v[124:127]\n\t" \
  "s_waitcnt lgkmcnt(8)\n\t" \
  "v_mfma_f32_16x16x32_bf16 v[132:135], v[4:7], v[52:55], v[132:135]\n\t" \
  "ds_read_b64_tr_b16 v[48:49], v60 offset:25600\n\t" \
  "v_mfma_f32_16x16x32_bf16 v[140:143], v[8:11], v[52:55], v[140:143]\n\t" \
  "ds_read_b64_tr_b16 v[50:51], v60 offset:26112\n\t" \
  "v_mfma_f32_16x16x32_bf16 v[148:151], v[12:15], v[52:55], v[148:151]\n\t" \
  "ds_read_b128 v[36:39], v57 offset:45056\n\t" \
  "v_mfma_f32_16x16x32_bf16 v[156:159], v[16:19], v[52:55], v[156:159]\n\t" \
  "s_waitcnt lgkmcnt(9)\n\t" \
  "v_mfma_f32_16x16x32_bf16 v[64:67], v[40:43], v[4:7], v[64:67]\n\t" \
  "ds_read_b64_tr_b16 v[52:53], v60 offset:26624\n\t" \
  "v_mfma_f32_16x16x32_bf16 v[72:75], v[40:43], v[8:11], v[72:75]\n\t" \
  "ds_read_b64_tr_b16 v[54:55], v60 offset:27136\n\t" \
  "v_mfma_f32_16x16x32_bf16 v[80:83], v[40:43], v[12:15], v[80:83]\n\t" \
  "v_mfma_f32_16x16x32_bf16 v[88:91], v[40:43], v[16:19], v[88:91]\n\t" \
  "s_waitcnt lgkmcnt(7)\n\t" \
  "v_mfma_f32_16x16x32_bf16 v[68:71], v[44:47], v[4:7], v[68:71]\n\t" \
  "ds_read_b64_tr_b16 v[40:41], v61 offset:25600\n\t" \
  "v_mfma_f32_16x16x32_bf16 v[76:79], v[44:47], v[8:11], v[76:79]\n\t" \
  "ds_read_b64_tr_b16 v[42:43], v61 offset:26112\n\t" \
  "v_mfma_f32_16x16x32_bf16 v[84:87], v[44:47], v[12:15], v[84:87]\n\t" \
  "v_mfma_f32_16x16x32_bf16 v[92:95], v[44:47], v[16:19], v[92:95]\n\t" \
  "N5a_%=:\n\t" \
  "s_waitcnt lgkmcnt(4)\n\t" \
  "v_mfma_f32_16x16x32_bf16 v[160:163], v[48:51], v[20:23], v[160:163]\n\t" \
  "ds_read_b64_tr_b16 v[44:45], v61 offset:26624\n\t" \
  "v_mfma_f32_16x16x32_bf16 v[96:99], v[48:51], v[24:27], v[96:99]\n\t" \
  "ds_read_b64_tr_b16 v[46:47], v61 offset:27136\n\t" \
  "v_mfma_f32_16x16x32_bf16 v[104:107], v[48:51], v[28:31], v[104:107]\n\t" \
  "s_barrier\n\t" \
  "v_mfma_f32_16x16x32_bf16 v[112:115], v[48:51], v[32:35], v[112:115]\n\t" \
  "v_mfma_f32_16x16x32_bf16 v[120:123], v[48:51], v[36:39], v[120:123]\n\t" \
  "s_waitcnt lgkmcnt(4)\n\t" \
  "v_mfma_f32_16x16x32_bf16 v[128:131], v[24:27], v[52:55], v[128:131]\n\t" \
  "ds_read_b64_tr_b16 v[48:49], v60 offset:24576\n\t" \
  "v_mfma_f32_16x16x32_bf16 v[136:139], v[28:31], v[52:55], v[136:139]\n\t" \
  "ds_read_b64_tr_b16 v[50:51], v60 offset:25088\n\t" \
  "v_mfma_f32_16x16x32_bf16 v[144:147], v[32:35], v[52:55], v[144:147]\n\t" \
  "ds_read_b128 v[0:3], v56 offset:0\n\t" \
  "v_mfma_f32_16x16x32_bf16 v[152:155], v[36:39], v[52:55], v[152:155]\n\t" \
  "ds_read_b128 v[4:7], v56 offset:2048\n\t" \
  "s_waitcnt lgkmcnt(6)\n\t" \
  "v_mfma_f32_16x16x32_bf16 v[164:167], v[40:43], v[20:23], v[164:167]\n\t" \
  "ds_read_b64_tr_b16 v[52:53], v61 offset:24576\n\t" \
  "v_mfma_f32_16x16x32_bf16 v[100:103], v[40:43], v[24:27], v[100:103]\n\t" \
  "ds_read_b64_tr_b16 v[54:55], v61 offset:25088\n\t" \
  "v_mfma_f32_16x16x32_bf16 v[108:111], v[40:43], v[28:31], v[108:111]\n\t" \
  "ds_read_b128 v[8:11], v56 offset:4096\n\t" \
  "v_mfma_f32_16x16x32_bf16 v[116:119], v[40:43], v[32:35], v[116:119]\n\t" \
  "ds_read_b128 v[12:15], v56 offset:6144\n\t" \
  "v_mfma_f32_16x16x32_bf16 v[124:127], v[40:43], v[36:39], v[124:127]\n\t" \
  "s_waitcnt lgkmcnt(8)\n\t" \
  "v_mfma_f32_16x16x32_bf16 v[132:135], v[24:27], v[44:47], v[132:135]\n\t" \
  "ds_read_b64_tr_b16 v[40:41], v58 offset:1024\n\t" \
  "v_mfma_f32_16x16x32_bf16 v[140:143], v[28:31], v[44:47], v[140:143]\n\t" \
  "ds_read_b64_tr_b16 v[42:43], v58 offset:1536\n\t" \
  "v_mfma_f32_16x16x32_bf16 v[148:151], v[32:35], v[44:47], v[148:151]\n\t" \
  "ds_read_b128 v[16:19], v56 offset:8192\n\t" \
  "v_mfma_f32_16x16x32_bf16 v[156:159], v[36:39], v[44:47], v[156:159]\n\t" \
  "s_waitcnt lgkmcnt(9)\n\t" \
  "v_mfma_f32_16x16x32_bf16 v[64:67], v[48:51], v[24:27], v[64:67]\n\t" \
  "ds_read_b64_tr_b16 v[44:45], v58 offset:2048\n\t" \
  "v_mfma_f32_16x16x32_bf16 v[72:75], v[48:51], v[28:31], v[72:75]\n\t" \
  "ds_read_b64_tr_b16 v[46:47], v58 offset:2560\n\t" \
  "v_mfma_f32_16x16x32_bf16 v[80:83], v[48:51], v[32:35], v[80:83]\n\t" \
  "v_mfma_f32_16x16x32_bf16 v[88:91], v[48:51], v[36:39], v[88:91]\n\t" \
  "s_waitcnt lgkmcnt(7)\n\t" \
  "v_mfma_f32_16x16x32_bf16 v[68:71], v[52:55], v[24:27], v[68:71]\n\t" \
  "ds_read_b64_tr_b16 v[48:49], v59 offset:1024\n\t" \
  "v_mfma_f32_16x16x32_bf16 v[76:79], v[52:55], v[28:31], v[76:79]\n\t" \
  "ds_read_b64_tr_b16 v[50:51], v59 offset:1536\n\t" \
  "s_addk_i32 %[it], 1\n\t" \
  "v_mfma_f32_16x16x32_bf16 v[84:87], v[52:55], v[32:35], v[84:87]\n\t" \
  "s_cmp_lt_u32 %[it], 6\n\t" \
  "v_mfma_f32_16x16x32_bf16 v[92:95], v[52:55], v[36:39], v[92:95]\n\t" \
  "s_cbranch_scc1 N0a_%=\n\t" \
  "s_memrealtime %[st2]\n\t" \
  "s_setprio 3\n\t" \
  "v_cvt_pk_bf16_f32 v120, v120, v121\n\t" \
  "v_cvt_pk_bf16_f32 v121, v122, v123\n\t" \
  "v_cvt_pk_bf16_f32 v122, v124, v125\n\t" \
  "v_cvt_pk_bf16_f32 v123, v126, v127\n\t" \
  "v_cvt_pk_bf16_f32 v28, v152, v153\n\t" \
  "v_cvt_pk_bf16_f32 v29, v154, v155\n\t" \
  "v_cvt_pk_bf16_f32 v38, v156, v157\n\t" \
  "v_cvt_pk_bf16_f32 v39, v158, v159\n\t" \
  "ds_write_b128 v63, v[120:123]\n\t" \
  "ds_write2_b64 v63, v[28:29], v[38:39] offset0:128 offset1:129\n\t" \
  "v_mov_b32 v57, %[fa]\n\t" \
  "v_mov_b32 v58, 1\n\t" \
  "s_mov_b64 exec, 1\n\t" \
  "ds_write_b32 v57, v58\n\t" \
  "s_mov_b64 exec, -1\n\t" \
  "v_cvt_pk_bf16_f32 v160, v160, v161\n\t" \
  "v_cvt_pk_bf16_f32 v161, v162, v163\n\t" \
  "v_cvt_pk_bf16_f32 v162, v164, v165\n\t" \
  "v_cvt_pk_bf16_f32 v163, v166, v167\n\t" \
  "s_branch NX_%=\n\t" \
  "NB_%=:\n\t" \
  "ds_read_b64_tr_b16 v[40:41], v60 offset:14336\n\t" \
  "ds_read_b64_tr_b16 v[42:43], v60 offset:14848\n\t" \
  "ds_read_b64_tr_b16 v[44:45], v60 offset:13312\n\t" \
  "ds_read_b64_tr_b16 v[46:47], v60 offset:13824\n\t" \
  "ds_read_b64_tr_b16 v[48:49], v61 offset:14336\n\t" \
  "ds_read_b64_tr_b16 v[50:51], v61 offset:14848\n\t" \
  "ds_read_b128 v[0:3], v56 offset:36864\n\t" \
  "ds_read_b128 v[4:7], v56 offset:47104\n\t" \
  "ds_read_b128 v[8:11], v56 offset:49152\n\t" \
  "ds_read_b128 v[12:15], v56 offset:51200\n\t" \
  "ds_read_b128 v[16:19], v56 offset:53248\n\t" \
  "s_mov_b32 s68, 0x50000\n\t" \
  "s_mov_b32 s69, 0x5550055\n\t" \
  "s_mov_b32 s70, 0xa0000\n\t" \
  "s_mov_b32 s71, 0xaaa00aa\n\t" \
  "s_mov_b32 s72, 0x150001\n\t" \
  "s_mov_b32 s73, 0x15550155\n\t" \
  "s_mov_b32 s74, 0x2a0002\n\t" \
  "s_mov_b32 s75, 0x2aaa02aa\n\t" \
  "s_mov_b32 s76, 0x55505555\n\t" \
  "s_mov_b32 s77, 0x50005500\n\t" \
  "s_mov_b32 s78, 0xaaa0aaaa\n\t" \
  "s_mov_b32 s79, 0xa000aa00\n\t" \
  "s_mov_b32 s80, 0x55405554\n\t" \
  "s_mov_b32 s81, 0x40005400\n\t" \
  "s_mov_b32 s82, 0xaa80aaa8\n\t" \
  "s_mov_b32 s83, 0x8000a800\n\t" \
  "s_mov_b32 s84, 0xaaaaaaaa\n\t" \
  "s_mov_b32 s85, 0xaaaaaaaa\n\t" \
  "s_waitcnt lgkmcnt(0)\n\t" \
  "s_branch N4b_%=\n\t" \
  "N0b_%=:\n\t" \
  "s_waitcnt lgkmcnt(4)\n\t" \
  "v_mfma_f32_16x16x32_bf16 v[160:163], v[0:3], v[40:43], v[160:163]\n\t" \
  "ds_read_b64_tr_b16 v[52:53], v59 offset:1024\n\t" \
  "v_mfma_f32_16x16x32_bf16 v[128:131], v[4:7], v[40:43], v[128:131]\n\t" \
  "ds_read_b64_tr_b16 v[54:55], v59 offset:1536\n\t" \
  "v_mfma_f32_16x16x32_bf16 v[136:139], v[8:11], v[40:43], v[136:139]\n\t" \
  "s_barrier\n\t" \
  "v_mfma_f32_16x16x32_bf16 v[144:147], v[12:15], v[40:43], v[144:147]\n\t" \
  "v_mfma_f32_16x16x32_bf16 v[152:155], v[16:19], v[40:43], v[152:155]\n\t" \
  "s_waitcnt lgkmcnt(4)\n\t" \
  "v_mfma_f32_16x16x32_bf16 v[96:99], v[44:47], v[4:7], v[96:99]\n\t" \
  "ds_read_b64_tr_b16 v[40:41], v58 offset:0\n\t" \
  "v_mfma_f32_16x16x32_bf16 v[104:107], v[44:47], v[8:11], v[104:107]\n\t" \
  "ds_read_b64_tr_b16 v[42:43], v58 offset:512\n\t" \
  "v_mfma_f32_16x16x32_bf16 v[112:115], v[44:47], v[12:15], v[112:115]\n\t" \
  "ds_read_b128 v[20:23], v57 offset:0\n\t" \
  "v_mfma_f32_16x16x32_bf16 v[120:123], v[44:47], v[16:19], v[120:123]\n\t" \
  "ds_read_b128 v[24:27], v57 offset:10240\n\t" \
  "s_waitcnt lgkmcnt(6)\n\t" \
  "v_mfma_f32_16x16x32_bf16 v[164:167], v[0:3], v[48:51], v[164:167]\n\t" \
  "ds_read_b64_tr_b16 v[44:45], v59 offset:0\n\t" \
  "v_mfma_f32_16x16x32_bf16 v[132:135], v[4:7], v[48:51], v[132:135]\n\t" \
  "ds_read_b64_tr_b16 v[46:47], v59 offset:512\n\t" \
  "v_mfma_f32_16x16x32_bf16 v[140:143], v[8:11], v[48:51], v[140:143]\n\t" \
  "ds_read_b128 v[28:31], v57 offset:12288\n\t" \
  "v_mfma_f32_16x16x32_bf16 v[148:151], v[12:15], v[48:51], v[148:151]\n\t" \
  "ds_read_b128 v[32:35], v57 offset:14336\n\t" \
  "v_mfma_f32_16x16x32_bf16 v[156:159], v[16:19], v[48:51], v[156:159]\n\t" \
  "s_waitcnt lgkmcnt(8)\n\t" \
  "v_mfma_f32_16x16x32_bf16 v[100:103], v[52:55], v[4:7], v[100:103]\n\t" \
  "ds_read_b64_tr_b16 v[48:49], v58 offset:14336\n\t" \
  "v_mfma_f32_16x16x32_bf16 v[108:111], v[52:55], v[8:11], v[108:111]\n\t" \
  "ds_read_b64_tr_b16 v[50:51], v58 offset:14848\n\t" \
  "v_mfma_f32_16x16x32_bf16 v[116:119], v[52:55], v[12:15], v[116:119]\n\t" \
  "ds_read_b128 v[36:39], v57 offset:16384\n\t" \
  "v_mfma_f32_16x16x32_bf16 v[124:127], v[52:55], v[16:19], v[124:127]\n\t" \
  "s_waitcnt lgkmcnt(9)\n\t" \
  "v_mfma_f32_16x16x32_bf16 v[64:67], v[40:43], v[4:7], v[64:67]\n\t" \
  "ds_read_b64_tr_b16 v[52:53], v58 offset:13312\n\t" \
  "v_mfma_f32_16x16x32_bf16 v[72:75], v[40:43], v[8:11], v[72:75]\n\t" \
  "ds_read_b64_tr_b16 v[54:55], v58 offset:13824\n\t" \
  "v_mfma_f32_16x16x32_bf16 v[80:83], v[40:43], v[12:15], v[80:83]\n\t" \
  "v_mfma_f32_16x16x32_bf16 v[88:91], v[40:43], v[16:19], v[88:91]\n\t" \
  "s_waitcnt lgkmcnt(7)\n\t" \
  "v_mfma_f32_16x16x32_bf16 v[68:71], v[44:47], v[4:7], v[68:71]\n\t" \
  "ds_read_b64_tr_b16 v[40:41], v59 offset:14336\n\t" \
  "v_mfma_f32_16x16x32_bf16 v[76:79], v[44:47], v[8:11], v[76:79]\n\t" \
  "ds_read_b64_tr_b16 v[42:43], v59 offset:14848\n\t" \
  "v_mfma_f32_16x16x32_bf16 v[84:87], v[44:47], v[12:15], v[84:87]\n\t" \
  "v_mfma_f32_16x16x32_bf16 v[92:95], v[44:47], v[16:19], v[92:95]\n\t" \
  "N1b_%=:\n\t" \
  "s_waitcnt lgkmcnt(4)\n\t" \
  "v_mfma_f32_16x16x32_bf16 v[160:163], v[20:23], v[48:51], v[160:163]\n\t" \
  "ds_read_b64_tr_b16 v[44:45], v59 offset:13312\n\t" \
  "v_mfma_f32_16x16x32_bf16 v[128:131], v[24:27], v[48:51], v[128:131]\n\t" \
  "ds_read_b64_tr_b16 v[46:47], v59 offset:13824\n\t" \
  "v_mfma_f32_16x16x32_bf16 v[136:139], v[28:31], v[48:51], v[136:139]\n\t" \
  "s_barrier\n\t" \
  "v_mfma_f32_16x16x32_bf16 v[144:147], v[32:35], v[48:51], v[144:147]\n\t" \
  "v_mfma_f32_16x16x32_bf16 v[152:155], v[36:39], v[48:51], v[152:155]\n\t" \
  "s_waitcnt lgkmcnt(4)\n\t" \
  "v_mfma_f32_16x16x32_bf16 v[96:99], v[52:55], v[24:27], v[96:99]\n\t" \
  "ds_read_b64_tr_b16 v[48:49], v58 offset:12288\n\t" \
  "v_mfma_f32_16x16x32_bf16 v[104:107], v[52:55], v[28:31], v[104:107]\n\t" \
  "ds_read_b64_tr_b16 v[50:51], v58 offset:12800\n\t" \
  "v_mfma_f32_16x16x32_bf16 v[112:115], v[52:55], v[32:35], v[112:115]\n\t" \
  "ds_read_b128 v[0:3], v56 offset:18432\n\t" \
  "v_mfma_f32_16x16x32_bf16 v[120:123], v[52:55], v[36:39], v[120:123]\n\t" \
  "ds_read_b128 v[4:7], v56 offset:28672\n\t" \
  "s_waitcnt lgkmcnt(6)\n\t" \
  "v_mfma_f32_16x16x32_bf16 v[164:167], v[20:23], v[40:43], v[164:167]\n\t" \
  "ds_read_b64_tr_b16 v[52:53], v59 offset:12288\n\t" \
  "v_mfma_f32_16x16x32_bf16 v[132:135], v[24:27], v[40:43], v[132:135]\n\t" \
  "ds_read_b64_tr_b16 v[54:55], v59 offset:12800\n\t" \
  "v_mfma_f32_16x16x32_bf16 v[140:143], v[28:31], v[40:43], v[140:143]\n\t" \
  "ds_read_b128 v[8:11], v56 offset:30720\n\t" \
  "v_mfma_f32_16x16x32_bf16 v[148:151], v[32:35], v[40:43], v[148:151]\n\t" \
  "ds_read_b128 v[12:15], v56 offset:32768\n\t" \
  "v_mfma_f32_16x16x32_bf16 v[156:159], v[36:39], v[40:43], v[156:159]\n\t" \
  "s_waitcnt lgkmcnt(8)\n\t" \
  "v_mfma_f32_16x16x32_bf16 v[100:103], v[44:47], v[24:27], v[100:103]\n\t" \
  "ds_read_b64_tr_b16 v[40:41], v58 offset:26624\n\t" \
  "v_mfma_f32_16x16x32_bf16 v[108:111], v[44:47], v[28:31], v[108:111]\n\t" \
  "ds_read_b64_tr_b16 v[42:43], v58 offset:27136\n\t" \
  "v_mfma_f32_16x16x32_bf16 v[116:119], v[44:47], v[32:35], v[116:119]\n\t" \
  "ds_read_b128 v[16:19], v56 offset:34816\n\t" \
  "v_mfma_f32_16x16x32_bf16 v[124:127], v[44:47], v[36:39], v[124:127]\n\t" \
  "s_waitcnt lgkmcnt(9)\n\t" \
  "v_mfma_f32_16x16x32_bf16 v[64:67], v[48:51], v[24:27], v[64:67]\n\t" \
  "ds_read_b64_tr_b16 v[44:45], v58 offset:25600\n\t" \
  "v_mfma_f32_16x16x32_bf16 v[72:75], v[48:51], v[28:31], v[72:75]\n\t" \
  "ds_read_b64_tr_b16 v[46:47], v58 offset:26112\n\t" \
  "v_mfma_f32_16x16x32_bf16 v[80:83], v[48:51], v[32:35], v[80:83]\n\t" \
  "v_mfma_f32_16x16x32_bf16 v[88:91], v[48:51], v[36:39], v[88:91]\n\t" \
  "s_waitcnt lgkmcnt(7)\n\t" \
  "v_mfma_f32_16x16x32_bf16 v[68:71], v[52:55], v[24:27], v[68:71]\n\t" \
  "ds_read_b64_tr_b16 v[48:49], v59 offset:26624\n\t" \
  "v_mfma_f32_16x16x32_bf16 v[76:79], v[52:55], v[28:31], v[76:79]\n\t" \
  "ds_read_b64_tr_b16 v[50:51], v59 offset:27136\n\t" \
  "v_mfma_f32_16x16x32_bf16 v[84:87], v[52:55], v[32:35], v[84:87]\n\t" \
  "v_mfma_f32_16x16x32_bf16 v[92:95], v[52:55], v[36:39], v[92:95]\n\t" \
  "N2b_%=:\n\t" \
  "s_waitcnt lgkmcnt(4)\n\t" \
  "v_mfma_f32_16x16x32_bf16 v[160:163], v[0:3], v[40:43], v[160:163]\n\t" \
  "ds_read_b64_tr_b16 v[52:53], v59 offset:25600\n\t" \
  "v_mfma_f32_16x16x32_bf16 v[128:131], v[4:7], v[40:43], v[128:131]\n\t" \
  "ds_read_b64_tr_b16 v[54:55], v59 offset:26112\n\t" \
  "v_mfma_f32_16x16x32_bf16 v[136:139], v[8:11], v[40:43], v[136:139]\n\t" \
  "s_barrier\n\t" \
  "v_mfma_f32_16x16x32_bf16 v[144:147], v[12:15], v[40:43], v[144:147]\n\t" \
  "v_mfma_f32_16x16x32_bf16 v[152:155], v[16:19], v[40:43], v[152:155]\n\t" \
  "s_waitcnt lgkmcnt(4)\n\t" \
  "v_mfma_f32_16x16x32_bf16 v[96:99], v[44:47], v[4:7], v[96:99]\n\t" \
  "ds_read_b64_tr_b16 v[40:41], v58 offset:24576\n\t" \
  "v_mfma_f32_16x16x32_bf16 v[104:107], v[44:47], v[8:11], v[104:107]\n\t" \
  "ds_read_b64_tr_b16 v[42:43], v58 offset:25088\n\t" \
  "v_mfma_f32_16x16x32_bf16 v[112:115], v[44:47], v[12:15], v[112:115]\n\t" \
  "ds_read_b128 v[20:23], v57 offset:18432\n\t" \
  "v_mfma_f32_16x16x32_bf16 v[120:123], v[44:47], v[16:19], v[120:123]\n\t" \
  "ds_read_b128 v[24:27], v57 offset:28672\n\t" \
  "s_waitcnt lgkmcnt(6)\n\t" \
  "v_mfma_f32_16x16x32_bf16 v[164:167], v[0:3], v[48:51], v[164:167]\n\t" \
  "ds_read_b64_tr_b16 v[44:45], v59 offset:24576\n\t" \
  "v_mfma_f32_16x16x32_bf16 v[132:135], v[4:7], v[48:51], v[132:135]\n\t" \
  "ds_read_b64_tr_b16 v[46:47], v59 offset:25088\n\t" \
  "v_mfma_f32_16x16x32_bf16 v[140:143], v[8:11], v[48:51], v[140:143]\n\t" \
  "ds_read_b128 v[28:31], v57 offset:30720\n\t" \
  "v_mfma_f32_16x16x32_bf16 v[148:151], v[12:15], v[48:51], v[148:151]\n\t" \
  "ds_read_b128 v[32:35], v57 offset:32768\n\t" \
  "v_mfma_f32_16x16x32_bf16 v[156:159], v[16:19], v[48:51], v[156:159]\n\t" \
  "s_waitcnt lgkmcnt(8)\n\t" \
  "v_mfma_f32_16x16x32_bf16 v[100:103], v[52:55], v[4:7], v[100:103]\n\t" \
  "ds_read_b64_tr_b16 v[48:49], v60 offset:2048\n\t" \
  "v_mfma_f32_16x16x32_bf16 v[108:111], v[52:55], v[8:11], v[108:111]\n\t" \
  "ds_read_b64_tr_b16 v[50:51], v60 offset:2560\n\t" \
  "v_mfma_f32_16x16x32_bf16 v[116:119], v[52:55], v[12:15], v[116:119]\n\t" \
  "ds_read_b128 v[36:39], v57 offset:34816\n\t" \
  "v_mfma_f32_16x16x32_bf16 v[124:127], v[52:55], v[16:19], v[124:127]\n\t" \
  "s_waitcnt lgkmcnt(9)\n\t" \
  "v_mfma_f32_16x16x32_bf16 v[64:67], v[40:43], v[4:7], v[64:67]\n\t" \
  "ds_read_b64_tr_b16 v[52:53], v60 offset:1024\n\t" \
  "v_mfma_f32_16x16x32_bf16 v[72:75], v[40:43], v[8:11], v[72:75]\n\t" \
  "ds_read_b64_tr_b16 v[54:55], v60 offset:1536\n\t" \
  "v_mfma_f32_16x16x32_bf16 v[80:83], v[40:43], v[12:15], v[80:83]\n\t" \
  "v_mfma_f32_16x16x32_bf16 v[88:91], v[40:43], v[16:19], v[88:91]\n\t" \
  "s_waitcnt lgkmcnt(7)\n\t" \
  "v_mfma_f32_16x16x32_bf16 v[68:71], v[44:47], v[4:7], v[68:71]\n\t" \
  "ds_read_b64_tr_b16 v[40:41], v61 offset:2048\n\t" \
  "v_mfma_f32_16x16x32_bf16 v[76:79], v[44:47], v[8:11], v[76:79]\n\t" \
  "ds_read_b64_tr_b16 v[42:43], v61 offset:2560\n\t" \
  "v_mfma_f32_16x16x32_bf16 v[84:87], v[44:47], v[12:15], v[84:87]\n\t" \
  "v_mfma_f32_16x16x32_bf16 v[92:95], v[44:47], v[16:19], v[92:95]\n\t" \
  "N3b_%=:\n\t" \
  "s_waitcnt lgkmcnt(4)\n\t" \
  "v_mfma_f32_16x16x32_bf16 v[160:163], v[20:23], v[48:51], v[160:163]\n\t" \
  "ds_read_b64_tr_b16 v[44:45], v61 offset:1024\n\t" \
  "v_mfma_f32_16x16x32_bf16 v[128:131], v[24:27], v[48:51], v[128:131]\n\t" \
  "ds_read_b64_tr_b16 v[46:47], v61 offset:1536\n\t" \
  "v_mfma_f32_16x16x32_bf16 v[136:139], v[28:31], v[48:51], v[136:139]\n\t" \
  "s_barrier\n\t" \
  "v_mfma_f32_16x16x32_bf16 v[144:147], v[32:35], v[48:51], v[144:147]\n\t" \
  "v_mfma_f32_16x16x32_bf16 v[152:155], v[36:39], v[48:51], v[152:155]\n\t" \
  "s_waitcnt lgkmcnt(4)\n\t" \
  "v_mfma_f32_16x16x32_bf16 v[96:99], v[52:55], v[24:27], v[96:99]\n\t" \
  "ds_read_b64_tr_b16 v[48:49], v60 offset:0\n\t" \
  "v_mfma_f32_16x16x32_bf16 v[104:107], v[52:55], v[28:31], v[104:107]\n\t" \
  "ds_read_b64_tr_b16 v[50:51], v60 offset:512\n\t" \
  "v_mfma_f32_16x16x32_bf16 v[112:115], v[52:55], v[32:35], v[112:115]\n\t" \
  "ds_read_b128 v[0:3], v56 offset:36864\n\t" \
  "v_mfma_f32_16x16x32_bf16 v[120:123], v[52:55], v[36:39], v[120:123]\n\t" \
  "ds_read_b128 v[4:7], v56 offset:47104\n\t" \
  "s_waitcnt lgkmcnt(6)\n\t" \
  "v_mfma_f32_16x16x32_bf16 v[164:167], v[20:23], v[40:43], v[164:167]\n\t" \
  "ds_read_b64_tr_b16 v[52:53], v61 offset:0\n\t" \
  "v_mfma_f32_16x16x32_bf16 v[132:135], v[24:27], v[40:43], v[132:135]\n\t" \
  "ds_read_b64_tr_b16 v[54:55], v61 offset:512\n\t" \
  "v_mfma_f32_16x16x32_bf16 v[140:143], v[28:31], v[40:43], v[140:143]\n\t" \
  "ds_read_b128 v[8:11], v56 offset:49152\n\t" \
  "v_mfma_f32_16x16x32_bf16 v[148:151], v[32:35], v[40:43], v[148:151]\n\t" \
  "ds_read_b128 v[12:15], v56 offset:51200\n\t" \
  "v_mfma_f32_16x16x32_bf16 v[156:159], v[36:39], v[40:43], v[156:159]\n\t" \
  "s_waitcnt lgkmcnt(8)\n\t" \
  "v_mfma_f32_16x16x32_bf16 v[100:103], v[44:47], v[24:27], v[100:103]\n\t" \
  "ds_read_b64_tr_b16 v[40:41], v60 offset:14336\n\t" \
  "v_mfma_f32_16x16x32_bf16 v[108:111], v[44:47], v[28:31], v[108:111]\n\t" \
  "ds_read_b64_tr_b16 v[42:43], v60 offset:14848\n\t" \
  "v_mfma_f32_16x16x32_bf16 v[116:119], v[44:47], v[32:35], v[116:119]\n\t" \
  "ds_read_b128 v[16:19], v56 offset:53248\n\t" \
  "v_mfma_f32_16x16x32_bf16 v[124:127], v[44:47], v[36:39], v[124:127]\n\t" \
  "s_waitcnt lgkmcnt(9)\n\t" \
  "v_mfma_f32_16x16x32_bf16 v[64:67], v[48:51], v[24:27], v[64:67]\n\t" \
  "ds_read_b64_tr_b16 v[44:45], v60 offset:13312\n\t" \
  "v_mfma_f32_16x16x32_bf16 v[72:75], v[48:51], v[28:31], v[72:75]\n\t" \
  "ds_read_b64_tr_b16 v[46:47], v60 offset:13824\n\t" \
  "v_mfma_f32_16x16x32_bf16 v[80:83], v[48:51], v[32:35], v[80:83]\n\t" \
  "v_mfma_f32_16x16x32_bf16 v[88:91], v[48:51], v[36:39], v[88:91]\n\t" \
  "s_waitcnt lgkmcnt(7)\n\t" \
  "v_mfma_f32_16x16x32_bf16 v[68:71], v[52:55], v[24:27], v[68:71]\n\t" \
  "ds_read_b64_tr_b16 v[48:49], v61 offset:14336\n\t" \
  "v_mfma_f32_16x16x32_bf16 v[76:79], v[52:55], v[28:31], v[76:79]\n\t" \
  "ds_read_b64_tr_b16 v[50:51], v61 offset:14848\n\t" \
  "v_mfma_f32_16x16x32_bf16 v[84:87], v[52:55], v[32:35], v[84:87]\n\t" \
  "v_mfma_f32_16x16x32_bf16 v[92:95], v[52:55], v[36:39], v[92:95]\n\t" \
  "N4b_%=:\n\t" \
  "s_waitcnt lgkmcnt(4)\n\t" \
  "v_mfma_f32_16x16x32_bf16 v[160:163], v[0:3], v[40:43], v[160:163]\n\t" \
  "ds_read_b64_tr_b16 v[52:53], v61 offset:13312\n\t" \
  "v_mfma_f32_16x16x32_bf16 v[128:131], v[4:7], v[40:43], v[128:131]\n\t" \
  "ds_read_b64_tr_b16 v[54:55], v61 offset:13824\n\t" \
  "v_mfma_f32_16x16x32_bf16 v[136:139], v[8:11], v[40:43], v[136:139]\n\t" \
  "s_barrier\n\t" \
  "v_mfma_f32_16x16x32_bf16 v[144:147], v[12:15], v[40:43], v[144:147]\n\t" \
  "v_mfma_f32_16x16x32_bf16 v[152:155], v[16:19], v[40:43], v[152:155]\n\t" \
  "s_waitcnt lgkmcnt(4)\n\t" \
  "v_mfma_f32_16x16x32_bf16 v[96:99], v[44:47], v[4:7], v[96:99]\n\t" \
  "ds_read_b64_tr_b16 v[40:41], v60 offset:12288\n\t" \
  "v_mfma_f32_16x16x32_bf16 v[104:107], v[44:47], v[8:11], v[104:107]\n\t" \
  "ds_read_b64_tr_b16 v[42:43], v60 offset:12800\n\t" \
  "v_mfma_f32_16x16x32_bf16 v[112:115], v[44:47], v[12:15], v[112:115]\n\t" \
  "ds_read_b128 v[20:23], v57 offset:36864\n\t" \
  "v_mfma_f32_16x16x32_bf16 v[120:123], v[44:47], v[16:19], v[120:123]\n\t" \
  "ds_read_b128 v[24:27], v57 offset:47104\n\t" \
  "s_waitcnt lgkmcnt(6)\n\t" \
  "v_mfma_f32_16x16x32_bf16 v[164:167], v[0:3], v[48:51], v[164:167]\n\t" \
  "ds_read_b64_tr_b16 v[44:45], v61 offset:12288\n\t" \
  "v_mfma_f32_16x16x32_bf16 v[132:135], v[4:7], v[48:51], v[132:135]\n\t" \
  "ds_read_b64_tr_b16 v[46:47], v61 offset:12800\n\t" \
  "v_mfma_f32_16x16x32_bf16 v[140:143], v[8:11], v[48:51], v[140:143]\n\t" \
  "ds_read_b128 v[28:31], v57 offset:49152\n\t" \
  "v_mfma_f32_16x16x32_bf16 v[148:151], v[12:15], v[48:51], v[148:151]\n\t" \
  "ds_read_b128 v[32:35], v57 offset:51200\n\t" \
  "v_mfma_f32_16x16x32_bf16 v[156:159], v[16:19], v[48:51], v[156:159]\n\t" \
  "s_waitcnt lgkmcnt(8)\n\t" \
  "v_mfma_f32_16x16x32_bf16 v[100:103], v[52:55], v[4:7], v[100:103]\n\t" \
  "ds_read_b64_tr_b16 v[48:49], v60 offset:26624\n\t" \
  "v_mfma_f32_16x16x32_bf16 v[108:111], v[52:55], v[8:11], v[108:111]\n\t" \
  "ds_read_b64_tr_b16 v[50:51], v60 offset:27136\n\t" \
  "v_mfma_f32_16x16x32_bf16 v[116:119], v[52:55], v[12:15], v[116:119]\n\t" \
  "ds_read_b128 v[36:39], v57 offset:53248\n\t" \
  "v_mfma_f32_16x16x32_bf16 v[124:127], v[52:55], v[16:19], v[124:127]\n\t" \
  "s_waitcnt lgkmcnt(9)\n\t" \
  "v_mfma_f32_16x16x32_bf16 v[64:67], v[40:43], v[4:7], v[64:67]\n\t" \
  "ds_read_b64_tr_b16 v[52:53], v60 offset:25600\n\t" \
  "v_mfma_f32_16x16x32_bf16 v[72:75], v[40:43], v[8:11], v[72:75]\n\t" \
  "ds_read_b64_tr_b16 v[54:55], v60 offset:26112\n\t" \
  "v_mfma_f32_16x16x32_bf16 v[80:83], v[40:43], v[12:15], v[80:83]\n\t" \
  "v_mfma_f32_16x16x32_bf16 v[88:91], v[40:43], v[16:19], v[88:91]\n\t" \
  "s_waitcnt lgkmcnt(7)\n\t" \
  "v_mfma_f32_16x16x32_bf16 v[68:71], v[44:47], v[4:7], v[68:71]\n\t" \
  "ds_read_b64_tr_b16 v[40:41], v61 offset:26624\n\t" \
  "v_mfma_f32_16x16x32_bf16 v[76:79], v[44:47], v[8:11], v[76:79]\n\t" \
  "ds_read_b64_tr_b16 v[42:43], v61 offset:27136\n\t" \
  "v_mfma_f32_16x16x32_bf16 v[84:87], v[44:47], v[12:15], v[84:87]\n\t" \
  "v_mfma_f32_16x16x32_bf16 v[92:95], v[44:47], v[16:19], v[92:95]\n\t" \
  "N5b_%=:\n\t" \
  "s_waitcnt lgkmcnt(4)\n\t" \
  "v_mfma_f32_16x16x32_bf16 v[160:163], v[20:23], v[48:51], v[160:163]\n\t" \
  "ds_read_b64_tr_b16 v[44:45], v61 offset:25600\n\t" \
  "v_mfma_f32_16x16x32_bf16 v[128:131], v[24:27], v[48:51], v[128:131]\n\t" \
  "ds_read_b64_tr_b16 v[46:47], v61 offset:26112\n\t" \
  "v_mfma_f32_16x16x32_bf16 v[136:139], v[28:31], v[48:51], v[136:139]\n\t" \
  "s_barrier\n\t" \
  "v_mfma_f32_16x16x32_bf16 v[144:147], v[32:35], v[48:51], v[144:147]\n\t" \
  "v_mfma_f32_16x16x32_bf16 v[152:155], v[36:39], v[48:51], v[152:155]\n\t" \
  "s_waitcnt lgkmcnt(4)\n\t" \
  "v_mfma_f32_16x16x32_bf16 v[96:99], v[52:55], v[24:27], v[96:99]\n\t" \
  "ds_read_b64_tr_b16 v[48:49], v60 offset:24576\n\t" \
  "v_mfma_f32_16x16x32_bf16 v[104:107], v[52:55], v[28:31], v[104:107]\n\t" \
  "ds_read_b64_tr_b16 v[50:51], v60 offset:25088\n\t" \
  "v_mfma_f32_16x16x32_bf16 v[112:115], v[52:55], v[32:35], v[112:115]\n\t" \
  "ds_read_b128 v[0:3], v56 offset:0\n\t" \
  "v_mfma_f32_16x16x32_bf16 v[120:123], v[52:55], v[36:39], v[120:123]\n\t" \
  "ds_read_b128 v[4:7], v56 offset:10240\n\t" \
  "s_waitcnt lgkmcnt(6)\n\t" \
  "v_mfma_f32_16x16x32_bf16 v[164:167], v[20:23], v[40:43], v[164:167]\n\t" \
  "ds_read_b64_tr_b16 v[52:53], v61 offset:24576\n\t" \
  "v_mfma_f32_16x16x32_bf16 v[132:135], v[24:27], v[40:43], v[132:135]\n\t" \
  "ds_read_b64_tr_b16 v[54:55], v61 offset:25088\n\t" \
  "v_mfma_f32_16x16x32_bf16 v[140:143], v[28:31], v[40:43], v[140:143]\n\t" \
  "ds_read_b128 v[8:11], v56 offset:12288\n\t" \
  "v_mfma_f32_16x16x32_bf16 v[148:151], v[32:35], v[40:43], v[148:151]\n\t" \
  "ds_read_b128 v[12:15], v56 offset:14336\n\t" \
  "v_mfma_f32_16x16x32_bf16 v[156:159], v[36:39], v[40:43], v[156:159]\n\t" \
  "s_waitcnt lgkmcnt(8)\n\t" \
  "v_mfma_f32_16x16x32_bf16 v[100:103], v[44:47], v[24:27], v[100:103]\n\t" \
  "ds_read_b64_tr_b16 v[40:41], v58 offset:2048\n\t" \
  "v_mfma_f32_16x16x32_bf16 v[108:111], v[44:47], v[28:31], v[108:111]\n\t" \
  "ds_read_b64_tr_b16 v[42:43], v58 offset:2560\n\t" \
  "v_mfma_f32_16x16x32_bf16 v[116:119], v[44:47], v[32:35], v[116:119]\n\t" \
  "ds_read_b128 v[16:19], v56 offset:16384\n\t" \
  "v_mfma_f32_16x16x32_bf16 v[124:127], v[44:47], v[36:39], v[124:127]\n\t" \
  "s_waitcnt lgkmcnt(9)\n\t" \
  "v_mfma_f32_16x16x32_bf16 v[64:67], v[48:51], v[24:27], v[64:67]\n\t" \
  "ds_read_b64_tr_b16 v[44:45], v58 offset:1024\n\t" \
  "v_mfma_f32_16x16x32_bf16 v[72:75], v[48:51], v[28:31], v[72:75]\n\t" \
  "ds_read_b64_tr_b16 v[46:47], v58 offset:1536\n\t" \
  "v_mfma_f32_16x16x32_bf16 v[80:83], v[48:51], v[32:35], v[80:83]\n\t" \
  "v_mfma_f32_16x16x32_bf16 v[88:91], v[48:51], v[36:39], v[88:91]\n\t" \
  "s_waitcnt lgkmcnt(7)\n\t" \
  "v_mfma_f32_16x16x32_bf16 v[68:71], v[52:55], v[24:27], v[68:71]\n\t" \
  "ds_read_b64_tr_b16 v[48:49], v59 offset:2048\n\t" \
  "v_mfma_f32_16x16x32_bf16 v[76:79], v[52:55], v[28:31], v[76:79]\n\t" \
  "ds_read_b64_tr_b16 v[50:51], v59 offset:2560\n\t" \
  "s_addk_i32 %[it], 1\n\t" \
  "v_mfma_f32_16x16x32_bf16 v[84:87], v[52:55], v[32:35], v[84:87]\n\t" \
  "s_cmp_lt_u32 %[it], 6\n\t" \
  "v_mfma_f32_16x16x32_bf16 v[92:95], v[52:55], v[36:39], v[92:95]\n\t" \
  "s_cbranch_scc1 N0b_%=\n\t" \
  "s_memrealtime %[st2]\n\t" \
  "s_setprio 3\n\t" \
  "v_cvt_pk_bf16_f32 v160, v160, v161\n\t" \
  "v_cvt_pk_bf16_f32 v161, v162, v163\n\t" \
  "v_cvt_pk_bf16_f32 v162, v164, v165\n\t" \
  "v_cvt_pk_bf16_f32 v163, v166, v167\n\t" \
  "ds_write_b128 v63, v[160:163] offset:2048\n\t" \
  "v_mov_b32 v57, %[fa]\n\t" \
  "v_mov_b32 v58, 1\n\t" \
  "s_mov_b64 exec, 1\n\t" \
  "ds_write_b32 v57, v58\n\t" \
  "s_mov_b64 exec, -1\n\t" \
  "v_cvt_pk_bf16_f32 v120, v120, v121\n\t" \
  "v_cvt_pk_bf16_f32 v121, v122, v123\n\t" \
  "v_cvt_pk_bf16_f32 v122, v124, v125\n\t" \
  "v_cvt_pk_bf16_f32 v123, v126, v127\n\t" \
  "v_cvt_pk_bf16_f32 v28, v152, v153\n\t" \
  "v_cvt_pk_bf16_f32 v29, v154, v155\n\t" \
  "v_cvt_pk_bf16_f32 v38, v156, v157\n\t" \
  "v_cvt_pk_bf16_f32 v39, v158, v159\n\t" \
  "NX_%=:\n\t" \
  "v_cvt_pk_bf16_f32 v96, v96, v97\n\t" \
  "v_cvt_pk_bf16_f32 v97, v98, v99\n\t" \
  "v_cvt_pk_bf16_f32 v98, v100, v101\n\t" \
  "v_cvt_pk_bf16_f32 v99, v102, v103\n\t" \
  "v_cvt_pk_bf16_f32 v104, v104, v105\n\t" \
  "v_cvt_pk_bf16_f32 v105, v106, v107\n\t" \
  "v_cvt_pk_bf16_f32 v106, v108, v109\n\t" \
  "v_cvt_pk_bf16_f32 v107, v110, v111\n\t" \
  "v_mov_b32 v60, 0xff61b1e6\n\t" \
  "v_cndmask_b32_e64 v56, v60, 0, s[68:69]\n\t" \
  "v_cndmask_b32_e64 v57, v60, 0, s[70:71]\n\t" \
  "v_cndmask_b32_e64 v58, v60, 0, s[72:73]\n\t" \
  "v_cndmask_b32_e64 v59, v60, 0, s[74:75]\n\t" \
  "v_cvt_pk_bf16_f32 v72, v72, v73\n\t" \
  "v_cvt_pk_bf16_f32 v73, v74, v75\n\t" \
  "v_cvt_pk_bf16_f32 v74, v76, v77\n\t" \
  "v_cvt_pk_bf16_f32 v75, v78, v79\n\t" \
  "v_cndmask_b32_e64 v52, v60, 0, s[76:77]\n\t" \
  "v_cndmask_b32_e64 v53, v60, 0, s[78:79]\n\t" \
  "v_cndmask_b32_e64 v54, v60, 0, s[80:81]\n\t" \
  "v_cndmask_b32_e64 v55, v60, 0, s[82:83]\n\t" \
  "v_cvt_pk_bf16_f32 v24, v136, v137\n\t" \
  "v_cvt_pk_bf16_f32 v25, v138, v139\n\t" \
  "v_cvt_pk_bf16_f32 v34, v140, v141\n\t" \
  "v_cvt_pk_bf16_f32 v35, v142, v143\n\t" \
  "v_cvt_pk_bf16_f32 v26, v144, v145\n\t" \
  "v_cvt_pk_bf16_f32 v27, v146, v147\n\t" \
  "v_cvt_pk_bf16_f32 v36, v148, v149\n\t" \
  "v_cvt_pk_bf16_f32 v37, v150, v151\n\t" \
  "v_mfma_f32_16x16x32_bf16 v[136:139], v[96:99], v[72:75], v[56:59]\n\t" \
  "v_cvt_pk_bf16_f32 v112, v112, v113\n\t" \
  "v_cvt_pk_bf16_f32 v113, v114, v115\n\t" \
  "v_mfma_f32_16x16x32_bf16 v[140:143], v[104:107], v[72:75], v[52:55]\n\t" \
  "v_cvt_pk_bf16_f32 v114, v116, v117\n\t" \
  "v_cvt_pk_bf16_f32 v115, v118, v119\n\t" \
  "v_cvt_pk_bf16_f32 v80, v80, v81\n\t" \
  "v_cvt_pk_bf16_f32 v81, v82, v83\n\t" \
  "v_cvt_pk_bf16_f32 v82, v84, v85\n\t" \
  "v_cvt_pk_bf16_f32 v83, v86, v87\n\t" \
  "v_cvt_pk_bf16_f32 v22, v128, v129\n\t" \
  "v_cvt_pk_bf16_f32 v23, v130, v131\n\t" \
  "v_mfma_f32_16x16x32_bf16 v[144:147], v[104:107], v[80:83], v[56:59]\n\t" \
  "v_cvt_pk_bf16_f32 v32, v132, v133\n\t" \
  "v_cvt_pk_bf16_f32 v33, v134, v135\n\t" \
  "v_mfma_f32_16x16x32_bf16 v[148:151], v[112:115], v[80:83], v[52:55]\n\t" \
  "v_cvt_pk_bf16_f32 v88, v88, v89\n\t" \
  "v_cvt_pk_bf16_f32 v89, v90, v91\n\t" \
  "v_cvt_pk_bf16_f32 v90, v92, v93\n\t" \
  "v_cvt_pk_bf16_f32 v91, v94, v95\n\t" \
  "v_mov_b32 v61, %[fp]\n\t" \
  "v_add_u32 v60, %[vo], v63\n\t" \
  "v_mfma_f32_16x16x32_bf16 v[152:155], v[112:115], v[88:91], v[56:59]\n\t" \
  "ds_read_b32 v0, v61\n\t" \
  "ds_read_b64 v[20:21], v60\n\t" \
  "ds_read_b64 v[30:31], v60 offset:8\n\t" \
  "s_mov_b64 exec, %[rm]\n\t" \
  "ds_read_b128 v[160:163], v63\n\t" \
  "s_mov_b64 exec, -1\n\t" \
  "v_mfma_f32_16x16x32_bf16 v[156:159], v[120:123], v[88:91], v[52:55]\n\t" \
  "v_cvt_pk_bf16_f32 v64, v64, v65\n\t" \
  "v_cvt_pk_bf16_f32 v65, v66, v67\n\t" \
  "v_cvt_pk_bf16_f32 v66, v68, v69\n\t" \
  "v_cvt_pk_bf16_f32 v67, v70, v71\n\t" \
  "v_max_f32 v136, v136, v137\n\t" \
  "v_max_f32 v138, v138, v139\n\t" \
  "v_mfma_f32_16x16x32_bf16 v[132:135], v[96:99], v[64:67], v[52:55]\n\t" \
  "v_max_f32 v140, v140, v141\n\t" \
  "v_max_f32 v142, v142, v143\n\t" \
  "AW_%=:\n\t" \
  "s_waitcnt lgkmcnt(0)\n\t" \
  "v_cmp_eq_u32 vcc, 0, v0\n\t" \
  "v_max3_f32 v5, v136, v138, v140\n\t" \
  "v_max_f32 v5, v5, v142\n\t" \
  "s_cbranch_vccnz AR_%=\n\t" \
  "s_cmp_eq_u32 %[z], 0\n\t" \
  "s_cbranch_scc1 AZ_%=\n\t" \
  "AY_%=:\n\t" \
  "v_mfma_f32_16x16x32_bf16 v[128:131], v[160:163], v[64:67], v[56:59]\n\t" \
  "v_max_f32 v144, v144, v145\n\t" \
  "v_max_f32 v146, v146, v147\n\t" \
  "v_max_f32 v148, v148, v149\n\t" \
  "v_max_f32 v150, v150, v151\n\t" \
  "v_max3_f32 v6, v144, v146, v148\n\t" \
  "v_max_f32 v6, v6, v150\n\t" \
  "v_max_f32 v152, v152, v153\n\t" \
  "v_max_f32 v154, v154, v155\n\t" \
  "v_max_f32 v156, v156, v157\n\t" \
  "v_max_f32 v158, v158, v159\n\t" \
  "v_max3_f32 v7, v152, v154, v156\n\t" \
  "v_max_f32 v7, v7, v158\n\t" \
  "v_mov_b32 v8, 0xc0c0100\n\t" \
  "v_mov_b32 v9, 0x1000c0c\n\t" \
  "v_max_f32 v128, v128, v129\n\t" \
  "v_max_f32 v130, v130, v131\n\t" \
  "v_max_f32 v132, v132, v133\n\t" \
  "v_max_f32 v134, v134, v135\n\t" \
  "v_max3_f32 v4, v128, v130, v132\n\t" \
  "v_max_f32 v4, v4, v134\n\t" \
  "v_cndmask_b32_e64 v60, v8, v9, s[84:85]\n\t" \
  "v_mov_b32 v8, 0xc0c0302\n\t" \
  "v_permlane16_swap_b32 v6, v7\n\t" \
  "v_permlane16_swap_b32 v4, v5\n\t" \
  "v_max_f32 v6, v6, v7\n\t" \
  "v_max_f32 v4, v4, v5\n\t" \
  "v_mov_b32 v9, 0x3020c0c\n\t" \
  "v_cndmask_b32_e64 v61, v8, v9, s[84:85]\n\t" \
  "v_permlane32_swap_b32 v4, v6\n\t" \
  "v_max_f32 v4, v4, v6\n\t" \
  "v_mul_f32 v6, 0xbe8293ee, v4\n\t" \
  "v_mul_f32 v4, 0xbe8293ee, v4\n\t" \
  "s_nop 1\n\t" \
  "v_permlane32_swap_b32 v4, v6\n\t" \
  "v_mov_b32 v5, v4\n\t" \
  "v_mov_b32 v7, v6\n\t" \
  "s_nop 0\n\t" \
  "v_permlane16_swap_b32 v4, v5\n\t" \
  "v_permlane16_swap_b32 v6, v7\n\t" \
  "v_fmamk_f32 v128, v128, 0x3e8293ee, v4\n\t" \
  "v_fmamk_f32 v130, v130, 0x3e8293ee, v4\n\t" \
  "v_fmamk_f32 v132, v132, 0x3e8293ee, v4\n\t" \
  "v_fmamk_f32 v134, v134, 0x3e8293ee, v4\n\t" \
  "v_fmamk_f32 v136, v136, 0x3e8293ee, v5\n\t" \
  "v_fmamk_f32 v138, v138, 0x3e8293ee, v5\n\t" \
  "v_fmamk_f32 v140, v140, 0x3e8293ee, v5\n\t" \
  "v_fmamk_f32 v142, v142, 0x3e8293ee, v5\n\t" \
  "v_fmamk_f32 v144, v144, 0x3e8293ee, v6\n\t" \
  "v_fmamk_f32 v146, v146, 0x3e8293ee, v6\n\t" \
  "v_fmamk_f32 v148, v148, 0x3e8293ee, v6\n\t" \
  "v_fmamk_f32 v150, v150, 0x3e8293ee, v6\n\t" \
  "v_fmamk_f32 v152, v152, 0x3e8293ee, v7\n\t" \
  "v_fmamk_f32 v154, v154, 0x3e8293ee, v7\n\t" \
  "v_fmamk_f32 v156, v156, 0x3e8293ee, v7\n\t" \
  "v_fmamk_f32 v158, v158, 0x3e8293ee, v7\n\t" \
  "v_exp_f32 v128, v128\n\t" \
  "v_exp_f32 v130, v130\n\t" \
  "v_exp_f32 v132, v132\n\t" \
  "v_exp_f32 v134, v134\n\t" \
  "v_exp_f32 v136, v136\n\t" \
  "v_exp_f32 v138, v138\n\t" \
  "v_exp_f32 v140, v140\n\t" \
  "v_exp_f32 v142, v142\n\t" \
  "v_exp_f32 v144, v144\n\t" \
  "v_exp_f32 v146, v146\n\t" \
  "v_exp_f32 v148, v148\n\t" \
  "v_exp_f32 v150, v150\n\t" \
  "v_exp_f32 v152, v152\n\t" \
  "v_exp_f32 v154, v154\n\t" \
  "v_exp_f32 v156, v156\n\t" \
  "v_exp_f32 v158, v158\n\t" \
  "v_add_f32 v4, v128, v130\n\t" \
  "v_add_f32 v8, v132, v134\n\t" \
  "v_add_f32 v5, v136, v138\n\t" \
  "v_add_f32 v9, v140, v142\n\t" \
  "v_add_f32 v6, v144, v146\n\t" \
  "v_add_f32 v10, v148, v150\n\t" \
  "v_add_f32 v7, v152, v154\n\t" \
  "v_add_f32 v11, v156, v158\n\t" \
  "v_add_f32 v4, v4, v8\n\t" \
  "v_add_f32 v5, v5, v9\n\t" \
  "v_add_f32 v6, v6, v10\n\t" \
  "v_add_f32 v7, v7, v11\n\t" \
  "v_permlane16_swap_b32 v4, v5\n\t" \
  "s_nop 0\n\t" \
  "v_permlane16_swap_b32 v6, v7\n\t" \
  "v_add_f32 v4, v4, v5\n\t" \
  "v_add_f32 v6, v6, v7\n\t" \
  "s_nop 1\n\t" \
  "v_permlane32_swap_b32 v4, v6\n\t" \
  "v_add_f32 v4, v4, v6\n\t" \
  "v_rcp_f32 v4, v4\n\t" \
  "s_nop 0\n\t" \
  "v_mov_b32 v6, v4\n\t" \
  "s_nop 1\n\t" \
  "v_permlane32_swap_b32 v4, v6\n\t" \
  "v_mov_b32 v5, v4\n\t" \
  "v_mov_b32 v7, v6\n\t" \
  "s_nop 0\n\t" \
  "v_permlane16_swap_b32 v4, v5\n\t" \
  "v_permlane16_swap_b32 v6, v7\n\t" \
  "v_mul_f32 v128, v128, v4\n\t" \
  "v_mul_f32 v130, v130, v4\n\t" \
  "v_mul_f32 v132, v132, v4\n\t" \
  "v_mul_f32 v134, v134, v4\n\t" \
  "v_cvt_pk_bf16_f32 v128, v128, v130\n\t" \
  "v_cvt_pk_bf16_f32 v132, v132, v134\n\t" \
  "v_perm_b32 v68, 0, v128, v60\n\t" \
  "v_perm_b32 v69, 0, v128, v61\n\t" \
  "v_perm_b32 v70, 0, v132, v60\n\t" \
  "v_perm_b32 v71, 0, v132, v61\n\t" \
  "v_mul_f32 v136, v136, v5\n\t" \
  "v_mul_f32 v138, v138, v5\n\t" \
  "v_mfma_f32_16x16x32_bf16 v[128:131], v[20:23], v[68:71], 0\n\t" \
  "v_mul_f32 v140, v140, v5\n\t" \
  "v_mul_f32 v142, v142, v5\n\t" \
  "v_cvt_pk_bf16_f32 v136, v136, v138\n\t" \
  "v_mfma_f32_16x16x32_bf16 v[132:135], v[30:33], v[68:71], 0\n\t" \
  "v_cvt_pk_bf16_f32 v140, v140, v142\n\t" \
  "v_perm_b32 v76, 0, v136, v60\n\t" \
  "v_perm_b32 v77, 0, v136, v61\n\t" \
  "v_perm_b32 v78, 0, v140, v60\n\t" \
  "v_perm_b32 v79, 0, v140, v61\n\t" \
  "v_mul_f32 v144, v144, v6\n\t" \
  "v_mul_f32 v146, v146, v6\n\t" \
  "v_mfma_f32_16x16x32_bf16 v[136:139], v[22:25], v[76:79], 0\n\t" \
  "v_mul_f32 v148, v148, v6\n\t" \
  "v_mul_f32 v150, v150, v6\n\t" \
  "v_cvt_pk_bf16_f32 v144, v144, v146\n\t" \
  "v_mfma_f32_16x16x32_bf16 v[140:143], v[32:35], v[76:79], 0\n\t" \
  "v_cvt_pk_bf16_f32 v148, v148, v150\n\t" \
  "v_perm_b32 v84, 0, v144, v60\n\t" \
  "v_perm_b32 v85, 0, v144, v61\n\t" \
  "v_perm_b32 v86, 0, v148, v60\n\t" \
  "v_perm_b32 v87, 0, v148, v61\n\t" \
  "v_cvt_pk_bf16_f32 v100, v128, v129\n\t" \
  "v_cvt_pk_bf16_f32 v101, v130, v131\n\t" \
  "v_mfma_f32_16x16x32_bf16 v[144:147], v[24:27], v[84:87], 0\n\t" \
  "v_cvt_pk_bf16_f32 v102, v132, v133\n\t" \
  "v_cvt_pk_bf16_f32 v103, v134, v135\n\t" \
  "global_store_dwordx4 v62, v[100:103], %[ot] offset:0 sc1\n\t" \
  "v_mfma_f32_16x16x32_bf16 v[148:151], v[34:37], v[84:87], 0\n\t" \
  "v_mul_f32 v152, v152, v7\n\t" \
  "v_mul_f32 v154, v154, v7\n\t" \
  "v_mul_f32 v156, v156, v7\n\t" \
  "v_mul_f32 v158, v158, v7\n\t" \
  "v_cvt_pk_bf16_f32 v152, v152, v154\n\t" \
  "v_cvt_pk_bf16_f32 v156, v156, v158\n\t" \
  "v_perm_b32 v92, 0, v152, v60\n\t" \
  "v_perm_b32 v93, 0, v152, v61\n\t" \
  "v_perm_b32 v94, 0, v156, v60\n\t" \
  "v_perm_b32 v95, 0, v156, v61\n\t" \
  "v_cvt_pk_bf16_f32 v108, v136, v137\n\t" \
  "v_cvt_pk_bf16_f32 v109, v138, v139\n\t" \
  "v_mfma_f32_16x16x32_bf16 v[152:155], v[26:29], v[92:95], 0\n\t" \
  "v_cvt_pk_bf16_f32 v110, v140, v141\n\t" \
  "v_cvt_pk_bf16_f32 v111, v142, v143\n\t" \
  "global_store_dwordx4 v62, v[108:111], %[ot] offset:1024 sc1\n\t" \
  "v_mfma_f32_16x16x32_bf16 v[156:159], v[36:39], v[92:95], 0\n\t" \
  "v_cvt_pk_bf16_f32 v116, v144, v145\n\t" \
  "v_cvt_pk_bf16_f32 v117, v146, v147\n\t" \
  "v_cvt_pk_bf16_f32 v118, v148, v149\n\t" \
  "v_cvt_pk_bf16_f32 v119, v150, v151\n\t" \
  "global_store_dwordx4 v62, v[116:119], %[ou] offset:0 sc1\n\t" \
  "v_cvt_pk_bf16_f32 v124, v152, v153\n\t" \
  "v_cvt_pk_bf16_f32 v125, v154, v155\n\t" \
  "s_nop 0\n\t" \
  "v_cvt_pk_bf16_f32 v126, v156, v157\n\t" \
  "v_cvt_pk_bf16_f32 v127, v158, v159\n\t" \
  "global_store_dwordx4 v62, v[124:127], %[ou] offset:1024 sc1\n\t" \
  "s_branch AE_%=\n\t" \
  "AR_%=:\n\t" \
  "s_addk_i32 %[it], 1\n\t" \
  "s_cmp_gt_u32 %[it], 0x400000\n\t" \
  "s_cbranch_scc1 AY_%=\n\t" \
  "ds_read_b32 v0, v61\n\t" \
  "ds_read_b64 v[20:21], v60\n\t" \
  "ds_read_b64 v[30:31], v60 offset:8\n\t" \
  "s_mov_b64 exec, %[rm]\n\t" \
  "ds_read_b128 v[160:163], v63\n\t" \
  "s_mov_b64 exec, -1\n\t" \
  "s_branch AW_%=\n\t" \
  "AZ_%=:\n\t" \
  "v_mov_b32 v0, 0xff61b1e6\n\t" \
  "v_mov_b32 v1, 0xcfa8965f\n\t" \
  "s_nop 3\n\t" \
  "v_cndmask_b32_e64 v56, v0, v1, s[68:69]\n\t" \
  "v_cndmask_b32_e64 v57, v0, v1, s[70:71]\n\t" \
  "v_cndmask_b32_e64 v58, v0, v1, s[72:73]\n\t" \
  "v_cndmask_b32_e64 v59, v0, v1, s[74:75]\n\t" \
  "s_nop 1\n\t" \
  "s_branch AY_%=\n\t" \
  "AE_%=:\n\t" \
  "s_nop 1\n\t"
#define N2_CLOB "v0", "v1", "v2", "v3", "v4", "v5", "v6", "v7", "v8", "v9", "v10", "v11", "v12", "v13", "v14", "v15", "v16", "v17", "v18", "v19", "v20", "v21", "v22", "v23", "v24", "v25", "v26", "v27", "v28", "v29", "v30", "v31", "v32", "v33", "v34", "v35", "v36", "v37", "v38", "v39", "v40", "v41", "v42", "v43", "v44", "v45", "v46", "v47", "v48", "v49", "v50", "v51", "v52", "v53", "v54", "v55", "v60", "v61", "v64", "v65", "v66", "v67", "v68", "v69", "v70", "v71", "v72", "v73", "v74", "v75", "v76", "v77", "v78", "v79", "v80", "v81", "v82", "v83", "v84", "v85", "v86", "v87", "v88", "v89", "v90", "v91", "v92", "v93", "v94", "v95", "v96", "v97", "v98", "v99", "v100", "v101", "v102", "v103", "v104", "v105", "v106", "v107", "v108", "v109", "v110", "v111", "v112", "v113", "v114", "v115", "v116", "v117", "v118", "v119", "v120", "v121", "v122", "v123", "v124", "v125", "v126", "v127", "v128", "v129", "v130", "v131", "v132", "v133", "v134", "v135", "v136", "v137", "v138", "v139", "v140", "v141", "v142", "v143", "v144", "v145", "v146", "v147", "v148", "v149", "v150", "v151", "v152", "v153", "v154", "v155", "v156", "v157", "v158", "v159", "v160", "v161", "v162", "v163", "v164", "v165", "v166", "v167", "s68", "s69", "s70", "s71", "s72", "s73", "s74", "s75", "s76", "s77", "s78", "s79", "s80", "s81", "s82", "s83", "s84", "s85"

#define CV_Y 1
#define CV_ASM \
  "s_mov_b64 vcc, 1\n\t" \
  "ds_read_b64 v[0:1], v28 offset:0\n\t" \
  "ds_read_b64 v[2:3], v29 offset:0\n\t" \
  "ds_read_b64 v[4:5], v30 offset:1024\n\t" \
  "ds_read_b64 v[6:7], v31 offset:1024\n\t" \
  "ds_read_b64 v[8:9], v24 offset:2048\n\t" \
  "ds_read_b64 v[10:11], v25 offset:2048\n\t" \
  "ds_read_b64 v[12:13], v26 offset:3072\n\t" \
  "ds_read_b64 v[14:15], v27 offset:3072\n\t" \
  "ds_read_b64 v[16:17], v28 offset:4096\n\t" \
  "ds_read_b64 v[18:19], v29 offset:4096\n\t" \
  "ds_read_b64 v[20:21], v30 offset:5120\n\t" \
  "ds_read_b64 v[22:23], v31 offset:5120\n\t" \
  "s_waitcnt lgkmcnt(0)\n\t" \
  "v_mfma_f32_16x16x32_bf16 v[128:131], v[68:71], v[0:3], 0\n\t" \
  "v_mfma_f32_16x16x32_bf16 v[128:131], v[64:67], v[4:7], v[128:131]\n\t" \
  "v_mfma_f32_16x16x32_bf16 v[132:135], v[76:79], v[0:3], 0\n\t" \
  "v_mfma_f32_16x16x32_bf16 v[132:135], v[72:75], v[4:7], v[132:135]\n\t" \
  "v_mfma_f32_16x16x32_bf16 v[136:139], v[84:87], v[0:3], 0\n\t" \
  "v_mfma_f32_16x16x32_bf16 v[136:139], v[80:83], v[4:7], v[136:139]\n\t" \
  "v_mfma_f32_16x16x32_bf16 v[140:143], v[92:95], v[0:3], 0\n\t" \
  "v_mfma_f32_16x16x32_bf16 v[140:143], v[88:91], v[4:7], v[140:143]\n\t" \
  "v_mfma_f32_16x16x32_bf16 v[144:147], v[100:103], v[0:3], 0\n\t" \
  "v_mfma_f32_16x16x32_bf16 v[144:147], v[96:99], v[4:7], v[144:147]\n\t" \
  "ds_bpermute_b32 v34, v32, v131\n\t" \
  "v_mfma_f32_16x16x32_bf16 v[148:151], v[108:111], v[0:3], 0\n\t" \
  "v_mov_b32 v131, v130\n\t" \
  "v_mov_b32 v130, v129\n\t" \
  "v_mfma_f32_16x16x32_bf16 v[148:151], v[104:107], v[4:7], v[148:151]\n\t" \
  "v_mov_b32 v129, v128\n\t" \
  "v_mfma_f32_16x16x32_bf16 v[152:155], v[116:119], v[0:3], 0\n\t" \
  "v_mfma_f32_16x16x32_bf16 v[152:155], v[112:115], v[4:7], v[152:155]\n\t" \
  "v_mfma_f32_16x16x32_bf16 v[156:159], v[124:127], v[0:3], 0\n\t" \
  "ds_read_b64 v[0:1], v24 offset:6144\n\t" \
  "ds_read_b64 v[2:3], v25 offset:6144\n\t" \
  "v_mfma_f32_16x16x32_bf16 v[156:159], v[120:123], v[4:7], v[156:159]\n\t" \
  "ds_read_b64 v[4:5], v26 offset:7168\n\t" \
  "ds_read_b64 v[6:7], v27 offset:7168\n\t" \
  "v_mfma_f32_16x16x32_bf16 v[132:135], v[68:71], v[8:11], v[132:135]\n\t" \
  "v_mfma_f32_16x16x32_bf16 v[132:135], v[64:67], v[12:15], v[132:135]\n\t" \
  "v_mfma_f32_16x16x32_bf16 v[136:139], v[76:79], v[8:11], v[136:139]\n\t" \
  "v_mfma_f32_16x16x32_bf16 v[136:139], v[72:75], v[12:15], v[136:139]\n\t" \
  "v_mfma_f32_16x16x32_bf16 v[140:143], v[84:87], v[8:11], v[140:143]\n\t" \
  "s_waitcnt lgkmcnt(4)\n\t" \
  "v_cndmask_b32_e64 v128, v34, 0, vcc\n\t" \
  "v_mfma_f32_16x16x32_bf16 v[140:143], v[80:83], v[12:15], v[140:143]\n\t" \
  "v_mfma_f32_16x16x32_bf16 v[144:147], v[92:95], v[8:11], v[144:147]\n\t" \
  "v_mfma_f32_16x16x32_bf16 v[144:147], v[88:91], v[12:15], v[144:147]\n\t" \
  "ds_bpermute_b32 v35, v32, v135\n\t" \
  "v_mfma_f32_16x16x32_bf16 v[148:151], v[100:103], v[8:11], v[148:151]\n\t" \
  "v_mov_b32 v135, v134\n\t" \
  "v_mov_b32 v134, v133\n\t" \
  "v_mfma_f32_16x16x32_bf16 v[148:151], v[96:99], v[12:15], v[148:151]\n\t" \
  "v_mov_b32 v133, v132\n\t" \
  "v_mfma_f32_16x16x32_bf16 v[152:155], v[108:111], v[8:11], v[152:155]\n\t" \
  "v_mfma_f32_16x16x32_bf16 v[152:155], v[104:107], v[12:15], v[152:155]\n\t" \
  "v_mfma_f32_16x16x32_bf16 v[156:159], v[116:119], v[8:11], v[156:159]\n\t" \
  "ds_read_b64 v[8:9], v28 offset:8192\n\t" \
  "ds_read_b64 v[10:11], v29 offset:8192\n\t" \
  "v_mfma_f32_16x16x32_bf16 v[156:159], v[112:115], v[12:15], v[156:159]\n\t" \
  "ds_read_b64 v[12:13], v30 offset:9216\n\t" \
  "ds_read_b64 v[14:15], v31 offset:9216\n\t" \
  "v_mfma_f32_16x16x32_bf16 v[136:139], v[68:71], v[16:19], v[136:139]\n\t" \
  "v_mfma_f32_16x16x32_bf16 v[136:139], v[64:67], v[20:23], v[136:139]\n\t" \
  "v_mfma_f32_16x16x32_bf16 v[140:143], v[76:79], v[16:19], v[140:143]\n\t" \
  "v_mfma_f32_16x16x32_bf16 v[140:143], v[72:75], v[20:23], v[140:143]\n\t" \
  "v_mfma_f32_16x16x32_bf16 v[144:147], v[84:87], v[16:19], v[144:147]\n\t" \
  "s_waitcnt lgkmcnt(4)\n\t" \
  "v_cndmask_b32_e64 v132, v35, v34, vcc\n\t" \
  "v_mfma_f32_16x16x32_bf16 v[144:147], v[80:83], v[20:23], v[144:147]\n\t" \
  "v_mfma_f32_16x16x32_bf16 v[148:151], v[92:95], v[16:19], v[148:151]\n\t" \
  "v_mfma_f32_16x16x32_bf16 v[148:151], v[88:91], v[20:23], v[148:151]\n\t" \
  "ds_bpermute_b32 v36, v32, v139\n\t" \
  "v_mfma_f32_16x16x32_bf16 v[152:155], v[100:103], v[16:19], v[152:155]\n\t" \
  "v_mov_b32 v139, v138\n\t" \
  "v_mov_b32 v138, v137\n\t" \
  "v_mfma_f32_16x16x32_bf16 v[152:155], v[96:99], v[20:23], v[152:155]\n\t" \
  "v_mov_b32 v137, v136\n\t" \
  "v_mfma_f32_16x16x32_bf16 v[156:159], v[108:111], v[16:19], v[156:159]\n\t" \
  "ds_read_b64 v[16:17], v24 offset:10240\n\t" \
  "ds_read_b64 v[18:19], v25 offset:10240\n\t" \
  "v_mfma_f32_16x16x32_bf16 v[156:159], v[104:107], v[20:23], v[156:159]\n\t" \
  "ds_read_b64 v[20:21], v26 offset:11264\n\t" \
  "ds_read_b64 v[22:23], v27 offset:11264\n\t" \
  "v_mfma_f32_16x16x32_bf16 v[140:143], v[68:71], v[0:3], v[140:143]\n\t" \
  "v_mfma_f32_16x16x32_bf16 v[140:143], v[64:67], v[4:7], v[140:143]\n\t" \
  "v_mfma_f32_16x16x32_bf16 v[144:147], v[76:79], v[0:3], v[144:147]\n\t" \
  "v_mfma_f32_16x16x32_bf16 v[144:147], v[72:75], v[4:7], v[144:147]\n\t" \
  "v_mfma_f32_16x16x32_bf16 v[148:151], v[84:87], v[0:3], v[148:151]\n\t" \
  "v_mfma_f32_16x16x32_bf16 v[148:151], v[80:83], v[4:7], v[148:151]\n\t" \
  "v_mfma_f32_16x16x32_bf16 v[152:155], v[92:95], v[0:3], v[152:155]\n\t" \
  "s_waitcnt lgkmcnt(4)\n\t" \
  "v_cndmask_b32_e64 v136, v36, v35, vcc\n\t" \
  "v_mfma_f32_16x16x32_bf16 v[152:155], v[88:91], v[4:7], v[152:155]\n\t" \
  "ds_bpermute_b32 v37, v32, v143\n\t" \
  "v_mfma_f32_16x16x32_bf16 v[156:159], v[100:103], v[0:3], v[156:159]\n\t" \
  "ds_read_b64 v[0:1], v28 offset:12288\n\t" \
  "ds_read_b64 v[2:3], v29 offset:12288\n\t" \
  "v_mfma_f32_16x16x32_bf16 v[156:159], v[96:99], v[4:7], v[156:159]\n\t" \
  "ds_read_b64 v[4:5], v30 offset:13312\n\t" \
  "ds_read_b64 v[6:7], v31 offset:13312\n\t" \
  "v_mfma_f32_16x16x32_bf16 v[144:147], v[68:71], v[8:11], v[144:147]\n\t" \
  "v_mov_b32 v143, v142\n\t" \
  "v_mov_b32 v142, v141\n\t" \
  "v_mfma_f32_16x16x32_bf16 v[144:147], v[64:67], v[12:15], v[144:147]\n\t" \
  "v_mov_b32 v141, v140\n\t" \
  "v_mfma_f32_16x16x32_bf16 v[148:151], v[76:79], v[8:11], v[148:151]\n\t" \
  "v_mfma_f32_16x16x32_bf16 v[148:151], v[72:75], v[12:15], v[148:151]\n\t" \
  "v_mfma_f32_16x16x32_bf16 v[152:155], v[84:87], v[8:11], v[152:155]\n\t" \
  "v_mfma_f32_16x16x32_bf16 v[152:155], v[80:83], v[12:15], v[152:155]\n\t" \
  "v_mfma_f32_16x16x32_bf16 v[156:159], v[92:95], v[8:11], v[156:159]\n\t" \
  "ds_read_b64 v[8:9], v24 offset:14336\n\t" \
  "ds_read_b64 v[10:11], v25 offset:14336\n\t" \
  "v_mfma_f32_16x16x32_bf16 v[156:159], v[88:91], v[12:15], v[156:159]\n\t" \
  "ds_read_b64 v[12:13], v26 offset:15360\n\t" \
  "s_waitcnt lgkmcnt(8)\n\t" \
  "v_mfma_f32_16x16x32_bf16 v[148:151], v[68:71], v[16:19], v[148:151]\n\t" \
  "ds_read_b64 v[14:15], v27 offset:15360\n\t" \
  "ds_bpermute_b32 v38, v32, v147\n\t" \
  "v_mfma_f32_16x16x32_bf16 v[148:151], v[64:67], v[20:23], v[148:151]\n\t" \
  "v_mov_b32 v147, v146\n\t" \
  "v_mov_b32 v146, v145\n\t" \
  "v_mfma_f32_16x16x32_bf16 v[152:155], v[76:79], v[16:19], v[152:155]\n\t" \
  "s_waitcnt lgkmcnt(9)\n\t" \
  "v_cndmask_b32_e64 v140, v37, v36, vcc\n\t" \
  "v_mfma_f32_16x16x32_bf16 v[152:155], v[72:75], v[20:23], v[152:155]\n\t" \
  "v_mov_b32 v145, v144\n\t" \
  "v_mfma_f32_16x16x32_bf16 v[156:159], v[84:87], v[16:19], v[156:159]\n\t" \
  "ds_read_b64 v[16:17], v26 offset:0\n\t" \
  "ds_read_b64 v[18:19], v27 offset:0\n\t" \
  "v_mfma_f32_16x16x32_bf16 v[156:159], v[80:83], v[20:23], v[156:159]\n\t" \
  "ds_read_b64 v[20:21], v28 offset:1024\n\t" \
  "ds_read_b64 v[22:23], v29 offset:1024\n\t" \
  "s_waitcnt lgkmcnt(9)\n\t" \
  "v_mfma_f32_16x16x32_bf16 v[152:155], v[68:71], v[0:3], v[152:155]\n\t" \
  "ds_bpermute_b32 v39, v32, v151\n\t" \
  "v_mfma_f32_16x16x32_bf16 v[152:155], v[64:67], v[4:7], v[152:155]\n\t" \
  "v_mov_b32 v151, v150\n\t" \
  "v_mov_b32 v150, v149\n\t" \
  "v_mfma_f32_16x16x32_bf16 v[156:159], v[76:79], v[0:3], v[156:159]\n\t" \
  "ds_read_b64 v[0:1], v30 offset:2048\n\t" \
  "ds_read_b64 v[2:3], v31 offset:2048\n\t" \
  "v_mfma_f32_16x16x32_bf16 v[156:159], v[72:75], v[4:7], v[156:159]\n\t" \
  "ds_read_b64 v[4:5], v24 offset:3072\n\t" \
  "s_waitcnt lgkmcnt(11)\n\t" \
  "v_mfma_f32_16x16x32_bf16 v[156:159], v[68:71], v[8:11], v[156:159]\n\t" \
  "ds_bpermute_b32 v40, v32, v155\n\t" \
  "s_waitcnt lgkmcnt(10)\n\t" \
  "v_mfma_f32_16x16x32_bf16 v[156:159], v[64:67], v[12:15], v[156:159]\n\t" \
  "ds_read_b64 v[6:7], v25 offset:3072\n\t" \
  "s_waitcnt lgkmcnt(8)\n\t" \
  "v_mfma_f32_16x16x32_bf16 v[128:131], v[68:71], v[16:19], v[128:131]\n\t" \
  "v_cndmask_b32_e64 v144, v38, v37, vcc\n\t" \
  "s_waitcnt lgkmcnt(6)\n\t" \
  "v_mfma_f32_16x16x32_bf16 v[128:131], v[64:67], v[20:23], v[128:131]\n\t" \
  "v_mov_b32 v149, v148\n\t" \
  "v_mov_b32 v155, v154\n\t" \
  "v_mfma_f32_16x16x32_bf16 v[132:135], v[76:79], v[16:19], v[132:135]\n\t" \
  "ds_bpermute_b32 v41, v32, v159\n\t" \
  "v_mov_b32 v154, v153\n\t" \
  "v_mfma_f32_16x16x32_bf16 v[132:135], v[72:75], v[20:23], v[132:135]\n\t" \
  "v_mov_b32 v153, v152\n\t" \
  "v_mov_b32 v159, v158\n\t" \
  "v_mfma_f32_16x16x32_bf16 v[136:139], v[84:87], v[16:19], v[136:139]\n\t" \
  "v_mov_b32 v158, v157\n\t" \
  "v_mov_b32 v157, v156\n\t" \
  "v_mfma_f32_16x16x32_bf16 v[136:139], v[80:83], v[20:23], v[136:139]\n\t" \
  "s_waitcnt lgkmcnt(6)\n\t" \
  "v_cndmask_b32_e64 v148, v39, v38, vcc\n\t" \
  "v_mfma_f32_16x16x32_bf16 v[140:143], v[92:95], v[16:19], v[140:143]\n\t" \
  "global_load_dwordx4 v[44:47], v43, %[q0] offset:0\n\t" \
  "v_mfma_f32_16x16x32_bf16 v[140:143], v[88:91], v[20:23], v[140:143]\n\t" \
  "ds_read_b64 v[8:9], v26 offset:4096\n\t" \
  "ds_read_b64 v[10:11], v27 offset:4096\n\t" \
  "v_mfma_f32_16x16x32_bf16 v[144:147], v[100:103], v[16:19], v[144:147]\n\t" \
  "s_waitcnt lgkmcnt(4)\n\t" \
  "v_cndmask_b32_e64 v152, v40, v39, vcc\n\t" \
  "v_mfma_f32_16x16x32_bf16 v[144:147], v[96:99], v[20:23], v[144:147]\n\t" \
  "ds_read_b64 v[12:13], v28 offset:5120\n\t" \
  "ds_read_b64 v[14:15], v29 offset:5120\n\t" \
  "v_mfma_f32_16x16x32_bf16 v[148:151], v[108:111], v[16:19], v[148:151]\n\t" \
  "s_waitcnt lgkmcnt(4)\n\t" \
  "v_cndmask_b32_e64 v156, v41, v40, vcc\n\t" \
  "v_mfma_f32_16x16x32_bf16 v[148:151], v[104:107], v[20:23], v[148:151]\n\t" \
  "global_load_dwordx4 v[48:51], v43, %[q0] offset:2048\n\t" \
  "v_mfma_f32_16x16x32_bf16 v[152:155], v[116:119], v[16:19], v[152:155]\n\t" \
  "ds_bpermute_b32 v34, v32, v131\n\t" \
  "v_mfma_f32_16x16x32_bf16 v[152:155], v[112:115], v[20:23], v[152:155]\n\t" \
  "v_mov_b32 v131, v130\n\t" \
  "v_mov_b32 v130, v129\n\t" \
  "v_mfma_f32_16x16x32_bf16 v[156:159], v[124:127], v[16:19], v[156:159]\n\t" \
  "global_load_dwordx4 v[52:55], v43, %[q2] offset:0\n\t" \
  "v_mfma_f32_16x16x32_bf16 v[156:159], v[120:123], v[20:23], v[156:159]\n\t" \
  "ds_read_b64 v[16:17], v30 offset:6144\n\t" \
  "ds_read_b64 v[18:19], v31 offset:6144\n\t" \
  "v_mfma_f32_16x16x32_bf16 v[132:135], v[68:71], v[0:3], v[132:135]\n\t" \
  "ds_read_b64 v[20:21], v24 offset:7168\n\t" \
  "ds_read_b64 v[22:23], v25 offset:7168\n\t" \
  "v_mfma_f32_16x16x32_bf16 v[132:135], v[64:67], v[4:7], v[132:135]\n\t" \
  "v_mov_b32 v129, v128\n\t" \
  "v_mfma_f32_16x16x32_bf16 v[136:139], v[76:79], v[0:3], v[136:139]\n\t" \
  "global_load_dwordx4 v[56:59], v43, %[q2] offset:2048\n\t" \
  "v_mfma_f32_16x16x32_bf16 v[136:139], v[72:75], v[4:7], v[136:139]\n\t" \
  "v_mfma_f32_16x16x32_bf16 v[140:143], v[84:87], v[0:3], v[140:143]\n\t" \
  "v_mfma_f32_16x16x32_bf16 v[140:143], v[80:83], v[4:7], v[140:143]\n\t" \
  "v_mfma_f32_16x16x32_bf16 v[144:147], v[92:95], v[0:3], v[144:147]\n\t" \
  "v_mfma_f32_16x16x32_bf16 v[144:147], v[88:91], v[4:7], v[144:147]\n\t" \
  "ds_bpermute_b32 v35, v32, v135\n\t" \
  "v_mfma_f32_16x16x32_bf16 v[148:151], v[100:103], v[0:3], v[148:151]\n\t" \
  "v_mov_b32 v135, v134\n\t" \
  "v_mov_b32 v134, v133\n\t" \
  "v_mfma_f32_16x16x32_bf16 v[148:151], v[96:99], v[4:7], v[148:151]\n\t" \
  "s_waitcnt lgkmcnt(5)\n\t" \
  "v_cndmask_b32_e64 v128, v34, 0, vcc\n\t" \
  "v_mfma_f32_16x16x32_bf16 v[152:155], v[108:111], v[0:3], v[152:155]\n\t" \
  "v_mov_b32 v133, v132\n\t" \
  "v_mfma_f32_16x16x32_bf16 v[152:155], v[104:107], v[4:7], v[152:155]\n\t" \
  "v_mfma_f32_16x16x32_bf16 v[156:159], v[116:119], v[0:3], v[156:159]\n\t" \
  "ds_read_b64 v[0:1], v26 offset:8192\n\t" \
  "ds_read_b64 v[2:3], v27 offset:8192\n\t" \
  "v_mfma_f32_16x16x32_bf16 v[156:159], v[112:115], v[4:7], v[156:159]\n\t" \
  "ds_read_b64 v[4:5], v28 offset:9216\n\t" \
  "ds_read_b64 v[6:7], v29 offset:9216\n\t" \
  "v_mfma_f32_16x16x32_bf16 v[136:139], v[68:71], v[8:11], v[136:139]\n\t" \
  "v_mfma_f32_16x16x32_bf16 v[136:139], v[64:67], v[12:15], v[136:139]\n\t" \
  "v_mfma_f32_16x16x32_bf16 v[140:143], v[76:79], v[8:11], v[140:143]\n\t" \
  "v_mfma_f32_16x16x32_bf16 v[140:143], v[72:75], v[12:15], v[140:143]\n\t" \
  "v_mfma_f32_16x16x32_bf16 v[144:147], v[84:87], v[8:11], v[144:147]\n\t" \
  "s_waitcnt lgkmcnt(4)\n\t" \
  "v_cndmask_b32_e64 v132, v35, v34, vcc\n\t" \
  "v_mfma_f32_16x16x32_bf16 v[144:147], v[80:83], v[12:15], v[144:147]\n\t" \
  "v_mfma_f32_16x16x32_bf16 v[148:151], v[92:95], v[8:11], v[148:151]\n\t" \
  "v_mfma_f32_16x16x32_bf16 v[148:151], v[88:91], v[12:15], v[148:151]\n\t" \
  "ds_bpermute_b32 v36, v32, v139\n\t" \
  "v_mfma_f32_16x16x32_bf16 v[152:155], v[100:103], v[8:11], v[152:155]\n\t" \
  "v_mov_b32 v139, v138\n\t" \
  "v_mov_b32 v138, v137\n\t" \
  "v_mfma_f32_16x16x32_bf16 v[152:155], v[96:99], v[12:15], v[152:155]\n\t" \
  "v_mov_b32 v137, v136\n\t" \
  "v_mfma_f32_16x16x32_bf16 v[156:159], v[108:111], v[8:11], v[156:159]\n\t" \
  "ds_read_b64 v[8:9], v30 offset:10240\n\t" \
  "ds_read_b64 v[10:11], v31 offset:10240\n\t" \
  "v_mfma_f32_16x16x32_bf16 v[156:159], v[104:107], v[12:15], v[156:159]\n\t" \
  "ds_read_b64 v[12:13], v24 offset:11264\n\t" \
  "ds_read_b64 v[14:15], v25 offset:11264\n\t" \
  "v_mfma_f32_16x16x32_bf16 v[140:143], v[68:71], v[16:19], v[140:143]\n\t" \
  "v_mfma_f32_16x16x32_bf16 v[140:143], v[64:67], v[20:23], v[140:143]\n\t" \
  "v_mfma_f32_16x16x32_bf16 v[144:147], v[76:79], v[16:19], v[144:147]\n\t" \
  "v_mfma_f32_16x16x32_bf16 v[144:147], v[72:75], v[20:23], v[144:147]\n\t" \
  "v_mfma_f32_16x16x32_bf16 v[148:151], v[84:87], v[16:19], v[148:151]\n\t" \
  "v_mfma_f32_16x16x32_bf16 v[148:151], v[80:83], v[20:23], v[148:151]\n\t" \
  "v_mfma_f32_16x16x32_bf16 v[152:155], v[92:95], v[16:19], v[152:155]\n\t" \
  "s_waitcnt lgkmcnt(4)\n\t" \
  "v_cndmask_b32_e64 v136, v36, v35, vcc\n\t" \
  "v_mfma_f32_16x16x32_bf16 v[152:155], v[88:91], v[20:23], v[152:155]\n\t" \
  "ds_bpermute_b32 v37, v32, v143\n\t" \
  "v_mfma_f32_16x16x32_bf16 v[156:159], v[100:103], v[16:19], v[156:159]\n\t" \
  "ds_read_b64 v[16:17], v26 offset:12288\n\t" \
  "ds_read_b64 v[18:19], v27 offset:12288\n\t" \
  "v_mfma_f32_16x16x32_bf16 v[156:159], v[96:99], v[20:23], v[156:159]\n\t" \
  "ds_read_b64 v[20:21], v28 offset:13312\n\t" \
  "ds_read_b64 v[22:23], v29 offset:13312\n\t" \
  "v_mfma_f32_16x16x32_bf16 v[144:147], v[68:71], v[0:3], v[144:147]\n\t" \
  "v_mov_b32 v143, v142\n\t" \
  "v_mov_b32 v142, v141\n\t" \
  "v_mfma_f32_16x16x32_bf16 v[144:147], v[64:67], v[4:7], v[144:147]\n\t" \
  "v_mov_b32 v141, v140\n\t" \
  "v_mfma_f32_16x16x32_bf16 v[148:151], v[76:79], v[0:3], v[148:151]\n\t" \
  "v_mfma_f32_16x16x32_bf16 v[148:151], v[72:75], v[4:7], v[148:151]\n\t" \
  "v_mfma_f32_16x16x32_bf16 v[152:155], v[84:87], v[0:3], v[152:155]\n\t" \
  "v_mfma_f32_16x16x32_bf16 v[152:155], v[80:83], v[4:7], v[152:155]\n\t" \
  "v_mfma_f32_16x16x32_bf16 v[156:159], v[92:95], v[0:3], v[156:159]\n\t" \
  "ds_read_b64 v[0:1], v30 offset:14336\n\t" \
  "ds_read_b64 v[2:3], v31 offset:14336\n\t" \
  "v_mfma_f32_16x16x32_bf16 v[156:159], v[88:91], v[4:7], v[156:159]\n\t" \
  "ds_read_b64 v[4:5], v24 offset:15360\n\t" \
  "s_waitcnt lgkmcnt(8)\n\t" \
  "v_mfma_f32_16x16x32_bf16 v[148:151], v[68:71], v[8:11], v[148:151]\n\t" \
  "ds_read_b64 v[6:7], v25 offset:15360\n\t" \
  "ds_bpermute_b32 v38, v32, v147\n\t" \
  "v_mfma_f32_16x16x32_bf16 v[148:151], v[64:67], v[12:15], v[148:151]\n\t" \
  "v_mov_b32 v147, v146\n\t" \
  "v_mov_b32 v146, v145\n\t" \
  "v_mfma_f32_16x16x32_bf16 v[152:155], v[76:79], v[8:11], v[152:155]\n\t" \
  "s_waitcnt lgkmcnt(9)\n\t" \
  "v_cndmask_b32_e64 v140, v37, v36, vcc\n\t" \
  "v_mfma_f32_16x16x32_bf16 v[152:155], v[72:75], v[12:15], v[152:155]\n\t" \
  "v_mov_b32 v145, v144\n\t" \
  "v_mfma_f32_16x16x32_bf16 v[156:159], v[84:87], v[8:11], v[156:159]\n\t" \
  "ds_read_b64 v[8:9], v24 offset:0\n\t" \
  "ds_read_b64 v[10:11], v25 offset:0\n\t" \
  "v_mfma_f32_16x16x32_bf16 v[156:159], v[80:83], v[12:15], v[156:159]\n\t" \
  "ds_read_b64 v[12:13], v26 offset:1024\n\t" \
  "ds_read_b64 v[14:15], v27 offset:1024\n\t" \
  "s_waitcnt lgkmcnt(9)\n\t" \
  "v_mfma_f32_16x16x32_bf16 v[152:155], v[68:71], v[16:19], v[152:155]\n\t" \
  "ds_bpermute_b32 v39, v32, v151\n\t" \
  "v_mfma_f32_16x16x32_bf16 v[152:155], v[64:67], v[20:23], v[152:155]\n\t" \
  "v_mov_b32 v151, v150\n\t" \
  "v_mov_b32 v150, v149\n\t" \
  "v_mfma_f32_16x16x32_bf16 v[156:159], v[76:79], v[16:19], v[156:159]\n\t" \
  "ds_read_b64 v[16:17], v28 offset:2048\n\t" \
  "ds_read_b64 v[18:19], v29 offset:2048\n\t" \
  "v_mfma_f32_16x16x32_bf16 v[156:159], v[72:75], v[20:23], v[156:159]\n\t" \
  "ds_read_b64 v[20:21], v30 offset:3072\n\t" \
  "s_waitcnt lgkmcnt(11)\n\t" \
  "v_mfma_f32_16x16x32_bf16 v[156:159], v[68:71], v[0:3], v[156:159]\n\t" \
  "ds_bpermute_b32 v40, v32, v155\n\t" \
  "s_waitcnt lgkmcnt(10)\n\t" \
  "v_mfma_f32_16x16x32_bf16 v[156:159], v[64:67], v[4:7], v[156:159]\n\t" \
  "ds_read_b64 v[22:23], v31 offset:3072\n\t" \
  "s_waitcnt lgkmcnt(8)\n\t" \
  "v_mfma_f32_16x16x32_bf16 v[128:131], v[68:71], v[8:11], v[128:131]\n\t" \
  "v_cndmask_b32_e64 v144, v38, v37, vcc\n\t" \
  "s_waitcnt lgkmcnt(6)\n\t" \
  "v_mfma_f32_16x16x32_bf16 v[128:131], v[64:67], v[12:15], v[128:131]\n\t" \
  "v_mov_b32 v149, v148\n\t" \
  "v_mov_b32 v155, v154\n\t" \
  "v_mfma_f32_16x16x32_bf16 v[132:135], v[76:79], v[8:11], v[132:135]\n\t" \
  "ds_bpermute_b32 v41, v32, v159\n\t" \
  "v_mov_b32 v154, v153\n\t" \
  "v_mfma_f32_16x16x32_bf16 v[132:135], v[72:75], v[12:15], v[132:135]\n\t" \
  "v_mov_b32 v153, v152\n\t" \
  "v_mov_b32 v159, v158\n\t" \
  "v_mfma_f32_16x16x32_bf16 v[136:139], v[84:87], v[8:11], v[136:139]\n\t" \
  "v_mov_b32 v158, v157\n\t" \
  "v_mov_b32 v157, v156\n\t" \
  "v_mfma_f32_16x16x32_bf16 v[136:139], v[80:83], v[12:15], v[136:139]\n\t" \
  "s_waitcnt lgkmcnt(6)\n\t" \
  "v_cndmask_b32_e64 v148, v39, v38, vcc\n\t" \
  "v_mfma_f32_16x16x32_bf16 v[140:143], v[92:95], v[8:11], v[140:143]\n\t" \
  "ds_read_b64 v[0:1], v24 offset:4096\n\t" \
  "ds_read_b64 v[2:3], v25 offset:4096\n\t" \
  "v_mfma_f32_16x16x32_bf16 v[140:143], v[88:91], v[12:15], v[140:143]\n\t" \
  "ds_read_b64 v[4:5], v26 offset:5120\n\t" \
  "ds_read_b64 v[6:7], v27 offset:5120\n\t" \
  "v_mfma_f32_16x16x32_bf16 v[144:147], v[100:103], v[8:11], v[144:147]\n\t" \
  "s_waitcnt lgkmcnt(6)\n\t" \
  "v_cndmask_b32_e64 v152, v40, v39, vcc\n\t" \
  "v_mfma_f32_16x16x32_bf16 v[144:147], v[96:99], v[12:15], v[144:147]\n\t" \
  "ds_bpermute_b32 v34, v32, v131\n\t" \
  "v_mfma_f32_16x16x32_bf16 v[148:151], v[108:111], v[8:11], v[148:151]\n\t" \
  "s_waitcnt lgkmcnt(5)\n\t" \
  "v_cndmask_b32_e64 v156, v41, v40, vcc\n\t" \
  "v_mfma_f32_16x16x32_bf16 v[148:151], v[104:107], v[12:15], v[148:151]\n\t" \
  "v_mov_b32 v131, v130\n\t" \
  "v_mov_b32 v130, v129\n\t" \
  "v_mfma_f32_16x16x32_bf16 v[152:155], v[116:119], v[8:11], v[152:155]\n\t" \
  "v_mov_b32 v129, v128\n\t" \
  "v_mfma_f32_16x16x32_bf16 v[152:155], v[112:115], v[12:15], v[152:155]\n\t" \
  "v_mfma_f32_16x16x32_bf16 v[156:159], v[124:127], v[8:11], v[156:159]\n\t" \
  "ds_read_b64 v[8:9], v28 offset:6144\n\t" \
  "ds_read_b64 v[10:11], v29 offset:6144\n\t" \
  "v_mfma_f32_16x16x32_bf16 v[156:159], v[120:123], v[12:15], v[156:159]\n\t" \
  "ds_read_b64 v[12:13], v30 offset:7168\n\t" \
  "ds_read_b64 v[14:15], v31 offset:7168\n\t" \
  "v_mfma_f32_16x16x32_bf16 v[132:135], v[68:71], v[16:19], v[132:135]\n\t" \
  "v_mfma_f32_16x16x32_bf16 v[132:135], v[64:67], v[20:23], v[132:135]\n\t" \
  "v_mfma_f32_16x16x32_bf16 v[136:139], v[76:79], v[16:19], v[136:139]\n\t" \
  "v_mfma_f32_16x16x32_bf16 v[136:139], v[72:75], v[20:23], v[136:139]\n\t" \
  "v_mfma_f32_16x16x32_bf16 v[140:143], v[84:87], v[16:19], v[140:143]\n\t" \
  "s_waitcnt lgkmcnt(4)\n\t" \
  "v_cndmask_b32_e64 v128, v34, 0, vcc\n\t" \
  "v_mfma_f32_16x16x32_bf16 v[140:143], v[80:83], v[20:23], v[140:143]\n\t" \
  "v_mfma_f32_16x16x32_bf16 v[144:147], v[92:95], v[16:19], v[144:147]\n\t" \
  "v_mfma_f32_16x16x32_bf16 v[144:147], v[88:91], v[20:23], v[144:147]\n\t" \
  "ds_bpermute_b32 v35, v32, v135\n\t" \
  "v_mfma_f32_16x16x32_bf16 v[148:151], v[100:103], v[16:19], v[148:151]\n\t" \
  "v_mov_b32 v135, v134\n\t" \
  "v_mov_b32 v134, v133\n\t" \
  "v_mfma_f32_16x16x32_bf16 v[148:151], v[96:99], v[20:23], v[148:151]\n\t" \
  "v_mov_b32 v133, v132\n\t" \
  "v_mfma_f32_16x16x32_bf16 v[152:155], v[108:111], v[16:19], v[152:155]\n\t" \
  "v_mfma_f32_16x16x32_bf16 v[152:155], v[104:107], v[20:23], v[152:155]\n\t" \
  "v_mfma_f32_16x16x32_bf16 v[156:159], v[116:119], v[16:19], v[156:159]\n\t" \
  "ds_read_b64 v[16:17], v24 offset:8192\n\t" \
  "ds_read_b64 v[18:19], v25 offset:8192\n\t" \
  "v_mfma_f32_16x16x32_bf16 v[156:159], v[112:115], v[20:23], v[156:159]\n\t" \
  "ds_read_b64 v[20:21], v26 offset:9216\n\t" \
  "ds_read_b64 v[22:23], v27 offset:9216\n\t" \
  "v_mfma_f32_16x16x32_bf16 v[136:139], v[68:71], v[0:3], v[136:139]\n\t" \
  "v_mfma_f32_16x16x32_bf16 v[136:139], v[64:67], v[4:7], v[136:139]\n\t" \
  "v_mfma_f32_16x16x32_bf16 v[140:143], v[76:79], v[0:3], v[140:143]\n\t" \
  "v_mfma_f32_16x16x32_bf16 v[140:143], v[72:75], v[4:7], v[140:143]\n\t" \
  "v_mfma_f32_16x16x32_bf16 v[144:147], v[84:87], v[0:3], v[144:147]\n\t" \
  "s_waitcnt lgkmcnt(4)\n\t" \
  "v_cndmask_b32_e64 v132, v35, v34, vcc\n\t" \
  "v_mfma_f32_16x16x32_bf16 v[144:147], v[80:83], v[4:7], v[144:147]\n\t" \
  "v_mfma_f32_16x16x32_bf16 v[148:151], v[92:95], v[0:3], v[148:151]\n\t" \
  "v_mfma_f32_16x16x32_bf16 v[148:151], v[88:91], v[4:7], v[148:151]\n\t" \
  "ds_bpermute_b32 v36, v32, v139\n\t" \
  "v_mfma_f32_16x16x32_bf16 v[152:155], v[100:103], v[0:3], v[152:155]\n\t" \
  "v_mov_b32 v139, v138\n\t" \
  "v_mov_b32 v138, v137\n\t" \
  "v_mfma_f32_16x16x32_bf16 v[152:155], v[96:99], v[4:7], v[152:155]\n\t" \
  "v_mov_b32 v137, v136\n\t" \
  "v_mfma_f32_16x16x32_bf16 v[156:159], v[108:111], v[0:3], v[156:159]\n\t" \
  "ds_read_b64 v[0:1], v28 offset:10240\n\t" \
  "ds_read_b64 v[2:3], v29 offset:10240\n\t" \
  "v_mfma_f32_16x16x32_bf16 v[156:159], v[104:107], v[4:7], v[156:159]\n\t" \
  "ds_read_b64 v[4:5], v30 offset:11264\n\t" \
  "ds_read_b64 v[6:7], v31 offset:11264\n\t" \
  "v_mfma_f32_16x16x32_bf16 v[140:143], v[68:71], v[8:11], v[140:143]\n\t" \
  "v_mfma_f32_16x16x32_bf16 v[140:143], v[64:67], v[12:15], v[140:143]\n\t" \
  "v_mfma_f32_16x16x32_bf16 v[144:147], v[76:79], v[8:11], v[144:147]\n\t" \
  "v_mfma_f32_16x16x32_bf16 v[144:147], v[72:75], v[12:15], v[144:147]\n\t" \
  "v_mfma_f32_16x16x32_bf16 v[148:151], v[84:87], v[8:11], v[148:151]\n\t" \
  "v_mfma_f32_16x16x32_bf16 v[148:151], v[80:83], v[12:15], v[148:151]\n\t" \
  "v_mfma_f32_16x16x32_bf16 v[152:155], v[92:95], v[8:11], v[152:155]\n\t" \
  "s_waitcnt lgkmcnt(4)\n\t" \
  "v_cndmask_b32_e64 v136, v36, v35, vcc\n\t" \
  "v_mfma_f32_16x16x32_bf16 v[152:155], v[88:91], v[12:15], v[152:155]\n\t" \
  "ds_bpermute_b32 v37, v32, v143\n\t" \
  "v_mfma_f32_16x16x32_bf16 v[156:159], v[100:103], v[8:11], v[156:159]\n\t" \
  "ds_read_b64 v[8:9], v24 offset:12288\n\t" \
  "ds_read_b64 v[10:11], v25 offset:12288\n\t" \
  "v_mfma_f32_16x16x32_bf16 v[156:159], v[96:99], v[12:15], v[156:159]\n\t" \
  "ds_read_b64 v[12:13], v26 offset:13312\n\t" \
  "ds_read_b64 v[14:15], v27 offset:13312\n\t" \
  "v_mfma_f32_16x16x32_bf16 v[144:147], v[68:71], v[16:19], v[144:147]\n\t" \
  "v_mov_b32 v143, v142\n\t" \
  "v_mov_b32 v142, v141\n\t" \
  "v_mfma_f32_16x16x32_bf16 v[144:147], v[64:67], v[20:23], v[144:147]\n\t" \
  "v_mov_b32 v141, v140\n\t" \
  "v_mfma_f32_16x16x32_bf16 v[148:151], v[76:79], v[16:19], v[148:151]\n\t" \
  "v_mfma_f32_16x16x32_bf16 v[148:151], v[72:75], v[20:23], v[148:151]\n\t" \
  "v_mfma_f32_16x16x32_bf16 v[152:155], v[84:87], v[16:19], v[152:155]\n\t" \
  "v_mfma_f32_16x16x32_bf16 v[152:155], v[80:83], v[20:23], v[152:155]\n\t" \
  "v_mfma_f32_16x16x32_bf16 v[156:159], v[92:95], v[16:19], v[156:159]\n\t" \
  "ds_read_b64 v[16:17], v28 offset:14336\n\t" \
  "ds_read_b64 v[18:19], v29 offset:14336\n\t" \
  "v_mfma_f32_16x16x32_bf16 v[156:159], v[88:91], v[20:23], v[156:159]\n\t" \
  "ds_read_b64 v[20:21], v30 offset:15360\n\t" \
  "s_waitcnt lgkmcnt(8)\n\t" \
  "v_mfma_f32_16x16x32_bf16 v[148:151], v[68:71], v[0:3], v[148:151]\n\t" \
  "ds_read_b64 v[22:23], v31 offset:15360\n\t" \
  "ds_bpermute_b32 v38, v32, v147\n\t" \
  "v_mfma_f32_16x16x32_bf16 v[148:151], v[64:67], v[4:7], v[148:151]\n\t" \
  "v_mov_b32 v147, v146\n\t" \
  "v_mov_b32 v146, v145\n\t" \
  "v_mfma_f32_16x16x32_bf16 v[152:155], v[76:79], v[0:3], v[152:155]\n\t" \
  "s_waitcnt lgkmcnt(9)\n\t" \
  "v_cndmask_b32_e64 v140, v37, v36, vcc\n\t" \
  "v_mfma_f32_16x16x32_bf16 v[152:155], v[72:75], v[4:7], v[152:155]\n\t" \
  "v_mov_b32 v145, v144\n\t" \
  "v_mfma_f32_16x16x32_bf16 v[156:159], v[84:87], v[0:3], v[156:159]\n\t" \
  "ds_read_b64 v[0:1], v30 offset:0\n\t" \
  "ds_read_b64 v[2:3], v31 offset:0\n\t" \
  "v_mfma_f32_16x16x32_bf16 v[156:159], v[80:83], v[4:7], v[156:159]\n\t" \
  "ds_read_b64 v[4:5], v24 offset:1024\n\t" \
  "ds_read_b64 v[6:7], v25 offset:1024\n\t" \
  "s_waitcnt lgkmcnt(9)\n\t" \
  "v_mfma_f32_16x16x32_bf16 v[152:155], v[68:71], v[8:11], v[152:155]\n\t" \
  "ds_bpermute_b32 v39, v32, v151\n\t" \
  "v_mfma_f32_16x16x32_bf16 v[152:155], v[64:67], v[12:15], v[152:155]\n\t" \
  "v_mov_b32 v151, v150\n\t" \
  "v_mov_b32 v150, v149\n\t" \
  "v_mfma_f32_16x16x32_bf16 v[156:159], v[76:79], v[8:11], v[156:159]\n\t" \
  "ds_read_b64 v[8:9], v26 offset:2048\n\t" \
  "ds_read_b64 v[10:11], v27 offset:2048\n\t" \
  "v_mfma_f32_16x16x32_bf16 v[156:159], v[72:75], v[12:15], v[156:159]\n\t" \
  "ds_read_b64 v[12:13], v28 offset:3072\n\t" \
  "s_waitcnt lgkmcnt(11)\n\t" \
  "v_mfma_f32_16x16x32_bf16 v[156:159], v[68:71], v[16:19], v[156:159]\n\t" \
  "ds_bpermute_b32 v40, v32, v155\n\t" \
  "s_waitcnt lgkmcnt(10)\n\t" \
  "v_mfma_f32_16x16x32_bf16 v[156:159], v[64:67], v[20:23], v[156:159]\n\t" \
  "ds_read_b64 v[14:15], v29 offset:3072\n\t" \
  "s_waitcnt lgkmcnt(8)\n\t" \
  "v_mfma_f32_16x16x32_bf16 v[128:131], v[68:71], v[0:3], v[128:131]\n\t" \
  "v_cndmask_b32_e64 v144, v38, v37, vcc\n\t" \
  "s_waitcnt lgkmcnt(6)\n\t" \
  "v_mfma_f32_16x16x32_bf16 v[128:131], v[64:67], v[4:7], v[128:131]\n\t" \
  "v_mov_b32 v149, v148\n\t" \
  "v_mov_b32 v155, v154\n\t" \
  "v_mfma_f32_16x16x32_bf16 v[132:135], v[76:79], v[0:3], v[132:135]\n\t" \
  "ds_bpermute_b32 v41, v32, v159\n\t" \
  "v_mov_b32 v154, v153\n\t" \
  "v_mfma_f32_16x16x32_bf16 v[132:135], v[72:75], v[4:7], v[132:135]\n\t" \
  "v_mov_b32 v153, v152\n\t" \
  "v_mov_b32 v159, v158\n\t" \
  "v_mfma_f32_16x16x32_bf16 v[136:139], v[84:87], v[0:3], v[136:139]\n\t" \
  "v_mov_b32 v158, v157\n\t" \
  "v_mov_b32 v157, v156\n\t" \
  "v_mfma_f32_16x16x32_bf16 v[136:139], v[80:83], v[4:7], v[136:139]\n\t" \
  "s_waitcnt lgkmcnt(6)\n\t" \
  "v_cndmask_b32_e64 v148, v39, v38, vcc\n\t" \
  "v_mfma_f32_16x16x32_bf16 v[140:143], v[92:95], v[0:3], v[140:143]\n\t" \
  "ds_read_b64 v[16:17], v30 offset:4096\n\t" \
  "ds_read_b64 v[18:19], v31 offset:4096\n\t" \
  "v_mfma_f32_16x16x32_bf16 v[140:143], v[88:91], v[4:7], v[140:143]\n\t" \
  "ds_read_b64 v[20:21], v24 offset:5120\n\t" \
  "ds_read_b64 v[22:23], v25 offset:5120\n\t" \
  "v_mfma_f32_16x16x32_bf16 v[144:147], v[100:103], v[0:3], v[144:147]\n\t" \
  "s_waitcnt lgkmcnt(6)\n\t" \
  "v_cndmask_b32_e64 v152, v40, v39, vcc\n\t" \
  "v_mfma_f32_16x16x32_bf16 v[144:147], v[96:99], v[4:7], v[144:147]\n\t" \
  "s_mov_b64 exec, 1\n\t" \
  "ds_write_b32 v33, v33\n\t" \
  "s_mov_b64 exec, -1\n\t" \
  "v_mfma_f32_16x16x32_bf16 v[148:151], v[108:111], v[0:3], v[148:151]\n\t" \
  "s_waitcnt lgkmcnt(5)\n\t" \
  "v_cndmask_b32_e64 v156, v41, v40, vcc\n\t" \
  "v_mfma_f32_16x16x32_bf16 v[148:151], v[104:107], v[4:7], v[148:151]\n\t" \
  "v_cvt_pk_bf16_f32 v34, v128, v129\n\t" \
  "v_cvt_pk_bf16_f32 v35, v130, v131\n\t" \
  "v_mfma_f32_16x16x32_bf16 v[152:155], v[116:119], v[0:3], v[152:155]\n\t" \
  "v_mfma_f32_16x16x32_bf16 v[152:155], v[112:115], v[4:7], v[152:155]\n\t" \
  "v_mfma_f32_16x16x32_bf16 v[156:159], v[124:127], v[0:3], v[156:159]\n\t" \
  "ds_read_b64 v[0:1], v26 offset:6144\n\t" \
  "ds_read_b64 v[2:3], v27 offset:6144\n\t" \
  "v_mfma_f32_16x16x32_bf16 v[156:159], v[120:123], v[4:7], v[156:159]\n\t" \
  "ds_read_b64 v[4:5], v28 offset:7168\n\t" \
  "ds_read_b64 v[6:7], v29 offset:7168\n\t" \
  "v_mfma_f32_16x16x32_bf16 v[132:135], v[68:71], v[8:11], v[132:135]\n\t" \
  "v_mfma_f32_16x16x32_bf16 v[132:135], v[64:67], v[12:15], v[132:135]\n\t" \
  "v_mfma_f32_16x16x32_bf16 v[136:139], v[76:79], v[8:11], v[136:139]\n\t" \
  "v_mfma_f32_16x16x32_bf16 v[136:139], v[72:75], v[12:15], v[136:139]\n\t" \
  "v_mfma_f32_16x16x32_bf16 v[140:143], v[84:87], v[8:11], v[140:143]\n\t" \
  "v_mfma_f32_16x16x32_bf16 v[140:143], v[80:83], v[12:15], v[140:143]\n\t" \
  "v_mfma_f32_16x16x32_bf16 v[144:147], v[92:95], v[8:11], v[144:147]\n\t" \
  "v_mfma_f32_16x16x32_bf16 v[144:147], v[88:91], v[12:15], v[144:147]\n\t" \
  "v_mfma_f32_16x16x32_bf16 v[148:151], v[100:103], v[8:11], v[148:151]\n\t" \
  "v_mfma_f32_16x16x32_bf16 v[148:151], v[96:99], v[12:15], v[148:151]\n\t" \
  "v_cvt_pk_bf16_f32 v36, v132, v133\n\t" \
  "v_cvt_pk_bf16_f32 v37, v134, v135\n\t" \
  "v_mfma_f32_16x16x32_bf16 v[152:155], v[108:111], v[8:11], v[152:155]\n\t" \
  "v_mfma_f32_16x16x32_bf16 v[152:155], v[104:107], v[12:15], v[152:155]\n\t" \
  "v_permlane16_swap_b32 v34, v36\n\t" \
  "v_permlane16_swap_b32 v35, v37\n\t" \
  "v_mfma_f32_16x16x32_bf16 v[156:159], v[116:119], v[8:11], v[156:159]\n\t" \
  "global_store_dwordx4 v42, v[34:37], %[yb] offset:0 sc1\n\t" \
  "v_mfma_f32_16x16x32_bf16 v[156:159], v[112:115], v[12:15], v[156:159]\n\t" \
  "ds_read_b64 v[8:9], v30 offset:8192\n\t" \
  "s_waitcnt lgkmcnt(1)\n\t" \
  "v_mfma_f32_16x16x32_bf16 v[136:139], v[68:71], v[16:19], v[136:139]\n\t" \
  "ds_read_b64 v[10:11], v31 offset:8192\n\t" \
  "ds_read_b64 v[12:13], v24 offset:9216\n\t" \
  "v_mfma_f32_16x16x32_bf16 v[136:139], v[64:67], v[20:23], v[136:139]\n\t" \
  "ds_read_b64 v[14:15], v25 offset:9216\n\t" \
  "v_mfma_f32_16x16x32_bf16 v[140:143], v[76:79], v[16:19], v[140:143]\n\t" \
  "v_mfma_f32_16x16x32_bf16 v[140:143], v[72:75], v[20:23], v[140:143]\n\t" \
  "v_mfma_f32_16x16x32_bf16 v[144:147], v[84:87], v[16:19], v[144:147]\n\t" \
  "v_mfma_f32_16x16x32_bf16 v[144:147], v[80:83], v[20:23], v[144:147]\n\t" \
  "v_mfma_f32_16x16x32_bf16 v[148:151], v[92:95], v[16:19], v[148:151]\n\t" \
  "v_mfma_f32_16x16x32_bf16 v[148:151], v[88:91], v[20:23], v[148:151]\n\t" \
  "v_mfma_f32_16x16x32_bf16 v[152:155], v[100:103], v[16:19], v[152:155]\n\t" \
  "v_cvt_pk_bf16_f32 v38, v136, v137\n\t" \
  "v_cvt_pk_bf16_f32 v39, v138, v139\n\t" \
  "v_mfma_f32_16x16x32_bf16 v[152:155], v[96:99], v[20:23], v[152:155]\n\t" \
  "v_mfma_f32_16x16x32_bf16 v[156:159], v[108:111], v[16:19], v[156:159]\n\t" \
  "ds_read_b64 v[16:17], v26 offset:10240\n\t" \
  "ds_read_b64 v[18:19], v27 offset:10240\n\t" \
  "v_mfma_f32_16x16x32_bf16 v[156:159], v[104:107], v[20:23], v[156:159]\n\t" \
  "ds_read_b64 v[20:21], v28 offset:11264\n\t" \
  "ds_read_b64 v[22:23], v29 offset:11264\n\t" \
  "v_mfma_f32_16x16x32_bf16 v[140:143], v[68:71], v[0:3], v[140:143]\n\t" \
  "v_mfma_f32_16x16x32_bf16 v[140:143], v[64:67], v[4:7], v[140:143]\n\t" \
  "v_mfma_f32_16x16x32_bf16 v[144:147], v[76:79], v[0:3], v[144:147]\n\t" \
  "v_mfma_f32_16x16x32_bf16 v[144:147], v[72:75], v[4:7], v[144:147]\n\t" \
  "v_mfma_f32_16x16x32_bf16 v[148:151], v[84:87], v[0:3], v[148:151]\n\t" \
  "v_mfma_f32_16x16x32_bf16 v[148:151], v[80:83], v[4:7], v[148:151]\n\t" \
  "v_mfma_f32_16x16x32_bf16 v[152:155], v[92:95], v[0:3], v[152:155]\n\t" \
  "v_mfma_f32_16x16x32_bf16 v[152:155], v[88:91], v[4:7], v[152:155]\n\t" \
  "v_mfma_f32_16x16x32_bf16 v[156:159], v[100:103], v[0:3], v[156:159]\n\t" \
  "ds_read_b64 v[0:1], v30 offset:12288\n\t" \
  "v_cvt_pk_bf16_f32 v40, v140, v141\n\t" \
  "v_mfma_f32_16x16x32_bf16 v[156:159], v[96:99], v[4:7], v[156:159]\n\t" \
  "v_cvt_pk_bf16_f32 v41, v142, v143\n\t" \
  "s_waitcnt lgkmcnt(3)\n\t" \
  "v_mfma_f32_16x16x32_bf16 v[144:147], v[68:71], v[8:11], v[144:147]\n\t" \
  "ds_read_b64 v[2:3], v31 offset:12288\n\t" \
  "ds_read_b64 v[4:5], v24 offset:13312\n\t" \
  "v_mfma_f32_16x16x32_bf16 v[144:147], v[64:67], v[12:15], v[144:147]\n\t" \
  "v_permlane16_swap_b32 v38, v40\n\t" \
  "v_permlane16_swap_b32 v39, v41\n\t" \
  "v_mfma_f32_16x16x32_bf16 v[148:151], v[76:79], v[8:11], v[148:151]\n\t" \
  "ds_read_b64 v[6:7], v25 offset:13312\n\t" \
  "v_mfma_f32_16x16x32_bf16 v[148:151], v[72:75], v[12:15], v[148:151]\n\t" \
  "global_store_dwordx4 v42, v[38:41], %[yb] offset:1024 sc1\n\t" \
  "v_mfma_f32_16x16x32_bf16 v[152:155], v[84:87], v[8:11], v[152:155]\n\t" \
  "v_mfma_f32_16x16x32_bf16 v[152:155], v[80:83], v[12:15], v[152:155]\n\t" \
  "v_cvt_pk_bf16_f32 v34, v144, v145\n\t" \
  "v_cvt_pk_bf16_f32 v35, v146, v147\n\t" \
  "v_mfma_f32_16x16x32_bf16 v[156:159], v[92:95], v[8:11], v[156:159]\n\t" \
  "ds_read_b64 v[8:9], v26 offset:14336\n\t" \
  "ds_read_b64 v[10:11], v27 offset:14336\n\t" \
  "v_mfma_f32_16x16x32_bf16 v[156:159], v[88:91], v[12:15], v[156:159]\n\t" \
  "ds_read_b64 v[12:13], v28 offset:15360\n\t" \
  "ds_read_b64 v[14:15], v29 offset:15360\n\t" \
  "v_mfma_f32_16x16x32_bf16 v[148:151], v[68:71], v[16:19], v[148:151]\n\t" \
  "s_waitcnt lgkmcnt(8)\n\t" \
  "v_mfma_f32_16x16x32_bf16 v[148:151], v[64:67], v[20:23], v[148:151]\n\t" \
  "v_mfma_f32_16x16x32_bf16 v[152:155], v[76:79], v[16:19], v[152:155]\n\t" \
  "v_mfma_f32_16x16x32_bf16 v[152:155], v[72:75], v[20:23], v[152:155]\n\t" \
  "v_mfma_f32_16x16x32_bf16 v[156:159], v[84:87], v[16:19], v[156:159]\n\t" \
  "v_mfma_f32_16x16x32_bf16 v[156:159], v[80:83], v[20:23], v[156:159]\n\t" \
  "s_waitcnt lgkmcnt(4)\n\t" \
  "v_mfma_f32_16x16x32_bf16 v[152:155], v[68:71], v[0:3], v[152:155]\n\t" \
  "v_mfma_f32_16x16x32_bf16 v[152:155], v[64:67], v[4:7], v[152:155]\n\t" \
  "v_mfma_f32_16x16x32_bf16 v[156:159], v[76:79], v[0:3], v[156:159]\n\t" \
  "v_cvt_pk_bf16_f32 v36, v148, v149\n\t" \
  "v_cvt_pk_bf16_f32 v37, v150, v151\n\t" \
  "v_mfma_f32_16x16x32_bf16 v[156:159], v[72:75], v[4:7], v[156:159]\n\t" \
  "s_waitcnt lgkmcnt(2)\n\t" \
  "v_mfma_f32_16x16x32_bf16 v[156:159], v[68:71], v[8:11], v[156:159]\n\t" \
  "v_permlane16_swap_b32 v34, v36\n\t" \
  "s_waitcnt lgkmcnt(0)\n\t" \
  "v_mfma_f32_16x16x32_bf16 v[156:159], v[64:67], v[12:15], v[156:159]\n\t" \
  "v_permlane16_swap_b32 v35, v37\n\t" \
  "global_store_dwordx4 v42, v[34:37], %[yb] offset:2048 sc1\n\t" \
  "s_waitcnt lgkmcnt(0)\n\t" \
  "s_nop 4\n\t" \
  "v_cvt_pk_bf16_f32 v38, v152, v153\n\t" \
  "v_cvt_pk_bf16_f32 v39, v154, v155\n\t" \
  "v_cvt_pk_bf16_f32 v40, v156, v157\n\t" \
  "v_cvt_pk_bf16_f32 v41, v158, v159\n\t" \
  "s_nop 1\n\t" \
  "v_permlane16_swap_b32 v38, v40\n\t" \
  "v_permlane16_swap_b32 v39, v41\n\t" \
  "global_store_dwordx4 v42, v[38:41], %[yb] offset:3072 sc1\n\t" \
  "s_nop 1\n\t"
#define CV_Q 1
#define CV_CLOB "vcc", "v0", "v1", "v2", "v3", "v4", "v5", "v6", "v7", "v8", "v9", "v10", "v11", "v12", "v13", "v14", "v15", "v16", "v17", "v18", "v19", "v20", "v21", "v22", "v23", "v34", "v35", "v36", "v37", "v38", "v39", "v40", "v41"

#define G3_ASM \
  "s_mov_b32 %[c], -1\n\t" \
  "s_getpc_b64 s[92:93]\n\t" \
  "s_branch QC_%=\n\t" \
  "QH_%=:\n\t" \
  "v_add_u32 v166, %[go], v165\n\t" \
  "ds_read_b32 v166, v166\n\t" \
  "s_addk_i32 %[cn], 1\n\t" \
  "s_cmp_gt_u32 %[cn], 0x400000\n\t" \
  "s_cbranch_scc1 QI_%=\n\t" \
  "s_waitcnt lgkmcnt(0)\n\t" \
  "v_cmp_eq_u32 vcc, 0, v166\n\t" \
  "s_cbranch_vccz QI_%=\n\t" \
  "s_sleep 1\n\t" \
  "s_getpc_b64 s[92:93]\n\t" \
  "s_branch QC_%=\n\t" \
  "s_branch QH_%=\n\t" \
  "QI_%=:\n\t" \
  "s_mov_b32 %[c], 0\n\t" \
  "s_getpc_b64 s[92:93]\n\t" \
  "s_branch QC_%=\n\t" \
  "s_memrealtime %[st5]\n\t" \
  "s_waitcnt lgkmcnt(0)\n\t" \
  "s_setprio 0\n\t" \
  "ds_read_b32 v166, v165\n\t" \
  "ds_read_b64_tr_b16 v[96:97], v160 offset:0\n\t" \
  "ds_read_b64_tr_b16 v[98:99], v160 offset:2048\n\t" \
  "ds_read_b64_tr_b16 v[100:101], v161 offset:0\n\t" \
  "ds_read_b64_tr_b16 v[102:103], v161 offset:2048\n\t" \
  "ds_read_b64_tr_b16 v[104:105], v162 offset:0\n\t" \
  "ds_read_b64_tr_b16 v[106:107], v162 offset:2048\n\t" \
  "ds_read_b64_tr_b16 v[108:109], v163 offset:0\n\t" \
  "ds_read_b64_tr_b16 v[110:111], v163 offset:2048\n\t" \
  "ds_read_b64_tr_b16 v[112:113], v160 offset:4096\n\t" \
  "ds_read_b64_tr_b16 v[114:115], v160 offset:6144\n\t" \
  "ds_read_b64_tr_b16 v[116:117], v161 offset:4096\n\t" \
  "ds_read_b64_tr_b16 v[118:119], v161 offset:6144\n\t" \
  "ds_read_b64_tr_b16 v[120:121], v162 offset:4096\n\t" \
  "ds_read_b64_tr_b16 v[122:123], v162 offset:6144\n\t" \
  "s_waitcnt lgkmcnt(10)\n\t" \
  "v_mfma_f32_16x16x32_bf16 v[128:131], v[96:99], v[0:3], 0\n\t" \
  "ds_read_b64_tr_b16 v[124:125], v163 offset:4096\n\t" \
  "v_readlane_b32 s94, v167, 4\n\t" \
  "v_mfma_f32_16x16x32_bf16 v[144:147], v[96:99], v[4:7], 0\n\t" \
  "ds_read_b64_tr_b16 v[126:127], v163 offset:6144\n\t" \
  "v_cmp_lt_u32 vcc, %[th], v166\n\t" \
  "v_mfma_f32_16x16x32_bf16 v[132:135], v[100:103], v[0:3], 0\n\t" \
  "s_mov_b64 exec, vcc\n\t" \
  "buffer_load_dwordx4 v[64:67], v164, %[ob], s94 offen offset:0 sc1\n\t" \
  "s_mov_b64 exec, -1\n\t" \
  "v_mfma_f32_16x16x32_bf16 v[148:151], v[100:103], v[4:7], 0\n\t" \
  "ds_read_b64_tr_b16 v[96:97], v160 offset:8192\n\t" \
  "ds_read_b64_tr_b16 v[98:99], v160 offset:10240\n\t" \
  "s_waitcnt lgkmcnt(10)\n\t" \
  "v_mfma_f32_16x16x32_bf16 v[136:139], v[104:107], v[0:3], 0\n\t" \
  "s_mov_b64 exec, vcc\n\t" \
  "buffer_load_dwordx4 v[68:71], v164, %[ob], s94 offen offset:1024 sc1\n\t" \
  "s_mov_b64 exec, -1\n\t" \
  "v_mfma_f32_16x16x32_bf16 v[152:155], v[104:107], v[4:7], 0\n\t" \
  "ds_read_b64_tr_b16 v[100:101], v161 offset:8192\n\t" \
  "ds_read_b64_tr_b16 v[102:103], v161 offset:10240\n\t" \
  "v_mfma_f32_16x16x32_bf16 v[140:143], v[108:111], v[0:3], 0\n\t" \
  "s_mov_b64 exec, vcc\n\t" \
  "buffer_load_dwordx4 v[72:75], v164, %[ob], s94 offen offset:2048 sc1\n\t" \
  "s_mov_b64 exec, -1\n\t" \
  "v_mfma_f32_16x16x32_bf16 v[156:159], v[108:111], v[4:7], 0\n\t" \
  "ds_read_b64_tr_b16 v[104:105], v162 offset:8192\n\t" \
  "ds_read_b64_tr_b16 v[106:107], v162 offset:10240\n\t" \
  "s_waitcnt lgkmcnt(10)\n\t" \
  "v_mfma_f32_16x16x32_bf16 v[128:131], v[112:115], v[8:11], v[128:131]\n\t" \
  "s_mov_b64 exec, vcc\n\t" \
  "buffer_load_dwordx4 v[76:79], v164, %[ob], s94 offen offset:3072 sc1\n\t" \
  "s_mov_b64 exec, -1\n\t" \
  "v_mfma_f32_16x16x32_bf16 v[144:147], v[112:115], v[12:15], v[144:147]\n\t" \
  "ds_read_b64_tr_b16 v[108:109], v163 offset:8192\n\t" \
  "ds_read_b64_tr_b16 v[110:111], v163 offset:10240\n\t" \
  "v_mfma_f32_16x16x32_bf16 v[132:135], v[116:119], v[8:11], v[132:135]\n\t" \
  "ds_read_b64_tr_b16 v[112:113], v160 offset:12288\n\t" \
  "ds_read_b32 v166, v165\n\t" \
  "v_mfma_f32_16x16x32_bf16 v[148:151], v[116:119], v[12:15], v[148:151]\n\t" \
  "ds_read_b64_tr_b16 v[114:115], v160 offset:14336\n\t" \
  "s_waitcnt lgkmcnt(11)\n\t" \
  "v_mfma_f32_16x16x32_bf16 v[136:139], v[120:123], v[8:11], v[136:139]\n\t" \
  "ds_read_b64_tr_b16 v[116:117], v161 offset:12288\n\t" \
  "v_mfma_f32_16x16x32_bf16 v[152:155], v[120:123], v[12:15], v[152:155]\n\t" \
  "ds_read_b64_tr_b16 v[118:119], v161 offset:14336\n\t" \
  "s_sub_u32 %[iss], %[iss], vcc_lo\n\t" \
  "v_mfma_f32_16x16x32_bf16 v[140:143], v[124:127], v[8:11], v[140:143]\n\t" \
  "ds_read_b64_tr_b16 v[120:121], v162 offset:12288\n\t" \
  "v_mfma_f32_16x16x32_bf16 v[156:159], v[124:127], v[12:15], v[156:159]\n\t" \
  "ds_read_b64_tr_b16 v[122:123], v162 offset:14336\n\t" \
  "s_add_u32 %[th], %[th], 1\n\t" \
  "s_cbranch_vccz QF1_%=\n\t" \
  "s_waitcnt vmcnt(12)\n\t" \
  "QG1_%=:\n\t" \
  "s_waitcnt lgkmcnt(11)\n\t" \
  "v_mfma_f32_16x16x32_bf16 v[128:131], v[96:99], v[16:19], v[128:131]\n\t" \
  "ds_read_b64_tr_b16 v[124:125], v163 offset:12288\n\t" \
  "v_readlane_b32 s94, v167, 5\n\t" \
  "v_mfma_f32_16x16x32_bf16 v[144:147], v[96:99], v[20:23], v[144:147]\n\t" \
  "ds_read_b64_tr_b16 v[126:127], v163 offset:14336\n\t" \
  "s_waitcnt lgkmcnt(7)\n\t" \
  "v_cmp_lt_u32 vcc, %[th], v166\n\t" \
  "v_mfma_f32_16x16x32_bf16 v[132:135], v[100:103], v[16:19], v[132:135]\n\t" \
  "s_mov_b64 exec, vcc\n\t" \
  "buffer_load_dwordx4 v[0:3], v164, %[ob], s94 offen offset:0 sc1\n\t" \
  "s_mov_b64 exec, -1\n\t" \
  "v_mfma_f32_16x16x32_bf16 v[148:151], v[100:103], v[20:23], v[148:151]\n\t" \
  "ds_read_b64_tr_b16 v[96:97], v160 offset:16384\n\t" \
  "ds_read_b64_tr_b16 v[98:99], v160 offset:18432\n\t" \
  "v_mfma_f32_16x16x32_bf16 v[136:139], v[104:107], v[16:19], v[136:139]\n\t" \
  "s_mov_b64 exec, vcc\n\t" \
  "buffer_load_dwordx4 v[4:7], v164, %[ob], s94 offen offset:1024 sc1\n\t" \
  "s_mov_b64 exec, -1\n\t" \
  "v_mfma_f32_16x16x32_bf16 v[152:155], v[104:107], v[20:23], v[152:155]\n\t" \
  "ds_read_b64_tr_b16 v[100:101], v161 offset:16384\n\t" \
  "ds_read_b64_tr_b16 v[102:103], v161 offset:18432\n\t" \
  "v_mfma_f32_16x16x32_bf16 v[140:143], v[108:111], v[16:19], v[140:143]\n\t" \
  "s_mov_b64 exec, vcc\n\t" \
  "buffer_load_dwordx4 v[8:11], v164, %[ob], s94 offen offset:2048 sc1\n\t" \
  "s_mov_b64 exec, -1\n\t" \
  "v_mfma_f32_16x16x32_bf16 v[156:159], v[108:111], v[20:23], v[156:159]\n\t" \
  "ds_read_b64_tr_b16 v[104:105], v162 offset:16384\n\t" \
  "ds_read_b64_tr_b16 v[106:107], v162 offset:18432\n\t" \
  "s_waitcnt lgkmcnt(10)\n\t" \
  "v_mfma_f32_16x16x32_bf16 v[128:131], v[112:115], v[24:27], v[128:131]\n\t" \
  "s_mov_b64 exec, vcc\n\t" \
  "buffer_load_dwordx4 v[12:15], v164, %[ob], s94 offen offset:3072 sc1\n\t" \
  "s_mov_b64 exec, -1\n\t" \
  "v_mfma_f32_16x16x32_bf16 v[144:147], v[112:115], v[28:31], v[144:147]\n\t" \
  "ds_read_b64_tr_b16 v[108:109], v163 offset:16384\n\t" \
  "ds_read_b64_tr_b16 v[110:111], v163 offset:18432\n\t" \
  "v_mfma_f32_16x16x32_bf16 v[132:135], v[116:119], v[24:27], v[132:135]\n\t" \
  "ds_read_b64_tr_b16 v[112:113], v160 offset:20480\n\t" \
  "ds_read_b32 v166, v165\n\t" \
  "v_mfma_f32_16x16x32_bf16 v[148:151], v[116:119], v[28:31], v[148:151]\n\t" \
  "ds_read_b64_tr_b16 v[114:115], v160 offset:22528\n\t" \
  "s_waitcnt lgkmcnt(11)\n\t" \
  "v_mfma_f32_16x16x32_bf16 v[136:139], v[120:123], v[24:27], v[136:139]\n\t" \
  "ds_read_b64_tr_b16 v[116:117], v161 offset:20480\n\t" \
  "v_mfma_f32_16x16x32_bf16 v[152:155], v[120:123], v[28:31], v[152:155]\n\t" \
  "ds_read_b64_tr_b16 v[118:119], v161 offset:22528\n\t" \
  "s_sub_u32 %[iss], %[iss], vcc_lo\n\t" \
  "v_mfma_f32_16x16x32_bf16 v[140:143], v[124:127], v[24:27], v[140:143]\n\t" \
  "ds_read_b64_tr_b16 v[120:121], v162 offset:20480\n\t" \
  "v_mfma_f32_16x16x32_bf16 v[156:159], v[124:127], v[28:31], v[156:159]\n\t" \
  "ds_read_b64_tr_b16 v[122:123], v162 offset:22528\n\t" \
  "s_add_u32 %[th], %[th], 1\n\t" \
  "s_cbranch_vccz QF2_%=\n\t" \
  "s_waitcnt vmcnt(12)\n\t" \
  "QG2_%=:\n\t" \
  "s_waitcnt lgkmcnt(11)\n\t" \
  "v_mfma_f32_16x16x32_bf16 v[128:131], v[96:99], v[32:35], v[128:131]\n\t" \
  "ds_read_b64_tr_b16 v[124:125], v163 offset:20480\n\t" \
  "v_readlane_b32 s94, v167, 6\n\t" \
  "v_mfma_f32_16x16x32_bf16 v[144:147], v[96:99], v[36:39], v[144:147]\n\t" \
  "ds_read_b64_tr_b16 v[126:127], v163 offset:22528\n\t" \
  "s_waitcnt lgkmcnt(7)\n\t" \
  "v_cmp_lt_u32 vcc, %[th], v166\n\t" \
  "v_mfma_f32_16x16x32_bf16 v[132:135], v[100:103], v[32:35], v[132:135]\n\t" \
  "s_mov_b64 exec, vcc\n\t" \
  "buffer_load_dwordx4 v[16:19], v164, %[ob], s94 offen offset:0 sc1\n\t" \
  "s_mov_b64 exec, -1\n\t" \
  "v_mfma_f32_16x16x32_bf16 v[148:151], v[100:103], v[36:39], v[148:151]\n\t" \
  "ds_read_b64_tr_b16 v[96:97], v160 offset:24576\n\t" \
  "ds_read_b64_tr_b16 v[98:99], v160 offset:26624\n\t" \
  "v_mfma_f32_16x16x32_bf16 v[136:139], v[104:107], v[32:35], v[136:139]\n\t" \
  "s_mov_b64 exec, vcc\n\t" \
  "buffer_load_dwordx4 v[20:23], v164, %[ob], s94 offen offset:1024 sc1\n\t" \
  "s_mov_b64 exec, -1\n\t" \
  "v_mfma_f32_16x16x32_bf16 v[152:155], v[104:107], v[36:39], v[152:155]\n\t" \
  "ds_read_b64_tr_b16 v[100:101], v161 offset:24576\n\t" \
  "ds_read_b64_tr_b16 v[102:103], v161 offset:26624\n\t" \
  "v_mfma_f32_16x16x32_bf16 v[140:143], v[108:111], v[32:35], v[140:143]\n\t" \
  "s_mov_b64 exec, vcc\n\t" \
  "buffer_load_dwordx4 v[24:27], v164, %[ob], s94 offen offset:2048 sc1\n\t" \
  "s_mov_b64 exec, -1\n\t" \
  "v_mfma_f32_16x16x32_bf16 v[156:159], v[108:111], v[36:39], v[156:159]\n\t" \
  "ds_read_b64_tr_b16 v[104:105], v162 offset:24576\n\t" \
  "ds_read_b64_tr_b16 v[106:107], v162 offset:26624\n\t" \
  "s_waitcnt lgkmcnt(10)\n\t" \
  "v_mfma_f32_16x16x32_bf16 v[128:131], v[112:115], v[40:43], v[128:131]\n\t" \
  "s_mov_b64 exec, vcc\n\t" \
  "buffer_load_dwordx4 v[28:31], v164, %[ob], s94 offen offset:3072 sc1\n\t" \
  "s_mov_b64 exec, -1\n\t" \
  "v_mfma_f32_16x16x32_bf16 v[144:147], v[112:115], v[44:47], v[144:147]\n\t" \
  "ds_read_b64_tr_b16 v[108:109], v163 offset:24576\n\t" \
  "ds_read_b64_tr_b16 v[110:111], v163 offset:26624\n\t" \
  "v_mfma_f32_16x16x32_bf16 v[132:135], v[116:119], v[40:43], v[132:135]\n\t" \
  "ds_read_b64_tr_b16 v[112:113], v160 offset:28672\n\t" \
  "ds_read_b32 v166, v165\n\t" \
  "v_mfma_f32_16x16x32_bf16 v[148:151], v[116:119], v[44:47], v[148:151]\n\t" \
  "ds_read_b64_tr_b16 v[114:115], v160 offset:30720\n\t" \
  "s_waitcnt lgkmcnt(11)\n\t" \
  "v_mfma_f32_16x16x32_bf16 v[136:139], v[120:123], v[40:43], v[136:139]\n\t" \
  "ds_read_b64_tr_b16 v[116:117], v161 offset:28672\n\t" \
  "v_mfma_f32_16x16x32_bf16 v[152:155], v[120:123], v[44:47], v[152:155]\n\t" \
  "ds_read_b64_tr_b16 v[118:119], v161 offset:30720\n\t" \
  "s_sub_u32 %[iss], %[iss], vcc_lo\n\t" \
  "v_mfma_f32_16x16x32_bf16 v[140:143], v[124:127], v[40:43], v[140:143]\n\t" \
  "ds_read_b64_tr_b16 v[120:121], v162 offset:28672\n\t" \
  "v_mfma_f32_16x16x32_bf16 v[156:159], v[124:127], v[44:47], v[156:159]\n\t" \
  "ds_read_b64_tr_b16 v[122:123], v162 offset:30720\n\t" \
  "s_add_u32 %[th], %[th], 1\n\t" \
  "s_cbranch_vccz QF3_%=\n\t" \
  "s_waitcnt vmcnt(12)\n\t" \
  "QG3_%=:\n\t" \
  "s_waitcnt lgkmcnt(11)\n\t" \
  "v_mfma_f32_16x16x32_bf16 v[128:131], v[96:99], v[48:51], v[128:131]\n\t" \
  "ds_read_b64_tr_b16 v[124:125], v163 offset:28672\n\t" \
  "v_readlane_b32 s94, v167, 7\n\t" \
  "v_mfma_f32_16x16x32_bf16 v[144:147], v[96:99], v[52:55], v[144:147]\n\t" \
  "ds_read_b64_tr_b16 v[126:127], v163 offset:30720\n\t" \
  "s_waitcnt lgkmcnt(7)\n\t" \
  "v_cmp_lt_u32 vcc, %[th], v166\n\t" \
  "v_mfma_f32_16x16x32_bf16 v[132:135], v[100:103], v[48:51], v[132:135]\n\t" \
  "s_mov_b64 exec, vcc\n\t" \
  "buffer_load_dwordx4 v[32:35], v164, %[ob], s94 offen offset:0 sc1\n\t" \
  "s_mov_b64 exec, -1\n\t" \
  "v_mfma_f32_16x16x32_bf16 v[148:151], v[100:103], v[52:55], v[148:151]\n\t" \
  "ds_read_b64_tr_b16 v[96:97], v160 offset:32768\n\t" \
  "ds_read_b64_tr_b16 v[98:99], v160 offset:34816\n\t" \
  "v_mfma_f32_16x16x32_bf16 v[136:139], v[104:107], v[48:51], v[136:139]\n\t" \
  "s_mov_b64 exec, vcc\n\t" \
  "buffer_load_dwordx4 v[36:39], v164, %[ob], s94 offen offset:1024 sc1\n\t" \
  "s_mov_b64 exec, -1\n\t" \
  "v_mfma_f32_16x16x32_bf16 v[152:155], v[104:107], v[52:55], v[152:155]\n\t" \
  "ds_read_b64_tr_b16 v[100:101], v161 offset:32768\n\t" \
  "ds_read_b64_tr_b16 v[102:103], v161 offset:34816\n\t" \
  "v_mfma_f32_16x16x32_bf16 v[140:143], v[108:111], v[48:51], v[140:143]\n\t" \
  "s_mov_b64 exec, vcc\n\t" \
  "buffer_load_dwordx4 v[40:43], v164, %[ob], s94 offen offset:2048 sc1\n\t" \
  "s_mov_b64 exec, -1\n\t" \
  "v_mfma_f32_16x16x32_bf16 v[156:159], v[108:111], v[52:55], v[156:159]\n\t" \
  "ds_read_b64_tr_b16 v[104:105], v162 offset:32768\n\t" \
  "ds_read_b64_tr_b16 v[106:107], v162 offset:34816\n\t" \
  "s_waitcnt lgkmcnt(10)\n\t" \
  "v_mfma_f32_16x16x32_bf16 v[128:131], v[112:115], v[56:59], v[128:131]\n\t" \
  "s_mov_b64 exec, vcc\n\t" \
  "buffer_load_dwordx4 v[44:47], v164, %[ob], s94 offen offset:3072 sc1\n\t" \
  "s_mov_b64 exec, -1\n\t" \
  "v_mfma_f32_16x16x32_bf16 v[144:147], v[112:115], v[60:63], v[144:147]\n\t" \
  "ds_read_b64_tr_b16 v[108:109], v163 offset:32768\n\t" \
  "ds_read_b64_tr_b16 v[110:111], v163 offset:34816\n\t" \
  "v_mfma_f32_16x16x32_bf16 v[132:135], v[116:119], v[56:59], v[132:135]\n\t" \
  "ds_read_b64_tr_b16 v[112:113], v160 offset:36864\n\t" \
  "ds_read_b32 v166, v165\n\t" \
  "v_mfma_f32_16x16x32_bf16 v[148:151], v[116:119], v[60:63], v[148:151]\n\t" \
  "ds_read_b64_tr_b16 v[114:115], v160 offset:38912\n\t" \
  "s_waitcnt lgkmcnt(11)\n\t" \
  "v_mfma_f32_16x16x32_bf16 v[136:139], v[120:123], v[56:59], v[136:139]\n\t" \
  "ds_read_b64_tr_b16 v[116:117], v161 offset:36864\n\t" \
  "v_mfma_f32_16x16x32_bf16 v[152:155], v[120:123], v[60:63], v[152:155]\n\t" \
  "ds_read_b64_tr_b16 v[118:119], v161 offset:38912\n\t" \
  "s_sub_u32 %[iss], %[iss], vcc_lo\n\t" \
  "v_mfma_f32_16x16x32_bf16 v[140:143], v[124:127], v[56:59], v[140:143]\n\t" \
  "ds_read_b64_tr_b16 v[120:121], v162 offset:36864\n\t" \
  "v_mfma_f32_16x16x32_bf16 v[156:159], v[124:127], v[60:63], v[156:159]\n\t" \
  "ds_read_b64_tr_b16 v[122:123], v162 offset:38912\n\t" \
  "s_add_u32 %[th], %[th], 1\n\t" \
  "s_cbranch_vccz QF4_%=\n\t" \
  "s_waitcnt vmcnt(12)\n\t" \
  "QG4_%=:\n\t" \
  "s_waitcnt lgkmcnt(11)\n\t" \
  "v_mfma_f32_16x16x32_bf16 v[128:131], v[96:99], v[64:67], v[128:131]\n\t" \
  "ds_read_b64_tr_b16 v[124:125], v163 offset:36864\n\t" \
  "v_readlane_b32 s94, v167, 8\n\t" \
  "v_mfma_f32_16x16x32_bf16 v[144:147], v[96:99], v[68:71], v[144:147]\n\t" \
  "ds_read_b64_tr_b16 v[126:127], v163 offset:38912\n\t" \
  "s_waitcnt lgkmcnt(7)\n\t" \
  "v_cmp_lt_u32 vcc, %[th], v166\n\t" \
  "v_mfma_f32_16x16x32_bf16 v[132:135], v[100:103], v[64:67], v[132:135]\n\t" \
  "s_mov_b64 exec, vcc\n\t" \
  "buffer_load_dwordx4 v[48:51], v164, %[ob], s94 offen offset:0 sc1\n\t" \
  "s_mov_b64 exec, -1\n\t" \
  "v_mfma_f32_16x16x32_bf16 v[148:151], v[100:103], v[68:71], v[148:151]\n\t" \
  "ds_read_b64_tr_b16 v[96:97], v160 offset:40960\n\t" \
  "ds_read_b64_tr_b16 v[98:99], v160 offset:43008\n\t" \
  "v_mfma_f32_16x16x32_bf16 v[136:139], v[104:107], v[64:67], v[136:139]\n\t" \
  "s_mov_b64 exec, vcc\n\t" \
  "buffer_load_dwordx4 v[52:55], v164, %[ob], s94 offen offset:1024 sc1\n\t" \
  "s_mov_b64 exec, -1\n\t" \
  "v_mfma_f32_16x16x32_bf16 v[152:155], v[104:107], v[68:71], v[152:155]\n\t" \
  "ds_read_b64_tr_b16 v[100:101], v161 offset:40960\n\t" \
  "ds_read_b64_tr_b16 v[102:103], v161 offset:43008\n\t" \
  "v_mfma_f32_16x16x32_bf16 v[140:143], v[108:111], v[64:67], v[140:143]\n\t" \
  "s_mov_b64 exec, vcc\n\t" \
  "buffer_load_dwordx4 v[56:59], v164, %[ob], s94 offen offset:2048 sc1\n\t" \
  "s_mov_b64 exec, -1\n\t" \
  "v_mfma_f32_16x16x32_bf16 v[156:159], v[108:111], v[68:71], v[156:159]\n\t" \
  "ds_read_b64_tr_b16 v[104:105], v162 offset:40960\n\t" \
  "ds_read_b64_tr_b16 v[106:107], v162 offset:43008\n\t" \
  "s_waitcnt lgkmcnt(10)\n\t" \
  "v_mfma_f32_16x16x32_bf16 v[128:131], v[112:115], v[72:75], v[128:131]\n\t" \
  "s_mov_b64 exec, vcc\n\t" \
  "buffer_load_dwordx4 v[60:63], v164, %[ob], s94 offen offset:3072 sc1\n\t" \
  "s_mov_b64 exec, -1\n\t" \
  "v_mfma_f32_16x16x32_bf16 v[144:147], v[112:115], v[76:79], v[144:147]\n\t" \
  "ds_read_b64_tr_b16 v[108:109], v163 offset:40960\n\t" \
  "ds_read_b64_tr_b16 v[110:111], v163 offset:43008\n\t" \
  "v_mfma_f32_16x16x32_bf16 v[132:135], v[116:119], v[72:75], v[132:135]\n\t" \
  "ds_read_b64_tr_b16 v[112:113], v160 offset:45056\n\t" \
  "ds_read_b32 v166, v165\n\t" \
  "v_mfma_f32_16x16x32_bf16 v[148:151], v[116:119], v[76:79], v[148:151]\n\t" \
  "ds_read_b64_tr_b16 v[114:115], v160 offset:47104\n\t" \
  "s_waitcnt lgkmcnt(11)\n\t" \
  "v_mfma_f32_16x16x32_bf16 v[136:139], v[120:123], v[72:75], v[136:139]\n\t" \
  "ds_read_b64_tr_b16 v[116:117], v161 offset:45056\n\t" \
  "v_mfma_f32_16x16x32_bf16 v[152:155], v[120:123], v[76:79], v[152:155]\n\t" \
  "ds_read_b64_tr_b16 v[118:119], v161 offset:47104\n\t" \
  "s_sub_u32 %[iss], %[iss], vcc_lo\n\t" \
  "v_mfma_f32_16x16x32_bf16 v[140:143], v[124:127], v[72:75], v[140:143]\n\t" \
  "ds_read_b64_tr_b16 v[120:121], v162 offset:45056\n\t" \
  "v_mfma_f32_16x16x32_bf16 v[156:159], v[124:127], v[76:79], v[156:159]\n\t" \
  "ds_read_b64_tr_b16 v[122:123], v162 offset:47104\n\t" \
  "s_add_u32 %[th], %[th], 1\n\t" \
  "s_cbranch_vccz QF5_%=\n\t" \
  "s_waitcnt vmcnt(12)\n\t" \
  "QG5_%=:\n\t" \
  "s_waitcnt lgkmcnt(11)\n\t" \
  "v_mfma_f32_16x16x32_bf16 v[128:131], v[96:99], v[0:3], v[128:131]\n\t" \
  "ds_read_b64_tr_b16 v[124:125], v163 offset:45056\n\t" \
  "v_readlane_b32 s94, v167, 9\n\t" \
  "v_mfma_f32_16x16x32_bf16 v[144:147], v[96:99], v[4:7], v[144:147]\n\t" \
  "ds_read_b64_tr_b16 v[126:127], v163 offset:47104\n\t" \
  "s_waitcnt lgkmcnt(7)\n\t" \
  "v_cmp_lt_u32 vcc, %[th], v166\n\t" \
  "v_mfma_f32_16x16x32_bf16 v[132:135], v[100:103], v[0:3], v[132:135]\n\t" \
  "s_mov_b64 exec, vcc\n\t" \
  "buffer_load_dwordx4 v[64:67], v164, %[ob], s94 offen offset:0 sc1\n\t" \
  "s_mov_b64 exec, -1\n\t" \
  "v_mfma_f32_16x16x32_bf16 v[148:151], v[100:103], v[4:7], v[148:151]\n\t" \
  "ds_read_b64_tr_b16 v[96:97], v160 offset:49152\n\t" \
  "ds_read_b64_tr_b16 v[98:99], v160 offset:51200\n\t" \
  "v_mfma_f32_16x16x32_bf16 v[136:139], v[104:107], v[0:3], v[136:139]\n\t" \
  "s_mov_b64 exec, vcc\n\t" \
  "buffer_load_dwordx4 v[68:71], v164, %[ob], s94 offen offset:1024 sc1\n\t" \
  "s_mov_b64 exec, -1\n\t" \
  "v_mfma_f32_16x16x32_bf16 v[152:155], v[104:107], v[4:7], v[152:155]\n\t" \
  "ds_read_b64_tr_b16 v[100:101], v161 offset:49152\n\t" \
  "ds_read_b64_tr_b16 v[102:103], v161 offset:51200\n\t" \
  "v_mfma_f32_16x16x32_bf16 v[140:143], v[108:111], v[0:3], v[140:143]\n\t" \
  "s_mov_b64 exec, vcc\n\t" \
  "buffer_load_dwordx4 v[72:75], v164, %[ob], s94 offen offset:2048 sc1\n\t" \
  "s_mov_b64 exec, -1\n\t" \
  "v_mfma_f32_16x16x32_bf16 v[156:159], v[108:111], v[4:7], v[156:159]\n\t" \
  "ds_read_b64_tr_b16 v[104:105], v162 offset:49152\n\t" \
  "ds_read_b64_tr_b16 v[106:107], v162 offset:51200\n\t" \
  "s_waitcnt lgkmcnt(10)\n\t" \
  "v_mfma_f32_16x16x32_bf16 v[128:131], v[112:115], v[8:11], v[128:131]\n\t" \
  "s_mov_b64 exec, vcc\n\t" \
  "buffer_load_dwordx4 v[76:79], v164, %[ob], s94 offen offset:3072 sc1\n\t" \
  "s_mov_b64 exec, -1\n\t" \
  "v_mfma_f32_16x16x32_bf16 v[144:147], v[112:115], v[12:15], v[144:147]\n\t" \
  "ds_read_b64_tr_b16 v[108:109], v163 offset:49152\n\t" \
  "ds_read_b64_tr_b16 v[110:111], v163 offset:51200\n\t" \
  "v_mfma_f32_16x16x32_bf16 v[132:135], v[116:119], v[8:11], v[132:135]\n\t" \
  "ds_read_b64_tr_b16 v[112:113], v160 offset:53248\n\t" \
  "ds_read_b32 v166, v165\n\t" \
  "v_mfma_f32_16x16x32_bf16 v[148:151], v[116:119], v[12:15], v[148:151]\n\t" \
  "ds_read_b64_tr_b16 v[114:115], v160 offset:55296\n\t" \
  "s_waitcnt lgkmcnt(11)\n\t" \
  "v_mfma_f32_16x16x32_bf16 v[136:139], v[120:123], v[8:11], v[136:139]\n\t" \
  "ds_read_b64_tr_b16 v[116:117], v161 offset:53248\n\t" \
  "v_mfma_f32_16x16x32_bf16 v[152:155], v[120:123], v[12:15], v[152:155]\n\t" \
  "ds_read_b64_tr_b16 v[118:119], v161 offset:55296\n\t" \
  "s_sub_u32 %[iss], %[iss], vcc_lo\n\t" \
  "v_mfma_f32_16x16x32_bf16 v[140:143], v[124:127], v[8:11], v[140:143]\n\t" \
  "ds_read_b64_tr_b16 v[120:121], v162 offset:53248\n\t" \
  "v_mfma_f32_16x16x32_bf16 v[156:159], v[124:127], v[12:15], v[156:159]\n\t" \
  "ds_read_b64_tr_b16 v[122:123], v162 offset:55296\n\t" \
  "s_add_u32 %[th], %[th], 1\n\t" \
  "s_cbranch_vccz QF6_%=\n\t" \
  "s_waitcnt vmcnt(12)\n\t" \
  "QG6_%=:\n\t" \
  "s_waitcnt lgkmcnt(11)\n\t" \
  "v_mfma_f32_16x16x32_bf16 v[128:131], v[96:99], v[16:19], v[128:131]\n\t" \
  "ds_read_b64_tr_b16 v[124:125], v163 offset:53248\n\t" \
  "v_readlane_b32 s94, v167, 10\n\t" \
  "v_mfma_f32_16x16x32_bf16 v[144:147], v[96:99], v[20:23], v[144:147]\n\t" \
  "ds_read_b64_tr_b16 v[126:127], v163 offset:55296\n\t" \
  "s_waitcnt lgkmcnt(7)\n\t" \
  "v_cmp_lt_u32 vcc, %[th], v166\n\t" \
  "v_mfma_f32_16x16x32_bf16 v[132:135], v[100:103], v[16:19], v[132:135]\n\t" \
  "s_mov_b64 exec, vcc\n\t" \
  "buffer_load_dwordx4 v[0:3], v164, %[ob], s94 offen offset:0 sc1\n\t" \
  "s_mov_b64 exec, -1\n\t" \
  "v_mfma_f32_16x16x32_bf16 v[148:151], v[100:103], v[20:23], v[148:151]\n\t" \
  "ds_read_b64_tr_b16 v[96:97], v160 offset:57344\n\t" \
  "ds_read_b64_tr_b16 v[98:99], v160 offset:59392\n\t" \
  "v_mfma_f32_16x16x32_bf16 v[136:139], v[104:107], v[16:19], v[136:139]\n\t" \
  "s_mov_b64 exec, vcc\n\t" \
  "buffer_load_dwordx4 v[4:7], v164, %[ob], s94 offen offset:1024 sc1\n\t" \
  "s_mov_b64 exec, -1\n\t" \
  "v_mfma_f32_16x16x32_bf16 v[152:155], v[104:107], v[20:23], v[152:155]\n\t" \
  "ds_read_b64_tr_b16 v[100:101], v161 offset:57344\n\t" \
  "ds_read_b64_tr_b16 v[102:103], v161 offset:59392\n\t" \
  "v_mfma_f32_16x16x32_bf16 v[140:143], v[108:111], v[16:19], v[140:143]\n\t" \
  "s_mov_b64 exec, vcc\n\t" \
  "buffer_load_dwordx4 v[8:11], v164, %[ob], s94 offen offset:2048 sc1\n\t" \
  "s_mov_b64 exec, -1\n\t" \
  "v_mfma_f32_16x16x32_bf16 v[156:159], v[108:111], v[20:23], v[156:159]\n\t" \
  "ds_read_b64_tr_b16 v[104:105], v162 offset:57344\n\t" \
  "ds_read_b64_tr_b16 v[106:107], v162 offset:59392\n\t" \
  "s_waitcnt lgkmcnt(10)\n\t" \
  "v_mfma_f32_16x16x32_bf16 v[128:131], v[112:115], v[24:27], v[128:131]\n\t" \
  "s_mov_b64 exec, vcc\n\t" \
  "buffer_load_dwordx4 v[12:15], v164, %[ob], s94 offen offset:3072 sc1\n\t" \
  "s_mov_b64 exec, -1\n\t" \
  "v_mfma_f32_16x16x32_bf16 v[144:147], v[112:115], v[28:31], v[144:147]\n\t" \
  "ds_read_b64_tr_b16 v[108:109], v163 offset:57344\n\t" \
  "ds_read_b64_tr_b16 v[110:111], v163 offset:59392\n\t" \
  "v_mfma_f32_16x16x32_bf16 v[132:135], v[116:119], v[24:27], v[132:135]\n\t" \
  "ds_read_b64_tr_b16 v[112:113], v160 offset:61440\n\t" \
  "ds_read_b32 v166, v165\n\t" \
  "v_mfma_f32_16x16x32_bf16 v[148:151], v[116:119], v[28:31], v[148:151]\n\t" \
  "ds_read_b64_tr_b16 v[114:115], v160 offset:63488\n\t" \
  "s_waitcnt lgkmcnt(11)\n\t" \
  "v_mfma_f32_16x16x32_bf16 v[136:139], v[120:123], v[24:27], v[136:139]\n\t" \
  "ds_read_b64_tr_b16 v[116:117], v161 offset:61440\n\t" \
  "v_mfma_f32_16x16x32_bf16 v[152:155], v[120:123], v[28:31], v[152:155]\n\t" \
  "ds_read_b64_tr_b16 v[118:119], v161 offset:63488\n\t" \
  "s_sub_u32 %[iss], %[iss], vcc_lo\n\t" \
  "v_mfma_f32_16x16x32_bf16 v[140:143], v[124:127], v[24:27], v[140:143]\n\t" \
  "ds_read_b64_tr_b16 v[120:121], v162 offset:61440\n\t" \
  "s_mov_b64 exec, 1\n\t" \
  "ds_write_b32 v165, v165 offset:76\n\t" \
  "s_mov_b64 exec, -1\n\t" \
  "v_mfma_f32_16x16x32_bf16 v[156:159], v[124:127], v[28:31], v[156:159]\n\t" \
  "ds_read_b64_tr_b16 v[122:123], v162 offset:63488\n\t" \
  "s_add_u32 %[th], %[th], 1\n\t" \
  "s_cbranch_vccz QF7_%=\n\t" \
  "s_waitcnt vmcnt(12)\n\t" \
  "QG7_%=:\n\t" \
  "s_waitcnt lgkmcnt(12)\n\t" \
  "v_mfma_f32_16x16x32_bf16 v[128:131], v[96:99], v[32:35], v[128:131]\n\t" \
  "ds_read_b64_tr_b16 v[124:125], v163 offset:61440\n\t" \
  "v_readlane_b32 s94, v167, 11\n\t" \
  "v_mfma_f32_16x16x32_bf16 v[144:147], v[96:99], v[36:39], v[144:147]\n\t" \
  "ds_read_b64_tr_b16 v[126:127], v163 offset:63488\n\t" \
  "s_waitcnt lgkmcnt(8)\n\t" \
  "v_cmp_lt_u32 vcc, %[th], v166\n\t" \
  "v_mfma_f32_16x16x32_bf16 v[132:135], v[100:103], v[32:35], v[132:135]\n\t" \
  "v_add_u32 v160, 0x10000, v160\n\t" \
  "s_mov_b64 exec, vcc\n\t" \
  "buffer_load_dwordx4 v[16:19], v164, %[ob], s94 offen offset:0 sc1\n\t" \
  "s_mov_b64 exec, -1\n\t" \
  "v_mfma_f32_16x16x32_bf16 v[148:151], v[100:103], v[36:39], v[148:151]\n\t" \
  "ds_read_b64_tr_b16 v[96:97], v160 offset:0\n\t" \
  "ds_read_b64_tr_b16 v[98:99], v160 offset:2048\n\t" \
  "v_mfma_f32_16x16x32_bf16 v[136:139], v[104:107], v[32:35], v[136:139]\n\t" \
  "v_add_u32 v161, 0x10000, v161\n\t" \
  "s_mov_b64 exec, vcc\n\t" \
  "buffer_load_dwordx4 v[20:23], v164, %[ob], s94 offen offset:1024 sc1\n\t" \
  "s_mov_b64 exec, -1\n\t" \
  "v_mfma_f32_16x16x32_bf16 v[152:155], v[104:107], v[36:39], v[152:155]\n\t" \
  "ds_read_b64_tr_b16 v[100:101], v161 offset:0\n\t" \
  "ds_read_b64_tr_b16 v[102:103], v161 offset:2048\n\t" \
  "v_mfma_f32_16x16x32_bf16 v[140:143], v[108:111], v[32:35], v[140:143]\n\t" \
  "v_add_u32 v162, 0x10000, v162\n\t" \
  "s_mov_b64 exec, vcc\n\t" \
  "buffer_load_dwordx4 v[24:27], v164, %[ob], s94 offen offset:2048 sc1\n\t" \
  "s_mov_b64 exec, -1\n\t" \
  "v_mfma_f32_16x16x32_bf16 v[156:159], v[108:111], v[36:39], v[156:159]\n\t" \
  "ds_read_b64_tr_b16 v[104:105], v162 offset:0\n\t" \
  "ds_read_b64_tr_b16 v[106:107], v162 offset:2048\n\t" \
  "s_waitcnt lgkmcnt(11)\n\t" \
  "v_mfma_f32_16x16x32_bf16 v[128:131], v[112:115], v[40:43], v[128:131]\n\t" \
  "v_add_u32 v163, 0x10000, v163\n\t" \
  "s_mov_b64 exec, vcc\n\t" \
  "buffer_load_dwordx4 v[28:31], v164, %[ob], s94 offen offset:3072 sc1\n\t" \
  "s_mov_b64 exec, -1\n\t" \
  "v_mfma_f32_16x16x32_bf16 v[144:147], v[112:115], v[44:47], v[144:147]\n\t" \
  "ds_read_b64_tr_b16 v[108:109], v163 offset:0\n\t" \
  "ds_read_b64_tr_b16 v[110:111], v163 offset:2048\n\t" \
  "v_mfma_f32_16x16x32_bf16 v[132:135], v[116:119], v[40:43], v[132:135]\n\t" \
  "ds_read_b64_tr_b16 v[112:113], v160 offset:4096\n\t" \
  "ds_read_b32 v166, v165\n\t" \
  "v_mfma_f32_16x16x32_bf16 v[148:151], v[116:119], v[44:47], v[148:151]\n\t" \
  "ds_read_b64_tr_b16 v[114:115], v160 offset:6144\n\t" \
  "s_waitcnt lgkmcnt(11)\n\t" \
  "v_mfma_f32_16x16x32_bf16 v[136:139], v[120:123], v[40:43], v[136:139]\n\t" \
  "ds_read_b64_tr_b16 v[116:117], v161 offset:4096\n\t" \
  "v_mfma_f32_16x16x32_bf16 v[152:155], v[120:123], v[44:47], v[152:155]\n\t" \
  "ds_read_b64_tr_b16 v[118:119], v161 offset:6144\n\t" \
  "s_sub_u32 %[iss], %[iss], vcc_lo\n\t" \
  "v_mfma_f32_16x16x32_bf16 v[140:143], v[124:127], v[40:43], v[140:143]\n\t" \
  "ds_read_b64_tr_b16 v[120:121], v162 offset:4096\n\t" \
  "v_mfma_f32_16x16x32_bf16 v[156:159], v[124:127], v[44:47], v[156:159]\n\t" \
  "ds_read_b64_tr_b16 v[122:123], v162 offset:6144\n\t" \
  "s_add_u32 %[th], %[th], 1\n\t" \
  "s_cbranch_vccz QF8_%=\n\t" \
  "s_waitcnt vmcnt(12)\n\t" \
  "QG8_%=:\n\t" \
  "s_waitcnt lgkmcnt(11)\n\t" \
  "v_mfma_f32_16x16x32_bf16 v[128:131], v[96:99], v[48:51], v[128:131]\n\t" \
  "ds_read_b64_tr_b16 v[124:125], v163 offset:4096\n\t" \
  "v_readlane_b32 s94, v167, 12\n\t" \
  "v_mfma_f32_16x16x32_bf16 v[144:147], v[96:99], v[52:55], v[144:147]\n\t" \
  "ds_read_b64_tr_b16 v[126:127], v163 offset:6144\n\t" \
  "s_waitcnt lgkmcnt(7)\n\t" \
  "v_cmp_lt_u32 vcc, %[th], v166\n\t" \
  "v_mfma_f32_16x16x32_bf16 v[132:135], v[100:103], v[48:51], v[132:135]\n\t" \
  "s_mov_b64 exec, vcc\n\t" \
  "buffer_load_dwordx4 v[32:35], v164, %[ob], s94 offen offset:0 sc1\n\t" \
  "s_mov_b64 exec, -1\n\t" \
  "v_mfma_f32_16x16x32_bf16 v[148:151], v[100:103], v[52:55], v[148:151]\n\t" \
  "ds_read_b64_tr_b16 v[96:97], v160 offset:8192\n\t" \
  "ds_read_b64_tr_b16 v[98:99], v160 offset:10240\n\t" \
  "v_mfma_f32_16x16x32_bf16 v[136:139], v[104:107], v[48:51], v[136:139]\n\t" \
  "s_mov_b64 exec, vcc\n\t" \
  "buffer_load_dwordx4 v[36:39], v164, %[ob], s94 offen offset:1024 sc1\n\t" \
  "s_mov_b64 exec, -1\n\t" \
  "v_mfma_f32_16x16x32_bf16 v[152:155], v[104:107], v[52:55], v[152:155]\n\t" \
  "ds_read_b64_tr_b16 v[100:101], v161 offset:8192\n\t" \
  "ds_read_b64_tr_b16 v[102:103], v161 offset:10240\n\t" \
  "v_mfma_f32_16x16x32_bf16 v[140:143], v[108:111], v[48:51], v[140:143]\n\t" \
  "s_mov_b64 exec, vcc\n\t" \
  "buffer_load_dwordx4 v[40:43], v164, %[ob], s94 offen offset:2048 sc1\n\t" \
  "s_mov_b64 exec, -1\n\t" \
  "v_mfma_f32_16x16x32_bf16 v[156:159], v[108:111], v[52:55], v[156:159]\n\t" \
  "ds_read_b64_tr_b16 v[104:105], v162 offset:8192\n\t" \
  "ds_read_b64_tr_b16 v[106:107], v162 offset:10240\n\t" \
  "s_waitcnt lgkmcnt(10)\n\t" \
  "v_mfma_f32_16x16x32_bf16 v[128:131], v[112:115], v[56:59], v[128:131]\n\t" \
  "s_mov_b64 exec, vcc\n\t" \
  "buffer_load_dwordx4 v[44:47], v164, %[ob], s94 offen offset:3072 sc1\n\t" \
  "s_mov_b64 exec, -1\n\t" \
  "v_mfma_f32_16x16x32_bf16 v[144:147], v[112:115], v[60:63], v[144:147]\n\t" \
  "ds_read_b64_tr_b16 v[108:109], v163 offset:8192\n\t" \
  "ds_read_b64_tr_b16 v[110:111], v163 offset:10240\n\t" \
  "v_mfma_f32_16x16x32_bf16 v[132:135], v[116:119], v[56:59], v[132:135]\n\t" \
  "ds_read_b64_tr_b16 v[112:113], v160 offset:12288\n\t" \
  "ds_read_b32 v166, v165\n\t" \
  "v_mfma_f32_16x16x32_bf16 v[148:151], v[116:119], v[60:63], v[148:151]\n\t" \
  "ds_read_b64_tr_b16 v[114:115], v160 offset:14336\n\t" \
  "s_waitcnt lgkmcnt(11)\n\t" \
  "v_mfma_f32_16x16x32_bf16 v[136:139], v[120:123], v[56:59], v[136:139]\n\t" \
  "ds_read_b64_tr_b16 v[116:117], v161 offset:12288\n\t" \
  "v_mfma_f32_16x16x32_bf16 v[152:155], v[120:123], v[60:63], v[152:155]\n\t" \
  "ds_read_b64_tr_b16 v[118:119], v161 offset:14336\n\t" \
  "s_sub_u32 %[iss], %[iss], vcc_lo\n\t" \
  "v_mfma_f32_16x16x32_bf16 v[140:143], v[124:127], v[56:59], v[140:143]\n\t" \
  "ds_read_b64_tr_b16 v[120:121], v162 offset:12288\n\t" \
  "v_mfma_f32_16x16x32_bf16 v[156:159], v[124:127], v[60:63], v[156:159]\n\t" \
  "ds_read_b64_tr_b16 v[122:123], v162 offset:14336\n\t" \
  "s_add_u32 %[th], %[th], 1\n\t" \
  "s_cbranch_vccz QF9_%=\n\t" \
  "s_waitcnt vmcnt(12)\n\t" \
  "QG9_%=:\n\t" \
  "s_waitcnt lgkmcnt(11)\n\t" \
  "v_mfma_f32_16x16x32_bf16 v[128:131], v[96:99], v[64:67], v[128:131]\n\t" \
  "ds_read_b64_tr_b16 v[124:125], v163 offset:12288\n\t" \
  "v_readlane_b32 s94, v167, 13\n\t" \
  "v_mfma_f32_16x16x32_bf16 v[144:147], v[96:99], v[68:71], v[144:147]\n\t" \
  "ds_read_b64_tr_b16 v[126:127], v163 offset:14336\n\t" \
  "s_waitcnt lgkmcnt(7)\n\t" \
  "v_cmp_lt_u32 vcc, %[th], v166\n\t" \
  "v_mfma_f32_16x16x32_bf16 v[132:135], v[100:103], v[64:67], v[132:135]\n\t" \
  "s_mov_b64 exec, vcc\n\t" \
  "buffer_load_dwordx4 v[48:51], v164, %[ob], s94 offen offset:0 sc1\n\t" \
  "s_mov_b64 exec, -1\n\t" \
  "v_mfma_f32_16x16x32_bf16 v[148:151], v[100:103], v[68:71], v[148:151]\n\t" \
  "ds_read_b64_tr_b16 v[96:97], v160 offset:16384\n\t" \
  "ds_read_b64_tr_b16 v[98:99], v160 offset:18432\n\t" \
  "v_mfma_f32_16x16x32_bf16 v[136:139], v[104:107], v[64:67], v[136:139]\n\t" \
  "s_mov_b64 exec, vcc\n\t" \
  "buffer_load_dwordx4 v[52:55], v164, %[ob], s94 offen offset:1024 sc1\n\t" \
  "s_mov_b64 exec, -1\n\t" \
  "v_mfma_f32_16x16x32_bf16 v[152:155], v[104:107], v[68:71], v[152:155]\n\t" \
  "ds_read_b64_tr_b16 v[100:101], v161 offset:16384\n\t" \
  "ds_read_b64_tr_b16 v[102:103], v161 offset:18432\n\t" \
  "v_mfma_f32_16x16x32_bf16 v[140:143], v[108:111], v[64:67], v[140:143]\n\t" \
  "s_mov_b64 exec, vcc\n\t" \
  "buffer_load_dwordx4 v[56:59], v164, %[ob], s94 offen offset:2048 sc1\n\t" \
  "s_mov_b64 exec, -1\n\t" \
  "v_mfma_f32_16x16x32_bf16 v[156:159], v[108:111], v[68:71], v[156:159]\n\t" \
  "ds_read_b64_tr_b16 v[104:105], v162 offset:16384\n\t" \
  "ds_read_b64_tr_b16 v[106:107], v162 offset:18432\n\t" \
  "s_waitcnt lgkmcnt(10)\n\t" \
  "v_mfma_f32_16x16x32_bf16 v[128:131], v[112:115], v[72:75], v[128:131]\n\t" \
  "s_mov_b64 exec, vcc\n\t" \
  "buffer_load_dwordx4 v[60:63], v164, %[ob], s94 offen offset:3072 sc1\n\t" \
  "s_mov_b64 exec, -1\n\t" \
  "v_mfma_f32_16x16x32_bf16 v[144:147], v[112:115], v[76:79], v[144:147]\n\t" \
  "ds_read_b64_tr_b16 v[108:109], v163 offset:16384\n\t" \
  "ds_read_b64_tr_b16 v[110:111], v163 offset:18432\n\t" \
  "v_mfma_f32_16x16x32_bf16 v[132:135], v[116:119], v[72:75], v[132:135]\n\t" \
  "ds_read_b64_tr_b16 v[112:113], v160 offset:20480\n\t" \
  "ds_read_b32 v166, v165\n\t" \
  "v_mfma_f32_16x16x32_bf16 v[148:151], v[116:119], v[76:79], v[148:151]\n\t" \
  "ds_read_b64_tr_b16 v[114:115], v160 offset:22528\n\t" \
  "s_waitcnt lgkmcnt(11)\n\t" \
  "v_mfma_f32_16x16x32_bf16 v[136:139], v[120:123], v[72:75], v[136:139]\n\t" \
  "ds_read_b64_tr_b16 v[116:117], v161 offset:20480\n\t" \
  "v_mfma_f32_16x16x32_bf16 v[152:155], v[120:123], v[76:79], v[152:155]\n\t" \
  "ds_read_b64_tr_b16 v[118:119], v161 offset:22528\n\t" \
  "s_sub_u32 %[iss], %[iss], vcc_lo\n\t" \
  "v_mfma_f32_16x16x32_bf16 v[140:143], v[124:127], v[72:75], v[140:143]\n\t" \
  "ds_read_b64_tr_b16 v[120:121], v162 offset:20480\n\t" \
  "v_mfma_f32_16x16x32_bf16 v[156:159], v[124:127], v[76:79], v[156:159]\n\t" \
  "ds_read_b64_tr_b16 v[122:123], v162 offset:22528\n\t" \
  "s_add_u32 %[th], %[th], 1\n\t" \
  "s_cbranch_vccz QF10_%=\n\t" \
  "s_waitcnt vmcnt(12)\n\t" \
  "QG10_%=:\n\t" \
  "s_waitcnt lgkmcnt(11)\n\t" \
  "v_mfma_f32_16x16x32_bf16 v[128:131], v[96:99], v[0:3], v[128:131]\n\t" \
  "ds_read_b64_tr_b16 v[124:125], v163 offset:20480\n\t" \
  "v_readlane_b32 s94, v167, 14\n\t" \
  "v_mfma_f32_16x16x32_bf16 v[144:147], v[96:99], v[4:7], v[144:147]\n\t" \
  "ds_read_b64_tr_b16 v[126:127], v163 offset:22528\n\t" \
  "s_waitcnt lgkmcnt(7)\n\t" \
  "v_cmp_lt_u32 vcc, %[th], v166\n\t" \
  "v_mfma_f32_16x16x32_bf16 v[132:135], v[100:103], v[0:3], v[132:135]\n\t" \
  "s_mov_b64 exec, vcc\n\t" \
  "buffer_load_dwordx4 v[64:67], v164, %[ob], s94 offen offset:0 sc1\n\t" \
  "s_mov_b64 exec, -1\n\t" \
  "v_mfma_f32_16x16x32_bf16 v[148:151], v[100:103], v[4:7], v[148:151]\n\t" \
  "ds_read_b64_tr_b16 v[96:97], v160 offset:24576\n\t" \
  "ds_read_b64_tr_b16 v[98:99], v160 offset:26624\n\t" \
  "v_mfma_f32_16x16x32_bf16 v[136:139], v[104:107], v[0:3], v[136:139]\n\t" \
  "s_mov_b64 exec, vcc\n\t" \
  "buffer_load_dwordx4 v[68:71], v164, %[ob], s94 offen offset:1024 sc1\n\t" \
  "s_mov_b64 exec, -1\n\t" \
  "v_mfma_f32_16x16x32_bf16 v[152:155], v[104:107], v[4:7], v[152:155]\n\t" \
  "ds_read_b64_tr_b16 v[100:101], v161 offset:24576\n\t" \
  "ds_read_b64_tr_b16 v[102:103], v161 offset:26624\n\t" \
  "v_mfma_f32_16x16x32_bf16 v[140:143], v[108:111], v[0:3], v[140:143]\n\t" \
  "s_mov_b64 exec, vcc\n\t" \
  "buffer_load_dwordx4 v[72:75], v164, %[ob], s94 offen offset:2048 sc1\n\t" \
  "s_mov_b64 exec, -1\n\t" \
  "v_mfma_f32_16x16x32_bf16 v[156:159], v[108:111], v[4:7], v[156:159]\n\t" \
  "ds_read_b64_tr_b16 v[104:105], v162 offset:24576\n\t" \
  "ds_read_b64_tr_b16 v[106:107], v162 offset:26624\n\t" \
  "s_waitcnt lgkmcnt(10)\n\t" \
  "v_mfma_f32_16x16x32_bf16 v[128:131], v[112:115], v[8:11], v[128:131]\n\t" \
  "s_mov_b64 exec, vcc\n\t" \
  "buffer_load_dwordx4 v[76:79], v164, %[ob], s94 offen offset:3072 sc1\n\t" \
  "s_mov_b64 exec, -1\n\t" \
  "v_mfma_f32_16x16x32_bf16 v[144:147], v[112:115], v[12:15], v[144:147]\n\t" \
  "ds_read_b64_tr_b16 v[108:109], v163 offset:24576\n\t" \
  "ds_read_b64_tr_b16 v[110:111], v163 offset:26624\n\t" \
  "v_mfma_f32_16x16x32_bf16 v[132:135], v[116:119], v[8:11], v[132:135]\n\t" \
  "ds_read_b64_tr_b16 v[112:113], v160 offset:28672\n\t" \
  "ds_read_b32 v166, v165\n\t" \
  "v_mfma_f32_16x16x32_bf16 v[148:151], v[116:119], v[12:15], v[148:151]\n\t" \
  "ds_read_b64_tr_b16 v[114:115], v160 offset:30720\n\t" \
  "s_waitcnt lgkmcnt(11)\n\t" \
  "v_mfma_f32_16x16x32_bf16 v[136:139], v[120:123], v[8:11], v[136:139]\n\t" \
  "ds_read_b64_tr_b16 v[116:117], v161 offset:28672\n\t" \
  "v_mfma_f32_16x16x32_bf16 v[152:155], v[120:123], v[12:15], v[152:155]\n\t" \
  "ds_read_b64_tr_b16 v[118:119], v161 offset:30720\n\t" \
  "s_sub_u32 %[iss], %[iss], vcc_lo\n\t" \
  "v_mfma_f32_16x16x32_bf16 v[140:143], v[124:127], v[8:11], v[140:143]\n\t" \
  "ds_read_b64_tr_b16 v[120:121], v162 offset:28672\n\t" \
  "v_mfma_f32_16x16x32_bf16 v[156:159], v[124:127], v[12:15], v[156:159]\n\t" \
  "ds_read_b64_tr_b16 v[122:123], v162 offset:30720\n\t" \
  "s_add_u32 %[th], %[th], 1\n\t" \
  "s_cbranch_vccz QF11_%=\n\t" \
  "s_waitcnt vmcnt(12)\n\t" \
  "QG11_%=:\n\t" \
  "s_waitcnt lgkmcnt(11)\n\t" \
  "v_mfma_f32_16x16x32_bf16 v[128:131], v[96:99], v[16:19], v[128:131]\n\t" \
  "ds_read_b64_tr_b16 v[124:125], v163 offset:28672\n\t" \
  "v_readlane_b32 s94, v167, 15\n\t" \
  "v_mfma_f32_16x16x32_bf16 v[144:147], v[96:99], v[20:23], v[144:147]\n\t" \
  "ds_read_b64_tr_b16 v[126:127], v163 offset:30720\n\t" \
  "s_waitcnt lgkmcnt(7)\n\t" \
  "v_cmp_lt_u32 vcc, %[th], v166\n\t" \
  "v_mfma_f32_16x16x32_bf16 v[132:135], v[100:103], v[16:19], v[132:135]\n\t" \
  "s_mov_b64 exec, vcc\n\t" \
  "buffer_load_dwordx4 v[0:3], v164, %[ob], s94 offen offset:0 sc1\n\t" \
  "s_mov_b64 exec, -1\n\t" \
  "v_mfma_f32_16x16x32_bf16 v[148:151], v[100:103], v[20:23], v[148:151]\n\t" \
  "ds_read_b64_tr_b16 v[96:97], v160 offset:32768\n\t" \
  "ds_read_b64_tr_b16 v[98:99], v160 offset:34816\n\t" \
  "v_mfma_f32_16x16x32_bf16 v[136:139], v[104:107], v[16:19], v[136:139]\n\t" \
  "s_mov_b64 exec, vcc\n\t" \
  "buffer_load_dwordx4 v[4:7], v164, %[ob], s94 offen offset:1024 sc1\n\t" \
  "s_mov_b64 exec, -1\n\t" \
  "v_mfma_f32_16x16x32_bf16 v[152:155], v[104:107], v[20:23], v[152:155]\n\t" \
  "ds_read_b64_tr_b16 v[100:101], v161 offset:32768\n\t" \
  "ds_read_b64_tr_b16 v[102:103], v161 offset:34816\n\t" \
  "v_mfma_f32_16x16x32_bf16 v[140:143], v[108:111], v[16:19], v[140:143]\n\t" \
  "s_mov_b64 exec, vcc\n\t" \
  "buffer_load_dwordx4 v[8:11], v164, %[ob], s94 offen offset:2048 sc1\n\t" \
  "s_mov_b64 exec, -1\n\t" \
  "v_mfma_f32_16x16x32_bf16 v[156:159], v[108:111], v[20:23], v[156:159]\n\t" \
  "ds_read_b64_tr_b16 v[104:105], v162 offset:32768\n\t" \
  "ds_read_b64_tr_b16 v[106:107], v162 offset:34816\n\t" \
  "s_waitcnt lgkmcnt(10)\n\t" \
  "v_mfma_f32_16x16x32_bf16 v[128:131], v[112:115], v[24:27], v[128:131]\n\t" \
  "s_mov_b64 exec, vcc\n\t" \
  "buffer_load_dwordx4 v[12:15], v164, %[ob], s94 offen offset:3072 sc1\n\t" \
  "s_mov_b64 exec, -1\n\t" \
  "v_mfma_f32_16x16x32_bf16 v[144:147], v[112:115], v[28:31], v[144:147]\n\t" \
  "ds_read_b64_tr_b16 v[108:109], v163 offset:32768\n\t" \
  "ds_read_b64_tr_b16 v[110:111], v163 offset:34816\n\t" \
  "v_mfma_f32_16x16x32_bf16 v[132:135], v[116:119], v[24:27], v[132:135]\n\t" \
  "ds_read_b64_tr_b16 v[112:113], v160 offset:36864\n\t" \
  "v_mfma_f32_16x16x32_bf16 v[148:151], v[116:119], v[28:31], v[148:151]\n\t" \
  "ds_read_b64_tr_b16 v[114:115], v160 offset:38912\n\t" \
  "s_waitcnt lgkmcnt(10)\n\t" \
  "v_mfma_f32_16x16x32_bf16 v[136:139], v[120:123], v[24:27], v[136:139]\n\t" \
  "ds_read_b64_tr_b16 v[116:117], v161 offset:36864\n\t" \
  "v_mfma_f32_16x16x32_bf16 v[152:155], v[120:123], v[28:31], v[152:155]\n\t" \
  "ds_read_b64_tr_b16 v[118:119], v161 offset:38912\n\t" \
  "s_sub_u32 %[iss], %[iss], vcc_lo\n\t" \
  "v_mfma_f32_16x16x32_bf16 v[140:143], v[124:127], v[24:27], v[140:143]\n\t" \
  "ds_read_b64_tr_b16 v[120:121], v162 offset:36864\n\t" \
  "v_mfma_f32_16x16x32_bf16 v[156:159], v[124:127], v[28:31], v[156:159]\n\t" \
  "ds_read_b64_tr_b16 v[122:123], v162 offset:38912\n\t" \
  "s_add_u32 %[th], %[th], 1\n\t" \
  "s_cbranch_vccz QF12_%=\n\t" \
  "s_waitcnt vmcnt(12)\n\t" \
  "QG12_%=:\n\t" \
  "s_waitcnt lgkmcnt(10)\n\t" \
  "v_mfma_f32_16x16x32_bf16 v[128:131], v[96:99], v[32:35], v[128:131]\n\t" \
  "ds_read_b64_tr_b16 v[124:125], v163 offset:36864\n\t" \
  "v_mfma_f32_16x16x32_bf16 v[144:147], v[96:99], v[36:39], v[144:147]\n\t" \
  "ds_read_b64_tr_b16 v[126:127], v163 offset:38912\n\t" \
  "v_mfma_f32_16x16x32_bf16 v[132:135], v[100:103], v[32:35], v[132:135]\n\t" \
  "ds_read_b64_tr_b16 v[96:97], v160 offset:40960\n\t" \
  "v_mfma_f32_16x16x32_bf16 v[148:151], v[100:103], v[36:39], v[148:151]\n\t" \
  "ds_read_b64_tr_b16 v[98:99], v160 offset:43008\n\t" \
  "s_waitcnt lgkmcnt(10)\n\t" \
  "v_mfma_f32_16x16x32_bf16 v[136:139], v[104:107], v[32:35], v[136:139]\n\t" \
  "ds_read_b64_tr_b16 v[100:101], v161 offset:40960\n\t" \
  "v_mfma_f32_16x16x32_bf16 v[152:155], v[104:107], v[36:39], v[152:155]\n\t" \
  "ds_read_b64_tr_b16 v[102:103], v161 offset:43008\n\t" \
  "v_mfma_f32_16x16x32_bf16 v[140:143], v[108:111], v[32:35], v[140:143]\n\t" \
  "ds_read_b64_tr_b16 v[104:105], v162 offset:40960\n\t" \
  "v_mfma_f32_16x16x32_bf16 v[156:159], v[108:111], v[36:39], v[156:159]\n\t" \
  "ds_read_b64_tr_b16 v[106:107], v162 offset:43008\n\t" \
  "s_waitcnt lgkmcnt(10)\n\t" \
  "v_mfma_f32_16x16x32_bf16 v[128:131], v[112:115], v[40:43], v[128:131]\n\t" \
  "ds_read_b64_tr_b16 v[108:109], v163 offset:40960\n\t" \
  "v_mfma_f32_16x16x32_bf16 v[144:147], v[112:115], v[44:47], v[144:147]\n\t" \
  "ds_read_b64_tr_b16 v[110:111], v163 offset:43008\n\t" \
  "v_mfma_f32_16x16x32_bf16 v[132:135], v[116:119], v[40:43], v[132:135]\n\t" \
  "ds_read_b64_tr_b16 v[112:113], v160 offset:45056\n\t" \
  "v_mfma_f32_16x16x32_bf16 v[148:151], v[116:119], v[44:47], v[148:151]\n\t" \
  "ds_read_b64_tr_b16 v[114:115], v160 offset:47104\n\t" \
  "s_waitcnt lgkmcnt(10)\n\t" \
  "v_mfma_f32_16x16x32_bf16 v[136:139], v[120:123], v[40:43], v[136:139]\n\t" \
  "ds_read_b64_tr_b16 v[116:117], v161 offset:45056\n\t" \
  "v_mfma_f32_16x16x32_bf16 v[152:155], v[120:123], v[44:47], v[152:155]\n\t" \
  "ds_read_b64_tr_b16 v[118:119], v161 offset:47104\n\t" \
  "v_mfma_f32_16x16x32_bf16 v[140:143], v[124:127], v[40:43], v[140:143]\n\t" \
  "ds_read_b64_tr_b16 v[120:121], v162 offset:45056\n\t" \
  "v_mfma_f32_16x16x32_bf16 v[156:159], v[124:127], v[44:47], v[156:159]\n\t" \
  "ds_read_b64_tr_b16 v[122:123], v162 offset:47104\n\t" \
  "s_cbranch_vccz QF13_%=\n\t" \
  "s_waitcnt vmcnt(8)\n\t" \
  "QG13_%=:\n\t" \
  "s_waitcnt lgkmcnt(10)\n\t" \
  "v_mfma_f32_16x16x32_bf16 v[128:131], v[96:99], v[48:51], v[128:131]\n\t" \
  "ds_read_b64_tr_b16 v[124:125], v163 offset:45056\n\t" \
  "v_mfma_f32_16x16x32_bf16 v[144:147], v[96:99], v[52:55], v[144:147]\n\t" \
  "ds_read_b64_tr_b16 v[126:127], v163 offset:47104\n\t" \
  "v_mfma_f32_16x16x32_bf16 v[132:135], v[100:103], v[48:51], v[132:135]\n\t" \
  "ds_read_b64_tr_b16 v[96:97], v160 offset:49152\n\t" \
  "v_mfma_f32_16x16x32_bf16 v[148:151], v[100:103], v[52:55], v[148:151]\n\t" \
  "ds_read_b64_tr_b16 v[98:99], v160 offset:51200\n\t" \
  "s_waitcnt lgkmcnt(10)\n\t" \
  "v_mfma_f32_16x16x32_bf16 v[136:139], v[104:107], v[48:51], v[136:139]\n\t" \
  "ds_read_b64_tr_b16 v[100:101], v161 offset:49152\n\t" \
  "v_mfma_f32_16x16x32_bf16 v[152:155], v[104:107], v[52:55], v[152:155]\n\t" \
  "ds_read_b64_tr_b16 v[102:103], v161 offset:51200\n\t" \
  "v_mfma_f32_16x16x32_bf16 v[140:143], v[108:111], v[48:51], v[140:143]\n\t" \
  "ds_read_b64_tr_b16 v[104:105], v162 offset:49152\n\t" \
  "v_mfma_f32_16x16x32_bf16 v[156:159], v[108:111], v[52:55], v[156:159]\n\t" \
  "ds_read_b64_tr_b16 v[106:107], v162 offset:51200\n\t" \
  "s_waitcnt lgkmcnt(10)\n\t" \
  "v_mfma_f32_16x16x32_bf16 v[128:131], v[112:115], v[56:59], v[128:131]\n\t" \
  "ds_read_b64_tr_b16 v[108:109], v163 offset:49152\n\t" \
  "v_mfma_f32_16x16x32_bf16 v[144:147], v[112:115], v[60:63], v[144:147]\n\t" \
  "ds_read_b64_tr_b16 v[110:111], v163 offset:51200\n\t" \
  "v_mfma_f32_16x16x32_bf16 v[132:135], v[116:119], v[56:59], v[132:135]\n\t" \
  "ds_read_b64_tr_b16 v[112:113], v160 offset:53248\n\t" \
  "v_mfma_f32_16x16x32_bf16 v[148:151], v[116:119], v[60:63], v[148:151]\n\t" \
  "ds_read_b64_tr_b16 v[114:115], v160 offset:55296\n\t" \
  "s_waitcnt lgkmcnt(10)\n\t" \
  "v_mfma_f32_16x16x32_bf16 v[136:139], v[120:123], v[56:59], v[136:139]\n\t" \
  "ds_read_b64_tr_b16 v[116:117], v161 offset:53248\n\t" \
  "v_mfma_f32_16x16x32_bf16 v[152:155], v[120:123], v[60:63], v[152:155]\n\t" \
  "ds_read_b64_tr_b16 v[118:119], v161 offset:55296\n\t" \
  "v_mfma_f32_16x16x32_bf16 v[140:143], v[124:127], v[56:59], v[140:143]\n\t" \
  "ds_read_b64_tr_b16 v[120:121], v162 offset:53248\n\t" \
  "v_mfma_f32_16x16x32_bf16 v[156:159], v[124:127], v[60:63], v[156:159]\n\t" \
  "ds_read_b64_tr_b16 v[122:123], v162 offset:55296\n\t" \
  "s_cbranch_vccz QF14_%=\n\t" \
  "s_waitcnt vmcnt(4)\n\t" \
  "QG14_%=:\n\t" \
  "s_waitcnt lgkmcnt(10)\n\t" \
  "v_mfma_f32_16x16x32_bf16 v[128:131], v[96:99], v[64:67], v[128:131]\n\t" \
  "ds_read_b64_tr_b16 v[124:125], v163 offset:53248\n\t" \
  "v_mfma_f32_16x16x32_bf16 v[144:147], v[96:99], v[68:71], v[144:147]\n\t" \
  "ds_read_b64_tr_b16 v[126:127], v163 offset:55296\n\t" \
  "v_mfma_f32_16x16x32_bf16 v[132:135], v[100:103], v[64:67], v[132:135]\n\t" \
  "ds_read_b64_tr_b16 v[96:97], v160 offset:57344\n\t" \
  "v_mfma_f32_16x16x32_bf16 v[148:151], v[100:103], v[68:71], v[148:151]\n\t" \
  "ds_read_b64_tr_b16 v[98:99], v160 offset:59392\n\t" \
  "s_waitcnt lgkmcnt(10)\n\t" \
  "v_mfma_f32_16x16x32_bf16 v[136:139], v[104:107], v[64:67], v[136:139]\n\t" \
  "ds_read_b64_tr_b16 v[100:101], v161 offset:57344\n\t" \
  "v_mfma_f32_16x16x32_bf16 v[152:155], v[104:107], v[68:71], v[152:155]\n\t" \
  "ds_read_b64_tr_b16 v[102:103], v161 offset:59392\n\t" \
  "v_mfma_f32_16x16x32_bf16 v[140:143], v[108:111], v[64:67], v[140:143]\n\t" \
  "ds_read_b64_tr_b16 v[104:105], v162 offset:57344\n\t" \
  "v_mfma_f32_16x16x32_bf16 v[156:159], v[108:111], v[68:71], v[156:159]\n\t" \
  "ds_read_b64_tr_b16 v[106:107], v162 offset:59392\n\t" \
  "s_waitcnt lgkmcnt(10)\n\t" \
  "v_mfma_f32_16x16x32_bf16 v[128:131], v[112:115], v[72:75], v[128:131]\n\t" \
  "ds_read_b64_tr_b16 v[108:109], v163 offset:57344\n\t" \
  "v_mfma_f32_16x16x32_bf16 v[144:147], v[112:115], v[76:79], v[144:147]\n\t" \
  "ds_read_b64_tr_b16 v[110:111], v163 offset:59392\n\t" \
  "v_mfma_f32_16x16x32_bf16 v[132:135], v[116:119], v[72:75], v[132:135]\n\t" \
  "ds_read_b64_tr_b16 v[112:113], v160 offset:61440\n\t" \
  "v_mfma_f32_16x16x32_bf16 v[148:151], v[116:119], v[76:79], v[148:151]\n\t" \
  "ds_read_b64_tr_b16 v[114:115], v160 offset:63488\n\t" \
  "s_waitcnt lgkmcnt(10)\n\t" \
  "v_mfma_f32_16x16x32_bf16 v[136:139], v[120:123], v[72:75], v[136:139]\n\t" \
  "ds_read_b64_tr_b16 v[116:117], v161 offset:61440\n\t" \
  "v_mfma_f32_16x16x32_bf16 v[152:155], v[120:123], v[76:79], v[152:155]\n\t" \
  "ds_read_b64_tr_b16 v[118:119], v161 offset:63488\n\t" \
  "v_mfma_f32_16x16x32_bf16 v[140:143], v[124:127], v[72:75], v[140:143]\n\t" \
  "ds_read_b64_tr_b16 v[120:121], v162 offset:61440\n\t" \
  "v_mfma_f32_16x16x32_bf16 v[156:159], v[124:127], v[76:79], v[156:159]\n\t" \
  "ds_read_b64_tr_b16 v[122:123], v162 offset:63488\n\t" \
  "s_cbranch_vccz QF15_%=\n\t" \
  "s_waitcnt vmcnt(0)\n\t" \
  "QG15_%=:\n\t" \
  "s_waitcnt lgkmcnt(10)\n\t" \
  "v_mfma_f32_16x16x32_bf16 v[128:131], v[96:99], v[0:3], v[128:131]\n\t" \
  "ds_read_b64_tr_b16 v[124:125], v163 offset:61440\n\t" \
  "v_mfma_f32_16x16x32_bf16 v[144:147], v[96:99], v[4:7], v[144:147]\n\t" \
  "ds_read_b64_tr_b16 v[126:127], v163 offset:63488\n\t" \
  "v_mfma_f32_16x16x32_bf16 v[132:135], v[100:103], v[0:3], v[132:135]\n\t" \
  "v_mfma_f32_16x16x32_bf16 v[148:151], v[100:103], v[4:7], v[148:151]\n\t" \
  "s_waitcnt lgkmcnt(8)\n\t" \
  "v_mfma_f32_16x16x32_bf16 v[136:139], v[104:107], v[0:3], v[136:139]\n\t" \
  "v_mfma_f32_16x16x32_bf16 v[152:155], v[104:107], v[4:7], v[152:155]\n\t" \
  "v_mfma_f32_16x16x32_bf16 v[140:143], v[108:111], v[0:3], v[140:143]\n\t" \
  "v_mfma_f32_16x16x32_bf16 v[156:159], v[108:111], v[4:7], v[156:159]\n\t" \
  "s_waitcnt lgkmcnt(4)\n\t" \
  "v_mfma_f32_16x16x32_bf16 v[128:131], v[112:115], v[8:11], v[128:131]\n\t" \
  "v_mfma_f32_16x16x32_bf16 v[144:147], v[112:115], v[12:15], v[144:147]\n\t" \
  "v_mfma_f32_16x16x32_bf16 v[132:135], v[116:119], v[8:11], v[132:135]\n\t" \
  "v_mfma_f32_16x16x32_bf16 v[148:151], v[116:119], v[12:15], v[148:151]\n\t" \
  "s_waitcnt lgkmcnt(0)\n\t" \
  "v_mfma_f32_16x16x32_bf16 v[136:139], v[120:123], v[8:11], v[136:139]\n\t" \
  "v_mfma_f32_16x16x32_bf16 v[152:155], v[120:123], v[12:15], v[152:155]\n\t" \
  "v_mfma_f32_16x16x32_bf16 v[140:143], v[124:127], v[8:11], v[140:143]\n\t" \
  "v_mfma_f32_16x16x32_bf16 v[156:159], v[124:127], v[12:15], v[156:159]\n\t" \
  "s_branch QX_%=\n\t" \
  "QF1_%=:\n\t" \
  "s_mov_b32 %[c], 1\n\t" \
  "s_getpc_b64 s[92:93]\n\t" \
  "s_branch QC_%=\n\t" \
  "s_branch QG1_%=\n\t" \
  "QF2_%=:\n\t" \
  "s_mov_b32 %[c], 2\n\t" \
  "s_getpc_b64 s[92:93]\n\t" \
  "s_branch QC_%=\n\t" \
  "s_branch QG2_%=\n\t" \
  "QF3_%=:\n\t" \
  "s_mov_b32 %[c], 3\n\t" \
  "s_getpc_b64 s[92:93]\n\t" \
  "s_branch QC_%=\n\t" \
  "s_branch QG3_%=\n\t" \
  "QF4_%=:\n\t" \
  "s_mov_b32 %[c], 4\n\t" \
  "s_getpc_b64 s[92:93]\n\t" \
  "s_branch QC_%=\n\t" \
  "s_branch QG4_%=\n\t" \
  "QF5_%=:\n\t" \
  "s_mov_b32 %[c], 5\n\t" \
  "s_getpc_b64 s[92:93]\n\t" \
  "s_branch QC_%=\n\t" \
  "s_branch QG5_%=\n\t" \
  "QF6_%=:\n\t" \
  "s_mov_b32 %[c], 6\n\t" \
  "s_getpc_b64 s[92:93]\n\t" \
  "s_branch QC_%=\n\t" \
  "s_branch QG6_%=\n\t" \
  "QF7_%=:\n\t" \
  "s_mov_b32 %[c], 7\n\t" \
  "s_getpc_b64 s[92:93]\n\t" \
  "s_branch QC_%=\n\t" \
  "s_branch QG7_%=\n\t" \
  "QF8_%=:\n\t" \
  "s_mov_b32 %[c], 8\n\t" \
  "s_getpc_b64 s[92:93]\n\t" \
  "s_branch QC_%=\n\t" \
  "s_branch QG8_%=\n\t" \
  "QF9_%=:\n\t" \
  "s_mov_b32 %[c], 9\n\t" \
  "s_getpc_b64 s[92:93]\n\t" \
  "s_branch QC_%=\n\t" \
  "s_branch QG9_%=\n\t" \
  "QF10_%=:\n\t" \
  "s_mov_b32 %[c], 10\n\t" \
  "s_getpc_b64 s[92:93]\n\t" \
  "s_branch QC_%=\n\t" \
  "s_branch QG10_%=\n\t" \
  "QF11_%=:\n\t" \
  "s_mov_b32 %[c], 11\n\t" \
  "s_getpc_b64 s[92:93]\n\t" \
  "s_branch QC_%=\n\t" \
  "s_branch QG11_%=\n\t" \
  "QF12_%=:\n\t" \
  "s_mov_b32 %[c], 12\n\t" \
  "s_getpc_b64 s[92:93]\n\t" \
  "s_branch QC_%=\n\t" \
  "s_branch QG12_%=\n\t" \
  "QF13_%=:\n\t" \
  "s_mov_b32 %[c], 13\n\t" \
  "s_getpc_b64 s[92:93]\n\t" \
  "s_branch QC_%=\n\t" \
  "s_branch QG13_%=\n\t" \
  "QF14_%=:\n\t" \
  "s_mov_b32 %[c], 14\n\t" \
  "s_getpc_b64 s[92:93]\n\t" \
  "s_branch QC_%=\n\t" \
  "s_branch QG14_%=\n\t" \
  "QF15_%=:\n\t" \
  "s_mov_b32 %[c], 15\n\t" \
  "s_getpc_b64 s[92:93]\n\t" \
  "s_branch QC_%=\n\t" \
  "s_branch QG15_%=\n\t" \
  "QC_%=:\n\t" \
  "s_add_u32 s92, s92, 4\n\t" \
  "s_addc_u32 s93, s93, 0\n\t" \
  "QU_%=:\n\t" \
  "ds_read_b32 v166, v165\n\t" \
  "s_max_i32 %[t1], %[c], 0\n\t" \
  "s_add_u32 %[t1], %[t1], 4\n\t" \
  "s_waitcnt lgkmcnt(0)\n\t" \
  "v_readfirstlane_b32 %[t0], v166\n\t" \
  "s_nop 0\n\t" \
  "s_min_u32 %[t1], %[t1], %[t0]\n\t" \
  "QL_%=:\n\t" \
  "s_cmp_ge_u32 %[iss], %[t1]\n\t" \
  "s_cbranch_scc1 QD_%=\n\t" \
  "s_mov_b32 %[t0], %[iss]\n\t" \
  "s_sub_u32 %[th], %[t0], 5\n\t" \
  "s_cselect_b32 %[t0], %[t0], %[th]\n\t" \
  "s_sub_u32 %[th], %[t0], 5\n\t" \
  "s_cselect_b32 %[t0], %[t0], %[th]\n\t" \
  "s_sub_u32 %[th], %[t0], 5\n\t" \
  "s_cselect_b32 %[t0], %[t0], %[th]\n\t" \
  "s_cmp_eq_u32 %[t0], 1\n\t" \
  "s_cbranch_scc1 QS1_%=\n\t" \
  "s_cmp_eq_u32 %[t0], 2\n\t" \
  "s_cbranch_scc1 QS2_%=\n\t" \
  "s_cmp_eq_u32 %[t0], 3\n\t" \
  "s_cbranch_scc1 QS3_%=\n\t" \
  "s_cmp_eq_u32 %[t0], 4\n\t" \
  "s_cbranch_scc1 QS4_%=\n\t" \
  "QS0_%=:\n\t" \
  "v_readlane_b32 s94, v167, %[iss]\n\t" \
  "s_nop 4\n\t" \
  "buffer_load_dwordx4 v[0:3], v164, %[ob], s94 offen offset:0 sc1\n\t" \
  "buffer_load_dwordx4 v[4:7], v164, %[ob], s94 offen offset:1024 sc1\n\t" \
  "buffer_load_dwordx4 v[8:11], v164, %[ob], s94 offen offset:2048 sc1\n\t" \
  "buffer_load_dwordx4 v[12:15], v164, %[ob], s94 offen offset:3072 sc1\n\t" \
  "s_add_u32 %[iss], %[iss], 1\n\t" \
  "s_cmp_ge_u32 %[iss], %[t1]\n\t" \
  "s_cbranch_scc1 QD_%=\n\t" \
  "QS1_%=:\n\t" \
  "v_readlane_b32 s94, v167, %[iss]\n\t" \
  "s_nop 4\n\t" \
  "buffer_load_dwordx4 v[16:19], v164, %[ob], s94 offen offset:0 sc1\n\t" \
  "buffer_load_dwordx4 v[20:23], v164, %[ob], s94 offen offset:1024 sc1\n\t" \
  "buffer_load_dwordx4 v[24:27], v164, %[ob], s94 offen offset:2048 sc1\n\t" \
  "buffer_load_dwordx4 v[28:31], v164, %[ob], s94 offen offset:3072 sc1\n\t" \
  "s_add_u32 %[iss], %[iss], 1\n\t" \
  "s_cmp_ge_u32 %[iss], %[t1]\n\t" \
  "s_cbranch_scc1 QD_%=\n\t" \
  "QS2_%=:\n\t" \
  "v_readlane_b32 s94, v167, %[iss]\n\t" \
  "s_nop 4\n\t" \
  "buffer_load_dwordx4 v[32:35], v164, %[ob], s94 offen offset:0 sc1\n\t" \
  "buffer_load_dwordx4 v[36:39], v164, %[ob], s94 offen offset:1024 sc1\n\t" \
  "buffer_load_dwordx4 v[40:43], v164, %[ob], s94 offen offset:2048 sc1\n\t" \
  "buffer_load_dwordx4 v[44:47], v164, %[ob], s94 offen offset:3072 sc1\n\t" \
  "s_add_u32 %[iss], %[iss], 1\n\t" \
  "s_cmp_ge_u32 %[iss], %[t1]\n\t" \
  "s_cbranch_scc1 QD_%=\n\t" \
  "QS3_%=:\n\t" \
  "v_readlane_b32 s94, v167, %[iss]\n\t" \
  "s_nop 4\n\t" \
  "buffer_load_dwordx4 v[48:51], v164, %[ob], s94 offen offset:0 sc1\n\t" \
  "buffer_load_dwordx4 v[52:55], v164, %[ob], s94 offen offset:1024 sc1\n\t" \
  "buffer_load_dwordx4 v[56:59], v164, %[ob], s94 offen offset:2048 sc1\n\t" \
  "buffer_load_dwordx4 v[60:63], v164, %[ob], s94 offen offset:3072 sc1\n\t" \
  "s_add_u32 %[iss], %[iss], 1\n\t" \
  "s_cmp_ge_u32 %[iss], %[t1]\n\t" \
  "s_cbranch_scc1 QD_%=\n\t" \
  "QS4_%=:\n\t" \
  "v_readlane_b32 s94, v167, %[iss]\n\t" \
  "s_nop 4\n\t" \
  "buffer_load_dwordx4 v[64:67], v164, %[ob], s94 offen offset:0 sc1\n\t" \
  "buffer_load_dwordx4 v[68:71], v164, %[ob], s94 offen offset:1024 sc1\n\t" \
  "buffer_load_dwordx4 v[72:75], v164, %[ob], s94 offen offset:2048 sc1\n\t" \
  "buffer_load_dwordx4 v[76:79], v164, %[ob], s94 offen offset:3072 sc1\n\t" \
  "s_add_u32 %[iss], %[iss], 1\n\t" \
  "s_cmp_ge_u32 %[iss], %[t1]\n\t" \
  "s_cbranch_scc0 QS0_%=\n\t" \
  "QD_%=:\n\t" \
  "s_cmp_lt_i32 %[c], 0\n\t" \
  "s_cbranch_scc1 QR_%=\n\t" \
  "s_cmp_gt_u32 %[iss], %[c]\n\t" \
  "s_cbranch_scc1 QW_%=\n\t" \
  "s_addk_i32 %[cn], 1\n\t" \
  "s_cmp_gt_u32 %[cn], 0x400000\n\t" \
  "s_cbranch_scc1 QT_%=\n\t" \
  "s_sleep 1\n\t" \
  "s_branch QU_%=\n\t" \
  "QT_%=:\n\t" \
  "s_add_u32 %[t1], %[c], 1\n\t" \
  "s_branch QL_%=\n\t" \
  "QW_%=:\n\t" \
  "s_cmp_eq_u32 %[iss], 16\n\t" \
  "s_cselect_b64 vcc, -1, 0\n\t" \
  "s_sub_u32 %[t0], %[iss], %[c]\n\t" \
  "s_cmp_eq_u32 %[t0], 4\n\t" \
  "s_cselect_b32 %[th], %[iss], 99\n\t" \
  "s_waitcnt vmcnt(12)\n\t" \
  "s_cmp_ge_u32 %[t0], 4\n\t" \
  "s_cbranch_scc1 QR_%=\n\t" \
  "s_waitcnt vmcnt(8)\n\t" \
  "s_cmp_ge_u32 %[t0], 3\n\t" \
  "s_cbranch_scc1 QR_%=\n\t" \
  "s_waitcnt vmcnt(4)\n\t" \
  "s_cmp_ge_u32 %[t0], 2\n\t" \
  "s_cbranch_scc1 QR_%=\n\t" \
  "s_waitcnt vmcnt(0)\n\t" \
  "QR_%=:\n\t" \
  "s_setpc_b64 s[92:93]\n\t" \
  "QX_%=:\n\t" \
  "s_nop 7\n\t"
#define G3_CLOB "v0", "v1", "v2", "v3", "v4", "v5", "v6", "v7", "v8", "v9", "v10", "v11", "v12", "v13", "v14", "v15", "v16", "v17", "v18", "v19", "v20", "v21", "v22", "v23", "v24", "v25", "v26", "v27", "v28", "v29", "v30", "v31", "v32", "v33", "v34", "v35", "v36", "v37", "v38", "v39", "v40", "v41", "v42", "v43", "v44", "v45", "v46", "v47", "v48", "v49", "v50", "v51", "v52", "v53", "v54", "v55", "v56", "v57", "v58", "v59", "v60", "v61", "v62", "v63", "v64", "v65", "v66", "v67", "v68", "v69", "v70", "v71", "v72", "v73", "v74", "v75", "v76", "v77", "v78", "v79", "v80", "v81", "v82", "v83", "v84", "v85", "v86", "v87", "v88", "v89", "v90", "v91", "v92", "v93", "v94", "v95", "v96", "v97", "v98", "v99", "v100", "v101", "v102", "v103", "v104", "v105", "v106", "v107", "v108", "v109", "v110", "v111", "v112", "v113", "v114", "v115", "v116", "v117", "v118", "v119", "v120", "v121", "v122", "v123", "v124", "v125", "v126", "v127", "v166", "s88", "s89", "s92", "s93", "s94"

#define PP_ASM \
  "v_mov_b32 v60, 0\n\t" \
  "s_mov_b32 %[t0], %[n0]\n\t" \
  "s_cmp_eq_u32 %[t0], 0\n\t" \
  "s_cbranch_scc1 PAx_%=\n\t" \
  "PA_%=:\n\t" \
  "s_barrier\n\t" \
  "s_sub_u32 %[t0], %[t0], 1\n\t" \
  "s_cmp_lg_u32 %[t0], 0\n\t" \
  "s_cbranch_scc1 PA_%=\n\t" \
  "PAx_%=:\n\t" \
  "s_mov_b32 m0, %[shd]\n\t" \
  "global_load_dword v160, v60, %[fl] sc1\n\t" \
  "global_load_lds_dwordx4 v163, %[shp]\n\t" \
  "s_mov_b32 m0, %[tgd]\n\t" \
  "s_mov_b64 exec, %[em]\n\t" \
  "global_load_lds_dword v60, %[fl] sc1\n\t" \
  "s_mov_b64 exec, -1\n\t" \
  "global_load_ushort v161, v60, %[hp]\n\t" \
  "global_load_ushort v162, v60, %[dp]\n\t" \
  "global_load_dwordx4 v[0:3], v152, %[kb0]\n\t" \
  "global_load_dwordx4 v[4:7], v153, %[kb0]\n\t" \
  "global_load_dwordx4 v[8:11], v154, %[kb0]\n\t" \
  "global_load_dwordx4 v[12:15], v155, %[kb3] offset:0\n\t" \
  "global_load_dwordx4 v[28:31], v155, %[vb0] offset:0\n\t" \
  "s_setprio 2\n\t" \
  "s_barrier\n\t" \
  "s_barrier\n\t" \
  "s_waitcnt vmcnt(5)\n\t" \
  "ds_read_b128 v[128:131], v156 offset:0\n\t" \
  "ds_read_b128 v[132:135], v156 offset:16\n\t" \
  "ds_read_b128 v[136:139], v156 offset:32\n\t" \
  "ds_read_b128 v[140:143], v156 offset:48\n\t" \
  "s_waitcnt lgkmcnt(0)\n\t" \
  "v_lshlrev_b32 v64, 16, v128\n\t" \
  "v_and_b32 v65, 0xffff0000, v128\n\t" \
  "v_lshlrev_b32 v66, 16, v129\n\t" \
  "v_and_b32 v67, 0xffff0000, v129\n\t" \
  "v_lshlrev_b32 v68, 16, v130\n\t" \
  "v_and_b32 v69, 0xffff0000, v130\n\t" \
  "v_lshlrev_b32 v70, 16, v131\n\t" \
  "v_and_b32 v71, 0xffff0000, v131\n\t" \
  "v_lshlrev_b32 v72, 16, v132\n\t" \
  "v_and_b32 v73, 0xffff0000, v132\n\t" \
  "v_lshlrev_b32 v74, 16, v133\n\t" \
  "v_and_b32 v75, 0xffff0000, v133\n\t" \
  "v_lshlrev_b32 v76, 16, v134\n\t" \
  "v_and_b32 v77, 0xffff0000, v134\n\t" \
  "v_lshlrev_b32 v78, 16, v135\n\t" \
  "v_and_b32 v79, 0xffff0000, v135\n\t" \
  "v_lshlrev_b32 v80, 16, v136\n\t" \
  "v_and_b32 v81, 0xffff0000, v136\n\t" \
  "v_lshlrev_b32 v82, 16, v137\n\t" \
  "v_and_b32 v83, 0xffff0000, v137\n\t" \
  "v_lshlrev_b32 v84, 16, v138\n\t" \
  "v_and_b32 v85, 0xffff0000, v138\n\t" \
  "v_lshlrev_b32 v86, 16, v139\n\t" \
  "v_and_b32 v87, 0xffff0000, v139\n\t" \
  "v_lshlrev_b32 v88, 16, v140\n\t" \
  "v_and_b32 v89, 0xffff0000, v140\n\t" \
  "v_lshlrev_b32 v90, 16, v141\n\t" \
  "v_and_b32 v91, 0xffff0000, v141\n\t" \
  "v_lshlrev_b32 v92, 16, v142\n\t" \
  "v_and_b32 v93, 0xffff0000, v142\n\t" \
  "v_lshlrev_b32 v94, 16, v143\n\t" \
  "v_and_b32 v95, 0xffff0000, v143\n\t" \
  "s_waitcnt vmcnt(4)\n\t" \
  "s_memrealtime %[st1]\n\t" \
  "s_mov_b64 exec, %[zm]\n\t" \
  "v_mov_b32 v0, 0\n\t" \
  "v_mov_b32 v1, 0\n\t" \
  "v_mov_b32 v2, 0\n\t" \
  "v_mov_b32 v3, 0\n\t" \
  "s_mov_b64 exec, -1\n\t" \
  "v_lshlrev_b32 v128, 16, v0\n\t" \
  "v_and_b32 v129, 0xffff0000, v0\n\t" \
  "s_setprio 3\n\t" \
  "s_barrier\n\t" \
  "global_load_dwordx4 v[16:19], v155, %[kb3] offset:2048\n\t" \
  "global_load_dwordx4 v[32:35], v155, %[vb0] offset:2048\n\t" \
  "global_load_dwordx4 v[20:23], v155, %[kb5] offset:0\n\t" \
  "s_setprio 2\n\t" \
  "v_lshlrev_b32 v130, 16, v1\n\t" \
  "v_and_b32 v131, 0xffff0000, v1\n\t" \
  "v_lshlrev_b32 v132, 16, v2\n\t" \
  "v_and_b32 v133, 0xffff0000, v2\n\t" \
  "v_lshlrev_b32 v134, 16, v3\n\t" \
  "v_and_b32 v135, 0xffff0000, v3\n\t" \
  "v_mul_f32 v96, v128, v67\n\t" \
  "v_mul_f32 v97, v129, v71\n\t" \
  "v_mul_f32 v98, v130, v75\n\t" \
  "v_mul_f32 v99, v131, v79\n\t" \
  "v_mul_f32 v100, v132, v83\n\t" \
  "v_mul_f32 v101, v133, v87\n\t" \
  "v_mul_f32 v102, v134, v91\n\t" \
  "v_mul_f32 v103, v135, v95\n\t" \
  "s_waitcnt vmcnt(6)\n\t" \
  "s_mov_b64 exec, %[zm]\n\t" \
  "v_mov_b32 v4, 0\n\t" \
  "v_mov_b32 v5, 0\n\t" \
  "v_mov_b32 v6, 0\n\t" \
  "v_mov_b32 v7, 0\n\t" \
  "s_mov_b64 exec, -1\n\t" \
  "v_lshlrev_b32 v128, 16, v4\n\t" \
  "v_and_b32 v129, 0xffff0000, v4\n\t" \
  "v_lshlrev_b32 v130, 16, v5\n\t" \
  "v_and_b32 v131, 0xffff0000, v5\n\t" \
  "v_lshlrev_b32 v132, 16, v6\n\t" \
  "v_and_b32 v133, 0xffff0000, v6\n\t" \
  "v_lshlrev_b32 v134, 16, v7\n\t" \
  "v_and_b32 v135, 0xffff0000, v7\n\t" \
  "v_fmac_f32 v96, v128, v66\n\t" \
  "v_fmac_f32 v97, v129, v70\n\t" \
  "v_fmac_f32 v98, v130, v74\n\t" \
  "v_fmac_f32 v99, v131, v78\n\t" \
  "v_fmac_f32 v100, v132, v82\n\t" \
  "v_fmac_f32 v101, v133, v86\n\t" \
  "v_fmac_f32 v102, v134, v90\n\t" \
  "v_fmac_f32 v103, v135, v94\n\t" \
  "v_mul_f32 v104, v128, v67\n\t" \
  "v_mul_f32 v105, v129, v71\n\t" \
  "v_mul_f32 v106, v130, v75\n\t" \
  "v_mul_f32 v107, v131, v79\n\t" \
  "v_mul_f32 v108, v132, v83\n\t" \
  "v_mul_f32 v109, v133, v87\n\t" \
  "v_mul_f32 v110, v134, v91\n\t" \
  "v_mul_f32 v111, v135, v95\n\t" \
  "s_waitcnt vmcnt(5)\n\t" \
  "s_mov_b64 exec, %[zm]\n\t" \
  "v_mov_b32 v8, 0\n\t" \
  "v_mov_b32 v9, 0\n\t" \
  "v_mov_b32 v10, 0\n\t" \
  "v_mov_b32 v11, 0\n\t" \
  "s_mov_b64 exec, -1\n\t" \
  "s_barrier\n\t" \
  "v_lshlrev_b32 v128, 16, v8\n\t" \
  "v_and_b32 v129, 0xffff0000, v8\n\t" \
  "v_lshlrev_b32 v130, 16, v9\n\t" \
  "v_and_b32 v131, 0xffff0000, v9\n\t" \
  "v_lshlrev_b32 v132, 16, v10\n\t" \
  "v_and_b32 v133, 0xffff0000, v10\n\t" \
  "v_lshlrev_b32 v134, 16, v11\n\t" \
  "v_and_b32 v135, 0xffff0000, v11\n\t" \
  "v_fmac_f32 v96, v128, v65\n\t" \
  "v_fmac_f32 v97, v129, v69\n\t" \
  "v_fmac_f32 v98, v130, v73\n\t" \
  "v_fmac_f32 v99, v131, v77\n\t" \
  "v_fmac_f32 v100, v132, v81\n\t" \
  "v_fmac_f32 v101, v133, v85\n\t" \
  "v_fmac_f32 v102, v134, v89\n\t" \
  "v_fmac_f32 v103, v135, v93\n\t" \
  "v_fmac_f32 v104, v128, v66\n\t" \
  "v_fmac_f32 v105, v129, v70\n\t" \
  "v_fmac_f32 v106, v130, v74\n\t" \
  "v_fmac_f32 v107, v131, v78\n\t" \
  "v_fmac_f32 v108, v132, v82\n\t" \
  "v_fmac_f32 v109, v133, v86\n\t" \
  "v_fmac_f32 v110, v134, v90\n\t" \
  "v_fmac_f32 v111, v135, v94\n\t" \
  "v_mul_f32 v112, v128, v67\n\t" \
  "v_mul_f32 v113, v129, v71\n\t" \
  "v_mul_f32 v114, v130, v75\n\t" \
  "v_mul_f32 v115, v131, v79\n\t" \
  "v_mul_f32 v116, v132, v83\n\t" \
  "v_mul_f32 v117, v133, v87\n\t" \
  "v_mul_f32 v118, v134, v91\n\t" \
  "v_mul_f32 v119, v135, v95\n\t" \
  "s_waitcnt vmcnt(4)\n\t" \
  "v_lshlrev_b32 v128, 16, v12\n\t" \
  "v_and_b32 v129, 0xffff0000, v12\n\t" \
  "v_lshlrev_b32 v130, 16, v13\n\t" \
  "v_and_b32 v131, 0xffff0000, v13\n\t" \
  "v_lshlrev_b32 v132, 16, v14\n\t" \
  "v_and_b32 v133, 0xffff0000, v14\n\t" \
  "v_lshlrev_b32 v134, 16, v15\n\t" \
  "v_and_b32 v135, 0xffff0000, v15\n\t" \
  "s_setprio 3\n\t" \
  "s_barrier\n\t" \
  "global_load_dwordx4 v[36:39], v155, %[vb2] offset:0\n\t" \
  "global_load_dwordx4 v[24:27], v155, %[kb5] offset:2048\n\t" \
  "global_load_dwordx4 v[40:43], v155, %[vb2] offset:2048\n\t" \
  "s_setprio 2\n\t" \
  "v_fmac_f32 v96, v128, v64\n\t" \
  "v_fmac_f32 v97, v129, v68\n\t" \
  "v_fmac_f32 v98, v130, v72\n\t" \
  "v_fmac_f32 v99, v131, v76\n\t" \
  "v_fmac_f32 v100, v132, v80\n\t" \
  "v_fmac_f32 v101, v133, v84\n\t" \
  "v_fmac_f32 v102, v134, v88\n\t" \
  "v_fmac_f32 v103, v135, v92\n\t" \
  "v_fmac_f32 v104, v128, v65\n\t" \
  "v_fmac_f32 v105, v129, v69\n\t" \
  "v_fmac_f32 v106, v130, v73\n\t" \
  "v_fmac_f32 v107, v131, v77\n\t" \
  "v_fmac_f32 v108, v132, v81\n\t" \
  "v_fmac_f32 v109, v133, v85\n\t" \
  "v_fmac_f32 v110, v134, v89\n\t" \
  "v_fmac_f32 v111, v135, v93\n\t" \
  "v_fmac_f32 v112, v128, v66\n\t" \
  "v_fmac_f32 v113, v129, v70\n\t" \
  "v_fmac_f32 v114, v130, v74\n\t" \
  "v_fmac_f32 v115, v131, v78\n\t" \
  "v_fmac_f32 v116, v132, v82\n\t" \
  "v_fmac_f32 v117, v133, v86\n\t" \
  "v_fmac_f32 v118, v134, v90\n\t" \
  "v_fmac_f32 v119, v135, v94\n\t" \
  "v_mul_f32 v120, v128, v67\n\t" \
  "v_mul_f32 v121, v129, v71\n\t" \
  "v_mul_f32 v122, v130, v75\n\t" \
  "v_mul_f32 v123, v131, v79\n\t" \
  "v_mul_f32 v124, v132, v83\n\t" \
  "v_mul_f32 v125, v133, v87\n\t" \
  "v_mul_f32 v126, v134, v91\n\t" \
  "v_mul_f32 v127, v135, v95\n\t" \
  "s_waitcnt vmcnt(6)\n\t" \
  "v_lshlrev_b32 v128, 16, v28\n\t" \
  "v_and_b32 v129, 0xffff0000, v28\n\t" \
  "v_lshlrev_b32 v130, 16, v29\n\t" \
  "v_and_b32 v131, 0xffff0000, v29\n\t" \
  "v_lshlrev_b32 v132, 16, v30\n\t" \
  "v_and_b32 v133, 0xffff0000, v30\n\t" \
  "v_lshlrev_b32 v134, 16, v31\n\t" \
  "v_and_b32 v135, 0xffff0000, v31\n\t" \
  "s_barrier\n\t" \
  "v_mul_f32 v96, v96, v128\n\t" \
  "v_mul_f32 v97, v97, v129\n\t" \
  "v_mul_f32 v98, v98, v130\n\t" \
  "v_mul_f32 v99, v99, v131\n\t" \
  "v_mul_f32 v100, v100, v132\n\t" \
  "v_mul_f32 v101, v101, v133\n\t" \
  "v_mul_f32 v102, v102, v134\n\t" \
  "v_mul_f32 v103, v103, v135\n\t" \
  "v_cvt_pk_bf16_f32 v136, v96, v97\n\t" \
  "v_cvt_pk_bf16_f32 v137, v98, v99\n\t" \
  "v_cvt_pk_bf16_f32 v138, v100, v101\n\t" \
  "v_cvt_pk_bf16_f32 v139, v102, v103\n\t" \
  "v_mov_b32_dpp v128, v136 row_ror:8 row_mask:0xf bank_mask:0xf\n\t" \
  "v_mov_b32_dpp v129, v137 row_ror:8 row_mask:0xf bank_mask:0xf\n\t" \
  "v_mov_b32_dpp v130, v138 row_ror:8 row_mask:0xf bank_mask:0xf\n\t" \
  "v_mov_b32_dpp v131, v139 row_ror:8 row_mask:0xf bank_mask:0xf\n\t" \
  "v_perm_b32 v136, v128, v136, v157\n\t" \
  "v_perm_b32 v137, v129, v137, v157\n\t" \
  "v_perm_b32 v138, v130, v138, v157\n\t" \
  "v_perm_b32 v139, v131, v139, v157\n\t" \
  "v_permlane16_swap_b32 v136, v137\n\t" \
  "s_nop 0\n\t" \
  "v_permlane16_swap_b32 v138, v139\n\t" \
  "s_nop 1\n\t" \
  "v_permlane32_swap_b32 v136, v138\n\t" \
  "v_permlane32_swap_b32 v137, v139\n\t" \
  "s_waitcnt vmcnt(5)\n\t" \
  "v_lshlrev_b32 v128, 16, v16\n\t" \
  "v_and_b32 v129, 0xffff0000, v16\n\t" \
  "v_lshlrev_b32 v130, 16, v17\n\t" \
  "v_and_b32 v131, 0xffff0000, v17\n\t" \
  "v_lshlrev_b32 v132, 16, v18\n\t" \
  "v_and_b32 v133, 0xffff0000, v18\n\t" \
  "v_lshlrev_b32 v134, 16, v19\n\t" \
  "v_and_b32 v135, 0xffff0000, v19\n\t" \
  "v_fmac_f32 v104, v128, v64\n\t" \
  "v_fmac_f32 v105, v129, v68\n\t" \
  "v_fmac_f32 v106, v130, v72\n\t" \
  "v_fmac_f32 v107, v131, v76\n\t" \
  "v_fmac_f32 v108, v132, v80\n\t" \
  "v_fmac_f32 v109, v133, v84\n\t" \
  "s_setprio 3\n\t" \
  "s_barrier\n\t" \
  "global_store_dwordx4 v159, v[136:139], %[sb] sc1\n\t" \
  "v_add_u32 v158, 0x80, v159\n\t" \
  "v_bfi_b32 v159, %[mk], v158, v159\n\t" \
  "s_setprio 2\n\t" \
  "v_fmac_f32 v110, v134, v88\n\t" \
  "v_fmac_f32 v111, v135, v92\n\t" \
  "v_fmac_f32 v112, v128, v65\n\t" \
  "v_fmac_f32 v113, v129, v69\n\t" \
  "v_fmac_f32 v114, v130, v73\n\t" \
  "v_fmac_f32 v115, v131, v77\n\t" \
  "v_fmac_f32 v116, v132, v81\n\t" \
  "v_fmac_f32 v117, v133, v85\n\t" \
  "v_fmac_f32 v118, v134, v89\n\t" \
  "v_fmac_f32 v119, v135, v93\n\t" \
  "v_fmac_f32 v120, v128, v66\n\t" \
  "v_fmac_f32 v121, v129, v70\n\t" \
  "v_fmac_f32 v122, v130, v74\n\t" \
  "v_fmac_f32 v123, v131, v78\n\t" \
  "v_fmac_f32 v124, v132, v82\n\t" \
  "v_fmac_f32 v125, v133, v86\n\t" \
  "v_fmac_f32 v126, v134, v90\n\t" \
  "v_fmac_f32 v127, v135, v94\n\t" \
  "s_waitcnt vmcnt(5)\n\t" \
  "v_lshlrev_b32 v128, 16, v32\n\t" \
  "v_and_b32 v129, 0xffff0000, v32\n\t" \
  "v_lshlrev_b32 v130, 16, v33\n\t" \
  "v_and_b32 v131, 0xffff0000, v33\n\t" \
  "v_lshlrev_b32 v132, 16, v34\n\t" \
  "v_and_b32 v133, 0xffff0000, v34\n\t" \
  "v_lshlrev_b32 v134, 16, v35\n\t" \
  "v_and_b32 v135, 0xffff0000, v35\n\t" \
  "v_mul_f32 v104, v104, v128\n\t" \
  "v_mul_f32 v105, v105, v129\n\t" \
  "v_mul_f32 v106, v106, v130\n\t" \
  "v_mul_f32 v107, v107, v131\n\t" \
  "v_mul_f32 v108, v108, v132\n\t" \
  "v_mul_f32 v109, v109, v133\n\t" \
  "v_mul_f32 v110, v110, v134\n\t" \
  "v_mul_f32 v111, v111, v135\n\t" \
  "v_cvt_pk_bf16_f32 v140, v104, v105\n\t" \
  "v_cvt_pk_bf16_f32 v141, v106, v107\n\t" \
  "v_cvt_pk_bf16_f32 v142, v108, v109\n\t" \
  "v_cvt_pk_bf16_f32 v143, v110, v111\n\t" \
  "v_mov_b32_dpp v128, v140 row_ror:8 row_mask:0xf bank_mask:0xf\n\t" \
  "v_mov_b32_dpp v129, v141 row_ror:8 row_mask:0xf bank_mask:0xf\n\t" \
  "s_barrier\n\t" \
  "v_mov_b32_dpp v130, v142 row_ror:8 row_mask:0xf bank_mask:0xf\n\t" \
  "v_mov_b32_dpp v131, v143 row_ror:8 row_mask:0xf bank_mask:0xf\n\t" \
  "v_perm_b32 v140, v128, v140, v157\n\t" \
  "v_perm_b32 v141, v129, v141, v157\n\t" \
  "v_perm_b32 v142, v130, v142, v157\n\t" \
  "v_perm_b32 v143, v131, v143, v157\n\t" \
  "v_permlane16_swap_b32 v140, v141\n\t" \
  "s_nop 0\n\t" \
  "v_permlane16_swap_b32 v142, v143\n\t" \
  "s_nop 1\n\t" \
  "v_permlane32_swap_b32 v140, v142\n\t" \
  "v_permlane32_swap_b32 v141, v143\n\t" \
  "s_waitcnt vmcnt(4)\n\t" \
  "v_lshlrev_b32 v128, 16, v20\n\t" \
  "v_and_b32 v129, 0xffff0000, v20\n\t" \
  "v_lshlrev_b32 v130, 16, v21\n\t" \
  "v_and_b32 v131, 0xffff0000, v21\n\t" \
  "v_lshlrev_b32 v132, 16, v22\n\t" \
  "v_and_b32 v133, 0xffff0000, v22\n\t" \
  "v_lshlrev_b32 v134, 16, v23\n\t" \
  "v_and_b32 v135, 0xffff0000, v23\n\t" \
  "v_fmac_f32 v112, v128, v64\n\t" \
  "v_fmac_f32 v113, v129, v68\n\t" \
  "v_fmac_f32 v114, v130, v72\n\t" \
  "v_fmac_f32 v115, v131, v76\n\t" \
  "v_fmac_f32 v116, v132, v80\n\t" \
  "v_fmac_f32 v117, v133, v84\n\t" \
  "v_fmac_f32 v118, v134, v88\n\t" \
  "v_fmac_f32 v119, v135, v92\n\t" \
  "v_fmac_f32 v120, v128, v65\n\t" \
  "v_fmac_f32 v121, v129, v69\n\t" \
  "v_fmac_f32 v122, v130, v73\n\t" \
  "v_fmac_f32 v123, v131, v77\n\t" \
  "v_fmac_f32 v124, v132, v81\n\t" \
  "v_fmac_f32 v125, v133, v85\n\t" \
  "v_fmac_f32 v126, v134, v89\n\t" \
  "v_fmac_f32 v127, v135, v93\n\t" \
  "s_waitcnt vmcnt(3)\n\t" \
  "v_lshlrev_b32 v128, 16, v36\n\t" \
  "v_and_b32 v129, 0xffff0000, v36\n\t" \
  "v_lshlrev_b32 v130, 16, v37\n\t" \
  "v_and_b32 v131, 0xffff0000, v37\n\t" \
  "s_setprio 3\n\t" \
  "s_barrier\n\t" \
  "global_store_dwordx4 v159, v[140:143], %[sb] sc1\n\t" \
  "v_add_u32 v158, 0x80, v159\n\t" \
  "v_bfi_b32 v159, %[mk], v158, v159\n\t" \
  "s_mov_b32 m0, %[hd]\n\t" \
  "s_nop 0\n\t" \
  "global_load_lds_dwordx4 v163, %[hp] offset:0\n\t" \
  "global_load_lds_dwordx4 v163, %[hp] offset:1024\n\t" \
  "global_load_lds_dwordx4 v163, %[hp] offset:2048\n\t" \
  "global_load_lds_dwordx4 v163, %[hp] offset:3072\n\t" \
  "s_setprio 2\n\t" \
  "v_lshlrev_b32 v132, 16, v38\n\t" \
  "v_and_b32 v133, 0xffff0000, v38\n\t" \
  "v_lshlrev_b32 v134, 16, v39\n\t" \
  "v_and_b32 v135, 0xffff0000, v39\n\t" \
  "v_mul_f32 v112, v112, v128\n\t" \
  "v_mul_f32 v113, v113, v129\n\t" \
  "v_mul_f32 v114, v114, v130\n\t" \
  "v_mul_f32 v115, v115, v131\n\t" \
  "v_mul_f32 v116, v116, v132\n\t" \
  "v_mul_f32 v117, v117, v133\n\t" \
  "v_mul_f32 v118, v118, v134\n\t" \
  "v_mul_f32 v119, v119, v135\n\t" \
  "v_cvt_pk_bf16_f32 v144, v112, v113\n\t" \
  "v_cvt_pk_bf16_f32 v145, v114, v115\n\t" \
  "v_cvt_pk_bf16_f32 v146, v116, v117\n\t" \
  "v_cvt_pk_bf16_f32 v147, v118, v119\n\t" \
  "v_mov_b32_dpp v128, v144 row_ror:8 row_mask:0xf bank_mask:0xf\n\t" \
  "v_mov_b32_dpp v129, v145 row_ror:8 row_mask:0xf bank_mask:0xf\n\t" \
  "v_mov_b32_dpp v130, v146 row_ror:8 row_mask:0xf bank_mask:0xf\n\t" \
  "v_mov_b32_dpp v131, v147 row_ror:8 row_mask:0xf bank_mask:0xf\n\t" \
  "v_perm_b32 v144, v128, v144, v157\n\t" \
  "v_perm_b32 v145, v129, v145, v157\n\t" \
  "v_perm_b32 v146, v130, v146, v157\n\t" \
  "v_perm_b32 v147, v131, v147, v157\n\t" \
  "v_permlane16_swap_b32 v144, v145\n\t" \
  "s_nop 0\n\t" \
  "v_permlane16_swap_b32 v146, v147\n\t" \
  "s_nop 1\n\t" \
  "v_permlane32_swap_b32 v144, v146\n\t" \
  "v_permlane32_swap_b32 v145, v147\n\t" \
  "s_waitcnt vmcnt(7)\n\t" \
  "v_lshlrev_b32 v128, 16, v24\n\t" \
  "v_and_b32 v129, 0xffff0000, v24\n\t" \
  "v_lshlrev_b32 v130, 16, v25\n\t" \
  "v_and_b32 v131, 0xffff0000, v25\n\t" \
  "v_lshlrev_b32 v132, 16, v26\n\t" \
  "v_and_b32 v133, 0xffff0000, v26\n\t" \
  "v_lshlrev_b32 v134, 16, v27\n\t" \
  "v_and_b32 v135, 0xffff0000, v27\n\t" \
  "v_fmac_f32 v120, v128, v64\n\t" \
  "v_fmac_f32 v121, v129, v68\n\t" \
  "s_setprio 3\n\t" \
  "s_barrier\n\t" \
  "global_store_dwordx4 v159, v[144:147], %[sb] sc1\n\t" \
  "v_add_u32 v158, 0x80, v159\n\t" \
  "v_bfi_b32 v159, %[mk], v158, v159\n\t" \
  "s_setprio 2\n\t" \
  "v_fmac_f32 v122, v130, v72\n\t" \
  "v_fmac_f32 v123, v131, v76\n\t" \
  "v_fmac_f32 v124, v132, v80\n\t" \
  "v_fmac_f32 v125, v133, v84\n\t" \
  "v_fmac_f32 v126, v134, v88\n\t" \
  "v_fmac_f32 v127, v135, v92\n\t" \
  "s_waitcnt vmcnt(7)\n\t" \
  "v_lshlrev_b32 v128, 16, v40\n\t" \
  "v_and_b32 v129, 0xffff0000, v40\n\t" \
  "v_lshlrev_b32 v130, 16, v41\n\t" \
  "v_and_b32 v131, 0xffff0000, v41\n\t" \
  "v_lshlrev_b32 v132, 16, v42\n\t" \
  "v_and_b32 v133, 0xffff0000, v42\n\t" \
  "v_lshlrev_b32 v134, 16, v43\n\t" \
  "v_and_b32 v135, 0xffff0000, v43\n\t" \
  "v_mul_f32 v120, v120, v128\n\t" \
  "v_mul_f32 v121, v121, v129\n\t" \
  "v_mul_f32 v122, v122, v130\n\t" \
  "v_mul_f32 v123, v123, v131\n\t" \
  "v_mul_f32 v124, v124, v132\n\t" \
  "v_mul_f32 v125, v125, v133\n\t" \
  "v_mul_f32 v126, v126, v134\n\t" \
  "v_mul_f32 v127, v127, v135\n\t" \
  "v_cvt_pk_bf16_f32 v148, v120, v121\n\t" \
  "v_cvt_pk_bf16_f32 v149, v122, v123\n\t" \
  "v_cvt_pk_bf16_f32 v150, v124, v125\n\t" \
  "v_cvt_pk_bf16_f32 v151, v126, v127\n\t" \
  "v_mov_b32_dpp v128, v148 row_ror:8 row_mask:0xf bank_mask:0xf\n\t" \
  "v_mov_b32_dpp v129, v149 row_ror:8 row_mask:0xf bank_mask:0xf\n\t" \
  "v_mov_b32_dpp v130, v150 row_ror:8 row_mask:0xf bank_mask:0xf\n\t" \
  "v_mov_b32_dpp v131, v151 row_ror:8 row_mask:0xf bank_mask:0xf\n\t" \
  "v_perm_b32 v148, v128, v148, v157\n\t" \
  "v_perm_b32 v149, v129, v149, v157\n\t" \
  "v_perm_b32 v150, v130, v150, v157\n\t" \
  "v_perm_b32 v151, v131, v151, v157\n\t" \
  "v_permlane16_swap_b32 v148, v149\n\t" \
  "s_nop 0\n\t" \
  "v_permlane16_swap_b32 v150, v151\n\t" \
  "s_nop 1\n\t" \
  "v_permlane32_swap_b32 v148, v150\n\t" \
  "v_permlane32_swap_b32 v149, v151\n\t" \
  "s_setprio 3\n\t" \
  "s_barrier\n\t" \
  "global_store_dwordx4 v159, v[148:151], %[sb] sc1\n\t" \
  "s_setprio 2\n\t" \
  "s_waitcnt vmcnt(2)\n\t" \
  "ds_read_b64_tr_b16 v[64:65], v165 offset:32\n\t" \
  "ds_read_b64_tr_b16 v[66:67], v165 offset:0\n\t" \
  "ds_read_b64_tr_b16 v[68:69], v165 offset:288\n\t" \
  "ds_read_b64_tr_b16 v[70:71], v165 offset:256\n\t" \
  "ds_read_b64_tr_b16 v[72:73], v165 offset:544\n\t" \
  "ds_read_b64_tr_b16 v[74:75], v165 offset:512\n\t" \
  "ds_read_b64_tr_b16 v[76:77], v165 offset:800\n\t" \
  "ds_read_b64_tr_b16 v[78:79], v165 offset:768\n\t" \
  "v_mov_b32 v24, v166\n\t" \
  "v_mov_b32 v25, v167\n\t" \
  "v_mov_b32 v63, 0xc0\n\t" \
  "v_add_u32 v158, 64, v24\n\t" \
  "v_bfi_b32 v26, v63, v158, v24\n\t" \
  "v_add_u32 v158, 64, v25\n\t" \
  "v_bfi_b32 v27, v63, v158, v25\n\t" \
  "v_add_u32 v158, 64, v26\n\t" \
  "v_bfi_b32 v28, v63, v158, v26\n\t" \
  "v_add_u32 v158, 64, v27\n\t" \
  "v_bfi_b32 v29, v63, v158, v27\n\t" \
  "v_add_u32 v158, 64, v28\n\t" \
  "v_bfi_b32 v30, v63, v158, v28\n\t" \
  "v_add_u32 v158, 64, v29\n\t" \
  "v_bfi_b32 v31, v63, v158, v29\n\t" \
  "s_barrier\n\t" \
  "ds_read_b64_tr_b16 v[80:81], v165 offset:1056\n\t" \
  "ds_read_b64_tr_b16 v[82:83], v165 offset:1024\n\t" \
  "ds_read_b64_tr_b16 v[84:85], v165 offset:1312\n\t" \
  "ds_read_b64_tr_b16 v[86:87], v165 offset:1280\n\t" \
  "ds_read_b64_tr_b16 v[88:89], v165 offset:1568\n\t" \
  "ds_read_b64_tr_b16 v[90:91], v165 offset:1536\n\t" \
  "ds_read_b64_tr_b16 v[92:93], v165 offset:1824\n\t" \
  "ds_read_b64_tr_b16 v[94:95], v165 offset:1792\n\t" \
  "s_setprio 3\n\t" \
  "s_barrier\n\t" \
  "s_waitcnt vmcnt(0)\n\t" \
  "v_add_u32 v61, 1, v160\n\t" \
  "s_mov_b64 exec, 1\n\t" \
  "global_store_dword v60, v61, %[ff] sc1\n\t" \
  "s_mov_b64 exec, -1\n\t" \
  "s_memrealtime %[st2]\n\t" \
  "s_setprio 2\n\t" \
  "ds_read_b64_tr_b16 v[96:97], v165 offset:2080\n\t" \
  "ds_read_b64_tr_b16 v[98:99], v165 offset:2048\n\t" \
  "ds_read_b64_tr_b16 v[100:101], v165 offset:2336\n\t" \
  "ds_read_b64_tr_b16 v[102:103], v165 offset:2304\n\t" \
  "ds_read_b64_tr_b16 v[104:105], v165 offset:2592\n\t" \
  "ds_read_b64_tr_b16 v[106:107], v165 offset:2560\n\t" \
  "ds_read_b64_tr_b16 v[108:109], v165 offset:2848\n\t" \
  "ds_read_b64_tr_b16 v[110:111], v165 offset:2816\n\t" \
  "s_barrier\n\t" \
  "ds_read_b64_tr_b16 v[112:113], v165 offset:3104\n\t" \
  "ds_read_b64_tr_b16 v[114:115], v165 offset:3072\n\t" \
  "ds_read_b64_tr_b16 v[116:117], v165 offset:3360\n\t" \
  "ds_read_b64_tr_b16 v[118:119], v165 offset:3328\n\t" \
  "ds_read_b64_tr_b16 v[120:121], v165 offset:3616\n\t" \
  "ds_read_b64_tr_b16 v[122:123], v165 offset:3584\n\t" \
  "ds_read_b64_tr_b16 v[124:125], v165 offset:3872\n\t" \
  "ds_read_b64_tr_b16 v[126:127], v165 offset:3840\n\t" \
  "s_barrier\n\t" \
  "s_waitcnt lgkmcnt(0)\n\t" \
  "v_lshlrev_b32 v61, 16, v161\n\t" \
  "v_lshlrev_b32 v62, 16, v162\n\t" \
  "v_add_f32 v61, v61, v62\n\t" \
  "v_mov_b32 v62, 0xffff\n\t" \
  "v_cvt_pk_bf16_f32 v61, v61, v61\n\t" \
  "s_mov_b64 exec, 1\n\t" \
  "v_bfi_b32 v66, v62, v61, v66\n\t" \
  "s_mov_b64 exec, 15\n\t" \
  "v_and_b32 v66, v62, v66\n\t" \
  "s_mov_b64 exec, 16\n\t" \
  "v_bfi_b32 v66, v62, v66, v61\n\t" \
  "s_mov_b64 exec, 0xff\n\t" \
  "v_mov_b32 v67, 0\n\t" \
  "s_mov_b64 exec, 0x100\n\t" \
  "v_mov_b32 v67, v61\n\t" \
  "s_mov_b64 exec, 0xf00\n\t" \
  "v_and_b32 v67, v62, v67\n\t" \
  "s_mov_b64 exec, 0x1000\n\t" \
  "v_bfi_b32 v67, v62, v67, v61\n\t" \
  "s_mov_b64 exec, -1\n\t" \
  "s_waitcnt lgkmcnt(0)\n\t"
#define PP_NB 15
#define PP_Q 0
#define PP_CLOB "v0", "v1", "v2", "v3", "v4", "v5", "v6", "v7", "v8", "v9", "v10", "v11", "v12", "v13", "v14", "v15", "v16", "v17", "v18", "v19", "v20", "v21", "v22", "v23", "v32", "v33", "v34", "v35", "v36", "v37", "v38", "v39", "v40", "v41", "v42", "v43", "v60", "v61", "v62", "v63", "v128", "v129", "v130", "v131", "v132", "v133", "v134", "v135", "v136", "v137", "v138", "v139", "v140", "v141", "v142", "v143", "v144", "v145", "v146", "v147", "v148", "v149", "v150", "v151", "v158", "v161", "v162"

#define LD_ASM1 \
  "s_add_u32 m0, s39, 49152\n\t" \
  "s_mov_b32 s68, 0x0\n\t" \
  "buffer_load_dwordx4 v5, s[44:47], s68 offen offset:0 lds\n\t" \
  "buffer_load_dwordx4 v6, s[44:47], s68 offen offset:1024 lds\n\t" \
  "buffer_load_dwordx4 v7, s[44:47], s68 offen offset:2048 lds\n\t" \
  "s_add_u32 m0, s38, 36864\n\t" \
  "s_mov_b32 s68, 0x0\n\t" \
  "buffer_load_dwordx4 v0, s[40:43], s68 offen offset:0 lds\n\t" \
  "buffer_load_dwordx4 v1, s[40:43], s68 offen offset:1024 lds\n\t" \
  "s_mov_b64 exec, 0xffff\n\t" \
  "buffer_load_dwordx4 v4, s[40:43], s68 offen offset:2048 lds\n\t" \
  "s_mov_b64 exec, -1\n\t" \
  "s_add_u32 m0, s38, 46080\n\t" \
  "s_mov_b32 s68, 0x24000\n\t" \
  "buffer_load_dwordx4 v2, s[40:43], s68 offen offset:0 lds\n\t" \
  "buffer_load_dwordx4 v3, s[40:43], s68 offen offset:1024 lds\n\t" \
  "s_mov_b64 exec, 0xffff\n\t" \
  "buffer_load_dwordx4 v4, s[40:43], s68 offen offset:2048 lds\n\t" \
  "s_mov_b64 exec, -1\n\t" \
  "s_mov_b32 m0, %[sk]\n\t" \
  "s_mov_b32 exec_hi, 0\n\t" \
  "buffer_load_dword v9, s[40:43], 0 offen lds\n\t" \
  "s_mov_b32 exec_lo, %[wm]\n\t" \
  "buffer_load_dword v10, s[44:47], 0 offen lds\n\t" \
  "s_mov_b64 exec, -1\n\t" \
  "s_add_u32 m0, s39, 61440\n\t" \
  "s_mov_b32 s68, 0x30000\n\t" \
  "buffer_load_dwordx4 v5, s[44:47], s68 offen offset:0 lds\n\t" \
  "buffer_load_dwordx4 v6, s[44:47], s68 offen offset:1024 lds\n\t" \
  "buffer_load_dwordx4 v7, s[44:47], s68 offen offset:2048 lds\n\t"
#define LD_ASM2 \
  "s_mov_b32 s85, 0x180000\n\t" \
  "s_load_dword s84, %[pw], s85\n\t" \
  "s_mov_b32 s85, 0x180800\n\t" \
  "s_load_dword s84, %[pw], s85\n\t" \
  "s_mov_b32 s85, 0x181000\n\t" \
  "s_load_dword s84, %[pw], s85\n\t" \
  "s_mov_b32 s85, 0x1b0000\n\t" \
  "s_load_dword s84, %[pw], s85\n\t" \
  "s_mov_b32 s85, 0x1b0800\n\t" \
  "s_load_dword s84, %[pw], s85\n\t" \
  "s_mov_b32 s85, 0x1b1000\n\t" \
  "s_load_dword s84, %[pw], s85\n\t" \
  "s_mov_b32 s85, 0x1e0000\n\t" \
  "s_load_dword s84, %[pw], s85\n\t" \
  "s_mov_b32 s85, 0x1e0800\n\t" \
  "s_load_dword s84, %[pw], s85\n\t" \
  "s_mov_b32 m0, %[tsk]\n\t" \
  "s_mov_b32 s85, 0x0\n\t" \
  "s_mov_b64 exec, 63\n\t" \
  "buffer_load_dword v16, %[sh], s85 offen lds\n\t" \
  "s_mov_b64 exec, -1\n\t" \
  "s_mov_b32 s68, 0x80\n\t" \
  "s_mov_b32 s69, 0x24080\n\t" \
  "s_mov_b32 s70, 0x60000\n\t" \
  "s_add_u32 m0, s38, 0\n\t" \
  "s_waitcnt vmcnt(6)\n\t" \
  "s_barrier\n\t" \
  "buffer_load_dwordx4 v0, s[40:43], s68 offen offset:0 lds\n\t" \
  "s_mov_b32 s85, 0x1e1000\n\t" \
  "s_load_dword s84, %[pw], s85\n\t" \
  "buffer_load_dwordx4 v1, s[40:43], s68 offen offset:1024 lds\n\t" \
  "s_mov_b32 s85, 0x210000\n\t" \
  "s_load_dword s84, %[pw], s85\n\t" \
  "s_mov_b64 exec, 0xffff\n\t" \
  "buffer_load_dwordx4 v4, s[40:43], s68 offen offset:2048 lds\n\t" \
  "s_add_u32 m0, s38, 9216\n\t" \
  "s_mov_b64 exec, -1\n\t" \
  "buffer_load_dwordx4 v2, s[40:43], s69 offen offset:0 lds\n\t" \
  "s_mov_b32 s85, 0x210800\n\t" \
  "s_load_dword s84, %[pw], s85\n\t" \
  "buffer_load_dwordx4 v3, s[40:43], s69 offen offset:1024 lds\n\t" \
  "s_mov_b64 exec, 0xffff\n\t" \
  "buffer_load_dwordx4 v4, s[40:43], s69 offen offset:2048 lds\n\t" \
  "s_add_u32 m0, s39, 0\n\t" \
  "s_mov_b64 exec, -1\n\t" \
  "buffer_load_dwordx4 v5, s[44:47], s70 offen offset:0 lds\n\t" \
  "buffer_load_dwordx4 v6, s[44:47], s70 offen offset:1024 lds\n\t" \
  "buffer_load_dwordx4 v7, s[44:47], s70 offen offset:2048 lds\n\t" \
  "s_mov_b32 m0, %[tsk]\n\t" \
  "s_mov_b32 s85, 0x180000\n\t" \
  "s_mov_b64 exec, 63\n\t" \
  "buffer_load_dword v16, %[sh], s85 offen lds\n\t" \
  "s_mov_b64 exec, -1\n\t" \
  "s_mov_b32 s68, 0x90000\n\t" \
  "s_mov_b32 s69, 0x100\n\t" \
  "s_mov_b32 s70, 0x24100\n\t" \
  "s_add_u32 m0, s39, 12288\n\t" \
  "s_waitcnt vmcnt(11)\n\t" \
  "s_barrier\n\t" \
  "buffer_load_dwordx4 v5, s[44:47], s68 offen offset:0 lds\n\t" \
  "s_mov_b32 s85, 0x211000\n\t" \
  "s_load_dword s84, %[pw], s85\n\t" \
  "buffer_load_dwordx4 v6, s[44:47], s68 offen offset:1024 lds\n\t" \
  "s_mov_b32 s85, 0x240000\n\t" \
  "s_load_dword s84, %[pw], s85\n\t" \
  "buffer_load_dwordx4 v7, s[44:47], s68 offen offset:2048 lds\n\t" \
  "s_mov_b32 s85, 0x240800\n\t" \
  "s_load_dword s84, %[pw], s85\n\t" \
  "s_add_u32 m0, s38, 18432\n\t" \
  "s_nop 0\n\t" \
  "buffer_load_dwordx4 v0, s[40:43], s69 offen offset:0 lds\n\t" \
  "buffer_load_dwordx4 v1, s[40:43], s69 offen offset:1024 lds\n\t" \
  "s_mov_b64 exec, 0xffff\n\t" \
  "buffer_load_dwordx4 v4, s[40:43], s69 offen offset:2048 lds\n\t" \
  "s_add_u32 m0, s38, 27648\n\t" \
  "s_mov_b64 exec, -1\n\t" \
  "buffer_load_dwordx4 v2, s[40:43], s70 offen offset:0 lds\n\t" \
  "buffer_load_dwordx4 v3, s[40:43], s70 offen offset:1024 lds\n\t" \
  "s_mov_b64 exec, 0xffff\n\t" \
  "buffer_load_dwordx4 v4, s[40:43], s70 offen offset:2048 lds\n\t" \
  "s_mov_b64 exec, -1\n\t" \
  "s_mov_b32 s68, 0xc0000\n\t" \
  "s_mov_b32 s69, 0xf0000\n\t" \
  "s_mov_b32 s70, 0x180\n\t" \
  "s_add_u32 m0, s39, 24576\n\t" \
  "s_waitcnt vmcnt(10)\n\t" \
  "s_barrier\n\t" \
  "buffer_load_dwordx4 v5, s[44:47], s68 offen offset:0 lds\n\t" \
  "s_mov_b32 s85, 0x241000\n\t" \
  "s_load_dword s84, %[pw], s85\n\t" \
  "buffer_load_dwordx4 v6, s[44:47], s68 offen offset:1024 lds\n\t" \
  "s_mov_b32 s85, 0x270000\n\t" \
  "s_load_dword s84, %[pw], s85\n\t" \
  "buffer_load_dwordx4 v7, s[44:47], s68 offen offset:2048 lds\n\t" \
  "s_mov_b32 s85, 0x270800\n\t" \
  "s_load_dword s84, %[pw], s85\n\t" \
  "s_add_u32 m0, s39, 36864\n\t" \
  "s_nop 0\n\t" \
  "buffer_load_dwordx4 v5, s[44:47], s69 offen offset:0 lds\n\t" \
  "buffer_load_dwordx4 v6, s[44:47], s69 offen offset:1024 lds\n\t" \
  "buffer_load_dwordx4 v7, s[44:47], s69 offen offset:2048 lds\n\t" \
  "s_add_u32 m0, s38, 36864\n\t" \
  "s_nop 0\n\t" \
  "buffer_load_dwordx4 v0, s[40:43], s70 offen offset:0 lds\n\t" \
  "buffer_load_dwordx4 v1, s[40:43], s70 offen offset:1024 lds\n\t" \
  "s_mov_b64 exec, 0xffff\n\t" \
  "buffer_load_dwordx4 v4, s[40:43], s70 offen offset:2048 lds\n\t" \
  "s_mov_b64 exec, -1\n\t" \
  "s_mov_b32 s68, 0x120000\n\t" \
  "s_mov_b32 s69, 0x24180\n\t" \
  "s_mov_b32 s70, 0x150000\n\t" \
  "s_add_u32 m0, s39, 49152\n\t" \
  "s_waitcnt vmcnt(15)\n\t" \
  "s_barrier\n\t" \
  "buffer_load_dwordx4 v5, s[44:47], s68 offen offset:0 lds\n\t" \
  "s_mov_b32 s85, 0x271000\n\t" \
  "s_load_dword s84, %[pw], s85\n\t" \
  "buffer_load_dwordx4 v6, s[44:47], s68 offen offset:1024 lds\n\t" \
  "s_mov_b32 s85, 0x2a0000\n\t" \
  "s_load_dword s84, %[pw], s85\n\t" \
  "buffer_load_dwordx4 v7, s[44:47], s68 offen offset:2048 lds\n\t" \
  "s_mov_b32 s85, 0x2a0800\n\t" \
  "s_load_dword s84, %[pw], s85\n\t" \
  "s_add_u32 m0, s38, 46080\n\t" \
  "s_nop 0\n\t" \
  "buffer_load_dwordx4 v2, s[40:43], s69 offen offset:0 lds\n\t" \
  "buffer_load_dwordx4 v3, s[40:43], s69 offen offset:1024 lds\n\t" \
  "s_mov_b64 exec, 0xffff\n\t" \
  "buffer_load_dwordx4 v4, s[40:43], s69 offen offset:2048 lds\n\t" \
  "s_add_u32 m0, s39, 61440\n\t" \
  "s_mov_b64 exec, -1\n\t" \
  "buffer_load_dwordx4 v5, s[44:47], s70 offen offset:0 lds\n\t" \
  "buffer_load_dwordx4 v6, s[44:47], s70 offen offset:1024 lds\n\t" \
  "buffer_load_dwordx4 v7, s[44:47], s70 offen offset:2048 lds\n\t" \
  "s_mov_b32 s68, 0x200\n\t" \
  "s_mov_b32 s69, 0x180000\n\t" \
  "s_mov_b32 s70, 0x24200\n\t" \
  "s_add_u32 m0, s38, 0\n\t" \
  "s_waitcnt vmcnt(15)\n\t" \
  "s_barrier\n\t" \
  "buffer_load_dwordx4 v0, s[40:43], s68 offen offset:0 lds\n\t" \
  "s_mov_b32 s85, 0x2a1000\n\t" \
  "s_load_dword s84, %[pw], s85\n\t" \
  "buffer_load_dwordx4 v1, s[40:43], s68 offen offset:1024 lds\n\t" \
  "s_mov_b32 s85, 0x2d0000\n\t" \
  "s_load_dword s84, %[pw], s85\n\t" \
  "s_mov_b64 exec, 0xffff\n\t" \
  "buffer_load_dwordx4 v4, s[40:43], s68 offen offset:2048 lds\n\t" \
  "s_add_u32 m0, s39, 0\n\t" \
  "s_mov_b64 exec, -1\n\t" \
  "buffer_load_dwordx4 v5, s[44:47], s69 offen offset:0 lds\n\t" \
  "s_mov_b32 s85, 0x2d0800\n\t" \
  "s_load_dword s84, %[pw], s85\n\t" \
  "buffer_load_dwordx4 v6, s[44:47], s69 offen offset:1024 lds\n\t" \
  "buffer_load_dwordx4 v7, s[44:47], s69 offen offset:2048 lds\n\t" \
  "s_add_u32 m0, s38, 9216\n\t" \
  "s_nop 0\n\t" \
  "buffer_load_dwordx4 v2, s[40:43], s70 offen offset:0 lds\n\t" \
  "buffer_load_dwordx4 v3, s[40:43], s70 offen offset:1024 lds\n\t" \
  "s_mov_b64 exec, 0xffff\n\t" \
  "buffer_load_dwordx4 v4, s[40:43], s70 offen offset:2048 lds\n\t" \
  "s_mov_b64 exec, -1\n\t" \
  "s_sleep 2\n\t" \
  "s_mov_b32 s68, 0x1b0000\n\t" \
  "s_add_u32 m0, s39, 12288\n\t" \
  "s_waitcnt vmcnt(21)\n\t" \
  "s_barrier\n\t" \
  "buffer_load_dwordx4 v5, s[44:47], s68 offen offset:0 lds\n\t" \
  "s_mov_b32 s85, 0x2d1000\n\t" \
  "s_load_dword s84, %[pw], s85\n\t" \
  "buffer_load_dwordx4 v6, s[44:47], s68 offen offset:1024 lds\n\t" \
  "s_mov_b32 s85, 0x300000\n\t" \
  "s_load_dword s84, %[pw], s85\n\t" \
  "buffer_load_dwordx4 v7, s[44:47], s68 offen offset:2048 lds\n\t" \
  "s_mov_b32 s85, 0x300800\n\t" \
  "s_load_dword s84, %[pw], s85\n\t" \
  "s_sleep 2\n\t" \
  "s_mov_b32 s68, 0x280\n\t" \
  "s_mov_b32 s69, 0x1e0000\n\t" \
  "s_mov_b32 s70, 0x24280\n\t" \
  "s_add_u32 m0, s38, 18432\n\t" \
  "s_waitcnt vmcnt(15)\n\t" \
  "s_barrier\n\t" \
  "buffer_load_dwordx4 v0, s[40:43], s68 offen offset:0 lds\n\t" \
  "s_mov_b32 s85, 0x301000\n\t" \
  "s_load_dword s84, %[pw], s85\n\t" \
  "buffer_load_dwordx4 v1, s[40:43], s68 offen offset:1024 lds\n\t" \
  "s_mov_b32 s85, 0x330000\n\t" \
  "s_load_dword s84, %[pw], s85\n\t" \
  "s_mov_b64 exec, 0xffff\n\t" \
  "buffer_load_dwordx4 v4, s[40:43], s68 offen offset:2048 lds\n\t" \
  "s_add_u32 m0, s39, 24576\n\t" \
  "s_mov_b64 exec, -1\n\t" \
  "buffer_load_dwordx4 v5, s[44:47], s69 offen offset:0 lds\n\t" \
  "s_mov_b32 s85, 0x330800\n\t" \
  "s_load_dword s84, %[pw], s85\n\t" \
  "buffer_load_dwordx4 v6, s[44:47], s69 offen offset:1024 lds\n\t" \
  "buffer_load_dwordx4 v7, s[44:47], s69 offen offset:2048 lds\n\t" \
  "s_add_u32 m0, s38, 27648\n\t" \
  "s_nop 0\n\t" \
  "buffer_load_dwordx4 v2, s[40:43], s70 offen offset:0 lds\n\t" \
  "buffer_load_dwordx4 v3, s[40:43], s70 offen offset:1024 lds\n\t" \
  "s_mov_b64 exec, 0xffff\n\t" \
  "buffer_load_dwordx4 v4, s[40:43], s70 offen offset:2048 lds\n\t" \
  "s_mov_b64 exec, -1\n\t" \
  "s_sleep 2\n\t" \
  "s_mov_b32 s68, 0x210000\n\t" \
  "s_add_u32 m0, s39, 36864\n\t" \
  "s_waitcnt vmcnt(21)\n\t" \
  "s_barrier\n\t" \
  "buffer_load_dwordx4 v5, s[44:47], s68 offen offset:0 lds\n\t" \
  "s_mov_b32 s85, 0x331000\n\t" \
  "s_load_dword s84, %[pw], s85\n\t" \
  "buffer_load_dwordx4 v6, s[44:47], s68 offen offset:1024 lds\n\t" \
  "s_mov_b32 s85, 0x360000\n\t" \
  "s_load_dword s84, %[pw], s85\n\t" \
  "buffer_load_dwordx4 v7, s[44:47], s68 offen offset:2048 lds\n\t" \
  "s_mov_b32 s85, 0x360800\n\t" \
  "s_load_dword s84, %[pw], s85\n\t" \
  "s_sleep 2\n\t" \
  "s_mov_b32 s68, 0x300\n\t" \
  "s_mov_b32 s69, 0x240000\n\t" \
  "s_add_u32 m0, s38, 36864\n\t" \
  "s_waitcnt vmcnt(15)\n\t" \
  "s_barrier\n\t" \
  "buffer_load_dwordx4 v0, s[40:43], s68 offen offset:0 lds\n\t" \
  "s_mov_b32 s85, 0x361000\n\t" \
  "s_load_dword s84, %[pw], s85\n\t" \
  "buffer_load_dwordx4 v1, s[40:43], s68 offen offset:1024 lds\n\t" \
  "s_mov_b32 s85, 0x390000\n\t" \
  "s_load_dword s84, %[pw], s85\n\t" \
  "s_mov_b64 exec, 0xffff\n\t" \
  "buffer_load_dwordx4 v4, s[40:43], s68 offen offset:2048 lds\n\t" \
  "s_add_u32 m0, s39, 49152\n\t" \
  "s_mov_b64 exec, -1\n\t" \
  "buffer_load_dwordx4 v5, s[44:47], s69 offen offset:0 lds\n\t" \
  "s_mov_b32 s85, 0x390800\n\t" \
  "s_load_dword s84, %[pw], s85\n\t" \
  "buffer_load_dwordx4 v6, s[44:47], s69 offen offset:1024 lds\n\t" \
  "buffer_load_dwordx4 v7, s[44:47], s69 offen offset:2048 lds\n\t" \
  "s_mov_b32 m0, %[tsk]\n\t" \
  "s_mov_b32 s85, 0x300000\n\t" \
  "s_mov_b64 exec, 63\n\t" \
  "buffer_load_dword v16, %[sh], s85 offen lds\n\t" \
  "s_mov_b64 exec, -1\n\t" \
  "s_sleep 2\n\t" \
  "s_mov_b32 s68, 0x24300\n\t" \
  "s_mov_b32 s69, 0x270000\n\t" \
  "s_add_u32 m0, s38, 46080\n\t" \
  "s_waitcnt vmcnt(19)\n\t" \
  "s_barrier\n\t" \
  "buffer_load_dwordx4 v2, s[40:43], s68 offen offset:0 lds\n\t" \
  "s_mov_b32 s85, 0x391000\n\t" \
  "s_load_dword s84, %[pw], s85\n\t" \
  "buffer_load_dwordx4 v3, s[40:43], s68 offen offset:1024 lds\n\t" \
  "s_mov_b32 s85, 0x3c0000\n\t" \
  "s_load_dword s84, %[pw], s85\n\t" \
  "s_mov_b64 exec, 0xffff\n\t" \
  "buffer_load_dwordx4 v4, s[40:43], s68 offen offset:2048 lds\n\t" \
  "s_add_u32 m0, s39, 61440\n\t" \
  "s_mov_b64 exec, -1\n\t" \
  "buffer_load_dwordx4 v5, s[44:47], s69 offen offset:0 lds\n\t" \
  "s_mov_b32 s85, 0x3c0800\n\t" \
  "s_load_dword s84, %[pw], s85\n\t" \
  "buffer_load_dwordx4 v6, s[44:47], s69 offen offset:1024 lds\n\t" \
  "buffer_load_dwordx4 v7, s[44:47], s69 offen offset:2048 lds\n\t" \
  "s_sleep 2\n\t" \
  "s_mov_b32 s68, 0x380\n\t" \
  "s_mov_b32 s69, 0x2a0000\n\t" \
  "s_add_u32 m0, s38, 0\n\t" \
  "s_waitcnt vmcnt(16)\n\t" \
  "s_barrier\n\t" \
  "buffer_load_dwordx4 v0, s[40:43], s68 offen offset:0 lds\n\t" \
  "s_mov_b32 s85, 0x3c1000\n\t" \
  "s_load_dword s84, %[pw], s85\n\t" \
  "buffer_load_dwordx4 v1, s[40:43], s68 offen offset:1024 lds\n\t" \
  "s_mov_b32 s85, 0x3f0000\n\t" \
  "s_load_dword s84, %[pw], s85\n\t" \
  "s_mov_b64 exec, 0xffff\n\t" \
  "buffer_load_dwordx4 v4, s[40:43], s68 offen offset:2048 lds\n\t" \
  "s_add_u32 m0, s39, 0\n\t" \
  "s_mov_b64 exec, -1\n\t" \
  "buffer_load_dwordx4 v5, s[44:47], s69 offen offset:0 lds\n\t" \
  "s_mov_b32 s85, 0x3f0800\n\t" \
  "s_load_dword s84, %[pw], s85\n\t" \
  "buffer_load_dwordx4 v6, s[44:47], s69 offen offset:1024 lds\n\t" \
  "buffer_load_dwordx4 v7, s[44:47], s69 offen offset:2048 lds\n\t" \
  "s_mov_b32 s68, 0x24380\n\t" \
  "s_mov_b32 s69, 0x2d0000\n\t" \
  "s_add_u32 m0, s38, 9216\n\t" \
  "s_waitcnt vmcnt(19)\n\t" \
  "s_barrier\n\t" \
  "buffer_load_dwordx4 v2, s[40:43], s68 offen offset:0 lds\n\t" \
  "s_mov_b32 s85, 0x3f1000\n\t" \
  "s_load_dword s84, %[pw], s85\n\t" \
  "buffer_load_dwordx4 v3, s[40:43], s68 offen offset:1024 lds\n\t" \
  "s_mov_b32 s85, 0x420000\n\t" \
  "s_load_dword s84, %[pw], s85\n\t" \
  "s_mov_b64 exec, 0xffff\n\t" \
  "buffer_load_dwordx4 v4, s[40:43], s68 offen offset:2048 lds\n\t" \
  "s_add_u32 m0, s39, 12288\n\t" \
  "s_mov_b64 exec, -1\n\t" \
  "buffer_load_dwordx4 v5, s[44:47], s69 offen offset:0 lds\n\t" \
  "s_mov_b32 s85, 0x420800\n\t" \
  "s_load_dword s84, %[pw], s85\n\t" \
  "buffer_load_dwordx4 v6, s[44:47], s69 offen offset:1024 lds\n\t" \
  "buffer_load_dwordx4 v7, s[44:47], s69 offen offset:2048 lds\n\t" \
  "s_mov_b32 s68, 0x400\n\t" \
  "s_mov_b32 s69, 0x300000\n\t" \
  "s_add_u32 m0, s38, 18432\n\t" \
  "s_waitcnt vmcnt(15)\n\t" \
  "s_barrier\n\t" \
  "buffer_load_dwordx4 v0, s[40:43], s68 offen offset:0 lds\n\t" \
  "s_mov_b32 s85, 0x421000\n\t" \
  "s_load_dword s84, %[pw], s85\n\t" \
  "buffer_load_dwordx4 v1, s[40:43], s68 offen offset:1024 lds\n\t" \
  "s_mov_b32 s85, 0x450000\n\t" \
  "s_load_dword s84, %[pw], s85\n\t" \
  "s_mov_b64 exec, 0xffff\n\t" \
  "buffer_load_dwordx4 v4, s[40:43], s68 offen offset:2048 lds\n\t" \
  "s_add_u32 m0, s39, 24576\n\t" \
  "s_mov_b64 exec, -1\n\t" \
  "buffer_load_dwordx4 v5, s[44:47], s69 offen offset:0 lds\n\t" \
  "s_mov_b32 s85, 0x450800\n\t" \
  "s_load_dword s84, %[pw], s85\n\t" \
  "buffer_load_dwordx4 v6, s[44:47], s69 offen offset:1024 lds\n\t" \
  "buffer_load_dwordx4 v7, s[44:47], s69 offen offset:2048 lds\n\t" \
  "s_mov_b32 s68, 0x24400\n\t" \
  "s_mov_b32 s69, 0x330000\n\t" \
  "s_add_u32 m0, s38, 27648\n\t" \
  "s_waitcnt vmcnt(18)\n\t" \
  "s_barrier\n\t" \
  "buffer_load_dwordx4 v2, s[40:43], s68 offen offset:0 lds\n\t" \
  "s_mov_b32 s85, 0x451000\n\t" \
  "s_load_dword s84, %[pw], s85\n\t" \
  "buffer_load_dwordx4 v3, s[40:43], s68 offen offset:1024 lds\n\t" \
  "s_mov_b32 s85, 0x480000\n\t" \
  "s_load_dword s84, %[pw], s85\n\t" \
  "s_mov_b64 exec, 0xffff\n\t" \
  "buffer_load_dwordx4 v4, s[40:43], s68 offen offset:2048 lds\n\t" \
  "s_add_u32 m0, s39, 36864\n\t" \
  "s_mov_b64 exec, -1\n\t" \
  "buffer_load_dwordx4 v5, s[44:47], s69 offen offset:0 lds\n\t" \
  "s_mov_b32 s85, 0x480800\n\t" \
  "s_load_dword s84, %[pw], s85\n\t" \
  "buffer_load_dwordx4 v6, s[44:47], s69 offen offset:1024 lds\n\t" \
  "buffer_load_dwordx4 v7, s[44:47], s69 offen offset:2048 lds\n\t" \
  "s_mov_b32 s68, 0x480\n\t" \
  "s_mov_b32 s69, 0x360000\n\t" \
  "s_add_u32 m0, s38, 36864\n\t" \
  "s_waitcnt vmcnt(15)\n\t" \
  "s_barrier\n\t" \
  "buffer_load_dwordx4 v0, s[40:43], s68 offen offset:0 lds\n\t" \
  "s_mov_b32 s85, 0x481000\n\t" \
  "s_load_dword s84, %[pw], s85\n\t" \
  "buffer_load_dwordx4 v1, s[40:43], s68 offen offset:1024 lds\n\t" \
  "s_mov_b32 s85, 0x4b0000\n\t" \
  "s_load_dword s84, %[pw], s85\n\t" \
  "s_mov_b64 exec, 0xffff\n\t" \
  "buffer_load_dwordx4 v4, s[40:43], s68 offen offset:2048 lds\n\t" \
  "s_add_u32 m0, s39, 49152\n\t" \
  "s_mov_b64 exec, -1\n\t" \
  "buffer_load_dwordx4 v5, s[44:47], s69 offen offset:0 lds\n\t" \
  "s_mov_b32 s85, 0x4b0800\n\t" \
  "s_load_dword s84, %[pw], s85\n\t" \
  "buffer_load_dwordx4 v6, s[44:47], s69 offen offset:1024 lds\n\t" \
  "buffer_load_dwordx4 v7, s[44:47], s69 offen offset:2048 lds\n\t" \
  "s_mov_b32 s68, 0x24480\n\t" \
  "s_mov_b32 s69, 0x390000\n\t" \
  "s_add_u32 m0, s38, 46080\n\t" \
  "s_waitcnt vmcnt(18)\n\t" \
  "s_barrier\n\t" \
  "buffer_load_dwordx4 v2, s[40:43], s68 offen offset:0 lds\n\t" \
  "s_mov_b32 s85, 0x4b1000\n\t" \
  "s_load_dword s84, %[pw], s85\n\t" \
  "buffer_load_dwordx4 v3, s[40:43], s68 offen offset:1024 lds\n\t" \
  "s_mov_b32 s85, 0x4e0000\n\t" \
  "s_load_dword s84, %[pw], s85\n\t" \
  "s_mov_b64 exec, 0xffff\n\t" \
  "buffer_load_dwordx4 v4, s[40:43], s68 offen offset:2048 lds\n\t" \
  "s_add_u32 m0, s39, 61440\n\t" \
  "s_mov_b64 exec, -1\n\t" \
  "buffer_load_dwordx4 v5, s[44:47], s69 offen offset:0 lds\n\t" \
  "s_mov_b32 s85, 0x4e0800\n\t" \
  "s_load_dword s84, %[pw], s85\n\t" \
  "buffer_load_dwordx4 v6, s[44:47], s69 offen offset:1024 lds\n\t" \
  "buffer_load_dwordx4 v7, s[44:47], s69 offen offset:2048 lds\n\t" \
  "s_mov_b32 s68, 0x500\n\t" \
  "s_mov_b32 s69, 0x3c0000\n\t" \
  "s_add_u32 m0, s38, 0\n\t" \
  "s_waitcnt vmcnt(15)\n\t" \
  "s_barrier\n\t" \
  "buffer_load_dwordx4 v0, s[40:43], s68 offen offset:0 lds\n\t" \
  "s_mov_b32 s85, 0x4e1000\n\t" \
  "s_load_dword s84, %[pw], s85\n\t" \
  "buffer_load_dwordx4 v1, s[40:43], s68 offen offset:1024 lds\n\t" \
  "s_mov_b32 s85, 0x510000\n\t" \
  "s_load_dword s84, %[pw], s85\n\t" \
  "s_mov_b64 exec, 0xffff\n\t" \
  "buffer_load_dwordx4 v4, s[40:43], s68 offen offset:2048 lds\n\t" \
  "s_add_u32 m0, s39, 0\n\t" \
  "s_mov_b64 exec, -1\n\t" \
  "buffer_load_dwordx4 v5, s[44:47], s69 offen offset:0 lds\n\t" \
  "s_mov_b32 s85, 0x510800\n\t" \
  "s_load_dword s84, %[pw], s85\n\t" \
  "buffer_load_dwordx4 v6, s[44:47], s69 offen offset:1024 lds\n\t" \
  "buffer_load_dwordx4 v7, s[44:47], s69 offen offset:2048 lds\n\t" \
  "s_mov_b32 m0, %[tsk]\n\t" \
  "s_mov_b32 s85, 0x480000\n\t" \
  "s_mov_b64 exec, 63\n\t" \
  "buffer_load_dword v16, %[sh], s85 offen lds\n\t" \
  "s_mov_b64 exec, -1\n\t" \
  "s_mov_b32 s68, 0x24500\n\t" \
  "s_mov_b32 s69, 0x3f0000\n\t" \
  "s_add_u32 m0, s38, 9216\n\t" \
  "s_waitcnt vmcnt(19)\n\t" \
  "s_barrier\n\t" \
  "buffer_load_dwordx4 v2, s[40:43], s68 offen offset:0 lds\n\t" \
  "s_mov_b32 s85, 0x511000\n\t" \
  "s_load_dword s84, %[pw], s85\n\t" \
  "buffer_load_dwordx4 v3, s[40:43], s68 offen offset:1024 lds\n\t" \
  "s_mov_b32 s85, 0x540000\n\t" \
  "s_load_dword s84, %[pw], s85\n\t" \
  "s_mov_b64 exec, 0xffff\n\t" \
  "buffer_load_dwordx4 v4, s[40:43], s68 offen offset:2048 lds\n\t" \
  "s_add_u32 m0, s39, 12288\n\t" \
  "s_mov_b64 exec, -1\n\t" \
  "buffer_load_dwordx4 v5, s[44:47], s69 offen offset:0 lds\n\t" \
  "s_mov_b32 s85, 0x540800\n\t" \
  "s_load_dword s84, %[pw], s85\n\t" \
  "buffer_load_dwordx4 v6, s[44:47], s69 offen offset:1024 lds\n\t" \
  "buffer_load_dwordx4 v7, s[44:47], s69 offen offset:2048 lds\n\t" \
  "s_mov_b32 m0, %[tsk]\n\t" \
  "s_mov_b64 exec, %[tm]\n\t" \
  "global_load_lds_dword v12, %[tp]\n\t" \
  "s_mov_b64 exec, -1\n\t" \
  "s_mov_b32 s68, 0x580\n\t" \
  "s_mov_b32 s69, 0x420000\n\t" \
  "s_add_u32 m0, s38, 18432\n\t" \
  "s_waitcnt vmcnt(16)\n\t" \
  "s_barrier\n\t" \
  "buffer_load_dwordx4 v0, s[40:43], s68 offen offset:0 lds\n\t" \
  "s_mov_b32 s85, 0x541000\n\t" \
  "s_load_dword s84, %[pw], s85\n\t" \
  "buffer_load_dwordx4 v1, s[40:43], s68 offen offset:1024 lds\n\t" \
  "s_mov_b32 s85, 0x570000\n\t" \
  "s_load_dword s84, %[pw], s85\n\t" \
  "s_mov_b64 exec, 0xffff\n\t" \
  "buffer_load_dwordx4 v4, s[40:43], s68 offen offset:2048 lds\n\t" \
  "s_add_u32 m0, s39, 24576\n\t" \
  "s_mov_b64 exec, -1\n\t" \
  "buffer_load_dwordx4 v5, s[44:47], s69 offen offset:0 lds\n\t" \
  "s_mov_b32 s85, 0x570800\n\t" \
  "s_load_dword s84, %[pw], s85\n\t" \
  "buffer_load_dwordx4 v6, s[44:47], s69 offen offset:1024 lds\n\t" \
  "buffer_load_dwordx4 v7, s[44:47], s69 offen offset:2048 lds\n\t" \
  "s_mov_b32 s68, 0x24580\n\t" \
  "s_mov_b32 s69, 0x450000\n\t" \
  "s_add_u32 m0, s38, 27648\n\t" \
  "s_waitcnt vmcnt(19)\n\t" \
  "s_barrier\n\t" \
  "buffer_load_dwordx4 v2, s[40:43], s68 offen offset:0 lds\n\t" \
  "s_mov_b32 s85, 0x571000\n\t" \
  "s_load_dword s84, %[pw], s85\n\t" \
  "buffer_load_dwordx4 v3, s[40:43], s68 offen offset:1024 lds\n\t" \
  "s_mov_b32 s85, 0x5a0000\n\t" \
  "s_load_dword s84, %[pw], s85\n\t" \
  "s_mov_b64 exec, 0xffff\n\t" \
  "buffer_load_dwordx4 v4, s[40:43], s68 offen offset:2048 lds\n\t" \
  "s_add_u32 m0, s39, 36864\n\t" \
  "s_mov_b64 exec, -1\n\t" \
  "buffer_load_dwordx4 v5, s[44:47], s69 offen offset:0 lds\n\t" \
  "s_mov_b32 s85, 0x5a0800\n\t" \
  "s_load_dword s84, %[pw], s85\n\t" \
  "buffer_load_dwordx4 v6, s[44:47], s69 offen offset:1024 lds\n\t" \
  "buffer_load_dwordx4 v7, s[44:47], s69 offen offset:2048 lds\n\t" \
  "s_mov_b32 s68, 0x600\n\t" \
  "s_mov_b32 s69, 0x480000\n\t" \
  "s_add_u32 m0, s38, 36864\n\t" \
  "s_waitcnt vmcnt(15)\n\t" \
  "s_barrier\n\t" \
  "buffer_load_dwordx4 v0, s[40:43], s68 offen offset:0 lds\n\t" \
  "s_mov_b32 s85, 0x5a1000\n\t" \
  "s_load_dword s84, %[pw], s85\n\t" \
  "buffer_load_dwordx4 v1, s[40:43], s68 offen offset:1024 lds\n\t" \
  "s_mov_b32 s85, 0x5d0000\n\t" \
  "s_load_dword s84, %[pw], s85\n\t" \
  "s_mov_b64 exec, 0xffff\n\t" \
  "buffer_load_dwordx4 v4, s[40:43], s68 offen offset:2048 lds\n\t" \
  "s_add_u32 m0, s39, 49152\n\t" \
  "s_mov_b64 exec, -1\n\t" \
  "buffer_load_dwordx4 v5, s[44:47], s69 offen offset:0 lds\n\t" \
  "s_mov_b32 s85, 0x5d0800\n\t" \
  "s_load_dword s84, %[pw], s85\n\t" \
  "buffer_load_dwordx4 v6, s[44:47], s69 offen offset:1024 lds\n\t" \
  "buffer_load_dwordx4 v7, s[44:47], s69 offen offset:2048 lds\n\t" \
  "s_mov_b32 s68, 0x24600\n\t" \
  "s_mov_b32 s69, 0x4b0000\n\t" \
  "s_add_u32 m0, s38, 46080\n\t" \
  "s_waitcnt vmcnt(18)\n\t" \
  "s_barrier\n\t" \
  "buffer_load_dwordx4 v2, s[40:43], s68 offen offset:0 lds\n\t" \
  "s_mov_b32 s85, 0x5d1000\n\t" \
  "s_load_dword s84, %[pw], s85\n\t" \
  "buffer_load_dwordx4 v3, s[40:43], s68 offen offset:1024 lds\n\t" \
  "s_load_dword s84, %[po], 0x0\n\t" \
  "s_mov_b64 exec, 0xffff\n\t" \
  "buffer_load_dwordx4 v4, s[40:43], s68 offen offset:2048 lds\n\t" \
  "s_add_u32 m0, s39, 61440\n\t" \
  "s_mov_b64 exec, -1\n\t" \
  "buffer_load_dwordx4 v5, s[44:47], s69 offen offset:0 lds\n\t" \
  "s_load_dword s84, %[po], 0x40\n\t" \
  "buffer_load_dwordx4 v6, s[44:47], s69 offen offset:1024 lds\n\t" \
  "buffer_load_dwordx4 v7, s[44:47], s69 offen offset:2048 lds\n\t" \
  "s_mov_b32 s68, 0x680\n\t" \
  "s_mov_b32 s69, 0x4e0000\n\t" \
  "s_add_u32 m0, s38, 0\n\t" \
  "s_waitcnt vmcnt(15)\n\t" \
  "s_barrier\n\t" \
  "buffer_load_dwordx4 v0, s[40:43], s68 offen offset:0 lds\n\t" \
  "s_load_dword s84, %[po], 0x80\n\t" \
  "buffer_load_dwordx4 v1, s[40:43], s68 offen offset:1024 lds\n\t" \
  "s_load_dword s84, %[po], 0xc0\n\t" \
  "s_mov_b64 exec, 0xffff\n\t" \
  "buffer_load_dwordx4 v4, s[40:43], s68 offen offset:2048 lds\n\t" \
  "s_add_u32 m0, s39, 0\n\t" \
  "s_mov_b64 exec, -1\n\t" \
  "buffer_load_dwordx4 v5, s[44:47], s69 offen offset:0 lds\n\t" \
  "s_load_dword s84, %[po], 0x100\n\t" \
  "buffer_load_dwordx4 v6, s[44:47], s69 offen offset:1024 lds\n\t" \
  "buffer_load_dwordx4 v7, s[44:47], s69 offen offset:2048 lds\n\t" \
  "s_mov_b32 s68, 0x24680\n\t" \
  "s_mov_b32 s69, 0x510000\n\t" \
  "s_add_u32 m0, s38, 9216\n\t" \
  "s_waitcnt vmcnt(18)\n\t" \
  "s_barrier\n\t" \
  "buffer_load_dwordx4 v2, s[40:43], s68 offen offset:0 lds\n\t" \
  "s_load_dword s84, %[po], 0x140\n\t" \
  "buffer_load_dwordx4 v3, s[40:43], s68 offen offset:1024 lds\n\t" \
  "s_load_dword s84, %[po], 0x180\n\t" \
  "s_mov_b64 exec, 0xffff\n\t" \
  "buffer_load_dwordx4 v4, s[40:43], s68 offen offset:2048 lds\n\t" \
  "s_add_u32 m0, s39, 12288\n\t" \
  "s_mov_b64 exec, -1\n\t" \
  "buffer_load_dwordx4 v5, s[44:47], s69 offen offset:0 lds\n\t" \
  "s_load_dword s84, %[po], 0x1c0\n\t" \
  "buffer_load_dwordx4 v6, s[44:47], s69 offen offset:1024 lds\n\t" \
  "buffer_load_dwordx4 v7, s[44:47], s69 offen offset:2048 lds\n\t" \
  "s_mov_b32 s68, 0x700\n\t" \
  "s_mov_b32 s69, 0x540000\n\t" \
  "s_add_u32 m0, s38, 18432\n\t" \
  "s_waitcnt vmcnt(15)\n\t" \
  "s_barrier\n\t" \
  "buffer_load_dwordx4 v0, s[40:43], s68 offen offset:0 lds\n\t" \
  "s_load_dword s84, %[po], 0x200\n\t" \
  "buffer_load_dwordx4 v1, s[40:43], s68 offen offset:1024 lds\n\t" \
  "s_load_dword s84, %[po], 0x240\n\t" \
  "s_mov_b64 exec, 0xffff\n\t" \
  "buffer_load_dwordx4 v4, s[40:43], s68 offen offset:2048 lds\n\t" \
  "s_add_u32 m0, s39, 24576\n\t" \
  "s_mov_b64 exec, -1\n\t" \
  "buffer_load_dwordx4 v5, s[44:47], s69 offen offset:0 lds\n\t" \
  "s_load_dword s84, %[po], 0x280\n\t" \
  "buffer_load_dwordx4 v6, s[44:47], s69 offen offset:1024 lds\n\t" \
  "buffer_load_dwordx4 v7, s[44:47], s69 offen offset:2048 lds\n\t" \
  "s_mov_b32 s68, 0x24700\n\t" \
  "s_mov_b32 s69, 0x570000\n\t" \
  "s_add_u32 m0, s38, 27648\n\t" \
  "s_waitcnt vmcnt(18)\n\t" \
  "s_barrier\n\t" \
  "buffer_load_dwordx4 v2, s[40:43], s68 offen offset:0 lds\n\t" \
  "s_load_dword s84, %[po], 0x2c0\n\t" \
  "buffer_load_dwordx4 v3, s[40:43], s68 offen offset:1024 lds\n\t" \
  "s_load_dword s84, %[po], 0x300\n\t" \
  "s_mov_b64 exec, 0xffff\n\t" \
  "buffer_load_dwordx4 v4, s[40:43], s68 offen offset:2048 lds\n\t" \
  "s_add_u32 m0, s39, 36864\n\t" \
  "s_mov_b64 exec, -1\n\t" \
  "buffer_load_dwordx4 v5, s[44:47], s69 offen offset:0 lds\n\t" \
  "s_load_dword s84, %[po], 0x340\n\t" \
  "buffer_load_dwordx4 v6, s[44:47], s69 offen offset:1024 lds\n\t" \
  "buffer_load_dwordx4 v7, s[44:47], s69 offen offset:2048 lds\n\t" \
  "s_mov_b32 s68, 0x780\n\t" \
  "s_mov_b32 s69, 0x5a0000\n\t" \
  "s_add_u32 m0, s38, 36864\n\t" \
  "s_waitcnt vmcnt(15)\n\t" \
  "s_barrier\n\t" \
  "buffer_load_dwordx4 v0, s[40:43], s68 offen offset:0 lds\n\t" \
  "s_load_dword s84, %[po], 0x380\n\t" \
  "buffer_load_dwordx4 v1, s[40:43], s68 offen offset:1024 lds\n\t" \
  "s_load_dword s84, %[po], 0x3c0\n\t" \
  "s_mov_b64 exec, 0xffff\n\t" \
  "buffer_load_dwordx4 v4, s[40:43], s68 offen offset:2048 lds\n\t" \
  "s_add_u32 m0, s39, 49152\n\t" \
  "s_mov_b64 exec, -1\n\t" \
  "buffer_load_dwordx4 v5, s[44:47], s69 offen offset:0 lds\n\t" \
  "s_load_dword s84, %[po], 0x400\n\t" \
  "buffer_load_dwordx4 v6, s[44:47], s69 offen offset:1024 lds\n\t" \
  "buffer_load_dwordx4 v7, s[44:47], s69 offen offset:2048 lds\n\t" \
  "s_mov_b32 s68, 0x24780\n\t" \
  "s_mov_b32 s69, 0x5d0000\n\t" \
  "s_add_u32 m0, s38, 46080\n\t" \
  "s_waitcnt vmcnt(18)\n\t" \
  "s_barrier\n\t" \
  "buffer_load_dwordx4 v2, s[40:43], s68 offen offset:0 lds\n\t" \
  "s_load_dword s84, %[po], 0x440\n\t" \
  "buffer_load_dwordx4 v3, s[40:43], s68 offen offset:1024 lds\n\t" \
  "s_load_dword s84, %[po], 0x480\n\t" \
  "s_mov_b64 exec, 0xffff\n\t" \
  "buffer_load_dwordx4 v4, s[40:43], s68 offen offset:2048 lds\n\t" \
  "s_add_u32 m0, s39, 61440\n\t" \
  "s_mov_b64 exec, -1\n\t" \
  "buffer_load_dwordx4 v5, s[44:47], s69 offen offset:0 lds\n\t" \
  "s_load_dword s84, %[po], 0x4c0\n\t" \
  "buffer_load_dwordx4 v6, s[44:47], s69 offen offset:1024 lds\n\t" \
  "buffer_load_dwordx4 v7, s[44:47], s69 offen offset:2048 lds\n\t" \
  "s_memrealtime %[st1]\n\t" \
  "v_readlane_b32 s68, v9, 0\n\t" \
  "v_readlane_b32 s69, v9, 1\n\t" \
  "v_readlane_b32 s70, v9, 2\n\t" \
  "v_readlane_b32 s71, v9, 18\n\t" \
  "v_readlane_b32 s72, v9, 19\n\t" \
  "s_add_u32 m0, %[ob], 0\n\t" \
  "s_waitcnt vmcnt(15)\n\t" \
  "s_barrier\n\t" \
  "s_nop 2\n\t" \
  "buffer_load_dwordx4 v8, %[so], s68 offen lds\n\t" \
  "s_load_dword s84, %[po], 0x500\n\t" \
  "s_add_u32 m0, %[ob], 4096\n\t" \
  "s_nop 0\n\t" \
  "buffer_load_dwordx4 v8, %[so], s69 offen lds\n\t" \
  "s_load_dword s84, %[po], 0x540\n\t" \
  "s_add_u32 m0, %[ob], 8192\n\t" \
  "s_nop 0\n\t" \
  "buffer_load_dwordx4 v8, %[so], s70 offen lds\n\t" \
  "s_load_dword s84, %[po], 0x580\n\t" \
  "s_add_u32 m0, %[ob], 73728\n\t" \
  "s_nop 0\n\t" \
  "buffer_load_dwordx4 v8, %[so], s71 offen lds\n\t" \
  "s_add_u32 m0, %[ob], 77824\n\t" \
  "s_nop 0\n\t" \
  "buffer_load_dwordx4 v8, %[so], s72 offen lds\n\t" \
  "v_readlane_b32 s68, v9, 3\n\t" \
  "v_readlane_b32 s69, v9, 4\n\t" \
  "v_readlane_b32 s70, v9, 5\n\t" \
  "v_readlane_b32 s71, v9, 20\n\t" \
  "v_readlane_b32 s72, v9, 21\n\t" \
  "s_add_u32 m0, %[ob], 12288\n\t" \
  "s_waitcnt vmcnt(17)\n\t" \
  "s_barrier\n\t" \
  "s_nop 2\n\t" \
  "buffer_load_dwordx4 v8, %[so], s68 offen lds\n\t" \
  "s_load_dword s84, %[po], 0x5c0\n\t" \
  "s_add_u32 m0, %[ob], 16384\n\t" \
  "s_nop 0\n\t" \
  "buffer_load_dwordx4 v8, %[so], s69 offen lds\n\t" \
  "s_load_dword s84, %[po], 0x600\n\t" \
  "s_add_u32 m0, %[ob], 20480\n\t" \
  "s_nop 0\n\t" \
  "buffer_load_dwordx4 v8, %[so], s70 offen lds\n\t" \
  "s_load_dword s84, %[po], 0x640\n\t" \
  "s_add_u32 m0, %[ob], 81920\n\t" \
  "s_nop 0\n\t" \
  "buffer_load_dwordx4 v8, %[so], s71 offen lds\n\t" \
  "s_add_u32 m0, %[ob], 86016\n\t" \
  "s_nop 0\n\t" \
  "buffer_load_dwordx4 v8, %[so], s72 offen lds\n\t" \
  "v_readlane_b32 s68, v9, 6\n\t" \
  "v_readlane_b32 s69, v9, 7\n\t" \
  "v_readlane_b32 s70, v9, 8\n\t" \
  "v_readlane_b32 s71, v9, 22\n\t" \
  "v_readlane_b32 s72, v9, 23\n\t" \
  "v_readlane_b32 s73, v9, 24\n\t" \
  "s_add_u32 m0, %[ob], 24576\n\t" \
  "s_waitcnt vmcnt(13)\n\t" \
  "s_barrier\n\t" \
  "s_nop 2\n\t" \
  "buffer_load_dwordx4 v8, %[so], s68 offen lds\n\t" \
  "s_load_dword s84, %[po], 0x680\n\t" \
  "s_add_u32 m0, %[ob], 28672\n\t" \
  "s_nop 0\n\t" \
  "buffer_load_dwordx4 v8, %[so], s69 offen lds\n\t" \
  "s_load_dword s84, %[po], 0x6c0\n\t" \
  "s_add_u32 m0, %[ob], 32768\n\t" \
  "s_nop 0\n\t" \
  "buffer_load_dwordx4 v8, %[so], s70 offen lds\n\t" \
  "s_load_dword s84, %[po], 0x700\n\t" \
  "s_add_u32 m0, %[ob], 90112\n\t" \
  "s_nop 0\n\t" \
  "buffer_load_dwordx4 v8, %[so], s71 offen lds\n\t" \
  "s_add_u32 m0, %[ob], 94208\n\t" \
  "s_nop 0\n\t" \
  "buffer_load_dwordx4 v8, %[so], s72 offen lds\n\t" \
  "s_add_u32 m0, %[ob], 98304\n\t" \
  "s_nop 0\n\t" \
  "buffer_load_dwordx4 v8, %[so], s73 offen lds\n\t" \
  "v_readlane_b32 s68, v9, 9\n\t" \
  "v_readlane_b32 s69, v9, 10\n\t" \
  "v_readlane_b32 s70, v9, 11\n\t" \
  "v_readlane_b32 s71, v9, 25\n\t" \
  "v_readlane_b32 s72, v9, 26\n\t" \
  "s_add_u32 m0, %[ob], 36864\n\t" \
  "s_waitcnt vmcnt(16)\n\t" \
  "s_barrier\n\t" \
  "s_nop 2\n\t" \
  "buffer_load_dwordx4 v8, %[so], s68 offen lds\n\t" \
  "s_load_dword s84, %[po], 0x740\n\t" \
  "s_add_u32 m0, %[ob], 40960\n\t" \
  "s_nop 0\n\t" \
  "buffer_load_dwordx4 v8, %[so], s69 offen lds\n\t" \
  "s_load_dword s84, %[po], 0x780\n\t" \
  "s_add_u32 m0, %[ob], 45056\n\t" \
  "s_nop 0\n\t" \
  "buffer_load_dwordx4 v8, %[so], s70 offen lds\n\t" \
  "s_load_dword s84, %[po], 0x7c0\n\t" \
  "s_add_u32 m0, %[ob], 102400\n\t" \
  "s_nop 0\n\t" \
  "buffer_load_dwordx4 v8, %[so], s71 offen lds\n\t" \
  "s_add_u32 m0, %[ob], 106496\n\t" \
  "s_nop 0\n\t" \
  "buffer_load_dwordx4 v8, %[so], s72 offen lds\n\t" \
  "v_readlane_b32 s68, v9, 12\n\t" \
  "v_readlane_b32 s69, v9, 13\n\t" \
  "v_readlane_b32 s70, v9, 14\n\t" \
  "v_readlane_b32 s71, v9, 27\n\t" \
  "v_readlane_b32 s72, v9, 28\n\t" \
  "v_readlane_b32 s73, v9, 29\n\t" \
  "s_add_u32 m0, %[ob], 49152\n\t" \
  "s_nop 3\n\t" \
  "s_barrier\n\t" \
  "s_nop 2\n\t" \
  "buffer_load_dwordx4 v8, %[so], s68 offen lds\n\t" \
  "s_add_u32 m0, %[ob], 53248\n\t" \
  "s_nop 0\n\t" \
  "buffer_load_dwordx4 v8, %[so], s69 offen lds\n\t" \
  "s_add_u32 m0, %[ob], 57344\n\t" \
  "s_nop 0\n\t" \
  "buffer_load_dwordx4 v8, %[so], s70 offen lds\n\t" \
  "s_add_u32 m0, %[ob], 110592\n\t" \
  "s_nop 0\n\t" \
  "buffer_load_dwordx4 v8, %[so], s71 offen lds\n\t" \
  "s_add_u32 m0, %[ob], 114688\n\t" \
  "s_nop 0\n\t" \
  "buffer_load_dwordx4 v8, %[so], s72 offen lds\n\t" \
  "s_add_u32 m0, %[ob], 118784\n\t" \
  "s_nop 0\n\t" \
  "buffer_load_dwordx4 v8, %[so], s73 offen lds\n\t" \
  "v_readlane_b32 s68, v9, 15\n\t" \
  "v_readlane_b32 s69, v9, 16\n\t" \
  "v_readlane_b32 s70, v9, 17\n\t" \
  "v_readlane_b32 s71, v9, 30\n\t" \
  "v_readlane_b32 s72, v9, 31\n\t" \
  "s_add_u32 m0, %[ob], 61440\n\t" \
  "LS_%=:\n\t" \
  "ds_read_b32 v11, v10\n\t" \
  "s_addk_i32 %[cn], 1\n\t" \
  "s_cmp_gt_u32 %[cn], 0x400000\n\t" \
  "s_cbranch_scc1 LX_%=\n\t" \
  "s_waitcnt lgkmcnt(0)\n\t" \
  "v_cmp_eq_u32 vcc, 0, v11\n\t" \
  "s_cbranch_vccz LX_%=\n\t" \
  "s_sleep 1\n\t" \
  "s_branch LS_%=\n\t" \
  "LX_%=:\n\t" \
  "s_nop 2\n\t" \
  "buffer_load_dwordx4 v8, %[so], s68 offen lds\n\t" \
  "s_add_u32 m0, %[ob], 65536\n\t" \
  "s_nop 0\n\t" \
  "buffer_load_dwordx4 v8, %[so], s69 offen lds\n\t" \
  "s_add_u32 m0, %[ob], 69632\n\t" \
  "s_nop 0\n\t" \
  "buffer_load_dwordx4 v8, %[so], s70 offen lds\n\t" \
  "s_add_u32 m0, %[ob], 122880\n\t" \
  "s_nop 0\n\t" \
  "buffer_load_dwordx4 v8, %[so], s71 offen lds\n\t" \
  "s_add_u32 m0, %[ob], 126976\n\t" \
  "s_nop 0\n\t" \
  "buffer_load_dwordx4 v8, %[so], s72 offen lds\n\t" \
  "s_waitcnt vmcnt(0) lgkmcnt(0)\n\t"
#define LD_HT 8
#define LD_TP 2
#define LD_CLOB "v11", "s68", "s69", "s70", "s71", "s72", "s73", "s74", "s75", "s76", "s77", "s78", "s79", "s80", "s81", "s82", "s83", "s84", "s85"

#define ENDW WAITV(0);
#define TS(i) do{ T[i]=__builtin_amdgcn_s_memrealtime(); }while(0)
#define TSS(i)
#define TOUT(n) do{ if(l==0){ unsigned long long* dbg=(unsigned long long*)(flags+65536)+(bid*12+w8)*8; for(int i_=1;i_<(n);i_++) dbg[i_-1]=T[i_]-T[0]; dbg[6]=R0; dbg[7]=__builtin_amdgcn_s_memrealtime(); } }while(0)

#define SPIN(cond,slp) do{ int sp_=0; while(cond){ __builtin_amdgcn_s_sleep(slp); if(++sp_>(1<<22)) break; } }while(0)
__device__ __forceinline__ unsigned rd1(const char* p){ return __builtin_amdgcn_readfirstlane(*(volatile LDS_AS unsigned*)(LDS_AS char*)p); }
__device__ __forceinline__ unsigned lds_inc(const char* p, int l){ unsigned r=0; if(l==0) r=__hip_atomic_fetch_add((LDS_AS unsigned*)(LDS_AS char*)p, 1u, __ATOMIC_RELAXED, __HIP_MEMORY_SCOPE_WORKGROUP); return __builtin_amdgcn_readfirstlane(r); }
__device__ __forceinline__ void lds_add(unsigned adr){ asm volatile("s_mov_b64 exec, 1\n\tds_add_u32 %0, %1\n\ts_mov_b64 exec, -1" :: "v"(adr), "v"(1u) : "memory"); }
__device__ __forceinline__ void publish(unsigned cnt, const unsigned* base, unsigned voff, unsigned tag){
  unsigned t;
  asm volatile("s_waitcnt vmcnt(0)\n\ts_mov_b64 exec, 1\n\tds_add_rtn_u32 %0, %1, %2\n\ts_waitcnt lgkmcnt(0)\n\tv_cmp_eq_u32 vcc, 3, %0\n\ts_cbranch_vccz PB_%=\n\t"
    "s_mov_b64 exec, 0xffff\n\tglobal_store_dword %3, %4, %5 sc1\n\tPB_%=:\n\ts_mov_b64 exec, -1"
    : "=&v"(t) : "v"(cnt), "v"(1u), "v"(voff), "v"(tag), "s"(base) : "memory", "vcc");
}
__device__ __forceinline__ void bars(int n){
  asm volatile("BL_%=:\n\ts_barrier\n\ts_sub_u32 %0, %0, 1\n\ts_cmp_lg_u32 %0, 0\n\ts_cbranch_scc1 BL_%=" : "+s"(n) :: "memory", "scc");
}
#define barc(rem) barc_(rem)
#define CTRA
#define CTRP
__device__ __forceinline__ void barc_(unsigned& rem){
  asm volatile("s_cmp_eq_u32 %0, 0\n\ts_cbranch_scc1 BC_%=\n\ts_barrier\n\ts_sub_u32 %0, %0, 1\n\tBC_%=:" : "+s"(rem) :: "memory", "scc");
}
template<int SLP> __device__ __forceinline__ void wait_flag_b(const unsigned* p, unsigned voff, unsigned tag, unsigned& rem CTRA){
  int spins=0;
  while(true){
    unsigned v; asm volatile("global_load_dword %0, %1, %2 sc1" : "=v"(v) : "v"(voff), "s"(p) : "memory");
    barc(rem); if(PB2) barc(rem);
    asm volatile("s_waitcnt vmcnt(0)" : "+v"(v) :: "memory");
    if(__all(v==tag) || ++spins>(1<<21)) break;
    if(rem==0) __builtin_amdgcn_s_sleep(SLP);
  }
}
__device__ __forceinline__ void stout(void* sbase, unsigned voff, u32x4 v){
  st16cs(sbase,voff,v);
}

typedef unsigned u32x8 __attribute__((ext_vector_type(8)));
typedef unsigned u32x16 __attribute__((ext_vector_type(16)));
__device__ __forceinline__ char* ptr64(unsigned lo, unsigned hi){ return (char*)(((unsigned long long)hi<<32)|lo); }
__device__ __forceinline__ u32x4 srd(const char* p, unsigned bytes){ return u32x4{(unsigned)(size_t)p,(unsigned)((size_t)p>>32)&0xffffu,bytes,0x20000u}; }
constexpr size_t WS_O=0, WS_KV=(size_t)S*DM*2, WS_Y=(size_t)S*DM*4, WS_FL=(size_t)S*DM*12;

__global__ __launch_bounds__(768,3) void fused_kernel(const u16* x_, const u16* wqkv_, const u16* wo_, char* ws_, u16* out_,
    const u16* kin_, const u16* vin_, const u16* shk_, const u16* hk_, const u16* qin_, const u16* sD_)
{
  __shared__ __attribute__((aligned(128))) char smem[ALL_LDS];
  unsigned long long T[8]; unsigned long long R0=__builtin_amdgcn_s_memrealtime();
  TS(0);
  int wv;
  asm volatile(
      "s_load_dwordx16 s[48:63], %[ka], 0x0\n\ts_load_dwordx4 s[64:67], %[ka], 0x40\n\ts_load_dwordx2 s[78:79], %[ka], 0x50\n\ts_getpc_b64 s[72:73]\n\tv_readfirstlane_b32 s75, v0\n\ts_lshr_b32 s74, %[b], 3\n\ts_mul_i32 s74, s74, 12\n\ts_lshr_b32 s75, s75, 6\n\t"
      "s_add_u32 s74, s74, s75\n\ts_min_u32 s74, s74, %[cl]\n\ts_lshl_b32 s74, s74, 7\n\ts_mov_b32 m0, %[sk]\n\tv_mov_b32 v1, s74\n\ts_mov_b64 exec, 1\n\tglobal_load_lds_dword v1, s[72:73]\n\ts_mov_b64 exec, -1\n\t"
      "s_mov_b32 s76, %[b]\n\ts_cmp_lt_u32 s75, 4\n\ts_cbranch_scc1 FK_G\n\ts_cmp_lt_u32 s75, 8\n\ts_cbranch_scc0 FK_C\n\t"
      "v_and_b32 v9, 7, v0\n\tv_and_b32 v10, 63, v0\n\ts_lshl_b32 s74, %[b], 19\n\ts_lshl_b32 s73, %[b], 11\n\ts_or_b32 s74, s74, s73\n\ts_and_b32 s74, s74, 0x3fc000\n\ts_lshl_b32 s73, s75, 9\n\t"
      "s_add_u32 s74, s74, s73\n\ts_add_u32 s74, s74, 0x7800\n\tv_lshl_add_u32 v9, v9, 11, s74\n\tv_and_b32 v1, 24, v0\n\ts_lshl2_add_u32 s74, %[b], s75\n\ts_mul_i32 s74, s74, %[nw]\n\tv_lshl_add_u32 v9, v1, 4, v9\n\t"
      "s_add_u32 s74, s74, %[w0]\n\tv_add_lshl_u32 v10, v10, s74, 7\n\t"
      "s_setprio %[lp]\n\ts_and_b32 s74, s75, 3\n\tv_lshrrev_b32 v1, 3, v0\n\tv_lshrrev_b32 v3, 4, v0\n\ts_and_b32 s72, %[b], 1\n\ts_and_b32 s73, %[b], 0xe0\n\ts_lshl_b32 s72, s72, 10\n\ts_lshl2_add_u32 s72, s73, s72\n\t"
      "v_and_b32 v4, 2, v1\n\tv_and_or_b32 v3, v3, 4, v4\n\tv_and_b32 v4, 31, v1\n\tv_xor_b32 v3, v3, v0\n\tv_mul_u32_u24 v4, 0x1800, v4\n\tv_and_b32 v3, 7, v3\n\tv_lshl_add_u32 v3, v3, 4, s72\n\t"
      "s_mul_i32 s39, s74, 0xc00\n\ts_mul_i32 s38, s74, 0x900\n\ts_mul_i32 s74, s74, 18\n\tv_add_u32 v5, v3, v4\n\tv_and_b32 v1, 7, v1\n\tv_add_u32 v6, 0x400, v5\n\tv_add_u32 v1, s74, v1\n\tv_add_u32 v7, 0x800, v5\n\t"
      "s_and_b32 s72, %[b], 6\n\ts_and_b32 s73, %[b], 24\n\ts_lshl4_add_u32 s72, s72, s73\n\tv_xor_b32 v3, v1, v0\n\ts_lshl_b32 s72, s72, 15\n\tv_and_b32 v3, 7, v3\n\tv_lshl_add_u32 v1, v1, 11, s72\n\t"
      "s_mov_b32 s47, 0x20000\n\ts_mov_b32 s46, 0x600000\n\tv_lshl_add_u32 v2, v3, 4, v1\n\ts_mov_b32 s43, s47\n\ts_mov_b32 s42, 0x408000\n\ts_movk_i32 s73, 0x3c00\n\t"
      "v_add_u32 v3, s73, v2\n\ts_add_u32 s38, s38, %[xf]\n\tv_max_u32 v0, 0x8000, v2\n\tv_add_u32 v4, s73, v3\n\tv_max_u32 v1, 0x8000, v3\n\t"
      "s_waitcnt lgkmcnt(0)\n\ts_mov_b64 s[44:45], s[50:51]\n\ts_sub_u32 s40, s48, 0x8000\n\ts_subb_u32 s41, s49, 0\n\t"
      LD_ASM1
      "s_branch FK_L"
      : "=s"(wv)
      : "{v0}"(threadIdx.x), [ka]"s"(__builtin_amdgcn_kernarg_segment_ptr()), [b]"s"(blockIdx.x), [cl]"n"(CODEL-1), [sk]"n"(SINK+256), [lp]"n"(LPR), [xf]"n"(X_OFF), [nw]"n"(3*TPW), [w0]"n"(TW0*3072-12*TPW), [wm]"n"((1<<(3*TPW))-1)
      : "memory", "scc", "s48", "s49", "s50", "s51", "s52", "s53", "s54", "s55", "s72", "s73", "s74", "s75", "s76", "s78", "s79", "v1", "v2", "v3", "v4", "v5", "v6", "v7", "v9", "v10",
        "s40", "s41", "s42", "s43", "s44", "s45", "s46", "s47", "s38", "s39", "s56", "s57", "s58", "s59", "s60", "s61", "s62", "s63", "s64", "s65", "s66", "s67", "s68");
  #define ENTER(lbl) int w8, bid; u32x8 a0, a1; u32x4 a2; u32x2 a3; asm volatile(lbl ":" : "={s75}"(w8), "={s76}"(bid), "={s[48:55]}"(a0), "={s[56:63]}"(a1), "={s[64:67]}"(a2), "={s[78:79]}"(a3) :: "memory");
  #define ARGW asm volatile("s_waitcnt lgkmcnt(0)" : "+{s[48:55]}"(a0), "+{s[56:63]}"(a1), "+{s[64:67]}"(a2), "+{s[78:79]}"(a3));
  #define ROLE const int w=w8&3, xcd=bid&7, idx=bid>>3, rb=(xcd>>1)*4+(idx&3), hp=(xcd&1)*8+(idx>>2), s0=rb*128; (void)s0; (void)hp;
  #define LANEVARS int l; asm volatile("v_mbcnt_lo_u32_b32 %0, -1, 0\n\tv_mbcnt_hi_u32_b32 %0, -1, %0" : "=v"(l)); const int g=l>>4, fr=l&15, tq=(l&15)>>2, tp=l&3; (void)g; (void)fr; (void)tq; (void)tp;
  #define ARGS0 const char* x=ptr64(a0[0],a0[1]); const char* wqkv=ptr64(a0[2],a0[3]); const char* wo=ptr64(a0[4],a0[5]); char* ws=ptr64(a0[6],a0[7]); \
    unsigned* flags=(unsigned*)(ws+WS_FL); const int hot0=(3-(int)(a0[2]>>7))&7; (void)x; (void)wqkv; (void)wo; (void)flags; (void)hot0;
  #define ORA ((unsigned)(hp-hot0-1)&7u)
  #define ORE(c) (ORA==7u? (c)-1u : (c))
  #define ORO(c) (ORE(c)>=NF && ORE(c)<NF+7u)
  #define ORD(c) (((((ORE(c)<14u? ((ORA==7u? 0u:ORA)+(ORO(c)? ORE(c)-NF : (ORE(c)<NF? ORE(c) : ORE(c)-7u)))%7u : 7u)+hot0+1)&7u))+8u*(((hp>>3)^(ORO(c) || (c)==15u))&1u))
  const unsigned sb0=__builtin_amdgcn_readfirstlane(lds_addr(smem));
  const char* syn=smem+SYN;

  if(__builtin_expect(wv==1,1)){
    ENTER("FK_L")
    u32x4 sx, sw; unsigned xl, wl, xoA, xoB, xo, xo1, xo2, wof[3];
    asm volatile("; descriptors and LDS bases" : "={s[40:43]}"(sx), "={s[44:47]}"(sw), "={s38}"(xl), "={s39}"(wl));
    asm volatile("; lane offsets" : "={v0}"(xoA), "={v1}"(xoB), "={v2}"(xo), "={v3}"(xo1), "={v4}"(xo2), "={v5}"(wof[0]), "={v6}"(wof[1]), "={v7}"(wof[2]));
    ROLE
    LANEVARS
    unsigned ltr=0, ltr2=0, ltr3=0; (void)ltr; (void)ltr2; (void)ltr3;
    TSS(2); TSS(3);
    ARGS0
    const unsigned wd=hp*128;
    {
      const unsigned wof2=(unsigned)(w*16384+(l>>3)*2048+((((l&7)>>1)^((l>>4)&3))*32)+(l&1)*16);
      const unsigned to=ORD((unsigned)(l>>1)&15u);
      const unsigned tab=to*131072u+(l&1)*65536;
      unsigned long long tm; asm("s_cmp_eq_u32 %1, 0\n\ts_cselect_b64 %0, %2, 0" : "=s"(tm) : "s"(w), "s"(WTL==2? 0xffffffffull : 0ull) : "scc");
      unsigned cn=0;
      const unsigned hn=(unsigned)(w*(3*LD_HT/4)+l), hto=(hn/6u)*393216u+(hn%6u)*2048u;
      asm volatile(LD_ASM2
        : [cn]"+&s"(cn)
          , [st1]"=&s"(T[1])
        : "{v0}"(xoA), "{v1}"(xoB), "{v2}"(xo), "{v3}"(xo1), "{v4}"(xo2), "{v5}"(wof[0]), "{v6}"(wof[1]), "{v7}"(wof[2]), "{v8}"(wof2), "{v9}"(tab),
          "{v10}"(sb0+SYN+32+(l&3)*4), "{v12}"((l&31)*2048),
          "{v16}"(hto), [sh]"s"(srd(wqkv+((xcd&1)*8+hot0)*128+idx*12288,6291456u-2048u-idx*12288u+128u)),
          "{s[40:43]}"(sx), "{s[44:47]}"(sw), [so]"s"(srd(wo+wd,2097152u-wd)), "{s38}"(xl), "{s39}"(wl), [ob]"s"(sb0+w*1024),
          [pw]"s"(wqkv+idx*6144+(xcd&1)*1024+(xcd>>1)*256+w*64), [po]"s"(wo+(bid*4+w)*2048),
          [tsk]"s"(sb0+SINK+256), [tm]"s"(tm), [tp]"s"(wo+idx*65536+(xcd&1)*1024+((3-(int)(a0[4]>>7))&7)*128)
        : "memory", "scc", "vcc", LD_CLOB);
    }
    const unsigned tag=rd1(syn+64)+1u;
    lds_add(sb0+SYN+12);
    TS(2);
    if(w>=4-NPOL){
      const unsigned* fp=flags+F_O+(rb*16+hp)*16; const unsigned fo=ORD((unsigned)l&15u)*4u;
      const unsigned madr=sb0+(l==0? SYN+20 : SINK+l*4);
      SPIN(rd1(syn+12)<4u || rd1(syn+16)<4u,1);
      if(w==3) PUBM(madr,1u);
      if(PD0) __builtin_amdgcn_s_sleep(PD0);
      if(w==2) __builtin_amdgcn_s_sleep(PD1); if(w==1) __builtin_amdgcn_s_sleep(2*PD1); if(w==0) __builtin_amdgcn_s_sleep(3*PD1);
      unsigned rdy=1; int spins=0;
      while(rdy!=0xFFFFu){
        unsigned v;
        asm volatile("s_mov_b64 exec, 0xffff\n\tglobal_atomic_or %0, %1, %3, %2 sc0 sc1\n\ts_mov_b64 exec, -1\n\ts_waitcnt vmcnt(0)" : "=&v"(v) : "v"(fo), "s"(fp), "v"(0u) : "memory");
        unsigned r=((unsigned)__ballot(v==tag)&0xFFFFu)|1u;
        if(++spins>(1<<21)) r=0xFFFFu;
        if(r!=rdy){ PUBM(madr,(unsigned)__builtin_ctz(~r)); rdy=r; if(w<3 && (r&0x3FFFu)==0x3FFFu) break; }
        else __builtin_amdgcn_s_sleep(OSLP);
      }
      TS(3);
      if(bid==0 && w==3){
        int sp=0;
        while(true){
          u32x4 v; asm volatile("global_load_dwordx4 %0, %1, %2 sc1\n\ts_waitcnt vmcnt(0)" : "=&v"(v) : "v"(l*16), "s"(flags+F_Y) : "memory");
          if(__all(v[0]==tag && v[1]==tag && v[2]==tag && v[3]==tag) || ++sp>(1<<20)) break;
          __builtin_amdgcn_s_sleep(12);
        }
        if(l==0) stflag(flags, tag);
      }
    } else { TS(3); }
    TOUT(4);
    __builtin_amdgcn_endpgm();
  }

  if(wv==0){
    ENTER("FK_G")
    ROLE
    __builtin_amdgcn_s_setprio(GPR);
    const int h=w&1, r=w>>1;
    f32x16 e0, e1;
    LANEVARS
    TSS(3);
    ARGW
    ARGS0
    if(PGT&1){ if(w==1 && l<7) touch(ws, l<6? l*2097152+PGO : (unsigned)WS_FL+2048, sb0+SINK+512); }
    if(PGT&2){ if(w==2 && l<4) touch(ptr64(a1[0],a1[1]), l*2097152+PGO, sb0+SINK+512); }
    if(PGT&4){ if(w==3 && l<1) touch(wo, PGO, sb0+SINK+512); if(w==0 && l<2) touch(wqkv, l*2097152+2097152, sb0+SINK+512); }
    TSS(4);
    {
      unsigned lx0=sb0+X_OFF+fr*128+((g^(fr&7))*16), lx1=sb0+X_OFF+fr*128+(((4+g)^(fr&7))*16);
      const int sw=wswz(8*g+tq);
      unsigned lw0=sb0+g*3072+tq*128+(unsigned)((((2*h)^sw)*32)+8*tp), lw1=sb0+g*3072+tq*128+(unsigned)((((2*h+1)^sw)*32)+8*tp);
      char* ot=ws+WS_O+(rb*16+hp)*16384+r*8192+h*2048;
      unsigned it; unsigned long long rm; asm("s_cmp_lg_u32 %1, 0\n\ts_cselect_b64 %0, -1, 0" : "=s"(rm) : "s"(r) : "scc");
      asm volatile(N2_ASM
        : [it]"=&s"(it), "+{v56}"(lx0), "+{v57}"(lx1), "+{v58}"(lw0), "+{v59}"(lw1)
          , [st1]"=&s"(T[1]), [st2]"=&s"(T[2])
        : "{v62}"(l*16), "{v63}"(sb0+EX_OFF+h*3072+l*16), [r]"s"(r), [ot]"s"(ot), [ou]"s"(ot+4096), [fa]"s"(sb0+SYN+32+w*4), [fp]"s"(sb0+SYN+32+(w^2)*4),
          [vo]"s"(2048-1024*r), [rm]"s"(rm), [z]"s"(rb|r)
        : "memory", "scc", "vcc", N2_CLOB);
    }
    TS(3);
    unsigned tb;
    {
      LANEVARS
      const unsigned tag=rd1(syn+64)+1u;
      const unsigned ra=sb0+(4*g+tq)*128+8*tp; const int sw=(2*g+(tq>>1))&3;
      unsigned b0=ra+((0^sw)*32), b1=ra+((1^sw)*32), b2=ra+((2^sw)*32), b3=ra+((3^sw)*32), vo=(unsigned)(w*4096+l*16);
      tb=ORD((unsigned)l&15u)*16384u;
      asm volatile("" : "+{v160}"(b0), "+{v161}"(b1), "+{v162}"(b2), "+{v163}"(b3), "+{v164}"(vo), "+{v167}"(tb));
      publish(sb0+SYN+16, flags+F_O+rb*256+hp, l*64, tag);
      TS(4);
      __builtin_amdgcn_s_setprio(G2P);
      unsigned c=0, iss=0, th, q0, q1, cn=0;
      asm volatile(G3_ASM
        : "={v[128:143]}"(e0), "={v[144:159]}"(e1), [c]"+s"(c), [iss]"+s"(iss), [th]"=&s"(th), [t0]"=&s"(q0), [t1]"=&s"(q1), [cn]"+&s"(cn),
          "+{v160}"(b0), "+{v161}"(b1), "+{v162}"(b2), "+{v163}"(b3), "+{v167}"(tb)
          , [st5]"=&s"(T[5])
        : "{v164}"(vo), "{v165}"(sb0+SYN+20), [go]"s"(60+w*4), [ob]"s"(srd(ws+WS_O+rb*262144,262144u))
        : "memory", "scc", "vcc", G3_CLOB);
    }
    {
      LANEVARS
      char* sg=smem+EX_OFF+w*2048;
      char* op=ptr64(a1[0],a1[1])+(s0+w*32)*4096+2048+hp*128; const unsigned oo=(unsigned)((l>>3)*4096+(l&7)*16);
      u32x4 ov[4];
      #pragma unroll
      for(int t=0;t<2;t++){
        #pragma unroll
        for(int j=0;j<4;j++){
          #define E2(i) (t? e1[(i)&15] : e0[(i)&15])
          *(volatile LDS_AS u32x2*)(LDS_AS char*)(sg+fr*128+(((2*j+(g>>1))^(fr&7))*16)+(g&1)*8)=u32x2{pack2(E2(4*j),E2(4*j+1)),pack2(E2(4*j+2),E2(4*j+3))};
        }
        #pragma unroll
        for(int i=0;i<2;i++) ov[2*t+i]=*(volatile LDS_AS u32x4*)(LDS_AS char*)(sg+(i*8+(l>>3))*128+(((l&7)^(l>>3))*16));
      }
      #pragma unroll
      for(int i=0;i<4;i++) stout(op+i*32768, oo, ov[i]);
      ENDW TS(6); TOUT(7);
    }
    __builtin_amdgcn_endpgm();
  }

  {
    ENTER("FK_C")
    ROLE
    char* cr=smem+C_OFF+2048+w*4096;
    const unsigned crs=sb0+C_OFF+2048+w*4096;
    const int sblk=bid>>3, td0=(bid&7)*128+(w&1)*64;
    const int vb=(LOCA&1)? xcd*32+(idx&16)+(idx&1)*8+((idx>>1)&7) : bid, fb=(LOCA&2)? (idx&15)*16+xcd*2+(idx>>4) : bid;
    const int d=(vb>>4)*64+(((vb>>3)&1)*4+w)*8+(vb&7);
    int kv0=KV0+(sblk&KVS);
    static_assert(KV0+KVS+PP_NB<=33 && KV0>=EB, "barrier budget");
    unsigned tagv, ctr; f32x8 RB;
    const int fd0=(fb&15)*64, fs0=(fb>>4)*128+w*32;
    u32x4 qv[4];
    f32x16 A0,A1,A2,A3;
    const int rw=sblk*64+(w>>1)*32, j=rw>>5;
    unsigned k0,k1,k2,ko,ta,se,so,ha,l16,rb0[2];
    {
      LANEVARS
      if(w==0 && l<25 && l!=16) ((unsigned*)(smem+SYN))[l]=0u;
      if(l<32) *(u32x4*)(smem+C_OFF+w*512+l*16)=u32x4{0,0,0,0};
      asm volatile("" ::: "memory");
      __builtin_amdgcn_s_setprio(CPR);
      const int dg=l&7, rg=l>>3;
      ko=(unsigned)(rg*8192+dg*16);
      const bool top=(rw==0 && rg==0);
      k0=top? ko+6144:ko; k1=top? ko+6144:ko+2048; k2=top? ko+6144:ko+4096;
      ta=sb0+EX_OFF+w*1024+(w&1)*512+dg*64; se=(l&8)? 0x03020706u : 0x05040100u; ha=crs+(32*g-4*tq+4*tp)*2; l16=l*16;
      so=(unsigned)(rg*32768+((j>>2)&3)*128+((dg&4)|((dg+j)&3))*16);
      #pragma unroll
      for(int k=0;k<2;k++){ const int q=fr-1-2*g+k; rb0[k]=sb0+C_OFF+(4+(q>>1))*256+((q>>3)&3)*64+((w+(q>>1))&3)*16+(q&1)*8; }
      asm volatile("" : "+{v152}"(k0), "+{v153}"(k1), "+{v154}"(k2), "+{v155}"(ko), "+{v156}"(ta), "+{v157}"(se), "+{v159}"(so), "+{v163}"(l16), "+{v165}"(ha), "+{v166}"(rb0[0]), "+{v167}"(rb0[1]));
    }
    TSS(3);
    ARGW
    if(EB==1) asm volatile("s_barrier" : "+s"(a1), "+s"(a2), "+s"(a3), "+s"(a0) :: "memory");
    if(EB==2) asm volatile("s_barrier\n\ts_barrier" : "+s"(a1), "+s"(a2), "+s"(a3), "+s"(a0) :: "memory");
    ARGS0
    char* out=ptr64(a1[0],a1[1]); const char* qp=ptr64(a2[2],a2[3])+(fs0*DM+fd0)*2;
    {
      const char* kb=ptr64(a1[2],a1[3])+(rw*DM+td0)*2; const char* vb=ptr64(a1[4],a1[5])+(rw*DM+td0)*2;
      unsigned long long zm; asm("s_cmp_eq_u32 %1, 0\n\ts_cselect_b64 %0, 0xff, 0" : "=s"(zm) : "s"(rw) : "scc");
      unsigned t0;
      if(PGT&8){ const char* tp= w==0? kb : w==1? vb : w==2? qp : ptr64(a2[0],a2[1])+d*4096;
        asm volatile("s_mov_b32 m0, %1\n\ts_mov_b64 exec, 1\n\tglobal_load_lds_dword v163, %0\n\ts_mov_b64 exec, -1" :: "s"(tp), "s"(sb0+SINK+512), "{v163}"(l16) : "memory"); }
      TSS(5);
      asm volatile(PP_ASM
        : [t0]"=&s"(t0), "=&{v164}"(ctr), "+{v159}"(so), "=&{v160}"(tagv), "=&{v[24:31]}"(RB), "=&{v[44:47]}"(qv[0]), "=&{v[48:51]}"(qv[1]), "=&{v[52:55]}"(qv[2]), "=&{v[56:59]}"(qv[3]),
          "=&{v[64:79]}"(A0), "=&{v[80:95]}"(A1), "=&{v[96:111]}"(A2), "=&{v[112:127]}"(A3)
          , [st1]"=&s"(T[1]), [st2]"=&s"(T[2])
        : "{v152}"(k0), "{v153}"(k1), "{v154}"(k2), "{v155}"(ko), "{v156}"(ta), "{v157}"(se), "{v166}"(rb0[0]), "{v167}"(rb0[1]), "{v165}"(ha), "{v163}"(l16),
          [qb0]"s"(qp), [qb2]"s"(qp+4096), [mk]"s"(0x180u), [em]"s"(1ull|(512ull<<w)), [hd]"s"(crs),
          [kb0]"s"(kb-6144), [kb3]"s"(kb), [kb5]"s"(kb+4096), [vb0]"s"(vb), [vb2]"s"(vb+4096), [sb]"s"(ws+WS_KV+td0*4096+j*512),
          [n0]"s"(kv0-EB-(HH==2)), [ff]"s"(flags+F_PP+(td0>>6)*64+j), [zm]"s"(zm), [shd]"s"(sb0+EX_OFF+w*1024), [tgd]"s"(sb0+SYN+64), [shp]"s"(ptr64(a1[6],a1[7])+(bid&7)*1024), [fl]"s"(flags),
          [hp]"s"(ptr64(a2[0],a2[1])+d*4096), [dp]"s"(ptr64(a3[0],a3[1])+d*2)
        : "memory", "scc", PP_CLOB);
    }
    LANEVARS
    const unsigned tag=__builtin_amdgcn_readfirstlane(tagv)+1u;
    unsigned rem=33-PP_NB-kv0;
    barc(rem);
    const unsigned* pf=flags+F_PP+(d>>6)*64;
    const char* kvp=ws+WS_KV+((vb>>4)*8+(vb&7))*32768+((vb>>3)&1)*64+w*8192;
    const unsigned kvo=(unsigned)((l>>2)*128+(l&3)*16);
    int bidx=((l>=16)? l-16 : (l==0? 63 : l+47))*4;
    asm volatile("" : "+{v32}"(bidx));
    __builtin_amdgcn_s_setprio(3);
    while(rem>PREM) barc(rem);
    wait_flag_b<8>(pf, l*4, tag, rem CTRP);
    #pragma unroll
    for(int i=0;i<4;i++){ if(i%KTH==0 && i) barc(rem); gldsc(kvp+i*2048, kvo, crs+i*1024); }
    barc(rem); barc(rem);
    WAITV(0);
    asm volatile("s_mov_b32 m0, %2\n\ts_mov_b64 exec, 1\n\tglobal_load_lds_dword %0, %1 sc1\n\ts_mov_b64 exec, -1" :: "v"(0u), "s"(pf), "s"(sb0+SYN+100+w*4) : "memory");
    while(rem) barc(rem);
    { int sp=0; while(!__all(*(volatile LDS_AS unsigned*)(LDS_AS char*)(syn+100+(l&3)*4)==tag) && ++sp<(1<<22)) __builtin_amdgcn_s_sleep(1); }
    __builtin_amdgcn_s_setprio(CVP);
    TS(3);
    f32x16 ac0, ac1;
    {
      asm volatile("s_nop 1\n\t" CV_ASM
        : "=&{v[128:143]}"(ac0), "=&{v[144:159]}"(ac1)
          , "+{v[44:47]}"(qv[0]), "+{v[48:51]}"(qv[1]), "+{v[52:55]}"(qv[2]), "+{v[56:59]}"(qv[3])
        : "{v[64:79]}"(A0), "{v[80:95]}"(A1), "{v[96:111]}"(A2), "{v[112:127]}"(A3),
          "{v[24:31]}"(RB), "{v32}"(bidx), "{v33}"(sb0+SYN+80+w*4)
          , "{v42}"((256*(g&1)+16*fr+4*(g&2))*2), [yb]"s"(ws+WS_Y+d*4096)
          , "{v43}"((l>>3)*8192+(l&7)*16), [q0]"s"(qp), [q2]"s"(qp+4096)
        : "memory", "scc", CV_CLOB);
    }
    TS(4);
    __builtin_amdgcn_s_setprio(3);
    publish(sb0+SYN+24, flags+F_Y+vb, l*1024, tag);
    asm volatile("" : "+v"(qv[0]), "+v"(qv[1]), "+v"(qv[2]), "+v"(qv[3]));
    TS(5);
    float qf[32];
    #pragma unroll
    for(int i=0;i<16;i++){ qf[2*i]=bflo(qv[i>>2][i&3]); qf[2*i+1]=bfhi(qv[i>>2][i&3]); }
    unsigned ya=crs+(g*8+tq)*64+tp*8, wa0=crs+fr*128+((g^(fr&7))*16), wa1=crs+fr*128+(((4+g)^(fr&7))*16), ra[4], oo=(unsigned)((l>>3)*16384+(l&7)*16), yo2=(l>>2)*4096+(l&3)*16;
    #pragma unroll
    for(int i=0;i<4;i++){ const int row=4*(l>>3)+i; ra[i]=crs+row*128+(((l&7)^(row&7))*16); }
    char* op=out+fs0*4096+fd0*2; char* op2=op+8192; const char* yg=ws+WS_Y+fd0*4096+fs0*2;
    #pragma unroll
    for(int i=0;i<32;i++) asm volatile("" : "+v"(qf[i]));
    asm volatile("" : "+v"(ya), "+v"(wa0), "+v"(wa1), "+v"(ra[0]), "+v"(ra[1]), "+v"(ra[2]), "+v"(ra[3]), "+v"(oo), "+v"(yo2));
    asm volatile("" : "+s"(op), "+s"(op2), "+s"(yg));
    if(w==0){
      SPIN(rd1(syn+24)<4u,1);
      SPIN(rd1(syn+96)==0u,2);
      { int sp=0;
        while(true){
          unsigned v=tag; asm volatile("s_mov_b64 exec, 0xffff\n\tglobal_atomic_or %0, %1, %3, %2 sc0 sc1\n\ts_mov_b64 exec, -1\n\ts_waitcnt vmcnt(0)" : "+&v"(v) : "v"(l*4), "s"(flags+F_Y+fb*16), "v"(0u) : "memory");
          if(__all(v==tag) || ++sp>(1<<21)) break;
          __builtin_amdgcn_s_sleep(YSLP);
        } }
      PUB(sb0+(l==0? SYN+28 : SINK+l*4), 1u);
    } else SPIN(rd1(syn+28)==0u,YS2);
    #pragma unroll
    for(int i=0;i<4;i++) gldsc(yg+i*65536, yo2, crs+i*1024);
    {
      u32x4 y4[4];
      asm volatile("s_waitcnt vmcnt(0)\n\t"
        "ds_read_b64_tr_b16 v[0:1], %4\n\tds_read_b64_tr_b16 v[2:3], %4 offset:256\n\tds_read_b64_tr_b16 v[4:5], %4 offset:32\n\tds_read_b64_tr_b16 v[6:7], %4 offset:288\n\t"
        "ds_read_b64_tr_b16 v[8:9], %4 offset:2048\n\tds_read_b64_tr_b16 v[10:11], %4 offset:2304\n\tds_read_b64_tr_b16 v[12:13], %4 offset:2080\n\tds_read_b64_tr_b16 v[14:15], %4 offset:2336\n\t"
        "s_waitcnt lgkmcnt(6)\n\tds_write_b128 %5, v[0:3]\n\ts_waitcnt lgkmcnt(5)\n\tds_write_b128 %5, v[4:7] offset:2048\n\t"
        "s_waitcnt lgkmcnt(4)\n\tds_write_b128 %6, v[8:11]\n\ts_waitcnt lgkmcnt(3)\n\tds_write_b128 %6, v[12:15] offset:2048\n\t"
        "ds_read_b128 %0, %7\n\tds_read_b128 %1, %8\n\tds_read_b128 %2, %9\n\tds_read_b128 %3, %10\n\ts_waitcnt lgkmcnt(0)"
        : "=&v"(y4[0]), "=&v"(y4[1]), "=&v"(y4[2]), "=&v"(y4[3])
        : "v"(ya), "v"(wa0), "v"(wa1), "v"(ra[0]), "v"(ra[1]), "v"(ra[2]), "v"(ra[3])
        : "memory", "v0", "v1", "v2", "v3", "v4", "v5", "v6", "v7", "v8", "v9", "v10", "v11", "v12", "v13", "v14", "v15");
      #pragma unroll
      for(int i=0;i<4;i++){
        u32x4 ov;
        #pragma unroll
        for(int e=0;e<4;e++) ov[e]=pack2(mul1(qf[8*i+2*e],bflo(y4[i][e])), mul1(qf[8*i+2*e+1],bfhi(y4[i][e])));
        stout((i&2? op2:op)+(i&1)*4096, oo, ov);
      }
    }
    ENDW TS(6); TOUT(7);
    __builtin_amdgcn_endpgm();
  }
}

#define K0G 1
#define K0B 64
__global__ void k0_kernel(unsigned* flags){ unsigned long long t=__builtin_amdgcn_s_memrealtime(); if(threadIdx.x==0 && blockIdx.x==0) ((unsigned long long*)(flags+65536))[40000]=t; }
__global__ void amp_kernel(unsigned* flags){
  const int l=threadIdx.x;
  const unsigned long long* tb=(const unsigned long long*)(flags+65536);
  const unsigned long long ref=tb[6]-5000;
  unsigned sum=0, mx=0, mns=0xffffffffu, mxe=0, mxs=0;
  for(int q=0;q<4;q++){ const int b=q*64+l;
    unsigned v=(unsigned)tb[(b*12+0)*8+0]-0u; sum+=v; mx=max(mx,v);
    for(int r=0;r<12;r++){ unsigned st=(unsigned)(tb[(b*12+r)*8+6]-ref), en=(unsigned)(tb[(b*12+r)*8+7]-ref); mns=min(mns,st); mxs=max(mxs,st); mxe=max(mxe,en); } }
  for(int o=32;o;o>>=1){ sum+=__shfl_xor(sum,o); mx=max(mx,(unsigned)__shfl_xor(mx,o)); mns=min(mns,(unsigned)__shfl_xor(mns,o)); mxs=max(mxs,(unsigned)__shfl_xor(mxs,o)); mxe=max(mxe,(unsigned)__shfl_xor(mxe,o)); }
  unsigned X4= 0u;
  if(X4>20000u) X4=20000u;
  const unsigned long long end=ref+mns+5000u+X4;
  int sp=0; while(__builtin_amdgcn_s_memrealtime()<end && ++sp<(1<<20)) __builtin_amdgcn_s_sleep(1);
}
extern "C" void kernel_launch(void* const* d_in, const int* in_sizes, int n_in, void* d_out, int out_size, void* d_ws, size_t ws_size, hipStream_t stream){
  const u16* q=(const u16*)d_in[0]; const u16* k=(const u16*)d_in[1]; const u16* v=(const u16*)d_in[2];
  const u16* shk=(const u16*)d_in[3]; const u16* hk=(const u16*)d_in[4]; const u16* sD=(const u16*)d_in[5];
  const u16* x=(const u16*)d_in[6]; const u16* wqkv=(const u16*)d_in[7]; const u16* wo=(const u16*)d_in[8];
  fused_kernel<<<256,768,0,stream>>>(x,wqkv,wo,(char*)d_ws,(u16*)d_out,k,v,shk,hk,q,sD);
  amp_kernel<<<1,64,0,stream>>>((unsigned*)((char*)d_ws+WS_FL));
}
```

```cpp
#include <hip/hip_runtime.h>
#include <hip/hip_bf16.h>
#include <stdint.h>

typedef unsigned short u16;
typedef short bf16x8 __attribute__((ext_vector_type(8)));
typedef short s16x4 __attribute__((ext_vector_type(4)));
typedef float f32x2 __attribute__((ext_vector_type(2)));
typedef float f32x4 __attribute__((ext_vector_type(4)));
typedef float f32x8 __attribute__((ext_vector_type(8)));
typedef float f32x16 __attribute__((ext_vector_type(16)));
typedef unsigned u32x4 __attribute__((ext_vector_type(4)));
typedef unsigned u32x2 __attribute__((ext_vector_type(2)));
typedef __bf16 bf16x2_t __attribute__((ext_vector_type(2)));
#define LDS_AS __attribute__((address_space(3)))

__device__ __forceinline__ float bflo(unsigned x){ return __uint_as_float(x<<16); }
__device__ __forceinline__ float bfhi(unsigned x){ return __uint_as_float(x&0xffff0000u); }
__device__ __forceinline__ unsigned pack2(float lo, float hi){ return __builtin_bit_cast(unsigned, __builtin_convertvector(f32x2{lo,hi}, bf16x2_t)); }
__device__ __forceinline__ float mul1(float a, float b){ float r; asm("v_mul_f32 %0, %1, %2" : "=v"(r) : "v"(a), "v"(b)); return r; }
__device__ __forceinline__ float fma1(float a, float b, float c){ float r; asm("v_fma_f32 %0, %1, %2, %3" : "=v"(r) : "v"(a), "v"(b), "v"(c)); return r; }

__device__ __forceinline__ void glds(const void* sbase, unsigned voff, unsigned ldsaddr){
  asm volatile("s_mov_b32 m0, %2\n\ts_nop 0\n\tglobal_load_lds_dwordx4 %0, %1" :: "v"(voff), "s"(sbase), "s"(ldsaddr) : "memory");
}
__device__ __forceinline__ void gldsc(const void* sbase, unsigned voff, unsigned ldsaddr){
  asm volatile("s_mov_b32 m0, %2\n\ts_nop 0\n\tglobal_load_lds_dwordx4 %0, %1 sc1" :: "v"(voff), "s"(sbase), "s"(ldsaddr) : "memory");
}
__device__ __forceinline__ void touch(const void* sbase, unsigned voff, unsigned ldsaddr){
  asm volatile("s_mov_b32 m0, %2\n\ts_nop 0\n\tglobal_load_lds_dword %0, %1" :: "v"(voff), "s"(sbase), "s"(ldsaddr) : "memory");
}
__device__ __forceinline__ void st16c(void* p, u32x4 v){ asm volatile("global_store_dwordx4 %0, %1, off sc1\n\ts_nop 1" :: "v"(p), "v"(v) : "memory"); }
__device__ __forceinline__ void st16cs(void* sbase, unsigned voff, u32x4 v){ asm volatile("global_store_dwordx4 %0, %1, %2 sc1\n\ts_nop 1" :: "v"(voff), "v"(v), "s"(sbase) : "memory"); }
#define GLB_AS __attribute__((address_space(1)))
__device__ __forceinline__ unsigned ldflag(const unsigned* p){ return __hip_atomic_load((const GLB_AS unsigned*)p, __ATOMIC_RELAXED, __HIP_MEMORY_SCOPE_AGENT); }
__device__ __forceinline__ void stflag(unsigned* p, unsigned v){ __hip_atomic_store((GLB_AS unsigned*)p, v, __ATOMIC_RELAXED, __HIP_MEMORY_SCOPE_AGENT); }
template<int SLP> __device__ __forceinline__ void wait_flag(const unsigned* p, unsigned tag){
  int spins=0;
  while(true){ unsigned v=ldflag(p); if(__all(v==tag)) break; __builtin_amdgcn_s_sleep(SLP); if(++spins>(1<<21)) break; }
}
__device__ __forceinline__ unsigned lds_addr(const void* p){ return (unsigned)(size_t)(const LDS_AS void*)p; }
#define WAITV(n) asm volatile("s_waitcnt vmcnt(" #n ")":::"memory")
#define WAITL() asm volatile("s_waitcnt lgkmcnt(0)":::"memory")
#define SBAR() asm volatile("s_barrier":::"memory")
#define PUBM(addr,val) asm volatile("ds_max_u32 %0, %1" :: "v"(addr), "v"(val) : "memory")
#define PUB(addr,val) asm volatile("ds_write_b32 %0, %1" :: "v"(addr), "v"(val) : "memory")
__device__ __forceinline__ s16x4 trread(const char* p){ return __builtin_amdgcn_ds_read_tr16_b64_v4i16((LDS_AS s16x4*)p); }
__device__ __forceinline__ bf16x8 cat44(s16x4 a, s16x4 b){ return __builtin_shufflevector(a,b,0,1,2,3,4,5,6,7); }
__device__ __forceinline__ int wswz(int row){ return ((row>>1)&1) | (((row>>3)&1)<<1); }
__device__ __forceinline__ u32x4 widen(u32x2 a, u32x2 b){
  auto r0=__builtin_amdgcn_permlane16_swap(a[0],b[0],false,false);
  auto r1=__builtin_amdgcn_permlane16_swap(a[1],b[1],false,false);
  return u32x4{r0[0],r1[0],r0[1],r1[1]};
}

constexpr int S=2048, DM=1024;
constexpr int WS=12288, XS=18432, X_OFF=6*WS;
constexpr int CREG=4608, C_OFF=131072;
constexpr int EX_OFF=C_OFF+4*CREG;
constexpr int SYN=EX_OFF+8192;
constexpr int SINK=SYN+256;
constexpr int ALL_LDS=SINK+768;
constexpr int F_PP=1024, F_O=4096, F_Y=8192;
#define KV0 1
#define KVS 3
#define LPR 2
#define CPR 2
#define GPR 1
#define CGATE 0
#define OSLP 2
#define YSLP 8
#define TPX 16
#define TPW 2
#define TWO 0
#define CVP 1
#define G2P 3
#define OUTC 1
#define ROT 0
#define WTL 2
#define PB2 1
#define PREM 9
#define KTH 1
#define YDEF 3
#define OPOL 1
#define CODEL 272
#define MIX 0
#define LOCA 3
#define XLOC 1
#define PGT 0
#define PGO 0
#define QREM 0
#define EB 0
#define TW0 1
#define ORDM 1
#define NF 7u
#define NPOL 1
#define PD0 0
#define PD1 4
#define HH 0
#define HW 3
#define HKV 4
#define YPOL 1
#define YS2 2
#define OSH 4
#define HSK 3
#define HGR 2
#define HLAST 1

#define N2_ASM \
  "v_mov_b64 v[64:65], 0\n\t" \
  "v_mov_b64 v[66:67], 0\n\t" \
  "v_mov_b64 v[68:69], 0\n\t" \
  "v_mov_b64 v[70:71], 0\n\t" \
  "v_mov_b64 v[72:73], 0\n\t" \
  "v_mov_b64 v[74:75], 0\n\t" \
  "v_mov_b64 v[76:77], 0\n\t" \
  "v_mov_b64 v[78:79], 0\n\t" \
  "v_mov_b64 v[80:81], 0\n\t" \
  "v_mov_b64 v[82:83], 0\n\t" \
  "v_mov_b64 v[84:85], 0\n\t" \
  "v_mov_b64 v[86:87], 0\n\t" \
  "v_mov_b64 v[88:89], 0\n\t" \
  "v_mov_b64 v[90:91], 0\n\t" \
  "v_mov_b64 v[92:93], 0\n\t" \
  "v_mov_b64 v[94:95], 0\n\t" \
  "v_mov_b64 v[96:97], 0\n\t" \
  "v_mov_b64 v[98:99], 0\n\t" \
  "v_mov_b64 v[100:101], 0\n\t" \
  "v_mov_b64 v[102:103], 0\n\t" \
  "v_mov_b64 v[104:105], 0\n\t" \
  "v_mov_b64 v[106:107], 0\n\t" \
  "v_mov_b64 v[108:109], 0\n\t" \
  "v_mov_b64 v[110:111], 0\n\t" \
  "v_mov_b64 v[112:113], 0\n\t" \
  "v_mov_b64 v[114:115], 0\n\t" \
  "v_mov_b64 v[116:117], 0\n\t" \
  "v_mov_b64 v[118:119], 0\n\t" \
  "v_mov_b64 v[120:121], 0\n\t" \
  "v_mov_b64 v[122:123], 0\n\t" \
  "v_mov_b64 v[124:125], 0\n\t" \
  "v_mov_b64 v[126:127], 0\n\t" \
  "v_mov_b64 v[128:129], 0\n\t" \
  "v_mov_b64 v[130:131], 0\n\t" \
  "v_mov_b64 v[132:133], 0\n\t" \
  "v_mov_b64 v[134:135], 0\n\t" \
  "v_mov_b64 v[136:137], 0\n\t" \
  "v_mov_b64 v[138:139], 0\n\t" \
  "v_mov_b64 v[140:141], 0\n\t" \
  "v_mov_b64 v[142:143], 0\n\t" \
  "v_mov_b64 v[144:145], 0\n\t" \
  "v_mov_b64 v[146:147], 0\n\t" \
  "v_mov_b64 v[148:149], 0\n\t" \
  "v_mov_b64 v[150:151], 0\n\t" \
  "v_mov_b64 v[152:153], 0\n\t" \
  "v_mov_b64 v[154:155], 0\n\t" \
  "v_mov_b64 v[156:157], 0\n\t" \
  "v_mov_b64 v[158:159], 0\n\t" \
  "v_mov_b64 v[160:161], 0\n\t" \
  "v_mov_b64 v[162:163], 0\n\t" \
  "v_mov_b64 v[164:165], 0\n\t" \
  "v_mov_b64 v[166:167], 0\n\t" \
  "v_add_u32 v60, 36864, v58\n\t" \
  "v_add_u32 v61, 36864, v59\n\t" \
  "s_mov_b32 %[it], 0\n\t" \
  "s_cmp_lg_u32 %[r], 0\n\t" \
  "s_barrier\n\t" \
  "s_cbranch_scc1 NB_%=\n\t" \
  "ds_read_b64_tr_b16 v[40:41], v60 offset:13312\n\t" \
  "ds_read_b64_tr_b16 v[42:43], v60 offset:13824\n\t" \
  "ds_read_b64_tr_b16 v[44:45], v60 offset:14336\n\t" \
  "ds_read_b64_tr_b16 v[46:47], v60 offset:14848\n\t" \
  "ds_read_b64_tr_b16 v[48:49], v61 offset:13312\n\t" \
  "ds_read_b64_tr_b16 v[50:51], v61 offset:13824\n\t" \
  "ds_read_b128 v[0:3], v56 offset:36864\n\t" \
  "ds_read_b128 v[4:7], v56 offset:38912\n\t" \
  "ds_read_b128 v[8:11], v56 offset:40960\n\t" \
  "ds_read_b128 v[12:15], v56 offset:43008\n\t" \
  "ds_read_b128 v[16:19], v56 offset:45056\n\t" \
  "s_mov_b32 s68, 0x50000\n\t" \
  "s_mov_b32 s69, 0x5550055\n\t" \
  "s_mov_b32 s70, 0xa0000\n\t" \
  "s_mov_b32 s71, 0xaaa00aa\n\t" \
  "s_mov_b32 s72, 0x150001\n\t" \
  "s_mov_b32 s73, 0x15550155\n\t" \
  "s_mov_b32 s74, 0x2a0002\n\t" \
  "s_mov_b32 s75, 0x2aaa02aa\n\t" \
  "s_mov_b32 s76, 0x55505555\n\t" \
  "s_mov_b32 s77, 0x50005500\n\t" \
  "s_mov_b32 s78, 0xaaa0aaaa\n\t" \
  "s_mov_b32 s79, 0xa000aa00\n\t" \
  "s_mov_b32 s80, 0x55405554\n\t" \
  "s_mov_b32 s81, 0x40005400\n\t" \
  "s_mov_b32 s82, 0xaa80aaa8\n\t" \
  "s_mov_b32 s83, 0x8000a800\n\t" \
  "s_mov_b32 s84, 0xaaaaaaaa\n\t" \
  "s_mov_b32 s85, 0xaaaaaaaa\n\t" \
  "s_waitcnt lgkmcnt(0)\n\t" \
  "s_branch N4a_%=\n\t" \
  "N0a_%=:\n\t" \
  "s_waitcnt lgkmcnt(4)\n\t" \
  "v_mfma_f32_16x16x32_bf16 v[160:163], v[40:43], v[0:3], v[160:163]\n\t" \
  "ds_read_b64_tr_b16 v[52:53], v59 offset:2048\n\t" \
  "v_mfma_f32_16x16x32_bf16 v[96:99], v[40:43], v[4:7], v[96:99]\n\t" \
  "ds_read_b64_tr_b16 v[54:55], v59 offset:2560\n\t" \
  "v_mfma_f32_16x16x32_bf16 v[104:107], v[40:43], v[8:11], v[104:107]\n\t" \
  "s_barrier\n\t" \
  "v_mfma_f32_16x16x32_bf16 v[112:115], v[40:43], v[12:15], v[112:115]\n\t" \
  "v_mfma_f32_16x16x32_bf16 v[120:123], v[40:43], v[16:19], v[120:123]\n\t" \
  "s_waitcnt lgkmcnt(4)\n\t" \
  "v_mfma_f32_16x16x32_bf16 v[128:131], v[4:7], v[44:47], v[128:131]\n\t" \
  "ds_read_b64_tr_b16 v[40:41], v58 offset:0\n\t" \
  "v_mfma_f32_16x16x32_bf16 v[136:139], v[8:11], v[44:47], v[136:139]\n\t" \
  "ds_read_b64_tr_b16 v[42:43], v58 offset:512\n\t" \
  "v_mfma_f32_16x16x32_bf16 v[144:147], v[12:15], v[44:47], v[144:147]\n\t" \
  "ds_read_b128 v[20:23], v57 offset:0\n\t" \
  "v_mfma_f32_16x16x32_bf16 v[152:155], v[16:19], v[44:47], v[152:155]\n\t" \
  "ds_read_b128 v[24:27], v57 offset:2048\n\t" \
  "s_waitcnt lgkmcnt(6)\n\t" \
  "v_mfma_f32_16x16x32_bf16 v[164:167], v[48:51], v[0:3], v[164:167]\n\t" \
  "ds_read_b64_tr_b16 v[44:45], v59 offset:0\n\t" \
  "v_mfma_f32_16x16x32_bf16 v[100:103], v[48:51], v[4:7], v[100:103]\n\t" \
  "ds_read_b64_tr_b16 v[46:47], v59 offset:512\n\t" \
  "v_mfma_f32_16x16x32_bf16 v[108:111], v[48:51], v[8:11], v[108:111]\n\t" \
  "ds_read_b128 v[28:31], v57 offset:4096\n\t" \
  "v_mfma_f32_16x16x32_bf16 v[116:119], v[48:51], v[12:15], v[116:119]\n\t" \
  "ds_read_b128 v[32:35], v57 offset:6144\n\t" \
  "v_mfma_f32_16x16x32_bf16 v[124:127], v[48:51], v[16:19], v[124:127]\n\t" \
  "s_waitcnt lgkmcnt(8)\n\t" \
  "v_mfma_f32_16x16x32_bf16 v[132:135], v[4:7], v[52:55], v[132:135]\n\t" \
  "ds_read_b64_tr_b16 v[48:49], v58 offset:13312\n\t" \
  "v_mfma_f32_16x16x32_bf16 v[140:143], v[8:11], v[52:55], v[140:143]\n\t" \
  "ds_read_b64_tr_b16 v[50:51], v58 offset:13824\n\t" \
  "v_mfma_f32_16x16x32_bf16 v[148:151], v[12:15], v[52:55], v[148:151]\n\t" \
  "ds_read_b128 v[36:39], v57 offset:8192\n\t" \
  "v_mfma_f32_16x16x32_bf16 v[156:159], v[16:19], v[52:55], v[156:159]\n\t" \
  "s_waitcnt lgkmcnt(9)\n\t" \
  "v_mfma_f32_16x16x32_bf16 v[64:67], v[40:43], v[4:7], v[64:67]\n\t" \
  "ds_read_b64_tr_b16 v[52:53], v58 offset:14336\n\t" \
  "v_mfma_f32_16x16x32_bf16 v[72:75], v[40:43], v[8:11], v[72:75]\n\t" \
  "ds_read_b64_tr_b16 v[54:55], v58 offset:14848\n\t" \
  "v_mfma_f32_16x16x32_bf16 v[80:83], v[40:43], v[12:15], v[80:83]\n\t" \
  "v_mfma_f32_16x16x32_bf16 v[88:91], v[40:43], v[16:19], v[88:91]\n\t" \
  "s_waitcnt lgkmcnt(7)\n\t" \
  "v_mfma_f32_16x16x32_bf16 v[68:71], v[44:47], v[4:7], v[68:71]\n\t" \
  "ds_read_b64_tr_b16 v[40:41], v59 offset:13312\n\t" \
  "v_mfma_f32_16x16x32_bf16 v[76:79], v[44:47], v[8:11], v[76:79]\n\t" \
  "ds_read_b64_tr_b16 v[42:43], v59 offset:13824\n\t" \
  "v_mfma_f32_16x16x32_bf16 v[84:87], v[44:47], v[12:15], v[84:87]\n\t" \
  "v_mfma_f32_16x16x32_bf16 v[92:95], v[44:47], v[16:19], v[92:95]\n\t" \
  "N1a_%=:\n\t" \
  "s_waitcnt lgkmcnt(4)\n\t" \
  "v_mfma_f32_16x16x32_bf16 v[160:163], v[48:51], v[20:23], v[160:163]\n\t" \
  "ds_read_b64_tr_b16 v[44:45], v59 offset:14336\n\t" \
  "v_mfma_f32_16x16x32_bf16 v[96:99], v[48:51], v[24:27], v[96:99]\n\t" \
  "ds_read_b64_tr_b16 v[46:47], v59 offset:14848\n\t" \
  "v_mfma_f32_16x16x32_bf16 v[104:107], v[48:51], v[28:31], v[104:107]\n\t" \
  "s_barrier\n\t" \
  "v_mfma_f32_16x16x32_bf16 v[112:115], v[48:51], v[32:35], v[112:115]\n\t" \
  "v_mfma_f32_16x16x32_bf16 v[120:123], v[48:51], v[36:39], v[120:123]\n\t" \
  "s_waitcnt lgkmcnt(4)\n\t" \
  "v_mfma_f32_16x16x32_bf16 v[128:131], v[24:27], v[52:55], v[128:131]\n\t" \
  "ds_read_b64_tr_b16 v[48:49], v58 offset:12288\n\t" \
  "v_mfma_f32_16x16x32_bf16 v[136:139], v[28:31], v[52:55], v[136:139]\n\t" \
  "ds_read_b64_tr_b16 v[50:51], v58 offset:12800\n\t" \
  "v_mfma_f32_16x16x32_bf16 v[144:147], v[32:35], v[52:55], v[144:147]\n\t" \
  "ds_read_b128 v[0:3], v56 offset:18432\n\t" \
  "v_mfma_f32_16x16x32_bf16 v[152:155], v[36:39], v[52:55], v[152:155]\n\t" \
  "ds_read_b128 v[4:7], v56 offset:20480\n\t" \
  "s_waitcnt lgkmcnt(6)\n\t" \
  "v_mfma_f32_16x16x32_bf16 v[164:167], v[40:43], v[20:23], v[164:167]\n\t" \
  "ds_read_b64_tr_b16 v[52:53], v59 offset:12288\n\t" \
  "v_mfma_f32_16x16x32_bf16 v[100:103], v[40:43], v[24:27], v[100:103]\n\t" \
  "ds_read_b64_tr_b16 v[54:55], v59 offset:12800\n\t" \
  "v_mfma_f32_16x16x32_bf16 v[108:111], v[40:43], v[28:31], v[108:111]\n\t" \
  "ds_read_b128 v[8:11], v56 offset:22528\n\t" \
  "v_mfma_f32_16x16x32_bf16 v[116:119], v[40:43], v[32:35], v[116:119]\n\t" \
  "ds_read_b128 v[12:15], v56 offset:24576\n\t" \
  "v_mfma_f32_16x16x32_bf16 v[124:127], v[40:43], v[36:39], v[124:127]\n\t" \
  "s_waitcnt lgkmcnt(8)\n\t" \
  "v_mfma_f32_16x16x32_bf16 v[132:135], v[24:27], v[44:47], v[132:135]\n\t" \
  "ds_read_b64_tr_b16 v[40:41], v58 offset:25600\n\t" \
  "v_mfma_f32_16x16x32_bf16 v[140:143], v[28:31], v[44:47], v[140:143]\n\t" \
  "ds_read_b64_tr_b16 v[42:43], v58 offset:26112\n\t" \
  "v_mfma_f32_16x16x32_bf16 v[148:151], v[32:35], v[44:47], v[148:151]\n\t" \
  "ds_read_b128 v[16:19], v56 offset:26624\n\t" \
  "v_mfma_f32_16x16x32_bf16 v[156:159], v[36:39], v[44:47], v[156:159]\n\t" \
  "s_waitcnt lgkmcnt(9)\n\t" \
  "v_mfma_f32_16x16x32_bf16 v[64:67], v[48:51], v[24:27], v[64:67]\n\t" \
  "ds_read_b64_tr_b16 v[44:45], v58 offset:26624\n\t" \
  "v_mfma_f32_16x16x32_bf16 v[72:75], v[48:51], v[28:31], v[72:75]\n\t" \
  "ds_read_b64_tr_b16 v[46:47], v58 offset:27136\n\t" \
  "v_mfma_f32_16x16x32_bf16 v[80:83], v[48:51], v[32:35], v[80:83]\n\t" \
  "v_mfma_f32_16x16x32_bf16 v[88:91], v[48:51], v[36:39], v[88:91]\n\t" \
  "s_waitcnt lgkmcnt(7)\n\t" \
  "v_mfma_f32_16x16x32_bf16 v[68:71], v[52:55], v[24:27], v[68:71]\n\t" \
  "ds_read_b64_tr_b16 v[48:49], v59 offset:25600\n\t" \
  "v_mfma_f32_16x16x32_bf16 v[76:79], v[52:55], v[28:31], v[76:79]\n\t" \
  "ds_read_b64_tr_b16 v[50:51], v59 offset:26112\n\t" \
  "v_mfma_f32_16x16x32_bf16 v[84:87], v[52:55], v[32:35], v[84:87]\n\t" \
  "v_mfma_f32_16x16x32_bf16 v[92:95], v[52:55], v[36:39], v[92:95]\n\t" \
  "N2a_%=:\n\t" \
  "s_waitcnt lgkmcnt(4)\n\t" \
  "v_mfma_f32_16x16x32_bf16 v[160:163], v[40:43], v[0:3], v[160:163]\n\t" \
  "ds_read_b64_tr_b16 v[52:53], v59 offset:26624\n\t" \
  "v_mfma_f32_16x16x32_bf16 v[96:99], v[40:43], v[4:7], v[96:99]\n\t" \
  "ds_read_b64_tr_b16 v[54:55], v59 offset:27136\n\t" \
  "v_mfma_f32_16x16x32_bf16 v[104:107], v[40:43], v[8:11], v[104:107]\n\t" \
  "s_barrier\n\t" \
  "v_mfma_f32_16x16x32_bf16 v[112:115], v[40:43], v[12:15], v[112:115]\n\t" \
  "v_mfma_f32_16x16x32_bf16 v[120:123], v[40:43], v[16:19], v[120:123]\n\t" \
  "s_waitcnt lgkmcnt(4)\n\t" \
  "v_mfma_f32_16x16x32_bf16 v[128:131], v[4:7], v[44:47], v[128:131]\n\t" \
  "ds_read_b64_tr_b16 v[40:41], v58 offset:24576\n\t" \
  "v_mfma_f32_16x16x32_bf16 v[136:139], v[8:11], v[44:47], v[136:139]\n\t" \
  "ds_read_b64_tr_b16 v[42:43], v58 offset:25088\n\t" \
  "v_mfma_f32_16x16x32_bf16 v[144:147], v[12:15], v[44:47], v[144:147]\n\t" \
  "ds_read_b128 v[20:23], v57 offset:18432\n\t" \
  "v_mfma_f32_16x16x32_bf16 v[152:155], v[16:19], v[44:47], v[152:155]\n\t" \
  "ds_read_b128 v[24:27], v57 offset:20480\n\t" \
  "s_waitcnt lgkmcnt(6)\n\t" \
  "v_mfma_f32_16x16x32_bf16 v[164:167], v[48:51], v[0:3], v[164:167]\n\t" \
  "ds_read_b64_tr_b16 v[44:45], v59 offset:24576\n\t" \
  "v_mfma_f32_16x16x32_bf16 v[100:103], v[48:51], v[4:7], v[100:103]\n\t" \
  "ds_read_b64_tr_b16 v[46:47], v59 offset:25088\n\t" \
  "v_mfma_f32_16x16x32_bf16 v[108:111], v[48:51], v[8:11], v[108:111]\n\t" \
  "ds_read_b128 v[28:31], v57 offset:22528\n\t" \
  "v_mfma_f32_16x16x32_bf16 v[116:119], v[48:51], v[12:15], v[116:119]\n\t" \
  "ds_read_b128 v[32:35], v57 offset:24576\n\t" \
  "v_mfma_f32_16x16x32_bf16 v[124:127], v[48:51], v[16:19], v[124:127]\n\t" \
  "s_waitcnt lgkmcnt(8)\n\t" \
  "v_mfma_f32_16x16x32_bf16 v[132:135], v[4:7], v[52:55], v[132:135]\n\t" \
  "ds_read_b64_tr_b16 v[48:49], v60 offset:1024\n\t" \
  "v_mfma_f32_16x16x32_bf16 v[140:143], v[8:11], v[52:55], v[140:143]\n\t" \
  "ds_read_b64_tr_b16 v[50:51], v60 offset:1536\n\t" \
  "v_mfma_f32_16x16x32_bf16 v[148:151], v[12:15], v[52:55], v[148:151]\n\t" \
  "ds_read_b128 v[36:39], v57 offset:26624\n\t" \
  "v_mfma_f32_16x16x32_bf16 v[156:159], v[16:19], v[52:55], v[156:159]\n\t" \
  "s_waitcnt lgkmcnt(9)\n\t" \
  "v_mfma_f32_16x16x32_bf16 v[64:67], v[40:43], v[4:7], v[64:67]\n\t" \
  "ds_read_b64_tr_b16 v[52:53], v60 offset:2048\n\t" \
  "v_mfma_f32_16x16x32_bf16 v[72:75], v[40:43], v[8:11], v[72:75]\n\t" \
  "ds_read_b64_tr_b16 v[54:55], v60 offset:2560\n\t" \
  "v_mfma_f32_16x16x32_bf16 v[80:83], v[40:43], v[12:15], v[80:83]\n\t" \
  "v_mfma_f32_16x16x32_bf16 v[88:91], v[40:43], v[16:19], v[88:91]\n\t" \
  "s_waitcnt lgkmcnt(7)\n\t" \
  "v_mfma_f32_16x16x32_bf16 v[68:71], v[44:47], v[4:7], v[68:71]\n\t" \
  "ds_read_b64_tr_b16 v[40:41], v61 offset:1024\n\t" \
  "v_mfma_f32_16x16x32_bf16 v[76:79], v[44:47], v[8:11], v[76:79]\n\t" \
  "ds_read_b64_tr_b16 v[42:43], v61 offset:1536\n\t" \
  "v_mfma_f32_16x16x32_bf16 v[84:87], v[44:47], v[12:15], v[84:87]\n\t" \
  "v_mfma_f32_16x16x32_bf16 v[92:95], v[44:47], v[16:19], v[92:95]\n\t" \
  "N3a_%=:\n\t" \
  "s_waitcnt lgkmcnt(4)\n\t" \
  "v_mfma_f32_16x16x32_bf16 v[160:163], v[48:51], v[20:23], v[160:163]\n\t" \
  "ds_read_b64_tr_b16 v[44:45], v61 offset:2048\n\t" \
  "v_mfma_f32_16x16x32_bf16 v[96:99], v[48:51], v[24:27], v[96:99]\n\t" \
  "ds_read_b64_tr_b16 v[46:47], v61 offset:2560\n\t" \
  "v_mfma_f32_16x16x32_bf16 v[104:107], v[48:51], v[28:31], v[104:107]\n\t" \
  "s_barrier\n\t" \
  "v_mfma_f32_16x16x32_bf16 v[112:115], v[48:51], v[32:35], v[112:115]\n\t" \
  "v_mfma_f32_16x16x32_bf16 v[120:123], v[48:51], v[36:39], v[120:123]\n\t" \
  "s_waitcnt lgkmcnt(4)\n\t" \
  "v_mfma_f32_16x16x32_bf16 v[128:131], v[24:27], v[52:55], v[128:131]\n\t" \
  "ds_read_b64_tr_b16 v[48:49], v60 offset:0\n\t" \
  "v_mfma_f32_16x16x32_bf16 v[136:139], v[28:31], v[52:55], v[136:139]\n\t" \
  "ds_read_b64_tr_b16 v[50:51], v60 offset:512\n\t" \
  "v_mfma_f32_16x16x32_bf16 v[144:147], v[32:35], v[52:55], v[144:147]\n\t" \
  "ds_read_b128 v[0:3], v56 offset:36864\n\t" \
  "v_mfma_f32_16x16x32_bf16 v[152:155], v[36:39], v[52:55], v[152:155]\n\t" \
  "ds_read_b128 v[4:7], v56 offset:38912\n\t" \
  "s_waitcnt lgkmcnt(6)\n\t" \
  "v_mfma_f32_16x16x32_bf16 v[164:167], v[40:43], v[20:23], v[164:167]\n\t" \
  "ds_read_b64_tr_b16 v[52:53], v61 offset:0\n\t" \
  "v_mfma_f32_16x16x32_bf16 v[100:103], v[40:43], v[24:27], v[100:103]\n\t" \
  "ds_read_b64_tr_b16 v[54:55], v61 offset:512\n\t" \
  "v_mfma_f32_16x16x32_bf16 v[108:111], v[40:43], v[28:31], v[108:111]\n\t" \
  "ds_read_b128 v[8:11], v56 offset:40960\n\t" \
  "v_mfma_f32_16x16x32_bf16 v[116:119], v[40:43], v[32:35], v[116:119]\n\t" \
  "ds_read_b128 v[12:15], v56 offset:43008\n\t" \
  "v_mfma_f32_16x16x32_bf16 v[124:127], v[40:43], v[36:39], v[124:127]\n\t" \
  "s_waitcnt lgkmcnt(8)\n\t" \
  "v_mfma_f32_16x16x32_bf16 v[132:135], v[24:27], v[44:47], v[132:135]\n\t" \
  "ds_read_b64_tr_b16 v[40:41], v60 offset:13312\n\t" \
  "v_mfma_f32_16x16x32_bf16 v[140:143], v[28:31], v[44:47], v[140:143]\n\t" \
  "ds_read_b64_tr_b16 v[42:43], v60 offset:13824\n\t" \
  "v_mfma_f32_16x16x32_bf16 v[148:151], v[32:35], v[44:47], v[148:151]\n\t" \
  "ds_read_b128 v[16:19], v56 offset:45056\n\t" \
  "v_mfma_f32_16x16x32_bf16 v[156:159], v[36:39], v[44:47], v[156:159]\n\t" \
  "s_waitcnt lgkmcnt(9)\n\t" \
  "v_mfma_f32_16x16x32_bf16 v[64:67], v[48:51], v[24:27], v[64:67]\n\t" \
  "ds_read_b64_tr_b16 v[44:45], v60 offset:14336\n\t" \
  "v_mfma_f32_16x16x32_bf16 v[72:75], v[48:51], v[28:31], v[72:75]\n\t" \
  "ds_read_b64_tr_b16 v[46:47], v60 offset:14848\n\t" \
  "v_mfma_f32_16x16x32_bf16 v[80:83], v[48:51], v[32:35], v[80:83]\n\t" \
  "v_mfma_f32_16x16x32_bf16 v[88:91], v[48:51], v[36:39], v[88:91]\n\t" \
  "s_waitcnt lgkmcnt(7)\n\t" \
  "v_mfma_f32_16x16x32_bf16 v[68:71], v[52:55], v[24:27], v[68:71]\n\t" \
  "ds_read_b64_tr_b16 v[48:49], v61 offset:13312\n\t" \
  "v_mfma_f32_16x16x32_bf16 v[76:79], v[52:55], v[28:31], v[76:79]\n\t" \
  "ds_read_b64_tr_b16 v[50:51], v61 offset:13824\n\t" \
  "v_mfma_f32_16x16x32_bf16 v[84:87], v[52:55], v[32:35], v[84:87]\n\t" \
  "v_mfma_f32_16x16x32_bf16 v[92:95], v[52:55], v[36:39], v[92:95]\n\t" \
  "N4a_%=:\n\t" \
  "s_waitcnt lgkmcnt(4)\n\t" \
  "v_mfma_f32_16x16x32_bf16 v[160:163], v[40:43], v[0:3], v[160:163]\n\t" \
  "ds_read_b64_tr_b16 v[52:53], v61 offset:14336\n\t" \
  "v_mfma_f32_16x16x32_bf16 v[96:99], v[40:43], v[4:7], v[96:99]\n\t" \
  "ds_read_b64_tr_b16 v[54:55], v61 offset:14848\n\t" \
  "v_mfma_f32_16x16x32_bf16 v[104:107], v[40:43], v[8:11], v[104:107]\n\t" \
  "s_barrier\n\t" \
  "v_mfma_f32_16x16x32_bf16 v[112:115], v[40:43], v[12:15], v[112:115]\n\t" \
  "v_mfma_f32_16x16x32_bf16 v[120:123], v[40:43], v[16:19], v[120:123]\n\t" \
  "s_waitcnt lgkmcnt(4)\n\t" \
  "v_mfma_f32_16x16x32_bf16 v[128:131], v[4:7], v[44:47], v[128:131]\n\t" \
  "ds_read_b64_tr_b16 v[40:41], v60 offset:12288\n\t" \
  "v_mfma_f32_16x16x32_bf16 v[136:139], v[8:11], v[44:47], v[136:139]\n\t" \
  "ds_read_b64_tr_b16 v[42:43], v60 offset:12800\n\t" \
  "v_mfma_f32_16x16x32_bf16 v[144:147], v[12:15], v[44:47], v[144:147]\n\t" \
  "ds_read_b128 v[20:23], v57 offset:36864\n\t" \
  "v_mfma_f32_16x16x32_bf16 v[152:155], v[16:19], v[44:47], v[152:155]\n\t" \
  "ds_read_b128 v[24:27], v57 offset:38912\n\t" \
  "s_waitcnt lgkmcnt(6)\n\t" \
  "v_mfma_f32_16x16x32_bf16 v[164:167], v[48:51], v[0:3], v[164:167]\n\t" \
  "ds_read_b64_tr_b16 v[44:45], v61 offset:12288\n\t" \
  "v_mfma_f32_16x16x32_bf16 v[100:103], v[48:51], v[4:7], v[100:103]\n\t" \
  "ds_read_b64_tr_b16 v[46:47], v61 offset:12800\n\t" \
  "v_mfma_f32_16x16x32_bf16 v[108:111], v[48:51], v[8:11], v[108:111]\n\t" \
  "ds_read_b128 v[28:31], v57 offset:40960\n\t" \
  "v_mfma_f32_16x16x32_bf16 v[116:119], v[48:51], v[12:15], v[116:119]\n\t" \
  "ds_read_b128 v[32:35], v57 offset:43008\n\t" \
  "v_mfma_f32_16x16x32_bf16 v[124:127], v[48:51], v[16:19], v[124:127]\n\t" \
  "s_waitcnt lgkmcnt(8)\n\t" \
  "v_mfma_f32_16x16x32_bf16 v[132:135], v[4:7], v[52:55], v[132:135]\n\t" \
  "ds_read_b64_tr_b16 v[48:49], v60 offset:25600\n\t" \
  "v_mfma_f32_16x16x32_bf16 v[140:143], v[8:11], v[52:55], v[140:143]\n\t" \
  "ds_read_b64_tr_b16 v[50:51], v60 offset:26112\n\t" \
  "v_mfma_f32_16x16x32_bf16 v[148:151], v[12:15], v[52:55], v[148:151]\n\t" \
  "ds_read_b128 v[36:39], v57 offset:45056\n\t" \
  "v_mfma_f32_16x16x32_bf16 v[156:159], v[16:19], v[52:55], v[156:159]\n\t" \
  "s_waitcnt lgkmcnt(9)\n\t" \
  "v_mfma_f32_16x16x32_bf16 v[64:67], v[40:43], v[4:7], v[64:67]\n\t" \
  "ds_read_b64_tr_b16 v[52:53], v60 offset:26624\n\t" \
  "v_mfma_f32_16x16x32_bf16 v[72:75], v[40:43], v[8:11], v[72:75]\n\t" \
  "ds_read_b64_tr_b16 v[54:55], v60 offset:27136\n\t" \
  "v_mfma_f32_16x16x32_bf16 v[80:83], v[40:43], v[12:15], v[80:83]\n\t" \
  "v_mfma_f32_16x16x32_bf16 v[88:91], v[40:43], v[16:19], v[88:91]\n\t" \
  "s_waitcnt lgkmcnt(7)\n\t" \
  "v_mfma_f32_16x16x32_bf16 v[68:71], v[44:47], v[4:7], v[68:71]\n\t" \
  "ds_read_b64_tr_b16 v[40:41], v61 offset:25600\n\t" \
  "v_mfma_f32_16x16x32_bf16 v[76:79], v[44:47], v[8:11], v[76:79]\n\t" \
  "ds_read_b64_tr_b16 v[42:43], v61 offset:26112\n\t" \
  "v_mfma_f32_16x16x32_bf16 v[84:87], v[44:47], v[12:15], v[84:87]\n\t" \
  "v_mfma_f32_16x16x32_bf16 v[92:95], v[44:47], v[16:19], v[92:95]\n\t" \
  "N5a_%=:\n\t" \
  "s_waitcnt lgkmcnt(4)\n\t" \
  "v_mfma_f32_16x16x32_bf16 v[160:163], v[48:51], v[20:23], v[160:163]\n\t" \
  "ds_read_b64_tr_b16 v[44:45], v61 offset:26624\n\t" \
  "v_mfma_f32_16x16x32_bf16 v[96:99], v[48:51], v[24:27], v[96:99]\n\t" \
  "ds_read_b64_tr_b16 v[46:47], v61 offset:27136\n\t" \
  "v_mfma_f32_16x16x32_bf16 v[104:107], v[48:51], v[28:31], v[104:107]\n\t" \
  "s_barrier\n\t" \
  "v_mfma_f32_16x16x32_bf16 v[112:115], v[48:51], v[32:35], v[112:115]\n\t" \
  "v_mfma_f32_16x16x32_bf16 v[120:123], v[48:51], v[36:39], v[120:123]\n\t" \
  "s_waitcnt lgkmcnt(4)\n\t" \
  "v_mfma_f32_16x16x32_bf16 v[128:131], v[24:27], v[52:55], v[128:131]\n\t" \
  "ds_read_b64_tr_b16 v[48:49], v60 offset:24576\n\t" \
  "v_mfma_f32_16x16x32_bf16 v[136:139], v[28:31], v[52:55], v[136:139]\n\t" \
  "ds_read_b64_tr_b16 v[50:51], v60 offset:25088\n\t" \
  "v_mfma_f32_16x16x32_bf16 v[144:147], v[32:35], v[52:55], v[144:147]\n\t" \
  "ds_read_b128 v[0:3], v56 offset:0\n\t" \
  "v_mfma_f32_16x16x32_bf16 v[152:155], v[36:39], v[52:55], v[152:155]\n\t" \
  "ds_read_b128 v[4:7], v56 offset:2048\n\t" \
  "s_waitcnt lgkmcnt(6)\n\t" \
  "v_mfma_f32_16x16x32_bf16 v[164:167], v[40:43], v[20:23], v[164:167]\n\t" \
  "ds_read_b64_tr_b16 v[52:53], v61 offset:24576\n\t" \
  "v_mfma_f32_16x16x32_bf16 v[100:103], v[40:43], v[24:27], v[100:103]\n\t" \
  "ds_read_b64_tr_b16 v[54:55], v61 offset:25088\n\t" \
  "v_mfma_f32_16x16x32_bf16 v[108:111], v[40:43], v[28:31], v[108:111]\n\t" \
  "ds_read_b128 v[8:11], v56 offset:4096\n\t" \
  "v_mfma_f32_16x16x32_bf16 v[116:119], v[40:43], v[32:35], v[116:119]\n\t" \
  "ds_read_b128 v[12:15], v56 offset:6144\n\t" \
  "v_mfma_f32_16x16x32_bf16 v[124:127], v[40:43], v[36:39], v[124:127]\n\t" \
  "s_waitcnt lgkmcnt(8)\n\t" \
  "v_mfma_f32_16x16x32_bf16 v[132:135], v[24:27], v[44:47], v[132:135]\n\t" \
  "ds_read_b64_tr_b16 v[40:41], v58 offset:1024\n\t" \
  "v_mfma_f32_16x16x32_bf16 v[140:143], v[28:31], v[44:47], v[140:143]\n\t" \
  "ds_read_b64_tr_b16 v[42:43], v58 offset:1536\n\t" \
  "v_mfma_f32_16x16x32_bf16 v[148:151], v[32:35], v[44:47], v[148:151]\n\t" \
  "ds_read_b128 v[16:19], v56 offset:8192\n\t" \
  "v_mfma_f32_16x16x32_bf16 v[156:159], v[36:39], v[44:47], v[156:159]\n\t" \
  "s_waitcnt lgkmcnt(9)\n\t" \
  "v_mfma_f32_16x16x32_bf16 v[64:67], v[48:51], v[24:27], v[64:67]\n\t" \
  "ds_read_b64_tr_b16 v[44:45], v58 offset:2048\n\t" \
  "v_mfma_f32_16x16x32_bf16 v[72:75], v[48:51], v[28:31], v[72:75]\n\t" \
  "ds_read_b64_tr_b16 v[46:47], v58 offset:2560\n\t" \
  "v_mfma_f32_16x16x32_bf16 v[80:83], v[48:51], v[32:35], v[80:83]\n\t" \
  "v_mfma_f32_16x16x32_bf16 v[88:91], v[48:51], v[36:39], v[88:91]\n\t" \
  "s_waitcnt lgkmcnt(7)\n\t" \
  "v_mfma_f32_16x16x32_bf16 v[68:71], v[52:55], v[24:27], v[68:71]\n\t" \
  "ds_read_b64_tr_b16 v[48:49], v59 offset:1024\n\t" \
  "v_mfma_f32_16x16x32_bf16 v[76:79], v[52:55], v[28:31], v[76:79]\n\t" \
  "ds_read_b64_tr_b16 v[50:51], v59 offset:1536\n\t" \
  "s_addk_i32 %[it], 1\n\t" \
  "v_mfma_f32_16x16x32_bf16 v[84:87], v[52:55], v[32:35], v[84:87]\n\t" \
  "s_cmp_lt_u32 %[it], 6\n\t" \
  "v_mfma_f32_16x16x32_bf16 v[92:95], v[52:55], v[36:39], v[92:95]\n\t" \
  "s_cbranch_scc1 N0a_%=\n\t" \
  "s_setprio 3\n\t" \
  "v_cvt_pk_bf16_f32 v120, v120, v121\n\t" \
  "v_cvt_pk_bf16_f32 v121, v122, v123\n\t" \
  "v_cvt_pk_bf16_f32 v122, v124, v125\n\t" \
  "v_cvt_pk_bf16_f32 v123, v126, v127\n\t" \
  "v_cvt_pk_bf16_f32 v28, v152, v153\n\t" \
  "v_cvt_pk_bf16_f32 v29, v154, v155\n\t" \
  "v_cvt_pk_bf16_f32 v38, v156, v157\n\t" \
  "v_cvt_pk_bf16_f32 v39, v158, v159\n\t" \
  "ds_write_b128 v63, v[120:123]\n\t" \
  "ds_write2_b64 v63, v[28:29], v[38:39] offset0:128 offset1:129\n\t" \
  "v_mov_b32 v57, %[fa]\n\t" \
  "v_mov_b32 v58, 1\n\t" \
  "s_mov_b64 exec, 1\n\t" \
  "ds_write_b32 v57, v58\n\t" \
  "s_mov_b64 exec, -1\n\t" \
  "v_cvt_pk_bf16_f32 v160, v160, v161\n\t" \
  "v_cvt_pk_bf16_f32 v161, v162, v163\n\t" \
  "v_cvt_pk_bf16_f32 v162, v164, v165\n\t" \
  "v_cvt_pk_bf16_f32 v163, v166, v167\n\t" \
  "s_branch NX_%=\n\t" \
  "NB_%=:\n\t" \
  "ds_read_b64_tr_b16 v[40:41], v60 offset:14336\n\t" \
  "ds_read_b64_tr_b16 v[42:43], v60 offset:14848\n\t" \
  "ds_read_b64_tr_b16 v[44:45], v60 offset:13312\n\t" \
  "ds_read_b64_tr_b16 v[46:47], v60 offset:13824\n\t" \
  "ds_read_b64_tr_b16 v[48:49], v61 offset:14336\n\t" \
  "ds_read_b64_tr_b16 v[50:51], v61 offset:14848\n\t" \
  "ds_read_b128 v[0:3], v56 offset:36864\n\t" \
  "ds_read_b128 v[4:7], v56 offset:47104\n\t" \
  "ds_read_b128 v[8:11], v56 offset:49152\n\t" \
  "ds_read_b128 v[12:15], v56 offset:51200\n\t" \
  "ds_read_b128 v[16:19], v56 offset:53248\n\t" \
  "s_mov_b32 s68, 0x50000\n\t" \
  "s_mov_b32 s69, 0x5550055\n\t" \
  "s_mov_b32 s70, 0xa0000\n\t" \
  "s_mov_b32 s71, 0xaaa00aa\n\t" \
  "s_mov_b32 s72, 0x150001\n\t" \
  "s_mov_b32 s73, 0x15550155\n\t" \
  "s_mov_b32 s74, 0x2a0002\n\t" \
  "s_mov_b32 s75, 0x2aaa02aa\n\t" \
  "s_mov_b32 s76, 0x55505555\n\t" \
  "s_mov_b32 s77, 0x50005500\n\t" \
  "s_mov_b32 s78, 0xaaa0aaaa\n\t" \
  "s_mov_b32 s79, 0xa000aa00\n\t" \
  "s_mov_b32 s80, 0x55405554\n\t" \
  "s_mov_b32 s81, 0x40005400\n\t" \
  "s_mov_b32 s82, 0xaa80aaa8\n\t" \
  "s_mov_b32 s83, 0x8000a800\n\t" \
  "s_mov_b32 s84, 0xaaaaaaaa\n\t" \
  "s_mov_b32 s85, 0xaaaaaaaa\n\t" \
  "s_waitcnt lgkmcnt(0)\n\t" \
  "s_branch N4b_%=\n\t" \
  "N0b_%=:\n\t" \
  "s_waitcnt lgkmcnt(4)\n\t" \
  "v_mfma_f32_16x16x32_bf16 v[160:163], v[0:3], v[40:43], v[160:163]\n\t" \
  "ds_read_b64_tr_b16 v[52:53], v59 offset:1024\n\t" \
  "v_mfma_f32_16x16x32_bf16 v[128:131], v[4:7], v[40:43], v[128:131]\n\t" \
  "ds_read_b64_tr_b16 v[54:55], v59 offset:1536\n\t" \
  "v_mfma_f32_16x16x32_bf16 v[136:139], v[8:11], v[40:43], v[136:139]\n\t" \
  "s_barrier\n\t" \
  "v_mfma_f32_16x16x32_bf16 v[144:147], v[12:15], v[40:43], v[144:147]\n\t" \
  "v_mfma_f32_16x16x32_bf16 v[152:155], v[16:19], v[40:43], v[152:155]\n\t" \
  "s_waitcnt lgkmcnt(4)\n\t" \
  "v_mfma_f32_16x16x32_bf16 v[96:99], v[44:47], v[4:7], v[96:99]\n\t" \
  "ds_read_b64_tr_b16 v[40:41], v58 offset:0\n\t" \
  "v_mfma_f32_16x16x32_bf16 v[104:107], v[44:47], v[8:11], v[104:107]\n\t" \
  "ds_read_b64_tr_b16 v[42:43], v58 offset:512\n\t" \
  "v_mfma_f32_16x16x32_bf16 v[112:115], v[44:47], v[12:15], v[112:115]\n\t" \
  "ds_read_b128 v[20:23], v57 offset:0\n\t" \
  "v_mfma_f32_16x16x32_bf16 v[120:123], v[44:47], v[16:19], v[120:123]\n\t" \
  "ds_read_b128 v[24:27], v57 offset:10240\n\t" \
  "s_waitcnt lgkmcnt(6)\n\t" \
  "v_mfma_f32_16x16x32_bf16 v[164:167], v[0:3], v[48:51], v[164:167]\n\t" \
  "ds_read_b64_tr_b16 v[44:45], v59 offset:0\n\t" \
  "v_mfma_f32_16x16x32_bf16 v[132:135], v[4:7], v[48:51], v[132:135]\n\t" \
  "ds_read_b64_tr_b16 v[46:47], v59 offset:512\n\t" \
  "v_mfma_f32_16x16x32_bf16 v[140:143], v[8:11], v[48:51], v[140:143]\n\t" \
  "ds_read_b128 v[28:31], v57 offset:12288\n\t" \
  "v_mfma_f32_16x16x32_bf16 v[148:151], v[12:15], v[48:51], v[148:151]\n\t" \
  "ds_read_b128 v[32:35], v57 offset:14336\n\t" \
  "v_mfma_f32_16x16x32_bf16 v[156:159], v[16:19], v[48:51], v[156:159]\n\t" \
  "s_waitcnt lgkmcnt(8)\n\t" \
  "v_mfma_f32_16x16x32_bf16 v[100:103], v[52:55], v[4:7], v[100:103]\n\t" \
  "ds_read_b64_tr_b16 v[48:49], v58 offset:14336\n\t" \
  "v_mfma_f32_16x16x32_bf16 v[108:111], v[52:55], v[8:11], v[108:111]\n\t" \
  "ds_read_b64_tr_b16 v[50:51], v58 offset:14848\n\t" \
  "v_mfma_f32_16x16x32_bf16 v[116:119], v[52:55], v[12:15], v[116:119]\n\t" \
  "ds_read_b128 v[36:39], v57 offset:16384\n\t" \
  "v_mfma_f32_16x16x32_bf16 v[124:127], v[52:55], v[16:19], v[124:127]\n\t" \
  "s_waitcnt lgkmcnt(9)\n\t" \
  "v_mfma_f32_16x16x32_bf16 v[64:67], v[40:43], v[4:7], v[64:67]\n\t" \
  "ds_read_b64_tr_b16 v[52:53], v58 offset:13312\n\t" \
  "v_mfma_f32_16x16x32_bf16 v[72:75], v[40:43], v[8:11], v[72:75]\n\t" \
  "ds_read_b64_tr_b16 v[54:55], v58 offset:13824\n\t" \
  "v_mfma_f32_16x16x32_bf16 v[80:83], v[40:43], v[12:15], v[80:83]\n\t" \
  "v_mfma_f32_16x16x32_bf16 v[88:91], v[40:43], v[16:19], v[88:91]\n\t" \
  "s_waitcnt lgkmcnt(7)\n\t" \
  "v_mfma_f32_16x16x32_bf16 v[68:71], v[44:47], v[4:7], v[68:71]\n\t" \
  "ds_read_b64_tr_b16 v[40:41], v59 offset:14336\n\t" \
  "v_mfma_f32_16x16x32_bf16 v[76:79], v[44:47], v[8:11], v[76:79]\n\t" \
  "ds_read_b64_tr_b16 v[42:43], v59 offset:14848\n\t" \
  "v_mfma_f32_16x16x32_bf16 v[84:87], v[44:47], v[12:15], v[84:87]\n\t" \
  "v_mfma_f32_16x16x32_bf16 v[92:95], v[44:47], v[16:19], v[92:95]\n\t" \
  "N1b_%=:\n\t" \
  "s_waitcnt lgkmcnt(4)\n\t" \
  "v_mfma_f32_16x16x32_bf16 v[160:163], v[20:23], v[48:51], v[160:163]\n\t" \
  "ds_read_b64_tr_b16 v[44:45], v59 offset:13312\n\t" \
  "v_mfma_f32_16x16x32_bf16 v[128:131], v[24:27], v[48:51], v[128:131]\n\t" \
  "ds_read_b64_tr_b16 v[46:47], v59 offset:13824\n\t" \
  "v_mfma_f32_16x16x32_bf16 v[136:139], v[28:31], v[48:51], v[136:139]\n\t" \
  "s_barrier\n\t" \
  "v_mfma_f32_16x16x32_bf16 v[144:147], v[32:35], v[48:51], v[144:147]\n\t" \
  "v_mfma_f32_16x16x32_bf16 v[152:155], v[36:39], v[48:51], v[152:155]\n\t" \
  "s_waitcnt lgkmcnt(4)\n\t" \
  "v_mfma_f32_16x16x32_bf16 v[96:99], v[52:55], v[24:27], v[96:99]\n\t" \
  "ds_read_b64_tr_b16 v[48:49], v58 offset:12288\n\t" \
  "v_mfma_f32_16x16x32_bf16 v[104:107], v[52:55], v[28:31], v[104:107]\n\t" \
  "ds_read_b64_tr_b16 v[50:51], v58 offset:12800\n\t" \
  "v_mfma_f32_16x16x32_bf16 v[112:115], v[52:55], v[32:35], v[112:115]\n\t" \
  "ds_read_b128 v[0:3], v56 offset:18432\n\t" \
  "v_mfma_f32_16x16x32_bf16 v[120:123], v[52:55], v[36:39], v[120:123]\n\t" \
  "ds_read_b128 v[4:7], v56 offset:28672\n\t" \
  "s_waitcnt lgkmcnt(6)\n\t" \
  "v_mfma_f32_16x16x32_bf16 v[164:167], v[20:23], v[40:43], v[164:167]\n\t" \
  "ds_read_b64_tr_b16 v[52:53], v59 offset:12288\n\t" \
  "v_mfma_f32_16x16x32_bf16 v[132:135], v[24:27], v[40:43], v[132:135]\n\t" \
  "ds_read_b64_tr_b16 v[54:55], v59 offset:12800\n\t" \
  "v_mfma_f32_16x16x32_bf16 v[140:143], v[28:31], v[40:43], v[140:143]\n\t" \
  "ds_read_b128 v[8:11], v56 offset:30720\n\t" \
  "v_mfma_f32_16x16x32_bf16 v[148:151], v[32:35], v[40:43], v[148:151]\n\t" \
  "ds_read_b128 v[12:15], v56 offset:32768\n\t" \
  "v_mfma_f32_16x16x32_bf16 v[156:159], v[36:39], v[40:43], v[156:159]\n\t" \
  "s_waitcnt lgkmcnt(8)\n\t" \
  "v_mfma_f32_16x16x32_bf16 v[100:103], v[44:47], v[24:27], v[100:103]\n\t" \
  "ds_read_b64_tr_b16 v[40:41], v58 offset:26624\n\t" \
  "v_mfma_f32_16x16x32_bf16 v[108:111], v[44:47], v[28:31], v[108:111]\n\t" \
  "ds_read_b64_tr_b16 v[42:43], v58 offset:27136\n\t" \
  "v_mfma_f32_16x16x32_bf16 v[116:119], v[44:47], v[32:35], v[116:119]\n\t" \
  "ds_read_b128 v[16:19], v56 offset:34816\n\t" \
  "v_mfma_f32_16x16x32_bf16 v[124:127], v[44:47], v[36:39], v[124:127]\n\t" \
  "s_waitcnt lgkmcnt(9)\n\t" \
  "v_mfma_f32_16x16x32_bf16 v[64:67], v[48:51], v[24:27], v[64:67]\n\t" \
  "ds_read_b64_tr_b16 v[44:45], v58 offset:25600\n\t" \
  "v_mfma_f32_16x16x32_bf16 v[72:75], v[48:51], v[28:31], v[72:75]\n\t" \
  "ds_read_b64_tr_b16 v[46:47], v58 offset:26112\n\t" \
  "v_mfma_f32_16x16x32_bf16 v[80:83], v[48:51], v[32:35], v[80:83]\n\t" \
  "v_mfma_f32_16x16x32_bf16 v[88:91], v[48:51], v[36:39], v[88:91]\n\t" \
  "s_waitcnt lgkmcnt(7)\n\t" \
  "v_mfma_f32_16x16x32_bf16 v[68:71], v[52:55], v[24:27], v[68:71]\n\t" \
  "ds_read_b64_tr_b16 v[48:49], v59 offset:26624\n\t" \
  "v_mfma_f32_16x16x32_bf16 v[76:79], v[52:55], v[28:31], v[76:79]\n\t" \
  "ds_read_b64_tr_b16 v[50:51], v59 offset:27136\n\t" \
  "v_mfma_f32_16x16x32_bf16 v[84:87], v[52:55], v[32:35], v[84:87]\n\t" \
  "v_mfma_f32_16x16x32_bf16 v[92:95], v[52:55], v[36:39], v[92:95]\n\t" \
  "N2b_%=:\n\t" \
  "s_waitcnt lgkmcnt(4)\n\t" \
  "v_mfma_f32_16x16x32_bf16 v[160:163], v[0:3], v[40:43], v[160:163]\n\t" \
  "ds_read_b64_tr_b16 v[52:53], v59 offset:25600\n\t" \
  "v_mfma_f32_16x16x32_bf16 v[128:131], v[4:7], v[40:43], v[128:131]\n\t" \
  "ds_read_b64_tr_b16 v[54:55], v59 offset:26112\n\t" \
  "v_mfma_f32_16x16x32_bf16 v[136:139], v[8:11], v[40:43], v[136:139]\n\t" \
  "s_barrier\n\t" \
  "v_mfma_f32_16x16x32_bf16 v[144:147], v[12:15], v[40:43], v[144:147]\n\t" \
  "v_mfma_f32_16x16x32_bf16 v[152:155], v[16:19], v[40:43], v[152:155]\n\t" \
  "s_waitcnt lgkmcnt(4)\n\t" \
  "v_mfma_f32_16x16x32_bf16 v[96:99], v[44:47], v[4:7], v[96:99]\n\t" \
  "ds_read_b64_tr_b16 v[40:41], v58 offset:24576\n\t" \
  "v_mfma_f32_16x16x32_bf16 v[104:107], v[44:47], v[8:11], v[104:107]\n\t" \
  "ds_read_b64_tr_b16 v[42:43], v58 offset:25088\n\t" \
  "v_mfma_f32_16x16x32_bf16 v[112:115], v[44:47], v[12:15], v[112:115]\n\t" \
  "ds_read_b128 v[20:23], v57 offset:18432\n\t" \
  "v_mfma_f32_16x16x32_bf16 v[120:123], v[44:47], v[16:19], v[120:123]\n\t" \
  "ds_read_b128 v[24:27], v57 offset:28672\n\t" \
  "s_waitcnt lgkmcnt(6)\n\t" \
  "v_mfma_f32_16x16x32_bf16 v[164:167], v[0:3], v[48:51], v[164:167]\n\t" \
  "ds_read_b64_tr_b16 v[44:45], v59 offset:24576\n\t" \
  "v_mfma_f32_16x16x32_bf16 v[132:135], v[4:7], v[48:51], v[132:135]\n\t" \
  "ds_read_b64_tr_b16 v[46:47], v59 offset:25088\n\t" \
  "v_mfma_f32_16x16x32_bf16 v[140:143], v[8:11], v[48:51], v[140:143]\n\t" \
  "ds_read_b128 v[28:31], v57 offset:30720\n\t" \
  "v_mfma_f32_16x16x32_bf16 v[148:151], v[12:15], v[48:51], v[148:151]\n\t" \
  "ds_read_b128 v[32:35], v57 offset:32768\n\t" \
  "v_mfma_f32_16x16x32_bf16 v[156:159], v[16:19], v[48:51], v[156:159]\n\t" \
  "s_waitcnt lgkmcnt(8)\n\t" \
  "v_mfma_f32_16x16x32_bf16 v[100:103], v[52:55], v[4:7], v[100:103]\n\t" \
  "ds_read_b64_tr_b16 v[48:49], v60 offset:2048\n\t" \
  "v_mfma_f32_16x16x32_bf16 v[108:111], v[52:55], v[8:11], v[108:111]\n\t" \
  "ds_read_b64_tr_b16 v[50:51], v60 offset:2560\n\t" \
  "v_mfma_f32_16x16x32_bf16 v[116:119], v[52:55], v[12:15], v[116:119]\n\t" \
  "ds_read_b128 v[36:39], v57 offset:34816\n\t" \
  "v_mfma_f32_16x16x32_bf16 v[124:127], v[52:55], v[16:19], v[124:127]\n\t" \
  "s_waitcnt lgkmcnt(9)\n\t" \
  "v_mfma_f32_16x16x32_bf16 v[64:67], v[40:43], v[4:7], v[64:67]\n\t" \
  "ds_read_b64_tr_b16 v[52:53], v60 offset:1024\n\t" \
  "v_mfma_f32_16x16x32_bf16 v[72:75], v[40:43], v[8:11], v[72:75]\n\t" \
  "ds_read_b64_tr_b16 v[54:55], v60 offset:1536\n\t" \
  "v_mfma_f32_16x16x32_bf16 v[80:83], v[40:43], v[12:15], v[80:83]\n\t" \
  "v_mfma_f32_16x16x32_bf16 v[88:91], v[40:43], v[16:19], v[88:91]\n\t" \
  "s_waitcnt lgkmcnt(7)\n\t" \
  "v_mfma_f32_16x16x32_bf16 v[68:71], v[44:47], v[4:7], v[68:71]\n\t" \
  "ds_read_b64_tr_b16 v[40:41], v61 offset:2048\n\t" \
  "v_mfma_f32_16x16x32_bf16 v[76:79], v[44:47], v[8:11], v[76:79]\n\t" \
  "ds_read_b64_tr_b16 v[42:43], v61 offset:2560\n\t" \
  "v_mfma_f32_16x16x32_bf16 v[84:87], v[44:47], v[12:15], v[84:87]\n\t" \
  "v_mfma_f32_16x16x32_bf16 v[92:95], v[44:47], v[16:19], v[92:95]\n\t" \
  "N3b_%=:\n\t" \
  "s_waitcnt lgkmcnt(4)\n\t" \
  "v_mfma_f32_16x16x32_bf16 v[160:163], v[20:23], v[48:51], v[160:163]\n\t" \
  "ds_read_b64_tr_b16 v[44:45], v61 offset:1024\n\t" \
  "v_mfma_f32_16x16x32_bf16 v[128:131], v[24:27], v[48:51], v[128:131]\n\t" \
  "ds_read_b64_tr_b16 v[46:47], v61 offset:1536\n\t" \
  "v_mfma_f32_16x16x32_bf16 v[136:139], v[28:31], v[48:51], v[136:139]\n\t" \
  "s_barrier\n\t" \
  "v_mfma_f32_16x16x32_bf16 v[144:147], v[32:35], v[48:51], v[144:147]\n\t" \
  "v_mfma_f32_16x16x32_bf16 v[152:155], v[36:39], v[48:51], v[152:155]\n\t" \
  "s_waitcnt lgkmcnt(4)\n\t" \
  "v_mfma_f32_16x16x32_bf16 v[96:99], v[52:55], v[24:27], v[96:99]\n\t" \
  "ds_read_b64_tr_b16 v[48:49], v60 offset:0\n\t" \
  "v_mfma_f32_16x16x32_bf16 v[104:107], v[52:55], v[28:31], v[104:107]\n\t" \
  "ds_read_b64_tr_b16 v[50:51], v60 offset:512\n\t" \
  "v_mfma_f32_16x16x32_bf16 v[112:115], v[52:55], v[32:35], v[112:115]\n\t" \
  "ds_read_b128 v[0:3], v56 offset:36864\n\t" \
  "v_mfma_f32_16x16x32_bf16 v[120:123], v[52:55], v[36:39], v[120:123]\n\t" \
  "ds_read_b128 v[4:7], v56 offset:47104\n\t" \
  "s_waitcnt lgkmcnt(6)\n\t" \
  "v_mfma_f32_16x16x32_bf16 v[164:167], v[20:23], v[40:43], v[164:167]\n\t" \
  "ds_read_b64_tr_b16 v[52:53], v61 offset:0\n\t" \
  "v_mfma_f32_16x16x32_bf16 v[132:135], v[24:27], v[40:43], v[132:135]\n\t" \
  "ds_read_b64_tr_b16 v[54:55], v61 offset:512\n\t" \
  "v_mfma_f32_16x16x32_bf16 v[140:143], v[28:31], v[40:43], v[140:143]\n\t" \
  "ds_read_b128 v[8:11], v56 offset:49152\n\t" \
  "v_mfma_f32_16x16x32_bf16 v[148:151], v[32:35], v[40:43], v[148:151]\n\t" \
  "ds_read_b128 v[12:15], v56 offset:51200\n\t" \
  "v_mfma_f32_16x16x32_bf16 v[156:159], v[36:39], v[40:43], v[156:159]\n\t" \
  "s_waitcnt lgkmcnt(8)\n\t" \
  "v_mfma_f32_16x16x32_bf16 v[100:103], v[44:47], v[24:27], v[100:103]\n\t" \
  "ds_read_b64_tr_b16 v[40:41], v60 offset:14336\n\t" \
  "v_mfma_f32_16x16x32_bf16 v[108:111], v[44:47], v[28:31], v[108:111]\n\t" \
  "ds_read_b64_tr_b16 v[42:43], v60 offset:14848\n\t" \
  "v_mfma_f32_16x16x32_bf16 v[116:119], v[44:47], v[32:35], v[116:119]\n\t" \
  "ds_read_b128 v[16:19], v56 offset:53248\n\t" \
  "v_mfma_f32_16x16x32_bf16 v[124:127], v[44:47], v[36:39], v[124:127]\n\t" \
  "s_waitcnt lgkmcnt(9)\n\t" \
  "v_mfma_f32_16x16x32_bf16 v[64:67], v[48:51], v[24:27], v[64:67]\n\t" \
  "ds_read_b64_tr_b16 v[44:45], v60 offset:13312\n\t" \
  "v_mfma_f32_16x16x32_bf16 v[72:75], v[48:51], v[28:31], v[72:75]\n\t" \
  "ds_read_b64_tr_b16 v[46:47], v60 offset:13824\n\t" \
  "v_mfma_f32_16x16x32_bf16 v[80:83], v[48:51], v[32:35], v[80:83]\n\t" \
  "v_mfma_f32_16x16x32_bf16 v[88:91], v[48:51], v[36:39], v[88:91]\n\t" \
  "s_waitcnt lgkmcnt(7)\n\t" \
  "v_mfma_f32_16x16x32_bf16 v[68:71], v[52:55], v[24:27], v[68:71]\n\t" \
  "ds_read_b64_tr_b16 v[48:49], v61 offset:14336\n\t" \
  "v_mfma_f32_16x16x32_bf16 v[76:79], v[52:55], v[28:31], v[76:79]\n\t" \
  "ds_read_b64_tr_b16 v[50:51], v61 offset:14848\n\t" \
  "v_mfma_f32_16x16x32_bf16 v[84:87], v[52:55], v[32:35], v[84:87]\n\t" \
  "v_mfma_f32_16x16x32_bf16 v[92:95], v[52:55], v[36:39], v[92:95]\n\t" \
  "N4b_%=:\n\t" \
  "s_waitcnt lgkmcnt(4)\n\t" \
  "v_mfma_f32_16x16x32_bf16 v[160:163], v[0:3], v[40:43], v[160:163]\n\t" \
  "ds_read_b64_tr_b16 v[52:53], v61 offset:13312\n\t" \
  "v_mfma_f32_16x16x32_bf16 v[128:131], v[4:7], v[40:43], v[128:131]\n\t" \
  "ds_read_b64_tr_b16 v[54:55], v61 offset:13824\n\t" \
  "v_mfma_f32_16x16x32_bf16 v[136:139], v[8:11], v[40:43], v[136:139]\n\t" \
  "s_barrier\n\t" \
  "v_mfma_f32_16x16x32_bf16 v[144:147], v[12:15], v[40:43], v[144:147]\n\t" \
  "v_mfma_f32_16x16x32_bf16 v[152:155], v[16:19], v[40:43], v[152:155]\n\t" \
  "s_waitcnt lgkmcnt(4)\n\t" \
  "v_mfma_f32_16x16x32_bf16 v[96:99], v[44:47], v[4:7], v[96:99]\n\t" \
  "ds_read_b64_tr_b16 v[40:41], v60 offset:12288\n\t" \
  "v_mfma_f32_16x16x32_bf16 v[104:107], v[44:47], v[8:11], v[104:107]\n\t" \
  "ds_read_b64_tr_b16 v[42:43], v60 offset:12800\n\t" \
  "v_mfma_f32_16x16x32_bf16 v[112:115], v[44:47], v[12:15], v[112:115]\n\t" \
  "ds_read_b128 v[20:23], v57 offset:36864\n\t" \
  "v_mfma_f32_16x16x32_bf16 v[120:123], v[44:47], v[16:19], v[120:123]\n\t" \
  "ds_read_b128 v[24:27], v57 offset:47104\n\t" \
  "s_waitcnt lgkmcnt(6)\n\t" \
  "v_mfma_f32_16x16x32_bf16 v[164:167], v[0:3], v[48:51], v[164:167]\n\t" \
  "ds_read_b64_tr_b16 v[44:45], v61 offset:12288\n\t" \
  "v_mfma_f32_16x16x32_bf16 v[132:135], v[4:7], v[48:51], v[132:135]\n\t" \
  "ds_read_b64_tr_b16 v[46:47], v61 offset:12800\n\t" \
  "v_mfma_f32_16x16x32_bf16 v[140:143], v[8:11], v[48:51], v[140:143]\n\t" \
  "ds_read_b128 v[28:31], v57 offset:49152\n\t" \
  "v_mfma_f32_16x16x32_bf16 v[148:151], v[12:15], v[48:51], v[148:151]\n\t" \
  "ds_read_b128 v[32:35], v57 offset:51200\n\t" \
  "v_mfma_f32_16x16x32_bf16 v[156:159], v[16:19], v[48:51], v[156:159]\n\t" \
  "s_waitcnt lgkmcnt(8)\n\t" \
  "v_mfma_f32_16x16x32_bf16 v[100:103], v[52:55], v[4:7], v[100:103]\n\t" \
  "ds_read_b64_tr_b16 v[48:49], v60 offset:26624\n\t" \
  "v_mfma_f32_16x16x32_bf16 v[108:111], v[52:55], v[8:11], v[108:111]\n\t" \
  "ds_read_b64_tr_b16 v[50:51], v60 offset:27136\n\t" \
  "v_mfma_f32_16x16x32_bf16 v[116:119], v[52:55], v[12:15], v[116:119]\n\t" \
  "ds_read_b128 v[36:39], v57 offset:53248\n\t" \
  "v_mfma_f32_16x16x32_bf16 v[124:127], v[52:55], v[16:19], v[124:127]\n\t" \
  "s_waitcnt lgkmcnt(9)\n\t" \
  "v_mfma_f32_16x16x32_bf16 v[64:67], v[40:43], v[4:7], v[64:67]\n\t" \
  "ds_read_b64_tr_b16 v[52:53], v60 offset:25600\n\t" \
  "v_mfma_f32_16x16x32_bf16 v[72:75], v[40:43], v[8:11], v[72:75]\n\t" \
  "ds_read_b64_tr_b16 v[54:55], v60 offset:26112\n\t" \
  "v_mfma_f32_16x16x32_bf16 v[80:83], v[40:43], v[12:15], v[80:83]\n\t" \
  "v_mfma_f32_16x16x32_bf16 v[88:91], v[40:43], v[16:19], v[88:91]\n\t" \
  "s_waitcnt lgkmcnt(7)\n\t" \
  "v_mfma_f32_16x16x32_bf16 v[68:71], v[44:47], v[4:7], v[68:71]\n\t" \
  "ds_read_b64_tr_b16 v[40:41], v61 offset:26624\n\t" \
  "v_mfma_f32_16x16x32_bf16 v[76:79], v[44:47], v[8:11], v[76:79]\n\t" \
  "ds_read_b64_tr_b16 v[42:43], v61 offset:27136\n\t" \
  "v_mfma_f32_16x16x32_bf16 v[84:87], v[44:47], v[12:15], v[84:87]\n\t" \
  "v_mfma_f32_16x16x32_bf16 v[92:95], v[44:47], v[16:19], v[92:95]\n\t" \
  "N5b_%=:\n\t" \
  "s_waitcnt lgkmcnt(4)\n\t" \
  "v_mfma_f32_16x16x32_bf16 v[160:163], v[20:23], v[48:51], v[160:163]\n\t" \
  "ds_read_b64_tr_b16 v[44:45], v61 offset:25600\n\t" \
  "v_mfma_f32_16x16x32_bf16 v[128:131], v[24:27], v[48:51], v[128:131]\n\t" \
  "ds_read_b64_tr_b16 v[46:47], v61 offset:26112\n\t" \
  "v_mfma_f32_16x16x32_bf16 v[136:139], v[28:31], v[48:51], v[136:139]\n\t" \
  "s_barrier\n\t" \
  "v_mfma_f32_16x16x32_bf16 v[144:147], v[32:35], v[48:51], v[144:147]\n\t" \
  "v_mfma_f32_16x16x32_bf16 v[152:155], v[36:39], v[48:51], v[152:155]\n\t" \
  "s_waitcnt lgkmcnt(4)\n\t" \
  "v_mfma_f32_16x16x32_bf16 v[96:99], v[52:55], v[24:27], v[96:99]\n\t" \
  "ds_read_b64_tr_b16 v[48:49], v60 offset:24576\n\t" \
  "v_mfma_f32_16x16x32_bf16 v[104:107], v[52:55], v[28:31], v[104:107]\n\t" \
  "ds_read_b64_tr_b16 v[50:51], v60 offset:25088\n\t" \
  "v_mfma_f32_16x16x32_bf16 v[112:115], v[52:55], v[32:35], v[112:115]\n\t" \
  "ds_read_b128 v[0:3], v56 offset:0\n\t" \
  "v_mfma_f32_16x16x32_bf16 v[120:123], v[52:55], v[36:39], v[120:123]\n\t" \
  "ds_read_b128 v[4:7], v56 offset:10240\n\t" \
  "s_waitcnt lgkmcnt(6)\n\t" \
  "v_mfma_f32_16x16x32_bf16 v[164:167], v[20:23], v[40:43], v[164:167]\n\t" \
  "ds_read_b64_tr_b16 v[52:53], v61 offset:24576\n\t" \
  "v_mfma_f32_16x16x32_bf16 v[132:135], v[24:27], v[40:43], v[132:135]\n\t" \
  "ds_read_b64_tr_b16 v[54:55], v61 offset:25088\n\t" \
  "v_mfma_f32_16x16x32_bf16 v[140:143], v[28:31], v[40:43], v[140:143]\n\t" \
  "ds_read_b128 v[8:11], v56 offset:12288\n\t" \
  "v_mfma_f32_16x16x32_bf16 v[148:151], v[32:35], v[40:43], v[148:151]\n\t" \
  "ds_read_b128 v[12:15], v56 offset:14336\n\t" \
  "v_mfma_f32_16x16x32_bf16 v[156:159], v[36:39], v[40:43], v[156:159]\n\t" \
  "s_waitcnt lgkmcnt(8)\n\t" \
  "v_mfma_f32_16x16x32_bf16 v[100:103], v[44:47], v[24:27], v[100:103]\n\t" \
  "ds_read_b64_tr_b16 v[40:41], v58 offset:2048\n\t" \
  "v_mfma_f32_16x16x32_bf16 v[108:111], v[44:47], v[28:31], v[108:111]\n\t" \
  "ds_read_b64_tr_b16 v[42:43], v58 offset:2560\n\t" \
  "v_mfma_f32_16x16x32_bf16 v[116:119], v[44:47], v[32:35], v[116:119]\n\t" \
  "ds_read_b128 v[16:19], v56 offset:16384\n\t" \
  "v_mfma_f32_16x16x32_bf16 v[124:127], v[44:47], v[36:39], v[124:127]\n\t" \
  "s_waitcnt lgkmcnt(9)\n\t" \
  "v_mfma_f32_16x16x32_bf16 v[64:67], v[48:51], v[24:27], v[64:67]\n\t" \
  "ds_read_b64_tr_b16 v[44:45], v58 offset:1024\n\t" \
  "v_mfma_f32_16x16x32_bf16 v[72:75], v[48:51], v[28:31], v[72:75]\n\t" \
  "ds_read_b64_tr_b16 v[46:47], v58 offset:1536\n\t" \
  "v_mfma_f32_16x16x32_bf16 v[80:83], v[48:51], v[32:35], v[80:83]\n\t" \
  "v_mfma_f32_16x16x32_bf16 v[88:91], v[48:51], v[36:39], v[88:91]\n\t" \
  "s_waitcnt lgkmcnt(7)\n\t" \
  "v_mfma_f32_16x16x32_bf16 v[68:71], v[52:55], v[24:27], v[68:71]\n\t" \
  "ds_read_b64_tr_b16 v[48:49], v59 offset:2048\n\t" \
  "v_mfma_f32_16x16x32_bf16 v[76:79], v[52:55], v[28:31], v[76:79]\n\t" \
  "ds_read_b64_tr_b16 v[50:51], v59 offset:2560\n\t" \
  "s_addk_i32 %[it], 1\n\t" \
  "v_mfma_f32_16x16x32_bf16 v[84:87], v[52:55], v[32:35], v[84:87]\n\t" \
  "s_cmp_lt_u32 %[it], 6\n\t" \
  "v_mfma_f32_16x16x32_bf16 v[92:95], v[52:55], v[36:39], v[92:95]\n\t" \
  "s_cbranch_scc1 N0b_%=\n\t" \
  "s_setprio 3\n\t" \
  "v_cvt_pk_bf16_f32 v160, v160, v161\n\t" \
  "v_cvt_pk_bf16_f32 v161, v162, v163\n\t" \
  "v_cvt_pk_bf16_f32 v162, v164, v165\n\t" \
  "v_cvt_pk_bf16_f32 v163, v166, v167\n\t" \
  "ds_write_b128 v63, v[160:163] offset:2048\n\t" \
  "v_mov_b32 v57, %[fa]\n\t" \
  "v_mov_b32 v58, 1\n\t" \
  "s_mov_b64 exec, 1\n\t" \
  "ds_write_b32 v57, v58\n\t" \
  "s_mov_b64 exec, -1\n\t" \
  "v_cvt_pk_bf16_f32 v120, v120, v121\n\t" \
  "v_cvt_pk_bf16_f32 v121, v122, v123\n\t" \
  "v_cvt_pk_bf16_f32 v122, v124, v125\n\t" \
  "v_cvt_pk_bf16_f32 v123, v126, v127\n\t" \
  "v_cvt_pk_bf16_f32 v28, v152, v153\n\t" \
  "v_cvt_pk_bf16_f32 v29, v154, v155\n\t" \
  "v_cvt_pk_bf16_f32 v38, v156, v157\n\t" \
  "v_cvt_pk_bf16_f32 v39, v158, v159\n\t" \
  "NX_%=:\n\t" \
  "v_cvt_pk_bf16_f32 v96, v96, v97\n\t" \
  "v_cvt_pk_bf16_f32 v97, v98, v99\n\t" \
  "v_cvt_pk_bf16_f32 v98, v100, v101\n\t" \
  "v_cvt_pk_bf16_f32 v99, v102, v103\n\t" \
  "v_cvt_pk_bf16_f32 v104, v104, v105\n\t" \
  "v_cvt_pk_bf16_f32 v105, v106, v107\n\t" \
  "v_cvt_pk_bf16_f32 v106, v108, v109\n\t" \
  "v_cvt_pk_bf16_f32 v107, v110, v111\n\t" \
  "v_mov_b32 v60, 0xff61b1e6\n\t" \
  "v_cndmask_b32_e64 v56, v60, 0, s[68:69]\n\t" \
  "v_cndmask_b32_e64 v57, v60, 0, s[70:71]\n\t" \
  "v_cndmask_b32_e64 v58, v60, 0, s[72:73]\n\t" \
  "v_cndmask_b32_e64 v59, v60, 0, s[74:75]\n\t" \
  "v_cvt_pk_bf16_f32 v72, v72, v73\n\t" \
  "v_cvt_pk_bf16_f32 v73, v74, v75\n\t" \
  "v_cvt_pk_bf16_f32 v74, v76, v77\n\t" \
  "v_cvt_pk_bf16_f32 v75, v78, v79\n\t" \
  "v_cndmask_b32_e64 v52, v60, 0, s[76:77]\n\t" \
  "v_cndmask_b32_e64 v53, v60, 0, s[78:79]\n\t" \
  "v_cndmask_b32_e64 v54, v60, 0, s[80:81]\n\t" \
  "v_cndmask_b32_e64 v55, v60, 0, s[82:83]\n\t" \
  "v_cvt_pk_bf16_f32 v24, v136, v137\n\t" \
  "v_cvt_pk_bf16_f32 v25, v138, v139\n\t" \
  "v_cvt_pk_bf16_f32 v34, v140, v141\n\t" \
  "v_cvt_pk_bf16_f32 v35, v142, v143\n\t" \
  "v_cvt_pk_bf16_f32 v26, v144, v145\n\t" \
  "v_cvt_pk_bf16_f32 v27, v146, v147\n\t" \
  "v_cvt_pk_bf16_f32 v36, v148, v149\n\t" \
  "v_cvt_pk_bf16_f32 v37, v150, v151\n\t" \
  "v_mfma_f32_16x16x32_bf16 v[136:139], v[96:99], v[72:75], v[56:59]\n\t" \
  "v_cvt_pk_bf16_f32 v112, v112, v113\n\t" \
  "v_cvt_pk_bf16_f32 v113, v114, v115\n\t" \
  "v_mfma_f32_16x16x32_bf16 v[140:143], v[104:107], v[72:75], v[52:55]\n\t" \
  "v_cvt_pk_bf16_f32 v114, v116, v117\n\t" \
  "v_cvt_pk_bf16_f32 v115, v118, v119\n\t" \
  "v_cvt_pk_bf16_f32 v80, v80, v81\n\t" \
  "v_cvt_pk_bf16_f32 v81, v82, v83\n\t" \
  "v_cvt_pk_bf16_f32 v82, v84, v85\n\t" \
  "v_cvt_pk_bf16_f32 v83, v86, v87\n\t" \
  "v_cvt_pk_bf16_f32 v22, v128, v129\n\t" \
  "v_cvt_pk_bf16_f32 v23, v130, v131\n\t" \
  "v_mfma_f32_16x16x32_bf16 v[144:147], v[104:107], v[80:83], v[56:59]\n\t" \
  "v_cvt_pk_bf16_f32 v32, v132, v133\n\t" \
  "v_cvt_pk_bf16_f32 v33, v134, v135\n\t" \
  "v_mfma_f32_16x16x32_bf16 v[148:151], v[112:115], v[80:83], v[52:55]\n\t" \
  "v_cvt_pk_bf16_f32 v88, v88, v89\n\t" \
  "v_cvt_pk_bf16_f32 v89, v90, v91\n\t" \
  "v_cvt_pk_bf16_f32 v90, v92, v93\n\t" \
  "v_cvt_pk_bf16_f32 v91, v94, v95\n\t" \
  "v_mov_b32 v61, %[fp]\n\t" \
  "v_add_u32 v60, %[vo], v63\n\t" \
  "v_mfma_f32_16x16x32_bf16 v[152:155], v[112:115], v[88:91], v[56:59]\n\t" \
  "ds_read_b32 v0, v61\n\t" \
  "ds_read_b64 v[20:21], v60\n\t" \
  "ds_read_b64 v[30:31], v60 offset:8\n\t" \
  "s_mov_b64 exec, %[rm]\n\t" \
  "ds_read_b128 v[160:163], v63\n\t" \
  "s_mov_b64 exec, -1\n\t" \
  "v_mfma_f32_16x16x32_bf16 v[156:159], v[120:123], v[88:91], v[52:55]\n\t" \
  "v_cvt_pk_bf16_f32 v64, v64, v65\n\t" \
  "v_cvt_pk_bf16_f32 v65, v66, v67\n\t" \
  "v_cvt_pk_bf16_f32 v66, v68, v69\n\t" \
  "v_cvt_pk_bf16_f32 v67, v70, v71\n\t" \
  "v_max_f32 v136, v136, v137\n\t" \
  "v_max_f32 v138, v138, v139\n\t" \
  "v_mfma_f32_16x16x32_bf16 v[132:135], v[96:99], v[64:67], v[52:55]\n\t" \
  "v_max_f32 v140, v140, v141\n\t" \
  "v_max_f32 v142, v142, v143\n\t" \
  "AW_%=:\n\t" \
  "s_waitcnt lgkmcnt(0)\n\t" \
  "v_cmp_eq_u32 vcc, 0, v0\n\t" \
  "v_max3_f32 v5, v136, v138, v140\n\t" \
  "v_max_f32 v5, v5, v142\n\t" \
  "s_cbranch_vccnz AR_%=\n\t" \
  "s_cmp_eq_u32 %[z], 0\n\t" \
  "s_cbranch_scc1 AZ_%=\n\t" \
  "AY_%=:\n\t" \
  "v_mfma_f32_16x16x32_bf16 v[128:131], v[160:163], v[64:67], v[56:59]\n\t" \
  "v_max_f32 v144, v144, v145\n\t" \
  "v_max_f32 v146, v146, v147\n\t" \
  "v_max_f32 v148, v148, v149\n\t" \
  "v_max_f32 v150, v150, v151\n\t" \
  "v_max3_f32 v6, v144, v146, v148\n\t" \
  "v_max_f32 v6, v6, v150\n\t" \
  "v_max_f32 v152, v152, v153\n\t" \
  "v_max_f32 v154, v154, v155\n\t" \
  "v_max_f32 v156, v156, v157\n\t" \
  "v_max_f32 v158, v158, v159\n\t" \
  "v_max3_f32 v7, v152, v154, v156\n\t" \
  "v_max_f32 v7, v7, v158\n\t" \
  "v_mov_b32 v8, 0xc0c0100\n\t" \
  "v_mov_b32 v9, 0x1000c0c\n\t" \
  "v_max_f32 v128, v128, v129\n\t" \
  "v_max_f32 v130, v130, v131\n\t" \
  "v_max_f32 v132, v132, v133\n\t" \
  "v_max_f32 v134, v134, v135\n\t" \
  "v_max3_f32 v4, v128, v130, v132\n\t" \
  "v_max_f32 v4, v4, v134\n\t" \
  "v_cndmask_b32_e64 v60, v8, v9, s[84:85]\n\t" \
  "v_mov_b32 v8, 0xc0c0302\n\t" \
  "v_permlane16_swap_b32 v6, v7\n\t" \
  "v_permlane16_swap_b32 v4, v5\n\t" \
  "v_max_f32 v6, v6, v7\n\t" \
  "v_max_f32 v4, v4, v5\n\t" \
  "v_mov_b32 v9, 0x3020c0c\n\t" \
  "v_cndmask_b32_e64 v61, v8, v9, s[84:85]\n\t" \
  "v_permlane32_swap_b32 v4, v6\n\t" \
  "v_max_f32 v4, v4, v6\n\t" \
  "v_mul_f32 v6, 0xbe8293ee, v4\n\t" \
  "v_mul_f32 v4, 0xbe8293ee, v4\n\t" \
  "s_nop 1\n\t" \
  "v_permlane32_swap_b32 v4, v6\n\t" \
  "v_mov_b32 v5, v4\n\t" \
  "v_mov_b32 v7, v6\n\t" \
  "s_nop 0\n\t" \
  "v_permlane16_swap_b32 v4, v5\n\t" \
  "v_permlane16_swap_b32 v6, v7\n\t" \
  "v_fmamk_f32 v128, v128, 0x3e8293ee, v4\n\t" \
  "v_fmamk_f32 v130, v130, 0x3e8293ee, v4\n\t" \
  "v_fmamk_f32 v132, v132, 0x3e8293ee, v4\n\t" \
  "v_fmamk_f32 v134, v134, 0x3e8293ee, v4\n\t" \
  "v_fmamk_f32 v136, v136, 0x3e8293ee, v5\n\t" \
  "v_fmamk_f32 v138, v138, 0x3e8293ee, v5\n\t" \
  "v_fmamk_f32 v140, v140, 0x3e8293ee, v5\n\t" \
  "v_fmamk_f32 v142, v142, 0x3e8293ee, v5\n\t" \
  "v_fmamk_f32 v144, v144, 0x3e8293ee, v6\n\t" \
  "v_fmamk_f32 v146, v146, 0x3e8293ee, v6\n\t" \
  "v_fmamk_f32 v148, v148, 0x3e8293ee, v6\n\t" \
  "v_fmamk_f32 v150, v150, 0x3e8293ee, v6\n\t" \
  "v_fmamk_f32 v152, v152, 0x3e8293ee, v7\n\t" \
  "v_fmamk_f32 v154, v154, 0x3e8293ee, v7\n\t" \
  "v_fmamk_f32 v156, v156, 0x3e8293ee, v7\n\t" \
  "v_fmamk_f32 v158, v158, 0x3e8293ee, v7\n\t" \
  "v_exp_f32 v128, v128\n\t" \
  "v_exp_f32 v130, v130\n\t" \
  "v_exp_f32 v132, v132\n\t" \
  "v_exp_f32 v134, v134\n\t" \
  "v_exp_f32 v136, v136\n\t" \
  "v_exp_f32 v138, v138\n\t" \
  "v_exp_f32 v140, v140\n\t" \
  "v_exp_f32 v142, v142\n\t" \
  "v_exp_f32 v144, v144\n\t" \
  "v_exp_f32 v146, v146\n\t" \
  "v_exp_f32 v148, v148\n\t" \
  "v_exp_f32 v150, v150\n\t" \
  "v_exp_f32 v152, v152\n\t" \
  "v_exp_f32 v154, v154\n\t" \
  "v_exp_f32 v156, v156\n\t" \
  "v_exp_f32 v158, v158\n\t" \
  "v_add_f32 v4, v128, v130\n\t" \
  "v_add_f32 v8, v132, v134\n\t" \
  "v_add_f32 v5, v136, v138\n\t" \
  "v_add_f32 v9, v140, v142\n\t" \
  "v_add_f32 v6, v144, v146\n\t" \
  "v_add_f32 v10, v148, v150\n\t" \
  "v_add_f32 v7, v152, v154\n\t" \
  "v_add_f32 v11, v156, v158\n\t" \
  "v_add_f32 v4, v4, v8\n\t" \
  "v_add_f32 v5, v5, v9\n\t" \
  "v_add_f32 v6, v6, v10\n\t" \
  "v_add_f32 v7, v7, v11\n\t" \
  "v_permlane16_swap_b32 v4, v5\n\t" \
  "s_nop 0\n\t" \
  "v_permlane16_swap_b32 v6, v7\n\t" \
  "v_add_f32 v4, v4, v5\n\t" \
  "v_add_f32 v6, v6, v7\n\t" \
  "s_nop 1\n\t" \
  "v_permlane32_swap_b32 v4, v6\n\t" \
  "v_add_f32 v4, v4, v6\n\t" \
  "v_rcp_f32 v4, v4\n\t" \
  "s_nop 0\n\t" \
  "v_mov_b32 v6, v4\n\t" \
  "s_nop 1\n\t" \
  "v_permlane32_swap_b32 v4, v6\n\t" \
  "v_mov_b32 v5, v4\n\t" \
  "v_mov_b32 v7, v6\n\t" \
  "s_nop 0\n\t" \
  "v_permlane16_swap_b32 v4, v5\n\t" \
  "v_permlane16_swap_b32 v6, v7\n\t" \
  "v_mul_f32 v128, v128, v4\n\t" \
  "v_mul_f32 v130, v130, v4\n\t" \
  "v_mul_f32 v132, v132, v4\n\t" \
  "v_mul_f32 v134, v134, v4\n\t" \
  "v_cvt_pk_bf16_f32 v128, v128, v130\n\t" \
  "v_cvt_pk_bf16_f32 v132, v132, v134\n\t" \
  "v_perm_b32 v68, 0, v128, v60\n\t" \
  "v_perm_b32 v69, 0, v128, v61\n\t" \
  "v_perm_b32 v70, 0, v132, v60\n\t" \
  "v_perm_b32 v71, 0, v132, v61\n\t" \
  "v_mul_f32 v136, v136, v5\n\t" \
  "v_mul_f32 v138, v138, v5\n\t" \
  "v_mfma_f32_16x16x32_bf16 v[128:131], v[20:23], v[68:71], 0\n\t" \
  "v_mul_f32 v140, v140, v5\n\t" \
  "v_mul_f32 v142, v142, v5\n\t" \
  "v_cvt_pk_bf16_f32 v136, v136, v138\n\t" \
  "v_mfma_f32_16x16x32_bf16 v[132:135], v[30:33], v[68:71], 0\n\t" \
  "v_cvt_pk_bf16_f32 v140, v140, v142\n\t" \
  "v_perm_b32 v76, 0, v136, v60\n\t" \
  "v_perm_b32 v77, 0, v136, v61\n\t" \
  "v_perm_b32 v78, 0, v140, v60\n\t" \
  "v_perm_b32 v79, 0, v140, v61\n\t" \
  "v_mul_f32 v144, v144, v6\n\t" \
  "v_mul_f32 v146, v146, v6\n\t" \
  "v_mfma_f32_16x16x32_bf16 v[136:139], v[22:25], v[76:79], 0\n\t" \
  "v_mul_f32 v148, v148, v6\n\t" \
  "v_mul_f32 v150, v150, v6\n\t" \
  "v_cvt_pk_bf16_f32 v144, v144, v146\n\t" \
  "v_mfma_f32_16x16x32_bf16 v[140:143], v[32:35], v[76:79], 0\n\t" \
  "v_cvt_pk_bf16_f32 v148, v148, v150\n\t" \
  "v_perm_b32 v84, 0, v144, v60\n\t" \
  "v_perm_b32 v85, 0, v144, v61\n\t" \
  "v_perm_b32 v86, 0, v148, v60\n\t" \
  "v_perm_b32 v87, 0, v148, v61\n\t" \
  "v_cvt_pk_bf16_f32 v100, v128, v129\n\t" \
  "v_cvt_pk_bf16_f32 v101, v130, v131\n\t" \
  "v_mfma_f32_16x16x32_bf16 v[144:147], v[24:27], v[84:87], 0\n\t" \
  "v_cvt_pk_bf16_f32 v102, v132, v133\n\t" \
  "v_cvt_pk_bf16_f32 v103, v134, v135\n\t" \
  "global_store_dwordx4 v62, v[100:103], %[ot] offset:0 sc1\n\t" \
  "v_mfma_f32_16x16x32_bf16 v[148:151], v[34:37], v[84:87], 0\n\t" \
  "v_mul_f32 v152, v152, v7\n\t" \
  "v_mul_f32 v154, v154, v7\n\t" \
  "v_mul_f32 v156, v156, v7\n\t" \
  "v_mul_f32 v158, v158, v7\n\t" \
  "v_cvt_pk_bf16_f32 v152, v152, v154\n\t" \
  "v_cvt_pk_bf16_f32 v156, v156, v158\n\t" \
  "v_perm_b32 v92, 0, v152, v60\n\t" \
  "v_perm_b32 v93, 0, v152, v61\n\t" \
  "v_perm_b32 v94, 0, v156, v60\n\t" \
  "v_perm_b32 v95, 0, v156, v61\n\t" \
  "v_cvt_pk_bf16_f32 v108, v136, v137\n\t" \
  "v_cvt_pk_bf16_f32 v109, v138, v139\n\t" \
  "v_mfma_f32_16x16x32_bf16 v[152:155], v[26:29], v[92:95], 0\n\t" \
  "v_cvt_pk_bf16_f32 v110, v140, v141\n\t" \
  "v_cvt_pk_bf16_f32 v111, v142, v143\n\t" \
  "global_store_dwordx4 v62, v[108:111], %[ot] offset:1024 sc1\n\t" \
  "v_mfma_f32_16x16x32_bf16 v[156:159], v[36:39], v[92:95], 0\n\t" \
  "v_cvt_pk_bf16_f32 v116, v144, v145\n\t" \
  "v_cvt_pk_bf16_f32 v117, v146, v147\n\t" \
  "v_cvt_pk_bf16_f32 v118, v148, v149\n\t" \
  "v_cvt_pk_bf16_f32 v119, v150, v151\n\t" \
  "global_store_dwordx4 v62, v[116:119], %[ou] offset:0 sc1\n\t" \
  "v_cvt_pk_bf16_f32 v124, v152, v153\n\t" \
  "v_cvt_pk_bf16_f32 v125, v154, v155\n\t" \
  "s_nop 0\n\t" \
  "v_cvt_pk_bf16_f32 v126, v156, v157\n\t" \
  "v_cvt_pk_bf16_f32 v127, v158, v159\n\t" \
  "global_store_dwordx4 v62, v[124:127], %[ou] offset:1024 sc1\n\t" \
  "s_branch AE_%=\n\t" \
  "AR_%=:\n\t" \
  "s_addk_i32 %[it], 1\n\t" \
  "s_cmp_gt_u32 %[it], 0x400000\n\t" \
  "s_cbranch_scc1 AY_%=\n\t" \
  "ds_read_b32 v0, v61\n\t" \
  "ds_read_b64 v[20:21], v60\n\t" \
  "ds_read_b64 v[30:31], v60 offset:8\n\t" \
  "s_mov_b64 exec, %[rm]\n\t" \
  "ds_read_b128 v[160:163], v63\n\t" \
  "s_mov_b64 exec, -1\n\t" \
  "s_branch AW_%=\n\t" \
  "AZ_%=:\n\t" \
  "v_mov_b32 v0, 0xff61b1e6\n\t" \
  "v_mov_b32 v1, 0xcfa8965f\n\t" \
  "s_nop 3\n\t" \
  "v_cndmask_b32_e64 v56, v0, v1, s[68:69]\n\t" \
  "v_cndmask_b32_e64 v57, v0, v1, s[70:71]\n\t" \
  "v_cndmask_b32_e64 v58, v0, v1, s[72:73]\n\t" \
  "v_cndmask_b32_e64 v59, v0, v1, s[74:75]\n\t" \
  "s_nop 1\n\t" \
  "s_branch AY_%=\n\t" \
  "AE_%=:\n\t" \
  "s_nop 1\n\t"
#define N2_CLOB "v0", "v1", "v2", "v3", "v4", "v5", "v6", "v7", "v8", "v9", "v10", "v11", "v12", "v13", "v14", "v15", "v16", "v17", "v18", "v19", "v20", "v21", "v22", "v23", "v24", "v25", "v26", "v27", "v28", "v29", "v30", "v31", "v32", "v33", "v34", "v35", "v36", "v37", "v38", "v39", "v40", "v41", "v42", "v43", "v44", "v45", "v46", "v47", "v48", "v49", "v50", "v51", "v52", "v53", "v54", "v55", "v60", "v61", "v64", "v65", "v66", "v67", "v68", "v69", "v70", "v71", "v72", "v73", "v74", "v75", "v76", "v77", "v78", "v79", "v80", "v81", "v82", "v83", "v84", "v85", "v86", "v87", "v88", "v89", "v90", "v91", "v92", "v93", "v94", "v95", "v96", "v97", "v98", "v99", "v100", "v101", "v102", "v103", "v104", "v105", "v106", "v107", "v108", "v109", "v110", "v111", "v112", "v113", "v114", "v115", "v116", "v117", "v118", "v119", "v120", "v121", "v122", "v123", "v124", "v125", "v126", "v127", "v128", "v129", "v130", "v131", "v132", "v133", "v134", "v135", "v136", "v137", "v138", "v139", "v140", "v141", "v142", "v143", "v144", "v145", "v146", "v147", "v148", "v149", "v150", "v151", "v152", "v153", "v154", "v155", "v156", "v157", "v158", "v159", "v160", "v161", "v162", "v163", "v164", "v165", "v166", "v167", "s68", "s69", "s70", "s71", "s72", "s73", "s74", "s75", "s76", "s77", "s78", "s79", "s80", "s81", "s82", "s83", "s84", "s85"

#define CV_PRE 0
#define CV_Y 1
#define CV_ASM \
  "s_mov_b64 vcc, 1\n\t" \
  "ds_read_b64 v[0:1], v28 offset:0\n\t" \
  "ds_read_b64 v[2:3], v29 offset:0\n\t" \
  "ds_read_b64 v[4:5], v30 offset:1024\n\t" \
  "ds_read_b64 v[6:7], v31 offset:1024\n\t" \
  "ds_read_b64 v[8:9], v24 offset:2048\n\t" \
  "ds_read_b64 v[10:11], v25 offset:2048\n\t" \
  "ds_read_b64 v[12:13], v26 offset:3072\n\t" \
  "ds_read_b64 v[14:15], v27 offset:3072\n\t" \
  "ds_read_b64 v[16:17], v28 offset:4096\n\t" \
  "ds_read_b64 v[18:19], v29 offset:4096\n\t" \
  "ds_read_b64 v[20:21], v30 offset:5120\n\t" \
  "ds_read_b64 v[22:23], v31 offset:5120\n\t" \
  "s_waitcnt lgkmcnt(0)\n\t" \
  "v_mfma_f32_16x16x32_bf16 v[128:131], v[68:71], v[0:3], 0\n\t" \
  "v_mfma_f32_16x16x32_bf16 v[128:131], v[64:67], v[4:7], v[128:131]\n\t" \
  "v_mfma_f32_16x16x32_bf16 v[132:135], v[76:79], v[0:3], 0\n\t" \
  "v_mfma_f32_16x16x32_bf16 v[132:135], v[72:75], v[4:7], v[132:135]\n\t" \
  "v_mfma_f32_16x16x32_bf16 v[136:139], v[84:87], v[0:3], 0\n\t" \
  "v_mfma_f32_16x16x32_bf16 v[136:139], v[80:83], v[4:7], v[136:139]\n\t" \
  "v_mfma_f32_16x16x32_bf16 v[140:143], v[92:95], v[0:3], 0\n\t" \
  "v_mfma_f32_16x16x32_bf16 v[140:143], v[88:91], v[4:7], v[140:143]\n\t" \
  "v_mfma_f32_16x16x32_bf16 v[144:147], v[100:103], v[0:3], 0\n\t" \
  "v_mfma_f32_16x16x32_bf16 v[144:147], v[96:99], v[4:7], v[144:147]\n\t" \
  "ds_bpermute_b32 v34, v32, v131\n\t" \
  "v_mfma_f32_16x16x32_bf16 v[148:151], v[108:111], v[0:3], 0\n\t" \
  "v_mov_b32 v131, v130\n\t" \
  "v_mov_b32 v130, v129\n\t" \
  "v_mfma_f32_16x16x32_bf16 v[148:151], v[104:107], v[4:7], v[148:151]\n\t" \
  "v_mov_b32 v129, v128\n\t" \
  "v_mfma_f32_16x16x32_bf16 v[152:155], v[116:119], v[0:3], 0\n\t" \
  "v_mfma_f32_16x16x32_bf16 v[152:155], v[112:115], v[4:7], v[152:155]\n\t" \
  "v_mfma_f32_16x16x32_bf16 v[156:159], v[124:127], v[0:3], 0\n\t" \
  "ds_read_b64 v[0:1], v24 offset:6144\n\t" \
  "ds_read_b64 v[2:3], v25 offset:6144\n\t" \
  "v_mfma_f32_16x16x32_bf16 v[156:159], v[120:123], v[4:7], v[156:159]\n\t" \
  "ds_read_b64 v[4:5], v26 offset:7168\n\t" \
  "ds_read_b64 v[6:7], v27 offset:7168\n\t" \
  "v_mfma_f32_16x16x32_bf16 v[132:135], v[68:71], v[8:11], v[132:135]\n\t" \
  "v_mfma_f32_16x16x32_bf16 v[132:135], v[64:67], v[12:15], v[132:135]\n\t" \
  "v_mfma_f32_16x16x32_bf16 v[136:139], v[76:79], v[8:11], v[136:139]\n\t" \
  "v_mfma_f32_16x16x32_bf16 v[136:139], v[72:75], v[12:15], v[136:139]\n\t" \
  "v_mfma_f32_16x16x32_bf16 v[140:143], v[84:87], v[8:11], v[140:143]\n\t" \
  "s_waitcnt lgkmcnt(4)\n\t" \
  "v_cndmask_b32_e64 v128, v34, 0, vcc\n\t" \
  "v_mfma_f32_16x16x32_bf16 v[140:143], v[80:83], v[12:15], v[140:143]\n\t" \
  "v_mfma_f32_16x16x32_bf16 v[144:147], v[92:95], v[8:11], v[144:147]\n\t" \
  "v_mfma_f32_16x16x32_bf16 v[144:147], v[88:91], v[12:15], v[144:147]\n\t" \
  "ds_bpermute_b32 v35, v32, v135\n\t" \
  "v_mfma_f32_16x16x32_bf16 v[148:151], v[100:103], v[8:11], v[148:151]\n\t" \
  "v_mov_b32 v135, v134\n\t" \
  "v_mov_b32 v134, v133\n\t" \
  "v_mfma_f32_16x16x32_bf16 v[148:151], v[96:99], v[12:15], v[148:151]\n\t" \
  "v_mov_b32 v133, v132\n\t" \
  "v_mfma_f32_16x16x32_bf16 v[152:155], v[108:111], v[8:11], v[152:155]\n\t" \
  "v_mfma_f32_16x16x32_bf16 v[152:155], v[104:107], v[12:15], v[152:155]\n\t" \
  "v_mfma_f32_16x16x32_bf16 v[156:159], v[116:119], v[8:11], v[156:159]\n\t" \
  "ds_read_b64 v[8:9], v28 offset:8192\n\t" \
  "ds_read_b64 v[10:11], v29 offset:8192\n\t" \
  "v_mfma_f32_16x16x32_bf16 v[156:159], v[112:115], v[12:15], v[156:159]\n\t" \
  "ds_read_b64 v[12:13], v30 offset:9216\n\t" \
  "ds_read_b64 v[14:15], v31 offset:9216\n\t" \
  "v_mfma_f32_16x16x32_bf16 v[136:139], v[68:71], v[16:19], v[136:139]\n\t" \
  "v_mfma_f32_16x16x32_bf16 v[136:139], v[64:67], v[20:23], v[136:139]\n\t" \
  "v_mfma_f32_16x16x32_bf16 v[140:143], v[76:79], v[16:19], v[140:143]\n\t" \
  "v_mfma_f32_16x16x32_bf16 v[140:143], v[72:75], v[20:23], v[140:143]\n\t" \
  "v_mfma_f32_16x16x32_bf16 v[144:147], v[84:87], v[16:19], v[144:147]\n\t" \
  "s_waitcnt lgkmcnt(4)\n\t" \
  "v_cndmask_b32_e64 v132, v35, v34, vcc\n\t" \
  "v_mfma_f32_16x16x32_bf16 v[144:147], v[80:83], v[20:23], v[144:147]\n\t" \
  "v_mfma_f32_16x16x32_bf16 v[148:151], v[92:95], v[16:19], v[148:151]\n\t" \
  "v_mfma_f32_16x16x32_bf16 v[148:151], v[88:91], v[20:23], v[148:151]\n\t" \
  "ds_bpermute_b32 v36, v32, v139\n\t" \
  "v_mfma_f32_16x16x32_bf16 v[152:155], v[100:103], v[16:19], v[152:155]\n\t" \
  "v_mov_b32 v139, v138\n\t" \
  "v_mov_b32 v138, v137\n\t" \
  "v_mfma_f32_16x16x32_bf16 v[152:155], v[96:99], v[20:23], v[152:155]\n\t" \
  "v_mov_b32 v137, v136\n\t" \
  "v_mfma_f32_16x16x32_bf16 v[156:159], v[108:111], v[16:19], v[156:159]\n\t" \
  "ds_read_b64 v[16:17], v24 offset:10240\n\t" \
  "ds_read_b64 v[18:19], v25 offset:10240\n\t" \
  "v_mfma_f32_16x16x32_bf16 v[156:159], v[104:107], v[20:23], v[156:159]\n\t" \
  "ds_read_b64 v[20:21], v26 offset:11264\n\t" \
  "ds_read_b64 v[22:23], v27 offset:11264\n\t" \
  "v_mfma_f32_16x16x32_bf16 v[140:143], v[68:71], v[0:3], v[140:143]\n\t" \
  "v_mfma_f32_16x16x32_bf16 v[140:143], v[64:67], v[4:7], v[140:143]\n\t" \
  "v_mfma_f32_16x16x32_bf16 v[144:147], v[76:79], v[0:3], v[144:147]\n\t" \
  "v_mfma_f32_16x16x32_bf16 v[144:147], v[72:75], v[4:7], v[144:147]\n\t" \
  "v_mfma_f32_16x16x32_bf16 v[148:151], v[84:87], v[0:3], v[148:151]\n\t" \
  "v_mfma_f32_16x16x32_bf16 v[148:151], v[80:83], v[4:7], v[148:151]\n\t" \
  "v_mfma_f32_16x16x32_bf16 v[152:155], v[92:95], v[0:3], v[152:155]\n\t" \
  "s_waitcnt lgkmcnt(4)\n\t" \
  "v_cndmask_b32_e64 v136, v36, v35, vcc\n\t" \
  "v_mfma_f32_16x16x32_bf16 v[152:155], v[88:91], v[4:7], v[152:155]\n\t" \
  "ds_bpermute_b32 v37, v32, v143\n\t" \
  "v_mfma_f32_16x16x32_bf16 v[156:159], v[100:103], v[0:3], v[156:159]\n\t" \
  "ds_read_b64 v[0:1], v28 offset:12288\n\t" \
  "ds_read_b64 v[2:3], v29 offset:12288\n\t" \
  "v_mfma_f32_16x16x32_bf16 v[156:159], v[96:99], v[4:7], v[156:159]\n\t" \
  "ds_read_b64 v[4:5], v30 offset:13312\n\t" \
  "ds_read_b64 v[6:7], v31 offset:13312\n\t" \
  "v_mfma_f32_16x16x32_bf16 v[144:147], v[68:71], v[8:11], v[144:147]\n\t" \
  "v_mov_b32 v143, v142\n\t" \
  "v_mov_b32 v142, v141\n\t" \
  "v_mfma_f32_16x16x32_bf16 v[144:147], v[64:67], v[12:15], v[144:147]\n\t" \
  "v_mov_b32 v141, v140\n\t" \
  "v_mfma_f32_16x16x32_bf16 v[148:151], v[76:79], v[8:11], v[148:151]\n\t" \
  "v_mfma_f32_16x16x32_bf16 v[148:151], v[72:75], v[12:15], v[148:151]\n\t" \
  "v_mfma_f32_16x16x32_bf16 v[152:155], v[84:87], v[8:11], v[152:155]\n\t" \
  "v_mfma_f32_16x16x32_bf16 v[152:155], v[80:83], v[12:15], v[152:155]\n\t" \
  "v_mfma_f32_16x16x32_bf16 v[156:159], v[92:95], v[8:11], v[156:159]\n\t" \
  "ds_read_b64 v[8:9], v24 offset:14336\n\t" \
  "ds_read_b64 v[10:11], v25 offset:14336\n\t" \
  "v_mfma_f32_16x16x32_bf16 v[156:159], v[88:91], v[12:15], v[156:159]\n\t" \
  "ds_read_b64 v[12:13], v26 offset:15360\n\t" \
  "s_waitcnt lgkmcnt(8)\n\t" \
  "v_mfma_f32_16x16x32_bf16 v[148:151], v[68:71], v[16:19], v[148:151]\n\t" \
  "ds_read_b64 v[14:15], v27 offset:15360\n\t" \
  "ds_bpermute_b32 v38, v32, v147\n\t" \
  "v_mfma_f32_16x16x32_bf16 v[148:151], v[64:67], v[20:23], v[148:151]\n\t" \
  "v_mov_b32 v147, v146\n\t" \
  "v_mov_b32 v146, v145\n\t" \
  "v_mfma_f32_16x16x32_bf16 v[152:155], v[76:79], v[16:19], v[152:155]\n\t" \
  "s_waitcnt lgkmcnt(9)\n\t" \
  "v_cndmask_b32_e64 v140, v37, v36, vcc\n\t" \
  "v_mfma_f32_16x16x32_bf16 v[152:155], v[72:75], v[20:23], v[152:155]\n\t" \
  "v_mov_b32 v145, v144\n\t" \
  "v_mfma_f32_16x16x32_bf16 v[156:159], v[84:87], v[16:19], v[156:159]\n\t" \
  "ds_read_b64 v[16:17], v26 offset:0\n\t" \
  "ds_read_b64 v[18:19], v27 offset:0\n\t" \
  "v_mfma_f32_16x16x32_bf16 v[156:159], v[80:83], v[20:23], v[156:159]\n\t" \
  "ds_read_b64 v[20:21], v28 offset:1024\n\t" \
  "ds_read_b64 v[22:23], v29 offset:1024\n\t" \
  "s_waitcnt lgkmcnt(9)\n\t" \
  "v_mfma_f32_16x16x32_bf16 v[152:155], v[68:71], v[0:3], v[152:155]\n\t" \
  "ds_bpermute_b32 v39, v32, v151\n\t" \
  "v_mfma_f32_16x16x32_bf16 v[152:155], v[64:67], v[4:7], v[152:155]\n\t" \
  "v_mov_b32 v151, v150\n\t" \
  "v_mov_b32 v150, v149\n\t" \
  "v_mfma_f32_16x16x32_bf16 v[156:159], v[76:79], v[0:3], v[156:159]\n\t" \
  "ds_read_b64 v[0:1], v30 offset:2048\n\t" \
  "ds_read_b64 v[2:3], v31 offset:2048\n\t" \
  "v_mfma_f32_16x16x32_bf16 v[156:159], v[72:75], v[4:7], v[156:159]\n\t" \
  "ds_read_b64 v[4:5], v24 offset:3072\n\t" \
  "s_waitcnt lgkmcnt(11)\n\t" \
  "v_mfma_f32_16x16x32_bf16 v[156:159], v[68:71], v[8:11], v[156:159]\n\t" \
  "ds_bpermute_b32 v40, v32, v155\n\t" \
  "s_waitcnt lgkmcnt(10)\n\t" \
  "v_mfma_f32_16x16x32_bf16 v[156:159], v[64:67], v[12:15], v[156:159]\n\t" \
  "ds_read_b64 v[6:7], v25 offset:3072\n\t" \
  "s_waitcnt lgkmcnt(8)\n\t" \
  "v_mfma_f32_16x16x32_bf16 v[128:131], v[68:71], v[16:19], v[128:131]\n\t" \
  "v_cndmask_b32_e64 v144, v38, v37, vcc\n\t" \
  "s_waitcnt lgkmcnt(6)\n\t" \
  "v_mfma_f32_16x16x32_bf16 v[128:131], v[64:67], v[20:23], v[128:131]\n\t" \
  "v_mov_b32 v149, v148\n\t" \
  "v_mov_b32 v155, v154\n\t" \
  "v_mfma_f32_16x16x32_bf16 v[132:135], v[76:79], v[16:19], v[132:135]\n\t" \
  "ds_bpermute_b32 v41, v32, v159\n\t" \
  "v_mov_b32 v154, v153\n\t" \
  "v_mfma_f32_16x16x32_bf16 v[132:135], v[72:75], v[20:23], v[132:135]\n\t" \
  "v_mov_b32 v153, v152\n\t" \
  "v_mov_b32 v159, v158\n\t" \
  "v_mfma_f32_16x16x32_bf16 v[136:139], v[84:87], v[16:19], v[136:139]\n\t" \
  "v_mov_b32 v158, v157\n\t" \
  "v_mov_b32 v157, v156\n\t" \
  "v_mfma_f32_16x16x32_bf16 v[136:139], v[80:83], v[20:23], v[136:139]\n\t" \
  "s_waitcnt lgkmcnt(6)\n\t" \
  "v_cndmask_b32_e64 v148, v39, v38, vcc\n\t" \
  "v_mfma_f32_16x16x32_bf16 v[140:143], v[92:95], v[16:19], v[140:143]\n\t" \
  "global_load_dwordx4 v[44:47], v43, %[q0] offset:0\n\t" \
  "v_mfma_f32_16x16x32_bf16 v[140:143], v[88:91], v[20:23], v[140:143]\n\t" \
  "ds_read_b64 v[8:9], v26 offset:4096\n\t" \
  "ds_read_b64 v[10:11], v27 offset:4096\n\t" \
  "v_mfma_f32_16x16x32_bf16 v[144:147], v[100:103], v[16:19], v[144:147]\n\t" \
  "s_waitcnt lgkmcnt(4)\n\t" \
  "v_cndmask_b32_e64 v152, v40, v39, vcc\n\t" \
  "v_mfma_f32_16x16x32_bf16 v[144:147], v[96:99], v[20:23], v[144:147]\n\t" \
  "ds_read_b64 v[12:13], v28 offset:5120\n\t" \
  "ds_read_b64 v[14:15], v29 offset:5120\n\t" \
  "v_mfma_f32_16x16x32_bf16 v[148:151], v[108:111], v[16:19], v[148:151]\n\t" \
  "s_waitcnt lgkmcnt(4)\n\t" \
  "v_cndmask_b32_e64 v156, v41, v40, vcc\n\t" \
  "v_mfma_f32_16x16x32_bf16 v[148:151], v[104:107], v[20:23], v[148:151]\n\t" \
  "global_load_dwordx4 v[48:51], v43, %[q0] offset:2048\n\t" \
  "v_mfma_f32_16x16x32_bf16 v[152:155], v[116:119], v[16:19], v[152:155]\n\t" \
  "ds_bpermute_b32 v34, v32, v131\n\t" \
  "v_mfma_f32_16x16x32_bf16 v[152:155], v[112:115], v[20:23], v[152:155]\n\t" \
  "v_mov_b32 v131, v130\n\t" \
  "v_mov_b32 v130, v129\n\t" \
  "v_mfma_f32_16x16x32_bf16 v[156:159], v[124:127], v[16:19], v[156:159]\n\t" \
  "global_load_dwordx4 v[52:55], v43, %[q2] offset:0\n\t" \
  "v_mfma_f32_16x16x32_bf16 v[156:159], v[120:123], v[20:23], v[156:159]\n\t" \
  "ds_read_b64 v[16:17], v30 offset:6144\n\t" \
  "ds_read_b64 v[18:19], v31 offset:6144\n\t" \
  "v_mfma_f32_16x16x32_bf16 v[132:135], v[68:71], v[0:3], v[132:135]\n\t" \
  "ds_read_b64 v[20:21], v24 offset:7168\n\t" \
  "ds_read_b64 v[22:23], v25 offset:7168\n\t" \
  "v_mfma_f32_16x16x32_bf16 v[132:135], v[64:67], v[4:7], v[132:135]\n\t" \
  "v_mov_b32 v129, v128\n\t" \
  "v_mfma_f32_16x16x32_bf16 v[136:139], v[76:79], v[0:3], v[136:139]\n\t" \
  "global_load_dwordx4 v[56:59], v43, %[q2] offset:2048\n\t" \
  "v_mfma_f32_16x16x32_bf16 v[136:139], v[72:75], v[4:7], v[136:139]\n\t" \
  "v_mfma_f32_16x16x32_bf16 v[140:143], v[84:87], v[0:3], v[140:143]\n\t" \
  "v_mfma_f32_16x16x32_bf16 v[140:143], v[80:83], v[4:7], v[140:143]\n\t" \
  "v_mfma_f32_16x16x32_bf16 v[144:147], v[92:95], v[0:3], v[144:147]\n\t" \
  "v_mfma_f32_16x16x32_bf16 v[144:147], v[88:91], v[4:7], v[144:147]\n\t" \
  "ds_bpermute_b32 v35, v32, v135\n\t" \
  "v_mfma_f32_16x16x32_bf16 v[148:151], v[100:103], v[0:3], v[148:151]\n\t" \
  "v_mov_b32 v135, v134\n\t" \
  "v_mov_b32 v134, v133\n\t" \
  "v_mfma_f32_16x16x32_bf16 v[148:151], v[96:99], v[4:7], v[148:151]\n\t" \
  "s_waitcnt lgkmcnt(5)\n\t" \
  "v_cndmask_b32_e64 v128, v34, 0, vcc\n\t" \
  "v_mfma_f32_16x16x32_bf16 v[152:155], v[108:111], v[0:3], v[152:155]\n\t" \
  "v_mov_b32 v133, v132\n\t" \
  "v_mfma_f32_16x16x32_bf16 v[152:155], v[104:107], v[4:7], v[152:155]\n\t" \
  "v_mfma_f32_16x16x32_bf16 v[156:159], v[116:119], v[0:3], v[156:159]\n\t" \
  "ds_read_b64 v[0:1], v26 offset:8192\n\t" \
  "ds_read_b64 v[2:3], v27 offset:8192\n\t" \
  "v_mfma_f32_16x16x32_bf16 v[156:159], v[112:115], v[4:7], v[156:159]\n\t" \
  "ds_read_b64 v[4:5], v28 offset:9216\n\t" \
  "ds_read_b64 v[6:7], v29 offset:9216\n\t" \
  "v_mfma_f32_16x16x32_bf16 v[136:139], v[68:71], v[8:11], v[136:139]\n\t" \
  "v_mfma_f32_16x16x32_bf16 v[136:139], v[64:67], v[12:15], v[136:139]\n\t" \
  "v_mfma_f32_16x16x32_bf16 v[140:143], v[76:79], v[8:11], v[140:143]\n\t" \
  "v_mfma_f32_16x16x32_bf16 v[140:143], v[72:75], v[12:15], v[140:143]\n\t" \
  "v_mfma_f32_16x16x32_bf16 v[144:147], v[84:87], v[8:11], v[144:147]\n\t" \
  "s_waitcnt lgkmcnt(4)\n\t" \
  "v_cndmask_b32_e64 v132, v35, v34, vcc\n\t" \
  "v_mfma_f32_16x16x32_bf16 v[144:147], v[80:83], v[12:15], v[144:147]\n\t" \
  "v_mfma_f32_16x16x32_bf16 v[148:151], v[92:95], v[8:11], v[148:151]\n\t" \
  "v_mfma_f32_16x16x32_bf16 v[148:151], v[88:91], v[12:15], v[148:151]\n\t" \
  "ds_bpermute_b32 v36, v32, v139\n\t" \
  "v_mfma_f32_16x16x32_bf16 v[152:155], v[100:103], v[8:11], v[152:155]\n\t" \
  "v_mov_b32 v139, v138\n\t" \
  "v_mov_b32 v138, v137\n\t" \
  "v_mfma_f32_16x16x32_bf16 v[152:155], v[96:99], v[12:15], v[152:155]\n\t" \
  "v_mov_b32 v137, v136\n\t" \
  "v_mfma_f32_16x16x32_bf16 v[156:159], v[108:111], v[8:11], v[156:159]\n\t" \
  "ds_read_b64 v[8:9], v30 offset:10240\n\t" \
  "ds_read_b64 v[10:11], v31 offset:10240\n\t" \
  "v_mfma_f32_16x16x32_bf16 v[156:159], v[104:107], v[12:15], v[156:159]\n\t" \
  "ds_read_b64 v[12:13], v24 offset:11264\n\t" \
  "ds_read_b64 v[14:15], v25 offset:11264\n\t" \
  "v_mfma_f32_16x16x32_bf16 v[140:143], v[68:71], v[16:19], v[140:143]\n\t" \
  "v_mfma_f32_16x16x32_bf16 v[140:143], v[64:67], v[20:23], v[140:143]\n\t" \
  "v_mfma_f32_16x16x32_bf16 v[144:147], v[76:79], v[16:19], v[144:147]\n\t" \
  "v_mfma_f32_16x16x32_bf16 v[144:147], v[72:75], v[20:23], v[144:147]\n\t" \
  "v_mfma_f32_16x16x32_bf16 v[148:151], v[84:87], v[16:19], v[148:151]\n\t" \
  "v_mfma_f32_16x16x32_bf16 v[148:151], v[80:83], v[20:23], v[148:151]\n\t" \
  "v_mfma_f32_16x16x32_bf16 v[152:155], v[92:95], v[16:19], v[152:155]\n\t" \
  "s_waitcnt lgkmcnt(4)\n\t" \
  "v_cndmask_b32_e64 v136, v36, v35, vcc\n\t" \
  "v_mfma_f32_16x16x32_bf16 v[152:155], v[88:91], v[20:23], v[152:155]\n\t" \
  "ds_bpermute_b32 v37, v32, v143\n\t" \
  "v_mfma_f32_16x16x32_bf16 v[156:159], v[100:103], v[16:19], v[156:159]\n\t" \
  "ds_read_b64 v[16:17], v26 offset:12288\n\t" \
  "ds_read_b64 v[18:19], v27 offset:12288\n\t" \
  "v_mfma_f32_16x16x32_bf16 v[156:159], v[96:99], v[20:23], v[156:159]\n\t" \
  "ds_read_b64 v[20:21], v28 offset:13312\n\t" \
  "ds_read_b64 v[22:23], v29 offset:13312\n\t" \
  "v_mfma_f32_16x16x32_bf16 v[144:147], v[68:71], v[0:3], v[144:147]\n\t" \
  "v_mov_b32 v143, v142\n\t" \
  "v_mov_b32 v142, v141\n\t" \
  "v_mfma_f32_16x16x32_bf16 v[144:147], v[64:67], v[4:7], v[144:147]\n\t" \
  "v_mov_b32 v141, v140\n\t" \
  "v_mfma_f32_16x16x32_bf16 v[148:151], v[76:79], v[0:3], v[148:151]\n\t" \
  "v_mfma_f32_16x16x32_bf16 v[148:151], v[72:75], v[4:7], v[148:151]\n\t" \
  "v_mfma_f32_16x16x32_bf16 v[152:155], v[84:87], v[0:3], v[152:155]\n\t" \
  "v_mfma_f32_16x16x32_bf16 v[152:155], v[80:83], v[4:7], v[152:155]\n\t" \
  "v_mfma_f32_16x16x32_bf16 v[156:159], v[92:95], v[0:3], v[156:159]\n\t" \
  "ds_read_b64 v[0:1], v30 offset:14336\n\t" \
  "ds_read_b64 v[2:3], v31 offset:14336\n\t" \
  "v_mfma_f32_16x16x32_bf16 v[156:159], v[88:91], v[4:7], v[156:159]\n\t" \
  "ds_read_b64 v[4:5], v24 offset:15360\n\t" \
  "s_waitcnt lgkmcnt(8)\n\t" \
  "v_mfma_f32_16x16x32_bf16 v[148:151], v[68:71], v[8:11], v[148:151]\n\t" \
  "ds_read_b64 v[6:7], v25 offset:15360\n\t" \
  "ds_bpermute_b32 v38, v32, v147\n\t" \
  "v_mfma_f32_16x16x32_bf16 v[148:151], v[64:67], v[12:15], v[148:151]\n\t" \
  "v_mov_b32 v147, v146\n\t" \
  "v_mov_b32 v146, v145\n\t" \
  "v_mfma_f32_16x16x32_bf16 v[152:155], v[76:79], v[8:11], v[152:155]\n\t" \
  "s_waitcnt lgkmcnt(9)\n\t" \
  "v_cndmask_b32_e64 v140, v37, v36, vcc\n\t" \
  "v_mfma_f32_16x16x32_bf16 v[152:155], v[72:75], v[12:15], v[152:155]\n\t" \
  "v_mov_b32 v145, v144\n\t" \
  "v_mfma_f32_16x16x32_bf16 v[156:159], v[84:87], v[8:11], v[156:159]\n\t" \
  "ds_read_b64 v[8:9], v24 offset:0\n\t" \
  "ds_read_b64 v[10:11], v25 offset:0\n\t" \
  "v_mfma_f32_16x16x32_bf16 v[156:159], v[80:83], v[12:15], v[156:159]\n\t" \
  "ds_read_b64 v[12:13], v26 offset:1024\n\t" \
  "ds_read_b64 v[14:15], v27 offset:1024\n\t" \
  "s_waitcnt lgkmcnt(9)\n\t" \
  "v_mfma_f32_16x16x32_bf16 v[152:155], v[68:71], v[16:19], v[152:155]\n\t" \
  "ds_bpermute_b32 v39, v32, v151\n\t" \
  "v_mfma_f32_16x16x32_bf16 v[152:155], v[64:67], v[20:23], v[152:155]\n\t" \
  "v_mov_b32 v151, v150\n\t" \
  "v_mov_b32 v150, v149\n\t" \
  "v_mfma_f32_16x16x32_bf16 v[156:159], v[76:79], v[16:19], v[156:159]\n\t" \
  "ds_read_b64 v[16:17], v28 offset:2048\n\t" \
  "ds_read_b64 v[18:19], v29 offset:2048\n\t" \
  "v_mfma_f32_16x16x32_bf16 v[156:159], v[72:75], v[20:23], v[156:159]\n\t" \
  "ds_read_b64 v[20:21], v30 offset:3072\n\t" \
  "s_waitcnt lgkmcnt(11)\n\t" \
  "v_mfma_f32_16x16x32_bf16 v[156:159], v[68:71], v[0:3], v[156:159]\n\t" \
  "ds_bpermute_b32 v40, v32, v155\n\t" \
  "s_waitcnt lgkmcnt(10)\n\t" \
  "v_mfma_f32_16x16x32_bf16 v[156:159], v[64:67], v[4:7], v[156:159]\n\t" \
  "ds_read_b64 v[22:23], v31 offset:3072\n\t" \
  "s_waitcnt lgkmcnt(8)\n\t" \
  "v_mfma_f32_16x16x32_bf16 v[128:131], v[68:71], v[8:11], v[128:131]\n\t" \
  "v_cndmask_b32_e64 v144, v38, v37, vcc\n\t" \
  "s_waitcnt lgkmcnt(6)\n\t" \
  "v_mfma_f32_16x16x32_bf16 v[128:131], v[64:67], v[12:15], v[128:131]\n\t" \
  "v_mov_b32 v149, v148\n\t" \
  "v_mov_b32 v155, v154\n\t" \
  "v_mfma_f32_16x16x32_bf16 v[132:135], v[76:79], v[8:11], v[132:135]\n\t" \
  "ds_bpermute_b32 v41, v32, v159\n\t" \
  "v_mov_b32 v154, v153\n\t" \
  "v_mfma_f32_16x16x32_bf16 v[132:135], v[72:75], v[12:15], v[132:135]\n\t" \
  "v_mov_b32 v153, v152\n\t" \
  "v_mov_b32 v159, v158\n\t" \
  "v_mfma_f32_16x16x32_bf16 v[136:139], v[84:87], v[8:11], v[136:139]\n\t" \
  "v_mov_b32 v158, v157\n\t" \
  "v_mov_b32 v157, v156\n\t" \
  "v_mfma_f32_16x16x32_bf16 v[136:139], v[80:83], v[12:15], v[136:139]\n\t" \
  "s_waitcnt lgkmcnt(6)\n\t" \
  "v_cndmask_b32_e64 v148, v39, v38, vcc\n\t" \
  "v_mfma_f32_16x16x32_bf16 v[140:143], v[92:95], v[8:11], v[140:143]\n\t" \
  "ds_read_b64 v[0:1], v24 offset:4096\n\t" \
  "ds_read_b64 v[2:3], v25 offset:4096\n\t" \
  "v_mfma_f32_16x16x32_bf16 v[140:143], v[88:91], v[12:15], v[140:143]\n\t" \
  "ds_read_b64 v[4:5], v26 offset:5120\n\t" \
  "ds_read_b64 v[6:7], v27 offset:5120\n\t" \
  "v_mfma_f32_16x16x32_bf16 v[144:147], v[100:103], v[8:11], v[144:147]\n\t" \
  "s_waitcnt lgkmcnt(6)\n\t" \
  "v_cndmask_b32_e64 v152, v40, v39, vcc\n\t" \
  "v_mfma_f32_16x16x32_bf16 v[144:147], v[96:99], v[12:15], v[144:147]\n\t" \
  "ds_bpermute_b32 v34, v32, v131\n\t" \
  "v_mfma_f32_16x16x32_bf16 v[148:151], v[108:111], v[8:11], v[148:151]\n\t" \
  "s_waitcnt lgkmcnt(5)\n\t" \
  "v_cndmask_b32_e64 v156, v41, v40, vcc\n\t" \
  "v_mfma_f32_16x16x32_bf16 v[148:151], v[104:107], v[12:15], v[148:151]\n\t" \
  "v_mov_b32 v131, v130\n\t" \
  "v_mov_b32 v130, v129\n\t" \
  "v_mfma_f32_16x16x32_bf16 v[152:155], v[116:119], v[8:11], v[152:155]\n\t" \
  "v_mov_b32 v129, v128\n\t" \
  "v_mfma_f32_16x16x32_bf16 v[152:155], v[112:115], v[12:15], v[152:155]\n\t" \
  "v_mfma_f32_16x16x32_bf16 v[156:159], v[124:127], v[8:11], v[156:159]\n\t" \
  "ds_read_b64 v[8:9], v28 offset:6144\n\t" \
  "ds_read_b64 v[10:11], v29 offset:6144\n\t" \
  "v_mfma_f32_16x16x32_bf16 v[156:159], v[120:123], v[12:15], v[156:159]\n\t" \
  "ds_read_b64 v[12:13], v30 offset:7168\n\t" \
  "ds_read_b64 v[14:15], v31 offset:7168\n\t" \
  "v_mfma_f32_16x16x32_bf16 v[132:135], v[68:71], v[16:19], v[132:135]\n\t" \
  "v_mfma_f32_16x16x32_bf16 v[132:135], v[64:67], v[20:23], v[132:135]\n\t" \
  "v_mfma_f32_16x16x32_bf16 v[136:139], v[76:79], v[16:19], v[136:139]\n\t" \
  "v_mfma_f32_16x16x32_bf16 v[136:139], v[72:75], v[20:23], v[136:139]\n\t" \
  "v_mfma_f32_16x16x32_bf16 v[140:143], v[84:87], v[16:19], v[140:143]\n\t" \
  "s_waitcnt lgkmcnt(4)\n\t" \
  "v_cndmask_b32_e64 v128, v34, 0, vcc\n\t" \
  "v_mfma_f32_16x16x32_bf16 v[140:143], v[80:83], v[20:23], v[140:143]\n\t" \
  "v_mfma_f32_16x16x32_bf16 v[144:147], v[92:95], v[16:19], v[144:147]\n\t" \
  "v_mfma_f32_16x16x32_bf16 v[144:147], v[88:91], v[20:23], v[144:147]\n\t" \
  "ds_bpermute_b32 v35, v32, v135\n\t" \
  "v_mfma_f32_16x16x32_bf16 v[148:151], v[100:103], v[16:19], v[148:151]\n\t" \
  "v_mov_b32 v135, v134\n\t" \
  "v_mov_b32 v134, v133\n\t" \
  "v_mfma_f32_16x16x32_bf16 v[148:151], v[96:99], v[20:23], v[148:151]\n\t" \
  "v_mov_b32 v133, v132\n\t" \
  "v_mfma_f32_16x16x32_bf16 v[152:155], v[108:111], v[16:19], v[152:155]\n\t" \
  "v_mfma_f32_16x16x32_bf16 v[152:155], v[104:107], v[20:23], v[152:155]\n\t" \
  "v_mfma_f32_16x16x32_bf16 v[156:159], v[116:119], v[16:19], v[156:159]\n\t" \
  "ds_read_b64 v[16:17], v24 offset:8192\n\t" \
  "ds_read_b64 v[18:19], v25 offset:8192\n\t" \
  "v_mfma_f32_16x16x32_bf16 v[156:159], v[112:115], v[20:23], v[156:159]\n\t" \
  "ds_read_b64 v[20:21], v26 offset:9216\n\t" \
  "ds_read_b64 v[22:23], v27 offset:9216\n\t" \
  "v_mfma_f32_16x16x32_bf16 v[136:139], v[68:71], v[0:3], v[136:139]\n\t" \
  "v_mfma_f32_16x16x32_bf16 v[136:139], v[64:67], v[4:7], v[136:139]\n\t" \
  "v_mfma_f32_16x16x32_bf16 v[140:143], v[76:79], v[0:3], v[140:143]\n\t" \
  "v_mfma_f32_16x16x32_bf16 v[140:143], v[72:75], v[4:7], v[140:143]\n\t" \
  "v_mfma_f32_16x16x32_bf16 v[144:147], v[84:87], v[0:3], v[144:147]\n\t" \
  "s_waitcnt lgkmcnt(4)\n\t" \
  "v_cndmask_b32_e64 v132, v35, v34, vcc\n\t" \
  "v_mfma_f32_16x16x32_bf16 v[144:147], v[80:83], v[4:7], v[144:147]\n\t" \
  "v_mfma_f32_16x16x32_bf16 v[148:151], v[92:95], v[0:3], v[148:151]\n\t" \
  "v_mfma_f32_16x16x32_bf16 v[148:151], v[88:91], v[4:7], v[148:151]\n\t" \
  "ds_bpermute_b32 v36, v32, v139\n\t" \
  "v_mfma_f32_16x16x32_bf16 v[152:155], v[100:103], v[0:3], v[152:155]\n\t" \
  "v_mov_b32 v139, v138\n\t" \
  "v_mov_b32 v138, v137\n\t" \
  "v_mfma_f32_16x16x32_bf16 v[152:155], v[96:99], v[4:7], v[152:155]\n\t" \
  "v_mov_b32 v137, v136\n\t" \
  "v_mfma_f32_16x16x32_bf16 v[156:159], v[108:111], v[0:3], v[156:159]\n\t" \
  "ds_read_b64 v[0:1], v28 offset:10240\n\t" \
  "ds_read_b64 v[2:3], v29 offset:10240\n\t" \
  "v_mfma_f32_16x16x32_bf16 v[156:159], v[104:107], v[4:7], v[156:159]\n\t" \
  "ds_read_b64 v[4:5], v30 offset:11264\n\t" \
  "ds_read_b64 v[6:7], v31 offset:11264\n\t" \
  "v_mfma_f32_16x16x32_bf16 v[140:143], v[68:71], v[8:11], v[140:143]\n\t" \
  "v_mfma_f32_16x16x32_bf16 v[140:143], v[64:67], v[12:15], v[140:143]\n\t" \
  "v_mfma_f32_16x16x32_bf16 v[144:147], v[76:79], v[8:11], v[144:147]\n\t" \
  "v_mfma_f32_16x16x32_bf16 v[144:147], v[72:75], v[12:15], v[144:147]\n\t" \
  "v_mfma_f32_16x16x32_bf16 v[148:151], v[84:87], v[8:11], v[148:151]\n\t" \
  "v_mfma_f32_16x16x32_bf16 v[148:151], v[80:83], v[12:15], v[148:151]\n\t" \
  "v_mfma_f32_16x16x32_bf16 v[152:155], v[92:95], v[8:11], v[152:155]\n\t" \
  "s_waitcnt lgkmcnt(4)\n\t" \
  "v_cndmask_b32_e64 v136, v36, v35, vcc\n\t" \
  "v_mfma_f32_16x16x32_bf16 v[152:155], v[88:91], v[12:15], v[152:155]\n\t" \
  "ds_bpermute_b32 v37, v32, v143\n\t" \
  "v_mfma_f32_16x16x32_bf16 v[156:159], v[100:103], v[8:11], v[156:159]\n\t" \
  "ds_read_b64 v[8:9], v24 offset:12288\n\t" \
  "ds_read_b64 v[10:11], v25 offset:12288\n\t" \
  "v_mfma_f32_16x16x32_bf16 v[156:159], v[96:99], v[12:15], v[156:159]\n\t" \
  "ds_read_b64 v[12:13], v26 offset:13312\n\t" \
  "ds_read_b64 v[14:15], v27 offset:13312\n\t" \
  "v_mfma_f32_16x16x32_bf16 v[144:147], v[68:71], v[16:19], v[144:147]\n\t" \
  "v_mov_b32 v143, v142\n\t" \
  "v_mov_b32 v142, v141\n\t" \
  "v_mfma_f32_16x16x32_bf16 v[144:147], v[64:67], v[20:23], v[144:147]\n\t" \
  "v_mov_b32 v141, v140\n\t" \
  "v_mfma_f32_16x16x32_bf16 v[148:151], v[76:79], v[16:19], v[148:151]\n\t" \
  "v_mfma_f32_16x16x32_bf16 v[148:151], v[72:75], v[20:23], v[148:151]\n\t" \
  "v_mfma_f32_16x16x32_bf16 v[152:155], v[84:87], v[16:19], v[152:155]\n\t" \
  "v_mfma_f32_16x16x32_bf16 v[152:155], v[80:83], v[20:23], v[152:155]\n\t" \
  "v_mfma_f32_16x16x32_bf16 v[156:159], v[92:95], v[16:19], v[156:159]\n\t" \
  "ds_read_b64 v[16:17], v28 offset:14336\n\t" \
  "ds_read_b64 v[18:19], v29 offset:14336\n\t" \
  "v_mfma_f32_16x16x32_bf16 v[156:159], v[88:91], v[20:23], v[156:159]\n\t" \
  "ds_read_b64 v[20:21], v30 offset:15360\n\t" \
  "s_waitcnt lgkmcnt(8)\n\t" \
  "v_mfma_f32_16x16x32_bf16 v[148:151], v[68:71], v[0:3], v[148:151]\n\t" \
  "ds_read_b64 v[22:23], v31 offset:15360\n\t" \
  "ds_bpermute_b32 v38, v32, v147\n\t" \
  "v_mfma_f32_16x16x32_bf16 v[148:151], v[64:67], v[4:7], v[148:151]\n\t" \
  "v_mov_b32 v147, v146\n\t" \
  "v_mov_b32 v146, v145\n\t" \
  "v_mfma_f32_16x16x32_bf16 v[152:155], v[76:79], v[0:3], v[152:155]\n\t" \
  "s_waitcnt lgkmcnt(9)\n\t" \
  "v_cndmask_b32_e64 v140, v37, v36, vcc\n\t" \
  "v_mfma_f32_16x16x32_bf16 v[152:155], v[72:75], v[4:7], v[152:155]\n\t" \
  "v_mov_b32 v145, v144\n\t" \
  "v_mfma_f32_16x16x32_bf16 v[156:159], v[84:87], v[0:3], v[156:159]\n\t" \
  "ds_read_b64 v[0:1], v30 offset:0\n\t" \
  "ds_read_b64 v[2:3], v31 offset:0\n\t" \
  "v_mfma_f32_16x16x32_bf16 v[156:159], v[80:83], v[4:7], v[156:159]\n\t" \
  "ds_read_b64 v[4:5], v24 offset:1024\n\t" \
  "ds_read_b64 v[6:7], v25 offset:1024\n\t" \
  "s_waitcnt lgkmcnt(9)\n\t" \
  "v_mfma_f32_16x16x32_bf16 v[152:155], v[68:71], v[8:11], v[152:155]\n\t" \
  "ds_bpermute_b32 v39, v32, v151\n\t" \
  "v_mfma_f32_16x16x32_bf16 v[152:155], v[64:67], v[12:15], v[152:155]\n\t" \
  "v_mov_b32 v151, v150\n\t" \
  "v_mov_b32 v150, v149\n\t" \
  "v_mfma_f32_16x16x32_bf16 v[156:159], v[76:79], v[8:11], v[156:159]\n\t" \
  "ds_read_b64 v[8:9], v26 offset:2048\n\t" \
  "ds_read_b64 v[10:11], v27 offset:2048\n\t" \
  "v_mfma_f32_16x16x32_bf16 v[156:159], v[72:75], v[12:15], v[156:159]\n\t" \
  "ds_read_b64 v[12:13], v28 offset:3072\n\t" \
  "s_waitcnt lgkmcnt(11)\n\t" \
  "v_mfma_f32_16x16x32_bf16 v[156:159], v[68:71], v[16:19], v[156:159]\n\t" \
  "ds_bpermute_b32 v40, v32, v155\n\t" \
  "s_waitcnt lgkmcnt(10)\n\t" \
  "v_mfma_f32_16x16x32_bf16 v[156:159], v[64:67], v[20:23], v[156:159]\n\t" \
  "ds_read_b64 v[14:15], v29 offset:3072\n\t" \
  "s_waitcnt lgkmcnt(8)\n\t" \
  "v_mfma_f32_16x16x32_bf16 v[128:131], v[68:71], v[0:3], v[128:131]\n\t" \
  "v_cndmask_b32_e64 v144, v38, v37, vcc\n\t" \
  "s_waitcnt lgkmcnt(6)\n\t" \
  "v_mfma_f32_16x16x32_bf16 v[128:131], v[64:67], v[4:7], v[128:131]\n\t" \
  "v_mov_b32 v149, v148\n\t" \
  "v_mov_b32 v155, v154\n\t" \
  "v_mfma_f32_16x16x32_bf16 v[132:135], v[76:79], v[0:3], v[132:135]\n\t" \
  "ds_bpermute_b32 v41, v32, v159\n\t" \
  "v_mov_b32 v154, v153\n\t" \
  "v_mfma_f32_16x16x32_bf16 v[132:135], v[72:75], v[4:7], v[132:135]\n\t" \
  "v_mov_b32 v153, v152\n\t" \
  "v_mov_b32 v159, v158\n\t" \
  "v_mfma_f32_16x16x32_bf16 v[136:139], v[84:87], v[0:3], v[136:139]\n\t" \
  "v_mov_b32 v158, v157\n\t" \
  "v_mov_b32 v157, v156\n\t" \
  "v_mfma_f32_16x16x32_bf16 v[136:139], v[80:83], v[4:7], v[136:139]\n\t" \
  "s_waitcnt lgkmcnt(6)\n\t" \
  "v_cndmask_b32_e64 v148, v39, v38, vcc\n\t" \
  "v_mfma_f32_16x16x32_bf16 v[140:143], v[92:95], v[0:3], v[140:143]\n\t" \
  "ds_read_b64 v[16:17], v30 offset:4096\n\t" \
  "ds_read_b64 v[18:19], v31 offset:4096\n\t" \
  "v_mfma_f32_16x16x32_bf16 v[140:143], v[88:91], v[4:7], v[140:143]\n\t" \
  "ds_read_b64 v[20:21], v24 offset:5120\n\t" \
  "ds_read_b64 v[22:23], v25 offset:5120\n\t" \
  "v_mfma_f32_16x16x32_bf16 v[144:147], v[100:103], v[0:3], v[144:147]\n\t" \
  "s_waitcnt lgkmcnt(6)\n\t" \
  "v_cndmask_b32_e64 v152, v40, v39, vcc\n\t" \
  "v_mfma_f32_16x16x32_bf16 v[144:147], v[96:99], v[4:7], v[144:147]\n\t" \
  "s_mov_b64 exec, 1\n\t" \
  "ds_write_b32 v33, v33\n\t" \
  "s_mov_b64 exec, -1\n\t" \
  "v_mfma_f32_16x16x32_bf16 v[148:151], v[108:111], v[0:3], v[148:151]\n\t" \
  "s_waitcnt lgkmcnt(5)\n\t" \
  "v_cndmask_b32_e64 v156, v41, v40, vcc\n\t" \
  "v_mfma_f32_16x16x32_bf16 v[148:151], v[104:107], v[4:7], v[148:151]\n\t" \
  "v_cvt_pk_bf16_f32 v34, v128, v129\n\t" \
  "v_cvt_pk_bf16_f32 v35, v130, v131\n\t" \
  "v_mfma_f32_16x16x32_bf16 v[152:155], v[116:119], v[0:3], v[152:155]\n\t" \
  "v_mfma_f32_16x16x32_bf16 v[152:155], v[112:115], v[4:7], v[152:155]\n\t" \
  "v_mfma_f32_16x16x32_bf16 v[156:159], v[124:127], v[0:3], v[156:159]\n\t" \
  "ds_read_b64 v[0:1], v26 offset:6144\n\t" \
  "ds_read_b64 v[2:3], v27 offset:6144\n\t" \
  "v_mfma_f32_16x16x32_bf16 v[156:159], v[120:123], v[4:7], v[156:159]\n\t" \
  "ds_read_b64 v[4:5], v28 offset:7168\n\t" \
  "ds_read_b64 v[6:7], v29 offset:7168\n\t" \
  "v_mfma_f32_16x16x32_bf16 v[132:135], v[68:71], v[8:11], v[132:135]\n\t" \
  "v_mfma_f32_16x16x32_bf16 v[132:135], v[64:67], v[12:15], v[132:135]\n\t" \
  "v_mfma_f32_16x16x32_bf16 v[136:139], v[76:79], v[8:11], v[136:139]\n\t" \
  "v_mfma_f32_16x16x32_bf16 v[136:139], v[72:75], v[12:15], v[136:139]\n\t" \
  "v_mfma_f32_16x16x32_bf16 v[140:143], v[84:87], v[8:11], v[140:143]\n\t" \
  "v_mfma_f32_16x16x32_bf16 v[140:143], v[80:83], v[12:15], v[140:143]\n\t" \
  "v_mfma_f32_16x16x32_bf16 v[144:147], v[92:95], v[8:11], v[144:147]\n\t" \
  "v_mfma_f32_16x16x32_bf16 v[144:147], v[88:91], v[12:15], v[144:147]\n\t" \
  "v_mfma_f32_16x16x32_bf16 v[148:151], v[100:103], v[8:11], v[148:151]\n\t" \
  "v_mfma_f32_16x16x32_bf16 v[148:151], v[96:99], v[12:15], v[148:151]\n\t" \
  "v_cvt_pk_bf16_f32 v36, v132, v133\n\t" \
  "v_cvt_pk_bf16_f32 v37, v134, v135\n\t" \
  "v_mfma_f32_16x16x32_bf16 v[152:155], v[108:111], v[8:11], v[152:155]\n\t" \
  "v_mfma_f32_16x16x32_bf16 v[152:155], v[104:107], v[12:15], v[152:155]\n\t" \
  "v_permlane16_swap_b32 v34, v36\n\t" \
  "v_permlane16_swap_b32 v35, v37\n\t" \
  "v_mfma_f32_16x16x32_bf16 v[156:159], v[116:119], v[8:11], v[156:159]\n\t" \
  "global_store_dwordx4 v42, v[34:37], %[yb] offset:0 sc1\n\t" \
  "v_mfma_f32_16x16x32_bf16 v[156:159], v[112:115], v[12:15], v[156:159]\n\t" \
  "ds_read_b64 v[8:9], v30 offset:8192\n\t" \
  "s_waitcnt lgkmcnt(1)\n\t" \
  "v_mfma_f32_16x16x32_bf16 v[136:139], v[68:71], v[16:19], v[136:139]\n\t" \
  "ds_read_b64 v[10:11], v31 offset:8192\n\t" \
  "ds_read_b64 v[12:13], v24 offset:9216\n\t" \
  "v_mfma_f32_16x16x32_bf16 v[136:139], v[64:67], v[20:23], v[136:139]\n\t" \
  "ds_read_b64 v[14:15], v25 offset:9216\n\t" \
  "v_mfma_f32_16x16x32_bf16 v[140:143], v[76:79], v[16:19], v[140:143]\n\t" \
  "v_mfma_f32_16x16x32_bf16 v[140:143], v[72:75], v[20:23], v[140:143]\n\t" \
  "v_mfma_f32_16x16x32_bf16 v[144:147], v[84:87], v[16:19], v[144:147]\n\t" \
  "v_mfma_f32_16x16x32_bf16 v[144:147], v[80:83], v[20:23], v[144:147]\n\t" \
  "v_mfma_f32_16x16x32_bf16 v[148:151], v[92:95], v[16:19], v[148:151]\n\t" \
  "v_mfma_f32_16x16x32_bf16 v[148:151], v[88:91], v[20:23], v[148:151]\n\t" \
  "v_mfma_f32_16x16x32_bf16 v[152:155], v[100:103], v[16:19], v[152:155]\n\t" \
  "v_cvt_pk_bf16_f32 v38, v136, v137\n\t" \
  "v_cvt_pk_bf16_f32 v39, v138, v139\n\t" \
  "v_mfma_f32_16x16x32_bf16 v[152:155], v[96:99], v[20:23], v[152:155]\n\t" \
  "v_mfma_f32_16x16x32_bf16 v[156:159], v[108:111], v[16:19], v[156:159]\n\t" \
  "ds_read_b64 v[16:17], v26 offset:10240\n\t" \
  "ds_read_b64 v[18:19], v27 offset:10240\n\t" \
  "v_mfma_f32_16x16x32_bf16 v[156:159], v[104:107], v[20:23], v[156:159]\n\t" \
  "ds_read_b64 v[20:21], v28 offset:11264\n\t" \
  "ds_read_b64 v[22:23], v29 offset:11264\n\t" \
  "v_mfma_f32_16x16x32_bf16 v[140:143], v[68:71], v[0:3], v[140:143]\n\t" \
  "v_mfma_f32_16x16x32_bf16 v[140:143], v[64:67], v[4:7], v[140:143]\n\t" \
  "v_mfma_f32_16x16x32_bf16 v[144:147], v[76:79], v[0:3], v[144:147]\n\t" \
  "v_mfma_f32_16x16x32_bf16 v[144:147], v[72:75], v[4:7], v[144:147]\n\t" \
  "v_mfma_f32_16x16x32_bf16 v[148:151], v[84:87], v[0:3], v[148:151]\n\t" \
  "v_mfma_f32_16x16x32_bf16 v[148:151], v[80:83], v[4:7], v[148:151]\n\t" \
  "v_mfma_f32_16x16x32_bf16 v[152:155], v[92:95], v[0:3], v[152:155]\n\t" \
  "v_mfma_f32_16x16x32_bf16 v[152:155], v[88:91], v[4:7], v[152:155]\n\t" \
  "v_mfma_f32_16x16x32_bf16 v[156:159], v[100:103], v[0:3], v[156:159]\n\t" \
  "ds_read_b64 v[0:1], v30 offset:12288\n\t" \
  "v_cvt_pk_bf16_f32 v40, v140, v141\n\t" \
  "v_mfma_f32_16x16x32_bf16 v[156:159], v[96:99], v[4:7], v[156:159]\n\t" \
  "v_cvt_pk_bf16_f32 v41, v142, v143\n\t" \
  "s_waitcnt lgkmcnt(3)\n\t" \
  "v_mfma_f32_16x16x32_bf16 v[144:147], v[68:71], v[8:11], v[144:147]\n\t" \
  "ds_read_b64 v[2:3], v31 offset:12288\n\t" \
  "ds_read_b64 v[4:5], v24 offset:13312\n\t" \
  "v_mfma_f32_16x16x32_bf16 v[144:147], v[64:67], v[12:15], v[144:147]\n\t" \
  "v_permlane16_swap_b32 v38, v40\n\t" \
  "v_permlane16_swap_b32 v39, v41\n\t" \
  "v_mfma_f32_16x16x32_bf16 v[148:151], v[76:79], v[8:11], v[148:151]\n\t" \
  "ds_read_b64 v[6:7], v25 offset:13312\n\t" \
  "v_mfma_f32_16x16x32_bf16 v[148:151], v[72:75], v[12:15], v[148:151]\n\t" \
  "global_store_dwordx4 v42, v[38:41], %[yb] offset:1024 sc1\n\t" \
  "v_mfma_f32_16x16x32_bf16 v[152:155], v[84:87], v[8:11], v[152:155]\n\t" \
  "v_mfma_f32_16x16x32_bf16 v[152:155], v[80:83], v[12:15], v[152:155]\n\t" \
  "v_cvt_pk_bf16_f32 v34, v144, v145\n\t" \
  "v_cvt_pk_bf16_f32 v35, v146, v147\n\t" \
  "v_mfma_f32_16x16x32_bf16 v[156:159], v[92:95], v[8:11], v[156:159]\n\t" \
  "ds_read_b64 v[8:9], v26 offset:14336\n\t" \
  "ds_read_b64 v[10:11], v27 offset:14336\n\t" \
  "v_mfma_f32_16x16x32_bf16 v[156:159], v[88:91], v[12:15], v[156:159]\n\t" \
  "ds_read_b64 v[12:13], v28 offset:15360\n\t" \
  "ds_read_b64 v[14:15], v29 offset:15360\n\t" \
  "v_mfma_f32_16x16x32_bf16 v[148:151], v[68:71], v[16:19], v[148:151]\n\t" \
  "s_waitcnt lgkmcnt(8)\n\t" \
  "v_mfma_f32_16x16x32_bf16 v[148:151], v[64:67], v[20:23], v[148:151]\n\t" \
  "v_mfma_f32_16x16x32_bf16 v[152:155], v[76:79], v[16:19], v[152:155]\n\t" \
  "v_mfma_f32_16x16x32_bf16 v[152:155], v[72:75], v[20:23], v[152:155]\n\t" \
  "v_mfma_f32_16x16x32_bf16 v[156:159], v[84:87], v[16:19], v[156:159]\n\t" \
  "v_mfma_f32_16x16x32_bf16 v[156:159], v[80:83], v[20:23], v[156:159]\n\t" \
  "s_waitcnt lgkmcnt(4)\n\t" \
  "v_mfma_f32_16x16x32_bf16 v[152:155], v[68:71], v[0:3], v[152:155]\n\t" \
  "v_mfma_f32_16x16x32_bf16 v[152:155], v[64:67], v[4:7], v[152:155]\n\t" \
  "v_mfma_f32_16x16x32_bf16 v[156:159], v[76:79], v[0:3], v[156:159]\n\t" \
  "v_cvt_pk_bf16_f32 v36, v148, v149\n\t" \
  "v_cvt_pk_bf16_f32 v37, v150, v151\n\t" \
  "v_mfma_f32_16x16x32_bf16 v[156:159], v[72:75], v[4:7], v[156:159]\n\t" \
  "s_waitcnt lgkmcnt(2)\n\t" \
  "v_mfma_f32_16x16x32_bf16 v[156:159], v[68:71], v[8:11], v[156:159]\n\t" \
  "v_permlane16_swap_b32 v34, v36\n\t" \
  "s_waitcnt lgkmcnt(0)\n\t" \
  "v_mfma_f32_16x16x32_bf16 v[156:159], v[64:67], v[12:15], v[156:159]\n\t" \
  "v_permlane16_swap_b32 v35, v37\n\t" \
  "global_store_dwordx4 v42, v[34:37], %[yb] offset:2048 sc1\n\t" \
  "s_waitcnt lgkmcnt(0)\n\t" \
  "s_nop 4\n\t" \
  "v_cvt_pk_bf16_f32 v38, v152, v153\n\t" \
  "v_cvt_pk_bf16_f32 v39, v154, v155\n\t" \
  "v_cvt_pk_bf16_f32 v40, v156, v157\n\t" \
  "v_cvt_pk_bf16_f32 v41, v158, v159\n\t" \
  "s_nop 1\n\t" \
  "v_permlane16_swap_b32 v38, v40\n\t" \
  "v_permlane16_swap_b32 v39, v41\n\t" \
  "global_store_dwordx4 v42, v[38:41], %[yb] offset:3072 sc1\n\t" \
  "s_nop 1\n\t"
#define CV_Q 1
#define CV_CLOB "vcc", "v0", "v1", "v2", "v3", "v4", "v5", "v6", "v7", "v8", "v9", "v10", "v11", "v12", "v13", "v14", "v15", "v16", "v17", "v18", "v19", "v20", "v21", "v22", "v23", "v34", "v35", "v36", "v37", "v38", "v39", "v40", "v41"

#define G3_ASM \
  "s_mov_b32 %[c], -1\n\t" \
  "s_getpc_b64 s[92:93]\n\t" \
  "s_branch QC_%=\n\t" \
  "QH_%=:\n\t" \
  "v_add_u32 v166, %[go], v165\n\t" \
  "ds_read_b32 v166, v166\n\t" \
  "s_addk_i32 %[cn], 1\n\t" \
  "s_cmp_gt_u32 %[cn], 0x400000\n\t" \
  "s_cbranch_scc1 QI_%=\n\t" \
  "s_waitcnt lgkmcnt(0)\n\t" \
  "v_cmp_eq_u32 vcc, 0, v166\n\t" \
  "s_cbranch_vccz QI_%=\n\t" \
  "s_sleep 1\n\t" \
  "s_getpc_b64 s[92:93]\n\t" \
  "s_branch QC_%=\n\t" \
  "s_branch QH_%=\n\t" \
  "QI_%=:\n\t" \
  "s_mov_b32 %[c], 0\n\t" \
  "s_getpc_b64 s[92:93]\n\t" \
  "s_branch QC_%=\n\t" \
  "s_setprio 0\n\t" \
  "ds_read_b32 v166, v165\n\t" \
  "ds_read_b64_tr_b16 v[96:97], v160 offset:0\n\t" \
  "ds_read_b64_tr_b16 v[98:99], v160 offset:2048\n\t" \
  "ds_read_b64_tr_b16 v[100:101], v161 offset:0\n\t" \
  "ds_read_b64_tr_b16 v[102:103], v161 offset:2048\n\t" \
  "ds_read_b64_tr_b16 v[104:105], v162 offset:0\n\t" \
  "ds_read_b64_tr_b16 v[106:107], v162 offset:2048\n\t" \
  "ds_read_b64_tr_b16 v[108:109], v163 offset:0\n\t" \
  "ds_read_b64_tr_b16 v[110:111], v163 offset:2048\n\t" \
  "ds_read_b64_tr_b16 v[112:113], v160 offset:4096\n\t" \
  "ds_read_b64_tr_b16 v[114:115], v160 offset:6144\n\t" \
  "ds_read_b64_tr_b16 v[116:117], v161 offset:4096\n\t" \
  "ds_read_b64_tr_b16 v[118:119], v161 offset:6144\n\t" \
  "ds_read_b64_tr_b16 v[120:121], v162 offset:4096\n\t" \
  "ds_read_b64_tr_b16 v[122:123], v162 offset:6144\n\t" \
  "s_waitcnt lgkmcnt(10)\n\t" \
  "v_mfma_f32_16x16x32_bf16 v[128:131], v[96:99], v[0:3], 0\n\t" \
  "ds_read_b64_tr_b16 v[124:125], v163 offset:4096\n\t" \
  "v_readlane_b32 s94, v167, 4\n\t" \
  "v_mfma_f32_16x16x32_bf16 v[144:147], v[96:99], v[4:7], 0\n\t" \
  "ds_read_b64_tr_b16 v[126:127], v163 offset:6144\n\t" \
  "v_cmp_lt_u32 vcc, %[th], v166\n\t" \
  "v_mfma_f32_16x16x32_bf16 v[132:135], v[100:103], v[0:3], 0\n\t" \
  "s_mov_b64 exec, vcc\n\t" \
  "buffer_load_dwordx4 v[64:67], v164, %[ob], s94 offen offset:0 sc1\n\t" \
  "s_mov_b64 exec, -1\n\t" \
  "v_mfma_f32_16x16x32_bf16 v[148:151], v[100:103], v[4:7], 0\n\t" \
  "ds_read_b64_tr_b16 v[96:97], v160 offset:8192\n\t" \
  "ds_read_b64_tr_b16 v[98:99], v160 offset:10240\n\t" \
  "s_waitcnt lgkmcnt(10)\n\t" \
  "v_mfma_f32_16x16x32_bf16 v[136:139], v[104:107], v[0:3], 0\n\t" \
  "s_mov_b64 exec, vcc\n\t" \
  "buffer_load_dwordx4 v[68:71], v164, %[ob], s94 offen offset:1024 sc1\n\t" \
  "s_mov_b64 exec, -1\n\t" \
  "v_mfma_f32_16x16x32_bf16 v[152:155], v[104:107], v[4:7], 0\n\t" \
  "ds_read_b64_tr_b16 v[100:101], v161 offset:8192\n\t" \
  "ds_read_b64_tr_b16 v[102:103], v161 offset:10240\n\t" \
  "v_mfma_f32_16x16x32_bf16 v[140:143], v[108:111], v[0:3], 0\n\t" \
  "s_mov_b64 exec, vcc\n\t" \
  "buffer_load_dwordx4 v[72:75], v164, %[ob], s94 offen offset:2048 sc1\n\t" \
  "s_mov_b64 exec, -1\n\t" \
  "v_mfma_f32_16x16x32_bf16 v[156:159], v[108:111], v[4:7], 0\n\t" \
  "ds_read_b64_tr_b16 v[104:105], v162 offset:8192\n\t" \
  "ds_read_b64_tr_b16 v[106:107], v162 offset:10240\n\t" \
  "s_waitcnt lgkmcnt(10)\n\t" \
  "v_mfma_f32_16x16x32_bf16 v[128:131], v[112:115], v[8:11], v[128:131]\n\t" \
  "s_mov_b64 exec, vcc\n\t" \
  "buffer_load_dwordx4 v[76:79], v164, %[ob], s94 offen offset:3072 sc1\n\t" \
  "s_mov_b64 exec, -1\n\t" \
  "v_mfma_f32_16x16x32_bf16 v[144:147], v[112:115], v[12:15], v[144:147]\n\t" \
  "ds_read_b64_tr_b16 v[108:109], v163 offset:8192\n\t" \
  "ds_read_b64_tr_b16 v[110:111], v163 offset:10240\n\t" \
  "v_mfma_f32_16x16x32_bf16 v[132:135], v[116:119], v[8:11], v[132:135]\n\t" \
  "ds_read_b64_tr_b16 v[112:113], v160 offset:12288\n\t" \
  "ds_read_b32 v166, v165\n\t" \
  "v_mfma_f32_16x16x32_bf16 v[148:151], v[116:119], v[12:15], v[148:151]\n\t" \
  "ds_read_b64_tr_b16 v[114:115], v160 offset:14336\n\t" \
  "s_waitcnt lgkmcnt(11)\n\t" \
  "v_mfma_f32_16x16x32_bf16 v[136:139], v[120:123], v[8:11], v[136:139]\n\t" \
  "ds_read_b64_tr_b16 v[116:117], v161 offset:12288\n\t" \
  "v_mfma_f32_16x16x32_bf16 v[152:155], v[120:123], v[12:15], v[152:155]\n\t" \
  "ds_read_b64_tr_b16 v[118:119], v161 offset:14336\n\t" \
  "s_sub_u32 %[iss], %[iss], vcc_lo\n\t" \
  "v_mfma_f32_16x16x32_bf16 v[140:143], v[124:127], v[8:11], v[140:143]\n\t" \
  "ds_read_b64_tr_b16 v[120:121], v162 offset:12288\n\t" \
  "v_mfma_f32_16x16x32_bf16 v[156:159], v[124:127], v[12:15], v[156:159]\n\t" \
  "ds_read_b64_tr_b16 v[122:123], v162 offset:14336\n\t" \
  "s_add_u32 %[th], %[th], 1\n\t" \
  "s_cbranch_vccz QF1_%=\n\t" \
  "s_waitcnt vmcnt(12)\n\t" \
  "QG1_%=:\n\t" \
  "s_waitcnt lgkmcnt(11)\n\t" \
  "v_mfma_f32_16x16x32_bf16 v[128:131], v[96:99], v[16:19], v[128:131]\n\t" \
  "ds_read_b64_tr_b16 v[124:125], v163 offset:12288\n\t" \
  "v_readlane_b32 s94, v167, 5\n\t" \
  "v_mfma_f32_16x16x32_bf16 v[144:147], v[96:99], v[20:23], v[144:147]\n\t" \
  "ds_read_b64_tr_b16 v[126:127], v163 offset:14336\n\t" \
  "s_waitcnt lgkmcnt(7)\n\t" \
  "v_cmp_lt_u32 vcc, %[th], v166\n\t" \
  "v_mfma_f32_16x16x32_bf16 v[132:135], v[100:103], v[16:19], v[132:135]\n\t" \
  "s_mov_b64 exec, vcc\n\t" \
  "buffer_load_dwordx4 v[0:3], v164, %[ob], s94 offen offset:0 sc1\n\t" \
  "s_mov_b64 exec, -1\n\t" \
  "v_mfma_f32_16x16x32_bf16 v[148:151], v[100:103], v[20:23], v[148:151]\n\t" \
  "ds_read_b64_tr_b16 v[96:97], v160 offset:16384\n\t" \
  "ds_read_b64_tr_b16 v[98:99], v160 offset:18432\n\t" \
  "v_mfma_f32_16x16x32_bf16 v[136:139], v[104:107], v[16:19], v[136:139]\n\t" \
  "s_mov_b64 exec, vcc\n\t" \
  "buffer_load_dwordx4 v[4:7], v164, %[ob], s94 offen offset:1024 sc1\n\t" \
  "s_mov_b64 exec, -1\n\t" \
  "v_mfma_f32_16x16x32_bf16 v[152:155], v[104:107], v[20:23], v[152:155]\n\t" \
  "ds_read_b64_tr_b16 v[100:101], v161 offset:16384\n\t" \
  "ds_read_b64_tr_b16 v[102:103], v161 offset:18432\n\t" \
  "v_mfma_f32_16x16x32_bf16 v[140:143], v[108:111], v[16:19], v[140:143]\n\t" \
  "s_mov_b64 exec, vcc\n\t" \
  "buffer_load_dwordx4 v[8:11], v164, %[ob], s94 offen offset:2048 sc1\n\t" \
  "s_mov_b64 exec, -1\n\t" \
  "v_mfma_f32_16x16x32_bf16 v[156:159], v[108:111], v[20:23], v[156:159]\n\t" \
  "ds_read_b64_tr_b16 v[104:105], v162 offset:16384\n\t" \
  "ds_read_b64_tr_b16 v[106:107], v162 offset:18432\n\t" \
  "s_waitcnt lgkmcnt(10)\n\t" \
  "v_mfma_f32_16x16x32_bf16 v[128:131], v[112:115], v[24:27], v[128:131]\n\t" \
  "s_mov_b64 exec, vcc\n\t" \
  "buffer_load_dwordx4 v[12:15], v164, %[ob], s94 offen offset:3072 sc1\n\t" \
  "s_mov_b64 exec, -1\n\t" \
  "v_mfma_f32_16x16x32_bf16 v[144:147], v[112:115], v[28:31], v[144:147]\n\t" \
  "ds_read_b64_tr_b16 v[108:109], v163 offset:16384\n\t" \
  "ds_read_b64_tr_b16 v[110:111], v163 offset:18432\n\t" \
  "v_mfma_f32_16x16x32_bf16 v[132:135], v[116:119], v[24:27], v[132:135]\n\t" \
  "ds_read_b64_tr_b16 v[112:113], v160 offset:20480\n\t" \
  "ds_read_b32 v166, v165\n\t" \
  "v_mfma_f32_16x16x32_bf16 v[148:151], v[116:119], v[28:31], v[148:151]\n\t" \
  "ds_read_b64_tr_b16 v[114:115], v160 offset:22528\n\t" \
  "s_waitcnt lgkmcnt(11)\n\t" \
  "v_mfma_f32_16x16x32_bf16 v[136:139], v[120:123], v[24:27], v[136:139]\n\t" \
  "ds_read_b64_tr_b16 v[116:117], v161 offset:20480\n\t" \
  "v_mfma_f32_16x16x32_bf16 v[152:155], v[120:123], v[28:31], v[152:155]\n\t" \
  "ds_read_b64_tr_b16 v[118:119], v161 offset:22528\n\t" \
  "s_sub_u32 %[iss], %[iss], vcc_lo\n\t" \
  "v_mfma_f32_16x16x32_bf16 v[140:143], v[124:127], v[24:27], v[140:143]\n\t" \
  "ds_read_b64_tr_b16 v[120:121], v162 offset:20480\n\t" \
  "v_mfma_f32_16x16x32_bf16 v[156:159], v[124:127], v[28:31], v[156:159]\n\t" \
  "ds_read_b64_tr_b16 v[122:123], v162 offset:22528\n\t" \
  "s_add_u32 %[th], %[th], 1\n\t" \
  "s_cbranch_vccz QF2_%=\n\t" \
  "s_waitcnt vmcnt(12)\n\t" \
  "QG2_%=:\n\t" \
  "s_waitcnt lgkmcnt(11)\n\t" \
  "v_mfma_f32_16x16x32_bf16 v[128:131], v[96:99], v[32:35], v[128:131]\n\t" \
  "ds_read_b64_tr_b16 v[124:125], v163 offset:20480\n\t" \
  "v_readlane_b32 s94, v167, 6\n\t" \
  "v_mfma_f32_16x16x32_bf16 v[144:147], v[96:99], v[36:39], v[144:147]\n\t" \
  "ds_read_b64_tr_b16 v[126:127], v163 offset:22528\n\t" \
  "s_waitcnt lgkmcnt(7)\n\t" \
  "v_cmp_lt_u32 vcc, %[th], v166\n\t" \
  "v_mfma_f32_16x16x32_bf16 v[132:135], v[100:103], v[32:35], v[132:135]\n\t" \
  "s_mov_b64 exec, vcc\n\t" \
  "buffer_load_dwordx4 v[16:19], v164, %[ob], s94 offen offset:0 sc1\n\t" \
  "s_mov_b64 exec, -1\n\t" \
  "v_mfma_f32_16x16x32_bf16 v[148:151], v[100:103], v[36:39], v[148:151]\n\t" \
  "ds_read_b64_tr_b16 v[96:97], v160 offset:24576\n\t" \
  "ds_read_b64_tr_b16 v[98:99], v160 offset:26624\n\t" \
  "v_mfma_f32_16x16x32_bf16 v[136:139], v[104:107], v[32:35], v[136:139]\n\t" \
  "s_mov_b64 exec, vcc\n\t" \
  "buffer_load_dwordx4 v[20:23], v164, %[ob], s94 offen offset:1024 sc1\n\t" \
  "s_mov_b64 exec, -1\n\t" \
  "v_mfma_f32_16x16x32_bf16 v[152:155], v[104:107], v[36:39], v[152:155]\n\t" \
  "ds_read_b64_tr_b16 v[100:101], v161 offset:24576\n\t" \
  "ds_read_b64_tr_b16 v[102:103], v161 offset:26624\n\t" \
  "v_mfma_f32_16x16x32_bf16 v[140:143], v[108:111], v[32:35], v[140:143]\n\t" \
  "s_mov_b64 exec, vcc\n\t" \
  "buffer_load_dwordx4 v[24:27], v164, %[ob], s94 offen offset:2048 sc1\n\t" \
  "s_mov_b64 exec, -1\n\t" \
  "v_mfma_f32_16x16x32_bf16 v[156:159], v[108:111], v[36:39], v[156:159]\n\t" \
  "ds_read_b64_tr_b16 v[104:105], v162 offset:24576\n\t" \
  "ds_read_b64_tr_b16 v[106:107], v162 offset:26624\n\t" \
  "s_waitcnt lgkmcnt(10)\n\t" \
  "v_mfma_f32_16x16x32_bf16 v[128:131], v[112:115], v[40:43], v[128:131]\n\t" \
  "s_mov_b64 exec, vcc\n\t" \
  "buffer_load_dwordx4 v[28:31], v164, %[ob], s94 offen offset:3072 sc1\n\t" \
  "s_mov_b64 exec, -1\n\t" \
  "v_mfma_f32_16x16x32_bf16 v[144:147], v[112:115], v[44:47], v[144:147]\n\t" \
  "ds_read_b64_tr_b16 v[108:109], v163 offset:24576\n\t" \
  "ds_read_b64_tr_b16 v[110:111], v163 offset:26624\n\t" \
  "v_mfma_f32_16x16x32_bf16 v[132:135], v[116:119], v[40:43], v[132:135]\n\t" \
  "ds_read_b64_tr_b16 v[112:113], v160 offset:28672\n\t" \
  "ds_read_b32 v166, v165\n\t" \
  "v_mfma_f32_16x16x32_bf16 v[148:151], v[116:119], v[44:47], v[148:151]\n\t" \
  "ds_read_b64_tr_b16 v[114:115], v160 offset:30720\n\t" \
  "s_waitcnt lgkmcnt(11)\n\t" \
  "v_mfma_f32_16x16x32_bf16 v[136:139], v[120:123], v[40:43], v[136:139]\n\t" \
  "ds_read_b64_tr_b16 v[116:117], v161 offset:28672\n\t" \
  "v_mfma_f32_16x16x32_bf16 v[152:155], v[120:123], v[44:47], v[152:155]\n\t" \
  "ds_read_b64_tr_b16 v[118:119], v161 offset:30720\n\t" \
  "s_sub_u32 %[iss], %[iss], vcc_lo\n\t" \
  "v_mfma_f32_16x16x32_bf16 v[140:143], v[124:127], v[40:43], v[140:143]\n\t" \
  "ds_read_b64_tr_b16 v[120:121], v162 offset:28672\n\t" \
  "v_mfma_f32_16x16x32_bf16 v[156:159], v[124:127], v[44:47], v[156:159]\n\t" \
  "ds_read_b64_tr_b16 v[122:123], v162 offset:30720\n\t" \
  "s_add_u32 %[th], %[th], 1\n\t" \
  "s_cbranch_vccz QF3_%=\n\t" \
  "s_waitcnt vmcnt(12)\n\t" \
  "QG3_%=:\n\t" \
  "s_waitcnt lgkmcnt(11)\n\t" \
  "v_mfma_f32_16x16x32_bf16 v[128:131], v[96:99], v[48:51], v[128:131]\n\t" \
  "ds_read_b64_tr_b16 v[124:125], v163 offset:28672\n\t" \
  "v_readlane_b32 s94, v167, 7\n\t" \
  "v_mfma_f32_16x16x32_bf16 v[144:147], v[96:99], v[52:55], v[144:147]\n\t" \
  "ds_read_b64_tr_b16 v[126:127], v163 offset:30720\n\t" \
  "s_waitcnt lgkmcnt(7)\n\t" \
  "v_cmp_lt_u32 vcc, %[th], v166\n\t" \
  "v_mfma_f32_16x16x32_bf16 v[132:135], v[100:103], v[48:51], v[132:135]\n\t" \
  "s_mov_b64 exec, vcc\n\t" \
  "buffer_load_dwordx4 v[32:35], v164, %[ob], s94 offen offset:0 sc1\n\t" \
  "s_mov_b64 exec, -1\n\t" \
  "v_mfma_f32_16x16x32_bf16 v[148:151], v[100:103], v[52:55], v[148:151]\n\t" \
  "ds_read_b64_tr_b16 v[96:97], v160 offset:32768\n\t" \
  "ds_read_b64_tr_b16 v[98:99], v160 offset:34816\n\t" \
  "v_mfma_f32_16x16x32_bf16 v[136:139], v[104:107], v[48:51], v[136:139]\n\t" \
  "s_mov_b64 exec, vcc\n\t" \
  "buffer_load_dwordx4 v[36:39], v164, %[ob], s94 offen offset:1024 sc1\n\t" \
  "s_mov_b64 exec, -1\n\t" \
  "v_mfma_f32_16x16x32_bf16 v[152:155], v[104:107], v[52:55], v[152:155]\n\t" \
  "ds_read_b64_tr_b16 v[100:101], v161 offset:32768\n\t" \
  "ds_read_b64_tr_b16 v[102:103], v161 offset:34816\n\t" \
  "v_mfma_f32_16x16x32_bf16 v[140:143], v[108:111], v[48:51], v[140:143]\n\t" \
  "s_mov_b64 exec, vcc\n\t" \
  "buffer_load_dwordx4 v[40:43], v164, %[ob], s94 offen offset:2048 sc1\n\t" \
  "s_mov_b64 exec, -1\n\t" \
  "v_mfma_f32_16x16x32_bf16 v[156:159], v[108:111], v[52:55], v[156:159]\n\t" \
  "ds_read_b64_tr_b16 v[104:105], v162 offset:32768\n\t" \
  "ds_read_b64_tr_b16 v[106:107], v162 offset:34816\n\t" \
  "s_waitcnt lgkmcnt(10)\n\t" \
  "v_mfma_f32_16x16x32_bf16 v[128:131], v[112:115], v[56:59], v[128:131]\n\t" \
  "s_mov_b64 exec, vcc\n\t" \
  "buffer_load_dwordx4 v[44:47], v164, %[ob], s94 offen offset:3072 sc1\n\t" \
  "s_mov_b64 exec, -1\n\t" \
  "v_mfma_f32_16x16x32_bf16 v[144:147], v[112:115], v[60:63], v[144:147]\n\t" \
  "ds_read_b64_tr_b16 v[108:109], v163 offset:32768\n\t" \
  "ds_read_b64_tr_b16 v[110:111], v163 offset:34816\n\t" \
  "v_mfma_f32_16x16x32_bf16 v[132:135], v[116:119], v[56:59], v[132:135]\n\t" \
  "ds_read_b64_tr_b16 v[112:113], v160 offset:36864\n\t" \
  "ds_read_b32 v166, v165\n\t" \
  "v_mfma_f32_16x16x32_bf16 v[148:151], v[116:119], v[60:63], v[148:151]\n\t" \
  "ds_read_b64_tr_b16 v[114:115], v160 offset:38912\n\t" \
  "s_waitcnt lgkmcnt(11)\n\t" \
  "v_mfma_f32_16x16x32_bf16 v[136:139], v[120:123], v[56:59], v[136:139]\n\t" \
  "ds_read_b64_tr_b16 v[116:117], v161 offset:36864\n\t" \
  "v_mfma_f32_16x16x32_bf16 v[152:155], v[120:123], v[60:63], v[152:155]\n\t" \
  "ds_read_b64_tr_b16 v[118:119], v161 offset:38912\n\t" \
  "s_sub_u32 %[iss], %[iss], vcc_lo\n\t" \
  "v_mfma_f32_16x16x32_bf16 v[140:143], v[124:127], v[56:59], v[140:143]\n\t" \
  "ds_read_b64_tr_b16 v[120:121], v162 offset:36864\n\t" \
  "v_mfma_f32_16x16x32_bf16 v[156:159], v[124:127], v[60:63], v[156:159]\n\t" \
  "ds_read_b64_tr_b16 v[122:123], v162 offset:38912\n\t" \
  "s_add_u32 %[th], %[th], 1\n\t" \
  "s_cbranch_vccz QF4_%=\n\t" \
  "s_waitcnt vmcnt(12)\n\t" \
  "QG4_%=:\n\t" \
  "s_waitcnt lgkmcnt(11)\n\t" \
  "v_mfma_f32_16x16x32_bf16 v[128:131], v[96:99], v[64:67], v[128:131]\n\t" \
  "ds_read_b64_tr_b16 v[124:125], v163 offset:36864\n\t" \
  "v_readlane_b32 s94, v167, 8\n\t" \
  "v_mfma_f32_16x16x32_bf16 v[144:147], v[96:99], v[68:71], v[144:147]\n\t" \
  "ds_read_b64_tr_b16 v[126:127], v163 offset:38912\n\t" \
  "s_waitcnt lgkmcnt(7)\n\t" \
  "v_cmp_lt_u32 vcc, %[th], v166\n\t" \
  "v_mfma_f32_16x16x32_bf16 v[132:135], v[100:103], v[64:67], v[132:135]\n\t" \
  "s_mov_b64 exec, vcc\n\t" \
  "buffer_load_dwordx4 v[48:51], v164, %[ob], s94 offen offset:0 sc1\n\t" \
  "s_mov_b64 exec, -1\n\t" \
  "v_mfma_f32_16x16x32_bf16 v[148:151], v[100:103], v[68:71], v[148:151]\n\t" \
  "ds_read_b64_tr_b16 v[96:97], v160 offset:40960\n\t" \
  "ds_read_b64_tr_b16 v[98:99], v160 offset:43008\n\t" \
  "v_mfma_f32_16x16x32_bf16 v[136:139], v[104:107], v[64:67], v[136:139]\n\t" \
  "s_mov_b64 exec, vcc\n\t" \
  "buffer_load_dwordx4 v[52:55], v164, %[ob], s94 offen offset:1024 sc1\n\t" \
  "s_mov_b64 exec, -1\n\t" \
  "v_mfma_f32_16x16x32_bf16 v[152:155], v[104:107], v[68:71], v[152:155]\n\t" \
  "ds_read_b64_tr_b16 v[100:101], v161 offset:40960\n\t" \
  "ds_read_b64_tr_b16 v[102:103], v161 offset:43008\n\t" \
  "v_mfma_f32_16x16x32_bf16 v[140:143], v[108:111], v[64:67], v[140:143]\n\t" \
  "s_mov_b64 exec, vcc\n\t" \
  "buffer_load_dwordx4 v[56:59], v164, %[ob], s94 offen offset:2048 sc1\n\t" \
  "s_mov_b64 exec, -1\n\t" \
  "v_mfma_f32_16x16x32_bf16 v[156:159], v[108:111], v[68:71], v[156:159]\n\t" \
  "ds_read_b64_tr_b16 v[104:105], v162 offset:40960\n\t" \
  "ds_read_b64_tr_b16 v[106:107], v162 offset:43008\n\t" \
  "s_waitcnt lgkmcnt(10)\n\t" \
  "v_mfma_f32_16x16x32_bf16 v[128:131], v[112:115], v[72:75], v[128:131]\n\t" \
  "s_mov_b64 exec, vcc\n\t" \
  "buffer_load_dwordx4 v[60:63], v164, %[ob], s94 offen offset:3072 sc1\n\t" \
  "s_mov_b64 exec, -1\n\t" \
  "v_mfma_f32_16x16x32_bf16 v[144:147], v[112:115], v[76:79], v[144:147]\n\t" \
  "ds_read_b64_tr_b16 v[108:109], v163 offset:40960\n\t" \
  "ds_read_b64_tr_b16 v[110:111], v163 offset:43008\n\t" \
  "v_mfma_f32_16x16x32_bf16 v[132:135], v[116:119], v[72:75], v[132:135]\n\t" \
  "ds_read_b64_tr_b16 v[112:113], v160 offset:45056\n\t" \
  "ds_read_b32 v166, v165\n\t" \
  "v_mfma_f32_16x16x32_bf16 v[148:151], v[116:119], v[76:79], v[148:151]\n\t" \
  "ds_read_b64_tr_b16 v[114:115], v160 offset:47104\n\t" \
  "s_waitcnt lgkmcnt(11)\n\t" \
  "v_mfma_f32_16x16x32_bf16 v[136:139], v[120:123], v[72:75], v[136:139]\n\t" \
  "ds_read_b64_tr_b16 v[116:117], v161 offset:45056\n\t" \
  "v_mfma_f32_16x16x32_bf16 v[152:155], v[120:123], v[76:79], v[152:155]\n\t" \
  "ds_read_b64_tr_b16 v[118:119], v161 offset:47104\n\t" \
  "s_sub_u32 %[iss], %[iss], vcc_lo\n\t" \
  "v_mfma_f32_16x16x32_bf16 v[140:143], v[124:127], v[72:75], v[140:143]\n\t" \
  "ds_read_b64_tr_b16 v[120:121], v162 offset:45056\n\t" \
  "v_mfma_f32_16x16x32_bf16 v[156:159], v[124:127], v[76:79], v[156:159]\n\t" \
  "ds_read_b64_tr_b16 v[122:123], v162 offset:47104\n\t" \
  "s_add_u32 %[th], %[th], 1\n\t" \
  "s_cbranch_vccz QF5_%=\n\t" \
  "s_waitcnt vmcnt(12)\n\t" \
  "QG5_%=:\n\t" \
  "s_waitcnt lgkmcnt(11)\n\t" \
  "v_mfma_f32_16x16x32_bf16 v[128:131], v[96:99], v[0:3], v[128:131]\n\t" \
  "ds_read_b64_tr_b16 v[124:125], v163 offset:45056\n\t" \
  "v_readlane_b32 s94, v167, 9\n\t" \
  "v_mfma_f32_16x16x32_bf16 v[144:147], v[96:99], v[4:7], v[144:147]\n\t" \
  "ds_read_b64_tr_b16 v[126:127], v163 offset:47104\n\t" \
  "s_waitcnt lgkmcnt(7)\n\t" \
  "v_cmp_lt_u32 vcc, %[th], v166\n\t" \
  "v_mfma_f32_16x16x32_bf16 v[132:135], v[100:103], v[0:3], v[132:135]\n\t" \
  "s_mov_b64 exec, vcc\n\t" \
  "buffer_load_dwordx4 v[64:67], v164, %[ob], s94 offen offset:0 sc1\n\t" \
  "s_mov_b64 exec, -1\n\t" \
  "v_mfma_f32_16x16x32_bf16 v[148:151], v[100:103], v[4:7], v[148:151]\n\t" \
  "ds_read_b64_tr_b16 v[96:97], v160 offset:49152\n\t" \
  "ds_read_b64_tr_b16 v[98:99], v160 offset:51200\n\t" \
  "v_mfma_f32_16x16x32_bf16 v[136:139], v[104:107], v[0:3], v[136:139]\n\t" \
  "s_mov_b64 exec, vcc\n\t" \
  "buffer_load_dwordx4 v[68:71], v164, %[ob], s94 offen offset:1024 sc1\n\t" \
  "s_mov_b64 exec, -1\n\t" \
  "v_mfma_f32_16x16x32_bf16 v[152:155], v[104:107], v[4:7], v[152:155]\n\t" \
  "ds_read_b64_tr_b16 v[100:101], v161 offset:49152\n\t" \
  "ds_read_b64_tr_b16 v[102:103], v161 offset:51200\n\t" \
  "v_mfma_f32_16x16x32_bf16 v[140:143], v[108:111], v[0:3], v[140:143]\n\t" \
  "s_mov_b64 exec, vcc\n\t" \
  "buffer_load_dwordx4 v[72:75], v164, %[ob], s94 offen offset:2048 sc1\n\t" \
  "s_mov_b64 exec, -1\n\t" \
  "v_mfma_f32_16x16x32_bf16 v[156:159], v[108:111], v[4:7], v[156:159]\n\t" \
  "ds_read_b64_tr_b16 v[104:105], v162 offset:49152\n\t" \
  "ds_read_b64_tr_b16 v[106:107], v162 offset:51200\n\t" \
  "s_waitcnt lgkmcnt(10)\n\t" \
  "v_mfma_f32_16x16x32_bf16 v[128:131], v[112:115], v[8:11], v[128:131]\n\t" \
  "s_mov_b64 exec, vcc\n\t" \
  "buffer_load_dwordx4 v[76:79], v164, %[ob], s94 offen offset:3072 sc1\n\t" \
  "s_mov_b64 exec, -1\n\t" \
  "v_mfma_f32_16x16x32_bf16 v[144:147], v[112:115], v[12:15], v[144:147]\n\t" \
  "ds_read_b64_tr_b16 v[108:109], v163 offset:49152\n\t" \
  "ds_read_b64_tr_b16 v[110:111], v163 offset:51200\n\t" \
  "v_mfma_f32_16x16x32_bf16 v[132:135], v[116:119], v[8:11], v[132:135]\n\t" \
  "ds_read_b64_tr_b16 v[112:113], v160 offset:53248\n\t" \
  "ds_read_b32 v166, v165\n\t" \
  "v_mfma_f32_16x16x32_bf16 v[148:151], v[116:119], v[12:15], v[148:151]\n\t" \
  "ds_read_b64_tr_b16 v[114:115], v160 offset:55296\n\t" \
  "s_waitcnt lgkmcnt(11)\n\t" \
  "v_mfma_f32_16x16x32_bf16 v[136:139], v[120:123], v[8:11], v[136:139]\n\t" \
  "ds_read_b64_tr_b16 v[116:117], v161 offset:53248\n\t" \
  "v_mfma_f32_16x16x32_bf16 v[152:155], v[120:123], v[12:15], v[152:155]\n\t" \
  "ds_read_b64_tr_b16 v[118:119], v161 offset:55296\n\t" \
  "s_sub_u32 %[iss], %[iss], vcc_lo\n\t" \
  "v_mfma_f32_16x16x32_bf16 v[140:143], v[124:127], v[8:11], v[140:143]\n\t" \
  "ds_read_b64_tr_b16 v[120:121], v162 offset:53248\n\t" \
  "v_mfma_f32_16x16x32_bf16 v[156:159], v[124:127], v[12:15], v[156:159]\n\t" \
  "ds_read_b64_tr_b16 v[122:123], v162 offset:55296\n\t" \
  "s_add_u32 %[th], %[th], 1\n\t" \
  "s_cbranch_vccz QF6_%=\n\t" \
  "s_waitcnt vmcnt(12)\n\t" \
  "QG6_%=:\n\t" \
  "s_waitcnt lgkmcnt(11)\n\t" \
  "v_mfma_f32_16x16x32_bf16 v[128:131], v[96:99], v[16:19], v[128:131]\n\t" \
  "ds_read_b64_tr_b16 v[124:125], v163 offset:53248\n\t" \
  "v_readlane_b32 s94, v167, 10\n\t" \
  "v_mfma_f32_16x16x32_bf16 v[144:147], v[96:99], v[20:23], v[144:147]\n\t" \
  "ds_read_b64_tr_b16 v[126:127], v163 offset:55296\n\t" \
  "s_waitcnt lgkmcnt(7)\n\t" \
  "v_cmp_lt_u32 vcc, %[th], v166\n\t" \
  "v_mfma_f32_16x16x32_bf16 v[132:135], v[100:103], v[16:19], v[132:135]\n\t" \
  "s_mov_b64 exec, vcc\n\t" \
  "buffer_load_dwordx4 v[0:3], v164, %[ob], s94 offen offset:0 sc1\n\t" \
  "s_mov_b64 exec, -1\n\t" \
  "v_mfma_f32_16x16x32_bf16 v[148:151], v[100:103], v[20:23], v[148:151]\n\t" \
  "ds_read_b64_tr_b16 v[96:97], v160 offset:57344\n\t" \
  "ds_read_b64_tr_b16 v[98:99], v160 offset:59392\n\t" \
  "v_mfma_f32_16x16x32_bf16 v[136:139], v[104:107], v[16:19], v[136:139]\n\t" \
  "s_mov_b64 exec, vcc\n\t" \
  "buffer_load_dwordx4 v[4:7], v164, %[ob], s94 offen offset:1024 sc1\n\t" \
  "s_mov_b64 exec, -1\n\t" \
  "v_mfma_f32_16x16x32_bf16 v[152:155], v[104:107], v[20:23], v[152:155]\n\t" \
  "ds_read_b64_tr_b16 v[100:101], v161 offset:57344\n\t" \
  "ds_read_b64_tr_b16 v[102:103], v161 offset:59392\n\t" \
  "v_mfma_f32_16x16x32_bf16 v[140:143], v[108:111], v[16:19], v[140:143]\n\t" \
  "s_mov_b64 exec, vcc\n\t" \
  "buffer_load_dwordx4 v[8:11], v164, %[ob], s94 offen offset:2048 sc1\n\t" \
  "s_mov_b64 exec, -1\n\t" \
  "v_mfma_f32_16x16x32_bf16 v[156:159], v[108:111], v[20:23], v[156:159]\n\t" \
  "ds_read_b64_tr_b16 v[104:105], v162 offset:57344\n\t" \
  "ds_read_b64_tr_b16 v[106:107], v162 offset:59392\n\t" \
  "s_waitcnt lgkmcnt(10)\n\t" \
  "v_mfma_f32_16x16x32_bf16 v[128:131], v[112:115], v[24:27], v[128:131]\n\t" \
  "s_mov_b64 exec, vcc\n\t" \
  "buffer_load_dwordx4 v[12:15], v164, %[ob], s94 offen offset:3072 sc1\n\t" \
  "s_mov_b64 exec, -1\n\t" \
  "v_mfma_f32_16x16x32_bf16 v[144:147], v[112:115], v[28:31], v[144:147]\n\t" \
  "ds_read_b64_tr_b16 v[108:109], v163 offset:57344\n\t" \
  "ds_read_b64_tr_b16 v[110:111], v163 offset:59392\n\t" \
  "v_mfma_f32_16x16x32_bf16 v[132:135], v[116:119], v[24:27], v[132:135]\n\t" \
  "ds_read_b64_tr_b16 v[112:113], v160 offset:61440\n\t" \
  "ds_read_b32 v166, v165\n\t" \
  "v_mfma_f32_16x16x32_bf16 v[148:151], v[116:119], v[28:31], v[148:151]\n\t" \
  "ds_read_b64_tr_b16 v[114:115], v160 offset:63488\n\t" \
  "s_waitcnt lgkmcnt(11)\n\t" \
  "v_mfma_f32_16x16x32_bf16 v[136:139], v[120:123], v[24:27], v[136:139]\n\t" \
  "ds_read_b64_tr_b16 v[116:117], v161 offset:61440\n\t" \
  "v_mfma_f32_16x16x32_bf16 v[152:155], v[120:123], v[28:31], v[152:155]\n\t" \
  "ds_read_b64_tr_b16 v[118:119], v161 offset:63488\n\t" \
  "s_sub_u32 %[iss], %[iss], vcc_lo\n\t" \
  "v_mfma_f32_16x16x32_bf16 v[140:143], v[124:127], v[24:27], v[140:143]\n\t" \
  "ds_read_b64_tr_b16 v[120:121], v162 offset:61440\n\t" \
  "s_mov_b64 exec, 1\n\t" \
  "ds_write_b32 v165, v165 offset:76\n\t" \
  "s_mov_b64 exec, -1\n\t" \
  "v_mfma_f32_16x16x32_bf16 v[156:159], v[124:127], v[28:31], v[156:159]\n\t" \
  "ds_read_b64_tr_b16 v[122:123], v162 offset:63488\n\t" \
  "s_add_u32 %[th], %[th], 1\n\t" \
  "s_cbranch_vccz QF7_%=\n\t" \
  "s_waitcnt vmcnt(12)\n\t" \
  "QG7_%=:\n\t" \
  "s_waitcnt lgkmcnt(12)\n\t" \
  "v_mfma_f32_16x16x32_bf16 v[128:131], v[96:99], v[32:35], v[128:131]\n\t" \
  "ds_read_b64_tr_b16 v[124:125], v163 offset:61440\n\t" \
  "v_readlane_b32 s94, v167, 11\n\t" \
  "v_mfma_f32_16x16x32_bf16 v[144:147], v[96:99], v[36:39], v[144:147]\n\t" \
  "ds_read_b64_tr_b16 v[126:127], v163 offset:63488\n\t" \
  "s_waitcnt lgkmcnt(8)\n\t" \
  "v_cmp_lt_u32 vcc, %[th], v166\n\t" \
  "v_mfma_f32_16x16x32_bf16 v[132:135], v[100:103], v[32:35], v[132:135]\n\t" \
  "v_add_u32 v160, 0x10000, v160\n\t" \
  "s_mov_b64 exec, vcc\n\t" \
  "buffer_load_dwordx4 v[16:19], v164, %[ob], s94 offen offset:0 sc1\n\t" \
  "s_mov_b64 exec, -1\n\t" \
  "v_mfma_f32_16x16x32_bf16 v[148:151], v[100:103], v[36:39], v[148:151]\n\t" \
  "ds_read_b64_tr_b16 v[96:97], v160 offset:0\n\t" \
  "ds_read_b64_tr_b16 v[98:99], v160 offset:2048\n\t" \
  "v_mfma_f32_16x16x32_bf16 v[136:139], v[104:107], v[32:35], v[136:139]\n\t" \
  "v_add_u32 v161, 0x10000, v161\n\t" \
  "s_mov_b64 exec, vcc\n\t" \
  "buffer_load_dwordx4 v[20:23], v164, %[ob], s94 offen offset:1024 sc1\n\t" \
  "s_mov_b64 exec, -1\n\t" \
  "v_mfma_f32_16x16x32_bf16 v[152:155], v[104:107], v[36:39], v[152:155]\n\t" \
  "ds_read_b64_tr_b16 v[100:101], v161 offset:0\n\t" \
  "ds_read_b64_tr_b16 v[102:103], v161 offset:2048\n\t" \
  "v_mfma_f32_16x16x32_bf16 v[140:143], v[108:111], v[32:35], v[140:143]\n\t" \
  "v_add_u32 v162, 0x10000, v162\n\t" \
  "s_mov_b64 exec, vcc\n\t" \
  "buffer_load_dwordx4 v[24:27], v164, %[ob], s94 offen offset:2048 sc1\n\t" \
  "s_mov_b64 exec, -1\n\t" \
  "v_mfma_f32_16x16x32_bf16 v[156:159], v[108:111], v[36:39], v[156:159]\n\t" \
  "ds_read_b64_tr_b16 v[104:105], v162 offset:0\n\t" \
  "ds_read_b64_tr_b16 v[106:107], v162 offset:2048\n\t" \
  "s_waitcnt lgkmcnt(11)\n\t" \
  "v_mfma_f32_16x16x32_bf16 v[128:131], v[112:115], v[40:43], v[128:131]\n\t" \
  "v_add_u32 v163, 0x10000, v163\n\t" \
  "s_mov_b64 exec, vcc\n\t" \
  "buffer_load_dwordx4 v[28:31], v164, %[ob], s94 offen offset:3072 sc1\n\t" \
  "s_mov_b64 exec, -1\n\t" \
  "v_mfma_f32_16x16x32_bf16 v[144:147], v[112:115], v[44:47], v[144:147]\n\t" \
  "ds_read_b64_tr_b16 v[108:109], v163 offset:0\n\t" \
  "ds_read_b64_tr_b16 v[110:111], v163 offset:2048\n\t" \
  "v_mfma_f32_16x16x32_bf16 v[132:135], v[116:119], v[40:43], v[132:135]\n\t" \
  "ds_read_b64_tr_b16 v[112:113], v160 offset:4096\n\t" \
  "ds_read_b32 v166, v165\n\t" \
  "v_mfma_f32_16x16x32_bf16 v[148:151], v[116:119], v[44:47], v[148:151]\n\t" \
  "ds_read_b64_tr_b16 v[114:115], v160 offset:6144\n\t" \
  "s_waitcnt lgkmcnt(11)\n\t" \
  "v_mfma_f32_16x16x32_bf16 v[136:139], v[120:123], v[40:43], v[136:139]\n\t" \
  "ds_read_b64_tr_b16 v[116:117], v161 offset:4096\n\t" \
  "v_mfma_f32_16x16x32_bf16 v[152:155], v[120:123], v[44:47], v[152:155]\n\t" \
  "ds_read_b64_tr_b16 v[118:119], v161 offset:6144\n\t" \
  "s_sub_u32 %[iss], %[iss], vcc_lo\n\t" \
  "v_mfma_f32_16x16x32_bf16 v[140:143], v[124:127], v[40:43], v[140:143]\n\t" \
  "ds_read_b64_tr_b16 v[120:121], v162 offset:4096\n\t" \
  "v_mfma_f32_16x16x32_bf16 v[156:159], v[124:127], v[44:47], v[156:159]\n\t" \
  "ds_read_b64_tr_b16 v[122:123], v162 offset:6144\n\t" \
  "s_add_u32 %[th], %[th], 1\n\t" \
  "s_cbranch_vccz QF8_%=\n\t" \
  "s_waitcnt vmcnt(12)\n\t" \
  "QG8_%=:\n\t" \
  "s_waitcnt lgkmcnt(11)\n\t" \
  "v_mfma_f32_16x16x32_bf16 v[128:131], v[96:99], v[48:51], v[128:131]\n\t" \
  "ds_read_b64_tr_b16 v[124:125], v163 offset:4096\n\t" \
  "v_readlane_b32 s94, v167, 12\n\t" \
  "v_mfma_f32_16x16x32_bf16 v[144:147], v[96:99], v[52:55], v[144:147]\n\t" \
  "ds_read_b64_tr_b16 v[126:127], v163 offset:6144\n\t" \
  "s_waitcnt lgkmcnt(7)\n\t" \
  "v_cmp_lt_u32 vcc, %[th], v166\n\t" \
  "v_mfma_f32_16x16x32_bf16 v[132:135], v[100:103], v[48:51], v[132:135]\n\t" \
  "s_mov_b64 exec, vcc\n\t" \
  "buffer_load_dwordx4 v[32:35], v164, %[ob], s94 offen offset:0 sc1\n\t" \
  "s_mov_b64 exec, -1\n\t" \
  "v_mfma_f32_16x16x32_bf16 v[148:151], v[100:103], v[52:55], v[148:151]\n\t" \
  "ds_read_b64_tr_b16 v[96:97], v160 offset:8192\n\t" \
  "ds_read_b64_tr_b16 v[98:99], v160 offset:10240\n\t" \
  "v_mfma_f32_16x16x32_bf16 v[136:139], v[104:107], v[48:51], v[136:139]\n\t" \
  "s_mov_b64 exec, vcc\n\t" \
  "buffer_load_dwordx4 v[36:39], v164, %[ob], s94 offen offset:1024 sc1\n\t" \
  "s_mov_b64 exec, -1\n\t" \
  "v_mfma_f32_16x16x32_bf16 v[152:155], v[104:107], v[52:55], v[152:155]\n\t" \
  "ds_read_b64_tr_b16 v[100:101], v161 offset:8192\n\t" \
  "ds_read_b64_tr_b16 v[102:103], v161 offset:10240\n\t" \
  "v_mfma_f32_16x16x32_bf16 v[140:143], v[108:111], v[48:51], v[140:143]\n\t" \
  "s_mov_b64 exec, vcc\n\t" \
  "buffer_load_dwordx4 v[40:43], v164, %[ob], s94 offen offset:2048 sc1\n\t" \
  "s_mov_b64 exec, -1\n\t" \
  "v_mfma_f32_16x16x32_bf16 v[156:159], v[108:111], v[52:55], v[156:159]\n\t" \
  "ds_read_b64_tr_b16 v[104:105], v162 offset:8192\n\t" \
  "ds_read_b64_tr_b16 v[106:107], v162 offset:10240\n\t" \
  "s_waitcnt lgkmcnt(10)\n\t" \
  "v_mfma_f32_16x16x32_bf16 v[128:131], v[112:115], v[56:59], v[128:131]\n\t" \
  "s_mov_b64 exec, vcc\n\t" \
  "buffer_load_dwordx4 v[44:47], v164, %[ob], s94 offen offset:3072 sc1\n\t" \
  "s_mov_b64 exec, -1\n\t" \
  "v_mfma_f32_16x16x32_bf16 v[144:147], v[112:115], v[60:63], v[144:147]\n\t" \
  "ds_read_b64_tr_b16 v[108:109], v163 offset:8192\n\t" \
  "ds_read_b64_tr_b16 v[110:111], v163 offset:10240\n\t" \
  "v_mfma_f32_16x16x32_bf16 v[132:135], v[116:119], v[56:59], v[132:135]\n\t" \
  "ds_read_b64_tr_b16 v[112:113], v160 offset:12288\n\t" \
  "ds_read_b32 v166, v165\n\t" \
  "v_mfma_f32_16x16x32_bf16 v[148:151], v[116:119], v[60:63], v[148:151]\n\t" \
  "ds_read_b64_tr_b16 v[114:115], v160 offset:14336\n\t" \
  "s_waitcnt lgkmcnt(11)\n\t" \
  "v_mfma_f32_16x16x32_bf16 v[136:139], v[120:123], v[56:59], v[136:139]\n\t" \
  "ds_read_b64_tr_b16 v[116:117], v161 offset:12288\n\t" \
  "v_mfma_f32_16x16x32_bf16 v[152:155], v[120:123], v[60:63], v[152:155]\n\t" \
  "ds_read_b64_tr_b16 v[118:119], v161 offset:14336\n\t" \
  "s_sub_u32 %[iss], %[iss], vcc_lo\n\t" \
  "v_mfma_f32_16x16x32_bf16 v[140:143], v[124:127], v[56:59], v[140:143]\n\t" \
  "ds_read_b64_tr_b16 v[120:121], v162 offset:12288\n\t" \
  "v_mfma_f32_16x16x32_bf16 v[156:159], v[124:127], v[60:63], v[156:159]\n\t" \
  "ds_read_b64_tr_b16 v[122:123], v162 offset:14336\n\t" \
  "s_add_u32 %[th], %[th], 1\n\t" \
  "s_cbranch_vccz QF9_%=\n\t" \
  "s_waitcnt vmcnt(12)\n\t" \
  "QG9_%=:\n\t" \
  "s_waitcnt lgkmcnt(11)\n\t" \
  "v_mfma_f32_16x16x32_bf16 v[128:131], v[96:99], v[64:67], v[128:131]\n\t" \
  "ds_read_b64_tr_b16 v[124:125], v163 offset:12288\n\t" \
  "v_readlane_b32 s94, v167, 13\n\t" \
  "v_mfma_f32_16x16x32_bf16 v[144:147], v[96:99], v[68:71], v[144:147]\n\t" \
  "ds_read_b64_tr_b16 v[126:127], v163 offset:14336\n\t" \
  "s_waitcnt lgkmcnt(7)\n\t" \
  "v_cmp_lt_u32 vcc, %[th], v166\n\t" \
  "v_mfma_f32_16x16x32_bf16 v[132:135], v[100:103], v[64:67], v[132:135]\n\t" \
  "s_mov_b64 exec, vcc\n\t" \
  "buffer_load_dwordx4 v[48:51], v164, %[ob], s94 offen offset:0 sc1\n\t" \
  "s_mov_b64 exec, -1\n\t" \
  "v_mfma_f32_16x16x32_bf16 v[148:151], v[100:103], v[68:71], v[148:151]\n\t" \
  "ds_read_b64_tr_b16 v[96:97], v160 offset:16384\n\t" \
  "ds_read_b64_tr_b16 v[98:99], v160 offset:18432\n\t" \
  "v_mfma_f32_16x16x32_bf16 v[136:139], v[104:107], v[64:67], v[136:139]\n\t" \
  "s_mov_b64 exec, vcc\n\t" \
  "buffer_load_dwordx4 v[52:55], v164, %[ob], s94 offen offset:1024 sc1\n\t" \
  "s_mov_b64 exec, -1\n\t" \
  "v_mfma_f32_16x16x32_bf16 v[152:155], v[104:107], v[68:71], v[152:155]\n\t" \
  "ds_read_b64_tr_b16 v[100:101], v161 offset:16384\n\t" \
  "ds_read_b64_tr_b16 v[102:103], v161 offset:18432\n\t" \
  "v_mfma_f32_16x16x32_bf16 v[140:143], v[108:111], v[64:67], v[140:143]\n\t" \
  "s_mov_b64 exec, vcc\n\t" \
  "buffer_load_dwordx4 v[56:59], v164, %[ob], s94 offen offset:2048 sc1\n\t" \
  "s_mov_b64 exec, -1\n\t" \
  "v_mfma_f32_16x16x32_bf16 v[156:159], v[108:111], v[68:71], v[156:159]\n\t" \
  "ds_read_b64_tr_b16 v[104:105], v162 offset:16384\n\t" \
  "ds_read_b64_tr_b16 v[106:107], v162 offset:18432\n\t" \
  "s_waitcnt lgkmcnt(10)\n\t" \
  "v_mfma_f32_16x16x32_bf16 v[128:131], v[112:115], v[72:75], v[128:131]\n\t" \
  "s_mov_b64 exec, vcc\n\t" \
  "buffer_load_dwordx4 v[60:63], v164, %[ob], s94 offen offset:3072 sc1\n\t" \
  "s_mov_b64 exec, -1\n\t" \
  "v_mfma_f32_16x16x32_bf16 v[144:147], v[112:115], v[76:79], v[144:147]\n\t" \
  "ds_read_b64_tr_b16 v[108:109], v163 offset:16384\n\t" \
  "ds_read_b64_tr_b16 v[110:111], v163 offset:18432\n\t" \
  "v_mfma_f32_16x16x32_bf16 v[132:135], v[116:119], v[72:75], v[132:135]\n\t" \
  "ds_read_b64_tr_b16 v[112:113], v160 offset:20480\n\t" \
  "ds_read_b32 v166, v165\n\t" \
  "v_mfma_f32_16x16x32_bf16 v[148:151], v[116:119], v[76:79], v[148:151]\n\t" \
  "ds_read_b64_tr_b16 v[114:115], v160 offset:22528\n\t" \
  "s_waitcnt lgkmcnt(11)\n\t" \
  "v_mfma_f32_16x16x32_bf16 v[136:139], v[120:123], v[72:75], v[136:139]\n\t" \
  "ds_read_b64_tr_b16 v[116:117], v161 offset:20480\n\t" \
  "v_mfma_f32_16x16x32_bf16 v[152:155], v[120:123], v[76:79], v[152:155]\n\t" \
  "ds_read_b64_tr_b16 v[118:119], v161 offset:22528\n\t" \
  "s_sub_u32 %[iss], %[iss], vcc_lo\n\t" \
  "v_mfma_f32_16x16x32_bf16 v[140:143], v[124:127], v[72:75], v[140:143]\n\t" \
  "ds_read_b64_tr_b16 v[120:121], v162 offset:20480\n\t" \
  "v_mfma_f32_16x16x32_bf16 v[156:159], v[124:127], v[76:79], v[156:159]\n\t" \
  "ds_read_b64_tr_b16 v[122:123], v162 offset:22528\n\t" \
  "s_add_u32 %[th], %[th], 1\n\t" \
  "s_cbranch_vccz QF10_%=\n\t" \
  "s_waitcnt vmcnt(12)\n\t" \
  "QG10_%=:\n\t" \
  "s_waitcnt lgkmcnt(11)\n\t" \
  "v_mfma_f32_16x16x32_bf16 v[128:131], v[96:99], v[0:3], v[128:131]\n\t" \
  "ds_read_b64_tr_b16 v[124:125], v163 offset:20480\n\t" \
  "v_readlane_b32 s94, v167, 14\n\t" \
  "v_mfma_f32_16x16x32_bf16 v[144:147], v[96:99], v[4:7], v[144:147]\n\t" \
  "ds_read_b64_tr_b16 v[126:127], v163 offset:22528\n\t" \
  "s_waitcnt lgkmcnt(7)\n\t" \
  "v_cmp_lt_u32 vcc, %[th], v166\n\t" \
  "v_mfma_f32_16x16x32_bf16 v[132:135], v[100:103], v[0:3], v[132:135]\n\t" \
  "s_mov_b64 exec, vcc\n\t" \
  "buffer_load_dwordx4 v[64:67], v164, %[ob], s94 offen offset:0 sc1\n\t" \
  "s_mov_b64 exec, -1\n\t" \
  "v_mfma_f32_16x16x32_bf16 v[148:151], v[100:103], v[4:7], v[148:151]\n\t" \
  "ds_read_b64_tr_b16 v[96:97], v160 offset:24576\n\t" \
  "ds_read_b64_tr_b16 v[98:99], v160 offset:26624\n\t" \
  "v_mfma_f32_16x16x32_bf16 v[136:139], v[104:107], v[0:3], v[136:139]\n\t" \
  "s_mov_b64 exec, vcc\n\t" \
  "buffer_load_dwordx4 v[68:71], v164, %[ob], s94 offen offset:1024 sc1\n\t" \
  "s_mov_b64 exec, -1\n\t" \
  "v_mfma_f32_16x16x32_bf16 v[152:155], v[104:107], v[4:7], v[152:155]\n\t" \
  "ds_read_b64_tr_b16 v[100:101], v161 offset:24576\n\t" \
  "ds_read_b64_tr_b16 v[102:103], v161 offset:26624\n\t" \
  "v_mfma_f32_16x16x32_bf16 v[140:143], v[108:111], v[0:3], v[140:143]\n\t" \
  "s_mov_b64 exec, vcc\n\t" \
  "buffer_load_dwordx4 v[72:75], v164, %[ob], s94 offen offset:2048 sc1\n\t" \
  "s_mov_b64 exec, -1\n\t" \
  "v_mfma_f32_16x16x32_bf16 v[156:159], v[108:111], v[4:7], v[156:159]\n\t" \
  "ds_read_b64_tr_b16 v[104:105], v162 offset:24576\n\t" \
  "ds_read_b64_tr_b16 v[106:107], v162 offset:26624\n\t" \
  "s_waitcnt lgkmcnt(10)\n\t" \
  "v_mfma_f32_16x16x32_bf16 v[128:131], v[112:115], v[8:11], v[128:131]\n\t" \
  "s_mov_b64 exec, vcc\n\t" \
  "buffer_load_dwordx4 v[76:79], v164, %[ob], s94 offen offset:3072 sc1\n\t" \
  "s_mov_b64 exec, -1\n\t" \
  "v_mfma_f32_16x16x32_bf16 v[144:147], v[112:115], v[12:15], v[144:147]\n\t" \
  "ds_read_b64_tr_b16 v[108:109], v163 offset:24576\n\t" \
  "ds_read_b64_tr_b16 v[110:111], v163 offset:26624\n\t" \
  "v_mfma_f32_16x16x32_bf16 v[132:135], v[116:119], v[8:11], v[132:135]\n\t" \
  "ds_read_b64_tr_b16 v[112:113], v160 offset:28672\n\t" \
  "ds_read_b32 v166, v165\n\t" \
  "v_mfma_f32_16x16x32_bf16 v[148:151], v[116:119], v[12:15], v[148:151]\n\t" \
  "ds_read_b64_tr_b16 v[114:115], v160 offset:30720\n\t" \
  "s_waitcnt lgkmcnt(11)\n\t" \
  "v_mfma_f32_16x16x32_bf16 v[136:139], v[120:123], v[8:11], v[136:139]\n\t" \
  "ds_read_b64_tr_b16 v[116:117], v161 offset:28672\n\t" \
  "v_mfma_f32_16x16x32_bf16 v[152:155], v[120:123], v[12:15], v[152:155]\n\t" \
  "ds_read_b64_tr_b16 v[118:119], v161 offset:30720\n\t" \
  "s_sub_u32 %[iss], %[iss], vcc_lo\n\t" \
  "v_mfma_f32_16x16x32_bf16 v[140:143], v[124:127], v[8:11], v[140:143]\n\t" \
  "ds_read_b64_tr_b16 v[120:121], v162 offset:28672\n\t" \
  "v_mfma_f32_16x16x32_bf16 v[156:159], v[124:127], v[12:15], v[156:159]\n\t" \
  "ds_read_b64_tr_b16 v[122:123], v162 offset:30720\n\t" \
  "s_add_u32 %[th], %[th], 1\n\t" \
  "s_cbranch_vccz QF11_%=\n\t" \
  "s_waitcnt vmcnt(12)\n\t" \
  "QG11_%=:\n\t" \
  "s_waitcnt lgkmcnt(11)\n\t" \
  "v_mfma_f32_16x16x32_bf16 v[128:131], v[96:99], v[16:19], v[128:131]\n\t" \
  "ds_read_b64_tr_b16 v[124:125], v163 offset:28672\n\t" \
  "v_readlane_b32 s94, v167, 15\n\t" \
  "v_mfma_f32_16x16x32_bf16 v[144:147], v[96:99], v[20:23], v[144:147]\n\t" \
  "ds_read_b64_tr_b16 v[126:127], v163 offset:30720\n\t" \
  "s_waitcnt lgkmcnt(7)\n\t" \
  "v_cmp_lt_u32 vcc, %[th], v166\n\t" \
  "v_mfma_f32_16x16x32_bf16 v[132:135], v[100:103], v[16:19], v[132:135]\n\t" \
  "s_mov_b64 exec, vcc\n\t" \
  "buffer_load_dwordx4 v[0:3], v164, %[ob], s94 offen offset:0 sc1\n\t" \
  "s_mov_b64 exec, -1\n\t" \
  "v_mfma_f32_16x16x32_bf16 v[148:151], v[100:103], v[20:23], v[148:151]\n\t" \
  "ds_read_b64_tr_b16 v[96:97], v160 offset:32768\n\t" \
  "ds_read_b64_tr_b16 v[98:99], v160 offset:34816\n\t" \
  "v_mfma_f32_16x16x32_bf16 v[136:139], v[104:107], v[16:19], v[136:139]\n\t" \
  "s_mov_b64 exec, vcc\n\t" \
  "buffer_load_dwordx4 v[4:7], v164, %[ob], s94 offen offset:1024 sc1\n\t" \
  "s_mov_b64 exec, -1\n\t" \
  "v_mfma_f32_16x16x32_bf16 v[152:155], v[104:107], v[20:23], v[152:155]\n\t" \
  "ds_read_b64_tr_b16 v[100:101], v161 offset:32768\n\t" \
  "ds_read_b64_tr_b16 v[102:103], v161 offset:34816\n\t" \
  "v_mfma_f32_16x16x32_bf16 v[140:143], v[108:111], v[16:19], v[140:143]\n\t" \
  "s_mov_b64 exec, vcc\n\t" \
  "buffer_load_dwordx4 v[8:11], v164, %[ob], s94 offen offset:2048 sc1\n\t" \
  "s_mov_b64 exec, -1\n\t" \
  "v_mfma_f32_16x16x32_bf16 v[156:159], v[108:111], v[20:23], v[156:159]\n\t" \
  "ds_read_b64_tr_b16 v[104:105], v162 offset:32768\n\t" \
  "ds_read_b64_tr_b16 v[106:107], v162 offset:34816\n\t" \
  "s_waitcnt lgkmcnt(10)\n\t" \
  "v_mfma_f32_16x16x32_bf16 v[128:131], v[112:115], v[24:27], v[128:131]\n\t" \
  "s_mov_b64 exec, vcc\n\t" \
  "buffer_load_dwordx4 v[12:15], v164, %[ob], s94 offen offset:3072 sc1\n\t" \
  "s_mov_b64 exec, -1\n\t" \
  "v_mfma_f32_16x16x32_bf16 v[144:147], v[112:115], v[28:31], v[144:147]\n\t" \
  "ds_read_b64_tr_b16 v[108:109], v163 offset:32768\n\t" \
  "ds_read_b64_tr_b16 v[110:111], v163 offset:34816\n\t" \
  "v_mfma_f32_16x16x32_bf16 v[132:135], v[116:119], v[24:27], v[132:135]\n\t" \
  "ds_read_b64_tr_b16 v[112:113], v160 offset:36864\n\t" \
  "v_mfma_f32_16x16x32_bf16 v[148:151], v[116:119], v[28:31], v[148:151]\n\t" \
  "ds_read_b64_tr_b16 v[114:115], v160 offset:38912\n\t" \
  "s_waitcnt lgkmcnt(10)\n\t" \
  "v_mfma_f32_16x16x32_bf16 v[136:139], v[120:123], v[24:27], v[136:139]\n\t" \
  "ds_read_b64_tr_b16 v[116:117], v161 offset:36864\n\t" \
  "v_mfma_f32_16x16x32_bf16 v[152:155], v[120:123], v[28:31], v[152:155]\n\t" \
  "ds_read_b64_tr_b16 v[118:119], v161 offset:38912\n\t" \
  "s_sub_u32 %[iss], %[iss], vcc_lo\n\t" \
  "v_mfma_f32_16x16x32_bf16 v[140:143], v[124:127], v[24:27], v[140:143]\n\t" \
  "ds_read_b64_tr_b16 v[120:121], v162 offset:36864\n\t" \
  "v_mfma_f32_16x16x32_bf16 v[156:159], v[124:127], v[28:31], v[156:159]\n\t" \
  "ds_read_b64_tr_b16 v[122:123], v162 offset:38912\n\t" \
  "s_add_u32 %[th], %[th], 1\n\t" \
  "s_cbranch_vccz QF12_%=\n\t" \
  "s_waitcnt vmcnt(12)\n\t" \
  "QG12_%=:\n\t" \
  "s_waitcnt lgkmcnt(10)\n\t" \
  "v_mfma_f32_16x16x32_bf16 v[128:131], v[96:99], v[32:35], v[128:131]\n\t" \
  "ds_read_b64_tr_b16 v[124:125], v163 offset:36864\n\t" \
  "v_mfma_f32_16x16x32_bf16 v[144:147], v[96:99], v[36:39], v[144:147]\n\t" \
  "ds_read_b64_tr_b16 v[126:127], v163 offset:38912\n\t" \
  "v_mfma_f32_16x16x32_bf16 v[132:135], v[100:103], v[32:35], v[132:135]\n\t" \
  "ds_read_b64_tr_b16 v[96:97], v160 offset:40960\n\t" \
  "v_mfma_f32_16x16x32_bf16 v[148:151], v[100:103], v[36:39], v[148:151]\n\t" \
  "ds_read_b64_tr_b16 v[98:99], v160 offset:43008\n\t" \
  "s_waitcnt lgkmcnt(10)\n\t" \
  "v_mfma_f32_16x16x32_bf16 v[136:139], v[104:107], v[32:35], v[136:139]\n\t" \
  "ds_read_b64_tr_b16 v[100:101], v161 offset:40960\n\t" \
  "v_mfma_f32_16x16x32_bf16 v[152:155], v[104:107], v[36:39], v[152:155]\n\t" \
  "ds_read_b64_tr_b16 v[102:103], v161 offset:43008\n\t" \
  "v_mfma_f32_16x16x32_bf16 v[140:143], v[108:111], v[32:35], v[140:143]\n\t" \
  "ds_read_b64_tr_b16 v[104:105], v162 offset:40960\n\t" \
  "v_mfma_f32_16x16x32_bf16 v[156:159], v[108:111], v[36:39], v[156:159]\n\t" \
  "ds_read_b64_tr_b16 v[106:107], v162 offset:43008\n\t" \
  "s_waitcnt lgkmcnt(10)\n\t" \
  "v_mfma_f32_16x16x32_bf16 v[128:131], v[112:115], v[40:43], v[128:131]\n\t" \
  "ds_read_b64_tr_b16 v[108:109], v163 offset:40960\n\t" \
  "v_mfma_f32_16x16x32_bf16 v[144:147], v[112:115], v[44:47], v[144:147]\n\t" \
  "ds_read_b64_tr_b16 v[110:111], v163 offset:43008\n\t" \
  "v_mfma_f32_16x16x32_bf16 v[132:135], v[116:119], v[40:43], v[132:135]\n\t" \
  "ds_read_b64_tr_b16 v[112:113], v160 offset:45056\n\t" \
  "v_mfma_f32_16x16x32_bf16 v[148:151], v[116:119], v[44:47], v[148:151]\n\t" \
  "ds_read_b64_tr_b16 v[114:115], v160 offset:47104\n\t" \
  "s_waitcnt lgkmcnt(10)\n\t" \
  "v_mfma_f32_16x16x32_bf16 v[136:139], v[120:123], v[40:43], v[136:139]\n\t" \
  "ds_read_b64_tr_b16 v[116:117], v161 offset:45056\n\t" \
  "v_mfma_f32_16x16x32_bf16 v[152:155], v[120:123], v[44:47], v[152:155]\n\t" \
  "ds_read_b64_tr_b16 v[118:119], v161 offset:47104\n\t" \
  "v_mfma_f32_16x16x32_bf16 v[140:143], v[124:127], v[40:43], v[140:143]\n\t" \
  "ds_read_b64_tr_b16 v[120:121], v162 offset:45056\n\t" \
  "v_mfma_f32_16x16x32_bf16 v[156:159], v[124:127], v[44:47], v[156:159]\n\t" \
  "ds_read_b64_tr_b16 v[122:123], v162 offset:47104\n\t" \
  "s_cbranch_vccz QF13_%=\n\t" \
  "s_waitcnt vmcnt(8)\n\t" \
  "QG13_%=:\n\t" \
  "s_waitcnt lgkmcnt(10)\n\t" \
  "v_mfma_f32_16x16x32_bf16 v[128:131], v[96:99], v[48:51], v[128:131]\n\t" \
  "ds_read_b64_tr_b16 v[124:125], v163 offset:45056\n\t" \
  "v_mfma_f32_16x16x32_bf16 v[144:147], v[96:99], v[52:55], v[144:147]\n\t" \
  "ds_read_b64_tr_b16 v[126:127], v163 offset:47104\n\t" \
  "v_mfma_f32_16x16x32_bf16 v[132:135], v[100:103], v[48:51], v[132:135]\n\t" \
  "ds_read_b64_tr_b16 v[96:97], v160 offset:49152\n\t" \
  "v_mfma_f32_16x16x32_bf16 v[148:151], v[100:103], v[52:55], v[148:151]\n\t" \
  "ds_read_b64_tr_b16 v[98:99], v160 offset:51200\n\t" \
  "s_waitcnt lgkmcnt(10)\n\t" \
  "v_mfma_f32_16x16x32_bf16 v[136:139], v[104:107], v[48:51], v[136:139]\n\t" \
  "ds_read_b64_tr_b16 v[100:101], v161 offset:49152\n\t" \
  "v_mfma_f32_16x16x32_bf16 v[152:155], v[104:107], v[52:55], v[152:155]\n\t" \
  "ds_read_b64_tr_b16 v[102:103], v161 offset:51200\n\t" \
  "v_mfma_f32_16x16x32_bf16 v[140:143], v[108:111], v[48:51], v[140:143]\n\t" \
  "ds_read_b64_tr_b16 v[104:105], v162 offset:49152\n\t" \
  "v_mfma_f32_16x16x32_bf16 v[156:159], v[108:111], v[52:55], v[156:159]\n\t" \
  "ds_read_b64_tr_b16 v[106:107], v162 offset:51200\n\t" \
  "s_waitcnt lgkmcnt(10)\n\t" \
  "v_mfma_f32_16x16x32_bf16 v[128:131], v[112:115], v[56:59], v[128:131]\n\t" \
  "ds_read_b64_tr_b16 v[108:109], v163 offset:49152\n\t" \
  "v_mfma_f32_16x16x32_bf16 v[144:147], v[112:115], v[60:63], v[144:147]\n\t" \
  "ds_read_b64_tr_b16 v[110:111], v163 offset:51200\n\t" \
  "v_mfma_f32_16x16x32_bf16 v[132:135], v[116:119], v[56:59], v[132:135]\n\t" \
  "ds_read_b64_tr_b16 v[112:113], v160 offset:53248\n\t" \
  "v_mfma_f32_16x16x32_bf16 v[148:151], v[116:119], v[60:63], v[148:151]\n\t" \
  "ds_read_b64_tr_b16 v[114:115], v160 offset:55296\n\t" \
  "s_waitcnt lgkmcnt(10)\n\t" \
  "v_mfma_f32_16x16x32_bf16 v[136:139], v[120:123], v[56:59], v[136:139]\n\t" \
  "ds_read_b64_tr_b16 v[116:117], v161 offset:53248\n\t" \
  "v_mfma_f32_16x16x32_bf16 v[152:155], v[120:123], v[60:63], v[152:155]\n\t" \
  "ds_read_b64_tr_b16 v[118:119], v161 offset:55296\n\t" \
  "v_mfma_f32_16x16x32_bf16 v[140:143], v[124:127], v[56:59], v[140:143]\n\t" \
  "ds_read_b64_tr_b16 v[120:121], v162 offset:53248\n\t" \
  "v_mfma_f32_16x16x32_bf16 v[156:159], v[124:127], v[60:63], v[156:159]\n\t" \
  "ds_read_b64_tr_b16 v[122:123], v162 offset:55296\n\t" \
  "s_cbranch_vccz QF14_%=\n\t" \
  "s_waitcnt vmcnt(4)\n\t" \
  "QG14_%=:\n\t" \
  "s_waitcnt lgkmcnt(10)\n\t" \
  "v_mfma_f32_16x16x32_bf16 v[128:131], v[96:99], v[64:67], v[128:131]\n\t" \
  "ds_read_b64_tr_b16 v[124:125], v163 offset:53248\n\t" \
  "v_mfma_f32_16x16x32_bf16 v[144:147], v[96:99], v[68:71], v[144:147]\n\t" \
  "ds_read_b64_tr_b16 v[126:127], v163 offset:55296\n\t" \
  "v_mfma_f32_16x16x32_bf16 v[132:135], v[100:103], v[64:67], v[132:135]\n\t" \
  "ds_read_b64_tr_b16 v[96:97], v160 offset:57344\n\t" \
  "v_mfma_f32_16x16x32_bf16 v[148:151], v[100:103], v[68:71], v[148:151]\n\t" \
  "ds_read_b64_tr_b16 v[98:99], v160 offset:59392\n\t" \
  "s_waitcnt lgkmcnt(10)\n\t" \
  "v_mfma_f32_16x16x32_bf16 v[136:139], v[104:107], v[64:67], v[136:139]\n\t" \
  "ds_read_b64_tr_b16 v[100:101], v161 offset:57344\n\t" \
  "v_mfma_f32_16x16x32_bf16 v[152:155], v[104:107], v[68:71], v[152:155]\n\t" \
  "ds_read_b64_tr_b16 v[102:103], v161 offset:59392\n\t" \
  "v_mfma_f32_16x16x32_bf16 v[140:143], v[108:111], v[64:67], v[140:143]\n\t" \
  "ds_read_b64_tr_b16 v[104:105], v162 offset:57344\n\t" \
  "v_mfma_f32_16x16x32_bf16 v[156:159], v[108:111], v[68:71], v[156:159]\n\t" \
  "ds_read_b64_tr_b16 v[106:107], v162 offset:59392\n\t" \
  "s_waitcnt lgkmcnt(10)\n\t" \
  "v_mfma_f32_16x16x32_bf16 v[128:131], v[112:115], v[72:75], v[128:131]\n\t" \
  "ds_read_b64_tr_b16 v[108:109], v163 offset:57344\n\t" \
  "v_mfma_f32_16x16x32_bf16 v[144:147], v[112:115], v[76:79], v[144:147]\n\t" \
  "ds_read_b64_tr_b16 v[110:111], v163 offset:59392\n\t" \
  "v_mfma_f32_16x16x32_bf16 v[132:135], v[116:119], v[72:75], v[132:135]\n\t" \
  "ds_read_b64_tr_b16 v[112:113], v160 offset:61440\n\t" \
  "v_mfma_f32_16x16x32_bf16 v[148:151], v[116:119], v[76:79], v[148:151]\n\t" \
  "ds_read_b64_tr_b16 v[114:115], v160 offset:63488\n\t" \
  "s_waitcnt lgkmcnt(10)\n\t" \
  "v_mfma_f32_16x16x32_bf16 v[136:139], v[120:123], v[72:75], v[136:139]\n\t" \
  "ds_read_b64_tr_b16 v[116:117], v161 offset:61440\n\t" \
  "v_mfma_f32_16x16x32_bf16 v[152:155], v[120:123], v[76:79], v[152:155]\n\t" \
  "ds_read_b64_tr_b16 v[118:119], v161 offset:63488\n\t" \
  "v_mfma_f32_16x16x32_bf16 v[140:143], v[124:127], v[72:75], v[140:143]\n\t" \
  "ds_read_b64_tr_b16 v[120:121], v162 offset:61440\n\t" \
  "v_mfma_f32_16x16x32_bf16 v[156:159], v[124:127], v[76:79], v[156:159]\n\t" \
  "ds_read_b64_tr_b16 v[122:123], v162 offset:63488\n\t" \
  "s_cbranch_vccz QF15_%=\n\t" \
  "s_waitcnt vmcnt(0)\n\t" \
  "QG15_%=:\n\t" \
  "s_waitcnt lgkmcnt(10)\n\t" \
  "v_mfma_f32_16x16x32_bf16 v[128:131], v[96:99], v[0:3], v[128:131]\n\t" \
  "ds_read_b64_tr_b16 v[124:125], v163 offset:61440\n\t" \
  "v_mfma_f32_16x16x32_bf16 v[144:147], v[96:99], v[4:7], v[144:147]\n\t" \
  "ds_read_b64_tr_b16 v[126:127], v163 offset:63488\n\t" \
  "v_mfma_f32_16x16x32_bf16 v[132:135], v[100:103], v[0:3], v[132:135]\n\t" \
  "v_mfma_f32_16x16x32_bf16 v[148:151], v[100:103], v[4:7], v[148:151]\n\t" \
  "s_waitcnt lgkmcnt(8)\n\t" \
  "v_mfma_f32_16x16x32_bf16 v[136:139], v[104:107], v[0:3], v[136:139]\n\t" \
  "v_mfma_f32_16x16x32_bf16 v[152:155], v[104:107], v[4:7], v[152:155]\n\t" \
  "v_mfma_f32_16x16x32_bf16 v[140:143], v[108:111], v[0:3], v[140:143]\n\t" \
  "v_mfma_f32_16x16x32_bf16 v[156:159], v[108:111], v[4:7], v[156:159]\n\t" \
  "s_waitcnt lgkmcnt(4)\n\t" \
  "v_mfma_f32_16x16x32_bf16 v[128:131], v[112:115], v[8:11], v[128:131]\n\t" \
  "v_mfma_f32_16x16x32_bf16 v[144:147], v[112:115], v[12:15], v[144:147]\n\t" \
  "v_mfma_f32_16x16x32_bf16 v[132:135], v[116:119], v[8:11], v[132:135]\n\t" \
  "v_mfma_f32_16x16x32_bf16 v[148:151], v[116:119], v[12:15], v[148:151]\n\t" \
  "s_waitcnt lgkmcnt(0)\n\t" \
  "v_mfma_f32_16x16x32_bf16 v[136:139], v[120:123], v[8:11], v[136:139]\n\t" \
  "v_mfma_f32_16x16x32_bf16 v[152:155], v[120:123], v[12:15], v[152:155]\n\t" \
  "v_mfma_f32_16x16x32_bf16 v[140:143], v[124:127], v[8:11], v[140:143]\n\t" \
  "v_mfma_f32_16x16x32_bf16 v[156:159], v[124:127], v[12:15], v[156:159]\n\t" \
  "s_branch QX_%=\n\t" \
  "QF1_%=:\n\t" \
  "s_mov_b32 %[c], 1\n\t" \
  "s_getpc_b64 s[92:93]\n\t" \
  "s_branch QC_%=\n\t" \
  "s_branch QG1_%=\n\t" \
  "QF2_%=:\n\t" \
  "s_mov_b32 %[c], 2\n\t" \
  "s_getpc_b64 s[92:93]\n\t" \
  "s_branch QC_%=\n\t" \
  "s_branch QG2_%=\n\t" \
  "QF3_%=:\n\t" \
  "s_mov_b32 %[c], 3\n\t" \
  "s_getpc_b64 s[92:93]\n\t" \
  "s_branch QC_%=\n\t" \
  "s_branch QG3_%=\n\t" \
  "QF4_%=:\n\t" \
  "s_mov_b32 %[c], 4\n\t" \
  "s_getpc_b64 s[92:93]\n\t" \
  "s_branch QC_%=\n\t" \
  "s_branch QG4_%=\n\t" \
  "QF5_%=:\n\t" \
  "s_mov_b32 %[c], 5\n\t" \
  "s_getpc_b64 s[92:93]\n\t" \
  "s_branch QC_%=\n\t" \
  "s_branch QG5_%=\n\t" \
  "QF6_%=:\n\t" \
  "s_mov_b32 %[c], 6\n\t" \
  "s_getpc_b64 s[92:93]\n\t" \
  "s_branch QC_%=\n\t" \
  "s_branch QG6_%=\n\t" \
  "QF7_%=:\n\t" \
  "s_mov_b32 %[c], 7\n\t" \
  "s_getpc_b64 s[92:93]\n\t" \
  "s_branch QC_%=\n\t" \
  "s_branch QG7_%=\n\t" \
  "QF8_%=:\n\t" \
  "s_mov_b32 %[c], 8\n\t" \
  "s_getpc_b64 s[92:93]\n\t" \
  "s_branch QC_%=\n\t" \
  "s_branch QG8_%=\n\t" \
  "QF9_%=:\n\t" \
  "s_mov_b32 %[c], 9\n\t" \
  "s_getpc_b64 s[92:93]\n\t" \
  "s_branch QC_%=\n\t" \
  "s_branch QG9_%=\n\t" \
  "QF10_%=:\n\t" \
  "s_mov_b32 %[c], 10\n\t" \
  "s_getpc_b64 s[92:93]\n\t" \
  "s_branch QC_%=\n\t" \
  "s_branch QG10_%=\n\t" \
  "QF11_%=:\n\t" \
  "s_mov_b32 %[c], 11\n\t" \
  "s_getpc_b64 s[92:93]\n\t" \
  "s_branch QC_%=\n\t" \
  "s_branch QG11_%=\n\t" \
  "QF12_%=:\n\t" \
  "s_mov_b32 %[c], 12\n\t" \
  "s_getpc_b64 s[92:93]\n\t" \
  "s_branch QC_%=\n\t" \
  "s_branch QG12_%=\n\t" \
  "QF13_%=:\n\t" \
  "s_mov_b32 %[c], 13\n\t" \
  "s_getpc_b64 s[92:93]\n\t" \
  "s_branch QC_%=\n\t" \
  "s_branch QG13_%=\n\t" \
  "QF14_%=:\n\t" \
  "s_mov_b32 %[c], 14\n\t" \
  "s_getpc_b64 s[92:93]\n\t" \
  "s_branch QC_%=\n\t" \
  "s_branch QG14_%=\n\t" \
  "QF15_%=:\n\t" \
  "s_mov_b32 %[c], 15\n\t" \
  "s_getpc_b64 s[92:93]\n\t" \
  "s_branch QC_%=\n\t" \
  "s_branch QG15_%=\n\t" \
  "QC_%=:\n\t" \
  "s_add_u32 s92, s92, 4\n\t" \
  "s_addc_u32 s93, s93, 0\n\t" \
  "QU_%=:\n\t" \
  "ds_read_b32 v166, v165\n\t" \
  "s_max_i32 %[t1], %[c], 0\n\t" \
  "s_add_u32 %[t1], %[t1], 4\n\t" \
  "s_waitcnt lgkmcnt(0)\n\t" \
  "v_readfirstlane_b32 %[t0], v166\n\t" \
  "s_nop 0\n\t" \
  "s_min_u32 %[t1], %[t1], %[t0]\n\t" \
  "QL_%=:\n\t" \
  "s_cmp_ge_u32 %[iss], %[t1]\n\t" \
  "s_cbranch_scc1 QD_%=\n\t" \
  "s_mov_b32 %[t0], %[iss]\n\t" \
  "s_sub_u32 %[th], %[t0], 5\n\t" \
  "s_cselect_b32 %[t0], %[t0], %[th]\n\t" \
  "s_sub_u32 %[th], %[t0], 5\n\t" \
  "s_cselect_b32 %[t0], %[t0], %[th]\n\t" \
  "s_sub_u32 %[th], %[t0], 5\n\t" \
  "s_cselect_b32 %[t0], %[t0], %[th]\n\t" \
  "s_cmp_eq_u32 %[t0], 1\n\t" \
  "s_cbranch_scc1 QS1_%=\n\t" \
  "s_cmp_eq_u32 %[t0], 2\n\t" \
  "s_cbranch_scc1 QS2_%=\n\t" \
  "s_cmp_eq_u32 %[t0], 3\n\t" \
  "s_cbranch_scc1 QS3_%=\n\t" \
  "s_cmp_eq_u32 %[t0], 4\n\t" \
  "s_cbranch_scc1 QS4_%=\n\t" \
  "QS0_%=:\n\t" \
  "v_readlane_b32 s94, v167, %[iss]\n\t" \
  "s_nop 4\n\t" \
  "buffer_load_dwordx4 v[0:3], v164, %[ob], s94 offen offset:0 sc1\n\t" \
  "buffer_load_dwordx4 v[4:7], v164, %[ob], s94 offen offset:1024 sc1\n\t" \
  "buffer_load_dwordx4 v[8:11], v164, %[ob], s94 offen offset:2048 sc1\n\t" \
  "buffer_load_dwordx4 v[12:15], v164, %[ob], s94 offen offset:3072 sc1\n\t" \
  "s_add_u32 %[iss], %[iss], 1\n\t" \
  "s_cmp_ge_u32 %[iss], %[t1]\n\t" \
  "s_cbranch_scc1 QD_%=\n\t" \
  "QS1_%=:\n\t" \
  "v_readlane_b32 s94, v167, %[iss]\n\t" \
  "s_nop 4\n\t" \
  "buffer_load_dwordx4 v[16:19], v164, %[ob], s94 offen offset:0 sc1\n\t" \
  "buffer_load_dwordx4 v[20:23], v164, %[ob], s94 offen offset:1024 sc1\n\t" \
  "buffer_load_dwordx4 v[24:27], v164, %[ob], s94 offen offset:2048 sc1\n\t" \
  "buffer_load_dwordx4 v[28:31], v164, %[ob], s94 offen offset:3072 sc1\n\t" \
  "s_add_u32 %[iss], %[iss], 1\n\t" \
  "s_cmp_ge_u32 %[iss], %[t1]\n\t" \
  "s_cbranch_scc1 QD_%=\n\t" \
  "QS2_%=:\n\t" \
  "v_readlane_b32 s94, v167, %[iss]\n\t" \
  "s_nop 4\n\t" \
  "buffer_load_dwordx4 v[32:35], v164, %[ob], s94 offen offset:0 sc1\n\t" \
  "buffer_load_dwordx4 v[36:39], v164, %[ob], s94 offen offset:1024 sc1\n\t" \
  "buffer_load_dwordx4 v[40:43], v164, %[ob], s94 offen offset:2048 sc1\n\t" \
  "buffer_load_dwordx4 v[44:47], v164, %[ob], s94 offen offset:3072 sc1\n\t" \
  "s_add_u32 %[iss], %[iss], 1\n\t" \
  "s_cmp_ge_u32 %[iss], %[t1]\n\t" \
  "s_cbranch_scc1 QD_%=\n\t" \
  "QS3_%=:\n\t" \
  "v_readlane_b32 s94, v167, %[iss]\n\t" \
  "s_nop 4\n\t" \
  "buffer_load_dwordx4 v[48:51], v164, %[ob], s94 offen offset:0 sc1\n\t" \
  "buffer_load_dwordx4 v[52:55], v164, %[ob], s94 offen offset:1024 sc1\n\t" \
  "buffer_load_dwordx4 v[56:59], v164, %[ob], s94 offen offset:2048 sc1\n\t" \
  "buffer_load_dwordx4 v[60:63], v164, %[ob], s94 offen offset:3072 sc1\n\t" \
  "s_add_u32 %[iss], %[iss], 1\n\t" \
  "s_cmp_ge_u32 %[iss], %[t1]\n\t" \
  "s_cbranch_scc1 QD_%=\n\t" \
  "QS4_%=:\n\t" \
  "v_readlane_b32 s94, v167, %[iss]\n\t" \
  "s_nop 4\n\t" \
  "buffer_load_dwordx4 v[64:67], v164, %[ob], s94 offen offset:0 sc1\n\t" \
  "buffer_load_dwordx4 v[68:71], v164, %[ob], s94 offen offset:1024 sc1\n\t" \
  "buffer_load_dwordx4 v[72:75], v164, %[ob], s94 offen offset:2048 sc1\n\t" \
  "buffer_load_dwordx4 v[76:79], v164, %[ob], s94 offen offset:3072 sc1\n\t" \
  "s_add_u32 %[iss], %[iss], 1\n\t" \
  "s_cmp_ge_u32 %[iss], %[t1]\n\t" \
  "s_cbranch_scc0 QS0_%=\n\t" \
  "QD_%=:\n\t" \
  "s_cmp_lt_i32 %[c], 0\n\t" \
  "s_cbranch_scc1 QR_%=\n\t" \
  "s_cmp_gt_u32 %[iss], %[c]\n\t" \
  "s_cbranch_scc1 QW_%=\n\t" \
  "s_addk_i32 %[cn], 1\n\t" \
  "s_cmp_gt_u32 %[cn], 0x400000\n\t" \
  "s_cbranch_scc1 QT_%=\n\t" \
  "s_sleep 1\n\t" \
  "s_branch QU_%=\n\t" \
  "QT_%=:\n\t" \
  "s_add_u32 %[t1], %[c], 1\n\t" \
  "s_branch QL_%=\n\t" \
  "QW_%=:\n\t" \
  "s_cmp_eq_u32 %[iss], 16\n\t" \
  "s_cselect_b64 vcc, -1, 0\n\t" \
  "s_sub_u32 %[t0], %[iss], %[c]\n\t" \
  "s_cmp_eq_u32 %[t0], 4\n\t" \
  "s_cselect_b32 %[th], %[iss], 99\n\t" \
  "s_waitcnt vmcnt(12)\n\t" \
  "s_cmp_ge_u32 %[t0], 4\n\t" \
  "s_cbranch_scc1 QR_%=\n\t" \
  "s_waitcnt vmcnt(8)\n\t" \
  "s_cmp_ge_u32 %[t0], 3\n\t" \
  "s_cbranch_scc1 QR_%=\n\t" \
  "s_waitcnt vmcnt(4)\n\t" \
  "s_cmp_ge_u32 %[t0], 2\n\t" \
  "s_cbranch_scc1 QR_%=\n\t" \
  "s_waitcnt vmcnt(0)\n\t" \
  "QR_%=:\n\t" \
  "s_setpc_b64 s[92:93]\n\t" \
  "QX_%=:\n\t" \
  "s_nop 7\n\t"
#define G3_CLOB "v0", "v1", "v2", "v3", "v4", "v5", "v6", "v7", "v8", "v9", "v10", "v11", "v12", "v13", "v14", "v15", "v16", "v17", "v18", "v19", "v20", "v21", "v22", "v23", "v24", "v25", "v26", "v27", "v28", "v29", "v30", "v31", "v32", "v33", "v34", "v35", "v36", "v37", "v38", "v39", "v40", "v41", "v42", "v43", "v44", "v45", "v46", "v47", "v48", "v49", "v50", "v51", "v52", "v53", "v54", "v55", "v56", "v57", "v58", "v59", "v60", "v61", "v62", "v63", "v64", "v65", "v66", "v67", "v68", "v69", "v70", "v71", "v72", "v73", "v74", "v75", "v76", "v77", "v78", "v79", "v80", "v81", "v82", "v83", "v84", "v85", "v86", "v87", "v88", "v89", "v90", "v91", "v92", "v93", "v94", "v95", "v96", "v97", "v98", "v99", "v100", "v101", "v102", "v103", "v104", "v105", "v106", "v107", "v108", "v109", "v110", "v111", "v112", "v113", "v114", "v115", "v116", "v117", "v118", "v119", "v120", "v121", "v122", "v123", "v124", "v125", "v126", "v127", "v166", "s88", "s89", "s92", "s93", "s94"

#define PP_ASM \
  "v_mov_b32 v60, 0\n\t" \
  "s_mov_b32 %[t0], %[n0]\n\t" \
  "s_cmp_eq_u32 %[t0], 0\n\t" \
  "s_cbranch_scc1 PAx_%=\n\t" \
  "PA_%=:\n\t" \
  "s_barrier\n\t" \
  "s_sub_u32 %[t0], %[t0], 1\n\t" \
  "s_cmp_lg_u32 %[t0], 0\n\t" \
  "s_cbranch_scc1 PA_%=\n\t" \
  "PAx_%=:\n\t" \
  "s_mov_b32 m0, %[shd]\n\t" \
  "global_load_dword v160, v60, %[fl] sc1\n\t" \
  "global_load_lds_dwordx4 v163, %[shp]\n\t" \
  "s_mov_b32 m0, %[tgd]\n\t" \
  "s_mov_b64 exec, %[em]\n\t" \
  "global_load_lds_dword v60, %[fl] sc1\n\t" \
  "s_mov_b64 exec, -1\n\t" \
  "global_load_ushort v161, v60, %[hp]\n\t" \
  "global_load_ushort v162, v60, %[dp]\n\t" \
  "global_load_dwordx4 v[0:3], v152, %[kb0]\n\t" \
  "global_load_dwordx4 v[4:7], v153, %[kb0]\n\t" \
  "global_load_dwordx4 v[8:11], v154, %[kb0]\n\t" \
  "global_load_dwordx4 v[12:15], v155, %[kb3] offset:0\n\t" \
  "global_load_dwordx4 v[28:31], v155, %[vb0] offset:0\n\t" \
  "s_setprio 2\n\t" \
  "s_barrier\n\t" \
  "s_barrier\n\t" \
  "s_waitcnt vmcnt(5)\n\t" \
  "ds_read_b128 v[128:131], v156 offset:0\n\t" \
  "ds_read_b128 v[132:135], v156 offset:16\n\t" \
  "ds_read_b128 v[136:139], v156 offset:32\n\t" \
  "ds_read_b128 v[140:143], v156 offset:48\n\t" \
  "s_waitcnt lgkmcnt(0)\n\t" \
  "v_lshlrev_b32 v64, 16, v128\n\t" \
  "v_and_b32 v65, 0xffff0000, v128\n\t" \
  "v_lshlrev_b32 v66, 16, v129\n\t" \
  "v_and_b32 v67, 0xffff0000, v129\n\t" \
  "v_lshlrev_b32 v68, 16, v130\n\t" \
  "v_and_b32 v69, 0xffff0000, v130\n\t" \
  "v_lshlrev_b32 v70, 16, v131\n\t" \
  "v_and_b32 v71, 0xffff0000, v131\n\t" \
  "v_lshlrev_b32 v72, 16, v132\n\t" \
  "v_and_b32 v73, 0xffff0000, v132\n\t" \
  "v_lshlrev_b32 v74, 16, v133\n\t" \
  "v_and_b32 v75, 0xffff0000, v133\n\t" \
  "v_lshlrev_b32 v76, 16, v134\n\t" \
  "v_and_b32 v77, 0xffff0000, v134\n\t" \
  "v_lshlrev_b32 v78, 16, v135\n\t" \
  "v_and_b32 v79, 0xffff0000, v135\n\t" \
  "v_lshlrev_b32 v80, 16, v136\n\t" \
  "v_and_b32 v81, 0xffff0000, v136\n\t" \
  "v_lshlrev_b32 v82, 16, v137\n\t" \
  "v_and_b32 v83, 0xffff0000, v137\n\t" \
  "v_lshlrev_b32 v84, 16, v138\n\t" \
  "v_and_b32 v85, 0xffff0000, v138\n\t" \
  "v_lshlrev_b32 v86, 16, v139\n\t" \
  "v_and_b32 v87, 0xffff0000, v139\n\t" \
  "v_lshlrev_b32 v88, 16, v140\n\t" \
  "v_and_b32 v89, 0xffff0000, v140\n\t" \
  "v_lshlrev_b32 v90, 16, v141\n\t" \
  "v_and_b32 v91, 0xffff0000, v141\n\t" \
  "v_lshlrev_b32 v92, 16, v142\n\t" \
  "v_and_b32 v93, 0xffff0000, v142\n\t" \
  "v_lshlrev_b32 v94, 16, v143\n\t" \
  "v_and_b32 v95, 0xffff0000, v143\n\t" \
  "s_waitcnt vmcnt(4)\n\t" \
  "s_mov_b64 exec, %[zm]\n\t" \
  "v_mov_b32 v0, 0\n\t" \
  "v_mov_b32 v1, 0\n\t" \
  "v_mov_b32 v2, 0\n\t" \
  "v_mov_b32 v3, 0\n\t" \
  "s_mov_b64 exec, -1\n\t" \
  "v_lshlrev_b32 v128, 16, v0\n\t" \
  "v_and_b32 v129, 0xffff0000, v0\n\t" \
  "s_setprio 3\n\t" \
  "s_barrier\n\t" \
  "global_load_dwordx4 v[16:19], v155, %[kb3] offset:2048\n\t" \
  "global_load_dwordx4 v[32:35], v155, %[vb0] offset:2048\n\t" \
  "global_load_dwordx4 v[20:23], v155, %[kb5] offset:0\n\t" \
  "s_setprio 2\n\t" \
  "v_lshlrev_b32 v130, 16, v1\n\t" \
  "v_and_b32 v131, 0xffff0000, v1\n\t" \
  "v_lshlrev_b32 v132, 16, v2\n\t" \
  "v_and_b32 v133, 0xffff0000, v2\n\t" \
  "v_lshlrev_b32 v134, 16, v3\n\t" \
  "v_and_b32 v135, 0xffff0000, v3\n\t" \
  "v_mul_f32 v96, v128, v67\n\t" \
  "v_mul_f32 v97, v129, v71\n\t" \
  "v_mul_f32 v98, v130, v75\n\t" \
  "v_mul_f32 v99, v131, v79\n\t" \
  "v_mul_f32 v100, v132, v83\n\t" \
  "v_mul_f32 v101, v133, v87\n\t" \
  "v_mul_f32 v102, v134, v91\n\t" \
  "v_mul_f32 v103, v135, v95\n\t" \
  "s_waitcnt vmcnt(6)\n\t" \
  "s_mov_b64 exec, %[zm]\n\t" \
  "v_mov_b32 v4, 0\n\t" \
  "v_mov_b32 v5, 0\n\t" \
  "v_mov_b32 v6, 0\n\t" \
  "v_mov_b32 v7, 0\n\t" \
  "s_mov_b64 exec, -1\n\t" \
  "v_lshlrev_b32 v128, 16, v4\n\t" \
  "v_and_b32 v129, 0xffff0000, v4\n\t" \
  "v_lshlrev_b32 v130, 16, v5\n\t" \
  "v_and_b32 v131, 0xffff0000, v5\n\t" \
  "v_lshlrev_b32 v132, 16, v6\n\t" \
  "v_and_b32 v133, 0xffff0000, v6\n\t" \
  "v_lshlrev_b32 v134, 16, v7\n\t" \
  "v_and_b32 v135, 0xffff0000, v7\n\t" \
  "v_fmac_f32 v96, v128, v66\n\t" \
  "v_fmac_f32 v97, v129, v70\n\t" \
  "v_fmac_f32 v98, v130, v74\n\t" \
  "v_fmac_f32 v99, v131, v78\n\t" \
  "v_fmac_f32 v100, v132, v82\n\t" \
  "v_fmac_f32 v101, v133, v86\n\t" \
  "v_fmac_f32 v102, v134, v90\n\t" \
  "v_fmac_f32 v103, v135, v94\n\t" \
  "v_mul_f32 v104, v128, v67\n\t" \
  "v_mul_f32 v105, v129, v71\n\t" \
  "v_mul_f32 v106, v130, v75\n\t" \
  "v_mul_f32 v107, v131, v79\n\t" \
  "v_mul_f32 v108, v132, v83\n\t" \
  "v_mul_f32 v109, v133, v87\n\t" \
  "v_mul_f32 v110, v134, v91\n\t" \
  "v_mul_f32 v111, v135, v95\n\t" \
  "s_waitcnt vmcnt(5)\n\t" \
  "s_mov_b64 exec, %[zm]\n\t" \
  "v_mov_b32 v8, 0\n\t" \
  "v_mov_b32 v9, 0\n\t" \
  "v_mov_b32 v10, 0\n\t" \
  "v_mov_b32 v11, 0\n\t" \
  "s_mov_b64 exec, -1\n\t" \
  "s_barrier\n\t" \
  "v_lshlrev_b32 v128, 16, v8\n\t" \
  "v_and_b32 v129, 0xffff0000, v8\n\t" \
  "v_lshlrev_b32 v130, 16, v9\n\t" \
  "v_and_b32 v131, 0xffff0000, v9\n\t" \
  "v_lshlrev_b32 v132, 16, v10\n\t" \
  "v_and_b32 v133, 0xffff0000, v10\n\t" \
  "v_lshlrev_b32 v134, 16, v11\n\t" \
  "v_and_b32 v135, 0xffff0000, v11\n\t" \
  "v_fmac_f32 v96, v128, v65\n\t" \
  "v_fmac_f32 v97, v129, v69\n\t" \
  "v_fmac_f32 v98, v130, v73\n\t" \
  "v_fmac_f32 v99, v131, v77\n\t" \
  "v_fmac_f32 v100, v132, v81\n\t" \
  "v_fmac_f32 v101, v133, v85\n\t" \
  "v_fmac_f32 v102, v134, v89\n\t" \
  "v_fmac_f32 v103, v135, v93\n\t" \
  "v_fmac_f32 v104, v128, v66\n\t" \
  "v_fmac_f32 v105, v129, v70\n\t" \
  "v_fmac_f32 v106, v130, v74\n\t" \
  "v_fmac_f32 v107, v131, v78\n\t" \
  "v_fmac_f32 v108, v132, v82\n\t" \
  "v_fmac_f32 v109, v133, v86\n\t" \
  "v_fmac_f32 v110, v134, v90\n\t" \
  "v_fmac_f32 v111, v135, v94\n\t" \
  "v_mul_f32 v112, v128, v67\n\t" \
  "v_mul_f32 v113, v129, v71\n\t" \
  "v_mul_f32 v114, v130, v75\n\t" \
  "v_mul_f32 v115, v131, v79\n\t" \
  "v_mul_f32 v116, v132, v83\n\t" \
  "v_mul_f32 v117, v133, v87\n\t" \
  "v_mul_f32 v118, v134, v91\n\t" \
  "v_mul_f32 v119, v135, v95\n\t" \
  "s_waitcnt vmcnt(4)\n\t" \
  "v_lshlrev_b32 v128, 16, v12\n\t" \
  "v_and_b32 v129, 0xffff0000, v12\n\t" \
  "v_lshlrev_b32 v130, 16, v13\n\t" \
  "v_and_b32 v131, 0xffff0000, v13\n\t" \
  "v_lshlrev_b32 v132, 16, v14\n\t" \
  "v_and_b32 v133, 0xffff0000, v14\n\t" \
  "v_lshlrev_b32 v134, 16, v15\n\t" \
  "v_and_b32 v135, 0xffff0000, v15\n\t" \
  "s_setprio 3\n\t" \
  "s_barrier\n\t" \
  "global_load_dwordx4 v[36:39], v155, %[vb2] offset:0\n\t" \
  "global_load_dwordx4 v[24:27], v155, %[kb5] offset:2048\n\t" \
  "global_load_dwordx4 v[40:43], v155, %[vb2] offset:2048\n\t" \
  "s_setprio 2\n\t" \
  "v_fmac_f32 v96, v128, v64\n\t" \
  "v_fmac_f32 v97, v129, v68\n\t" \
  "v_fmac_f32 v98, v130, v72\n\t" \
  "v_fmac_f32 v99, v131, v76\n\t" \
  "v_fmac_f32 v100, v132, v80\n\t" \
  "v_fmac_f32 v101, v133, v84\n\t" \
  "v_fmac_f32 v102, v134, v88\n\t" \
  "v_fmac_f32 v103, v135, v92\n\t" \
  "v_fmac_f32 v104, v128, v65\n\t" \
  "v_fmac_f32 v105, v129, v69\n\t" \
  "v_fmac_f32 v106, v130, v73\n\t" \
  "v_fmac_f32 v107, v131, v77\n\t" \
  "v_fmac_f32 v108, v132, v81\n\t" \
  "v_fmac_f32 v109, v133, v85\n\t" \
  "v_fmac_f32 v110, v134, v89\n\t" \
  "v_fmac_f32 v111, v135, v93\n\t" \
  "v_fmac_f32 v112, v128, v66\n\t" \
  "v_fmac_f32 v113, v129, v70\n\t" \
  "v_fmac_f32 v114, v130, v74\n\t" \
  "v_fmac_f32 v115, v131, v78\n\t" \
  "v_fmac_f32 v116, v132, v82\n\t" \
  "v_fmac_f32 v117, v133, v86\n\t" \
  "v_fmac_f32 v118, v134, v90\n\t" \
  "v_fmac_f32 v119, v135, v94\n\t" \
  "v_mul_f32 v120, v128, v67\n\t" \
  "v_mul_f32 v121, v129, v71\n\t" \
  "v_mul_f32 v122, v130, v75\n\t" \
  "v_mul_f32 v123, v131, v79\n\t" \
  "v_mul_f32 v124, v132, v83\n\t" \
  "v_mul_f32 v125, v133, v87\n\t" \
  "v_mul_f32 v126, v134, v91\n\t" \
  "v_mul_f32 v127, v135, v95\n\t" \
  "s_waitcnt vmcnt(6)\n\t" \
  "v_lshlrev_b32 v128, 16, v28\n\t" \
  "v_and_b32 v129, 0xffff0000, v28\n\t" \
  "v_lshlrev_b32 v130, 16, v29\n\t" \
  "v_and_b32 v131, 0xffff0000, v29\n\t" \
  "v_lshlrev_b32 v132, 16, v30\n\t" \
  "v_and_b32 v133, 0xffff0000, v30\n\t" \
  "v_lshlrev_b32 v134, 16, v31\n\t" \
  "v_and_b32 v135, 0xffff0000, v31\n\t" \
  "s_barrier\n\t" \
  "v_mul_f32 v96, v96, v128\n\t" \
  "v_mul_f32 v97, v97, v129\n\t" \
  "v_mul_f32 v98, v98, v130\n\t" \
  "v_mul_f32 v99, v99, v131\n\t" \
  "v_mul_f32 v100, v100, v132\n\t" \
  "v_mul_f32 v101, v101, v133\n\t" \
  "v_mul_f32 v102, v102, v134\n\t" \
  "v_mul_f32 v103, v103, v135\n\t" \
  "v_cvt_pk_bf16_f32 v136, v96, v97\n\t" \
  "v_cvt_pk_bf16_f32 v137, v98, v99\n\t" \
  "v_cvt_pk_bf16_f32 v138, v100, v101\n\t" \
  "v_cvt_pk_bf16_f32 v139, v102, v103\n\t" \
  "v_mov_b32_dpp v128, v136 row_ror:8 row_mask:0xf bank_mask:0xf\n\t" \
  "v_mov_b32_dpp v129, v137 row_ror:8 row_mask:0xf bank_mask:0xf\n\t" \
  "v_mov_b32_dpp v130, v138 row_ror:8 row_mask:0xf bank_mask:0xf\n\t" \
  "v_mov_b32_dpp v131, v139 row_ror:8 row_mask:0xf bank_mask:0xf\n\t" \
  "v_perm_b32 v136, v128, v136, v157\n\t" \
  "v_perm_b32 v137, v129, v137, v157\n\t" \
  "v_perm_b32 v138, v130, v138, v157\n\t" \
  "v_perm_b32 v139, v131, v139, v157\n\t" \
  "v_permlane16_swap_b32 v136, v137\n\t" \
  "s_nop 0\n\t" \
  "v_permlane16_swap_b32 v138, v139\n\t" \
  "s_nop 1\n\t" \
  "v_permlane32_swap_b32 v136, v138\n\t" \
  "v_permlane32_swap_b32 v137, v139\n\t" \
  "s_waitcnt vmcnt(5)\n\t" \
  "v_lshlrev_b32 v128, 16, v16\n\t" \
  "v_and_b32 v129, 0xffff0000, v16\n\t" \
  "v_lshlrev_b32 v130, 16, v17\n\t" \
  "v_and_b32 v131, 0xffff0000, v17\n\t" \
  "v_lshlrev_b32 v132, 16, v18\n\t" \
  "v_and_b32 v133, 0xffff0000, v18\n\t" \
  "v_lshlrev_b32 v134, 16, v19\n\t" \
  "v_and_b32 v135, 0xffff0000, v19\n\t" \
  "v_fmac_f32 v104, v128, v64\n\t" \
  "v_fmac_f32 v105, v129, v68\n\t" \
  "v_fmac_f32 v106, v130, v72\n\t" \
  "v_fmac_f32 v107, v131, v76\n\t" \
  "v_fmac_f32 v108, v132, v80\n\t" \
  "v_fmac_f32 v109, v133, v84\n\t" \
  "s_setprio 3\n\t" \
  "s_barrier\n\t" \
  "global_store_dwordx4 v159, v[136:139], %[sb] sc1\n\t" \
  "v_add_u32 v158, 0x80, v159\n\t" \
  "v_bfi_b32 v159, %[mk], v158, v159\n\t" \
  "s_setprio 2\n\t" \
  "v_fmac_f32 v110, v134, v88\n\t" \
  "v_fmac_f32 v111, v135, v92\n\t" \
  "v_fmac_f32 v112, v128, v65\n\t" \
  "v_fmac_f32 v113, v129, v69\n\t" \
  "v_fmac_f32 v114, v130, v73\n\t" \
  "v_fmac_f32 v115, v131, v77\n\t" \
  "v_fmac_f32 v116, v132, v81\n\t" \
  "v_fmac_f32 v117, v133, v85\n\t" \
  "v_fmac_f32 v118, v134, v89\n\t" \
  "v_fmac_f32 v119, v135, v93\n\t" \
  "v_fmac_f32 v120, v128, v66\n\t" \
  "v_fmac_f32 v121, v129, v70\n\t" \
  "v_fmac_f32 v122, v130, v74\n\t" \
  "v_fmac_f32 v123, v131, v78\n\t" \
  "v_fmac_f32 v124, v132, v82\n\t" \
  "v_fmac_f32 v125, v133, v86\n\t" \
  "v_fmac_f32 v126, v134, v90\n\t" \
  "v_fmac_f32 v127, v135, v94\n\t" \
  "s_waitcnt vmcnt(5)\n\t" \
  "v_lshlrev_b32 v128, 16, v32\n\t" \
  "v_and_b32 v129, 0xffff0000, v32\n\t" \
  "v_lshlrev_b32 v130, 16, v33\n\t" \
  "v_and_b32 v131, 0xffff0000, v33\n\t" \
  "v_lshlrev_b32 v132, 16, v34\n\t" \
  "v_and_b32 v133, 0xffff0000, v34\n\t" \
  "v_lshlrev_b32 v134, 16, v35\n\t" \
  "v_and_b32 v135, 0xffff0000, v35\n\t" \
  "v_mul_f32 v104, v104, v128\n\t" \
  "v_mul_f32 v105, v105, v129\n\t" \
  "v_mul_f32 v106, v106, v130\n\t" \
  "v_mul_f32 v107, v107, v131\n\t" \
  "v_mul_f32 v108, v108, v132\n\t" \
  "v_mul_f32 v109, v109, v133\n\t" \
  "v_mul_f32 v110, v110, v134\n\t" \
  "v_mul_f32 v111, v111, v135\n\t" \
  "v_cvt_pk_bf16_f32 v140, v104, v105\n\t" \
  "v_cvt_pk_bf16_f32 v141, v106, v107\n\t" \
  "v_cvt_pk_bf16_f32 v142, v108, v109\n\t" \
  "v_cvt_pk_bf16_f32 v143, v110, v111\n\t" \
  "v_mov_b32_dpp v128, v140 row_ror:8 row_mask:0xf bank_mask:0xf\n\t" \
  "v_mov_b32_dpp v129, v141 row_ror:8 row_mask:0xf bank_mask:0xf\n\t" \
  "s_barrier\n\t" \
  "v_mov_b32_dpp v130, v142 row_ror:8 row_mask:0xf bank_mask:0xf\n\t" \
  "v_mov_b32_dpp v131, v143 row_ror:8 row_mask:0xf bank_mask:0xf\n\t" \
  "v_perm_b32 v140, v128, v140, v157\n\t" \
  "v_perm_b32 v141, v129, v141, v157\n\t" \
  "v_perm_b32 v142, v130, v142, v157\n\t" \
  "v_perm_b32 v143, v131, v143, v157\n\t" \
  "v_permlane16_swap_b32 v140, v141\n\t" \
  "s_nop 0\n\t" \
  "v_permlane16_swap_b32 v142, v143\n\t" \
  "s_nop 1\n\t" \
  "v_permlane32_swap_b32 v140, v142\n\t" \
  "v_permlane32_swap_b32 v141, v143\n\t" \
  "s_waitcnt vmcnt(4)\n\t" \
  "v_lshlrev_b32 v128, 16, v20\n\t" \
  "v_and_b32 v129, 0xffff0000, v20\n\t" \
  "v_lshlrev_b32 v130, 16, v21\n\t" \
  "v_and_b32 v131, 0xffff0000, v21\n\t" \
  "v_lshlrev_b32 v132, 16, v22\n\t" \
  "v_and_b32 v133, 0xffff0000, v22\n\t" \
  "v_lshlrev_b32 v134, 16, v23\n\t" \
  "v_and_b32 v135, 0xffff0000, v23\n\t" \
  "v_fmac_f32 v112, v128, v64\n\t" \
  "v_fmac_f32 v113, v129, v68\n\t" \
  "v_fmac_f32 v114, v130, v72\n\t" \
  "v_fmac_f32 v115, v131, v76\n\t" \
  "v_fmac_f32 v116, v132, v80\n\t" \
  "v_fmac_f32 v117, v133, v84\n\t" \
  "v_fmac_f32 v118, v134, v88\n\t" \
  "v_fmac_f32 v119, v135, v92\n\t" \
  "v_fmac_f32 v120, v128, v65\n\t" \
  "v_fmac_f32 v121, v129, v69\n\t" \
  "v_fmac_f32 v122, v130, v73\n\t" \
  "v_fmac_f32 v123, v131, v77\n\t" \
  "v_fmac_f32 v124, v132, v81\n\t" \
  "v_fmac_f32 v125, v133, v85\n\t" \
  "v_fmac_f32 v126, v134, v89\n\t" \
  "v_fmac_f32 v127, v135, v93\n\t" \
  "s_waitcnt vmcnt(3)\n\t" \
  "v_lshlrev_b32 v128, 16, v36\n\t" \
  "v_and_b32 v129, 0xffff0000, v36\n\t" \
  "v_lshlrev_b32 v130, 16, v37\n\t" \
  "v_and_b32 v131, 0xffff0000, v37\n\t" \
  "s_setprio 3\n\t" \
  "s_barrier\n\t" \
  "global_store_dwordx4 v159, v[140:143], %[sb] sc1\n\t" \
  "v_add_u32 v158, 0x80, v159\n\t" \
  "v_bfi_b32 v159, %[mk], v158, v159\n\t" \
  "s_mov_b32 m0, %[hd]\n\t" \
  "s_nop 0\n\t" \
  "global_load_lds_dwordx4 v163, %[hp] offset:0\n\t" \
  "global_load_lds_dwordx4 v163, %[hp] offset:1024\n\t" \
  "global_load_lds_dwordx4 v163, %[hp] offset:2048\n\t" \
  "global_load_lds_dwordx4 v163, %[hp] offset:3072\n\t" \
  "s_setprio 2\n\t" \
  "v_lshlrev_b32 v132, 16, v38\n\t" \
  "v_and_b32 v133, 0xffff0000, v38\n\t" \
  "v_lshlrev_b32 v134, 16, v39\n\t" \
  "v_and_b32 v135, 0xffff0000, v39\n\t" \
  "v_mul_f32 v112, v112, v128\n\t" \
  "v_mul_f32 v113, v113, v129\n\t" \
  "v_mul_f32 v114, v114, v130\n\t" \
  "v_mul_f32 v115, v115, v131\n\t" \
  "v_mul_f32 v116, v116, v132\n\t" \
  "v_mul_f32 v117, v117, v133\n\t" \
  "v_mul_f32 v118, v118, v134\n\t" \
  "v_mul_f32 v119, v119, v135\n\t" \
  "v_cvt_pk_bf16_f32 v144, v112, v113\n\t" \
  "v_cvt_pk_bf16_f32 v145, v114, v115\n\t" \
  "v_cvt_pk_bf16_f32 v146, v116, v117\n\t" \
  "v_cvt_pk_bf16_f32 v147, v118, v119\n\t" \
  "v_mov_b32_dpp v128, v144 row_ror:8 row_mask:0xf bank_mask:0xf\n\t" \
  "v_mov_b32_dpp v129, v145 row_ror:8 row_mask:0xf bank_mask:0xf\n\t" \
  "v_mov_b32_dpp v130, v146 row_ror:8 row_mask:0xf bank_mask:0xf\n\t" \
  "v_mov_b32_dpp v131, v147 row_ror:8 row_mask:0xf bank_mask:0xf\n\t" \
  "v_perm_b32 v144, v128, v144, v157\n\t" \
  "v_perm_b32 v145, v129, v145, v157\n\t" \
  "v_perm_b32 v146, v130, v146, v157\n\t" \
  "v_perm_b32 v147, v131, v147, v157\n\t" \
  "v_permlane16_swap_b32 v144, v145\n\t" \
  "s_nop 0\n\t" \
  "v_permlane16_swap_b32 v146, v147\n\t" \
  "s_nop 1\n\t" \
  "v_permlane32_swap_b32 v144, v146\n\t" \
  "v_permlane32_swap_b32 v145, v147\n\t" \
  "s_waitcnt vmcnt(7)\n\t" \
  "v_lshlrev_b32 v128, 16, v24\n\t" \
  "v_and_b32 v129, 0xffff0000, v24\n\t" \
  "v_lshlrev_b32 v130, 16, v25\n\t" \
  "v_and_b32 v131, 0xffff0000, v25\n\t" \
  "v_lshlrev_b32 v132, 16, v26\n\t" \
  "v_and_b32 v133, 0xffff0000, v26\n\t" \
  "v_lshlrev_b32 v134, 16, v27\n\t" \
  "v_and_b32 v135, 0xffff0000, v27\n\t" \
  "v_fmac_f32 v120, v128, v64\n\t" \
  "v_fmac_f32 v121, v129, v68\n\t" \
  "s_setprio 3\n\t" \
  "s_barrier\n\t" \
  "global_store_dwordx4 v159, v[144:147], %[sb] sc1\n\t" \
  "v_add_u32 v158, 0x80, v159\n\t" \
  "v_bfi_b32 v159, %[mk], v158, v159\n\t" \
  "s_setprio 2\n\t" \
  "v_fmac_f32 v122, v130, v72\n\t" \
  "v_fmac_f32 v123, v131, v76\n\t" \
  "v_fmac_f32 v124, v132, v80\n\t" \
  "v_fmac_f32 v125, v133, v84\n\t" \
  "v_fmac_f32 v126, v134, v88\n\t" \
  "v_fmac_f32 v127, v135, v92\n\t" \
  "s_waitcnt vmcnt(7)\n\t" \
  "v_lshlrev_b32 v128, 16, v40\n\t" \
  "v_and_b32 v129, 0xffff0000, v40\n\t" \
  "v_lshlrev_b32 v130, 16, v41\n\t" \
  "v_and_b32 v131, 0xffff0000, v41\n\t" \
  "v_lshlrev_b32 v132, 16, v42\n\t" \
  "v_and_b32 v133, 0xffff0000, v42\n\t" \
  "v_lshlrev_b32 v134, 16, v43\n\t" \
  "v_and_b32 v135, 0xffff0000, v43\n\t" \
  "v_mul_f32 v120, v120, v128\n\t" \
  "v_mul_f32 v121, v121, v129\n\t" \
  "v_mul_f32 v122, v122, v130\n\t" \
  "v_mul_f32 v123, v123, v131\n\t" \
  "v_mul_f32 v124, v124, v132\n\t" \
  "v_mul_f32 v125, v125, v133\n\t" \
  "v_mul_f32 v126, v126, v134\n\t" \
  "v_mul_f32 v127, v127, v135\n\t" \
  "v_cvt_pk_bf16_f32 v148, v120, v121\n\t" \
  "v_cvt_pk_bf16_f32 v149, v122, v123\n\t" \
  "v_cvt_pk_bf16_f32 v150, v124, v125\n\t" \
  "v_cvt_pk_bf16_f32 v151, v126, v127\n\t" \
  "v_mov_b32_dpp v128, v148 row_ror:8 row_mask:0xf bank_mask:0xf\n\t" \
  "v_mov_b32_dpp v129, v149 row_ror:8 row_mask:0xf bank_mask:0xf\n\t" \
  "v_mov_b32_dpp v130, v150 row_ror:8 row_mask:0xf bank_mask:0xf\n\t" \
  "v_mov_b32_dpp v131, v151 row_ror:8 row_mask:0xf bank_mask:0xf\n\t" \
  "v_perm_b32 v148, v128, v148, v157\n\t" \
  "v_perm_b32 v149, v129, v149, v157\n\t" \
  "v_perm_b32 v150, v130, v150, v157\n\t" \
  "v_perm_b32 v151, v131, v151, v157\n\t" \
  "v_permlane16_swap_b32 v148, v149\n\t" \
  "s_nop 0\n\t" \
  "v_permlane16_swap_b32 v150, v151\n\t" \
  "s_nop 1\n\t" \
  "v_permlane32_swap_b32 v148, v150\n\t" \
  "v_permlane32_swap_b32 v149, v151\n\t" \
  "s_setprio 3\n\t" \
  "s_barrier\n\t" \
  "global_store_dwordx4 v159, v[148:151], %[sb] sc1\n\t" \
  "s_setprio 2\n\t" \
  "s_waitcnt vmcnt(2)\n\t" \
  "ds_read_b64_tr_b16 v[64:65], v165 offset:32\n\t" \
  "ds_read_b64_tr_b16 v[66:67], v165 offset:0\n\t" \
  "ds_read_b64_tr_b16 v[68:69], v165 offset:288\n\t" \
  "ds_read_b64_tr_b16 v[70:71], v165 offset:256\n\t" \
  "ds_read_b64_tr_b16 v[72:73], v165 offset:544\n\t" \
  "ds_read_b64_tr_b16 v[74:75], v165 offset:512\n\t" \
  "ds_read_b64_tr_b16 v[76:77], v165 offset:800\n\t" \
  "ds_read_b64_tr_b16 v[78:79], v165 offset:768\n\t" \
  "v_mov_b32 v24, v166\n\t" \
  "v_mov_b32 v25, v167\n\t" \
  "v_mov_b32 v63, 0xc0\n\t" \
  "v_add_u32 v158, 64, v24\n\t" \
  "v_bfi_b32 v26, v63, v158, v24\n\t" \
  "v_add_u32 v158, 64, v25\n\t" \
  "v_bfi_b32 v27, v63, v158, v25\n\t" \
  "v_add_u32 v158, 64, v26\n\t" \
  "v_bfi_b32 v28, v63, v158, v26\n\t" \
  "v_add_u32 v158, 64, v27\n\t" \
  "v_bfi_b32 v29, v63, v158, v27\n\t" \
  "v_add_u32 v158, 64, v28\n\t" \
  "v_bfi_b32 v30, v63, v158, v28\n\t" \
  "v_add_u32 v158, 64, v29\n\t" \
  "v_bfi_b32 v31, v63, v158, v29\n\t" \
  "s_barrier\n\t" \
  "ds_read_b64_tr_b16 v[80:81], v165 offset:1056\n\t" \
  "ds_read_b64_tr_b16 v[82:83], v165 offset:1024\n\t" \
  "ds_read_b64_tr_b16 v[84:85], v165 offset:1312\n\t" \
  "ds_read_b64_tr_b16 v[86:87], v165 offset:1280\n\t" \
  "ds_read_b64_tr_b16 v[88:89], v165 offset:1568\n\t" \
  "ds_read_b64_tr_b16 v[90:91], v165 offset:1536\n\t" \
  "ds_read_b64_tr_b16 v[92:93], v165 offset:1824\n\t" \
  "ds_read_b64_tr_b16 v[94:95], v165 offset:1792\n\t" \
  "s_setprio 3\n\t" \
  "s_barrier\n\t" \
  "s_waitcnt vmcnt(0)\n\t" \
  "v_add_u32 v61, 1, v160\n\t" \
  "s_mov_b64 exec, 1\n\t" \
  "global_store_dword v60, v61, %[ff] sc1\n\t" \
  "s_mov_b64 exec, -1\n\t" \
  "s_setprio 2\n\t" \
  "ds_read_b64_tr_b16 v[96:97], v165 offset:2080\n\t" \
  "ds_read_b64_tr_b16 v[98:99], v165 offset:2048\n\t" \
  "ds_read_b64_tr_b16 v[100:101], v165 offset:2336\n\t" \
  "ds_read_b64_tr_b16 v[102:103], v165 offset:2304\n\t" \
  "ds_read_b64_tr_b16 v[104:105], v165 offset:2592\n\t" \
  "ds_read_b64_tr_b16 v[106:107], v165 offset:2560\n\t" \
  "ds_read_b64_tr_b16 v[108:109], v165 offset:2848\n\t" \
  "ds_read_b64_tr_b16 v[110:111], v165 offset:2816\n\t" \
  "s_barrier\n\t" \
  "ds_read_b64_tr_b16 v[112:113], v165 offset:3104\n\t" \
  "ds_read_b64_tr_b16 v[114:115], v165 offset:3072\n\t" \
  "ds_read_b64_tr_b16 v[116:117], v165 offset:3360\n\t" \
  "ds_read_b64_tr_b16 v[118:119], v165 offset:3328\n\t" \
  "ds_read_b64_tr_b16 v[120:121], v165 offset:3616\n\t" \
  "ds_read_b64_tr_b16 v[122:123], v165 offset:3584\n\t" \
  "ds_read_b64_tr_b16 v[124:125], v165 offset:3872\n\t" \
  "ds_read_b64_tr_b16 v[126:127], v165 offset:3840\n\t" \
  "s_barrier\n\t" \
  "s_waitcnt lgkmcnt(0)\n\t" \
  "v_lshlrev_b32 v61, 16, v161\n\t" \
  "v_lshlrev_b32 v62, 16, v162\n\t" \
  "v_add_f32 v61, v61, v62\n\t" \
  "v_mov_b32 v62, 0xffff\n\t" \
  "v_cvt_pk_bf16_f32 v61, v61, v61\n\t" \
  "s_mov_b64 exec, 1\n\t" \
  "v_bfi_b32 v66, v62, v61, v66\n\t" \
  "s_mov_b64 exec, 15\n\t" \
  "v_and_b32 v66, v62, v66\n\t" \
  "s_mov_b64 exec, 16\n\t" \
  "v_bfi_b32 v66, v62, v66, v61\n\t" \
  "s_mov_b64 exec, 0xff\n\t" \
  "v_mov_b32 v67, 0\n\t" \
  "s_mov_b64 exec, 0x100\n\t" \
  "v_mov_b32 v67, v61\n\t" \
  "s_mov_b64 exec, 0xf00\n\t" \
  "v_and_b32 v67, v62, v67\n\t" \
  "s_mov_b64 exec, 0x1000\n\t" \
  "v_bfi_b32 v67, v62, v67, v61\n\t" \
  "s_mov_b64 exec, -1\n\t" \
  "s_waitcnt lgkmcnt(0)\n\t"
#define PP_NB 15
#define PP_Q 0
#define PP_CLOB "v0", "v1", "v2", "v3", "v4", "v5", "v6", "v7", "v8", "v9", "v10", "v11", "v12", "v13", "v14", "v15", "v16", "v17", "v18", "v19", "v20", "v21", "v22", "v23", "v32", "v33", "v34", "v35", "v36", "v37", "v38", "v39", "v40", "v41", "v42", "v43", "v60", "v61", "v62", "v63", "v128", "v129", "v130", "v131", "v132", "v133", "v134", "v135", "v136", "v137", "v138", "v139", "v140", "v141", "v142", "v143", "v144", "v145", "v146", "v147", "v148", "v149", "v150", "v151", "v158", "v161", "v162"

#define LD_ASM1 \
  "s_add_u32 m0, s39, 49152\n\t" \
  "s_mov_b32 s68, 0x0\n\t" \
  "buffer_load_dwordx4 v5, s[44:47], s68 offen offset:0 lds\n\t" \
  "buffer_load_dwordx4 v6, s[44:47], s68 offen offset:1024 lds\n\t" \
  "buffer_load_dwordx4 v7, s[44:47], s68 offen offset:2048 lds\n\t" \
  "s_add_u32 m0, s38, 36864\n\t" \
  "s_mov_b32 s68, 0x0\n\t" \
  "buffer_load_dwordx4 v0, s[40:43], s68 offen offset:0 lds\n\t" \
  "buffer_load_dwordx4 v1, s[40:43], s68 offen offset:1024 lds\n\t" \
  "s_mov_b64 exec, 0xffff\n\t" \
  "buffer_load_dwordx4 v4, s[40:43], s68 offen offset:2048 lds\n\t" \
  "s_mov_b64 exec, -1\n\t" \
  "s_add_u32 m0, s38, 46080\n\t" \
  "s_mov_b32 s68, 0x24000\n\t" \
  "buffer_load_dwordx4 v2, s[40:43], s68 offen offset:0 lds\n\t" \
  "buffer_load_dwordx4 v3, s[40:43], s68 offen offset:1024 lds\n\t" \
  "s_mov_b64 exec, 0xffff\n\t" \
  "buffer_load_dwordx4 v4, s[40:43], s68 offen offset:2048 lds\n\t" \
  "s_mov_b64 exec, -1\n\t" \
  "s_mov_b32 m0, %[sk]\n\t" \
  "s_mov_b32 exec_hi, 0\n\t" \
  "buffer_load_dword v9, s[40:43], 0 offen lds\n\t" \
  "s_mov_b32 exec_lo, %[wm]\n\t" \
  "buffer_load_dword v10, s[44:47], 0 offen lds\n\t" \
  "s_mov_b64 exec, -1\n\t" \
  "s_add_u32 m0, s39, 61440\n\t" \
  "s_mov_b32 s68, 0x30000\n\t" \
  "buffer_load_dwordx4 v5, s[44:47], s68 offen offset:0 lds\n\t" \
  "buffer_load_dwordx4 v6, s[44:47], s68 offen offset:1024 lds\n\t" \
  "buffer_load_dwordx4 v7, s[44:47], s68 offen offset:2048 lds\n\t"
#define LD_ASM2 \
  "s_mov_b32 s85, 0x180000\n\t" \
  "s_load_dword s84, %[pw], s85\n\t" \
  "s_mov_b32 s85, 0x180800\n\t" \
  "s_load_dword s84, %[pw], s85\n\t" \
  "s_mov_b32 s85, 0x181000\n\t" \
  "s_load_dword s84, %[pw], s85\n\t" \
  "s_mov_b32 s85, 0x1b0000\n\t" \
  "s_load_dword s84, %[pw], s85\n\t" \
  "s_mov_b32 s85, 0x1b0800\n\t" \
  "s_load_dword s84, %[pw], s85\n\t" \
  "s_mov_b32 s85, 0x1b1000\n\t" \
  "s_load_dword s84, %[pw], s85\n\t" \
  "s_mov_b32 s85, 0x1e0000\n\t" \
  "s_load_dword s84, %[pw], s85\n\t" \
  "s_mov_b32 s85, 0x1e0800\n\t" \
  "s_load_dword s84, %[pw], s85\n\t" \
  "s_mov_b32 m0, %[tsk]\n\t" \
  "s_mov_b32 s85, 0x0\n\t" \
  "s_mov_b64 exec, %[hm]\n\t" \
  "buffer_load_dword v16, %[sh], s85 offen lds\n\t" \
  "s_mov_b64 exec, -1\n\t" \
  "s_mov_b32 s68, 0x80\n\t" \
  "s_mov_b32 s69, 0x24080\n\t" \
  "s_mov_b32 s70, 0x60000\n\t" \
  "s_add_u32 m0, s38, 0\n\t" \
  "s_waitcnt vmcnt(6)\n\t" \
  "s_barrier\n\t" \
  "buffer_load_dwordx4 v0, s[40:43], s68 offen offset:0 lds\n\t" \
  "s_mov_b32 s85, 0x1e1000\n\t" \
  "s_load_dword s84, %[pw], s85\n\t" \
  "buffer_load_dwordx4 v1, s[40:43], s68 offen offset:1024 lds\n\t" \
  "s_mov_b32 s85, 0x210000\n\t" \
  "s_load_dword s84, %[pw], s85\n\t" \
  "s_mov_b64 exec, 0xffff\n\t" \
  "buffer_load_dwordx4 v4, s[40:43], s68 offen offset:2048 lds\n\t" \
  "s_add_u32 m0, s38, 9216\n\t" \
  "s_mov_b64 exec, -1\n\t" \
  "buffer_load_dwordx4 v2, s[40:43], s69 offen offset:0 lds\n\t" \
  "s_mov_b32 s85, 0x210800\n\t" \
  "s_load_dword s84, %[pw], s85\n\t" \
  "buffer_load_dwordx4 v3, s[40:43], s69 offen offset:1024 lds\n\t" \
  "s_mov_b64 exec, 0xffff\n\t" \
  "buffer_load_dwordx4 v4, s[40:43], s69 offen offset:2048 lds\n\t" \
  "s_add_u32 m0, s39, 0\n\t" \
  "s_mov_b64 exec, -1\n\t" \
  "buffer_load_dwordx4 v5, s[44:47], s70 offen offset:0 lds\n\t" \
  "buffer_load_dwordx4 v6, s[44:47], s70 offen offset:1024 lds\n\t" \
  "buffer_load_dwordx4 v7, s[44:47], s70 offen offset:2048 lds\n\t" \
  "s_mov_b32 m0, %[tsk]\n\t" \
  "s_mov_b32 s85, 0x180000\n\t" \
  "s_mov_b64 exec, %[hm]\n\t" \
  "buffer_load_dword v16, %[sh], s85 offen lds\n\t" \
  "s_mov_b64 exec, -1\n\t" \
  "s_mov_b32 s68, 0x90000\n\t" \
  "s_mov_b32 s69, 0x100\n\t" \
  "s_mov_b32 s70, 0x24100\n\t" \
  "s_add_u32 m0, s39, 12288\n\t" \
  "s_waitcnt vmcnt(11)\n\t" \
  "s_barrier\n\t" \
  "buffer_load_dwordx4 v5, s[44:47], s68 offen offset:0 lds\n\t" \
  "s_mov_b32 s85, 0x211000\n\t" \
  "s_load_dword s84, %[pw], s85\n\t" \
  "buffer_load_dwordx4 v6, s[44:47], s68 offen offset:1024 lds\n\t" \
  "s_mov_b32 s85, 0x240000\n\t" \
  "s_load_dword s84, %[pw], s85\n\t" \
  "buffer_load_dwordx4 v7, s[44:47], s68 offen offset:2048 lds\n\t" \
  "s_mov_b32 s85, 0x240800\n\t" \
  "s_load_dword s84, %[pw], s85\n\t" \
  "s_add_u32 m0, s38, 18432\n\t" \
  "s_nop 0\n\t" \
  "buffer_load_dwordx4 v0, s[40:43], s69 offen offset:0 lds\n\t" \
  "buffer_load_dwordx4 v1, s[40:43], s69 offen offset:1024 lds\n\t" \
  "s_mov_b64 exec, 0xffff\n\t" \
  "buffer_load_dwordx4 v4, s[40:43], s69 offen offset:2048 lds\n\t" \
  "s_add_u32 m0, s38, 27648\n\t" \
  "s_mov_b64 exec, -1\n\t" \
  "buffer_load_dwordx4 v2, s[40:43], s70 offen offset:0 lds\n\t" \
  "buffer_load_dwordx4 v3, s[40:43], s70 offen offset:1024 lds\n\t" \
  "s_mov_b64 exec, 0xffff\n\t" \
  "buffer_load_dwordx4 v4, s[40:43], s70 offen offset:2048 lds\n\t" \
  "s_mov_b64 exec, -1\n\t" \
  "s_mov_b32 s68, 0xc0000\n\t" \
  "s_mov_b32 s69, 0xf0000\n\t" \
  "s_mov_b32 s70, 0x180\n\t" \
  "s_add_u32 m0, s39, 24576\n\t" \
  "s_waitcnt vmcnt(10)\n\t" \
  "s_barrier\n\t" \
  "buffer_load_dwordx4 v5, s[44:47], s68 offen offset:0 lds\n\t" \
  "s_mov_b32 s85, 0x241000\n\t" \
  "s_load_dword s84, %[pw], s85\n\t" \
  "buffer_load_dwordx4 v6, s[44:47], s68 offen offset:1024 lds\n\t" \
  "s_mov_b32 s85, 0x270000\n\t" \
  "s_load_dword s84, %[pw], s85\n\t" \
  "buffer_load_dwordx4 v7, s[44:47], s68 offen offset:2048 lds\n\t" \
  "s_mov_b32 s85, 0x270800\n\t" \
  "s_load_dword s84, %[pw], s85\n\t" \
  "s_add_u32 m0, s39, 36864\n\t" \
  "s_nop 0\n\t" \
  "buffer_load_dwordx4 v5, s[44:47], s69 offen offset:0 lds\n\t" \
  "buffer_load_dwordx4 v6, s[44:47], s69 offen offset:1024 lds\n\t" \
  "buffer_load_dwordx4 v7, s[44:47], s69 offen offset:2048 lds\n\t" \
  "s_add_u32 m0, s38, 36864\n\t" \
  "s_nop 0\n\t" \
  "buffer_load_dwordx4 v0, s[40:43], s70 offen offset:0 lds\n\t" \
  "buffer_load_dwordx4 v1, s[40:43], s70 offen offset:1024 lds\n\t" \
  "s_mov_b64 exec, 0xffff\n\t" \
  "buffer_load_dwordx4 v4, s[40:43], s70 offen offset:2048 lds\n\t" \
  "s_mov_b64 exec, -1\n\t" \
  "s_mov_b32 s68, 0x120000\n\t" \
  "s_mov_b32 s69, 0x24180\n\t" \
  "s_mov_b32 s70, 0x150000\n\t" \
  "s_add_u32 m0, s39, 49152\n\t" \
  "s_waitcnt vmcnt(15)\n\t" \
  "s_barrier\n\t" \
  "buffer_load_dwordx4 v5, s[44:47], s68 offen offset:0 lds\n\t" \
  "s_mov_b32 s85, 0x300000\n\t" \
  "s_load_dword s84, %[ph], s85\n\t" \
  "buffer_load_dwordx4 v6, s[44:47], s68 offen offset:1024 lds\n\t" \
  "s_mov_b32 s85, 0x300800\n\t" \
  "s_load_dword s84, %[ph], s85\n\t" \
  "buffer_load_dwordx4 v7, s[44:47], s68 offen offset:2048 lds\n\t" \
  "s_mov_b32 s85, 0x271000\n\t" \
  "s_load_dword s84, %[pw], s85\n\t" \
  "s_add_u32 m0, s38, 46080\n\t" \
  "s_nop 0\n\t" \
  "buffer_load_dwordx4 v2, s[40:43], s69 offen offset:0 lds\n\t" \
  "s_mov_b32 s85, 0x2a0000\n\t" \
  "s_load_dword s84, %[pw], s85\n\t" \
  "buffer_load_dwordx4 v3, s[40:43], s69 offen offset:1024 lds\n\t" \
  "s_mov_b32 s85, 0x2a0800\n\t" \
  "s_load_dword s84, %[pw], s85\n\t" \
  "s_mov_b64 exec, 0xffff\n\t" \
  "buffer_load_dwordx4 v4, s[40:43], s69 offen offset:2048 lds\n\t" \
  "s_add_u32 m0, s39, 61440\n\t" \
  "s_mov_b64 exec, -1\n\t" \
  "buffer_load_dwordx4 v5, s[44:47], s70 offen offset:0 lds\n\t" \
  "buffer_load_dwordx4 v6, s[44:47], s70 offen offset:1024 lds\n\t" \
  "buffer_load_dwordx4 v7, s[44:47], s70 offen offset:2048 lds\n\t" \
  "s_mov_b32 s68, 0x200\n\t" \
  "s_mov_b32 s69, 0x180000\n\t" \
  "s_mov_b32 s70, 0x24200\n\t" \
  "s_add_u32 m0, s38, 0\n\t" \
  "s_waitcnt vmcnt(15)\n\t" \
  "s_barrier\n\t" \
  "buffer_load_dwordx4 v0, s[40:43], s68 offen offset:0 lds\n\t" \
  "s_mov_b32 s85, 0x301000\n\t" \
  "s_load_dword s84, %[ph], s85\n\t" \
  "buffer_load_dwordx4 v1, s[40:43], s68 offen offset:1024 lds\n\t" \
  "s_mov_b32 s85, 0x2a1000\n\t" \
  "s_load_dword s84, %[pw], s85\n\t" \
  "s_mov_b64 exec, 0xffff\n\t" \
  "buffer_load_dwordx4 v4, s[40:43], s68 offen offset:2048 lds\n\t" \
  "s_add_u32 m0, s39, 0\n\t" \
  "s_mov_b64 exec, -1\n\t" \
  "buffer_load_dwordx4 v5, s[44:47], s69 offen offset:0 lds\n\t" \
  "s_mov_b32 s85, 0x2d0000\n\t" \
  "s_load_dword s84, %[pw], s85\n\t" \
  "buffer_load_dwordx4 v6, s[44:47], s69 offen offset:1024 lds\n\t" \
  "s_mov_b32 s85, 0x2d0800\n\t" \
  "s_load_dword s84, %[pw], s85\n\t" \
  "buffer_load_dwordx4 v7, s[44:47], s69 offen offset:2048 lds\n\t" \
  "s_add_u32 m0, s38, 9216\n\t" \
  "s_nop 0\n\t" \
  "buffer_load_dwordx4 v2, s[40:43], s70 offen offset:0 lds\n\t" \
  "buffer_load_dwordx4 v3, s[40:43], s70 offen offset:1024 lds\n\t" \
  "s_mov_b64 exec, 0xffff\n\t" \
  "buffer_load_dwordx4 v4, s[40:43], s70 offen offset:2048 lds\n\t" \
  "s_mov_b64 exec, -1\n\t" \
  "s_sleep 2\n\t" \
  "s_mov_b32 s68, 0x1b0000\n\t" \
  "s_add_u32 m0, s39, 12288\n\t" \
  "s_waitcnt vmcnt(21)\n\t" \
  "s_barrier\n\t" \
  "buffer_load_dwordx4 v5, s[44:47], s68 offen offset:0 lds\n\t" \
  "s_mov_b32 s85, 0x360000\n\t" \
  "s_load_dword s84, %[ph], s85\n\t" \
  "buffer_load_dwordx4 v6, s[44:47], s68 offen offset:1024 lds\n\t" \
  "s_mov_b32 s85, 0x360800\n\t" \
  "s_load_dword s84, %[ph], s85\n\t" \
  "buffer_load_dwordx4 v7, s[44:47], s68 offen offset:2048 lds\n\t" \
  "s_mov_b32 s85, 0x2d1000\n\t" \
  "s_load_dword s84, %[pw], s85\n\t" \
  "s_mov_b32 s85, 0x300000\n\t" \
  "s_load_dword s84, %[pw], s85\n\t" \
  "s_mov_b32 s85, 0x300800\n\t" \
  "s_load_dword s84, %[pw], s85\n\t" \
  "s_sleep 2\n\t" \
  "s_mov_b32 s68, 0x280\n\t" \
  "s_mov_b32 s69, 0x1e0000\n\t" \
  "s_mov_b32 s70, 0x24280\n\t" \
  "s_add_u32 m0, s38, 18432\n\t" \
  "s_waitcnt vmcnt(15)\n\t" \
  "s_barrier\n\t" \
  "buffer_load_dwordx4 v0, s[40:43], s68 offen offset:0 lds\n\t" \
  "s_mov_b32 s85, 0x361000\n\t" \
  "s_load_dword s84, %[ph], s85\n\t" \
  "buffer_load_dwordx4 v1, s[40:43], s68 offen offset:1024 lds\n\t" \
  "s_mov_b32 s85, 0x301000\n\t" \
  "s_load_dword s84, %[pw], s85\n\t" \
  "s_mov_b64 exec, 0xffff\n\t" \
  "buffer_load_dwordx4 v4, s[40:43], s68 offen offset:2048 lds\n\t" \
  "s_add_u32 m0, s39, 24576\n\t" \
  "s_mov_b64 exec, -1\n\t" \
  "buffer_load_dwordx4 v5, s[44:47], s69 offen offset:0 lds\n\t" \
  "s_mov_b32 s85, 0x330000\n\t" \
  "s_load_dword s84, %[pw], s85\n\t" \
  "buffer_load_dwordx4 v6, s[44:47], s69 offen offset:1024 lds\n\t" \
  "s_mov_b32 s85, 0x330800\n\t" \
  "s_load_dword s84, %[pw], s85\n\t" \
  "buffer_load_dwordx4 v7, s[44:47], s69 offen offset:2048 lds\n\t" \
  "s_add_u32 m0, s38, 27648\n\t" \
  "s_nop 0\n\t" \
  "buffer_load_dwordx4 v2, s[40:43], s70 offen offset:0 lds\n\t" \
  "buffer_load_dwordx4 v3, s[40:43], s70 offen offset:1024 lds\n\t" \
  "s_mov_b64 exec, 0xffff\n\t" \
  "buffer_load_dwordx4 v4, s[40:43], s70 offen offset:2048 lds\n\t" \
  "s_mov_b64 exec, -1\n\t" \
  "s_sleep 2\n\t" \
  "s_mov_b32 s68, 0x210000\n\t" \
  "s_add_u32 m0, s39, 36864\n\t" \
  "s_waitcnt vmcnt(21)\n\t" \
  "s_barrier\n\t" \
  "buffer_load_dwordx4 v5, s[44:47], s68 offen offset:0 lds\n\t" \
  "s_mov_b32 s85, 0x3c0000\n\t" \
  "s_load_dword s84, %[ph], s85\n\t" \
  "buffer_load_dwordx4 v6, s[44:47], s68 offen offset:1024 lds\n\t" \
  "s_mov_b32 s85, 0x3c0800\n\t" \
  "s_load_dword s84, %[ph], s85\n\t" \
  "buffer_load_dwordx4 v7, s[44:47], s68 offen offset:2048 lds\n\t" \
  "s_mov_b32 s85, 0x331000\n\t" \
  "s_load_dword s84, %[pw], s85\n\t" \
  "s_mov_b32 s85, 0x360000\n\t" \
  "s_load_dword s84, %[pw], s85\n\t" \
  "s_mov_b32 s85, 0x360800\n\t" \
  "s_load_dword s84, %[pw], s85\n\t" \
  "s_sleep 2\n\t" \
  "s_mov_b32 s68, 0x300\n\t" \
  "s_mov_b32 s69, 0x240000\n\t" \
  "s_add_u32 m0, s38, 36864\n\t" \
  "s_waitcnt vmcnt(15)\n\t" \
  "s_barrier\n\t" \
  "buffer_load_dwordx4 v0, s[40:43], s68 offen offset:0 lds\n\t" \
  "s_mov_b32 s85, 0x3c1000\n\t" \
  "s_load_dword s84, %[ph], s85\n\t" \
  "buffer_load_dwordx4 v1, s[40:43], s68 offen offset:1024 lds\n\t" \
  "s_mov_b32 s85, 0x361000\n\t" \
  "s_load_dword s84, %[pw], s85\n\t" \
  "s_mov_b64 exec, 0xffff\n\t" \
  "buffer_load_dwordx4 v4, s[40:43], s68 offen offset:2048 lds\n\t" \
  "s_add_u32 m0, s39, 49152\n\t" \
  "s_mov_b64 exec, -1\n\t" \
  "buffer_load_dwordx4 v5, s[44:47], s69 offen offset:0 lds\n\t" \
  "s_mov_b32 s85, 0x390000\n\t" \
  "s_load_dword s84, %[pw], s85\n\t" \
  "buffer_load_dwordx4 v6, s[44:47], s69 offen offset:1024 lds\n\t" \
  "s_mov_b32 s85, 0x390800\n\t" \
  "s_load_dword s84, %[pw], s85\n\t" \
  "buffer_load_dwordx4 v7, s[44:47], s69 offen offset:2048 lds\n\t" \
  "s_sleep 2\n\t" \
  "s_mov_b32 s68, 0x24300\n\t" \
  "s_mov_b32 s69, 0x270000\n\t" \
  "s_add_u32 m0, s38, 46080\n\t" \
  "s_waitcnt vmcnt(18)\n\t" \
  "s_barrier\n\t" \
  "buffer_load_dwordx4 v2, s[40:43], s68 offen offset:0 lds\n\t" \
  "s_mov_b32 s85, 0x420000\n\t" \
  "s_load_dword s84, %[ph], s85\n\t" \
  "buffer_load_dwordx4 v3, s[40:43], s68 offen offset:1024 lds\n\t" \
  "s_mov_b32 s85, 0x420800\n\t" \
  "s_load_dword s84, %[ph], s85\n\t" \
  "s_mov_b64 exec, 0xffff\n\t" \
  "buffer_load_dwordx4 v4, s[40:43], s68 offen offset:2048 lds\n\t" \
  "s_add_u32 m0, s39, 61440\n\t" \
  "s_mov_b64 exec, -1\n\t" \
  "buffer_load_dwordx4 v5, s[44:47], s69 offen offset:0 lds\n\t" \
  "s_mov_b32 s85, 0x391000\n\t" \
  "s_load_dword s84, %[pw], s85\n\t" \
  "buffer_load_dwordx4 v6, s[44:47], s69 offen offset:1024 lds\n\t" \
  "s_mov_b32 s85, 0x3c0000\n\t" \
  "s_load_dword s84, %[pw], s85\n\t" \
  "buffer_load_dwordx4 v7, s[44:47], s69 offen offset:2048 lds\n\t" \
  "s_mov_b32 s85, 0x3c0800\n\t" \
  "s_load_dword s84, %[pw], s85\n\t" \
  "s_sleep 2\n\t" \
  "s_mov_b32 s68, 0x380\n\t" \
  "s_mov_b32 s69, 0x2a0000\n\t" \
  "s_add_u32 m0, s38, 0\n\t" \
  "s_waitcnt vmcnt(15)\n\t" \
  "s_barrier\n\t" \
  "buffer_load_dwordx4 v0, s[40:43], s68 offen offset:0 lds\n\t" \
  "s_mov_b32 s85, 0x421000\n\t" \
  "s_load_dword s84, %[ph], s85\n\t" \
  "buffer_load_dwordx4 v1, s[40:43], s68 offen offset:1024 lds\n\t" \
  "s_mov_b32 s85, 0x3c1000\n\t" \
  "s_load_dword s84, %[pw], s85\n\t" \
  "s_mov_b64 exec, 0xffff\n\t" \
  "buffer_load_dwordx4 v4, s[40:43], s68 offen offset:2048 lds\n\t" \
  "s_add_u32 m0, s39, 0\n\t" \
  "s_mov_b64 exec, -1\n\t" \
  "buffer_load_dwordx4 v5, s[44:47], s69 offen offset:0 lds\n\t" \
  "s_mov_b32 s85, 0x3f0000\n\t" \
  "s_load_dword s84, %[pw], s85\n\t" \
  "buffer_load_dwordx4 v6, s[44:47], s69 offen offset:1024 lds\n\t" \
  "s_mov_b32 s85, 0x3f0800\n\t" \
  "s_load_dword s84, %[pw], s85\n\t" \
  "buffer_load_dwordx4 v7, s[44:47], s69 offen offset:2048 lds\n\t" \
  "s_mov_b32 s68, 0x24380\n\t" \
  "s_mov_b32 s69, 0x2d0000\n\t" \
  "s_add_u32 m0, s38, 9216\n\t" \
  "s_waitcnt vmcnt(18)\n\t" \
  "s_barrier\n\t" \
  "buffer_load_dwordx4 v2, s[40:43], s68 offen offset:0 lds\n\t" \
  "s_mov_b32 s85, 0x480000\n\t" \
  "s_load_dword s84, %[ph], s85\n\t" \
  "buffer_load_dwordx4 v3, s[40:43], s68 offen offset:1024 lds\n\t" \
  "s_mov_b32 s85, 0x480800\n\t" \
  "s_load_dword s84, %[ph], s85\n\t" \
  "s_mov_b64 exec, 0xffff\n\t" \
  "buffer_load_dwordx4 v4, s[40:43], s68 offen offset:2048 lds\n\t" \
  "s_add_u32 m0, s39, 12288\n\t" \
  "s_mov_b64 exec, -1\n\t" \
  "buffer_load_dwordx4 v5, s[44:47], s69 offen offset:0 lds\n\t" \
  "s_mov_b32 s85, 0x3f1000\n\t" \
  "s_load_dword s84, %[pw], s85\n\t" \
  "buffer_load_dwordx4 v6, s[44:47], s69 offen offset:1024 lds\n\t" \
  "s_mov_b32 s85, 0x420000\n\t" \
  "s_load_dword s84, %[pw], s85\n\t" \
  "buffer_load_dwordx4 v7, s[44:47], s69 offen offset:2048 lds\n\t" \
  "s_mov_b32 s85, 0x420800\n\t" \
  "s_load_dword s84, %[pw], s85\n\t" \
  "s_mov_b32 s68, 0x400\n\t" \
  "s_mov_b32 s69, 0x300000\n\t" \
  "s_add_u32 m0, s38, 18432\n\t" \
  "s_waitcnt vmcnt(15)\n\t" \
  "s_barrier\n\t" \
  "buffer_load_dwordx4 v0, s[40:43], s68 offen offset:0 lds\n\t" \
  "s_mov_b32 s85, 0x481000\n\t" \
  "s_load_dword s84, %[ph], s85\n\t" \
  "buffer_load_dwordx4 v1, s[40:43], s68 offen offset:1024 lds\n\t" \
  "s_mov_b32 s85, 0x421000\n\t" \
  "s_load_dword s84, %[pw], s85\n\t" \
  "s_mov_b64 exec, 0xffff\n\t" \
  "buffer_load_dwordx4 v4, s[40:43], s68 offen offset:2048 lds\n\t" \
  "s_add_u32 m0, s39, 24576\n\t" \
  "s_mov_b64 exec, -1\n\t" \
  "buffer_load_dwordx4 v5, s[44:47], s69 offen offset:0 lds\n\t" \
  "s_mov_b32 s85, 0x450000\n\t" \
  "s_load_dword s84, %[pw], s85\n\t" \
  "buffer_load_dwordx4 v6, s[44:47], s69 offen offset:1024 lds\n\t" \
  "s_mov_b32 s85, 0x450800\n\t" \
  "s_load_dword s84, %[pw], s85\n\t" \
  "buffer_load_dwordx4 v7, s[44:47], s69 offen offset:2048 lds\n\t" \
  "s_mov_b32 s68, 0x24400\n\t" \
  "s_mov_b32 s69, 0x330000\n\t" \
  "s_add_u32 m0, s38, 27648\n\t" \
  "s_waitcnt vmcnt(18)\n\t" \
  "s_barrier\n\t" \
  "buffer_load_dwordx4 v2, s[40:43], s68 offen offset:0 lds\n\t" \
  "s_mov_b32 s85, 0x4e0000\n\t" \
  "s_load_dword s84, %[ph], s85\n\t" \
  "buffer_load_dwordx4 v3, s[40:43], s68 offen offset:1024 lds\n\t" \
  "s_mov_b32 s85, 0x4e0800\n\t" \
  "s_load_dword s84, %[ph], s85\n\t" \
  "s_mov_b64 exec, 0xffff\n\t" \
  "buffer_load_dwordx4 v4, s[40:43], s68 offen offset:2048 lds\n\t" \
  "s_add_u32 m0, s39, 36864\n\t" \
  "s_mov_b64 exec, -1\n\t" \
  "buffer_load_dwordx4 v5, s[44:47], s69 offen offset:0 lds\n\t" \
  "s_mov_b32 s85, 0x451000\n\t" \
  "s_load_dword s84, %[pw], s85\n\t" \
  "buffer_load_dwordx4 v6, s[44:47], s69 offen offset:1024 lds\n\t" \
  "s_mov_b32 s85, 0x480000\n\t" \
  "s_load_dword s84, %[pw], s85\n\t" \
  "buffer_load_dwordx4 v7, s[44:47], s69 offen offset:2048 lds\n\t" \
  "s_mov_b32 s85, 0x480800\n\t" \
  "s_load_dword s84, %[pw], s85\n\t" \
  "s_mov_b32 s68, 0x480\n\t" \
  "s_mov_b32 s69, 0x360000\n\t" \
  "s_add_u32 m0, s38, 36864\n\t" \
  "s_waitcnt vmcnt(15)\n\t" \
  "s_barrier\n\t" \
  "buffer_load_dwordx4 v0, s[40:43], s68 offen offset:0 lds\n\t" \
  "s_mov_b32 s85, 0x4e1000\n\t" \
  "s_load_dword s84, %[ph], s85\n\t" \
  "buffer_load_dwordx4 v1, s[40:43], s68 offen offset:1024 lds\n\t" \
  "s_mov_b32 s85, 0x481000\n\t" \
  "s_load_dword s84, %[pw], s85\n\t" \
  "s_mov_b64 exec, 0xffff\n\t" \
  "buffer_load_dwordx4 v4, s[40:43], s68 offen offset:2048 lds\n\t" \
  "s_add_u32 m0, s39, 49152\n\t" \
  "s_mov_b64 exec, -1\n\t" \
  "buffer_load_dwordx4 v5, s[44:47], s69 offen offset:0 lds\n\t" \
  "s_mov_b32 s85, 0x4b0000\n\t" \
  "s_load_dword s84, %[pw], s85\n\t" \
  "buffer_load_dwordx4 v6, s[44:47], s69 offen offset:1024 lds\n\t" \
  "s_mov_b32 s85, 0x4b0800\n\t" \
  "s_load_dword s84, %[pw], s85\n\t" \
  "buffer_load_dwordx4 v7, s[44:47], s69 offen offset:2048 lds\n\t" \
  "s_mov_b32 s68, 0x24480\n\t" \
  "s_mov_b32 s69, 0x390000\n\t" \
  "s_add_u32 m0, s38, 46080\n\t" \
  "s_waitcnt vmcnt(18)\n\t" \
  "s_barrier\n\t" \
  "buffer_load_dwordx4 v2, s[40:43], s68 offen offset:0 lds\n\t" \
  "s_mov_b32 s85, 0x540000\n\t" \
  "s_load_dword s84, %[ph], s85\n\t" \
  "buffer_load_dwordx4 v3, s[40:43], s68 offen offset:1024 lds\n\t" \
  "s_mov_b32 s85, 0x540800\n\t" \
  "s_load_dword s84, %[ph], s85\n\t" \
  "s_mov_b64 exec, 0xffff\n\t" \
  "buffer_load_dwordx4 v4, s[40:43], s68 offen offset:2048 lds\n\t" \
  "s_add_u32 m0, s39, 61440\n\t" \
  "s_mov_b64 exec, -1\n\t" \
  "buffer_load_dwordx4 v5, s[44:47], s69 offen offset:0 lds\n\t" \
  "s_mov_b32 s85, 0x4b1000\n\t" \
  "s_load_dword s84, %[pw], s85\n\t" \
  "buffer_load_dwordx4 v6, s[44:47], s69 offen offset:1024 lds\n\t" \
  "s_mov_b32 s85, 0x4e0000\n\t" \
  "s_load_dword s84, %[pw], s85\n\t" \
  "buffer_load_dwordx4 v7, s[44:47], s69 offen offset:2048 lds\n\t" \
  "s_mov_b32 s85, 0x4e0800\n\t" \
  "s_load_dword s84, %[pw], s85\n\t" \
  "s_mov_b32 s68, 0x500\n\t" \
  "s_mov_b32 s69, 0x3c0000\n\t" \
  "s_add_u32 m0, s38, 0\n\t" \
  "s_waitcnt vmcnt(15)\n\t" \
  "s_barrier\n\t" \
  "buffer_load_dwordx4 v0, s[40:43], s68 offen offset:0 lds\n\t" \
  "s_mov_b32 s85, 0x541000\n\t" \
  "s_load_dword s84, %[ph], s85\n\t" \
  "buffer_load_dwordx4 v1, s[40:43], s68 offen offset:1024 lds\n\t" \
  "s_mov_b32 s85, 0x4e1000\n\t" \
  "s_load_dword s84, %[pw], s85\n\t" \
  "s_mov_b64 exec, 0xffff\n\t" \
  "buffer_load_dwordx4 v4, s[40:43], s68 offen offset:2048 lds\n\t" \
  "s_add_u32 m0, s39, 0\n\t" \
  "s_mov_b64 exec, -1\n\t" \
  "buffer_load_dwordx4 v5, s[44:47], s69 offen offset:0 lds\n\t" \
  "s_mov_b32 s85, 0x510000\n\t" \
  "s_load_dword s84, %[pw], s85\n\t" \
  "buffer_load_dwordx4 v6, s[44:47], s69 offen offset:1024 lds\n\t" \
  "s_mov_b32 s85, 0x510800\n\t" \
  "s_load_dword s84, %[pw], s85\n\t" \
  "buffer_load_dwordx4 v7, s[44:47], s69 offen offset:2048 lds\n\t" \
  "s_mov_b32 s68, 0x24500\n\t" \
  "s_mov_b32 s69, 0x3f0000\n\t" \
  "s_add_u32 m0, s38, 9216\n\t" \
  "s_waitcnt vmcnt(18)\n\t" \
  "s_barrier\n\t" \
  "buffer_load_dwordx4 v2, s[40:43], s68 offen offset:0 lds\n\t" \
  "s_mov_b32 s85, 0x5a0000\n\t" \
  "s_load_dword s84, %[ph], s85\n\t" \
  "buffer_load_dwordx4 v3, s[40:43], s68 offen offset:1024 lds\n\t" \
  "s_mov_b32 s85, 0x5a0800\n\t" \
  "s_load_dword s84, %[ph], s85\n\t" \
  "s_mov_b64 exec, 0xffff\n\t" \
  "buffer_load_dwordx4 v4, s[40:43], s68 offen offset:2048 lds\n\t" \
  "s_add_u32 m0, s39, 12288\n\t" \
  "s_mov_b64 exec, -1\n\t" \
  "buffer_load_dwordx4 v5, s[44:47], s69 offen offset:0 lds\n\t" \
  "s_load_dword s84, %[pq], 0x0\n\t" \
  "buffer_load_dwordx4 v6, s[44:47], s69 offen offset:1024 lds\n\t" \
  "s_load_dword s84, %[pq], 0x40\n\t" \
  "buffer_load_dwordx4 v7, s[44:47], s69 offen offset:2048 lds\n\t" \
  "s_mov_b32 s85, 0x511000\n\t" \
  "s_load_dword s84, %[pw], s85\n\t" \
  "s_mov_b32 s85, 0x540000\n\t" \
  "s_load_dword s84, %[pw], s85\n\t" \
  "s_mov_b32 s85, 0x540800\n\t" \
  "s_load_dword s84, %[pw], s85\n\t" \
  "s_mov_b32 s68, 0x580\n\t" \
  "s_mov_b32 s69, 0x420000\n\t" \
  "s_add_u32 m0, s38, 18432\n\t" \
  "s_waitcnt vmcnt(15)\n\t" \
  "s_barrier\n\t" \
  "buffer_load_dwordx4 v0, s[40:43], s68 offen offset:0 lds\n\t" \
  "s_mov_b32 s85, 0x5a1000\n\t" \
  "s_load_dword s84, %[ph], s85\n\t" \
  "buffer_load_dwordx4 v1, s[40:43], s68 offen offset:1024 lds\n\t" \
  "s_load_dword s84, %[pq], 0x800\n\t" \
  "s_mov_b64 exec, 0xffff\n\t" \
  "buffer_load_dwordx4 v4, s[40:43], s68 offen offset:2048 lds\n\t" \
  "s_add_u32 m0, s39, 24576\n\t" \
  "s_mov_b64 exec, -1\n\t" \
  "buffer_load_dwordx4 v5, s[44:47], s69 offen offset:0 lds\n\t" \
  "s_load_dword s84, %[pq], 0x840\n\t" \
  "buffer_load_dwordx4 v6, s[44:47], s69 offen offset:1024 lds\n\t" \
  "s_mov_b32 s85, 0x541000\n\t" \
  "s_load_dword s84, %[pw], s85\n\t" \
  "buffer_load_dwordx4 v7, s[44:47], s69 offen offset:2048 lds\n\t" \
  "s_mov_b32 s85, 0x570000\n\t" \
  "s_load_dword s84, %[pw], s85\n\t" \
  "s_mov_b32 s85, 0x570800\n\t" \
  "s_load_dword s84, %[pw], s85\n\t" \
  "s_mov_b32 s68, 0x24580\n\t" \
  "s_mov_b32 s69, 0x450000\n\t" \
  "s_add_u32 m0, s38, 27648\n\t" \
  "s_waitcnt vmcnt(18)\n\t" \
  "s_barrier\n\t" \
  "buffer_load_dwordx4 v2, s[40:43], s68 offen offset:0 lds\n\t" \
  "s_load_dword s84, %[pq], 0x1000\n\t" \
  "buffer_load_dwordx4 v3, s[40:43], s68 offen offset:1024 lds\n\t" \
  "s_load_dword s84, %[pq], 0x1040\n\t" \
  "s_mov_b64 exec, 0xffff\n\t" \
  "buffer_load_dwordx4 v4, s[40:43], s68 offen offset:2048 lds\n\t" \
  "s_add_u32 m0, s39, 36864\n\t" \
  "s_mov_b64 exec, -1\n\t" \
  "buffer_load_dwordx4 v5, s[44:47], s69 offen offset:0 lds\n\t" \
  "s_mov_b32 s85, 0x571000\n\t" \
  "s_load_dword s84, %[pw], s85\n\t" \
  "buffer_load_dwordx4 v6, s[44:47], s69 offen offset:1024 lds\n\t" \
  "s_mov_b32 s85, 0x5a0000\n\t" \
  "s_load_dword s84, %[pw], s85\n\t" \
  "buffer_load_dwordx4 v7, s[44:47], s69 offen offset:2048 lds\n\t" \
  "s_mov_b32 s85, 0x5a0800\n\t" \
  "s_load_dword s84, %[pw], s85\n\t" \
  "s_mov_b32 s68, 0x600\n\t" \
  "s_mov_b32 s69, 0x480000\n\t" \
  "s_add_u32 m0, s38, 36864\n\t" \
  "s_waitcnt vmcnt(15)\n\t" \
  "s_barrier\n\t" \
  "buffer_load_dwordx4 v0, s[40:43], s68 offen offset:0 lds\n\t" \
  "s_load_dword s84, %[pq], 0x1800\n\t" \
  "buffer_load_dwordx4 v1, s[40:43], s68 offen offset:1024 lds\n\t" \
  "s_load_dword s84, %[pq], 0x1840\n\t" \
  "s_mov_b64 exec, 0xffff\n\t" \
  "buffer_load_dwordx4 v4, s[40:43], s68 offen offset:2048 lds\n\t" \
  "s_add_u32 m0, s39, 49152\n\t" \
  "s_mov_b64 exec, -1\n\t" \
  "buffer_load_dwordx4 v5, s[44:47], s69 offen offset:0 lds\n\t" \
  "s_mov_b32 s85, 0x5a1000\n\t" \
  "s_load_dword s84, %[pw], s85\n\t" \
  "buffer_load_dwordx4 v6, s[44:47], s69 offen offset:1024 lds\n\t" \
  "s_mov_b32 s85, 0x5d0000\n\t" \
  "s_load_dword s84, %[pw], s85\n\t" \
  "buffer_load_dwordx4 v7, s[44:47], s69 offen offset:2048 lds\n\t" \
  "s_mov_b32 s85, 0x5d0800\n\t" \
  "s_load_dword s84, %[pw], s85\n\t" \
  "s_mov_b32 s68, 0x24600\n\t" \
  "s_mov_b32 s69, 0x4b0000\n\t" \
  "s_add_u32 m0, s38, 46080\n\t" \
  "s_waitcnt vmcnt(18)\n\t" \
  "s_barrier\n\t" \
  "buffer_load_dwordx4 v2, s[40:43], s68 offen offset:0 lds\n\t" \
  "s_load_dword s84, %[pq], 0x2000\n\t" \
  "buffer_load_dwordx4 v3, s[40:43], s68 offen offset:1024 lds\n\t" \
  "s_load_dword s84, %[pq], 0x2040\n\t" \
  "s_mov_b64 exec, 0xffff\n\t" \
  "buffer_load_dwordx4 v4, s[40:43], s68 offen offset:2048 lds\n\t" \
  "s_add_u32 m0, s39, 61440\n\t" \
  "s_mov_b64 exec, -1\n\t" \
  "buffer_load_dwordx4 v5, s[44:47], s69 offen offset:0 lds\n\t" \
  "s_mov_b32 s85, 0x5d1000\n\t" \
  "s_load_dword s84, %[pw], s85\n\t" \
  "buffer_load_dwordx4 v6, s[44:47], s69 offen offset:1024 lds\n\t" \
  "s_load_dword s84, %[po], 0x0\n\t" \
  "buffer_load_dwordx4 v7, s[44:47], s69 offen offset:2048 lds\n\t" \
  "s_load_dword s84, %[po], 0x40\n\t" \
  "s_mov_b32 s68, 0x680\n\t" \
  "s_mov_b32 s69, 0x4e0000\n\t" \
  "s_add_u32 m0, s38, 0\n\t" \
  "s_waitcnt vmcnt(15)\n\t" \
  "s_barrier\n\t" \
  "buffer_load_dwordx4 v0, s[40:43], s68 offen offset:0 lds\n\t" \
  "s_load_dword s84, %[pq], 0x2800\n\t" \
  "buffer_load_dwordx4 v1, s[40:43], s68 offen offset:1024 lds\n\t" \
  "s_load_dword s84, %[pq], 0x2840\n\t" \
  "s_mov_b64 exec, 0xffff\n\t" \
  "buffer_load_dwordx4 v4, s[40:43], s68 offen offset:2048 lds\n\t" \
  "s_add_u32 m0, s39, 0\n\t" \
  "s_mov_b64 exec, -1\n\t" \
  "buffer_load_dwordx4 v5, s[44:47], s69 offen offset:0 lds\n\t" \
  "s_load_dword s84, %[po], 0x80\n\t" \
  "buffer_load_dwordx4 v6, s[44:47], s69 offen offset:1024 lds\n\t" \
  "s_load_dword s84, %[po], 0xc0\n\t" \
  "buffer_load_dwordx4 v7, s[44:47], s69 offen offset:2048 lds\n\t" \
  "s_load_dword s84, %[po], 0x100\n\t" \
  "s_mov_b32 s68, 0x24680\n\t" \
  "s_mov_b32 s69, 0x510000\n\t" \
  "s_add_u32 m0, s38, 9216\n\t" \
  "s_waitcnt vmcnt(18)\n\t" \
  "s_barrier\n\t" \
  "buffer_load_dwordx4 v2, s[40:43], s68 offen offset:0 lds\n\t" \
  "s_load_dword s84, %[pq], 0x3000\n\t" \
  "buffer_load_dwordx4 v3, s[40:43], s68 offen offset:1024 lds\n\t" \
  "s_load_dword s84, %[pq], 0x3040\n\t" \
  "s_mov_b64 exec, 0xffff\n\t" \
  "buffer_load_dwordx4 v4, s[40:43], s68 offen offset:2048 lds\n\t" \
  "s_add_u32 m0, s39, 12288\n\t" \
  "s_mov_b64 exec, -1\n\t" \
  "buffer_load_dwordx4 v5, s[44:47], s69 offen offset:0 lds\n\t" \
  "s_load_dword s84, %[po], 0x140\n\t" \
  "buffer_load_dwordx4 v6, s[44:47], s69 offen offset:1024 lds\n\t" \
  "s_load_dword s84, %[po], 0x180\n\t" \
  "buffer_load_dwordx4 v7, s[44:47], s69 offen offset:2048 lds\n\t" \
  "s_load_dword s84, %[po], 0x1c0\n\t" \
  "s_mov_b32 s68, 0x700\n\t" \
  "s_mov_b32 s69, 0x540000\n\t" \
  "s_add_u32 m0, s38, 18432\n\t" \
  "s_waitcnt vmcnt(15)\n\t" \
  "s_barrier\n\t" \
  "buffer_load_dwordx4 v0, s[40:43], s68 offen offset:0 lds\n\t" \
  "s_load_dword s84, %[pq], 0x3800\n\t" \
  "buffer_load_dwordx4 v1, s[40:43], s68 offen offset:1024 lds\n\t" \
  "s_load_dword s84, %[pq], 0x3840\n\t" \
  "s_mov_b64 exec, 0xffff\n\t" \
  "buffer_load_dwordx4 v4, s[40:43], s68 offen offset:2048 lds\n\t" \
  "s_add_u32 m0, s39, 24576\n\t" \
  "s_mov_b64 exec, -1\n\t" \
  "buffer_load_dwordx4 v5, s[44:47], s69 offen offset:0 lds\n\t" \
  "s_load_dword s84, %[po], 0x200\n\t" \
  "buffer_load_dwordx4 v6, s[44:47], s69 offen offset:1024 lds\n\t" \
  "s_load_dword s84, %[po], 0x240\n\t" \
  "buffer_load_dwordx4 v7, s[44:47], s69 offen offset:2048 lds\n\t" \
  "s_load_dword s84, %[po], 0x280\n\t" \
  "s_mov_b32 s68, 0x24700\n\t" \
  "s_mov_b32 s69, 0x570000\n\t" \
  "s_add_u32 m0, s38, 27648\n\t" \
  "s_waitcnt vmcnt(18)\n\t" \
  "s_barrier\n\t" \
  "buffer_load_dwordx4 v2, s[40:43], s68 offen offset:0 lds\n\t" \
  "s_load_dword s84, %[po], 0x2c0\n\t" \
  "buffer_load_dwordx4 v3, s[40:43], s68 offen offset:1024 lds\n\t" \
  "s_load_dword s84, %[po], 0x300\n\t" \
  "s_mov_b64 exec, 0xffff\n\t" \
  "buffer_load_dwordx4 v4, s[40:43], s68 offen offset:2048 lds\n\t" \
  "s_add_u32 m0, s39, 36864\n\t" \
  "s_mov_b64 exec, -1\n\t" \
  "buffer_load_dwordx4 v5, s[44:47], s69 offen offset:0 lds\n\t" \
  "s_load_dword s84, %[po], 0x340\n\t" \
  "buffer_load_dwordx4 v6, s[44:47], s69 offen offset:1024 lds\n\t" \
  "buffer_load_dwordx4 v7, s[44:47], s69 offen offset:2048 lds\n\t" \
  "s_mov_b32 s68, 0x780\n\t" \
  "s_mov_b32 s69, 0x5a0000\n\t" \
  "s_add_u32 m0, s38, 36864\n\t" \
  "s_waitcnt vmcnt(15)\n\t" \
  "s_barrier\n\t" \
  "buffer_load_dwordx4 v0, s[40:43], s68 offen offset:0 lds\n\t" \
  "s_load_dword s84, %[po], 0x380\n\t" \
  "buffer_load_dwordx4 v1, s[40:43], s68 offen offset:1024 lds\n\t" \
  "s_load_dword s84, %[po], 0x3c0\n\t" \
  "s_mov_b64 exec, 0xffff\n\t" \
  "buffer_load_dwordx4 v4, s[40:43], s68 offen offset:2048 lds\n\t" \
  "s_add_u32 m0, s39, 49152\n\t" \
  "s_mov_b64 exec, -1\n\t" \
  "buffer_load_dwordx4 v5, s[44:47], s69 offen offset:0 lds\n\t" \
  "s_load_dword s84, %[po], 0x400\n\t" \
  "buffer_load_dwordx4 v6, s[44:47], s69 offen offset:1024 lds\n\t" \
  "buffer_load_dwordx4 v7, s[44:47], s69 offen offset:2048 lds\n\t" \
  "s_mov_b32 s68, 0x24780\n\t" \
  "s_mov_b32 s69, 0x5d0000\n\t" \
  "s_add_u32 m0, s38, 46080\n\t" \
  "s_waitcnt vmcnt(18)\n\t" \
  "s_barrier\n\t" \
  "buffer_load_dwordx4 v2, s[40:43], s68 offen offset:0 lds\n\t" \
  "s_load_dword s84, %[po], 0x440\n\t" \
  "buffer_load_dwordx4 v3, s[40:43], s68 offen offset:1024 lds\n\t" \
  "s_load_dword s84, %[po], 0x480\n\t" \
  "s_mov_b64 exec, 0xffff\n\t" \
  "buffer_load_dwordx4 v4, s[40:43], s68 offen offset:2048 lds\n\t" \
  "s_add_u32 m0, s39, 61440\n\t" \
  "s_mov_b64 exec, -1\n\t" \
  "buffer_load_dwordx4 v5, s[44:47], s69 offen offset:0 lds\n\t" \
  "s_load_dword s84, %[po], 0x4c0\n\t" \
  "buffer_load_dwordx4 v6, s[44:47], s69 offen offset:1024 lds\n\t" \
  "buffer_load_dwordx4 v7, s[44:47], s69 offen offset:2048 lds\n\t" \
  "v_readlane_b32 s68, v9, 0\n\t" \
  "v_readlane_b32 s69, v9, 1\n\t" \
  "v_readlane_b32 s70, v9, 2\n\t" \
  "v_readlane_b32 s71, v9, 18\n\t" \
  "v_readlane_b32 s72, v9, 19\n\t" \
  "s_add_u32 m0, %[ob], 0\n\t" \
  "s_waitcnt vmcnt(15)\n\t" \
  "s_barrier\n\t" \
  "s_nop 2\n\t" \
  "buffer_load_dwordx4 v8, %[so], s68 offen lds\n\t" \
  "s_load_dword s84, %[po], 0x500\n\t" \
  "s_add_u32 m0, %[ob], 4096\n\t" \
  "s_nop 0\n\t" \
  "buffer_load_dwordx4 v8, %[so], s69 offen lds\n\t" \
  "s_load_dword s84, %[po], 0x540\n\t" \
  "s_add_u32 m0, %[ob], 8192\n\t" \
  "s_nop 0\n\t" \
  "buffer_load_dwordx4 v8, %[so], s70 offen lds\n\t" \
  "s_load_dword s84, %[po], 0x580\n\t" \
  "s_add_u32 m0, %[ob], 73728\n\t" \
  "s_nop 0\n\t" \
  "buffer_load_dwordx4 v8, %[so], s71 offen lds\n\t" \
  "s_add_u32 m0, %[ob], 77824\n\t" \
  "s_nop 0\n\t" \
  "buffer_load_dwordx4 v8, %[so], s72 offen lds\n\t" \
  "v_readlane_b32 s68, v9, 3\n\t" \
  "v_readlane_b32 s69, v9, 4\n\t" \
  "v_readlane_b32 s70, v9, 5\n\t" \
  "v_readlane_b32 s71, v9, 20\n\t" \
  "v_readlane_b32 s72, v9, 21\n\t" \
  "s_add_u32 m0, %[ob], 12288\n\t" \
  "s_waitcnt vmcnt(17)\n\t" \
  "s_barrier\n\t" \
  "s_nop 2\n\t" \
  "buffer_load_dwordx4 v8, %[so], s68 offen lds\n\t" \
  "s_load_dword s84, %[po], 0x5c0\n\t" \
  "s_add_u32 m0, %[ob], 16384\n\t" \
  "s_nop 0\n\t" \
  "buffer_load_dwordx4 v8, %[so], s69 offen lds\n\t" \
  "s_load_dword s84, %[po], 0x600\n\t" \
  "s_add_u32 m0, %[ob], 20480\n\t" \
  "s_nop 0\n\t" \
  "buffer_load_dwordx4 v8, %[so], s70 offen lds\n\t" \
  "s_load_dword s84, %[po], 0x640\n\t" \
  "s_add_u32 m0, %[ob], 81920\n\t" \
  "s_nop 0\n\t" \
  "buffer_load_dwordx4 v8, %[so], s71 offen lds\n\t" \
  "s_add_u32 m0, %[ob], 86016\n\t" \
  "s_nop 0\n\t" \
  "buffer_load_dwordx4 v8, %[so], s72 offen lds\n\t" \
  "v_readlane_b32 s68, v9, 6\n\t" \
  "v_readlane_b32 s69, v9, 7\n\t" \
  "v_readlane_b32 s70, v9, 8\n\t" \
  "v_readlane_b32 s71, v9, 22\n\t" \
  "v_readlane_b32 s72, v9, 23\n\t" \
  "v_readlane_b32 s73, v9, 24\n\t" \
  "s_add_u32 m0, %[ob], 24576\n\t" \
  "s_waitcnt vmcnt(13)\n\t" \
  "s_barrier\n\t" \
  "s_nop 2\n\t" \
  "buffer_load_dwordx4 v8, %[so], s68 offen lds\n\t" \
  "s_load_dword s84, %[po], 0x680\n\t" \
  "s_add_u32 m0, %[ob], 28672\n\t" \
  "s_nop 0\n\t" \
  "buffer_load_dwordx4 v8, %[so], s69 offen lds\n\t" \
  "s_load_dword s84, %[po], 0x6c0\n\t" \
  "s_add_u32 m0, %[ob], 32768\n\t" \
  "s_nop 0\n\t" \
  "buffer_load_dwordx4 v8, %[so], s70 offen lds\n\t" \
  "s_load_dword s84, %[po], 0x700\n\t" \
  "s_add_u32 m0, %[ob], 90112\n\t" \
  "s_nop 0\n\t" \
  "buffer_load_dwordx4 v8, %[so], s71 offen lds\n\t" \
  "s_add_u32 m0, %[ob], 94208\n\t" \
  "s_nop 0\n\t" \
  "buffer_load_dwordx4 v8, %[so], s72 offen lds\n\t" \
  "s_add_u32 m0, %[ob], 98304\n\t" \
  "s_nop 0\n\t" \
  "buffer_load_dwordx4 v8, %[so], s73 offen lds\n\t" \
  "v_readlane_b32 s68, v9, 9\n\t" \
  "v_readlane_b32 s69, v9, 10\n\t" \
  "v_readlane_b32 s70, v9, 11\n\t" \
  "v_readlane_b32 s71, v9, 25\n\t" \
  "v_readlane_b32 s72, v9, 26\n\t" \
  "s_add_u32 m0, %[ob], 36864\n\t" \
  "s_waitcnt vmcnt(16)\n\t" \
  "s_barrier\n\t" \
  "s_nop 2\n\t" \
  "buffer_load_dwordx4 v8, %[so], s68 offen lds\n\t" \
  "s_load_dword s84, %[po], 0x740\n\t" \
  "s_add_u32 m0, %[ob], 40960\n\t" \
  "s_nop 0\n\t" \
  "buffer_load_dwordx4 v8, %[so], s69 offen lds\n\t" \
  "s_load_dword s84, %[po], 0x780\n\t" \
  "s_add_u32 m0, %[ob], 45056\n\t" \
  "s_nop 0\n\t" \
  "buffer_load_dwordx4 v8, %[so], s70 offen lds\n\t" \
  "s_load_dword s84, %[po], 0x7c0\n\t" \
  "s_add_u32 m0, %[ob], 102400\n\t" \
  "s_nop 0\n\t" \
  "buffer_load_dwordx4 v8, %[so], s71 offen lds\n\t" \
  "s_add_u32 m0, %[ob], 106496\n\t" \
  "s_nop 0\n\t" \
  "buffer_load_dwordx4 v8, %[so], s72 offen lds\n\t" \
  "v_readlane_b32 s68, v9, 12\n\t" \
  "v_readlane_b32 s69, v9, 13\n\t" \
  "v_readlane_b32 s70, v9, 14\n\t" \
  "v_readlane_b32 s71, v9, 27\n\t" \
  "v_readlane_b32 s72, v9, 28\n\t" \
  "v_readlane_b32 s73, v9, 29\n\t" \
  "s_add_u32 m0, %[ob], 49152\n\t" \
  "s_nop 3\n\t" \
  "s_barrier\n\t" \
  "s_nop 2\n\t" \
  "buffer_load_dwordx4 v8, %[so], s68 offen lds\n\t" \
  "s_add_u32 m0, %[ob], 53248\n\t" \
  "s_nop 0\n\t" \
  "buffer_load_dwordx4 v8, %[so], s69 offen lds\n\t" \
  "s_add_u32 m0, %[ob], 57344\n\t" \
  "s_nop 0\n\t" \
  "buffer_load_dwordx4 v8, %[so], s70 offen lds\n\t" \
  "s_add_u32 m0, %[ob], 110592\n\t" \
  "s_nop 0\n\t" \
  "buffer_load_dwordx4 v8, %[so], s71 offen lds\n\t" \
  "s_add_u32 m0, %[ob], 114688\n\t" \
  "s_nop 0\n\t" \
  "buffer_load_dwordx4 v8, %[so], s72 offen lds\n\t" \
  "s_add_u32 m0, %[ob], 118784\n\t" \
  "s_nop 0\n\t" \
  "buffer_load_dwordx4 v8, %[so], s73 offen lds\n\t" \
  "v_readlane_b32 s68, v9, 15\n\t" \
  "v_readlane_b32 s69, v9, 16\n\t" \
  "v_readlane_b32 s70, v9, 17\n\t" \
  "v_readlane_b32 s71, v9, 30\n\t" \
  "v_readlane_b32 s72, v9, 31\n\t" \
  "s_add_u32 m0, %[ob], 61440\n\t" \
  "LS_%=:\n\t" \
  "ds_read_b32 v11, v10\n\t" \
  "s_addk_i32 %[cn], 1\n\t" \
  "s_cmp_gt_u32 %[cn], 0x400000\n\t" \
  "s_cbranch_scc1 LX_%=\n\t" \
  "s_waitcnt lgkmcnt(0)\n\t" \
  "v_cmp_eq_u32 vcc, 0, v11\n\t" \
  "s_cbranch_vccz LX_%=\n\t" \
  "s_sleep 1\n\t" \
  "s_branch LS_%=\n\t" \
  "LX_%=:\n\t" \
  "s_nop 2\n\t" \
  "buffer_load_dwordx4 v8, %[so], s68 offen lds\n\t" \
  "s_add_u32 m0, %[ob], 65536\n\t" \
  "s_nop 0\n\t" \
  "buffer_load_dwordx4 v8, %[so], s69 offen lds\n\t" \
  "s_add_u32 m0, %[ob], 69632\n\t" \
  "s_nop 0\n\t" \
  "buffer_load_dwordx4 v8, %[so], s70 offen lds\n\t" \
  "s_add_u32 m0, %[ob], 122880\n\t" \
  "s_nop 0\n\t" \
  "buffer_load_dwordx4 v8, %[so], s71 offen lds\n\t" \
  "s_add_u32 m0, %[ob], 126976\n\t" \
  "s_nop 0\n\t" \
  "buffer_load_dwordx4 v8, %[so], s72 offen lds\n\t" \
  "s_waitcnt vmcnt(0) lgkmcnt(0)\n\t"
#define LD_HT 8
#define LD_TP 2
#define LD_CLOB "v11", "s68", "s69", "s70", "s71", "s72", "s73", "s74", "s75", "s76", "s77", "s78", "s79", "s80", "s81", "s82", "s83", "s84", "s85"

#define ENDW
#define TS(i)
#define TSS(i)
#define TOUT(n)

#define SPIN(cond,slp) do{ int sp_=0; while(cond){ __builtin_amdgcn_s_sleep(slp); if(++sp_>(1<<22)) break; } }while(0)
__device__ __forceinline__ unsigned rd1(const char* p){ return __builtin_amdgcn_readfirstlane(*(volatile LDS_AS unsigned*)(LDS_AS char*)p); }
__device__ __forceinline__ unsigned lds_inc(const char* p, int l){ unsigned r=0; if(l==0) r=__hip_atomic_fetch_add((LDS_AS unsigned*)(LDS_AS char*)p, 1u, __ATOMIC_RELAXED, __HIP_MEMORY_SCOPE_WORKGROUP); return __builtin_amdgcn_readfirstlane(r); }
__device__ __forceinline__ void lds_add(unsigned adr){ asm volatile("s_mov_b64 exec, 1\n\tds_add_u32 %0, %1\n\ts_mov_b64 exec, -1" :: "v"(adr), "v"(1u) : "memory"); }
__device__ __forceinline__ void publish(unsigned cnt, const unsigned* base, unsigned voff, unsigned tag){
  unsigned t;
  asm volatile("s_waitcnt vmcnt(0)\n\ts_mov_b64 exec, 1\n\tds_add_rtn_u32 %0, %1, %2\n\ts_waitcnt lgkmcnt(0)\n\tv_cmp_eq_u32 vcc, 3, %0\n\ts_cbranch_vccz PB_%=\n\t"
    "s_mov_b64 exec, 0xffff\n\tglobal_store_dword %3, %4, %5 sc1\n\tPB_%=:\n\ts_mov_b64 exec, -1"
    : "=&v"(t) : "v"(cnt), "v"(1u), "v"(voff), "v"(tag), "s"(base) : "memory", "vcc");
}
__device__ __forceinline__ void bars(int n){
  asm volatile("BL_%=:\n\ts_barrier\n\ts_sub_u32 %0, %0, 1\n\ts_cmp_lg_u32 %0, 0\n\ts_cbranch_scc1 BL_%=" : "+s"(n) :: "memory", "scc");
}
#define barc(rem) barc_(rem)
#define CTRA
#define CTRP
__device__ __forceinline__ void barc_(unsigned& rem){
  asm volatile("s_cmp_eq_u32 %0, 0\n\ts_cbranch_scc1 BC_%=\n\ts_barrier\n\ts_sub_u32 %0, %0, 1\n\tBC_%=:" : "+s"(rem) :: "memory", "scc");
}
template<int SLP> __device__ __forceinline__ void wait_flag_b(const unsigned* p, unsigned voff, unsigned tag, unsigned& rem CTRA){
  int spins=0;
  while(true){
    unsigned v; asm volatile("global_load_dword %0, %1, %2 sc1" : "=v"(v) : "v"(voff), "s"(p) : "memory");
    barc(rem); if(PB2) barc(rem);
    asm volatile("s_waitcnt vmcnt(0)" : "+v"(v) :: "memory");
    if(__all(v==tag) || ++spins>(1<<21)) break;
    if(rem==0) __builtin_amdgcn_s_sleep(SLP);
  }
}
__device__ __forceinline__ void stout(void* sbase, unsigned voff, u32x4 v){
  st16cs(sbase,voff,v);
}

typedef unsigned u32x8 __attribute__((ext_vector_type(8)));
typedef unsigned u32x16 __attribute__((ext_vector_type(16)));
__device__ __forceinline__ char* ptr64(unsigned lo, unsigned hi){ return (char*)(((unsigned long long)hi<<32)|lo); }
__device__ __forceinline__ u32x4 srd(const char* p, unsigned bytes){ return u32x4{(unsigned)(size_t)p,(unsigned)((size_t)p>>32)&0xffffu,bytes,0x20000u}; }
constexpr size_t WS_O=0, WS_KV=(size_t)S*DM*2, WS_Y=(size_t)S*DM*4, WS_FL=(size_t)S*DM*12;

__global__ __launch_bounds__(768,3) void fused_kernel(const u16* x_, const u16* wqkv_, const u16* wo_, char* ws_, u16* out_,
    const u16* kin_, const u16* vin_, const u16* shk_, const u16* hk_, const u16* qin_, const u16* sD_)
{
  __shared__ __attribute__((aligned(128))) char smem[ALL_LDS];
  TS(0);
  int wv;
  asm volatile(
      "s_load_dwordx16 s[48:63], %[ka], 0x0\n\ts_load_dwordx4 s[64:67], %[ka], 0x40\n\ts_load_dwordx2 s[78:79], %[ka], 0x50\n\ts_getpc_b64 s[72:73]\n\tv_readfirstlane_b32 s75, v0\n\ts_lshr_b32 s74, %[b], 3\n\ts_mul_i32 s74, s74, 12\n\ts_lshr_b32 s75, s75, 6\n\t"
      "s_add_u32 s74, s74, s75\n\ts_min_u32 s74, s74, %[cl]\n\ts_lshl_b32 s74, s74, 7\n\ts_mov_b32 m0, %[sk]\n\tv_mov_b32 v1, s74\n\ts_mov_b64 exec, 1\n\tglobal_load_lds_dword v1, s[72:73]\n\ts_mov_b64 exec, -1\n\t"
      "s_mov_b32 s76, %[b]\n\ts_cmp_lt_u32 s75, 4\n\ts_cbranch_scc1 FK_G\n\ts_cmp_lt_u32 s75, 8\n\ts_cbranch_scc0 FK_C\n\t"
      "v_and_b32 v9, 7, v0\n\tv_and_b32 v10, 63, v0\n\ts_lshl_b32 s74, %[b], 19\n\ts_lshl_b32 s73, %[b], 11\n\ts_or_b32 s74, s74, s73\n\ts_and_b32 s74, s74, 0x3fc000\n\ts_lshl_b32 s73, s75, 9\n\t"
      "s_add_u32 s74, s74, s73\n\ts_add_u32 s74, s74, 0x7800\n\tv_lshl_add_u32 v9, v9, 11, s74\n\tv_and_b32 v1, 24, v0\n\ts_lshl2_add_u32 s74, %[b], s75\n\ts_mul_i32 s74, s74, %[nw]\n\tv_lshl_add_u32 v9, v1, 4, v9\n\t"
      "s_add_u32 s74, s74, %[w0]\n\tv_add_lshl_u32 v10, v10, s74, 7\n\t"
      "s_setprio %[lp]\n\ts_and_b32 s74, s75, 3\n\tv_lshrrev_b32 v1, 3, v0\n\tv_lshrrev_b32 v3, 4, v0\n\ts_and_b32 s72, %[b], 1\n\ts_and_b32 s73, %[b], 0xe0\n\ts_lshl_b32 s72, s72, 10\n\ts_lshl2_add_u32 s72, s73, s72\n\t"
      "v_and_b32 v4, 2, v1\n\tv_and_or_b32 v3, v3, 4, v4\n\tv_and_b32 v4, 31, v1\n\tv_xor_b32 v3, v3, v0\n\tv_mul_u32_u24 v4, 0x1800, v4\n\tv_and_b32 v3, 7, v3\n\tv_lshl_add_u32 v3, v3, 4, s72\n\t"
      "s_mul_i32 s39, s74, 0xc00\n\ts_mul_i32 s38, s74, 0x900\n\ts_mul_i32 s74, s74, 18\n\tv_add_u32 v5, v3, v4\n\tv_and_b32 v1, 7, v1\n\tv_add_u32 v6, 0x400, v5\n\tv_add_u32 v1, s74, v1\n\tv_add_u32 v7, 0x800, v5\n\t"
      "s_and_b32 s72, %[b], 6\n\ts_and_b32 s73, %[b], 24\n\ts_lshl4_add_u32 s72, s72, s73\n\tv_xor_b32 v3, v1, v0\n\ts_lshl_b32 s72, s72, 15\n\tv_and_b32 v3, 7, v3\n\tv_lshl_add_u32 v1, v1, 11, s72\n\t"
      "s_mov_b32 s47, 0x20000\n\ts_mov_b32 s46, 0x600000\n\tv_lshl_add_u32 v2, v3, 4, v1\n\ts_mov_b32 s43, s47\n\ts_mov_b32 s42, 0x408000\n\ts_movk_i32 s73, 0x3c00\n\t"
      "v_add_u32 v3, s73, v2\n\ts_add_u32 s38, s38, %[xf]\n\tv_max_u32 v0, 0x8000, v2\n\tv_add_u32 v4, s73, v3\n\tv_max_u32 v1, 0x8000, v3\n\t"
      "s_waitcnt lgkmcnt(0)\n\ts_mov_b64 s[44:45], s[50:51]\n\ts_sub_u32 s40, s48, 0x8000\n\ts_subb_u32 s41, s49, 0\n\t"
      LD_ASM1
      "s_branch FK_L"
      : "=s"(wv)
      : "{v0}"(threadIdx.x), [ka]"s"(__builtin_amdgcn_kernarg_segment_ptr()), [b]"s"(blockIdx.x), [cl]"n"(CODEL-1), [sk]"n"(SINK+256), [lp]"n"(LPR), [xf]"n"(X_OFF), [nw]"n"(3*TPW), [w0]"n"(TW0*3072-12*TPW), [wm]"n"((1<<(3*TPW))-1)
      : "memory", "scc", "s48", "s49", "s50", "s51", "s52", "s53", "s54", "s55", "s72", "s73", "s74", "s75", "s76", "s78", "s79", "v1", "v2", "v3", "v4", "v5", "v6", "v7", "v9", "v10",
        "s40", "s41", "s42", "s43", "s44", "s45", "s46", "s47", "s38", "s39", "s56", "s57", "s58", "s59", "s60", "s61", "s62", "s63", "s64", "s65", "s66", "s67", "s68");
  #define ENTER(lbl) int w8, bid; u32x8 a0, a1; u32x4 a2; u32x2 a3; asm volatile(lbl ":" : "={s75}"(w8), "={s76}"(bid), "={s[48:55]}"(a0), "={s[56:63]}"(a1), "={s[64:67]}"(a2), "={s[78:79]}"(a3) :: "memory");
  #define ARGW asm volatile("s_waitcnt lgkmcnt(0)" : "+{s[48:55]}"(a0), "+{s[56:63]}"(a1), "+{s[64:67]}"(a2), "+{s[78:79]}"(a3));
  #define ROLE const int w=w8&3, xcd=bid&7, idx=bid>>3, rb=(xcd>>1)*4+(idx&3), hp=(xcd&1)*8+(idx>>2), s0=rb*128; (void)s0; (void)hp;
  #define LANEVARS int l; asm volatile("v_mbcnt_lo_u32_b32 %0, -1, 0\n\tv_mbcnt_hi_u32_b32 %0, -1, %0" : "=v"(l)); const int g=l>>4, fr=l&15, tq=(l&15)>>2, tp=l&3; (void)g; (void)fr; (void)tq; (void)tp;
  #define ARGS0 const char* x=ptr64(a0[0],a0[1]); const char* wqkv=ptr64(a0[2],a0[3]); const char* wo=ptr64(a0[4],a0[5]); char* ws=ptr64(a0[6],a0[7]); \
    unsigned* flags=(unsigned*)(ws+WS_FL); const int hot0=(3-(int)(a0[2]>>7))&7; (void)x; (void)wqkv; (void)wo; (void)flags; (void)hot0;
  #define OCOL ((hp&8)|((hp+(int)(a0[2]>>7)-(int)(a0[4]>>7)+OSH)&7))
  #define ORA ((unsigned)(hp-hot0-1)&7u)
  #define ORE(c) (ORA==7u? (c)-1u : (c))
  #define ORO(c) (ORE(c)>=NF && ORE(c)<NF+7u)
  #define ORD(c) (((((ORE(c)<14u? ((ORA==7u? 0u:ORA)+(ORO(c)? ORE(c)-NF : (ORE(c)<NF? ORE(c) : ORE(c)-7u)))%7u : 7u)+hot0+1)&7u))+8u*(((hp>>3)^(ORO(c) || (c)==15u))&1u))
  const unsigned sb0=__builtin_amdgcn_readfirstlane(lds_addr(smem));
  const char* syn=smem+SYN;

  if(__builtin_expect(wv==1,1)){
    ENTER("FK_L")
    u32x4 sx, sw; unsigned xl, wl, xoA, xoB, xo, xo1, xo2, wof[3];
    asm volatile("; descriptors and LDS bases" : "={s[40:43]}"(sx), "={s[44:47]}"(sw), "={s38}"(xl), "={s39}"(wl));
    asm volatile("; lane offsets" : "={v0}"(xoA), "={v1}"(xoB), "={v2}"(xo), "={v3}"(xo1), "={v4}"(xo2), "={v5}"(wof[0]), "={v6}"(wof[1]), "={v7}"(wof[2]));
    ROLE
    LANEVARS
    unsigned ltr=0, ltr2=0, ltr3=0; (void)ltr; (void)ltr2; (void)ltr3;
    TSS(2); TSS(3);
    ARGS0
    const unsigned wd=OCOL*128;
    {
      const unsigned wof2=(unsigned)(w*16384+(l>>3)*2048+((((l&7)>>1)^((l>>4)&3))*32)+(l&1)*16);
      const unsigned to=ORD((unsigned)(l>>1)&15u);
      const unsigned tab=to*131072u+(l&1)*65536;
      const int hg=((idx>>2)-hot0)&7, hi=hot0*4+(idx&3);
      const unsigned long long tm= w? 0ull : (HSK&1) && hg==0? 0ull : (HSK&1) && hg==HGR? ~0ull : 0xffffffffull;
      const unsigned long long hm= (HSK&2) && hg==0? 0ull : (HSK&2) && hg==HGR? 0xfffull : 0x3full;
      unsigned cn=0;
      const unsigned hto=(unsigned)(w*393216+(l<6? l*2048+idx*12288 : (l-6)*2048+hi*12288));
      asm volatile(LD_ASM2
        : [cn]"+&s"(cn)
        : "{v0}"(xoA), "{v1}"(xoB), "{v2}"(xo), "{v3}"(xo1), "{v4}"(xo2), "{v5}"(wof[0]), "{v6}"(wof[1]), "{v7}"(wof[2]), "{v8}"(wof2), "{v9}"(tab),
          "{v10}"(sb0+SYN+32+(l&3)*4), "{v12}"((l&31)*2048+(l<32? idx:hi)*65536),
          "{v16}"(hto), [sh]"s"(srd(wqkv+((xcd&1)*8+hot0)*128,6291456u-1920u)), [hm]"s"(hm),
          "{s[40:43]}"(sx), "{s[44:47]}"(sw), [so]"s"(srd(wo+wd,2097152u-wd)), "{s38}"(xl), "{s39}"(wl), [ob]"s"(sb0+w*1024),
          [pw]"s"(wqkv+idx*6144+(xcd&1)*1024+(xcd>>1)*256+w*64), [po]"s"(wo+(bid*4+w)*2048),
          [ph]"s"(wqkv+((xcd&1)*8+hot0)*128+idx*6144+(w>>1)*196608+(w&1)*64), [pq]"s"(wo+(xcd&1)*1024+((3-(int)(a0[4]>>7))&7)*128+(idx*32+w*8)*2048),
          [tsk]"s"(sb0+SINK+256), [tm]"s"(tm), [tp]"s"(wo+(xcd&1)*1024+((3-(int)(a0[4]>>7))&7)*128)
        : "memory", "scc", "vcc", LD_CLOB);
    }
    const unsigned tag=rd1(syn+64)+1u;
    lds_add(sb0+SYN+12);
    TS(2);
    if(w>=4-NPOL){
      const unsigned* fp=flags+F_O+(rb*16+hp)*16; const unsigned fo=ORD((unsigned)l&15u)*4u;
      const unsigned madr=sb0+(l==0? SYN+20 : SINK+l*4);
      SPIN(rd1(syn+12)<4u || rd1(syn+16)<4u,1);
      if(w==3) PUBM(madr,1u);
      if(PD0) __builtin_amdgcn_s_sleep(PD0);
      if(w==2) __builtin_amdgcn_s_sleep(PD1); if(w==1) __builtin_amdgcn_s_sleep(2*PD1); if(w==0) __builtin_amdgcn_s_sleep(3*PD1);
      unsigned rdy=1; int spins=0;
      while(rdy!=0xFFFFu){
        unsigned v;
        asm volatile("s_mov_b64 exec, 0xffff\n\tglobal_atomic_or %0, %1, %3, %2 sc0 sc1\n\ts_mov_b64 exec, -1\n\ts_waitcnt vmcnt(0)" : "=&v"(v) : "v"(fo), "s"(fp), "v"(0u) : "memory");
        unsigned r=((unsigned)__ballot(v==tag)&0xFFFFu)|1u;
        if(++spins>(1<<21)) r=0xFFFFu;
        if(r!=rdy){ PUBM(madr,(unsigned)__builtin_ctz(~r)); rdy=r; if(w<3 && (r&0x3FFFu)==0x3FFFu) break; }
        else __builtin_amdgcn_s_sleep(OSLP);
      }
      TS(3);
      if(bid==0 && w==3){
        int sp=0;
        while(true){
          u32x4 v; asm volatile("global_load_dwordx4 %0, %1, %2 sc1\n\ts_waitcnt vmcnt(0)" : "=&v"(v) : "v"(l*16), "s"(flags+F_Y) : "memory");
          if(__all(v[0]==tag && v[1]==tag && v[2]==tag && v[3]==tag) || ++sp>(1<<20)) break;
          __builtin_amdgcn_s_sleep(12);
        }
        if(l==0) stflag(flags, tag);
      }
    } else { TS(3); }
    TOUT(4);
    __builtin_amdgcn_endpgm();
  }

  if(wv==0){
    ENTER("FK_G")
    ROLE
    __builtin_amdgcn_s_setprio(GPR);
    const int h=w&1, r=w>>1;
    f32x16 e0, e1;
    LANEVARS
    TSS(3);
    ARGW
    ARGS0
    if(PGT&1){ if(w==1 && l<7) touch(ws, l<6? l*2097152+PGO : (unsigned)WS_FL+2048, sb0+SINK+512); }
    if(PGT&2){ if(w==2 && l<4) touch(ptr64(a1[0],a1[1]), l*2097152+PGO, sb0+SINK+512); }
    if(PGT&4){ if(w==3 && l<1) touch(wo, PGO, sb0+SINK+512); if(w==0 && l<2) touch(wqkv, l*2097152+2097152, sb0+SINK+512); }
    TSS(4);
    {
      unsigned lx0=sb0+X_OFF+fr*128+((g^(fr&7))*16), lx1=sb0+X_OFF+fr*128+(((4+g)^(fr&7))*16);
      const int sw=wswz(8*g+tq);
      unsigned lw0=sb0+g*3072+tq*128+(unsigned)((((2*h)^sw)*32)+8*tp), lw1=sb0+g*3072+tq*128+(unsigned)((((2*h+1)^sw)*32)+8*tp);
      char* ot=ws+WS_O+(rb*16+hp)*16384+r*8192+h*2048;
      unsigned it; unsigned long long rm; asm("s_cmp_lg_u32 %1, 0\n\ts_cselect_b64 %0, -1, 0" : "=s"(rm) : "s"(r) : "scc");
      asm volatile(N2_ASM
        : [it]"=&s"(it), "+{v56}"(lx0), "+{v57}"(lx1), "+{v58}"(lw0), "+{v59}"(lw1)
        : "{v62}"(l*16), "{v63}"(sb0+EX_OFF+h*3072+l*16), [r]"s"(r), [ot]"s"(ot), [ou]"s"(ot+4096), [fa]"s"(sb0+SYN+32+w*4), [fp]"s"(sb0+SYN+32+(w^2)*4),
          [vo]"s"(2048-1024*r), [rm]"s"(rm), [z]"s"(rb|r)
        : "memory", "scc", "vcc", N2_CLOB);
    }
    TS(3);
    unsigned tb;
    {
      LANEVARS
      const unsigned tag=rd1(syn+64)+1u;
      const unsigned ra=sb0+(4*g+tq)*128+8*tp; const int sw=(2*g+(tq>>1))&3;
      unsigned b0=ra+((0^sw)*32), b1=ra+((1^sw)*32), b2=ra+((2^sw)*32), b3=ra+((3^sw)*32), vo=(unsigned)(w*4096+l*16);
      tb=ORD((unsigned)l&15u)*16384u;
      asm volatile("" : "+{v160}"(b0), "+{v161}"(b1), "+{v162}"(b2), "+{v163}"(b3), "+{v164}"(vo), "+{v167}"(tb));
      publish(sb0+SYN+16, flags+F_O+rb*256+hp, l*64, tag);
      TS(4);
      __builtin_amdgcn_s_setprio(G2P);
      unsigned c=0, iss=0, th, q0, q1, cn=0;
      asm volatile(G3_ASM
        : "={v[128:143]}"(e0), "={v[144:159]}"(e1), [c]"+s"(c), [iss]"+s"(iss), [th]"=&s"(th), [t0]"=&s"(q0), [t1]"=&s"(q1), [cn]"+&s"(cn),
          "+{v160}"(b0), "+{v161}"(b1), "+{v162}"(b2), "+{v163}"(b3), "+{v167}"(tb)
        : "{v164}"(vo), "{v165}"(sb0+SYN+20), [go]"s"(60+w*4), [ob]"s"(srd(ws+WS_O+rb*262144,262144u))
        : "memory", "scc", "vcc", G3_CLOB);
    }
    {
      LANEVARS
      char* sg=smem+EX_OFF+w*2048;
      char* op=ptr64(a1[0],a1[1])+(s0+w*32)*4096+2048+OCOL*128; const unsigned oo=(unsigned)((l>>3)*4096+(l&7)*16);
      u32x4 ov[4];
      #pragma unroll
      for(int t=0;t<2;t++){
        #pragma unroll
        for(int j=0;j<4;j++){
          #define E2(i) (t? e1[(i)&15] : e0[(i)&15])
          *(volatile LDS_AS u32x2*)(LDS_AS char*)(sg+fr*128+(((2*j+(g>>1))^(fr&7))*16)+(g&1)*8)=u32x2{pack2(E2(4*j),E2(4*j+1)),pack2(E2(4*j+2),E2(4*j+3))};
        }
        #pragma unroll
        for(int i=0;i<2;i++) ov[2*t+i]=*(volatile LDS_AS u32x4*)(LDS_AS char*)(sg+(i*8+(l>>3))*128+(((l&7)^(l>>3))*16));
      }
      #pragma unroll
      for(int i=0;i<4;i++) stout(op+i*32768, oo, ov[i]);
      ENDW TS(6); TOUT(7);
    }
    __builtin_amdgcn_endpgm();
  }

  {
    ENTER("FK_C")
    ROLE
    char* cr=smem+C_OFF+2048+w*4096;
    const unsigned crs=sb0+C_OFF+2048+w*4096;
    const int sblk=bid>>3, td0=(bid&7)*128+(w&1)*64;
    const int vb=(LOCA&1)? xcd*32+(idx&16)+(idx&1)*8+((idx>>1)&7) : bid, fb=(LOCA&2)? (idx&15)*16+xcd*2+(idx>>4) : bid;
    const int d=(vb>>4)*64+(((vb>>3)&1)*4+w)*8+(vb&7);
    int kv0=KV0+(sblk&KVS);
    static_assert(KV0+KVS+PP_NB<=33 && KV0>=EB, "barrier budget");
    unsigned tagv, ctr; f32x8 RB;
    const int fd0=(fb&15)*64, fs0=(fb>>4)*128+w*32;
    u32x4 qv[4];
    f32x16 A0,A1,A2,A3;
    const int rw=sblk*64+(w>>1)*32, j=rw>>5;
    unsigned k0,k1,k2,ko,ta,se,so,ha,l16,rb0[2];
    {
      LANEVARS
      if(w==0 && l<25 && l!=16) ((unsigned*)(smem+SYN))[l]=0u;
      if(l<32) *(u32x4*)(smem+C_OFF+w*512+l*16)=u32x4{0,0,0,0};
      asm volatile("" ::: "memory");
      __builtin_amdgcn_s_setprio(CPR);
      const int dg=l&7, rg=l>>3;
      ko=(unsigned)(rg*8192+dg*16);
      const bool top=(rw==0 && rg==0);
      k0=top? ko+6144:ko; k1=top? ko+6144:ko+2048; k2=top? ko+6144:ko+4096;
      ta=sb0+EX_OFF+w*1024+(w&1)*512+dg*64; se=(l&8)? 0x03020706u : 0x05040100u; ha=crs+(32*g-4*tq+4*tp)*2; l16=l*16;
      so=(unsigned)(rg*32768+((j>>2)&3)*128+((dg&4)|((dg+j)&3))*16);
      #pragma unroll
      for(int k=0;k<2;k++){ const int q=fr-1-2*g+k; rb0[k]=sb0+C_OFF+(4+(q>>1))*256+((q>>3)&3)*64+((w+(q>>1))&3)*16+(q&1)*8; }
      asm volatile("" : "+{v152}"(k0), "+{v153}"(k1), "+{v154}"(k2), "+{v155}"(ko), "+{v156}"(ta), "+{v157}"(se), "+{v159}"(so), "+{v163}"(l16), "+{v165}"(ha), "+{v166}"(rb0[0]), "+{v167}"(rb0[1]));
    }
    TSS(3);
    ARGW
    if(EB==1) asm volatile("s_barrier" : "+s"(a1), "+s"(a2), "+s"(a3), "+s"(a0) :: "memory");
    if(EB==2) asm volatile("s_barrier\n\ts_barrier" : "+s"(a1), "+s"(a2), "+s"(a3), "+s"(a0) :: "memory");
    ARGS0
    char* out=ptr64(a1[0],a1[1]); const char* qp=ptr64(a2[2],a2[3])+(fs0*DM+fd0)*2;
    {
      const char* kb=ptr64(a1[2],a1[3])+(rw*DM+td0)*2; const char* vb=ptr64(a1[4],a1[5])+(rw*DM+td0)*2;
      unsigned long long zm; asm("s_cmp_eq_u32 %1, 0\n\ts_cselect_b64 %0, 0xff, 0" : "=s"(zm) : "s"(rw) : "scc");
      unsigned t0;
      if(PGT&8){ const char* tp= w==0? kb : w==1? vb : w==2? qp : ptr64(a2[0],a2[1])+d*4096;
        asm volatile("s_mov_b32 m0, %1\n\ts_mov_b64 exec, 1\n\tglobal_load_lds_dword v163, %0\n\ts_mov_b64 exec, -1" :: "s"(tp), "s"(sb0+SINK+512), "{v163}"(l16) : "memory"); }
      TSS(5);
      asm volatile(PP_ASM
        : [t0]"=&s"(t0), "=&{v164}"(ctr), "+{v159}"(so), "=&{v160}"(tagv), "=&{v[24:31]}"(RB), "=&{v[44:47]}"(qv[0]), "=&{v[48:51]}"(qv[1]), "=&{v[52:55]}"(qv[2]), "=&{v[56:59]}"(qv[3]),
          "=&{v[64:79]}"(A0), "=&{v[80:95]}"(A1), "=&{v[96:111]}"(A2), "=&{v[112:127]}"(A3)
        : "{v152}"(k0), "{v153}"(k1), "{v154}"(k2), "{v155}"(ko), "{v156}"(ta), "{v157}"(se), "{v166}"(rb0[0]), "{v167}"(rb0[1]), "{v165}"(ha), "{v163}"(l16),
          [qb0]"s"(qp), [qb2]"s"(qp+4096), [mk]"s"(0x180u), [em]"s"(1ull|(512ull<<w)), [hd]"s"(crs),
          [kb0]"s"(kb-6144), [kb3]"s"(kb), [kb5]"s"(kb+4096), [vb0]"s"(vb), [vb2]"s"(vb+4096), [sb]"s"(ws+WS_KV+td0*4096+j*512),
          [n0]"s"(kv0-EB-(HH==2)), [ff]"s"(flags+F_PP+(td0>>6)*64+j), [zm]"s"(zm), [shd]"s"(sb0+EX_OFF+w*1024), [tgd]"s"(sb0+SYN+64), [shp]"s"(ptr64(a1[6],a1[7])+(bid&7)*1024), [fl]"s"(flags),
          [hp]"s"(ptr64(a2[0],a2[1])+d*4096), [dp]"s"(ptr64(a3[0],a3[1])+d*2)
        : "memory", "scc", PP_CLOB);
    }
    LANEVARS
    const unsigned tag=__builtin_amdgcn_readfirstlane(tagv)+1u;
    unsigned rem=33-PP_NB-kv0;
    barc(rem);
    const unsigned* pf=flags+F_PP+(d>>6)*64;
    const char* kvp=ws+WS_KV+((vb>>4)*8+(vb&7))*32768+((vb>>3)&1)*64+w*8192;
    const unsigned kvo=(unsigned)((l>>2)*128+(l&3)*16);
    int bidx=((l>=16)? l-16 : (l==0? 63 : l+47))*4;
    asm volatile("" : "+{v32}"(bidx));
    __builtin_amdgcn_s_setprio(3);
    while(rem>PREM) barc(rem);
    wait_flag_b<8>(pf, l*4, tag, rem CTRP);
    #pragma unroll
    for(int i=0;i<4;i++){ if(i%KTH==0 && i) barc(rem); gldsc(kvp+i*2048, kvo, crs+i*1024); }
    barc(rem); barc(rem);
    WAITV(0);
    asm volatile("s_mov_b32 m0, %2\n\ts_mov_b64 exec, 1\n\tglobal_load_lds_dword %0, %1 sc1\n\ts_mov_b64 exec, -1" :: "v"(0u), "s"(pf), "s"(sb0+SYN+100+w*4) : "memory");
    while(rem) barc(rem);
    { int sp=0; while(!__all(*(volatile LDS_AS unsigned*)(LDS_AS char*)(syn+100+(l&3)*4)==tag) && ++sp<(1<<22)) __builtin_amdgcn_s_sleep(1); }
    __builtin_amdgcn_s_setprio(CVP);
    TS(3);
    f32x16 ac0, ac1;
    {
      asm volatile("s_nop 1\n\t" CV_ASM
        : "=&{v[128:143]}"(ac0), "=&{v[144:159]}"(ac1)
          , "+{v[44:47]}"(qv[0]), "+{v[48:51]}"(qv[1]), "+{v[52:55]}"(qv[2]), "+{v[56:59]}"(qv[3])
        : "{v[64:79]}"(A0), "{v[80:95]}"(A1), "{v[96:111]}"(A2), "{v[112:127]}"(A3),
          "{v[24:31]}"(RB), "{v32}"(bidx), "{v33}"(sb0+SYN+80+w*4)
          , "{v42}"((256*(g&1)+16*fr+4*(g&2))*2), [yb]"s"(ws+WS_Y+d*4096)
          , "{v43}"((l>>3)*8192+(l&7)*16), [q0]"s"(qp), [q2]"s"(qp+4096)
        : "memory", "scc", CV_CLOB);
    }
    TS(4);
    __builtin_amdgcn_s_setprio(3);
    publish(sb0+SYN+24, flags+F_Y+vb, l*1024, tag);
    asm volatile("" : "+v"(qv[0]), "+v"(qv[1]), "+v"(qv[2]), "+v"(qv[3]));
    TS(5);
    float qf[32];
    #pragma unroll
    for(int i=0;i<16;i++){ qf[2*i]=bflo(qv[i>>2][i&3]); qf[2*i+1]=bfhi(qv[i>>2][i&3]); }
    unsigned ya=crs+(g*8+tq)*64+tp*8, wa0=crs+fr*128+((g^(fr&7))*16), wa1=crs+fr*128+(((4+g)^(fr&7))*16), ra[4], oo=(unsigned)((l>>3)*16384+(l&7)*16), yo2=(l>>2)*4096+(l&3)*16;
    #pragma unroll
    for(int i=0;i<4;i++){ const int row=4*(l>>3)+i; ra[i]=crs+row*128+(((l&7)^(row&7))*16); }
    char* op=out+fs0*4096+fd0*2; char* op2=op+8192; const char* yg=ws+WS_Y+fd0*4096+fs0*2;
    #pragma unroll
    for(int i=0;i<32;i++) asm volatile("" : "+v"(qf[i]));
    asm volatile("" : "+v"(ya), "+v"(wa0), "+v"(wa1), "+v"(ra[0]), "+v"(ra[1]), "+v"(ra[2]), "+v"(ra[3]), "+v"(oo), "+v"(yo2));
    asm volatile("" : "+s"(op), "+s"(op2), "+s"(yg));
    if(w==0){
      SPIN(rd1(syn+24)<4u,1);
      SPIN(rd1(syn+96)==0u,2);
      { int sp=0;
        while(true){
          unsigned v=tag; asm volatile("s_mov_b64 exec, 0xffff\n\tglobal_atomic_or %0, %1, %3, %2 sc0 sc1\n\ts_mov_b64 exec, -1\n\ts_waitcnt vmcnt(0)" : "+&v"(v) : "v"(l*4), "s"(flags+F_Y+fb*16), "v"(0u) : "memory");
          if(__all(v==tag) || ++sp>(1<<21)) break;
          __builtin_amdgcn_s_sleep(YSLP);
        } }
      PUB(sb0+(l==0? SYN+28 : SINK+l*4), 1u);
    } else SPIN(rd1(syn+28)==0u,YS2);
    #pragma unroll
    for(int i=0;i<4;i++) gldsc(yg+i*65536, yo2, crs+i*1024);
    {
      u32x4 y4[4];
      asm volatile("s_waitcnt vmcnt(0)\n\t"
        "ds_read_b64_tr_b16 v[0:1], %4\n\tds_read_b64_tr_b16 v[2:3], %4 offset:256\n\tds_read_b64_tr_b16 v[4:5], %4 offset:32\n\tds_read_b64_tr_b16 v[6:7], %4 offset:288\n\t"
        "ds_read_b64_tr_b16 v[8:9], %4 offset:2048\n\tds_read_b64_tr_b16 v[10:11], %4 offset:2304\n\tds_read_b64_tr_b16 v[12:13], %4 offset:2080\n\tds_read_b64_tr_b16 v[14:15], %4 offset:2336\n\t"
        "s_waitcnt lgkmcnt(6)\n\tds_write_b128 %5, v[0:3]\n\ts_waitcnt lgkmcnt(5)\n\tds_write_b128 %5, v[4:7] offset:2048\n\t"
        "s_waitcnt lgkmcnt(4)\n\tds_write_b128 %6, v[8:11]\n\ts_waitcnt lgkmcnt(3)\n\tds_write_b128 %6, v[12:15] offset:2048\n\t"
        "ds_read_b128 %0, %7\n\tds_read_b128 %1, %8\n\tds_read_b128 %2, %9\n\tds_read_b128 %3, %10\n\ts_waitcnt lgkmcnt(0)"
        : "=&v"(y4[0]), "=&v"(y4[1]), "=&v"(y4[2]), "=&v"(y4[3])
        : "v"(ya), "v"(wa0), "v"(wa1), "v"(ra[0]), "v"(ra[1]), "v"(ra[2]), "v"(ra[3])
        : "memory", "v0", "v1", "v2", "v3", "v4", "v5", "v6", "v7", "v8", "v9", "v10", "v11", "v12", "v13", "v14", "v15");
      #pragma unroll
      for(int i=0;i<4;i++){
        u32x4 ov;
        #pragma unroll
        for(int e=0;e<4;e++) ov[e]=pack2(mul1(qf[8*i+2*e],bflo(y4[i][e])), mul1(qf[8*i+2*e+1],bfhi(y4[i][e])));
        stout((i&2? op2:op)+(i&1)*4096, oo, ov);
      }
    }
    ENDW TS(6); TOUT(7);
    __builtin_amdgcn_endpgm();
  }
}

extern "C" void kernel_launch(void* const* d_in, const int* in_sizes, int n_in, void* d_out, int out_size, void* d_ws, size_t ws_size, hipStream_t stream){
  const u16* q=(const u16*)d_in[0]; const u16* k=(const u16*)d_in[1]; const u16* v=(const u16*)d_in[2];
  const u16* shk=(const u16*)d_in[3]; const u16* hk=(const u16*)d_in[4]; const u16* sD=(const u16*)d_in[5];
  const u16* x=(const u16*)d_in[6]; const u16* wqkv=(const u16*)d_in[7]; const u16* wo=(const u16*)d_in[8];
  fused_kernel<<<256,768,0,stream>>>(x,wqkv,wo,(char*)d_ws,(u16*)d_out,k,v,shk,hk,q,sD);
}
```

```cpp
#include <hip/hip_runtime.h>
#include <hip/hip_bf16.h>
#include <stdint.h>

typedef unsigned short u16;
typedef short bf16x8 __attribute__((ext_vector_type(8)));
typedef short s16x4 __attribute__((ext_vector_type(4)));
typedef float f32x2 __attribute__((ext_vector_type(2)));
typedef float f32x4 __attribute__((ext_vector_type(4)));
typedef float f32x8 __attribute__((ext_vector_type(8)));
typedef float f32x16 __attribute__((ext_vector_type(16)));
typedef unsigned u32x4 __attribute__((ext_vector_type(4)));
typedef unsigned u32x2 __attribute__((ext_vector_type(2)));
typedef __bf16 bf16x2_t __attribute__((ext_vector_type(2)));
#define LDS_AS __attribute__((address_space(3)))

__device__ __forceinline__ float bflo(unsigned x){ return __uint_as_float(x<<16); }
__device__ __forceinline__ float bfhi(unsigned x){ return __uint_as_float(x&0xffff0000u); }
__device__ __forceinline__ unsigned pack2(float lo, float hi){ return __builtin_bit_cast(unsigned, __builtin_convertvector(f32x2{lo,hi}, bf16x2_t)); }
__device__ __forceinline__ float mul1(float a, float b){ float r; asm("v_mul_f32 %0, %1, %2" : "=v"(r) : "v"(a), "v"(b)); return r; }
__device__ __forceinline__ float fma1(float a, float b, float c){ float r; asm("v_fma_f32 %0, %1, %2, %3" : "=v"(r) : "v"(a), "v"(b), "v"(c)); return r; }

__device__ __forceinline__ void glds(const void* sbase, unsigned voff, unsigned ldsaddr){
  asm volatile("s_mov_b32 m0, %2\n\ts_nop 0\n\tglobal_load_lds_dwordx4 %0, %1" :: "v"(voff), "s"(sbase), "s"(ldsaddr) : "memory");
}
__device__ __forceinline__ void gldsc(const void* sbase, unsigned voff, unsigned ldsaddr){
  asm volatile("s_mov_b32 m0, %2\n\ts_nop 0\n\tglobal_load_lds_dwordx4 %0, %1 sc1" :: "v"(voff), "s"(sbase), "s"(ldsaddr) : "memory");
}
__device__ __forceinline__ void touch(const void* sbase, unsigned voff, unsigned ldsaddr){
  asm volatile("s_mov_b32 m0, %2\n\ts_nop 0\n\tglobal_load_lds_dword %0, %1" :: "v"(voff), "s"(sbase), "s"(ldsaddr) : "memory");
}
__device__ __forceinline__ void st16c(void* p, u32x4 v){ asm volatile("global_store_dwordx4 %0, %1, off sc1\n\ts_nop 1" :: "v"(p), "v"(v) : "memory"); }
__device__ __forceinline__ void st16cs(void* sbase, unsigned voff, u32x4 v){ asm volatile("global_store_dwordx4 %0, %1, %2 sc1\n\ts_nop 1" :: "v"(voff), "v"(v), "s"(sbase) : "memory"); }
#define GLB_AS __attribute__((address_space(1)))
__device__ __forceinline__ unsigned ldflag(const unsigned* p){ return __hip_atomic_load((const GLB_AS unsigned*)p, __ATOMIC_RELAXED, __HIP_MEMORY_SCOPE_AGENT); }
__device__ __forceinline__ void stflag(unsigned* p, unsigned v){ __hip_atomic_store((GLB_AS unsigned*)p, v, __ATOMIC_RELAXED, __HIP_MEMORY_SCOPE_AGENT); }
template<int SLP> __device__ __forceinline__ void wait_flag(const unsigned* p, unsigned tag){
  int spins=0;
  while(true){ unsigned v=ldflag(p); if(__all(v==tag)) break; __builtin_amdgcn_s_sleep(SLP); if(++spins>(1<<21)) break; }
}
__device__ __forceinline__ unsigned lds_addr(const void* p){ return (unsigned)(size_t)(const LDS_AS void*)p; }
#define WAITV(n) asm volatile("s_waitcnt vmcnt(" #n ")":::"memory")
#define WAITL() asm volatile("s_waitcnt lgkmcnt(0)":::"memory")
#define SBAR() asm volatile("s_barrier":::"memory")
#define PUBM(addr,val) asm volatile("ds_max_u32 %0, %1" :: "v"(addr), "v"(val) : "memory")
#define PUB(addr,val) asm volatile("ds_write_b32 %0, %1" :: "v"(addr), "v"(val) : "memory")
__device__ __forceinline__ s16x4 trread(const char* p){ return __builtin_amdgcn_ds_read_tr16_b64_v4i16((LDS_AS s16x4*)p); }
__device__ __forceinline__ bf16x8 cat44(s16x4 a, s16x4 b){ return __builtin_shufflevector(a,b,0,1,2,3,4,5,6,7); }
__device__ __forceinline__ int wswz(int row){ return ((row>>1)&1) | (((row>>3)&1)<<1); }
__device__ __forceinline__ u32x4 widen(u32x2 a, u32x2 b){
  auto r0=__builtin_amdgcn_permlane16_swap(a[0],b[0],false,false);
  auto r1=__builtin_amdgcn_permlane16_swap(a[1],b[1],false,false);
  return u32x4{r0[0],r1[0],r0[1],r1[1]};
}

constexpr int S=2048, DM=1024;
constexpr int WS=12288, XS=18432, X_OFF=6*WS;
constexpr int CREG=4608, C_OFF=131072;
constexpr int EX_OFF=C_OFF+4*CREG;
constexpr int SYN=EX_OFF+8192;
constexpr int SINK=SYN+256;
constexpr int ALL_LDS=SINK+768;
constexpr int F_PP=1024, F_O=4096, F_Y=8192;
#define KV0 1
#define KVS 1
#define LPR 2
#define CPR 2
#define GPR 1
#define CGATE 0
#define OSLP 2
#define YSLP 8
#define TPX 16
#define TPW 2
#define TWO 0
#define CVP 1
#define G2P 3
#define OUTC 1
#define ROT 0
#define WTL 2
#define PB2 0
#define PREM 5
#define KTH 2
#define YDEF 3
#define OPOL 1
#define CODEL 272
#define MIX 0
#define LOCA 3
#define XLOC 1
#define PGT 0
#define PGO 0
#define QREM 0
#define EB 0
#define TW0 1
#define ORDM 1
#define NF 7u
#define NPOL 1
#define PD0 0
#define PD1 4
#define HH 0
#define HW 3
#define HKV 4
#define YPOL 1
#define YS2 2
#define OSH 4
#define HSK 3
#define HGR 2
#define KSH 0
#define HLAST 1

#define N2_ASM \
  "v_mov_b64 v[64:65], 0\n\t" \
  "v_mov_b64 v[66:67], 0\n\t" \
  "v_mov_b64 v[68:69], 0\n\t" \
  "v_mov_b64 v[70:71], 0\n\t" \
  "v_mov_b64 v[72:73], 0\n\t" \
  "v_mov_b64 v[74:75], 0\n\t" \
  "v_mov_b64 v[76:77], 0\n\t" \
  "v_mov_b64 v[78:79], 0\n\t" \
  "v_mov_b64 v[80:81], 0\n\t" \
  "v_mov_b64 v[82:83], 0\n\t" \
  "v_mov_b64 v[84:85], 0\n\t" \
  "v_mov_b64 v[86:87], 0\n\t" \
  "v_mov_b64 v[88:89], 0\n\t" \
  "v_mov_b64 v[90:91], 0\n\t" \
  "v_mov_b64 v[92:93], 0\n\t" \
  "v_mov_b64 v[94:95], 0\n\t" \
  "v_mov_b64 v[96:97], 0\n\t" \
  "v_mov_b64 v[98:99], 0\n\t" \
  "v_mov_b64 v[100:101], 0\n\t" \
  "v_mov_b64 v[102:103], 0\n\t" \
  "v_mov_b64 v[104:105], 0\n\t" \
  "v_mov_b64 v[106:107], 0\n\t" \
  "v_mov_b64 v[108:109], 0\n\t" \
  "v_mov_b64 v[110:111], 0\n\t" \
  "v_mov_b64 v[112:113], 0\n\t" \
  "v_mov_b64 v[114:115], 0\n\t" \
  "v_mov_b64 v[116:117], 0\n\t" \
  "v_mov_b64 v[118:119], 0\n\t" \
  "v_mov_b64 v[120:121], 0\n\t" \
  "v_mov_b64 v[122:123], 0\n\t" \
  "v_mov_b64 v[124:125], 0\n\t" \
  "v_mov_b64 v[126:127], 0\n\t" \
  "v_mov_b64 v[128:129], 0\n\t" \
  "v_mov_b64 v[130:131], 0\n\t" \
  "v_mov_b64 v[132:133], 0\n\t" \
  "v_mov_b64 v[134:135], 0\n\t" \
  "v_mov_b64 v[136:137], 0\n\t" \
  "v_mov_b64 v[138:139], 0\n\t" \
  "v_mov_b64 v[140:141], 0\n\t" \
  "v_mov_b64 v[142:143], 0\n\t" \
  "v_mov_b64 v[144:145], 0\n\t" \
  "v_mov_b64 v[146:147], 0\n\t" \
  "v_mov_b64 v[148:149], 0\n\t" \
  "v_mov_b64 v[150:151], 0\n\t" \
  "v_mov_b64 v[152:153], 0\n\t" \
  "v_mov_b64 v[154:155], 0\n\t" \
  "v_mov_b64 v[156:157], 0\n\t" \
  "v_mov_b64 v[158:159], 0\n\t" \
  "v_mov_b64 v[160:161], 0\n\t" \
  "v_mov_b64 v[162:163], 0\n\t" \
  "v_mov_b64 v[164:165], 0\n\t" \
  "v_mov_b64 v[166:167], 0\n\t" \
  "v_add_u32 v60, 36864, v58\n\t" \
  "v_add_u32 v61, 36864, v59\n\t" \
  "s_mov_b32 %[it], 0\n\t" \
  "s_cmp_lg_u32 %[r], 0\n\t" \
  "s_barrier\n\t" \
  "s_cbranch_scc1 NB_%=\n\t" \
  "ds_read_b64_tr_b16 v[48:49], v60 offset:14336\n\t" \
  "ds_read_b64_tr_b16 v[50:51], v60 offset:14848\n\t" \
  "ds_read_b128 v[0:3], v56 offset:38912\n\t" \
  "ds_read_b64_tr_b16 v[52:53], v60 offset:26624\n\t" \
  "ds_read_b64_tr_b16 v[54:55], v60 offset:27136\n\t" \
  "ds_read_b128 v[16:19], v57 offset:38912\n\t" \
  "ds_read_b128 v[4:7], v56 offset:40960\n\t" \
  "ds_read_b128 v[20:23], v57 offset:40960\n\t" \
  "ds_read_b128 v[8:11], v56 offset:43008\n\t" \
  "ds_read_b128 v[24:27], v57 offset:43008\n\t" \
  "ds_read_b128 v[12:15], v56 offset:45056\n\t" \
  "ds_read_b128 v[28:31], v57 offset:45056\n\t" \
  "ds_read_b64_tr_b16 v[32:33], v60 offset:13312\n\t" \
  "ds_read_b64_tr_b16 v[34:35], v60 offset:13824\n\t" \
  "s_mov_b32 s68, 0x50000\n\t" \
  "s_mov_b32 s69, 0x5550055\n\t" \
  "s_mov_b32 s70, 0xa0000\n\t" \
  "s_mov_b32 s71, 0xaaa00aa\n\t" \
  "s_mov_b32 s72, 0x150001\n\t" \
  "s_mov_b32 s73, 0x15550155\n\t" \
  "s_mov_b32 s74, 0x2a0002\n\t" \
  "s_mov_b32 s75, 0x2aaa02aa\n\t" \
  "s_mov_b32 s76, 0x55505555\n\t" \
  "s_mov_b32 s77, 0x50005500\n\t" \
  "s_mov_b32 s78, 0xaaa0aaaa\n\t" \
  "s_mov_b32 s79, 0xa000aa00\n\t" \
  "s_mov_b32 s80, 0x55405554\n\t" \
  "s_mov_b32 s81, 0x40005400\n\t" \
  "s_mov_b32 s82, 0xaa80aaa8\n\t" \
  "s_mov_b32 s83, 0x8000a800\n\t" \
  "s_mov_b32 s84, 0xaaaaaaaa\n\t" \
  "s_mov_b32 s85, 0xaaaaaaaa\n\t" \
  "s_waitcnt lgkmcnt(0)\n\t" \
  "s_branch N2a_%=\n\t" \
  "N0a_%=:\n\t" \
  "s_waitcnt lgkmcnt(10)\n\t" \
  "v_mfma_f32_16x16x32_bf16 v[128:131], v[0:3], v[32:35], v[128:131]\n\t" \
  "ds_read_b64_tr_b16 v[44:45], v58 offset:13312\n\t" \
  "s_waitcnt lgkmcnt(7)\n\t" \
  "v_mfma_f32_16x16x32_bf16 v[128:131], v[16:19], v[36:39], v[128:131]\n\t" \
  "ds_read_b64_tr_b16 v[46:47], v58 offset:13824\n\t" \
  "v_mfma_f32_16x16x32_bf16 v[136:139], v[4:7], v[32:35], v[136:139]\n\t" \
  "ds_read_b128 v[48:51], v56 offset:0\n\t" \
  "v_mfma_f32_16x16x32_bf16 v[136:139], v[20:23], v[36:39], v[136:139]\n\t" \
  "ds_read_b128 v[52:55], v57 offset:0\n\t" \
  "s_waitcnt lgkmcnt(8)\n\t" \
  "v_mfma_f32_16x16x32_bf16 v[144:147], v[8:11], v[32:35], v[144:147]\n\t" \
  "v_mfma_f32_16x16x32_bf16 v[144:147], v[24:27], v[36:39], v[144:147]\n\t" \
  "s_waitcnt lgkmcnt(4)\n\t" \
  "v_mfma_f32_16x16x32_bf16 v[152:155], v[12:15], v[32:35], v[152:155]\n\t" \
  "v_mfma_f32_16x16x32_bf16 v[152:155], v[28:31], v[36:39], v[152:155]\n\t" \
  "ds_read_b64_tr_b16 v[32:33], v59 offset:1024\n\t" \
  "v_mfma_f32_16x16x32_bf16 v[96:99], v[40:43], v[0:3], v[96:99]\n\t" \
  "ds_read_b64_tr_b16 v[34:35], v59 offset:1536\n\t" \
  "s_waitcnt lgkmcnt(4)\n\t" \
  "v_mfma_f32_16x16x32_bf16 v[96:99], v[44:47], v[16:19], v[96:99]\n\t" \
  "ds_read_b64_tr_b16 v[36:37], v59 offset:13312\n\t" \
  "v_mfma_f32_16x16x32_bf16 v[104:107], v[40:43], v[4:7], v[104:107]\n\t" \
  "v_mfma_f32_16x16x32_bf16 v[104:107], v[44:47], v[20:23], v[104:107]\n\t" \
  "ds_read_b64_tr_b16 v[38:39], v59 offset:13824\n\t" \
  "v_mfma_f32_16x16x32_bf16 v[112:115], v[40:43], v[8:11], v[112:115]\n\t" \
  "v_mfma_f32_16x16x32_bf16 v[112:115], v[44:47], v[24:27], v[112:115]\n\t" \
  "v_mfma_f32_16x16x32_bf16 v[120:123], v[40:43], v[12:15], v[120:123]\n\t" \
  "v_mfma_f32_16x16x32_bf16 v[120:123], v[44:47], v[28:31], v[120:123]\n\t" \
  "s_waitcnt lgkmcnt(4)\n\t" \
  "v_mfma_f32_16x16x32_bf16 v[160:163], v[40:43], v[48:51], v[160:163]\n\t" \
  "v_mfma_f32_16x16x32_bf16 v[160:163], v[44:47], v[52:55], v[160:163]\n\t" \
  "ds_read_b64_tr_b16 v[40:41], v59 offset:2048\n\t" \
  "s_waitcnt lgkmcnt(1)\n\t" \
  "v_mfma_f32_16x16x32_bf16 v[164:167], v[32:35], v[48:51], v[164:167]\n\t" \
  "ds_read_b64_tr_b16 v[42:43], v59 offset:2560\n\t" \
  "v_mfma_f32_16x16x32_bf16 v[164:167], v[36:39], v[52:55], v[164:167]\n\t" \
  "ds_read_b64_tr_b16 v[44:45], v59 offset:14336\n\t" \
  "v_mfma_f32_16x16x32_bf16 v[100:103], v[32:35], v[0:3], v[100:103]\n\t" \
  "ds_read_b64_tr_b16 v[46:47], v59 offset:14848\n\t" \
  "v_mfma_f32_16x16x32_bf16 v[100:103], v[36:39], v[16:19], v[100:103]\n\t" \
  "ds_read_b64_tr_b16 v[48:49], v58 offset:0\n\t" \
  "v_mfma_f32_16x16x32_bf16 v[108:111], v[32:35], v[4:7], v[108:111]\n\t" \
  "ds_read_b64_tr_b16 v[50:51], v58 offset:512\n\t" \
  "v_mfma_f32_16x16x32_bf16 v[108:111], v[36:39], v[20:23], v[108:111]\n\t" \
  "ds_read_b64_tr_b16 v[52:53], v58 offset:12288\n\t" \
  "v_mfma_f32_16x16x32_bf16 v[116:119], v[32:35], v[8:11], v[116:119]\n\t" \
  "ds_read_b64_tr_b16 v[54:55], v58 offset:12800\n\t" \
  "v_mfma_f32_16x16x32_bf16 v[116:119], v[36:39], v[24:27], v[116:119]\n\t" \
  "v_mfma_f32_16x16x32_bf16 v[124:127], v[32:35], v[12:15], v[124:127]\n\t" \
  "ds_read_b64_tr_b16 v[32:33], v59 offset:0\n\t" \
  "v_mfma_f32_16x16x32_bf16 v[124:127], v[36:39], v[28:31], v[124:127]\n\t" \
  "ds_read_b64_tr_b16 v[34:35], v59 offset:512\n\t" \
  "s_waitcnt lgkmcnt(6)\n\t" \
  "v_mfma_f32_16x16x32_bf16 v[132:135], v[0:3], v[40:43], v[132:135]\n\t" \
  "ds_read_b64_tr_b16 v[36:37], v59 offset:12288\n\t" \
  "v_mfma_f32_16x16x32_bf16 v[132:135], v[16:19], v[44:47], v[132:135]\n\t" \
  "ds_read_b64_tr_b16 v[38:39], v59 offset:12800\n\t" \
  "v_mfma_f32_16x16x32_bf16 v[140:143], v[4:7], v[40:43], v[140:143]\n\t" \
  "v_mfma_f32_16x16x32_bf16 v[140:143], v[20:23], v[44:47], v[140:143]\n\t" \
  "v_mfma_f32_16x16x32_bf16 v[148:151], v[8:11], v[40:43], v[148:151]\n\t" \
  "v_mfma_f32_16x16x32_bf16 v[148:151], v[24:27], v[44:47], v[148:151]\n\t" \
  "v_mfma_f32_16x16x32_bf16 v[156:159], v[12:15], v[40:43], v[156:159]\n\t" \
  "v_mfma_f32_16x16x32_bf16 v[156:159], v[28:31], v[44:47], v[156:159]\n\t" \
  "s_waitcnt lgkmcnt(4)\n\t" \
  "v_mfma_f32_16x16x32_bf16 v[64:67], v[48:51], v[0:3], v[64:67]\n\t" \
  "s_waitcnt lgkmcnt(0)\n\t" \
  "s_barrier\n\t" \
  "v_mfma_f32_16x16x32_bf16 v[64:67], v[52:55], v[16:19], v[64:67]\n\t" \
  "ds_read_b64_tr_b16 v[40:41], v58 offset:26624\n\t" \
  "v_mfma_f32_16x16x32_bf16 v[68:71], v[32:35], v[0:3], v[68:71]\n\t" \
  "ds_read_b128 v[0:3], v56 offset:20480\n\t" \
  "v_mfma_f32_16x16x32_bf16 v[68:71], v[36:39], v[16:19], v[68:71]\n\t" \
  "ds_read_b128 v[16:19], v57 offset:20480\n\t" \
  "v_mfma_f32_16x16x32_bf16 v[72:75], v[48:51], v[4:7], v[72:75]\n\t" \
  "ds_read_b64_tr_b16 v[42:43], v58 offset:27136\n\t" \
  "v_mfma_f32_16x16x32_bf16 v[72:75], v[52:55], v[20:23], v[72:75]\n\t" \
  "ds_read_b64_tr_b16 v[44:45], v60 offset:2048\n\t" \
  "v_mfma_f32_16x16x32_bf16 v[76:79], v[32:35], v[4:7], v[76:79]\n\t" \
  "ds_read_b128 v[4:7], v56 offset:22528\n\t" \
  "v_mfma_f32_16x16x32_bf16 v[76:79], v[36:39], v[20:23], v[76:79]\n\t" \
  "ds_read_b128 v[20:23], v57 offset:22528\n\t" \
  "v_mfma_f32_16x16x32_bf16 v[80:83], v[48:51], v[8:11], v[80:83]\n\t" \
  "ds_read_b64_tr_b16 v[46:47], v60 offset:2560\n\t" \
  "v_mfma_f32_16x16x32_bf16 v[80:83], v[52:55], v[24:27], v[80:83]\n\t" \
  "v_mfma_f32_16x16x32_bf16 v[84:87], v[32:35], v[8:11], v[84:87]\n\t" \
  "ds_read_b128 v[8:11], v56 offset:24576\n\t" \
  "v_mfma_f32_16x16x32_bf16 v[84:87], v[36:39], v[24:27], v[84:87]\n\t" \
  "ds_read_b128 v[24:27], v57 offset:24576\n\t" \
  "v_mfma_f32_16x16x32_bf16 v[88:91], v[48:51], v[12:15], v[88:91]\n\t" \
  "ds_read_b64_tr_b16 v[48:49], v58 offset:25600\n\t" \
  "v_mfma_f32_16x16x32_bf16 v[88:91], v[52:55], v[28:31], v[88:91]\n\t" \
  "ds_read_b64_tr_b16 v[50:51], v58 offset:26112\n\t" \
  "v_mfma_f32_16x16x32_bf16 v[92:95], v[32:35], v[12:15], v[92:95]\n\t" \
  "ds_read_b128 v[12:15], v56 offset:26624\n\t" \
  "v_mfma_f32_16x16x32_bf16 v[92:95], v[36:39], v[28:31], v[92:95]\n\t" \
  "ds_read_b128 v[28:31], v57 offset:26624\n\t" \
  "N1a_%=:\n\t" \
  "s_waitcnt lgkmcnt(10)\n\t" \
  "v_mfma_f32_16x16x32_bf16 v[128:131], v[0:3], v[40:43], v[128:131]\n\t" \
  "ds_read_b64_tr_b16 v[52:53], v60 offset:1024\n\t" \
  "s_waitcnt lgkmcnt(7)\n\t" \
  "v_mfma_f32_16x16x32_bf16 v[128:131], v[16:19], v[44:47], v[128:131]\n\t" \
  "ds_read_b64_tr_b16 v[54:55], v60 offset:1536\n\t" \
  "v_mfma_f32_16x16x32_bf16 v[136:139], v[4:7], v[40:43], v[136:139]\n\t" \
  "ds_read_b128 v[32:35], v56 offset:18432\n\t" \
  "v_mfma_f32_16x16x32_bf16 v[136:139], v[20:23], v[44:47], v[136:139]\n\t" \
  "ds_read_b128 v[36:39], v57 offset:18432\n\t" \
  "s_waitcnt lgkmcnt(8)\n\t" \
  "v_mfma_f32_16x16x32_bf16 v[144:147], v[8:11], v[40:43], v[144:147]\n\t" \
  "v_mfma_f32_16x16x32_bf16 v[144:147], v[24:27], v[44:47], v[144:147]\n\t" \
  "s_waitcnt lgkmcnt(4)\n\t" \
  "v_mfma_f32_16x16x32_bf16 v[152:155], v[12:15], v[40:43], v[152:155]\n\t" \
  "v_mfma_f32_16x16x32_bf16 v[152:155], v[28:31], v[44:47], v[152:155]\n\t" \
  "ds_read_b64_tr_b16 v[40:41], v59 offset:25600\n\t" \
  "v_mfma_f32_16x16x32_bf16 v[96:99], v[48:51], v[0:3], v[96:99]\n\t" \
  "ds_read_b64_tr_b16 v[42:43], v59 offset:26112\n\t" \
  "s_waitcnt lgkmcnt(4)\n\t" \
  "v_mfma_f32_16x16x32_bf16 v[96:99], v[52:55], v[16:19], v[96:99]\n\t" \
  "ds_read_b64_tr_b16 v[44:45], v61 offset:1024\n\t" \
  "v_mfma_f32_16x16x32_bf16 v[104:107], v[48:51], v[4:7], v[104:107]\n\t" \
  "v_mfma_f32_16x16x32_bf16 v[104:107], v[52:55], v[20:23], v[104:107]\n\t" \
  "ds_read_b64_tr_b16 v[46:47], v61 offset:1536\n\t" \
  "v_mfma_f32_16x16x32_bf16 v[112:115], v[48:51], v[8:11], v[112:115]\n\t" \
  "v_mfma_f32_16x16x32_bf16 v[112:115], v[52:55], v[24:27], v[112:115]\n\t" \
  "v_mfma_f32_16x16x32_bf16 v[120:123], v[48:51], v[12:15], v[120:123]\n\t" \
  "v_mfma_f32_16x16x32_bf16 v[120:123], v[52:55], v[28:31], v[120:123]\n\t" \
  "s_waitcnt lgkmcnt(4)\n\t" \
  "v_mfma_f32_16x16x32_bf16 v[160:163], v[48:51], v[32:35], v[160:163]\n\t" \
  "v_mfma_f32_16x16x32_bf16 v[160:163], v[52:55], v[36:39], v[160:163]\n\t" \
  "ds_read_b64_tr_b16 v[48:49], v59 offset:26624\n\t" \
  "s_waitcnt lgkmcnt(1)\n\t" \
  "v_mfma_f32_16x16x32_bf16 v[164:167], v[40:43], v[32:35], v[164:167]\n\t" \
  "ds_read_b64_tr_b16 v[50:51], v59 offset:27136\n\t" \
  "v_mfma_f32_16x16x32_bf16 v[164:167], v[44:47], v[36:39], v[164:167]\n\t" \
  "ds_read_b64_tr_b16 v[52:53], v61 offset:2048\n\t" \
  "v_mfma_f32_16x16x32_bf16 v[100:103], v[40:43], v[0:3], v[100:103]\n\t" \
  "ds_read_b64_tr_b16 v[54:55], v61 offset:2560\n\t" \
  "v_mfma_f32_16x16x32_bf16 v[100:103], v[44:47], v[16:19], v[100:103]\n\t" \
  "ds_read_b64_tr_b16 v[32:33], v58 offset:24576\n\t" \
  "v_mfma_f32_16x16x32_bf16 v[108:111], v[40:43], v[4:7], v[108:111]\n\t" \
  "ds_read_b64_tr_b16 v[34:35], v58 offset:25088\n\t" \
  "v_mfma_f32_16x16x32_bf16 v[108:111], v[44:47], v[20:23], v[108:111]\n\t" \
  "ds_read_b64_tr_b16 v[36:37], v60 offset:0\n\t" \
  "v_mfma_f32_16x16x32_bf16 v[116:119], v[40:43], v[8:11], v[116:119]\n\t" \
  "ds_read_b64_tr_b16 v[38:39], v60 offset:512\n\t" \
  "v_mfma_f32_16x16x32_bf16 v[116:119], v[44:47], v[24:27], v[116:119]\n\t" \
  "v_mfma_f32_16x16x32_bf16 v[124:127], v[40:43], v[12:15], v[124:127]\n\t" \
  "ds_read_b64_tr_b16 v[40:41], v59 offset:24576\n\t" \
  "v_mfma_f32_16x16x32_bf16 v[124:127], v[44:47], v[28:31], v[124:127]\n\t" \
  "ds_read_b64_tr_b16 v[42:43], v59 offset:25088\n\t" \
  "s_waitcnt lgkmcnt(6)\n\t" \
  "v_mfma_f32_16x16x32_bf16 v[132:135], v[0:3], v[48:51], v[132:135]\n\t" \
  "ds_read_b64_tr_b16 v[44:45], v61 offset:0\n\t" \
  "v_mfma_f32_16x16x32_bf16 v[132:135], v[16:19], v[52:55], v[132:135]\n\t" \
  "ds_read_b64_tr_b16 v[46:47], v61 offset:512\n\t" \
  "v_mfma_f32_16x16x32_bf16 v[140:143], v[4:7], v[48:51], v[140:143]\n\t" \
  "v_mfma_f32_16x16x32_bf16 v[140:143], v[20:23], v[52:55], v[140:143]\n\t" \
  "v_mfma_f32_16x16x32_bf16 v[148:151], v[8:11], v[48:51], v[148:151]\n\t" \
  "v_mfma_f32_16x16x32_bf16 v[148:151], v[24:27], v[52:55], v[148:151]\n\t" \
  "v_mfma_f32_16x16x32_bf16 v[156:159], v[12:15], v[48:51], v[156:159]\n\t" \
  "v_mfma_f32_16x16x32_bf16 v[156:159], v[28:31], v[52:55], v[156:159]\n\t" \
  "s_waitcnt lgkmcnt(4)\n\t" \
  "v_mfma_f32_16x16x32_bf16 v[64:67], v[32:35], v[0:3], v[64:67]\n\t" \
  "s_waitcnt lgkmcnt(0)\n\t" \
  "s_barrier\n\t" \
  "v_mfma_f32_16x16x32_bf16 v[64:67], v[36:39], v[16:19], v[64:67]\n\t" \
  "ds_read_b64_tr_b16 v[48:49], v60 offset:14336\n\t" \
  "v_mfma_f32_16x16x32_bf16 v[68:71], v[40:43], v[0:3], v[68:71]\n\t" \
  "ds_read_b128 v[0:3], v56 offset:38912\n\t" \
  "v_mfma_f32_16x16x32_bf16 v[68:71], v[44:47], v[16:19], v[68:71]\n\t" \
  "ds_read_b128 v[16:19], v57 offset:38912\n\t" \
  "v_mfma_f32_16x16x32_bf16 v[72:75], v[32:35], v[4:7], v[72:75]\n\t" \
  "ds_read_b64_tr_b16 v[50:51], v60 offset:14848\n\t" \
  "v_mfma_f32_16x16x32_bf16 v[72:75], v[36:39], v[20:23], v[72:75]\n\t" \
  "ds_read_b64_tr_b16 v[52:53], v60 offset:26624\n\t" \
  "v_mfma_f32_16x16x32_bf16 v[76:79], v[40:43], v[4:7], v[76:79]\n\t" \
  "ds_read_b128 v[4:7], v56 offset:40960\n\t" \
  "v_mfma_f32_16x16x32_bf16 v[76:79], v[44:47], v[20:23], v[76:79]\n\t" \
  "ds_read_b128 v[20:23], v57 offset:40960\n\t" \
  "v_mfma_f32_16x16x32_bf16 v[80:83], v[32:35], v[8:11], v[80:83]\n\t" \
  "ds_read_b64_tr_b16 v[54:55], v60 offset:27136\n\t" \
  "v_mfma_f32_16x16x32_bf16 v[80:83], v[36:39], v[24:27], v[80:83]\n\t" \
  "v_mfma_f32_16x16x32_bf16 v[84:87], v[40:43], v[8:11], v[84:87]\n\t" \
  "ds_read_b128 v[8:11], v56 offset:43008\n\t" \
  "v_mfma_f32_16x16x32_bf16 v[84:87], v[44:47], v[24:27], v[84:87]\n\t" \
  "ds_read_b128 v[24:27], v57 offset:43008\n\t" \
  "v_mfma_f32_16x16x32_bf16 v[88:91], v[32:35], v[12:15], v[88:91]\n\t" \
  "ds_read_b64_tr_b16 v[32:33], v60 offset:13312\n\t" \
  "v_mfma_f32_16x16x32_bf16 v[88:91], v[36:39], v[28:31], v[88:91]\n\t" \
  "ds_read_b64_tr_b16 v[34:35], v60 offset:13824\n\t" \
  "v_mfma_f32_16x16x32_bf16 v[92:95], v[40:43], v[12:15], v[92:95]\n\t" \
  "ds_read_b128 v[12:15], v56 offset:45056\n\t" \
  "v_mfma_f32_16x16x32_bf16 v[92:95], v[44:47], v[28:31], v[92:95]\n\t" \
  "ds_read_b128 v[28:31], v57 offset:45056\n\t" \
  "N2a_%=:\n\t" \
  "s_waitcnt lgkmcnt(10)\n\t" \
  "v_mfma_f32_16x16x32_bf16 v[128:131], v[0:3], v[48:51], v[128:131]\n\t" \
  "ds_read_b64_tr_b16 v[36:37], v60 offset:25600\n\t" \
  "s_waitcnt lgkmcnt(7)\n\t" \
  "v_mfma_f32_16x16x32_bf16 v[128:131], v[16:19], v[52:55], v[128:131]\n\t" \
  "ds_read_b64_tr_b16 v[38:39], v60 offset:26112\n\t" \
  "v_mfma_f32_16x16x32_bf16 v[136:139], v[4:7], v[48:51], v[136:139]\n\t" \
  "ds_read_b128 v[40:43], v56 offset:36864\n\t" \
  "v_mfma_f32_16x16x32_bf16 v[136:139], v[20:23], v[52:55], v[136:139]\n\t" \
  "ds_read_b128 v[44:47], v57 offset:36864\n\t" \
  "s_waitcnt lgkmcnt(8)\n\t" \
  "v_mfma_f32_16x16x32_bf16 v[144:147], v[8:11], v[48:51], v[144:147]\n\t" \
  "v_mfma_f32_16x16x32_bf16 v[144:147], v[24:27], v[52:55], v[144:147]\n\t" \
  "s_waitcnt lgkmcnt(4)\n\t" \
  "v_mfma_f32_16x16x32_bf16 v[152:155], v[12:15], v[48:51], v[152:155]\n\t" \
  "v_mfma_f32_16x16x32_bf16 v[152:155], v[28:31], v[52:55], v[152:155]\n\t" \
  "ds_read_b64_tr_b16 v[48:49], v61 offset:13312\n\t" \
  "v_mfma_f32_16x16x32_bf16 v[96:99], v[32:35], v[0:3], v[96:99]\n\t" \
  "ds_read_b64_tr_b16 v[50:51], v61 offset:13824\n\t" \
  "s_waitcnt lgkmcnt(4)\n\t" \
  "v_mfma_f32_16x16x32_bf16 v[96:99], v[36:39], v[16:19], v[96:99]\n\t" \
  "ds_read_b64_tr_b16 v[52:53], v61 offset:25600\n\t" \
  "v_mfma_f32_16x16x32_bf16 v[104:107], v[32:35], v[4:7], v[104:107]\n\t" \
  "v_mfma_f32_16x16x32_bf16 v[104:107], v[36:39], v[20:23], v[104:107]\n\t" \
  "ds_read_b64_tr_b16 v[54:55], v61 offset:26112\n\t" \
  "v_mfma_f32_16x16x32_bf16 v[112:115], v[32:35], v[8:11], v[112:115]\n\t" \
  "v_mfma_f32_16x16x32_bf16 v[112:115], v[36:39], v[24:27], v[112:115]\n\t" \
  "v_mfma_f32_16x16x32_bf16 v[120:123], v[32:35], v[12:15], v[120:123]\n\t" \
  "v_mfma_f32_16x16x32_bf16 v[120:123], v[36:39], v[28:31], v[120:123]\n\t" \
  "s_waitcnt lgkmcnt(4)\n\t" \
  "v_mfma_f32_16x16x32_bf16 v[160:163], v[32:35], v[40:43], v[160:163]\n\t" \
  "v_mfma_f32_16x16x32_bf16 v[160:163], v[36:39], v[44:47], v[160:163]\n\t" \
  "ds_read_b64_tr_b16 v[32:33], v61 offset:14336\n\t" \
  "s_waitcnt lgkmcnt(1)\n\t" \
  "v_mfma_f32_16x16x32_bf16 v[164:167], v[48:51], v[40:43], v[164:167]\n\t" \
  "ds_read_b64_tr_b16 v[34:35], v61 offset:14848\n\t" \
  "v_mfma_f32_16x16x32_bf16 v[164:167], v[52:55], v[44:47], v[164:167]\n\t" \
  "ds_read_b64_tr_b16 v[36:37], v61 offset:26624\n\t" \
  "v_mfma_f32_16x16x32_bf16 v[100:103], v[48:51], v[0:3], v[100:103]\n\t" \
  "ds_read_b64_tr_b16 v[38:39], v61 offset:27136\n\t" \
  "v_mfma_f32_16x16x32_bf16 v[100:103], v[52:55], v[16:19], v[100:103]\n\t" \
  "ds_read_b64_tr_b16 v[40:41], v60 offset:12288\n\t" \
  "v_mfma_f32_16x16x32_bf16 v[108:111], v[48:51], v[4:7], v[108:111]\n\t" \
  "ds_read_b64_tr_b16 v[42:43], v60 offset:12800\n\t" \
  "v_mfma_f32_16x16x32_bf16 v[108:111], v[52:55], v[20:23], v[108:111]\n\t" \
  "ds_read_b64_tr_b16 v[44:45], v60 offset:24576\n\t" \
  "v_mfma_f32_16x16x32_bf16 v[116:119], v[48:51], v[8:11], v[116:119]\n\t" \
  "ds_read_b64_tr_b16 v[46:47], v60 offset:25088\n\t" \
  "v_mfma_f32_16x16x32_bf16 v[116:119], v[52:55], v[24:27], v[116:119]\n\t" \
  "v_mfma_f32_16x16x32_bf16 v[124:127], v[48:51], v[12:15], v[124:127]\n\t" \
  "ds_read_b64_tr_b16 v[48:49], v61 offset:12288\n\t" \
  "v_mfma_f32_16x16x32_bf16 v[124:127], v[52:55], v[28:31], v[124:127]\n\t" \
  "ds_read_b64_tr_b16 v[50:51], v61 offset:12800\n\t" \
  "s_waitcnt lgkmcnt(6)\n\t" \
  "v_mfma_f32_16x16x32_bf16 v[132:135], v[0:3], v[32:35], v[132:135]\n\t" \
  "ds_read_b64_tr_b16 v[52:53], v61 offset:24576\n\t" \
  "v_mfma_f32_16x16x32_bf16 v[132:135], v[16:19], v[36:39], v[132:135]\n\t" \
  "ds_read_b64_tr_b16 v[54:55], v61 offset:25088\n\t" \
  "v_mfma_f32_16x16x32_bf16 v[140:143], v[4:7], v[32:35], v[140:143]\n\t" \
  "v_mfma_f32_16x16x32_bf16 v[140:143], v[20:23], v[36:39], v[140:143]\n\t" \
  "s_addk_i32 %[it], 1\n\t" \
  "v_mfma_f32_16x16x32_bf16 v[148:151], v[8:11], v[32:35], v[148:151]\n\t" \
  "s_cmp_lt_u32 %[it], 6\n\t" \
  "v_mfma_f32_16x16x32_bf16 v[148:151], v[24:27], v[36:39], v[148:151]\n\t" \
  "v_mfma_f32_16x16x32_bf16 v[156:159], v[12:15], v[32:35], v[156:159]\n\t" \
  "v_mfma_f32_16x16x32_bf16 v[156:159], v[28:31], v[36:39], v[156:159]\n\t" \
  "s_waitcnt lgkmcnt(4)\n\t" \
  "v_mfma_f32_16x16x32_bf16 v[64:67], v[40:43], v[0:3], v[64:67]\n\t" \
  "s_waitcnt lgkmcnt(0)\n\t" \
  "s_barrier\n\t" \
  "v_mfma_f32_16x16x32_bf16 v[64:67], v[44:47], v[16:19], v[64:67]\n\t" \
  "ds_read_b64_tr_b16 v[32:33], v58 offset:2048\n\t" \
  "v_mfma_f32_16x16x32_bf16 v[68:71], v[48:51], v[0:3], v[68:71]\n\t" \
  "ds_read_b128 v[0:3], v56 offset:2048\n\t" \
  "v_mfma_f32_16x16x32_bf16 v[68:71], v[52:55], v[16:19], v[68:71]\n\t" \
  "ds_read_b128 v[16:19], v57 offset:2048\n\t" \
  "v_mfma_f32_16x16x32_bf16 v[72:75], v[40:43], v[4:7], v[72:75]\n\t" \
  "ds_read_b64_tr_b16 v[34:35], v58 offset:2560\n\t" \
  "v_mfma_f32_16x16x32_bf16 v[72:75], v[44:47], v[20:23], v[72:75]\n\t" \
  "ds_read_b64_tr_b16 v[36:37], v58 offset:14336\n\t" \
  "v_mfma_f32_16x16x32_bf16 v[76:79], v[48:51], v[4:7], v[76:79]\n\t" \
  "ds_read_b128 v[4:7], v56 offset:4096\n\t" \
  "v_mfma_f32_16x16x32_bf16 v[76:79], v[52:55], v[20:23], v[76:79]\n\t" \
  "ds_read_b128 v[20:23], v57 offset:4096\n\t" \
  "v_mfma_f32_16x16x32_bf16 v[80:83], v[40:43], v[8:11], v[80:83]\n\t" \
  "ds_read_b64_tr_b16 v[38:39], v58 offset:14848\n\t" \
  "v_mfma_f32_16x16x32_bf16 v[80:83], v[44:47], v[24:27], v[80:83]\n\t" \
  "v_mfma_f32_16x16x32_bf16 v[84:87], v[48:51], v[8:11], v[84:87]\n\t" \
  "ds_read_b128 v[8:11], v56 offset:6144\n\t" \
  "v_mfma_f32_16x16x32_bf16 v[84:87], v[52:55], v[24:27], v[84:87]\n\t" \
  "ds_read_b128 v[24:27], v57 offset:6144\n\t" \
  "v_mfma_f32_16x16x32_bf16 v[88:91], v[40:43], v[12:15], v[88:91]\n\t" \
  "ds_read_b64_tr_b16 v[40:41], v58 offset:1024\n\t" \
  "v_mfma_f32_16x16x32_bf16 v[88:91], v[44:47], v[28:31], v[88:91]\n\t" \
  "ds_read_b64_tr_b16 v[42:43], v58 offset:1536\n\t" \
  "v_mfma_f32_16x16x32_bf16 v[92:95], v[48:51], v[12:15], v[92:95]\n\t" \
  "ds_read_b128 v[12:15], v56 offset:8192\n\t" \
  "v_mfma_f32_16x16x32_bf16 v[92:95], v[52:55], v[28:31], v[92:95]\n\t" \
  "ds_read_b128 v[28:31], v57 offset:8192\n\t" \
  "s_cbranch_scc1 N0a_%=\n\t" \
  "s_setprio 3\n\t" \
  "s_waitcnt lgkmcnt(0)\n\t" \
  "v_cvt_pk_bf16_f32 v120, v120, v121\n\t" \
  "v_cvt_pk_bf16_f32 v121, v122, v123\n\t" \
  "v_cvt_pk_bf16_f32 v122, v124, v125\n\t" \
  "v_cvt_pk_bf16_f32 v123, v126, v127\n\t" \
  "v_cvt_pk_bf16_f32 v28, v152, v153\n\t" \
  "v_cvt_pk_bf16_f32 v29, v154, v155\n\t" \
  "v_cvt_pk_bf16_f32 v38, v156, v157\n\t" \
  "v_cvt_pk_bf16_f32 v39, v158, v159\n\t" \
  "ds_write_b128 v63, v[120:123]\n\t" \
  "ds_write2_b64 v63, v[28:29], v[38:39] offset0:128 offset1:129\n\t" \
  "v_mov_b32 v57, %[fa]\n\t" \
  "v_mov_b32 v58, 1\n\t" \
  "s_mov_b64 exec, 1\n\t" \
  "ds_write_b32 v57, v58\n\t" \
  "s_mov_b64 exec, -1\n\t" \
  "v_cvt_pk_bf16_f32 v160, v160, v161\n\t" \
  "v_cvt_pk_bf16_f32 v161, v162, v163\n\t" \
  "v_cvt_pk_bf16_f32 v162, v164, v165\n\t" \
  "v_cvt_pk_bf16_f32 v163, v166, v167\n\t" \
  "s_branch NX_%=\n\t" \
  "NB_%=:\n\t" \
  "ds_read_b64_tr_b16 v[48:49], v60 offset:13312\n\t" \
  "ds_read_b64_tr_b16 v[50:51], v60 offset:13824\n\t" \
  "ds_read_b128 v[0:3], v56 offset:47104\n\t" \
  "ds_read_b64_tr_b16 v[52:53], v60 offset:25600\n\t" \
  "ds_read_b64_tr_b16 v[54:55], v60 offset:26112\n\t" \
  "ds_read_b128 v[16:19], v57 offset:47104\n\t" \
  "ds_read_b128 v[4:7], v56 offset:49152\n\t" \
  "ds_read_b128 v[20:23], v57 offset:49152\n\t" \
  "ds_read_b128 v[8:11], v56 offset:51200\n\t" \
  "ds_read_b128 v[24:27], v57 offset:51200\n\t" \
  "ds_read_b128 v[12:15], v56 offset:53248\n\t" \
  "ds_read_b128 v[28:31], v57 offset:53248\n\t" \
  "ds_read_b64_tr_b16 v[32:33], v60 offset:14336\n\t" \
  "ds_read_b64_tr_b16 v[34:35], v60 offset:14848\n\t" \
  "s_mov_b32 s68, 0x50000\n\t" \
  "s_mov_b32 s69, 0x5550055\n\t" \
  "s_mov_b32 s70, 0xa0000\n\t" \
  "s_mov_b32 s71, 0xaaa00aa\n\t" \
  "s_mov_b32 s72, 0x150001\n\t" \
  "s_mov_b32 s73, 0x15550155\n\t" \
  "s_mov_b32 s74, 0x2a0002\n\t" \
  "s_mov_b32 s75, 0x2aaa02aa\n\t" \
  "s_mov_b32 s76, 0x55505555\n\t" \
  "s_mov_b32 s77, 0x50005500\n\t" \
  "s_mov_b32 s78, 0xaaa0aaaa\n\t" \
  "s_mov_b32 s79, 0xa000aa00\n\t" \
  "s_mov_b32 s80, 0x55405554\n\t" \
  "s_mov_b32 s81, 0x40005400\n\t" \
  "s_mov_b32 s82, 0xaa80aaa8\n\t" \
  "s_mov_b32 s83, 0x8000a800\n\t" \
  "s_mov_b32 s84, 0xaaaaaaaa\n\t" \
  "s_mov_b32 s85, 0xaaaaaaaa\n\t" \
  "s_waitcnt lgkmcnt(0)\n\t" \
  "s_branch N2b_%=\n\t" \
  "N0b_%=:\n\t" \
  "s_waitcnt lgkmcnt(10)\n\t" \
  "v_mfma_f32_16x16x32_bf16 v[96:99], v[32:35], v[0:3], v[96:99]\n\t" \
  "ds_read_b64_tr_b16 v[44:45], v58 offset:14336\n\t" \
  "s_waitcnt lgkmcnt(7)\n\t" \
  "v_mfma_f32_16x16x32_bf16 v[96:99], v[36:39], v[16:19], v[96:99]\n\t" \
  "ds_read_b64_tr_b16 v[46:47], v58 offset:14848\n\t" \
  "v_mfma_f32_16x16x32_bf16 v[104:107], v[32:35], v[4:7], v[104:107]\n\t" \
  "ds_read_b128 v[48:51], v56 offset:0\n\t" \
  "v_mfma_f32_16x16x32_bf16 v[104:107], v[36:39], v[20:23], v[104:107]\n\t" \
  "ds_read_b128 v[52:55], v57 offset:0\n\t" \
  "s_waitcnt lgkmcnt(8)\n\t" \
  "v_mfma_f32_16x16x32_bf16 v[112:115], v[32:35], v[8:11], v[112:115]\n\t" \
  "v_mfma_f32_16x16x32_bf16 v[112:115], v[36:39], v[24:27], v[112:115]\n\t" \
  "s_waitcnt lgkmcnt(4)\n\t" \
  "v_mfma_f32_16x16x32_bf16 v[120:123], v[32:35], v[12:15], v[120:123]\n\t" \
  "v_mfma_f32_16x16x32_bf16 v[120:123], v[36:39], v[28:31], v[120:123]\n\t" \
  "ds_read_b64_tr_b16 v[32:33], v59 offset:2048\n\t" \
  "v_mfma_f32_16x16x32_bf16 v[128:131], v[0:3], v[40:43], v[128:131]\n\t" \
  "ds_read_b64_tr_b16 v[34:35], v59 offset:2560\n\t" \
  "s_waitcnt lgkmcnt(4)\n\t" \
  "v_mfma_f32_16x16x32_bf16 v[128:131], v[16:19], v[44:47], v[128:131]\n\t" \
  "ds_read_b64_tr_b16 v[36:37], v59 offset:14336\n\t" \
  "v_mfma_f32_16x16x32_bf16 v[136:139], v[4:7], v[40:43], v[136:139]\n\t" \
  "v_mfma_f32_16x16x32_bf16 v[136:139], v[20:23], v[44:47], v[136:139]\n\t" \
  "ds_read_b64_tr_b16 v[38:39], v59 offset:14848\n\t" \
  "v_mfma_f32_16x16x32_bf16 v[144:147], v[8:11], v[40:43], v[144:147]\n\t" \
  "v_mfma_f32_16x16x32_bf16 v[144:147], v[24:27], v[44:47], v[144:147]\n\t" \
  "v_mfma_f32_16x16x32_bf16 v[152:155], v[12:15], v[40:43], v[152:155]\n\t" \
  "v_mfma_f32_16x16x32_bf16 v[152:155], v[28:31], v[44:47], v[152:155]\n\t" \
  "s_waitcnt lgkmcnt(4)\n\t" \
  "v_mfma_f32_16x16x32_bf16 v[160:163], v[48:51], v[40:43], v[160:163]\n\t" \
  "v_mfma_f32_16x16x32_bf16 v[160:163], v[52:55], v[44:47], v[160:163]\n\t" \
  "ds_read_b64_tr_b16 v[40:41], v59 offset:1024\n\t" \
  "s_waitcnt lgkmcnt(1)\n\t" \
  "v_mfma_f32_16x16x32_bf16 v[164:167], v[48:51], v[32:35], v[164:167]\n\t" \
  "ds_read_b64_tr_b16 v[42:43], v59 offset:1536\n\t" \
  "v_mfma_f32_16x16x32_bf16 v[164:167], v[52:55], v[36:39], v[164:167]\n\t" \
  "ds_read_b64_tr_b16 v[44:45], v59 offset:13312\n\t" \
  "v_mfma_f32_16x16x32_bf16 v[132:135], v[0:3], v[32:35], v[132:135]\n\t" \
  "ds_read_b64_tr_b16 v[46:47], v59 offset:13824\n\t" \
  "v_mfma_f32_16x16x32_bf16 v[132:135], v[16:19], v[36:39], v[132:135]\n\t" \
  "ds_read_b64_tr_b16 v[48:49], v58 offset:0\n\t" \
  "v_mfma_f32_16x16x32_bf16 v[140:143], v[4:7], v[32:35], v[140:143]\n\t" \
  "ds_read_b64_tr_b16 v[50:51], v58 offset:512\n\t" \
  "v_mfma_f32_16x16x32_bf16 v[140:143], v[20:23], v[36:39], v[140:143]\n\t" \
  "ds_read_b64_tr_b16 v[52:53], v58 offset:12288\n\t" \
  "v_mfma_f32_16x16x32_bf16 v[148:151], v[8:11], v[32:35], v[148:151]\n\t" \
  "ds_read_b64_tr_b16 v[54:55], v58 offset:12800\n\t" \
  "v_mfma_f32_16x16x32_bf16 v[148:151], v[24:27], v[36:39], v[148:151]\n\t" \
  "v_mfma_f32_16x16x32_bf16 v[156:159], v[12:15], v[32:35], v[156:159]\n\t" \
  "ds_read_b64_tr_b16 v[32:33], v59 offset:0\n\t" \
  "v_mfma_f32_16x16x32_bf16 v[156:159], v[28:31], v[36:39], v[156:159]\n\t" \
  "ds_read_b64_tr_b16 v[34:35], v59 offset:512\n\t" \
  "s_waitcnt lgkmcnt(6)\n\t" \
  "v_mfma_f32_16x16x32_bf16 v[100:103], v[40:43], v[0:3], v[100:103]\n\t" \
  "ds_read_b64_tr_b16 v[36:37], v59 offset:12288\n\t" \
  "v_mfma_f32_16x16x32_bf16 v[100:103], v[44:47], v[16:19], v[100:103]\n\t" \
  "ds_read_b64_tr_b16 v[38:39], v59 offset:12800\n\t" \
  "v_mfma_f32_16x16x32_bf16 v[108:111], v[40:43], v[4:7], v[108:111]\n\t" \
  "v_mfma_f32_16x16x32_bf16 v[108:111], v[44:47], v[20:23], v[108:111]\n\t" \
  "v_mfma_f32_16x16x32_bf16 v[116:119], v[40:43], v[8:11], v[116:119]\n\t" \
  "v_mfma_f32_16x16x32_bf16 v[116:119], v[44:47], v[24:27], v[116:119]\n\t" \
  "v_mfma_f32_16x16x32_bf16 v[124:127], v[40:43], v[12:15], v[124:127]\n\t" \
  "v_mfma_f32_16x16x32_bf16 v[124:127], v[44:47], v[28:31], v[124:127]\n\t" \
  "s_waitcnt lgkmcnt(4)\n\t" \
  "v_mfma_f32_16x16x32_bf16 v[64:67], v[48:51], v[0:3], v[64:67]\n\t" \
  "s_waitcnt lgkmcnt(0)\n\t" \
  "s_barrier\n\t" \
  "v_mfma_f32_16x16x32_bf16 v[64:67], v[52:55], v[16:19], v[64:67]\n\t" \
  "ds_read_b64_tr_b16 v[40:41], v58 offset:25600\n\t" \
  "v_mfma_f32_16x16x32_bf16 v[68:71], v[32:35], v[0:3], v[68:71]\n\t" \
  "ds_read_b128 v[0:3], v56 offset:28672\n\t" \
  "v_mfma_f32_16x16x32_bf16 v[68:71], v[36:39], v[16:19], v[68:71]\n\t" \
  "ds_read_b128 v[16:19], v57 offset:28672\n\t" \
  "v_mfma_f32_16x16x32_bf16 v[72:75], v[48:51], v[4:7], v[72:75]\n\t" \
  "ds_read_b64_tr_b16 v[42:43], v58 offset:26112\n\t" \
  "v_mfma_f32_16x16x32_bf16 v[72:75], v[52:55], v[20:23], v[72:75]\n\t" \
  "ds_read_b64_tr_b16 v[44:45], v60 offset:1024\n\t" \
  "v_mfma_f32_16x16x32_bf16 v[76:79], v[32:35], v[4:7], v[76:79]\n\t" \
  "ds_read_b128 v[4:7], v56 offset:30720\n\t" \
  "v_mfma_f32_16x16x32_bf16 v[76:79], v[36:39], v[20:23], v[76:79]\n\t" \
  "ds_read_b128 v[20:23], v57 offset:30720\n\t" \
  "v_mfma_f32_16x16x32_bf16 v[80:83], v[48:51], v[8:11], v[80:83]\n\t" \
  "ds_read_b64_tr_b16 v[46:47], v60 offset:1536\n\t" \
  "v_mfma_f32_16x16x32_bf16 v[80:83], v[52:55], v[24:27], v[80:83]\n\t" \
  "v_mfma_f32_16x16x32_bf16 v[84:87], v[32:35], v[8:11], v[84:87]\n\t" \
  "ds_read_b128 v[8:11], v56 offset:32768\n\t" \
  "v_mfma_f32_16x16x32_bf16 v[84:87], v[36:39], v[24:27], v[84:87]\n\t" \
  "ds_read_b128 v[24:27], v57 offset:32768\n\t" \
  "v_mfma_f32_16x16x32_bf16 v[88:91], v[48:51], v[12:15], v[88:91]\n\t" \
  "ds_read_b64_tr_b16 v[48:49], v58 offset:26624\n\t" \
  "v_mfma_f32_16x16x32_bf16 v[88:91], v[52:55], v[28:31], v[88:91]\n\t" \
  "ds_read_b64_tr_b16 v[50:51], v58 offset:27136\n\t" \
  "v_mfma_f32_16x16x32_bf16 v[92:95], v[32:35], v[12:15], v[92:95]\n\t" \
  "ds_read_b128 v[12:15], v56 offset:34816\n\t" \
  "v_mfma_f32_16x16x32_bf16 v[92:95], v[36:39], v[28:31], v[92:95]\n\t" \
  "ds_read_b128 v[28:31], v57 offset:34816\n\t" \
  "N1b_%=:\n\t" \
  "s_waitcnt lgkmcnt(10)\n\t" \
  "v_mfma_f32_16x16x32_bf16 v[96:99], v[40:43], v[0:3], v[96:99]\n\t" \
  "ds_read_b64_tr_b16 v[52:53], v60 offset:2048\n\t" \
  "s_waitcnt lgkmcnt(7)\n\t" \
  "v_mfma_f32_16x16x32_bf16 v[96:99], v[44:47], v[16:19], v[96:99]\n\t" \
  "ds_read_b64_tr_b16 v[54:55], v60 offset:2560\n\t" \
  "v_mfma_f32_16x16x32_bf16 v[104:107], v[40:43], v[4:7], v[104:107]\n\t" \
  "ds_read_b128 v[32:35], v56 offset:18432\n\t" \
  "v_mfma_f32_16x16x32_bf16 v[104:107], v[44:47], v[20:23], v[104:107]\n\t" \
  "ds_read_b128 v[36:39], v57 offset:18432\n\t" \
  "s_waitcnt lgkmcnt(8)\n\t" \
  "v_mfma_f32_16x16x32_bf16 v[112:115], v[40:43], v[8:11], v[112:115]\n\t" \
  "v_mfma_f32_16x16x32_bf16 v[112:115], v[44:47], v[24:27], v[112:115]\n\t" \
  "s_waitcnt lgkmcnt(4)\n\t" \
  "v_mfma_f32_16x16x32_bf16 v[120:123], v[40:43], v[12:15], v[120:123]\n\t" \
  "v_mfma_f32_16x16x32_bf16 v[120:123], v[44:47], v[28:31], v[120:123]\n\t" \
  "ds_read_b64_tr_b16 v[40:41], v59 offset:26624\n\t" \
  "v_mfma_f32_16x16x32_bf16 v[128:131], v[0:3], v[48:51], v[128:131]\n\t" \
  "ds_read_b64_tr_b16 v[42:43], v59 offset:27136\n\t" \
  "s_waitcnt lgkmcnt(4)\n\t" \
  "v_mfma_f32_16x16x32_bf16 v[128:131], v[16:19], v[52:55], v[128:131]\n\t" \
  "ds_read_b64_tr_b16 v[44:45], v61 offset:2048\n\t" \
  "v_mfma_f32_16x16x32_bf16 v[136:139], v[4:7], v[48:51], v[136:139]\n\t" \
  "v_mfma_f32_16x16x32_bf16 v[136:139], v[20:23], v[52:55], v[136:139]\n\t" \
  "ds_read_b64_tr_b16 v[46:47], v61 offset:2560\n\t" \
  "v_mfma_f32_16x16x32_bf16 v[144:147], v[8:11], v[48:51], v[144:147]\n\t" \
  "v_mfma_f32_16x16x32_bf16 v[144:147], v[24:27], v[52:55], v[144:147]\n\t" \
  "v_mfma_f32_16x16x32_bf16 v[152:155], v[12:15], v[48:51], v[152:155]\n\t" \
  "v_mfma_f32_16x16x32_bf16 v[152:155], v[28:31], v[52:55], v[152:155]\n\t" \
  "s_waitcnt lgkmcnt(4)\n\t" \
  "v_mfma_f32_16x16x32_bf16 v[160:163], v[32:35], v[48:51], v[160:163]\n\t" \
  "v_mfma_f32_16x16x32_bf16 v[160:163], v[36:39], v[52:55], v[160:163]\n\t" \
  "ds_read_b64_tr_b16 v[48:49], v59 offset:25600\n\t" \
  "s_waitcnt lgkmcnt(1)\n\t" \
  "v_mfma_f32_16x16x32_bf16 v[164:167], v[32:35], v[40:43], v[164:167]\n\t" \
  "ds_read_b64_tr_b16 v[50:51], v59 offset:26112\n\t" \
  "v_mfma_f32_16x16x32_bf16 v[164:167], v[36:39], v[44:47], v[164:167]\n\t" \
  "ds_read_b64_tr_b16 v[52:53], v61 offset:1024\n\t" \
  "v_mfma_f32_16x16x32_bf16 v[132:135], v[0:3], v[40:43], v[132:135]\n\t" \
  "ds_read_b64_tr_b16 v[54:55], v61 offset:1536\n\t" \
  "v_mfma_f32_16x16x32_bf16 v[132:135], v[16:19], v[44:47], v[132:135]\n\t" \
  "ds_read_b64_tr_b16 v[32:33], v58 offset:24576\n\t" \
  "v_mfma_f32_16x16x32_bf16 v[140:143], v[4:7], v[40:43], v[140:143]\n\t" \
  "ds_read_b64_tr_b16 v[34:35], v58 offset:25088\n\t" \
  "v_mfma_f32_16x16x32_bf16 v[140:143], v[20:23], v[44:47], v[140:143]\n\t" \
  "ds_read_b64_tr_b16 v[36:37], v60 offset:0\n\t" \
  "v_mfma_f32_16x16x32_bf16 v[148:151], v[8:11], v[40:43], v[148:151]\n\t" \
  "ds_read_b64_tr_b16 v[38:39], v60 offset:512\n\t" \
  "v_mfma_f32_16x16x32_bf16 v[148:151], v[24:27], v[44:47], v[148:151]\n\t" \
  "v_mfma_f32_16x16x32_bf16 v[156:159], v[12:15], v[40:43], v[156:159]\n\t" \
  "ds_read_b64_tr_b16 v[40:41], v59 offset:24576\n\t" \
  "v_mfma_f32_16x16x32_bf16 v[156:159], v[28:31], v[44:47], v[156:159]\n\t" \
  "ds_read_b64_tr_b16 v[42:43], v59 offset:25088\n\t" \
  "s_waitcnt lgkmcnt(6)\n\t" \
  "v_mfma_f32_16x16x32_bf16 v[100:103], v[48:51], v[0:3], v[100:103]\n\t" \
  "ds_read_b64_tr_b16 v[44:45], v61 offset:0\n\t" \
  "v_mfma_f32_16x16x32_bf16 v[100:103], v[52:55], v[16:19], v[100:103]\n\t" \
  "ds_read_b64_tr_b16 v[46:47], v61 offset:512\n\t" \
  "v_mfma_f32_16x16x32_bf16 v[108:111], v[48:51], v[4:7], v[108:111]\n\t" \
  "v_mfma_f32_16x16x32_bf16 v[108:111], v[52:55], v[20:23], v[108:111]\n\t" \
  "v_mfma_f32_16x16x32_bf16 v[116:119], v[48:51], v[8:11], v[116:119]\n\t" \
  "v_mfma_f32_16x16x32_bf16 v[116:119], v[52:55], v[24:27], v[116:119]\n\t" \
  "v_mfma_f32_16x16x32_bf16 v[124:127], v[48:51], v[12:15], v[124:127]\n\t" \
  "v_mfma_f32_16x16x32_bf16 v[124:127], v[52:55], v[28:31], v[124:127]\n\t" \
  "s_waitcnt lgkmcnt(4)\n\t" \
  "v_mfma_f32_16x16x32_bf16 v[64:67], v[32:35], v[0:3], v[64:67]\n\t" \
  "s_waitcnt lgkmcnt(0)\n\t" \
  "s_barrier\n\t" \
  "v_mfma_f32_16x16x32_bf16 v[64:67], v[36:39], v[16:19], v[64:67]\n\t" \
  "ds_read_b64_tr_b16 v[48:49], v60 offset:13312\n\t" \
  "v_mfma_f32_16x16x32_bf16 v[68:71], v[40:43], v[0:3], v[68:71]\n\t" \
  "ds_read_b128 v[0:3], v56 offset:47104\n\t" \
  "v_mfma_f32_16x16x32_bf16 v[68:71], v[44:47], v[16:19], v[68:71]\n\t" \
  "ds_read_b128 v[16:19], v57 offset:47104\n\t" \
  "v_mfma_f32_16x16x32_bf16 v[72:75], v[32:35], v[4:7], v[72:75]\n\t" \
  "ds_read_b64_tr_b16 v[50:51], v60 offset:13824\n\t" \
  "v_mfma_f32_16x16x32_bf16 v[72:75], v[36:39], v[20:23], v[72:75]\n\t" \
  "ds_read_b64_tr_b16 v[52:53], v60 offset:25600\n\t" \
  "v_mfma_f32_16x16x32_bf16 v[76:79], v[40:43], v[4:7], v[76:79]\n\t" \
  "ds_read_b128 v[4:7], v56 offset:49152\n\t" \
  "v_mfma_f32_16x16x32_bf16 v[76:79], v[44:47], v[20:23], v[76:79]\n\t" \
  "ds_read_b128 v[20:23], v57 offset:49152\n\t" \
  "v_mfma_f32_16x16x32_bf16 v[80:83], v[32:35], v[8:11], v[80:83]\n\t" \
  "ds_read_b64_tr_b16 v[54:55], v60 offset:26112\n\t" \
  "v_mfma_f32_16x16x32_bf16 v[80:83], v[36:39], v[24:27], v[80:83]\n\t" \
  "v_mfma_f32_16x16x32_bf16 v[84:87], v[40:43], v[8:11], v[84:87]\n\t" \
  "ds_read_b128 v[8:11], v56 offset:51200\n\t" \
  "v_mfma_f32_16x16x32_bf16 v[84:87], v[44:47], v[24:27], v[84:87]\n\t" \
  "ds_read_b128 v[24:27], v57 offset:51200\n\t" \
  "v_mfma_f32_16x16x32_bf16 v[88:91], v[32:35], v[12:15], v[88:91]\n\t" \
  "ds_read_b64_tr_b16 v[32:33], v60 offset:14336\n\t" \
  "v_mfma_f32_16x16x32_bf16 v[88:91], v[36:39], v[28:31], v[88:91]\n\t" \
  "ds_read_b64_tr_b16 v[34:35], v60 offset:14848\n\t" \
  "v_mfma_f32_16x16x32_bf16 v[92:95], v[40:43], v[12:15], v[92:95]\n\t" \
  "ds_read_b128 v[12:15], v56 offset:53248\n\t" \
  "v_mfma_f32_16x16x32_bf16 v[92:95], v[44:47], v[28:31], v[92:95]\n\t" \
  "ds_read_b128 v[28:31], v57 offset:53248\n\t" \
  "N2b_%=:\n\t" \
  "s_waitcnt lgkmcnt(10)\n\t" \
  "v_mfma_f32_16x16x32_bf16 v[96:99], v[48:51], v[0:3], v[96:99]\n\t" \
  "ds_read_b64_tr_b16 v[36:37], v60 offset:26624\n\t" \
  "s_waitcnt lgkmcnt(7)\n\t" \
  "v_mfma_f32_16x16x32_bf16 v[96:99], v[52:55], v[16:19], v[96:99]\n\t" \
  "ds_read_b64_tr_b16 v[38:39], v60 offset:27136\n\t" \
  "v_mfma_f32_16x16x32_bf16 v[104:107], v[48:51], v[4:7], v[104:107]\n\t" \
  "ds_read_b128 v[40:43], v56 offset:36864\n\t" \
  "v_mfma_f32_16x16x32_bf16 v[104:107], v[52:55], v[20:23], v[104:107]\n\t" \
  "ds_read_b128 v[44:47], v57 offset:36864\n\t" \
  "s_waitcnt lgkmcnt(8)\n\t" \
  "v_mfma_f32_16x16x32_bf16 v[112:115], v[48:51], v[8:11], v[112:115]\n\t" \
  "v_mfma_f32_16x16x32_bf16 v[112:115], v[52:55], v[24:27], v[112:115]\n\t" \
  "s_waitcnt lgkmcnt(4)\n\t" \
  "v_mfma_f32_16x16x32_bf16 v[120:123], v[48:51], v[12:15], v[120:123]\n\t" \
  "v_mfma_f32_16x16x32_bf16 v[120:123], v[52:55], v[28:31], v[120:123]\n\t" \
  "ds_read_b64_tr_b16 v[48:49], v61 offset:14336\n\t" \
  "v_mfma_f32_16x16x32_bf16 v[128:131], v[0:3], v[32:35], v[128:131]\n\t" \
  "ds_read_b64_tr_b16 v[50:51], v61 offset:14848\n\t" \
  "s_waitcnt lgkmcnt(4)\n\t" \
  "v_mfma_f32_16x16x32_bf16 v[128:131], v[16:19], v[36:39], v[128:131]\n\t" \
  "ds_read_b64_tr_b16 v[52:53], v61 offset:26624\n\t" \
  "v_mfma_f32_16x16x32_bf16 v[136:139], v[4:7], v[32:35], v[136:139]\n\t" \
  "v_mfma_f32_16x16x32_bf16 v[136:139], v[20:23], v[36:39], v[136:139]\n\t" \
  "ds_read_b64_tr_b16 v[54:55], v61 offset:27136\n\t" \
  "v_mfma_f32_16x16x32_bf16 v[144:147], v[8:11], v[32:35], v[144:147]\n\t" \
  "v_mfma_f32_16x16x32_bf16 v[144:147], v[24:27], v[36:39], v[144:147]\n\t" \
  "v_mfma_f32_16x16x32_bf16 v[152:155], v[12:15], v[32:35], v[152:155]\n\t" \
  "v_mfma_f32_16x16x32_bf16 v[152:155], v[28:31], v[36:39], v[152:155]\n\t" \
  "s_waitcnt lgkmcnt(4)\n\t" \
  "v_mfma_f32_16x16x32_bf16 v[160:163], v[40:43], v[32:35], v[160:163]\n\t" \
  "v_mfma_f32_16x16x32_bf16 v[160:163], v[44:47], v[36:39], v[160:163]\n\t" \
  "ds_read_b64_tr_b16 v[32:33], v61 offset:13312\n\t" \
  "s_waitcnt lgkmcnt(1)\n\t" \
  "v_mfma_f32_16x16x32_bf16 v[164:167], v[40:43], v[48:51], v[164:167]\n\t" \
  "ds_read_b64_tr_b16 v[34:35], v61 offset:13824\n\t" \
  "v_mfma_f32_16x16x32_bf16 v[164:167], v[44:47], v[52:55], v[164:167]\n\t" \
  "ds_read_b64_tr_b16 v[36:37], v61 offset:25600\n\t" \
  "v_mfma_f32_16x16x32_bf16 v[132:135], v[0:3], v[48:51], v[132:135]\n\t" \
  "ds_read_b64_tr_b16 v[38:39], v61 offset:26112\n\t" \
  "v_mfma_f32_16x16x32_bf16 v[132:135], v[16:19], v[52:55], v[132:135]\n\t" \
  "ds_read_b64_tr_b16 v[40:41], v60 offset:12288\n\t" \
  "v_mfma_f32_16x16x32_bf16 v[140:143], v[4:7], v[48:51], v[140:143]\n\t" \
  "ds_read_b64_tr_b16 v[42:43], v60 offset:12800\n\t" \
  "v_mfma_f32_16x16x32_bf16 v[140:143], v[20:23], v[52:55], v[140:143]\n\t" \
  "ds_read_b64_tr_b16 v[44:45], v60 offset:24576\n\t" \
  "v_mfma_f32_16x16x32_bf16 v[148:151], v[8:11], v[48:51], v[148:151]\n\t" \
  "ds_read_b64_tr_b16 v[46:47], v60 offset:25088\n\t" \
  "v_mfma_f32_16x16x32_bf16 v[148:151], v[24:27], v[52:55], v[148:151]\n\t" \
  "v_mfma_f32_16x16x32_bf16 v[156:159], v[12:15], v[48:51], v[156:159]\n\t" \
  "ds_read_b64_tr_b16 v[48:49], v61 offset:12288\n\t" \
  "v_mfma_f32_16x16x32_bf16 v[156:159], v[28:31], v[52:55], v[156:159]\n\t" \
  "ds_read_b64_tr_b16 v[50:51], v61 offset:12800\n\t" \
  "s_waitcnt lgkmcnt(6)\n\t" \
  "v_mfma_f32_16x16x32_bf16 v[100:103], v[32:35], v[0:3], v[100:103]\n\t" \
  "ds_read_b64_tr_b16 v[52:53], v61 offset:24576\n\t" \
  "v_mfma_f32_16x16x32_bf16 v[100:103], v[36:39], v[16:19], v[100:103]\n\t" \
  "ds_read_b64_tr_b16 v[54:55], v61 offset:25088\n\t" \
  "v_mfma_f32_16x16x32_bf16 v[108:111], v[32:35], v[4:7], v[108:111]\n\t" \
  "v_mfma_f32_16x16x32_bf16 v[108:111], v[36:39], v[20:23], v[108:111]\n\t" \
  "s_addk_i32 %[it], 1\n\t" \
  "v_mfma_f32_16x16x32_bf16 v[116:119], v[32:35], v[8:11], v[116:119]\n\t" \
  "s_cmp_lt_u32 %[it], 6\n\t" \
  "v_mfma_f32_16x16x32_bf16 v[116:119], v[36:39], v[24:27], v[116:119]\n\t" \
  "v_mfma_f32_16x16x32_bf16 v[124:127], v[32:35], v[12:15], v[124:127]\n\t" \
  "v_mfma_f32_16x16x32_bf16 v[124:127], v[36:39], v[28:31], v[124:127]\n\t" \
  "s_waitcnt lgkmcnt(4)\n\t" \
  "v_mfma_f32_16x16x32_bf16 v[64:67], v[40:43], v[0:3], v[64:67]\n\t" \
  "s_waitcnt lgkmcnt(0)\n\t" \
  "s_barrier\n\t" \
  "v_mfma_f32_16x16x32_bf16 v[64:67], v[44:47], v[16:19], v[64:67]\n\t" \
  "ds_read_b64_tr_b16 v[32:33], v58 offset:1024\n\t" \
  "v_mfma_f32_16x16x32_bf16 v[68:71], v[48:51], v[0:3], v[68:71]\n\t" \
  "ds_read_b128 v[0:3], v56 offset:10240\n\t" \
  "v_mfma_f32_16x16x32_bf16 v[68:71], v[52:55], v[16:19], v[68:71]\n\t" \
  "ds_read_b128 v[16:19], v57 offset:10240\n\t" \
  "v_mfma_f32_16x16x32_bf16 v[72:75], v[40:43], v[4:7], v[72:75]\n\t" \
  "ds_read_b64_tr_b16 v[34:35], v58 offset:1536\n\t" \
  "v_mfma_f32_16x16x32_bf16 v[72:75], v[44:47], v[20:23], v[72:75]\n\t" \
  "ds_read_b64_tr_b16 v[36:37], v58 offset:13312\n\t" \
  "v_mfma_f32_16x16x32_bf16 v[76:79], v[48:51], v[4:7], v[76:79]\n\t" \
  "ds_read_b128 v[4:7], v56 offset:12288\n\t" \
  "v_mfma_f32_16x16x32_bf16 v[76:79], v[52:55], v[20:23], v[76:79]\n\t" \
  "ds_read_b128 v[20:23], v57 offset:12288\n\t" \
  "v_mfma_f32_16x16x32_bf16 v[80:83], v[40:43], v[8:11], v[80:83]\n\t" \
  "ds_read_b64_tr_b16 v[38:39], v58 offset:13824\n\t" \
  "v_mfma_f32_16x16x32_bf16 v[80:83], v[44:47], v[24:27], v[80:83]\n\t" \
  "v_mfma_f32_16x16x32_bf16 v[84:87], v[48:51], v[8:11], v[84:87]\n\t" \
  "ds_read_b128 v[8:11], v56 offset:14336\n\t" \
  "v_mfma_f32_16x16x32_bf16 v[84:87], v[52:55], v[24:27], v[84:87]\n\t" \
  "ds_read_b128 v[24:27], v57 offset:14336\n\t" \
  "v_mfma_f32_16x16x32_bf16 v[88:91], v[40:43], v[12:15], v[88:91]\n\t" \
  "ds_read_b64_tr_b16 v[40:41], v58 offset:2048\n\t" \
  "v_mfma_f32_16x16x32_bf16 v[88:91], v[44:47], v[28:31], v[88:91]\n\t" \
  "ds_read_b64_tr_b16 v[42:43], v58 offset:2560\n\t" \
  "v_mfma_f32_16x16x32_bf16 v[92:95], v[48:51], v[12:15], v[92:95]\n\t" \
  "ds_read_b128 v[12:15], v56 offset:16384\n\t" \
  "v_mfma_f32_16x16x32_bf16 v[92:95], v[52:55], v[28:31], v[92:95]\n\t" \
  "ds_read_b128 v[28:31], v57 offset:16384\n\t" \
  "s_cbranch_scc1 N0b_%=\n\t" \
  "s_setprio 3\n\t" \
  "s_waitcnt lgkmcnt(0)\n\t" \
  "v_cvt_pk_bf16_f32 v160, v160, v161\n\t" \
  "v_cvt_pk_bf16_f32 v161, v162, v163\n\t" \
  "v_cvt_pk_bf16_f32 v162, v164, v165\n\t" \
  "v_cvt_pk_bf16_f32 v163, v166, v167\n\t" \
  "ds_write_b128 v63, v[160:163] offset:2048\n\t" \
  "v_mov_b32 v57, %[fa]\n\t" \
  "v_mov_b32 v58, 1\n\t" \
  "s_mov_b64 exec, 1\n\t" \
  "ds_write_b32 v57, v58\n\t" \
  "s_mov_b64 exec, -1\n\t" \
  "v_cvt_pk_bf16_f32 v120, v120, v121\n\t" \
  "v_cvt_pk_bf16_f32 v121, v122, v123\n\t" \
  "v_cvt_pk_bf16_f32 v122, v124, v125\n\t" \
  "v_cvt_pk_bf16_f32 v123, v126, v127\n\t" \
  "v_cvt_pk_bf16_f32 v28, v152, v153\n\t" \
  "v_cvt_pk_bf16_f32 v29, v154, v155\n\t" \
  "v_cvt_pk_bf16_f32 v38, v156, v157\n\t" \
  "v_cvt_pk_bf16_f32 v39, v158, v159\n\t" \
  "NX_%=:\n\t" \
  "v_cvt_pk_bf16_f32 v96, v96, v97\n\t" \
  "v_cvt_pk_bf16_f32 v97, v98, v99\n\t" \
  "v_cvt_pk_bf16_f32 v98, v100, v101\n\t" \
  "v_cvt_pk_bf16_f32 v99, v102, v103\n\t" \
  "v_cvt_pk_bf16_f32 v104, v104, v105\n\t" \
  "v_cvt_pk_bf16_f32 v105, v106, v107\n\t" \
  "v_cvt_pk_bf16_f32 v106, v108, v109\n\t" \
  "v_cvt_pk_bf16_f32 v107, v110, v111\n\t" \
  "v_mov_b32 v60, 0xff61b1e6\n\t" \
  "v_cndmask_b32_e64 v56, v60, 0, s[68:69]\n\t" \
  "v_cndmask_b32_e64 v57, v60, 0, s[70:71]\n\t" \
  "v_cndmask_b32_e64 v58, v60, 0, s[72:73]\n\t" \
  "v_cndmask_b32_e64 v59, v60, 0, s[74:75]\n\t" \
  "v_cvt_pk_bf16_f32 v72, v72, v73\n\t" \
  "v_cvt_pk_bf16_f32 v73, v74, v75\n\t" \
  "v_cvt_pk_bf16_f32 v74, v76, v77\n\t" \
  "v_cvt_pk_bf16_f32 v75, v78, v79\n\t" \
  "v_cndmask_b32_e64 v52, v60, 0, s[76:77]\n\t" \
  "v_cndmask_b32_e64 v53, v60, 0, s[78:79]\n\t" \
  "v_cndmask_b32_e64 v54, v60, 0, s[80:81]\n\t" \
  "v_cndmask_b32_e64 v55, v60, 0, s[82:83]\n\t" \
  "v_cvt_pk_bf16_f32 v24, v136, v137\n\t" \
  "v_cvt_pk_bf16_f32 v25, v138, v139\n\t" \
  "v_cvt_pk_bf16_f32 v34, v140, v141\n\t" \
  "v_cvt_pk_bf16_f32 v35, v142, v143\n\t" \
  "v_cvt_pk_bf16_f32 v26, v144, v145\n\t" \
  "v_cvt_pk_bf16_f32 v27, v146, v147\n\t" \
  "v_cvt_pk_bf16_f32 v36, v148, v149\n\t" \
  "v_cvt_pk_bf16_f32 v37, v150, v151\n\t" \
  "v_mfma_f32_16x16x32_bf16 v[136:139], v[96:99], v[72:75], v[56:59]\n\t" \
  "v_cvt_pk_bf16_f32 v112, v112, v113\n\t" \
  "v_cvt_pk_bf16_f32 v113, v114, v115\n\t" \
  "v_mfma_f32_16x16x32_bf16 v[140:143], v[104:107], v[72:75], v[52:55]\n\t" \
  "v_cvt_pk_bf16_f32 v114, v116, v117\n\t" \
  "v_cvt_pk_bf16_f32 v115, v118, v119\n\t" \
  "v_cvt_pk_bf16_f32 v80, v80, v81\n\t" \
  "v_cvt_pk_bf16_f32 v81, v82, v83\n\t" \
  "v_cvt_pk_bf16_f32 v82, v84, v85\n\t" \
  "v_cvt_pk_bf16_f32 v83, v86, v87\n\t" \
  "v_cvt_pk_bf16_f32 v22, v128, v129\n\t" \
  "v_cvt_pk_bf16_f32 v23, v130, v131\n\t" \
  "v_mfma_f32_16x16x32_bf16 v[144:147], v[104:107], v[80:83], v[56:59]\n\t" \
  "v_cvt_pk_bf16_f32 v32, v132, v133\n\t" \
  "v_cvt_pk_bf16_f32 v33, v134, v135\n\t" \
  "v_mfma_f32_16x16x32_bf16 v[148:151], v[112:115], v[80:83], v[52:55]\n\t" \
  "v_cvt_pk_bf16_f32 v88, v88, v89\n\t" \
  "v_cvt_pk_bf16_f32 v89, v90, v91\n\t" \
  "v_cvt_pk_bf16_f32 v90, v92, v93\n\t" \
  "v_cvt_pk_bf16_f32 v91, v94, v95\n\t" \
  "v_mov_b32 v61, %[fp]\n\t" \
  "v_add_u32 v60, %[vo], v63\n\t" \
  "v_mfma_f32_16x16x32_bf16 v[152:155], v[112:115], v[88:91], v[56:59]\n\t" \
  "ds_read_b32 v0, v61\n\t" \
  "ds_read_b64 v[20:21], v60\n\t" \
  "ds_read_b64 v[30:31], v60 offset:8\n\t" \
  "s_mov_b64 exec, %[rm]\n\t" \
  "ds_read_b128 v[160:163], v63\n\t" \
  "s_mov_b64 exec, -1\n\t" \
  "v_mfma_f32_16x16x32_bf16 v[156:159], v[120:123], v[88:91], v[52:55]\n\t" \
  "v_cvt_pk_bf16_f32 v64, v64, v65\n\t" \
  "v_cvt_pk_bf16_f32 v65, v66, v67\n\t" \
  "v_cvt_pk_bf16_f32 v66, v68, v69\n\t" \
  "v_cvt_pk_bf16_f32 v67, v70, v71\n\t" \
  "v_max_f32 v136, v136, v137\n\t" \
  "v_max_f32 v138, v138, v139\n\t" \
  "v_mfma_f32_16x16x32_bf16 v[132:135], v[96:99], v[64:67], v[52:55]\n\t" \
  "v_max_f32 v140, v140, v141\n\t" \
  "v_max_f32 v142, v142, v143\n\t" \
  "AW_%=:\n\t" \
  "s_waitcnt lgkmcnt(0)\n\t" \
  "v_cmp_eq_u32 vcc, 0, v0\n\t" \
  "v_max3_f32 v5, v136, v138, v140\n\t" \
  "v_max_f32 v5, v5, v142\n\t" \
  "s_cbranch_vccnz AR_%=\n\t" \
  "s_cmp_eq_u32 %[z], 0\n\t" \
  "s_cbranch_scc1 AZ_%=\n\t" \
  "AY_%=:\n\t" \
  "v_mfma_f32_16x16x32_bf16 v[128:131], v[160:163], v[64:67], v[56:59]\n\t" \
  "v_max_f32 v144, v144, v145\n\t" \
  "v_max_f32 v146, v146, v147\n\t" \
  "v_max_f32 v148, v148, v149\n\t" \
  "v_max_f32 v150, v150, v151\n\t" \
  "v_max3_f32 v6, v144, v146, v148\n\t" \
  "v_max_f32 v6, v6, v150\n\t" \
  "v_max_f32 v152, v152, v153\n\t" \
  "v_max_f32 v154, v154, v155\n\t" \
  "v_max_f32 v156, v156, v157\n\t" \
  "v_max_f32 v158, v158, v159\n\t" \
  "v_max3_f32 v7, v152, v154, v156\n\t" \
  "v_max_f32 v7, v7, v158\n\t" \
  "v_mov_b32 v8, 0xc0c0100\n\t" \
  "v_mov_b32 v9, 0x1000c0c\n\t" \
  "v_max_f32 v128, v128, v129\n\t" \
  "v_max_f32 v130, v130, v131\n\t" \
  "v_max_f32 v132, v132, v133\n\t" \
  "v_max_f32 v134, v134, v135\n\t" \
  "v_max3_f32 v4, v128, v130, v132\n\t" \
  "v_max_f32 v4, v4, v134\n\t" \
  "v_cndmask_b32_e64 v60, v8, v9, s[84:85]\n\t" \
  "v_mov_b32 v8, 0xc0c0302\n\t" \
  "v_permlane16_swap_b32 v6, v7\n\t" \
  "v_permlane16_swap_b32 v4, v5\n\t" \
  "v_max_f32 v6, v6, v7\n\t" \
  "v_max_f32 v4, v4, v5\n\t" \
  "v_mov_b32 v9, 0x3020c0c\n\t" \
  "v_cndmask_b32_e64 v61, v8, v9, s[84:85]\n\t" \
  "v_permlane32_swap_b32 v4, v6\n\t" \
  "v_max_f32 v4, v4, v6\n\t" \
  "v_mul_f32 v6, 0xbe8293ee, v4\n\t" \
  "v_mul_f32 v4, 0xbe8293ee, v4\n\t" \
  "s_nop 1\n\t" \
  "v_permlane32_swap_b32 v4, v6\n\t" \
  "v_mov_b32 v5, v4\n\t" \
  "v_mov_b32 v7, v6\n\t" \
  "s_nop 0\n\t" \
  "v_permlane16_swap_b32 v4, v5\n\t" \
  "v_permlane16_swap_b32 v6, v7\n\t" \
  "v_fmamk_f32 v128, v128, 0x3e8293ee, v4\n\t" \
  "v_fmamk_f32 v130, v130, 0x3e8293ee, v4\n\t" \
  "v_fmamk_f32 v132, v132, 0x3e8293ee, v4\n\t" \
  "v_fmamk_f32 v134, v134, 0x3e8293ee, v4\n\t" \
  "v_fmamk_f32 v136, v136, 0x3e8293ee, v5\n\t" \
  "v_fmamk_f32 v138, v138, 0x3e8293ee, v5\n\t" \
  "v_fmamk_f32 v140, v140, 0x3e8293ee, v5\n\t" \
  "v_fmamk_f32 v142, v142, 0x3e8293ee, v5\n\t" \
  "v_fmamk_f32 v144, v144, 0x3e8293ee, v6\n\t" \
  "v_fmamk_f32 v146, v146, 0x3e8293ee, v6\n\t" \
  "v_fmamk_f32 v148, v148, 0x3e8293ee, v6\n\t" \
  "v_fmamk_f32 v150, v150, 0x3e8293ee, v6\n\t" \
  "v_fmamk_f32 v152, v152, 0x3e8293ee, v7\n\t" \
  "v_fmamk_f32 v154, v154, 0x3e8293ee, v7\n\t" \
  "v_fmamk_f32 v156, v156, 0x3e8293ee, v7\n\t" \
  "v_fmamk_f32 v158, v158, 0x3e8293ee, v7\n\t" \
  "v_exp_f32 v128, v128\n\t" \
  "v_exp_f32 v130, v130\n\t" \
  "v_exp_f32 v132, v132\n\t" \
  "v_exp_f32 v134, v134\n\t" \
  "v_exp_f32 v136, v136\n\t" \
  "v_exp_f32 v138, v138\n\t" \
  "v_exp_f32 v140, v140\n\t" \
  "v_exp_f32 v142, v142\n\t" \
  "v_exp_f32 v144, v144\n\t" \
  "v_exp_f32 v146, v146\n\t" \
  "v_exp_f32 v148, v148\n\t" \
  "v_exp_f32 v150, v150\n\t" \
  "v_exp_f32 v152, v152\n\t" \
  "v_exp_f32 v154, v154\n\t" \
  "v_exp_f32 v156, v156\n\t" \
  "v_exp_f32 v158, v158\n\t" \
  "v_add_f32 v4, v128, v130\n\t" \
  "v_add_f32 v8, v132, v134\n\t" \
  "v_add_f32 v5, v136, v138\n\t" \
  "v_add_f32 v9, v140, v142\n\t" \
  "v_add_f32 v6, v144, v146\n\t" \
  "v_add_f32 v10, v148, v150\n\t" \
  "v_add_f32 v7, v152, v154\n\t" \
  "v_add_f32 v11, v156, v158\n\t" \
  "v_add_f32 v4, v4, v8\n\t" \
  "v_add_f32 v5, v5, v9\n\t" \
  "v_add_f32 v6, v6, v10\n\t" \
  "v_add_f32 v7, v7, v11\n\t" \
  "v_permlane16_swap_b32 v4, v5\n\t" \
  "s_nop 0\n\t" \
  "v_permlane16_swap_b32 v6, v7\n\t" \
  "v_add_f32 v4, v4, v5\n\t" \
  "v_add_f32 v6, v6, v7\n\t" \
  "s_nop 1\n\t" \
  "v_permlane32_swap_b32 v4, v6\n\t" \
  "v_add_f32 v4, v4, v6\n\t" \
  "v_rcp_f32 v4, v4\n\t" \
  "s_nop 0\n\t" \
  "v_mov_b32 v6, v4\n\t" \
  "s_nop 1\n\t" \
  "v_permlane32_swap_b32 v4, v6\n\t" \
  "v_mov_b32 v5, v4\n\t" \
  "v_mov_b32 v7, v6\n\t" \
  "s_nop 0\n\t" \
  "v_permlane16_swap_b32 v4, v5\n\t" \
  "v_permlane16_swap_b32 v6, v7\n\t" \
  "v_mul_f32 v128, v128, v4\n\t" \
  "v_mul_f32 v130, v130, v4\n\t" \
  "v_mul_f32 v132, v132, v4\n\t" \
  "v_mul_f32 v134, v134, v4\n\t" \
  "v_cvt_pk_bf16_f32 v128, v128, v130\n\t" \
  "v_cvt_pk_bf16_f32 v132, v132, v134\n\t" \
  "v_perm_b32 v68, 0, v128, v60\n\t" \
  "v_perm_b32 v69, 0, v128, v61\n\t" \
  "v_perm_b32 v70, 0, v132, v60\n\t" \
  "v_perm_b32 v71, 0, v132, v61\n\t" \
  "v_mul_f32 v136, v136, v5\n\t" \
  "v_mul_f32 v138, v138, v5\n\t" \
  "v_mfma_f32_16x16x32_bf16 v[128:131], v[20:23], v[68:71], 0\n\t" \
  "v_mul_f32 v140, v140, v5\n\t" \
  "v_mul_f32 v142, v142, v5\n\t" \
  "v_cvt_pk_bf16_f32 v136, v136, v138\n\t" \
  "v_mfma_f32_16x16x32_bf16 v[132:135], v[30:33], v[68:71], 0\n\t" \
  "v_cvt_pk_bf16_f32 v140, v140, v142\n\t" \
  "v_perm_b32 v76, 0, v136, v60\n\t" \
  "v_perm_b32 v77, 0, v136, v61\n\t" \
  "v_perm_b32 v78, 0, v140, v60\n\t" \
  "v_perm_b32 v79, 0, v140, v61\n\t" \
  "v_mul_f32 v144, v144, v6\n\t" \
  "v_mul_f32 v146, v146, v6\n\t" \
  "v_mfma_f32_16x16x32_bf16 v[136:139], v[22:25], v[76:79], 0\n\t" \
  "v_mul_f32 v148, v148, v6\n\t" \
  "v_mul_f32 v150, v150, v6\n\t" \
  "v_cvt_pk_bf16_f32 v144, v144, v146\n\t" \
  "v_mfma_f32_16x16x32_bf16 v[140:143], v[32:35], v[76:79], 0\n\t" \
  "v_cvt_pk_bf16_f32 v148, v148, v150\n\t" \
  "v_perm_b32 v84, 0, v144, v60\n\t" \
  "v_perm_b32 v85, 0, v144, v61\n\t" \
  "v_perm_b32 v86, 0, v148, v60\n\t" \
  "v_perm_b32 v87, 0, v148, v61\n\t" \
  "v_cvt_pk_bf16_f32 v100, v128, v129\n\t" \
  "v_cvt_pk_bf16_f32 v101, v130, v131\n\t" \
  "v_mfma_f32_16x16x32_bf16 v[144:147], v[24:27], v[84:87], 0\n\t" \
  "v_cvt_pk_bf16_f32 v102, v132, v133\n\t" \
  "v_cvt_pk_bf16_f32 v103, v134, v135\n\t" \
  "global_store_dwordx4 v62, v[100:103], %[ot] offset:0 sc1\n\t" \
  "v_mfma_f32_16x16x32_bf16 v[148:151], v[34:37], v[84:87], 0\n\t" \
  "v_mul_f32 v152, v152, v7\n\t" \
  "v_mul_f32 v154, v154, v7\n\t" \
  "v_mul_f32 v156, v156, v7\n\t" \
  "v_mul_f32 v158, v158, v7\n\t" \
  "v_cvt_pk_bf16_f32 v152, v152, v154\n\t" \
  "v_cvt_pk_bf16_f32 v156, v156, v158\n\t" \
  "v_perm_b32 v92, 0, v152, v60\n\t" \
  "v_perm_b32 v93, 0, v152, v61\n\t" \
  "v_perm_b32 v94, 0, v156, v60\n\t" \
  "v_perm_b32 v95, 0, v156, v61\n\t" \
  "v_cvt_pk_bf16_f32 v108, v136, v137\n\t" \
  "v_cvt_pk_bf16_f32 v109, v138, v139\n\t" \
  "v_mfma_f32_16x16x32_bf16 v[152:155], v[26:29], v[92:95], 0\n\t" \
  "v_cvt_pk_bf16_f32 v110, v140, v141\n\t" \
  "v_cvt_pk_bf16_f32 v111, v142, v143\n\t" \
  "global_store_dwordx4 v62, v[108:111], %[ot] offset:1024 sc1\n\t" \
  "v_mfma_f32_16x16x32_bf16 v[156:159], v[36:39], v[92:95], 0\n\t" \
  "v_cvt_pk_bf16_f32 v116, v144, v145\n\t" \
  "v_cvt_pk_bf16_f32 v117, v146, v147\n\t" \
  "v_cvt_pk_bf16_f32 v118, v148, v149\n\t" \
  "v_cvt_pk_bf16_f32 v119, v150, v151\n\t" \
  "global_store_dwordx4 v62, v[116:119], %[ou] offset:0 sc1\n\t" \
  "v_cvt_pk_bf16_f32 v124, v152, v153\n\t" \
  "v_cvt_pk_bf16_f32 v125, v154, v155\n\t" \
  "s_nop 0\n\t" \
  "v_cvt_pk_bf16_f32 v126, v156, v157\n\t" \
  "v_cvt_pk_bf16_f32 v127, v158, v159\n\t" \
  "global_store_dwordx4 v62, v[124:127], %[ou] offset:1024 sc1\n\t" \
  "s_branch AE_%=\n\t" \
  "AR_%=:\n\t" \
  "s_addk_i32 %[it], 1\n\t" \
  "s_cmp_gt_u32 %[it], 0x400000\n\t" \
  "s_cbranch_scc1 AY_%=\n\t" \
  "ds_read_b32 v0, v61\n\t" \
  "ds_read_b64 v[20:21], v60\n\t" \
  "ds_read_b64 v[30:31], v60 offset:8\n\t" \
  "s_mov_b64 exec, %[rm]\n\t" \
  "ds_read_b128 v[160:163], v63\n\t" \
  "s_mov_b64 exec, -1\n\t" \
  "s_branch AW_%=\n\t" \
  "AZ_%=:\n\t" \
  "v_mov_b32 v0, 0xff61b1e6\n\t" \
  "v_mov_b32 v1, 0xcfa8965f\n\t" \
  "s_nop 3\n\t" \
  "v_cndmask_b32_e64 v56, v0, v1, s[68:69]\n\t" \
  "v_cndmask_b32_e64 v57, v0, v1, s[70:71]\n\t" \
  "v_cndmask_b32_e64 v58, v0, v1, s[72:73]\n\t" \
  "v_cndmask_b32_e64 v59, v0, v1, s[74:75]\n\t" \
  "s_nop 1\n\t" \
  "s_branch AY_%=\n\t" \
  "AE_%=:\n\t" \
  "s_nop 1\n\t"
#define N2_CLOB "v0", "v1", "v2", "v3", "v4", "v5", "v6", "v7", "v8", "v9", "v10", "v11", "v12", "v13", "v14", "v15", "v16", "v17", "v18", "v19", "v20", "v21", "v22", "v23", "v24", "v25", "v26", "v27", "v28", "v29", "v30", "v31", "v32", "v33", "v34", "v35", "v36", "v37", "v38", "v39", "v40", "v41", "v42", "v43", "v44", "v45", "v46", "v47", "v48", "v49", "v50", "v51", "v52", "v53", "v54", "v55", "v60", "v61", "v64", "v65", "v66", "v67", "v68", "v69", "v70", "v71", "v72", "v73", "v74", "v75", "v76", "v77", "v78", "v79", "v80", "v81", "v82", "v83", "v84", "v85", "v86", "v87", "v88", "v89", "v90", "v91", "v92", "v93", "v94", "v95", "v96", "v97", "v98", "v99", "v100", "v101", "v102", "v103", "v104", "v105", "v106", "v107", "v108", "v109", "v110", "v111", "v112", "v113", "v114", "v115", "v116", "v117", "v118", "v119", "v120", "v121", "v122", "v123", "v124", "v125", "v126", "v127", "v128", "v129", "v130", "v131", "v132", "v133", "v134", "v135", "v136", "v137", "v138", "v139", "v140", "v141", "v142", "v143", "v144", "v145", "v146", "v147", "v148", "v149", "v150", "v151", "v152", "v153", "v154", "v155", "v156", "v157", "v158", "v159", "v160", "v161", "v162", "v163", "v164", "v165", "v166", "v167", "s68", "s69", "s70", "s71", "s72", "s73", "s74", "s75", "s76", "s77", "s78", "s79", "s80", "s81", "s82", "s83", "s84", "s85"

#define CV_PRE 0
#define CV_Y 1
#define CV_ASM \
  "s_mov_b64 vcc, 1\n\t" \
  "ds_read_b64 v[0:1], v28 offset:0\n\t" \
  "ds_read_b64 v[2:3], v29 offset:0\n\t" \
  "ds_read_b64 v[4:5], v30 offset:1024\n\t" \
  "ds_read_b64 v[6:7], v31 offset:1024\n\t" \
  "ds_read_b64 v[8:9], v24 offset:2048\n\t" \
  "ds_read_b64 v[10:11], v25 offset:2048\n\t" \
  "ds_read_b64 v[12:13], v26 offset:3072\n\t" \
  "ds_read_b64 v[14:15], v27 offset:3072\n\t" \
  "ds_read_b64 v[16:17], v28 offset:4096\n\t" \
  "ds_read_b64 v[18:19], v29 offset:4096\n\t" \
  "ds_read_b64 v[20:21], v30 offset:5120\n\t" \
  "ds_read_b64 v[22:23], v31 offset:5120\n\t" \
  "s_waitcnt lgkmcnt(0)\n\t" \
  "v_mfma_f32_16x16x32_bf16 v[128:131], v[68:71], v[0:3], 0\n\t" \
  "v_mfma_f32_16x16x32_bf16 v[128:131], v[64:67], v[4:7], v[128:131]\n\t" \
  "v_mfma_f32_16x16x32_bf16 v[132:135], v[76:79], v[0:3], 0\n\t" \
  "v_mfma_f32_16x16x32_bf16 v[132:135], v[72:75], v[4:7], v[132:135]\n\t" \
  "v_mfma_f32_16x16x32_bf16 v[136:139], v[84:87], v[0:3], 0\n\t" \
  "v_mfma_f32_16x16x32_bf16 v[136:139], v[80:83], v[4:7], v[136:139]\n\t" \
  "v_mfma_f32_16x16x32_bf16 v[140:143], v[92:95], v[0:3], 0\n\t" \
  "v_mfma_f32_16x16x32_bf16 v[140:143], v[88:91], v[4:7], v[140:143]\n\t" \
  "v_mfma_f32_16x16x32_bf16 v[144:147], v[100:103], v[0:3], 0\n\t" \
  "v_mfma_f32_16x16x32_bf16 v[144:147], v[96:99], v[4:7], v[144:147]\n\t" \
  "ds_bpermute_b32 v34, v32, v131\n\t" \
  "v_mfma_f32_16x16x32_bf16 v[148:151], v[108:111], v[0:3], 0\n\t" \
  "v_mov_b32 v131, v130\n\t" \
  "v_mov_b32 v130, v129\n\t" \
  "v_mfma_f32_16x16x32_bf16 v[148:151], v[104:107], v[4:7], v[148:151]\n\t" \
  "v_mov_b32 v129, v128\n\t" \
  "v_mfma_f32_16x16x32_bf16 v[152:155], v[116:119], v[0:3], 0\n\t" \
  "v_mfma_f32_16x16x32_bf16 v[152:155], v[112:115], v[4:7], v[152:155]\n\t" \
  "v_mfma_f32_16x16x32_bf16 v[156:159], v[124:127], v[0:3], 0\n\t" \
  "ds_read_b64 v[0:1], v24 offset:6144\n\t" \
  "ds_read_b64 v[2:3], v25 offset:6144\n\t" \
  "v_mfma_f32_16x16x32_bf16 v[156:159], v[120:123], v[4:7], v[156:159]\n\t" \
  "ds_read_b64 v[4:5], v26 offset:7168\n\t" \
  "ds_read_b64 v[6:7], v27 offset:7168\n\t" \
  "v_mfma_f32_16x16x32_bf16 v[132:135], v[68:71], v[8:11], v[132:135]\n\t" \
  "v_mfma_f32_16x16x32_bf16 v[132:135], v[64:67], v[12:15], v[132:135]\n\t" \
  "v_mfma_f32_16x16x32_bf16 v[136:139], v[76:79], v[8:11], v[136:139]\n\t" \
  "v_mfma_f32_16x16x32_bf16 v[136:139], v[72:75], v[12:15], v[136:139]\n\t" \
  "v_mfma_f32_16x16x32_bf16 v[140:143], v[84:87], v[8:11], v[140:143]\n\t" \
  "s_waitcnt lgkmcnt(4)\n\t" \
  "v_cndmask_b32_e64 v128, v34, 0, vcc\n\t" \
  "v_mfma_f32_16x16x32_bf16 v[140:143], v[80:83], v[12:15], v[140:143]\n\t" \
  "v_mfma_f32_16x16x32_bf16 v[144:147], v[92:95], v[8:11], v[144:147]\n\t" \
  "v_mfma_f32_16x16x32_bf16 v[144:147], v[88:91], v[12:15], v[144:147]\n\t" \
  "ds_bpermute_b32 v35, v32, v135\n\t" \
  "v_mfma_f32_16x16x32_bf16 v[148:151], v[100:103], v[8:11], v[148:151]\n\t" \
  "v_mov_b32 v135, v134\n\t" \
  "v_mov_b32 v134, v133\n\t" \
  "v_mfma_f32_16x16x32_bf16 v[148:151], v[96:99], v[12:15], v[148:151]\n\t" \
  "v_mov_b32 v133, v132\n\t" \
  "v_mfma_f32_16x16x32_bf16 v[152:155], v[108:111], v[8:11], v[152:155]\n\t" \
  "v_mfma_f32_16x16x32_bf16 v[152:155], v[104:107], v[12:15], v[152:155]\n\t" \
  "v_mfma_f32_16x16x32_bf16 v[156:159], v[116:119], v[8:11], v[156:159]\n\t" \
  "ds_read_b64 v[8:9], v28 offset:8192\n\t" \
  "ds_read_b64 v[10:11], v29 offset:8192\n\t" \
  "v_mfma_f32_16x16x32_bf16 v[156:159], v[112:115], v[12:15], v[156:159]\n\t" \
  "ds_read_b64 v[12:13], v30 offset:9216\n\t" \
  "ds_read_b64 v[14:15], v31 offset:9216\n\t" \
  "v_mfma_f32_16x16x32_bf16 v[136:139], v[68:71], v[16:19], v[136:139]\n\t" \
  "v_mfma_f32_16x16x32_bf16 v[136:139], v[64:67], v[20:23], v[136:139]\n\t" \
  "v_mfma_f32_16x16x32_bf16 v[140:143], v[76:79], v[16:19], v[140:143]\n\t" \
  "v_mfma_f32_16x16x32_bf16 v[140:143], v[72:75], v[20:23], v[140:143]\n\t" \
  "v_mfma_f32_16x16x32_bf16 v[144:147], v[84:87], v[16:19], v[144:147]\n\t" \
  "s_waitcnt lgkmcnt(4)\n\t" \
  "v_cndmask_b32_e64 v132, v35, v34, vcc\n\t" \
  "v_mfma_f32_16x16x32_bf16 v[144:147], v[80:83], v[20:23], v[144:147]\n\t" \
  "v_mfma_f32_16x16x32_bf16 v[148:151], v[92:95], v[16:19], v[148:151]\n\t" \
  "v_mfma_f32_16x16x32_bf16 v[148:151], v[88:91], v[20:23], v[148:151]\n\t" \
  "ds_bpermute_b32 v36, v32, v139\n\t" \
  "v_mfma_f32_16x16x32_bf16 v[152:155], v[100:103], v[16:19], v[152:155]\n\t" \
  "v_mov_b32 v139, v138\n\t" \
  "v_mov_b32 v138, v137\n\t" \
  "v_mfma_f32_16x16x32_bf16 v[152:155], v[96:99], v[20:23], v[152:155]\n\t" \
  "v_mov_b32 v137, v136\n\t" \
  "v_mfma_f32_16x16x32_bf16 v[156:159], v[108:111], v[16:19], v[156:159]\n\t" \
  "ds_read_b64 v[16:17], v24 offset:10240\n\t" \
  "ds_read_b64 v[18:19], v25 offset:10240\n\t" \
  "v_mfma_f32_16x16x32_bf16 v[156:159], v[104:107], v[20:23], v[156:159]\n\t" \
  "ds_read_b64 v[20:21], v26 offset:11264\n\t" \
  "ds_read_b64 v[22:23], v27 offset:11264\n\t" \
  "v_mfma_f32_16x16x32_bf16 v[140:143], v[68:71], v[0:3], v[140:143]\n\t" \
  "v_mfma_f32_16x16x32_bf16 v[140:143], v[64:67], v[4:7], v[140:143]\n\t" \
  "v_mfma_f32_16x16x32_bf16 v[144:147], v[76:79], v[0:3], v[144:147]\n\t" \
  "v_mfma_f32_16x16x32_bf16 v[144:147], v[72:75], v[4:7], v[144:147]\n\t" \
  "v_mfma_f32_16x16x32_bf16 v[148:151], v[84:87], v[0:3], v[148:151]\n\t" \
  "v_mfma_f32_16x16x32_bf16 v[148:151], v[80:83], v[4:7], v[148:151]\n\t" \
  "v_mfma_f32_16x16x32_bf16 v[152:155], v[92:95], v[0:3], v[152:155]\n\t" \
  "s_waitcnt lgkmcnt(4)\n\t" \
  "v_cndmask_b32_e64 v136, v36, v35, vcc\n\t" \
  "v_mfma_f32_16x16x32_bf16 v[152:155], v[88:91], v[4:7], v[152:155]\n\t" \
  "ds_bpermute_b32 v37, v32, v143\n\t" \
  "v_mfma_f32_16x16x32_bf16 v[156:159], v[100:103], v[0:3], v[156:159]\n\t" \
  "ds_read_b64 v[0:1], v28 offset:12288\n\t" \
  "ds_read_b64 v[2:3], v29 offset:12288\n\t" \
  "v_mfma_f32_16x16x32_bf16 v[156:159], v[96:99], v[4:7], v[156:159]\n\t" \
  "ds_read_b64 v[4:5], v30 offset:13312\n\t" \
  "ds_read_b64 v[6:7], v31 offset:13312\n\t" \
  "v_mfma_f32_16x16x32_bf16 v[144:147], v[68:71], v[8:11], v[144:147]\n\t" \
  "v_mov_b32 v143, v142\n\t" \
  "v_mov_b32 v142, v141\n\t" \
  "v_mfma_f32_16x16x32_bf16 v[144:147], v[64:67], v[12:15], v[144:147]\n\t" \
  "v_mov_b32 v141, v140\n\t" \
  "v_mfma_f32_16x16x32_bf16 v[148:151], v[76:79], v[8:11], v[148:151]\n\t" \
  "v_mfma_f32_16x16x32_bf16 v[148:151], v[72:75], v[12:15], v[148:151]\n\t" \
  "v_mfma_f32_16x16x32_bf16 v[152:155], v[84:87], v[8:11], v[152:155]\n\t" \
  "v_mfma_f32_16x16x32_bf16 v[152:155], v[80:83], v[12:15], v[152:155]\n\t" \
  "v_mfma_f32_16x16x32_bf16 v[156:159], v[92:95], v[8:11], v[156:159]\n\t" \
  "ds_read_b64 v[8:9], v24 offset:14336\n\t" \
  "ds_read_b64 v[10:11], v25 offset:14336\n\t" \
  "v_mfma_f32_16x16x32_bf16 v[156:159], v[88:91], v[12:15], v[156:159]\n\t" \
  "ds_read_b64 v[12:13], v26 offset:15360\n\t" \
  "s_waitcnt lgkmcnt(8)\n\t" \
  "v_mfma_f32_16x16x32_bf16 v[148:151], v[68:71], v[16:19], v[148:151]\n\t" \
  "ds_read_b64 v[14:15], v27 offset:15360\n\t" \
  "ds_bpermute_b32 v38, v32, v147\n\t" \
  "v_mfma_f32_16x16x32_bf16 v[148:151], v[64:67], v[20:23], v[148:151]\n\t" \
  "v_mov_b32 v147, v146\n\t" \
  "v_mov_b32 v146, v145\n\t" \
  "v_mfma_f32_16x16x32_bf16 v[152:155], v[76:79], v[16:19], v[152:155]\n\t" \
  "s_waitcnt lgkmcnt(9)\n\t" \
  "v_cndmask_b32_e64 v140, v37, v36, vcc\n\t" \
  "v_mfma_f32_16x16x32_bf16 v[152:155], v[72:75], v[20:23], v[152:155]\n\t" \
  "v_mov_b32 v145, v144\n\t" \
  "v_mfma_f32_16x16x32_bf16 v[156:159], v[84:87], v[16:19], v[156:159]\n\t" \
  "ds_read_b64 v[16:17], v26 offset:0\n\t" \
  "ds_read_b64 v[18:19], v27 offset:0\n\t" \
  "v_mfma_f32_16x16x32_bf16 v[156:159], v[80:83], v[20:23], v[156:159]\n\t" \
  "ds_read_b64 v[20:21], v28 offset:1024\n\t" \
  "ds_read_b64 v[22:23], v29 offset:1024\n\t" \
  "s_waitcnt lgkmcnt(9)\n\t" \
  "v_mfma_f32_16x16x32_bf16 v[152:155], v[68:71], v[0:3], v[152:155]\n\t" \
  "ds_bpermute_b32 v39, v32, v151\n\t" \
  "v_mfma_f32_16x16x32_bf16 v[152:155], v[64:67], v[4:7], v[152:155]\n\t" \
  "v_mov_b32 v151, v150\n\t" \
  "v_mov_b32 v150, v149\n\t" \
  "v_mfma_f32_16x16x32_bf16 v[156:159], v[76:79], v[0:3], v[156:159]\n\t" \
  "ds_read_b64 v[0:1], v30 offset:2048\n\t" \
  "ds_read_b64 v[2:3], v31 offset:2048\n\t" \
  "v_mfma_f32_16x16x32_bf16 v[156:159], v[72:75], v[4:7], v[156:159]\n\t" \
  "ds_read_b64 v[4:5], v24 offset:3072\n\t" \
  "s_waitcnt lgkmcnt(11)\n\t" \
  "v_mfma_f32_16x16x32_bf16 v[156:159], v[68:71], v[8:11], v[156:159]\n\t" \
  "ds_bpermute_b32 v40, v32, v155\n\t" \
  "s_waitcnt lgkmcnt(10)\n\t" \
  "v_mfma_f32_16x16x32_bf16 v[156:159], v[64:67], v[12:15], v[156:159]\n\t" \
  "ds_read_b64 v[6:7], v25 offset:3072\n\t" \
  "s_waitcnt lgkmcnt(8)\n\t" \
  "v_mfma_f32_16x16x32_bf16 v[128:131], v[68:71], v[16:19], v[128:131]\n\t" \
  "v_cndmask_b32_e64 v144, v38, v37, vcc\n\t" \
  "s_waitcnt lgkmcnt(6)\n\t" \
  "v_mfma_f32_16x16x32_bf16 v[128:131], v[64:67], v[20:23], v[128:131]\n\t" \
  "v_mov_b32 v149, v148\n\t" \
  "v_mov_b32 v155, v154\n\t" \
  "v_mfma_f32_16x16x32_bf16 v[132:135], v[76:79], v[16:19], v[132:135]\n\t" \
  "ds_bpermute_b32 v41, v32, v159\n\t" \
  "v_mov_b32 v154, v153\n\t" \
  "v_mfma_f32_16x16x32_bf16 v[132:135], v[72:75], v[20:23], v[132:135]\n\t" \
  "v_mov_b32 v153, v152\n\t" \
  "v_mov_b32 v159, v158\n\t" \
  "v_mfma_f32_16x16x32_bf16 v[136:139], v[84:87], v[16:19], v[136:139]\n\t" \
  "v_mov_b32 v158, v157\n\t" \
  "v_mov_b32 v157, v156\n\t" \
  "v_mfma_f32_16x16x32_bf16 v[136:139], v[80:83], v[20:23], v[136:139]\n\t" \
  "s_waitcnt lgkmcnt(6)\n\t" \
  "v_cndmask_b32_e64 v148, v39, v38, vcc\n\t" \
  "v_mfma_f32_16x16x32_bf16 v[140:143], v[92:95], v[16:19], v[140:143]\n\t" \
  "global_load_dwordx4 v[44:47], v43, %[q0] offset:0\n\t" \
  "v_mfma_f32_16x16x32_bf16 v[140:143], v[88:91], v[20:23], v[140:143]\n\t" \
  "ds_read_b64 v[8:9], v26 offset:4096\n\t" \
  "ds_read_b64 v[10:11], v27 offset:4096\n\t" \
  "v_mfma_f32_16x16x32_bf16 v[144:147], v[100:103], v[16:19], v[144:147]\n\t" \
  "s_waitcnt lgkmcnt(4)\n\t" \
  "v_cndmask_b32_e64 v152, v40, v39, vcc\n\t" \
  "v_mfma_f32_16x16x32_bf16 v[144:147], v[96:99], v[20:23], v[144:147]\n\t" \
  "ds_read_b64 v[12:13], v28 offset:5120\n\t" \
  "ds_read_b64 v[14:15], v29 offset:5120\n\t" \
  "v_mfma_f32_16x16x32_bf16 v[148:151], v[108:111], v[16:19], v[148:151]\n\t" \
  "s_waitcnt lgkmcnt(4)\n\t" \
  "v_cndmask_b32_e64 v156, v41, v40, vcc\n\t" \
  "v_mfma_f32_16x16x32_bf16 v[148:151], v[104:107], v[20:23], v[148:151]\n\t" \
  "global_load_dwordx4 v[48:51], v43, %[q0] offset:2048\n\t" \
  "v_mfma_f32_16x16x32_bf16 v[152:155], v[116:119], v[16:19], v[152:155]\n\t" \
  "ds_bpermute_b32 v34, v32, v131\n\t" \
  "v_mfma_f32_16x16x32_bf16 v[152:155], v[112:115], v[20:23], v[152:155]\n\t" \
  "v_mov_b32 v131, v130\n\t" \
  "v_mov_b32 v130, v129\n\t" \
  "v_mfma_f32_16x16x32_bf16 v[156:159], v[124:127], v[16:19], v[156:159]\n\t" \
  "global_load_dwordx4 v[52:55], v43, %[q2] offset:0\n\t" \
  "v_mfma_f32_16x16x32_bf16 v[156:159], v[120:123], v[20:23], v[156:159]\n\t" \
  "ds_read_b64 v[16:17], v30 offset:6144\n\t" \
  "ds_read_b64 v[18:19], v31 offset:6144\n\t" \
  "v_mfma_f32_16x16x32_bf16 v[132:135], v[68:71], v[0:3], v[132:135]\n\t" \
  "ds_read_b64 v[20:21], v24 offset:7168\n\t" \
  "ds_read_b64 v[22:23], v25 offset:7168\n\t" \
  "v_mfma_f32_16x16x32_bf16 v[132:135], v[64:67], v[4:7], v[132:135]\n\t" \
  "v_mov_b32 v129, v128\n\t" \
  "v_mfma_f32_16x16x32_bf16 v[136:139], v[76:79], v[0:3], v[136:139]\n\t" \
  "global_load_dwordx4 v[56:59], v43, %[q2] offset:2048\n\t" \
  "v_mfma_f32_16x16x32_bf16 v[136:139], v[72:75], v[4:7], v[136:139]\n\t" \
  "v_mfma_f32_16x16x32_bf16 v[140:143], v[84:87], v[0:3], v[140:143]\n\t" \
  "v_mfma_f32_16x16x32_bf16 v[140:143], v[80:83], v[4:7], v[140:143]\n\t" \
  "v_mfma_f32_16x16x32_bf16 v[144:147], v[92:95], v[0:3], v[144:147]\n\t" \
  "v_mfma_f32_16x16x32_bf16 v[144:147], v[88:91], v[4:7], v[144:147]\n\t" \
  "ds_bpermute_b32 v35, v32, v135\n\t" \
  "v_mfma_f32_16x16x32_bf16 v[148:151], v[100:103], v[0:3], v[148:151]\n\t" \
  "v_mov_b32 v135, v134\n\t" \
  "v_mov_b32 v134, v133\n\t" \
  "v_mfma_f32_16x16x32_bf16 v[148:151], v[96:99], v[4:7], v[148:151]\n\t" \
  "s_waitcnt lgkmcnt(5)\n\t" \
  "v_cndmask_b32_e64 v128, v34, 0, vcc\n\t" \
  "v_mfma_f32_16x16x32_bf16 v[152:155], v[108:111], v[0:3], v[152:155]\n\t" \
  "v_mov_b32 v133, v132\n\t" \
  "v_mfma_f32_16x16x32_bf16 v[152:155], v[104:107], v[4:7], v[152:155]\n\t" \
  "v_mfma_f32_16x16x32_bf16 v[156:159], v[116:119], v[0:3], v[156:159]\n\t" \
  "ds_read_b64 v[0:1], v26 offset:8192\n\t" \
  "ds_read_b64 v[2:3], v27 offset:8192\n\t" \
  "v_mfma_f32_16x16x32_bf16 v[156:159], v[112:115], v[4:7], v[156:159]\n\t" \
  "ds_read_b64 v[4:5], v28 offset:9216\n\t" \
  "ds_read_b64 v[6:7], v29 offset:9216\n\t" \
  "v_mfma_f32_16x16x32_bf16 v[136:139], v[68:71], v[8:11], v[136:139]\n\t" \
  "v_mfma_f32_16x16x32_bf16 v[136:139], v[64:67], v[12:15], v[136:139]\n\t" \
  "v_mfma_f32_16x16x32_bf16 v[140:143], v[76:79], v[8:11], v[140:143]\n\t" \
  "v_mfma_f32_16x16x32_bf16 v[140:143], v[72:75], v[12:15], v[140:143]\n\t" \
  "v_mfma_f32_16x16x32_bf16 v[144:147], v[84:87], v[8:11], v[144:147]\n\t" \
  "s_waitcnt lgkmcnt(4)\n\t" \
  "v_cndmask_b32_e64 v132, v35, v34, vcc\n\t" \
  "v_mfma_f32_16x16x32_bf16 v[144:147], v[80:83], v[12:15], v[144:147]\n\t" \
  "v_mfma_f32_16x16x32_bf16 v[148:151], v[92:95], v[8:11], v[148:151]\n\t" \
  "v_mfma_f32_16x16x32_bf16 v[148:151], v[88:91], v[12:15], v[148:151]\n\t" \
  "ds_bpermute_b32 v36, v32, v139\n\t" \
  "v_mfma_f32_16x16x32_bf16 v[152:155], v[100:103], v[8:11], v[152:155]\n\t" \
  "v_mov_b32 v139, v138\n\t" \
  "v_mov_b32 v138, v137\n\t" \
  "v_mfma_f32_16x16x32_bf16 v[152:155], v[96:99], v[12:15], v[152:155]\n\t" \
  "v_mov_b32 v137, v136\n\t" \
  "v_mfma_f32_16x16x32_bf16 v[156:159], v[108:111], v[8:11], v[156:159]\n\t" \
  "ds_read_b64 v[8:9], v30 offset:10240\n\t" \
  "ds_read_b64 v[10:11], v31 offset:10240\n\t" \
  "v_mfma_f32_16x16x32_bf16 v[156:159], v[104:107], v[12:15], v[156:159]\n\t" \
  "ds_read_b64 v[12:13], v24 offset:11264\n\t" \
  "ds_read_b64 v[14:15], v25 offset:11264\n\t" \
  "v_mfma_f32_16x16x32_bf16 v[140:143], v[68:71], v[16:19], v[140:143]\n\t" \
  "v_mfma_f32_16x16x32_bf16 v[140:143], v[64:67], v[20:23], v[140:143]\n\t" \
  "v_mfma_f32_16x16x32_bf16 v[144:147], v[76:79], v[16:19], v[144:147]\n\t" \
  "v_mfma_f32_16x16x32_bf16 v[144:147], v[72:75], v[20:23], v[144:147]\n\t" \
  "v_mfma_f32_16x16x32_bf16 v[148:151], v[84:87], v[16:19], v[148:151]\n\t" \
  "v_mfma_f32_16x16x32_bf16 v[148:151], v[80:83], v[20:23], v[148:151]\n\t" \
  "v_mfma_f32_16x16x32_bf16 v[152:155], v[92:95], v[16:19], v[152:155]\n\t" \
  "s_waitcnt lgkmcnt(4)\n\t" \
  "v_cndmask_b32_e64 v136, v36, v35, vcc\n\t" \
  "v_mfma_f32_16x16x32_bf16 v[152:155], v[88:91], v[20:23], v[152:155]\n\t" \
  "ds_bpermute_b32 v37, v32, v143\n\t" \
  "v_mfma_f32_16x16x32_bf16 v[156:159], v[100:103], v[16:19], v[156:159]\n\t" \
  "ds_read_b64 v[16:17], v26 offset:12288\n\t" \
  "ds_read_b64 v[18:19], v27 offset:12288\n\t" \
  "v_mfma_f32_16x16x32_bf16 v[156:159], v[96:99], v[20:23], v[156:159]\n\t" \
  "ds_read_b64 v[20:21], v28 offset:13312\n\t" \
  "ds_read_b64 v[22:23], v29 offset:13312\n\t" \
  "v_mfma_f32_16x16x32_bf16 v[144:147], v[68:71], v[0:3], v[144:147]\n\t" \
  "v_mov_b32 v143, v142\n\t" \
  "v_mov_b32 v142, v141\n\t" \
  "v_mfma_f32_16x16x32_bf16 v[144:147], v[64:67], v[4:7], v[144:147]\n\t" \
  "v_mov_b32 v141, v140\n\t" \
  "v_mfma_f32_16x16x32_bf16 v[148:151], v[76:79], v[0:3], v[148:151]\n\t" \
  "v_mfma_f32_16x16x32_bf16 v[148:151], v[72:75], v[4:7], v[148:151]\n\t" \
  "v_mfma_f32_16x16x32_bf16 v[152:155], v[84:87], v[0:3], v[152:155]\n\t" \
  "v_mfma_f32_16x16x32_bf16 v[152:155], v[80:83], v[4:7], v[152:155]\n\t" \
  "v_mfma_f32_16x16x32_bf16 v[156:159], v[92:95], v[0:3], v[156:159]\n\t" \
  "ds_read_b64 v[0:1], v30 offset:14336\n\t" \
  "ds_read_b64 v[2:3], v31 offset:14336\n\t" \
  "v_mfma_f32_16x16x32_bf16 v[156:159], v[88:91], v[4:7], v[156:159]\n\t" \
  "ds_read_b64 v[4:5], v24 offset:15360\n\t" \
  "s_waitcnt lgkmcnt(8)\n\t" \
  "v_mfma_f32_16x16x32_bf16 v[148:151], v[68:71], v[8:11], v[148:151]\n\t" \
  "ds_read_b64 v[6:7], v25 offset:15360\n\t" \
  "ds_bpermute_b32 v38, v32, v147\n\t" \
  "v_mfma_f32_16x16x32_bf16 v[148:151], v[64:67], v[12:15], v[148:151]\n\t" \
  "v_mov_b32 v147, v146\n\t" \
  "v_mov_b32 v146, v145\n\t" \
  "v_mfma_f32_16x16x32_bf16 v[152:155], v[76:79], v[8:11], v[152:155]\n\t" \
  "s_waitcnt lgkmcnt(9)\n\t" \
  "v_cndmask_b32_e64 v140, v37, v36, vcc\n\t" \
  "v_mfma_f32_16x16x32_bf16 v[152:155], v[72:75], v[12:15], v[152:155]\n\t" \
  "v_mov_b32 v145, v144\n\t" \
  "v_mfma_f32_16x16x32_bf16 v[156:159], v[84:87], v[8:11], v[156:159]\n\t" \
  "ds_read_b64 v[8:9], v24 offset:0\n\t" \
  "ds_read_b64 v[10:11], v25 offset:0\n\t" \
  "v_mfma_f32_16x16x32_bf16 v[156:159], v[80:83], v[12:15], v[156:159]\n\t" \
  "ds_read_b64 v[12:13], v26 offset:1024\n\t" \
  "ds_read_b64 v[14:15], v27 offset:1024\n\t" \
  "s_waitcnt lgkmcnt(9)\n\t" \
  "v_mfma_f32_16x16x32_bf16 v[152:155], v[68:71], v[16:19], v[152:155]\n\t" \
  "ds_bpermute_b32 v39, v32, v151\n\t" \
  "v_mfma_f32_16x16x32_bf16 v[152:155], v[64:67], v[20:23], v[152:155]\n\t" \
  "v_mov_b32 v151, v150\n\t" \
  "v_mov_b32 v150, v149\n\t" \
  "v_mfma_f32_16x16x32_bf16 v[156:159], v[76:79], v[16:19], v[156:159]\n\t" \
  "ds_read_b64 v[16:17], v28 offset:2048\n\t" \
  "ds_read_b64 v[18:19], v29 offset:2048\n\t" \
  "v_mfma_f32_16x16x32_bf16 v[156:159], v[72:75], v[20:23], v[156:159]\n\t" \
  "ds_read_b64 v[20:21], v30 offset:3072\n\t" \
  "s_waitcnt lgkmcnt(11)\n\t" \
  "v_mfma_f32_16x16x32_bf16 v[156:159], v[68:71], v[0:3], v[156:159]\n\t" \
  "ds_bpermute_b32 v40, v32, v155\n\t" \
  "s_waitcnt lgkmcnt(10)\n\t" \
  "v_mfma_f32_16x16x32_bf16 v[156:159], v[64:67], v[4:7], v[156:159]\n\t" \
  "ds_read_b64 v[22:23], v31 offset:3072\n\t" \
  "s_waitcnt lgkmcnt(8)\n\t" \
  "v_mfma_f32_16x16x32_bf16 v[128:131], v[68:71], v[8:11], v[128:131]\n\t" \
  "v_cndmask_b32_e64 v144, v38, v37, vcc\n\t" \
  "s_waitcnt lgkmcnt(6)\n\t" \
  "v_mfma_f32_16x16x32_bf16 v[128:131], v[64:67], v[12:15], v[128:131]\n\t" \
  "v_mov_b32 v149, v148\n\t" \
  "v_mov_b32 v155, v154\n\t" \
  "v_mfma_f32_16x16x32_bf16 v[132:135], v[76:79], v[8:11], v[132:135]\n\t" \
  "ds_bpermute_b32 v41, v32, v159\n\t" \
  "v_mov_b32 v154, v153\n\t" \
  "v_mfma_f32_16x16x32_bf16 v[132:135], v[72:75], v[12:15], v[132:135]\n\t" \
  "v_mov_b32 v153, v152\n\t" \
  "v_mov_b32 v159, v158\n\t" \
  "v_mfma_f32_16x16x32_bf16 v[136:139], v[84:87], v[8:11], v[136:139]\n\t" \
  "v_mov_b32 v158, v157\n\t" \
  "v_mov_b32 v157, v156\n\t" \
  "v_mfma_f32_16x16x32_bf16 v[136:139], v[80:83], v[12:15], v[136:139]\n\t" \
  "s_waitcnt lgkmcnt(6)\n\t" \
  "v_cndmask_b32_e64 v148, v39, v38, vcc\n\t" \
  "v_mfma_f32_16x16x32_bf16 v[140:143], v[92:95], v[8:11], v[140:143]\n\t" \
  "ds_read_b64 v[0:1], v24 offset:4096\n\t" \
  "ds_read_b64 v[2:3], v25 offset:4096\n\t" \
  "v_mfma_f32_16x16x32_bf16 v[140:143], v[88:91], v[12:15], v[140:143]\n\t" \
  "ds_read_b64 v[4:5], v26 offset:5120\n\t" \
  "ds_read_b64 v[6:7], v27 offset:5120\n\t" \
  "v_mfma_f32_16x16x32_bf16 v[144:147], v[100:103], v[8:11], v[144:147]\n\t" \
  "s_waitcnt lgkmcnt(6)\n\t" \
  "v_cndmask_b32_e64 v152, v40, v39, vcc\n\t" \
  "v_mfma_f32_16x16x32_bf16 v[144:147], v[96:99], v[12:15], v[144:147]\n\t" \
  "ds_bpermute_b32 v34, v32, v131\n\t" \
  "v_mfma_f32_16x16x32_bf16 v[148:151], v[108:111], v[8:11], v[148:151]\n\t" \
  "s_waitcnt lgkmcnt(5)\n\t" \
  "v_cndmask_b32_e64 v156, v41, v40, vcc\n\t" \
  "v_mfma_f32_16x16x32_bf16 v[148:151], v[104:107], v[12:15], v[148:151]\n\t" \
  "v_mov_b32 v131, v130\n\t" \
  "v_mov_b32 v130, v129\n\t" \
  "v_mfma_f32_16x16x32_bf16 v[152:155], v[116:119], v[8:11], v[152:155]\n\t" \
  "v_mov_b32 v129, v128\n\t" \
  "v_mfma_f32_16x16x32_bf16 v[152:155], v[112:115], v[12:15], v[152:155]\n\t" \
  "v_mfma_f32_16x16x32_bf16 v[156:159], v[124:127], v[8:11], v[156:159]\n\t" \
  "ds_read_b64 v[8:9], v28 offset:6144\n\t" \
  "ds_read_b64 v[10:11], v29 offset:6144\n\t" \
  "v_mfma_f32_16x16x32_bf16 v[156:159], v[120:123], v[12:15], v[156:159]\n\t" \
  "ds_read_b64 v[12:13], v30 offset:7168\n\t" \
  "ds_read_b64 v[14:15], v31 offset:7168\n\t" \
  "v_mfma_f32_16x16x32_bf16 v[132:135], v[68:71], v[16:19], v[132:135]\n\t" \
  "v_mfma_f32_16x16x32_bf16 v[132:135], v[64:67], v[20:23], v[132:135]\n\t" \
  "v_mfma_f32_16x16x32_bf16 v[136:139], v[76:79], v[16:19], v[136:139]\n\t" \
  "v_mfma_f32_16x16x32_bf16 v[136:139], v[72:75], v[20:23], v[136:139]\n\t" \
  "v_mfma_f32_16x16x32_bf16 v[140:143], v[84:87], v[16:19], v[140:143]\n\t" \
  "s_waitcnt lgkmcnt(4)\n\t" \
  "v_cndmask_b32_e64 v128, v34, 0, vcc\n\t" \
  "v_mfma_f32_16x16x32_bf16 v[140:143], v[80:83], v[20:23], v[140:143]\n\t" \
  "v_mfma_f32_16x16x32_bf16 v[144:147], v[92:95], v[16:19], v[144:147]\n\t" \
  "v_mfma_f32_16x16x32_bf16 v[144:147], v[88:91], v[20:23], v[144:147]\n\t" \
  "ds_bpermute_b32 v35, v32, v135\n\t" \
  "v_mfma_f32_16x16x32_bf16 v[148:151], v[100:103], v[16:19], v[148:151]\n\t" \
  "v_mov_b32 v135, v134\n\t" \
  "v_mov_b32 v134, v133\n\t" \
  "v_mfma_f32_16x16x32_bf16 v[148:151], v[96:99], v[20:23], v[148:151]\n\t" \
  "v_mov_b32 v133, v132\n\t" \
  "v_mfma_f32_16x16x32_bf16 v[152:155], v[108:111], v[16:19], v[152:155]\n\t" \
  "v_mfma_f32_16x16x32_bf16 v[152:155], v[104:107], v[20:23], v[152:155]\n\t" \
  "v_mfma_f32_16x16x32_bf16 v[156:159], v[116:119], v[16:19], v[156:159]\n\t" \
  "ds_read_b64 v[16:17], v24 offset:8192\n\t" \
  "ds_read_b64 v[18:19], v25 offset:8192\n\t" \
  "v_mfma_f32_16x16x32_bf16 v[156:159], v[112:115], v[20:23], v[156:159]\n\t" \
  "ds_read_b64 v[20:21], v26 offset:9216\n\t" \
  "ds_read_b64 v[22:23], v27 offset:9216\n\t" \
  "v_mfma_f32_16x16x32_bf16 v[136:139], v[68:71], v[0:3], v[136:139]\n\t" \
  "v_mfma_f32_16x16x32_bf16 v[136:139], v[64:67], v[4:7], v[136:139]\n\t" \
  "v_mfma_f32_16x16x32_bf16 v[140:143], v[76:79], v[0:3], v[140:143]\n\t" \
  "v_mfma_f32_16x16x32_bf16 v[140:143], v[72:75], v[4:7], v[140:143]\n\t" \
  "v_mfma_f32_16x16x32_bf16 v[144:147], v[84:87], v[0:3], v[144:147]\n\t" \
  "s_waitcnt lgkmcnt(4)\n\t" \
  "v_cndmask_b32_e64 v132, v35, v34, vcc\n\t" \
  "v_mfma_f32_16x16x32_bf16 v[144:147], v[80:83], v[4:7], v[144:147]\n\t" \
  "v_mfma_f32_16x16x32_bf16 v[148:151], v[92:95], v[0:3], v[148:151]\n\t" \
  "v_mfma_f32_16x16x32_bf16 v[148:151], v[88:91], v[4:7], v[148:151]\n\t" \
  "ds_bpermute_b32 v36, v32, v139\n\t" \
  "v_mfma_f32_16x16x32_bf16 v[152:155], v[100:103], v[0:3], v[152:155]\n\t" \
  "v_mov_b32 v139, v138\n\t" \
  "v_mov_b32 v138, v137\n\t" \
  "v_mfma_f32_16x16x32_bf16 v[152:155], v[96:99], v[4:7], v[152:155]\n\t" \
  "v_mov_b32 v137, v136\n\t" \
  "v_mfma_f32_16x16x32_bf16 v[156:159], v[108:111], v[0:3], v[156:159]\n\t" \
  "ds_read_b64 v[0:1], v28 offset:10240\n\t" \
  "ds_read_b64 v[2:3], v29 offset:10240\n\t" \
  "v_mfma_f32_16x16x32_bf16 v[156:159], v[104:107], v[4:7], v[156:159]\n\t" \
  "ds_read_b64 v[4:5], v30 offset:11264\n\t" \
  "ds_read_b64 v[6:7], v31 offset:11264\n\t" \
  "v_mfma_f32_16x16x32_bf16 v[140:143], v[68:71], v[8:11], v[140:143]\n\t" \
  "v_mfma_f32_16x16x32_bf16 v[140:143], v[64:67], v[12:15], v[140:143]\n\t" \
  "v_mfma_f32_16x16x32_bf16 v[144:147], v[76:79], v[8:11], v[144:147]\n\t" \
  "v_mfma_f32_16x16x32_bf16 v[144:147], v[72:75], v[12:15], v[144:147]\n\t" \
  "v_mfma_f32_16x16x32_bf16 v[148:151], v[84:87], v[8:11], v[148:151]\n\t" \
  "v_mfma_f32_16x16x32_bf16 v[148:151], v[80:83], v[12:15], v[148:151]\n\t" \
  "v_mfma_f32_16x16x32_bf16 v[152:155], v[92:95], v[8:11], v[152:155]\n\t" \
  "s_waitcnt lgkmcnt(4)\n\t" \
  "v_cndmask_b32_e64 v136, v36, v35, vcc\n\t" \
  "v_mfma_f32_16x16x32_bf16 v[152:155], v[88:91], v[12:15], v[152:155]\n\t" \
  "ds_bpermute_b32 v37, v32, v143\n\t" \
  "v_mfma_f32_16x16x32_bf16 v[156:159], v[100:103], v[8:11], v[156:159]\n\t" \
  "ds_read_b64 v[8:9], v24 offset:12288\n\t" \
  "ds_read_b64 v[10:11], v25 offset:12288\n\t" \
  "v_mfma_f32_16x16x32_bf16 v[156:159], v[96:99], v[12:15], v[156:159]\n\t" \
  "ds_read_b64 v[12:13], v26 offset:13312\n\t" \
  "ds_read_b64 v[14:15], v27 offset:13312\n\t" \
  "v_mfma_f32_16x16x32_bf16 v[144:147], v[68:71], v[16:19], v[144:147]\n\t" \
  "v_mov_b32 v143, v142\n\t" \
  "v_mov_b32 v142, v141\n\t" \
  "v_mfma_f32_16x16x32_bf16 v[144:147], v[64:67], v[20:23], v[144:147]\n\t" \
  "v_mov_b32 v141, v140\n\t" \
  "v_mfma_f32_16x16x32_bf16 v[148:151], v[76:79], v[16:19], v[148:151]\n\t" \
  "v_mfma_f32_16x16x32_bf16 v[148:151], v[72:75], v[20:23], v[148:151]\n\t" \
  "v_mfma_f32_16x16x32_bf16 v[152:155], v[84:87], v[16:19], v[152:155]\n\t" \
  "v_mfma_f32_16x16x32_bf16 v[152:155], v[80:83], v[20:23], v[152:155]\n\t" \
  "v_mfma_f32_16x16x32_bf16 v[156:159], v[92:95], v[16:19], v[156:159]\n\t" \
  "ds_read_b64 v[16:17], v28 offset:14336\n\t" \
  "ds_read_b64 v[18:19], v29 offset:14336\n\t" \
  "v_mfma_f32_16x16x32_bf16 v[156:159], v[88:91], v[20:23], v[156:159]\n\t" \
  "ds_read_b64 v[20:21], v30 offset:15360\n\t" \
  "s_waitcnt lgkmcnt(8)\n\t" \
  "v_mfma_f32_16x16x32_bf16 v[148:151], v[68:71], v[0:3], v[148:151]\n\t" \
  "ds_read_b64 v[22:23], v31 offset:15360\n\t" \
  "ds_bpermute_b32 v38, v32, v147\n\t" \
  "v_mfma_f32_16x16x32_bf16 v[148:151], v[64:67], v[4:7], v[148:151]\n\t" \
  "v_mov_b32 v147, v146\n\t" \
  "v_mov_b32 v146, v145\n\t" \
  "v_mfma_f32_16x16x32_bf16 v[152:155], v[76:79], v[0:3], v[152:155]\n\t" \
  "s_waitcnt lgkmcnt(9)\n\t" \
  "v_cndmask_b32_e64 v140, v37, v36, vcc\n\t" \
  "v_mfma_f32_16x16x32_bf16 v[152:155], v[72:75], v[4:7], v[152:155]\n\t" \
  "v_mov_b32 v145, v144\n\t" \
  "v_mfma_f32_16x16x32_bf16 v[156:159], v[84:87], v[0:3], v[156:159]\n\t" \
  "ds_read_b64 v[0:1], v30 offset:0\n\t" \
  "ds_read_b64 v[2:3], v31 offset:0\n\t" \
  "v_mfma_f32_16x16x32_bf16 v[156:159], v[80:83], v[4:7], v[156:159]\n\t" \
  "ds_read_b64 v[4:5], v24 offset:1024\n\t" \
  "ds_read_b64 v[6:7], v25 offset:1024\n\t" \
  "s_waitcnt lgkmcnt(9)\n\t" \
  "v_mfma_f32_16x16x32_bf16 v[152:155], v[68:71], v[8:11], v[152:155]\n\t" \
  "ds_bpermute_b32 v39, v32, v151\n\t" \
  "v_mfma_f32_16x16x32_bf16 v[152:155], v[64:67], v[12:15], v[152:155]\n\t" \
  "v_mov_b32 v151, v150\n\t" \
  "v_mov_b32 v150, v149\n\t" \
  "v_mfma_f32_16x16x32_bf16 v[156:159], v[76:79], v[8:11], v[156:159]\n\t" \
  "ds_read_b64 v[8:9], v26 offset:2048\n\t" \
  "ds_read_b64 v[10:11], v27 offset:2048\n\t" \
  "v_mfma_f32_16x16x32_bf16 v[156:159], v[72:75], v[12:15], v[156:159]\n\t" \
  "ds_read_b64 v[12:13], v28 offset:3072\n\t" \
  "s_waitcnt lgkmcnt(11)\n\t" \
  "v_mfma_f32_16x16x32_bf16 v[156:159], v[68:71], v[16:19], v[156:159]\n\t" \
  "ds_bpermute_b32 v40, v32, v155\n\t" \
  "s_waitcnt lgkmcnt(10)\n\t" \
  "v_mfma_f32_16x16x32_bf16 v[156:159], v[64:67], v[20:23], v[156:159]\n\t" \
  "ds_read_b64 v[14:15], v29 offset:3072\n\t" \
  "s_waitcnt lgkmcnt(8)\n\t" \
  "v_mfma_f32_16x16x32_bf16 v[128:131], v[68:71], v[0:3], v[128:131]\n\t" \
  "v_cndmask_b32_e64 v144, v38, v37, vcc\n\t" \
  "s_waitcnt lgkmcnt(6)\n\t" \
  "v_mfma_f32_16x16x32_bf16 v[128:131], v[64:67], v[4:7], v[128:131]\n\t" \
  "v_mov_b32 v149, v148\n\t" \
  "v_mov_b32 v155, v154\n\t" \
  "v_mfma_f32_16x16x32_bf16 v[132:135], v[76:79], v[0:3], v[132:135]\n\t" \
  "ds_bpermute_b32 v41, v32, v159\n\t" \
  "v_mov_b32 v154, v153\n\t" \
  "v_mfma_f32_16x16x32_bf16 v[132:135], v[72:75], v[4:7], v[132:135]\n\t" \
  "v_mov_b32 v153, v152\n\t" \
  "v_mov_b32 v159, v158\n\t" \
  "v_mfma_f32_16x16x32_bf16 v[136:139], v[84:87], v[0:3], v[136:139]\n\t" \
  "v_mov_b32 v158, v157\n\t" \
  "v_mov_b32 v157, v156\n\t" \
  "v_mfma_f32_16x16x32_bf16 v[136:139], v[80:83], v[4:7], v[136:139]\n\t" \
  "s_waitcnt lgkmcnt(6)\n\t" \
  "v_cndmask_b32_e64 v148, v39, v38, vcc\n\t" \
  "v_mfma_f32_16x16x32_bf16 v[140:143], v[92:95], v[0:3], v[140:143]\n\t" \
  "ds_read_b64 v[16:17], v30 offset:4096\n\t" \
  "ds_read_b64 v[18:19], v31 offset:4096\n\t" \
  "v_mfma_f32_16x16x32_bf16 v[140:143], v[88:91], v[4:7], v[140:143]\n\t" \
  "ds_read_b64 v[20:21], v24 offset:5120\n\t" \
  "ds_read_b64 v[22:23], v25 offset:5120\n\t" \
  "v_mfma_f32_16x16x32_bf16 v[144:147], v[100:103], v[0:3], v[144:147]\n\t" \
  "s_waitcnt lgkmcnt(6)\n\t" \
  "v_cndmask_b32_e64 v152, v40, v39, vcc\n\t" \
  "v_mfma_f32_16x16x32_bf16 v[144:147], v[96:99], v[4:7], v[144:147]\n\t" \
  "s_mov_b64 exec, 1\n\t" \
  "ds_write_b32 v33, v33\n\t" \
  "s_mov_b64 exec, -1\n\t" \
  "v_mfma_f32_16x16x32_bf16 v[148:151], v[108:111], v[0:3], v[148:151]\n\t" \
  "s_waitcnt lgkmcnt(5)\n\t" \
  "v_cndmask_b32_e64 v156, v41, v40, vcc\n\t" \
  "v_mfma_f32_16x16x32_bf16 v[148:151], v[104:107], v[4:7], v[148:151]\n\t" \
  "v_cvt_pk_bf16_f32 v34, v128, v129\n\t" \
  "v_cvt_pk_bf16_f32 v35, v130, v131\n\t" \
  "v_mfma_f32_16x16x32_bf16 v[152:155], v[116:119], v[0:3], v[152:155]\n\t" \
  "v_mfma_f32_16x16x32_bf16 v[152:155], v[112:115], v[4:7], v[152:155]\n\t" \
  "v_mfma_f32_16x16x32_bf16 v[156:159], v[124:127], v[0:3], v[156:159]\n\t" \
  "ds_read_b64 v[0:1], v26 offset:6144\n\t" \
  "ds_read_b64 v[2:3], v27 offset:6144\n\t" \
  "v_mfma_f32_16x16x32_bf16 v[156:159], v[120:123], v[4:7], v[156:159]\n\t" \
  "ds_read_b64 v[4:5], v28 offset:7168\n\t" \
  "ds_read_b64 v[6:7], v29 offset:7168\n\t" \
  "v_mfma_f32_16x16x32_bf16 v[132:135], v[68:71], v[8:11], v[132:135]\n\t" \
  "v_mfma_f32_16x16x32_bf16 v[132:135], v[64:67], v[12:15], v[132:135]\n\t" \
  "v_mfma_f32_16x16x32_bf16 v[136:139], v[76:79], v[8:11], v[136:139]\n\t" \
  "v_mfma_f32_16x16x32_bf16 v[136:139], v[72:75], v[12:15], v[136:139]\n\t" \
  "v_mfma_f32_16x16x32_bf16 v[140:143], v[84:87], v[8:11], v[140:143]\n\t" \
  "v_mfma_f32_16x16x32_bf16 v[140:143], v[80:83], v[12:15], v[140:143]\n\t" \
  "v_mfma_f32_16x16x32_bf16 v[144:147], v[92:95], v[8:11], v[144:147]\n\t" \
  "v_mfma_f32_16x16x32_bf16 v[144:147], v[88:91], v[12:15], v[144:147]\n\t" \
  "v_mfma_f32_16x16x32_bf16 v[148:151], v[100:103], v[8:11], v[148:151]\n\t" \
  "v_mfma_f32_16x16x32_bf16 v[148:151], v[96:99], v[12:15], v[148:151]\n\t" \
  "v_cvt_pk_bf16_f32 v36, v132, v133\n\t" \
  "v_cvt_pk_bf16_f32 v37, v134, v135\n\t" \
  "v_mfma_f32_16x16x32_bf16 v[152:155], v[108:111], v[8:11], v[152:155]\n\t" \
  "v_mfma_f32_16x16x32_bf16 v[152:155], v[104:107], v[12:15], v[152:155]\n\t" \
  "v_permlane16_swap_b32 v34, v36\n\t" \
  "v_permlane16_swap_b32 v35, v37\n\t" \
  "v_mfma_f32_16x16x32_bf16 v[156:159], v[116:119], v[8:11], v[156:159]\n\t" \
  "global_store_dwordx4 v42, v[34:37], %[yb] offset:0 sc1\n\t" \
  "v_mfma_f32_16x16x32_bf16 v[156:159], v[112:115], v[12:15], v[156:159]\n\t" \
  "ds_read_b64 v[8:9], v30 offset:8192\n\t" \
  "s_waitcnt lgkmcnt(1)\n\t" \
  "v_mfma_f32_16x16x32_bf16 v[136:139], v[68:71], v[16:19], v[136:139]\n\t" \
  "ds_read_b64 v[10:11], v31 offset:8192\n\t" \
  "ds_read_b64 v[12:13], v24 offset:9216\n\t" \
  "v_mfma_f32_16x16x32_bf16 v[136:139], v[64:67], v[20:23], v[136:139]\n\t" \
  "ds_read_b64 v[14:15], v25 offset:9216\n\t" \
  "v_mfma_f32_16x16x32_bf16 v[140:143], v[76:79], v[16:19], v[140:143]\n\t" \
  "v_mfma_f32_16x16x32_bf16 v[140:143], v[72:75], v[20:23], v[140:143]\n\t" \
  "v_mfma_f32_16x16x32_bf16 v[144:147], v[84:87], v[16:19], v[144:147]\n\t" \
  "v_mfma_f32_16x16x32_bf16 v[144:147], v[80:83], v[20:23], v[144:147]\n\t" \
  "v_mfma_f32_16x16x32_bf16 v[148:151], v[92:95], v[16:19], v[148:151]\n\t" \
  "v_mfma_f32_16x16x32_bf16 v[148:151], v[88:91], v[20:23], v[148:151]\n\t" \
  "v_mfma_f32_16x16x32_bf16 v[152:155], v[100:103], v[16:19], v[152:155]\n\t" \
  "v_cvt_pk_bf16_f32 v38, v136, v137\n\t" \
  "v_cvt_pk_bf16_f32 v39, v138, v139\n\t" \
  "v_mfma_f32_16x16x32_bf16 v[152:155], v[96:99], v[20:23], v[152:155]\n\t" \
  "v_mfma_f32_16x16x32_bf16 v[156:159], v[108:111], v[16:19], v[156:159]\n\t" \
  "ds_read_b64 v[16:17], v26 offset:10240\n\t" \
  "ds_read_b64 v[18:19], v27 offset:10240\n\t" \
  "v_mfma_f32_16x16x32_bf16 v[156:159], v[104:107], v[20:23], v[156:159]\n\t" \
  "ds_read_b64 v[20:21], v28 offset:11264\n\t" \
  "ds_read_b64 v[22:23], v29 offset:11264\n\t" \
  "v_mfma_f32_16x16x32_bf16 v[140:143], v[68:71], v[0:3], v[140:143]\n\t" \
  "v_mfma_f32_16x16x32_bf16 v[140:143], v[64:67], v[4:7], v[140:143]\n\t" \
  "v_mfma_f32_16x16x32_bf16 v[144:147], v[76:79], v[0:3], v[144:147]\n\t" \
  "v_mfma_f32_16x16x32_bf16 v[144:147], v[72:75], v[4:7], v[144:147]\n\t" \
  "v_mfma_f32_16x16x32_bf16 v[148:151], v[84:87], v[0:3], v[148:151]\n\t" \
  "v_mfma_f32_16x16x32_bf16 v[148:151], v[80:83], v[4:7], v[148:151]\n\t" \
  "v_mfma_f32_16x16x32_bf16 v[152:155], v[92:95], v[0:3], v[152:155]\n\t" \
  "v_mfma_f32_16x16x32_bf16 v[152:155], v[88:91], v[4:7], v[152:155]\n\t" \
  "v_mfma_f32_16x16x32_bf16 v[156:159], v[100:103], v[0:3], v[156:159]\n\t" \
  "ds_read_b64 v[0:1], v30 offset:12288\n\t" \
  "v_cvt_pk_bf16_f32 v40, v140, v141\n\t" \
  "v_mfma_f32_16x16x32_bf16 v[156:159], v[96:99], v[4:7], v[156:159]\n\t" \
  "v_cvt_pk_bf16_f32 v41, v142, v143\n\t" \
  "s_waitcnt lgkmcnt(3)\n\t" \
  "v_mfma_f32_16x16x32_bf16 v[144:147], v[68:71], v[8:11], v[144:147]\n\t" \
  "ds_read_b64 v[2:3], v31 offset:12288\n\t" \
  "ds_read_b64 v[4:5], v24 offset:13312\n\t" \
  "v_mfma_f32_16x16x32_bf16 v[144:147], v[64:67], v[12:15], v[144:147]\n\t" \
  "v_permlane16_swap_b32 v38, v40\n\t" \
  "v_permlane16_swap_b32 v39, v41\n\t" \
  "v_mfma_f32_16x16x32_bf16 v[148:151], v[76:79], v[8:11], v[148:151]\n\t" \
  "ds_read_b64 v[6:7], v25 offset:13312\n\t" \
  "v_mfma_f32_16x16x32_bf16 v[148:151], v[72:75], v[12:15], v[148:151]\n\t" \
  "global_store_dwordx4 v42, v[38:41], %[yb] offset:1024 sc1\n\t" \
  "v_mfma_f32_16x16x32_bf16 v[152:155], v[84:87], v[8:11], v[152:155]\n\t" \
  "v_mfma_f32_16x16x32_bf16 v[152:155], v[80:83], v[12:15], v[152:155]\n\t" \
  "v_cvt_pk_bf16_f32 v34, v144, v145\n\t" \
  "v_cvt_pk_bf16_f32 v35, v146, v147\n\t" \
  "v_mfma_f32_16x16x32_bf16 v[156:159], v[92:95], v[8:11], v[156:159]\n\t" \
  "ds_read_b64 v[8:9], v26 offset:14336\n\t" \
  "ds_read_b64 v[10:11], v27 offset:14336\n\t" \
  "v_mfma_f32_16x16x32_bf16 v[156:159], v[88:91], v[12:15], v[156:159]\n\t" \
  "ds_read_b64 v[12:13], v28 offset:15360\n\t" \
  "ds_read_b64 v[14:15], v29 offset:15360\n\t" \
  "v_mfma_f32_16x16x32_bf16 v[148:151], v[68:71], v[16:19], v[148:151]\n\t" \
  "s_waitcnt lgkmcnt(8)\n\t" \
  "v_mfma_f32_16x16x32_bf16 v[148:151], v[64:67], v[20:23], v[148:151]\n\t" \
  "v_mfma_f32_16x16x32_bf16 v[152:155], v[76:79], v[16:19], v[152:155]\n\t" \
  "v_mfma_f32_16x16x32_bf16 v[152:155], v[72:75], v[20:23], v[152:155]\n\t" \
  "v_mfma_f32_16x16x32_bf16 v[156:159], v[84:87], v[16:19], v[156:159]\n\t" \
  "v_mfma_f32_16x16x32_bf16 v[156:159], v[80:83], v[20:23], v[156:159]\n\t" \
  "s_waitcnt lgkmcnt(4)\n\t" \
  "v_mfma_f32_16x16x32_bf16 v[152:155], v[68:71], v[0:3], v[152:155]\n\t" \
  "v_mfma_f32_16x16x32_bf16 v[152:155], v[64:67], v[4:7], v[152:155]\n\t" \
  "v_mfma_f32_16x16x32_bf16 v[156:159], v[76:79], v[0:3], v[156:159]\n\t" \
  "v_cvt_pk_bf16_f32 v36, v148, v149\n\t" \
  "v_cvt_pk_bf16_f32 v37, v150, v151\n\t" \
  "v_mfma_f32_16x16x32_bf16 v[156:159], v[72:75], v[4:7], v[156:159]\n\t" \
  "s_waitcnt lgkmcnt(2)\n\t" \
  "v_mfma_f32_16x16x32_bf16 v[156:159], v[68:71], v[8:11], v[156:159]\n\t" \
  "v_permlane16_swap_b32 v34, v36\n\t" \
  "s_waitcnt lgkmcnt(0)\n\t" \
  "v_mfma_f32_16x16x32_bf16 v[156:159], v[64:67], v[12:15], v[156:159]\n\t" \
  "v_permlane16_swap_b32 v35, v37\n\t" \
  "global_store_dwordx4 v42, v[34:37], %[yb] offset:2048 sc1\n\t" \
  "s_waitcnt lgkmcnt(0)\n\t" \
  "s_nop 4\n\t" \
  "v_cvt_pk_bf16_f32 v38, v152, v153\n\t" \
  "v_cvt_pk_bf16_f32 v39, v154, v155\n\t" \
  "v_cvt_pk_bf16_f32 v40, v156, v157\n\t" \
  "v_cvt_pk_bf16_f32 v41, v158, v159\n\t" \
  "s_nop 1\n\t" \
  "v_permlane16_swap_b32 v38, v40\n\t" \
  "v_permlane16_swap_b32 v39, v41\n\t" \
  "global_store_dwordx4 v42, v[38:41], %[yb] offset:3072 sc1\n\t" \
  "s_nop 1\n\t"
#define CV_Q 1
#define CV_CLOB "vcc", "v0", "v1", "v2", "v3", "v4", "v5", "v6", "v7", "v8", "v9", "v10", "v11", "v12", "v13", "v14", "v15", "v16", "v17", "v18", "v19", "v20", "v21", "v22", "v23", "v34", "v35", "v36", "v37", "v38", "v39", "v40", "v41"

#define G3_ASM \
  "s_mov_b32 %[c], -1\n\t" \
  "s_getpc_b64 s[92:93]\n\t" \
  "s_branch QC_%=\n\t" \
  "QH_%=:\n\t" \
  "v_add_u32 v166, %[go], v165\n\t" \
  "ds_read_b32 v166, v166\n\t" \
  "s_addk_i32 %[cn], 1\n\t" \
  "s_cmp_gt_u32 %[cn], 0x400000\n\t" \
  "s_cbranch_scc1 QI_%=\n\t" \
  "s_waitcnt lgkmcnt(0)\n\t" \
  "v_cmp_eq_u32 vcc, 0, v166\n\t" \
  "s_cbranch_vccz QI_%=\n\t" \
  "s_sleep 1\n\t" \
  "s_getpc_b64 s[92:93]\n\t" \
  "s_branch QC_%=\n\t" \
  "s_branch QH_%=\n\t" \
  "QI_%=:\n\t" \
  "s_mov_b32 %[c], 0\n\t" \
  "s_getpc_b64 s[92:93]\n\t" \
  "s_branch QC_%=\n\t" \
  "s_setprio 0\n\t" \
  "ds_read_b32 v166, v165\n\t" \
  "ds_read_b64_tr_b16 v[96:97], v160 offset:0\n\t" \
  "ds_read_b64_tr_b16 v[98:99], v160 offset:2048\n\t" \
  "ds_read_b64_tr_b16 v[100:101], v161 offset:0\n\t" \
  "ds_read_b64_tr_b16 v[102:103], v161 offset:2048\n\t" \
  "ds_read_b64_tr_b16 v[104:105], v162 offset:0\n\t" \
  "ds_read_b64_tr_b16 v[106:107], v162 offset:2048\n\t" \
  "ds_read_b64_tr_b16 v[108:109], v163 offset:0\n\t" \
  "ds_read_b64_tr_b16 v[110:111], v163 offset:2048\n\t" \
  "ds_read_b64_tr_b16 v[112:113], v160 offset:4096\n\t" \
  "ds_read_b64_tr_b16 v[114:115], v160 offset:6144\n\t" \
  "ds_read_b64_tr_b16 v[116:117], v161 offset:4096\n\t" \
  "ds_read_b64_tr_b16 v[118:119], v161 offset:6144\n\t" \
  "ds_read_b64_tr_b16 v[120:121], v162 offset:4096\n\t" \
  "ds_read_b64_tr_b16 v[122:123], v162 offset:6144\n\t" \
  "s_waitcnt lgkmcnt(10)\n\t" \
  "v_mfma_f32_16x16x32_bf16 v[128:131], v[96:99], v[0:3], 0\n\t" \
  "ds_read_b64_tr_b16 v[124:125], v163 offset:4096\n\t" \
  "v_readlane_b32 s94, v167, 4\n\t" \
  "v_mfma_f32_16x16x32_bf16 v[144:147], v[96:99], v[4:7], 0\n\t" \
  "ds_read_b64_tr_b16 v[126:127], v163 offset:6144\n\t" \
  "v_cmp_lt_u32 vcc, %[th], v166\n\t" \
  "v_mfma_f32_16x16x32_bf16 v[132:135], v[100:103], v[0:3], 0\n\t" \
  "s_mov_b64 exec, vcc\n\t" \
  "buffer_load_dwordx4 v[64:67], v164, %[ob], s94 offen offset:0 sc1\n\t" \
  "s_mov_b64 exec, -1\n\t" \
  "v_mfma_f32_16x16x32_bf16 v[148:151], v[100:103], v[4:7], 0\n\t" \
  "ds_read_b64_tr_b16 v[96:97], v160 offset:8192\n\t" \
  "ds_read_b64_tr_b16 v[98:99], v160 offset:10240\n\t" \
  "s_waitcnt lgkmcnt(10)\n\t" \
  "v_mfma_f32_16x16x32_bf16 v[136:139], v[104:107], v[0:3], 0\n\t" \
  "s_mov_b64 exec, vcc\n\t" \
  "buffer_load_dwordx4 v[68:71], v164, %[ob], s94 offen offset:1024 sc1\n\t" \
  "s_mov_b64 exec, -1\n\t" \
  "v_mfma_f32_16x16x32_bf16 v[152:155], v[104:107], v[4:7], 0\n\t" \
  "ds_read_b64_tr_b16 v[100:101], v161 offset:8192\n\t" \
  "ds_read_b64_tr_b16 v[102:103], v161 offset:10240\n\t" \
  "v_mfma_f32_16x16x32_bf16 v[140:143], v[108:111], v[0:3], 0\n\t" \
  "s_mov_b64 exec, vcc\n\t" \
  "buffer_load_dwordx4 v[72:75], v164, %[ob], s94 offen offset:2048 sc1\n\t" \
  "s_mov_b64 exec, -1\n\t" \
  "v_mfma_f32_16x16x32_bf16 v[156:159], v[108:111], v[4:7], 0\n\t" \
  "ds_read_b64_tr_b16 v[104:105], v162 offset:8192\n\t" \
  "ds_read_b64_tr_b16 v[106:107], v162 offset:10240\n\t" \
  "s_waitcnt lgkmcnt(10)\n\t" \
  "v_mfma_f32_16x16x32_bf16 v[128:131], v[112:115], v[8:11], v[128:131]\n\t" \
  "s_mov_b64 exec, vcc\n\t" \
  "buffer_load_dwordx4 v[76:79], v164, %[ob], s94 offen offset:3072 sc1\n\t" \
  "s_mov_b64 exec, -1\n\t" \
  "v_mfma_f32_16x16x32_bf16 v[144:147], v[112:115], v[12:15], v[144:147]\n\t" \
  "ds_read_b64_tr_b16 v[108:109], v163 offset:8192\n\t" \
  "ds_read_b64_tr_b16 v[110:111], v163 offset:10240\n\t" \
  "v_mfma_f32_16x16x32_bf16 v[132:135], v[116:119], v[8:11], v[132:135]\n\t" \
  "ds_read_b64_tr_b16 v[112:113], v160 offset:12288\n\t" \
  "ds_read_b32 v166, v165\n\t" \
  "v_mfma_f32_16x16x32_bf16 v[148:151], v[116:119], v[12:15], v[148:151]\n\t" \
  "ds_read_b64_tr_b16 v[114:115], v160 offset:14336\n\t" \
  "s_waitcnt lgkmcnt(11)\n\t" \
  "v_mfma_f32_16x16x32_bf16 v[136:139], v[120:123], v[8:11], v[136:139]\n\t" \
  "ds_read_b64_tr_b16 v[116:117], v161 offset:12288\n\t" \
  "v_mfma_f32_16x16x32_bf16 v[152:155], v[120:123], v[12:15], v[152:155]\n\t" \
  "ds_read_b64_tr_b16 v[118:119], v161 offset:14336\n\t" \
  "s_sub_u32 %[iss], %[iss], vcc_lo\n\t" \
  "v_mfma_f32_16x16x32_bf16 v[140:143], v[124:127], v[8:11], v[140:143]\n\t" \
  "ds_read_b64_tr_b16 v[120:121], v162 offset:12288\n\t" \
  "v_mfma_f32_16x16x32_bf16 v[156:159], v[124:127], v[12:15], v[156:159]\n\t" \
  "ds_read_b64_tr_b16 v[122:123], v162 offset:14336\n\t" \
  "s_add_u32 %[th], %[th], 1\n\t" \
  "s_cbranch_vccz QF1_%=\n\t" \
  "s_waitcnt vmcnt(12)\n\t" \
  "QG1_%=:\n\t" \
  "s_waitcnt lgkmcnt(11)\n\t" \
  "v_mfma_f32_16x16x32_bf16 v[128:131], v[96:99], v[16:19], v[128:131]\n\t" \
  "ds_read_b64_tr_b16 v[124:125], v163 offset:12288\n\t" \
  "v_readlane_b32 s94, v167, 5\n\t" \
  "v_mfma_f32_16x16x32_bf16 v[144:147], v[96:99], v[20:23], v[144:147]\n\t" \
  "ds_read_b64_tr_b16 v[126:127], v163 offset:14336\n\t" \
  "s_waitcnt lgkmcnt(7)\n\t" \
  "v_cmp_lt_u32 vcc, %[th], v166\n\t" \
  "v_mfma_f32_16x16x32_bf16 v[132:135], v[100:103], v[16:19], v[132:135]\n\t" \
  "s_mov_b64 exec, vcc\n\t" \
  "buffer_load_dwordx4 v[0:3], v164, %[ob], s94 offen offset:0 sc1\n\t" \
  "s_mov_b64 exec, -1\n\t" \
  "v_mfma_f32_16x16x32_bf16 v[148:151], v[100:103], v[20:23], v[148:151]\n\t" \
  "ds_read_b64_tr_b16 v[96:97], v160 offset:16384\n\t" \
  "ds_read_b64_tr_b16 v[98:99], v160 offset:18432\n\t" \
  "v_mfma_f32_16x16x32_bf16 v[136:139], v[104:107], v[16:19], v[136:139]\n\t" \
  "s_mov_b64 exec, vcc\n\t" \
  "buffer_load_dwordx4 v[4:7], v164, %[ob], s94 offen offset:1024 sc1\n\t" \
  "s_mov_b64 exec, -1\n\t" \
  "v_mfma_f32_16x16x32_bf16 v[152:155], v[104:107], v[20:23], v[152:155]\n\t" \
  "ds_read_b64_tr_b16 v[100:101], v161 offset:16384\n\t" \
  "ds_read_b64_tr_b16 v[102:103], v161 offset:18432\n\t" \
  "v_mfma_f32_16x16x32_bf16 v[140:143], v[108:111], v[16:19], v[140:143]\n\t" \
  "s_mov_b64 exec, vcc\n\t" \
  "buffer_load_dwordx4 v[8:11], v164, %[ob], s94 offen offset:2048 sc1\n\t" \
  "s_mov_b64 exec, -1\n\t" \
  "v_mfma_f32_16x16x32_bf16 v[156:159], v[108:111], v[20:23], v[156:159]\n\t" \
  "ds_read_b64_tr_b16 v[104:105], v162 offset:16384\n\t" \
  "ds_read_b64_tr_b16 v[106:107], v162 offset:18432\n\t" \
  "s_waitcnt lgkmcnt(10)\n\t" \
  "v_mfma_f32_16x16x32_bf16 v[128:131], v[112:115], v[24:27], v[128:131]\n\t" \
  "s_mov_b64 exec, vcc\n\t" \
  "buffer_load_dwordx4 v[12:15], v164, %[ob], s94 offen offset:3072 sc1\n\t" \
  "s_mov_b64 exec, -1\n\t" \
  "v_mfma_f32_16x16x32_bf16 v[144:147], v[112:115], v[28:31], v[144:147]\n\t" \
  "ds_read_b64_tr_b16 v[108:109], v163 offset:16384\n\t" \
  "ds_read_b64_tr_b16 v[110:111], v163 offset:18432\n\t" \
  "v_mfma_f32_16x16x32_bf16 v[132:135], v[116:119], v[24:27], v[132:135]\n\t" \
  "ds_read_b64_tr_b16 v[112:113], v160 offset:20480\n\t" \
  "ds_read_b32 v166, v165\n\t" \
  "v_mfma_f32_16x16x32_bf16 v[148:151], v[116:119], v[28:31], v[148:151]\n\t" \
  "ds_read_b64_tr_b16 v[114:115], v160 offset:22528\n\t" \
  "s_waitcnt lgkmcnt(11)\n\t" \
  "v_mfma_f32_16x16x32_bf16 v[136:139], v[120:123], v[24:27], v[136:139]\n\t" \
  "ds_read_b64_tr_b16 v[116:117], v161 offset:20480\n\t" \
  "v_mfma_f32_16x16x32_bf16 v[152:155], v[120:123], v[28:31], v[152:155]\n\t" \
  "ds_read_b64_tr_b16 v[118:119], v161 offset:22528\n\t" \
  "s_sub_u32 %[iss], %[iss], vcc_lo\n\t" \
  "v_mfma_f32_16x16x32_bf16 v[140:143], v[124:127], v[24:27], v[140:143]\n\t" \
  "ds_read_b64_tr_b16 v[120:121], v162 offset:20480\n\t" \
  "v_mfma_f32_16x16x32_bf16 v[156:159], v[124:127], v[28:31], v[156:159]\n\t" \
  "ds_read_b64_tr_b16 v[122:123], v162 offset:22528\n\t" \
  "s_add_u32 %[th], %[th], 1\n\t" \
  "s_cbranch_vccz QF2_%=\n\t" \
  "s_waitcnt vmcnt(12)\n\t" \
  "QG2_%=:\n\t" \
  "s_waitcnt lgkmcnt(11)\n\t" \
  "v_mfma_f32_16x16x32_bf16 v[128:131], v[96:99], v[32:35], v[128:131]\n\t" \
  "ds_read_b64_tr_b16 v[124:125], v163 offset:20480\n\t" \
  "v_readlane_b32 s94, v167, 6\n\t" \
  "v_mfma_f32_16x16x32_bf16 v[144:147], v[96:99], v[36:39], v[144:147]\n\t" \
  "ds_read_b64_tr_b16 v[126:127], v163 offset:22528\n\t" \
  "s_waitcnt lgkmcnt(7)\n\t" \
  "v_cmp_lt_u32 vcc, %[th], v166\n\t" \
  "v_mfma_f32_16x16x32_bf16 v[132:135], v[100:103], v[32:35], v[132:135]\n\t" \
  "s_mov_b64 exec, vcc\n\t" \
  "buffer_load_dwordx4 v[16:19], v164, %[ob], s94 offen offset:0 sc1\n\t" \
  "s_mov_b64 exec, -1\n\t" \
  "v_mfma_f32_16x16x32_bf16 v[148:151], v[100:103], v[36:39], v[148:151]\n\t" \
  "ds_read_b64_tr_b16 v[96:97], v160 offset:24576\n\t" \
  "ds_read_b64_tr_b16 v[98:99], v160 offset:26624\n\t" \
  "v_mfma_f32_16x16x32_bf16 v[136:139], v[104:107], v[32:35], v[136:139]\n\t" \
  "s_mov_b64 exec, vcc\n\t" \
  "buffer_load_dwordx4 v[20:23], v164, %[ob], s94 offen offset:1024 sc1\n\t" \
  "s_mov_b64 exec, -1\n\t" \
  "v_mfma_f32_16x16x32_bf16 v[152:155], v[104:107], v[36:39], v[152:155]\n\t" \
  "ds_read_b64_tr_b16 v[100:101], v161 offset:24576\n\t" \
  "ds_read_b64_tr_b16 v[102:103], v161 offset:26624\n\t" \
  "v_mfma_f32_16x16x32_bf16 v[140:143], v[108:111], v[32:35], v[140:143]\n\t" \
  "s_mov_b64 exec, vcc\n\t" \
  "buffer_load_dwordx4 v[24:27], v164, %[ob], s94 offen offset:2048 sc1\n\t" \
  "s_mov_b64 exec, -1\n\t" \
  "v_mfma_f32_16x16x32_bf16 v[156:159], v[108:111], v[36:39], v[156:159]\n\t" \
  "ds_read_b64_tr_b16 v[104:105], v162 offset:24576\n\t" \
  "ds_read_b64_tr_b16 v[106:107], v162 offset:26624\n\t" \
  "s_waitcnt lgkmcnt(10)\n\t" \
  "v_mfma_f32_16x16x32_bf16 v[128:131], v[112:115], v[40:43], v[128:131]\n\t" \
  "s_mov_b64 exec, vcc\n\t" \
  "buffer_load_dwordx4 v[28:31], v164, %[ob], s94 offen offset:3072 sc1\n\t" \
  "s_mov_b64 exec, -1\n\t" \
  "v_mfma_f32_16x16x32_bf16 v[144:147], v[112:115], v[44:47], v[144:147]\n\t" \
  "ds_read_b64_tr_b16 v[108:109], v163 offset:24576\n\t" \
  "ds_read_b64_tr_b16 v[110:111], v163 offset:26624\n\t" \
  "v_mfma_f32_16x16x32_bf16 v[132:135], v[116:119], v[40:43], v[132:135]\n\t" \
  "ds_read_b64_tr_b16 v[112:113], v160 offset:28672\n\t" \
  "ds_read_b32 v166, v165\n\t" \
  "v_mfma_f32_16x16x32_bf16 v[148:151], v[116:119], v[44:47], v[148:151]\n\t" \
  "ds_read_b64_tr_b16 v[114:115], v160 offset:30720\n\t" \
  "s_waitcnt lgkmcnt(11)\n\t" \
  "v_mfma_f32_16x16x32_bf16 v[136:139], v[120:123], v[40:43], v[136:139]\n\t" \
  "ds_read_b64_tr_b16 v[116:117], v161 offset:28672\n\t" \
  "v_mfma_f32_16x16x32_bf16 v[152:155], v[120:123], v[44:47], v[152:155]\n\t" \
  "ds_read_b64_tr_b16 v[118:119], v161 offset:30720\n\t" \
  "s_sub_u32 %[iss], %[iss], vcc_lo\n\t" \
  "v_mfma_f32_16x16x32_bf16 v[140:143], v[124:127], v[40:43], v[140:143]\n\t" \
  "ds_read_b64_tr_b16 v[120:121], v162 offset:28672\n\t" \
  "v_mfma_f32_16x16x32_bf16 v[156:159], v[124:127], v[44:47], v[156:159]\n\t" \
  "ds_read_b64_tr_b16 v[122:123], v162 offset:30720\n\t" \
  "s_add_u32 %[th], %[th], 1\n\t" \
  "s_cbranch_vccz QF3_%=\n\t" \
  "s_waitcnt vmcnt(12)\n\t" \
  "QG3_%=:\n\t" \
  "s_waitcnt lgkmcnt(11)\n\t" \
  "v_mfma_f32_16x16x32_bf16 v[128:131], v[96:99], v[48:51], v[128:131]\n\t" \
  "ds_read_b64_tr_b16 v[124:125], v163 offset:28672\n\t" \
  "v_readlane_b32 s94, v167, 7\n\t" \
  "v_mfma_f32_16x16x32_bf16 v[144:147], v[96:99], v[52:55], v[144:147]\n\t" \
  "ds_read_b64_tr_b16 v[126:127], v163 offset:30720\n\t" \
  "s_waitcnt lgkmcnt(7)\n\t" \
  "v_cmp_lt_u32 vcc, %[th], v166\n\t" \
  "v_mfma_f32_16x16x32_bf16 v[132:135], v[100:103], v[48:51], v[132:135]\n\t" \
  "s_mov_b64 exec, vcc\n\t" \
  "buffer_load_dwordx4 v[32:35], v164, %[ob], s94 offen offset:0 sc1\n\t" \
  "s_mov_b64 exec, -1\n\t" \
  "v_mfma_f32_16x16x32_bf16 v[148:151], v[100:103], v[52:55], v[148:151]\n\t" \
  "ds_read_b64_tr_b16 v[96:97], v160 offset:32768\n\t" \
  "ds_read_b64_tr_b16 v[98:99], v160 offset:34816\n\t" \
  "v_mfma_f32_16x16x32_bf16 v[136:139], v[104:107], v[48:51], v[136:139]\n\t" \
  "s_mov_b64 exec, vcc\n\t" \
  "buffer_load_dwordx4 v[36:39], v164, %[ob], s94 offen offset:1024 sc1\n\t" \
  "s_mov_b64 exec, -1\n\t" \
  "v_mfma_f32_16x16x32_bf16 v[152:155], v[104:107], v[52:55], v[152:155]\n\t" \
  "ds_read_b64_tr_b16 v[100:101], v161 offset:32768\n\t" \
  "ds_read_b64_tr_b16 v[102:103], v161 offset:34816\n\t" \
  "v_mfma_f32_16x16x32_bf16 v[140:143], v[108:111], v[48:51], v[140:143]\n\t" \
  "s_mov_b64 exec, vcc\n\t" \
  "buffer_load_dwordx4 v[40:43], v164, %[ob], s94 offen offset:2048 sc1\n\t" \
  "s_mov_b64 exec, -1\n\t" \
  "v_mfma_f32_16x16x32_bf16 v[156:159], v[108:111], v[52:55], v[156:159]\n\t" \
  "ds_read_b64_tr_b16 v[104:105], v162 offset:32768\n\t" \
  "ds_read_b64_tr_b16 v[106:107], v162 offset:34816\n\t" \
  "s_waitcnt lgkmcnt(10)\n\t" \
  "v_mfma_f32_16x16x32_bf16 v[128:131], v[112:115], v[56:59], v[128:131]\n\t" \
  "s_mov_b64 exec, vcc\n\t" \
  "buffer_load_dwordx4 v[44:47], v164, %[ob], s94 offen offset:3072 sc1\n\t" \
  "s_mov_b64 exec, -1\n\t" \
  "v_mfma_f32_16x16x32_bf16 v[144:147], v[112:115], v[60:63], v[144:147]\n\t" \
  "ds_read_b64_tr_b16 v[108:109], v163 offset:32768\n\t" \
  "ds_read_b64_tr_b16 v[110:111], v163 offset:34816\n\t" \
  "v_mfma_f32_16x16x32_bf16 v[132:135], v[116:119], v[56:59], v[132:135]\n\t" \
  "ds_read_b64_tr_b16 v[112:113], v160 offset:36864\n\t" \
  "ds_read_b32 v166, v165\n\t" \
  "v_mfma_f32_16x16x32_bf16 v[148:151], v[116:119], v[60:63], v[148:151]\n\t" \
  "ds_read_b64_tr_b16 v[114:115], v160 offset:38912\n\t" \
  "s_waitcnt lgkmcnt(11)\n\t" \
  "v_mfma_f32_16x16x32_bf16 v[136:139], v[120:123], v[56:59], v[136:139]\n\t" \
  "ds_read_b64_tr_b16 v[116:117], v161 offset:36864\n\t" \
  "v_mfma_f32_16x16x32_bf16 v[152:155], v[120:123], v[60:63], v[152:155]\n\t" \
  "ds_read_b64_tr_b16 v[118:119], v161 offset:38912\n\t" \
  "s_sub_u32 %[iss], %[iss], vcc_lo\n\t" \
  "v_mfma_f32_16x16x32_bf16 v[140:143], v[124:127], v[56:59], v[140:143]\n\t" \
  "ds_read_b64_tr_b16 v[120:121], v162 offset:36864\n\t" \
  "v_mfma_f32_16x16x32_bf16 v[156:159], v[124:127], v[60:63], v[156:159]\n\t" \
  "ds_read_b64_tr_b16 v[122:123], v162 offset:38912\n\t" \
  "s_add_u32 %[th], %[th], 1\n\t" \
  "s_cbranch_vccz QF4_%=\n\t" \
  "s_waitcnt vmcnt(12)\n\t" \
  "QG4_%=:\n\t" \
  "s_waitcnt lgkmcnt(11)\n\t" \
  "v_mfma_f32_16x16x32_bf16 v[128:131], v[96:99], v[64:67], v[128:131]\n\t" \
  "ds_read_b64_tr_b16 v[124:125], v163 offset:36864\n\t" \
  "v_readlane_b32 s94, v167, 8\n\t" \
  "v_mfma_f32_16x16x32_bf16 v[144:147], v[96:99], v[68:71], v[144:147]\n\t" \
  "ds_read_b64_tr_b16 v[126:127], v163 offset:38912\n\t" \
  "s_waitcnt lgkmcnt(7)\n\t" \
  "v_cmp_lt_u32 vcc, %[th], v166\n\t" \
  "v_mfma_f32_16x16x32_bf16 v[132:135], v[100:103], v[64:67], v[132:135]\n\t" \
  "s_mov_b64 exec, vcc\n\t" \
  "buffer_load_dwordx4 v[48:51], v164, %[ob], s94 offen offset:0 sc1\n\t" \
  "s_mov_b64 exec, -1\n\t" \
  "v_mfma_f32_16x16x32_bf16 v[148:151], v[100:103], v[68:71], v[148:151]\n\t" \
  "ds_read_b64_tr_b16 v[96:97], v160 offset:40960\n\t" \
  "ds_read_b64_tr_b16 v[98:99], v160 offset:43008\n\t" \
  "v_mfma_f32_16x16x32_bf16 v[136:139], v[104:107], v[64:67], v[136:139]\n\t" \
  "s_mov_b64 exec, vcc\n\t" \
  "buffer_load_dwordx4 v[52:55], v164, %[ob], s94 offen offset:1024 sc1\n\t" \
  "s_mov_b64 exec, -1\n\t" \
  "v_mfma_f32_16x16x32_bf16 v[152:155], v[104:107], v[68:71], v[152:155]\n\t" \
  "ds_read_b64_tr_b16 v[100:101], v161 offset:40960\n\t" \
  "ds_read_b64_tr_b16 v[102:103], v161 offset:43008\n\t" \
  "v_mfma_f32_16x16x32_bf16 v[140:143], v[108:111], v[64:67], v[140:143]\n\t" \
  "s_mov_b64 exec, vcc\n\t" \
  "buffer_load_dwordx4 v[56:59], v164, %[ob], s94 offen offset:2048 sc1\n\t" \
  "s_mov_b64 exec, -1\n\t" \
  "v_mfma_f32_16x16x32_bf16 v[156:159], v[108:111], v[68:71], v[156:159]\n\t" \
  "ds_read_b64_tr_b16 v[104:105], v162 offset:40960\n\t" \
  "ds_read_b64_tr_b16 v[106:107], v162 offset:43008\n\t" \
  "s_waitcnt lgkmcnt(10)\n\t" \
  "v_mfma_f32_16x16x32_bf16 v[128:131], v[112:115], v[72:75], v[128:131]\n\t" \
  "s_mov_b64 exec, vcc\n\t" \
  "buffer_load_dwordx4 v[60:63], v164, %[ob], s94 offen offset:3072 sc1\n\t" \
  "s_mov_b64 exec, -1\n\t" \
  "v_mfma_f32_16x16x32_bf16 v[144:147], v[112:115], v[76:79], v[144:147]\n\t" \
  "ds_read_b64_tr_b16 v[108:109], v163 offset:40960\n\t" \
  "ds_read_b64_tr_b16 v[110:111], v163 offset:43008\n\t" \
  "v_mfma_f32_16x16x32_bf16 v[132:135], v[116:119], v[72:75], v[132:135]\n\t" \
  "ds_read_b64_tr_b16 v[112:113], v160 offset:45056\n\t" \
  "ds_read_b32 v166, v165\n\t" \
  "v_mfma_f32_16x16x32_bf16 v[148:151], v[116:119], v[76:79], v[148:151]\n\t" \
  "ds_read_b64_tr_b16 v[114:115], v160 offset:47104\n\t" \
  "s_waitcnt lgkmcnt(11)\n\t" \
  "v_mfma_f32_16x16x32_bf16 v[136:139], v[120:123], v[72:75], v[136:139]\n\t" \
  "ds_read_b64_tr_b16 v[116:117], v161 offset:45056\n\t" \
  "v_mfma_f32_16x16x32_bf16 v[152:155], v[120:123], v[76:79], v[152:155]\n\t" \
  "ds_read_b64_tr_b16 v[118:119], v161 offset:47104\n\t" \
  "s_sub_u32 %[iss], %[iss], vcc_lo\n\t" \
  "v_mfma_f32_16x16x32_bf16 v[140:143], v[124:127], v[72:75], v[140:143]\n\t" \
  "ds_read_b64_tr_b16 v[120:121], v162 offset:45056\n\t" \
  "v_mfma_f32_16x16x32_bf16 v[156:159], v[124:127], v[76:79], v[156:159]\n\t" \
  "ds_read_b64_tr_b16 v[122:123], v162 offset:47104\n\t" \
  "s_add_u32 %[th], %[th], 1\n\t" \
  "s_cbranch_vccz QF5_%=\n\t" \
  "s_waitcnt vmcnt(12)\n\t" \
  "QG5_%=:\n\t" \
  "s_waitcnt lgkmcnt(11)\n\t" \
  "v_mfma_f32_16x16x32_bf16 v[128:131], v[96:99], v[0:3], v[128:131]\n\t" \
  "ds_read_b64_tr_b16 v[124:125], v163 offset:45056\n\t" \
  "v_readlane_b32 s94, v167, 9\n\t" \
  "v_mfma_f32_16x16x32_bf16 v[144:147], v[96:99], v[4:7], v[144:147]\n\t" \
  "ds_read_b64_tr_b16 v[126:127], v163 offset:47104\n\t" \
  "s_waitcnt lgkmcnt(7)\n\t" \
  "v_cmp_lt_u32 vcc, %[th], v166\n\t" \
  "v_mfma_f32_16x16x32_bf16 v[132:135], v[100:103], v[0:3], v[132:135]\n\t" \
  "s_mov_b64 exec, vcc\n\t" \
  "buffer_load_dwordx4 v[64:67], v164, %[ob], s94 offen offset:0 sc1\n\t" \
  "s_mov_b64 exec, -1\n\t" \
  "v_mfma_f32_16x16x32_bf16 v[148:151], v[100:103], v[4:7], v[148:151]\n\t" \
  "ds_read_b64_tr_b16 v[96:97], v160 offset:49152\n\t" \
  "ds_read_b64_tr_b16 v[98:99], v160 offset:51200\n\t" \
  "v_mfma_f32_16x16x32_bf16 v[136:139], v[104:107], v[0:3], v[136:139]\n\t" \
  "s_mov_b64 exec, vcc\n\t" \
  "buffer_load_dwordx4 v[68:71], v164, %[ob], s94 offen offset:1024 sc1\n\t" \
  "s_mov_b64 exec, -1\n\t" \
  "v_mfma_f32_16x16x32_bf16 v[152:155], v[104:107], v[4:7], v[152:155]\n\t" \
  "ds_read_b64_tr_b16 v[100:101], v161 offset:49152\n\t" \
  "ds_read_b64_tr_b16 v[102:103], v161 offset:51200\n\t" \
  "v_mfma_f32_16x16x32_bf16 v[140:143], v[108:111], v[0:3], v[140:143]\n\t" \
  "s_mov_b64 exec, vcc\n\t" \
  "buffer_load_dwordx4 v[72:75], v164, %[ob], s94 offen offset:2048 sc1\n\t" \
  "s_mov_b64 exec, -1\n\t" \
  "v_mfma_f32_16x16x32_bf16 v[156:159], v[108:111], v[4:7], v[156:159]\n\t" \
  "ds_read_b64_tr_b16 v[104:105], v162 offset:49152\n\t" \
  "ds_read_b64_tr_b16 v[106:107], v162 offset:51200\n\t" \
  "s_waitcnt lgkmcnt(10)\n\t" \
  "v_mfma_f32_16x16x32_bf16 v[128:131], v[112:115], v[8:11], v[128:131]\n\t" \
  "s_mov_b64 exec, vcc\n\t" \
  "buffer_load_dwordx4 v[76:79], v164, %[ob], s94 offen offset:3072 sc1\n\t" \
  "s_mov_b64 exec, -1\n\t" \
  "v_mfma_f32_16x16x32_bf16 v[144:147], v[112:115], v[12:15], v[144:147]\n\t" \
  "ds_read_b64_tr_b16 v[108:109], v163 offset:49152\n\t" \
  "ds_read_b64_tr_b16 v[110:111], v163 offset:51200\n\t" \
  "v_mfma_f32_16x16x32_bf16 v[132:135], v[116:119], v[8:11], v[132:135]\n\t" \
  "ds_read_b64_tr_b16 v[112:113], v160 offset:53248\n\t" \
  "ds_read_b32 v166, v165\n\t" \
  "v_mfma_f32_16x16x32_bf16 v[148:151], v[116:119], v[12:15], v[148:151]\n\t" \
  "ds_read_b64_tr_b16 v[114:115], v160 offset:55296\n\t" \
  "s_waitcnt lgkmcnt(11)\n\t" \
  "v_mfma_f32_16x16x32_bf16 v[136:139], v[120:123], v[8:11], v[136:139]\n\t" \
  "ds_read_b64_tr_b16 v[116:117], v161 offset:53248\n\t" \
  "v_mfma_f32_16x16x32_bf16 v[152:155], v[120:123], v[12:15], v[152:155]\n\t" \
  "ds_read_b64_tr_b16 v[118:119], v161 offset:55296\n\t" \
  "s_sub_u32 %[iss], %[iss], vcc_lo\n\t" \
  "v_mfma_f32_16x16x32_bf16 v[140:143], v[124:127], v[8:11], v[140:143]\n\t" \
  "ds_read_b64_tr_b16 v[120:121], v162 offset:53248\n\t" \
  "v_mfma_f32_16x16x32_bf16 v[156:159], v[124:127], v[12:15], v[156:159]\n\t" \
  "ds_read_b64_tr_b16 v[122:123], v162 offset:55296\n\t" \
  "s_add_u32 %[th], %[th], 1\n\t" \
  "s_cbranch_vccz QF6_%=\n\t" \
  "s_waitcnt vmcnt(12)\n\t" \
  "QG6_%=:\n\t" \
  "s_waitcnt lgkmcnt(11)\n\t" \
  "v_mfma_f32_16x16x32_bf16 v[128:131], v[96:99], v[16:19], v[128:131]\n\t" \
  "ds_read_b64_tr_b16 v[124:125], v163 offset:53248\n\t" \
  "v_readlane_b32 s94, v167, 10\n\t" \
  "v_mfma_f32_16x16x32_bf16 v[144:147], v[96:99], v[20:23], v[144:147]\n\t" \
  "ds_read_b64_tr_b16 v[126:127], v163 offset:55296\n\t" \
  "s_waitcnt lgkmcnt(7)\n\t" \
  "v_cmp_lt_u32 vcc, %[th], v166\n\t" \
  "v_mfma_f32_16x16x32_bf16 v[132:135], v[100:103], v[16:19], v[132:135]\n\t" \
  "s_mov_b64 exec, vcc\n\t" \
  "buffer_load_dwordx4 v[0:3], v164, %[ob], s94 offen offset:0 sc1\n\t" \
  "s_mov_b64 exec, -1\n\t" \
  "v_mfma_f32_16x16x32_bf16 v[148:151], v[100:103], v[20:23], v[148:151]\n\t" \
  "ds_read_b64_tr_b16 v[96:97], v160 offset:57344\n\t" \
  "ds_read_b64_tr_b16 v[98:99], v160 offset:59392\n\t" \
  "v_mfma_f32_16x16x32_bf16 v[136:139], v[104:107], v[16:19], v[136:139]\n\t" \
  "s_mov_b64 exec, vcc\n\t" \
  "buffer_load_dwordx4 v[4:7], v164, %[ob], s94 offen offset:1024 sc1\n\t" \
  "s_mov_b64 exec, -1\n\t" \
  "v_mfma_f32_16x16x32_bf16 v[152:155], v[104:107], v[20:23], v[152:155]\n\t" \
  "ds_read_b64_tr_b16 v[100:101], v161 offset:57344\n\t" \
  "ds_read_b64_tr_b16 v[102:103], v161 offset:59392\n\t" \
  "v_mfma_f32_16x16x32_bf16 v[140:143], v[108:111], v[16:19], v[140:143]\n\t" \
  "s_mov_b64 exec, vcc\n\t" \
  "buffer_load_dwordx4 v[8:11], v164, %[ob], s94 offen offset:2048 sc1\n\t" \
  "s_mov_b64 exec, -1\n\t" \
  "v_mfma_f32_16x16x32_bf16 v[156:159], v[108:111], v[20:23], v[156:159]\n\t" \
  "ds_read_b64_tr_b16 v[104:105], v162 offset:57344\n\t" \
  "ds_read_b64_tr_b16 v[106:107], v162 offset:59392\n\t" \
  "s_waitcnt lgkmcnt(10)\n\t" \
  "v_mfma_f32_16x16x32_bf16 v[128:131], v[112:115], v[24:27], v[128:131]\n\t" \
  "s_mov_b64 exec, vcc\n\t" \
  "buffer_load_dwordx4 v[12:15], v164, %[ob], s94 offen offset:3072 sc1\n\t" \
  "s_mov_b64 exec, -1\n\t" \
  "v_mfma_f32_16x16x32_bf16 v[144:147], v[112:115], v[28:31], v[144:147]\n\t" \
  "ds_read_b64_tr_b16 v[108:109], v163 offset:57344\n\t" \
  "ds_read_b64_tr_b16 v[110:111], v163 offset:59392\n\t" \
  "v_mfma_f32_16x16x32_bf16 v[132:135], v[116:119], v[24:27], v[132:135]\n\t" \
  "ds_read_b64_tr_b16 v[112:113], v160 offset:61440\n\t" \
  "ds_read_b32 v166, v165\n\t" \
  "v_mfma_f32_16x16x32_bf16 v[148:151], v[116:119], v[28:31], v[148:151]\n\t" \
  "ds_read_b64_tr_b16 v[114:115], v160 offset:63488\n\t" \
  "s_waitcnt lgkmcnt(11)\n\t" \
  "v_mfma_f32_16x16x32_bf16 v[136:139], v[120:123], v[24:27], v[136:139]\n\t" \
  "ds_read_b64_tr_b16 v[116:117], v161 offset:61440\n\t" \
  "v_mfma_f32_16x16x32_bf16 v[152:155], v[120:123], v[28:31], v[152:155]\n\t" \
  "ds_read_b64_tr_b16 v[118:119], v161 offset:63488\n\t" \
  "s_sub_u32 %[iss], %[iss], vcc_lo\n\t" \
  "v_mfma_f32_16x16x32_bf16 v[140:143], v[124:127], v[24:27], v[140:143]\n\t" \
  "ds_read_b64_tr_b16 v[120:121], v162 offset:61440\n\t" \
  "s_mov_b64 exec, 1\n\t" \
  "ds_write_b32 v165, v165 offset:76\n\t" \
  "s_mov_b64 exec, -1\n\t" \
  "v_mfma_f32_16x16x32_bf16 v[156:159], v[124:127], v[28:31], v[156:159]\n\t" \
  "ds_read_b64_tr_b16 v[122:123], v162 offset:63488\n\t" \
  "s_add_u32 %[th], %[th], 1\n\t" \
  "s_cbranch_vccz QF7_%=\n\t" \
  "s_waitcnt vmcnt(12)\n\t" \
  "QG7_%=:\n\t" \
  "s_waitcnt lgkmcnt(12)\n\t" \
  "v_mfma_f32_16x16x32_bf16 v[128:131], v[96:99], v[32:35], v[128:131]\n\t" \
  "ds_read_b64_tr_b16 v[124:125], v163 offset:61440\n\t" \
  "v_readlane_b32 s94, v167, 11\n\t" \
  "v_mfma_f32_16x16x32_bf16 v[144:147], v[96:99], v[36:39], v[144:147]\n\t" \
  "ds_read_b64_tr_b16 v[126:127], v163 offset:63488\n\t" \
  "s_waitcnt lgkmcnt(8)\n\t" \
  "v_cmp_lt_u32 vcc, %[th], v166\n\t" \
  "v_mfma_f32_16x16x32_bf16 v[132:135], v[100:103], v[32:35], v[132:135]\n\t" \
  "v_add_u32 v160, 0x10000, v160\n\t" \
  "s_mov_b64 exec, vcc\n\t" \
  "buffer_load_dwordx4 v[16:19], v164, %[ob], s94 offen offset:0 sc1\n\t" \
  "s_mov_b64 exec, -1\n\t" \
  "v_mfma_f32_16x16x32_bf16 v[148:151], v[100:103], v[36:39], v[148:151]\n\t" \
  "ds_read_b64_tr_b16 v[96:97], v160 offset:0\n\t" \
  "ds_read_b64_tr_b16 v[98:99], v160 offset:2048\n\t" \
  "v_mfma_f32_16x16x32_bf16 v[136:139], v[104:107], v[32:35], v[136:139]\n\t" \
  "v_add_u32 v161, 0x10000, v161\n\t" \
  "s_mov_b64 exec, vcc\n\t" \
  "buffer_load_dwordx4 v[20:23], v164, %[ob], s94 offen offset:1024 sc1\n\t" \
  "s_mov_b64 exec, -1\n\t" \
  "v_mfma_f32_16x16x32_bf16 v[152:155], v[104:107], v[36:39], v[152:155]\n\t" \
  "ds_read_b64_tr_b16 v[100:101], v161 offset:0\n\t" \
  "ds_read_b64_tr_b16 v[102:103], v161 offset:2048\n\t" \
  "v_mfma_f32_16x16x32_bf16 v[140:143], v[108:111], v[32:35], v[140:143]\n\t" \
  "v_add_u32 v162, 0x10000, v162\n\t" \
  "s_mov_b64 exec, vcc\n\t" \
  "buffer_load_dwordx4 v[24:27], v164, %[ob], s94 offen offset:2048 sc1\n\t" \
  "s_mov_b64 exec, -1\n\t" \
  "v_mfma_f32_16x16x32_bf16 v[156:159], v[108:111], v[36:39], v[156:159]\n\t" \
  "ds_read_b64_tr_b16 v[104:105], v162 offset:0\n\t" \
  "ds_read_b64_tr_b16 v[106:107], v162 offset:2048\n\t" \
  "s_waitcnt lgkmcnt(11)\n\t" \
  "v_mfma_f32_16x16x32_bf16 v[128:131], v[112:115], v[40:43], v[128:131]\n\t" \
  "v_add_u32 v163, 0x10000, v163\n\t" \
  "s_mov_b64 exec, vcc\n\t" \
  "buffer_load_dwordx4 v[28:31], v164, %[ob], s94 offen offset:3072 sc1\n\t" \
  "s_mov_b64 exec, -1\n\t" \
  "v_mfma_f32_16x16x32_bf16 v[144:147], v[112:115], v[44:47], v[144:147]\n\t" \
  "ds_read_b64_tr_b16 v[108:109], v163 offset:0\n\t" \
  "ds_read_b64_tr_b16 v[110:111], v163 offset:2048\n\t" \
  "v_mfma_f32_16x16x32_bf16 v[132:135], v[116:119], v[40:43], v[132:135]\n\t" \
  "ds_read_b64_tr_b16 v[112:113], v160 offset:4096\n\t" \
  "ds_read_b32 v166, v165\n\t" \
  "v_mfma_f32_16x16x32_bf16 v[148:151], v[116:119], v[44:47], v[148:151]\n\t" \
  "ds_read_b64_tr_b16 v[114:115], v160 offset:6144\n\t" \
  "s_waitcnt lgkmcnt(11)\n\t" \
  "v_mfma_f32_16x16x32_bf16 v[136:139], v[120:123], v[40:43], v[136:139]\n\t" \
  "ds_read_b64_tr_b16 v[116:117], v161 offset:4096\n\t" \
  "v_mfma_f32_16x16x32_bf16 v[152:155], v[120:123], v[44:47], v[152:155]\n\t" \
  "ds_read_b64_tr_b16 v[118:119], v161 offset:6144\n\t" \
  "s_sub_u32 %[iss], %[iss], vcc_lo\n\t" \
  "v_mfma_f32_16x16x32_bf16 v[140:143], v[124:127], v[40:43], v[140:143]\n\t" \
  "ds_read_b64_tr_b16 v[120:121], v162 offset:4096\n\t" \
  "v_mfma_f32_16x16x32_bf16 v[156:159], v[124:127], v[44:47], v[156:159]\n\t" \
  "ds_read_b64_tr_b16 v[122:123], v162 offset:6144\n\t" \
  "s_add_u32 %[th], %[th], 1\n\t" \
  "s_cbranch_vccz QF8_%=\n\t" \
  "s_waitcnt vmcnt(12)\n\t" \
  "QG8_%=:\n\t" \
  "s_waitcnt lgkmcnt(11)\n\t" \
  "v_mfma_f32_16x16x32_bf16 v[128:131], v[96:99], v[48:51], v[128:131]\n\t" \
  "ds_read_b64_tr_b16 v[124:125], v163 offset:4096\n\t" \
  "v_readlane_b32 s94, v167, 12\n\t" \
  "v_mfma_f32_16x16x32_bf16 v[144:147], v[96:99], v[52:55], v[144:147]\n\t" \
  "ds_read_b64_tr_b16 v[126:127], v163 offset:6144\n\t" \
  "s_waitcnt lgkmcnt(7)\n\t" \
  "v_cmp_lt_u32 vcc, %[th], v166\n\t" \
  "v_mfma_f32_16x16x32_bf16 v[132:135], v[100:103], v[48:51], v[132:135]\n\t" \
  "s_mov_b64 exec, vcc\n\t" \
  "buffer_load_dwordx4 v[32:35], v164, %[ob], s94 offen offset:0 sc1\n\t" \
  "s_mov_b64 exec, -1\n\t" \
  "v_mfma_f32_16x16x32_bf16 v[148:151], v[100:103], v[52:55], v[148:151]\n\t" \
  "ds_read_b64_tr_b16 v[96:97], v160 offset:8192\n\t" \
  "ds_read_b64_tr_b16 v[98:99], v160 offset:10240\n\t" \
  "v_mfma_f32_16x16x32_bf16 v[136:139], v[104:107], v[48:51], v[136:139]\n\t" \
  "s_mov_b64 exec, vcc\n\t" \
  "buffer_load_dwordx4 v[36:39], v164, %[ob], s94 offen offset:1024 sc1\n\t" \
  "s_mov_b64 exec, -1\n\t" \
  "v_mfma_f32_16x16x32_bf16 v[152:155], v[104:107], v[52:55], v[152:155]\n\t" \
  "ds_read_b64_tr_b16 v[100:101], v161 offset:8192\n\t" \
  "ds_read_b64_tr_b16 v[102:103], v161 offset:10240\n\t" \
  "v_mfma_f32_16x16x32_bf16 v[140:143], v[108:111], v[48:51], v[140:143]\n\t" \
  "s_mov_b64 exec, vcc\n\t" \
  "buffer_load_dwordx4 v[40:43], v164, %[ob], s94 offen offset:2048 sc1\n\t" \
  "s_mov_b64 exec, -1\n\t" \
  "v_mfma_f32_16x16x32_bf16 v[156:159], v[108:111], v[52:55], v[156:159]\n\t" \
  "ds_read_b64_tr_b16 v[104:105], v162 offset:8192\n\t" \
  "ds_read_b64_tr_b16 v[106:107], v162 offset:10240\n\t" \
  "s_waitcnt lgkmcnt(10)\n\t" \
  "v_mfma_f32_16x16x32_bf16 v[128:131], v[112:115], v[56:59], v[128:131]\n\t" \
  "s_mov_b64 exec, vcc\n\t" \
  "buffer_load_dwordx4 v[44:47], v164, %[ob], s94 offen offset:3072 sc1\n\t" \
  "s_mov_b64 exec, -1\n\t" \
  "v_mfma_f32_16x16x32_bf16 v[144:147], v[112:115], v[60:63], v[144:147]\n\t" \
  "ds_read_b64_tr_b16 v[108:109], v163 offset:8192\n\t" \
  "ds_read_b64_tr_b16 v[110:111], v163 offset:10240\n\t" \
  "v_mfma_f32_16x16x32_bf16 v[132:135], v[116:119], v[56:59], v[132:135]\n\t" \
  "ds_read_b64_tr_b16 v[112:113], v160 offset:12288\n\t" \
  "ds_read_b32 v166, v165\n\t" \
  "v_mfma_f32_16x16x32_bf16 v[148:151], v[116:119], v[60:63], v[148:151]\n\t" \
  "ds_read_b64_tr_b16 v[114:115], v160 offset:14336\n\t" \
  "s_waitcnt lgkmcnt(11)\n\t" \
  "v_mfma_f32_16x16x32_bf16 v[136:139], v[120:123], v[56:59], v[136:139]\n\t" \
  "ds_read_b64_tr_b16 v[116:117], v161 offset:12288\n\t" \
  "v_mfma_f32_16x16x32_bf16 v[152:155], v[120:123], v[60:63], v[152:155]\n\t" \
  "ds_read_b64_tr_b16 v[118:119], v161 offset:14336\n\t" \
  "s_sub_u32 %[iss], %[iss], vcc_lo\n\t" \
  "v_mfma_f32_16x16x32_bf16 v[140:143], v[124:127], v[56:59], v[140:143]\n\t" \
  "ds_read_b64_tr_b16 v[120:121], v162 offset:12288\n\t" \
  "v_mfma_f32_16x16x32_bf16 v[156:159], v[124:127], v[60:63], v[156:159]\n\t" \
  "ds_read_b64_tr_b16 v[122:123], v162 offset:14336\n\t" \
  "s_add_u32 %[th], %[th], 1\n\t" \
  "s_cbranch_vccz QF9_%=\n\t" \
  "s_waitcnt vmcnt(12)\n\t" \
  "QG9_%=:\n\t" \
  "s_waitcnt lgkmcnt(11)\n\t" \
  "v_mfma_f32_16x16x32_bf16 v[128:131], v[96:99], v[64:67], v[128:131]\n\t" \
  "ds_read_b64_tr_b16 v[124:125], v163 offset:12288\n\t" \
  "v_readlane_b32 s94, v167, 13\n\t" \
  "v_mfma_f32_16x16x32_bf16 v[144:147], v[96:99], v[68:71], v[144:147]\n\t" \
  "ds_read_b64_tr_b16 v[126:127], v163 offset:14336\n\t" \
  "s_waitcnt lgkmcnt(7)\n\t" \
  "v_cmp_lt_u32 vcc, %[th], v166\n\t" \
  "v_mfma_f32_16x16x32_bf16 v[132:135], v[100:103], v[64:67], v[132:135]\n\t" \
  "s_mov_b64 exec, vcc\n\t" \
  "buffer_load_dwordx4 v[48:51], v164, %[ob], s94 offen offset:0 sc1\n\t" \
  "s_mov_b64 exec, -1\n\t" \
  "v_mfma_f32_16x16x32_bf16 v[148:151], v[100:103], v[68:71], v[148:151]\n\t" \
  "ds_read_b64_tr_b16 v[96:97], v160 offset:16384\n\t" \
  "ds_read_b64_tr_b16 v[98:99], v160 offset:18432\n\t" \
  "v_mfma_f32_16x16x32_bf16 v[136:139], v[104:107], v[64:67], v[136:139]\n\t" \
  "s_mov_b64 exec, vcc\n\t" \
  "buffer_load_dwordx4 v[52:55], v164, %[ob], s94 offen offset:1024 sc1\n\t" \
  "s_mov_b64 exec, -1\n\t" \
  "v_mfma_f32_16x16x32_bf16 v[152:155], v[104:107], v[68:71], v[152:155]\n\t" \
  "ds_read_b64_tr_b16 v[100:101], v161 offset:16384\n\t" \
  "ds_read_b64_tr_b16 v[102:103], v161 offset:18432\n\t" \
  "v_mfma_f32_16x16x32_bf16 v[140:143], v[108:111], v[64:67], v[140:143]\n\t" \
  "s_mov_b64 exec, vcc\n\t" \
  "buffer_load_dwordx4 v[56:59], v164, %[ob], s94 offen offset:2048 sc1\n\t" \
  "s_mov_b64 exec, -1\n\t" \
  "v_mfma_f32_16x16x32_bf16 v[156:159], v[108:111], v[68:71], v[156:159]\n\t" \
  "ds_read_b64_tr_b16 v[104:105], v162 offset:16384\n\t" \
  "ds_read_b64_tr_b16 v[106:107], v162 offset:18432\n\t" \
  "s_waitcnt lgkmcnt(10)\n\t" \
  "v_mfma_f32_16x16x32_bf16 v[128:131], v[112:115], v[72:75], v[128:131]\n\t" \
  "s_mov_b64 exec, vcc\n\t" \
  "buffer_load_dwordx4 v[60:63], v164, %[ob], s94 offen offset:3072 sc1\n\t" \
  "s_mov_b64 exec, -1\n\t" \
  "v_mfma_f32_16x16x32_bf16 v[144:147], v[112:115], v[76:79], v[144:147]\n\t" \
  "ds_read_b64_tr_b16 v[108:109], v163 offset:16384\n\t" \
  "ds_read_b64_tr_b16 v[110:111], v163 offset:18432\n\t" \
  "v_mfma_f32_16x16x32_bf16 v[132:135], v[116:119], v[72:75], v[132:135]\n\t" \
  "ds_read_b64_tr_b16 v[112:113], v160 offset:20480\n\t" \
  "ds_read_b32 v166, v165\n\t" \
  "v_mfma_f32_16x16x32_bf16 v[148:151], v[116:119], v[76:79], v[148:151]\n\t" \
  "ds_read_b64_tr_b16 v[114:115], v160 offset:22528\n\t" \
  "s_waitcnt lgkmcnt(11)\n\t" \
  "v_mfma_f32_16x16x32_bf16 v[136:139], v[120:123], v[72:75], v[136:139]\n\t" \
  "ds_read_b64_tr_b16 v[116:117], v161 offset:20480\n\t" \
  "v_mfma_f32_16x16x32_bf16 v[152:155], v[120:123], v[76:79], v[152:155]\n\t" \
  "ds_read_b64_tr_b16 v[118:119], v161 offset:22528\n\t" \
  "s_sub_u32 %[iss], %[iss], vcc_lo\n\t" \
  "v_mfma_f32_16x16x32_bf16 v[140:143], v[124:127], v[72:75], v[140:143]\n\t" \
  "ds_read_b64_tr_b16 v[120:121], v162 offset:20480\n\t" \
  "v_mfma_f32_16x16x32_bf16 v[156:159], v[124:127], v[76:79], v[156:159]\n\t" \
  "ds_read_b64_tr_b16 v[122:123], v162 offset:22528\n\t" \
  "s_add_u32 %[th], %[th], 1\n\t" \
  "s_cbranch_vccz QF10_%=\n\t" \
  "s_waitcnt vmcnt(12)\n\t" \
  "QG10_%=:\n\t" \
  "s_waitcnt lgkmcnt(11)\n\t" \
  "v_mfma_f32_16x16x32_bf16 v[128:131], v[96:99], v[0:3], v[128:131]\n\t" \
  "ds_read_b64_tr_b16 v[124:125], v163 offset:20480\n\t" \
  "v_readlane_b32 s94, v167, 14\n\t" \
  "v_mfma_f32_16x16x32_bf16 v[144:147], v[96:99], v[4:7], v[144:147]\n\t" \
  "ds_read_b64_tr_b16 v[126:127], v163 offset:22528\n\t" \
  "s_waitcnt lgkmcnt(7)\n\t" \
  "v_cmp_lt_u32 vcc, %[th], v166\n\t" \
  "v_mfma_f32_16x16x32_bf16 v[132:135], v[100:103], v[0:3], v[132:135]\n\t" \
  "s_mov_b64 exec, vcc\n\t" \
  "buffer_load_dwordx4 v[64:67], v164, %[ob], s94 offen offset:0 sc1\n\t" \
  "s_mov_b64 exec, -1\n\t" \
  "v_mfma_f32_16x16x32_bf16 v[148:151], v[100:103], v[4:7], v[148:151]\n\t" \
  "ds_read_b64_tr_b16 v[96:97], v160 offset:24576\n\t" \
  "ds_read_b64_tr_b16 v[98:99], v160 offset:26624\n\t" \
  "v_mfma_f32_16x16x32_bf16 v[136:139], v[104:107], v[0:3], v[136:139]\n\t" \
  "s_mov_b64 exec, vcc\n\t" \
  "buffer_load_dwordx4 v[68:71], v164, %[ob], s94 offen offset:1024 sc1\n\t" \
  "s_mov_b64 exec, -1\n\t" \
  "v_mfma_f32_16x16x32_bf16 v[152:155], v[104:107], v[4:7], v[152:155]\n\t" \
  "ds_read_b64_tr_b16 v[100:101], v161 offset:24576\n\t" \
  "ds_read_b64_tr_b16 v[102:103], v161 offset:26624\n\t" \
  "v_mfma_f32_16x16x32_bf16 v[140:143], v[108:111], v[0:3], v[140:143]\n\t" \
  "s_mov_b64 exec, vcc\n\t" \
  "buffer_load_dwordx4 v[72:75], v164, %[ob], s94 offen offset:2048 sc1\n\t" \
  "s_mov_b64 exec, -1\n\t" \
  "v_mfma_f32_16x16x32_bf16 v[156:159], v[108:111], v[4:7], v[156:159]\n\t" \
  "ds_read_b64_tr_b16 v[104:105], v162 offset:24576\n\t" \
  "ds_read_b64_tr_b16 v[106:107], v162 offset:26624\n\t" \
  "s_waitcnt lgkmcnt(10)\n\t" \
  "v_mfma_f32_16x16x32_bf16 v[128:131], v[112:115], v[8:11], v[128:131]\n\t" \
  "s_mov_b64 exec, vcc\n\t" \
  "buffer_load_dwordx4 v[76:79], v164, %[ob], s94 offen offset:3072 sc1\n\t" \
  "s_mov_b64 exec, -1\n\t" \
  "v_mfma_f32_16x16x32_bf16 v[144:147], v[112:115], v[12:15], v[144:147]\n\t" \
  "ds_read_b64_tr_b16 v[108:109], v163 offset:24576\n\t" \
  "ds_read_b64_tr_b16 v[110:111], v163 offset:26624\n\t" \
  "v_mfma_f32_16x16x32_bf16 v[132:135], v[116:119], v[8:11], v[132:135]\n\t" \
  "ds_read_b64_tr_b16 v[112:113], v160 offset:28672\n\t" \
  "ds_read_b32 v166, v165\n\t" \
  "v_mfma_f32_16x16x32_bf16 v[148:151], v[116:119], v[12:15], v[148:151]\n\t" \
  "ds_read_b64_tr_b16 v[114:115], v160 offset:30720\n\t" \
  "s_waitcnt lgkmcnt(11)\n\t" \
  "v_mfma_f32_16x16x32_bf16 v[136:139], v[120:123], v[8:11], v[136:139]\n\t" \
  "ds_read_b64_tr_b16 v[116:117], v161 offset:28672\n\t" \
  "v_mfma_f32_16x16x32_bf16 v[152:155], v[120:123], v[12:15], v[152:155]\n\t" \
  "ds_read_b64_tr_b16 v[118:119], v161 offset:30720\n\t" \
  "s_sub_u32 %[iss], %[iss], vcc_lo\n\t" \
  "v_mfma_f32_16x16x32_bf16 v[140:143], v[124:127], v[8:11], v[140:143]\n\t" \
  "ds_read_b64_tr_b16 v[120:121], v162 offset:28672\n\t" \
  "v_mfma_f32_16x16x32_bf16 v[156:159], v[124:127], v[12:15], v[156:159]\n\t" \
  "ds_read_b64_tr_b16 v[122:123], v162 offset:30720\n\t" \
  "s_add_u32 %[th], %[th], 1\n\t" \
  "s_cbranch_vccz QF11_%=\n\t" \
  "s_waitcnt vmcnt(12)\n\t" \
  "QG11_%=:\n\t" \
  "s_waitcnt lgkmcnt(11)\n\t" \
  "v_mfma_f32_16x16x32_bf16 v[128:131], v[96:99], v[16:19], v[128:131]\n\t" \
  "ds_read_b64_tr_b16 v[124:125], v163 offset:28672\n\t" \
  "v_readlane_b32 s94, v167, 15\n\t" \
  "v_mfma_f32_16x16x32_bf16 v[144:147], v[96:99], v[20:23], v[144:147]\n\t" \
  "ds_read_b64_tr_b16 v[126:127], v163 offset:30720\n\t" \
  "s_waitcnt lgkmcnt(7)\n\t" \
  "v_cmp_lt_u32 vcc, %[th], v166\n\t" \
  "v_mfma_f32_16x16x32_bf16 v[132:135], v[100:103], v[16:19], v[132:135]\n\t" \
  "s_mov_b64 exec, vcc\n\t" \
  "buffer_load_dwordx4 v[0:3], v164, %[ob], s94 offen offset:0 sc1\n\t" \
  "s_mov_b64 exec, -1\n\t" \
  "v_mfma_f32_16x16x32_bf16 v[148:151], v[100:103], v[20:23], v[148:151]\n\t" \
  "ds_read_b64_tr_b16 v[96:97], v160 offset:32768\n\t" \
  "ds_read_b64_tr_b16 v[98:99], v160 offset:34816\n\t" \
  "v_mfma_f32_16x16x32_bf16 v[136:139], v[104:107], v[16:19], v[136:139]\n\t" \
  "s_mov_b64 exec, vcc\n\t" \
  "buffer_load_dwordx4 v[4:7], v164, %[ob], s94 offen offset:1024 sc1\n\t" \
  "s_mov_b64 exec, -1\n\t" \
  "v_mfma_f32_16x16x32_bf16 v[152:155], v[104:107], v[20:23], v[152:155]\n\t" \
  "ds_read_b64_tr_b16 v[100:101], v161 offset:32768\n\t" \
  "ds_read_b64_tr_b16 v[102:103], v161 offset:34816\n\t" \
  "v_mfma_f32_16x16x32_bf16 v[140:143], v[108:111], v[16:19], v[140:143]\n\t" \
  "s_mov_b64 exec, vcc\n\t" \
  "buffer_load_dwordx4 v[8:11], v164, %[ob], s94 offen offset:2048 sc1\n\t" \
  "s_mov_b64 exec, -1\n\t" \
  "v_mfma_f32_16x16x32_bf16 v[156:159], v[108:111], v[20:23], v[156:159]\n\t" \
  "ds_read_b64_tr_b16 v[104:105], v162 offset:32768\n\t" \
  "ds_read_b64_tr_b16 v[106:107], v162 offset:34816\n\t" \
  "s_waitcnt lgkmcnt(10)\n\t" \
  "v_mfma_f32_16x16x32_bf16 v[128:131], v[112:115], v[24:27], v[128:131]\n\t" \
  "s_mov_b64 exec, vcc\n\t" \
  "buffer_load_dwordx4 v[12:15], v164, %[ob], s94 offen offset:3072 sc1\n\t" \
  "s_mov_b64 exec, -1\n\t" \
  "v_mfma_f32_16x16x32_bf16 v[144:147], v[112:115], v[28:31], v[144:147]\n\t" \
  "ds_read_b64_tr_b16 v[108:109], v163 offset:32768\n\t" \
  "ds_read_b64_tr_b16 v[110:111], v163 offset:34816\n\t" \
  "v_mfma_f32_16x16x32_bf16 v[132:135], v[116:119], v[24:27], v[132:135]\n\t" \
  "ds_read_b64_tr_b16 v[112:113], v160 offset:36864\n\t" \
  "v_mfma_f32_16x16x32_bf16 v[148:151], v[116:119], v[28:31], v[148:151]\n\t" \
  "ds_read_b64_tr_b16 v[114:115], v160 offset:38912\n\t" \
  "s_waitcnt lgkmcnt(10)\n\t" \
  "v_mfma_f32_16x16x32_bf16 v[136:139], v[120:123], v[24:27], v[136:139]\n\t" \
  "ds_read_b64_tr_b16 v[116:117], v161 offset:36864\n\t" \
  "v_mfma_f32_16x16x32_bf16 v[152:155], v[120:123], v[28:31], v[152:155]\n\t" \
  "ds_read_b64_tr_b16 v[118:119], v161 offset:38912\n\t" \
  "s_sub_u32 %[iss], %[iss], vcc_lo\n\t" \
  "v_mfma_f32_16x16x32_bf16 v[140:143], v[124:127], v[24:27], v[140:143]\n\t" \
  "ds_read_b64_tr_b16 v[120:121], v162 offset:36864\n\t" \
  "v_mfma_f32_16x16x32_bf16 v[156:159], v[124:127], v[28:31], v[156:159]\n\t" \
  "ds_read_b64_tr_b16 v[122:123], v162 offset:38912\n\t" \
  "s_add_u32 %[th], %[th], 1\n\t" \
  "s_cbranch_vccz QF12_%=\n\t" \
  "s_waitcnt vmcnt(12)\n\t" \
  "QG12_%=:\n\t" \
  "s_waitcnt lgkmcnt(10)\n\t" \
  "v_mfma_f32_16x16x32_bf16 v[128:131], v[96:99], v[32:35], v[128:131]\n\t" \
  "ds_read_b64_tr_b16 v[124:125], v163 offset:36864\n\t" \
  "v_mfma_f32_16x16x32_bf16 v[144:147], v[96:99], v[36:39], v[144:147]\n\t" \
  "ds_read_b64_tr_b16 v[126:127], v163 offset:38912\n\t" \
  "v_mfma_f32_16x16x32_bf16 v[132:135], v[100:103], v[32:35], v[132:135]\n\t" \
  "ds_read_b64_tr_b16 v[96:97], v160 offset:40960\n\t" \
  "v_mfma_f32_16x16x32_bf16 v[148:151], v[100:103], v[36:39], v[148:151]\n\t" \
  "ds_read_b64_tr_b16 v[98:99], v160 offset:43008\n\t" \
  "s_waitcnt lgkmcnt(10)\n\t" \
  "v_mfma_f32_16x16x32_bf16 v[136:139], v[104:107], v[32:35], v[136:139]\n\t" \
  "ds_read_b64_tr_b16 v[100:101], v161 offset:40960\n\t" \
  "v_mfma_f32_16x16x32_bf16 v[152:155], v[104:107], v[36:39], v[152:155]\n\t" \
  "ds_read_b64_tr_b16 v[102:103], v161 offset:43008\n\t" \
  "v_mfma_f32_16x16x32_bf16 v[140:143], v[108:111], v[32:35], v[140:143]\n\t" \
  "ds_read_b64_tr_b16 v[104:105], v162 offset:40960\n\t" \
  "v_mfma_f32_16x16x32_bf16 v[156:159], v[108:111], v[36:39], v[156:159]\n\t" \
  "ds_read_b64_tr_b16 v[106:107], v162 offset:43008\n\t" \
  "s_waitcnt lgkmcnt(10)\n\t" \
  "v_mfma_f32_16x16x32_bf16 v[128:131], v[112:115], v[40:43], v[128:131]\n\t" \
  "ds_read_b64_tr_b16 v[108:109], v163 offset:40960\n\t" \
  "v_mfma_f32_16x16x32_bf16 v[144:147], v[112:115], v[44:47], v[144:147]\n\t" \
  "ds_read_b64_tr_b16 v[110:111], v163 offset:43008\n\t" \
  "v_mfma_f32_16x16x32_bf16 v[132:135], v[116:119], v[40:43], v[132:135]\n\t" \
  "ds_read_b64_tr_b16 v[112:113], v160 offset:45056\n\t" \
  "v_mfma_f32_16x16x32_bf16 v[148:151], v[116:119], v[44:47], v[148:151]\n\t" \
  "ds_read_b64_tr_b16 v[114:115], v160 offset:47104\n\t" \
  "s_waitcnt lgkmcnt(10)\n\t" \
  "v_mfma_f32_16x16x32_bf16 v[136:139], v[120:123], v[40:43], v[136:139]\n\t" \
  "ds_read_b64_tr_b16 v[116:117], v161 offset:45056\n\t" \
  "v_mfma_f32_16x16x32_bf16 v[152:155], v[120:123], v[44:47], v[152:155]\n\t" \
  "ds_read_b64_tr_b16 v[118:119], v161 offset:47104\n\t" \
  "v_mfma_f32_16x16x32_bf16 v[140:143], v[124:127], v[40:43], v[140:143]\n\t" \
  "ds_read_b64_tr_b16 v[120:121], v162 offset:45056\n\t" \
  "v_mfma_f32_16x16x32_bf16 v[156:159], v[124:127], v[44:47], v[156:159]\n\t" \
  "ds_read_b64_tr_b16 v[122:123], v162 offset:47104\n\t" \
  "s_cbranch_vccz QF13_%=\n\t" \
  "s_waitcnt vmcnt(8)\n\t" \
  "QG13_%=:\n\t" \
  "s_waitcnt lgkmcnt(10)\n\t" \
  "v_mfma_f32_16x16x32_bf16 v[128:131], v[96:99], v[48:51], v[128:131]\n\t" \
  "ds_read_b64_tr_b16 v[124:125], v163 offset:45056\n\t" \
  "v_mfma_f32_16x16x32_bf16 v[144:147], v[96:99], v[52:55], v[144:147]\n\t" \
  "ds_read_b64_tr_b16 v[126:127], v163 offset:47104\n\t" \
  "v_mfma_f32_16x16x32_bf16 v[132:135], v[100:103], v[48:51], v[132:135]\n\t" \
  "ds_read_b64_tr_b16 v[96:97], v160 offset:49152\n\t" \
  "v_mfma_f32_16x16x32_bf16 v[148:151], v[100:103], v[52:55], v[148:151]\n\t" \
  "ds_read_b64_tr_b16 v[98:99], v160 offset:51200\n\t" \
  "s_waitcnt lgkmcnt(10)\n\t" \
  "v_mfma_f32_16x16x32_bf16 v[136:139], v[104:107], v[48:51], v[136:139]\n\t" \
  "ds_read_b64_tr_b16 v[100:101], v161 offset:49152\n\t" \
  "v_mfma_f32_16x16x32_bf16 v[152:155], v[104:107], v[52:55], v[152:155]\n\t" \
  "ds_read_b64_tr_b16 v[102:103], v161 offset:51200\n\t" \
  "v_mfma_f32_16x16x32_bf16 v[140:143], v[108:111], v[48:51], v[140:143]\n\t" \
  "ds_read_b64_tr_b16 v[104:105], v162 offset:49152\n\t" \
  "v_mfma_f32_16x16x32_bf16 v[156:159], v[108:111], v[52:55], v[156:159]\n\t" \
  "ds_read_b64_tr_b16 v[106:107], v162 offset:51200\n\t" \
  "s_waitcnt lgkmcnt(10)\n\t" \
  "v_mfma_f32_16x16x32_bf16 v[128:131], v[112:115], v[56:59], v[128:131]\n\t" \
  "ds_read_b64_tr_b16 v[108:109], v163 offset:49152\n\t" \
  "v_mfma_f32_16x16x32_bf16 v[144:147], v[112:115], v[60:63], v[144:147]\n\t" \
  "ds_read_b64_tr_b16 v[110:111], v163 offset:51200\n\t" \
  "v_mfma_f32_16x16x32_bf16 v[132:135], v[116:119], v[56:59], v[132:135]\n\t" \
  "ds_read_b64_tr_b16 v[112:113], v160 offset:53248\n\t" \
  "v_mfma_f32_16x16x32_bf16 v[148:151], v[116:119], v[60:63], v[148:151]\n\t" \
  "ds_read_b64_tr_b16 v[114:115], v160 offset:55296\n\t" \
  "s_waitcnt lgkmcnt(10)\n\t" \
  "v_mfma_f32_16x16x32_bf16 v[136:139], v[120:123], v[56:59], v[136:139]\n\t" \
  "ds_read_b64_tr_b16 v[116:117], v161 offset:53248\n\t" \
  "v_mfma_f32_16x16x32_bf16 v[152:155], v[120:123], v[60:63], v[152:155]\n\t" \
  "ds_read_b64_tr_b16 v[118:119], v161 offset:55296\n\t" \
  "v_mfma_f32_16x16x32_bf16 v[140:143], v[124:127], v[56:59], v[140:143]\n\t" \
  "ds_read_b64_tr_b16 v[120:121], v162 offset:53248\n\t" \
  "v_mfma_f32_16x16x32_bf16 v[156:159], v[124:127], v[60:63], v[156:159]\n\t" \
  "ds_read_b64_tr_b16 v[122:123], v162 offset:55296\n\t" \
  "s_cbranch_vccz QF14_%=\n\t" \
  "s_waitcnt vmcnt(4)\n\t" \
  "QG14_%=:\n\t" \
  "s_waitcnt lgkmcnt(10)\n\t" \
  "v_mfma_f32_16x16x32_bf16 v[128:131], v[96:99], v[64:67], v[128:131]\n\t" \
  "ds_read_b64_tr_b16 v[124:125], v163 offset:53248\n\t" \
  "v_mfma_f32_16x16x32_bf16 v[144:147], v[96:99], v[68:71], v[144:147]\n\t" \
  "ds_read_b64_tr_b16 v[126:127], v163 offset:55296\n\t" \
  "v_mfma_f32_16x16x32_bf16 v[132:135], v[100:103], v[64:67], v[132:135]\n\t" \
  "ds_read_b64_tr_b16 v[96:97], v160 offset:57344\n\t" \
  "v_mfma_f32_16x16x32_bf16 v[148:151], v[100:103], v[68:71], v[148:151]\n\t" \
  "ds_read_b64_tr_b16 v[98:99], v160 offset:59392\n\t" \
  "s_waitcnt lgkmcnt(10)\n\t" \
  "v_mfma_f32_16x16x32_bf16 v[136:139], v[104:107], v[64:67], v[136:139]\n\t" \
  "ds_read_b64_tr_b16 v[100:101], v161 offset:57344\n\t" \
  "v_mfma_f32_16x16x32_bf16 v[152:155], v[104:107], v[68:71], v[152:155]\n\t" \
  "ds_read_b64_tr_b16 v[102:103], v161 offset:59392\n\t" \
  "v_mfma_f32_16x16x32_bf16 v[140:143], v[108:111], v[64:67], v[140:143]\n\t" \
  "ds_read_b64_tr_b16 v[104:105], v162 offset:57344\n\t" \
  "v_mfma_f32_16x16x32_bf16 v[156:159], v[108:111], v[68:71], v[156:159]\n\t" \
  "ds_read_b64_tr_b16 v[106:107], v162 offset:59392\n\t" \
  "s_waitcnt lgkmcnt(10)\n\t" \
  "v_mfma_f32_16x16x32_bf16 v[128:131], v[112:115], v[72:75], v[128:131]\n\t" \
  "ds_read_b64_tr_b16 v[108:109], v163 offset:57344\n\t" \
  "v_mfma_f32_16x16x32_bf16 v[144:147], v[112:115], v[76:79], v[144:147]\n\t" \
  "ds_read_b64_tr_b16 v[110:111], v163 offset:59392\n\t" \
  "v_mfma_f32_16x16x32_bf16 v[132:135], v[116:119], v[72:75], v[132:135]\n\t" \
  "ds_read_b64_tr_b16 v[112:113], v160 offset:61440\n\t" \
  "v_mfma_f32_16x16x32_bf16 v[148:151], v[116:119], v[76:79], v[148:151]\n\t" \
  "ds_read_b64_tr_b16 v[114:115], v160 offset:63488\n\t" \
  "s_waitcnt lgkmcnt(10)\n\t" \
  "v_mfma_f32_16x16x32_bf16 v[136:139], v[120:123], v[72:75], v[136:139]\n\t" \
  "ds_read_b64_tr_b16 v[116:117], v161 offset:61440\n\t" \
  "v_mfma_f32_16x16x32_bf16 v[152:155], v[120:123], v[76:79], v[152:155]\n\t" \
  "ds_read_b64_tr_b16 v[118:119], v161 offset:63488\n\t" \
  "v_mfma_f32_16x16x32_bf16 v[140:143], v[124:127], v[72:75], v[140:143]\n\t" \
  "ds_read_b64_tr_b16 v[120:121], v162 offset:61440\n\t" \
  "v_mfma_f32_16x16x32_bf16 v[156:159], v[124:127], v[76:79], v[156:159]\n\t" \
  "ds_read_b64_tr_b16 v[122:123], v162 offset:63488\n\t" \
  "s_cbranch_vccz QF15_%=\n\t" \
  "s_waitcnt vmcnt(0)\n\t" \
  "QG15_%=:\n\t" \
  "s_waitcnt lgkmcnt(10)\n\t" \
  "v_mfma_f32_16x16x32_bf16 v[128:131], v[96:99], v[0:3], v[128:131]\n\t" \
  "ds_read_b64_tr_b16 v[124:125], v163 offset:61440\n\t" \
  "v_mfma_f32_16x16x32_bf16 v[144:147], v[96:99], v[4:7], v[144:147]\n\t" \
  "ds_read_b64_tr_b16 v[126:127], v163 offset:63488\n\t" \
  "v_mfma_f32_16x16x32_bf16 v[132:135], v[100:103], v[0:3], v[132:135]\n\t" \
  "v_mfma_f32_16x16x32_bf16 v[148:151], v[100:103], v[4:7], v[148:151]\n\t" \
  "s_waitcnt lgkmcnt(8)\n\t" \
  "v_mfma_f32_16x16x32_bf16 v[136:139], v[104:107], v[0:3], v[136:139]\n\t" \
  "v_mfma_f32_16x16x32_bf16 v[152:155], v[104:107], v[4:7], v[152:155]\n\t" \
  "v_mfma_f32_16x16x32_bf16 v[140:143], v[108:111], v[0:3], v[140:143]\n\t" \
  "v_mfma_f32_16x16x32_bf16 v[156:159], v[108:111], v[4:7], v[156:159]\n\t" \
  "s_waitcnt lgkmcnt(4)\n\t" \
  "v_mfma_f32_16x16x32_bf16 v[128:131], v[112:115], v[8:11], v[128:131]\n\t" \
  "v_mfma_f32_16x16x32_bf16 v[144:147], v[112:115], v[12:15], v[144:147]\n\t" \
  "v_mfma_f32_16x16x32_bf16 v[132:135], v[116:119], v[8:11], v[132:135]\n\t" \
  "v_mfma_f32_16x16x32_bf16 v[148:151], v[116:119], v[12:15], v[148:151]\n\t" \
  "s_waitcnt lgkmcnt(0)\n\t" \
  "v_mfma_f32_16x16x32_bf16 v[136:139], v[120:123], v[8:11], v[136:139]\n\t" \
  "v_mfma_f32_16x16x32_bf16 v[152:155], v[120:123], v[12:15], v[152:155]\n\t" \
  "v_mfma_f32_16x16x32_bf16 v[140:143], v[124:127], v[8:11], v[140:143]\n\t" \
  "v_mfma_f32_16x16x32_bf16 v[156:159], v[124:127], v[12:15], v[156:159]\n\t" \
  "s_branch QX_%=\n\t" \
  "QF1_%=:\n\t" \
  "s_mov_b32 %[c], 1\n\t" \
  "s_getpc_b64 s[92:93]\n\t" \
  "s_branch QC_%=\n\t" \
  "s_branch QG1_%=\n\t" \
  "QF2_%=:\n\t" \
  "s_mov_b32 %[c], 2\n\t" \
  "s_getpc_b64 s[92:93]\n\t" \
  "s_branch QC_%=\n\t" \
  "s_branch QG2_%=\n\t" \
  "QF3_%=:\n\t" \
  "s_mov_b32 %[c], 3\n\t" \
  "s_getpc_b64 s[92:93]\n\t" \
  "s_branch QC_%=\n\t" \
  "s_branch QG3_%=\n\t" \
  "QF4_%=:\n\t" \
  "s_mov_b32 %[c], 4\n\t" \
  "s_getpc_b64 s[92:93]\n\t" \
  "s_branch QC_%=\n\t" \
  "s_branch QG4_%=\n\t" \
  "QF5_%=:\n\t" \
  "s_mov_b32 %[c], 5\n\t" \
  "s_getpc_b64 s[92:93]\n\t" \
  "s_branch QC_%=\n\t" \
  "s_branch QG5_%=\n\t" \
  "QF6_%=:\n\t" \
  "s_mov_b32 %[c], 6\n\t" \
  "s_getpc_b64 s[92:93]\n\t" \
  "s_branch QC_%=\n\t" \
  "s_branch QG6_%=\n\t" \
  "QF7_%=:\n\t" \
  "s_mov_b32 %[c], 7\n\t" \
  "s_getpc_b64 s[92:93]\n\t" \
  "s_branch QC_%=\n\t" \
  "s_branch QG7_%=\n\t" \
  "QF8_%=:\n\t" \
  "s_mov_b32 %[c], 8\n\t" \
  "s_getpc_b64 s[92:93]\n\t" \
  "s_branch QC_%=\n\t" \
  "s_branch QG8_%=\n\t" \
  "QF9_%=:\n\t" \
  "s_mov_b32 %[c], 9\n\t" \
  "s_getpc_b64 s[92:93]\n\t" \
  "s_branch QC_%=\n\t" \
  "s_branch QG9_%=\n\t" \
  "QF10_%=:\n\t" \
  "s_mov_b32 %[c], 10\n\t" \
  "s_getpc_b64 s[92:93]\n\t" \
  "s_branch QC_%=\n\t" \
  "s_branch QG10_%=\n\t" \
  "QF11_%=:\n\t" \
  "s_mov_b32 %[c], 11\n\t" \
  "s_getpc_b64 s[92:93]\n\t" \
  "s_branch QC_%=\n\t" \
  "s_branch QG11_%=\n\t" \
  "QF12_%=:\n\t" \
  "s_mov_b32 %[c], 12\n\t" \
  "s_getpc_b64 s[92:93]\n\t" \
  "s_branch QC_%=\n\t" \
  "s_branch QG12_%=\n\t" \
  "QF13_%=:\n\t" \
  "s_mov_b32 %[c], 13\n\t" \
  "s_getpc_b64 s[92:93]\n\t" \
  "s_branch QC_%=\n\t" \
  "s_branch QG13_%=\n\t" \
  "QF14_%=:\n\t" \
  "s_mov_b32 %[c], 14\n\t" \
  "s_getpc_b64 s[92:93]\n\t" \
  "s_branch QC_%=\n\t" \
  "s_branch QG14_%=\n\t" \
  "QF15_%=:\n\t" \
  "s_mov_b32 %[c], 15\n\t" \
  "s_getpc_b64 s[92:93]\n\t" \
  "s_branch QC_%=\n\t" \
  "s_branch QG15_%=\n\t" \
  "QC_%=:\n\t" \
  "s_add_u32 s92, s92, 4\n\t" \
  "s_addc_u32 s93, s93, 0\n\t" \
  "QU_%=:\n\t" \
  "ds_read_b32 v166, v165\n\t" \
  "s_max_i32 %[t1], %[c], 0\n\t" \
  "s_add_u32 %[t1], %[t1], 4\n\t" \
  "s_waitcnt lgkmcnt(0)\n\t" \
  "v_readfirstlane_b32 %[t0], v166\n\t" \
  "s_nop 0\n\t" \
  "s_min_u32 %[t1], %[t1], %[t0]\n\t" \
  "QL_%=:\n\t" \
  "s_cmp_ge_u32 %[iss], %[t1]\n\t" \
  "s_cbranch_scc1 QD_%=\n\t" \
  "s_mov_b32 %[t0], %[iss]\n\t" \
  "s_sub_u32 %[th], %[t0], 5\n\t" \
  "s_cselect_b32 %[t0], %[t0], %[th]\n\t" \
  "s_sub_u32 %[th], %[t0], 5\n\t" \
  "s_cselect_b32 %[t0], %[t0], %[th]\n\t" \
  "s_sub_u32 %[th], %[t0], 5\n\t" \
  "s_cselect_b32 %[t0], %[t0], %[th]\n\t" \
  "s_cmp_eq_u32 %[t0], 1\n\t" \
  "s_cbranch_scc1 QS1_%=\n\t" \
  "s_cmp_eq_u32 %[t0], 2\n\t" \
  "s_cbranch_scc1 QS2_%=\n\t" \
  "s_cmp_eq_u32 %[t0], 3\n\t" \
  "s_cbranch_scc1 QS3_%=\n\t" \
  "s_cmp_eq_u32 %[t0], 4\n\t" \
  "s_cbranch_scc1 QS4_%=\n\t" \
  "QS0_%=:\n\t" \
  "v_readlane_b32 s94, v167, %[iss]\n\t" \
  "s_nop 4\n\t" \
  "buffer_load_dwordx4 v[0:3], v164, %[ob], s94 offen offset:0 sc1\n\t" \
  "buffer_load_dwordx4 v[4:7], v164, %[ob], s94 offen offset:1024 sc1\n\t" \
  "buffer_load_dwordx4 v[8:11], v164, %[ob], s94 offen offset:2048 sc1\n\t" \
  "buffer_load_dwordx4 v[12:15], v164, %[ob], s94 offen offset:3072 sc1\n\t" \
  "s_add_u32 %[iss], %[iss], 1\n\t" \
  "s_cmp_ge_u32 %[iss], %[t1]\n\t" \
  "s_cbranch_scc1 QD_%=\n\t" \
  "QS1_%=:\n\t" \
  "v_readlane_b32 s94, v167, %[iss]\n\t" \
  "s_nop 4\n\t" \
  "buffer_load_dwordx4 v[16:19], v164, %[ob], s94 offen offset:0 sc1\n\t" \
  "buffer_load_dwordx4 v[20:23], v164, %[ob], s94 offen offset:1024 sc1\n\t" \
  "buffer_load_dwordx4 v[24:27], v164, %[ob], s94 offen offset:2048 sc1\n\t" \
  "buffer_load_dwordx4 v[28:31], v164, %[ob], s94 offen offset:3072 sc1\n\t" \
  "s_add_u32 %[iss], %[iss], 1\n\t" \
  "s_cmp_ge_u32 %[iss], %[t1]\n\t" \
  "s_cbranch_scc1 QD_%=\n\t" \
  "QS2_%=:\n\t" \
  "v_readlane_b32 s94, v167, %[iss]\n\t" \
  "s_nop 4\n\t" \
  "buffer_load_dwordx4 v[32:35], v164, %[ob], s94 offen offset:0 sc1\n\t" \
  "buffer_load_dwordx4 v[36:39], v164, %[ob], s94 offen offset:1024 sc1\n\t" \
  "buffer_load_dwordx4 v[40:43], v164, %[ob], s94 offen offset:2048 sc1\n\t" \
  "buffer_load_dwordx4 v[44:47], v164, %[ob], s94 offen offset:3072 sc1\n\t" \
  "s_add_u32 %[iss], %[iss], 1\n\t" \
  "s_cmp_ge_u32 %[iss], %[t1]\n\t" \
  "s_cbranch_scc1 QD_%=\n\t" \
  "QS3_%=:\n\t" \
  "v_readlane_b32 s94, v167, %[iss]\n\t" \
  "s_nop 4\n\t" \
  "buffer_load_dwordx4 v[48:51], v164, %[ob], s94 offen offset:0 sc1\n\t" \
  "buffer_load_dwordx4 v[52:55], v164, %[ob], s94 offen offset:1024 sc1\n\t" \
  "buffer_load_dwordx4 v[56:59], v164, %[ob], s94 offen offset:2048 sc1\n\t" \
  "buffer_load_dwordx4 v[60:63], v164, %[ob], s94 offen offset:3072 sc1\n\t" \
  "s_add_u32 %[iss], %[iss], 1\n\t" \
  "s_cmp_ge_u32 %[iss], %[t1]\n\t" \
  "s_cbranch_scc1 QD_%=\n\t" \
  "QS4_%=:\n\t" \
  "v_readlane_b32 s94, v167, %[iss]\n\t" \
  "s_nop 4\n\t" \
  "buffer_load_dwordx4 v[64:67], v164, %[ob], s94 offen offset:0 sc1\n\t" \
  "buffer_load_dwordx4 v[68:71], v164, %[ob], s94 offen offset:1024 sc1\n\t" \
  "buffer_load_dwordx4 v[72:75], v164, %[ob], s94 offen offset:2048 sc1\n\t" \
  "buffer_load_dwordx4 v[76:79], v164, %[ob], s94 offen offset:3072 sc1\n\t" \
  "s_add_u32 %[iss], %[iss], 1\n\t" \
  "s_cmp_ge_u32 %[iss], %[t1]\n\t" \
  "s_cbranch_scc0 QS0_%=\n\t" \
  "QD_%=:\n\t" \
  "s_cmp_lt_i32 %[c], 0\n\t" \
  "s_cbranch_scc1 QR_%=\n\t" \
  "s_cmp_gt_u32 %[iss], %[c]\n\t" \
  "s_cbranch_scc1 QW_%=\n\t" \
  "s_addk_i32 %[cn], 1\n\t" \
  "s_cmp_gt_u32 %[cn], 0x400000\n\t" \
  "s_cbranch_scc1 QT_%=\n\t" \
  "s_sleep 1\n\t" \
  "s_branch QU_%=\n\t" \
  "QT_%=:\n\t" \
  "s_add_u32 %[t1], %[c], 1\n\t" \
  "s_branch QL_%=\n\t" \
  "QW_%=:\n\t" \
  "s_cmp_eq_u32 %[iss], 16\n\t" \
  "s_cselect_b64 vcc, -1, 0\n\t" \
  "s_sub_u32 %[t0], %[iss], %[c]\n\t" \
  "s_cmp_eq_u32 %[t0], 4\n\t" \
  "s_cselect_b32 %[th], %[iss], 99\n\t" \
  "s_waitcnt vmcnt(12)\n\t" \
  "s_cmp_ge_u32 %[t0], 4\n\t" \
  "s_cbranch_scc1 QR_%=\n\t" \
  "s_waitcnt vmcnt(8)\n\t" \
  "s_cmp_ge_u32 %[t0], 3\n\t" \
  "s_cbranch_scc1 QR_%=\n\t" \
  "s_waitcnt vmcnt(4)\n\t" \
  "s_cmp_ge_u32 %[t0], 2\n\t" \
  "s_cbranch_scc1 QR_%=\n\t" \
  "s_waitcnt vmcnt(0)\n\t" \
  "QR_%=:\n\t" \
  "s_setpc_b64 s[92:93]\n\t" \
  "QX_%=:\n\t" \
  "s_nop 7\n\t"
#define G3_CLOB "v0", "v1", "v2", "v3", "v4", "v5", "v6", "v7", "v8", "v9", "v10", "v11", "v12", "v13", "v14", "v15", "v16", "v17", "v18", "v19", "v20", "v21", "v22", "v23", "v24", "v25", "v26", "v27", "v28", "v29", "v30", "v31", "v32", "v33", "v34", "v35", "v36", "v37", "v38", "v39", "v40", "v41", "v42", "v43", "v44", "v45", "v46", "v47", "v48", "v49", "v50", "v51", "v52", "v53", "v54", "v55", "v56", "v57", "v58", "v59", "v60", "v61", "v62", "v63", "v64", "v65", "v66", "v67", "v68", "v69", "v70", "v71", "v72", "v73", "v74", "v75", "v76", "v77", "v78", "v79", "v80", "v81", "v82", "v83", "v84", "v85", "v86", "v87", "v88", "v89", "v90", "v91", "v92", "v93", "v94", "v95", "v96", "v97", "v98", "v99", "v100", "v101", "v102", "v103", "v104", "v105", "v106", "v107", "v108", "v109", "v110", "v111", "v112", "v113", "v114", "v115", "v116", "v117", "v118", "v119", "v120", "v121", "v122", "v123", "v124", "v125", "v126", "v127", "v166", "s88", "s89", "s92", "s93", "s94"

#define PP_ASM \
  "v_mov_b32 v60, 0\n\t" \
  "s_mov_b32 %[t0], %[n0]\n\t" \
  "s_cmp_eq_u32 %[t0], 0\n\t" \
  "s_cbranch_scc1 PAx_%=\n\t" \
  "PA_%=:\n\t" \
  "s_barrier\n\t" \
  "s_sub_u32 %[t0], %[t0], 1\n\t" \
  "s_cmp_lg_u32 %[t0], 0\n\t" \
  "s_cbranch_scc1 PA_%=\n\t" \
  "PAx_%=:\n\t" \
  "s_mov_b32 m0, %[shd]\n\t" \
  "global_load_dword v160, v60, %[fl] sc1\n\t" \
  "global_load_lds_dwordx4 v163, %[shp]\n\t" \
  "s_mov_b32 m0, %[tgd]\n\t" \
  "s_mov_b64 exec, %[em]\n\t" \
  "global_load_lds_dword v60, %[fl] sc1\n\t" \
  "s_mov_b64 exec, -1\n\t" \
  "global_load_ushort v161, v60, %[hp]\n\t" \
  "global_load_ushort v162, v60, %[dp]\n\t" \
  "global_load_dwordx4 v[0:3], v152, %[kb0]\n\t" \
  "global_load_dwordx4 v[4:7], v153, %[kb0]\n\t" \
  "global_load_dwordx4 v[8:11], v154, %[kb0]\n\t" \
  "global_load_dwordx4 v[12:15], v155, %[kb3] offset:0\n\t" \
  "global_load_dwordx4 v[28:31], v155, %[vb0] offset:0\n\t" \
  "s_setprio 2\n\t" \
  "s_setprio 3\n\t" \
  "s_barrier\n\t" \
  "global_load_dwordx4 v[16:19], v155, %[kb3] offset:2048\n\t" \
  "global_load_dwordx4 v[32:35], v155, %[vb0] offset:2048\n\t" \
  "global_load_dwordx4 v[20:23], v155, %[kb5] offset:0\n\t" \
  "s_setprio 2\n\t" \
  "s_waitcnt vmcnt(8)\n\t" \
  "ds_read_b128 v[128:131], v156 offset:0\n\t" \
  "ds_read_b128 v[132:135], v156 offset:16\n\t" \
  "ds_read_b128 v[136:139], v156 offset:32\n\t" \
  "ds_read_b128 v[140:143], v156 offset:48\n\t" \
  "s_waitcnt lgkmcnt(0)\n\t" \
  "v_lshlrev_b32 v64, 16, v128\n\t" \
  "v_and_b32 v65, 0xffff0000, v128\n\t" \
  "v_lshlrev_b32 v66, 16, v129\n\t" \
  "v_and_b32 v67, 0xffff0000, v129\n\t" \
  "v_lshlrev_b32 v68, 16, v130\n\t" \
  "v_and_b32 v69, 0xffff0000, v130\n\t" \
  "v_lshlrev_b32 v70, 16, v131\n\t" \
  "v_and_b32 v71, 0xffff0000, v131\n\t" \
  "v_lshlrev_b32 v72, 16, v132\n\t" \
  "v_and_b32 v73, 0xffff0000, v132\n\t" \
  "v_lshlrev_b32 v74, 16, v133\n\t" \
  "v_and_b32 v75, 0xffff0000, v133\n\t" \
  "v_lshlrev_b32 v76, 16, v134\n\t" \
  "v_and_b32 v77, 0xffff0000, v134\n\t" \
  "v_lshlrev_b32 v78, 16, v135\n\t" \
  "v_and_b32 v79, 0xffff0000, v135\n\t" \
  "v_lshlrev_b32 v80, 16, v136\n\t" \
  "v_and_b32 v81, 0xffff0000, v136\n\t" \
  "v_lshlrev_b32 v82, 16, v137\n\t" \
  "v_and_b32 v83, 0xffff0000, v137\n\t" \
  "v_lshlrev_b32 v84, 16, v138\n\t" \
  "v_and_b32 v85, 0xffff0000, v138\n\t" \
  "v_lshlrev_b32 v86, 16, v139\n\t" \
  "v_and_b32 v87, 0xffff0000, v139\n\t" \
  "v_lshlrev_b32 v88, 16, v140\n\t" \
  "v_and_b32 v89, 0xffff0000, v140\n\t" \
  "v_lshlrev_b32 v90, 16, v141\n\t" \
  "v_and_b32 v91, 0xffff0000, v141\n\t" \
  "v_lshlrev_b32 v92, 16, v142\n\t" \
  "v_and_b32 v93, 0xffff0000, v142\n\t" \
  "v_lshlrev_b32 v94, 16, v143\n\t" \
  "v_and_b32 v95, 0xffff0000, v143\n\t" \
  "s_waitcnt vmcnt(7)\n\t" \
  "s_mov_b64 exec, %[zm]\n\t" \
  "v_mov_b32 v0, 0\n\t" \
  "v_mov_b32 v1, 0\n\t" \
  "v_mov_b32 v2, 0\n\t" \
  "v_mov_b32 v3, 0\n\t" \
  "s_mov_b64 exec, -1\n\t" \
  "v_lshlrev_b32 v128, 16, v0\n\t" \
  "v_and_b32 v129, 0xffff0000, v0\n\t" \
  "v_lshlrev_b32 v130, 16, v1\n\t" \
  "v_and_b32 v131, 0xffff0000, v1\n\t" \
  "v_lshlrev_b32 v132, 16, v2\n\t" \
  "v_and_b32 v133, 0xffff0000, v2\n\t" \
  "v_lshlrev_b32 v134, 16, v3\n\t" \
  "v_and_b32 v135, 0xffff0000, v3\n\t" \
  "v_mul_f32 v96, v128, v67\n\t" \
  "v_mul_f32 v97, v129, v71\n\t" \
  "v_mul_f32 v98, v130, v75\n\t" \
  "v_mul_f32 v99, v131, v79\n\t" \
  "v_mul_f32 v100, v132, v83\n\t" \
  "v_mul_f32 v101, v133, v87\n\t" \
  "v_mul_f32 v102, v134, v91\n\t" \
  "v_mul_f32 v103, v135, v95\n\t" \
  "s_waitcnt vmcnt(6)\n\t" \
  "s_mov_b64 exec, %[zm]\n\t" \
  "v_mov_b32 v4, 0\n\t" \
  "v_mov_b32 v5, 0\n\t" \
  "v_mov_b32 v6, 0\n\t" \
  "v_mov_b32 v7, 0\n\t" \
  "s_mov_b64 exec, -1\n\t" \
  "v_lshlrev_b32 v128, 16, v4\n\t" \
  "v_and_b32 v129, 0xffff0000, v4\n\t" \
  "v_lshlrev_b32 v130, 16, v5\n\t" \
  "v_and_b32 v131, 0xffff0000, v5\n\t" \
  "v_lshlrev_b32 v132, 16, v6\n\t" \
  "v_and_b32 v133, 0xffff0000, v6\n\t" \
  "v_lshlrev_b32 v134, 16, v7\n\t" \
  "v_and_b32 v135, 0xffff0000, v7\n\t" \
  "v_fmac_f32 v96, v128, v66\n\t" \
  "v_fmac_f32 v97, v129, v70\n\t" \
  "v_fmac_f32 v98, v130, v74\n\t" \
  "v_fmac_f32 v99, v131, v78\n\t" \
  "v_fmac_f32 v100, v132, v82\n\t" \
  "v_fmac_f32 v101, v133, v86\n\t" \
  "v_fmac_f32 v102, v134, v90\n\t" \
  "v_fmac_f32 v103, v135, v94\n\t" \
  "v_mul_f32 v104, v128, v67\n\t" \
  "v_mul_f32 v105, v129, v71\n\t" \
  "v_mul_f32 v106, v130, v75\n\t" \
  "v_mul_f32 v107, v131, v79\n\t" \
  "v_mul_f32 v108, v132, v83\n\t" \
  "v_mul_f32 v109, v133, v87\n\t" \
  "v_mul_f32 v110, v134, v91\n\t" \
  "v_mul_f32 v111, v135, v95\n\t" \
  "s_waitcnt vmcnt(5)\n\t" \
  "s_mov_b64 exec, %[zm]\n\t" \
  "v_mov_b32 v8, 0\n\t" \
  "v_mov_b32 v9, 0\n\t" \
  "v_mov_b32 v10, 0\n\t" \
  "v_mov_b32 v11, 0\n\t" \
  "s_mov_b64 exec, -1\n\t" \
  "s_setprio 3\n\t" \
  "s_barrier\n\t" \
  "global_load_dwordx4 v[36:39], v155, %[vb2] offset:0\n\t" \
  "global_load_dwordx4 v[24:27], v155, %[kb5] offset:2048\n\t" \
  "global_load_dwordx4 v[40:43], v155, %[vb2] offset:2048\n\t" \
  "s_setprio 2\n\t" \
  "v_lshlrev_b32 v128, 16, v8\n\t" \
  "v_and_b32 v129, 0xffff0000, v8\n\t" \
  "v_lshlrev_b32 v130, 16, v9\n\t" \
  "v_and_b32 v131, 0xffff0000, v9\n\t" \
  "v_lshlrev_b32 v132, 16, v10\n\t" \
  "v_and_b32 v133, 0xffff0000, v10\n\t" \
  "v_lshlrev_b32 v134, 16, v11\n\t" \
  "v_and_b32 v135, 0xffff0000, v11\n\t" \
  "v_fmac_f32 v96, v128, v65\n\t" \
  "v_fmac_f32 v97, v129, v69\n\t" \
  "v_fmac_f32 v98, v130, v73\n\t" \
  "v_fmac_f32 v99, v131, v77\n\t" \
  "v_fmac_f32 v100, v132, v81\n\t" \
  "v_fmac_f32 v101, v133, v85\n\t" \
  "v_fmac_f32 v102, v134, v89\n\t" \
  "v_fmac_f32 v103, v135, v93\n\t" \
  "v_fmac_f32 v104, v128, v66\n\t" \
  "v_fmac_f32 v105, v129, v70\n\t" \
  "v_fmac_f32 v106, v130, v74\n\t" \
  "v_fmac_f32 v107, v131, v78\n\t" \
  "v_fmac_f32 v108, v132, v82\n\t" \
  "v_fmac_f32 v109, v133, v86\n\t" \
  "v_fmac_f32 v110, v134, v90\n\t" \
  "v_fmac_f32 v111, v135, v94\n\t" \
  "v_mul_f32 v112, v128, v67\n\t" \
  "v_mul_f32 v113, v129, v71\n\t" \
  "v_mul_f32 v114, v130, v75\n\t" \
  "v_mul_f32 v115, v131, v79\n\t" \
  "v_mul_f32 v116, v132, v83\n\t" \
  "v_mul_f32 v117, v133, v87\n\t" \
  "v_mul_f32 v118, v134, v91\n\t" \
  "v_mul_f32 v119, v135, v95\n\t" \
  "s_waitcnt vmcnt(7)\n\t" \
  "v_lshlrev_b32 v128, 16, v12\n\t" \
  "v_and_b32 v129, 0xffff0000, v12\n\t" \
  "v_lshlrev_b32 v130, 16, v13\n\t" \
  "v_and_b32 v131, 0xffff0000, v13\n\t" \
  "v_lshlrev_b32 v132, 16, v14\n\t" \
  "v_and_b32 v133, 0xffff0000, v14\n\t" \
  "v_lshlrev_b32 v134, 16, v15\n\t" \
  "v_and_b32 v135, 0xffff0000, v15\n\t" \
  "v_fmac_f32 v96, v128, v64\n\t" \
  "v_fmac_f32 v97, v129, v68\n\t" \
  "v_fmac_f32 v98, v130, v72\n\t" \
  "v_fmac_f32 v99, v131, v76\n\t" \
  "v_fmac_f32 v100, v132, v80\n\t" \
  "v_fmac_f32 v101, v133, v84\n\t" \
  "v_fmac_f32 v102, v134, v88\n\t" \
  "v_fmac_f32 v103, v135, v92\n\t" \
  "v_fmac_f32 v104, v128, v65\n\t" \
  "v_fmac_f32 v105, v129, v69\n\t" \
  "v_fmac_f32 v106, v130, v73\n\t" \
  "v_fmac_f32 v107, v131, v77\n\t" \
  "v_fmac_f32 v108, v132, v81\n\t" \
  "v_fmac_f32 v109, v133, v85\n\t" \
  "v_fmac_f32 v110, v134, v89\n\t" \
  "v_fmac_f32 v111, v135, v93\n\t" \
  "v_fmac_f32 v112, v128, v66\n\t" \
  "v_fmac_f32 v113, v129, v70\n\t" \
  "v_fmac_f32 v114, v130, v74\n\t" \
  "v_fmac_f32 v115, v131, v78\n\t" \
  "v_fmac_f32 v116, v132, v82\n\t" \
  "v_fmac_f32 v117, v133, v86\n\t" \
  "v_fmac_f32 v118, v134, v90\n\t" \
  "v_fmac_f32 v119, v135, v94\n\t" \
  "v_mul_f32 v120, v128, v67\n\t" \
  "v_mul_f32 v121, v129, v71\n\t" \
  "v_mul_f32 v122, v130, v75\n\t" \
  "v_mul_f32 v123, v131, v79\n\t" \
  "v_mul_f32 v124, v132, v83\n\t" \
  "v_mul_f32 v125, v133, v87\n\t" \
  "v_mul_f32 v126, v134, v91\n\t" \
  "v_mul_f32 v127, v135, v95\n\t" \
  "s_waitcnt vmcnt(6)\n\t" \
  "v_lshlrev_b32 v128, 16, v28\n\t" \
  "v_and_b32 v129, 0xffff0000, v28\n\t" \
  "v_lshlrev_b32 v130, 16, v29\n\t" \
  "v_and_b32 v131, 0xffff0000, v29\n\t" \
  "v_lshlrev_b32 v132, 16, v30\n\t" \
  "v_and_b32 v133, 0xffff0000, v30\n\t" \
  "v_lshlrev_b32 v134, 16, v31\n\t" \
  "v_and_b32 v135, 0xffff0000, v31\n\t" \
  "s_barrier\n\t" \
  "v_mul_f32 v96, v96, v128\n\t" \
  "v_mul_f32 v97, v97, v129\n\t" \
  "v_mul_f32 v98, v98, v130\n\t" \
  "v_mul_f32 v99, v99, v131\n\t" \
  "v_mul_f32 v100, v100, v132\n\t" \
  "v_mul_f32 v101, v101, v133\n\t" \
  "v_mul_f32 v102, v102, v134\n\t" \
  "v_mul_f32 v103, v103, v135\n\t" \
  "v_cvt_pk_bf16_f32 v136, v96, v97\n\t" \
  "v_cvt_pk_bf16_f32 v137, v98, v99\n\t" \
  "v_cvt_pk_bf16_f32 v138, v100, v101\n\t" \
  "v_cvt_pk_bf16_f32 v139, v102, v103\n\t" \
  "v_mov_b32_dpp v128, v136 row_ror:8 row_mask:0xf bank_mask:0xf\n\t" \
  "v_mov_b32_dpp v129, v137 row_ror:8 row_mask:0xf bank_mask:0xf\n\t" \
  "v_mov_b32_dpp v130, v138 row_ror:8 row_mask:0xf bank_mask:0xf\n\t" \
  "v_mov_b32_dpp v131, v139 row_ror:8 row_mask:0xf bank_mask:0xf\n\t" \
  "v_perm_b32 v136, v128, v136, v157\n\t" \
  "v_perm_b32 v137, v129, v137, v157\n\t" \
  "v_perm_b32 v138, v130, v138, v157\n\t" \
  "v_perm_b32 v139, v131, v139, v157\n\t" \
  "v_permlane16_swap_b32 v136, v137\n\t" \
  "s_nop 0\n\t" \
  "v_permlane16_swap_b32 v138, v139\n\t" \
  "s_nop 1\n\t" \
  "v_permlane32_swap_b32 v136, v138\n\t" \
  "v_permlane32_swap_b32 v137, v139\n\t" \
  "s_waitcnt vmcnt(5)\n\t" \
  "v_lshlrev_b32 v128, 16, v16\n\t" \
  "v_and_b32 v129, 0xffff0000, v16\n\t" \
  "v_lshlrev_b32 v130, 16, v17\n\t" \
  "v_and_b32 v131, 0xffff0000, v17\n\t" \
  "v_lshlrev_b32 v132, 16, v18\n\t" \
  "v_and_b32 v133, 0xffff0000, v18\n\t" \
  "v_lshlrev_b32 v134, 16, v19\n\t" \
  "v_and_b32 v135, 0xffff0000, v19\n\t" \
  "v_fmac_f32 v104, v128, v64\n\t" \
  "v_fmac_f32 v105, v129, v68\n\t" \
  "v_fmac_f32 v106, v130, v72\n\t" \
  "v_fmac_f32 v107, v131, v76\n\t" \
  "v_fmac_f32 v108, v132, v80\n\t" \
  "v_fmac_f32 v109, v133, v84\n\t" \
  "v_fmac_f32 v110, v134, v88\n\t" \
  "v_fmac_f32 v111, v135, v92\n\t" \
  "v_fmac_f32 v112, v128, v65\n\t" \
  "v_fmac_f32 v113, v129, v69\n\t" \
  "v_fmac_f32 v114, v130, v73\n\t" \
  "v_fmac_f32 v115, v131, v77\n\t" \
  "v_fmac_f32 v116, v132, v81\n\t" \
  "v_fmac_f32 v117, v133, v85\n\t" \
  "v_fmac_f32 v118, v134, v89\n\t" \
  "v_fmac_f32 v119, v135, v93\n\t" \
  "v_fmac_f32 v120, v128, v66\n\t" \
  "v_fmac_f32 v121, v129, v70\n\t" \
  "v_fmac_f32 v122, v130, v74\n\t" \
  "v_fmac_f32 v123, v131, v78\n\t" \
  "v_fmac_f32 v124, v132, v82\n\t" \
  "v_fmac_f32 v125, v133, v86\n\t" \
  "v_fmac_f32 v126, v134, v90\n\t" \
  "v_fmac_f32 v127, v135, v94\n\t" \
  "s_waitcnt vmcnt(4)\n\t" \
  "v_lshlrev_b32 v128, 16, v32\n\t" \
  "v_and_b32 v129, 0xffff0000, v32\n\t" \
  "v_lshlrev_b32 v130, 16, v33\n\t" \
  "v_and_b32 v131, 0xffff0000, v33\n\t" \
  "v_lshlrev_b32 v132, 16, v34\n\t" \
  "v_and_b32 v133, 0xffff0000, v34\n\t" \
  "v_lshlrev_b32 v134, 16, v35\n\t" \
  "v_and_b32 v135, 0xffff0000, v35\n\t" \
  "v_mul_f32 v104, v104, v128\n\t" \
  "v_mul_f32 v105, v105, v129\n\t" \
  "v_mul_f32 v106, v106, v130\n\t" \
  "v_mul_f32 v107, v107, v131\n\t" \
  "v_mul_f32 v108, v108, v132\n\t" \
  "v_mul_f32 v109, v109, v133\n\t" \
  "v_mul_f32 v110, v110, v134\n\t" \
  "v_mul_f32 v111, v111, v135\n\t" \
  "v_cvt_pk_bf16_f32 v140, v104, v105\n\t" \
  "v_cvt_pk_bf16_f32 v141, v106, v107\n\t" \
  "v_cvt_pk_bf16_f32 v142, v108, v109\n\t" \
  "v_cvt_pk_bf16_f32 v143, v110, v111\n\t" \
  "v_mov_b32_dpp v128, v140 row_ror:8 row_mask:0xf bank_mask:0xf\n\t" \
  "v_mov_b32_dpp v129, v141 row_ror:8 row_mask:0xf bank_mask:0xf\n\t" \
  "s_setprio 3\n\t" \
  "s_barrier\n\t" \
  "global_store_dwordx4 v159, v[136:139], %[sb] sc1\n\t" \
  "v_add_u32 v158, 0x80, v159\n\t" \
  "v_bfi_b32 v159, %[mk], v158, v159\n\t" \
  "s_setprio 2\n\t" \
  "v_mov_b32_dpp v130, v142 row_ror:8 row_mask:0xf bank_mask:0xf\n\t" \
  "v_mov_b32_dpp v131, v143 row_ror:8 row_mask:0xf bank_mask:0xf\n\t" \
  "v_perm_b32 v140, v128, v140, v157\n\t" \
  "v_perm_b32 v141, v129, v141, v157\n\t" \
  "v_perm_b32 v142, v130, v142, v157\n\t" \
  "v_perm_b32 v143, v131, v143, v157\n\t" \
  "v_permlane16_swap_b32 v140, v141\n\t" \
  "s_nop 0\n\t" \
  "v_permlane16_swap_b32 v142, v143\n\t" \
  "s_nop 1\n\t" \
  "v_permlane32_swap_b32 v140, v142\n\t" \
  "v_permlane32_swap_b32 v141, v143\n\t" \
  "s_waitcnt vmcnt(4)\n\t" \
  "v_lshlrev_b32 v128, 16, v20\n\t" \
  "v_and_b32 v129, 0xffff0000, v20\n\t" \
  "v_lshlrev_b32 v130, 16, v21\n\t" \
  "v_and_b32 v131, 0xffff0000, v21\n\t" \
  "v_lshlrev_b32 v132, 16, v22\n\t" \
  "v_and_b32 v133, 0xffff0000, v22\n\t" \
  "v_lshlrev_b32 v134, 16, v23\n\t" \
  "v_and_b32 v135, 0xffff0000, v23\n\t" \
  "v_fmac_f32 v112, v128, v64\n\t" \
  "v_fmac_f32 v113, v129, v68\n\t" \
  "v_fmac_f32 v114, v130, v72\n\t" \
  "v_fmac_f32 v115, v131, v76\n\t" \
  "v_fmac_f32 v116, v132, v80\n\t" \
  "v_fmac_f32 v117, v133, v84\n\t" \
  "v_fmac_f32 v118, v134, v88\n\t" \
  "v_fmac_f32 v119, v135, v92\n\t" \
  "v_fmac_f32 v120, v128, v65\n\t" \
  "v_fmac_f32 v121, v129, v69\n\t" \
  "v_fmac_f32 v122, v130, v73\n\t" \
  "v_fmac_f32 v123, v131, v77\n\t" \
  "v_fmac_f32 v124, v132, v81\n\t" \
  "v_fmac_f32 v125, v133, v85\n\t" \
  "v_fmac_f32 v126, v134, v89\n\t" \
  "v_fmac_f32 v127, v135, v93\n\t" \
  "s_waitcnt vmcnt(3)\n\t" \
  "v_lshlrev_b32 v128, 16, v36\n\t" \
  "v_and_b32 v129, 0xffff0000, v36\n\t" \
  "v_lshlrev_b32 v130, 16, v37\n\t" \
  "v_and_b32 v131, 0xffff0000, v37\n\t" \
  "v_lshlrev_b32 v132, 16, v38\n\t" \
  "v_and_b32 v133, 0xffff0000, v38\n\t" \
  "v_lshlrev_b32 v134, 16, v39\n\t" \
  "v_and_b32 v135, 0xffff0000, v39\n\t" \
  "v_mul_f32 v112, v112, v128\n\t" \
  "v_mul_f32 v113, v113, v129\n\t" \
  "v_mul_f32 v114, v114, v130\n\t" \
  "v_mul_f32 v115, v115, v131\n\t" \
  "v_mul_f32 v116, v116, v132\n\t" \
  "v_mul_f32 v117, v117, v133\n\t" \
  "v_mul_f32 v118, v118, v134\n\t" \
  "v_mul_f32 v119, v119, v135\n\t" \
  "v_cvt_pk_bf16_f32 v144, v112, v113\n\t" \
  "v_cvt_pk_bf16_f32 v145, v114, v115\n\t" \
  "v_cvt_pk_bf16_f32 v146, v116, v117\n\t" \
  "v_cvt_pk_bf16_f32 v147, v118, v119\n\t" \
  "v_mov_b32_dpp v128, v144 row_ror:8 row_mask:0xf bank_mask:0xf\n\t" \
  "v_mov_b32_dpp v129, v145 row_ror:8 row_mask:0xf bank_mask:0xf\n\t" \
  "v_mov_b32_dpp v130, v146 row_ror:8 row_mask:0xf bank_mask:0xf\n\t" \
  "v_mov_b32_dpp v131, v147 row_ror:8 row_mask:0xf bank_mask:0xf\n\t" \
  "v_perm_b32 v144, v128, v144, v157\n\t" \
  "v_perm_b32 v145, v129, v145, v157\n\t" \
  "v_perm_b32 v146, v130, v146, v157\n\t" \
  "v_perm_b32 v147, v131, v147, v157\n\t" \
  "v_permlane16_swap_b32 v144, v145\n\t" \
  "s_nop 0\n\t" \
  "v_permlane16_swap_b32 v146, v147\n\t" \
  "s_nop 1\n\t" \
  "v_permlane32_swap_b32 v144, v146\n\t" \
  "v_permlane32_swap_b32 v145, v147\n\t" \
  "s_waitcnt vmcnt(2)\n\t" \
  "v_lshlrev_b32 v128, 16, v24\n\t" \
  "v_and_b32 v129, 0xffff0000, v24\n\t" \
  "v_lshlrev_b32 v130, 16, v25\n\t" \
  "v_and_b32 v131, 0xffff0000, v25\n\t" \
  "v_lshlrev_b32 v132, 16, v26\n\t" \
  "v_and_b32 v133, 0xffff0000, v26\n\t" \
  "v_lshlrev_b32 v134, 16, v27\n\t" \
  "v_and_b32 v135, 0xffff0000, v27\n\t" \
  "v_fmac_f32 v120, v128, v64\n\t" \
  "v_fmac_f32 v121, v129, v68\n\t" \
  "s_setprio 3\n\t" \
  "s_barrier\n\t" \
  "global_store_dwordx4 v159, v[140:143], %[sb] sc1\n\t" \
  "v_add_u32 v158, 0x80, v159\n\t" \
  "v_bfi_b32 v159, %[mk], v158, v159\n\t" \
  "global_store_dwordx4 v159, v[144:147], %[sb] sc1\n\t" \
  "v_add_u32 v158, 0x80, v159\n\t" \
  "v_bfi_b32 v159, %[mk], v158, v159\n\t" \
  "s_mov_b32 m0, %[hd]\n\t" \
  "s_nop 0\n\t" \
  "global_load_lds_dwordx4 v163, %[hp] offset:0\n\t" \
  "global_load_lds_dwordx4 v163, %[hp] offset:1024\n\t" \
  "global_load_lds_dwordx4 v163, %[hp] offset:2048\n\t" \
  "global_load_lds_dwordx4 v163, %[hp] offset:3072\n\t" \
  "s_setprio 2\n\t" \
  "v_fmac_f32 v122, v130, v72\n\t" \
  "v_fmac_f32 v123, v131, v76\n\t" \
  "v_fmac_f32 v124, v132, v80\n\t" \
  "v_fmac_f32 v125, v133, v84\n\t" \
  "v_fmac_f32 v126, v134, v88\n\t" \
  "v_fmac_f32 v127, v135, v92\n\t" \
  "s_waitcnt vmcnt(7)\n\t" \
  "v_lshlrev_b32 v128, 16, v40\n\t" \
  "v_and_b32 v129, 0xffff0000, v40\n\t" \
  "v_lshlrev_b32 v130, 16, v41\n\t" \
  "v_and_b32 v131, 0xffff0000, v41\n\t" \
  "v_lshlrev_b32 v132, 16, v42\n\t" \
  "v_and_b32 v133, 0xffff0000, v42\n\t" \
  "v_lshlrev_b32 v134, 16, v43\n\t" \
  "v_and_b32 v135, 0xffff0000, v43\n\t" \
  "v_mul_f32 v120, v120, v128\n\t" \
  "v_mul_f32 v121, v121, v129\n\t" \
  "v_mul_f32 v122, v122, v130\n\t" \
  "v_mul_f32 v123, v123, v131\n\t" \
  "v_mul_f32 v124, v124, v132\n\t" \
  "v_mul_f32 v125, v125, v133\n\t" \
  "v_mul_f32 v126, v126, v134\n\t" \
  "v_mul_f32 v127, v127, v135\n\t" \
  "v_cvt_pk_bf16_f32 v148, v120, v121\n\t" \
  "v_cvt_pk_bf16_f32 v149, v122, v123\n\t" \
  "v_cvt_pk_bf16_f32 v150, v124, v125\n\t" \
  "v_cvt_pk_bf16_f32 v151, v126, v127\n\t" \
  "v_mov_b32_dpp v128, v148 row_ror:8 row_mask:0xf bank_mask:0xf\n\t" \
  "v_mov_b32_dpp v129, v149 row_ror:8 row_mask:0xf bank_mask:0xf\n\t" \
  "v_mov_b32_dpp v130, v150 row_ror:8 row_mask:0xf bank_mask:0xf\n\t" \
  "v_mov_b32_dpp v131, v151 row_ror:8 row_mask:0xf bank_mask:0xf\n\t" \
  "v_perm_b32 v148, v128, v148, v157\n\t" \
  "v_perm_b32 v149, v129, v149, v157\n\t" \
  "v_perm_b32 v150, v130, v150, v157\n\t" \
  "v_perm_b32 v151, v131, v151, v157\n\t" \
  "v_permlane16_swap_b32 v148, v149\n\t" \
  "s_nop 0\n\t" \
  "v_permlane16_swap_b32 v150, v151\n\t" \
  "s_nop 1\n\t" \
  "v_permlane32_swap_b32 v148, v150\n\t" \
  "v_permlane32_swap_b32 v149, v151\n\t" \
  "s_setprio 3\n\t" \
  "s_barrier\n\t" \
  "global_store_dwordx4 v159, v[148:151], %[sb] sc1\n\t" \
  "s_setprio 2\n\t" \
  "s_waitcnt vmcnt(1)\n\t" \
  "ds_read_b64_tr_b16 v[64:65], v165 offset:32\n\t" \
  "ds_read_b64_tr_b16 v[66:67], v165 offset:0\n\t" \
  "ds_read_b64_tr_b16 v[68:69], v165 offset:288\n\t" \
  "ds_read_b64_tr_b16 v[70:71], v165 offset:256\n\t" \
  "ds_read_b64_tr_b16 v[72:73], v165 offset:544\n\t" \
  "ds_read_b64_tr_b16 v[74:75], v165 offset:512\n\t" \
  "ds_read_b64_tr_b16 v[76:77], v165 offset:800\n\t" \
  "ds_read_b64_tr_b16 v[78:79], v165 offset:768\n\t" \
  "ds_read_b64_tr_b16 v[80:81], v165 offset:1056\n\t" \
  "ds_read_b64_tr_b16 v[82:83], v165 offset:1024\n\t" \
  "ds_read_b64_tr_b16 v[84:85], v165 offset:1312\n\t" \
  "ds_read_b64_tr_b16 v[86:87], v165 offset:1280\n\t" \
  "ds_read_b64_tr_b16 v[88:89], v165 offset:1568\n\t" \
  "ds_read_b64_tr_b16 v[90:91], v165 offset:1536\n\t" \
  "ds_read_b64_tr_b16 v[92:93], v165 offset:1824\n\t" \
  "ds_read_b64_tr_b16 v[94:95], v165 offset:1792\n\t" \
  "v_mov_b32 v24, v166\n\t" \
  "v_mov_b32 v25, v167\n\t" \
  "v_mov_b32 v63, 0xc0\n\t" \
  "v_add_u32 v158, 64, v24\n\t" \
  "v_bfi_b32 v26, v63, v158, v24\n\t" \
  "v_add_u32 v158, 64, v25\n\t" \
  "v_bfi_b32 v27, v63, v158, v25\n\t" \
  "v_add_u32 v158, 64, v26\n\t" \
  "v_bfi_b32 v28, v63, v158, v26\n\t" \
  "v_add_u32 v158, 64, v27\n\t" \
  "v_bfi_b32 v29, v63, v158, v27\n\t" \
  "v_add_u32 v158, 64, v28\n\t" \
  "v_bfi_b32 v30, v63, v158, v28\n\t" \
  "v_add_u32 v158, 64, v29\n\t" \
  "v_bfi_b32 v31, v63, v158, v29\n\t" \
  "s_setprio 3\n\t" \
  "s_barrier\n\t" \
  "s_waitcnt vmcnt(0)\n\t" \
  "v_add_u32 v61, 1, v160\n\t" \
  "s_mov_b64 exec, 1\n\t" \
  "global_store_dword v60, v61, %[ff] sc1\n\t" \
  "s_mov_b64 exec, -1\n\t" \
  "s_setprio 2\n\t" \
  "ds_read_b64_tr_b16 v[96:97], v165 offset:2080\n\t" \
  "ds_read_b64_tr_b16 v[98:99], v165 offset:2048\n\t" \
  "ds_read_b64_tr_b16 v[100:101], v165 offset:2336\n\t" \
  "ds_read_b64_tr_b16 v[102:103], v165 offset:2304\n\t" \
  "ds_read_b64_tr_b16 v[104:105], v165 offset:2592\n\t" \
  "ds_read_b64_tr_b16 v[106:107], v165 offset:2560\n\t" \
  "ds_read_b64_tr_b16 v[108:109], v165 offset:2848\n\t" \
  "ds_read_b64_tr_b16 v[110:111], v165 offset:2816\n\t" \
  "ds_read_b64_tr_b16 v[112:113], v165 offset:3104\n\t" \
  "ds_read_b64_tr_b16 v[114:115], v165 offset:3072\n\t" \
  "ds_read_b64_tr_b16 v[116:117], v165 offset:3360\n\t" \
  "ds_read_b64_tr_b16 v[118:119], v165 offset:3328\n\t" \
  "ds_read_b64_tr_b16 v[120:121], v165 offset:3616\n\t" \
  "ds_read_b64_tr_b16 v[122:123], v165 offset:3584\n\t" \
  "ds_read_b64_tr_b16 v[124:125], v165 offset:3872\n\t" \
  "ds_read_b64_tr_b16 v[126:127], v165 offset:3840\n\t" \
  "s_barrier\n\t" \
  "s_waitcnt lgkmcnt(0)\n\t" \
  "v_lshlrev_b32 v61, 16, v161\n\t" \
  "v_lshlrev_b32 v62, 16, v162\n\t" \
  "v_add_f32 v61, v61, v62\n\t" \
  "v_mov_b32 v62, 0xffff\n\t" \
  "v_cvt_pk_bf16_f32 v61, v61, v61\n\t" \
  "s_mov_b64 exec, 1\n\t" \
  "v_bfi_b32 v66, v62, v61, v66\n\t" \
  "s_mov_b64 exec, 15\n\t" \
  "v_and_b32 v66, v62, v66\n\t" \
  "s_mov_b64 exec, 16\n\t" \
  "v_bfi_b32 v66, v62, v66, v61\n\t" \
  "s_mov_b64 exec, 0xff\n\t" \
  "v_mov_b32 v67, 0\n\t" \
  "s_mov_b64 exec, 0x100\n\t" \
  "v_mov_b32 v67, v61\n\t" \
  "s_mov_b64 exec, 0xf00\n\t" \
  "v_and_b32 v67, v62, v67\n\t" \
  "s_mov_b64 exec, 0x1000\n\t" \
  "v_bfi_b32 v67, v62, v67, v61\n\t" \
  "s_mov_b64 exec, -1\n\t" \
  "s_waitcnt lgkmcnt(0)\n\t"
#define PP_NB 8
#define PP_Q 0
#define PP_CLOB "v0", "v1", "v2", "v3", "v4", "v5", "v6", "v7", "v8", "v9", "v10", "v11", "v12", "v13", "v14", "v15", "v16", "v17", "v18", "v19", "v20", "v21", "v22", "v23", "v32", "v33", "v34", "v35", "v36", "v37", "v38", "v39", "v40", "v41", "v42", "v43", "v60", "v61", "v62", "v63", "v128", "v129", "v130", "v131", "v132", "v133", "v134", "v135", "v136", "v137", "v138", "v139", "v140", "v141", "v142", "v143", "v144", "v145", "v146", "v147", "v148", "v149", "v150", "v151", "v158", "v161", "v162"

#define LD_ASM1 \
  "s_add_u32 m0, s39, 49152\n\t" \
  "s_mov_b32 s68, 0x0\n\t" \
  "buffer_load_dwordx4 v5, s[44:47], s68 offen offset:0 lds\n\t" \
  "buffer_load_dwordx4 v6, s[44:47], s68 offen offset:1024 lds\n\t" \
  "buffer_load_dwordx4 v7, s[44:47], s68 offen offset:2048 lds\n\t" \
  "s_add_u32 m0, s38, 36864\n\t" \
  "s_mov_b32 s68, 0x0\n\t" \
  "buffer_load_dwordx4 v0, s[40:43], s68 offen offset:0 lds\n\t" \
  "buffer_load_dwordx4 v1, s[40:43], s68 offen offset:1024 lds\n\t" \
  "s_mov_b64 exec, 0xffff\n\t" \
  "buffer_load_dwordx4 v4, s[40:43], s68 offen offset:2048 lds\n\t" \
  "s_mov_b64 exec, -1\n\t" \
  "s_add_u32 m0, s38, 46080\n\t" \
  "s_mov_b32 s68, 0x24000\n\t" \
  "buffer_load_dwordx4 v2, s[40:43], s68 offen offset:0 lds\n\t" \
  "buffer_load_dwordx4 v3, s[40:43], s68 offen offset:1024 lds\n\t" \
  "s_mov_b64 exec, 0xffff\n\t" \
  "buffer_load_dwordx4 v4, s[40:43], s68 offen offset:2048 lds\n\t" \
  "s_mov_b64 exec, -1\n\t" \
  "s_add_u32 m0, s39, 61440\n\t" \
  "s_mov_b32 s68, 0x30000\n\t" \
  "buffer_load_dwordx4 v5, s[44:47], s68 offen offset:0 lds\n\t" \
  "buffer_load_dwordx4 v6, s[44:47], s68 offen offset:1024 lds\n\t" \
  "buffer_load_dwordx4 v7, s[44:47], s68 offen offset:2048 lds\n\t" \
  "s_mov_b32 m0, %[sk]\n\t" \
  "s_mov_b32 exec_hi, 0\n\t" \
  "buffer_load_dword v9, s[40:43], 0 offen lds\n\t" \
  "s_mov_b32 exec_lo, %[wm]\n\t" \
  "buffer_load_dword v10, s[44:47], 0 offen lds\n\t" \
  "s_mov_b64 exec, -1\n\t"
#define LD_ASM2 \
  "s_add_u32 m0, s38, 0\n\t" \
  "s_mov_b32 s68, 0x80\n\t" \
  "buffer_load_dwordx4 v0, s[40:43], s68 offen offset:0 lds\n\t" \
  "s_mov_b32 s85, 0x180000\n\t" \
  "s_load_dword s84, %[pw], s85\n\t" \
  "buffer_load_dwordx4 v1, s[40:43], s68 offen offset:1024 lds\n\t" \
  "s_mov_b32 s85, 0x180800\n\t" \
  "s_load_dword s84, %[pw], s85\n\t" \
  "s_mov_b64 exec, 0xffff\n\t" \
  "buffer_load_dwordx4 v4, s[40:43], s68 offen offset:2048 lds\n\t" \
  "s_mov_b64 exec, -1\n\t" \
  "s_add_u32 m0, s38, 9216\n\t" \
  "s_mov_b32 s68, 0x24080\n\t" \
  "buffer_load_dwordx4 v2, s[40:43], s68 offen offset:0 lds\n\t" \
  "s_mov_b32 s85, 0x181000\n\t" \
  "s_load_dword s84, %[pw], s85\n\t" \
  "buffer_load_dwordx4 v3, s[40:43], s68 offen offset:1024 lds\n\t" \
  "s_mov_b32 s85, 0x1b0000\n\t" \
  "s_load_dword s84, %[pw], s85\n\t" \
  "s_mov_b64 exec, 0xffff\n\t" \
  "buffer_load_dwordx4 v4, s[40:43], s68 offen offset:2048 lds\n\t" \
  "s_mov_b64 exec, -1\n\t" \
  "s_mov_b32 s85, 0x1b0800\n\t" \
  "s_load_dword s84, %[pw], s85\n\t" \
  "s_mov_b32 s85, 0x1b1000\n\t" \
  "s_load_dword s84, %[pw], s85\n\t" \
  "s_mov_b32 s85, 0x1e0000\n\t" \
  "s_load_dword s84, %[pw], s85\n\t" \
  "s_mov_b32 s85, 0x1e0800\n\t" \
  "s_load_dword s84, %[pw], s85\n\t" \
  "s_mov_b32 m0, %[tsk]\n\t" \
  "s_mov_b32 s85, 0x0\n\t" \
  "s_mov_b64 exec, %[hm]\n\t" \
  "buffer_load_dword v16, %[sh], s85 offen lds\n\t" \
  "s_mov_b64 exec, -1\n\t" \
  "s_mov_b32 s68, 0x60000\n\t" \
  "s_mov_b32 s69, 0x90000\n\t" \
  "s_mov_b32 s70, 0x100\n\t" \
  "s_mov_b32 s71, 0x24100\n\t" \
  "s_mov_b32 s72, 0xc0000\n\t" \
  "s_mov_b32 s73, 0xf0000\n\t" \
  "s_add_u32 m0, s39, 0\n\t" \
  "s_waitcnt vmcnt(9)\n\t" \
  "s_barrier\n\t" \
  "buffer_load_dwordx4 v5, s[44:47], s68 offen offset:0 lds\n\t" \
  "s_mov_b32 s85, 0x1e1000\n\t" \
  "s_load_dword s84, %[pw], s85\n\t" \
  "buffer_load_dwordx4 v6, s[44:47], s68 offen offset:1024 lds\n\t" \
  "s_mov_b32 s85, 0x210000\n\t" \
  "s_load_dword s84, %[pw], s85\n\t" \
  "buffer_load_dwordx4 v7, s[44:47], s68 offen offset:2048 lds\n\t" \
  "s_mov_b32 s85, 0x210800\n\t" \
  "s_load_dword s84, %[pw], s85\n\t" \
  "s_add_u32 m0, s39, 12288\n\t" \
  "s_nop 0\n\t" \
  "buffer_load_dwordx4 v5, s[44:47], s69 offen offset:0 lds\n\t" \
  "s_mov_b32 s85, 0x211000\n\t" \
  "s_load_dword s84, %[pw], s85\n\t" \
  "buffer_load_dwordx4 v6, s[44:47], s69 offen offset:1024 lds\n\t" \
  "s_mov_b32 s85, 0x240000\n\t" \
  "s_load_dword s84, %[pw], s85\n\t" \
  "buffer_load_dwordx4 v7, s[44:47], s69 offen offset:2048 lds\n\t" \
  "s_mov_b32 s85, 0x240800\n\t" \
  "s_load_dword s84, %[pw], s85\n\t" \
  "s_add_u32 m0, s38, 18432\n\t" \
  "s_nop 0\n\t" \
  "buffer_load_dwordx4 v0, s[40:43], s70 offen offset:0 lds\n\t" \
  "buffer_load_dwordx4 v1, s[40:43], s70 offen offset:1024 lds\n\t" \
  "s_mov_b64 exec, 0xffff\n\t" \
  "buffer_load_dwordx4 v4, s[40:43], s70 offen offset:2048 lds\n\t" \
  "s_add_u32 m0, s38, 27648\n\t" \
  "s_mov_b64 exec, -1\n\t" \
  "buffer_load_dwordx4 v2, s[40:43], s71 offen offset:0 lds\n\t" \
  "buffer_load_dwordx4 v3, s[40:43], s71 offen offset:1024 lds\n\t" \
  "s_mov_b64 exec, 0xffff\n\t" \
  "buffer_load_dwordx4 v4, s[40:43], s71 offen offset:2048 lds\n\t" \
  "s_add_u32 m0, s39, 24576\n\t" \
  "s_mov_b64 exec, -1\n\t" \
  "buffer_load_dwordx4 v5, s[44:47], s72 offen offset:0 lds\n\t" \
  "buffer_load_dwordx4 v6, s[44:47], s72 offen offset:1024 lds\n\t" \
  "buffer_load_dwordx4 v7, s[44:47], s72 offen offset:2048 lds\n\t" \
  "s_add_u32 m0, s39, 36864\n\t" \
  "s_nop 0\n\t" \
  "buffer_load_dwordx4 v5, s[44:47], s73 offen offset:0 lds\n\t" \
  "buffer_load_dwordx4 v6, s[44:47], s73 offen offset:1024 lds\n\t" \
  "buffer_load_dwordx4 v7, s[44:47], s73 offen offset:2048 lds\n\t" \
  "s_mov_b32 m0, %[tsk]\n\t" \
  "s_mov_b32 s85, 0x180000\n\t" \
  "s_mov_b64 exec, %[hm]\n\t" \
  "buffer_load_dword v16, %[sh], s85 offen lds\n\t" \
  "s_mov_b64 exec, -1\n\t"
#define LD_ASM3 \
  "s_mov_b32 s68, 0x180\n\t" \
  "s_mov_b32 s69, 0x120000\n\t" \
  "s_mov_b32 s70, 0x24180\n\t" \
  "s_mov_b32 s71, 0x150000\n\t" \
  "s_add_u32 m0, s38, 36864\n\t" \
  "s_waitcnt vmcnt(13)\n\t" \
  "s_barrier\n\t" \
  "buffer_load_dwordx4 v0, s[40:43], s68 offen offset:0 lds\n\t" \
  "s_mov_b32 s85, 0x300000\n\t" \
  "s_load_dword s84, %[ph], s85\n\t" \
  "buffer_load_dwordx4 v1, s[40:43], s68 offen offset:1024 lds\n\t" \
  "s_mov_b32 s85, 0x300800\n\t" \
  "s_load_dword s84, %[ph], s85\n\t" \
  "s_mov_b64 exec, 0xffff\n\t" \
  "buffer_load_dwordx4 v4, s[40:43], s68 offen offset:2048 lds\n\t" \
  "s_add_u32 m0, s39, 49152\n\t" \
  "s_mov_b64 exec, -1\n\t" \
  "buffer_load_dwordx4 v5, s[44:47], s69 offen offset:0 lds\n\t" \
  "s_mov_b32 s85, 0x301000\n\t" \
  "s_load_dword s84, %[ph], s85\n\t" \
  "buffer_load_dwordx4 v6, s[44:47], s69 offen offset:1024 lds\n\t" \
  "s_mov_b32 s85, 0x241000\n\t" \
  "s_load_dword s84, %[pw], s85\n\t" \
  "buffer_load_dwordx4 v7, s[44:47], s69 offen offset:2048 lds\n\t" \
  "s_mov_b32 s85, 0x270000\n\t" \
  "s_load_dword s84, %[pw], s85\n\t" \
  "s_add_u32 m0, s38, 46080\n\t" \
  "s_nop 0\n\t" \
  "buffer_load_dwordx4 v2, s[40:43], s70 offen offset:0 lds\n\t" \
  "s_mov_b32 s85, 0x270800\n\t" \
  "s_load_dword s84, %[pw], s85\n\t" \
  "buffer_load_dwordx4 v3, s[40:43], s70 offen offset:1024 lds\n\t" \
  "s_mov_b32 s85, 0x271000\n\t" \
  "s_load_dword s84, %[pw], s85\n\t" \
  "s_mov_b64 exec, 0xffff\n\t" \
  "buffer_load_dwordx4 v4, s[40:43], s70 offen offset:2048 lds\n\t" \
  "s_add_u32 m0, s39, 61440\n\t" \
  "s_mov_b64 exec, -1\n\t" \
  "buffer_load_dwordx4 v5, s[44:47], s71 offen offset:0 lds\n\t" \
  "s_mov_b32 s85, 0x2a0000\n\t" \
  "s_load_dword s84, %[pw], s85\n\t" \
  "buffer_load_dwordx4 v6, s[44:47], s71 offen offset:1024 lds\n\t" \
  "s_mov_b32 s85, 0x2a0800\n\t" \
  "s_load_dword s84, %[pw], s85\n\t" \
  "buffer_load_dwordx4 v7, s[44:47], s71 offen offset:2048 lds\n\t" \
  "s_mov_b32 s68, 0x200\n\t" \
  "s_mov_b32 s69, 0x180000\n\t" \
  "s_mov_b32 s70, 0x24200\n\t" \
  "s_mov_b32 s71, 0x1b0000\n\t" \
  "s_add_u32 m0, s38, 0\n\t" \
  "s_waitcnt vmcnt(13)\n\t" \
  "s_barrier\n\t" \
  "buffer_load_dwordx4 v0, s[40:43], s68 offen offset:0 lds\n\t" \
  "s_mov_b32 s85, 0x360000\n\t" \
  "s_load_dword s84, %[ph], s85\n\t" \
  "buffer_load_dwordx4 v1, s[40:43], s68 offen offset:1024 lds\n\t" \
  "s_mov_b32 s85, 0x360800\n\t" \
  "s_load_dword s84, %[ph], s85\n\t" \
  "s_mov_b64 exec, 0xffff\n\t" \
  "buffer_load_dwordx4 v4, s[40:43], s68 offen offset:2048 lds\n\t" \
  "s_add_u32 m0, s39, 0\n\t" \
  "s_mov_b64 exec, -1\n\t" \
  "buffer_load_dwordx4 v5, s[44:47], s69 offen offset:0 lds\n\t" \
  "s_mov_b32 s85, 0x361000\n\t" \
  "s_load_dword s84, %[ph], s85\n\t" \
  "buffer_load_dwordx4 v6, s[44:47], s69 offen offset:1024 lds\n\t" \
  "s_mov_b32 s85, 0x2a1000\n\t" \
  "s_load_dword s84, %[pw], s85\n\t" \
  "buffer_load_dwordx4 v7, s[44:47], s69 offen offset:2048 lds\n\t" \
  "s_mov_b32 s85, 0x2d0000\n\t" \
  "s_load_dword s84, %[pw], s85\n\t" \
  "s_add_u32 m0, s38, 9216\n\t" \
  "s_nop 0\n\t" \
  "buffer_load_dwordx4 v2, s[40:43], s70 offen offset:0 lds\n\t" \
  "s_mov_b32 s85, 0x2d0800\n\t" \
  "s_load_dword s84, %[pw], s85\n\t" \
  "buffer_load_dwordx4 v3, s[40:43], s70 offen offset:1024 lds\n\t" \
  "s_mov_b32 s85, 0x2d1000\n\t" \
  "s_load_dword s84, %[pw], s85\n\t" \
  "s_mov_b64 exec, 0xffff\n\t" \
  "buffer_load_dwordx4 v4, s[40:43], s70 offen offset:2048 lds\n\t" \
  "s_add_u32 m0, s39, 12288\n\t" \
  "s_mov_b64 exec, -1\n\t" \
  "buffer_load_dwordx4 v5, s[44:47], s71 offen offset:0 lds\n\t" \
  "s_mov_b32 s85, 0x300000\n\t" \
  "s_load_dword s84, %[pw], s85\n\t" \
  "buffer_load_dwordx4 v6, s[44:47], s71 offen offset:1024 lds\n\t" \
  "s_mov_b32 s85, 0x300800\n\t" \
  "s_load_dword s84, %[pw], s85\n\t" \
  "buffer_load_dwordx4 v7, s[44:47], s71 offen offset:2048 lds\n\t" \
  "s_mov_b32 s68, 0x280\n\t" \
  "s_mov_b32 s69, 0x1e0000\n\t" \
  "s_mov_b32 s70, 0x24280\n\t" \
  "s_mov_b32 s71, 0x210000\n\t" \
  "s_add_u32 m0, s38, 18432\n\t" \
  "s_waitcnt vmcnt(12)\n\t" \
  "s_barrier\n\t" \
  "buffer_load_dwordx4 v0, s[40:43], s68 offen offset:0 lds\n\t" \
  "s_mov_b32 s85, 0x3c0000\n\t" \
  "s_load_dword s84, %[ph], s85\n\t" \
  "buffer_load_dwordx4 v1, s[40:43], s68 offen offset:1024 lds\n\t" \
  "s_mov_b32 s85, 0x3c0800\n\t" \
  "s_load_dword s84, %[ph], s85\n\t" \
  "s_mov_b64 exec, 0xffff\n\t" \
  "buffer_load_dwordx4 v4, s[40:43], s68 offen offset:2048 lds\n\t" \
  "s_add_u32 m0, s39, 24576\n\t" \
  "s_mov_b64 exec, -1\n\t" \
  "buffer_load_dwordx4 v5, s[44:47], s69 offen offset:0 lds\n\t" \
  "s_mov_b32 s85, 0x3c1000\n\t" \
  "s_load_dword s84, %[ph], s85\n\t" \
  "buffer_load_dwordx4 v6, s[44:47], s69 offen offset:1024 lds\n\t" \
  "s_mov_b32 s85, 0x301000\n\t" \
  "s_load_dword s84, %[pw], s85\n\t" \
  "buffer_load_dwordx4 v7, s[44:47], s69 offen offset:2048 lds\n\t" \
  "s_mov_b32 s85, 0x330000\n\t" \
  "s_load_dword s84, %[pw], s85\n\t" \
  "s_add_u32 m0, s38, 27648\n\t" \
  "s_nop 0\n\t" \
  "buffer_load_dwordx4 v2, s[40:43], s70 offen offset:0 lds\n\t" \
  "s_mov_b32 s85, 0x330800\n\t" \
  "s_load_dword s84, %[pw], s85\n\t" \
  "buffer_load_dwordx4 v3, s[40:43], s70 offen offset:1024 lds\n\t" \
  "s_mov_b32 s85, 0x331000\n\t" \
  "s_load_dword s84, %[pw], s85\n\t" \
  "s_mov_b64 exec, 0xffff\n\t" \
  "buffer_load_dwordx4 v4, s[40:43], s70 offen offset:2048 lds\n\t" \
  "s_add_u32 m0, s39, 36864\n\t" \
  "s_mov_b64 exec, -1\n\t" \
  "buffer_load_dwordx4 v5, s[44:47], s71 offen offset:0 lds\n\t" \
  "s_mov_b32 s85, 0x360000\n\t" \
  "s_load_dword s84, %[pw], s85\n\t" \
  "buffer_load_dwordx4 v6, s[44:47], s71 offen offset:1024 lds\n\t" \
  "s_mov_b32 s85, 0x360800\n\t" \
  "s_load_dword s84, %[pw], s85\n\t" \
  "buffer_load_dwordx4 v7, s[44:47], s71 offen offset:2048 lds\n\t" \
  "s_mov_b32 s68, 0x300\n\t" \
  "s_mov_b32 s69, 0x240000\n\t" \
  "s_mov_b32 s70, 0x24300\n\t" \
  "s_mov_b32 s71, 0x270000\n\t" \
  "s_add_u32 m0, s38, 36864\n\t" \
  "s_waitcnt vmcnt(12)\n\t" \
  "s_barrier\n\t" \
  "buffer_load_dwordx4 v0, s[40:43], s68 offen offset:0 lds\n\t" \
  "s_mov_b32 s85, 0x420000\n\t" \
  "s_load_dword s84, %[ph], s85\n\t" \
  "buffer_load_dwordx4 v1, s[40:43], s68 offen offset:1024 lds\n\t" \
  "s_mov_b32 s85, 0x420800\n\t" \
  "s_load_dword s84, %[ph], s85\n\t" \
  "s_mov_b64 exec, 0xffff\n\t" \
  "buffer_load_dwordx4 v4, s[40:43], s68 offen offset:2048 lds\n\t" \
  "s_add_u32 m0, s39, 49152\n\t" \
  "s_mov_b64 exec, -1\n\t" \
  "buffer_load_dwordx4 v5, s[44:47], s69 offen offset:0 lds\n\t" \
  "s_mov_b32 s85, 0x421000\n\t" \
  "s_load_dword s84, %[ph], s85\n\t" \
  "buffer_load_dwordx4 v6, s[44:47], s69 offen offset:1024 lds\n\t" \
  "s_mov_b32 s85, 0x361000\n\t" \
  "s_load_dword s84, %[pw], s85\n\t" \
  "buffer_load_dwordx4 v7, s[44:47], s69 offen offset:2048 lds\n\t" \
  "s_mov_b32 s85, 0x390000\n\t" \
  "s_load_dword s84, %[pw], s85\n\t" \
  "s_add_u32 m0, s38, 46080\n\t" \
  "s_nop 0\n\t" \
  "buffer_load_dwordx4 v2, s[40:43], s70 offen offset:0 lds\n\t" \
  "s_mov_b32 s85, 0x390800\n\t" \
  "s_load_dword s84, %[pw], s85\n\t" \
  "buffer_load_dwordx4 v3, s[40:43], s70 offen offset:1024 lds\n\t" \
  "s_mov_b32 s85, 0x391000\n\t" \
  "s_load_dword s84, %[pw], s85\n\t" \
  "s_mov_b64 exec, 0xffff\n\t" \
  "buffer_load_dwordx4 v4, s[40:43], s70 offen offset:2048 lds\n\t" \
  "s_add_u32 m0, s39, 61440\n\t" \
  "s_mov_b64 exec, -1\n\t" \
  "buffer_load_dwordx4 v5, s[44:47], s71 offen offset:0 lds\n\t" \
  "s_mov_b32 s85, 0x3c0000\n\t" \
  "s_load_dword s84, %[pw], s85\n\t" \
  "buffer_load_dwordx4 v6, s[44:47], s71 offen offset:1024 lds\n\t" \
  "s_mov_b32 s85, 0x3c0800\n\t" \
  "s_load_dword s84, %[pw], s85\n\t" \
  "buffer_load_dwordx4 v7, s[44:47], s71 offen offset:2048 lds\n\t" \
  "s_mov_b32 s68, 0x380\n\t" \
  "s_mov_b32 s69, 0x2a0000\n\t" \
  "s_mov_b32 s70, 0x24380\n\t" \
  "s_mov_b32 s71, 0x2d0000\n\t" \
  "s_add_u32 m0, s38, 0\n\t" \
  "s_waitcnt vmcnt(12)\n\t" \
  "s_barrier\n\t" \
  "buffer_load_dwordx4 v0, s[40:43], s68 offen offset:0 lds\n\t" \
  "s_mov_b32 s85, 0x480000\n\t" \
  "s_load_dword s84, %[ph], s85\n\t" \
  "buffer_load_dwordx4 v1, s[40:43], s68 offen offset:1024 lds\n\t" \
  "s_mov_b32 s85, 0x480800\n\t" \
  "s_load_dword s84, %[ph], s85\n\t" \
  "s_mov_b64 exec, 0xffff\n\t" \
  "buffer_load_dwordx4 v4, s[40:43], s68 offen offset:2048 lds\n\t" \
  "s_add_u32 m0, s39, 0\n\t" \
  "s_mov_b64 exec, -1\n\t" \
  "buffer_load_dwordx4 v5, s[44:47], s69 offen offset:0 lds\n\t" \
  "s_mov_b32 s85, 0x481000\n\t" \
  "s_load_dword s84, %[ph], s85\n\t" \
  "buffer_load_dwordx4 v6, s[44:47], s69 offen offset:1024 lds\n\t" \
  "s_mov_b32 s85, 0x3c1000\n\t" \
  "s_load_dword s84, %[pw], s85\n\t" \
  "buffer_load_dwordx4 v7, s[44:47], s69 offen offset:2048 lds\n\t" \
  "s_mov_b32 s85, 0x3f0000\n\t" \
  "s_load_dword s84, %[pw], s85\n\t" \
  "s_add_u32 m0, s38, 9216\n\t" \
  "s_nop 0\n\t" \
  "buffer_load_dwordx4 v2, s[40:43], s70 offen offset:0 lds\n\t" \
  "s_mov_b32 s85, 0x3f0800\n\t" \
  "s_load_dword s84, %[pw], s85\n\t" \
  "buffer_load_dwordx4 v3, s[40:43], s70 offen offset:1024 lds\n\t" \
  "s_mov_b32 s85, 0x3f1000\n\t" \
  "s_load_dword s84, %[pw], s85\n\t" \
  "s_mov_b64 exec, 0xffff\n\t" \
  "buffer_load_dwordx4 v4, s[40:43], s70 offen offset:2048 lds\n\t" \
  "s_add_u32 m0, s39, 12288\n\t" \
  "s_mov_b64 exec, -1\n\t" \
  "buffer_load_dwordx4 v5, s[44:47], s71 offen offset:0 lds\n\t" \
  "s_mov_b32 s85, 0x420000\n\t" \
  "s_load_dword s84, %[pw], s85\n\t" \
  "buffer_load_dwordx4 v6, s[44:47], s71 offen offset:1024 lds\n\t" \
  "s_mov_b32 s85, 0x420800\n\t" \
  "s_load_dword s84, %[pw], s85\n\t" \
  "buffer_load_dwordx4 v7, s[44:47], s71 offen offset:2048 lds\n\t" \
  "s_mov_b32 s68, 0x400\n\t" \
  "s_mov_b32 s69, 0x300000\n\t" \
  "s_mov_b32 s70, 0x24400\n\t" \
  "s_mov_b32 s71, 0x330000\n\t" \
  "s_add_u32 m0, s38, 18432\n\t" \
  "s_waitcnt vmcnt(12)\n\t" \
  "s_barrier\n\t" \
  "buffer_load_dwordx4 v0, s[40:43], s68 offen offset:0 lds\n\t" \
  "s_mov_b32 s85, 0x4e0000\n\t" \
  "s_load_dword s84, %[ph], s85\n\t" \
  "buffer_load_dwordx4 v1, s[40:43], s68 offen offset:1024 lds\n\t" \
  "s_mov_b32 s85, 0x4e0800\n\t" \
  "s_load_dword s84, %[ph], s85\n\t" \
  "s_mov_b64 exec, 0xffff\n\t" \
  "buffer_load_dwordx4 v4, s[40:43], s68 offen offset:2048 lds\n\t" \
  "s_add_u32 m0, s39, 24576\n\t" \
  "s_mov_b64 exec, -1\n\t" \
  "buffer_load_dwordx4 v5, s[44:47], s69 offen offset:0 lds\n\t" \
  "s_mov_b32 s85, 0x4e1000\n\t" \
  "s_load_dword s84, %[ph], s85\n\t" \
  "buffer_load_dwordx4 v6, s[44:47], s69 offen offset:1024 lds\n\t" \
  "s_mov_b32 s85, 0x421000\n\t" \
  "s_load_dword s84, %[pw], s85\n\t" \
  "buffer_load_dwordx4 v7, s[44:47], s69 offen offset:2048 lds\n\t" \
  "s_mov_b32 s85, 0x450000\n\t" \
  "s_load_dword s84, %[pw], s85\n\t" \
  "s_add_u32 m0, s38, 27648\n\t" \
  "s_nop 0\n\t" \
  "buffer_load_dwordx4 v2, s[40:43], s70 offen offset:0 lds\n\t" \
  "s_mov_b32 s85, 0x450800\n\t" \
  "s_load_dword s84, %[pw], s85\n\t" \
  "buffer_load_dwordx4 v3, s[40:43], s70 offen offset:1024 lds\n\t" \
  "s_mov_b32 s85, 0x451000\n\t" \
  "s_load_dword s84, %[pw], s85\n\t" \
  "s_mov_b64 exec, 0xffff\n\t" \
  "buffer_load_dwordx4 v4, s[40:43], s70 offen offset:2048 lds\n\t" \
  "s_add_u32 m0, s39, 36864\n\t" \
  "s_mov_b64 exec, -1\n\t" \
  "buffer_load_dwordx4 v5, s[44:47], s71 offen offset:0 lds\n\t" \
  "s_mov_b32 s85, 0x480000\n\t" \
  "s_load_dword s84, %[pw], s85\n\t" \
  "buffer_load_dwordx4 v6, s[44:47], s71 offen offset:1024 lds\n\t" \
  "s_mov_b32 s85, 0x480800\n\t" \
  "s_load_dword s84, %[pw], s85\n\t" \
  "buffer_load_dwordx4 v7, s[44:47], s71 offen offset:2048 lds\n\t" \
  "s_mov_b32 s68, 0x480\n\t" \
  "s_mov_b32 s69, 0x360000\n\t" \
  "s_mov_b32 s70, 0x24480\n\t" \
  "s_mov_b32 s71, 0x390000\n\t" \
  "s_add_u32 m0, s38, 36864\n\t" \
  "s_waitcnt vmcnt(12)\n\t" \
  "s_barrier\n\t" \
  "buffer_load_dwordx4 v0, s[40:43], s68 offen offset:0 lds\n\t" \
  "s_mov_b32 s85, 0x540000\n\t" \
  "s_load_dword s84, %[ph], s85\n\t" \
  "buffer_load_dwordx4 v1, s[40:43], s68 offen offset:1024 lds\n\t" \
  "s_mov_b32 s85, 0x540800\n\t" \
  "s_load_dword s84, %[ph], s85\n\t" \
  "s_mov_b64 exec, 0xffff\n\t" \
  "buffer_load_dwordx4 v4, s[40:43], s68 offen offset:2048 lds\n\t" \
  "s_add_u32 m0, s39, 49152\n\t" \
  "s_mov_b64 exec, -1\n\t" \
  "buffer_load_dwordx4 v5, s[44:47], s69 offen offset:0 lds\n\t" \
  "s_mov_b32 s85, 0x541000\n\t" \
  "s_load_dword s84, %[ph], s85\n\t" \
  "buffer_load_dwordx4 v6, s[44:47], s69 offen offset:1024 lds\n\t" \
  "s_mov_b32 s85, 0x481000\n\t" \
  "s_load_dword s84, %[pw], s85\n\t" \
  "buffer_load_dwordx4 v7, s[44:47], s69 offen offset:2048 lds\n\t" \
  "s_mov_b32 s85, 0x4b0000\n\t" \
  "s_load_dword s84, %[pw], s85\n\t" \
  "s_add_u32 m0, s38, 46080\n\t" \
  "s_nop 0\n\t" \
  "buffer_load_dwordx4 v2, s[40:43], s70 offen offset:0 lds\n\t" \
  "s_mov_b32 s85, 0x4b0800\n\t" \
  "s_load_dword s84, %[pw], s85\n\t" \
  "buffer_load_dwordx4 v3, s[40:43], s70 offen offset:1024 lds\n\t" \
  "s_mov_b32 s85, 0x4b1000\n\t" \
  "s_load_dword s84, %[pw], s85\n\t" \
  "s_mov_b64 exec, 0xffff\n\t" \
  "buffer_load_dwordx4 v4, s[40:43], s70 offen offset:2048 lds\n\t" \
  "s_add_u32 m0, s39, 61440\n\t" \
  "s_mov_b64 exec, -1\n\t" \
  "buffer_load_dwordx4 v5, s[44:47], s71 offen offset:0 lds\n\t" \
  "s_mov_b32 s85, 0x4e0000\n\t" \
  "s_load_dword s84, %[pw], s85\n\t" \
  "buffer_load_dwordx4 v6, s[44:47], s71 offen offset:1024 lds\n\t" \
  "s_mov_b32 s85, 0x4e0800\n\t" \
  "s_load_dword s84, %[pw], s85\n\t" \
  "buffer_load_dwordx4 v7, s[44:47], s71 offen offset:2048 lds\n\t" \
  "s_mov_b32 s68, 0x500\n\t" \
  "s_mov_b32 s69, 0x3c0000\n\t" \
  "s_mov_b32 s70, 0x24500\n\t" \
  "s_mov_b32 s71, 0x3f0000\n\t" \
  "s_add_u32 m0, s38, 0\n\t" \
  "s_waitcnt vmcnt(12)\n\t" \
  "s_barrier\n\t" \
  "buffer_load_dwordx4 v0, s[40:43], s68 offen offset:0 lds\n\t" \
  "s_mov_b32 s85, 0x5a0000\n\t" \
  "s_load_dword s84, %[ph], s85\n\t" \
  "buffer_load_dwordx4 v1, s[40:43], s68 offen offset:1024 lds\n\t" \
  "s_mov_b32 s85, 0x5a0800\n\t" \
  "s_load_dword s84, %[ph], s85\n\t" \
  "s_mov_b64 exec, 0xffff\n\t" \
  "buffer_load_dwordx4 v4, s[40:43], s68 offen offset:2048 lds\n\t" \
  "s_add_u32 m0, s39, 0\n\t" \
  "s_mov_b64 exec, -1\n\t" \
  "buffer_load_dwordx4 v5, s[44:47], s69 offen offset:0 lds\n\t" \
  "s_mov_b32 s85, 0x5a1000\n\t" \
  "s_load_dword s84, %[ph], s85\n\t" \
  "buffer_load_dwordx4 v6, s[44:47], s69 offen offset:1024 lds\n\t" \
  "s_mov_b32 s85, 0x4e1000\n\t" \
  "s_load_dword s84, %[pw], s85\n\t" \
  "buffer_load_dwordx4 v7, s[44:47], s69 offen offset:2048 lds\n\t" \
  "s_mov_b32 s85, 0x510000\n\t" \
  "s_load_dword s84, %[pw], s85\n\t" \
  "s_add_u32 m0, s38, 9216\n\t" \
  "s_nop 0\n\t" \
  "buffer_load_dwordx4 v2, s[40:43], s70 offen offset:0 lds\n\t" \
  "s_mov_b32 s85, 0x510800\n\t" \
  "s_load_dword s84, %[pw], s85\n\t" \
  "buffer_load_dwordx4 v3, s[40:43], s70 offen offset:1024 lds\n\t" \
  "s_mov_b32 s85, 0x511000\n\t" \
  "s_load_dword s84, %[pw], s85\n\t" \
  "s_mov_b64 exec, 0xffff\n\t" \
  "buffer_load_dwordx4 v4, s[40:43], s70 offen offset:2048 lds\n\t" \
  "s_add_u32 m0, s39, 12288\n\t" \
  "s_mov_b64 exec, -1\n\t" \
  "buffer_load_dwordx4 v5, s[44:47], s71 offen offset:0 lds\n\t" \
  "s_mov_b32 s85, 0x540000\n\t" \
  "s_load_dword s84, %[pw], s85\n\t" \
  "buffer_load_dwordx4 v6, s[44:47], s71 offen offset:1024 lds\n\t" \
  "s_mov_b32 s85, 0x540800\n\t" \
  "s_load_dword s84, %[pw], s85\n\t" \
  "buffer_load_dwordx4 v7, s[44:47], s71 offen offset:2048 lds\n\t" \
  "s_mov_b32 s68, 0x580\n\t" \
  "s_mov_b32 s69, 0x420000\n\t" \
  "s_mov_b32 s70, 0x24580\n\t" \
  "s_mov_b32 s71, 0x450000\n\t" \
  "s_add_u32 m0, s38, 18432\n\t" \
  "s_waitcnt vmcnt(12)\n\t" \
  "s_barrier\n\t" \
  "buffer_load_dwordx4 v0, s[40:43], s68 offen offset:0 lds\n\t" \
  "s_load_dword s84, %[pq], 0x0\n\t" \
  "buffer_load_dwordx4 v1, s[40:43], s68 offen offset:1024 lds\n\t" \
  "s_load_dword s84, %[pq], 0x40\n\t" \
  "s_mov_b64 exec, 0xffff\n\t" \
  "buffer_load_dwordx4 v4, s[40:43], s68 offen offset:2048 lds\n\t" \
  "s_add_u32 m0, s39, 24576\n\t" \
  "s_mov_b64 exec, -1\n\t" \
  "buffer_load_dwordx4 v5, s[44:47], s69 offen offset:0 lds\n\t" \
  "s_load_dword s84, %[pq], 0x800\n\t" \
  "buffer_load_dwordx4 v6, s[44:47], s69 offen offset:1024 lds\n\t" \
  "s_load_dword s84, %[pq], 0x840\n\t" \
  "buffer_load_dwordx4 v7, s[44:47], s69 offen offset:2048 lds\n\t" \
  "s_mov_b32 s85, 0x541000\n\t" \
  "s_load_dword s84, %[pw], s85\n\t" \
  "s_add_u32 m0, s38, 27648\n\t" \
  "s_nop 0\n\t" \
  "buffer_load_dwordx4 v2, s[40:43], s70 offen offset:0 lds\n\t" \
  "s_mov_b32 s85, 0x570000\n\t" \
  "s_load_dword s84, %[pw], s85\n\t" \
  "buffer_load_dwordx4 v3, s[40:43], s70 offen offset:1024 lds\n\t" \
  "s_mov_b32 s85, 0x570800\n\t" \
  "s_load_dword s84, %[pw], s85\n\t" \
  "s_mov_b64 exec, 0xffff\n\t" \
  "buffer_load_dwordx4 v4, s[40:43], s70 offen offset:2048 lds\n\t" \
  "s_add_u32 m0, s39, 36864\n\t" \
  "s_mov_b64 exec, -1\n\t" \
  "buffer_load_dwordx4 v5, s[44:47], s71 offen offset:0 lds\n\t" \
  "s_mov_b32 s85, 0x571000\n\t" \
  "s_load_dword s84, %[pw], s85\n\t" \
  "buffer_load_dwordx4 v6, s[44:47], s71 offen offset:1024 lds\n\t" \
  "s_mov_b32 s85, 0x5a0000\n\t" \
  "s_load_dword s84, %[pw], s85\n\t" \
  "buffer_load_dwordx4 v7, s[44:47], s71 offen offset:2048 lds\n\t" \
  "s_mov_b32 s85, 0x5a0800\n\t" \
  "s_load_dword s84, %[pw], s85\n\t" \
  "s_mov_b32 s68, 0x600\n\t" \
  "s_mov_b32 s69, 0x480000\n\t" \
  "s_mov_b32 s70, 0x24600\n\t" \
  "s_mov_b32 s71, 0x4b0000\n\t" \
  "s_add_u32 m0, s38, 36864\n\t" \
  "s_waitcnt vmcnt(12)\n\t" \
  "s_barrier\n\t" \
  "buffer_load_dwordx4 v0, s[40:43], s68 offen offset:0 lds\n\t" \
  "s_load_dword s84, %[pq], 0x1000\n\t" \
  "buffer_load_dwordx4 v1, s[40:43], s68 offen offset:1024 lds\n\t" \
  "s_load_dword s84, %[pq], 0x1040\n\t" \
  "s_mov_b64 exec, 0xffff\n\t" \
  "buffer_load_dwordx4 v4, s[40:43], s68 offen offset:2048 lds\n\t" \
  "s_add_u32 m0, s39, 49152\n\t" \
  "s_mov_b64 exec, -1\n\t" \
  "buffer_load_dwordx4 v5, s[44:47], s69 offen offset:0 lds\n\t" \
  "s_load_dword s84, %[pq], 0x1800\n\t" \
  "buffer_load_dwordx4 v6, s[44:47], s69 offen offset:1024 lds\n\t" \
  "s_load_dword s84, %[pq], 0x1840\n\t" \
  "buffer_load_dwordx4 v7, s[44:47], s69 offen offset:2048 lds\n\t" \
  "s_mov_b32 s85, 0x5a1000\n\t" \
  "s_load_dword s84, %[pw], s85\n\t" \
  "s_add_u32 m0, s38, 46080\n\t" \
  "s_nop 0\n\t" \
  "buffer_load_dwordx4 v2, s[40:43], s70 offen offset:0 lds\n\t" \
  "s_mov_b32 s85, 0x5d0000\n\t" \
  "s_load_dword s84, %[pw], s85\n\t" \
  "buffer_load_dwordx4 v3, s[40:43], s70 offen offset:1024 lds\n\t" \
  "s_mov_b32 s85, 0x5d0800\n\t" \
  "s_load_dword s84, %[pw], s85\n\t" \
  "s_mov_b64 exec, 0xffff\n\t" \
  "buffer_load_dwordx4 v4, s[40:43], s70 offen offset:2048 lds\n\t" \
  "s_add_u32 m0, s39, 61440\n\t" \
  "s_mov_b64 exec, -1\n\t" \
  "buffer_load_dwordx4 v5, s[44:47], s71 offen offset:0 lds\n\t" \
  "s_mov_b32 s85, 0x5d1000\n\t" \
  "s_load_dword s84, %[pw], s85\n\t" \
  "buffer_load_dwordx4 v6, s[44:47], s71 offen offset:1024 lds\n\t" \
  "buffer_load_dwordx4 v7, s[44:47], s71 offen offset:2048 lds\n\t" \
  "s_mov_b32 s68, 0x680\n\t" \
  "s_mov_b32 s69, 0x4e0000\n\t" \
  "s_mov_b32 s70, 0x24680\n\t" \
  "s_mov_b32 s71, 0x510000\n\t" \
  "s_add_u32 m0, s38, 0\n\t" \
  "s_waitcnt vmcnt(12)\n\t" \
  "s_barrier\n\t" \
  "buffer_load_dwordx4 v0, s[40:43], s68 offen offset:0 lds\n\t" \
  "s_load_dword s84, %[pq], 0x2000\n\t" \
  "buffer_load_dwordx4 v1, s[40:43], s68 offen offset:1024 lds\n\t" \
  "s_load_dword s84, %[pq], 0x2040\n\t" \
  "s_mov_b64 exec, 0xffff\n\t" \
  "buffer_load_dwordx4 v4, s[40:43], s68 offen offset:2048 lds\n\t" \
  "s_add_u32 m0, s39, 0\n\t" \
  "s_mov_b64 exec, -1\n\t" \
  "buffer_load_dwordx4 v5, s[44:47], s69 offen offset:0 lds\n\t" \
  "s_load_dword s84, %[pq], 0x2800\n\t" \
  "buffer_load_dwordx4 v6, s[44:47], s69 offen offset:1024 lds\n\t" \
  "s_load_dword s84, %[pq], 0x2840\n\t" \
  "buffer_load_dwordx4 v7, s[44:47], s69 offen offset:2048 lds\n\t" \
  "s_add_u32 m0, s38, 9216\n\t" \
  "s_nop 0\n\t" \
  "buffer_load_dwordx4 v2, s[40:43], s70 offen offset:0 lds\n\t" \
  "buffer_load_dwordx4 v3, s[40:43], s70 offen offset:1024 lds\n\t" \
  "s_mov_b64 exec, 0xffff\n\t" \
  "buffer_load_dwordx4 v4, s[40:43], s70 offen offset:2048 lds\n\t" \
  "s_add_u32 m0, s39, 12288\n\t" \
  "s_mov_b64 exec, -1\n\t" \
  "buffer_load_dwordx4 v5, s[44:47], s71 offen offset:0 lds\n\t" \
  "buffer_load_dwordx4 v6, s[44:47], s71 offen offset:1024 lds\n\t" \
  "buffer_load_dwordx4 v7, s[44:47], s71 offen offset:2048 lds\n\t" \
  "s_mov_b32 s68, 0x700\n\t" \
  "s_mov_b32 s69, 0x540000\n\t" \
  "s_mov_b32 s70, 0x24700\n\t" \
  "s_mov_b32 s71, 0x570000\n\t" \
  "s_add_u32 m0, s38, 18432\n\t" \
  "s_waitcnt vmcnt(12)\n\t" \
  "s_barrier\n\t" \
  "buffer_load_dwordx4 v0, s[40:43], s68 offen offset:0 lds\n\t" \
  "s_load_dword s84, %[pq], 0x3000\n\t" \
  "buffer_load_dwordx4 v1, s[40:43], s68 offen offset:1024 lds\n\t" \
  "s_load_dword s84, %[pq], 0x3040\n\t" \
  "s_mov_b64 exec, 0xffff\n\t" \
  "buffer_load_dwordx4 v4, s[40:43], s68 offen offset:2048 lds\n\t" \
  "s_add_u32 m0, s39, 24576\n\t" \
  "s_mov_b64 exec, -1\n\t" \
  "buffer_load_dwordx4 v5, s[44:47], s69 offen offset:0 lds\n\t" \
  "s_load_dword s84, %[pq], 0x3800\n\t" \
  "buffer_load_dwordx4 v6, s[44:47], s69 offen offset:1024 lds\n\t" \
  "s_load_dword s84, %[pq], 0x3840\n\t" \
  "buffer_load_dwordx4 v7, s[44:47], s69 offen offset:2048 lds\n\t" \
  "s_add_u32 m0, s38, 27648\n\t" \
  "s_nop 0\n\t" \
  "buffer_load_dwordx4 v2, s[40:43], s70 offen offset:0 lds\n\t" \
  "buffer_load_dwordx4 v3, s[40:43], s70 offen offset:1024 lds\n\t" \
  "s_mov_b64 exec, 0xffff\n\t" \
  "buffer_load_dwordx4 v4, s[40:43], s70 offen offset:2048 lds\n\t" \
  "s_add_u32 m0, s39, 36864\n\t" \
  "s_mov_b64 exec, -1\n\t" \
  "buffer_load_dwordx4 v5, s[44:47], s71 offen offset:0 lds\n\t" \
  "buffer_load_dwordx4 v6, s[44:47], s71 offen offset:1024 lds\n\t" \
  "buffer_load_dwordx4 v7, s[44:47], s71 offen offset:2048 lds\n\t" \
  "s_mov_b32 s68, 0x780\n\t" \
  "s_mov_b32 s69, 0x5a0000\n\t" \
  "s_mov_b32 s70, 0x24780\n\t" \
  "s_mov_b32 s71, 0x5d0000\n\t" \
  "s_add_u32 m0, s38, 36864\n\t" \
  "s_waitcnt vmcnt(12)\n\t" \
  "s_barrier\n\t" \
  "buffer_load_dwordx4 v0, s[40:43], s68 offen offset:0 lds\n\t" \
  "buffer_load_dwordx4 v1, s[40:43], s68 offen offset:1024 lds\n\t" \
  "s_mov_b64 exec, 0xffff\n\t" \
  "buffer_load_dwordx4 v4, s[40:43], s68 offen offset:2048 lds\n\t" \
  "s_add_u32 m0, s39, 49152\n\t" \
  "s_mov_b64 exec, -1\n\t" \
  "buffer_load_dwordx4 v5, s[44:47], s69 offen offset:0 lds\n\t" \
  "buffer_load_dwordx4 v6, s[44:47], s69 offen offset:1024 lds\n\t" \
  "buffer_load_dwordx4 v7, s[44:47], s69 offen offset:2048 lds\n\t" \
  "s_add_u32 m0, s38, 46080\n\t" \
  "s_nop 0\n\t" \
  "buffer_load_dwordx4 v2, s[40:43], s70 offen offset:0 lds\n\t" \
  "buffer_load_dwordx4 v3, s[40:43], s70 offen offset:1024 lds\n\t" \
  "s_mov_b64 exec, 0xffff\n\t" \
  "buffer_load_dwordx4 v4, s[40:43], s70 offen offset:2048 lds\n\t" \
  "s_add_u32 m0, s39, 61440\n\t" \
  "s_mov_b64 exec, -1\n\t" \
  "buffer_load_dwordx4 v5, s[44:47], s71 offen offset:0 lds\n\t" \
  "buffer_load_dwordx4 v6, s[44:47], s71 offen offset:1024 lds\n\t" \
  "buffer_load_dwordx4 v7, s[44:47], s71 offen offset:2048 lds\n\t" \
  "v_readlane_b32 s68, v9, 18\n\t" \
  "v_readlane_b32 s69, v9, 19\n\t" \
  "v_readlane_b32 s70, v9, 20\n\t" \
  "v_readlane_b32 s71, v9, 21\n\t" \
  "v_readlane_b32 s72, v9, 0\n\t" \
  "v_readlane_b32 s73, v9, 1\n\t" \
  "v_readlane_b32 s74, v9, 2\n\t" \
  "v_readlane_b32 s75, v9, 3\n\t" \
  "v_readlane_b32 s76, v9, 4\n\t" \
  "v_readlane_b32 s77, v9, 5\n\t" \
  "s_add_u32 m0, %[ob], 73728\n\t" \
  "s_waitcnt vmcnt(12)\n\t" \
  "s_barrier\n\t" \
  "s_nop 2\n\t" \
  "buffer_load_dwordx4 v8, %[so], s68 offen lds\n\t" \
  "s_add_u32 m0, %[ob], 77824\n\t" \
  "s_nop 0\n\t" \
  "buffer_load_dwordx4 v8, %[so], s69 offen lds\n\t" \
  "s_add_u32 m0, %[ob], 81920\n\t" \
  "s_nop 0\n\t" \
  "buffer_load_dwordx4 v8, %[so], s70 offen lds\n\t" \
  "s_add_u32 m0, %[ob], 86016\n\t" \
  "s_nop 0\n\t" \
  "buffer_load_dwordx4 v8, %[so], s71 offen lds\n\t" \
  "s_add_u32 m0, %[ob], 0\n\t" \
  "s_nop 0\n\t" \
  "buffer_load_dwordx4 v8, %[so], s72 offen lds\n\t" \
  "s_add_u32 m0, %[ob], 4096\n\t" \
  "s_nop 0\n\t" \
  "buffer_load_dwordx4 v8, %[so], s73 offen lds\n\t" \
  "s_add_u32 m0, %[ob], 8192\n\t" \
  "s_nop 0\n\t" \
  "buffer_load_dwordx4 v8, %[so], s74 offen lds\n\t" \
  "s_add_u32 m0, %[ob], 12288\n\t" \
  "s_nop 0\n\t" \
  "buffer_load_dwordx4 v8, %[so], s75 offen lds\n\t" \
  "s_add_u32 m0, %[ob], 16384\n\t" \
  "s_nop 0\n\t" \
  "buffer_load_dwordx4 v8, %[so], s76 offen lds\n\t" \
  "s_add_u32 m0, %[ob], 20480\n\t" \
  "s_nop 0\n\t" \
  "buffer_load_dwordx4 v8, %[so], s77 offen lds\n\t" \
  "s_waitcnt vmcnt(10)\n\t" \
  "s_barrier\n\t" \
  "s_nop 2\n\t" \
  "s_add_u32 m0, %[ob], 90112\n\t" \
  "v_readlane_b32 s68, v9, 22\n\t" \
  "s_nop 4\n\t" \
  "buffer_load_dwordx4 v8, %[so], s68 offen lds\n\t" \
  "s_add_u32 m0, %[ob], 94208\n\t" \
  "v_readlane_b32 s68, v9, 23\n\t" \
  "s_nop 4\n\t" \
  "buffer_load_dwordx4 v8, %[so], s68 offen lds\n\t" \
  "s_add_u32 m0, %[ob], 98304\n\t" \
  "v_readlane_b32 s68, v9, 24\n\t" \
  "s_nop 4\n\t" \
  "buffer_load_dwordx4 v8, %[so], s68 offen lds\n\t" \
  "s_add_u32 m0, %[ob], 102400\n\t" \
  "v_readlane_b32 s68, v9, 25\n\t" \
  "s_nop 4\n\t" \
  "buffer_load_dwordx4 v8, %[so], s68 offen lds\n\t" \
  "s_add_u32 m0, %[ob], 106496\n\t" \
  "v_readlane_b32 s68, v9, 26\n\t" \
  "s_nop 4\n\t" \
  "buffer_load_dwordx4 v8, %[so], s68 offen lds\n\t" \
  "s_add_u32 m0, %[ob], 24576\n\t" \
  "v_readlane_b32 s68, v9, 6\n\t" \
  "s_nop 4\n\t" \
  "buffer_load_dwordx4 v8, %[so], s68 offen lds\n\t" \
  "s_add_u32 m0, %[ob], 28672\n\t" \
  "v_readlane_b32 s68, v9, 7\n\t" \
  "s_nop 4\n\t" \
  "buffer_load_dwordx4 v8, %[so], s68 offen lds\n\t" \
  "s_add_u32 m0, %[ob], 32768\n\t" \
  "v_readlane_b32 s68, v9, 8\n\t" \
  "s_nop 4\n\t" \
  "buffer_load_dwordx4 v8, %[so], s68 offen lds\n\t" \
  "s_add_u32 m0, %[ob], 36864\n\t" \
  "v_readlane_b32 s68, v9, 9\n\t" \
  "s_nop 4\n\t" \
  "buffer_load_dwordx4 v8, %[so], s68 offen lds\n\t" \
  "s_add_u32 m0, %[ob], 40960\n\t" \
  "v_readlane_b32 s68, v9, 10\n\t" \
  "s_nop 4\n\t" \
  "buffer_load_dwordx4 v8, %[so], s68 offen lds\n\t" \
  "s_add_u32 m0, %[ob], 45056\n\t" \
  "v_readlane_b32 s68, v9, 11\n\t" \
  "s_nop 4\n\t" \
  "buffer_load_dwordx4 v8, %[so], s68 offen lds\n\t" \
  "s_nop 3\n\t" \
  "s_barrier\n\t" \
  "s_nop 2\n\t" \
  "s_add_u32 m0, %[ob], 110592\n\t" \
  "v_readlane_b32 s68, v9, 27\n\t" \
  "s_nop 4\n\t" \
  "buffer_load_dwordx4 v8, %[so], s68 offen lds\n\t" \
  "s_add_u32 m0, %[ob], 114688\n\t" \
  "v_readlane_b32 s68, v9, 28\n\t" \
  "s_nop 4\n\t" \
  "buffer_load_dwordx4 v8, %[so], s68 offen lds\n\t" \
  "s_add_u32 m0, %[ob], 118784\n\t" \
  "v_readlane_b32 s68, v9, 29\n\t" \
  "s_nop 4\n\t" \
  "buffer_load_dwordx4 v8, %[so], s68 offen lds\n\t" \
  "s_add_u32 m0, %[ob], 122880\n\t" \
  "v_readlane_b32 s68, v9, 30\n\t" \
  "s_nop 4\n\t" \
  "buffer_load_dwordx4 v8, %[so], s68 offen lds\n\t" \
  "s_add_u32 m0, %[ob], 126976\n\t" \
  "v_readlane_b32 s68, v9, 31\n\t" \
  "s_nop 4\n\t" \
  "buffer_load_dwordx4 v8, %[so], s68 offen lds\n\t" \
  "s_add_u32 m0, %[ob], 49152\n\t" \
  "v_readlane_b32 s68, v9, 12\n\t" \
  "s_nop 4\n\t" \
  "buffer_load_dwordx4 v8, %[so], s68 offen lds\n\t" \
  "s_add_u32 m0, %[ob], 53248\n\t" \
  "v_readlane_b32 s68, v9, 13\n\t" \
  "s_nop 4\n\t" \
  "buffer_load_dwordx4 v8, %[so], s68 offen lds\n\t" \
  "s_add_u32 m0, %[ob], 57344\n\t" \
  "v_readlane_b32 s68, v9, 14\n\t" \
  "s_nop 4\n\t" \
  "buffer_load_dwordx4 v8, %[so], s68 offen lds\n\t" \
  "s_add_u32 m0, %[ob], 61440\n\t" \
  "v_readlane_b32 s68, v9, 15\n\t" \
  "s_nop 4\n\t" \
  "buffer_load_dwordx4 v8, %[so], s68 offen lds\n\t" \
  "s_add_u32 m0, %[ob], 65536\n\t" \
  "v_readlane_b32 s68, v9, 16\n\t" \
  "s_nop 4\n\t" \
  "buffer_load_dwordx4 v8, %[so], s68 offen lds\n\t" \
  "s_add_u32 m0, %[ob], 69632\n\t" \
  "v_readlane_b32 s68, v9, 17\n\t" \
  "s_nop 4\n\t" \
  "buffer_load_dwordx4 v8, %[so], s68 offen lds\n\t" \
  "s_waitcnt vmcnt(0) lgkmcnt(0)\n\t"
#define LD_NBAR 17
#define LD_HT 8
#define LD_TP 3
#define LD_CLOB "v11", "s68", "s69", "s70", "s71", "s72", "s73", "s74", "s75", "s76", "s77", "s78", "s79", "s80", "s81", "s82", "s83", "s85"

#define ENDW
#define TS(i)
#define TSS(i)
#define TOUT(n)

#define SPIN(cond,slp) do{ int sp_=0; while(cond){ __builtin_amdgcn_s_sleep(slp); if(++sp_>(1<<22)) break; } }while(0)
__device__ __forceinline__ unsigned rd1(const char* p){ return __builtin_amdgcn_readfirstlane(*(volatile LDS_AS unsigned*)(LDS_AS char*)p); }
__device__ __forceinline__ unsigned lds_inc(const char* p, int l){ unsigned r=0; if(l==0) r=__hip_atomic_fetch_add((LDS_AS unsigned*)(LDS_AS char*)p, 1u, __ATOMIC_RELAXED, __HIP_MEMORY_SCOPE_WORKGROUP); return __builtin_amdgcn_readfirstlane(r); }
__device__ __forceinline__ void lds_add(unsigned adr){ asm volatile("s_mov_b64 exec, 1\n\tds_add_u32 %0, %1\n\ts_mov_b64 exec, -1" :: "v"(adr), "v"(1u) : "memory"); }
__device__ __forceinline__ void publish(unsigned cnt, const unsigned* base, unsigned voff, unsigned tag){
  unsigned t;
  asm volatile("s_waitcnt vmcnt(0)\n\ts_mov_b64 exec, 1\n\tds_add_rtn_u32 %0, %1, %2\n\ts_waitcnt lgkmcnt(0)\n\tv_cmp_eq_u32 vcc, 3, %0\n\ts_cbranch_vccz PB_%=\n\t"
    "s_mov_b64 exec, 0xffff\n\tglobal_store_dword %3, %4, %5 sc1\n\tPB_%=:\n\ts_mov_b64 exec, -1"
    : "=&v"(t) : "v"(cnt), "v"(1u), "v"(voff), "v"(tag), "s"(base) : "memory", "vcc");
}
__device__ __forceinline__ void bars(int n){
  asm volatile("BL_%=:\n\ts_barrier\n\ts_sub_u32 %0, %0, 1\n\ts_cmp_lg_u32 %0, 0\n\ts_cbranch_scc1 BL_%=" : "+s"(n) :: "memory", "scc");
}
#define barc(rem) barc_(rem)
#define CTRA
#define CTRP
__device__ __forceinline__ void barc_(unsigned& rem){
  asm volatile("s_cmp_eq_u32 %0, 0\n\ts_cbranch_scc1 BC_%=\n\ts_barrier\n\ts_sub_u32 %0, %0, 1\n\tBC_%=:" : "+s"(rem) :: "memory", "scc");
}
template<int SLP> __device__ __forceinline__ void wait_flag_b(const unsigned* p, unsigned voff, unsigned tag, unsigned& rem CTRA){
  int spins=0;
  while(true){
    unsigned v; asm volatile("global_load_dword %0, %1, %2 sc1" : "=v"(v) : "v"(voff), "s"(p) : "memory");
    barc(rem); if(PB2) barc(rem);
    asm volatile("s_waitcnt vmcnt(0)" : "+v"(v) :: "memory");
    if(__all(v==tag) || ++spins>(1<<21)) break;
    if(rem==0) __builtin_amdgcn_s_sleep(SLP);
  }
}
__device__ __forceinline__ void stout(void* sbase, unsigned voff, u32x4 v){
  st16cs(sbase,voff,v);
}

typedef unsigned u32x8 __attribute__((ext_vector_type(8)));
typedef unsigned u32x16 __attribute__((ext_vector_type(16)));
__device__ __forceinline__ char* ptr64(unsigned lo, unsigned hi){ return (char*)(((unsigned long long)hi<<32)|lo); }
__device__ __forceinline__ u32x4 srd(const char* p, unsigned bytes){ return u32x4{(unsigned)(size_t)p,(unsigned)((size_t)p>>32)&0xffffu,bytes,0x20000u}; }
constexpr size_t WS_O=0, WS_KV=(size_t)S*DM*2, WS_Y=(size_t)S*DM*4, WS_FL=(size_t)S*DM*12;

__global__ __launch_bounds__(768,3) void fused_kernel(const u16* x_, const u16* wqkv_, const u16* wo_, char* ws_, u16* out_,
    const u16* kin_, const u16* vin_, const u16* shk_, const u16* hk_, const u16* qin_, const u16* sD_)
{
  __shared__ __attribute__((aligned(128))) char smem[ALL_LDS];
  TS(0);
  int wv;
  asm volatile(
      "s_load_dwordx16 s[48:63], %[ka], 0x0\n\ts_load_dwordx4 s[64:67], %[ka], 0x40\n\ts_load_dwordx2 s[78:79], %[ka], 0x50\n\ts_getpc_b64 s[72:73]\n\tv_readfirstlane_b32 s75, v0\n\ts_lshr_b32 s74, %[b], 3\n\ts_mul_i32 s74, s74, 12\n\ts_lshr_b32 s75, s75, 6\n\t"
      "s_add_u32 s74, s74, s75\n\ts_min_u32 s74, s74, %[cl]\n\ts_lshl_b32 s74, s74, 7\n\ts_mov_b32 m0, %[sk]\n\tv_mov_b32 v1, s74\n\ts_mov_b64 exec, 1\n\tglobal_load_lds_dword v1, s[72:73]\n\ts_mov_b64 exec, -1\n\t"
      "s_mov_b32 s76, %[b]\n\ts_cmp_lt_u32 s75, 4\n\ts_cbranch_scc1 FK_G\n\ts_cmp_lt_u32 s75, 8\n\ts_cbranch_scc0 FK_C\n\t"
      "v_and_b32 v9, 7, v0\n\tv_and_b32 v10, 63, v0\n\ts_lshl_b32 s74, %[b], 19\n\ts_lshl_b32 s73, %[b], 11\n\ts_or_b32 s74, s74, s73\n\ts_and_b32 s74, s74, 0x3fc000\n\ts_lshl_b32 s73, s75, 9\n\t"
      "s_add_u32 s74, s74, s73\n\ts_add_u32 s74, s74, 0x7800\n\tv_lshl_add_u32 v9, v9, 11, s74\n\tv_and_b32 v1, 24, v0\n\ts_lshl2_add_u32 s74, %[b], s75\n\ts_mul_i32 s74, s74, %[nw]\n\tv_lshl_add_u32 v9, v1, 4, v9\n\t"
      "s_add_u32 s74, s74, %[w0]\n\tv_add_lshl_u32 v10, v10, s74, 7\n\t"
      "s_setprio %[lp]\n\ts_and_b32 s74, s75, 3\n\tv_lshrrev_b32 v1, 3, v0\n\tv_lshrrev_b32 v3, 4, v0\n\ts_and_b32 s72, %[b], 1\n\ts_and_b32 s73, %[b], 0xe0\n\ts_lshl_b32 s72, s72, 10\n\ts_lshl2_add_u32 s72, s73, s72\n\t"
      "v_and_b32 v4, 2, v1\n\tv_and_or_b32 v3, v3, 4, v4\n\tv_and_b32 v4, 31, v1\n\tv_xor_b32 v3, v3, v0\n\tv_mul_u32_u24 v4, 0x1800, v4\n\tv_and_b32 v3, 7, v3\n\tv_lshl_add_u32 v3, v3, 4, s72\n\t"
      "s_mul_i32 s39, s74, 0xc00\n\ts_mul_i32 s38, s74, 0x900\n\ts_mul_i32 s74, s74, 18\n\tv_add_u32 v5, v3, v4\n\tv_and_b32 v1, 7, v1\n\tv_add_u32 v6, 0x400, v5\n\tv_add_u32 v1, s74, v1\n\tv_add_u32 v7, 0x800, v5\n\t"
      "s_and_b32 s72, %[b], 6\n\ts_and_b32 s73, %[b], 24\n\ts_lshl4_add_u32 s72, s72, s73\n\tv_xor_b32 v3, v1, v0\n\ts_lshl_b32 s72, s72, 15\n\tv_and_b32 v3, 7, v3\n\tv_lshl_add_u32 v1, v1, 11, s72\n\t"
      "s_mov_b32 s47, 0x20000\n\ts_mov_b32 s46, 0x600000\n\tv_lshl_add_u32 v2, v3, 4, v1\n\ts_mov_b32 s43, s47\n\ts_mov_b32 s42, 0x408000\n\ts_movk_i32 s73, 0x3c00\n\t"
      "v_add_u32 v3, s73, v2\n\ts_add_u32 s38, s38, %[xf]\n\tv_max_u32 v0, 0x8000, v2\n\tv_add_u32 v4, s73, v3\n\tv_max_u32 v1, 0x8000, v3\n\t"
      "s_waitcnt lgkmcnt(0)\n\ts_mov_b64 s[44:45], s[50:51]\n\ts_sub_u32 s40, s48, 0x8000\n\ts_subb_u32 s41, s49, 0\n\t"
      LD_ASM1
      "s_branch FK_L"
      : "=s"(wv)
      : "{v0}"(threadIdx.x), [ka]"s"(__builtin_amdgcn_kernarg_segment_ptr()), [b]"s"(blockIdx.x), [cl]"n"(CODEL-1), [sk]"n"(SINK+256), [lp]"n"(LPR), [xf]"n"(X_OFF), [nw]"n"(3*TPW), [w0]"n"(TW0*3072-12*TPW), [wm]"n"((1<<(3*TPW))-1)
      : "memory", "scc", "s48", "s49", "s50", "s51", "s52", "s53", "s54", "s55", "s72", "s73", "s74", "s75", "s76", "s78", "s79", "v1", "v2", "v3", "v4", "v5", "v6", "v7", "v9", "v10",
        "s40", "s41", "s42", "s43", "s44", "s45", "s46", "s47", "s38", "s39", "s56", "s57", "s58", "s59", "s60", "s61", "s62", "s63", "s64", "s65", "s66", "s67", "s68");
  #define ENTER(lbl) int w8, bid; u32x8 a0, a1; u32x4 a2; u32x2 a3; asm volatile(lbl ":" : "={s75}"(w8), "={s76}"(bid), "={s[48:55]}"(a0), "={s[56:63]}"(a1), "={s[64:67]}"(a2), "={s[78:79]}"(a3) :: "memory");
  #define ARGW asm volatile("s_waitcnt lgkmcnt(0)" : "+{s[48:55]}"(a0), "+{s[56:63]}"(a1), "+{s[64:67]}"(a2), "+{s[78:79]}"(a3));
  #define ROLE const int w=w8&3, xcd=bid&7, idx=bid>>3, rb=(xcd>>1)*4+(idx&3), hp=(xcd&1)*8+(idx>>2), s0=rb*128; (void)s0; (void)hp;
  #define LANEVARS int l; asm volatile("v_mbcnt_lo_u32_b32 %0, -1, 0\n\tv_mbcnt_hi_u32_b32 %0, -1, %0" : "=v"(l)); const int g=l>>4, fr=l&15, tq=(l&15)>>2, tp=l&3; (void)g; (void)fr; (void)tq; (void)tp;
  #define ARGS0 const char* x=ptr64(a0[0],a0[1]); const char* wqkv=ptr64(a0[2],a0[3]); const char* wo=ptr64(a0[4],a0[5]); char* ws=ptr64(a0[6],a0[7]); \
    unsigned* flags=(unsigned*)(ws+WS_FL); const int hot0=(3-(int)(a0[2]>>7))&7; (void)x; (void)wqkv; (void)wo; (void)flags; (void)hot0;
  #define OCOL ((hp&8)|((hp+(int)(a0[2]>>7)-(int)(a0[4]>>7)+OSH)&7))
  #define ORA ((unsigned)(hp-hot0-1)&7u)
  #define ORE(c) (ORA==7u? (c)-1u : (c))
  #define ORO(c) (ORE(c)>=NF && ORE(c)<NF+7u)
  #define ORD(c) (((((ORE(c)<14u? ((ORA==7u? 0u:ORA)+(ORO(c)? ORE(c)-NF : (ORE(c)<NF? ORE(c) : ORE(c)-7u)))%7u : 7u)+hot0+1)&7u))+8u*(((hp>>3)^(ORO(c) || (c)==15u))&1u))
  const unsigned sb0=__builtin_amdgcn_readfirstlane(lds_addr(smem));
  const char* syn=smem+SYN;

  if(__builtin_expect(wv==1,1)){
    ENTER("FK_L")
    u32x4 sx, sw; unsigned xl, wl, xoA, xoB, xo, xo1, xo2, wof[3];
    asm volatile("; descriptors and LDS bases" : "={s[40:43]}"(sx), "={s[44:47]}"(sw), "={s38}"(xl), "={s39}"(wl));
    asm volatile("; lane offsets" : "={v0}"(xoA), "={v1}"(xoB), "={v2}"(xo), "={v3}"(xo1), "={v4}"(xo2), "={v5}"(wof[0]), "={v6}"(wof[1]), "={v7}"(wof[2]));
    unsigned ltr=0, ltr2=0, ltr3=0, sd; (void)ltr; (void)ltr2; (void)ltr3;
    const char* pw;
    {
      ROLE
      LANEVARS
      ARGS0
      const int hg=((idx>>2)-hot0)&7, hi=hot0*4+(idx&3);
      const unsigned long long hm= hg==0? 0ull : hg==HGR? 0xfffull : 0x3full;
      const unsigned hto=(unsigned)(w*393216+(l<6? l*2048+idx*12288 : (l-6)*2048+hi*12288));
      pw=wqkv+(idx*6144+(xcd&1)*1024+(xcd>>1)*256+w*64);
      asm volatile(LD_ASM2
        : "=&{s84}"(sd), "+s"(w8), "+s"(bid), "+s"(a0)
        : "{v0}"(xoA), "{v1}"(xoB), "{v2}"(xo), "{v3}"(xo1), "{v4}"(xo2), "{v5}"(wof[0]), "{v6}"(wof[1]), "{v7}"(wof[2]),
          "{v16}"(hto), [sh]"s"(srd(wqkv+((xcd&1)*8+hot0)*128,6291456u-1920u)), [hm]"s"(hm),
          "{s[40:43]}"(sx), "{s[44:47]}"(sw), "{s38}"(xl), "{s39}"(wl), [pw]"s"(pw), [tsk]"n"(SINK+256)
        : "memory", "scc", "vcc", LD_CLOB);
    }
    ROLE
    LANEVARS
    ARGS0
    const unsigned wd=OCOL*128;
    {
      const unsigned wof2=(unsigned)(w*16384+(l>>3)*2048+((((l&7)>>1)^((l>>4)&3))*32)+(l&1)*16);
      const unsigned to=ORD((unsigned)(l>>1)&15u);
      const unsigned tab=to*131072u+(l&1)*65536;
      asm volatile(LD_ASM3
        : "+{s84}"(sd)
        : "{v0}"(xoA), "{v1}"(xoB), "{v2}"(xo), "{v3}"(xo1), "{v4}"(xo2), "{v5}"(wof[0]), "{v6}"(wof[1]), "{v7}"(wof[2]), "{v8}"(wof2), "{v9}"(tab),
          "{s[40:43]}"(sx), "{s[44:47]}"(sw), [so]"s"(srd(wo+wd,2097152u-wd)), "{s38}"(xl), "{s39}"(wl), [ob]"s"(sb0+w*1024), [pw]"s"(pw),
          [ph]"s"(wqkv+(((xcd&1)*8+hot0)*128+idx*6144+(w>>1)*196608+(w&1)*64)), [pq]"s"(wo+((xcd&1)*1024+((3-(int)(a0[4]>>7))&7)*128+(idx*32+w*8)*2048))
        : "memory", "scc", "vcc", LD_CLOB);
    }
    const unsigned tag=rd1(syn+64)+1u;
    lds_add(sb0+SYN+12);
    TS(2);
    if(w>=4-NPOL){
      const unsigned* fp=flags+F_O+(rb*16+hp)*16; const unsigned fo=ORD((unsigned)l&15u)*4u;
      const unsigned madr=sb0+(l==0? SYN+20 : SINK+l*4);
      SPIN(rd1(syn+12)<4u || rd1(syn+16)<4u,1);
      if(w==3) PUBM(madr,1u);
      if(PD0) __builtin_amdgcn_s_sleep(PD0);
      if(w==2) __builtin_amdgcn_s_sleep(PD1); if(w==1) __builtin_amdgcn_s_sleep(2*PD1); if(w==0) __builtin_amdgcn_s_sleep(3*PD1);
      unsigned rdy=1; int spins=0;
      while(rdy!=0xFFFFu){
        unsigned v;
        asm volatile("s_mov_b64 exec, 0xffff\n\tglobal_atomic_or %0, %1, %3, %2 sc0 sc1\n\ts_mov_b64 exec, -1\n\ts_waitcnt vmcnt(0)" : "=&v"(v) : "v"(fo), "s"(fp), "v"(0u) : "memory");
        unsigned r=((unsigned)__ballot(v==tag)&0xFFFFu)|1u;
        if(++spins>(1<<21)) r=0xFFFFu;
        if(r!=rdy){ PUBM(madr,(unsigned)__builtin_ctz(~r)); rdy=r; if(w<3 && (r&0x3FFFu)==0x3FFFu) break; }
        else __builtin_amdgcn_s_sleep(OSLP);
      }
      TS(3);
      if(bid==0 && w==3){
        int sp=0;
        while(true){
          u32x4 v; asm volatile("global_load_dwordx4 %0, %1, %2 sc1\n\ts_waitcnt vmcnt(0)" : "=&v"(v) : "v"(l*16), "s"(flags+F_Y) : "memory");
          if(__all(v[0]==tag && v[1]==tag && v[2]==tag && v[3]==tag) || ++sp>(1<<20)) break;
          __builtin_amdgcn_s_sleep(12);
        }
        if(l==0) stflag(flags, tag);
      }
    } else { TS(3); }
    TOUT(4);
    __builtin_amdgcn_endpgm();
  }

  if(wv==0){
    ENTER("FK_G")
    ROLE
    __builtin_amdgcn_s_setprio(GPR);
    const int h=w&1, r=w>>1;
    f32x16 e0, e1;
    LANEVARS
    TSS(3);
    ARGW
    ARGS0
    if(PGT&1){ if(w==1 && l<7) touch(ws, l<6? l*2097152+PGO : (unsigned)WS_FL+2048, sb0+SINK+512); }
    if(PGT&2){ if(w==2 && l<4) touch(ptr64(a1[0],a1[1]), l*2097152+PGO, sb0+SINK+512); }
    if(PGT&4){ if(w==3 && l<1) touch(wo, PGO, sb0+SINK+512); if(w==0 && l<2) touch(wqkv, l*2097152+2097152, sb0+SINK+512); }
    TSS(4);
    {
      unsigned lx0=sb0+X_OFF+fr*128+((g^(fr&7))*16), lx1=sb0+X_OFF+fr*128+(((4+g)^(fr&7))*16);
      const int sw=wswz(8*g+tq);
      unsigned lw0=sb0+g*3072+tq*128+(unsigned)((((2*h)^sw)*32)+8*tp), lw1=sb0+g*3072+tq*128+(unsigned)((((2*h+1)^sw)*32)+8*tp);
      char* ot=ws+WS_O+(rb*16+hp)*16384+r*8192+h*2048;
      unsigned it; unsigned long long rm; asm("s_cmp_lg_u32 %1, 0\n\ts_cselect_b64 %0, -1, 0" : "=s"(rm) : "s"(r) : "scc");
      asm volatile(N2_ASM
        : [it]"=&s"(it), "+{v56}"(lx0), "+{v57}"(lx1), "+{v58}"(lw0), "+{v59}"(lw1)
        : "{v62}"(l*16), "{v63}"(sb0+EX_OFF+h*3072+l*16), [r]"s"(r), [ot]"s"(ot), [ou]"s"(ot+4096), [fa]"s"(sb0+SYN+32+w*4), [fp]"s"(sb0+SYN+32+(w^2)*4),
          [vo]"s"(2048-1024*r), [rm]"s"(rm), [z]"s"(rb|r)
        : "memory", "scc", "vcc", N2_CLOB);
    }
    TS(3);
    unsigned tb;
    {
      LANEVARS
      const unsigned tag=rd1(syn+64)+1u;
      const unsigned ra=sb0+(4*g+tq)*128+8*tp; const int sw=(2*g+(tq>>1))&3;
      unsigned b0=ra+((0^sw)*32), b1=ra+((1^sw)*32), b2=ra+((2^sw)*32), b3=ra+((3^sw)*32), vo=(unsigned)(w*4096+l*16);
      tb=ORD((unsigned)l&15u)*16384u;
      asm volatile("" : "+{v160}"(b0), "+{v161}"(b1), "+{v162}"(b2), "+{v163}"(b3), "+{v164}"(vo), "+{v167}"(tb));
      publish(sb0+SYN+16, flags+F_O+rb*256+hp, l*64, tag);
      TS(4);
      __builtin_amdgcn_s_setprio(G2P);
      unsigned c=0, iss=0, th, q0, q1, cn=0;
      asm volatile(G3_ASM
        : "={v[128:143]}"(e0), "={v[144:159]}"(e1), [c]"+s"(c), [iss]"+s"(iss), [th]"=&s"(th), [t0]"=&s"(q0), [t1]"=&s"(q1), [cn]"+&s"(cn),
          "+{v160}"(b0), "+{v161}"(b1), "+{v162}"(b2), "+{v163}"(b3), "+{v167}"(tb)
        : "{v164}"(vo), "{v165}"(sb0+SYN+20), [go]"s"(60+w*4), [ob]"s"(srd(ws+WS_O+rb*262144,262144u))
        : "memory", "scc", "vcc", G3_CLOB);
    }
    {
      LANEVARS
      char* sg=smem+EX_OFF+w*2048;
      char* op=ptr64(a1[0],a1[1])+(s0+w*32)*4096+2048+OCOL*128; const unsigned oo=(unsigned)((l>>3)*4096+(l&7)*16);
      u32x4 ov[4];
      #pragma unroll
      for(int t=0;t<2;t++){
        #pragma unroll
        for(int j=0;j<4;j++){
          #define E2(i) (t? e1[(i)&15] : e0[(i)&15])
          *(volatile LDS_AS u32x2*)(LDS_AS char*)(sg+fr*128+(((2*j+(g>>1))^(fr&7))*16)+(g&1)*8)=u32x2{pack2(E2(4*j),E2(4*j+1)),pack2(E2(4*j+2),E2(4*j+3))};
        }
        #pragma unroll
        for(int i=0;i<2;i++) ov[2*t+i]=*(volatile LDS_AS u32x4*)(LDS_AS char*)(sg+(i*8+(l>>3))*128+(((l&7)^(l>>3))*16));
      }
      #pragma unroll
      for(int i=0;i<4;i++) stout(op+i*32768, oo, ov[i]);
      ENDW TS(6); TOUT(7);
    }
    __builtin_amdgcn_endpgm();
  }

  {
    ENTER("FK_C")
    ROLE
    char* cr=smem+C_OFF+2048+w*4096;
    const unsigned crs=sb0+C_OFF+2048+w*4096;
    const int sblk=bid>>3, td0=(bid&7)*128+(w&1)*64;
    const int vb=(LOCA&1)? xcd*32+(idx&16)+(idx&1)*8+((idx>>1)&7) : bid, fb=(LOCA&2)? (idx&15)*16+xcd*2+(idx>>4) : bid;
    const int d=(vb>>4)*64+(((vb>>3)&1)*4+w)*8+(vb&7);
    int kv0=KV0+((sblk>>KSH)&KVS);
    static_assert(KV0+KVS+PP_NB<=LD_NBAR && KV0>=EB, "barrier budget");
    unsigned tagv, ctr; f32x8 RB;
    const int fd0=(fb&15)*64, fs0=(fb>>4)*128+w*32;
    u32x4 qv[4];
    f32x16 A0,A1,A2,A3;
    const int rw=sblk*64+(w>>1)*32, j=rw>>5;
    unsigned k0,k1,k2,ko,ta,se,so,ha,l16,rb0[2];
    {
      LANEVARS
      if(w==0 && l<25 && l!=16) ((unsigned*)(smem+SYN))[l]=0u;
      if(l<32) *(u32x4*)(smem+C_OFF+w*512+l*16)=u32x4{0,0,0,0};
      asm volatile("" ::: "memory");
      __builtin_amdgcn_s_setprio(CPR);
      const int dg=l&7, rg=l>>3;
      ko=(unsigned)(rg*8192+dg*16);
      const bool top=(rw==0 && rg==0);
      k0=top? ko+6144:ko; k1=top? ko+6144:ko+2048; k2=top? ko+6144:ko+4096;
      ta=sb0+EX_OFF+w*1024+(w&1)*512+dg*64; se=(l&8)? 0x03020706u : 0x05040100u; ha=crs+(32*g-4*tq+4*tp)*2; l16=l*16;
      so=(unsigned)(rg*32768+((j>>2)&3)*128+((dg&4)|((dg+j)&3))*16);
      #pragma unroll
      for(int k=0;k<2;k++){ const int q=fr-1-2*g+k; rb0[k]=sb0+C_OFF+(4+(q>>1))*256+((q>>3)&3)*64+((w+(q>>1))&3)*16+(q&1)*8; }
      asm volatile("" : "+{v152}"(k0), "+{v153}"(k1), "+{v154}"(k2), "+{v155}"(ko), "+{v156}"(ta), "+{v157}"(se), "+{v159}"(so), "+{v163}"(l16), "+{v165}"(ha), "+{v166}"(rb0[0]), "+{v167}"(rb0[1]));
    }
    TSS(3);
    ARGW
    if(EB==1) asm volatile("s_barrier" : "+s"(a1), "+s"(a2), "+s"(a3), "+s"(a0) :: "memory");
    if(EB==2) asm volatile("s_barrier\n\ts_barrier" : "+s"(a1), "+s"(a2), "+s"(a3), "+s"(a0) :: "memory");
    ARGS0
    char* out=ptr64(a1[0],a1[1]); const char* qp=ptr64(a2[2],a2[3])+(fs0*DM+fd0)*2;
    {
      const char* kb=ptr64(a1[2],a1[3])+(rw*DM+td0)*2; const char* vb=ptr64(a1[4],a1[5])+(rw*DM+td0)*2;
      unsigned long long zm; asm("s_cmp_eq_u32 %1, 0\n\ts_cselect_b64 %0, 0xff, 0" : "=s"(zm) : "s"(rw) : "scc");
      unsigned t0;
      if(PGT&8){ const char* tp= w==0? kb : w==1? vb : w==2? qp : ptr64(a2[0],a2[1])+d*4096;
        asm volatile("s_mov_b32 m0, %1\n\ts_mov_b64 exec, 1\n\tglobal_load_lds_dword v163, %0\n\ts_mov_b64 exec, -1" :: "s"(tp), "s"(sb0+SINK+512), "{v163}"(l16) : "memory"); }
      TSS(5);
      asm volatile(PP_ASM
        : [t0]"=&s"(t0), "=&{v164}"(ctr), "+{v159}"(so), "=&{v160}"(tagv), "=&{v[24:31]}"(RB), "=&{v[44:47]}"(qv[0]), "=&{v[48:51]}"(qv[1]), "=&{v[52:55]}"(qv[2]), "=&{v[56:59]}"(qv[3]),
          "=&{v[64:79]}"(A0), "=&{v[80:95]}"(A1), "=&{v[96:111]}"(A2), "=&{v[112:127]}"(A3)
        : "{v152}"(k0), "{v153}"(k1), "{v154}"(k2), "{v155}"(ko), "{v156}"(ta), "{v157}"(se), "{v166}"(rb0[0]), "{v167}"(rb0[1]), "{v165}"(ha), "{v163}"(l16),
          [qb0]"s"(qp), [qb2]"s"(qp+4096), [mk]"s"(0x180u), [em]"s"(1ull|(512ull<<w)), [hd]"s"(crs),
          [kb0]"s"(kb-6144), [kb3]"s"(kb), [kb5]"s"(kb+4096), [vb0]"s"(vb), [vb2]"s"(vb+4096), [sb]"s"(ws+WS_KV+td0*4096+j*512),
          [n0]"s"(kv0-EB-(HH==2)), [ff]"s"(flags+F_PP+(td0>>6)*64+j), [zm]"s"(zm), [shd]"s"(sb0+EX_OFF+w*1024), [tgd]"s"(sb0+SYN+64), [shp]"s"(ptr64(a1[6],a1[7])+(bid&7)*1024), [fl]"s"(flags),
          [hp]"s"(ptr64(a2[0],a2[1])+d*4096), [dp]"s"(ptr64(a3[0],a3[1])+d*2)
        : "memory", "scc", PP_CLOB);
    }
    LANEVARS
    const unsigned tag=__builtin_amdgcn_readfirstlane(tagv)+1u;
    unsigned rem=LD_NBAR-PP_NB-kv0;
    barc(rem);
    const unsigned* pf=flags+F_PP+(d>>6)*64;
    const char* kvp=ws+WS_KV+((vb>>4)*8+(vb&7))*32768+((vb>>3)&1)*64+w*8192;
    const unsigned kvo=(unsigned)((l>>2)*128+(l&3)*16);
    int bidx=((l>=16)? l-16 : (l==0? 63 : l+47))*4;
    asm volatile("" : "+{v32}"(bidx));
    __builtin_amdgcn_s_setprio(3);
    while(rem>PREM) barc(rem);
    wait_flag_b<8>(pf, l*4, tag, rem CTRP);
    #pragma unroll
    for(int i=0;i<4;i++){ if(i%KTH==0 && i) barc(rem); gldsc(kvp+i*2048, kvo, crs+i*1024); }
    barc(rem); if(PB2) barc(rem);
    WAITV(0);
    asm volatile("s_mov_b32 m0, %2\n\ts_mov_b64 exec, 1\n\tglobal_load_lds_dword %0, %1 sc1\n\ts_mov_b64 exec, -1" :: "v"(0u), "s"(pf), "s"(sb0+SYN+100+w*4) : "memory");
    while(rem) barc(rem);
    { int sp=0; while(!__all(*(volatile LDS_AS unsigned*)(LDS_AS char*)(syn+100+(l&3)*4)==tag) && ++sp<(1<<22)) __builtin_amdgcn_s_sleep(1); }
    __builtin_amdgcn_s_setprio(CVP);
    TS(3);
    f32x16 ac0, ac1;
    {
      asm volatile("s_nop 1\n\t" CV_ASM
        : "=&{v[128:143]}"(ac0), "=&{v[144:159]}"(ac1)
          , "+{v[44:47]}"(qv[0]), "+{v[48:51]}"(qv[1]), "+{v[52:55]}"(qv[2]), "+{v[56:59]}"(qv[3])
        : "{v[64:79]}"(A0), "{v[80:95]}"(A1), "{v[96:111]}"(A2), "{v[112:127]}"(A3),
          "{v[24:31]}"(RB), "{v32}"(bidx), "{v33}"(sb0+SYN+80+w*4)
          , "{v42}"((256*(g&1)+16*fr+4*(g&2))*2), [yb]"s"(ws+WS_Y+d*4096)
          , "{v43}"((l>>3)*8192+(l&7)*16), [q0]"s"(qp), [q2]"s"(qp+4096)
        : "memory", "scc", CV_CLOB);
    }
    TS(4);
    __builtin_amdgcn_s_setprio(3);
    publish(sb0+SYN+24, flags+F_Y+vb, l*1024, tag);
    asm volatile("" : "+v"(qv[0]), "+v"(qv[1]), "+v"(qv[2]), "+v"(qv[3]));
    TS(5);
    float qf[32];
    #pragma unroll
    for(int i=0;i<16;i++){ qf[2*i]=bflo(qv[i>>2][i&3]); qf[2*i+1]=bfhi(qv[i>>2][i&3]); }
    unsigned ya=crs+(g*8+tq)*64+tp*8, wa0=crs+fr*128+((g^(fr&7))*16), wa1=crs+fr*128+(((4+g)^(fr&7))*16), ra[4], oo=(unsigned)((l>>3)*16384+(l&7)*16), yo2=(l>>2)*4096+(l&3)*16;
    #pragma unroll
    for(int i=0;i<4;i++){ const int row=4*(l>>3)+i; ra[i]=crs+row*128+(((l&7)^(row&7))*16); }
    char* op=out+fs0*4096+fd0*2; char* op2=op+8192; const char* yg=ws+WS_Y+fd0*4096+fs0*2;
    #pragma unroll
    for(int i=0;i<32;i++) asm volatile("" : "+v"(qf[i]));
    asm volatile("" : "+v"(ya), "+v"(wa0), "+v"(wa1), "+v"(ra[0]), "+v"(ra[1]), "+v"(ra[2]), "+v"(ra[3]), "+v"(oo), "+v"(yo2));
    asm volatile("" : "+s"(op), "+s"(op2), "+s"(yg));
    if(w==0){
      SPIN(rd1(syn+24)<4u,1);
      SPIN(rd1(syn+96)==0u,2);
      { int sp=0;
        while(true){
          unsigned v=tag; asm volatile("s_mov_b64 exec, 0xffff\n\tglobal_atomic_or %0, %1, %3, %2 sc0 sc1\n\ts_mov_b64 exec, -1\n\ts_waitcnt vmcnt(0)" : "+&v"(v) : "v"(l*4), "s"(flags+F_Y+fb*16), "v"(0u) : "memory");
          if(__all(v==tag) || ++sp>(1<<21)) break;
          __builtin_amdgcn_s_sleep(YSLP);
        } }
      PUB(sb0+(l==0? SYN+28 : SINK+l*4), 1u);
    } else SPIN(rd1(syn+28)==0u,YS2);
    #pragma unroll
    for(int i=0;i<4;i++) gldsc(yg+i*65536, yo2, crs+i*1024);
    {
      u32x4 y4[4];
      asm volatile("s_waitcnt vmcnt(0)\n\t"
        "ds_read_b64_tr_b16 v[0:1], %4\n\tds_read_b64_tr_b16 v[2:3], %4 offset:256\n\tds_read_b64_tr_b16 v[4:5], %4 offset:32\n\tds_read_b64_tr_b16 v[6:7], %4 offset:288\n\t"
        "ds_read_b64_tr_b16 v[8:9], %4 offset:2048\n\tds_read_b64_tr_b16 v[10:11], %4 offset:2304\n\tds_read_b64_tr_b16 v[12:13], %4 offset:2080\n\tds_read_b64_tr_b16 v[14:15], %4 offset:2336\n\t"
        "s_waitcnt lgkmcnt(6)\n\tds_write_b128 %5, v[0:3]\n\ts_waitcnt lgkmcnt(5)\n\tds_write_b128 %5, v[4:7] offset:2048\n\t"
        "s_waitcnt lgkmcnt(4)\n\tds_write_b128 %6, v[8:11]\n\ts_waitcnt lgkmcnt(3)\n\tds_write_b128 %6, v[12:15] offset:2048\n\t"
        "ds_read_b128 %0, %7\n\tds_read_b128 %1, %8\n\tds_read_b128 %2, %9\n\tds_read_b128 %3, %10\n\ts_waitcnt lgkmcnt(0)"
        : "=&v"(y4[0]), "=&v"(y4[1]), "=&v"(y4[2]), "=&v"(y4[3])
        : "v"(ya), "v"(wa0), "v"(wa1), "v"(ra[0]), "v"(ra[1]), "v"(ra[2]), "v"(ra[3])
        : "memory", "v0", "v1", "v2", "v3", "v4", "v5", "v6", "v7", "v8", "v9", "v10", "v11", "v12", "v13", "v14", "v15");
      #pragma unroll
      for(int i=0;i<4;i++){
        u32x4 ov;
        #pragma unroll
        for(int e=0;e<4;e++) ov[e]=pack2(mul1(qf[8*i+2*e],bflo(y4[i][e])), mul1(qf[8*i+2*e+1],bfhi(y4[i][e])));
        stout((i&2? op2:op)+(i&1)*4096, oo, ov);
      }
    }
    ENDW TS(6); TOUT(7);
    __builtin_amdgcn_endpgm();
  }
}

extern "C" void kernel_launch(void* const* d_in, const int* in_sizes, int n_in, void* d_out, int out_size, void* d_ws, size_t ws_size, hipStream_t stream){
  const u16* q=(const u16*)d_in[0]; const u16* k=(const u16*)d_in[1]; const u16* v=(const u16*)d_in[2];
  const u16* shk=(const u16*)d_in[3]; const u16* hk=(const u16*)d_in[4]; const u16* sD=(const u16*)d_in[5];
  const u16* x=(const u16*)d_in[6]; const u16* wqkv=(const u16*)d_in[7]; const u16* wo=(const u16*)d_in[8];
  fused_kernel<<<256,768,0,stream>>>(x,wqkv,wo,(char*)d_ws,(u16*)d_out,k,v,shk,hk,q,sD);
}
```

```cpp
#include <hip/hip_runtime.h>
#include <hip/hip_bf16.h>
#include <stdint.h>

typedef unsigned short u16;
typedef short bf16x8 __attribute__((ext_vector_type(8)));
typedef short s16x4 __attribute__((ext_vector_type(4)));
typedef float f32x2 __attribute__((ext_vector_type(2)));
typedef float f32x4 __attribute__((ext_vector_type(4)));
typedef float f32x8 __attribute__((ext_vector_type(8)));
typedef float f32x16 __attribute__((ext_vector_type(16)));
typedef unsigned u32x4 __attribute__((ext_vector_type(4)));
typedef unsigned u32x2 __attribute__((ext_vector_type(2)));
typedef __bf16 bf16x2_t __attribute__((ext_vector_type(2)));
#define LDS_AS __attribute__((address_space(3)))

__device__ __forceinline__ float bflo(unsigned x){ return __uint_as_float(x<<16); }
__device__ __forceinline__ float bfhi(unsigned x){ return __uint_as_float(x&0xffff0000u); }
__device__ __forceinline__ unsigned pack2(float lo, float hi){ return __builtin_bit_cast(unsigned, __builtin_convertvector(f32x2{lo,hi}, bf16x2_t)); }
__device__ __forceinline__ float mul1(float a, float b){ float r; asm("v_mul_f32 %0, %1, %2" : "=v"(r) : "v"(a), "v"(b)); return r; }
__device__ __forceinline__ float fma1(float a, float b, float c){ float r; asm("v_fma_f32 %0, %1, %2, %3" : "=v"(r) : "v"(a), "v"(b), "v"(c)); return r; }

__device__ __forceinline__ void glds(const void* sbase, unsigned voff, unsigned ldsaddr){
  asm volatile("s_mov_b32 m0, %2\n\ts_nop 0\n\tglobal_load_lds_dwordx4 %0, %1" :: "v"(voff), "s"(sbase), "s"(ldsaddr) : "memory");
}
__device__ __forceinline__ void gldsc(const void* sbase, unsigned voff, unsigned ldsaddr){
  asm volatile("s_mov_b32 m0, %2\n\ts_nop 0\n\tglobal_load_lds_dwordx4 %0, %1 sc1" :: "v"(voff), "s"(sbase), "s"(ldsaddr) : "memory");
}
__device__ __forceinline__ void touch(const void* sbase, unsigned voff, unsigned ldsaddr){
  asm volatile("s_mov_b32 m0, %2\n\ts_nop 0\n\tglobal_load_lds_dword %0, %1" :: "v"(voff), "s"(sbase), "s"(ldsaddr) : "memory");
}
__device__ __forceinline__ void st16c(void* p, u32x4 v){ asm volatile("global_store_dwordx4 %0, %1, off sc1\n\ts_nop 1" :: "v"(p), "v"(v) : "memory"); }
__device__ __forceinline__ void st16cs(void* sbase, unsigned voff, u32x4 v){ asm volatile("global_store_dwordx4 %0, %1, %2 sc1\n\ts_nop 1" :: "v"(voff), "v"(v), "s"(sbase) : "memory"); }
#define GLB_AS __attribute__((address_space(1)))
__device__ __forceinline__ unsigned ldflag(const unsigned* p){ return __hip_atomic_load((const GLB_AS unsigned*)p, __ATOMIC_RELAXED, __HIP_MEMORY_SCOPE_AGENT); }
__device__ __forceinline__ void stflag(unsigned* p, unsigned v){ __hip_atomic_store((GLB_AS unsigned*)p, v, __ATOMIC_RELAXED, __HIP_MEMORY_SCOPE_AGENT); }
template<int SLP> __device__ __forceinline__ void wait_flag(const unsigned* p, unsigned tag){
  int spins=0;
  while(true){ unsigned v=ldflag(p); if(__all(v==tag)) break; __builtin_amdgcn_s_sleep(SLP); if(++spins>(1<<21)) break; }
}
__device__ __forceinline__ unsigned lds_addr(const void* p){ return (unsigned)(size_t)(const LDS_AS void*)p; }
#define WAITV(n) asm volatile("s_waitcnt vmcnt(" #n ")":::"memory")
#define WAITL() asm volatile("s_waitcnt lgkmcnt(0)":::"memory")
#define SBAR() asm volatile("s_barrier":::"memory")
#define PUBM(addr,val) asm volatile("ds_max_u32 %0, %1" :: "v"(addr), "v"(val) : "memory")
#define PUB(addr,val) asm volatile("ds_write_b32 %0, %1" :: "v"(addr), "v"(val) : "memory")
__device__ __forceinline__ s16x4 trread(const char* p){ return __builtin_amdgcn_ds_read_tr16_b64_v4i16((LDS_AS s16x4*)p); }
__device__ __forceinline__ bf16x8 cat44(s16x4 a, s16x4 b){ return __builtin_shufflevector(a,b,0,1,2,3,4,5,6,7); }
__device__ __forceinline__ int wswz(int row){ return ((row>>1)&1) | (((row>>3)&1)<<1); }
__device__ __forceinline__ u32x4 widen(u32x2 a, u32x2 b){
  auto r0=__builtin_amdgcn_permlane16_swap(a[0],b[0],false,false);
  auto r1=__builtin_amdgcn_permlane16_swap(a[1],b[1],false,false);
  return u32x4{r0[0],r1[0],r0[1],r1[1]};
}

constexpr int S=2048, DM=1024;
constexpr int WS=12288, XS=18432, X_OFF=6*WS;
constexpr int CREG=4608, C_OFF=131072;
constexpr int EX_OFF=C_OFF+4*CREG;
constexpr int SYN=EX_OFF+8192;
constexpr int SINK=SYN+256;
constexpr int ALL_LDS=SINK+768;
constexpr int F_PP=1024, F_O=4096, F_Y=8192;
#define KV0 1
#define KVS 1
#define LPR 2
#define CPR 2
#define GPR 1
#define CGATE 3
#define OSLP 2
#define YSLP 8
#define TPX 16
#define TPW 2
#define TWO 0
#define CVP 1
#define G2P 3
#define OUTC 1
#define ROT 0
#define WTL 2
#define PB2 0
#define PREM 5
#define KTH 2
#define YDEF 3
#define OPOL 1
#define CODEL 268
#define MIX 0
#define LOCA 3
#define XLOC 1
#define PGT 0
#define PGO 0
#define QREM 0
#define EB 0
#define TW0 1
#define ORDM 1
#define NF 7u
#define NPOL 1
#define PD0 0
#define PD1 4
#define HH 0
#define HW 3
#define HKV 4
#define YPOL 1
#define YS2 2
#define OSH 4
#define HSK 3
#define HGR 2
#define KSH 0
#define EPI 0
#define YFL 0
#define HLAST 1

#define N2_ASM \
  "v_mov_b64 v[64:65], 0\n\t" \
  "v_mov_b64 v[66:67], 0\n\t" \
  "v_mov_b64 v[68:69], 0\n\t" \
  "v_mov_b64 v[70:71], 0\n\t" \
  "v_mov_b64 v[72:73], 0\n\t" \
  "v_mov_b64 v[74:75], 0\n\t" \
  "v_mov_b64 v[76:77], 0\n\t" \
  "v_mov_b64 v[78:79], 0\n\t" \
  "v_mov_b64 v[80:81], 0\n\t" \
  "v_mov_b64 v[82:83], 0\n\t" \
  "v_mov_b64 v[84:85], 0\n\t" \
  "v_mov_b64 v[86:87], 0\n\t" \
  "v_mov_b64 v[88:89], 0\n\t" \
  "v_mov_b64 v[90:91], 0\n\t" \
  "v_mov_b64 v[92:93], 0\n\t" \
  "v_mov_b64 v[94:95], 0\n\t" \
  "v_mov_b64 v[96:97], 0\n\t" \
  "v_mov_b64 v[98:99], 0\n\t" \
  "v_mov_b64 v[100:101], 0\n\t" \
  "v_mov_b64 v[102:103], 0\n\t" \
  "v_mov_b64 v[104:105], 0\n\t" \
  "v_mov_b64 v[106:107], 0\n\t" \
  "v_mov_b64 v[108:109], 0\n\t" \
  "v_mov_b64 v[110:111], 0\n\t" \
  "v_mov_b64 v[112:113], 0\n\t" \
  "v_mov_b64 v[114:115], 0\n\t" \
  "v_mov_b64 v[116:117], 0\n\t" \
  "v_mov_b64 v[118:119], 0\n\t" \
  "v_mov_b64 v[120:121], 0\n\t" \
  "v_mov_b64 v[122:123], 0\n\t" \
  "v_mov_b64 v[124:125], 0\n\t" \
  "v_mov_b64 v[126:127], 0\n\t" \
  "v_mov_b64 v[128:129], 0\n\t" \
  "v_mov_b64 v[130:131], 0\n\t" \
  "v_mov_b64 v[132:133], 0\n\t" \
  "v_mov_b64 v[134:135], 0\n\t" \
  "v_mov_b64 v[136:137], 0\n\t" \
  "v_mov_b64 v[138:139], 0\n\t" \
  "v_mov_b64 v[140:141], 0\n\t" \
  "v_mov_b64 v[142:143], 0\n\t" \
  "v_mov_b64 v[144:145], 0\n\t" \
  "v_mov_b64 v[146:147], 0\n\t" \
  "v_mov_b64 v[148:149], 0\n\t" \
  "v_mov_b64 v[150:151], 0\n\t" \
  "v_mov_b64 v[152:153], 0\n\t" \
  "v_mov_b64 v[154:155], 0\n\t" \
  "v_mov_b64 v[156:157], 0\n\t" \
  "v_mov_b64 v[158:159], 0\n\t" \
  "v_mov_b64 v[160:161], 0\n\t" \
  "v_mov_b64 v[162:163], 0\n\t" \
  "v_mov_b64 v[164:165], 0\n\t" \
  "v_mov_b64 v[166:167], 0\n\t" \
  "v_add_u32 v60, 36864, v58\n\t" \
  "v_add_u32 v61, 36864, v59\n\t" \
  "s_mov_b32 %[it], 0\n\t" \
  "s_cmp_lg_u32 %[r], 0\n\t" \
  "s_barrier\n\t" \
  "s_cbranch_scc1 NB_%=\n\t" \
  "ds_read_b64_tr_b16 v[48:49], v60 offset:14336\n\t" \
  "ds_read_b64_tr_b16 v[50:51], v60 offset:14848\n\t" \
  "ds_read_b128 v[0:3], v56 offset:38912\n\t" \
  "ds_read_b64_tr_b16 v[52:53], v60 offset:26624\n\t" \
  "ds_read_b64_tr_b16 v[54:55], v60 offset:27136\n\t" \
  "ds_read_b128 v[16:19], v57 offset:38912\n\t" \
  "ds_read_b128 v[4:7], v56 offset:40960\n\t" \
  "ds_read_b128 v[20:23], v57 offset:40960\n\t" \
  "ds_read_b128 v[8:11], v56 offset:43008\n\t" \
  "ds_read_b128 v[24:27], v57 offset:43008\n\t" \
  "ds_read_b128 v[12:15], v56 offset:45056\n\t" \
  "ds_read_b128 v[28:31], v57 offset:45056\n\t" \
  "ds_read_b64_tr_b16 v[32:33], v60 offset:13312\n\t" \
  "ds_read_b64_tr_b16 v[34:35], v60 offset:13824\n\t" \
  "s_mov_b32 s68, 0x50000\n\t" \
  "s_mov_b32 s69, 0x5550055\n\t" \
  "s_mov_b32 s70, 0xa0000\n\t" \
  "s_mov_b32 s71, 0xaaa00aa\n\t" \
  "s_mov_b32 s72, 0x150001\n\t" \
  "s_mov_b32 s73, 0x15550155\n\t" \
  "s_mov_b32 s74, 0x2a0002\n\t" \
  "s_mov_b32 s75, 0x2aaa02aa\n\t" \
  "s_mov_b32 s76, 0x55505555\n\t" \
  "s_mov_b32 s77, 0x50005500\n\t" \
  "s_mov_b32 s78, 0xaaa0aaaa\n\t" \
  "s_mov_b32 s79, 0xa000aa00\n\t" \
  "s_mov_b32 s80, 0x55405554\n\t" \
  "s_mov_b32 s81, 0x40005400\n\t" \
  "s_mov_b32 s82, 0xaa80aaa8\n\t" \
  "s_mov_b32 s83, 0x8000a800\n\t" \
  "s_mov_b32 s84, 0xaaaaaaaa\n\t" \
  "s_mov_b32 s85, 0xaaaaaaaa\n\t" \
  "s_waitcnt lgkmcnt(0)\n\t" \
  "s_branch N2a_%=\n\t" \
  "N0a_%=:\n\t" \
  "s_waitcnt lgkmcnt(10)\n\t" \
  "v_mfma_f32_16x16x32_bf16 v[128:131], v[0:3], v[32:35], v[128:131]\n\t" \
  "ds_read_b64_tr_b16 v[44:45], v58 offset:13312\n\t" \
  "s_waitcnt lgkmcnt(7)\n\t" \
  "v_mfma_f32_16x16x32_bf16 v[128:131], v[16:19], v[36:39], v[128:131]\n\t" \
  "ds_read_b64_tr_b16 v[46:47], v58 offset:13824\n\t" \
  "v_mfma_f32_16x16x32_bf16 v[136:139], v[4:7], v[32:35], v[136:139]\n\t" \
  "ds_read_b128 v[48:51], v56 offset:0\n\t" \
  "v_mfma_f32_16x16x32_bf16 v[136:139], v[20:23], v[36:39], v[136:139]\n\t" \
  "ds_read_b128 v[52:55], v57 offset:0\n\t" \
  "s_waitcnt lgkmcnt(8)\n\t" \
  "v_mfma_f32_16x16x32_bf16 v[144:147], v[8:11], v[32:35], v[144:147]\n\t" \
  "v_mfma_f32_16x16x32_bf16 v[144:147], v[24:27], v[36:39], v[144:147]\n\t" \
  "s_waitcnt lgkmcnt(4)\n\t" \
  "v_mfma_f32_16x16x32_bf16 v[152:155], v[12:15], v[32:35], v[152:155]\n\t" \
  "v_mfma_f32_16x16x32_bf16 v[152:155], v[28:31], v[36:39], v[152:155]\n\t" \
  "ds_read_b64_tr_b16 v[32:33], v59 offset:1024\n\t" \
  "v_mfma_f32_16x16x32_bf16 v[96:99], v[40:43], v[0:3], v[96:99]\n\t" \
  "ds_read_b64_tr_b16 v[34:35], v59 offset:1536\n\t" \
  "s_waitcnt lgkmcnt(4)\n\t" \
  "v_mfma_f32_16x16x32_bf16 v[96:99], v[44:47], v[16:19], v[96:99]\n\t" \
  "ds_read_b64_tr_b16 v[36:37], v59 offset:13312\n\t" \
  "v_mfma_f32_16x16x32_bf16 v[104:107], v[40:43], v[4:7], v[104:107]\n\t" \
  "v_mfma_f32_16x16x32_bf16 v[104:107], v[44:47], v[20:23], v[104:107]\n\t" \
  "ds_read_b64_tr_b16 v[38:39], v59 offset:13824\n\t" \
  "v_mfma_f32_16x16x32_bf16 v[112:115], v[40:43], v[8:11], v[112:115]\n\t" \
  "v_mfma_f32_16x16x32_bf16 v[112:115], v[44:47], v[24:27], v[112:115]\n\t" \
  "v_mfma_f32_16x16x32_bf16 v[120:123], v[40:43], v[12:15], v[120:123]\n\t" \
  "v_mfma_f32_16x16x32_bf16 v[120:123], v[44:47], v[28:31], v[120:123]\n\t" \
  "s_waitcnt lgkmcnt(4)\n\t" \
  "v_mfma_f32_16x16x32_bf16 v[160:163], v[40:43], v[48:51], v[160:163]\n\t" \
  "v_mfma_f32_16x16x32_bf16 v[160:163], v[44:47], v[52:55], v[160:163]\n\t" \
  "ds_read_b64_tr_b16 v[40:41], v59 offset:2048\n\t" \
  "s_waitcnt lgkmcnt(1)\n\t" \
  "v_mfma_f32_16x16x32_bf16 v[164:167], v[32:35], v[48:51], v[164:167]\n\t" \
  "ds_read_b64_tr_b16 v[42:43], v59 offset:2560\n\t" \
  "v_mfma_f32_16x16x32_bf16 v[164:167], v[36:39], v[52:55], v[164:167]\n\t" \
  "ds_read_b64_tr_b16 v[44:45], v59 offset:14336\n\t" \
  "v_mfma_f32_16x16x32_bf16 v[100:103], v[32:35], v[0:3], v[100:103]\n\t" \
  "ds_read_b64_tr_b16 v[46:47], v59 offset:14848\n\t" \
  "v_mfma_f32_16x16x32_bf16 v[100:103], v[36:39], v[16:19], v[100:103]\n\t" \
  "ds_read_b64_tr_b16 v[48:49], v58 offset:0\n\t" \
  "v_mfma_f32_16x16x32_bf16 v[108:111], v[32:35], v[4:7], v[108:111]\n\t" \
  "ds_read_b64_tr_b16 v[50:51], v58 offset:512\n\t" \
  "v_mfma_f32_16x16x32_bf16 v[108:111], v[36:39], v[20:23], v[108:111]\n\t" \
  "ds_read_b64_tr_b16 v[52:53], v58 offset:12288\n\t" \
  "v_mfma_f32_16x16x32_bf16 v[116:119], v[32:35], v[8:11], v[116:119]\n\t" \
  "ds_read_b64_tr_b16 v[54:55], v58 offset:12800\n\t" \
  "v_mfma_f32_16x16x32_bf16 v[116:119], v[36:39], v[24:27], v[116:119]\n\t" \
  "v_mfma_f32_16x16x32_bf16 v[124:127], v[32:35], v[12:15], v[124:127]\n\t" \
  "ds_read_b64_tr_b16 v[32:33], v59 offset:0\n\t" \
  "v_mfma_f32_16x16x32_bf16 v[124:127], v[36:39], v[28:31], v[124:127]\n\t" \
  "ds_read_b64_tr_b16 v[34:35], v59 offset:512\n\t" \
  "s_waitcnt lgkmcnt(6)\n\t" \
  "v_mfma_f32_16x16x32_bf16 v[132:135], v[0:3], v[40:43], v[132:135]\n\t" \
  "ds_read_b64_tr_b16 v[36:37], v59 offset:12288\n\t" \
  "v_mfma_f32_16x16x32_bf16 v[132:135], v[16:19], v[44:47], v[132:135]\n\t" \
  "ds_read_b64_tr_b16 v[38:39], v59 offset:12800\n\t" \
  "v_mfma_f32_16x16x32_bf16 v[140:143], v[4:7], v[40:43], v[140:143]\n\t" \
  "v_mfma_f32_16x16x32_bf16 v[140:143], v[20:23], v[44:47], v[140:143]\n\t" \
  "v_mfma_f32_16x16x32_bf16 v[148:151], v[8:11], v[40:43], v[148:151]\n\t" \
  "v_mfma_f32_16x16x32_bf16 v[148:151], v[24:27], v[44:47], v[148:151]\n\t" \
  "v_mfma_f32_16x16x32_bf16 v[156:159], v[12:15], v[40:43], v[156:159]\n\t" \
  "v_mfma_f32_16x16x32_bf16 v[156:159], v[28:31], v[44:47], v[156:159]\n\t" \
  "s_waitcnt lgkmcnt(4)\n\t" \
  "v_mfma_f32_16x16x32_bf16 v[64:67], v[48:51], v[0:3], v[64:67]\n\t" \
  "s_waitcnt lgkmcnt(0)\n\t" \
  "s_barrier\n\t" \
  "v_mfma_f32_16x16x32_bf16 v[64:67], v[52:55], v[16:19], v[64:67]\n\t" \
  "ds_read_b64_tr_b16 v[40:41], v58 offset:26624\n\t" \
  "v_mfma_f32_16x16x32_bf16 v[68:71], v[32:35], v[0:3], v[68:71]\n\t" \
  "ds_read_b128 v[0:3], v56 offset:20480\n\t" \
  "v_mfma_f32_16x16x32_bf16 v[68:71], v[36:39], v[16:19], v[68:71]\n\t" \
  "ds_read_b128 v[16:19], v57 offset:20480\n\t" \
  "v_mfma_f32_16x16x32_bf16 v[72:75], v[48:51], v[4:7], v[72:75]\n\t" \
  "ds_read_b64_tr_b16 v[42:43], v58 offset:27136\n\t" \
  "v_mfma_f32_16x16x32_bf16 v[72:75], v[52:55], v[20:23], v[72:75]\n\t" \
  "ds_read_b64_tr_b16 v[44:45], v60 offset:2048\n\t" \
  "v_mfma_f32_16x16x32_bf16 v[76:79], v[32:35], v[4:7], v[76:79]\n\t" \
  "ds_read_b128 v[4:7], v56 offset:22528\n\t" \
  "v_mfma_f32_16x16x32_bf16 v[76:79], v[36:39], v[20:23], v[76:79]\n\t" \
  "ds_read_b128 v[20:23], v57 offset:22528\n\t" \
  "v_mfma_f32_16x16x32_bf16 v[80:83], v[48:51], v[8:11], v[80:83]\n\t" \
  "ds_read_b64_tr_b16 v[46:47], v60 offset:2560\n\t" \
  "v_mfma_f32_16x16x32_bf16 v[80:83], v[52:55], v[24:27], v[80:83]\n\t" \
  "v_mfma_f32_16x16x32_bf16 v[84:87], v[32:35], v[8:11], v[84:87]\n\t" \
  "ds_read_b128 v[8:11], v56 offset:24576\n\t" \
  "v_mfma_f32_16x16x32_bf16 v[84:87], v[36:39], v[24:27], v[84:87]\n\t" \
  "ds_read_b128 v[24:27], v57 offset:24576\n\t" \
  "v_mfma_f32_16x16x32_bf16 v[88:91], v[48:51], v[12:15], v[88:91]\n\t" \
  "ds_read_b64_tr_b16 v[48:49], v58 offset:25600\n\t" \
  "v_mfma_f32_16x16x32_bf16 v[88:91], v[52:55], v[28:31], v[88:91]\n\t" \
  "ds_read_b64_tr_b16 v[50:51], v58 offset:26112\n\t" \
  "v_mfma_f32_16x16x32_bf16 v[92:95], v[32:35], v[12:15], v[92:95]\n\t" \
  "ds_read_b128 v[12:15], v56 offset:26624\n\t" \
  "v_mfma_f32_16x16x32_bf16 v[92:95], v[36:39], v[28:31], v[92:95]\n\t" \
  "ds_read_b128 v[28:31], v57 offset:26624\n\t" \
  "N1a_%=:\n\t" \
  "s_waitcnt lgkmcnt(10)\n\t" \
  "v_mfma_f32_16x16x32_bf16 v[128:131], v[0:3], v[40:43], v[128:131]\n\t" \
  "ds_read_b64_tr_b16 v[52:53], v60 offset:1024\n\t" \
  "s_waitcnt lgkmcnt(7)\n\t" \
  "v_mfma_f32_16x16x32_bf16 v[128:131], v[16:19], v[44:47], v[128:131]\n\t" \
  "ds_read_b64_tr_b16 v[54:55], v60 offset:1536\n\t" \
  "v_mfma_f32_16x16x32_bf16 v[136:139], v[4:7], v[40:43], v[136:139]\n\t" \
  "ds_read_b128 v[32:35], v56 offset:18432\n\t" \
  "v_mfma_f32_16x16x32_bf16 v[136:139], v[20:23], v[44:47], v[136:139]\n\t" \
  "ds_read_b128 v[36:39], v57 offset:18432\n\t" \
  "s_waitcnt lgkmcnt(8)\n\t" \
  "v_mfma_f32_16x16x32_bf16 v[144:147], v[8:11], v[40:43], v[144:147]\n\t" \
  "v_mfma_f32_16x16x32_bf16 v[144:147], v[24:27], v[44:47], v[144:147]\n\t" \
  "s_waitcnt lgkmcnt(4)\n\t" \
  "v_mfma_f32_16x16x32_bf16 v[152:155], v[12:15], v[40:43], v[152:155]\n\t" \
  "v_mfma_f32_16x16x32_bf16 v[152:155], v[28:31], v[44:47], v[152:155]\n\t" \
  "ds_read_b64_tr_b16 v[40:41], v59 offset:25600\n\t" \
  "v_mfma_f32_16x16x32_bf16 v[96:99], v[48:51], v[0:3], v[96:99]\n\t" \
  "ds_read_b64_tr_b16 v[42:43], v59 offset:26112\n\t" \
  "s_waitcnt lgkmcnt(4)\n\t" \
  "v_mfma_f32_16x16x32_bf16 v[96:99], v[52:55], v[16:19], v[96:99]\n\t" \
  "ds_read_b64_tr_b16 v[44:45], v61 offset:1024\n\t" \
  "v_mfma_f32_16x16x32_bf16 v[104:107], v[48:51], v[4:7], v[104:107]\n\t" \
  "v_mfma_f32_16x16x32_bf16 v[104:107], v[52:55], v[20:23], v[104:107]\n\t" \
  "ds_read_b64_tr_b16 v[46:47], v61 offset:1536\n\t" \
  "v_mfma_f32_16x16x32_bf16 v[112:115], v[48:51], v[8:11], v[112:115]\n\t" \
  "v_mfma_f32_16x16x32_bf16 v[112:115], v[52:55], v[24:27], v[112:115]\n\t" \
  "v_mfma_f32_16x16x32_bf16 v[120:123], v[48:51], v[12:15], v[120:123]\n\t" \
  "v_mfma_f32_16x16x32_bf16 v[120:123], v[52:55], v[28:31], v[120:123]\n\t" \
  "s_waitcnt lgkmcnt(4)\n\t" \
  "v_mfma_f32_16x16x32_bf16 v[160:163], v[48:51], v[32:35], v[160:163]\n\t" \
  "v_mfma_f32_16x16x32_bf16 v[160:163], v[52:55], v[36:39], v[160:163]\n\t" \
  "ds_read_b64_tr_b16 v[48:49], v59 offset:26624\n\t" \
  "s_waitcnt lgkmcnt(1)\n\t" \
  "v_mfma_f32_16x16x32_bf16 v[164:167], v[40:43], v[32:35], v[164:167]\n\t" \
  "ds_read_b64_tr_b16 v[50:51], v59 offset:27136\n\t" \
  "v_mfma_f32_16x16x32_bf16 v[164:167], v[44:47], v[36:39], v[164:167]\n\t" \
  "ds_read_b64_tr_b16 v[52:53], v61 offset:2048\n\t" \
  "v_mfma_f32_16x16x32_bf16 v[100:103], v[40:43], v[0:3], v[100:103]\n\t" \
  "ds_read_b64_tr_b16 v[54:55], v61 offset:2560\n\t" \
  "v_mfma_f32_16x16x32_bf16 v[100:103], v[44:47], v[16:19], v[100:103]\n\t" \
  "ds_read_b64_tr_b16 v[32:33], v58 offset:24576\n\t" \
  "v_mfma_f32_16x16x32_bf16 v[108:111], v[40:43], v[4:7], v[108:111]\n\t" \
  "ds_read_b64_tr_b16 v[34:35], v58 offset:25088\n\t" \
  "v_mfma_f32_16x16x32_bf16 v[108:111], v[44:47], v[20:23], v[108:111]\n\t" \
  "ds_read_b64_tr_b16 v[36:37], v60 offset:0\n\t" \
  "v_mfma_f32_16x16x32_bf16 v[116:119], v[40:43], v[8:11], v[116:119]\n\t" \
  "ds_read_b64_tr_b16 v[38:39], v60 offset:512\n\t" \
  "v_mfma_f32_16x16x32_bf16 v[116:119], v[44:47], v[24:27], v[116:119]\n\t" \
  "v_mfma_f32_16x16x32_bf16 v[124:127], v[40:43], v[12:15], v[124:127]\n\t" \
  "ds_read_b64_tr_b16 v[40:41], v59 offset:24576\n\t" \
  "v_mfma_f32_16x16x32_bf16 v[124:127], v[44:47], v[28:31], v[124:127]\n\t" \
  "ds_read_b64_tr_b16 v[42:43], v59 offset:25088\n\t" \
  "s_waitcnt lgkmcnt(6)\n\t" \
  "v_mfma_f32_16x16x32_bf16 v[132:135], v[0:3], v[48:51], v[132:135]\n\t" \
  "ds_read_b64_tr_b16 v[44:45], v61 offset:0\n\t" \
  "v_mfma_f32_16x16x32_bf16 v[132:135], v[16:19], v[52:55], v[132:135]\n\t" \
  "ds_read_b64_tr_b16 v[46:47], v61 offset:512\n\t" \
  "v_mfma_f32_16x16x32_bf16 v[140:143], v[4:7], v[48:51], v[140:143]\n\t" \
  "v_mfma_f32_16x16x32_bf16 v[140:143], v[20:23], v[52:55], v[140:143]\n\t" \
  "v_mfma_f32_16x16x32_bf16 v[148:151], v[8:11], v[48:51], v[148:151]\n\t" \
  "v_mfma_f32_16x16x32_bf16 v[148:151], v[24:27], v[52:55], v[148:151]\n\t" \
  "v_mfma_f32_16x16x32_bf16 v[156:159], v[12:15], v[48:51], v[156:159]\n\t" \
  "v_mfma_f32_16x16x32_bf16 v[156:159], v[28:31], v[52:55], v[156:159]\n\t" \
  "s_waitcnt lgkmcnt(4)\n\t" \
  "v_mfma_f32_16x16x32_bf16 v[64:67], v[32:35], v[0:3], v[64:67]\n\t" \
  "s_waitcnt lgkmcnt(0)\n\t" \
  "s_barrier\n\t" \
  "v_mfma_f32_16x16x32_bf16 v[64:67], v[36:39], v[16:19], v[64:67]\n\t" \
  "ds_read_b64_tr_b16 v[48:49], v60 offset:14336\n\t" \
  "v_mfma_f32_16x16x32_bf16 v[68:71], v[40:43], v[0:3], v[68:71]\n\t" \
  "ds_read_b128 v[0:3], v56 offset:38912\n\t" \
  "v_mfma_f32_16x16x32_bf16 v[68:71], v[44:47], v[16:19], v[68:71]\n\t" \
  "ds_read_b128 v[16:19], v57 offset:38912\n\t" \
  "v_mfma_f32_16x16x32_bf16 v[72:75], v[32:35], v[4:7], v[72:75]\n\t" \
  "ds_read_b64_tr_b16 v[50:51], v60 offset:14848\n\t" \
  "v_mfma_f32_16x16x32_bf16 v[72:75], v[36:39], v[20:23], v[72:75]\n\t" \
  "ds_read_b64_tr_b16 v[52:53], v60 offset:26624\n\t" \
  "v_mfma_f32_16x16x32_bf16 v[76:79], v[40:43], v[4:7], v[76:79]\n\t" \
  "ds_read_b128 v[4:7], v56 offset:40960\n\t" \
  "v_mfma_f32_16x16x32_bf16 v[76:79], v[44:47], v[20:23], v[76:79]\n\t" \
  "ds_read_b128 v[20:23], v57 offset:40960\n\t" \
  "v_mfma_f32_16x16x32_bf16 v[80:83], v[32:35], v[8:11], v[80:83]\n\t" \
  "ds_read_b64_tr_b16 v[54:55], v60 offset:27136\n\t" \
  "v_mfma_f32_16x16x32_bf16 v[80:83], v[36:39], v[24:27], v[80:83]\n\t" \
  "v_mfma_f32_16x16x32_bf16 v[84:87], v[40:43], v[8:11], v[84:87]\n\t" \
  "ds_read_b128 v[8:11], v56 offset:43008\n\t" \
  "v_mfma_f32_16x16x32_bf16 v[84:87], v[44:47], v[24:27], v[84:87]\n\t" \
  "ds_read_b128 v[24:27], v57 offset:43008\n\t" \
  "v_mfma_f32_16x16x32_bf16 v[88:91], v[32:35], v[12:15], v[88:91]\n\t" \
  "ds_read_b64_tr_b16 v[32:33], v60 offset:13312\n\t" \
  "v_mfma_f32_16x16x32_bf16 v[88:91], v[36:39], v[28:31], v[88:91]\n\t" \
  "ds_read_b64_tr_b16 v[34:35], v60 offset:13824\n\t" \
  "v_mfma_f32_16x16x32_bf16 v[92:95], v[40:43], v[12:15], v[92:95]\n\t" \
  "ds_read_b128 v[12:15], v56 offset:45056\n\t" \
  "v_mfma_f32_16x16x32_bf16 v[92:95], v[44:47], v[28:31], v[92:95]\n\t" \
  "ds_read_b128 v[28:31], v57 offset:45056\n\t" \
  "N2a_%=:\n\t" \
  "s_waitcnt lgkmcnt(10)\n\t" \
  "v_mfma_f32_16x16x32_bf16 v[128:131], v[0:3], v[48:51], v[128:131]\n\t" \
  "ds_read_b64_tr_b16 v[36:37], v60 offset:25600\n\t" \
  "s_waitcnt lgkmcnt(7)\n\t" \
  "v_mfma_f32_16x16x32_bf16 v[128:131], v[16:19], v[52:55], v[128:131]\n\t" \
  "ds_read_b64_tr_b16 v[38:39], v60 offset:26112\n\t" \
  "v_mfma_f32_16x16x32_bf16 v[136:139], v[4:7], v[48:51], v[136:139]\n\t" \
  "ds_read_b128 v[40:43], v56 offset:36864\n\t" \
  "v_mfma_f32_16x16x32_bf16 v[136:139], v[20:23], v[52:55], v[136:139]\n\t" \
  "ds_read_b128 v[44:47], v57 offset:36864\n\t" \
  "s_waitcnt lgkmcnt(8)\n\t" \
  "v_mfma_f32_16x16x32_bf16 v[144:147], v[8:11], v[48:51], v[144:147]\n\t" \
  "v_mfma_f32_16x16x32_bf16 v[144:147], v[24:27], v[52:55], v[144:147]\n\t" \
  "s_waitcnt lgkmcnt(4)\n\t" \
  "v_mfma_f32_16x16x32_bf16 v[152:155], v[12:15], v[48:51], v[152:155]\n\t" \
  "v_mfma_f32_16x16x32_bf16 v[152:155], v[28:31], v[52:55], v[152:155]\n\t" \
  "ds_read_b64_tr_b16 v[48:49], v61 offset:13312\n\t" \
  "v_mfma_f32_16x16x32_bf16 v[96:99], v[32:35], v[0:3], v[96:99]\n\t" \
  "ds_read_b64_tr_b16 v[50:51], v61 offset:13824\n\t" \
  "s_waitcnt lgkmcnt(4)\n\t" \
  "v_mfma_f32_16x16x32_bf16 v[96:99], v[36:39], v[16:19], v[96:99]\n\t" \
  "ds_read_b64_tr_b16 v[52:53], v61 offset:25600\n\t" \
  "v_mfma_f32_16x16x32_bf16 v[104:107], v[32:35], v[4:7], v[104:107]\n\t" \
  "v_mfma_f32_16x16x32_bf16 v[104:107], v[36:39], v[20:23], v[104:107]\n\t" \
  "ds_read_b64_tr_b16 v[54:55], v61 offset:26112\n\t" \
  "v_mfma_f32_16x16x32_bf16 v[112:115], v[32:35], v[8:11], v[112:115]\n\t" \
  "v_mfma_f32_16x16x32_bf16 v[112:115], v[36:39], v[24:27], v[112:115]\n\t" \
  "v_mfma_f32_16x16x32_bf16 v[120:123], v[32:35], v[12:15], v[120:123]\n\t" \
  "v_mfma_f32_16x16x32_bf16 v[120:123], v[36:39], v[28:31], v[120:123]\n\t" \
  "s_waitcnt lgkmcnt(4)\n\t" \
  "v_mfma_f32_16x16x32_bf16 v[160:163], v[32:35], v[40:43], v[160:163]\n\t" \
  "v_mfma_f32_16x16x32_bf16 v[160:163], v[36:39], v[44:47], v[160:163]\n\t" \
  "ds_read_b64_tr_b16 v[32:33], v61 offset:14336\n\t" \
  "s_waitcnt lgkmcnt(1)\n\t" \
  "v_mfma_f32_16x16x32_bf16 v[164:167], v[48:51], v[40:43], v[164:167]\n\t" \
  "ds_read_b64_tr_b16 v[34:35], v61 offset:14848\n\t" \
  "v_mfma_f32_16x16x32_bf16 v[164:167], v[52:55], v[44:47], v[164:167]\n\t" \
  "ds_read_b64_tr_b16 v[36:37], v61 offset:26624\n\t" \
  "v_mfma_f32_16x16x32_bf16 v[100:103], v[48:51], v[0:3], v[100:103]\n\t" \
  "ds_read_b64_tr_b16 v[38:39], v61 offset:27136\n\t" \
  "v_mfma_f32_16x16x32_bf16 v[100:103], v[52:55], v[16:19], v[100:103]\n\t" \
  "ds_read_b64_tr_b16 v[40:41], v60 offset:12288\n\t" \
  "v_mfma_f32_16x16x32_bf16 v[108:111], v[48:51], v[4:7], v[108:111]\n\t" \
  "ds_read_b64_tr_b16 v[42:43], v60 offset:12800\n\t" \
  "v_mfma_f32_16x16x32_bf16 v[108:111], v[52:55], v[20:23], v[108:111]\n\t" \
  "ds_read_b64_tr_b16 v[44:45], v60 offset:24576\n\t" \
  "v_mfma_f32_16x16x32_bf16 v[116:119], v[48:51], v[8:11], v[116:119]\n\t" \
  "ds_read_b64_tr_b16 v[46:47], v60 offset:25088\n\t" \
  "v_mfma_f32_16x16x32_bf16 v[116:119], v[52:55], v[24:27], v[116:119]\n\t" \
  "v_mfma_f32_16x16x32_bf16 v[124:127], v[48:51], v[12:15], v[124:127]\n\t" \
  "ds_read_b64_tr_b16 v[48:49], v61 offset:12288\n\t" \
  "v_mfma_f32_16x16x32_bf16 v[124:127], v[52:55], v[28:31], v[124:127]\n\t" \
  "ds_read_b64_tr_b16 v[50:51], v61 offset:12800\n\t" \
  "s_waitcnt lgkmcnt(6)\n\t" \
  "v_mfma_f32_16x16x32_bf16 v[132:135], v[0:3], v[32:35], v[132:135]\n\t" \
  "ds_read_b64_tr_b16 v[52:53], v61 offset:24576\n\t" \
  "v_mfma_f32_16x16x32_bf16 v[132:135], v[16:19], v[36:39], v[132:135]\n\t" \
  "ds_read_b64_tr_b16 v[54:55], v61 offset:25088\n\t" \
  "v_mfma_f32_16x16x32_bf16 v[140:143], v[4:7], v[32:35], v[140:143]\n\t" \
  "v_mfma_f32_16x16x32_bf16 v[140:143], v[20:23], v[36:39], v[140:143]\n\t" \
  "s_addk_i32 %[it], 1\n\t" \
  "v_mfma_f32_16x16x32_bf16 v[148:151], v[8:11], v[32:35], v[148:151]\n\t" \
  "s_cmp_lt_u32 %[it], 6\n\t" \
  "v_mfma_f32_16x16x32_bf16 v[148:151], v[24:27], v[36:39], v[148:151]\n\t" \
  "v_mfma_f32_16x16x32_bf16 v[156:159], v[12:15], v[32:35], v[156:159]\n\t" \
  "v_mfma_f32_16x16x32_bf16 v[156:159], v[28:31], v[36:39], v[156:159]\n\t" \
  "s_waitcnt lgkmcnt(4)\n\t" \
  "v_mfma_f32_16x16x32_bf16 v[64:67], v[40:43], v[0:3], v[64:67]\n\t" \
  "s_waitcnt lgkmcnt(0)\n\t" \
  "s_barrier\n\t" \
  "v_mfma_f32_16x16x32_bf16 v[64:67], v[44:47], v[16:19], v[64:67]\n\t" \
  "ds_read_b64_tr_b16 v[32:33], v58 offset:2048\n\t" \
  "v_mfma_f32_16x16x32_bf16 v[68:71], v[48:51], v[0:3], v[68:71]\n\t" \
  "ds_read_b128 v[0:3], v56 offset:2048\n\t" \
  "v_mfma_f32_16x16x32_bf16 v[68:71], v[52:55], v[16:19], v[68:71]\n\t" \
  "ds_read_b128 v[16:19], v57 offset:2048\n\t" \
  "v_mfma_f32_16x16x32_bf16 v[72:75], v[40:43], v[4:7], v[72:75]\n\t" \
  "ds_read_b64_tr_b16 v[34:35], v58 offset:2560\n\t" \
  "v_mfma_f32_16x16x32_bf16 v[72:75], v[44:47], v[20:23], v[72:75]\n\t" \
  "ds_read_b64_tr_b16 v[36:37], v58 offset:14336\n\t" \
  "v_mfma_f32_16x16x32_bf16 v[76:79], v[48:51], v[4:7], v[76:79]\n\t" \
  "ds_read_b128 v[4:7], v56 offset:4096\n\t" \
  "v_mfma_f32_16x16x32_bf16 v[76:79], v[52:55], v[20:23], v[76:79]\n\t" \
  "ds_read_b128 v[20:23], v57 offset:4096\n\t" \
  "v_mfma_f32_16x16x32_bf16 v[80:83], v[40:43], v[8:11], v[80:83]\n\t" \
  "ds_read_b64_tr_b16 v[38:39], v58 offset:14848\n\t" \
  "v_mfma_f32_16x16x32_bf16 v[80:83], v[44:47], v[24:27], v[80:83]\n\t" \
  "v_mfma_f32_16x16x32_bf16 v[84:87], v[48:51], v[8:11], v[84:87]\n\t" \
  "ds_read_b128 v[8:11], v56 offset:6144\n\t" \
  "v_mfma_f32_16x16x32_bf16 v[84:87], v[52:55], v[24:27], v[84:87]\n\t" \
  "ds_read_b128 v[24:27], v57 offset:6144\n\t" \
  "v_mfma_f32_16x16x32_bf16 v[88:91], v[40:43], v[12:15], v[88:91]\n\t" \
  "ds_read_b64_tr_b16 v[40:41], v58 offset:1024\n\t" \
  "v_mfma_f32_16x16x32_bf16 v[88:91], v[44:47], v[28:31], v[88:91]\n\t" \
  "ds_read_b64_tr_b16 v[42:43], v58 offset:1536\n\t" \
  "v_mfma_f32_16x16x32_bf16 v[92:95], v[48:51], v[12:15], v[92:95]\n\t" \
  "ds_read_b128 v[12:15], v56 offset:8192\n\t" \
  "v_mfma_f32_16x16x32_bf16 v[92:95], v[52:55], v[28:31], v[92:95]\n\t" \
  "ds_read_b128 v[28:31], v57 offset:8192\n\t" \
  "s_cbranch_scc1 N0a_%=\n\t" \
  "s_setprio 3\n\t" \
  "s_waitcnt lgkmcnt(0)\n\t" \
  "v_cvt_pk_bf16_f32 v120, v120, v121\n\t" \
  "v_cvt_pk_bf16_f32 v121, v122, v123\n\t" \
  "v_cvt_pk_bf16_f32 v122, v124, v125\n\t" \
  "v_cvt_pk_bf16_f32 v123, v126, v127\n\t" \
  "v_cvt_pk_bf16_f32 v28, v152, v153\n\t" \
  "v_cvt_pk_bf16_f32 v29, v154, v155\n\t" \
  "v_cvt_pk_bf16_f32 v38, v156, v157\n\t" \
  "v_cvt_pk_bf16_f32 v39, v158, v159\n\t" \
  "ds_write_b128 v63, v[120:123]\n\t" \
  "ds_write2_b64 v63, v[28:29], v[38:39] offset0:128 offset1:129\n\t" \
  "v_mov_b32 v57, %[fa]\n\t" \
  "v_mov_b32 v58, 1\n\t" \
  "s_mov_b64 exec, 1\n\t" \
  "ds_write_b32 v57, v58\n\t" \
  "s_mov_b64 exec, -1\n\t" \
  "v_cvt_pk_bf16_f32 v160, v160, v161\n\t" \
  "v_cvt_pk_bf16_f32 v161, v162, v163\n\t" \
  "v_cvt_pk_bf16_f32 v162, v164, v165\n\t" \
  "v_cvt_pk_bf16_f32 v163, v166, v167\n\t" \
  "s_branch NX_%=\n\t" \
  "NB_%=:\n\t" \
  "ds_read_b64_tr_b16 v[48:49], v60 offset:13312\n\t" \
  "ds_read_b64_tr_b16 v[50:51], v60 offset:13824\n\t" \
  "ds_read_b128 v[0:3], v56 offset:47104\n\t" \
  "ds_read_b64_tr_b16 v[52:53], v60 offset:25600\n\t" \
  "ds_read_b64_tr_b16 v[54:55], v60 offset:26112\n\t" \
  "ds_read_b128 v[16:19], v57 offset:47104\n\t" \
  "ds_read_b128 v[4:7], v56 offset:49152\n\t" \
  "ds_read_b128 v[20:23], v57 offset:49152\n\t" \
  "ds_read_b128 v[8:11], v56 offset:51200\n\t" \
  "ds_read_b128 v[24:27], v57 offset:51200\n\t" \
  "ds_read_b128 v[12:15], v56 offset:53248\n\t" \
  "ds_read_b128 v[28:31], v57 offset:53248\n\t" \
  "ds_read_b64_tr_b16 v[32:33], v60 offset:14336\n\t" \
  "ds_read_b64_tr_b16 v[34:35], v60 offset:14848\n\t" \
  "s_mov_b32 s68, 0x50000\n\t" \
  "s_mov_b32 s69, 0x5550055\n\t" \
  "s_mov_b32 s70, 0xa0000\n\t" \
  "s_mov_b32 s71, 0xaaa00aa\n\t" \
  "s_mov_b32 s72, 0x150001\n\t" \
  "s_mov_b32 s73, 0x15550155\n\t" \
  "s_mov_b32 s74, 0x2a0002\n\t" \
  "s_mov_b32 s75, 0x2aaa02aa\n\t" \
  "s_mov_b32 s76, 0x55505555\n\t" \
  "s_mov_b32 s77, 0x50005500\n\t" \
  "s_mov_b32 s78, 0xaaa0aaaa\n\t" \
  "s_mov_b32 s79, 0xa000aa00\n\t" \
  "s_mov_b32 s80, 0x55405554\n\t" \
  "s_mov_b32 s81, 0x40005400\n\t" \
  "s_mov_b32 s82, 0xaa80aaa8\n\t" \
  "s_mov_b32 s83, 0x8000a800\n\t" \
  "s_mov_b32 s84, 0xaaaaaaaa\n\t" \
  "s_mov_b32 s85, 0xaaaaaaaa\n\t" \
  "s_waitcnt lgkmcnt(0)\n\t" \
  "s_branch N2b_%=\n\t" \
  "N0b_%=:\n\t" \
  "s_waitcnt lgkmcnt(10)\n\t" \
  "v_mfma_f32_16x16x32_bf16 v[96:99], v[32:35], v[0:3], v[96:99]\n\t" \
  "ds_read_b64_tr_b16 v[44:45], v58 offset:14336\n\t" \
  "s_waitcnt lgkmcnt(7)\n\t" \
  "v_mfma_f32_16x16x32_bf16 v[96:99], v[36:39], v[16:19], v[96:99]\n\t" \
  "ds_read_b64_tr_b16 v[46:47], v58 offset:14848\n\t" \
  "v_mfma_f32_16x16x32_bf16 v[104:107], v[32:35], v[4:7], v[104:107]\n\t" \
  "ds_read_b128 v[48:51], v56 offset:0\n\t" \
  "v_mfma_f32_16x16x32_bf16 v[104:107], v[36:39], v[20:23], v[104:107]\n\t" \
  "ds_read_b128 v[52:55], v57 offset:0\n\t" \
  "s_waitcnt lgkmcnt(8)\n\t" \
  "v_mfma_f32_16x16x32_bf16 v[112:115], v[32:35], v[8:11], v[112:115]\n\t" \
  "v_mfma_f32_16x16x32_bf16 v[112:115], v[36:39], v[24:27], v[112:115]\n\t" \
  "s_waitcnt lgkmcnt(4)\n\t" \
  "v_mfma_f32_16x16x32_bf16 v[120:123], v[32:35], v[12:15], v[120:123]\n\t" \
  "v_mfma_f32_16x16x32_bf16 v[120:123], v[36:39], v[28:31], v[120:123]\n\t" \
  "ds_read_b64_tr_b16 v[32:33], v59 offset:2048\n\t" \
  "v_mfma_f32_16x16x32_bf16 v[128:131], v[0:3], v[40:43], v[128:131]\n\t" \
  "ds_read_b64_tr_b16 v[34:35], v59 offset:2560\n\t" \
  "s_waitcnt lgkmcnt(4)\n\t" \
  "v_mfma_f32_16x16x32_bf16 v[128:131], v[16:19], v[44:47], v[128:131]\n\t" \
  "ds_read_b64_tr_b16 v[36:37], v59 offset:14336\n\t" \
  "v_mfma_f32_16x16x32_bf16 v[136:139], v[4:7], v[40:43], v[136:139]\n\t" \
  "v_mfma_f32_16x16x32_bf16 v[136:139], v[20:23], v[44:47], v[136:139]\n\t" \
  "ds_read_b64_tr_b16 v[38:39], v59 offset:14848\n\t" \
  "v_mfma_f32_16x16x32_bf16 v[144:147], v[8:11], v[40:43], v[144:147]\n\t" \
  "v_mfma_f32_16x16x32_bf16 v[144:147], v[24:27], v[44:47], v[144:147]\n\t" \
  "v_mfma_f32_16x16x32_bf16 v[152:155], v[12:15], v[40:43], v[152:155]\n\t" \
  "v_mfma_f32_16x16x32_bf16 v[152:155], v[28:31], v[44:47], v[152:155]\n\t" \
  "s_waitcnt lgkmcnt(4)\n\t" \
  "v_mfma_f32_16x16x32_bf16 v[160:163], v[48:51], v[40:43], v[160:163]\n\t" \
  "v_mfma_f32_16x16x32_bf16 v[160:163], v[52:55], v[44:47], v[160:163]\n\t" \
  "ds_read_b64_tr_b16 v[40:41], v59 offset:1024\n\t" \
  "s_waitcnt lgkmcnt(1)\n\t" \
  "v_mfma_f32_16x16x32_bf16 v[164:167], v[48:51], v[32:35], v[164:167]\n\t" \
  "ds_read_b64_tr_b16 v[42:43], v59 offset:1536\n\t" \
  "v_mfma_f32_16x16x32_bf16 v[164:167], v[52:55], v[36:39], v[164:167]\n\t" \
  "ds_read_b64_tr_b16 v[44:45], v59 offset:13312\n\t" \
  "v_mfma_f32_16x16x32_bf16 v[132:135], v[0:3], v[32:35], v[132:135]\n\t" \
  "ds_read_b64_tr_b16 v[46:47], v59 offset:13824\n\t" \
  "v_mfma_f32_16x16x32_bf16 v[132:135], v[16:19], v[36:39], v[132:135]\n\t" \
  "ds_read_b64_tr_b16 v[48:49], v58 offset:0\n\t" \
  "v_mfma_f32_16x16x32_bf16 v[140:143], v[4:7], v[32:35], v[140:143]\n\t" \
  "ds_read_b64_tr_b16 v[50:51], v58 offset:512\n\t" \
  "v_mfma_f32_16x16x32_bf16 v[140:143], v[20:23], v[36:39], v[140:143]\n\t" \
  "ds_read_b64_tr_b16 v[52:53], v58 offset:12288\n\t" \
  "v_mfma_f32_16x16x32_bf16 v[148:151], v[8:11], v[32:35], v[148:151]\n\t" \
  "ds_read_b64_tr_b16 v[54:55], v58 offset:12800\n\t" \
  "v_mfma_f32_16x16x32_bf16 v[148:151], v[24:27], v[36:39], v[148:151]\n\t" \
  "v_mfma_f32_16x16x32_bf16 v[156:159], v[12:15], v[32:35], v[156:159]\n\t" \
  "ds_read_b64_tr_b16 v[32:33], v59 offset:0\n\t" \
  "v_mfma_f32_16x16x32_bf16 v[156:159], v[28:31], v[36:39], v[156:159]\n\t" \
  "ds_read_b64_tr_b16 v[34:35], v59 offset:512\n\t" \
  "s_waitcnt lgkmcnt(6)\n\t" \
  "v_mfma_f32_16x16x32_bf16 v[100:103], v[40:43], v[0:3], v[100:103]\n\t" \
  "ds_read_b64_tr_b16 v[36:37], v59 offset:12288\n\t" \
  "v_mfma_f32_16x16x32_bf16 v[100:103], v[44:47], v[16:19], v[100:103]\n\t" \
  "ds_read_b64_tr_b16 v[38:39], v59 offset:12800\n\t" \
  "v_mfma_f32_16x16x32_bf16 v[108:111], v[40:43], v[4:7], v[108:111]\n\t" \
  "v_mfma_f32_16x16x32_bf16 v[108:111], v[44:47], v[20:23], v[108:111]\n\t" \
  "v_mfma_f32_16x16x32_bf16 v[116:119], v[40:43], v[8:11], v[116:119]\n\t" \
  "v_mfma_f32_16x16x32_bf16 v[116:119], v[44:47], v[24:27], v[116:119]\n\t" \
  "v_mfma_f32_16x16x32_bf16 v[124:127], v[40:43], v[12:15], v[124:127]\n\t" \
  "v_mfma_f32_16x16x32_bf16 v[124:127], v[44:47], v[28:31], v[124:127]\n\t" \
  "s_waitcnt lgkmcnt(4)\n\t" \
  "v_mfma_f32_16x16x32_bf16 v[64:67], v[48:51], v[0:3], v[64:67]\n\t" \
  "s_waitcnt lgkmcnt(0)\n\t" \
  "s_barrier\n\t" \
  "v_mfma_f32_16x16x32_bf16 v[64:67], v[52:55], v[16:19], v[64:67]\n\t" \
  "ds_read_b64_tr_b16 v[40:41], v58 offset:25600\n\t" \
  "v_mfma_f32_16x16x32_bf16 v[68:71], v[32:35], v[0:3], v[68:71]\n\t" \
  "ds_read_b128 v[0:3], v56 offset:28672\n\t" \
  "v_mfma_f32_16x16x32_bf16 v[68:71], v[36:39], v[16:19], v[68:71]\n\t" \
  "ds_read_b128 v[16:19], v57 offset:28672\n\t" \
  "v_mfma_f32_16x16x32_bf16 v[72:75], v[48:51], v[4:7], v[72:75]\n\t" \
  "ds_read_b64_tr_b16 v[42:43], v58 offset:26112\n\t" \
  "v_mfma_f32_16x16x32_bf16 v[72:75], v[52:55], v[20:23], v[72:75]\n\t" \
  "ds_read_b64_tr_b16 v[44:45], v60 offset:1024\n\t" \
  "v_mfma_f32_16x16x32_bf16 v[76:79], v[32:35], v[4:7], v[76:79]\n\t" \
  "ds_read_b128 v[4:7], v56 offset:30720\n\t" \
  "v_mfma_f32_16x16x32_bf16 v[76:79], v[36:39], v[20:23], v[76:79]\n\t" \
  "ds_read_b128 v[20:23], v57 offset:30720\n\t" \
  "v_mfma_f32_16x16x32_bf16 v[80:83], v[48:51], v[8:11], v[80:83]\n\t" \
  "ds_read_b64_tr_b16 v[46:47], v60 offset:1536\n\t" \
  "v_mfma_f32_16x16x32_bf16 v[80:83], v[52:55], v[24:27], v[80:83]\n\t" \
  "v_mfma_f32_16x16x32_bf16 v[84:87], v[32:35], v[8:11], v[84:87]\n\t" \
  "ds_read_b128 v[8:11], v56 offset:32768\n\t" \
  "v_mfma_f32_16x16x32_bf16 v[84:87], v[36:39], v[24:27], v[84:87]\n\t" \
  "ds_read_b128 v[24:27], v57 offset:32768\n\t" \
  "v_mfma_f32_16x16x32_bf16 v[88:91], v[48:51], v[12:15], v[88:91]\n\t" \
  "ds_read_b64_tr_b16 v[48:49], v58 offset:26624\n\t" \
  "v_mfma_f32_16x16x32_bf16 v[88:91], v[52:55], v[28:31], v[88:91]\n\t" \
  "ds_read_b64_tr_b16 v[50:51], v58 offset:27136\n\t" \
  "v_mfma_f32_16x16x32_bf16 v[92:95], v[32:35], v[12:15], v[92:95]\n\t" \
  "ds_read_b128 v[12:15], v56 offset:34816\n\t" \
  "v_mfma_f32_16x16x32_bf16 v[92:95], v[36:39], v[28:31], v[92:95]\n\t" \
  "ds_read_b128 v[28:31], v57 offset:34816\n\t" \
  "N1b_%=:\n\t" \
  "s_waitcnt lgkmcnt(10)\n\t" \
  "v_mfma_f32_16x16x32_bf16 v[96:99], v[40:43], v[0:3], v[96:99]\n\t" \
  "ds_read_b64_tr_b16 v[52:53], v60 offset:2048\n\t" \
  "s_waitcnt lgkmcnt(7)\n\t" \
  "v_mfma_f32_16x16x32_bf16 v[96:99], v[44:47], v[16:19], v[96:99]\n\t" \
  "ds_read_b64_tr_b16 v[54:55], v60 offset:2560\n\t" \
  "v_mfma_f32_16x16x32_bf16 v[104:107], v[40:43], v[4:7], v[104:107]\n\t" \
  "ds_read_b128 v[32:35], v56 offset:18432\n\t" \
  "v_mfma_f32_16x16x32_bf16 v[104:107], v[44:47], v[20:23], v[104:107]\n\t" \
  "ds_read_b128 v[36:39], v57 offset:18432\n\t" \
  "s_waitcnt lgkmcnt(8)\n\t" \
  "v_mfma_f32_16x16x32_bf16 v[112:115], v[40:43], v[8:11], v[112:115]\n\t" \
  "v_mfma_f32_16x16x32_bf16 v[112:115], v[44:47], v[24:27], v[112:115]\n\t" \
  "s_waitcnt lgkmcnt(4)\n\t" \
  "v_mfma_f32_16x16x32_bf16 v[120:123], v[40:43], v[12:15], v[120:123]\n\t" \
  "v_mfma_f32_16x16x32_bf16 v[120:123], v[44:47], v[28:31], v[120:123]\n\t" \
  "ds_read_b64_tr_b16 v[40:41], v59 offset:26624\n\t" \
  "v_mfma_f32_16x16x32_bf16 v[128:131], v[0:3], v[48:51], v[128:131]\n\t" \
  "ds_read_b64_tr_b16 v[42:43], v59 offset:27136\n\t" \
  "s_waitcnt lgkmcnt(4)\n\t" \
  "v_mfma_f32_16x16x32_bf16 v[128:131], v[16:19], v[52:55], v[128:131]\n\t" \
  "ds_read_b64_tr_b16 v[44:45], v61 offset:2048\n\t" \
  "v_mfma_f32_16x16x32_bf16 v[136:139], v[4:7], v[48:51], v[136:139]\n\t" \
  "v_mfma_f32_16x16x32_bf16 v[136:139], v[20:23], v[52:55], v[136:139]\n\t" \
  "ds_read_b64_tr_b16 v[46:47], v61 offset:2560\n\t" \
  "v_mfma_f32_16x16x32_bf16 v[144:147], v[8:11], v[48:51], v[144:147]\n\t" \
  "v_mfma_f32_16x16x32_bf16 v[144:147], v[24:27], v[52:55], v[144:147]\n\t" \
  "v_mfma_f32_16x16x32_bf16 v[152:155], v[12:15], v[48:51], v[152:155]\n\t" \
  "v_mfma_f32_16x16x32_bf16 v[152:155], v[28:31], v[52:55], v[152:155]\n\t" \
  "s_waitcnt lgkmcnt(4)\n\t" \
  "v_mfma_f32_16x16x32_bf16 v[160:163], v[32:35], v[48:51], v[160:163]\n\t" \
  "v_mfma_f32_16x16x32_bf16 v[160:163], v[36:39], v[52:55], v[160:163]\n\t" \
  "ds_read_b64_tr_b16 v[48:49], v59 offset:25600\n\t" \
  "s_waitcnt lgkmcnt(1)\n\t" \
  "v_mfma_f32_16x16x32_bf16 v[164:167], v[32:35], v[40:43], v[164:167]\n\t" \
  "ds_read_b64_tr_b16 v[50:51], v59 offset:26112\n\t" \
  "v_mfma_f32_16x16x32_bf16 v[164:167], v[36:39], v[44:47], v[164:167]\n\t" \
  "ds_read_b64_tr_b16 v[52:53], v61 offset:1024\n\t" \
  "v_mfma_f32_16x16x32_bf16 v[132:135], v[0:3], v[40:43], v[132:135]\n\t" \
  "ds_read_b64_tr_b16 v[54:55], v61 offset:1536\n\t" \
  "v_mfma_f32_16x16x32_bf16 v[132:135], v[16:19], v[44:47], v[132:135]\n\t" \
  "ds_read_b64_tr_b16 v[32:33], v58 offset:24576\n\t" \
  "v_mfma_f32_16x16x32_bf16 v[140:143], v[4:7], v[40:43], v[140:143]\n\t" \
  "ds_read_b64_tr_b16 v[34:35], v58 offset:25088\n\t" \
  "v_mfma_f32_16x16x32_bf16 v[140:143], v[20:23], v[44:47], v[140:143]\n\t" \
  "ds_read_b64_tr_b16 v[36:37], v60 offset:0\n\t" \
  "v_mfma_f32_16x16x32_bf16 v[148:151], v[8:11], v[40:43], v[148:151]\n\t" \
  "ds_read_b64_tr_b16 v[38:39], v60 offset:512\n\t" \
  "v_mfma_f32_16x16x32_bf16 v[148:151], v[24:27], v[44:47], v[148:151]\n\t" \
  "v_mfma_f32_16x16x32_bf16 v[156:159], v[12:15], v[40:43], v[156:159]\n\t" \
  "ds_read_b64_tr_b16 v[40:41], v59 offset:24576\n\t" \
  "v_mfma_f32_16x16x32_bf16 v[156:159], v[28:31], v[44:47], v[156:159]\n\t" \
  "ds_read_b64_tr_b16 v[42:43], v59 offset:25088\n\t" \
  "s_waitcnt lgkmcnt(6)\n\t" \
  "v_mfma_f32_16x16x32_bf16 v[100:103], v[48:51], v[0:3], v[100:103]\n\t" \
  "ds_read_b64_tr_b16 v[44:45], v61 offset:0\n\t" \
  "v_mfma_f32_16x16x32_bf16 v[100:103], v[52:55], v[16:19], v[100:103]\n\t" \
  "ds_read_b64_tr_b16 v[46:47], v61 offset:512\n\t" \
  "v_mfma_f32_16x16x32_bf16 v[108:111], v[48:51], v[4:7], v[108:111]\n\t" \
  "v_mfma_f32_16x16x32_bf16 v[108:111], v[52:55], v[20:23], v[108:111]\n\t" \
  "v_mfma_f32_16x16x32_bf16 v[116:119], v[48:51], v[8:11], v[116:119]\n\t" \
  "v_mfma_f32_16x16x32_bf16 v[116:119], v[52:55], v[24:27], v[116:119]\n\t" \
  "v_mfma_f32_16x16x32_bf16 v[124:127], v[48:51], v[12:15], v[124:127]\n\t" \
  "v_mfma_f32_16x16x32_bf16 v[124:127], v[52:55], v[28:31], v[124:127]\n\t" \
  "s_waitcnt lgkmcnt(4)\n\t" \
  "v_mfma_f32_16x16x32_bf16 v[64:67], v[32:35], v[0:3], v[64:67]\n\t" \
  "s_waitcnt lgkmcnt(0)\n\t" \
  "s_barrier\n\t" \
  "v_mfma_f32_16x16x32_bf16 v[64:67], v[36:39], v[16:19], v[64:67]\n\t" \
  "ds_read_b64_tr_b16 v[48:49], v60 offset:13312\n\t" \
  "v_mfma_f32_16x16x32_bf16 v[68:71], v[40:43], v[0:3], v[68:71]\n\t" \
  "ds_read_b128 v[0:3], v56 offset:47104\n\t" \
  "v_mfma_f32_16x16x32_bf16 v[68:71], v[44:47], v[16:19], v[68:71]\n\t" \
  "ds_read_b128 v[16:19], v57 offset:47104\n\t" \
  "v_mfma_f32_16x16x32_bf16 v[72:75], v[32:35], v[4:7], v[72:75]\n\t" \
  "ds_read_b64_tr_b16 v[50:51], v60 offset:13824\n\t" \
  "v_mfma_f32_16x16x32_bf16 v[72:75], v[36:39], v[20:23], v[72:75]\n\t" \
  "ds_read_b64_tr_b16 v[52:53], v60 offset:25600\n\t" \
  "v_mfma_f32_16x16x32_bf16 v[76:79], v[40:43], v[4:7], v[76:79]\n\t" \
  "ds_read_b128 v[4:7], v56 offset:49152\n\t" \
  "v_mfma_f32_16x16x32_bf16 v[76:79], v[44:47], v[20:23], v[76:79]\n\t" \
  "ds_read_b128 v[20:23], v57 offset:49152\n\t" \
  "v_mfma_f32_16x16x32_bf16 v[80:83], v[32:35], v[8:11], v[80:83]\n\t" \
  "ds_read_b64_tr_b16 v[54:55], v60 offset:26112\n\t" \
  "v_mfma_f32_16x16x32_bf16 v[80:83], v[36:39], v[24:27], v[80:83]\n\t" \
  "v_mfma_f32_16x16x32_bf16 v[84:87], v[40:43], v[8:11], v[84:87]\n\t" \
  "ds_read_b128 v[8:11], v56 offset:51200\n\t" \
  "v_mfma_f32_16x16x32_bf16 v[84:87], v[44:47], v[24:27], v[84:87]\n\t" \
  "ds_read_b128 v[24:27], v57 offset:51200\n\t" \
  "v_mfma_f32_16x16x32_bf16 v[88:91], v[32:35], v[12:15], v[88:91]\n\t" \
  "ds_read_b64_tr_b16 v[32:33], v60 offset:14336\n\t" \
  "v_mfma_f32_16x16x32_bf16 v[88:91], v[36:39], v[28:31], v[88:91]\n\t" \
  "ds_read_b64_tr_b16 v[34:35], v60 offset:14848\n\t" \
  "v_mfma_f32_16x16x32_bf16 v[92:95], v[40:43], v[12:15], v[92:95]\n\t" \
  "ds_read_b128 v[12:15], v56 offset:53248\n\t" \
  "v_mfma_f32_16x16x32_bf16 v[92:95], v[44:47], v[28:31], v[92:95]\n\t" \
  "ds_read_b128 v[28:31], v57 offset:53248\n\t" \
  "N2b_%=:\n\t" \
  "s_waitcnt lgkmcnt(10)\n\t" \
  "v_mfma_f32_16x16x32_bf16 v[96:99], v[48:51], v[0:3], v[96:99]\n\t" \
  "ds_read_b64_tr_b16 v[36:37], v60 offset:26624\n\t" \
  "s_waitcnt lgkmcnt(7)\n\t" \
  "v_mfma_f32_16x16x32_bf16 v[96:99], v[52:55], v[16:19], v[96:99]\n\t" \
  "ds_read_b64_tr_b16 v[38:39], v60 offset:27136\n\t" \
  "v_mfma_f32_16x16x32_bf16 v[104:107], v[48:51], v[4:7], v[104:107]\n\t" \
  "ds_read_b128 v[40:43], v56 offset:36864\n\t" \
  "v_mfma_f32_16x16x32_bf16 v[104:107], v[52:55], v[20:23], v[104:107]\n\t" \
  "ds_read_b128 v[44:47], v57 offset:36864\n\t" \
  "s_waitcnt lgkmcnt(8)\n\t" \
  "v_mfma_f32_16x16x32_bf16 v[112:115], v[48:51], v[8:11], v[112:115]\n\t" \
  "v_mfma_f32_16x16x32_bf16 v[112:115], v[52:55], v[24:27], v[112:115]\n\t" \
  "s_waitcnt lgkmcnt(4)\n\t" \
  "v_mfma_f32_16x16x32_bf16 v[120:123], v[48:51], v[12:15], v[120:123]\n\t" \
  "v_mfma_f32_16x16x32_bf16 v[120:123], v[52:55], v[28:31], v[120:123]\n\t" \
  "ds_read_b64_tr_b16 v[48:49], v61 offset:14336\n\t" \
  "v_mfma_f32_16x16x32_bf16 v[128:131], v[0:3], v[32:35], v[128:131]\n\t" \
  "ds_read_b64_tr_b16 v[50:51], v61 offset:14848\n\t" \
  "s_waitcnt lgkmcnt(4)\n\t" \
  "v_mfma_f32_16x16x32_bf16 v[128:131], v[16:19], v[36:39], v[128:131]\n\t" \
  "ds_read_b64_tr_b16 v[52:53], v61 offset:26624\n\t" \
  "v_mfma_f32_16x16x32_bf16 v[136:139], v[4:7], v[32:35], v[136:139]\n\t" \
  "v_mfma_f32_16x16x32_bf16 v[136:139], v[20:23], v[36:39], v[136:139]\n\t" \
  "ds_read_b64_tr_b16 v[54:55], v61 offset:27136\n\t" \
  "v_mfma_f32_16x16x32_bf16 v[144:147], v[8:11], v[32:35], v[144:147]\n\t" \
  "v_mfma_f32_16x16x32_bf16 v[144:147], v[24:27], v[36:39], v[144:147]\n\t" \
  "v_mfma_f32_16x16x32_bf16 v[152:155], v[12:15], v[32:35], v[152:155]\n\t" \
  "v_mfma_f32_16x16x32_bf16 v[152:155], v[28:31], v[36:39], v[152:155]\n\t" \
  "s_waitcnt lgkmcnt(4)\n\t" \
  "v_mfma_f32_16x16x32_bf16 v[160:163], v[40:43], v[32:35], v[160:163]\n\t" \
  "v_mfma_f32_16x16x32_bf16 v[160:163], v[44:47], v[36:39], v[160:163]\n\t" \
  "ds_read_b64_tr_b16 v[32:33], v61 offset:13312\n\t" \
  "s_waitcnt lgkmcnt(1)\n\t" \
  "v_mfma_f32_16x16x32_bf16 v[164:167], v[40:43], v[48:51], v[164:167]\n\t" \
  "ds_read_b64_tr_b16 v[34:35], v61 offset:13824\n\t" \
  "v_mfma_f32_16x16x32_bf16 v[164:167], v[44:47], v[52:55], v[164:167]\n\t" \
  "ds_read_b64_tr_b16 v[36:37], v61 offset:25600\n\t" \
  "v_mfma_f32_16x16x32_bf16 v[132:135], v[0:3], v[48:51], v[132:135]\n\t" \
  "ds_read_b64_tr_b16 v[38:39], v61 offset:26112\n\t" \
  "v_mfma_f32_16x16x32_bf16 v[132:135], v[16:19], v[52:55], v[132:135]\n\t" \
  "ds_read_b64_tr_b16 v[40:41], v60 offset:12288\n\t" \
  "v_mfma_f32_16x16x32_bf16 v[140:143], v[4:7], v[48:51], v[140:143]\n\t" \
  "ds_read_b64_tr_b16 v[42:43], v60 offset:12800\n\t" \
  "v_mfma_f32_16x16x32_bf16 v[140:143], v[20:23], v[52:55], v[140:143]\n\t" \
  "ds_read_b64_tr_b16 v[44:45], v60 offset:24576\n\t" \
  "v_mfma_f32_16x16x32_bf16 v[148:151], v[8:11], v[48:51], v[148:151]\n\t" \
  "ds_read_b64_tr_b16 v[46:47], v60 offset:25088\n\t" \
  "v_mfma_f32_16x16x32_bf16 v[148:151], v[24:27], v[52:55], v[148:151]\n\t" \
  "v_mfma_f32_16x16x32_bf16 v[156:159], v[12:15], v[48:51], v[156:159]\n\t" \
  "ds_read_b64_tr_b16 v[48:49], v61 offset:12288\n\t" \
  "v_mfma_f32_16x16x32_bf16 v[156:159], v[28:31], v[52:55], v[156:159]\n\t" \
  "ds_read_b64_tr_b16 v[50:51], v61 offset:12800\n\t" \
  "s_waitcnt lgkmcnt(6)\n\t" \
  "v_mfma_f32_16x16x32_bf16 v[100:103], v[32:35], v[0:3], v[100:103]\n\t" \
  "ds_read_b64_tr_b16 v[52:53], v61 offset:24576\n\t" \
  "v_mfma_f32_16x16x32_bf16 v[100:103], v[36:39], v[16:19], v[100:103]\n\t" \
  "ds_read_b64_tr_b16 v[54:55], v61 offset:25088\n\t" \
  "v_mfma_f32_16x16x32_bf16 v[108:111], v[32:35], v[4:7], v[108:111]\n\t" \
  "v_mfma_f32_16x16x32_bf16 v[108:111], v[36:39], v[20:23], v[108:111]\n\t" \
  "s_addk_i32 %[it], 1\n\t" \
  "v_mfma_f32_16x16x32_bf16 v[116:119], v[32:35], v[8:11], v[116:119]\n\t" \
  "s_cmp_lt_u32 %[it], 6\n\t" \
  "v_mfma_f32_16x16x32_bf16 v[116:119], v[36:39], v[24:27], v[116:119]\n\t" \
  "v_mfma_f32_16x16x32_bf16 v[124:127], v[32:35], v[12:15], v[124:127]\n\t" \
  "v_mfma_f32_16x16x32_bf16 v[124:127], v[36:39], v[28:31], v[124:127]\n\t" \
  "s_waitcnt lgkmcnt(4)\n\t" \
  "v_mfma_f32_16x16x32_bf16 v[64:67], v[40:43], v[0:3], v[64:67]\n\t" \
  "s_waitcnt lgkmcnt(0)\n\t" \
  "s_barrier\n\t" \
  "v_mfma_f32_16x16x32_bf16 v[64:67], v[44:47], v[16:19], v[64:67]\n\t" \
  "ds_read_b64_tr_b16 v[32:33], v58 offset:1024\n\t" \
  "v_mfma_f32_16x16x32_bf16 v[68:71], v[48:51], v[0:3], v[68:71]\n\t" \
  "ds_read_b128 v[0:3], v56 offset:10240\n\t" \
  "v_mfma_f32_16x16x32_bf16 v[68:71], v[52:55], v[16:19], v[68:71]\n\t" \
  "ds_read_b128 v[16:19], v57 offset:10240\n\t" \
  "v_mfma_f32_16x16x32_bf16 v[72:75], v[40:43], v[4:7], v[72:75]\n\t" \
  "ds_read_b64_tr_b16 v[34:35], v58 offset:1536\n\t" \
  "v_mfma_f32_16x16x32_bf16 v[72:75], v[44:47], v[20:23], v[72:75]\n\t" \
  "ds_read_b64_tr_b16 v[36:37], v58 offset:13312\n\t" \
  "v_mfma_f32_16x16x32_bf16 v[76:79], v[48:51], v[4:7], v[76:79]\n\t" \
  "ds_read_b128 v[4:7], v56 offset:12288\n\t" \
  "v_mfma_f32_16x16x32_bf16 v[76:79], v[52:55], v[20:23], v[76:79]\n\t" \
  "ds_read_b128 v[20:23], v57 offset:12288\n\t" \
  "v_mfma_f32_16x16x32_bf16 v[80:83], v[40:43], v[8:11], v[80:83]\n\t" \
  "ds_read_b64_tr_b16 v[38:39], v58 offset:13824\n\t" \
  "v_mfma_f32_16x16x32_bf16 v[80:83], v[44:47], v[24:27], v[80:83]\n\t" \
  "v_mfma_f32_16x16x32_bf16 v[84:87], v[48:51], v[8:11], v[84:87]\n\t" \
  "ds_read_b128 v[8:11], v56 offset:14336\n\t" \
  "v_mfma_f32_16x16x32_bf16 v[84:87], v[52:55], v[24:27], v[84:87]\n\t" \
  "ds_read_b128 v[24:27], v57 offset:14336\n\t" \
  "v_mfma_f32_16x16x32_bf16 v[88:91], v[40:43], v[12:15], v[88:91]\n\t" \
  "ds_read_b64_tr_b16 v[40:41], v58 offset:2048\n\t" \
  "v_mfma_f32_16x16x32_bf16 v[88:91], v[44:47], v[28:31], v[88:91]\n\t" \
  "ds_read_b64_tr_b16 v[42:43], v58 offset:2560\n\t" \
  "v_mfma_f32_16x16x32_bf16 v[92:95], v[48:51], v[12:15], v[92:95]\n\t" \
  "ds_read_b128 v[12:15], v56 offset:16384\n\t" \
  "v_mfma_f32_16x16x32_bf16 v[92:95], v[52:55], v[28:31], v[92:95]\n\t" \
  "ds_read_b128 v[28:31], v57 offset:16384\n\t" \
  "s_cbranch_scc1 N0b_%=\n\t" \
  "s_setprio 3\n\t" \
  "s_waitcnt lgkmcnt(0)\n\t" \
  "v_cvt_pk_bf16_f32 v160, v160, v161\n\t" \
  "v_cvt_pk_bf16_f32 v161, v162, v163\n\t" \
  "v_cvt_pk_bf16_f32 v162, v164, v165\n\t" \
  "v_cvt_pk_bf16_f32 v163, v166, v167\n\t" \
  "ds_write_b128 v63, v[160:163] offset:2048\n\t" \
  "v_mov_b32 v57, %[fa]\n\t" \
  "v_mov_b32 v58, 1\n\t" \
  "s_mov_b64 exec, 1\n\t" \
  "ds_write_b32 v57, v58\n\t" \
  "s_mov_b64 exec, -1\n\t" \
  "v_cvt_pk_bf16_f32 v120, v120, v121\n\t" \
  "v_cvt_pk_bf16_f32 v121, v122, v123\n\t" \
  "v_cvt_pk_bf16_f32 v122, v124, v125\n\t" \
  "v_cvt_pk_bf16_f32 v123, v126, v127\n\t" \
  "v_cvt_pk_bf16_f32 v28, v152, v153\n\t" \
  "v_cvt_pk_bf16_f32 v29, v154, v155\n\t" \
  "v_cvt_pk_bf16_f32 v38, v156, v157\n\t" \
  "v_cvt_pk_bf16_f32 v39, v158, v159\n\t" \
  "NX_%=:\n\t" \
  "v_cvt_pk_bf16_f32 v96, v96, v97\n\t" \
  "v_cvt_pk_bf16_f32 v97, v98, v99\n\t" \
  "v_cvt_pk_bf16_f32 v98, v100, v101\n\t" \
  "v_cvt_pk_bf16_f32 v99, v102, v103\n\t" \
  "v_cvt_pk_bf16_f32 v104, v104, v105\n\t" \
  "v_cvt_pk_bf16_f32 v105, v106, v107\n\t" \
  "v_cvt_pk_bf16_f32 v106, v108, v109\n\t" \
  "v_cvt_pk_bf16_f32 v107, v110, v111\n\t" \
  "v_mov_b32 v60, 0xff61b1e6\n\t" \
  "v_cndmask_b32_e64 v56, v60, 0, s[68:69]\n\t" \
  "v_cndmask_b32_e64 v57, v60, 0, s[70:71]\n\t" \
  "v_cndmask_b32_e64 v58, v60, 0, s[72:73]\n\t" \
  "v_cndmask_b32_e64 v59, v60, 0, s[74:75]\n\t" \
  "v_cvt_pk_bf16_f32 v72, v72, v73\n\t" \
  "v_cvt_pk_bf16_f32 v73, v74, v75\n\t" \
  "v_cvt_pk_bf16_f32 v74, v76, v77\n\t" \
  "v_cvt_pk_bf16_f32 v75, v78, v79\n\t" \
  "v_cndmask_b32_e64 v52, v60, 0, s[76:77]\n\t" \
  "v_cndmask_b32_e64 v53, v60, 0, s[78:79]\n\t" \
  "v_cndmask_b32_e64 v54, v60, 0, s[80:81]\n\t" \
  "v_cndmask_b32_e64 v55, v60, 0, s[82:83]\n\t" \
  "v_cvt_pk_bf16_f32 v24, v136, v137\n\t" \
  "v_cvt_pk_bf16_f32 v25, v138, v139\n\t" \
  "v_cvt_pk_bf16_f32 v34, v140, v141\n\t" \
  "v_cvt_pk_bf16_f32 v35, v142, v143\n\t" \
  "v_cvt_pk_bf16_f32 v26, v144, v145\n\t" \
  "v_cvt_pk_bf16_f32 v27, v146, v147\n\t" \
  "v_cvt_pk_bf16_f32 v36, v148, v149\n\t" \
  "v_cvt_pk_bf16_f32 v37, v150, v151\n\t" \
  "v_mfma_f32_16x16x32_bf16 v[136:139], v[96:99], v[72:75], v[56:59]\n\t" \
  "v_cvt_pk_bf16_f32 v112, v112, v113\n\t" \
  "v_cvt_pk_bf16_f32 v113, v114, v115\n\t" \
  "v_mfma_f32_16x16x32_bf16 v[140:143], v[104:107], v[72:75], v[52:55]\n\t" \
  "v_cvt_pk_bf16_f32 v114, v116, v117\n\t" \
  "v_cvt_pk_bf16_f32 v115, v118, v119\n\t" \
  "v_cvt_pk_bf16_f32 v80, v80, v81\n\t" \
  "v_cvt_pk_bf16_f32 v81, v82, v83\n\t" \
  "v_cvt_pk_bf16_f32 v82, v84, v85\n\t" \
  "v_cvt_pk_bf16_f32 v83, v86, v87\n\t" \
  "v_cvt_pk_bf16_f32 v22, v128, v129\n\t" \
  "v_cvt_pk_bf16_f32 v23, v130, v131\n\t" \
  "v_mfma_f32_16x16x32_bf16 v[144:147], v[104:107], v[80:83], v[56:59]\n\t" \
  "v_cvt_pk_bf16_f32 v32, v132, v133\n\t" \
  "v_cvt_pk_bf16_f32 v33, v134, v135\n\t" \
  "v_mfma_f32_16x16x32_bf16 v[148:151], v[112:115], v[80:83], v[52:55]\n\t" \
  "v_cvt_pk_bf16_f32 v88, v88, v89\n\t" \
  "v_cvt_pk_bf16_f32 v89, v90, v91\n\t" \
  "v_cvt_pk_bf16_f32 v90, v92, v93\n\t" \
  "v_cvt_pk_bf16_f32 v91, v94, v95\n\t" \
  "v_mov_b32 v61, %[fp]\n\t" \
  "v_add_u32 v60, %[vo], v63\n\t" \
  "v_mfma_f32_16x16x32_bf16 v[152:155], v[112:115], v[88:91], v[56:59]\n\t" \
  "ds_read_b32 v0, v61\n\t" \
  "ds_read_b64 v[20:21], v60\n\t" \
  "ds_read_b64 v[30:31], v60 offset:8\n\t" \
  "s_mov_b64 exec, %[rm]\n\t" \
  "ds_read_b128 v[160:163], v63\n\t" \
  "s_mov_b64 exec, -1\n\t" \
  "v_mfma_f32_16x16x32_bf16 v[156:159], v[120:123], v[88:91], v[52:55]\n\t" \
  "v_cvt_pk_bf16_f32 v64, v64, v65\n\t" \
  "v_cvt_pk_bf16_f32 v65, v66, v67\n\t" \
  "v_cvt_pk_bf16_f32 v66, v68, v69\n\t" \
  "v_cvt_pk_bf16_f32 v67, v70, v71\n\t" \
  "v_max_f32 v136, v136, v137\n\t" \
  "v_max_f32 v138, v138, v139\n\t" \
  "v_mfma_f32_16x16x32_bf16 v[132:135], v[96:99], v[64:67], v[52:55]\n\t" \
  "v_max_f32 v140, v140, v141\n\t" \
  "v_max_f32 v142, v142, v143\n\t" \
  "AW_%=:\n\t" \
  "s_waitcnt lgkmcnt(0)\n\t" \
  "v_cmp_eq_u32 vcc, 0, v0\n\t" \
  "v_max3_f32 v5, v136, v138, v140\n\t" \
  "v_max_f32 v5, v5, v142\n\t" \
  "s_cbranch_vccnz AR_%=\n\t" \
  "s_cmp_eq_u32 %[z], 0\n\t" \
  "s_cbranch_scc1 AZ_%=\n\t" \
  "AY_%=:\n\t" \
  "v_mfma_f32_16x16x32_bf16 v[128:131], v[160:163], v[64:67], v[56:59]\n\t" \
  "v_max_f32 v144, v144, v145\n\t" \
  "v_max_f32 v146, v146, v147\n\t" \
  "v_max_f32 v148, v148, v149\n\t" \
  "v_max_f32 v150, v150, v151\n\t" \
  "v_max3_f32 v6, v144, v146, v148\n\t" \
  "v_max_f32 v6, v6, v150\n\t" \
  "v_max_f32 v152, v152, v153\n\t" \
  "v_max_f32 v154, v154, v155\n\t" \
  "v_max_f32 v156, v156, v157\n\t" \
  "v_max_f32 v158, v158, v159\n\t" \
  "v_max3_f32 v7, v152, v154, v156\n\t" \
  "v_max_f32 v7, v7, v158\n\t" \
  "v_mov_b32 v8, 0xc0c0100\n\t" \
  "v_mov_b32 v9, 0x1000c0c\n\t" \
  "v_max_f32 v128, v128, v129\n\t" \
  "v_max_f32 v130, v130, v131\n\t" \
  "v_max_f32 v132, v132, v133\n\t" \
  "v_max_f32 v134, v134, v135\n\t" \
  "v_max3_f32 v4, v128, v130, v132\n\t" \
  "v_max_f32 v4, v4, v134\n\t" \
  "v_cndmask_b32_e64 v60, v8, v9, s[84:85]\n\t" \
  "v_mov_b32 v8, 0xc0c0302\n\t" \
  "v_permlane16_swap_b32 v6, v7\n\t" \
  "v_permlane16_swap_b32 v4, v5\n\t" \
  "v_max_f32 v6, v6, v7\n\t" \
  "v_max_f32 v4, v4, v5\n\t" \
  "v_mov_b32 v9, 0x3020c0c\n\t" \
  "v_cndmask_b32_e64 v61, v8, v9, s[84:85]\n\t" \
  "v_permlane32_swap_b32 v4, v6\n\t" \
  "v_max_f32 v4, v4, v6\n\t" \
  "v_mul_f32 v6, 0xbe8293ee, v4\n\t" \
  "v_mul_f32 v4, 0xbe8293ee, v4\n\t" \
  "s_nop 1\n\t" \
  "v_permlane32_swap_b32 v4, v6\n\t" \
  "v_mov_b32 v5, v4\n\t" \
  "v_mov_b32 v7, v6\n\t" \
  "s_nop 0\n\t" \
  "v_permlane16_swap_b32 v4, v5\n\t" \
  "v_permlane16_swap_b32 v6, v7\n\t" \
  "v_fmamk_f32 v128, v128, 0x3e8293ee, v4\n\t" \
  "v_fmamk_f32 v130, v130, 0x3e8293ee, v4\n\t" \
  "v_fmamk_f32 v132, v132, 0x3e8293ee, v4\n\t" \
  "v_fmamk_f32 v134, v134, 0x3e8293ee, v4\n\t" \
  "v_fmamk_f32 v136, v136, 0x3e8293ee, v5\n\t" \
  "v_fmamk_f32 v138, v138, 0x3e8293ee, v5\n\t" \
  "v_fmamk_f32 v140, v140, 0x3e8293ee, v5\n\t" \
  "v_fmamk_f32 v142, v142, 0x3e8293ee, v5\n\t" \
  "v_fmamk_f32 v144, v144, 0x3e8293ee, v6\n\t" \
  "v_fmamk_f32 v146, v146, 0x3e8293ee, v6\n\t" \
  "v_fmamk_f32 v148, v148, 0x3e8293ee, v6\n\t" \
  "v_fmamk_f32 v150, v150, 0x3e8293ee, v6\n\t" \
  "v_fmamk_f32 v152, v152, 0x3e8293ee, v7\n\t" \
  "v_fmamk_f32 v154, v154, 0x3e8293ee, v7\n\t" \
  "v_fmamk_f32 v156, v156, 0x3e8293ee, v7\n\t" \
  "v_fmamk_f32 v158, v158, 0x3e8293ee, v7\n\t" \
  "v_exp_f32 v128, v128\n\t" \
  "v_exp_f32 v130, v130\n\t" \
  "v_exp_f32 v132, v132\n\t" \
  "v_exp_f32 v134, v134\n\t" \
  "v_exp_f32 v136, v136\n\t" \
  "v_exp_f32 v138, v138\n\t" \
  "v_exp_f32 v140, v140\n\t" \
  "v_exp_f32 v142, v142\n\t" \
  "v_exp_f32 v144, v144\n\t" \
  "v_exp_f32 v146, v146\n\t" \
  "v_exp_f32 v148, v148\n\t" \
  "v_exp_f32 v150, v150\n\t" \
  "v_exp_f32 v152, v152\n\t" \
  "v_exp_f32 v154, v154\n\t" \
  "v_exp_f32 v156, v156\n\t" \
  "v_exp_f32 v158, v158\n\t" \
  "v_add_f32 v4, v128, v130\n\t" \
  "v_add_f32 v8, v132, v134\n\t" \
  "v_add_f32 v5, v136, v138\n\t" \
  "v_add_f32 v9, v140, v142\n\t" \
  "v_add_f32 v6, v144, v146\n\t" \
  "v_add_f32 v10, v148, v150\n\t" \
  "v_add_f32 v7, v152, v154\n\t" \
  "v_add_f32 v11, v156, v158\n\t" \
  "v_add_f32 v4, v4, v8\n\t" \
  "v_add_f32 v5, v5, v9\n\t" \
  "v_add_f32 v6, v6, v10\n\t" \
  "v_add_f32 v7, v7, v11\n\t" \
  "v_permlane16_swap_b32 v4, v5\n\t" \
  "s_nop 0\n\t" \
  "v_permlane16_swap_b32 v6, v7\n\t" \
  "v_add_f32 v4, v4, v5\n\t" \
  "v_add_f32 v6, v6, v7\n\t" \
  "s_nop 1\n\t" \
  "v_permlane32_swap_b32 v4, v6\n\t" \
  "v_add_f32 v4, v4, v6\n\t" \
  "v_rcp_f32 v4, v4\n\t" \
  "s_nop 0\n\t" \
  "v_mov_b32 v6, v4\n\t" \
  "s_nop 1\n\t" \
  "v_permlane32_swap_b32 v4, v6\n\t" \
  "v_mov_b32 v5, v4\n\t" \
  "v_mov_b32 v7, v6\n\t" \
  "s_nop 0\n\t" \
  "v_permlane16_swap_b32 v4, v5\n\t" \
  "v_permlane16_swap_b32 v6, v7\n\t" \
  "v_mul_f32 v128, v128, v4\n\t" \
  "v_mul_f32 v130, v130, v4\n\t" \
  "v_mul_f32 v132, v132, v4\n\t" \
  "v_mul_f32 v134, v134, v4\n\t" \
  "v_cvt_pk_bf16_f32 v128, v128, v130\n\t" \
  "v_cvt_pk_bf16_f32 v132, v132, v134\n\t" \
  "v_perm_b32 v68, 0, v128, v60\n\t" \
  "v_perm_b32 v69, 0, v128, v61\n\t" \
  "v_perm_b32 v70, 0, v132, v60\n\t" \
  "v_perm_b32 v71, 0, v132, v61\n\t" \
  "v_mul_f32 v136, v136, v5\n\t" \
  "v_mul_f32 v138, v138, v5\n\t" \
  "v_mfma_f32_16x16x32_bf16 v[128:131], v[20:23], v[68:71], 0\n\t" \
  "v_mul_f32 v140, v140, v5\n\t" \
  "v_mul_f32 v142, v142, v5\n\t" \
  "v_cvt_pk_bf16_f32 v136, v136, v138\n\t" \
  "v_mfma_f32_16x16x32_bf16 v[132:135], v[30:33], v[68:71], 0\n\t" \
  "v_cvt_pk_bf16_f32 v140, v140, v142\n\t" \
  "v_perm_b32 v76, 0, v136, v60\n\t" \
  "v_perm_b32 v77, 0, v136, v61\n\t" \
  "v_perm_b32 v78, 0, v140, v60\n\t" \
  "v_perm_b32 v79, 0, v140, v61\n\t" \
  "v_mul_f32 v144, v144, v6\n\t" \
  "v_mul_f32 v146, v146, v6\n\t" \
  "v_mfma_f32_16x16x32_bf16 v[136:139], v[22:25], v[76:79], 0\n\t" \
  "v_mul_f32 v148, v148, v6\n\t" \
  "v_mul_f32 v150, v150, v6\n\t" \
  "v_cvt_pk_bf16_f32 v144, v144, v146\n\t" \
  "v_mfma_f32_16x16x32_bf16 v[140:143], v[32:35], v[76:79], 0\n\t" \
  "v_cvt_pk_bf16_f32 v148, v148, v150\n\t" \
  "v_perm_b32 v84, 0, v144, v60\n\t" \
  "v_perm_b32 v85, 0, v144, v61\n\t" \
  "v_perm_b32 v86, 0, v148, v60\n\t" \
  "v_perm_b32 v87, 0, v148, v61\n\t" \
  "v_cvt_pk_bf16_f32 v100, v128, v129\n\t" \
  "v_cvt_pk_bf16_f32 v101, v130, v131\n\t" \
  "v_mfma_f32_16x16x32_bf16 v[144:147], v[24:27], v[84:87], 0\n\t" \
  "v_cvt_pk_bf16_f32 v102, v132, v133\n\t" \
  "v_cvt_pk_bf16_f32 v103, v134, v135\n\t" \
  "global_store_dwordx4 v62, v[100:103], %[ot] offset:0 sc1\n\t" \
  "v_mfma_f32_16x16x32_bf16 v[148:151], v[34:37], v[84:87], 0\n\t" \
  "v_mul_f32 v152, v152, v7\n\t" \
  "v_mul_f32 v154, v154, v7\n\t" \
  "v_mul_f32 v156, v156, v7\n\t" \
  "v_mul_f32 v158, v158, v7\n\t" \
  "v_cvt_pk_bf16_f32 v152, v152, v154\n\t" \
  "v_cvt_pk_bf16_f32 v156, v156, v158\n\t" \
  "v_perm_b32 v92, 0, v152, v60\n\t" \
  "v_perm_b32 v93, 0, v152, v61\n\t" \
  "v_perm_b32 v94, 0, v156, v60\n\t" \
  "v_perm_b32 v95, 0, v156, v61\n\t" \
  "v_cvt_pk_bf16_f32 v108, v136, v137\n\t" \
  "v_cvt_pk_bf16_f32 v109, v138, v139\n\t" \
  "v_mfma_f32_16x16x32_bf16 v[152:155], v[26:29], v[92:95], 0\n\t" \
  "v_cvt_pk_bf16_f32 v110, v140, v141\n\t" \
  "v_cvt_pk_bf16_f32 v111, v142, v143\n\t" \
  "global_store_dwordx4 v62, v[108:111], %[ot] offset:1024 sc1\n\t" \
  "v_mfma_f32_16x16x32_bf16 v[156:159], v[36:39], v[92:95], 0\n\t" \
  "v_cvt_pk_bf16_f32 v116, v144, v145\n\t" \
  "v_cvt_pk_bf16_f32 v117, v146, v147\n\t" \
  "v_cvt_pk_bf16_f32 v118, v148, v149\n\t" \
  "v_cvt_pk_bf16_f32 v119, v150, v151\n\t" \
  "global_store_dwordx4 v62, v[116:119], %[ou] offset:0 sc1\n\t" \
  "v_cvt_pk_bf16_f32 v124, v152, v153\n\t" \
  "v_cvt_pk_bf16_f32 v125, v154, v155\n\t" \
  "s_nop 0\n\t" \
  "v_cvt_pk_bf16_f32 v126, v156, v157\n\t" \
  "v_cvt_pk_bf16_f32 v127, v158, v159\n\t" \
  "global_store_dwordx4 v62, v[124:127], %[ou] offset:1024 sc1\n\t" \
  "s_branch AE_%=\n\t" \
  "AR_%=:\n\t" \
  "s_addk_i32 %[it], 1\n\t" \
  "s_cmp_gt_u32 %[it], 0x400000\n\t" \
  "s_cbranch_scc1 AY_%=\n\t" \
  "ds_read_b32 v0, v61\n\t" \
  "ds_read_b64 v[20:21], v60\n\t" \
  "ds_read_b64 v[30:31], v60 offset:8\n\t" \
  "s_mov_b64 exec, %[rm]\n\t" \
  "ds_read_b128 v[160:163], v63\n\t" \
  "s_mov_b64 exec, -1\n\t" \
  "s_branch AW_%=\n\t" \
  "AZ_%=:\n\t" \
  "v_mov_b32 v0, 0xff61b1e6\n\t" \
  "v_mov_b32 v1, 0xcfa8965f\n\t" \
  "s_nop 3\n\t" \
  "v_cndmask_b32_e64 v56, v0, v1, s[68:69]\n\t" \
  "v_cndmask_b32_e64 v57, v0, v1, s[70:71]\n\t" \
  "v_cndmask_b32_e64 v58, v0, v1, s[72:73]\n\t" \
  "v_cndmask_b32_e64 v59, v0, v1, s[74:75]\n\t" \
  "s_nop 1\n\t" \
  "s_branch AY_%=\n\t" \
  "AE_%=:\n\t" \
  "s_nop 1\n\t"
#define N2_CLOB "v0", "v1", "v2", "v3", "v4", "v5", "v6", "v7", "v8", "v9", "v10", "v11", "v12", "v13", "v14", "v15", "v16", "v17", "v18", "v19", "v20", "v21", "v22", "v23", "v24", "v25", "v26", "v27", "v28", "v29", "v30", "v31", "v32", "v33", "v34", "v35", "v36", "v37", "v38", "v39", "v40", "v41", "v42", "v43", "v44", "v45", "v46", "v47", "v48", "v49", "v50", "v51", "v52", "v53", "v54", "v55", "v60", "v61", "v64", "v65", "v66", "v67", "v68", "v69", "v70", "v71", "v72", "v73", "v74", "v75", "v76", "v77", "v78", "v79", "v80", "v81", "v82", "v83", "v84", "v85", "v86", "v87", "v88", "v89", "v90", "v91", "v92", "v93", "v94", "v95", "v96", "v97", "v98", "v99", "v100", "v101", "v102", "v103", "v104", "v105", "v106", "v107", "v108", "v109", "v110", "v111", "v112", "v113", "v114", "v115", "v116", "v117", "v118", "v119", "v120", "v121", "v122", "v123", "v124", "v125", "v126", "v127", "v128", "v129", "v130", "v131", "v132", "v133", "v134", "v135", "v136", "v137", "v138", "v139", "v140", "v141", "v142", "v143", "v144", "v145", "v146", "v147", "v148", "v149", "v150", "v151", "v152", "v153", "v154", "v155", "v156", "v157", "v158", "v159", "v160", "v161", "v162", "v163", "v164", "v165", "v166", "v167", "s68", "s69", "s70", "s71", "s72", "s73", "s74", "s75", "s76", "s77", "s78", "s79", "s80", "s81", "s82", "s83", "s84", "s85"

#define CV_PRE 0
#define CV_Y 1
#define CV_ASM \
  "s_mov_b64 vcc, 1\n\t" \
  "ds_read_b64 v[0:1], v28 offset:0\n\t" \
  "ds_read_b64 v[2:3], v29 offset:0\n\t" \
  "ds_read_b64 v[4:5], v30 offset:1024\n\t" \
  "ds_read_b64 v[6:7], v31 offset:1024\n\t" \
  "ds_read_b64 v[8:9], v24 offset:2048\n\t" \
  "ds_read_b64 v[10:11], v25 offset:2048\n\t" \
  "ds_read_b64 v[12:13], v26 offset:3072\n\t" \
  "ds_read_b64 v[14:15], v27 offset:3072\n\t" \
  "ds_read_b64 v[16:17], v28 offset:4096\n\t" \
  "ds_read_b64 v[18:19], v29 offset:4096\n\t" \
  "ds_read_b64 v[20:21], v30 offset:5120\n\t" \
  "ds_read_b64 v[22:23], v31 offset:5120\n\t" \
  "s_waitcnt lgkmcnt(0)\n\t" \
  "v_mfma_f32_16x16x32_bf16 v[128:131], v[68:71], v[0:3], 0\n\t" \
  "v_mfma_f32_16x16x32_bf16 v[128:131], v[64:67], v[4:7], v[128:131]\n\t" \
  "v_mfma_f32_16x16x32_bf16 v[132:135], v[76:79], v[0:3], 0\n\t" \
  "v_mfma_f32_16x16x32_bf16 v[132:135], v[72:75], v[4:7], v[132:135]\n\t" \
  "v_mfma_f32_16x16x32_bf16 v[136:139], v[84:87], v[0:3], 0\n\t" \
  "v_mfma_f32_16x16x32_bf16 v[136:139], v[80:83], v[4:7], v[136:139]\n\t" \
  "v_mfma_f32_16x16x32_bf16 v[140:143], v[92:95], v[0:3], 0\n\t" \
  "v_mfma_f32_16x16x32_bf16 v[140:143], v[88:91], v[4:7], v[140:143]\n\t" \
  "v_mfma_f32_16x16x32_bf16 v[144:147], v[100:103], v[0:3], 0\n\t" \
  "v_mfma_f32_16x16x32_bf16 v[144:147], v[96:99], v[4:7], v[144:147]\n\t" \
  "ds_bpermute_b32 v34, v32, v131\n\t" \
  "v_mfma_f32_16x16x32_bf16 v[148:151], v[108:111], v[0:3], 0\n\t" \
  "v_mov_b32 v131, v130\n\t" \
  "v_mov_b32 v130, v129\n\t" \
  "v_mfma_f32_16x16x32_bf16 v[148:151], v[104:107], v[4:7], v[148:151]\n\t" \
  "v_mov_b32 v129, v128\n\t" \
  "v_mfma_f32_16x16x32_bf16 v[152:155], v[116:119], v[0:3], 0\n\t" \
  "v_mfma_f32_16x16x32_bf16 v[152:155], v[112:115], v[4:7], v[152:155]\n\t" \
  "v_mfma_f32_16x16x32_bf16 v[156:159], v[124:127], v[0:3], 0\n\t" \
  "ds_read_b64 v[0:1], v24 offset:6144\n\t" \
  "v_mfma_f32_16x16x32_bf16 v[156:159], v[120:123], v[4:7], v[156:159]\n\t" \
  "ds_read_b64 v[2:3], v25 offset:6144\n\t" \
  "v_mfma_f32_16x16x32_bf16 v[132:135], v[68:71], v[8:11], v[132:135]\n\t" \
  "ds_read_b64 v[4:5], v26 offset:7168\n\t" \
  "v_mfma_f32_16x16x32_bf16 v[132:135], v[64:67], v[12:15], v[132:135]\n\t" \
  "ds_read_b64 v[6:7], v27 offset:7168\n\t" \
  "v_mfma_f32_16x16x32_bf16 v[136:139], v[76:79], v[8:11], v[136:139]\n\t" \
  "v_mfma_f32_16x16x32_bf16 v[136:139], v[72:75], v[12:15], v[136:139]\n\t" \
  "v_mfma_f32_16x16x32_bf16 v[140:143], v[84:87], v[8:11], v[140:143]\n\t" \
  "s_waitcnt lgkmcnt(4)\n\t" \
  "v_cndmask_b32_e64 v128, v34, 0, vcc\n\t" \
  "v_mfma_f32_16x16x32_bf16 v[140:143], v[80:83], v[12:15], v[140:143]\n\t" \
  "v_mfma_f32_16x16x32_bf16 v[144:147], v[92:95], v[8:11], v[144:147]\n\t" \
  "ds_bpermute_b32 v35, v32, v135\n\t" \
  "v_mfma_f32_16x16x32_bf16 v[144:147], v[88:91], v[12:15], v[144:147]\n\t" \
  "v_mov_b32 v135, v134\n\t" \
  "v_mov_b32 v134, v133\n\t" \
  "v_mfma_f32_16x16x32_bf16 v[148:151], v[100:103], v[8:11], v[148:151]\n\t" \
  "v_mov_b32 v133, v132\n\t" \
  "v_mfma_f32_16x16x32_bf16 v[148:151], v[96:99], v[12:15], v[148:151]\n\t" \
  "v_mfma_f32_16x16x32_bf16 v[152:155], v[108:111], v[8:11], v[152:155]\n\t" \
  "v_mfma_f32_16x16x32_bf16 v[152:155], v[104:107], v[12:15], v[152:155]\n\t" \
  "v_mfma_f32_16x16x32_bf16 v[156:159], v[116:119], v[8:11], v[156:159]\n\t" \
  "ds_read_b64 v[8:9], v28 offset:8192\n\t" \
  "v_mfma_f32_16x16x32_bf16 v[156:159], v[112:115], v[12:15], v[156:159]\n\t" \
  "ds_read_b64 v[10:11], v29 offset:8192\n\t" \
  "v_mfma_f32_16x16x32_bf16 v[136:139], v[68:71], v[16:19], v[136:139]\n\t" \
  "ds_read_b64 v[12:13], v30 offset:9216\n\t" \
  "v_mfma_f32_16x16x32_bf16 v[136:139], v[64:67], v[20:23], v[136:139]\n\t" \
  "ds_read_b64 v[14:15], v31 offset:9216\n\t" \
  "v_mfma_f32_16x16x32_bf16 v[140:143], v[76:79], v[16:19], v[140:143]\n\t" \
  "v_mfma_f32_16x16x32_bf16 v[140:143], v[72:75], v[20:23], v[140:143]\n\t" \
  "s_waitcnt lgkmcnt(4)\n\t" \
  "v_cndmask_b32_e64 v132, v35, v34, vcc\n\t" \
  "v_mfma_f32_16x16x32_bf16 v[144:147], v[84:87], v[16:19], v[144:147]\n\t" \
  "v_mfma_f32_16x16x32_bf16 v[144:147], v[80:83], v[20:23], v[144:147]\n\t" \
  "v_mfma_f32_16x16x32_bf16 v[148:151], v[92:95], v[16:19], v[148:151]\n\t" \
  "ds_bpermute_b32 v36, v32, v139\n\t" \
  "v_mfma_f32_16x16x32_bf16 v[148:151], v[88:91], v[20:23], v[148:151]\n\t" \
  "v_mov_b32 v139, v138\n\t" \
  "v_mov_b32 v138, v137\n\t" \
  "v_mfma_f32_16x16x32_bf16 v[152:155], v[100:103], v[16:19], v[152:155]\n\t" \
  "v_mov_b32 v137, v136\n\t" \
  "v_mfma_f32_16x16x32_bf16 v[152:155], v[96:99], v[20:23], v[152:155]\n\t" \
  "v_mfma_f32_16x16x32_bf16 v[156:159], v[108:111], v[16:19], v[156:159]\n\t" \
  "ds_read_b64 v[16:17], v24 offset:10240\n\t" \
  "v_mfma_f32_16x16x32_bf16 v[156:159], v[104:107], v[20:23], v[156:159]\n\t" \
  "ds_read_b64 v[18:19], v25 offset:10240\n\t" \
  "v_mfma_f32_16x16x32_bf16 v[140:143], v[68:71], v[0:3], v[140:143]\n\t" \
  "ds_read_b64 v[20:21], v26 offset:11264\n\t" \
  "v_mfma_f32_16x16x32_bf16 v[140:143], v[64:67], v[4:7], v[140:143]\n\t" \
  "ds_read_b64 v[22:23], v27 offset:11264\n\t" \
  "v_mfma_f32_16x16x32_bf16 v[144:147], v[76:79], v[0:3], v[144:147]\n\t" \
  "v_mfma_f32_16x16x32_bf16 v[144:147], v[72:75], v[4:7], v[144:147]\n\t" \
  "v_mfma_f32_16x16x32_bf16 v[148:151], v[84:87], v[0:3], v[148:151]\n\t" \
  "v_mfma_f32_16x16x32_bf16 v[148:151], v[80:83], v[4:7], v[148:151]\n\t" \
  "s_waitcnt lgkmcnt(4)\n\t" \
  "v_cndmask_b32_e64 v136, v36, v35, vcc\n\t" \
  "v_mfma_f32_16x16x32_bf16 v[152:155], v[92:95], v[0:3], v[152:155]\n\t" \
  "ds_bpermute_b32 v37, v32, v143\n\t" \
  "v_mfma_f32_16x16x32_bf16 v[152:155], v[88:91], v[4:7], v[152:155]\n\t" \
  "v_mov_b32 v143, v142\n\t" \
  "v_mov_b32 v142, v141\n\t" \
  "v_mfma_f32_16x16x32_bf16 v[156:159], v[100:103], v[0:3], v[156:159]\n\t" \
  "ds_read_b64 v[0:1], v28 offset:12288\n\t" \
  "v_mov_b32 v141, v140\n\t" \
  "v_mfma_f32_16x16x32_bf16 v[156:159], v[96:99], v[4:7], v[156:159]\n\t" \
  "ds_read_b64 v[2:3], v29 offset:12288\n\t" \
  "v_mfma_f32_16x16x32_bf16 v[144:147], v[68:71], v[8:11], v[144:147]\n\t" \
  "ds_read_b64 v[4:5], v30 offset:13312\n\t" \
  "v_mfma_f32_16x16x32_bf16 v[144:147], v[64:67], v[12:15], v[144:147]\n\t" \
  "ds_read_b64 v[6:7], v31 offset:13312\n\t" \
  "v_mfma_f32_16x16x32_bf16 v[148:151], v[76:79], v[8:11], v[148:151]\n\t" \
  "v_mfma_f32_16x16x32_bf16 v[148:151], v[72:75], v[12:15], v[148:151]\n\t" \
  "v_mfma_f32_16x16x32_bf16 v[152:155], v[84:87], v[8:11], v[152:155]\n\t" \
  "v_mfma_f32_16x16x32_bf16 v[152:155], v[80:83], v[12:15], v[152:155]\n\t" \
  "v_mfma_f32_16x16x32_bf16 v[156:159], v[92:95], v[8:11], v[156:159]\n\t" \
  "ds_read_b64 v[8:9], v24 offset:14336\n\t" \
  "v_mfma_f32_16x16x32_bf16 v[156:159], v[88:91], v[12:15], v[156:159]\n\t" \
  "ds_read_b64 v[10:11], v25 offset:14336\n\t" \
  "s_waitcnt lgkmcnt(6)\n\t" \
  "v_mfma_f32_16x16x32_bf16 v[148:151], v[68:71], v[16:19], v[148:151]\n\t" \
  "ds_read_b64 v[12:13], v26 offset:15360\n\t" \
  "v_cndmask_b32_e64 v140, v37, v36, vcc\n\t" \
  "v_mfma_f32_16x16x32_bf16 v[148:151], v[64:67], v[20:23], v[148:151]\n\t" \
  "ds_read_b64 v[14:15], v27 offset:15360\n\t" \
  "v_mfma_f32_16x16x32_bf16 v[152:155], v[76:79], v[16:19], v[152:155]\n\t" \
  "ds_bpermute_b32 v38, v32, v147\n\t" \
  "v_mfma_f32_16x16x32_bf16 v[152:155], v[72:75], v[20:23], v[152:155]\n\t" \
  "v_mov_b32 v147, v146\n\t" \
  "v_mov_b32 v146, v145\n\t" \
  "v_mfma_f32_16x16x32_bf16 v[156:159], v[84:87], v[16:19], v[156:159]\n\t" \
  "ds_read_b64 v[16:17], v26 offset:0\n\t" \
  "ds_read_b64 v[18:19], v27 offset:0\n\t" \
  "v_mfma_f32_16x16x32_bf16 v[156:159], v[80:83], v[20:23], v[156:159]\n\t" \
  "ds_read_b64 v[20:21], v28 offset:1024\n\t" \
  "ds_read_b64 v[22:23], v29 offset:1024\n\t" \
  "s_waitcnt lgkmcnt(9)\n\t" \
  "v_mfma_f32_16x16x32_bf16 v[152:155], v[68:71], v[0:3], v[152:155]\n\t" \
  "ds_bpermute_b32 v39, v32, v151\n\t" \
  "v_mov_b32 v145, v144\n\t" \
  "v_mfma_f32_16x16x32_bf16 v[152:155], v[64:67], v[4:7], v[152:155]\n\t" \
  "v_mov_b32 v151, v150\n\t" \
  "v_mov_b32 v150, v149\n\t" \
  "v_mfma_f32_16x16x32_bf16 v[156:159], v[76:79], v[0:3], v[156:159]\n\t" \
  "ds_read_b64 v[0:1], v30 offset:2048\n\t" \
  "v_mov_b32 v149, v148\n\t" \
  "v_mfma_f32_16x16x32_bf16 v[156:159], v[72:75], v[4:7], v[156:159]\n\t" \
  "ds_read_b64 v[2:3], v31 offset:2048\n\t" \
  "s_waitcnt lgkmcnt(10)\n\t" \
  "v_mfma_f32_16x16x32_bf16 v[156:159], v[68:71], v[8:11], v[156:159]\n\t" \
  "ds_bpermute_b32 v40, v32, v155\n\t" \
  "s_waitcnt lgkmcnt(9)\n\t" \
  "v_mfma_f32_16x16x32_bf16 v[156:159], v[64:67], v[12:15], v[156:159]\n\t" \
  "ds_read_b64 v[4:5], v24 offset:3072\n\t" \
  "s_waitcnt lgkmcnt(7)\n\t" \
  "v_mfma_f32_16x16x32_bf16 v[128:131], v[68:71], v[16:19], v[128:131]\n\t" \
  "ds_read_b64 v[6:7], v25 offset:3072\n\t" \
  "s_waitcnt lgkmcnt(6)\n\t" \
  "v_mfma_f32_16x16x32_bf16 v[128:131], v[64:67], v[20:23], v[128:131]\n\t" \
  "v_cndmask_b32_e64 v144, v38, v37, vcc\n\t" \
  "v_mov_b32 v155, v154\n\t" \
  "v_mfma_f32_16x16x32_bf16 v[132:135], v[76:79], v[16:19], v[132:135]\n\t" \
  "ds_bpermute_b32 v41, v32, v159\n\t" \
  "v_mov_b32 v154, v153\n\t" \
  "v_mfma_f32_16x16x32_bf16 v[132:135], v[72:75], v[20:23], v[132:135]\n\t" \
  "global_load_dwordx4 v[44:47], v43, %[q0] offset:0\n\t" \
  "v_mfma_f32_16x16x32_bf16 v[136:139], v[84:87], v[16:19], v[136:139]\n\t" \
  "v_mov_b32 v153, v152\n\t" \
  "v_mov_b32 v159, v158\n\t" \
  "v_mfma_f32_16x16x32_bf16 v[136:139], v[80:83], v[20:23], v[136:139]\n\t" \
  "s_waitcnt lgkmcnt(6)\n\t" \
  "v_cndmask_b32_e64 v148, v39, v38, vcc\n\t" \
  "v_mfma_f32_16x16x32_bf16 v[140:143], v[92:95], v[16:19], v[140:143]\n\t" \
  "v_mov_b32 v158, v157\n\t" \
  "v_mov_b32 v157, v156\n\t" \
  "v_mfma_f32_16x16x32_bf16 v[140:143], v[88:91], v[20:23], v[140:143]\n\t" \
  "global_load_dwordx4 v[48:51], v43, %[q0] offset:2048\n\t" \
  "v_mfma_f32_16x16x32_bf16 v[144:147], v[100:103], v[16:19], v[144:147]\n\t" \
  "s_waitcnt lgkmcnt(3)\n\t" \
  "v_cndmask_b32_e64 v152, v40, v39, vcc\n\t" \
  "v_mfma_f32_16x16x32_bf16 v[144:147], v[96:99], v[20:23], v[144:147]\n\t" \
  "global_load_dwordx4 v[52:55], v43, %[q2] offset:0\n\t" \
  "v_mfma_f32_16x16x32_bf16 v[148:151], v[108:111], v[16:19], v[148:151]\n\t" \
  "s_waitcnt lgkmcnt(0)\n\t" \
  "v_cndmask_b32_e64 v156, v41, v40, vcc\n\t" \
  "v_mfma_f32_16x16x32_bf16 v[148:151], v[104:107], v[20:23], v[148:151]\n\t" \
  "ds_read_b64 v[8:9], v26 offset:4096\n\t" \
  "v_mfma_f32_16x16x32_bf16 v[152:155], v[116:119], v[16:19], v[152:155]\n\t" \
  "ds_read_b64 v[10:11], v27 offset:4096\n\t" \
  "v_mfma_f32_16x16x32_bf16 v[152:155], v[112:115], v[20:23], v[152:155]\n\t" \
  "global_load_dwordx4 v[56:59], v43, %[q2] offset:2048\n\t" \
  "v_mfma_f32_16x16x32_bf16 v[156:159], v[124:127], v[16:19], v[156:159]\n\t" \
  "ds_read_b64 v[12:13], v28 offset:5120\n\t" \
  "v_mfma_f32_16x16x32_bf16 v[156:159], v[120:123], v[20:23], v[156:159]\n\t" \
  "ds_read_b64 v[14:15], v29 offset:5120\n\t" \
  "v_mfma_f32_16x16x32_bf16 v[132:135], v[68:71], v[0:3], v[132:135]\n\t" \
  "ds_read_b64 v[16:17], v30 offset:6144\n\t" \
  "v_mfma_f32_16x16x32_bf16 v[132:135], v[64:67], v[4:7], v[132:135]\n\t" \
  "ds_read_b64 v[18:19], v31 offset:6144\n\t" \
  "v_mfma_f32_16x16x32_bf16 v[136:139], v[76:79], v[0:3], v[136:139]\n\t" \
  "ds_read_b64 v[20:21], v24 offset:7168\n\t" \
  "v_mfma_f32_16x16x32_bf16 v[136:139], v[72:75], v[4:7], v[136:139]\n\t" \
  "ds_read_b64 v[22:23], v25 offset:7168\n\t" \
  "v_mfma_f32_16x16x32_bf16 v[140:143], v[84:87], v[0:3], v[140:143]\n\t" \
  "ds_bpermute_b32 v34, v32, v131\n\t" \
  "v_mfma_f32_16x16x32_bf16 v[140:143], v[80:83], v[4:7], v[140:143]\n\t" \
  "ds_bpermute_b32 v35, v32, v135\n\t" \
  "v_mov_b32 v131, v130\n\t" \
  "v_mfma_f32_16x16x32_bf16 v[144:147], v[92:95], v[0:3], v[144:147]\n\t" \
  "v_mov_b32 v130, v129\n\t" \
  "v_mov_b32 v129, v128\n\t" \
  "v_mfma_f32_16x16x32_bf16 v[144:147], v[88:91], v[4:7], v[144:147]\n\t" \
  "v_mov_b32 v135, v134\n\t" \
  "v_mov_b32 v134, v133\n\t" \
  "v_mfma_f32_16x16x32_bf16 v[148:151], v[100:103], v[0:3], v[148:151]\n\t" \
  "v_mov_b32 v133, v132\n\t" \
  "v_mfma_f32_16x16x32_bf16 v[148:151], v[96:99], v[4:7], v[148:151]\n\t" \
  "v_mfma_f32_16x16x32_bf16 v[152:155], v[108:111], v[0:3], v[152:155]\n\t" \
  "v_mfma_f32_16x16x32_bf16 v[152:155], v[104:107], v[4:7], v[152:155]\n\t" \
  "v_mfma_f32_16x16x32_bf16 v[156:159], v[116:119], v[0:3], v[156:159]\n\t" \
  "ds_read_b64 v[0:1], v26 offset:8192\n\t" \
  "v_mfma_f32_16x16x32_bf16 v[156:159], v[112:115], v[4:7], v[156:159]\n\t" \
  "ds_read_b64 v[2:3], v27 offset:8192\n\t" \
  "s_waitcnt lgkmcnt(6)\n\t" \
  "v_mfma_f32_16x16x32_bf16 v[136:139], v[68:71], v[8:11], v[136:139]\n\t" \
  "ds_read_b64 v[4:5], v28 offset:9216\n\t" \
  "v_mfma_f32_16x16x32_bf16 v[136:139], v[64:67], v[12:15], v[136:139]\n\t" \
  "ds_read_b64 v[6:7], v29 offset:9216\n\t" \
  "v_mfma_f32_16x16x32_bf16 v[140:143], v[76:79], v[8:11], v[140:143]\n\t" \
  "s_waitcnt lgkmcnt(5)\n\t" \
  "v_cndmask_b32_e64 v128, v34, 0, vcc\n\t" \
  "v_mfma_f32_16x16x32_bf16 v[140:143], v[72:75], v[12:15], v[140:143]\n\t" \
  "s_waitcnt lgkmcnt(4)\n\t" \
  "v_cndmask_b32_e64 v132, v35, v34, vcc\n\t" \
  "v_mfma_f32_16x16x32_bf16 v[144:147], v[84:87], v[8:11], v[144:147]\n\t" \
  "ds_bpermute_b32 v36, v32, v139\n\t" \
  "v_mfma_f32_16x16x32_bf16 v[144:147], v[80:83], v[12:15], v[144:147]\n\t" \
  "v_mov_b32 v139, v138\n\t" \
  "v_mov_b32 v138, v137\n\t" \
  "v_mfma_f32_16x16x32_bf16 v[148:151], v[92:95], v[8:11], v[148:151]\n\t" \
  "v_mov_b32 v137, v136\n\t" \
  "v_mfma_f32_16x16x32_bf16 v[148:151], v[88:91], v[12:15], v[148:151]\n\t" \
  "v_mfma_f32_16x16x32_bf16 v[152:155], v[100:103], v[8:11], v[152:155]\n\t" \
  "v_mfma_f32_16x16x32_bf16 v[152:155], v[96:99], v[12:15], v[152:155]\n\t" \
  "v_mfma_f32_16x16x32_bf16 v[156:159], v[108:111], v[8:11], v[156:159]\n\t" \
  "ds_read_b64 v[8:9], v30 offset:10240\n\t" \
  "v_mfma_f32_16x16x32_bf16 v[156:159], v[104:107], v[12:15], v[156:159]\n\t" \
  "ds_read_b64 v[10:11], v31 offset:10240\n\t" \
  "v_mfma_f32_16x16x32_bf16 v[140:143], v[68:71], v[16:19], v[140:143]\n\t" \
  "ds_read_b64 v[12:13], v24 offset:11264\n\t" \
  "v_mfma_f32_16x16x32_bf16 v[140:143], v[64:67], v[20:23], v[140:143]\n\t" \
  "ds_read_b64 v[14:15], v25 offset:11264\n\t" \
  "v_mfma_f32_16x16x32_bf16 v[144:147], v[76:79], v[16:19], v[144:147]\n\t" \
  "v_mfma_f32_16x16x32_bf16 v[144:147], v[72:75], v[20:23], v[144:147]\n\t" \
  "s_waitcnt lgkmcnt(4)\n\t" \
  "v_cndmask_b32_e64 v136, v36, v35, vcc\n\t" \
  "v_mfma_f32_16x16x32_bf16 v[148:151], v[84:87], v[16:19], v[148:151]\n\t" \
  "v_mfma_f32_16x16x32_bf16 v[148:151], v[80:83], v[20:23], v[148:151]\n\t" \
  "v_mfma_f32_16x16x32_bf16 v[152:155], v[92:95], v[16:19], v[152:155]\n\t" \
  "ds_bpermute_b32 v37, v32, v143\n\t" \
  "v_mfma_f32_16x16x32_bf16 v[152:155], v[88:91], v[20:23], v[152:155]\n\t" \
  "v_mov_b32 v143, v142\n\t" \
  "v_mov_b32 v142, v141\n\t" \
  "v_mfma_f32_16x16x32_bf16 v[156:159], v[100:103], v[16:19], v[156:159]\n\t" \
  "ds_read_b64 v[16:17], v26 offset:12288\n\t" \
  "v_mov_b32 v141, v140\n\t" \
  "v_mfma_f32_16x16x32_bf16 v[156:159], v[96:99], v[20:23], v[156:159]\n\t" \
  "ds_read_b64 v[18:19], v27 offset:12288\n\t" \
  "v_mfma_f32_16x16x32_bf16 v[144:147], v[68:71], v[0:3], v[144:147]\n\t" \
  "ds_read_b64 v[20:21], v28 offset:13312\n\t" \
  "v_mfma_f32_16x16x32_bf16 v[144:147], v[64:67], v[4:7], v[144:147]\n\t" \
  "ds_read_b64 v[22:23], v29 offset:13312\n\t" \
  "v_mfma_f32_16x16x32_bf16 v[148:151], v[76:79], v[0:3], v[148:151]\n\t" \
  "v_mfma_f32_16x16x32_bf16 v[148:151], v[72:75], v[4:7], v[148:151]\n\t" \
  "v_mfma_f32_16x16x32_bf16 v[152:155], v[84:87], v[0:3], v[152:155]\n\t" \
  "v_mfma_f32_16x16x32_bf16 v[152:155], v[80:83], v[4:7], v[152:155]\n\t" \
  "v_mfma_f32_16x16x32_bf16 v[156:159], v[92:95], v[0:3], v[156:159]\n\t" \
  "ds_read_b64 v[0:1], v30 offset:14336\n\t" \
  "v_mfma_f32_16x16x32_bf16 v[156:159], v[88:91], v[4:7], v[156:159]\n\t" \
  "ds_read_b64 v[2:3], v31 offset:14336\n\t" \
  "s_waitcnt lgkmcnt(6)\n\t" \
  "v_mfma_f32_16x16x32_bf16 v[148:151], v[68:71], v[8:11], v[148:151]\n\t" \
  "ds_read_b64 v[4:5], v24 offset:15360\n\t" \
  "v_cndmask_b32_e64 v140, v37, v36, vcc\n\t" \
  "v_mfma_f32_16x16x32_bf16 v[148:151], v[64:67], v[12:15], v[148:151]\n\t" \
  "ds_read_b64 v[6:7], v25 offset:15360\n\t" \
  "v_mfma_f32_16x16x32_bf16 v[152:155], v[76:79], v[8:11], v[152:155]\n\t" \
  "ds_bpermute_b32 v38, v32, v147\n\t" \
  "v_mfma_f32_16x16x32_bf16 v[152:155], v[72:75], v[12:15], v[152:155]\n\t" \
  "v_mov_b32 v147, v146\n\t" \
  "v_mov_b32 v146, v145\n\t" \
  "v_mfma_f32_16x16x32_bf16 v[156:159], v[84:87], v[8:11], v[156:159]\n\t" \
  "ds_read_b64 v[8:9], v24 offset:0\n\t" \
  "ds_read_b64 v[10:11], v25 offset:0\n\t" \
  "v_mfma_f32_16x16x32_bf16 v[156:159], v[80:83], v[12:15], v[156:159]\n\t" \
  "ds_read_b64 v[12:13], v26 offset:1024\n\t" \
  "ds_read_b64 v[14:15], v27 offset:1024\n\t" \
  "s_waitcnt lgkmcnt(9)\n\t" \
  "v_mfma_f32_16x16x32_bf16 v[152:155], v[68:71], v[16:19], v[152:155]\n\t" \
  "ds_bpermute_b32 v39, v32, v151\n\t" \
  "v_mov_b32 v145, v144\n\t" \
  "v_mfma_f32_16x16x32_bf16 v[152:155], v[64:67], v[20:23], v[152:155]\n\t" \
  "v_mov_b32 v151, v150\n\t" \
  "v_mov_b32 v150, v149\n\t" \
  "v_mfma_f32_16x16x32_bf16 v[156:159], v[76:79], v[16:19], v[156:159]\n\t" \
  "ds_read_b64 v[16:17], v28 offset:2048\n\t" \
  "v_mov_b32 v149, v148\n\t" \
  "v_mfma_f32_16x16x32_bf16 v[156:159], v[72:75], v[20:23], v[156:159]\n\t" \
  "ds_read_b64 v[18:19], v29 offset:2048\n\t" \
  "s_waitcnt lgkmcnt(10)\n\t" \
  "v_mfma_f32_16x16x32_bf16 v[156:159], v[68:71], v[0:3], v[156:159]\n\t" \
  "ds_bpermute_b32 v40, v32, v155\n\t" \
  "s_waitcnt lgkmcnt(9)\n\t" \
  "v_mfma_f32_16x16x32_bf16 v[156:159], v[64:67], v[4:7], v[156:159]\n\t" \
  "ds_read_b64 v[20:21], v30 offset:3072\n\t" \
  "s_waitcnt lgkmcnt(7)\n\t" \
  "v_mfma_f32_16x16x32_bf16 v[128:131], v[68:71], v[8:11], v[128:131]\n\t" \
  "ds_read_b64 v[22:23], v31 offset:3072\n\t" \
  "s_waitcnt lgkmcnt(6)\n\t" \
  "v_mfma_f32_16x16x32_bf16 v[128:131], v[64:67], v[12:15], v[128:131]\n\t" \
  "v_cndmask_b32_e64 v144, v38, v37, vcc\n\t" \
  "v_mov_b32 v155, v154\n\t" \
  "v_mfma_f32_16x16x32_bf16 v[132:135], v[76:79], v[8:11], v[132:135]\n\t" \
  "ds_bpermute_b32 v41, v32, v159\n\t" \
  "v_mov_b32 v154, v153\n\t" \
  "v_mfma_f32_16x16x32_bf16 v[132:135], v[72:75], v[12:15], v[132:135]\n\t" \
  "v_mov_b32 v153, v152\n\t" \
  "v_mov_b32 v159, v158\n\t" \
  "v_mfma_f32_16x16x32_bf16 v[136:139], v[84:87], v[8:11], v[136:139]\n\t" \
  "v_mov_b32 v158, v157\n\t" \
  "v_mov_b32 v157, v156\n\t" \
  "v_mfma_f32_16x16x32_bf16 v[136:139], v[80:83], v[12:15], v[136:139]\n\t" \
  "s_waitcnt lgkmcnt(6)\n\t" \
  "v_cndmask_b32_e64 v148, v39, v38, vcc\n\t" \
  "v_mfma_f32_16x16x32_bf16 v[140:143], v[92:95], v[8:11], v[140:143]\n\t" \
  "ds_read_b64 v[0:1], v24 offset:4096\n\t" \
  "v_mfma_f32_16x16x32_bf16 v[140:143], v[88:91], v[12:15], v[140:143]\n\t" \
  "ds_read_b64 v[2:3], v25 offset:4096\n\t" \
  "v_mfma_f32_16x16x32_bf16 v[144:147], v[100:103], v[8:11], v[144:147]\n\t" \
  "s_waitcnt lgkmcnt(5)\n\t" \
  "v_cndmask_b32_e64 v152, v40, v39, vcc\n\t" \
  "v_mfma_f32_16x16x32_bf16 v[144:147], v[96:99], v[12:15], v[144:147]\n\t" \
  "ds_read_b64 v[4:5], v26 offset:5120\n\t" \
  "v_mfma_f32_16x16x32_bf16 v[148:151], v[108:111], v[8:11], v[148:151]\n\t" \
  "s_waitcnt lgkmcnt(3)\n\t" \
  "v_cndmask_b32_e64 v156, v41, v40, vcc\n\t" \
  "v_mfma_f32_16x16x32_bf16 v[148:151], v[104:107], v[12:15], v[148:151]\n\t" \
  "ds_read_b64 v[6:7], v27 offset:5120\n\t" \
  "v_mfma_f32_16x16x32_bf16 v[152:155], v[116:119], v[8:11], v[152:155]\n\t" \
  "ds_bpermute_b32 v34, v32, v131\n\t" \
  "v_mfma_f32_16x16x32_bf16 v[152:155], v[112:115], v[12:15], v[152:155]\n\t" \
  "v_mov_b32 v131, v130\n\t" \
  "v_mov_b32 v130, v129\n\t" \
  "v_mfma_f32_16x16x32_bf16 v[156:159], v[124:127], v[8:11], v[156:159]\n\t" \
  "ds_read_b64 v[8:9], v28 offset:6144\n\t" \
  "v_mov_b32 v129, v128\n\t" \
  "v_mfma_f32_16x16x32_bf16 v[156:159], v[120:123], v[12:15], v[156:159]\n\t" \
  "ds_read_b64 v[10:11], v29 offset:6144\n\t" \
  "v_mfma_f32_16x16x32_bf16 v[132:135], v[68:71], v[16:19], v[132:135]\n\t" \
  "ds_read_b64 v[12:13], v30 offset:7168\n\t" \
  "v_mfma_f32_16x16x32_bf16 v[132:135], v[64:67], v[20:23], v[132:135]\n\t" \
  "ds_read_b64 v[14:15], v31 offset:7168\n\t" \
  "v_mfma_f32_16x16x32_bf16 v[136:139], v[76:79], v[16:19], v[136:139]\n\t" \
  "v_mfma_f32_16x16x32_bf16 v[136:139], v[72:75], v[20:23], v[136:139]\n\t" \
  "v_mfma_f32_16x16x32_bf16 v[140:143], v[84:87], v[16:19], v[140:143]\n\t" \
  "v_mfma_f32_16x16x32_bf16 v[140:143], v[80:83], v[20:23], v[140:143]\n\t" \
  "v_mfma_f32_16x16x32_bf16 v[144:147], v[92:95], v[16:19], v[144:147]\n\t" \
  "v_mfma_f32_16x16x32_bf16 v[144:147], v[88:91], v[20:23], v[144:147]\n\t" \
  "s_waitcnt lgkmcnt(4)\n\t" \
  "v_cndmask_b32_e64 v128, v34, 0, vcc\n\t" \
  "v_mfma_f32_16x16x32_bf16 v[148:151], v[100:103], v[16:19], v[148:151]\n\t" \
  "ds_bpermute_b32 v35, v32, v135\n\t" \
  "v_mfma_f32_16x16x32_bf16 v[148:151], v[96:99], v[20:23], v[148:151]\n\t" \
  "v_mov_b32 v135, v134\n\t" \
  "v_mov_b32 v134, v133\n\t" \
  "v_mfma_f32_16x16x32_bf16 v[152:155], v[108:111], v[16:19], v[152:155]\n\t" \
  "v_mov_b32 v133, v132\n\t" \
  "v_mfma_f32_16x16x32_bf16 v[152:155], v[104:107], v[20:23], v[152:155]\n\t" \
  "v_mfma_f32_16x16x32_bf16 v[156:159], v[116:119], v[16:19], v[156:159]\n\t" \
  "ds_read_b64 v[16:17], v24 offset:8192\n\t" \
  "v_mfma_f32_16x16x32_bf16 v[156:159], v[112:115], v[20:23], v[156:159]\n\t" \
  "ds_read_b64 v[18:19], v25 offset:8192\n\t" \
  "v_mfma_f32_16x16x32_bf16 v[136:139], v[68:71], v[0:3], v[136:139]\n\t" \
  "ds_read_b64 v[20:21], v26 offset:9216\n\t" \
  "v_mfma_f32_16x16x32_bf16 v[136:139], v[64:67], v[4:7], v[136:139]\n\t" \
  "ds_read_b64 v[22:23], v27 offset:9216\n\t" \
  "v_mfma_f32_16x16x32_bf16 v[140:143], v[76:79], v[0:3], v[140:143]\n\t" \
  "v_mfma_f32_16x16x32_bf16 v[140:143], v[72:75], v[4:7], v[140:143]\n\t" \
  "v_mfma_f32_16x16x32_bf16 v[144:147], v[84:87], v[0:3], v[144:147]\n\t" \
  "v_mfma_f32_16x16x32_bf16 v[144:147], v[80:83], v[4:7], v[144:147]\n\t" \
  "s_waitcnt lgkmcnt(4)\n\t" \
  "v_cndmask_b32_e64 v132, v35, v34, vcc\n\t" \
  "v_mfma_f32_16x16x32_bf16 v[148:151], v[92:95], v[0:3], v[148:151]\n\t" \
  "ds_bpermute_b32 v36, v32, v139\n\t" \
  "v_mfma_f32_16x16x32_bf16 v[148:151], v[88:91], v[4:7], v[148:151]\n\t" \
  "v_mov_b32 v139, v138\n\t" \
  "v_mov_b32 v138, v137\n\t" \
  "v_mfma_f32_16x16x32_bf16 v[152:155], v[100:103], v[0:3], v[152:155]\n\t" \
  "v_mov_b32 v137, v136\n\t" \
  "v_mfma_f32_16x16x32_bf16 v[152:155], v[96:99], v[4:7], v[152:155]\n\t" \
  "v_mfma_f32_16x16x32_bf16 v[156:159], v[108:111], v[0:3], v[156:159]\n\t" \
  "ds_read_b64 v[0:1], v28 offset:10240\n\t" \
  "v_mfma_f32_16x16x32_bf16 v[156:159], v[104:107], v[4:7], v[156:159]\n\t" \
  "ds_read_b64 v[2:3], v29 offset:10240\n\t" \
  "v_mfma_f32_16x16x32_bf16 v[140:143], v[68:71], v[8:11], v[140:143]\n\t" \
  "ds_read_b64 v[4:5], v30 offset:11264\n\t" \
  "v_mfma_f32_16x16x32_bf16 v[140:143], v[64:67], v[12:15], v[140:143]\n\t" \
  "ds_read_b64 v[6:7], v31 offset:11264\n\t" \
  "v_mfma_f32_16x16x32_bf16 v[144:147], v[76:79], v[8:11], v[144:147]\n\t" \
  "v_mfma_f32_16x16x32_bf16 v[144:147], v[72:75], v[12:15], v[144:147]\n\t" \
  "v_mfma_f32_16x16x32_bf16 v[148:151], v[84:87], v[8:11], v[148:151]\n\t" \
  "v_mfma_f32_16x16x32_bf16 v[148:151], v[80:83], v[12:15], v[148:151]\n\t" \
  "s_waitcnt lgkmcnt(4)\n\t" \
  "v_cndmask_b32_e64 v136, v36, v35, vcc\n\t" \
  "v_mfma_f32_16x16x32_bf16 v[152:155], v[92:95], v[8:11], v[152:155]\n\t" \
  "ds_bpermute_b32 v37, v32, v143\n\t" \
  "v_mfma_f32_16x16x32_bf16 v[152:155], v[88:91], v[12:15], v[152:155]\n\t" \
  "v_mov_b32 v143, v142\n\t" \
  "v_mov_b32 v142, v141\n\t" \
  "v_mfma_f32_16x16x32_bf16 v[156:159], v[100:103], v[8:11], v[156:159]\n\t" \
  "ds_read_b64 v[8:9], v24 offset:12288\n\t" \
  "v_mov_b32 v141, v140\n\t" \
  "v_mfma_f32_16x16x32_bf16 v[156:159], v[96:99], v[12:15], v[156:159]\n\t" \
  "ds_read_b64 v[10:11], v25 offset:12288\n\t" \
  "v_mfma_f32_16x16x32_bf16 v[144:147], v[68:71], v[16:19], v[144:147]\n\t" \
  "ds_read_b64 v[12:13], v26 offset:13312\n\t" \
  "v_mfma_f32_16x16x32_bf16 v[144:147], v[64:67], v[20:23], v[144:147]\n\t" \
  "ds_read_b64 v[14:15], v27 offset:13312\n\t" \
  "v_mfma_f32_16x16x32_bf16 v[148:151], v[76:79], v[16:19], v[148:151]\n\t" \
  "v_mfma_f32_16x16x32_bf16 v[148:151], v[72:75], v[20:23], v[148:151]\n\t" \
  "v_mfma_f32_16x16x32_bf16 v[152:155], v[84:87], v[16:19], v[152:155]\n\t" \
  "v_mfma_f32_16x16x32_bf16 v[152:155], v[80:83], v[20:23], v[152:155]\n\t" \
  "v_mfma_f32_16x16x32_bf16 v[156:159], v[92:95], v[16:19], v[156:159]\n\t" \
  "ds_read_b64 v[16:17], v28 offset:14336\n\t" \
  "v_mfma_f32_16x16x32_bf16 v[156:159], v[88:91], v[20:23], v[156:159]\n\t" \
  "ds_read_b64 v[18:19], v29 offset:14336\n\t" \
  "s_waitcnt lgkmcnt(6)\n\t" \
  "v_mfma_f32_16x16x32_bf16 v[148:151], v[68:71], v[0:3], v[148:151]\n\t" \
  "ds_read_b64 v[20:21], v30 offset:15360\n\t" \
  "v_cndmask_b32_e64 v140, v37, v36, vcc\n\t" \
  "v_mfma_f32_16x16x32_bf16 v[148:151], v[64:67], v[4:7], v[148:151]\n\t" \
  "ds_read_b64 v[22:23], v31 offset:15360\n\t" \
  "v_mfma_f32_16x16x32_bf16 v[152:155], v[76:79], v[0:3], v[152:155]\n\t" \
  "ds_bpermute_b32 v38, v32, v147\n\t" \
  "v_mfma_f32_16x16x32_bf16 v[152:155], v[72:75], v[4:7], v[152:155]\n\t" \
  "v_mov_b32 v147, v146\n\t" \
  "v_mov_b32 v146, v145\n\t" \
  "v_mfma_f32_16x16x32_bf16 v[156:159], v[84:87], v[0:3], v[156:159]\n\t" \
  "ds_read_b64 v[0:1], v30 offset:0\n\t" \
  "ds_read_b64 v[2:3], v31 offset:0\n\t" \
  "v_mfma_f32_16x16x32_bf16 v[156:159], v[80:83], v[4:7], v[156:159]\n\t" \
  "ds_read_b64 v[4:5], v24 offset:1024\n\t" \
  "ds_read_b64 v[6:7], v25 offset:1024\n\t" \
  "s_waitcnt lgkmcnt(9)\n\t" \
  "v_mfma_f32_16x16x32_bf16 v[152:155], v[68:71], v[8:11], v[152:155]\n\t" \
  "ds_bpermute_b32 v39, v32, v151\n\t" \
  "v_mov_b32 v145, v144\n\t" \
  "v_mfma_f32_16x16x32_bf16 v[152:155], v[64:67], v[12:15], v[152:155]\n\t" \
  "v_mov_b32 v151, v150\n\t" \
  "v_mov_b32 v150, v149\n\t" \
  "v_mfma_f32_16x16x32_bf16 v[156:159], v[76:79], v[8:11], v[156:159]\n\t" \
  "ds_read_b64 v[8:9], v26 offset:2048\n\t" \
  "v_mov_b32 v149, v148\n\t" \
  "v_mfma_f32_16x16x32_bf16 v[156:159], v[72:75], v[12:15], v[156:159]\n\t" \
  "ds_read_b64 v[10:11], v27 offset:2048\n\t" \
  "s_waitcnt lgkmcnt(10)\n\t" \
  "v_mfma_f32_16x16x32_bf16 v[156:159], v[68:71], v[16:19], v[156:159]\n\t" \
  "ds_bpermute_b32 v40, v32, v155\n\t" \
  "s_waitcnt lgkmcnt(9)\n\t" \
  "v_mfma_f32_16x16x32_bf16 v[156:159], v[64:67], v[20:23], v[156:159]\n\t" \
  "ds_read_b64 v[12:13], v28 offset:3072\n\t" \
  "s_waitcnt lgkmcnt(7)\n\t" \
  "v_mfma_f32_16x16x32_bf16 v[128:131], v[68:71], v[0:3], v[128:131]\n\t" \
  "ds_read_b64 v[14:15], v29 offset:3072\n\t" \
  "s_waitcnt lgkmcnt(6)\n\t" \
  "v_mfma_f32_16x16x32_bf16 v[128:131], v[64:67], v[4:7], v[128:131]\n\t" \
  "v_cndmask_b32_e64 v144, v38, v37, vcc\n\t" \
  "v_mov_b32 v155, v154\n\t" \
  "v_mfma_f32_16x16x32_bf16 v[132:135], v[76:79], v[0:3], v[132:135]\n\t" \
  "ds_bpermute_b32 v41, v32, v159\n\t" \
  "v_mov_b32 v154, v153\n\t" \
  "v_mfma_f32_16x16x32_bf16 v[132:135], v[72:75], v[4:7], v[132:135]\n\t" \
  "v_mov_b32 v153, v152\n\t" \
  "v_mov_b32 v159, v158\n\t" \
  "v_mfma_f32_16x16x32_bf16 v[136:139], v[84:87], v[0:3], v[136:139]\n\t" \
  "v_mov_b32 v158, v157\n\t" \
  "v_mov_b32 v157, v156\n\t" \
  "v_mfma_f32_16x16x32_bf16 v[136:139], v[80:83], v[4:7], v[136:139]\n\t" \
  "s_waitcnt lgkmcnt(6)\n\t" \
  "v_cndmask_b32_e64 v148, v39, v38, vcc\n\t" \
  "v_mfma_f32_16x16x32_bf16 v[140:143], v[92:95], v[0:3], v[140:143]\n\t" \
  "ds_read_b64 v[16:17], v30 offset:4096\n\t" \
  "v_mfma_f32_16x16x32_bf16 v[140:143], v[88:91], v[4:7], v[140:143]\n\t" \
  "ds_read_b64 v[18:19], v31 offset:4096\n\t" \
  "v_mfma_f32_16x16x32_bf16 v[144:147], v[100:103], v[0:3], v[144:147]\n\t" \
  "s_waitcnt lgkmcnt(5)\n\t" \
  "v_cndmask_b32_e64 v152, v40, v39, vcc\n\t" \
  "v_mfma_f32_16x16x32_bf16 v[144:147], v[96:99], v[4:7], v[144:147]\n\t" \
  "ds_read_b64 v[20:21], v24 offset:5120\n\t" \
  "v_mfma_f32_16x16x32_bf16 v[148:151], v[108:111], v[0:3], v[148:151]\n\t" \
  "s_waitcnt lgkmcnt(3)\n\t" \
  "v_cndmask_b32_e64 v156, v41, v40, vcc\n\t" \
  "v_mfma_f32_16x16x32_bf16 v[148:151], v[104:107], v[4:7], v[148:151]\n\t" \
  "v_cvt_pk_bf16_f32 v34, v128, v129\n\t" \
  "v_cvt_pk_bf16_f32 v35, v130, v131\n\t" \
  "v_mfma_f32_16x16x32_bf16 v[152:155], v[116:119], v[0:3], v[152:155]\n\t" \
  "ds_read_b64 v[22:23], v25 offset:5120\n\t" \
  "v_mfma_f32_16x16x32_bf16 v[152:155], v[112:115], v[4:7], v[152:155]\n\t" \
  "v_mfma_f32_16x16x32_bf16 v[156:159], v[124:127], v[0:3], v[156:159]\n\t" \
  "ds_read_b64 v[0:1], v26 offset:6144\n\t" \
  "v_mfma_f32_16x16x32_bf16 v[156:159], v[120:123], v[4:7], v[156:159]\n\t" \
  "ds_read_b64 v[2:3], v27 offset:6144\n\t" \
  "v_mfma_f32_16x16x32_bf16 v[132:135], v[68:71], v[8:11], v[132:135]\n\t" \
  "ds_read_b64 v[4:5], v28 offset:7168\n\t" \
  "v_mfma_f32_16x16x32_bf16 v[132:135], v[64:67], v[12:15], v[132:135]\n\t" \
  "ds_read_b64 v[6:7], v29 offset:7168\n\t" \
  "v_mfma_f32_16x16x32_bf16 v[136:139], v[76:79], v[8:11], v[136:139]\n\t" \
  "v_mfma_f32_16x16x32_bf16 v[136:139], v[72:75], v[12:15], v[136:139]\n\t" \
  "v_mfma_f32_16x16x32_bf16 v[140:143], v[84:87], v[8:11], v[140:143]\n\t" \
  "v_mfma_f32_16x16x32_bf16 v[140:143], v[80:83], v[12:15], v[140:143]\n\t" \
  "v_mfma_f32_16x16x32_bf16 v[144:147], v[92:95], v[8:11], v[144:147]\n\t" \
  "v_mfma_f32_16x16x32_bf16 v[144:147], v[88:91], v[12:15], v[144:147]\n\t" \
  "v_mfma_f32_16x16x32_bf16 v[148:151], v[100:103], v[8:11], v[148:151]\n\t" \
  "v_cvt_pk_bf16_f32 v36, v132, v133\n\t" \
  "v_cvt_pk_bf16_f32 v37, v134, v135\n\t" \
  "v_mfma_f32_16x16x32_bf16 v[148:151], v[96:99], v[12:15], v[148:151]\n\t" \
  "s_mov_b64 exec, 1\n\t" \
  "ds_write_b32 v33, v33\n\t" \
  "s_mov_b64 exec, -1\n\t" \
  "v_mfma_f32_16x16x32_bf16 v[152:155], v[108:111], v[8:11], v[152:155]\n\t" \
  "v_permlane16_swap_b32 v34, v36\n\t" \
  "v_permlane16_swap_b32 v35, v37\n\t" \
  "v_mfma_f32_16x16x32_bf16 v[152:155], v[104:107], v[12:15], v[152:155]\n\t" \
  "global_store_dwordx4 v42, v[34:37], %[yb] offset:0 sc1\n\t" \
  "v_mfma_f32_16x16x32_bf16 v[156:159], v[116:119], v[8:11], v[156:159]\n\t" \
  "ds_read_b64 v[8:9], v30 offset:8192\n\t" \
  "v_mfma_f32_16x16x32_bf16 v[156:159], v[112:115], v[12:15], v[156:159]\n\t" \
  "ds_read_b64 v[10:11], v31 offset:8192\n\t" \
  "s_waitcnt lgkmcnt(3)\n\t" \
  "v_mfma_f32_16x16x32_bf16 v[136:139], v[68:71], v[16:19], v[136:139]\n\t" \
  "ds_read_b64 v[12:13], v24 offset:9216\n\t" \
  "v_mfma_f32_16x16x32_bf16 v[136:139], v[64:67], v[20:23], v[136:139]\n\t" \
  "ds_read_b64 v[14:15], v25 offset:9216\n\t" \
  "v_mfma_f32_16x16x32_bf16 v[140:143], v[76:79], v[16:19], v[140:143]\n\t" \
  "v_mfma_f32_16x16x32_bf16 v[140:143], v[72:75], v[20:23], v[140:143]\n\t" \
  "v_mfma_f32_16x16x32_bf16 v[144:147], v[84:87], v[16:19], v[144:147]\n\t" \
  "v_mfma_f32_16x16x32_bf16 v[144:147], v[80:83], v[20:23], v[144:147]\n\t" \
  "v_mfma_f32_16x16x32_bf16 v[148:151], v[92:95], v[16:19], v[148:151]\n\t" \
  "v_mfma_f32_16x16x32_bf16 v[148:151], v[88:91], v[20:23], v[148:151]\n\t" \
  "v_mfma_f32_16x16x32_bf16 v[152:155], v[100:103], v[16:19], v[152:155]\n\t" \
  "v_cvt_pk_bf16_f32 v38, v136, v137\n\t" \
  "v_cvt_pk_bf16_f32 v39, v138, v139\n\t" \
  "v_mfma_f32_16x16x32_bf16 v[152:155], v[96:99], v[20:23], v[152:155]\n\t" \
  "v_mfma_f32_16x16x32_bf16 v[156:159], v[108:111], v[16:19], v[156:159]\n\t" \
  "ds_read_b64 v[16:17], v26 offset:10240\n\t" \
  "v_mfma_f32_16x16x32_bf16 v[156:159], v[104:107], v[20:23], v[156:159]\n\t" \
  "ds_read_b64 v[18:19], v27 offset:10240\n\t" \
  "v_mfma_f32_16x16x32_bf16 v[140:143], v[68:71], v[0:3], v[140:143]\n\t" \
  "ds_read_b64 v[20:21], v28 offset:11264\n\t" \
  "v_mfma_f32_16x16x32_bf16 v[140:143], v[64:67], v[4:7], v[140:143]\n\t" \
  "ds_read_b64 v[22:23], v29 offset:11264\n\t" \
  "v_mfma_f32_16x16x32_bf16 v[144:147], v[76:79], v[0:3], v[144:147]\n\t" \
  "v_mfma_f32_16x16x32_bf16 v[144:147], v[72:75], v[4:7], v[144:147]\n\t" \
  "v_mfma_f32_16x16x32_bf16 v[148:151], v[84:87], v[0:3], v[148:151]\n\t" \
  "v_mfma_f32_16x16x32_bf16 v[148:151], v[80:83], v[4:7], v[148:151]\n\t" \
  "v_mfma_f32_16x16x32_bf16 v[152:155], v[92:95], v[0:3], v[152:155]\n\t" \
  "v_mfma_f32_16x16x32_bf16 v[152:155], v[88:91], v[4:7], v[152:155]\n\t" \
  "v_mfma_f32_16x16x32_bf16 v[156:159], v[100:103], v[0:3], v[156:159]\n\t" \
  "v_cvt_pk_bf16_f32 v40, v140, v141\n\t" \
  "v_cvt_pk_bf16_f32 v41, v142, v143\n\t" \
  "v_mfma_f32_16x16x32_bf16 v[156:159], v[96:99], v[4:7], v[156:159]\n\t" \
  "ds_read_b64 v[0:1], v30 offset:12288\n\t" \
  "s_waitcnt lgkmcnt(5)\n\t" \
  "v_mfma_f32_16x16x32_bf16 v[144:147], v[68:71], v[8:11], v[144:147]\n\t" \
  "v_permlane16_swap_b32 v38, v40\n\t" \
  "ds_read_b64 v[2:3], v31 offset:12288\n\t" \
  "v_mfma_f32_16x16x32_bf16 v[144:147], v[64:67], v[12:15], v[144:147]\n\t" \
  "v_permlane16_swap_b32 v39, v41\n\t" \
  "ds_read_b64 v[4:5], v24 offset:13312\n\t" \
  "v_mfma_f32_16x16x32_bf16 v[148:151], v[76:79], v[8:11], v[148:151]\n\t" \
  "ds_read_b64 v[6:7], v25 offset:13312\n\t" \
  "v_mfma_f32_16x16x32_bf16 v[148:151], v[72:75], v[12:15], v[148:151]\n\t" \
  "global_store_dwordx4 v42, v[38:41], %[yb] offset:1024 sc1\n\t" \
  "v_mfma_f32_16x16x32_bf16 v[152:155], v[84:87], v[8:11], v[152:155]\n\t" \
  "v_mfma_f32_16x16x32_bf16 v[152:155], v[80:83], v[12:15], v[152:155]\n\t" \
  "v_cvt_pk_bf16_f32 v34, v144, v145\n\t" \
  "v_cvt_pk_bf16_f32 v35, v146, v147\n\t" \
  "v_mfma_f32_16x16x32_bf16 v[156:159], v[92:95], v[8:11], v[156:159]\n\t" \
  "ds_read_b64 v[8:9], v26 offset:14336\n\t" \
  "v_mfma_f32_16x16x32_bf16 v[156:159], v[88:91], v[12:15], v[156:159]\n\t" \
  "ds_read_b64 v[10:11], v27 offset:14336\n\t" \
  "s_waitcnt lgkmcnt(6)\n\t" \
  "v_mfma_f32_16x16x32_bf16 v[148:151], v[68:71], v[16:19], v[148:151]\n\t" \
  "ds_read_b64 v[12:13], v28 offset:15360\n\t" \
  "v_mfma_f32_16x16x32_bf16 v[148:151], v[64:67], v[20:23], v[148:151]\n\t" \
  "ds_read_b64 v[14:15], v29 offset:15360\n\t" \
  "v_mfma_f32_16x16x32_bf16 v[152:155], v[76:79], v[16:19], v[152:155]\n\t" \
  "v_mfma_f32_16x16x32_bf16 v[152:155], v[72:75], v[20:23], v[152:155]\n\t" \
  "v_mfma_f32_16x16x32_bf16 v[156:159], v[84:87], v[16:19], v[156:159]\n\t" \
  "v_mfma_f32_16x16x32_bf16 v[156:159], v[80:83], v[20:23], v[156:159]\n\t" \
  "s_waitcnt lgkmcnt(4)\n\t" \
  "v_mfma_f32_16x16x32_bf16 v[152:155], v[68:71], v[0:3], v[152:155]\n\t" \
  "v_mfma_f32_16x16x32_bf16 v[152:155], v[64:67], v[4:7], v[152:155]\n\t" \
  "v_cvt_pk_bf16_f32 v36, v148, v149\n\t" \
  "v_cvt_pk_bf16_f32 v37, v150, v151\n\t" \
  "v_mfma_f32_16x16x32_bf16 v[156:159], v[76:79], v[0:3], v[156:159]\n\t" \
  "v_mfma_f32_16x16x32_bf16 v[156:159], v[72:75], v[4:7], v[156:159]\n\t" \
  "v_permlane16_swap_b32 v34, v36\n\t" \
  "s_waitcnt lgkmcnt(2)\n\t" \
  "v_mfma_f32_16x16x32_bf16 v[156:159], v[68:71], v[8:11], v[156:159]\n\t" \
  "v_permlane16_swap_b32 v35, v37\n\t" \
  "s_waitcnt lgkmcnt(0)\n\t" \
  "v_mfma_f32_16x16x32_bf16 v[156:159], v[64:67], v[12:15], v[156:159]\n\t" \
  "global_store_dwordx4 v42, v[34:37], %[yb] offset:2048 sc1\n\t" \
  "s_waitcnt lgkmcnt(0)\n\t" \
  "s_nop 4\n\t" \
  "v_cvt_pk_bf16_f32 v38, v152, v153\n\t" \
  "v_cvt_pk_bf16_f32 v39, v154, v155\n\t" \
  "v_cvt_pk_bf16_f32 v40, v156, v157\n\t" \
  "v_cvt_pk_bf16_f32 v41, v158, v159\n\t" \
  "s_nop 1\n\t" \
  "v_permlane16_swap_b32 v38, v40\n\t" \
  "v_permlane16_swap_b32 v39, v41\n\t" \
  "global_store_dwordx4 v42, v[38:41], %[yb] offset:3072 sc1\n\t" \
  "s_nop 1\n\t"
#define CV_Q 1
#define CV_CLOB "vcc", "v0", "v1", "v2", "v3", "v4", "v5", "v6", "v7", "v8", "v9", "v10", "v11", "v12", "v13", "v14", "v15", "v16", "v17", "v18", "v19", "v20", "v21", "v22", "v23", "v34", "v35", "v36", "v37", "v38", "v39", "v40", "v41"

#define G3_ASM \
  "v_readlane_b32 s64, v96, 0\n\t" \
  "v_readlane_b32 s65, v96, 1\n\t" \
  "v_readlane_b32 s66, v96, 2\n\t" \
  "v_readlane_b32 s67, v96, 3\n\t" \
  "v_readlane_b32 s68, v96, 4\n\t" \
  "v_readlane_b32 s69, v96, 5\n\t" \
  "v_readlane_b32 s70, v96, 6\n\t" \
  "v_readlane_b32 s71, v96, 7\n\t" \
  "v_readlane_b32 s72, v96, 8\n\t" \
  "v_readlane_b32 s73, v96, 9\n\t" \
  "v_readlane_b32 s74, v96, 10\n\t" \
  "v_readlane_b32 s75, v96, 11\n\t" \
  "v_readlane_b32 s76, v96, 12\n\t" \
  "v_readlane_b32 s77, v96, 13\n\t" \
  "v_readlane_b32 s78, v96, 14\n\t" \
  "v_readlane_b32 s79, v96, 15\n\t" \
  "v_add_u32 v97, %[go], v165\n\t" \
  "s_branch QG_%=\n\t" \
  "QA14_%=:\n\t" \
  "v_mov_b32 v165, %[sy]\n\t" \
  "s_sleep 1\n\t" \
  "ds_read_b32 v165, v165\n\t" \
  "s_addk_i32 %[cn], 1\n\t" \
  "s_cmp_gt_u32 %[cn], 0x400000\n\t" \
  "s_cbranch_scc1 QC14_%=\n\t" \
  "s_waitcnt lgkmcnt(0)\n\t" \
  "v_cmp_lt_u32 vcc, 15, v165\n\t" \
  "s_cbranch_vccz QA14_%=\n\t" \
  "QC14_%=:\n\t" \
  "s_waitcnt lgkmcnt(0)\n\t" \
  "s_branch QB14_%=\n\t" \
  "QS_%=:\n\t" \
  "s_sleep 1\n\t" \
  "QG_%=:\n\t" \
  "ds_read_b32 v98, v165\n\t" \
  "ds_read_b32 v99, v97\n\t" \
  "s_addk_i32 %[cn], 1\n\t" \
  "s_cmp_gt_u32 %[cn], 0x400000\n\t" \
  "s_cselect_b32 %[t2], 16, 0\n\t" \
  "s_waitcnt lgkmcnt(0)\n\t" \
  "v_readfirstlane_b32 %[t0], v98\n\t" \
  "v_readfirstlane_b32 %[t1], v99\n\t" \
  "s_max_u32 %[t0], %[t0], %[t2]\n\t" \
  "s_or_b32 %[t1], %[t1], %[t2]\n\t" \
  "s_min_u32 %[t2], %[t0], 6\n\t" \
  "s_cmp_ge_u32 %[iss], %[t2]\n\t" \
  "s_cbranch_scc1 QD_%=\n\t" \
  "s_cmp_eq_u32 %[iss], 1\n\t" \
  "s_cbranch_scc1 QL1_%=\n\t" \
  "s_cmp_eq_u32 %[iss], 2\n\t" \
  "s_cbranch_scc1 QL2_%=\n\t" \
  "s_cmp_eq_u32 %[iss], 3\n\t" \
  "s_cbranch_scc1 QL3_%=\n\t" \
  "s_cmp_eq_u32 %[iss], 4\n\t" \
  "s_cbranch_scc1 QL4_%=\n\t" \
  "s_cmp_eq_u32 %[iss], 5\n\t" \
  "s_cbranch_scc1 QL5_%=\n\t" \
  "QL0_%=:\n\t" \
  "buffer_load_dwordx4 v[0:3], v164, %[ob], s64 offen offset:0 sc1\n\t" \
  "buffer_load_dwordx4 v[4:7], v164, %[ob], s64 offen offset:1024 sc1\n\t" \
  "buffer_load_dwordx4 v[8:11], v164, %[ob], s64 offen offset:2048 sc1\n\t" \
  "buffer_load_dwordx4 v[12:15], v164, %[ob], s64 offen offset:3072 sc1\n\t" \
  "s_mov_b32 %[iss], 1\n\t" \
  "s_cmp_le_u32 %[t2], 1\n\t" \
  "s_cbranch_scc1 QD_%=\n\t" \
  "QL1_%=:\n\t" \
  "buffer_load_dwordx4 v[16:19], v164, %[ob], s65 offen offset:0 sc1\n\t" \
  "buffer_load_dwordx4 v[20:23], v164, %[ob], s65 offen offset:1024 sc1\n\t" \
  "buffer_load_dwordx4 v[24:27], v164, %[ob], s65 offen offset:2048 sc1\n\t" \
  "buffer_load_dwordx4 v[28:31], v164, %[ob], s65 offen offset:3072 sc1\n\t" \
  "s_mov_b32 %[iss], 2\n\t" \
  "s_cmp_le_u32 %[t2], 2\n\t" \
  "s_cbranch_scc1 QD_%=\n\t" \
  "QL2_%=:\n\t" \
  "buffer_load_dwordx4 v[32:35], v164, %[ob], s66 offen offset:0 sc1\n\t" \
  "buffer_load_dwordx4 v[36:39], v164, %[ob], s66 offen offset:1024 sc1\n\t" \
  "buffer_load_dwordx4 v[40:43], v164, %[ob], s66 offen offset:2048 sc1\n\t" \
  "buffer_load_dwordx4 v[44:47], v164, %[ob], s66 offen offset:3072 sc1\n\t" \
  "s_mov_b32 %[iss], 3\n\t" \
  "s_cmp_le_u32 %[t2], 3\n\t" \
  "s_cbranch_scc1 QD_%=\n\t" \
  "QL3_%=:\n\t" \
  "buffer_load_dwordx4 v[48:51], v164, %[ob], s67 offen offset:0 sc1\n\t" \
  "buffer_load_dwordx4 v[52:55], v164, %[ob], s67 offen offset:1024 sc1\n\t" \
  "buffer_load_dwordx4 v[56:59], v164, %[ob], s67 offen offset:2048 sc1\n\t" \
  "buffer_load_dwordx4 v[60:63], v164, %[ob], s67 offen offset:3072 sc1\n\t" \
  "s_mov_b32 %[iss], 4\n\t" \
  "s_cmp_le_u32 %[t2], 4\n\t" \
  "s_cbranch_scc1 QD_%=\n\t" \
  "QL4_%=:\n\t" \
  "buffer_load_dwordx4 v[64:67], v164, %[ob], s68 offen offset:0 sc1\n\t" \
  "buffer_load_dwordx4 v[68:71], v164, %[ob], s68 offen offset:1024 sc1\n\t" \
  "buffer_load_dwordx4 v[72:75], v164, %[ob], s68 offen offset:2048 sc1\n\t" \
  "buffer_load_dwordx4 v[76:79], v164, %[ob], s68 offen offset:3072 sc1\n\t" \
  "s_mov_b32 %[iss], 5\n\t" \
  "s_cmp_le_u32 %[t2], 5\n\t" \
  "s_cbranch_scc1 QD_%=\n\t" \
  "QL5_%=:\n\t" \
  "buffer_load_dwordx4 v[80:83], v164, %[ob], s69 offen offset:0 sc1\n\t" \
  "buffer_load_dwordx4 v[84:87], v164, %[ob], s69 offen offset:1024 sc1\n\t" \
  "buffer_load_dwordx4 v[88:91], v164, %[ob], s69 offen offset:2048 sc1\n\t" \
  "buffer_load_dwordx4 v[92:95], v164, %[ob], s69 offen offset:3072 sc1\n\t" \
  "s_mov_b32 %[iss], 6\n\t" \
  "QD_%=:\n\t" \
  "s_cmp_lt_u32 %[t0], 14\n\t" \
  "s_cbranch_scc1 QS_%=\n\t" \
  "s_cmp_eq_u32 %[t1], 0\n\t" \
  "s_cbranch_scc1 QS_%=\n\t" \
  "s_setprio 0\n\t" \
  "ds_read_b64_tr_b16 v[96:97], v160 offset:0\n\t" \
  "ds_read_b64_tr_b16 v[98:99], v160 offset:2048\n\t" \
  "ds_read_b64_tr_b16 v[100:101], v161 offset:0\n\t" \
  "ds_read_b64_tr_b16 v[102:103], v161 offset:2048\n\t" \
  "ds_read_b64_tr_b16 v[104:105], v162 offset:0\n\t" \
  "ds_read_b64_tr_b16 v[106:107], v162 offset:2048\n\t" \
  "ds_read_b64_tr_b16 v[108:109], v163 offset:0\n\t" \
  "ds_read_b64_tr_b16 v[110:111], v163 offset:2048\n\t" \
  "ds_read_b64_tr_b16 v[112:113], v160 offset:4096\n\t" \
  "ds_read_b64_tr_b16 v[114:115], v160 offset:6144\n\t" \
  "ds_read_b64_tr_b16 v[116:117], v161 offset:4096\n\t" \
  "ds_read_b64_tr_b16 v[118:119], v161 offset:6144\n\t" \
  "ds_read_b64_tr_b16 v[120:121], v162 offset:4096\n\t" \
  "ds_read_b64_tr_b16 v[122:123], v162 offset:6144\n\t" \
  "s_waitcnt vmcnt(22) lgkmcnt(10)\n\t" \
  "v_mfma_f32_16x16x32_bf16 v[128:131], v[96:99], v[0:3], 0\n\t" \
  "ds_read_b64_tr_b16 v[124:125], v163 offset:4096\n\t" \
  "v_mfma_f32_16x16x32_bf16 v[144:147], v[96:99], v[4:7], 0\n\t" \
  "ds_read_b64_tr_b16 v[126:127], v163 offset:6144\n\t" \
  "v_mfma_f32_16x16x32_bf16 v[132:135], v[100:103], v[0:3], 0\n\t" \
  "ds_read_b64_tr_b16 v[96:97], v160 offset:8192\n\t" \
  "v_mfma_f32_16x16x32_bf16 v[148:151], v[100:103], v[4:7], 0\n\t" \
  "ds_read_b64_tr_b16 v[98:99], v160 offset:10240\n\t" \
  "s_waitcnt lgkmcnt(10)\n\t" \
  "v_mfma_f32_16x16x32_bf16 v[136:139], v[104:107], v[0:3], 0\n\t" \
  "ds_read_b64_tr_b16 v[100:101], v161 offset:8192\n\t" \
  "v_mfma_f32_16x16x32_bf16 v[152:155], v[104:107], v[4:7], 0\n\t" \
  "ds_read_b64_tr_b16 v[102:103], v161 offset:10240\n\t" \
  "v_mfma_f32_16x16x32_bf16 v[140:143], v[108:111], v[0:3], 0\n\t" \
  "ds_read_b64_tr_b16 v[104:105], v162 offset:8192\n\t" \
  "v_mfma_f32_16x16x32_bf16 v[156:159], v[108:111], v[4:7], 0\n\t" \
  "ds_read_b64_tr_b16 v[106:107], v162 offset:10240\n\t" \
  "s_waitcnt vmcnt(20) lgkmcnt(10)\n\t" \
  "v_mfma_f32_16x16x32_bf16 v[128:131], v[112:115], v[8:11], v[128:131]\n\t" \
  "ds_read_b64_tr_b16 v[108:109], v163 offset:8192\n\t" \
  "v_mfma_f32_16x16x32_bf16 v[144:147], v[112:115], v[12:15], v[144:147]\n\t" \
  "ds_read_b64_tr_b16 v[110:111], v163 offset:10240\n\t" \
  "v_mfma_f32_16x16x32_bf16 v[132:135], v[116:119], v[8:11], v[132:135]\n\t" \
  "ds_read_b64_tr_b16 v[112:113], v160 offset:12288\n\t" \
  "v_mfma_f32_16x16x32_bf16 v[148:151], v[116:119], v[12:15], v[148:151]\n\t" \
  "ds_read_b64_tr_b16 v[114:115], v160 offset:14336\n\t" \
  "s_waitcnt lgkmcnt(10)\n\t" \
  "v_mfma_f32_16x16x32_bf16 v[136:139], v[120:123], v[8:11], v[136:139]\n\t" \
  "ds_read_b64_tr_b16 v[116:117], v161 offset:12288\n\t" \
  "v_mfma_f32_16x16x32_bf16 v[152:155], v[120:123], v[12:15], v[152:155]\n\t" \
  "ds_read_b64_tr_b16 v[118:119], v161 offset:14336\n\t" \
  "v_mfma_f32_16x16x32_bf16 v[140:143], v[124:127], v[8:11], v[140:143]\n\t" \
  "ds_read_b64_tr_b16 v[120:121], v162 offset:12288\n\t" \
  "v_mfma_f32_16x16x32_bf16 v[156:159], v[124:127], v[12:15], v[156:159]\n\t" \
  "ds_read_b64_tr_b16 v[122:123], v162 offset:14336\n\t" \
  "s_waitcnt vmcnt(18) lgkmcnt(10)\n\t" \
  "v_mfma_f32_16x16x32_bf16 v[128:131], v[96:99], v[16:19], v[128:131]\n\t" \
  "ds_read_b64_tr_b16 v[124:125], v163 offset:12288\n\t" \
  "buffer_load_dwordx4 v[0:3], v164, %[ob], s70 offen offset:0 sc1\n\t" \
  "v_mfma_f32_16x16x32_bf16 v[144:147], v[96:99], v[20:23], v[144:147]\n\t" \
  "ds_read_b64_tr_b16 v[126:127], v163 offset:14336\n\t" \
  "buffer_load_dwordx4 v[4:7], v164, %[ob], s70 offen offset:1024 sc1\n\t" \
  "v_mfma_f32_16x16x32_bf16 v[132:135], v[100:103], v[16:19], v[132:135]\n\t" \
  "ds_read_b64_tr_b16 v[96:97], v160 offset:16384\n\t" \
  "buffer_load_dwordx4 v[8:11], v164, %[ob], s70 offen offset:2048 sc1\n\t" \
  "v_mfma_f32_16x16x32_bf16 v[148:151], v[100:103], v[20:23], v[148:151]\n\t" \
  "ds_read_b64_tr_b16 v[98:99], v160 offset:18432\n\t" \
  "buffer_load_dwordx4 v[12:15], v164, %[ob], s70 offen offset:3072 sc1\n\t" \
  "s_waitcnt lgkmcnt(10)\n\t" \
  "v_mfma_f32_16x16x32_bf16 v[136:139], v[104:107], v[16:19], v[136:139]\n\t" \
  "ds_read_b64_tr_b16 v[100:101], v161 offset:16384\n\t" \
  "v_mfma_f32_16x16x32_bf16 v[152:155], v[104:107], v[20:23], v[152:155]\n\t" \
  "ds_read_b64_tr_b16 v[102:103], v161 offset:18432\n\t" \
  "v_mfma_f32_16x16x32_bf16 v[140:143], v[108:111], v[16:19], v[140:143]\n\t" \
  "ds_read_b64_tr_b16 v[104:105], v162 offset:16384\n\t" \
  "v_mfma_f32_16x16x32_bf16 v[156:159], v[108:111], v[20:23], v[156:159]\n\t" \
  "ds_read_b64_tr_b16 v[106:107], v162 offset:18432\n\t" \
  "s_waitcnt vmcnt(20) lgkmcnt(10)\n\t" \
  "v_mfma_f32_16x16x32_bf16 v[128:131], v[112:115], v[24:27], v[128:131]\n\t" \
  "ds_read_b64_tr_b16 v[108:109], v163 offset:16384\n\t" \
  "v_mfma_f32_16x16x32_bf16 v[144:147], v[112:115], v[28:31], v[144:147]\n\t" \
  "ds_read_b64_tr_b16 v[110:111], v163 offset:18432\n\t" \
  "v_mfma_f32_16x16x32_bf16 v[132:135], v[116:119], v[24:27], v[132:135]\n\t" \
  "ds_read_b64_tr_b16 v[112:113], v160 offset:20480\n\t" \
  "v_mfma_f32_16x16x32_bf16 v[148:151], v[116:119], v[28:31], v[148:151]\n\t" \
  "ds_read_b64_tr_b16 v[114:115], v160 offset:22528\n\t" \
  "s_waitcnt lgkmcnt(10)\n\t" \
  "v_mfma_f32_16x16x32_bf16 v[136:139], v[120:123], v[24:27], v[136:139]\n\t" \
  "ds_read_b64_tr_b16 v[116:117], v161 offset:20480\n\t" \
  "v_mfma_f32_16x16x32_bf16 v[152:155], v[120:123], v[28:31], v[152:155]\n\t" \
  "ds_read_b64_tr_b16 v[118:119], v161 offset:22528\n\t" \
  "v_mfma_f32_16x16x32_bf16 v[140:143], v[124:127], v[24:27], v[140:143]\n\t" \
  "ds_read_b64_tr_b16 v[120:121], v162 offset:20480\n\t" \
  "v_mfma_f32_16x16x32_bf16 v[156:159], v[124:127], v[28:31], v[156:159]\n\t" \
  "ds_read_b64_tr_b16 v[122:123], v162 offset:22528\n\t" \
  "s_waitcnt vmcnt(18) lgkmcnt(10)\n\t" \
  "v_mfma_f32_16x16x32_bf16 v[128:131], v[96:99], v[32:35], v[128:131]\n\t" \
  "ds_read_b64_tr_b16 v[124:125], v163 offset:20480\n\t" \
  "buffer_load_dwordx4 v[16:19], v164, %[ob], s71 offen offset:0 sc1\n\t" \
  "v_mfma_f32_16x16x32_bf16 v[144:147], v[96:99], v[36:39], v[144:147]\n\t" \
  "ds_read_b64_tr_b16 v[126:127], v163 offset:22528\n\t" \
  "buffer_load_dwordx4 v[20:23], v164, %[ob], s71 offen offset:1024 sc1\n\t" \
  "v_mfma_f32_16x16x32_bf16 v[132:135], v[100:103], v[32:35], v[132:135]\n\t" \
  "ds_read_b64_tr_b16 v[96:97], v160 offset:24576\n\t" \
  "buffer_load_dwordx4 v[24:27], v164, %[ob], s71 offen offset:2048 sc1\n\t" \
  "v_mfma_f32_16x16x32_bf16 v[148:151], v[100:103], v[36:39], v[148:151]\n\t" \
  "ds_read_b64_tr_b16 v[98:99], v160 offset:26624\n\t" \
  "buffer_load_dwordx4 v[28:31], v164, %[ob], s71 offen offset:3072 sc1\n\t" \
  "s_waitcnt lgkmcnt(10)\n\t" \
  "v_mfma_f32_16x16x32_bf16 v[136:139], v[104:107], v[32:35], v[136:139]\n\t" \
  "ds_read_b64_tr_b16 v[100:101], v161 offset:24576\n\t" \
  "v_mfma_f32_16x16x32_bf16 v[152:155], v[104:107], v[36:39], v[152:155]\n\t" \
  "ds_read_b64_tr_b16 v[102:103], v161 offset:26624\n\t" \
  "v_mfma_f32_16x16x32_bf16 v[140:143], v[108:111], v[32:35], v[140:143]\n\t" \
  "ds_read_b64_tr_b16 v[104:105], v162 offset:24576\n\t" \
  "v_mfma_f32_16x16x32_bf16 v[156:159], v[108:111], v[36:39], v[156:159]\n\t" \
  "ds_read_b64_tr_b16 v[106:107], v162 offset:26624\n\t" \
  "s_waitcnt vmcnt(20) lgkmcnt(10)\n\t" \
  "v_mfma_f32_16x16x32_bf16 v[128:131], v[112:115], v[40:43], v[128:131]\n\t" \
  "ds_read_b64_tr_b16 v[108:109], v163 offset:24576\n\t" \
  "v_mfma_f32_16x16x32_bf16 v[144:147], v[112:115], v[44:47], v[144:147]\n\t" \
  "ds_read_b64_tr_b16 v[110:111], v163 offset:26624\n\t" \
  "v_mfma_f32_16x16x32_bf16 v[132:135], v[116:119], v[40:43], v[132:135]\n\t" \
  "ds_read_b64_tr_b16 v[112:113], v160 offset:28672\n\t" \
  "v_mfma_f32_16x16x32_bf16 v[148:151], v[116:119], v[44:47], v[148:151]\n\t" \
  "ds_read_b64_tr_b16 v[114:115], v160 offset:30720\n\t" \
  "s_waitcnt lgkmcnt(10)\n\t" \
  "v_mfma_f32_16x16x32_bf16 v[136:139], v[120:123], v[40:43], v[136:139]\n\t" \
  "ds_read_b64_tr_b16 v[116:117], v161 offset:28672\n\t" \
  "v_mfma_f32_16x16x32_bf16 v[152:155], v[120:123], v[44:47], v[152:155]\n\t" \
  "ds_read_b64_tr_b16 v[118:119], v161 offset:30720\n\t" \
  "v_mfma_f32_16x16x32_bf16 v[140:143], v[124:127], v[40:43], v[140:143]\n\t" \
  "ds_read_b64_tr_b16 v[120:121], v162 offset:28672\n\t" \
  "v_mfma_f32_16x16x32_bf16 v[156:159], v[124:127], v[44:47], v[156:159]\n\t" \
  "ds_read_b64_tr_b16 v[122:123], v162 offset:30720\n\t" \
  "s_waitcnt vmcnt(18) lgkmcnt(10)\n\t" \
  "v_mfma_f32_16x16x32_bf16 v[128:131], v[96:99], v[48:51], v[128:131]\n\t" \
  "ds_read_b64_tr_b16 v[124:125], v163 offset:28672\n\t" \
  "buffer_load_dwordx4 v[32:35], v164, %[ob], s72 offen offset:0 sc1\n\t" \
  "v_mfma_f32_16x16x32_bf16 v[144:147], v[96:99], v[52:55], v[144:147]\n\t" \
  "ds_read_b64_tr_b16 v[126:127], v163 offset:30720\n\t" \
  "buffer_load_dwordx4 v[36:39], v164, %[ob], s72 offen offset:1024 sc1\n\t" \
  "v_mfma_f32_16x16x32_bf16 v[132:135], v[100:103], v[48:51], v[132:135]\n\t" \
  "ds_read_b64_tr_b16 v[96:97], v160 offset:32768\n\t" \
  "buffer_load_dwordx4 v[40:43], v164, %[ob], s72 offen offset:2048 sc1\n\t" \
  "v_mfma_f32_16x16x32_bf16 v[148:151], v[100:103], v[52:55], v[148:151]\n\t" \
  "ds_read_b64_tr_b16 v[98:99], v160 offset:34816\n\t" \
  "buffer_load_dwordx4 v[44:47], v164, %[ob], s72 offen offset:3072 sc1\n\t" \
  "s_waitcnt lgkmcnt(10)\n\t" \
  "v_mfma_f32_16x16x32_bf16 v[136:139], v[104:107], v[48:51], v[136:139]\n\t" \
  "ds_read_b64_tr_b16 v[100:101], v161 offset:32768\n\t" \
  "v_mfma_f32_16x16x32_bf16 v[152:155], v[104:107], v[52:55], v[152:155]\n\t" \
  "ds_read_b64_tr_b16 v[102:103], v161 offset:34816\n\t" \
  "v_mfma_f32_16x16x32_bf16 v[140:143], v[108:111], v[48:51], v[140:143]\n\t" \
  "ds_read_b64_tr_b16 v[104:105], v162 offset:32768\n\t" \
  "v_mfma_f32_16x16x32_bf16 v[156:159], v[108:111], v[52:55], v[156:159]\n\t" \
  "ds_read_b64_tr_b16 v[106:107], v162 offset:34816\n\t" \
  "s_waitcnt vmcnt(20) lgkmcnt(10)\n\t" \
  "v_mfma_f32_16x16x32_bf16 v[128:131], v[112:115], v[56:59], v[128:131]\n\t" \
  "ds_read_b64_tr_b16 v[108:109], v163 offset:32768\n\t" \
  "v_mfma_f32_16x16x32_bf16 v[144:147], v[112:115], v[60:63], v[144:147]\n\t" \
  "ds_read_b64_tr_b16 v[110:111], v163 offset:34816\n\t" \
  "v_mfma_f32_16x16x32_bf16 v[132:135], v[116:119], v[56:59], v[132:135]\n\t" \
  "ds_read_b64_tr_b16 v[112:113], v160 offset:36864\n\t" \
  "v_mfma_f32_16x16x32_bf16 v[148:151], v[116:119], v[60:63], v[148:151]\n\t" \
  "ds_read_b64_tr_b16 v[114:115], v160 offset:38912\n\t" \
  "s_waitcnt lgkmcnt(10)\n\t" \
  "v_mfma_f32_16x16x32_bf16 v[136:139], v[120:123], v[56:59], v[136:139]\n\t" \
  "ds_read_b64_tr_b16 v[116:117], v161 offset:36864\n\t" \
  "v_mfma_f32_16x16x32_bf16 v[152:155], v[120:123], v[60:63], v[152:155]\n\t" \
  "ds_read_b64_tr_b16 v[118:119], v161 offset:38912\n\t" \
  "v_mfma_f32_16x16x32_bf16 v[140:143], v[124:127], v[56:59], v[140:143]\n\t" \
  "ds_read_b64_tr_b16 v[120:121], v162 offset:36864\n\t" \
  "v_mfma_f32_16x16x32_bf16 v[156:159], v[124:127], v[60:63], v[156:159]\n\t" \
  "ds_read_b64_tr_b16 v[122:123], v162 offset:38912\n\t" \
  "s_waitcnt vmcnt(18) lgkmcnt(10)\n\t" \
  "v_mfma_f32_16x16x32_bf16 v[128:131], v[96:99], v[64:67], v[128:131]\n\t" \
  "ds_read_b64_tr_b16 v[124:125], v163 offset:36864\n\t" \
  "buffer_load_dwordx4 v[48:51], v164, %[ob], s73 offen offset:0 sc1\n\t" \
  "v_mfma_f32_16x16x32_bf16 v[144:147], v[96:99], v[68:71], v[144:147]\n\t" \
  "ds_read_b64_tr_b16 v[126:127], v163 offset:38912\n\t" \
  "buffer_load_dwordx4 v[52:55], v164, %[ob], s73 offen offset:1024 sc1\n\t" \
  "v_mfma_f32_16x16x32_bf16 v[132:135], v[100:103], v[64:67], v[132:135]\n\t" \
  "ds_read_b64_tr_b16 v[96:97], v160 offset:40960\n\t" \
  "buffer_load_dwordx4 v[56:59], v164, %[ob], s73 offen offset:2048 sc1\n\t" \
  "v_mfma_f32_16x16x32_bf16 v[148:151], v[100:103], v[68:71], v[148:151]\n\t" \
  "ds_read_b64_tr_b16 v[98:99], v160 offset:43008\n\t" \
  "buffer_load_dwordx4 v[60:63], v164, %[ob], s73 offen offset:3072 sc1\n\t" \
  "s_waitcnt lgkmcnt(10)\n\t" \
  "v_mfma_f32_16x16x32_bf16 v[136:139], v[104:107], v[64:67], v[136:139]\n\t" \
  "ds_read_b64_tr_b16 v[100:101], v161 offset:40960\n\t" \
  "v_mfma_f32_16x16x32_bf16 v[152:155], v[104:107], v[68:71], v[152:155]\n\t" \
  "ds_read_b64_tr_b16 v[102:103], v161 offset:43008\n\t" \
  "v_mfma_f32_16x16x32_bf16 v[140:143], v[108:111], v[64:67], v[140:143]\n\t" \
  "ds_read_b64_tr_b16 v[104:105], v162 offset:40960\n\t" \
  "v_mfma_f32_16x16x32_bf16 v[156:159], v[108:111], v[68:71], v[156:159]\n\t" \
  "ds_read_b64_tr_b16 v[106:107], v162 offset:43008\n\t" \
  "s_waitcnt vmcnt(20) lgkmcnt(10)\n\t" \
  "v_mfma_f32_16x16x32_bf16 v[128:131], v[112:115], v[72:75], v[128:131]\n\t" \
  "ds_read_b64_tr_b16 v[108:109], v163 offset:40960\n\t" \
  "v_mfma_f32_16x16x32_bf16 v[144:147], v[112:115], v[76:79], v[144:147]\n\t" \
  "ds_read_b64_tr_b16 v[110:111], v163 offset:43008\n\t" \
  "v_mfma_f32_16x16x32_bf16 v[132:135], v[116:119], v[72:75], v[132:135]\n\t" \
  "ds_read_b64_tr_b16 v[112:113], v160 offset:45056\n\t" \
  "v_mfma_f32_16x16x32_bf16 v[148:151], v[116:119], v[76:79], v[148:151]\n\t" \
  "ds_read_b64_tr_b16 v[114:115], v160 offset:47104\n\t" \
  "s_waitcnt lgkmcnt(10)\n\t" \
  "v_mfma_f32_16x16x32_bf16 v[136:139], v[120:123], v[72:75], v[136:139]\n\t" \
  "ds_read_b64_tr_b16 v[116:117], v161 offset:45056\n\t" \
  "v_mfma_f32_16x16x32_bf16 v[152:155], v[120:123], v[76:79], v[152:155]\n\t" \
  "ds_read_b64_tr_b16 v[118:119], v161 offset:47104\n\t" \
  "v_mfma_f32_16x16x32_bf16 v[140:143], v[124:127], v[72:75], v[140:143]\n\t" \
  "ds_read_b64_tr_b16 v[120:121], v162 offset:45056\n\t" \
  "v_mfma_f32_16x16x32_bf16 v[156:159], v[124:127], v[76:79], v[156:159]\n\t" \
  "ds_read_b64_tr_b16 v[122:123], v162 offset:47104\n\t" \
  "s_waitcnt vmcnt(18) lgkmcnt(10)\n\t" \
  "v_mfma_f32_16x16x32_bf16 v[128:131], v[96:99], v[80:83], v[128:131]\n\t" \
  "ds_read_b64_tr_b16 v[124:125], v163 offset:45056\n\t" \
  "buffer_load_dwordx4 v[64:67], v164, %[ob], s74 offen offset:0 sc1\n\t" \
  "v_mfma_f32_16x16x32_bf16 v[144:147], v[96:99], v[84:87], v[144:147]\n\t" \
  "ds_read_b64_tr_b16 v[126:127], v163 offset:47104\n\t" \
  "buffer_load_dwordx4 v[68:71], v164, %[ob], s74 offen offset:1024 sc1\n\t" \
  "v_mfma_f32_16x16x32_bf16 v[132:135], v[100:103], v[80:83], v[132:135]\n\t" \
  "ds_read_b64_tr_b16 v[96:97], v160 offset:49152\n\t" \
  "buffer_load_dwordx4 v[72:75], v164, %[ob], s74 offen offset:2048 sc1\n\t" \
  "v_mfma_f32_16x16x32_bf16 v[148:151], v[100:103], v[84:87], v[148:151]\n\t" \
  "ds_read_b64_tr_b16 v[98:99], v160 offset:51200\n\t" \
  "buffer_load_dwordx4 v[76:79], v164, %[ob], s74 offen offset:3072 sc1\n\t" \
  "s_waitcnt lgkmcnt(10)\n\t" \
  "v_mfma_f32_16x16x32_bf16 v[136:139], v[104:107], v[80:83], v[136:139]\n\t" \
  "ds_read_b64_tr_b16 v[100:101], v161 offset:49152\n\t" \
  "v_mfma_f32_16x16x32_bf16 v[152:155], v[104:107], v[84:87], v[152:155]\n\t" \
  "ds_read_b64_tr_b16 v[102:103], v161 offset:51200\n\t" \
  "v_mfma_f32_16x16x32_bf16 v[140:143], v[108:111], v[80:83], v[140:143]\n\t" \
  "ds_read_b64_tr_b16 v[104:105], v162 offset:49152\n\t" \
  "v_mfma_f32_16x16x32_bf16 v[156:159], v[108:111], v[84:87], v[156:159]\n\t" \
  "ds_read_b64_tr_b16 v[106:107], v162 offset:51200\n\t" \
  "s_waitcnt vmcnt(20) lgkmcnt(10)\n\t" \
  "v_mfma_f32_16x16x32_bf16 v[128:131], v[112:115], v[88:91], v[128:131]\n\t" \
  "ds_read_b64_tr_b16 v[108:109], v163 offset:49152\n\t" \
  "v_mfma_f32_16x16x32_bf16 v[144:147], v[112:115], v[92:95], v[144:147]\n\t" \
  "ds_read_b64_tr_b16 v[110:111], v163 offset:51200\n\t" \
  "v_mfma_f32_16x16x32_bf16 v[132:135], v[116:119], v[88:91], v[132:135]\n\t" \
  "ds_read_b64_tr_b16 v[112:113], v160 offset:53248\n\t" \
  "s_mov_b64 exec, 1\n\t" \
  "ds_write_b32 v165, v165 offset:76\n\t" \
  "s_mov_b64 exec, -1\n\t" \
  "v_mfma_f32_16x16x32_bf16 v[148:151], v[116:119], v[92:95], v[148:151]\n\t" \
  "ds_read_b64_tr_b16 v[114:115], v160 offset:55296\n\t" \
  "s_waitcnt lgkmcnt(11)\n\t" \
  "v_mfma_f32_16x16x32_bf16 v[136:139], v[120:123], v[88:91], v[136:139]\n\t" \
  "ds_read_b64_tr_b16 v[116:117], v161 offset:53248\n\t" \
  "v_mfma_f32_16x16x32_bf16 v[152:155], v[120:123], v[92:95], v[152:155]\n\t" \
  "ds_read_b64_tr_b16 v[118:119], v161 offset:55296\n\t" \
  "v_mfma_f32_16x16x32_bf16 v[140:143], v[124:127], v[88:91], v[140:143]\n\t" \
  "ds_read_b64_tr_b16 v[120:121], v162 offset:53248\n\t" \
  "v_mfma_f32_16x16x32_bf16 v[156:159], v[124:127], v[92:95], v[156:159]\n\t" \
  "ds_read_b64_tr_b16 v[122:123], v162 offset:55296\n\t" \
  "s_waitcnt vmcnt(18) lgkmcnt(11)\n\t" \
  "v_mfma_f32_16x16x32_bf16 v[128:131], v[96:99], v[0:3], v[128:131]\n\t" \
  "ds_read_b64_tr_b16 v[124:125], v163 offset:53248\n\t" \
  "buffer_load_dwordx4 v[80:83], v164, %[ob], s75 offen offset:0 sc1\n\t" \
  "v_mfma_f32_16x16x32_bf16 v[144:147], v[96:99], v[4:7], v[144:147]\n\t" \
  "ds_read_b64_tr_b16 v[126:127], v163 offset:55296\n\t" \
  "buffer_load_dwordx4 v[84:87], v164, %[ob], s75 offen offset:1024 sc1\n\t" \
  "v_mfma_f32_16x16x32_bf16 v[132:135], v[100:103], v[0:3], v[132:135]\n\t" \
  "ds_read_b64_tr_b16 v[96:97], v160 offset:57344\n\t" \
  "buffer_load_dwordx4 v[88:91], v164, %[ob], s75 offen offset:2048 sc1\n\t" \
  "v_mfma_f32_16x16x32_bf16 v[148:151], v[100:103], v[4:7], v[148:151]\n\t" \
  "ds_read_b64_tr_b16 v[98:99], v160 offset:59392\n\t" \
  "buffer_load_dwordx4 v[92:95], v164, %[ob], s75 offen offset:3072 sc1\n\t" \
  "s_waitcnt lgkmcnt(11)\n\t" \
  "v_mfma_f32_16x16x32_bf16 v[136:139], v[104:107], v[0:3], v[136:139]\n\t" \
  "ds_read_b64_tr_b16 v[100:101], v161 offset:57344\n\t" \
  "v_mfma_f32_16x16x32_bf16 v[152:155], v[104:107], v[4:7], v[152:155]\n\t" \
  "ds_read_b64_tr_b16 v[102:103], v161 offset:59392\n\t" \
  "v_mfma_f32_16x16x32_bf16 v[140:143], v[108:111], v[0:3], v[140:143]\n\t" \
  "ds_read_b64_tr_b16 v[104:105], v162 offset:57344\n\t" \
  "v_mfma_f32_16x16x32_bf16 v[156:159], v[108:111], v[4:7], v[156:159]\n\t" \
  "ds_read_b64_tr_b16 v[106:107], v162 offset:59392\n\t" \
  "s_waitcnt vmcnt(20) lgkmcnt(10)\n\t" \
  "v_mfma_f32_16x16x32_bf16 v[128:131], v[112:115], v[8:11], v[128:131]\n\t" \
  "ds_read_b64_tr_b16 v[108:109], v163 offset:57344\n\t" \
  "v_mfma_f32_16x16x32_bf16 v[144:147], v[112:115], v[12:15], v[144:147]\n\t" \
  "ds_read_b64_tr_b16 v[110:111], v163 offset:59392\n\t" \
  "v_mfma_f32_16x16x32_bf16 v[132:135], v[116:119], v[8:11], v[132:135]\n\t" \
  "ds_read_b64_tr_b16 v[112:113], v160 offset:61440\n\t" \
  "v_mfma_f32_16x16x32_bf16 v[148:151], v[116:119], v[12:15], v[148:151]\n\t" \
  "ds_read_b64_tr_b16 v[114:115], v160 offset:63488\n\t" \
  "s_waitcnt lgkmcnt(10)\n\t" \
  "v_mfma_f32_16x16x32_bf16 v[136:139], v[120:123], v[8:11], v[136:139]\n\t" \
  "ds_read_b64_tr_b16 v[116:117], v161 offset:61440\n\t" \
  "v_mfma_f32_16x16x32_bf16 v[152:155], v[120:123], v[12:15], v[152:155]\n\t" \
  "ds_read_b64_tr_b16 v[118:119], v161 offset:63488\n\t" \
  "v_mfma_f32_16x16x32_bf16 v[140:143], v[124:127], v[8:11], v[140:143]\n\t" \
  "ds_read_b64_tr_b16 v[120:121], v162 offset:61440\n\t" \
  "v_mfma_f32_16x16x32_bf16 v[156:159], v[124:127], v[12:15], v[156:159]\n\t" \
  "ds_read_b64_tr_b16 v[122:123], v162 offset:63488\n\t" \
  "s_waitcnt vmcnt(18) lgkmcnt(10)\n\t" \
  "v_mfma_f32_16x16x32_bf16 v[128:131], v[96:99], v[16:19], v[128:131]\n\t" \
  "ds_read_b64_tr_b16 v[124:125], v163 offset:61440\n\t" \
  "buffer_load_dwordx4 v[0:3], v164, %[ob], s76 offen offset:0 sc1\n\t" \
  "v_mfma_f32_16x16x32_bf16 v[144:147], v[96:99], v[20:23], v[144:147]\n\t" \
  "ds_read_b64_tr_b16 v[126:127], v163 offset:63488\n\t" \
  "buffer_load_dwordx4 v[4:7], v164, %[ob], s76 offen offset:1024 sc1\n\t" \
  "v_mfma_f32_16x16x32_bf16 v[132:135], v[100:103], v[16:19], v[132:135]\n\t" \
  "v_add_u32 v160, 0x10000, v160\n\t" \
  "ds_read_b64_tr_b16 v[96:97], v160 offset:0\n\t" \
  "buffer_load_dwordx4 v[8:11], v164, %[ob], s76 offen offset:2048 sc1\n\t" \
  "v_mfma_f32_16x16x32_bf16 v[148:151], v[100:103], v[20:23], v[148:151]\n\t" \
  "ds_read_b64_tr_b16 v[98:99], v160 offset:2048\n\t" \
  "buffer_load_dwordx4 v[12:15], v164, %[ob], s76 offen offset:3072 sc1\n\t" \
  "s_waitcnt lgkmcnt(10)\n\t" \
  "v_mfma_f32_16x16x32_bf16 v[136:139], v[104:107], v[16:19], v[136:139]\n\t" \
  "v_add_u32 v161, 0x10000, v161\n\t" \
  "ds_read_b64_tr_b16 v[100:101], v161 offset:0\n\t" \
  "v_mfma_f32_16x16x32_bf16 v[152:155], v[104:107], v[20:23], v[152:155]\n\t" \
  "ds_read_b64_tr_b16 v[102:103], v161 offset:2048\n\t" \
  "v_mfma_f32_16x16x32_bf16 v[140:143], v[108:111], v[16:19], v[140:143]\n\t" \
  "v_add_u32 v162, 0x10000, v162\n\t" \
  "ds_read_b64_tr_b16 v[104:105], v162 offset:0\n\t" \
  "v_mfma_f32_16x16x32_bf16 v[156:159], v[108:111], v[20:23], v[156:159]\n\t" \
  "ds_read_b64_tr_b16 v[106:107], v162 offset:2048\n\t" \
  "s_waitcnt vmcnt(20) lgkmcnt(10)\n\t" \
  "v_mfma_f32_16x16x32_bf16 v[128:131], v[112:115], v[24:27], v[128:131]\n\t" \
  "v_add_u32 v163, 0x10000, v163\n\t" \
  "ds_read_b64_tr_b16 v[108:109], v163 offset:0\n\t" \
  "v_mfma_f32_16x16x32_bf16 v[144:147], v[112:115], v[28:31], v[144:147]\n\t" \
  "ds_read_b64_tr_b16 v[110:111], v163 offset:2048\n\t" \
  "v_mfma_f32_16x16x32_bf16 v[132:135], v[116:119], v[24:27], v[132:135]\n\t" \
  "ds_read_b64_tr_b16 v[112:113], v160 offset:4096\n\t" \
  "v_mfma_f32_16x16x32_bf16 v[148:151], v[116:119], v[28:31], v[148:151]\n\t" \
  "ds_read_b64_tr_b16 v[114:115], v160 offset:6144\n\t" \
  "s_waitcnt lgkmcnt(10)\n\t" \
  "v_mfma_f32_16x16x32_bf16 v[136:139], v[120:123], v[24:27], v[136:139]\n\t" \
  "ds_read_b64_tr_b16 v[116:117], v161 offset:4096\n\t" \
  "v_mfma_f32_16x16x32_bf16 v[152:155], v[120:123], v[28:31], v[152:155]\n\t" \
  "ds_read_b64_tr_b16 v[118:119], v161 offset:6144\n\t" \
  "v_mfma_f32_16x16x32_bf16 v[140:143], v[124:127], v[24:27], v[140:143]\n\t" \
  "ds_read_b64_tr_b16 v[120:121], v162 offset:4096\n\t" \
  "v_mfma_f32_16x16x32_bf16 v[156:159], v[124:127], v[28:31], v[156:159]\n\t" \
  "ds_read_b64_tr_b16 v[122:123], v162 offset:6144\n\t" \
  "s_waitcnt vmcnt(18) lgkmcnt(10)\n\t" \
  "v_mfma_f32_16x16x32_bf16 v[128:131], v[96:99], v[32:35], v[128:131]\n\t" \
  "ds_read_b64_tr_b16 v[124:125], v163 offset:4096\n\t" \
  "buffer_load_dwordx4 v[16:19], v164, %[ob], s77 offen offset:0 sc1\n\t" \
  "v_mfma_f32_16x16x32_bf16 v[144:147], v[96:99], v[36:39], v[144:147]\n\t" \
  "ds_read_b64_tr_b16 v[126:127], v163 offset:6144\n\t" \
  "buffer_load_dwordx4 v[20:23], v164, %[ob], s77 offen offset:1024 sc1\n\t" \
  "v_mfma_f32_16x16x32_bf16 v[132:135], v[100:103], v[32:35], v[132:135]\n\t" \
  "ds_read_b64_tr_b16 v[96:97], v160 offset:8192\n\t" \
  "buffer_load_dwordx4 v[24:27], v164, %[ob], s77 offen offset:2048 sc1\n\t" \
  "v_mfma_f32_16x16x32_bf16 v[148:151], v[100:103], v[36:39], v[148:151]\n\t" \
  "ds_read_b64_tr_b16 v[98:99], v160 offset:10240\n\t" \
  "buffer_load_dwordx4 v[28:31], v164, %[ob], s77 offen offset:3072 sc1\n\t" \
  "s_waitcnt lgkmcnt(10)\n\t" \
  "v_mfma_f32_16x16x32_bf16 v[136:139], v[104:107], v[32:35], v[136:139]\n\t" \
  "ds_read_b64_tr_b16 v[100:101], v161 offset:8192\n\t" \
  "ds_read_b32 v165, v165\n\t" \
  "v_mfma_f32_16x16x32_bf16 v[152:155], v[104:107], v[36:39], v[152:155]\n\t" \
  "ds_read_b64_tr_b16 v[102:103], v161 offset:10240\n\t" \
  "v_mfma_f32_16x16x32_bf16 v[140:143], v[108:111], v[32:35], v[140:143]\n\t" \
  "ds_read_b64_tr_b16 v[104:105], v162 offset:8192\n\t" \
  "v_mfma_f32_16x16x32_bf16 v[156:159], v[108:111], v[36:39], v[156:159]\n\t" \
  "ds_read_b64_tr_b16 v[106:107], v162 offset:10240\n\t" \
  "s_waitcnt vmcnt(20) lgkmcnt(11)\n\t" \
  "v_mfma_f32_16x16x32_bf16 v[128:131], v[112:115], v[40:43], v[128:131]\n\t" \
  "ds_read_b64_tr_b16 v[108:109], v163 offset:8192\n\t" \
  "v_mfma_f32_16x16x32_bf16 v[144:147], v[112:115], v[44:47], v[144:147]\n\t" \
  "ds_read_b64_tr_b16 v[110:111], v163 offset:10240\n\t" \
  "v_mfma_f32_16x16x32_bf16 v[132:135], v[116:119], v[40:43], v[132:135]\n\t" \
  "ds_read_b64_tr_b16 v[112:113], v160 offset:12288\n\t" \
  "v_mfma_f32_16x16x32_bf16 v[148:151], v[116:119], v[44:47], v[148:151]\n\t" \
  "ds_read_b64_tr_b16 v[114:115], v160 offset:14336\n\t" \
  "s_waitcnt lgkmcnt(11)\n\t" \
  "v_mfma_f32_16x16x32_bf16 v[136:139], v[120:123], v[40:43], v[136:139]\n\t" \
  "ds_read_b64_tr_b16 v[116:117], v161 offset:12288\n\t" \
  "v_mfma_f32_16x16x32_bf16 v[152:155], v[120:123], v[44:47], v[152:155]\n\t" \
  "ds_read_b64_tr_b16 v[118:119], v161 offset:14336\n\t" \
  "s_waitcnt lgkmcnt(9)\n\t" \
  "v_mfma_f32_16x16x32_bf16 v[140:143], v[124:127], v[40:43], v[140:143]\n\t" \
  "ds_read_b64_tr_b16 v[120:121], v162 offset:12288\n\t" \
  "v_cmp_lt_u32 vcc, 15, v165\n\t" \
  "v_mfma_f32_16x16x32_bf16 v[156:159], v[124:127], v[44:47], v[156:159]\n\t" \
  "ds_read_b64_tr_b16 v[122:123], v162 offset:14336\n\t" \
  "s_cbranch_vccz QA14_%=\n\t" \
  "QB14_%=:\n\t" \
  "s_waitcnt vmcnt(18) lgkmcnt(10)\n\t" \
  "v_mfma_f32_16x16x32_bf16 v[128:131], v[96:99], v[48:51], v[128:131]\n\t" \
  "ds_read_b64_tr_b16 v[124:125], v163 offset:12288\n\t" \
  "buffer_load_dwordx4 v[32:35], v164, %[ob], s78 offen offset:0 sc1\n\t" \
  "v_mfma_f32_16x16x32_bf16 v[144:147], v[96:99], v[52:55], v[144:147]\n\t" \
  "ds_read_b64_tr_b16 v[126:127], v163 offset:14336\n\t" \
  "buffer_load_dwordx4 v[36:39], v164, %[ob], s78 offen offset:1024 sc1\n\t" \
  "v_mfma_f32_16x16x32_bf16 v[132:135], v[100:103], v[48:51], v[132:135]\n\t" \
  "ds_read_b64_tr_b16 v[96:97], v160 offset:16384\n\t" \
  "buffer_load_dwordx4 v[40:43], v164, %[ob], s78 offen offset:2048 sc1\n\t" \
  "v_mfma_f32_16x16x32_bf16 v[148:151], v[100:103], v[52:55], v[148:151]\n\t" \
  "ds_read_b64_tr_b16 v[98:99], v160 offset:18432\n\t" \
  "buffer_load_dwordx4 v[44:47], v164, %[ob], s78 offen offset:3072 sc1\n\t" \
  "s_waitcnt lgkmcnt(10)\n\t" \
  "v_mfma_f32_16x16x32_bf16 v[136:139], v[104:107], v[48:51], v[136:139]\n\t" \
  "ds_read_b64_tr_b16 v[100:101], v161 offset:16384\n\t" \
  "v_mfma_f32_16x16x32_bf16 v[152:155], v[104:107], v[52:55], v[152:155]\n\t" \
  "ds_read_b64_tr_b16 v[102:103], v161 offset:18432\n\t" \
  "v_mfma_f32_16x16x32_bf16 v[140:143], v[108:111], v[48:51], v[140:143]\n\t" \
  "ds_read_b64_tr_b16 v[104:105], v162 offset:16384\n\t" \
  "v_mfma_f32_16x16x32_bf16 v[156:159], v[108:111], v[52:55], v[156:159]\n\t" \
  "ds_read_b64_tr_b16 v[106:107], v162 offset:18432\n\t" \
  "s_waitcnt vmcnt(20) lgkmcnt(10)\n\t" \
  "v_mfma_f32_16x16x32_bf16 v[128:131], v[112:115], v[56:59], v[128:131]\n\t" \
  "ds_read_b64_tr_b16 v[108:109], v163 offset:16384\n\t" \
  "v_mfma_f32_16x16x32_bf16 v[144:147], v[112:115], v[60:63], v[144:147]\n\t" \
  "ds_read_b64_tr_b16 v[110:111], v163 offset:18432\n\t" \
  "v_mfma_f32_16x16x32_bf16 v[132:135], v[116:119], v[56:59], v[132:135]\n\t" \
  "ds_read_b64_tr_b16 v[112:113], v160 offset:20480\n\t" \
  "v_mfma_f32_16x16x32_bf16 v[148:151], v[116:119], v[60:63], v[148:151]\n\t" \
  "ds_read_b64_tr_b16 v[114:115], v160 offset:22528\n\t" \
  "s_waitcnt lgkmcnt(10)\n\t" \
  "v_mfma_f32_16x16x32_bf16 v[136:139], v[120:123], v[56:59], v[136:139]\n\t" \
  "ds_read_b64_tr_b16 v[116:117], v161 offset:20480\n\t" \
  "v_mfma_f32_16x16x32_bf16 v[152:155], v[120:123], v[60:63], v[152:155]\n\t" \
  "ds_read_b64_tr_b16 v[118:119], v161 offset:22528\n\t" \
  "v_mfma_f32_16x16x32_bf16 v[140:143], v[124:127], v[56:59], v[140:143]\n\t" \
  "ds_read_b64_tr_b16 v[120:121], v162 offset:20480\n\t" \
  "v_mfma_f32_16x16x32_bf16 v[156:159], v[124:127], v[60:63], v[156:159]\n\t" \
  "ds_read_b64_tr_b16 v[122:123], v162 offset:22528\n\t" \
  "s_waitcnt vmcnt(18) lgkmcnt(10)\n\t" \
  "v_mfma_f32_16x16x32_bf16 v[128:131], v[96:99], v[64:67], v[128:131]\n\t" \
  "ds_read_b64_tr_b16 v[124:125], v163 offset:20480\n\t" \
  "buffer_load_dwordx4 v[48:51], v164, %[ob], s79 offen offset:0 sc1\n\t" \
  "v_mfma_f32_16x16x32_bf16 v[144:147], v[96:99], v[68:71], v[144:147]\n\t" \
  "ds_read_b64_tr_b16 v[126:127], v163 offset:22528\n\t" \
  "buffer_load_dwordx4 v[52:55], v164, %[ob], s79 offen offset:1024 sc1\n\t" \
  "v_mfma_f32_16x16x32_bf16 v[132:135], v[100:103], v[64:67], v[132:135]\n\t" \
  "ds_read_b64_tr_b16 v[96:97], v160 offset:24576\n\t" \
  "buffer_load_dwordx4 v[56:59], v164, %[ob], s79 offen offset:2048 sc1\n\t" \
  "v_mfma_f32_16x16x32_bf16 v[148:151], v[100:103], v[68:71], v[148:151]\n\t" \
  "ds_read_b64_tr_b16 v[98:99], v160 offset:26624\n\t" \
  "buffer_load_dwordx4 v[60:63], v164, %[ob], s79 offen offset:3072 sc1\n\t" \
  "s_waitcnt lgkmcnt(10)\n\t" \
  "v_mfma_f32_16x16x32_bf16 v[136:139], v[104:107], v[64:67], v[136:139]\n\t" \
  "ds_read_b64_tr_b16 v[100:101], v161 offset:24576\n\t" \
  "v_mfma_f32_16x16x32_bf16 v[152:155], v[104:107], v[68:71], v[152:155]\n\t" \
  "ds_read_b64_tr_b16 v[102:103], v161 offset:26624\n\t" \
  "v_mfma_f32_16x16x32_bf16 v[140:143], v[108:111], v[64:67], v[140:143]\n\t" \
  "ds_read_b64_tr_b16 v[104:105], v162 offset:24576\n\t" \
  "v_mfma_f32_16x16x32_bf16 v[156:159], v[108:111], v[68:71], v[156:159]\n\t" \
  "ds_read_b64_tr_b16 v[106:107], v162 offset:26624\n\t" \
  "s_waitcnt vmcnt(20) lgkmcnt(10)\n\t" \
  "v_mfma_f32_16x16x32_bf16 v[128:131], v[112:115], v[72:75], v[128:131]\n\t" \
  "ds_read_b64_tr_b16 v[108:109], v163 offset:24576\n\t" \
  "v_mfma_f32_16x16x32_bf16 v[144:147], v[112:115], v[76:79], v[144:147]\n\t" \
  "ds_read_b64_tr_b16 v[110:111], v163 offset:26624\n\t" \
  "v_mfma_f32_16x16x32_bf16 v[132:135], v[116:119], v[72:75], v[132:135]\n\t" \
  "ds_read_b64_tr_b16 v[112:113], v160 offset:28672\n\t" \
  "v_mfma_f32_16x16x32_bf16 v[148:151], v[116:119], v[76:79], v[148:151]\n\t" \
  "ds_read_b64_tr_b16 v[114:115], v160 offset:30720\n\t" \
  "s_waitcnt lgkmcnt(10)\n\t" \
  "v_mfma_f32_16x16x32_bf16 v[136:139], v[120:123], v[72:75], v[136:139]\n\t" \
  "ds_read_b64_tr_b16 v[116:117], v161 offset:28672\n\t" \
  "v_mfma_f32_16x16x32_bf16 v[152:155], v[120:123], v[76:79], v[152:155]\n\t" \
  "ds_read_b64_tr_b16 v[118:119], v161 offset:30720\n\t" \
  "v_mfma_f32_16x16x32_bf16 v[140:143], v[124:127], v[72:75], v[140:143]\n\t" \
  "ds_read_b64_tr_b16 v[120:121], v162 offset:28672\n\t" \
  "v_mfma_f32_16x16x32_bf16 v[156:159], v[124:127], v[76:79], v[156:159]\n\t" \
  "ds_read_b64_tr_b16 v[122:123], v162 offset:30720\n\t" \
  "s_waitcnt vmcnt(18) lgkmcnt(10)\n\t" \
  "v_mfma_f32_16x16x32_bf16 v[128:131], v[96:99], v[80:83], v[128:131]\n\t" \
  "ds_read_b64_tr_b16 v[124:125], v163 offset:28672\n\t" \
  "v_mfma_f32_16x16x32_bf16 v[144:147], v[96:99], v[84:87], v[144:147]\n\t" \
  "ds_read_b64_tr_b16 v[126:127], v163 offset:30720\n\t" \
  "v_mfma_f32_16x16x32_bf16 v[132:135], v[100:103], v[80:83], v[132:135]\n\t" \
  "ds_read_b64_tr_b16 v[96:97], v160 offset:32768\n\t" \
  "v_mfma_f32_16x16x32_bf16 v[148:151], v[100:103], v[84:87], v[148:151]\n\t" \
  "ds_read_b64_tr_b16 v[98:99], v160 offset:34816\n\t" \
  "s_waitcnt lgkmcnt(10)\n\t" \
  "v_mfma_f32_16x16x32_bf16 v[136:139], v[104:107], v[80:83], v[136:139]\n\t" \
  "ds_read_b64_tr_b16 v[100:101], v161 offset:32768\n\t" \
  "v_mfma_f32_16x16x32_bf16 v[152:155], v[104:107], v[84:87], v[152:155]\n\t" \
  "ds_read_b64_tr_b16 v[102:103], v161 offset:34816\n\t" \
  "v_mfma_f32_16x16x32_bf16 v[140:143], v[108:111], v[80:83], v[140:143]\n\t" \
  "ds_read_b64_tr_b16 v[104:105], v162 offset:32768\n\t" \
  "v_mfma_f32_16x16x32_bf16 v[156:159], v[108:111], v[84:87], v[156:159]\n\t" \
  "ds_read_b64_tr_b16 v[106:107], v162 offset:34816\n\t" \
  "s_waitcnt vmcnt(16) lgkmcnt(10)\n\t" \
  "v_mfma_f32_16x16x32_bf16 v[128:131], v[112:115], v[88:91], v[128:131]\n\t" \
  "ds_read_b64_tr_b16 v[108:109], v163 offset:32768\n\t" \
  "v_mfma_f32_16x16x32_bf16 v[144:147], v[112:115], v[92:95], v[144:147]\n\t" \
  "ds_read_b64_tr_b16 v[110:111], v163 offset:34816\n\t" \
  "v_mfma_f32_16x16x32_bf16 v[132:135], v[116:119], v[88:91], v[132:135]\n\t" \
  "ds_read_b64_tr_b16 v[112:113], v160 offset:36864\n\t" \
  "v_mfma_f32_16x16x32_bf16 v[148:151], v[116:119], v[92:95], v[148:151]\n\t" \
  "ds_read_b64_tr_b16 v[114:115], v160 offset:38912\n\t" \
  "s_waitcnt lgkmcnt(10)\n\t" \
  "v_mfma_f32_16x16x32_bf16 v[136:139], v[120:123], v[88:91], v[136:139]\n\t" \
  "ds_read_b64_tr_b16 v[116:117], v161 offset:36864\n\t" \
  "v_mfma_f32_16x16x32_bf16 v[152:155], v[120:123], v[92:95], v[152:155]\n\t" \
  "ds_read_b64_tr_b16 v[118:119], v161 offset:38912\n\t" \
  "v_mfma_f32_16x16x32_bf16 v[140:143], v[124:127], v[88:91], v[140:143]\n\t" \
  "ds_read_b64_tr_b16 v[120:121], v162 offset:36864\n\t" \
  "v_mfma_f32_16x16x32_bf16 v[156:159], v[124:127], v[92:95], v[156:159]\n\t" \
  "ds_read_b64_tr_b16 v[122:123], v162 offset:38912\n\t" \
  "s_waitcnt vmcnt(14) lgkmcnt(10)\n\t" \
  "v_mfma_f32_16x16x32_bf16 v[128:131], v[96:99], v[0:3], v[128:131]\n\t" \
  "ds_read_b64_tr_b16 v[124:125], v163 offset:36864\n\t" \
  "v_mfma_f32_16x16x32_bf16 v[144:147], v[96:99], v[4:7], v[144:147]\n\t" \
  "ds_read_b64_tr_b16 v[126:127], v163 offset:38912\n\t" \
  "v_mfma_f32_16x16x32_bf16 v[132:135], v[100:103], v[0:3], v[132:135]\n\t" \
  "ds_read_b64_tr_b16 v[96:97], v160 offset:40960\n\t" \
  "v_mfma_f32_16x16x32_bf16 v[148:151], v[100:103], v[4:7], v[148:151]\n\t" \
  "ds_read_b64_tr_b16 v[98:99], v160 offset:43008\n\t" \
  "s_waitcnt lgkmcnt(10)\n\t" \
  "v_mfma_f32_16x16x32_bf16 v[136:139], v[104:107], v[0:3], v[136:139]\n\t" \
  "ds_read_b64_tr_b16 v[100:101], v161 offset:40960\n\t" \
  "v_mfma_f32_16x16x32_bf16 v[152:155], v[104:107], v[4:7], v[152:155]\n\t" \
  "ds_read_b64_tr_b16 v[102:103], v161 offset:43008\n\t" \
  "v_mfma_f32_16x16x32_bf16 v[140:143], v[108:111], v[0:3], v[140:143]\n\t" \
  "ds_read_b64_tr_b16 v[104:105], v162 offset:40960\n\t" \
  "v_mfma_f32_16x16x32_bf16 v[156:159], v[108:111], v[4:7], v[156:159]\n\t" \
  "ds_read_b64_tr_b16 v[106:107], v162 offset:43008\n\t" \
  "s_waitcnt vmcnt(12) lgkmcnt(10)\n\t" \
  "v_mfma_f32_16x16x32_bf16 v[128:131], v[112:115], v[8:11], v[128:131]\n\t" \
  "ds_read_b64_tr_b16 v[108:109], v163 offset:40960\n\t" \
  "v_mfma_f32_16x16x32_bf16 v[144:147], v[112:115], v[12:15], v[144:147]\n\t" \
  "ds_read_b64_tr_b16 v[110:111], v163 offset:43008\n\t" \
  "v_mfma_f32_16x16x32_bf16 v[132:135], v[116:119], v[8:11], v[132:135]\n\t" \
  "ds_read_b64_tr_b16 v[112:113], v160 offset:45056\n\t" \
  "v_mfma_f32_16x16x32_bf16 v[148:151], v[116:119], v[12:15], v[148:151]\n\t" \
  "ds_read_b64_tr_b16 v[114:115], v160 offset:47104\n\t" \
  "s_waitcnt lgkmcnt(10)\n\t" \
  "v_mfma_f32_16x16x32_bf16 v[136:139], v[120:123], v[8:11], v[136:139]\n\t" \
  "ds_read_b64_tr_b16 v[116:117], v161 offset:45056\n\t" \
  "v_mfma_f32_16x16x32_bf16 v[152:155], v[120:123], v[12:15], v[152:155]\n\t" \
  "ds_read_b64_tr_b16 v[118:119], v161 offset:47104\n\t" \
  "v_mfma_f32_16x16x32_bf16 v[140:143], v[124:127], v[8:11], v[140:143]\n\t" \
  "ds_read_b64_tr_b16 v[120:121], v162 offset:45056\n\t" \
  "v_mfma_f32_16x16x32_bf16 v[156:159], v[124:127], v[12:15], v[156:159]\n\t" \
  "ds_read_b64_tr_b16 v[122:123], v162 offset:47104\n\t" \
  "s_waitcnt vmcnt(10) lgkmcnt(10)\n\t" \
  "v_mfma_f32_16x16x32_bf16 v[128:131], v[96:99], v[16:19], v[128:131]\n\t" \
  "ds_read_b64_tr_b16 v[124:125], v163 offset:45056\n\t" \
  "v_mfma_f32_16x16x32_bf16 v[144:147], v[96:99], v[20:23], v[144:147]\n\t" \
  "ds_read_b64_tr_b16 v[126:127], v163 offset:47104\n\t" \
  "v_mfma_f32_16x16x32_bf16 v[132:135], v[100:103], v[16:19], v[132:135]\n\t" \
  "ds_read_b64_tr_b16 v[96:97], v160 offset:49152\n\t" \
  "v_mfma_f32_16x16x32_bf16 v[148:151], v[100:103], v[20:23], v[148:151]\n\t" \
  "ds_read_b64_tr_b16 v[98:99], v160 offset:51200\n\t" \
  "s_waitcnt lgkmcnt(10)\n\t" \
  "v_mfma_f32_16x16x32_bf16 v[136:139], v[104:107], v[16:19], v[136:139]\n\t" \
  "ds_read_b64_tr_b16 v[100:101], v161 offset:49152\n\t" \
  "v_xor_b32 v0, 32, v167\n\t" \
  "v_mfma_f32_16x16x32_bf16 v[152:155], v[104:107], v[20:23], v[152:155]\n\t" \
  "ds_read_b64_tr_b16 v[102:103], v161 offset:51200\n\t" \
  "v_xor_b32 v1, 64, v167\n\t" \
  "v_mfma_f32_16x16x32_bf16 v[140:143], v[108:111], v[16:19], v[140:143]\n\t" \
  "ds_read_b64_tr_b16 v[104:105], v162 offset:49152\n\t" \
  "v_xor_b32 v2, 0x60, v167\n\t" \
  "v_mfma_f32_16x16x32_bf16 v[156:159], v[108:111], v[20:23], v[156:159]\n\t" \
  "ds_read_b64_tr_b16 v[106:107], v162 offset:51200\n\t" \
  "s_waitcnt vmcnt(8) lgkmcnt(10)\n\t" \
  "v_mfma_f32_16x16x32_bf16 v[128:131], v[112:115], v[24:27], v[128:131]\n\t" \
  "ds_read_b64_tr_b16 v[108:109], v163 offset:49152\n\t" \
  "v_bfe_u32 v5, v164, 7, 3\n\t" \
  "v_mfma_f32_16x16x32_bf16 v[144:147], v[112:115], v[28:31], v[144:147]\n\t" \
  "ds_read_b64_tr_b16 v[110:111], v163 offset:51200\n\t" \
  "v_and_b32 v4, 0x70, v164\n\t" \
  "v_mfma_f32_16x16x32_bf16 v[132:135], v[116:119], v[24:27], v[132:135]\n\t" \
  "ds_read_b64_tr_b16 v[112:113], v160 offset:53248\n\t" \
  "v_lshl_or_b32 v4, v5, 12, v4\n\t" \
  "v_mfma_f32_16x16x32_bf16 v[148:151], v[116:119], v[28:31], v[148:151]\n\t" \
  "ds_read_b64_tr_b16 v[114:115], v160 offset:55296\n\t" \
  "s_waitcnt lgkmcnt(10)\n\t" \
  "v_mfma_f32_16x16x32_bf16 v[136:139], v[120:123], v[24:27], v[136:139]\n\t" \
  "ds_read_b64_tr_b16 v[116:117], v161 offset:53248\n\t" \
  "v_add_u32 v5, 0x8000, v4\n\t" \
  "v_mfma_f32_16x16x32_bf16 v[152:155], v[120:123], v[28:31], v[152:155]\n\t" \
  "ds_read_b64_tr_b16 v[118:119], v161 offset:55296\n\t" \
  "v_add_u32 v6, 0x10000, v4\n\t" \
  "v_mfma_f32_16x16x32_bf16 v[140:143], v[124:127], v[24:27], v[140:143]\n\t" \
  "ds_read_b64_tr_b16 v[120:121], v162 offset:53248\n\t" \
  "v_add_u32 v7, 0x18000, v4\n\t" \
  "v_mfma_f32_16x16x32_bf16 v[156:159], v[124:127], v[28:31], v[156:159]\n\t" \
  "ds_read_b64_tr_b16 v[122:123], v162 offset:55296\n\t" \
  "s_waitcnt vmcnt(6) lgkmcnt(10)\n\t" \
  "v_mfma_f32_16x16x32_bf16 v[128:131], v[96:99], v[32:35], v[128:131]\n\t" \
  "ds_read_b64_tr_b16 v[124:125], v163 offset:53248\n\t" \
  "v_mfma_f32_16x16x32_bf16 v[144:147], v[96:99], v[36:39], v[144:147]\n\t" \
  "ds_read_b64_tr_b16 v[126:127], v163 offset:55296\n\t" \
  "v_mfma_f32_16x16x32_bf16 v[132:135], v[100:103], v[32:35], v[132:135]\n\t" \
  "ds_read_b64_tr_b16 v[96:97], v160 offset:57344\n\t" \
  "v_mfma_f32_16x16x32_bf16 v[148:151], v[100:103], v[36:39], v[148:151]\n\t" \
  "ds_read_b64_tr_b16 v[98:99], v160 offset:59392\n\t" \
  "s_waitcnt lgkmcnt(10)\n\t" \
  "v_mfma_f32_16x16x32_bf16 v[136:139], v[104:107], v[32:35], v[136:139]\n\t" \
  "ds_read_b64_tr_b16 v[100:101], v161 offset:57344\n\t" \
  "v_mfma_f32_16x16x32_bf16 v[152:155], v[104:107], v[36:39], v[152:155]\n\t" \
  "ds_read_b64_tr_b16 v[102:103], v161 offset:59392\n\t" \
  "v_mfma_f32_16x16x32_bf16 v[140:143], v[108:111], v[32:35], v[140:143]\n\t" \
  "ds_read_b64_tr_b16 v[104:105], v162 offset:57344\n\t" \
  "v_mfma_f32_16x16x32_bf16 v[156:159], v[108:111], v[36:39], v[156:159]\n\t" \
  "ds_read_b64_tr_b16 v[106:107], v162 offset:59392\n\t" \
  "s_waitcnt vmcnt(4) lgkmcnt(10)\n\t" \
  "v_mfma_f32_16x16x32_bf16 v[128:131], v[112:115], v[40:43], v[128:131]\n\t" \
  "ds_read_b64_tr_b16 v[108:109], v163 offset:57344\n\t" \
  "v_mfma_f32_16x16x32_bf16 v[144:147], v[112:115], v[44:47], v[144:147]\n\t" \
  "ds_read_b64_tr_b16 v[110:111], v163 offset:59392\n\t" \
  "v_mfma_f32_16x16x32_bf16 v[132:135], v[116:119], v[40:43], v[132:135]\n\t" \
  "ds_read_b64_tr_b16 v[112:113], v160 offset:61440\n\t" \
  "v_mfma_f32_16x16x32_bf16 v[148:151], v[116:119], v[44:47], v[148:151]\n\t" \
  "ds_read_b64_tr_b16 v[114:115], v160 offset:63488\n\t" \
  "s_waitcnt lgkmcnt(10)\n\t" \
  "v_mfma_f32_16x16x32_bf16 v[136:139], v[120:123], v[40:43], v[136:139]\n\t" \
  "ds_read_b64_tr_b16 v[116:117], v161 offset:61440\n\t" \
  "v_mfma_f32_16x16x32_bf16 v[152:155], v[120:123], v[44:47], v[152:155]\n\t" \
  "ds_read_b64_tr_b16 v[118:119], v161 offset:63488\n\t" \
  "v_mfma_f32_16x16x32_bf16 v[140:143], v[124:127], v[40:43], v[140:143]\n\t" \
  "ds_read_b64_tr_b16 v[120:121], v162 offset:61440\n\t" \
  "v_mfma_f32_16x16x32_bf16 v[156:159], v[124:127], v[44:47], v[156:159]\n\t" \
  "ds_read_b64_tr_b16 v[122:123], v162 offset:63488\n\t" \
  "s_waitcnt vmcnt(0) lgkmcnt(10)\n\t" \
  "v_mfma_f32_16x16x32_bf16 v[128:131], v[96:99], v[48:51], v[128:131]\n\t" \
  "ds_read_b64_tr_b16 v[124:125], v163 offset:61440\n\t" \
  "v_mfma_f32_16x16x32_bf16 v[132:135], v[100:103], v[48:51], v[132:135]\n\t" \
  "ds_read_b64_tr_b16 v[126:127], v163 offset:63488\n\t" \
  "s_waitcnt lgkmcnt(6)\n\t" \
  "v_mfma_f32_16x16x32_bf16 v[136:139], v[104:107], v[48:51], v[136:139]\n\t" \
  "v_mfma_f32_16x16x32_bf16 v[140:143], v[108:111], v[48:51], v[140:143]\n\t" \
  "v_mfma_f32_16x16x32_bf16 v[128:131], v[112:115], v[56:59], v[128:131]\n\t" \
  "s_waitcnt lgkmcnt(2)\n\t" \
  "v_mfma_f32_16x16x32_bf16 v[132:135], v[116:119], v[56:59], v[132:135]\n\t" \
  "v_mfma_f32_16x16x32_bf16 v[136:139], v[120:123], v[56:59], v[136:139]\n\t" \
  "v_mfma_f32_16x16x32_bf16 v[144:147], v[96:99], v[52:55], v[144:147]\n\t" \
  "s_waitcnt lgkmcnt(0)\n\t" \
  "v_mfma_f32_16x16x32_bf16 v[140:143], v[124:127], v[56:59], v[140:143]\n\t" \
  "v_mfma_f32_16x16x32_bf16 v[144:147], v[112:115], v[60:63], v[144:147]\n\t" \
  "s_nop 0\n\t" \
  "v_cvt_pk_bf16_f32 v16, v128, v129\n\t" \
  "v_mfma_f32_16x16x32_bf16 v[148:151], v[100:103], v[52:55], v[148:151]\n\t" \
  "v_cvt_pk_bf16_f32 v17, v130, v131\n\t" \
  "v_cvt_pk_bf16_f32 v18, v132, v133\n\t" \
  "v_mfma_f32_16x16x32_bf16 v[148:151], v[116:119], v[60:63], v[148:151]\n\t" \
  "v_cvt_pk_bf16_f32 v19, v134, v135\n\t" \
  "ds_write_b64 v167, v[16:17]\n\t" \
  "v_mfma_f32_16x16x32_bf16 v[152:155], v[104:107], v[52:55], v[152:155]\n\t" \
  "v_cvt_pk_bf16_f32 v20, v136, v137\n\t" \
  "ds_write_b64 v0, v[18:19]\n\t" \
  "v_mfma_f32_16x16x32_bf16 v[152:155], v[120:123], v[60:63], v[152:155]\n\t" \
  "v_cvt_pk_bf16_f32 v21, v138, v139\n\t" \
  "v_cvt_pk_bf16_f32 v22, v140, v141\n\t" \
  "v_mfma_f32_16x16x32_bf16 v[156:159], v[108:111], v[52:55], v[156:159]\n\t" \
  "v_cvt_pk_bf16_f32 v23, v142, v143\n\t" \
  "ds_write_b64 v1, v[20:21]\n\t" \
  "v_mfma_f32_16x16x32_bf16 v[156:159], v[124:127], v[60:63], v[156:159]\n\t" \
  "v_cvt_pk_bf16_f32 v24, v144, v145\n\t" \
  "ds_write_b64 v2, v[22:23]\n\t" \
  "v_cvt_pk_bf16_f32 v25, v146, v147\n\t" \
  "ds_read_b128 v[64:67], v166 offset:0\n\t" \
  "v_cvt_pk_bf16_f32 v26, v148, v149\n\t" \
  "ds_read_b128 v[68:71], v166 offset:1024\n\t" \
  "v_cvt_pk_bf16_f32 v27, v150, v151\n\t" \
  "ds_write_b64 v167, v[24:25]\n\t" \
  "v_cvt_pk_bf16_f32 v28, v152, v153\n\t" \
  "v_cvt_pk_bf16_f32 v29, v154, v155\n\t" \
  "ds_write_b64 v0, v[26:27]\n\t" \
  "v_cvt_pk_bf16_f32 v30, v156, v157\n\t" \
  "v_cvt_pk_bf16_f32 v31, v158, v159\n\t" \
  "ds_write_b64 v1, v[28:29]\n\t" \
  "ds_write_b64 v2, v[30:31]\n\t" \
  "ds_read_b128 v[72:75], v166 offset:0\n\t" \
  "ds_read_b128 v[76:79], v166 offset:1024\n\t" \
  "s_waitcnt lgkmcnt(7)\n\t" \
  "global_store_dwordx4 v4, v[64:67], %[op] sc1\n\t" \
  "s_waitcnt lgkmcnt(6)\n\t" \
  "global_store_dwordx4 v5, v[68:71], %[op] sc1\n\t" \
  "s_waitcnt lgkmcnt(1)\n\t" \
  "global_store_dwordx4 v6, v[72:75], %[op] sc1\n\t" \
  "s_waitcnt lgkmcnt(0)\n\t" \
  "global_store_dwordx4 v7, v[76:79], %[op] sc1\n\t" \
  "s_nop 1\n\t"
#define G3_EP 2
#define G3_CLOB "v0", "v1", "v2", "v3", "v4", "v5", "v6", "v7", "v8", "v9", "v10", "v11", "v12", "v13", "v14", "v15", "v16", "v17", "v18", "v19", "v20", "v21", "v22", "v23", "v24", "v25", "v26", "v27", "v28", "v29", "v30", "v31", "v32", "v33", "v34", "v35", "v36", "v37", "v38", "v39", "v40", "v41", "v42", "v43", "v44", "v45", "v46", "v47", "v48", "v49", "v50", "v51", "v52", "v53", "v54", "v55", "v56", "v57", "v58", "v59", "v60", "v61", "v62", "v63", "v64", "v65", "v66", "v67", "v68", "v69", "v70", "v71", "v72", "v73", "v74", "v75", "v76", "v77", "v78", "v79", "v80", "v81", "v82", "v83", "v84", "v85", "v86", "v87", "v88", "v89", "v90", "v91", "v92", "v93", "v94", "v95", "v97", "v98", "v99", "v100", "v101", "v102", "v103", "v104", "v105", "v106", "v107", "v108", "v109", "v110", "v111", "v112", "v113", "v114", "v115", "v116", "v117", "v118", "v119", "v120", "v121", "v122", "v123", "v124", "v125", "v126", "v127", "v128", "v129", "v130", "v131", "v132", "v133", "v134", "v135", "v136", "v137", "v138", "v139", "v140", "v141", "v142", "v143", "v144", "v145", "v146", "v147", "v148", "v149", "v150", "v151", "v152", "v153", "v154", "v155", "v156", "v157", "v158", "v159", "s64", "s65", "s66", "s67", "s68", "s69", "s70", "s71", "s72", "s73", "s74", "s75", "s76", "s77", "s78", "s79"

#define PP_ASM \
  "v_mov_b32 v60, 0\n\t" \
  "s_mov_b32 %[t0], %[n0]\n\t" \
  "s_cmp_eq_u32 %[t0], 0\n\t" \
  "s_cbranch_scc1 PAx_%=\n\t" \
  "PA_%=:\n\t" \
  "s_barrier\n\t" \
  "s_sub_u32 %[t0], %[t0], 1\n\t" \
  "s_cmp_lg_u32 %[t0], 0\n\t" \
  "s_cbranch_scc1 PA_%=\n\t" \
  "PAx_%=:\n\t" \
  "s_mov_b32 m0, %[shd]\n\t" \
  "global_load_dword v160, v60, %[fl] sc1\n\t" \
  "global_load_lds_dwordx4 v163, %[shp]\n\t" \
  "s_mov_b32 m0, %[tgd]\n\t" \
  "s_mov_b64 exec, %[em]\n\t" \
  "global_load_lds_dword v60, %[fl] sc1\n\t" \
  "s_mov_b64 exec, -1\n\t" \
  "global_load_ushort v161, v60, %[hp]\n\t" \
  "global_load_ushort v162, v60, %[dp]\n\t" \
  "global_load_dwordx4 v[0:3], v152, %[kb0]\n\t" \
  "global_load_dwordx4 v[4:7], v153, %[kb0]\n\t" \
  "global_load_dwordx4 v[8:11], v154, %[kb0]\n\t" \
  "global_load_dwordx4 v[12:15], v155, %[kb3] offset:0\n\t" \
  "global_load_dwordx4 v[28:31], v155, %[vb0] offset:0\n\t" \
  "s_setprio 2\n\t" \
  "s_setprio 3\n\t" \
  "s_barrier\n\t" \
  "global_load_dwordx4 v[16:19], v155, %[kb3] offset:2048\n\t" \
  "global_load_dwordx4 v[32:35], v155, %[vb0] offset:2048\n\t" \
  "global_load_dwordx4 v[20:23], v155, %[kb5] offset:0\n\t" \
  "s_setprio 2\n\t" \
  "s_waitcnt vmcnt(8)\n\t" \
  "ds_read_b128 v[128:131], v156 offset:0\n\t" \
  "ds_read_b128 v[132:135], v156 offset:16\n\t" \
  "ds_read_b128 v[136:139], v156 offset:32\n\t" \
  "ds_read_b128 v[140:143], v156 offset:48\n\t" \
  "s_waitcnt lgkmcnt(0)\n\t" \
  "v_lshlrev_b32 v64, 16, v128\n\t" \
  "v_and_b32 v65, 0xffff0000, v128\n\t" \
  "v_lshlrev_b32 v66, 16, v129\n\t" \
  "v_and_b32 v67, 0xffff0000, v129\n\t" \
  "v_lshlrev_b32 v68, 16, v130\n\t" \
  "v_and_b32 v69, 0xffff0000, v130\n\t" \
  "v_lshlrev_b32 v70, 16, v131\n\t" \
  "v_and_b32 v71, 0xffff0000, v131\n\t" \
  "v_lshlrev_b32 v72, 16, v132\n\t" \
  "v_and_b32 v73, 0xffff0000, v132\n\t" \
  "v_lshlrev_b32 v74, 16, v133\n\t" \
  "v_and_b32 v75, 0xffff0000, v133\n\t" \
  "v_lshlrev_b32 v76, 16, v134\n\t" \
  "v_and_b32 v77, 0xffff0000, v134\n\t" \
  "v_lshlrev_b32 v78, 16, v135\n\t" \
  "v_and_b32 v79, 0xffff0000, v135\n\t" \
  "v_lshlrev_b32 v80, 16, v136\n\t" \
  "v_and_b32 v81, 0xffff0000, v136\n\t" \
  "v_lshlrev_b32 v82, 16, v137\n\t" \
  "v_and_b32 v83, 0xffff0000, v137\n\t" \
  "v_lshlrev_b32 v84, 16, v138\n\t" \
  "v_and_b32 v85, 0xffff0000, v138\n\t" \
  "v_lshlrev_b32 v86, 16, v139\n\t" \
  "v_and_b32 v87, 0xffff0000, v139\n\t" \
  "v_lshlrev_b32 v88, 16, v140\n\t" \
  "v_and_b32 v89, 0xffff0000, v140\n\t" \
  "v_lshlrev_b32 v90, 16, v141\n\t" \
  "v_and_b32 v91, 0xffff0000, v141\n\t" \
  "v_lshlrev_b32 v92, 16, v142\n\t" \
  "v_and_b32 v93, 0xffff0000, v142\n\t" \
  "v_lshlrev_b32 v94, 16, v143\n\t" \
  "v_and_b32 v95, 0xffff0000, v143\n\t" \
  "s_waitcnt vmcnt(7)\n\t" \
  "s_mov_b64 exec, %[zm]\n\t" \
  "v_mov_b32 v0, 0\n\t" \
  "v_mov_b32 v1, 0\n\t" \
  "v_mov_b32 v2, 0\n\t" \
  "v_mov_b32 v3, 0\n\t" \
  "s_mov_b64 exec, -1\n\t" \
  "v_lshlrev_b32 v128, 16, v0\n\t" \
  "v_and_b32 v129, 0xffff0000, v0\n\t" \
  "v_lshlrev_b32 v130, 16, v1\n\t" \
  "v_and_b32 v131, 0xffff0000, v1\n\t" \
  "v_lshlrev_b32 v132, 16, v2\n\t" \
  "v_and_b32 v133, 0xffff0000, v2\n\t" \
  "v_lshlrev_b32 v134, 16, v3\n\t" \
  "v_and_b32 v135, 0xffff0000, v3\n\t" \
  "v_mul_f32 v96, v128, v67\n\t" \
  "v_mul_f32 v97, v129, v71\n\t" \
  "v_mul_f32 v98, v130, v75\n\t" \
  "v_mul_f32 v99, v131, v79\n\t" \
  "v_mul_f32 v100, v132, v83\n\t" \
  "v_mul_f32 v101, v133, v87\n\t" \
  "v_mul_f32 v102, v134, v91\n\t" \
  "v_mul_f32 v103, v135, v95\n\t" \
  "s_waitcnt vmcnt(6)\n\t" \
  "s_mov_b64 exec, %[zm]\n\t" \
  "v_mov_b32 v4, 0\n\t" \
  "v_mov_b32 v5, 0\n\t" \
  "v_mov_b32 v6, 0\n\t" \
  "v_mov_b32 v7, 0\n\t" \
  "s_mov_b64 exec, -1\n\t" \
  "v_lshlrev_b32 v128, 16, v4\n\t" \
  "v_and_b32 v129, 0xffff0000, v4\n\t" \
  "v_lshlrev_b32 v130, 16, v5\n\t" \
  "v_and_b32 v131, 0xffff0000, v5\n\t" \
  "v_lshlrev_b32 v132, 16, v6\n\t" \
  "v_and_b32 v133, 0xffff0000, v6\n\t" \
  "v_lshlrev_b32 v134, 16, v7\n\t" \
  "v_and_b32 v135, 0xffff0000, v7\n\t" \
  "v_fmac_f32 v96, v128, v66\n\t" \
  "v_fmac_f32 v97, v129, v70\n\t" \
  "v_fmac_f32 v98, v130, v74\n\t" \
  "v_fmac_f32 v99, v131, v78\n\t" \
  "v_fmac_f32 v100, v132, v82\n\t" \
  "v_fmac_f32 v101, v133, v86\n\t" \
  "v_fmac_f32 v102, v134, v90\n\t" \
  "v_fmac_f32 v103, v135, v94\n\t" \
  "v_mul_f32 v104, v128, v67\n\t" \
  "v_mul_f32 v105, v129, v71\n\t" \
  "v_mul_f32 v106, v130, v75\n\t" \
  "v_mul_f32 v107, v131, v79\n\t" \
  "v_mul_f32 v108, v132, v83\n\t" \
  "v_mul_f32 v109, v133, v87\n\t" \
  "v_mul_f32 v110, v134, v91\n\t" \
  "v_mul_f32 v111, v135, v95\n\t" \
  "s_waitcnt vmcnt(5)\n\t" \
  "s_mov_b64 exec, %[zm]\n\t" \
  "v_mov_b32 v8, 0\n\t" \
  "v_mov_b32 v9, 0\n\t" \
  "v_mov_b32 v10, 0\n\t" \
  "v_mov_b32 v11, 0\n\t" \
  "s_mov_b64 exec, -1\n\t" \
  "s_mov_b32 s85, 0x300000\n\t" \
  "s_load_dword s84, %[ph], s85\n\t" \
  "s_mov_b32 s85, 0x300800\n\t" \
  "s_load_dword s84, %[ph], s85\n\t" \
  "s_mov_b32 s85, 0x301000\n\t" \
  "s_load_dword s84, %[ph], s85\n\t" \
  "s_mov_b32 s85, 0x360000\n\t" \
  "s_load_dword s84, %[ph], s85\n\t" \
  "s_setprio 3\n\t" \
  "s_barrier\n\t" \
  "global_load_dwordx4 v[36:39], v155, %[vb2] offset:0\n\t" \
  "global_load_dwordx4 v[24:27], v155, %[kb5] offset:2048\n\t" \
  "global_load_dwordx4 v[40:43], v155, %[vb2] offset:2048\n\t" \
  "s_setprio 2\n\t" \
  "v_lshlrev_b32 v128, 16, v8\n\t" \
  "v_and_b32 v129, 0xffff0000, v8\n\t" \
  "v_lshlrev_b32 v130, 16, v9\n\t" \
  "v_and_b32 v131, 0xffff0000, v9\n\t" \
  "v_lshlrev_b32 v132, 16, v10\n\t" \
  "v_and_b32 v133, 0xffff0000, v10\n\t" \
  "v_lshlrev_b32 v134, 16, v11\n\t" \
  "v_and_b32 v135, 0xffff0000, v11\n\t" \
  "v_fmac_f32 v96, v128, v65\n\t" \
  "v_fmac_f32 v97, v129, v69\n\t" \
  "v_fmac_f32 v98, v130, v73\n\t" \
  "v_fmac_f32 v99, v131, v77\n\t" \
  "v_fmac_f32 v100, v132, v81\n\t" \
  "v_fmac_f32 v101, v133, v85\n\t" \
  "v_fmac_f32 v102, v134, v89\n\t" \
  "v_fmac_f32 v103, v135, v93\n\t" \
  "v_fmac_f32 v104, v128, v66\n\t" \
  "v_fmac_f32 v105, v129, v70\n\t" \
  "v_fmac_f32 v106, v130, v74\n\t" \
  "v_fmac_f32 v107, v131, v78\n\t" \
  "v_fmac_f32 v108, v132, v82\n\t" \
  "v_fmac_f32 v109, v133, v86\n\t" \
  "v_fmac_f32 v110, v134, v90\n\t" \
  "v_fmac_f32 v111, v135, v94\n\t" \
  "v_mul_f32 v112, v128, v67\n\t" \
  "v_mul_f32 v113, v129, v71\n\t" \
  "v_mul_f32 v114, v130, v75\n\t" \
  "v_mul_f32 v115, v131, v79\n\t" \
  "v_mul_f32 v116, v132, v83\n\t" \
  "v_mul_f32 v117, v133, v87\n\t" \
  "v_mul_f32 v118, v134, v91\n\t" \
  "v_mul_f32 v119, v135, v95\n\t" \
  "s_waitcnt vmcnt(7)\n\t" \
  "v_lshlrev_b32 v128, 16, v12\n\t" \
  "v_and_b32 v129, 0xffff0000, v12\n\t" \
  "v_lshlrev_b32 v130, 16, v13\n\t" \
  "v_and_b32 v131, 0xffff0000, v13\n\t" \
  "v_lshlrev_b32 v132, 16, v14\n\t" \
  "v_and_b32 v133, 0xffff0000, v14\n\t" \
  "v_lshlrev_b32 v134, 16, v15\n\t" \
  "v_and_b32 v135, 0xffff0000, v15\n\t" \
  "v_fmac_f32 v96, v128, v64\n\t" \
  "v_fmac_f32 v97, v129, v68\n\t" \
  "v_fmac_f32 v98, v130, v72\n\t" \
  "v_fmac_f32 v99, v131, v76\n\t" \
  "v_fmac_f32 v100, v132, v80\n\t" \
  "v_fmac_f32 v101, v133, v84\n\t" \
  "v_fmac_f32 v102, v134, v88\n\t" \
  "v_fmac_f32 v103, v135, v92\n\t" \
  "v_fmac_f32 v104, v128, v65\n\t" \
  "v_fmac_f32 v105, v129, v69\n\t" \
  "v_fmac_f32 v106, v130, v73\n\t" \
  "v_fmac_f32 v107, v131, v77\n\t" \
  "v_fmac_f32 v108, v132, v81\n\t" \
  "v_fmac_f32 v109, v133, v85\n\t" \
  "v_fmac_f32 v110, v134, v89\n\t" \
  "v_fmac_f32 v111, v135, v93\n\t" \
  "v_fmac_f32 v112, v128, v66\n\t" \
  "v_fmac_f32 v113, v129, v70\n\t" \
  "v_fmac_f32 v114, v130, v74\n\t" \
  "v_fmac_f32 v115, v131, v78\n\t" \
  "v_fmac_f32 v116, v132, v82\n\t" \
  "v_fmac_f32 v117, v133, v86\n\t" \
  "v_fmac_f32 v118, v134, v90\n\t" \
  "v_fmac_f32 v119, v135, v94\n\t" \
  "v_mul_f32 v120, v128, v67\n\t" \
  "v_mul_f32 v121, v129, v71\n\t" \
  "v_mul_f32 v122, v130, v75\n\t" \
  "v_mul_f32 v123, v131, v79\n\t" \
  "v_mul_f32 v124, v132, v83\n\t" \
  "v_mul_f32 v125, v133, v87\n\t" \
  "v_mul_f32 v126, v134, v91\n\t" \
  "v_mul_f32 v127, v135, v95\n\t" \
  "s_waitcnt vmcnt(6)\n\t" \
  "v_lshlrev_b32 v128, 16, v28\n\t" \
  "v_and_b32 v129, 0xffff0000, v28\n\t" \
  "v_lshlrev_b32 v130, 16, v29\n\t" \
  "v_and_b32 v131, 0xffff0000, v29\n\t" \
  "v_lshlrev_b32 v132, 16, v30\n\t" \
  "v_and_b32 v133, 0xffff0000, v30\n\t" \
  "v_lshlrev_b32 v134, 16, v31\n\t" \
  "v_and_b32 v135, 0xffff0000, v31\n\t" \
  "s_mov_b32 s85, 0x360800\n\t" \
  "s_load_dword s84, %[ph], s85\n\t" \
  "s_mov_b32 s85, 0x361000\n\t" \
  "s_load_dword s84, %[ph], s85\n\t" \
  "s_mov_b32 s85, 0x3c0000\n\t" \
  "s_load_dword s84, %[ph], s85\n\t" \
  "s_mov_b32 s85, 0x3c0800\n\t" \
  "s_load_dword s84, %[ph], s85\n\t" \
  "s_barrier\n\t" \
  "v_mul_f32 v96, v96, v128\n\t" \
  "v_mul_f32 v97, v97, v129\n\t" \
  "v_mul_f32 v98, v98, v130\n\t" \
  "v_mul_f32 v99, v99, v131\n\t" \
  "v_mul_f32 v100, v100, v132\n\t" \
  "v_mul_f32 v101, v101, v133\n\t" \
  "v_mul_f32 v102, v102, v134\n\t" \
  "v_mul_f32 v103, v103, v135\n\t" \
  "v_cvt_pk_bf16_f32 v136, v96, v97\n\t" \
  "v_cvt_pk_bf16_f32 v137, v98, v99\n\t" \
  "v_cvt_pk_bf16_f32 v138, v100, v101\n\t" \
  "v_cvt_pk_bf16_f32 v139, v102, v103\n\t" \
  "v_mov_b32_dpp v128, v136 row_ror:8 row_mask:0xf bank_mask:0xf\n\t" \
  "v_mov_b32_dpp v129, v137 row_ror:8 row_mask:0xf bank_mask:0xf\n\t" \
  "v_mov_b32_dpp v130, v138 row_ror:8 row_mask:0xf bank_mask:0xf\n\t" \
  "v_mov_b32_dpp v131, v139 row_ror:8 row_mask:0xf bank_mask:0xf\n\t" \
  "v_perm_b32 v136, v128, v136, v157\n\t" \
  "v_perm_b32 v137, v129, v137, v157\n\t" \
  "v_perm_b32 v138, v130, v138, v157\n\t" \
  "v_perm_b32 v139, v131, v139, v157\n\t" \
  "v_permlane16_swap_b32 v136, v137\n\t" \
  "s_nop 0\n\t" \
  "v_permlane16_swap_b32 v138, v139\n\t" \
  "s_nop 1\n\t" \
  "v_permlane32_swap_b32 v136, v138\n\t" \
  "v_permlane32_swap_b32 v137, v139\n\t" \
  "s_waitcnt vmcnt(5)\n\t" \
  "v_lshlrev_b32 v128, 16, v16\n\t" \
  "v_and_b32 v129, 0xffff0000, v16\n\t" \
  "v_lshlrev_b32 v130, 16, v17\n\t" \
  "v_and_b32 v131, 0xffff0000, v17\n\t" \
  "v_lshlrev_b32 v132, 16, v18\n\t" \
  "v_and_b32 v133, 0xffff0000, v18\n\t" \
  "v_lshlrev_b32 v134, 16, v19\n\t" \
  "v_and_b32 v135, 0xffff0000, v19\n\t" \
  "v_fmac_f32 v104, v128, v64\n\t" \
  "v_fmac_f32 v105, v129, v68\n\t" \
  "v_fmac_f32 v106, v130, v72\n\t" \
  "v_fmac_f32 v107, v131, v76\n\t" \
  "v_fmac_f32 v108, v132, v80\n\t" \
  "v_fmac_f32 v109, v133, v84\n\t" \
  "v_fmac_f32 v110, v134, v88\n\t" \
  "v_fmac_f32 v111, v135, v92\n\t" \
  "v_fmac_f32 v112, v128, v65\n\t" \
  "v_fmac_f32 v113, v129, v69\n\t" \
  "v_fmac_f32 v114, v130, v73\n\t" \
  "v_fmac_f32 v115, v131, v77\n\t" \
  "v_fmac_f32 v116, v132, v81\n\t" \
  "v_fmac_f32 v117, v133, v85\n\t" \
  "v_fmac_f32 v118, v134, v89\n\t" \
  "v_fmac_f32 v119, v135, v93\n\t" \
  "v_fmac_f32 v120, v128, v66\n\t" \
  "v_fmac_f32 v121, v129, v70\n\t" \
  "v_fmac_f32 v122, v130, v74\n\t" \
  "v_fmac_f32 v123, v131, v78\n\t" \
  "v_fmac_f32 v124, v132, v82\n\t" \
  "v_fmac_f32 v125, v133, v86\n\t" \
  "v_fmac_f32 v126, v134, v90\n\t" \
  "v_fmac_f32 v127, v135, v94\n\t" \
  "s_waitcnt vmcnt(4)\n\t" \
  "v_lshlrev_b32 v128, 16, v32\n\t" \
  "v_and_b32 v129, 0xffff0000, v32\n\t" \
  "v_lshlrev_b32 v130, 16, v33\n\t" \
  "v_and_b32 v131, 0xffff0000, v33\n\t" \
  "v_lshlrev_b32 v132, 16, v34\n\t" \
  "v_and_b32 v133, 0xffff0000, v34\n\t" \
  "v_lshlrev_b32 v134, 16, v35\n\t" \
  "v_and_b32 v135, 0xffff0000, v35\n\t" \
  "v_mul_f32 v104, v104, v128\n\t" \
  "v_mul_f32 v105, v105, v129\n\t" \
  "v_mul_f32 v106, v106, v130\n\t" \
  "v_mul_f32 v107, v107, v131\n\t" \
  "v_mul_f32 v108, v108, v132\n\t" \
  "v_mul_f32 v109, v109, v133\n\t" \
  "v_mul_f32 v110, v110, v134\n\t" \
  "v_mul_f32 v111, v111, v135\n\t" \
  "v_cvt_pk_bf16_f32 v140, v104, v105\n\t" \
  "v_cvt_pk_bf16_f32 v141, v106, v107\n\t" \
  "v_cvt_pk_bf16_f32 v142, v108, v109\n\t" \
  "v_cvt_pk_bf16_f32 v143, v110, v111\n\t" \
  "v_mov_b32_dpp v128, v140 row_ror:8 row_mask:0xf bank_mask:0xf\n\t" \
  "v_mov_b32_dpp v129, v141 row_ror:8 row_mask:0xf bank_mask:0xf\n\t" \
  "s_mov_b32 s85, 0x3c1000\n\t" \
  "s_load_dword s84, %[ph], s85\n\t" \
  "s_mov_b32 s85, 0x420000\n\t" \
  "s_load_dword s84, %[ph], s85\n\t" \
  "s_mov_b32 s85, 0x420800\n\t" \
  "s_load_dword s84, %[ph], s85\n\t" \
  "s_mov_b32 s85, 0x421000\n\t" \
  "s_load_dword s84, %[ph], s85\n\t" \
  "s_setprio 3\n\t" \
  "s_barrier\n\t" \
  "global_store_dwordx4 v159, v[136:139], %[sb] sc1\n\t" \
  "v_add_u32 v158, 0x80, v159\n\t" \
  "v_bfi_b32 v159, %[mk], v158, v159\n\t" \
  "s_setprio 2\n\t" \
  "v_mov_b32_dpp v130, v142 row_ror:8 row_mask:0xf bank_mask:0xf\n\t" \
  "v_mov_b32_dpp v131, v143 row_ror:8 row_mask:0xf bank_mask:0xf\n\t" \
  "v_perm_b32 v140, v128, v140, v157\n\t" \
  "v_perm_b32 v141, v129, v141, v157\n\t" \
  "v_perm_b32 v142, v130, v142, v157\n\t" \
  "v_perm_b32 v143, v131, v143, v157\n\t" \
  "v_permlane16_swap_b32 v140, v141\n\t" \
  "s_nop 0\n\t" \
  "v_permlane16_swap_b32 v142, v143\n\t" \
  "s_nop 1\n\t" \
  "v_permlane32_swap_b32 v140, v142\n\t" \
  "v_permlane32_swap_b32 v141, v143\n\t" \
  "s_waitcnt vmcnt(4)\n\t" \
  "v_lshlrev_b32 v128, 16, v20\n\t" \
  "v_and_b32 v129, 0xffff0000, v20\n\t" \
  "v_lshlrev_b32 v130, 16, v21\n\t" \
  "v_and_b32 v131, 0xffff0000, v21\n\t" \
  "v_lshlrev_b32 v132, 16, v22\n\t" \
  "v_and_b32 v133, 0xffff0000, v22\n\t" \
  "v_lshlrev_b32 v134, 16, v23\n\t" \
  "v_and_b32 v135, 0xffff0000, v23\n\t" \
  "v_fmac_f32 v112, v128, v64\n\t" \
  "v_fmac_f32 v113, v129, v68\n\t" \
  "v_fmac_f32 v114, v130, v72\n\t" \
  "v_fmac_f32 v115, v131, v76\n\t" \
  "v_fmac_f32 v116, v132, v80\n\t" \
  "v_fmac_f32 v117, v133, v84\n\t" \
  "v_fmac_f32 v118, v134, v88\n\t" \
  "v_fmac_f32 v119, v135, v92\n\t" \
  "v_fmac_f32 v120, v128, v65\n\t" \
  "v_fmac_f32 v121, v129, v69\n\t" \
  "v_fmac_f32 v122, v130, v73\n\t" \
  "v_fmac_f32 v123, v131, v77\n\t" \
  "v_fmac_f32 v124, v132, v81\n\t" \
  "v_fmac_f32 v125, v133, v85\n\t" \
  "v_fmac_f32 v126, v134, v89\n\t" \
  "v_fmac_f32 v127, v135, v93\n\t" \
  "s_waitcnt vmcnt(3)\n\t" \
  "v_lshlrev_b32 v128, 16, v36\n\t" \
  "v_and_b32 v129, 0xffff0000, v36\n\t" \
  "v_lshlrev_b32 v130, 16, v37\n\t" \
  "v_and_b32 v131, 0xffff0000, v37\n\t" \
  "v_lshlrev_b32 v132, 16, v38\n\t" \
  "v_and_b32 v133, 0xffff0000, v38\n\t" \
  "v_lshlrev_b32 v134, 16, v39\n\t" \
  "v_and_b32 v135, 0xffff0000, v39\n\t" \
  "v_mul_f32 v112, v112, v128\n\t" \
  "v_mul_f32 v113, v113, v129\n\t" \
  "v_mul_f32 v114, v114, v130\n\t" \
  "v_mul_f32 v115, v115, v131\n\t" \
  "v_mul_f32 v116, v116, v132\n\t" \
  "v_mul_f32 v117, v117, v133\n\t" \
  "v_mul_f32 v118, v118, v134\n\t" \
  "v_mul_f32 v119, v119, v135\n\t" \
  "v_cvt_pk_bf16_f32 v144, v112, v113\n\t" \
  "v_cvt_pk_bf16_f32 v145, v114, v115\n\t" \
  "v_cvt_pk_bf16_f32 v146, v116, v117\n\t" \
  "v_cvt_pk_bf16_f32 v147, v118, v119\n\t" \
  "v_mov_b32_dpp v128, v144 row_ror:8 row_mask:0xf bank_mask:0xf\n\t" \
  "v_mov_b32_dpp v129, v145 row_ror:8 row_mask:0xf bank_mask:0xf\n\t" \
  "v_mov_b32_dpp v130, v146 row_ror:8 row_mask:0xf bank_mask:0xf\n\t" \
  "v_mov_b32_dpp v131, v147 row_ror:8 row_mask:0xf bank_mask:0xf\n\t" \
  "v_perm_b32 v144, v128, v144, v157\n\t" \
  "v_perm_b32 v145, v129, v145, v157\n\t" \
  "v_perm_b32 v146, v130, v146, v157\n\t" \
  "v_perm_b32 v147, v131, v147, v157\n\t" \
  "v_permlane16_swap_b32 v144, v145\n\t" \
  "s_nop 0\n\t" \
  "v_permlane16_swap_b32 v146, v147\n\t" \
  "s_nop 1\n\t" \
  "v_permlane32_swap_b32 v144, v146\n\t" \
  "v_permlane32_swap_b32 v145, v147\n\t" \
  "s_waitcnt vmcnt(2)\n\t" \
  "v_lshlrev_b32 v128, 16, v24\n\t" \
  "v_and_b32 v129, 0xffff0000, v24\n\t" \
  "v_lshlrev_b32 v130, 16, v25\n\t" \
  "v_and_b32 v131, 0xffff0000, v25\n\t" \
  "v_lshlrev_b32 v132, 16, v26\n\t" \
  "v_and_b32 v133, 0xffff0000, v26\n\t" \
  "v_lshlrev_b32 v134, 16, v27\n\t" \
  "v_and_b32 v135, 0xffff0000, v27\n\t" \
  "v_fmac_f32 v120, v128, v64\n\t" \
  "v_fmac_f32 v121, v129, v68\n\t" \
  "s_mov_b32 s85, 0x480000\n\t" \
  "s_load_dword s84, %[ph], s85\n\t" \
  "s_mov_b32 s85, 0x480800\n\t" \
  "s_load_dword s84, %[ph], s85\n\t" \
  "s_mov_b32 s85, 0x481000\n\t" \
  "s_load_dword s84, %[ph], s85\n\t" \
  "s_mov_b32 s85, 0x4e0000\n\t" \
  "s_load_dword s84, %[ph], s85\n\t" \
  "s_setprio 3\n\t" \
  "s_barrier\n\t" \
  "global_store_dwordx4 v159, v[140:143], %[sb] sc1\n\t" \
  "v_add_u32 v158, 0x80, v159\n\t" \
  "v_bfi_b32 v159, %[mk], v158, v159\n\t" \
  "global_store_dwordx4 v159, v[144:147], %[sb] sc1\n\t" \
  "v_add_u32 v158, 0x80, v159\n\t" \
  "v_bfi_b32 v159, %[mk], v158, v159\n\t" \
  "s_mov_b32 m0, %[hd]\n\t" \
  "s_nop 0\n\t" \
  "global_load_lds_dwordx4 v163, %[hp] offset:0\n\t" \
  "global_load_lds_dwordx4 v163, %[hp] offset:1024\n\t" \
  "global_load_lds_dwordx4 v163, %[hp] offset:2048\n\t" \
  "global_load_lds_dwordx4 v163, %[hp] offset:3072\n\t" \
  "s_setprio 2\n\t" \
  "v_fmac_f32 v122, v130, v72\n\t" \
  "v_fmac_f32 v123, v131, v76\n\t" \
  "v_fmac_f32 v124, v132, v80\n\t" \
  "v_fmac_f32 v125, v133, v84\n\t" \
  "v_fmac_f32 v126, v134, v88\n\t" \
  "v_fmac_f32 v127, v135, v92\n\t" \
  "s_waitcnt vmcnt(7)\n\t" \
  "v_lshlrev_b32 v128, 16, v40\n\t" \
  "v_and_b32 v129, 0xffff0000, v40\n\t" \
  "v_lshlrev_b32 v130, 16, v41\n\t" \
  "v_and_b32 v131, 0xffff0000, v41\n\t" \
  "v_lshlrev_b32 v132, 16, v42\n\t" \
  "v_and_b32 v133, 0xffff0000, v42\n\t" \
  "v_lshlrev_b32 v134, 16, v43\n\t" \
  "v_and_b32 v135, 0xffff0000, v43\n\t" \
  "v_mul_f32 v120, v120, v128\n\t" \
  "v_mul_f32 v121, v121, v129\n\t" \
  "v_mul_f32 v122, v122, v130\n\t" \
  "v_mul_f32 v123, v123, v131\n\t" \
  "v_mul_f32 v124, v124, v132\n\t" \
  "v_mul_f32 v125, v125, v133\n\t" \
  "v_mul_f32 v126, v126, v134\n\t" \
  "v_mul_f32 v127, v127, v135\n\t" \
  "v_cvt_pk_bf16_f32 v148, v120, v121\n\t" \
  "v_cvt_pk_bf16_f32 v149, v122, v123\n\t" \
  "v_cvt_pk_bf16_f32 v150, v124, v125\n\t" \
  "v_cvt_pk_bf16_f32 v151, v126, v127\n\t" \
  "v_mov_b32_dpp v128, v148 row_ror:8 row_mask:0xf bank_mask:0xf\n\t" \
  "v_mov_b32_dpp v129, v149 row_ror:8 row_mask:0xf bank_mask:0xf\n\t" \
  "v_mov_b32_dpp v130, v150 row_ror:8 row_mask:0xf bank_mask:0xf\n\t" \
  "v_mov_b32_dpp v131, v151 row_ror:8 row_mask:0xf bank_mask:0xf\n\t" \
  "v_perm_b32 v148, v128, v148, v157\n\t" \
  "v_perm_b32 v149, v129, v149, v157\n\t" \
  "v_perm_b32 v150, v130, v150, v157\n\t" \
  "v_perm_b32 v151, v131, v151, v157\n\t" \
  "v_permlane16_swap_b32 v148, v149\n\t" \
  "s_nop 0\n\t" \
  "v_permlane16_swap_b32 v150, v151\n\t" \
  "s_nop 1\n\t" \
  "v_permlane32_swap_b32 v148, v150\n\t" \
  "v_permlane32_swap_b32 v149, v151\n\t" \
  "s_mov_b32 s85, 0x4e0800\n\t" \
  "s_load_dword s84, %[ph], s85\n\t" \
  "s_mov_b32 s85, 0x4e1000\n\t" \
  "s_load_dword s84, %[ph], s85\n\t" \
  "s_mov_b32 s85, 0x540000\n\t" \
  "s_load_dword s84, %[ph], s85\n\t" \
  "s_mov_b32 s85, 0x540800\n\t" \
  "s_load_dword s84, %[ph], s85\n\t" \
  "s_setprio 3\n\t" \
  "s_barrier\n\t" \
  "global_store_dwordx4 v159, v[148:151], %[sb] sc1\n\t" \
  "s_setprio 2\n\t" \
  "s_waitcnt vmcnt(1)\n\t" \
  "ds_read_b64_tr_b16 v[64:65], v165 offset:32\n\t" \
  "ds_read_b64_tr_b16 v[66:67], v165 offset:0\n\t" \
  "ds_read_b64_tr_b16 v[68:69], v165 offset:288\n\t" \
  "ds_read_b64_tr_b16 v[70:71], v165 offset:256\n\t" \
  "ds_read_b64_tr_b16 v[72:73], v165 offset:544\n\t" \
  "ds_read_b64_tr_b16 v[74:75], v165 offset:512\n\t" \
  "ds_read_b64_tr_b16 v[76:77], v165 offset:800\n\t" \
  "ds_read_b64_tr_b16 v[78:79], v165 offset:768\n\t" \
  "ds_read_b64_tr_b16 v[80:81], v165 offset:1056\n\t" \
  "ds_read_b64_tr_b16 v[82:83], v165 offset:1024\n\t" \
  "ds_read_b64_tr_b16 v[84:85], v165 offset:1312\n\t" \
  "ds_read_b64_tr_b16 v[86:87], v165 offset:1280\n\t" \
  "ds_read_b64_tr_b16 v[88:89], v165 offset:1568\n\t" \
  "ds_read_b64_tr_b16 v[90:91], v165 offset:1536\n\t" \
  "ds_read_b64_tr_b16 v[92:93], v165 offset:1824\n\t" \
  "ds_read_b64_tr_b16 v[94:95], v165 offset:1792\n\t" \
  "v_mov_b32 v24, v166\n\t" \
  "v_mov_b32 v25, v167\n\t" \
  "v_mov_b32 v63, 0xc0\n\t" \
  "v_add_u32 v158, 64, v24\n\t" \
  "v_bfi_b32 v26, v63, v158, v24\n\t" \
  "v_add_u32 v158, 64, v25\n\t" \
  "v_bfi_b32 v27, v63, v158, v25\n\t" \
  "v_add_u32 v158, 64, v26\n\t" \
  "v_bfi_b32 v28, v63, v158, v26\n\t" \
  "v_add_u32 v158, 64, v27\n\t" \
  "v_bfi_b32 v29, v63, v158, v27\n\t" \
  "v_add_u32 v158, 64, v28\n\t" \
  "v_bfi_b32 v30, v63, v158, v28\n\t" \
  "v_add_u32 v158, 64, v29\n\t" \
  "v_bfi_b32 v31, v63, v158, v29\n\t" \
  "s_mov_b32 s85, 0x541000\n\t" \
  "s_load_dword s84, %[ph], s85\n\t" \
  "s_mov_b32 s85, 0x5a0000\n\t" \
  "s_load_dword s84, %[ph], s85\n\t" \
  "s_mov_b32 s85, 0x5a0800\n\t" \
  "s_load_dword s84, %[ph], s85\n\t" \
  "s_mov_b32 s85, 0x5a1000\n\t" \
  "s_load_dword s84, %[ph], s85\n\t" \
  "s_setprio 3\n\t" \
  "s_barrier\n\t" \
  "s_waitcnt vmcnt(0)\n\t" \
  "v_add_u32 v61, 1, v160\n\t" \
  "s_mov_b64 exec, 1\n\t" \
  "global_store_dword v60, v61, %[ff] sc1\n\t" \
  "s_mov_b64 exec, -1\n\t" \
  "s_setprio 2\n\t" \
  "ds_read_b64_tr_b16 v[96:97], v165 offset:2080\n\t" \
  "ds_read_b64_tr_b16 v[98:99], v165 offset:2048\n\t" \
  "ds_read_b64_tr_b16 v[100:101], v165 offset:2336\n\t" \
  "ds_read_b64_tr_b16 v[102:103], v165 offset:2304\n\t" \
  "ds_read_b64_tr_b16 v[104:105], v165 offset:2592\n\t" \
  "ds_read_b64_tr_b16 v[106:107], v165 offset:2560\n\t" \
  "ds_read_b64_tr_b16 v[108:109], v165 offset:2848\n\t" \
  "ds_read_b64_tr_b16 v[110:111], v165 offset:2816\n\t" \
  "ds_read_b64_tr_b16 v[112:113], v165 offset:3104\n\t" \
  "ds_read_b64_tr_b16 v[114:115], v165 offset:3072\n\t" \
  "ds_read_b64_tr_b16 v[116:117], v165 offset:3360\n\t" \
  "ds_read_b64_tr_b16 v[118:119], v165 offset:3328\n\t" \
  "ds_read_b64_tr_b16 v[120:121], v165 offset:3616\n\t" \
  "ds_read_b64_tr_b16 v[122:123], v165 offset:3584\n\t" \
  "ds_read_b64_tr_b16 v[124:125], v165 offset:3872\n\t" \
  "ds_read_b64_tr_b16 v[126:127], v165 offset:3840\n\t" \
  "s_barrier\n\t" \
  "s_waitcnt lgkmcnt(0)\n\t" \
  "v_lshlrev_b32 v61, 16, v161\n\t" \
  "v_lshlrev_b32 v62, 16, v162\n\t" \
  "v_add_f32 v61, v61, v62\n\t" \
  "v_mov_b32 v62, 0xffff\n\t" \
  "v_cvt_pk_bf16_f32 v61, v61, v61\n\t" \
  "s_mov_b64 exec, 1\n\t" \
  "v_bfi_b32 v66, v62, v61, v66\n\t" \
  "s_mov_b64 exec, 15\n\t" \
  "v_and_b32 v66, v62, v66\n\t" \
  "s_mov_b64 exec, 16\n\t" \
  "v_bfi_b32 v66, v62, v66, v61\n\t" \
  "s_mov_b64 exec, 0xff\n\t" \
  "v_mov_b32 v67, 0\n\t" \
  "s_mov_b64 exec, 0x100\n\t" \
  "v_mov_b32 v67, v61\n\t" \
  "s_mov_b64 exec, 0xf00\n\t" \
  "v_and_b32 v67, v62, v67\n\t" \
  "s_mov_b64 exec, 0x1000\n\t" \
  "v_bfi_b32 v67, v62, v67, v61\n\t" \
  "s_mov_b64 exec, -1\n\t" \
  "s_waitcnt lgkmcnt(0)\n\t"
#define PP_NB 8
#define PP_Q 0
#define PP_CLOB "v0", "v1", "v2", "v3", "v4", "v5", "v6", "v7", "v8", "v9", "v10", "v11", "v12", "v13", "v14", "v15", "v16", "v17", "v18", "v19", "v20", "v21", "v22", "v23", "v32", "v33", "v34", "v35", "v36", "v37", "v38", "v39", "v40", "v41", "v42", "v43", "v60", "v61", "v62", "v63", "v128", "v129", "v130", "v131", "v132", "v133", "v134", "v135", "v136", "v137", "v138", "v139", "v140", "v141", "v142", "v143", "v144", "v145", "v146", "v147", "v148", "v149", "v150", "v151", "v158", "v161", "v162", "s84", "s85"

#define LD_ASM1 \
  "s_add_u32 m0, s39, 49152\n\t" \
  "s_mov_b32 s68, 0x0\n\t" \
  "buffer_load_dwordx4 v5, s[44:47], s68 offen offset:0 lds\n\t" \
  "buffer_load_dwordx4 v6, s[44:47], s68 offen offset:1024 lds\n\t" \
  "buffer_load_dwordx4 v7, s[44:47], s68 offen offset:2048 lds\n\t" \
  "s_add_u32 m0, s38, 36864\n\t" \
  "s_mov_b32 s68, 0x0\n\t" \
  "buffer_load_dwordx4 v0, s[40:43], s68 offen offset:0 lds\n\t" \
  "buffer_load_dwordx4 v1, s[40:43], s68 offen offset:1024 lds\n\t" \
  "s_mov_b64 exec, 0xffff\n\t" \
  "buffer_load_dwordx4 v4, s[40:43], s68 offen offset:2048 lds\n\t" \
  "s_mov_b64 exec, -1\n\t" \
  "s_add_u32 m0, s38, 46080\n\t" \
  "s_mov_b32 s68, 0x24000\n\t" \
  "buffer_load_dwordx4 v2, s[40:43], s68 offen offset:0 lds\n\t" \
  "buffer_load_dwordx4 v3, s[40:43], s68 offen offset:1024 lds\n\t" \
  "s_mov_b64 exec, 0xffff\n\t" \
  "buffer_load_dwordx4 v4, s[40:43], s68 offen offset:2048 lds\n\t" \
  "s_mov_b64 exec, -1\n\t" \
  "s_add_u32 m0, s39, 61440\n\t" \
  "s_mov_b32 s68, 0x30000\n\t" \
  "buffer_load_dwordx4 v5, s[44:47], s68 offen offset:0 lds\n\t" \
  "buffer_load_dwordx4 v6, s[44:47], s68 offen offset:1024 lds\n\t" \
  "buffer_load_dwordx4 v7, s[44:47], s68 offen offset:2048 lds\n\t" \
  "s_mov_b32 m0, %[sk]\n\t" \
  "s_mov_b32 exec_hi, 0\n\t" \
  "buffer_load_dword v9, s[40:43], 0 offen lds\n\t" \
  "s_mov_b32 exec_lo, %[wm]\n\t" \
  "buffer_load_dword v10, s[44:47], 0 offen lds\n\t" \
  "s_mov_b64 exec, -1\n\t"
#define LD_ASM2 \
  "s_add_u32 m0, s38, 0\n\t" \
  "s_mov_b32 s68, 0x80\n\t" \
  "buffer_load_dwordx4 v0, s[40:43], s68 offen offset:0 lds\n\t" \
  "s_mov_b32 s85, 0x180000\n\t" \
  "s_load_dword s84, %[pw], s85\n\t" \
  "buffer_load_dwordx4 v1, s[40:43], s68 offen offset:1024 lds\n\t" \
  "s_mov_b32 s85, 0x180800\n\t" \
  "s_load_dword s84, %[pw], s85\n\t" \
  "s_mov_b64 exec, 0xffff\n\t" \
  "buffer_load_dwordx4 v4, s[40:43], s68 offen offset:2048 lds\n\t" \
  "s_mov_b64 exec, -1\n\t" \
  "s_add_u32 m0, s38, 9216\n\t" \
  "s_mov_b32 s68, 0x24080\n\t" \
  "buffer_load_dwordx4 v2, s[40:43], s68 offen offset:0 lds\n\t" \
  "s_mov_b32 s85, 0x181000\n\t" \
  "s_load_dword s84, %[pw], s85\n\t" \
  "buffer_load_dwordx4 v3, s[40:43], s68 offen offset:1024 lds\n\t" \
  "s_mov_b32 s85, 0x1b0000\n\t" \
  "s_load_dword s84, %[pw], s85\n\t" \
  "s_mov_b64 exec, 0xffff\n\t" \
  "buffer_load_dwordx4 v4, s[40:43], s68 offen offset:2048 lds\n\t" \
  "s_mov_b64 exec, -1\n\t" \
  "s_mov_b32 s85, 0x1b0800\n\t" \
  "s_load_dword s84, %[pw], s85\n\t" \
  "s_mov_b32 s85, 0x1b1000\n\t" \
  "s_load_dword s84, %[pw], s85\n\t" \
  "s_mov_b32 s85, 0x1e0000\n\t" \
  "s_load_dword s84, %[pw], s85\n\t" \
  "s_mov_b32 s85, 0x1e0800\n\t" \
  "s_load_dword s84, %[pw], s85\n\t" \
  "s_mov_b32 m0, %[tsk]\n\t" \
  "s_mov_b32 s85, 0x0\n\t" \
  "s_mov_b64 exec, %[hm]\n\t" \
  "buffer_load_dword v16, %[sh], s85 offen lds\n\t" \
  "s_mov_b64 exec, -1\n\t" \
  "s_mov_b32 s68, 0x60000\n\t" \
  "s_mov_b32 s69, 0x90000\n\t" \
  "s_mov_b32 s70, 0x100\n\t" \
  "s_mov_b32 s71, 0x24100\n\t" \
  "s_mov_b32 s72, 0xc0000\n\t" \
  "s_mov_b32 s73, 0xf0000\n\t" \
  "s_add_u32 m0, s39, 0\n\t" \
  "s_waitcnt vmcnt(9)\n\t" \
  "s_barrier\n\t" \
  "buffer_load_dwordx4 v5, s[44:47], s68 offen offset:0 lds\n\t" \
  "s_mov_b32 s85, 0x1e1000\n\t" \
  "s_load_dword s84, %[pw], s85\n\t" \
  "buffer_load_dwordx4 v6, s[44:47], s68 offen offset:1024 lds\n\t" \
  "s_mov_b32 s85, 0x210000\n\t" \
  "s_load_dword s84, %[pw], s85\n\t" \
  "buffer_load_dwordx4 v7, s[44:47], s68 offen offset:2048 lds\n\t" \
  "s_mov_b32 s85, 0x210800\n\t" \
  "s_load_dword s84, %[pw], s85\n\t" \
  "s_add_u32 m0, s39, 12288\n\t" \
  "s_nop 0\n\t" \
  "buffer_load_dwordx4 v5, s[44:47], s69 offen offset:0 lds\n\t" \
  "s_mov_b32 s85, 0x211000\n\t" \
  "s_load_dword s84, %[pw], s85\n\t" \
  "buffer_load_dwordx4 v6, s[44:47], s69 offen offset:1024 lds\n\t" \
  "s_mov_b32 s85, 0x240000\n\t" \
  "s_load_dword s84, %[pw], s85\n\t" \
  "buffer_load_dwordx4 v7, s[44:47], s69 offen offset:2048 lds\n\t" \
  "s_mov_b32 s85, 0x240800\n\t" \
  "s_load_dword s84, %[pw], s85\n\t" \
  "s_add_u32 m0, s38, 18432\n\t" \
  "s_nop 0\n\t" \
  "buffer_load_dwordx4 v0, s[40:43], s70 offen offset:0 lds\n\t" \
  "buffer_load_dwordx4 v1, s[40:43], s70 offen offset:1024 lds\n\t" \
  "s_mov_b64 exec, 0xffff\n\t" \
  "buffer_load_dwordx4 v4, s[40:43], s70 offen offset:2048 lds\n\t" \
  "s_add_u32 m0, s38, 27648\n\t" \
  "s_mov_b64 exec, -1\n\t" \
  "buffer_load_dwordx4 v2, s[40:43], s71 offen offset:0 lds\n\t" \
  "buffer_load_dwordx4 v3, s[40:43], s71 offen offset:1024 lds\n\t" \
  "s_mov_b64 exec, 0xffff\n\t" \
  "buffer_load_dwordx4 v4, s[40:43], s71 offen offset:2048 lds\n\t" \
  "s_add_u32 m0, s39, 24576\n\t" \
  "s_mov_b64 exec, -1\n\t" \
  "buffer_load_dwordx4 v5, s[44:47], s72 offen offset:0 lds\n\t" \
  "buffer_load_dwordx4 v6, s[44:47], s72 offen offset:1024 lds\n\t" \
  "buffer_load_dwordx4 v7, s[44:47], s72 offen offset:2048 lds\n\t" \
  "s_add_u32 m0, s39, 36864\n\t" \
  "s_nop 0\n\t" \
  "buffer_load_dwordx4 v5, s[44:47], s73 offen offset:0 lds\n\t" \
  "buffer_load_dwordx4 v6, s[44:47], s73 offen offset:1024 lds\n\t" \
  "buffer_load_dwordx4 v7, s[44:47], s73 offen offset:2048 lds\n\t" \
  "s_mov_b32 m0, %[tsk]\n\t" \
  "s_mov_b32 s85, 0x180000\n\t" \
  "s_mov_b64 exec, %[hm]\n\t" \
  "buffer_load_dword v16, %[sh], s85 offen lds\n\t" \
  "s_mov_b64 exec, -1\n\t"
#define LD_ASM3 \
  "s_mov_b32 s68, 0x180\n\t" \
  "s_mov_b32 s69, 0x120000\n\t" \
  "s_mov_b32 s70, 0x24180\n\t" \
  "s_mov_b32 s71, 0x150000\n\t" \
  "s_add_u32 m0, s38, 36864\n\t" \
  "s_waitcnt vmcnt(13)\n\t" \
  "s_barrier\n\t" \
  "buffer_load_dwordx4 v0, s[40:43], s68 offen offset:0 lds\n\t" \
  "s_mov_b32 s85, 0x241000\n\t" \
  "s_load_dword s84, %[pw], s85\n\t" \
  "buffer_load_dwordx4 v1, s[40:43], s68 offen offset:1024 lds\n\t" \
  "s_mov_b32 s85, 0x270000\n\t" \
  "s_load_dword s84, %[pw], s85\n\t" \
  "s_mov_b64 exec, 0xffff\n\t" \
  "buffer_load_dwordx4 v4, s[40:43], s68 offen offset:2048 lds\n\t" \
  "s_add_u32 m0, s39, 49152\n\t" \
  "s_mov_b64 exec, -1\n\t" \
  "buffer_load_dwordx4 v5, s[44:47], s69 offen offset:0 lds\n\t" \
  "s_mov_b32 s85, 0x270800\n\t" \
  "s_load_dword s84, %[pw], s85\n\t" \
  "buffer_load_dwordx4 v6, s[44:47], s69 offen offset:1024 lds\n\t" \
  "s_mov_b32 s85, 0x271000\n\t" \
  "s_load_dword s84, %[pw], s85\n\t" \
  "buffer_load_dwordx4 v7, s[44:47], s69 offen offset:2048 lds\n\t" \
  "s_mov_b32 s85, 0x2a0000\n\t" \
  "s_load_dword s84, %[pw], s85\n\t" \
  "s_add_u32 m0, s38, 46080\n\t" \
  "s_nop 0\n\t" \
  "buffer_load_dwordx4 v2, s[40:43], s70 offen offset:0 lds\n\t" \
  "s_mov_b32 s85, 0x2a0800\n\t" \
  "s_load_dword s84, %[pw], s85\n\t" \
  "buffer_load_dwordx4 v3, s[40:43], s70 offen offset:1024 lds\n\t" \
  "s_mov_b64 exec, 0xffff\n\t" \
  "buffer_load_dwordx4 v4, s[40:43], s70 offen offset:2048 lds\n\t" \
  "s_add_u32 m0, s39, 61440\n\t" \
  "s_mov_b64 exec, -1\n\t" \
  "buffer_load_dwordx4 v5, s[44:47], s71 offen offset:0 lds\n\t" \
  "buffer_load_dwordx4 v6, s[44:47], s71 offen offset:1024 lds\n\t" \
  "buffer_load_dwordx4 v7, s[44:47], s71 offen offset:2048 lds\n\t" \
  "s_mov_b32 s68, 0x200\n\t" \
  "s_mov_b32 s69, 0x180000\n\t" \
  "s_mov_b32 s70, 0x24200\n\t" \
  "s_mov_b32 s71, 0x1b0000\n\t" \
  "s_add_u32 m0, s38, 0\n\t" \
  "s_waitcnt vmcnt(13)\n\t" \
  "s_barrier\n\t" \
  "buffer_load_dwordx4 v0, s[40:43], s68 offen offset:0 lds\n\t" \
  "s_mov_b32 s85, 0x2a1000\n\t" \
  "s_load_dword s84, %[pw], s85\n\t" \
  "buffer_load_dwordx4 v1, s[40:43], s68 offen offset:1024 lds\n\t" \
  "s_mov_b32 s85, 0x2d0000\n\t" \
  "s_load_dword s84, %[pw], s85\n\t" \
  "s_mov_b64 exec, 0xffff\n\t" \
  "buffer_load_dwordx4 v4, s[40:43], s68 offen offset:2048 lds\n\t" \
  "s_add_u32 m0, s39, 0\n\t" \
  "s_mov_b64 exec, -1\n\t" \
  "buffer_load_dwordx4 v5, s[44:47], s69 offen offset:0 lds\n\t" \
  "s_mov_b32 s85, 0x2d0800\n\t" \
  "s_load_dword s84, %[pw], s85\n\t" \
  "buffer_load_dwordx4 v6, s[44:47], s69 offen offset:1024 lds\n\t" \
  "s_mov_b32 s85, 0x2d1000\n\t" \
  "s_load_dword s84, %[pw], s85\n\t" \
  "buffer_load_dwordx4 v7, s[44:47], s69 offen offset:2048 lds\n\t" \
  "s_mov_b32 s85, 0x300000\n\t" \
  "s_load_dword s84, %[pw], s85\n\t" \
  "s_add_u32 m0, s38, 9216\n\t" \
  "s_nop 0\n\t" \
  "buffer_load_dwordx4 v2, s[40:43], s70 offen offset:0 lds\n\t" \
  "s_mov_b32 s85, 0x300800\n\t" \
  "s_load_dword s84, %[pw], s85\n\t" \
  "buffer_load_dwordx4 v3, s[40:43], s70 offen offset:1024 lds\n\t" \
  "s_mov_b64 exec, 0xffff\n\t" \
  "buffer_load_dwordx4 v4, s[40:43], s70 offen offset:2048 lds\n\t" \
  "s_add_u32 m0, s39, 12288\n\t" \
  "s_mov_b64 exec, -1\n\t" \
  "buffer_load_dwordx4 v5, s[44:47], s71 offen offset:0 lds\n\t" \
  "buffer_load_dwordx4 v6, s[44:47], s71 offen offset:1024 lds\n\t" \
  "buffer_load_dwordx4 v7, s[44:47], s71 offen offset:2048 lds\n\t" \
  "s_mov_b32 s68, 0x280\n\t" \
  "s_mov_b32 s69, 0x1e0000\n\t" \
  "s_mov_b32 s70, 0x24280\n\t" \
  "s_mov_b32 s71, 0x210000\n\t" \
  "s_add_u32 m0, s38, 18432\n\t" \
  "s_waitcnt vmcnt(12)\n\t" \
  "s_barrier\n\t" \
  "buffer_load_dwordx4 v0, s[40:43], s68 offen offset:0 lds\n\t" \
  "s_mov_b32 s85, 0x301000\n\t" \
  "s_load_dword s84, %[pw], s85\n\t" \
  "buffer_load_dwordx4 v1, s[40:43], s68 offen offset:1024 lds\n\t" \
  "s_mov_b32 s85, 0x330000\n\t" \
  "s_load_dword s84, %[pw], s85\n\t" \
  "s_mov_b64 exec, 0xffff\n\t" \
  "buffer_load_dwordx4 v4, s[40:43], s68 offen offset:2048 lds\n\t" \
  "s_add_u32 m0, s39, 24576\n\t" \
  "s_mov_b64 exec, -1\n\t" \
  "buffer_load_dwordx4 v5, s[44:47], s69 offen offset:0 lds\n\t" \
  "s_mov_b32 s85, 0x330800\n\t" \
  "s_load_dword s84, %[pw], s85\n\t" \
  "buffer_load_dwordx4 v6, s[44:47], s69 offen offset:1024 lds\n\t" \
  "s_mov_b32 s85, 0x331000\n\t" \
  "s_load_dword s84, %[pw], s85\n\t" \
  "buffer_load_dwordx4 v7, s[44:47], s69 offen offset:2048 lds\n\t" \
  "s_mov_b32 s85, 0x360000\n\t" \
  "s_load_dword s84, %[pw], s85\n\t" \
  "s_add_u32 m0, s38, 27648\n\t" \
  "s_nop 0\n\t" \
  "buffer_load_dwordx4 v2, s[40:43], s70 offen offset:0 lds\n\t" \
  "s_mov_b32 s85, 0x360800\n\t" \
  "s_load_dword s84, %[pw], s85\n\t" \
  "buffer_load_dwordx4 v3, s[40:43], s70 offen offset:1024 lds\n\t" \
  "s_mov_b64 exec, 0xffff\n\t" \
  "buffer_load_dwordx4 v4, s[40:43], s70 offen offset:2048 lds\n\t" \
  "s_add_u32 m0, s39, 36864\n\t" \
  "s_mov_b64 exec, -1\n\t" \
  "buffer_load_dwordx4 v5, s[44:47], s71 offen offset:0 lds\n\t" \
  "buffer_load_dwordx4 v6, s[44:47], s71 offen offset:1024 lds\n\t" \
  "buffer_load_dwordx4 v7, s[44:47], s71 offen offset:2048 lds\n\t" \
  "s_mov_b32 s68, 0x300\n\t" \
  "s_mov_b32 s69, 0x240000\n\t" \
  "s_mov_b32 s70, 0x24300\n\t" \
  "s_mov_b32 s71, 0x270000\n\t" \
  "s_add_u32 m0, s38, 36864\n\t" \
  "s_waitcnt vmcnt(12)\n\t" \
  "s_barrier\n\t" \
  "buffer_load_dwordx4 v0, s[40:43], s68 offen offset:0 lds\n\t" \
  "s_mov_b32 s85, 0x361000\n\t" \
  "s_load_dword s84, %[pw], s85\n\t" \
  "buffer_load_dwordx4 v1, s[40:43], s68 offen offset:1024 lds\n\t" \
  "s_mov_b32 s85, 0x390000\n\t" \
  "s_load_dword s84, %[pw], s85\n\t" \
  "s_mov_b64 exec, 0xffff\n\t" \
  "buffer_load_dwordx4 v4, s[40:43], s68 offen offset:2048 lds\n\t" \
  "s_add_u32 m0, s39, 49152\n\t" \
  "s_mov_b64 exec, -1\n\t" \
  "buffer_load_dwordx4 v5, s[44:47], s69 offen offset:0 lds\n\t" \
  "s_mov_b32 s85, 0x390800\n\t" \
  "s_load_dword s84, %[pw], s85\n\t" \
  "buffer_load_dwordx4 v6, s[44:47], s69 offen offset:1024 lds\n\t" \
  "s_mov_b32 s85, 0x391000\n\t" \
  "s_load_dword s84, %[pw], s85\n\t" \
  "buffer_load_dwordx4 v7, s[44:47], s69 offen offset:2048 lds\n\t" \
  "s_mov_b32 s85, 0x3c0000\n\t" \
  "s_load_dword s84, %[pw], s85\n\t" \
  "s_add_u32 m0, s38, 46080\n\t" \
  "s_nop 0\n\t" \
  "buffer_load_dwordx4 v2, s[40:43], s70 offen offset:0 lds\n\t" \
  "s_mov_b32 s85, 0x3c0800\n\t" \
  "s_load_dword s84, %[pw], s85\n\t" \
  "buffer_load_dwordx4 v3, s[40:43], s70 offen offset:1024 lds\n\t" \
  "s_mov_b64 exec, 0xffff\n\t" \
  "buffer_load_dwordx4 v4, s[40:43], s70 offen offset:2048 lds\n\t" \
  "s_add_u32 m0, s39, 61440\n\t" \
  "s_mov_b64 exec, -1\n\t" \
  "buffer_load_dwordx4 v5, s[44:47], s71 offen offset:0 lds\n\t" \
  "buffer_load_dwordx4 v6, s[44:47], s71 offen offset:1024 lds\n\t" \
  "buffer_load_dwordx4 v7, s[44:47], s71 offen offset:2048 lds\n\t" \
  "s_mov_b32 s68, 0x380\n\t" \
  "s_mov_b32 s69, 0x2a0000\n\t" \
  "s_mov_b32 s70, 0x24380\n\t" \
  "s_mov_b32 s71, 0x2d0000\n\t" \
  "s_add_u32 m0, s38, 0\n\t" \
  "s_waitcnt vmcnt(12)\n\t" \
  "s_barrier\n\t" \
  "buffer_load_dwordx4 v0, s[40:43], s68 offen offset:0 lds\n\t" \
  "s_mov_b32 s85, 0x3c1000\n\t" \
  "s_load_dword s84, %[pw], s85\n\t" \
  "buffer_load_dwordx4 v1, s[40:43], s68 offen offset:1024 lds\n\t" \
  "s_mov_b32 s85, 0x3f0000\n\t" \
  "s_load_dword s84, %[pw], s85\n\t" \
  "s_mov_b64 exec, 0xffff\n\t" \
  "buffer_load_dwordx4 v4, s[40:43], s68 offen offset:2048 lds\n\t" \
  "s_add_u32 m0, s39, 0\n\t" \
  "s_mov_b64 exec, -1\n\t" \
  "buffer_load_dwordx4 v5, s[44:47], s69 offen offset:0 lds\n\t" \
  "s_mov_b32 s85, 0x3f0800\n\t" \
  "s_load_dword s84, %[pw], s85\n\t" \
  "buffer_load_dwordx4 v6, s[44:47], s69 offen offset:1024 lds\n\t" \
  "s_mov_b32 s85, 0x3f1000\n\t" \
  "s_load_dword s84, %[pw], s85\n\t" \
  "buffer_load_dwordx4 v7, s[44:47], s69 offen offset:2048 lds\n\t" \
  "s_mov_b32 s85, 0x420000\n\t" \
  "s_load_dword s84, %[pw], s85\n\t" \
  "s_add_u32 m0, s38, 9216\n\t" \
  "s_nop 0\n\t" \
  "buffer_load_dwordx4 v2, s[40:43], s70 offen offset:0 lds\n\t" \
  "s_mov_b32 s85, 0x420800\n\t" \
  "s_load_dword s84, %[pw], s85\n\t" \
  "buffer_load_dwordx4 v3, s[40:43], s70 offen offset:1024 lds\n\t" \
  "s_mov_b64 exec, 0xffff\n\t" \
  "buffer_load_dwordx4 v4, s[40:43], s70 offen offset:2048 lds\n\t" \
  "s_add_u32 m0, s39, 12288\n\t" \
  "s_mov_b64 exec, -1\n\t" \
  "buffer_load_dwordx4 v5, s[44:47], s71 offen offset:0 lds\n\t" \
  "buffer_load_dwordx4 v6, s[44:47], s71 offen offset:1024 lds\n\t" \
  "buffer_load_dwordx4 v7, s[44:47], s71 offen offset:2048 lds\n\t" \
  "s_mov_b32 s68, 0x400\n\t" \
  "s_mov_b32 s69, 0x300000\n\t" \
  "s_mov_b32 s70, 0x24400\n\t" \
  "s_mov_b32 s71, 0x330000\n\t" \
  "s_add_u32 m0, s38, 18432\n\t" \
  "s_waitcnt vmcnt(12)\n\t" \
  "s_barrier\n\t" \
  "buffer_load_dwordx4 v0, s[40:43], s68 offen offset:0 lds\n\t" \
  "s_mov_b32 s85, 0x421000\n\t" \
  "s_load_dword s84, %[pw], s85\n\t" \
  "buffer_load_dwordx4 v1, s[40:43], s68 offen offset:1024 lds\n\t" \
  "s_mov_b32 s85, 0x450000\n\t" \
  "s_load_dword s84, %[pw], s85\n\t" \
  "s_mov_b64 exec, 0xffff\n\t" \
  "buffer_load_dwordx4 v4, s[40:43], s68 offen offset:2048 lds\n\t" \
  "s_add_u32 m0, s39, 24576\n\t" \
  "s_mov_b64 exec, -1\n\t" \
  "buffer_load_dwordx4 v5, s[44:47], s69 offen offset:0 lds\n\t" \
  "s_mov_b32 s85, 0x450800\n\t" \
  "s_load_dword s84, %[pw], s85\n\t" \
  "buffer_load_dwordx4 v6, s[44:47], s69 offen offset:1024 lds\n\t" \
  "s_mov_b32 s85, 0x451000\n\t" \
  "s_load_dword s84, %[pw], s85\n\t" \
  "buffer_load_dwordx4 v7, s[44:47], s69 offen offset:2048 lds\n\t" \
  "s_mov_b32 s85, 0x480000\n\t" \
  "s_load_dword s84, %[pw], s85\n\t" \
  "s_add_u32 m0, s38, 27648\n\t" \
  "s_nop 0\n\t" \
  "buffer_load_dwordx4 v2, s[40:43], s70 offen offset:0 lds\n\t" \
  "s_mov_b32 s85, 0x480800\n\t" \
  "s_load_dword s84, %[pw], s85\n\t" \
  "buffer_load_dwordx4 v3, s[40:43], s70 offen offset:1024 lds\n\t" \
  "s_mov_b64 exec, 0xffff\n\t" \
  "buffer_load_dwordx4 v4, s[40:43], s70 offen offset:2048 lds\n\t" \
  "s_add_u32 m0, s39, 36864\n\t" \
  "s_mov_b64 exec, -1\n\t" \
  "buffer_load_dwordx4 v5, s[44:47], s71 offen offset:0 lds\n\t" \
  "buffer_load_dwordx4 v6, s[44:47], s71 offen offset:1024 lds\n\t" \
  "buffer_load_dwordx4 v7, s[44:47], s71 offen offset:2048 lds\n\t" \
  "s_mov_b32 s68, 0x480\n\t" \
  "s_mov_b32 s69, 0x360000\n\t" \
  "s_mov_b32 s70, 0x24480\n\t" \
  "s_mov_b32 s71, 0x390000\n\t" \
  "s_add_u32 m0, s38, 36864\n\t" \
  "s_waitcnt vmcnt(12)\n\t" \
  "s_barrier\n\t" \
  "buffer_load_dwordx4 v0, s[40:43], s68 offen offset:0 lds\n\t" \
  "s_mov_b32 s85, 0x481000\n\t" \
  "s_load_dword s84, %[pw], s85\n\t" \
  "buffer_load_dwordx4 v1, s[40:43], s68 offen offset:1024 lds\n\t" \
  "s_mov_b32 s85, 0x4b0000\n\t" \
  "s_load_dword s84, %[pw], s85\n\t" \
  "s_mov_b64 exec, 0xffff\n\t" \
  "buffer_load_dwordx4 v4, s[40:43], s68 offen offset:2048 lds\n\t" \
  "s_add_u32 m0, s39, 49152\n\t" \
  "s_mov_b64 exec, -1\n\t" \
  "buffer_load_dwordx4 v5, s[44:47], s69 offen offset:0 lds\n\t" \
  "s_mov_b32 s85, 0x4b0800\n\t" \
  "s_load_dword s84, %[pw], s85\n\t" \
  "buffer_load_dwordx4 v6, s[44:47], s69 offen offset:1024 lds\n\t" \
  "s_mov_b32 s85, 0x4b1000\n\t" \
  "s_load_dword s84, %[pw], s85\n\t" \
  "buffer_load_dwordx4 v7, s[44:47], s69 offen offset:2048 lds\n\t" \
  "s_mov_b32 s85, 0x4e0000\n\t" \
  "s_load_dword s84, %[pw], s85\n\t" \
  "s_add_u32 m0, s38, 46080\n\t" \
  "s_nop 0\n\t" \
  "buffer_load_dwordx4 v2, s[40:43], s70 offen offset:0 lds\n\t" \
  "s_mov_b32 s85, 0x4e0800\n\t" \
  "s_load_dword s84, %[pw], s85\n\t" \
  "buffer_load_dwordx4 v3, s[40:43], s70 offen offset:1024 lds\n\t" \
  "s_mov_b64 exec, 0xffff\n\t" \
  "buffer_load_dwordx4 v4, s[40:43], s70 offen offset:2048 lds\n\t" \
  "s_add_u32 m0, s39, 61440\n\t" \
  "s_mov_b64 exec, -1\n\t" \
  "buffer_load_dwordx4 v5, s[44:47], s71 offen offset:0 lds\n\t" \
  "buffer_load_dwordx4 v6, s[44:47], s71 offen offset:1024 lds\n\t" \
  "buffer_load_dwordx4 v7, s[44:47], s71 offen offset:2048 lds\n\t" \
  "s_mov_b32 s68, 0x500\n\t" \
  "s_mov_b32 s69, 0x3c0000\n\t" \
  "s_mov_b32 s70, 0x24500\n\t" \
  "s_mov_b32 s71, 0x3f0000\n\t" \
  "s_add_u32 m0, s38, 0\n\t" \
  "s_waitcnt vmcnt(12)\n\t" \
  "s_barrier\n\t" \
  "buffer_load_dwordx4 v0, s[40:43], s68 offen offset:0 lds\n\t" \
  "s_mov_b32 s85, 0x4e1000\n\t" \
  "s_load_dword s84, %[pw], s85\n\t" \
  "buffer_load_dwordx4 v1, s[40:43], s68 offen offset:1024 lds\n\t" \
  "s_mov_b32 s85, 0x510000\n\t" \
  "s_load_dword s84, %[pw], s85\n\t" \
  "s_mov_b64 exec, 0xffff\n\t" \
  "buffer_load_dwordx4 v4, s[40:43], s68 offen offset:2048 lds\n\t" \
  "s_add_u32 m0, s39, 0\n\t" \
  "s_mov_b64 exec, -1\n\t" \
  "buffer_load_dwordx4 v5, s[44:47], s69 offen offset:0 lds\n\t" \
  "s_mov_b32 s85, 0x510800\n\t" \
  "s_load_dword s84, %[pw], s85\n\t" \
  "buffer_load_dwordx4 v6, s[44:47], s69 offen offset:1024 lds\n\t" \
  "s_mov_b32 s85, 0x511000\n\t" \
  "s_load_dword s84, %[pw], s85\n\t" \
  "buffer_load_dwordx4 v7, s[44:47], s69 offen offset:2048 lds\n\t" \
  "s_mov_b32 s85, 0x540000\n\t" \
  "s_load_dword s84, %[pw], s85\n\t" \
  "s_add_u32 m0, s38, 9216\n\t" \
  "s_nop 0\n\t" \
  "buffer_load_dwordx4 v2, s[40:43], s70 offen offset:0 lds\n\t" \
  "s_mov_b32 s85, 0x540800\n\t" \
  "s_load_dword s84, %[pw], s85\n\t" \
  "buffer_load_dwordx4 v3, s[40:43], s70 offen offset:1024 lds\n\t" \
  "s_mov_b64 exec, 0xffff\n\t" \
  "buffer_load_dwordx4 v4, s[40:43], s70 offen offset:2048 lds\n\t" \
  "s_add_u32 m0, s39, 12288\n\t" \
  "s_mov_b64 exec, -1\n\t" \
  "buffer_load_dwordx4 v5, s[44:47], s71 offen offset:0 lds\n\t" \
  "buffer_load_dwordx4 v6, s[44:47], s71 offen offset:1024 lds\n\t" \
  "buffer_load_dwordx4 v7, s[44:47], s71 offen offset:2048 lds\n\t" \
  "s_mov_b32 s68, 0x580\n\t" \
  "s_mov_b32 s69, 0x420000\n\t" \
  "s_mov_b32 s70, 0x24580\n\t" \
  "s_mov_b32 s71, 0x450000\n\t" \
  "s_add_u32 m0, s38, 18432\n\t" \
  "s_waitcnt vmcnt(12)\n\t" \
  "s_barrier\n\t" \
  "buffer_load_dwordx4 v0, s[40:43], s68 offen offset:0 lds\n\t" \
  "s_load_dword s84, %[pq], 0x0\n\t" \
  "buffer_load_dwordx4 v1, s[40:43], s68 offen offset:1024 lds\n\t" \
  "s_load_dword s84, %[pq], 0x40\n\t" \
  "s_mov_b64 exec, 0xffff\n\t" \
  "buffer_load_dwordx4 v4, s[40:43], s68 offen offset:2048 lds\n\t" \
  "s_add_u32 m0, s39, 24576\n\t" \
  "s_mov_b64 exec, -1\n\t" \
  "buffer_load_dwordx4 v5, s[44:47], s69 offen offset:0 lds\n\t" \
  "s_load_dword s84, %[pq], 0x800\n\t" \
  "buffer_load_dwordx4 v6, s[44:47], s69 offen offset:1024 lds\n\t" \
  "s_load_dword s84, %[pq], 0x840\n\t" \
  "buffer_load_dwordx4 v7, s[44:47], s69 offen offset:2048 lds\n\t" \
  "s_mov_b32 s85, 0x541000\n\t" \
  "s_load_dword s84, %[pw], s85\n\t" \
  "s_add_u32 m0, s38, 27648\n\t" \
  "s_nop 0\n\t" \
  "buffer_load_dwordx4 v2, s[40:43], s70 offen offset:0 lds\n\t" \
  "s_mov_b32 s85, 0x570000\n\t" \
  "s_load_dword s84, %[pw], s85\n\t" \
  "buffer_load_dwordx4 v3, s[40:43], s70 offen offset:1024 lds\n\t" \
  "s_mov_b32 s85, 0x570800\n\t" \
  "s_load_dword s84, %[pw], s85\n\t" \
  "s_mov_b64 exec, 0xffff\n\t" \
  "buffer_load_dwordx4 v4, s[40:43], s70 offen offset:2048 lds\n\t" \
  "s_add_u32 m0, s39, 36864\n\t" \
  "s_mov_b64 exec, -1\n\t" \
  "buffer_load_dwordx4 v5, s[44:47], s71 offen offset:0 lds\n\t" \
  "s_mov_b32 s85, 0x571000\n\t" \
  "s_load_dword s84, %[pw], s85\n\t" \
  "buffer_load_dwordx4 v6, s[44:47], s71 offen offset:1024 lds\n\t" \
  "s_mov_b32 s85, 0x5a0000\n\t" \
  "s_load_dword s84, %[pw], s85\n\t" \
  "buffer_load_dwordx4 v7, s[44:47], s71 offen offset:2048 lds\n\t" \
  "s_mov_b32 s85, 0x5a0800\n\t" \
  "s_load_dword s84, %[pw], s85\n\t" \
  "s_mov_b32 s68, 0x600\n\t" \
  "s_mov_b32 s69, 0x480000\n\t" \
  "s_mov_b32 s70, 0x24600\n\t" \
  "s_mov_b32 s71, 0x4b0000\n\t" \
  "s_add_u32 m0, s38, 36864\n\t" \
  "s_waitcnt vmcnt(12)\n\t" \
  "s_barrier\n\t" \
  "buffer_load_dwordx4 v0, s[40:43], s68 offen offset:0 lds\n\t" \
  "s_load_dword s84, %[pq], 0x1000\n\t" \
  "buffer_load_dwordx4 v1, s[40:43], s68 offen offset:1024 lds\n\t" \
  "s_load_dword s84, %[pq], 0x1040\n\t" \
  "s_mov_b64 exec, 0xffff\n\t" \
  "buffer_load_dwordx4 v4, s[40:43], s68 offen offset:2048 lds\n\t" \
  "s_add_u32 m0, s39, 49152\n\t" \
  "s_mov_b64 exec, -1\n\t" \
  "buffer_load_dwordx4 v5, s[44:47], s69 offen offset:0 lds\n\t" \
  "s_load_dword s84, %[pq], 0x1800\n\t" \
  "buffer_load_dwordx4 v6, s[44:47], s69 offen offset:1024 lds\n\t" \
  "s_load_dword s84, %[pq], 0x1840\n\t" \
  "buffer_load_dwordx4 v7, s[44:47], s69 offen offset:2048 lds\n\t" \
  "s_mov_b32 s85, 0x5a1000\n\t" \
  "s_load_dword s84, %[pw], s85\n\t" \
  "s_add_u32 m0, s38, 46080\n\t" \
  "s_nop 0\n\t" \
  "buffer_load_dwordx4 v2, s[40:43], s70 offen offset:0 lds\n\t" \
  "s_mov_b32 s85, 0x5d0000\n\t" \
  "s_load_dword s84, %[pw], s85\n\t" \
  "buffer_load_dwordx4 v3, s[40:43], s70 offen offset:1024 lds\n\t" \
  "s_mov_b32 s85, 0x5d0800\n\t" \
  "s_load_dword s84, %[pw], s85\n\t" \
  "s_mov_b64 exec, 0xffff\n\t" \
  "buffer_load_dwordx4 v4, s[40:43], s70 offen offset:2048 lds\n\t" \
  "s_add_u32 m0, s39, 61440\n\t" \
  "s_mov_b64 exec, -1\n\t" \
  "buffer_load_dwordx4 v5, s[44:47], s71 offen offset:0 lds\n\t" \
  "s_mov_b32 s85, 0x5d1000\n\t" \
  "s_load_dword s84, %[pw], s85\n\t" \
  "buffer_load_dwordx4 v6, s[44:47], s71 offen offset:1024 lds\n\t" \
  "buffer_load_dwordx4 v7, s[44:47], s71 offen offset:2048 lds\n\t" \
  "s_mov_b32 s68, 0x680\n\t" \
  "s_mov_b32 s69, 0x4e0000\n\t" \
  "s_mov_b32 s70, 0x24680\n\t" \
  "s_mov_b32 s71, 0x510000\n\t" \
  "s_add_u32 m0, s38, 0\n\t" \
  "s_waitcnt vmcnt(12)\n\t" \
  "s_barrier\n\t" \
  "buffer_load_dwordx4 v0, s[40:43], s68 offen offset:0 lds\n\t" \
  "s_load_dword s84, %[pq], 0x2000\n\t" \
  "buffer_load_dwordx4 v1, s[40:43], s68 offen offset:1024 lds\n\t" \
  "s_load_dword s84, %[pq], 0x2040\n\t" \
  "s_mov_b64 exec, 0xffff\n\t" \
  "buffer_load_dwordx4 v4, s[40:43], s68 offen offset:2048 lds\n\t" \
  "s_add_u32 m0, s39, 0\n\t" \
  "s_mov_b64 exec, -1\n\t" \
  "buffer_load_dwordx4 v5, s[44:47], s69 offen offset:0 lds\n\t" \
  "s_load_dword s84, %[pq], 0x2800\n\t" \
  "buffer_load_dwordx4 v6, s[44:47], s69 offen offset:1024 lds\n\t" \
  "s_load_dword s84, %[pq], 0x2840\n\t" \
  "buffer_load_dwordx4 v7, s[44:47], s69 offen offset:2048 lds\n\t" \
  "s_add_u32 m0, s38, 9216\n\t" \
  "s_nop 0\n\t" \
  "buffer_load_dwordx4 v2, s[40:43], s70 offen offset:0 lds\n\t" \
  "buffer_load_dwordx4 v3, s[40:43], s70 offen offset:1024 lds\n\t" \
  "s_mov_b64 exec, 0xffff\n\t" \
  "buffer_load_dwordx4 v4, s[40:43], s70 offen offset:2048 lds\n\t" \
  "s_add_u32 m0, s39, 12288\n\t" \
  "s_mov_b64 exec, -1\n\t" \
  "buffer_load_dwordx4 v5, s[44:47], s71 offen offset:0 lds\n\t" \
  "buffer_load_dwordx4 v6, s[44:47], s71 offen offset:1024 lds\n\t" \
  "buffer_load_dwordx4 v7, s[44:47], s71 offen offset:2048 lds\n\t" \
  "s_mov_b32 s68, 0x700\n\t" \
  "s_mov_b32 s69, 0x540000\n\t" \
  "s_mov_b32 s70, 0x24700\n\t" \
  "s_mov_b32 s71, 0x570000\n\t" \
  "s_add_u32 m0, s38, 18432\n\t" \
  "s_waitcnt vmcnt(12)\n\t" \
  "s_barrier\n\t" \
  "buffer_load_dwordx4 v0, s[40:43], s68 offen offset:0 lds\n\t" \
  "s_load_dword s84, %[pq], 0x3000\n\t" \
  "buffer_load_dwordx4 v1, s[40:43], s68 offen offset:1024 lds\n\t" \
  "s_load_dword s84, %[pq], 0x3040\n\t" \
  "s_mov_b64 exec, 0xffff\n\t" \
  "buffer_load_dwordx4 v4, s[40:43], s68 offen offset:2048 lds\n\t" \
  "s_add_u32 m0, s39, 24576\n\t" \
  "s_mov_b64 exec, -1\n\t" \
  "buffer_load_dwordx4 v5, s[44:47], s69 offen offset:0 lds\n\t" \
  "s_load_dword s84, %[pq], 0x3800\n\t" \
  "buffer_load_dwordx4 v6, s[44:47], s69 offen offset:1024 lds\n\t" \
  "s_load_dword s84, %[pq], 0x3840\n\t" \
  "buffer_load_dwordx4 v7, s[44:47], s69 offen offset:2048 lds\n\t" \
  "s_add_u32 m0, s38, 27648\n\t" \
  "s_nop 0\n\t" \
  "buffer_load_dwordx4 v2, s[40:43], s70 offen offset:0 lds\n\t" \
  "buffer_load_dwordx4 v3, s[40:43], s70 offen offset:1024 lds\n\t" \
  "s_mov_b64 exec, 0xffff\n\t" \
  "buffer_load_dwordx4 v4, s[40:43], s70 offen offset:2048 lds\n\t" \
  "s_add_u32 m0, s39, 36864\n\t" \
  "s_mov_b64 exec, -1\n\t" \
  "buffer_load_dwordx4 v5, s[44:47], s71 offen offset:0 lds\n\t" \
  "buffer_load_dwordx4 v6, s[44:47], s71 offen offset:1024 lds\n\t" \
  "buffer_load_dwordx4 v7, s[44:47], s71 offen offset:2048 lds\n\t" \
  "s_mov_b32 s68, 0x780\n\t" \
  "s_mov_b32 s69, 0x5a0000\n\t" \
  "s_mov_b32 s70, 0x24780\n\t" \
  "s_mov_b32 s71, 0x5d0000\n\t" \
  "s_add_u32 m0, s38, 36864\n\t" \
  "s_waitcnt vmcnt(12)\n\t" \
  "s_barrier\n\t" \
  "buffer_load_dwordx4 v0, s[40:43], s68 offen offset:0 lds\n\t" \
  "buffer_load_dwordx4 v1, s[40:43], s68 offen offset:1024 lds\n\t" \
  "s_mov_b64 exec, 0xffff\n\t" \
  "buffer_load_dwordx4 v4, s[40:43], s68 offen offset:2048 lds\n\t" \
  "s_add_u32 m0, s39, 49152\n\t" \
  "s_mov_b64 exec, -1\n\t" \
  "buffer_load_dwordx4 v5, s[44:47], s69 offen offset:0 lds\n\t" \
  "buffer_load_dwordx4 v6, s[44:47], s69 offen offset:1024 lds\n\t" \
  "buffer_load_dwordx4 v7, s[44:47], s69 offen offset:2048 lds\n\t" \
  "s_add_u32 m0, s38, 46080\n\t" \
  "s_nop 0\n\t" \
  "buffer_load_dwordx4 v2, s[40:43], s70 offen offset:0 lds\n\t" \
  "buffer_load_dwordx4 v3, s[40:43], s70 offen offset:1024 lds\n\t" \
  "s_mov_b64 exec, 0xffff\n\t" \
  "buffer_load_dwordx4 v4, s[40:43], s70 offen offset:2048 lds\n\t" \
  "s_add_u32 m0, s39, 61440\n\t" \
  "s_mov_b64 exec, -1\n\t" \
  "buffer_load_dwordx4 v5, s[44:47], s71 offen offset:0 lds\n\t" \
  "buffer_load_dwordx4 v6, s[44:47], s71 offen offset:1024 lds\n\t" \
  "buffer_load_dwordx4 v7, s[44:47], s71 offen offset:2048 lds\n\t" \
  "v_readlane_b32 s68, v9, 18\n\t" \
  "v_readlane_b32 s69, v9, 19\n\t" \
  "v_readlane_b32 s70, v9, 20\n\t" \
  "v_readlane_b32 s71, v9, 21\n\t" \
  "v_readlane_b32 s72, v9, 0\n\t" \
  "v_readlane_b32 s73, v9, 1\n\t" \
  "v_readlane_b32 s74, v9, 2\n\t" \
  "v_readlane_b32 s75, v9, 3\n\t" \
  "v_readlane_b32 s76, v9, 4\n\t" \
  "v_readlane_b32 s77, v9, 5\n\t" \
  "s_add_u32 m0, %[ob], 73728\n\t" \
  "s_waitcnt vmcnt(12)\n\t" \
  "s_barrier\n\t" \
  "s_nop 2\n\t" \
  "buffer_load_dwordx4 v8, %[so], s68 offen lds\n\t" \
  "s_add_u32 m0, %[ob], 77824\n\t" \
  "s_nop 0\n\t" \
  "buffer_load_dwordx4 v8, %[so], s69 offen lds\n\t" \
  "s_add_u32 m0, %[ob], 81920\n\t" \
  "s_nop 0\n\t" \
  "buffer_load_dwordx4 v8, %[so], s70 offen lds\n\t" \
  "s_add_u32 m0, %[ob], 86016\n\t" \
  "s_nop 0\n\t" \
  "buffer_load_dwordx4 v8, %[so], s71 offen lds\n\t" \
  "s_add_u32 m0, %[ob], 0\n\t" \
  "s_nop 0\n\t" \
  "buffer_load_dwordx4 v8, %[so], s72 offen lds\n\t" \
  "s_add_u32 m0, %[ob], 4096\n\t" \
  "s_nop 0\n\t" \
  "buffer_load_dwordx4 v8, %[so], s73 offen lds\n\t" \
  "s_add_u32 m0, %[ob], 8192\n\t" \
  "s_nop 0\n\t" \
  "buffer_load_dwordx4 v8, %[so], s74 offen lds\n\t" \
  "s_add_u32 m0, %[ob], 12288\n\t" \
  "s_nop 0\n\t" \
  "buffer_load_dwordx4 v8, %[so], s75 offen lds\n\t" \
  "s_add_u32 m0, %[ob], 16384\n\t" \
  "s_nop 0\n\t" \
  "buffer_load_dwordx4 v8, %[so], s76 offen lds\n\t" \
  "s_add_u32 m0, %[ob], 20480\n\t" \
  "s_nop 0\n\t" \
  "buffer_load_dwordx4 v8, %[so], s77 offen lds\n\t" \
  "s_waitcnt vmcnt(10)\n\t" \
  "s_barrier\n\t" \
  "s_nop 2\n\t" \
  "s_add_u32 m0, %[ob], 90112\n\t" \
  "v_readlane_b32 s68, v9, 22\n\t" \
  "s_nop 4\n\t" \
  "buffer_load_dwordx4 v8, %[so], s68 offen lds\n\t" \
  "s_add_u32 m0, %[ob], 94208\n\t" \
  "v_readlane_b32 s68, v9, 23\n\t" \
  "s_nop 4\n\t" \
  "buffer_load_dwordx4 v8, %[so], s68 offen lds\n\t" \
  "s_add_u32 m0, %[ob], 98304\n\t" \
  "v_readlane_b32 s68, v9, 24\n\t" \
  "s_nop 4\n\t" \
  "buffer_load_dwordx4 v8, %[so], s68 offen lds\n\t" \
  "s_add_u32 m0, %[ob], 102400\n\t" \
  "v_readlane_b32 s68, v9, 25\n\t" \
  "s_nop 4\n\t" \
  "buffer_load_dwordx4 v8, %[so], s68 offen lds\n\t" \
  "s_add_u32 m0, %[ob], 106496\n\t" \
  "v_readlane_b32 s68, v9, 26\n\t" \
  "s_nop 4\n\t" \
  "buffer_load_dwordx4 v8, %[so], s68 offen lds\n\t" \
  "s_add_u32 m0, %[ob], 24576\n\t" \
  "v_readlane_b32 s68, v9, 6\n\t" \
  "s_nop 4\n\t" \
  "buffer_load_dwordx4 v8, %[so], s68 offen lds\n\t" \
  "s_add_u32 m0, %[ob], 28672\n\t" \
  "v_readlane_b32 s68, v9, 7\n\t" \
  "s_nop 4\n\t" \
  "buffer_load_dwordx4 v8, %[so], s68 offen lds\n\t" \
  "s_add_u32 m0, %[ob], 32768\n\t" \
  "v_readlane_b32 s68, v9, 8\n\t" \
  "s_nop 4\n\t" \
  "buffer_load_dwordx4 v8, %[so], s68 offen lds\n\t" \
  "s_add_u32 m0, %[ob], 36864\n\t" \
  "v_readlane_b32 s68, v9, 9\n\t" \
  "s_nop 4\n\t" \
  "buffer_load_dwordx4 v8, %[so], s68 offen lds\n\t" \
  "s_add_u32 m0, %[ob], 40960\n\t" \
  "v_readlane_b32 s68, v9, 10\n\t" \
  "s_nop 4\n\t" \
  "buffer_load_dwordx4 v8, %[so], s68 offen lds\n\t" \
  "s_add_u32 m0, %[ob], 45056\n\t" \
  "v_readlane_b32 s68, v9, 11\n\t" \
  "s_nop 4\n\t" \
  "buffer_load_dwordx4 v8, %[so], s68 offen lds\n\t" \
  "s_nop 3\n\t" \
  "s_barrier\n\t" \
  "s_nop 2\n\t" \
  "s_add_u32 m0, %[ob], 110592\n\t" \
  "v_readlane_b32 s68, v9, 27\n\t" \
  "s_nop 4\n\t" \
  "buffer_load_dwordx4 v8, %[so], s68 offen lds\n\t" \
  "s_add_u32 m0, %[ob], 114688\n\t" \
  "v_readlane_b32 s68, v9, 28\n\t" \
  "s_nop 4\n\t" \
  "buffer_load_dwordx4 v8, %[so], s68 offen lds\n\t" \
  "s_add_u32 m0, %[ob], 118784\n\t" \
  "v_readlane_b32 s68, v9, 29\n\t" \
  "s_nop 4\n\t" \
  "buffer_load_dwordx4 v8, %[so], s68 offen lds\n\t" \
  "s_add_u32 m0, %[ob], 122880\n\t" \
  "v_readlane_b32 s68, v9, 30\n\t" \
  "s_nop 4\n\t" \
  "buffer_load_dwordx4 v8, %[so], s68 offen lds\n\t" \
  "s_add_u32 m0, %[ob], 126976\n\t" \
  "v_readlane_b32 s68, v9, 31\n\t" \
  "s_nop 4\n\t" \
  "buffer_load_dwordx4 v8, %[so], s68 offen lds\n\t" \
  "s_add_u32 m0, %[ob], 49152\n\t" \
  "v_readlane_b32 s68, v9, 12\n\t" \
  "s_nop 4\n\t" \
  "buffer_load_dwordx4 v8, %[so], s68 offen lds\n\t" \
  "s_add_u32 m0, %[ob], 53248\n\t" \
  "v_readlane_b32 s68, v9, 13\n\t" \
  "s_nop 4\n\t" \
  "buffer_load_dwordx4 v8, %[so], s68 offen lds\n\t" \
  "s_add_u32 m0, %[ob], 57344\n\t" \
  "v_readlane_b32 s68, v9, 14\n\t" \
  "s_nop 4\n\t" \
  "buffer_load_dwordx4 v8, %[so], s68 offen lds\n\t" \
  "s_add_u32 m0, %[ob], 61440\n\t" \
  "v_readlane_b32 s68, v9, 15\n\t" \
  "s_nop 4\n\t" \
  "buffer_load_dwordx4 v8, %[so], s68 offen lds\n\t" \
  "s_add_u32 m0, %[ob], 65536\n\t" \
  "v_readlane_b32 s68, v9, 16\n\t" \
  "s_nop 4\n\t" \
  "buffer_load_dwordx4 v8, %[so], s68 offen lds\n\t" \
  "s_add_u32 m0, %[ob], 69632\n\t" \
  "v_readlane_b32 s68, v9, 17\n\t" \
  "s_nop 4\n\t" \
  "buffer_load_dwordx4 v8, %[so], s68 offen lds\n\t" \
  "s_waitcnt vmcnt(0) lgkmcnt(0)\n\t"
#define LD_NBAR 17
#define LD_CO 0
#define LD_HT 8
#define LD_TP 3
#define LD_CLOB "v11", "s68", "s69", "s70", "s71", "s72", "s73", "s74", "s75", "s76", "s77", "s78", "s79", "s80", "s81", "s82", "s83", "s85"

#define ENDW
#define TS(i)
#define TSS(i)
#define TOUT(n)

#define SPIN(cond,slp) do{ int sp_=0; while(cond){ __builtin_amdgcn_s_sleep(slp); if(++sp_>(1<<22)) break; } }while(0)
__device__ __forceinline__ unsigned rd1(const char* p){ return __builtin_amdgcn_readfirstlane(*(volatile LDS_AS unsigned*)(LDS_AS char*)p); }
__device__ __forceinline__ unsigned lds_inc(const char* p, int l){ unsigned r=0; if(l==0) r=__hip_atomic_fetch_add((LDS_AS unsigned*)(LDS_AS char*)p, 1u, __ATOMIC_RELAXED, __HIP_MEMORY_SCOPE_WORKGROUP); return __builtin_amdgcn_readfirstlane(r); }
__device__ __forceinline__ void lds_add(unsigned adr){ asm volatile("s_mov_b64 exec, 1\n\tds_add_u32 %0, %1\n\ts_mov_b64 exec, -1" :: "v"(adr), "v"(1u) : "memory"); }
__device__ __forceinline__ void publish(unsigned cnt, const unsigned* base, unsigned voff, unsigned tag){
  unsigned t;
  asm volatile("s_waitcnt vmcnt(0)\n\ts_mov_b64 exec, 1\n\tds_add_rtn_u32 %0, %1, %2\n\ts_waitcnt lgkmcnt(0)\n\tv_cmp_eq_u32 vcc, 3, %0\n\ts_cbranch_vccz PB_%=\n\t"
    "s_mov_b64 exec, 0xffff\n\tglobal_store_dword %3, %4, %5 sc1\n\tPB_%=:\n\ts_mov_b64 exec, -1"
    : "=&v"(t) : "v"(cnt), "v"(1u), "v"(voff), "v"(tag), "s"(base) : "memory", "vcc");
}
__device__ __forceinline__ void bars(int n){
  asm volatile("BL_%=:\n\ts_barrier\n\ts_sub_u32 %0, %0, 1\n\ts_cmp_lg_u32 %0, 0\n\ts_cbranch_scc1 BL_%=" : "+s"(n) :: "memory", "scc");
}
#define barc(rem) barc_(rem)
#define CTRA
#define CTRP
__device__ __forceinline__ void barc_(unsigned& rem){
  asm volatile("s_cmp_eq_u32 %0, 0\n\ts_cbranch_scc1 BC_%=\n\ts_barrier\n\ts_sub_u32 %0, %0, 1\n\tBC_%=:" : "+s"(rem) :: "memory", "scc");
}
template<int SLP> __device__ __forceinline__ void wait_flag_b(const unsigned* p, unsigned voff, unsigned tag, unsigned& rem CTRA){
  int spins=0;
  while(true){
    unsigned v; asm volatile("global_load_dword %0, %1, %2 sc1" : "=v"(v) : "v"(voff), "s"(p) : "memory");
    barc(rem); if(PB2) barc(rem);
    asm volatile("s_waitcnt vmcnt(0)" : "+v"(v) :: "memory");
    if(__all(v==tag) || ++spins>(1<<21)) break;
    if(rem==0) __builtin_amdgcn_s_sleep(SLP);
  }
}
__device__ __forceinline__ void stout(void* sbase, unsigned voff, u32x4 v){
  st16cs(sbase,voff,v);
}

typedef unsigned u32x8 __attribute__((ext_vector_type(8)));
typedef unsigned u32x16 __attribute__((ext_vector_type(16)));
__device__ __forceinline__ char* ptr64(unsigned lo, unsigned hi){ return (char*)(((unsigned long long)hi<<32)|lo); }
__device__ __forceinline__ u32x4 srd(const char* p, unsigned bytes){ return u32x4{(unsigned)(size_t)p,(unsigned)((size_t)p>>32)&0xffffu,bytes,0x20000u}; }
constexpr size_t WS_O=0, WS_KV=(size_t)S*DM*2, WS_Y=(size_t)S*DM*4, WS_FL=(size_t)S*DM*12;

__global__ __launch_bounds__(768,3) void fused_kernel(const u16* x_, const u16* wqkv_, const u16* wo_, char* ws_, u16* out_,
    const u16* kin_, const u16* vin_, const u16* shk_, const u16* hk_, const u16* qin_, const u16* sD_)
{
  __shared__ __attribute__((aligned(128))) char smem[ALL_LDS];
  TS(0);
  int wv;
  asm volatile(
      "s_load_dwordx16 s[48:63], %[ka], 0x0\n\ts_load_dwordx4 s[64:67], %[ka], 0x40\n\ts_load_dwordx2 s[78:79], %[ka], 0x50\n\ts_getpc_b64 s[72:73]\n\tv_readfirstlane_b32 s75, v0\n\ts_lshr_b32 s74, %[b], 3\n\ts_mul_i32 s74, s74, 12\n\ts_lshr_b32 s75, s75, 6\n\t"
      "s_add_u32 s74, s74, s75\n\ts_min_u32 s74, s74, %[cl]\n\ts_lshl_b32 s74, s74, 7\n\ts_mov_b32 m0, %[sk]\n\tv_mov_b32 v1, s74\n\ts_mov_b64 exec, 1\n\tglobal_load_lds_dword v1, s[72:73]\n\ts_mov_b64 exec, -1\n\t"
      "s_mov_b32 s76, %[b]\n\ts_cmp_lt_u32 s75, 4\n\ts_cbranch_scc1 FK_G\n\ts_cmp_lt_u32 s75, 8\n\ts_cbranch_scc0 FK_C\n\t"
      "v_and_b32 v9, 7, v0\n\tv_and_b32 v10, 63, v0\n\ts_lshl_b32 s74, %[b], 19\n\ts_lshl_b32 s73, %[b], 11\n\ts_or_b32 s74, s74, s73\n\ts_and_b32 s74, s74, 0x3fc000\n\ts_lshl_b32 s73, s75, 9\n\t"
      "s_add_u32 s74, s74, s73\n\ts_add_u32 s74, s74, 0x7800\n\tv_lshl_add_u32 v9, v9, 11, s74\n\tv_and_b32 v1, 24, v0\n\ts_lshl2_add_u32 s74, %[b], s75\n\ts_mul_i32 s74, s74, %[nw]\n\tv_lshl_add_u32 v9, v1, 4, v9\n\t"
      "s_add_u32 s74, s74, %[w0]\n\tv_add_lshl_u32 v10, v10, s74, 7\n\t"
      "s_setprio %[lp]\n\ts_and_b32 s74, s75, 3\n\tv_lshrrev_b32 v1, 3, v0\n\tv_lshrrev_b32 v3, 4, v0\n\ts_and_b32 s72, %[b], 1\n\ts_and_b32 s73, %[b], 0xe0\n\ts_lshl_b32 s72, s72, 10\n\ts_lshl2_add_u32 s72, s73, s72\n\t"
      "v_and_b32 v4, 2, v1\n\tv_and_or_b32 v3, v3, 4, v4\n\tv_and_b32 v4, 31, v1\n\tv_xor_b32 v3, v3, v0\n\tv_mul_u32_u24 v4, 0x1800, v4\n\tv_and_b32 v3, 7, v3\n\tv_lshl_add_u32 v3, v3, 4, s72\n\t"
      "s_mul_i32 s39, s74, 0xc00\n\ts_mul_i32 s38, s74, 0x900\n\ts_mul_i32 s74, s74, 18\n\tv_add_u32 v5, v3, v4\n\tv_and_b32 v1, 7, v1\n\tv_add_u32 v6, 0x400, v5\n\tv_add_u32 v1, s74, v1\n\tv_add_u32 v7, 0x800, v5\n\t"
      "s_and_b32 s72, %[b], 6\n\ts_and_b32 s73, %[b], 24\n\ts_lshl4_add_u32 s72, s72, s73\n\tv_xor_b32 v3, v1, v0\n\ts_lshl_b32 s72, s72, 15\n\tv_and_b32 v3, 7, v3\n\tv_lshl_add_u32 v1, v1, 11, s72\n\t"
      "s_mov_b32 s47, 0x20000\n\ts_mov_b32 s46, 0x600000\n\tv_lshl_add_u32 v2, v3, 4, v1\n\ts_mov_b32 s43, s47\n\ts_mov_b32 s42, 0x408000\n\ts_movk_i32 s73, 0x3c00\n\t"
      "v_add_u32 v3, s73, v2\n\ts_add_u32 s38, s38, %[xf]\n\tv_max_u32 v0, 0x8000, v2\n\tv_add_u32 v4, s73, v3\n\tv_max_u32 v1, 0x8000, v3\n\t"
      "s_waitcnt lgkmcnt(0)\n\ts_mov_b64 s[44:45], s[50:51]\n\ts_sub_u32 s40, s48, 0x8000\n\ts_subb_u32 s41, s49, 0\n\t"
      LD_ASM1
      "s_branch FK_L"
      : "=s"(wv)
      : "{v0}"(threadIdx.x), [ka]"s"(__builtin_amdgcn_kernarg_segment_ptr()), [b]"s"(blockIdx.x), [cl]"n"(CODEL-1), [sk]"n"(SINK+256), [lp]"n"(LPR), [xf]"n"(X_OFF), [nw]"n"(3*TPW), [w0]"n"(TW0*3072-12*TPW), [wm]"n"((1<<(3*TPW))-1)
      : "memory", "scc", "s48", "s49", "s50", "s51", "s52", "s53", "s54", "s55", "s72", "s73", "s74", "s75", "s76", "s78", "s79", "v1", "v2", "v3", "v4", "v5", "v6", "v7", "v9", "v10",
        "s40", "s41", "s42", "s43", "s44", "s45", "s46", "s47", "s38", "s39", "s56", "s57", "s58", "s59", "s60", "s61", "s62", "s63", "s64", "s65", "s66", "s67", "s68");
  #define ENTER(lbl) int w8, bid; u32x8 a0, a1; u32x4 a2; u32x2 a3; asm volatile(lbl ":" : "={s75}"(w8), "={s76}"(bid), "={s[48:55]}"(a0), "={s[56:63]}"(a1), "={s[64:67]}"(a2), "={s[78:79]}"(a3) :: "memory");
  #define ARGW asm volatile("s_waitcnt lgkmcnt(0)" : "+{s[48:55]}"(a0), "+{s[56:63]}"(a1), "+{s[64:67]}"(a2), "+{s[78:79]}"(a3));
  #define ROLE const int w=w8&3, xcd=bid&7, idx=bid>>3, rb=(xcd>>1)*4+(idx&3), hp=(xcd&1)*8+(idx>>2), s0=rb*128; (void)s0; (void)hp;
  #define LANEVARS int l; asm volatile("v_mbcnt_lo_u32_b32 %0, -1, 0\n\tv_mbcnt_hi_u32_b32 %0, -1, %0" : "=v"(l)); const int g=l>>4, fr=l&15, tq=(l&15)>>2, tp=l&3; (void)g; (void)fr; (void)tq; (void)tp;
  #define ARGS0 const char* x=ptr64(a0[0],a0[1]); const char* wqkv=ptr64(a0[2],a0[3]); const char* wo=ptr64(a0[4],a0[5]); char* ws=ptr64(a0[6],a0[7]); \
    unsigned* flags=(unsigned*)(ws+WS_FL); const int hot0=(3-(int)(a0[2]>>7))&7; (void)x; (void)wqkv; (void)wo; (void)flags; (void)hot0;
  #define OCOL ((hp&8)|((hp+(int)(a0[2]>>7)-(int)(a0[4]>>7)+OSH)&7))
  #define ORA ((unsigned)(hp-hot0-1)&7u)
  #define ORE(c) (ORA==7u? (c)-1u : (c))
  #define ORO(c) (ORE(c)>=NF && ORE(c)<NF+7u)
  #define ORD(c) (((((ORE(c)<14u? ((ORA==7u? 0u:ORA)+(ORO(c)? ORE(c)-NF : (ORE(c)<NF? ORE(c) : ORE(c)-7u)))%7u : 7u)+hot0+1)&7u))+8u*(((hp>>3)^(ORO(c) || (c)==15u))&1u))
  const unsigned sb0=__builtin_amdgcn_readfirstlane(lds_addr(smem));
  const char* syn=smem+SYN;

  if(__builtin_expect(wv==1,1)){
    ENTER("FK_L")
    u32x4 sx, sw; unsigned xl, wl, xoA, xoB, xo, xo1, xo2, wof[3];
    asm volatile("; descriptors and LDS bases" : "={s[40:43]}"(sx), "={s[44:47]}"(sw), "={s38}"(xl), "={s39}"(wl));
    asm volatile("; lane offsets" : "={v0}"(xoA), "={v1}"(xoB), "={v2}"(xo), "={v3}"(xo1), "={v4}"(xo2), "={v5}"(wof[0]), "={v6}"(wof[1]), "={v7}"(wof[2]));
    unsigned ltr=0, ltr2=0, ltr3=0, sd; (void)ltr; (void)ltr2; (void)ltr3;
    const char* pw;
    {
      ROLE
      LANEVARS
      ARGS0
      const int hg=((idx>>2)-hot0)&7, hi=hot0*4+(idx&3);
      const unsigned long long hm= hg==0? 0ull : hg==HGR? 0xfffull : 0x3full;
      const unsigned hto=(unsigned)(w*393216+(l<6? l*2048+idx*12288 : (l-6)*2048+hi*12288));
      pw=wqkv+(idx*6144+(xcd&1)*1024+(xcd>>1)*256+w*64);
      asm volatile(LD_ASM2
        : "=&{s84}"(sd), "+s"(w8), "+s"(bid), "+s"(a0)
        : "{v0}"(xoA), "{v1}"(xoB), "{v2}"(xo), "{v3}"(xo1), "{v4}"(xo2), "{v5}"(wof[0]), "{v6}"(wof[1]), "{v7}"(wof[2]),
          "{v16}"(hto), [sh]"s"(srd(wqkv+((xcd&1)*8+hot0)*128,6291456u-1920u)), [hm]"s"(hm),
          "{s[40:43]}"(sx), "{s[44:47]}"(sw), "{s38}"(xl), "{s39}"(wl), [pw]"s"(pw), [tsk]"n"(SINK+256)
        : "memory", "scc", "vcc", LD_CLOB);
    }
    ROLE
    LANEVARS
    ARGS0
    const unsigned wd=OCOL*128;
    {
      const unsigned wof2=(unsigned)(w*16384+(l>>3)*2048+((((l&7)>>1)^((l>>4)&3))*32)+(l&1)*16);
      const unsigned to=ORD((unsigned)(l>>1)&15u);
      const unsigned tab=to*131072u+(l&1)*65536;
      asm volatile(LD_ASM3
        : "+{s84}"(sd)
        : "{v0}"(xoA), "{v1}"(xoB), "{v2}"(xo), "{v3}"(xo1), "{v4}"(xo2), "{v5}"(wof[0]), "{v6}"(wof[1]), "{v7}"(wof[2]), "{v8}"(wof2), "{v9}"(tab),
          "{s[40:43]}"(sx), "{s[44:47]}"(sw), [so]"s"(srd(wo+wd,2097152u-wd)), "{s38}"(xl), "{s39}"(wl), [ob]"s"(sb0+w*1024), [pw]"s"(pw),
          [ph]"s"(wqkv+(((xcd&1)*8+hot0)*128+idx*6144+(w>>1)*196608+(w&1)*64)), [pq]"s"(wo+((xcd&1)*1024+((3-(int)(a0[4]>>7))&7)*128+(idx*32+w*8)*2048))
        : "memory", "scc", "vcc", LD_CLOB);
    }
    const unsigned tag=rd1(syn+64)+1u;
    lds_add(sb0+SYN+12);
    TS(2);
    if(w>=4-NPOL){
      const unsigned* fp=flags+F_O+(rb*16+hp)*16; const unsigned fo=ORD((unsigned)l&15u)*4u;
      const unsigned madr=sb0+(l==0? SYN+20 : SINK+l*4);
      SPIN(rd1(syn+12)<4u || rd1(syn+16)<4u,1);
      if(w==3) PUBM(madr,1u);
      if(PD0) __builtin_amdgcn_s_sleep(PD0);
      if(w==2) __builtin_amdgcn_s_sleep(PD1); if(w==1) __builtin_amdgcn_s_sleep(2*PD1); if(w==0) __builtin_amdgcn_s_sleep(3*PD1);
      unsigned rdy=1; int spins=0;
      while(rdy!=0xFFFFu){
        unsigned v;
        asm volatile("s_mov_b64 exec, 0xffff\n\tglobal_atomic_or %0, %1, %3, %2 sc0 sc1\n\ts_mov_b64 exec, -1\n\ts_waitcnt vmcnt(0)" : "=&v"(v) : "v"(fo), "s"(fp), "v"(0u) : "memory");
        unsigned r=((unsigned)__ballot(v==tag)&0xFFFFu)|1u;
        if(++spins>(1<<21)) r=0xFFFFu;
        if(r!=rdy){ PUBM(madr,(unsigned)__builtin_ctz(~r)); rdy=r; if(w<3 && (r&0x3FFFu)==0x3FFFu) break; }
        else __builtin_amdgcn_s_sleep(OSLP);
      }
      TS(3);
      if(bid==0 && w==3){
        int sp=0;
        while(true){
          u32x4 v; asm volatile("global_load_dwordx4 %0, %1, %2 sc1\n\ts_waitcnt vmcnt(0)" : "=&v"(v) : "v"(l*16), "s"(flags+F_Y) : "memory");
          if(__all(v[0]==tag && v[1]==tag && v[2]==tag && v[3]==tag) || ++sp>(1<<20)) break;
          __builtin_amdgcn_s_sleep(12);
        }
        if(l==0) stflag(flags, tag);
      }
    } else { TS(3); }
    TOUT(4);
    __builtin_amdgcn_endpgm();
  }

  if(wv==0){
    ENTER("FK_G")
    ROLE
    __builtin_amdgcn_s_setprio(GPR);
    const int h=w&1, r=w>>1;
    LANEVARS
    TSS(3);
    ARGW
    ARGS0
    if(PGT&1){ if(w==1 && l<7) touch(ws, l<6? l*2097152+PGO : (unsigned)WS_FL+2048, sb0+SINK+512); }
    if(PGT&2){ if(w==2 && l<4) touch(ptr64(a1[0],a1[1]), l*2097152+PGO, sb0+SINK+512); }
    if(PGT&4){ if(w==3 && l<1) touch(wo, PGO, sb0+SINK+512); if(w==0 && l<2) touch(wqkv, l*2097152+2097152, sb0+SINK+512); }
    TSS(4);
    {
      unsigned lx0=sb0+X_OFF+fr*128+((g^(fr&7))*16), lx1=sb0+X_OFF+fr*128+(((4+g)^(fr&7))*16);
      const int sw=wswz(8*g+tq);
      unsigned lw0=sb0+g*3072+tq*128+(unsigned)((((2*h)^sw)*32)+8*tp), lw1=sb0+g*3072+tq*128+(unsigned)((((2*h+1)^sw)*32)+8*tp);
      char* ot=ws+WS_O+(rb*16+hp)*16384+r*8192+h*2048;
      unsigned it; unsigned long long rm; asm("s_cmp_lg_u32 %1, 0\n\ts_cselect_b64 %0, -1, 0" : "=s"(rm) : "s"(r) : "scc");
      asm volatile(N2_ASM
        : [it]"=&s"(it), "+{v56}"(lx0), "+{v57}"(lx1), "+{v58}"(lw0), "+{v59}"(lw1)
        : "{v62}"(l*16), "{v63}"(sb0+EX_OFF+h*3072+l*16), [r]"s"(r), [ot]"s"(ot), [ou]"s"(ot+4096), [fa]"s"(sb0+SYN+32+w*4), [fp]"s"(sb0+SYN+32+(w^2)*4),
          [vo]"s"(2048-1024*r), [rm]"s"(rm), [z]"s"(rb|r)
        : "memory", "scc", "vcc", N2_CLOB);
    }
    TS(3);
    unsigned tb;
    {
      LANEVARS
      const unsigned tag=rd1(syn+64)+1u;
      const unsigned ra=sb0+(4*g+tq)*128+8*tp; const int sw=(2*g+(tq>>1))&3;
      unsigned b0=ra+((0^sw)*32), b1=ra+((1^sw)*32), b2=ra+((2^sw)*32), b3=ra+((3^sw)*32), vo=(unsigned)(w*4096+l*16), sy=sb0+SYN+20;
      unsigned sr=sb0+EX_OFF+w*2048+(l>>3)*128+(((l&7)^(l>>3))*16), sg=sb0+EX_OFF+w*2048+fr*128+(((g>>1)^(fr&7))*16)+(g&1)*8;
      tb=ORD((unsigned)l&15u)*16384u;
      asm volatile("" : "+{v160}"(b0), "+{v161}"(b1), "+{v162}"(b2), "+{v163}"(b3), "+{v164}"(vo), "+{v165}"(sy), "+{v166}"(sr), "+{v167}"(sg), "+{v96}"(tb));
      publish(sb0+SYN+16, flags+F_O+rb*256+hp, l*64, tag);
      TS(4);
      __builtin_amdgcn_s_setprio(G2P);
      unsigned iss=0, q0, q1, q2, cn=0;
      asm volatile(G3_ASM
        : [iss]"+&s"(iss), [t0]"=&s"(q0), [t1]"=&s"(q1), [t2]"=&s"(q2), [cn]"+&s"(cn),
          "+{v160}"(b0), "+{v161}"(b1), "+{v162}"(b2), "+{v163}"(b3), "+{v165}"(sy), "+{v96}"(tb)
        : "{v164}"(vo), "{v166}"(sr), "{v167}"(sg), [go]"s"(60+w*4), [sy]"n"(SYN+20), [ob]"s"(srd(ws+WS_O+rb*262144,262144u)), [op]"s"(ptr64(a1[0],a1[1])+((s0+w*32)*4096+2048+OCOL*128))
        : "memory", "scc", "vcc", G3_CLOB);
    }
    {
      ENDW TS(6); TOUT(7);
    }
    __builtin_amdgcn_endpgm();
  }

  {
    ENTER("FK_C")
    ROLE
    char* cr=smem+C_OFF+2048+w*4096;
    const unsigned crs=sb0+C_OFF+2048+w*4096;
    const int sblk=bid>>3, td0=(bid&7)*128+(w&1)*64;
    const int vb=(LOCA&1)? xcd*32+(idx&16)+(idx&1)*8+((idx>>1)&7) : bid, fb=(LOCA&2)? (idx&15)*16+xcd*2+(idx>>4) : bid;
    const int d=(vb>>4)*64+(((vb>>3)&1)*4+w)*8+(vb&7);
    int kv0=KV0+((sblk>>KSH)&KVS);
    static_assert(KV0+KVS+PP_NB<=LD_NBAR && KV0>=EB, "barrier budget");
    unsigned tagv, ctr; f32x8 RB;
    const int fd0=(fb&15)*64, fs0=(fb>>4)*128+w*32;
    u32x4 qv[4];
    f32x16 A0,A1,A2,A3;
    const int rw=sblk*64+(w>>1)*32, j=rw>>5;
    unsigned k0,k1,k2,ko,ta,se,so,ha,l16,rb0[2];
    {
      LANEVARS
      if(w==0 && l<25 && l!=16) ((unsigned*)(smem+SYN))[l]=0u;
      if(l<32) *(u32x4*)(smem+C_OFF+w*512+l*16)=u32x4{0,0,0,0};
      asm volatile("" ::: "memory");
      __builtin_amdgcn_s_setprio(CPR);
      const int dg=l&7, rg=l>>3;
      ko=(unsigned)(rg*8192+dg*16);
      const bool top=(rw==0 && rg==0);
      k0=top? ko+6144:ko; k1=top? ko+6144:ko+2048; k2=top? ko+6144:ko+4096;
      ta=sb0+EX_OFF+w*1024+(w&1)*512+dg*64; se=(l&8)? 0x03020706u : 0x05040100u; ha=crs+(32*g-4*tq+4*tp)*2; l16=l*16;
      so=(unsigned)(rg*32768+((j>>2)&3)*128+((dg&4)|((dg+j)&3))*16);
      #pragma unroll
      for(int k=0;k<2;k++){ const int q=fr-1-2*g+k; rb0[k]=sb0+C_OFF+(4+(q>>1))*256+((q>>3)&3)*64+((w+(q>>1))&3)*16+(q&1)*8; }
      asm volatile("" : "+{v152}"(k0), "+{v153}"(k1), "+{v154}"(k2), "+{v155}"(ko), "+{v156}"(ta), "+{v157}"(se), "+{v159}"(so), "+{v163}"(l16), "+{v165}"(ha), "+{v166}"(rb0[0]), "+{v167}"(rb0[1]));
    }
    TSS(3);
    ARGW
    if(EB==1) asm volatile("s_barrier" : "+s"(a1), "+s"(a2), "+s"(a3), "+s"(a0) :: "memory");
    if(EB==2) asm volatile("s_barrier\n\ts_barrier" : "+s"(a1), "+s"(a2), "+s"(a3), "+s"(a0) :: "memory");
    ARGS0
    char* out=ptr64(a1[0],a1[1]); const char* qp=ptr64(a2[2],a2[3])+(fs0*DM+fd0)*2;
    {
      const char* kb=ptr64(a1[2],a1[3])+(rw*DM+td0)*2; const char* vb=ptr64(a1[4],a1[5])+(rw*DM+td0)*2;
      unsigned long long zm; asm("s_cmp_eq_u32 %1, 0\n\ts_cselect_b64 %0, 0xff, 0" : "=s"(zm) : "s"(rw) : "scc");
      unsigned t0;
      if(PGT&8){ const char* tp= w==0? kb : w==1? vb : w==2? qp : ptr64(a2[0],a2[1])+d*4096;
        asm volatile("s_mov_b32 m0, %1\n\ts_mov_b64 exec, 1\n\tglobal_load_lds_dword v163, %0\n\ts_mov_b64 exec, -1" :: "s"(tp), "s"(sb0+SINK+512), "{v163}"(l16) : "memory"); }
      TSS(5);
      asm volatile(PP_ASM
        : [t0]"=&s"(t0), "=&{v164}"(ctr), "+{v159}"(so), "=&{v160}"(tagv), "=&{v[24:31]}"(RB), "=&{v[44:47]}"(qv[0]), "=&{v[48:51]}"(qv[1]), "=&{v[52:55]}"(qv[2]), "=&{v[56:59]}"(qv[3]),
          "=&{v[64:79]}"(A0), "=&{v[80:95]}"(A1), "=&{v[96:111]}"(A2), "=&{v[112:127]}"(A3)
        : "{v152}"(k0), "{v153}"(k1), "{v154}"(k2), "{v155}"(ko), "{v156}"(ta), "{v157}"(se), "{v166}"(rb0[0]), "{v167}"(rb0[1]), "{v165}"(ha), "{v163}"(l16),
          [qb0]"s"(qp), [qb2]"s"(qp+4096), [mk]"s"(0x180u), [em]"s"(1ull|(512ull<<w)), [hd]"s"(crs),
          [kb0]"s"(kb-6144), [kb3]"s"(kb), [kb5]"s"(kb+4096), [vb0]"s"(vb), [vb2]"s"(vb+4096), [sb]"s"(ws+WS_KV+td0*4096+j*512),
          [n0]"s"(kv0-EB-(HH==2)), [ff]"s"(flags+F_PP+(td0>>6)*64+j), [zm]"s"(zm), [shd]"s"(sb0+EX_OFF+w*1024), [tgd]"s"(sb0+SYN+64), [shp]"s"(ptr64(a1[6],a1[7])+(bid&7)*1024), [fl]"s"(flags),
          [hp]"s"(ptr64(a2[0],a2[1])+d*4096), [dp]"s"(ptr64(a3[0],a3[1])+d*2),
          [ph]"s"(wqkv+(((xcd&1)*8+hot0)*128+idx*6144+(w>>1)*196608+(w&1)*64))
        : "memory", "scc", PP_CLOB);
    }
    LANEVARS
    const unsigned tag=__builtin_amdgcn_readfirstlane(tagv)+1u;
    unsigned rem=LD_NBAR-PP_NB-kv0;
    barc(rem);
    const unsigned* pf=flags+F_PP+(d>>6)*64;
    const char* kvp=ws+WS_KV+((vb>>4)*8+(vb&7))*32768+((vb>>3)&1)*64+w*8192;
    const unsigned kvo=(unsigned)((l>>2)*128+(l&3)*16);
    int bidx=((l>=16)? l-16 : (l==0? 63 : l+47))*4;
    asm volatile("" : "+{v32}"(bidx));
    __builtin_amdgcn_s_setprio(3);
    while(rem>PREM) barc(rem);
    wait_flag_b<8>(pf, l*4, tag, rem CTRP);
    #pragma unroll
    for(int i=0;i<4;i++){ if(i%KTH==0 && i) barc(rem); gldsc(kvp+i*2048, kvo, crs+i*1024); }
    barc(rem); if(PB2) barc(rem);
    WAITV(0);
    asm volatile("s_mov_b32 m0, %2\n\ts_mov_b64 exec, 1\n\tglobal_load_lds_dword %0, %1 sc1\n\ts_mov_b64 exec, -1" :: "v"(0u), "s"(pf), "s"(sb0+SYN+100+w*4) : "memory");
    while(rem) barc(rem);
    { int sp=0; while(!__all(*(volatile LDS_AS unsigned*)(LDS_AS char*)(syn+100+(l&3)*4)==tag) && ++sp<(1<<22)) __builtin_amdgcn_s_sleep(1); }
    SPIN(rd1(syn+32+w*4)==0u,1); __builtin_amdgcn_s_sleep(CGATE);
    __builtin_amdgcn_s_setprio(CVP);
    TS(3);
    f32x16 ac0, ac1;
    {
      asm volatile("s_nop 1\n\t" CV_ASM
        : "=&{v[128:143]}"(ac0), "=&{v[144:159]}"(ac1)
          , "+{v[44:47]}"(qv[0]), "+{v[48:51]}"(qv[1]), "+{v[52:55]}"(qv[2]), "+{v[56:59]}"(qv[3])
        : "{v[64:79]}"(A0), "{v[80:95]}"(A1), "{v[96:111]}"(A2), "{v[112:127]}"(A3),
          "{v[24:31]}"(RB), "{v32}"(bidx), "{v33}"(sb0+SYN+80+w*4)
          , "{v42}"((256*(g&1)+16*fr+4*(g&2))*2), [yb]"s"(ws+WS_Y+d*4096)
          , "{v43}"((l>>3)*8192+(l&7)*16), [q0]"s"(qp), [q2]"s"(qp+4096)
        : "memory", "scc", CV_CLOB);
    }
    TS(4);
    __builtin_amdgcn_s_setprio(3);
    publish(sb0+SYN+24, flags+F_Y+vb, l*1024, tag);
    asm volatile("" : "+v"(qv[0]), "+v"(qv[1]), "+v"(qv[2]), "+v"(qv[3]));
    TS(5);
    float qf[32];
    #pragma unroll
    for(int i=0;i<16;i++){ qf[2*i]=bflo(qv[i>>2][i&3]); qf[2*i+1]=bfhi(qv[i>>2][i&3]); }
    unsigned ya=crs+(g*8+tq)*64+tp*8, wa0=crs+fr*128+((g^(fr&7))*16), wa1=crs+fr*128+(((4+g)^(fr&7))*16), ra[4], oo=(unsigned)((l>>3)*16384+(l&7)*16), yo2=(l>>2)*4096+(l&3)*16;
    #pragma unroll
    for(int i=0;i<4;i++){ const int row=4*(l>>3)+i; ra[i]=crs+row*128+(((l&7)^(row&7))*16); }
    char* op=out+fs0*4096+fd0*2; char* op2=op+8192; const char* yg=ws+WS_Y+fd0*4096+fs0*2;
    #pragma unroll
    for(int i=0;i<32;i++) asm volatile("" : "+v"(qf[i]));
    asm volatile("" : "+v"(ya), "+v"(wa0), "+v"(wa1), "+v"(ra[0]), "+v"(ra[1]), "+v"(ra[2]), "+v"(ra[3]), "+v"(oo), "+v"(yo2));
    asm volatile("" : "+s"(op), "+s"(op2), "+s"(yg));
    if(w==0){
      SPIN(rd1(syn+24)<4u,1);
      SPIN(rd1(syn+96)==0u,2);
      { int sp=0;
        while(true){
          unsigned v=tag; asm volatile("s_mov_b64 exec, 0xffff\n\tglobal_atomic_or %0, %1, %3, %2 sc0 sc1\n\ts_mov_b64 exec, -1\n\ts_waitcnt vmcnt(0)" : "+&v"(v) : "v"(l*4), "s"(flags+F_Y+fb*16), "v"(0u) : "memory");
          if(__all(v==tag) || ++sp>(1<<21)) break;
          __builtin_amdgcn_s_sleep(YSLP);
        } }
      PUB(sb0+(l==0? SYN+28 : SINK+l*4), 1u);
    } else SPIN(rd1(syn+28)==0u,YS2);
    #pragma unroll
    for(int i=0;i<4;i++) gldsc(yg+i*65536, yo2, crs+i*1024);
    {
      u32x4 y4[4];
      asm volatile("s_waitcnt vmcnt(0)\n\t"
        "ds_read_b64_tr_b16 v[0:1], %4\n\tds_read_b64_tr_b16 v[2:3], %4 offset:256\n\tds_read_b64_tr_b16 v[4:5], %4 offset:32\n\tds_read_b64_tr_b16 v[6:7], %4 offset:288\n\t"
        "ds_read_b64_tr_b16 v[8:9], %4 offset:2048\n\tds_read_b64_tr_b16 v[10:11], %4 offset:2304\n\tds_read_b64_tr_b16 v[12:13], %4 offset:2080\n\tds_read_b64_tr_b16 v[14:15], %4 offset:2336\n\t"
        "s_waitcnt lgkmcnt(6)\n\tds_write_b128 %5, v[0:3]\n\ts_waitcnt lgkmcnt(5)\n\tds_write_b128 %5, v[4:7] offset:2048\n\t"
        "s_waitcnt lgkmcnt(4)\n\tds_write_b128 %6, v[8:11]\n\ts_waitcnt lgkmcnt(3)\n\tds_write_b128 %6, v[12:15] offset:2048\n\t"
        "ds_read_b128 %0, %7\n\tds_read_b128 %1, %8\n\tds_read_b128 %2, %9\n\tds_read_b128 %3, %10\n\ts_waitcnt lgkmcnt(0)"
        : "=&v"(y4[0]), "=&v"(y4[1]), "=&v"(y4[2]), "=&v"(y4[3])
        : "v"(ya), "v"(wa0), "v"(wa1), "v"(ra[0]), "v"(ra[1]), "v"(ra[2]), "v"(ra[3])
        : "memory", "v0", "v1", "v2", "v3", "v4", "v5", "v6", "v7", "v8", "v9", "v10", "v11", "v12", "v13", "v14", "v15");
      #pragma unroll
      for(int i=0;i<4;i++){
        u32x4 ov;
        #pragma unroll
        for(int e=0;e<4;e++) ov[e]=pack2(mul1(qf[8*i+2*e],bflo(y4[i][e])), mul1(qf[8*i+2*e+1],bfhi(y4[i][e])));
        stout((i&2? op2:op)+(i&1)*4096, oo, ov);
      }
    }
    ENDW TS(6); TOUT(7);
    __builtin_amdgcn_endpgm();
  }
}

extern "C" void kernel_launch(void* const* d_in, const int* in_sizes, int n_in, void* d_out, int out_size, void* d_ws, size_t ws_size, hipStream_t stream){
  const u16* q=(const u16*)d_in[0]; const u16* k=(const u16*)d_in[1]; const u16* v=(const u16*)d_in[2];
  const u16* shk=(const u16*)d_in[3]; const u16* hk=(const u16*)d_in[4]; const u16* sD=(const u16*)d_in[5];
  const u16* x=(const u16*)d_in[6]; const u16* wqkv=(const u16*)d_in[7]; const u16* wo=(const u16*)d_in[8];
  fused_kernel<<<256,768,0,stream>>>(x,wqkv,wo,(char*)d_ws,(u16*)d_out,k,v,shk,hk,q,sD);
}
```

```cpp
#include <hip/hip_runtime.h>
#include <hip/hip_bf16.h>
#include <stdint.h>

typedef unsigned short u16;
typedef short bf16x8 __attribute__((ext_vector_type(8)));
typedef short s16x4 __attribute__((ext_vector_type(4)));
typedef float f32x2 __attribute__((ext_vector_type(2)));
typedef float f32x4 __attribute__((ext_vector_type(4)));
typedef float f32x8 __attribute__((ext_vector_type(8)));
typedef float f32x16 __attribute__((ext_vector_type(16)));
typedef unsigned u32x4 __attribute__((ext_vector_type(4)));
typedef unsigned u32x2 __attribute__((ext_vector_type(2)));
typedef __bf16 bf16x2_t __attribute__((ext_vector_type(2)));
#define LDS_AS __attribute__((address_space(3)))

__device__ __forceinline__ float bflo(unsigned x){ return __uint_as_float(x<<16); }
__device__ __forceinline__ float bfhi(unsigned x){ return __uint_as_float(x&0xffff0000u); }
__device__ __forceinline__ unsigned pack2(float lo, float hi){ return __builtin_bit_cast(unsigned, __builtin_convertvector(f32x2{lo,hi}, bf16x2_t)); }
__device__ __forceinline__ float mul1(float a, float b){ float r; asm("v_mul_f32 %0, %1, %2" : "=v"(r) : "v"(a), "v"(b)); return r; }
__device__ __forceinline__ float fma1(float a, float b, float c){ float r; asm("v_fma_f32 %0, %1, %2, %3" : "=v"(r) : "v"(a), "v"(b), "v"(c)); return r; }

__device__ __forceinline__ void glds(const void* sbase, unsigned voff, unsigned ldsaddr){
  asm volatile("s_mov_b32 m0, %2\n\ts_nop 0\n\tglobal_load_lds_dwordx4 %0, %1" :: "v"(voff), "s"(sbase), "s"(ldsaddr) : "memory");
}
__device__ __forceinline__ void gldsc(const void* sbase, unsigned voff, unsigned ldsaddr){
  asm volatile("s_mov_b32 m0, %2\n\ts_nop 0\n\tglobal_load_lds_dwordx4 %0, %1 sc1" :: "v"(voff), "s"(sbase), "s"(ldsaddr) : "memory");
}
__device__ __forceinline__ void touch(const void* sbase, unsigned voff, unsigned ldsaddr){
  asm volatile("s_mov_b32 m0, %2\n\ts_nop 0\n\tglobal_load_lds_dword %0, %1" :: "v"(voff), "s"(sbase), "s"(ldsaddr) : "memory");
}
__device__ __forceinline__ void st16c(void* p, u32x4 v){ asm volatile("global_store_dwordx4 %0, %1, off sc1\n\ts_nop 1" :: "v"(p), "v"(v) : "memory"); }
__device__ __forceinline__ void st16cs(void* sbase, unsigned voff, u32x4 v){ asm volatile("global_store_dwordx4 %0, %1, %2 sc1\n\ts_nop 1" :: "v"(voff), "v"(v), "s"(sbase) : "memory"); }
#define GLB_AS __attribute__((address_space(1)))
__device__ __forceinline__ unsigned ldflag(const unsigned* p){ return __hip_atomic_load((const GLB_AS unsigned*)p, __ATOMIC_RELAXED, __HIP_MEMORY_SCOPE_AGENT); }
__device__ __forceinline__ void stflag(unsigned* p, unsigned v){ __hip_atomic_store((GLB_AS unsigned*)p, v, __ATOMIC_RELAXED, __HIP_MEMORY_SCOPE_AGENT); }
template<int SLP> __device__ __forceinline__ void wait_flag(const unsigned* p, unsigned tag){
  int spins=0;
  while(true){ unsigned v=ldflag(p); if(__all(v==tag)) break; __builtin_amdgcn_s_sleep(SLP); if(++spins>(1<<21)) break; }
}
__device__ __forceinline__ unsigned lds_addr(const void* p){ return (unsigned)(size_t)(const LDS_AS void*)p; }
#define WAITV(n) asm volatile("s_waitcnt vmcnt(" #n ")":::"memory")
#define WAITL() asm volatile("s_waitcnt lgkmcnt(0)":::"memory")
#define SBAR() asm volatile("s_barrier":::"memory")
#define PUBM(addr,val) asm volatile("ds_max_u32 %0, %1" :: "v"(addr), "v"(val) : "memory")
#define PUB(addr,val) asm volatile("ds_write_b32 %0, %1" :: "v"(addr), "v"(val) : "memory")
__device__ __forceinline__ s16x4 trread(const char* p){ return __builtin_amdgcn_ds_read_tr16_b64_v4i16((LDS_AS s16x4*)p); }
__device__ __forceinline__ bf16x8 cat44(s16x4 a, s16x4 b){ return __builtin_shufflevector(a,b,0,1,2,3,4,5,6,7); }
__device__ __forceinline__ int wswz(int row){ return ((row>>1)&1) | (((row>>3)&1)<<1); }
__device__ __forceinline__ u32x4 widen(u32x2 a, u32x2 b){
  auto r0=__builtin_amdgcn_permlane16_swap(a[0],b[0],false,false);
  auto r1=__builtin_amdgcn_permlane16_swap(a[1],b[1],false,false);
  return u32x4{r0[0],r1[0],r0[1],r1[1]};
}

constexpr int S=2048, DM=1024;
constexpr int WS=12288, XS=18432, X_OFF=6*WS;
constexpr int CREG=4608, C_OFF=131072;
constexpr int EX_OFF=C_OFF+4*CREG;
constexpr int SYN=EX_OFF+8192;
constexpr int SINK=SYN+256;
constexpr int ALL_LDS=SINK+768;
constexpr int F_PP=1024, F_O=4096, F_Y=8192;
#define KV0 1
#define KVS 1
#define LPR 2
#define CPR 2
#define GPR 1
#define CGATE 3
#define OSLP 2
#define YSLP 8
#define TPX 16
#define TPW 2
#define TWO 0
#define CVP 1
#define G2P 3
#define OUTC 1
#define ROT 0
#define WTL 2
#define PB2 0
#define PREM 5
#define KTH 2
#define YDEF 3
#define OPOL 1
#define CODEL 268
#define MIX 0
#define LOCA 3
#define XLOC 1
#define PGT 0
#define PGO 0
#define QREM 0
#define EB 0
#define TW0 1
#define ORDM 1
#define NF 7u
#define NPOL 1
#define PD0 0
#define PD1 4
#define HH 0
#define HW 3
#define HKV 4
#define YPOL 1
#define YS2 2
#define OSH 4
#define HSK 3
#define HGR 2
#define KSH 0
#define EPI 0
#define YFL 0
#define HLAST 1

#define N2_ASM \
  "v_mov_b64 v[64:65], 0\n\t" \
  "v_mov_b64 v[66:67], 0\n\t" \
  "v_mov_b64 v[68:69], 0\n\t" \
  "v_mov_b64 v[70:71], 0\n\t" \
  "v_mov_b64 v[72:73], 0\n\t" \
  "v_mov_b64 v[74:75], 0\n\t" \
  "v_mov_b64 v[76:77], 0\n\t" \
  "v_mov_b64 v[78:79], 0\n\t" \
  "v_mov_b64 v[80:81], 0\n\t" \
  "v_mov_b64 v[82:83], 0\n\t" \
  "v_mov_b64 v[84:85], 0\n\t" \
  "v_mov_b64 v[86:87], 0\n\t" \
  "v_mov_b64 v[88:89], 0\n\t" \
  "v_mov_b64 v[90:91], 0\n\t" \
  "v_mov_b64 v[92:93], 0\n\t" \
  "v_mov_b64 v[94:95], 0\n\t" \
  "v_mov_b64 v[96:97], 0\n\t" \
  "v_mov_b64 v[98:99], 0\n\t" \
  "v_mov_b64 v[100:101], 0\n\t" \
  "v_mov_b64 v[102:103], 0\n\t" \
  "v_mov_b64 v[104:105], 0\n\t" \
  "v_mov_b64 v[106:107], 0\n\t" \
  "v_mov_b64 v[108:109], 0\n\t" \
  "v_mov_b64 v[110:111], 0\n\t" \
  "v_mov_b64 v[112:113], 0\n\t" \
  "v_mov_b64 v[114:115], 0\n\t" \
  "v_mov_b64 v[116:117], 0\n\t" \
  "v_mov_b64 v[118:119], 0\n\t" \
  "v_mov_b64 v[120:121], 0\n\t" \
  "v_mov_b64 v[122:123], 0\n\t" \
  "v_mov_b64 v[124:125], 0\n\t" \
  "v_mov_b64 v[126:127], 0\n\t" \
  "v_mov_b64 v[128:129], 0\n\t" \
  "v_mov_b64 v[130:131], 0\n\t" \
  "v_mov_b64 v[132:133], 0\n\t" \
  "v_mov_b64 v[134:135], 0\n\t" \
  "v_mov_b64 v[136:137], 0\n\t" \
  "v_mov_b64 v[138:139], 0\n\t" \
  "v_mov_b64 v[140:141], 0\n\t" \
  "v_mov_b64 v[142:143], 0\n\t" \
  "v_mov_b64 v[144:145], 0\n\t" \
  "v_mov_b64 v[146:147], 0\n\t" \
  "v_mov_b64 v[148:149], 0\n\t" \
  "v_mov_b64 v[150:151], 0\n\t" \
  "v_mov_b64 v[152:153], 0\n\t" \
  "v_mov_b64 v[154:155], 0\n\t" \
  "v_mov_b64 v[156:157], 0\n\t" \
  "v_mov_b64 v[158:159], 0\n\t" \
  "v_mov_b64 v[160:161], 0\n\t" \
  "v_mov_b64 v[162:163], 0\n\t" \
  "v_mov_b64 v[164:165], 0\n\t" \
  "v_mov_b64 v[166:167], 0\n\t" \
  "v_add_u32 v60, 36864, v58\n\t" \
  "v_add_u32 v61, 36864, v59\n\t" \
  "s_mov_b32 %[it], 0\n\t" \
  "s_cmp_lg_u32 %[r], 0\n\t" \
  "s_barrier\n\t" \
  "s_cbranch_scc1 NB_%=\n\t" \
  "ds_read_b64_tr_b16 v[48:49], v60 offset:14336\n\t" \
  "ds_read_b64_tr_b16 v[50:51], v60 offset:14848\n\t" \
  "ds_read_b128 v[0:3], v56 offset:38912\n\t" \
  "ds_read_b64_tr_b16 v[52:53], v60 offset:26624\n\t" \
  "ds_read_b64_tr_b16 v[54:55], v60 offset:27136\n\t" \
  "ds_read_b128 v[16:19], v57 offset:38912\n\t" \
  "ds_read_b128 v[4:7], v56 offset:40960\n\t" \
  "ds_read_b128 v[20:23], v57 offset:40960\n\t" \
  "ds_read_b128 v[8:11], v56 offset:43008\n\t" \
  "ds_read_b128 v[24:27], v57 offset:43008\n\t" \
  "ds_read_b128 v[12:15], v56 offset:45056\n\t" \
  "ds_read_b128 v[28:31], v57 offset:45056\n\t" \
  "ds_read_b64_tr_b16 v[32:33], v60 offset:13312\n\t" \
  "ds_read_b64_tr_b16 v[34:35], v60 offset:13824\n\t" \
  "s_mov_b32 s68, 0x50000\n\t" \
  "s_mov_b32 s69, 0x5550055\n\t" \
  "s_mov_b32 s70, 0xa0000\n\t" \
  "s_mov_b32 s71, 0xaaa00aa\n\t" \
  "s_mov_b32 s72, 0x150001\n\t" \
  "s_mov_b32 s73, 0x15550155\n\t" \
  "s_mov_b32 s74, 0x2a0002\n\t" \
  "s_mov_b32 s75, 0x2aaa02aa\n\t" \
  "s_mov_b32 s76, 0x55505555\n\t" \
  "s_mov_b32 s77, 0x50005500\n\t" \
  "s_mov_b32 s78, 0xaaa0aaaa\n\t" \
  "s_mov_b32 s79, 0xa000aa00\n\t" \
  "s_mov_b32 s80, 0x55405554\n\t" \
  "s_mov_b32 s81, 0x40005400\n\t" \
  "s_mov_b32 s82, 0xaa80aaa8\n\t" \
  "s_mov_b32 s83, 0x8000a800\n\t" \
  "s_mov_b32 s84, 0xaaaaaaaa\n\t" \
  "s_mov_b32 s85, 0xaaaaaaaa\n\t" \
  "s_waitcnt lgkmcnt(0)\n\t" \
  "s_branch N2a_%=\n\t" \
  "N0a_%=:\n\t" \
  "s_waitcnt lgkmcnt(10)\n\t" \
  "v_mfma_f32_16x16x32_bf16 v[128:131], v[0:3], v[32:35], v[128:131]\n\t" \
  "ds_read_b64_tr_b16 v[44:45], v58 offset:13312\n\t" \
  "s_waitcnt lgkmcnt(7)\n\t" \
  "v_mfma_f32_16x16x32_bf16 v[128:131], v[16:19], v[36:39], v[128:131]\n\t" \
  "ds_read_b64_tr_b16 v[46:47], v58 offset:13824\n\t" \
  "v_mfma_f32_16x16x32_bf16 v[136:139], v[4:7], v[32:35], v[136:139]\n\t" \
  "ds_read_b128 v[48:51], v56 offset:0\n\t" \
  "v_mfma_f32_16x16x32_bf16 v[136:139], v[20:23], v[36:39], v[136:139]\n\t" \
  "ds_read_b128 v[52:55], v57 offset:0\n\t" \
  "s_waitcnt lgkmcnt(8)\n\t" \
  "v_mfma_f32_16x16x32_bf16 v[144:147], v[8:11], v[32:35], v[144:147]\n\t" \
  "v_mfma_f32_16x16x32_bf16 v[144:147], v[24:27], v[36:39], v[144:147]\n\t" \
  "s_waitcnt lgkmcnt(4)\n\t" \
  "v_mfma_f32_16x16x32_bf16 v[152:155], v[12:15], v[32:35], v[152:155]\n\t" \
  "v_mfma_f32_16x16x32_bf16 v[152:155], v[28:31], v[36:39], v[152:155]\n\t" \
  "ds_read_b64_tr_b16 v[32:33], v59 offset:1024\n\t" \
  "v_mfma_f32_16x16x32_bf16 v[96:99], v[40:43], v[0:3], v[96:99]\n\t" \
  "ds_read_b64_tr_b16 v[34:35], v59 offset:1536\n\t" \
  "s_waitcnt lgkmcnt(4)\n\t" \
  "v_mfma_f32_16x16x32_bf16 v[96:99], v[44:47], v[16:19], v[96:99]\n\t" \
  "ds_read_b64_tr_b16 v[36:37], v59 offset:13312\n\t" \
  "v_mfma_f32_16x16x32_bf16 v[104:107], v[40:43], v[4:7], v[104:107]\n\t" \
  "v_mfma_f32_16x16x32_bf16 v[104:107], v[44:47], v[20:23], v[104:107]\n\t" \
  "ds_read_b64_tr_b16 v[38:39], v59 offset:13824\n\t" \
  "v_mfma_f32_16x16x32_bf16 v[112:115], v[40:43], v[8:11], v[112:115]\n\t" \
  "v_mfma_f32_16x16x32_bf16 v[112:115], v[44:47], v[24:27], v[112:115]\n\t" \
  "v_mfma_f32_16x16x32_bf16 v[120:123], v[40:43], v[12:15], v[120:123]\n\t" \
  "v_mfma_f32_16x16x32_bf16 v[120:123], v[44:47], v[28:31], v[120:123]\n\t" \
  "s_waitcnt lgkmcnt(4)\n\t" \
  "v_mfma_f32_16x16x32_bf16 v[160:163], v[40:43], v[48:51], v[160:163]\n\t" \
  "v_mfma_f32_16x16x32_bf16 v[160:163], v[44:47], v[52:55], v[160:163]\n\t" \
  "ds_read_b64_tr_b16 v[40:41], v59 offset:2048\n\t" \
  "s_waitcnt lgkmcnt(1)\n\t" \
  "v_mfma_f32_16x16x32_bf16 v[164:167], v[32:35], v[48:51], v[164:167]\n\t" \
  "ds_read_b64_tr_b16 v[42:43], v59 offset:2560\n\t" \
  "v_mfma_f32_16x16x32_bf16 v[164:167], v[36:39], v[52:55], v[164:167]\n\t" \
  "ds_read_b64_tr_b16 v[44:45], v59 offset:14336\n\t" \
  "v_mfma_f32_16x16x32_bf16 v[100:103], v[32:35], v[0:3], v[100:103]\n\t" \
  "ds_read_b64_tr_b16 v[46:47], v59 offset:14848\n\t" \
  "v_mfma_f32_16x16x32_bf16 v[100:103], v[36:39], v[16:19], v[100:103]\n\t" \
  "ds_read_b64_tr_b16 v[48:49], v58 offset:0\n\t" \
  "v_mfma_f32_16x16x32_bf16 v[108:111], v[32:35], v[4:7], v[108:111]\n\t" \
  "ds_read_b64_tr_b16 v[50:51], v58 offset:512\n\t" \
  "v_mfma_f32_16x16x32_bf16 v[108:111], v[36:39], v[20:23], v[108:111]\n\t" \
  "ds_read_b64_tr_b16 v[52:53], v58 offset:12288\n\t" \
  "v_mfma_f32_16x16x32_bf16 v[116:119], v[32:35], v[8:11], v[116:119]\n\t" \
  "ds_read_b64_tr_b16 v[54:55], v58 offset:12800\n\t" \
  "v_mfma_f32_16x16x32_bf16 v[116:119], v[36:39], v[24:27], v[116:119]\n\t" \
  "v_mfma_f32_16x16x32_bf16 v[124:127], v[32:35], v[12:15], v[124:127]\n\t" \
  "ds_read_b64_tr_b16 v[32:33], v59 offset:0\n\t" \
  "v_mfma_f32_16x16x32_bf16 v[124:127], v[36:39], v[28:31], v[124:127]\n\t" \
  "ds_read_b64_tr_b16 v[34:35], v59 offset:512\n\t" \
  "s_waitcnt lgkmcnt(6)\n\t" \
  "v_mfma_f32_16x16x32_bf16 v[132:135], v[0:3], v[40:43], v[132:135]\n\t" \
  "ds_read_b64_tr_b16 v[36:37], v59 offset:12288\n\t" \
  "v_mfma_f32_16x16x32_bf16 v[132:135], v[16:19], v[44:47], v[132:135]\n\t" \
  "ds_read_b64_tr_b16 v[38:39], v59 offset:12800\n\t" \
  "v_mfma_f32_16x16x32_bf16 v[140:143], v[4:7], v[40:43], v[140:143]\n\t" \
  "v_mfma_f32_16x16x32_bf16 v[140:143], v[20:23], v[44:47], v[140:143]\n\t" \
  "v_mfma_f32_16x16x32_bf16 v[148:151], v[8:11], v[40:43], v[148:151]\n\t" \
  "v_mfma_f32_16x16x32_bf16 v[148:151], v[24:27], v[44:47], v[148:151]\n\t" \
  "v_mfma_f32_16x16x32_bf16 v[156:159], v[12:15], v[40:43], v[156:159]\n\t" \
  "v_mfma_f32_16x16x32_bf16 v[156:159], v[28:31], v[44:47], v[156:159]\n\t" \
  "s_waitcnt lgkmcnt(4)\n\t" \
  "v_mfma_f32_16x16x32_bf16 v[64:67], v[48:51], v[0:3], v[64:67]\n\t" \
  "s_waitcnt lgkmcnt(0)\n\t" \
  "s_barrier\n\t" \
  "v_mfma_f32_16x16x32_bf16 v[64:67], v[52:55], v[16:19], v[64:67]\n\t" \
  "ds_read_b64_tr_b16 v[40:41], v58 offset:26624\n\t" \
  "v_mfma_f32_16x16x32_bf16 v[68:71], v[32:35], v[0:3], v[68:71]\n\t" \
  "ds_read_b128 v[0:3], v56 offset:20480\n\t" \
  "v_mfma_f32_16x16x32_bf16 v[68:71], v[36:39], v[16:19], v[68:71]\n\t" \
  "ds_read_b128 v[16:19], v57 offset:20480\n\t" \
  "v_mfma_f32_16x16x32_bf16 v[72:75], v[48:51], v[4:7], v[72:75]\n\t" \
  "ds_read_b64_tr_b16 v[42:43], v58 offset:27136\n\t" \
  "v_mfma_f32_16x16x32_bf16 v[72:75], v[52:55], v[20:23], v[72:75]\n\t" \
  "ds_read_b64_tr_b16 v[44:45], v60 offset:2048\n\t" \
  "v_mfma_f32_16x16x32_bf16 v[76:79], v[32:35], v[4:7], v[76:79]\n\t" \
  "ds_read_b128 v[4:7], v56 offset:22528\n\t" \
  "v_mfma_f32_16x16x32_bf16 v[76:79], v[36:39], v[20:23], v[76:79]\n\t" \
  "ds_read_b128 v[20:23], v57 offset:22528\n\t" \
  "v_mfma_f32_16x16x32_bf16 v[80:83], v[48:51], v[8:11], v[80:83]\n\t" \
  "ds_read_b64_tr_b16 v[46:47], v60 offset:2560\n\t" \
  "v_mfma_f32_16x16x32_bf16 v[80:83], v[52:55], v[24:27], v[80:83]\n\t" \
  "v_mfma_f32_16x16x32_bf16 v[84:87], v[32:35], v[8:11], v[84:87]\n\t" \
  "ds_read_b128 v[8:11], v56 offset:24576\n\t" \
  "v_mfma_f32_16x16x32_bf16 v[84:87], v[36:39], v[24:27], v[84:87]\n\t" \
  "ds_read_b128 v[24:27], v57 offset:24576\n\t" \
  "v_mfma_f32_16x16x32_bf16 v[88:91], v[48:51], v[12:15], v[88:91]\n\t" \
  "ds_read_b64_tr_b16 v[48:49], v58 offset:25600\n\t" \
  "v_mfma_f32_16x16x32_bf16 v[88:91], v[52:55], v[28:31], v[88:91]\n\t" \
  "ds_read_b64_tr_b16 v[50:51], v58 offset:26112\n\t" \
  "v_mfma_f32_16x16x32_bf16 v[92:95], v[32:35], v[12:15], v[92:95]\n\t" \
  "ds_read_b128 v[12:15], v56 offset:26624\n\t" \
  "v_mfma_f32_16x16x32_bf16 v[92:95], v[36:39], v[28:31], v[92:95]\n\t" \
  "ds_read_b128 v[28:31], v57 offset:26624\n\t" \
  "N1a_%=:\n\t" \
  "s_waitcnt lgkmcnt(10)\n\t" \
  "v_mfma_f32_16x16x32_bf16 v[128:131], v[0:3], v[40:43], v[128:131]\n\t" \
  "ds_read_b64_tr_b16 v[52:53], v60 offset:1024\n\t" \
  "s_waitcnt lgkmcnt(7)\n\t" \
  "v_mfma_f32_16x16x32_bf16 v[128:131], v[16:19], v[44:47], v[128:131]\n\t" \
  "ds_read_b64_tr_b16 v[54:55], v60 offset:1536\n\t" \
  "v_mfma_f32_16x16x32_bf16 v[136:139], v[4:7], v[40:43], v[136:139]\n\t" \
  "ds_read_b128 v[32:35], v56 offset:18432\n\t" \
  "v_mfma_f32_16x16x32_bf16 v[136:139], v[20:23], v[44:47], v[136:139]\n\t" \
  "ds_read_b128 v[36:39], v57 offset:18432\n\t" \
  "s_waitcnt lgkmcnt(8)\n\t" \
  "v_mfma_f32_16x16x32_bf16 v[144:147], v[8:11], v[40:43], v[144:147]\n\t" \
  "v_mfma_f32_16x16x32_bf16 v[144:147], v[24:27], v[44:47], v[144:147]\n\t" \
  "s_waitcnt lgkmcnt(4)\n\t" \
  "v_mfma_f32_16x16x32_bf16 v[152:155], v[12:15], v[40:43], v[152:155]\n\t" \
  "v_mfma_f32_16x16x32_bf16 v[152:155], v[28:31], v[44:47], v[152:155]\n\t" \
  "ds_read_b64_tr_b16 v[40:41], v59 offset:25600\n\t" \
  "v_mfma_f32_16x16x32_bf16 v[96:99], v[48:51], v[0:3], v[96:99]\n\t" \
  "ds_read_b64_tr_b16 v[42:43], v59 offset:26112\n\t" \
  "s_waitcnt lgkmcnt(4)\n\t" \
  "v_mfma_f32_16x16x32_bf16 v[96:99], v[52:55], v[16:19], v[96:99]\n\t" \
  "ds_read_b64_tr_b16 v[44:45], v61 offset:1024\n\t" \
  "v_mfma_f32_16x16x32_bf16 v[104:107], v[48:51], v[4:7], v[104:107]\n\t" \
  "v_mfma_f32_16x16x32_bf16 v[104:107], v[52:55], v[20:23], v[104:107]\n\t" \
  "ds_read_b64_tr_b16 v[46:47], v61 offset:1536\n\t" \
  "v_mfma_f32_16x16x32_bf16 v[112:115], v[48:51], v[8:11], v[112:115]\n\t" \
  "v_mfma_f32_16x16x32_bf16 v[112:115], v[52:55], v[24:27], v[112:115]\n\t" \
  "v_mfma_f32_16x16x32_bf16 v[120:123], v[48:51], v[12:15], v[120:123]\n\t" \
  "v_mfma_f32_16x16x32_bf16 v[120:123], v[52:55], v[28:31], v[120:123]\n\t" \
  "s_waitcnt lgkmcnt(4)\n\t" \
  "v_mfma_f32_16x16x32_bf16 v[160:163], v[48:51], v[32:35], v[160:163]\n\t" \
  "v_mfma_f32_16x16x32_bf16 v[160:163], v[52:55], v[36:39], v[160:163]\n\t" \
  "ds_read_b64_tr_b16 v[48:49], v59 offset:26624\n\t" \
  "s_waitcnt lgkmcnt(1)\n\t" \
  "v_mfma_f32_16x16x32_bf16 v[164:167], v[40:43], v[32:35], v[164:167]\n\t" \
  "ds_read_b64_tr_b16 v[50:51], v59 offset:27136\n\t" \
  "v_mfma_f32_16x16x32_bf16 v[164:167], v[44:47], v[36:39], v[164:167]\n\t" \
  "ds_read_b64_tr_b16 v[52:53], v61 offset:2048\n\t" \
  "v_mfma_f32_16x16x32_bf16 v[100:103], v[40:43], v[0:3], v[100:103]\n\t" \
  "ds_read_b64_tr_b16 v[54:55], v61 offset:2560\n\t" \
  "v_mfma_f32_16x16x32_bf16 v[100:103], v[44:47], v[16:19], v[100:103]\n\t" \
  "ds_read_b64_tr_b16 v[32:33], v58 offset:24576\n\t" \
  "v_mfma_f32_16x16x32_bf16 v[108:111], v[40:43], v[4:7], v[108:111]\n\t" \
  "ds_read_b64_tr_b16 v[34:35], v58 offset:25088\n\t" \
  "v_mfma_f32_16x16x32_bf16 v[108:111], v[44:47], v[20:23], v[108:111]\n\t" \
  "ds_read_b64_tr_b16 v[36:37], v60 offset:0\n\t" \
  "v_mfma_f32_16x16x32_bf16 v[116:119], v[40:43], v[8:11], v[116:119]\n\t" \
  "ds_read_b64_tr_b16 v[38:39], v60 offset:512\n\t" \
  "v_mfma_f32_16x16x32_bf16 v[116:119], v[44:47], v[24:27], v[116:119]\n\t" \
  "v_mfma_f32_16x16x32_bf16 v[124:127], v[40:43], v[12:15], v[124:127]\n\t" \
  "ds_read_b64_tr_b16 v[40:41], v59 offset:24576\n\t" \
  "v_mfma_f32_16x16x32_bf16 v[124:127], v[44:47], v[28:31], v[124:127]\n\t" \
  "ds_read_b64_tr_b16 v[42:43], v59 offset:25088\n\t" \
  "s_waitcnt lgkmcnt(6)\n\t" \
  "v_mfma_f32_16x16x32_bf16 v[132:135], v[0:3], v[48:51], v[132:135]\n\t" \
  "ds_read_b64_tr_b16 v[44:45], v61 offset:0\n\t" \
  "v_mfma_f32_16x16x32_bf16 v[132:135], v[16:19], v[52:55], v[132:135]\n\t" \
  "ds_read_b64_tr_b16 v[46:47], v61 offset:512\n\t" \
  "v_mfma_f32_16x16x32_bf16 v[140:143], v[4:7], v[48:51], v[140:143]\n\t" \
  "v_mfma_f32_16x16x32_bf16 v[140:143], v[20:23], v[52:55], v[140:143]\n\t" \
  "v_mfma_f32_16x16x32_bf16 v[148:151], v[8:11], v[48:51], v[148:151]\n\t" \
  "v_mfma_f32_16x16x32_bf16 v[148:151], v[24:27], v[52:55], v[148:151]\n\t" \
  "v_mfma_f32_16x16x32_bf16 v[156:159], v[12:15], v[48:51], v[156:159]\n\t" \
  "v_mfma_f32_16x16x32_bf16 v[156:159], v[28:31], v[52:55], v[156:159]\n\t" \
  "s_waitcnt lgkmcnt(4)\n\t" \
  "v_mfma_f32_16x16x32_bf16 v[64:67], v[32:35], v[0:3], v[64:67]\n\t" \
  "s_waitcnt lgkmcnt(0)\n\t" \
  "s_barrier\n\t" \
  "v_mfma_f32_16x16x32_bf16 v[64:67], v[36:39], v[16:19], v[64:67]\n\t" \
  "ds_read_b64_tr_b16 v[48:49], v60 offset:14336\n\t" \
  "v_mfma_f32_16x16x32_bf16 v[68:71], v[40:43], v[0:3], v[68:71]\n\t" \
  "ds_read_b128 v[0:3], v56 offset:38912\n\t" \
  "v_mfma_f32_16x16x32_bf16 v[68:71], v[44:47], v[16:19], v[68:71]\n\t" \
  "ds_read_b128 v[16:19], v57 offset:38912\n\t" \
  "v_mfma_f32_16x16x32_bf16 v[72:75], v[32:35], v[4:7], v[72:75]\n\t" \
  "ds_read_b64_tr_b16 v[50:51], v60 offset:14848\n\t" \
  "v_mfma_f32_16x16x32_bf16 v[72:75], v[36:39], v[20:23], v[72:75]\n\t" \
  "ds_read_b64_tr_b16 v[52:53], v60 offset:26624\n\t" \
  "v_mfma_f32_16x16x32_bf16 v[76:79], v[40:43], v[4:7], v[76:79]\n\t" \
  "ds_read_b128 v[4:7], v56 offset:40960\n\t" \
  "v_mfma_f32_16x16x32_bf16 v[76:79], v[44:47], v[20:23], v[76:79]\n\t" \
  "ds_read_b128 v[20:23], v57 offset:40960\n\t" \
  "v_mfma_f32_16x16x32_bf16 v[80:83], v[32:35], v[8:11], v[80:83]\n\t" \
  "ds_read_b64_tr_b16 v[54:55], v60 offset:27136\n\t" \
  "v_mfma_f32_16x16x32_bf16 v[80:83], v[36:39], v[24:27], v[80:83]\n\t" \
  "v_mfma_f32_16x16x32_bf16 v[84:87], v[40:43], v[8:11], v[84:87]\n\t" \
  "ds_read_b128 v[8:11], v56 offset:43008\n\t" \
  "v_mfma_f32_16x16x32_bf16 v[84:87], v[44:47], v[24:27], v[84:87]\n\t" \
  "ds_read_b128 v[24:27], v57 offset:43008\n\t" \
  "v_mfma_f32_16x16x32_bf16 v[88:91], v[32:35], v[12:15], v[88:91]\n\t" \
  "ds_read_b64_tr_b16 v[32:33], v60 offset:13312\n\t" \
  "v_mfma_f32_16x16x32_bf16 v[88:91], v[36:39], v[28:31], v[88:91]\n\t" \
  "ds_read_b64_tr_b16 v[34:35], v60 offset:13824\n\t" \
  "v_mfma_f32_16x16x32_bf16 v[92:95], v[40:43], v[12:15], v[92:95]\n\t" \
  "ds_read_b128 v[12:15], v56 offset:45056\n\t" \
  "v_mfma_f32_16x16x32_bf16 v[92:95], v[44:47], v[28:31], v[92:95]\n\t" \
  "ds_read_b128 v[28:31], v57 offset:45056\n\t" \
  "N2a_%=:\n\t" \
  "s_waitcnt lgkmcnt(10)\n\t" \
  "v_mfma_f32_16x16x32_bf16 v[128:131], v[0:3], v[48:51], v[128:131]\n\t" \
  "ds_read_b64_tr_b16 v[36:37], v60 offset:25600\n\t" \
  "s_waitcnt lgkmcnt(7)\n\t" \
  "v_mfma_f32_16x16x32_bf16 v[128:131], v[16:19], v[52:55], v[128:131]\n\t" \
  "ds_read_b64_tr_b16 v[38:39], v60 offset:26112\n\t" \
  "v_mfma_f32_16x16x32_bf16 v[136:139], v[4:7], v[48:51], v[136:139]\n\t" \
  "ds_read_b128 v[40:43], v56 offset:36864\n\t" \
  "v_mfma_f32_16x16x32_bf16 v[136:139], v[20:23], v[52:55], v[136:139]\n\t" \
  "ds_read_b128 v[44:47], v57 offset:36864\n\t" \
  "s_waitcnt lgkmcnt(8)\n\t" \
  "v_mfma_f32_16x16x32_bf16 v[144:147], v[8:11], v[48:51], v[144:147]\n\t" \
  "v_mfma_f32_16x16x32_bf16 v[144:147], v[24:27], v[52:55], v[144:147]\n\t" \
  "s_waitcnt lgkmcnt(4)\n\t" \
  "v_mfma_f32_16x16x32_bf16 v[152:155], v[12:15], v[48:51], v[152:155]\n\t" \
  "v_mfma_f32_16x16x32_bf16 v[152:155], v[28:31], v[52:55], v[152:155]\n\t" \
  "ds_read_b64_tr_b16 v[48:49], v61 offset:13312\n\t" \
  "v_mfma_f32_16x16x32_bf16 v[96:99], v[32:35], v[0:3], v[96:99]\n\t" \
  "ds_read_b64_tr_b16 v[50:51], v61 offset:13824\n\t" \
  "s_waitcnt lgkmcnt(4)\n\t" \
  "v_mfma_f32_16x16x32_bf16 v[96:99], v[36:39], v[16:19], v[96:99]\n\t" \
  "ds_read_b64_tr_b16 v[52:53], v61 offset:25600\n\t" \
  "v_mfma_f32_16x16x32_bf16 v[104:107], v[32:35], v[4:7], v[104:107]\n\t" \
  "v_mfma_f32_16x16x32_bf16 v[104:107], v[36:39], v[20:23], v[104:107]\n\t" \
  "ds_read_b64_tr_b16 v[54:55], v61 offset:26112\n\t" \
  "v_mfma_f32_16x16x32_bf16 v[112:115], v[32:35], v[8:11], v[112:115]\n\t" \
  "v_mfma_f32_16x16x32_bf16 v[112:115], v[36:39], v[24:27], v[112:115]\n\t" \
  "v_mfma_f32_16x16x32_bf16 v[120:123], v[32:35], v[12:15], v[120:123]\n\t" \
  "v_mfma_f32_16x16x32_bf16 v[120:123], v[36:39], v[28:31], v[120:123]\n\t" \
  "s_waitcnt lgkmcnt(4)\n\t" \
  "v_mfma_f32_16x16x32_bf16 v[160:163], v[32:35], v[40:43], v[160:163]\n\t" \
  "v_mfma_f32_16x16x32_bf16 v[160:163], v[36:39], v[44:47], v[160:163]\n\t" \
  "ds_read_b64_tr_b16 v[32:33], v61 offset:14336\n\t" \
  "s_waitcnt lgkmcnt(1)\n\t" \
  "v_mfma_f32_16x16x32_bf16 v[164:167], v[48:51], v[40:43], v[164:167]\n\t" \
  "ds_read_b64_tr_b16 v[34:35], v61 offset:14848\n\t" \
  "v_mfma_f32_16x16x32_bf16 v[164:167], v[52:55], v[44:47], v[164:167]\n\t" \
  "ds_read_b64_tr_b16 v[36:37], v61 offset:26624\n\t" \
  "v_mfma_f32_16x16x32_bf16 v[100:103], v[48:51], v[0:3], v[100:103]\n\t" \
  "ds_read_b64_tr_b16 v[38:39], v61 offset:27136\n\t" \
  "v_mfma_f32_16x16x32_bf16 v[100:103], v[52:55], v[16:19], v[100:103]\n\t" \
  "ds_read_b64_tr_b16 v[40:41], v60 offset:12288\n\t" \
  "v_mfma_f32_16x16x32_bf16 v[108:111], v[48:51], v[4:7], v[108:111]\n\t" \
  "ds_read_b64_tr_b16 v[42:43], v60 offset:12800\n\t" \
  "v_mfma_f32_16x16x32_bf16 v[108:111], v[52:55], v[20:23], v[108:111]\n\t" \
  "ds_read_b64_tr_b16 v[44:45], v60 offset:24576\n\t" \
  "v_mfma_f32_16x16x32_bf16 v[116:119], v[48:51], v[8:11], v[116:119]\n\t" \
  "ds_read_b64_tr_b16 v[46:47], v60 offset:25088\n\t" \
  "v_mfma_f32_16x16x32_bf16 v[116:119], v[52:55], v[24:27], v[116:119]\n\t" \
  "v_mfma_f32_16x16x32_bf16 v[124:127], v[48:51], v[12:15], v[124:127]\n\t" \
  "ds_read_b64_tr_b16 v[48:49], v61 offset:12288\n\t" \
  "v_mfma_f32_16x16x32_bf16 v[124:127], v[52:55], v[28:31], v[124:127]\n\t" \
  "ds_read_b64_tr_b16 v[50:51], v61 offset:12800\n\t" \
  "s_waitcnt lgkmcnt(6)\n\t" \
  "v_mfma_f32_16x16x32_bf16 v[132:135], v[0:3], v[32:35], v[132:135]\n\t" \
  "ds_read_b64_tr_b16 v[52:53], v61 offset:24576\n\t" \
  "v_mfma_f32_16x16x32_bf16 v[132:135], v[16:19], v[36:39], v[132:135]\n\t" \
  "ds_read_b64_tr_b16 v[54:55], v61 offset:25088\n\t" \
  "v_mfma_f32_16x16x32_bf16 v[140:143], v[4:7], v[32:35], v[140:143]\n\t" \
  "v_mfma_f32_16x16x32_bf16 v[140:143], v[20:23], v[36:39], v[140:143]\n\t" \
  "s_addk_i32 %[it], 1\n\t" \
  "v_mfma_f32_16x16x32_bf16 v[148:151], v[8:11], v[32:35], v[148:151]\n\t" \
  "s_cmp_lt_u32 %[it], 6\n\t" \
  "v_mfma_f32_16x16x32_bf16 v[148:151], v[24:27], v[36:39], v[148:151]\n\t" \
  "v_mfma_f32_16x16x32_bf16 v[156:159], v[12:15], v[32:35], v[156:159]\n\t" \
  "v_mfma_f32_16x16x32_bf16 v[156:159], v[28:31], v[36:39], v[156:159]\n\t" \
  "s_waitcnt lgkmcnt(4)\n\t" \
  "v_mfma_f32_16x16x32_bf16 v[64:67], v[40:43], v[0:3], v[64:67]\n\t" \
  "s_waitcnt lgkmcnt(0)\n\t" \
  "s_barrier\n\t" \
  "v_mfma_f32_16x16x32_bf16 v[64:67], v[44:47], v[16:19], v[64:67]\n\t" \
  "ds_read_b64_tr_b16 v[32:33], v58 offset:2048\n\t" \
  "v_mfma_f32_16x16x32_bf16 v[68:71], v[48:51], v[0:3], v[68:71]\n\t" \
  "ds_read_b128 v[0:3], v56 offset:2048\n\t" \
  "v_mfma_f32_16x16x32_bf16 v[68:71], v[52:55], v[16:19], v[68:71]\n\t" \
  "ds_read_b128 v[16:19], v57 offset:2048\n\t" \
  "v_mfma_f32_16x16x32_bf16 v[72:75], v[40:43], v[4:7], v[72:75]\n\t" \
  "ds_read_b64_tr_b16 v[34:35], v58 offset:2560\n\t" \
  "v_mfma_f32_16x16x32_bf16 v[72:75], v[44:47], v[20:23], v[72:75]\n\t" \
  "ds_read_b64_tr_b16 v[36:37], v58 offset:14336\n\t" \
  "v_mfma_f32_16x16x32_bf16 v[76:79], v[48:51], v[4:7], v[76:79]\n\t" \
  "ds_read_b128 v[4:7], v56 offset:4096\n\t" \
  "v_mfma_f32_16x16x32_bf16 v[76:79], v[52:55], v[20:23], v[76:79]\n\t" \
  "ds_read_b128 v[20:23], v57 offset:4096\n\t" \
  "v_mfma_f32_16x16x32_bf16 v[80:83], v[40:43], v[8:11], v[80:83]\n\t" \
  "ds_read_b64_tr_b16 v[38:39], v58 offset:14848\n\t" \
  "v_mfma_f32_16x16x32_bf16 v[80:83], v[44:47], v[24:27], v[80:83]\n\t" \
  "v_mfma_f32_16x16x32_bf16 v[84:87], v[48:51], v[8:11], v[84:87]\n\t" \
  "ds_read_b128 v[8:11], v56 offset:6144\n\t" \
  "v_mfma_f32_16x16x32_bf16 v[84:87], v[52:55], v[24:27], v[84:87]\n\t" \
  "ds_read_b128 v[24:27], v57 offset:6144\n\t" \
  "v_mfma_f32_16x16x32_bf16 v[88:91], v[40:43], v[12:15], v[88:91]\n\t" \
  "ds_read_b64_tr_b16 v[40:41], v58 offset:1024\n\t" \
  "v_mfma_f32_16x16x32_bf16 v[88:91], v[44:47], v[28:31], v[88:91]\n\t" \
  "ds_read_b64_tr_b16 v[42:43], v58 offset:1536\n\t" \
  "v_mfma_f32_16x16x32_bf16 v[92:95], v[48:51], v[12:15], v[92:95]\n\t" \
  "ds_read_b128 v[12:15], v56 offset:8192\n\t" \
  "v_mfma_f32_16x16x32_bf16 v[92:95], v[52:55], v[28:31], v[92:95]\n\t" \
  "ds_read_b128 v[28:31], v57 offset:8192\n\t" \
  "s_cbranch_scc1 N0a_%=\n\t" \
  "s_setprio 3\n\t" \
  "s_waitcnt lgkmcnt(0)\n\t" \
  "v_cvt_pk_bf16_f32 v120, v120, v121\n\t" \
  "v_cvt_pk_bf16_f32 v121, v122, v123\n\t" \
  "v_cvt_pk_bf16_f32 v122, v124, v125\n\t" \
  "v_cvt_pk_bf16_f32 v123, v126, v127\n\t" \
  "v_cvt_pk_bf16_f32 v28, v152, v153\n\t" \
  "v_cvt_pk_bf16_f32 v29, v154, v155\n\t" \
  "v_cvt_pk_bf16_f32 v38, v156, v157\n\t" \
  "v_cvt_pk_bf16_f32 v39, v158, v159\n\t" \
  "ds_write_b128 v63, v[120:123]\n\t" \
  "ds_write2_b64 v63, v[28:29], v[38:39] offset0:128 offset1:129\n\t" \
  "v_mov_b32 v57, %[fa]\n\t" \
  "v_mov_b32 v58, 1\n\t" \
  "s_mov_b64 exec, 1\n\t" \
  "ds_write_b32 v57, v58\n\t" \
  "s_mov_b64 exec, -1\n\t" \
  "v_cvt_pk_bf16_f32 v160, v160, v161\n\t" \
  "v_cvt_pk_bf16_f32 v161, v162, v163\n\t" \
  "v_cvt_pk_bf16_f32 v162, v164, v165\n\t" \
  "v_cvt_pk_bf16_f32 v163, v166, v167\n\t" \
  "s_branch NX_%=\n\t" \
  "NB_%=:\n\t" \
  "ds_read_b64_tr_b16 v[48:49], v60 offset:13312\n\t" \
  "ds_read_b64_tr_b16 v[50:51], v60 offset:13824\n\t" \
  "ds_read_b128 v[0:3], v56 offset:47104\n\t" \
  "ds_read_b64_tr_b16 v[52:53], v60 offset:25600\n\t" \
  "ds_read_b64_tr_b16 v[54:55], v60 offset:26112\n\t" \
  "ds_read_b128 v[16:19], v57 offset:47104\n\t" \
  "ds_read_b128 v[4:7], v56 offset:49152\n\t" \
  "ds_read_b128 v[20:23], v57 offset:49152\n\t" \
  "ds_read_b128 v[8:11], v56 offset:51200\n\t" \
  "ds_read_b128 v[24:27], v57 offset:51200\n\t" \
  "ds_read_b128 v[12:15], v56 offset:53248\n\t" \
  "ds_read_b128 v[28:31], v57 offset:53248\n\t" \
  "ds_read_b64_tr_b16 v[32:33], v60 offset:14336\n\t" \
  "ds_read_b64_tr_b16 v[34:35], v60 offset:14848\n\t" \
  "s_mov_b32 s68, 0x50000\n\t" \
  "s_mov_b32 s69, 0x5550055\n\t" \
  "s_mov_b32 s70, 0xa0000\n\t" \
  "s_mov_b32 s71, 0xaaa00aa\n\t" \
  "s_mov_b32 s72, 0x150001\n\t" \
  "s_mov_b32 s73, 0x15550155\n\t" \
  "s_mov_b32 s74, 0x2a0002\n\t" \
  "s_mov_b32 s75, 0x2aaa02aa\n\t" \
  "s_mov_b32 s76, 0x55505555\n\t" \
  "s_mov_b32 s77, 0x50005500\n\t" \
  "s_mov_b32 s78, 0xaaa0aaaa\n\t" \
  "s_mov_b32 s79, 0xa000aa00\n\t" \
  "s_mov_b32 s80, 0x55405554\n\t" \
  "s_mov_b32 s81, 0x40005400\n\t" \
  "s_mov_b32 s82, 0xaa80aaa8\n\t" \
  "s_mov_b32 s83, 0x8000a800\n\t" \
  "s_mov_b32 s84, 0xaaaaaaaa\n\t" \
  "s_mov_b32 s85, 0xaaaaaaaa\n\t" \
  "s_waitcnt lgkmcnt(0)\n\t" \
  "s_branch N2b_%=\n\t" \
  "N0b_%=:\n\t" \
  "s_waitcnt lgkmcnt(10)\n\t" \
  "v_mfma_f32_16x16x32_bf16 v[96:99], v[32:35], v[0:3], v[96:99]\n\t" \
  "ds_read_b64_tr_b16 v[44:45], v58 offset:14336\n\t" \
  "s_waitcnt lgkmcnt(7)\n\t" \
  "v_mfma_f32_16x16x32_bf16 v[96:99], v[36:39], v[16:19], v[96:99]\n\t" \
  "ds_read_b64_tr_b16 v[46:47], v58 offset:14848\n\t" \
  "v_mfma_f32_16x16x32_bf16 v[104:107], v[32:35], v[4:7], v[104:107]\n\t" \
  "ds_read_b128 v[48:51], v56 offset:0\n\t" \
  "v_mfma_f32_16x16x32_bf16 v[104:107], v[36:39], v[20:23], v[104:107]\n\t" \
  "ds_read_b128 v[52:55], v57 offset:0\n\t" \
  "s_waitcnt lgkmcnt(8)\n\t" \
  "v_mfma_f32_16x16x32_bf16 v[112:115], v[32:35], v[8:11], v[112:115]\n\t" \
  "v_mfma_f32_16x16x32_bf16 v[112:115], v[36:39], v[24:27], v[112:115]\n\t" \
  "s_waitcnt lgkmcnt(4)\n\t" \
  "v_mfma_f32_16x16x32_bf16 v[120:123], v[32:35], v[12:15], v[120:123]\n\t" \
  "v_mfma_f32_16x16x32_bf16 v[120:123], v[36:39], v[28:31], v[120:123]\n\t" \
  "ds_read_b64_tr_b16 v[32:33], v59 offset:2048\n\t" \
  "v_mfma_f32_16x16x32_bf16 v[128:131], v[0:3], v[40:43], v[128:131]\n\t" \
  "ds_read_b64_tr_b16 v[34:35], v59 offset:2560\n\t" \
  "s_waitcnt lgkmcnt(4)\n\t" \
  "v_mfma_f32_16x16x32_bf16 v[128:131], v[16:19], v[44:47], v[128:131]\n\t" \
  "ds_read_b64_tr_b16 v[36:37], v59 offset:14336\n\t" \
  "v_mfma_f32_16x16x32_bf16 v[136:139], v[4:7], v[40:43], v[136:139]\n\t" \
  "v_mfma_f32_16x16x32_bf16 v[136:139], v[20:23], v[44:47], v[136:139]\n\t" \
  "ds_read_b64_tr_b16 v[38:39], v59 offset:14848\n\t" \
  "v_mfma_f32_16x16x32_bf16 v[144:147], v[8:11], v[40:43], v[144:147]\n\t" \
  "v_mfma_f32_16x16x32_bf16 v[144:147], v[24:27], v[44:47], v[144:147]\n\t" \
  "v_mfma_f32_16x16x32_bf16 v[152:155], v[12:15], v[40:43], v[152:155]\n\t" \
  "v_mfma_f32_16x16x32_bf16 v[152:155], v[28:31], v[44:47], v[152:155]\n\t" \
  "s_waitcnt lgkmcnt(4)\n\t" \
  "v_mfma_f32_16x16x32_bf16 v[160:163], v[48:51], v[40:43], v[160:163]\n\t" \
  "v_mfma_f32_16x16x32_bf16 v[160:163], v[52:55], v[44:47], v[160:163]\n\t" \
  "ds_read_b64_tr_b16 v[40:41], v59 offset:1024\n\t" \
  "s_waitcnt lgkmcnt(1)\n\t" \
  "v_mfma_f32_16x16x32_bf16 v[164:167], v[48:51], v[32:35], v[164:167]\n\t" \
  "ds_read_b64_tr_b16 v[42:43], v59 offset:1536\n\t" \
  "v_mfma_f32_16x16x32_bf16 v[164:167], v[52:55], v[36:39], v[164:167]\n\t" \
  "ds_read_b64_tr_b16 v[44:45], v59 offset:13312\n\t" \
  "v_mfma_f32_16x16x32_bf16 v[132:135], v[0:3], v[32:35], v[132:135]\n\t" \
  "ds_read_b64_tr_b16 v[46:47], v59 offset:13824\n\t" \
  "v_mfma_f32_16x16x32_bf16 v[132:135], v[16:19], v[36:39], v[132:135]\n\t" \
  "ds_read_b64_tr_b16 v[48:49], v58 offset:0\n\t" \
  "v_mfma_f32_16x16x32_bf16 v[140:143], v[4:7], v[32:35], v[140:143]\n\t" \
  "ds_read_b64_tr_b16 v[50:51], v58 offset:512\n\t" \
  "v_mfma_f32_16x16x32_bf16 v[140:143], v[20:23], v[36:39], v[140:143]\n\t" \
  "ds_read_b64_tr_b16 v[52:53], v58 offset:12288\n\t" \
  "v_mfma_f32_16x16x32_bf16 v[148:151], v[8:11], v[32:35], v[148:151]\n\t" \
  "ds_read_b64_tr_b16 v[54:55], v58 offset:12800\n\t" \
  "v_mfma_f32_16x16x32_bf16 v[148:151], v[24:27], v[36:39], v[148:151]\n\t" \
  "v_mfma_f32_16x16x32_bf16 v[156:159], v[12:15], v[32:35], v[156:159]\n\t" \
  "ds_read_b64_tr_b16 v[32:33], v59 offset:0\n\t" \
  "v_mfma_f32_16x16x32_bf16 v[156:159], v[28:31], v[36:39], v[156:159]\n\t" \
  "ds_read_b64_tr_b16 v[34:35], v59 offset:512\n\t" \
  "s_waitcnt lgkmcnt(6)\n\t" \
  "v_mfma_f32_16x16x32_bf16 v[100:103], v[40:43], v[0:3], v[100:103]\n\t" \
  "ds_read_b64_tr_b16 v[36:37], v59 offset:12288\n\t" \
  "v_mfma_f32_16x16x32_bf16 v[100:103], v[44:47], v[16:19], v[100:103]\n\t" \
  "ds_read_b64_tr_b16 v[38:39], v59 offset:12800\n\t" \
  "v_mfma_f32_16x16x32_bf16 v[108:111], v[40:43], v[4:7], v[108:111]\n\t" \
  "v_mfma_f32_16x16x32_bf16 v[108:111], v[44:47], v[20:23], v[108:111]\n\t" \
  "v_mfma_f32_16x16x32_bf16 v[116:119], v[40:43], v[8:11], v[116:119]\n\t" \
  "v_mfma_f32_16x16x32_bf16 v[116:119], v[44:47], v[24:27], v[116:119]\n\t" \
  "v_mfma_f32_16x16x32_bf16 v[124:127], v[40:43], v[12:15], v[124:127]\n\t" \
  "v_mfma_f32_16x16x32_bf16 v[124:127], v[44:47], v[28:31], v[124:127]\n\t" \
  "s_waitcnt lgkmcnt(4)\n\t" \
  "v_mfma_f32_16x16x32_bf16 v[64:67], v[48:51], v[0:3], v[64:67]\n\t" \
  "s_waitcnt lgkmcnt(0)\n\t" \
  "s_barrier\n\t" \
  "v_mfma_f32_16x16x32_bf16 v[64:67], v[52:55], v[16:19], v[64:67]\n\t" \
  "ds_read_b64_tr_b16 v[40:41], v58 offset:25600\n\t" \
  "v_mfma_f32_16x16x32_bf16 v[68:71], v[32:35], v[0:3], v[68:71]\n\t" \
  "ds_read_b128 v[0:3], v56 offset:28672\n\t" \
  "v_mfma_f32_16x16x32_bf16 v[68:71], v[36:39], v[16:19], v[68:71]\n\t" \
  "ds_read_b128 v[16:19], v57 offset:28672\n\t" \
  "v_mfma_f32_16x16x32_bf16 v[72:75], v[48:51], v[4:7], v[72:75]\n\t" \
  "ds_read_b64_tr_b16 v[42:43], v58 offset:26112\n\t" \
  "v_mfma_f32_16x16x32_bf16 v[72:75], v[52:55], v[20:23], v[72:75]\n\t" \
  "ds_read_b64_tr_b16 v[44:45], v60 offset:1024\n\t" \
  "v_mfma_f32_16x16x32_bf16 v[76:79], v[32:35], v[4:7], v[76:79]\n\t" \
  "ds_read_b128 v[4:7], v56 offset:30720\n\t" \
  "v_mfma_f32_16x16x32_bf16 v[76:79], v[36:39], v[20:23], v[76:79]\n\t" \
  "ds_read_b128 v[20:23], v57 offset:30720\n\t" \
  "v_mfma_f32_16x16x32_bf16 v[80:83], v[48:51], v[8:11], v[80:83]\n\t" \
  "ds_read_b64_tr_b16 v[46:47], v60 offset:1536\n\t" \
  "v_mfma_f32_16x16x32_bf16 v[80:83], v[52:55], v[24:27], v[80:83]\n\t" \
  "v_mfma_f32_16x16x32_bf16 v[84:87], v[32:35], v[8:11], v[84:87]\n\t" \
  "ds_read_b128 v[8:11], v56 offset:32768\n\t" \
  "v_mfma_f32_16x16x32_bf16 v[84:87], v[36:39], v[24:27], v[84:87]\n\t" \
  "ds_read_b128 v[24:27], v57 offset:32768\n\t" \
  "v_mfma_f32_16x16x32_bf16 v[88:91], v[48:51], v[12:15], v[88:91]\n\t" \
  "ds_read_b64_tr_b16 v[48:49], v58 offset:26624\n\t" \
  "v_mfma_f32_16x16x32_bf16 v[88:91], v[52:55], v[28:31], v[88:91]\n\t" \
  "ds_read_b64_tr_b16 v[50:51], v58 offset:27136\n\t" \
  "v_mfma_f32_16x16x32_bf16 v[92:95], v[32:35], v[12:15], v[92:95]\n\t" \
  "ds_read_b128 v[12:15], v56 offset:34816\n\t" \
  "v_mfma_f32_16x16x32_bf16 v[92:95], v[36:39], v[28:31], v[92:95]\n\t" \
  "ds_read_b128 v[28:31], v57 offset:34816\n\t" \
  "N1b_%=:\n\t" \
  "s_waitcnt lgkmcnt(10)\n\t" \
  "v_mfma_f32_16x16x32_bf16 v[96:99], v[40:43], v[0:3], v[96:99]\n\t" \
  "ds_read_b64_tr_b16 v[52:53], v60 offset:2048\n\t" \
  "s_waitcnt lgkmcnt(7)\n\t" \
  "v_mfma_f32_16x16x32_bf16 v[96:99], v[44:47], v[16:19], v[96:99]\n\t" \
  "ds_read_b64_tr_b16 v[54:55], v60 offset:2560\n\t" \
  "v_mfma_f32_16x16x32_bf16 v[104:107], v[40:43], v[4:7], v[104:107]\n\t" \
  "ds_read_b128 v[32:35], v56 offset:18432\n\t" \
  "v_mfma_f32_16x16x32_bf16 v[104:107], v[44:47], v[20:23], v[104:107]\n\t" \
  "ds_read_b128 v[36:39], v57 offset:18432\n\t" \
  "s_waitcnt lgkmcnt(8)\n\t" \
  "v_mfma_f32_16x16x32_bf16 v[112:115], v[40:43], v[8:11], v[112:115]\n\t" \
  "v_mfma_f32_16x16x32_bf16 v[112:115], v[44:47], v[24:27], v[112:115]\n\t" \
  "s_waitcnt lgkmcnt(4)\n\t" \
  "v_mfma_f32_16x16x32_bf16 v[120:123], v[40:43], v[12:15], v[120:123]\n\t" \
  "v_mfma_f32_16x16x32_bf16 v[120:123], v[44:47], v[28:31], v[120:123]\n\t" \
  "ds_read_b64_tr_b16 v[40:41], v59 offset:26624\n\t" \
  "v_mfma_f32_16x16x32_bf16 v[128:131], v[0:3], v[48:51], v[128:131]\n\t" \
  "ds_read_b64_tr_b16 v[42:43], v59 offset:27136\n\t" \
  "s_waitcnt lgkmcnt(4)\n\t" \
  "v_mfma_f32_16x16x32_bf16 v[128:131], v[16:19], v[52:55], v[128:131]\n\t" \
  "ds_read_b64_tr_b16 v[44:45], v61 offset:2048\n\t" \
  "v_mfma_f32_16x16x32_bf16 v[136:139], v[4:7], v[48:51], v[136:139]\n\t" \
  "v_mfma_f32_16x16x32_bf16 v[136:139], v[20:23], v[52:55], v[136:139]\n\t" \
  "ds_read_b64_tr_b16 v[46:47], v61 offset:2560\n\t" \
  "v_mfma_f32_16x16x32_bf16 v[144:147], v[8:11], v[48:51], v[144:147]\n\t" \
  "v_mfma_f32_16x16x32_bf16 v[144:147], v[24:27], v[52:55], v[144:147]\n\t" \
  "v_mfma_f32_16x16x32_bf16 v[152:155], v[12:15], v[48:51], v[152:155]\n\t" \
  "v_mfma_f32_16x16x32_bf16 v[152:155], v[28:31], v[52:55], v[152:155]\n\t" \
  "s_waitcnt lgkmcnt(4)\n\t" \
  "v_mfma_f32_16x16x32_bf16 v[160:163], v[32:35], v[48:51], v[160:163]\n\t" \
  "v_mfma_f32_16x16x32_bf16 v[160:163], v[36:39], v[52:55], v[160:163]\n\t" \
  "ds_read_b64_tr_b16 v[48:49], v59 offset:25600\n\t" \
  "s_waitcnt lgkmcnt(1)\n\t" \
  "v_mfma_f32_16x16x32_bf16 v[164:167], v[32:35], v[40:43], v[164:167]\n\t" \
  "ds_read_b64_tr_b16 v[50:51], v59 offset:26112\n\t" \
  "v_mfma_f32_16x16x32_bf16 v[164:167], v[36:39], v[44:47], v[164:167]\n\t" \
  "ds_read_b64_tr_b16 v[52:53], v61 offset:1024\n\t" \
  "v_mfma_f32_16x16x32_bf16 v[132:135], v[0:3], v[40:43], v[132:135]\n\t" \
  "ds_read_b64_tr_b16 v[54:55], v61 offset:1536\n\t" \
  "v_mfma_f32_16x16x32_bf16 v[132:135], v[16:19], v[44:47], v[132:135]\n\t" \
  "ds_read_b64_tr_b16 v[32:33], v58 offset:24576\n\t" \
  "v_mfma_f32_16x16x32_bf16 v[140:143], v[4:7], v[40:43], v[140:143]\n\t" \
  "ds_read_b64_tr_b16 v[34:35], v58 offset:25088\n\t" \
  "v_mfma_f32_16x16x32_bf16 v[140:143], v[20:23], v[44:47], v[140:143]\n\t" \
  "ds_read_b64_tr_b16 v[36:37], v60 offset:0\n\t" \
  "v_mfma_f32_16x16x32_bf16 v[148:151], v[8:11], v[40:43], v[148:151]\n\t" \
  "ds_read_b64_tr_b16 v[38:39], v60 offset:512\n\t" \
  "v_mfma_f32_16x16x32_bf16 v[148:151], v[24:27], v[44:47], v[148:151]\n\t" \
  "v_mfma_f32_16x16x32_bf16 v[156:159], v[12:15], v[40:43], v[156:159]\n\t" \
  "ds_read_b64_tr_b16 v[40:41], v59 offset:24576\n\t" \
  "v_mfma_f32_16x16x32_bf16 v[156:159], v[28:31], v[44:47], v[156:159]\n\t" \
  "ds_read_b64_tr_b16 v[42:43], v59 offset:25088\n\t" \
  "s_waitcnt lgkmcnt(6)\n\t" \
  "v_mfma_f32_16x16x32_bf16 v[100:103], v[48:51], v[0:3], v[100:103]\n\t" \
  "ds_read_b64_tr_b16 v[44:45], v61 offset:0\n\t" \
  "v_mfma_f32_16x16x32_bf16 v[100:103], v[52:55], v[16:19], v[100:103]\n\t" \
  "ds_read_b64_tr_b16 v[46:47], v61 offset:512\n\t" \
  "v_mfma_f32_16x16x32_bf16 v[108:111], v[48:51], v[4:7], v[108:111]\n\t" \
  "v_mfma_f32_16x16x32_bf16 v[108:111], v[52:55], v[20:23], v[108:111]\n\t" \
  "v_mfma_f32_16x16x32_bf16 v[116:119], v[48:51], v[8:11], v[116:119]\n\t" \
  "v_mfma_f32_16x16x32_bf16 v[116:119], v[52:55], v[24:27], v[116:119]\n\t" \
  "v_mfma_f32_16x16x32_bf16 v[124:127], v[48:51], v[12:15], v[124:127]\n\t" \
  "v_mfma_f32_16x16x32_bf16 v[124:127], v[52:55], v[28:31], v[124:127]\n\t" \
  "s_waitcnt lgkmcnt(4)\n\t" \
  "v_mfma_f32_16x16x32_bf16 v[64:67], v[32:35], v[0:3], v[64:67]\n\t" \
  "s_waitcnt lgkmcnt(0)\n\t" \
  "s_barrier\n\t" \
  "v_mfma_f32_16x16x32_bf16 v[64:67], v[36:39], v[16:19], v[64:67]\n\t" \
  "ds_read_b64_tr_b16 v[48:49], v60 offset:13312\n\t" \
  "v_mfma_f32_16x16x32_bf16 v[68:71], v[40:43], v[0:3], v[68:71]\n\t" \
  "ds_read_b128 v[0:3], v56 offset:47104\n\t" \
  "v_mfma_f32_16x16x32_bf16 v[68:71], v[44:47], v[16:19], v[68:71]\n\t" \
  "ds_read_b128 v[16:19], v57 offset:47104\n\t" \
  "v_mfma_f32_16x16x32_bf16 v[72:75], v[32:35], v[4:7], v[72:75]\n\t" \
  "ds_read_b64_tr_b16 v[50:51], v60 offset:13824\n\t" \
  "v_mfma_f32_16x16x32_bf16 v[72:75], v[36:39], v[20:23], v[72:75]\n\t" \
  "ds_read_b64_tr_b16 v[52:53], v60 offset:25600\n\t" \
  "v_mfma_f32_16x16x32_bf16 v[76:79], v[40:43], v[4:7], v[76:79]\n\t" \
  "ds_read_b128 v[4:7], v56 offset:49152\n\t" \
  "v_mfma_f32_16x16x32_bf16 v[76:79], v[44:47], v[20:23], v[76:79]\n\t" \
  "ds_read_b128 v[20:23], v57 offset:49152\n\t" \
  "v_mfma_f32_16x16x32_bf16 v[80:83], v[32:35], v[8:11], v[80:83]\n\t" \
  "ds_read_b64_tr_b16 v[54:55], v60 offset:26112\n\t" \
  "v_mfma_f32_16x16x32_bf16 v[80:83], v[36:39], v[24:27], v[80:83]\n\t" \
  "v_mfma_f32_16x16x32_bf16 v[84:87], v[40:43], v[8:11], v[84:87]\n\t" \
  "ds_read_b128 v[8:11], v56 offset:51200\n\t" \
  "v_mfma_f32_16x16x32_bf16 v[84:87], v[44:47], v[24:27], v[84:87]\n\t" \
  "ds_read_b128 v[24:27], v57 offset:51200\n\t" \
  "v_mfma_f32_16x16x32_bf16 v[88:91], v[32:35], v[12:15], v[88:91]\n\t" \
  "ds_read_b64_tr_b16 v[32:33], v60 offset:14336\n\t" \
  "v_mfma_f32_16x16x32_bf16 v[88:91], v[36:39], v[28:31], v[88:91]\n\t" \
  "ds_read_b64_tr_b16 v[34:35], v60 offset:14848\n\t" \
  "v_mfma_f32_16x16x32_bf16 v[92:95], v[40:43], v[12:15], v[92:95]\n\t" \
  "ds_read_b128 v[12:15], v56 offset:53248\n\t" \
  "v_mfma_f32_16x16x32_bf16 v[92:95], v[44:47], v[28:31], v[92:95]\n\t" \
  "ds_read_b128 v[28:31], v57 offset:53248\n\t" \
  "N2b_%=:\n\t" \
  "s_waitcnt lgkmcnt(10)\n\t" \
  "v_mfma_f32_16x16x32_bf16 v[96:99], v[48:51], v[0:3], v[96:99]\n\t" \
  "ds_read_b64_tr_b16 v[36:37], v60 offset:26624\n\t" \
  "s_waitcnt lgkmcnt(7)\n\t" \
  "v_mfma_f32_16x16x32_bf16 v[96:99], v[52:55], v[16:19], v[96:99]\n\t" \
  "ds_read_b64_tr_b16 v[38:39], v60 offset:27136\n\t" \
  "v_mfma_f32_16x16x32_bf16 v[104:107], v[48:51], v[4:7], v[104:107]\n\t" \
  "ds_read_b128 v[40:43], v56 offset:36864\n\t" \
  "v_mfma_f32_16x16x32_bf16 v[104:107], v[52:55], v[20:23], v[104:107]\n\t" \
  "ds_read_b128 v[44:47], v57 offset:36864\n\t" \
  "s_waitcnt lgkmcnt(8)\n\t" \
  "v_mfma_f32_16x16x32_bf16 v[112:115], v[48:51], v[8:11], v[112:115]\n\t" \
  "v_mfma_f32_16x16x32_bf16 v[112:115], v[52:55], v[24:27], v[112:115]\n\t" \
  "s_waitcnt lgkmcnt(4)\n\t" \
  "v_mfma_f32_16x16x32_bf16 v[120:123], v[48:51], v[12:15], v[120:123]\n\t" \
  "v_mfma_f32_16x16x32_bf16 v[120:123], v[52:55], v[28:31], v[120:123]\n\t" \
  "ds_read_b64_tr_b16 v[48:49], v61 offset:14336\n\t" \
  "v_mfma_f32_16x16x32_bf16 v[128:131], v[0:3], v[32:35], v[128:131]\n\t" \
  "ds_read_b64_tr_b16 v[50:51], v61 offset:14848\n\t" \
  "s_waitcnt lgkmcnt(4)\n\t" \
  "v_mfma_f32_16x16x32_bf16 v[128:131], v[16:19], v[36:39], v[128:131]\n\t" \
  "ds_read_b64_tr_b16 v[52:53], v61 offset:26624\n\t" \
  "v_mfma_f32_16x16x32_bf16 v[136:139], v[4:7], v[32:35], v[136:139]\n\t" \
  "v_mfma_f32_16x16x32_bf16 v[136:139], v[20:23], v[36:39], v[136:139]\n\t" \
  "ds_read_b64_tr_b16 v[54:55], v61 offset:27136\n\t" \
  "v_mfma_f32_16x16x32_bf16 v[144:147], v[8:11], v[32:35], v[144:147]\n\t" \
  "v_mfma_f32_16x16x32_bf16 v[144:147], v[24:27], v[36:39], v[144:147]\n\t" \
  "v_mfma_f32_16x16x32_bf16 v[152:155], v[12:15], v[32:35], v[152:155]\n\t" \
  "v_mfma_f32_16x16x32_bf16 v[152:155], v[28:31], v[36:39], v[152:155]\n\t" \
  "s_waitcnt lgkmcnt(4)\n\t" \
  "v_mfma_f32_16x16x32_bf16 v[160:163], v[40:43], v[32:35], v[160:163]\n\t" \
  "v_mfma_f32_16x16x32_bf16 v[160:163], v[44:47], v[36:39], v[160:163]\n\t" \
  "ds_read_b64_tr_b16 v[32:33], v61 offset:13312\n\t" \
  "s_waitcnt lgkmcnt(1)\n\t" \
  "v_mfma_f32_16x16x32_bf16 v[164:167], v[40:43], v[48:51], v[164:167]\n\t" \
  "ds_read_b64_tr_b16 v[34:35], v61 offset:13824\n\t" \
  "v_mfma_f32_16x16x32_bf16 v[164:167], v[44:47], v[52:55], v[164:167]\n\t" \
  "ds_read_b64_tr_b16 v[36:37], v61 offset:25600\n\t" \
  "v_mfma_f32_16x16x32_bf16 v[132:135], v[0:3], v[48:51], v[132:135]\n\t" \
  "ds_read_b64_tr_b16 v[38:39], v61 offset:26112\n\t" \
  "v_mfma_f32_16x16x32_bf16 v[132:135], v[16:19], v[52:55], v[132:135]\n\t" \
  "ds_read_b64_tr_b16 v[40:41], v60 offset:12288\n\t" \
  "v_mfma_f32_16x16x32_bf16 v[140:143], v[4:7], v[48:51], v[140:143]\n\t" \
  "ds_read_b64_tr_b16 v[42:43], v60 offset:12800\n\t" \
  "v_mfma_f32_16x16x32_bf16 v[140:143], v[20:23], v[52:55], v[140:143]\n\t" \
  "ds_read_b64_tr_b16 v[44:45], v60 offset:24576\n\t" \
  "v_mfma_f32_16x16x32_bf16 v[148:151], v[8:11], v[48:51], v[148:151]\n\t" \
  "ds_read_b64_tr_b16 v[46:47], v60 offset:25088\n\t" \
  "v_mfma_f32_16x16x32_bf16 v[148:151], v[24:27], v[52:55], v[148:151]\n\t" \
  "v_mfma_f32_16x16x32_bf16 v[156:159], v[12:15], v[48:51], v[156:159]\n\t" \
  "ds_read_b64_tr_b16 v[48:49], v61 offset:12288\n\t" \
  "v_mfma_f32_16x16x32_bf16 v[156:159], v[28:31], v[52:55], v[156:159]\n\t" \
  "ds_read_b64_tr_b16 v[50:51], v61 offset:12800\n\t" \
  "s_waitcnt lgkmcnt(6)\n\t" \
  "v_mfma_f32_16x16x32_bf16 v[100:103], v[32:35], v[0:3], v[100:103]\n\t" \
  "ds_read_b64_tr_b16 v[52:53], v61 offset:24576\n\t" \
  "v_mfma_f32_16x16x32_bf16 v[100:103], v[36:39], v[16:19], v[100:103]\n\t" \
  "ds_read_b64_tr_b16 v[54:55], v61 offset:25088\n\t" \
  "v_mfma_f32_16x16x32_bf16 v[108:111], v[32:35], v[4:7], v[108:111]\n\t" \
  "v_mfma_f32_16x16x32_bf16 v[108:111], v[36:39], v[20:23], v[108:111]\n\t" \
  "s_addk_i32 %[it], 1\n\t" \
  "v_mfma_f32_16x16x32_bf16 v[116:119], v[32:35], v[8:11], v[116:119]\n\t" \
  "s_cmp_lt_u32 %[it], 6\n\t" \
  "v_mfma_f32_16x16x32_bf16 v[116:119], v[36:39], v[24:27], v[116:119]\n\t" \
  "v_mfma_f32_16x16x32_bf16 v[124:127], v[32:35], v[12:15], v[124:127]\n\t" \
  "v_mfma_f32_16x16x32_bf16 v[124:127], v[36:39], v[28:31], v[124:127]\n\t" \
  "s_waitcnt lgkmcnt(4)\n\t" \
  "v_mfma_f32_16x16x32_bf16 v[64:67], v[40:43], v[0:3], v[64:67]\n\t" \
  "s_waitcnt lgkmcnt(0)\n\t" \
  "s_barrier\n\t" \
  "v_mfma_f32_16x16x32_bf16 v[64:67], v[44:47], v[16:19], v[64:67]\n\t" \
  "ds_read_b64_tr_b16 v[32:33], v58 offset:1024\n\t" \
  "v_mfma_f32_16x16x32_bf16 v[68:71], v[48:51], v[0:3], v[68:71]\n\t" \
  "ds_read_b128 v[0:3], v56 offset:10240\n\t" \
  "v_mfma_f32_16x16x32_bf16 v[68:71], v[52:55], v[16:19], v[68:71]\n\t" \
  "ds_read_b128 v[16:19], v57 offset:10240\n\t" \
  "v_mfma_f32_16x16x32_bf16 v[72:75], v[40:43], v[4:7], v[72:75]\n\t" \
  "ds_read_b64_tr_b16 v[34:35], v58 offset:1536\n\t" \
  "v_mfma_f32_16x16x32_bf16 v[72:75], v[44:47], v[20:23], v[72:75]\n\t" \
  "ds_read_b64_tr_b16 v[36:37], v58 offset:13312\n\t" \
  "v_mfma_f32_16x16x32_bf16 v[76:79], v[48:51], v[4:7], v[76:79]\n\t" \
  "ds_read_b128 v[4:7], v56 offset:12288\n\t" \
  "v_mfma_f32_16x16x32_bf16 v[76:79], v[52:55], v[20:23], v[76:79]\n\t" \
  "ds_read_b128 v[20:23], v57 offset:12288\n\t" \
  "v_mfma_f32_16x16x32_bf16 v[80:83], v[40:43], v[8:11], v[80:83]\n\t" \
  "ds_read_b64_tr_b16 v[38:39], v58 offset:13824\n\t" \
  "v_mfma_f32_16x16x32_bf16 v[80:83], v[44:47], v[24:27], v[80:83]\n\t" \
  "v_mfma_f32_16x16x32_bf16 v[84:87], v[48:51], v[8:11], v[84:87]\n\t" \
  "ds_read_b128 v[8:11], v56 offset:14336\n\t" \
  "v_mfma_f32_16x16x32_bf16 v[84:87], v[52:55], v[24:27], v[84:87]\n\t" \
  "ds_read_b128 v[24:27], v57 offset:14336\n\t" \
  "v_mfma_f32_16x16x32_bf16 v[88:91], v[40:43], v[12:15], v[88:91]\n\t" \
  "ds_read_b64_tr_b16 v[40:41], v58 offset:2048\n\t" \
  "v_mfma_f32_16x16x32_bf16 v[88:91], v[44:47], v[28:31], v[88:91]\n\t" \
  "ds_read_b64_tr_b16 v[42:43], v58 offset:2560\n\t" \
  "v_mfma_f32_16x16x32_bf16 v[92:95], v[48:51], v[12:15], v[92:95]\n\t" \
  "ds_read_b128 v[12:15], v56 offset:16384\n\t" \
  "v_mfma_f32_16x16x32_bf16 v[92:95], v[52:55], v[28:31], v[92:95]\n\t" \
  "ds_read_b128 v[28:31], v57 offset:16384\n\t" \
  "s_cbranch_scc1 N0b_%=\n\t" \
  "s_setprio 3\n\t" \
  "s_waitcnt lgkmcnt(0)\n\t" \
  "v_cvt_pk_bf16_f32 v160, v160, v161\n\t" \
  "v_cvt_pk_bf16_f32 v161, v162, v163\n\t" \
  "v_cvt_pk_bf16_f32 v162, v164, v165\n\t" \
  "v_cvt_pk_bf16_f32 v163, v166, v167\n\t" \
  "ds_write_b128 v63, v[160:163] offset:2048\n\t" \
  "v_mov_b32 v57, %[fa]\n\t" \
  "v_mov_b32 v58, 1\n\t" \
  "s_mov_b64 exec, 1\n\t" \
  "ds_write_b32 v57, v58\n\t" \
  "s_mov_b64 exec, -1\n\t" \
  "v_cvt_pk_bf16_f32 v120, v120, v121\n\t" \
  "v_cvt_pk_bf16_f32 v121, v122, v123\n\t" \
  "v_cvt_pk_bf16_f32 v122, v124, v125\n\t" \
  "v_cvt_pk_bf16_f32 v123, v126, v127\n\t" \
  "v_cvt_pk_bf16_f32 v28, v152, v153\n\t" \
  "v_cvt_pk_bf16_f32 v29, v154, v155\n\t" \
  "v_cvt_pk_bf16_f32 v38, v156, v157\n\t" \
  "v_cvt_pk_bf16_f32 v39, v158, v159\n\t" \
  "NX_%=:\n\t" \
  "v_cvt_pk_bf16_f32 v96, v96, v97\n\t" \
  "v_cvt_pk_bf16_f32 v97, v98, v99\n\t" \
  "v_cvt_pk_bf16_f32 v98, v100, v101\n\t" \
  "v_cvt_pk_bf16_f32 v99, v102, v103\n\t" \
  "v_cvt_pk_bf16_f32 v104, v104, v105\n\t" \
  "v_cvt_pk_bf16_f32 v105, v106, v107\n\t" \
  "v_cvt_pk_bf16_f32 v106, v108, v109\n\t" \
  "v_cvt_pk_bf16_f32 v107, v110, v111\n\t" \
  "v_mov_b32 v60, 0xff61b1e6\n\t" \
  "v_cndmask_b32_e64 v56, v60, 0, s[68:69]\n\t" \
  "v_cndmask_b32_e64 v57, v60, 0, s[70:71]\n\t" \
  "v_cndmask_b32_e64 v58, v60, 0, s[72:73]\n\t" \
  "v_cndmask_b32_e64 v59, v60, 0, s[74:75]\n\t" \
  "v_cvt_pk_bf16_f32 v72, v72, v73\n\t" \
  "v_cvt_pk_bf16_f32 v73, v74, v75\n\t" \
  "v_cvt_pk_bf16_f32 v74, v76, v77\n\t" \
  "v_cvt_pk_bf16_f32 v75, v78, v79\n\t" \
  "v_cndmask_b32_e64 v52, v60, 0, s[76:77]\n\t" \
  "v_cndmask_b32_e64 v53, v60, 0, s[78:79]\n\t" \
  "v_cndmask_b32_e64 v54, v60, 0, s[80:81]\n\t" \
  "v_cndmask_b32_e64 v55, v60, 0, s[82:83]\n\t" \
  "v_cvt_pk_bf16_f32 v24, v136, v137\n\t" \
  "v_cvt_pk_bf16_f32 v25, v138, v139\n\t" \
  "v_cvt_pk_bf16_f32 v34, v140, v141\n\t" \
  "v_cvt_pk_bf16_f32 v35, v142, v143\n\t" \
  "v_cvt_pk_bf16_f32 v26, v144, v145\n\t" \
  "v_cvt_pk_bf16_f32 v27, v146, v147\n\t" \
  "v_cvt_pk_bf16_f32 v36, v148, v149\n\t" \
  "v_cvt_pk_bf16_f32 v37, v150, v151\n\t" \
  "v_mfma_f32_16x16x32_bf16 v[136:139], v[96:99], v[72:75], v[56:59]\n\t" \
  "v_cvt_pk_bf16_f32 v112, v112, v113\n\t" \
  "v_cvt_pk_bf16_f32 v113, v114, v115\n\t" \
  "v_mfma_f32_16x16x32_bf16 v[140:143], v[104:107], v[72:75], v[52:55]\n\t" \
  "v_cvt_pk_bf16_f32 v114, v116, v117\n\t" \
  "v_cvt_pk_bf16_f32 v115, v118, v119\n\t" \
  "v_cvt_pk_bf16_f32 v80, v80, v81\n\t" \
  "v_cvt_pk_bf16_f32 v81, v82, v83\n\t" \
  "v_cvt_pk_bf16_f32 v82, v84, v85\n\t" \
  "v_cvt_pk_bf16_f32 v83, v86, v87\n\t" \
  "v_cvt_pk_bf16_f32 v22, v128, v129\n\t" \
  "v_cvt_pk_bf16_f32 v23, v130, v131\n\t" \
  "v_mfma_f32_16x16x32_bf16 v[144:147], v[104:107], v[80:83], v[56:59]\n\t" \
  "v_cvt_pk_bf16_f32 v32, v132, v133\n\t" \
  "v_cvt_pk_bf16_f32 v33, v134, v135\n\t" \
  "v_mfma_f32_16x16x32_bf16 v[148:151], v[112:115], v[80:83], v[52:55]\n\t" \
  "v_cvt_pk_bf16_f32 v88, v88, v89\n\t" \
  "v_cvt_pk_bf16_f32 v89, v90, v91\n\t" \
  "v_cvt_pk_bf16_f32 v90, v92, v93\n\t" \
  "v_cvt_pk_bf16_f32 v91, v94, v95\n\t" \
  "v_mov_b32 v61, %[fp]\n\t" \
  "v_add_u32 v60, %[vo], v63\n\t" \
  "v_mfma_f32_16x16x32_bf16 v[152:155], v[112:115], v[88:91], v[56:59]\n\t" \
  "ds_read_b32 v0, v61\n\t" \
  "ds_read_b64 v[20:21], v60\n\t" \
  "ds_read_b64 v[30:31], v60 offset:8\n\t" \
  "s_mov_b64 exec, %[rm]\n\t" \
  "ds_read_b128 v[160:163], v63\n\t" \
  "s_mov_b64 exec, -1\n\t" \
  "v_mfma_f32_16x16x32_bf16 v[156:159], v[120:123], v[88:91], v[52:55]\n\t" \
  "v_cvt_pk_bf16_f32 v64, v64, v65\n\t" \
  "v_cvt_pk_bf16_f32 v65, v66, v67\n\t" \
  "v_cvt_pk_bf16_f32 v66, v68, v69\n\t" \
  "v_cvt_pk_bf16_f32 v67, v70, v71\n\t" \
  "v_max_f32 v136, v136, v137\n\t" \
  "v_max_f32 v138, v138, v139\n\t" \
  "v_mfma_f32_16x16x32_bf16 v[132:135], v[96:99], v[64:67], v[52:55]\n\t" \
  "v_max_f32 v140, v140, v141\n\t" \
  "v_max_f32 v142, v142, v143\n\t" \
  "AW_%=:\n\t" \
  "s_waitcnt lgkmcnt(0)\n\t" \
  "v_cmp_eq_u32 vcc, 0, v0\n\t" \
  "v_max3_f32 v5, v136, v138, v140\n\t" \
  "v_max_f32 v5, v5, v142\n\t" \
  "s_cbranch_vccnz AR_%=\n\t" \
  "s_cmp_eq_u32 %[z], 0\n\t" \
  "s_cbranch_scc1 AZ_%=\n\t" \
  "AY_%=:\n\t" \
  "v_mfma_f32_16x16x32_bf16 v[128:131], v[160:163], v[64:67], v[56:59]\n\t" \
  "v_max_f32 v144, v144, v145\n\t" \
  "v_max_f32 v146, v146, v147\n\t" \
  "v_max_f32 v148, v148, v149\n\t" \
  "v_max_f32 v150, v150, v151\n\t" \
  "v_max3_f32 v6, v144, v146, v148\n\t" \
  "v_max_f32 v6, v6, v150\n\t" \
  "v_max_f32 v152, v152, v153\n\t" \
  "v_max_f32 v154, v154, v155\n\t" \
  "v_max_f32 v156, v156, v157\n\t" \
  "v_max_f32 v158, v158, v159\n\t" \
  "v_max3_f32 v7, v152, v154, v156\n\t" \
  "v_max_f32 v7, v7, v158\n\t" \
  "v_mov_b32 v8, 0xc0c0100\n\t" \
  "v_mov_b32 v9, 0x1000c0c\n\t" \
  "v_max_f32 v128, v128, v129\n\t" \
  "v_max_f32 v130, v130, v131\n\t" \
  "v_max_f32 v132, v132, v133\n\t" \
  "v_max_f32 v134, v134, v135\n\t" \
  "v_max3_f32 v4, v128, v130, v132\n\t" \
  "v_max_f32 v4, v4, v134\n\t" \
  "v_cndmask_b32_e64 v60, v8, v9, s[84:85]\n\t" \
  "v_mov_b32 v8, 0xc0c0302\n\t" \
  "v_permlane16_swap_b32 v6, v7\n\t" \
  "v_permlane16_swap_b32 v4, v5\n\t" \
  "v_max_f32 v6, v6, v7\n\t" \
  "v_max_f32 v4, v4, v5\n\t" \
  "v_mov_b32 v9, 0x3020c0c\n\t" \
  "v_cndmask_b32_e64 v61, v8, v9, s[84:85]\n\t" \
  "v_permlane32_swap_b32 v4, v6\n\t" \
  "v_max_f32 v4, v4, v6\n\t" \
  "v_mul_f32 v6, 0xbe8293ee, v4\n\t" \
  "v_mul_f32 v4, 0xbe8293ee, v4\n\t" \
  "s_nop 1\n\t" \
  "v_permlane32_swap_b32 v4, v6\n\t" \
  "v_mov_b32 v5, v4\n\t" \
  "v_mov_b32 v7, v6\n\t" \
  "s_nop 0\n\t" \
  "v_permlane16_swap_b32 v4, v5\n\t" \
  "v_permlane16_swap_b32 v6, v7\n\t" \
  "v_fmamk_f32 v128, v128, 0x3e8293ee, v4\n\t" \
  "v_fmamk_f32 v130, v130, 0x3e8293ee, v4\n\t" \
  "v_fmamk_f32 v132, v132, 0x3e8293ee, v4\n\t" \
  "v_fmamk_f32 v134, v134, 0x3e8293ee, v4\n\t" \
  "v_fmamk_f32 v136, v136, 0x3e8293ee, v5\n\t" \
  "v_fmamk_f32 v138, v138, 0x3e8293ee, v5\n\t" \
  "v_fmamk_f32 v140, v140, 0x3e8293ee, v5\n\t" \
  "v_fmamk_f32 v142, v142, 0x3e8293ee, v5\n\t" \
  "v_fmamk_f32 v144, v144, 0x3e8293ee, v6\n\t" \
  "v_fmamk_f32 v146, v146, 0x3e8293ee, v6\n\t" \
  "v_fmamk_f32 v148, v148, 0x3e8293ee, v6\n\t" \
  "v_fmamk_f32 v150, v150, 0x3e8293ee, v6\n\t" \
  "v_fmamk_f32 v152, v152, 0x3e8293ee, v7\n\t" \
  "v_fmamk_f32 v154, v154, 0x3e8293ee, v7\n\t" \
  "v_fmamk_f32 v156, v156, 0x3e8293ee, v7\n\t" \
  "v_fmamk_f32 v158, v158, 0x3e8293ee, v7\n\t" \
  "v_exp_f32 v128, v128\n\t" \
  "v_exp_f32 v130, v130\n\t" \
  "v_exp_f32 v132, v132\n\t" \
  "v_exp_f32 v134, v134\n\t" \
  "v_exp_f32 v136, v136\n\t" \
  "v_exp_f32 v138, v138\n\t" \
  "v_exp_f32 v140, v140\n\t" \
  "v_exp_f32 v142, v142\n\t" \
  "v_exp_f32 v144, v144\n\t" \
  "v_exp_f32 v146, v146\n\t" \
  "v_exp_f32 v148, v148\n\t" \
  "v_exp_f32 v150, v150\n\t" \
  "v_exp_f32 v152, v152\n\t" \
  "v_exp_f32 v154, v154\n\t" \
  "v_exp_f32 v156, v156\n\t" \
  "v_exp_f32 v158, v158\n\t" \
  "v_add_f32 v4, v128, v130\n\t" \
  "v_add_f32 v8, v132, v134\n\t" \
  "v_add_f32 v5, v136, v138\n\t" \
  "v_add_f32 v9, v140, v142\n\t" \
  "v_add_f32 v6, v144, v146\n\t" \
  "v_add_f32 v10, v148, v150\n\t" \
  "v_add_f32 v7, v152, v154\n\t" \
  "v_add_f32 v11, v156, v158\n\t" \
  "v_add_f32 v4, v4, v8\n\t" \
  "v_add_f32 v5, v5, v9\n\t" \
  "v_add_f32 v6, v6, v10\n\t" \
  "v_add_f32 v7, v7, v11\n\t" \
  "v_permlane16_swap_b32 v4, v5\n\t" \
  "s_nop 0\n\t" \
  "v_permlane16_swap_b32 v6, v7\n\t" \
  "v_add_f32 v4, v4, v5\n\t" \
  "v_add_f32 v6, v6, v7\n\t" \
  "s_nop 1\n\t" \
  "v_permlane32_swap_b32 v4, v6\n\t" \
  "v_add_f32 v4, v4, v6\n\t" \
  "v_rcp_f32 v4, v4\n\t" \
  "s_nop 0\n\t" \
  "v_mov_b32 v6, v4\n\t" \
  "s_nop 1\n\t" \
  "v_permlane32_swap_b32 v4, v6\n\t" \
  "v_mov_b32 v5, v4\n\t" \
  "v_mov_b32 v7, v6\n\t" \
  "s_nop 0\n\t" \
  "v_permlane16_swap_b32 v4, v5\n\t" \
  "v_permlane16_swap_b32 v6, v7\n\t" \
  "v_mul_f32 v128, v128, v4\n\t" \
  "v_mul_f32 v130, v130, v4\n\t" \
  "v_mul_f32 v132, v132, v4\n\t" \
  "v_mul_f32 v134, v134, v4\n\t" \
  "v_cvt_pk_bf16_f32 v128, v128, v130\n\t" \
  "v_cvt_pk_bf16_f32 v132, v132, v134\n\t" \
  "v_perm_b32 v68, 0, v128, v60\n\t" \
  "v_perm_b32 v69, 0, v128, v61\n\t" \
  "v_perm_b32 v70, 0, v132, v60\n\t" \
  "v_perm_b32 v71, 0, v132, v61\n\t" \
  "v_mul_f32 v136, v136, v5\n\t" \
  "v_mul_f32 v138, v138, v5\n\t" \
  "v_mfma_f32_16x16x32_bf16 v[128:131], v[20:23], v[68:71], 0\n\t" \
  "v_mul_f32 v140, v140, v5\n\t" \
  "v_mul_f32 v142, v142, v5\n\t" \
  "v_cvt_pk_bf16_f32 v136, v136, v138\n\t" \
  "v_mfma_f32_16x16x32_bf16 v[132:135], v[30:33], v[68:71], 0\n\t" \
  "v_cvt_pk_bf16_f32 v140, v140, v142\n\t" \
  "v_perm_b32 v76, 0, v136, v60\n\t" \
  "v_perm_b32 v77, 0, v136, v61\n\t" \
  "v_perm_b32 v78, 0, v140, v60\n\t" \
  "v_perm_b32 v79, 0, v140, v61\n\t" \
  "v_mul_f32 v144, v144, v6\n\t" \
  "v_mul_f32 v146, v146, v6\n\t" \
  "v_mfma_f32_16x16x32_bf16 v[136:139], v[22:25], v[76:79], 0\n\t" \
  "v_mul_f32 v148, v148, v6\n\t" \
  "v_mul_f32 v150, v150, v6\n\t" \
  "v_cvt_pk_bf16_f32 v144, v144, v146\n\t" \
  "v_mfma_f32_16x16x32_bf16 v[140:143], v[32:35], v[76:79], 0\n\t" \
  "v_cvt_pk_bf16_f32 v148, v148, v150\n\t" \
  "v_perm_b32 v84, 0, v144, v60\n\t" \
  "v_perm_b32 v85, 0, v144, v61\n\t" \
  "v_perm_b32 v86, 0, v148, v60\n\t" \
  "v_perm_b32 v87, 0, v148, v61\n\t" \
  "v_cvt_pk_bf16_f32 v100, v128, v129\n\t" \
  "v_cvt_pk_bf16_f32 v101, v130, v131\n\t" \
  "v_mfma_f32_16x16x32_bf16 v[144:147], v[24:27], v[84:87], 0\n\t" \
  "v_cvt_pk_bf16_f32 v102, v132, v133\n\t" \
  "v_cvt_pk_bf16_f32 v103, v134, v135\n\t" \
  "global_store_dwordx4 v62, v[100:103], %[ot] offset:0 sc1\n\t" \
  "v_mfma_f32_16x16x32_bf16 v[148:151], v[34:37], v[84:87], 0\n\t" \
  "v_mul_f32 v152, v152, v7\n\t" \
  "v_mul_f32 v154, v154, v7\n\t" \
  "v_mul_f32 v156, v156, v7\n\t" \
  "v_mul_f32 v158, v158, v7\n\t" \
  "v_cvt_pk_bf16_f32 v152, v152, v154\n\t" \
  "v_cvt_pk_bf16_f32 v156, v156, v158\n\t" \
  "v_perm_b32 v92, 0, v152, v60\n\t" \
  "v_perm_b32 v93, 0, v152, v61\n\t" \
  "v_perm_b32 v94, 0, v156, v60\n\t" \
  "v_perm_b32 v95, 0, v156, v61\n\t" \
  "v_cvt_pk_bf16_f32 v108, v136, v137\n\t" \
  "v_cvt_pk_bf16_f32 v109, v138, v139\n\t" \
  "v_mfma_f32_16x16x32_bf16 v[152:155], v[26:29], v[92:95], 0\n\t" \
  "v_cvt_pk_bf16_f32 v110, v140, v141\n\t" \
  "v_cvt_pk_bf16_f32 v111, v142, v143\n\t" \
  "global_store_dwordx4 v62, v[108:111], %[ot] offset:1024 sc1\n\t" \
  "v_mfma_f32_16x16x32_bf16 v[156:159], v[36:39], v[92:95], 0\n\t" \
  "v_cvt_pk_bf16_f32 v116, v144, v145\n\t" \
  "v_cvt_pk_bf16_f32 v117, v146, v147\n\t" \
  "v_cvt_pk_bf16_f32 v118, v148, v149\n\t" \
  "v_cvt_pk_bf16_f32 v119, v150, v151\n\t" \
  "global_store_dwordx4 v62, v[116:119], %[ou] offset:0 sc1\n\t" \
  "v_cvt_pk_bf16_f32 v124, v152, v153\n\t" \
  "v_cvt_pk_bf16_f32 v125, v154, v155\n\t" \
  "s_nop 0\n\t" \
  "v_cvt_pk_bf16_f32 v126, v156, v157\n\t" \
  "v_cvt_pk_bf16_f32 v127, v158, v159\n\t" \
  "global_store_dwordx4 v62, v[124:127], %[ou] offset:1024 sc1\n\t" \
  "s_branch AE_%=\n\t" \
  "AR_%=:\n\t" \
  "s_addk_i32 %[it], 1\n\t" \
  "s_cmp_gt_u32 %[it], 0x400000\n\t" \
  "s_cbranch_scc1 AY_%=\n\t" \
  "ds_read_b32 v0, v61\n\t" \
  "ds_read_b64 v[20:21], v60\n\t" \
  "ds_read_b64 v[30:31], v60 offset:8\n\t" \
  "s_mov_b64 exec, %[rm]\n\t" \
  "ds_read_b128 v[160:163], v63\n\t" \
  "s_mov_b64 exec, -1\n\t" \
  "s_branch AW_%=\n\t" \
  "AZ_%=:\n\t" \
  "v_mov_b32 v0, 0xff61b1e6\n\t" \
  "v_mov_b32 v1, 0xcfa8965f\n\t" \
  "s_nop 3\n\t" \
  "v_cndmask_b32_e64 v56, v0, v1, s[68:69]\n\t" \
  "v_cndmask_b32_e64 v57, v0, v1, s[70:71]\n\t" \
  "v_cndmask_b32_e64 v58, v0, v1, s[72:73]\n\t" \
  "v_cndmask_b32_e64 v59, v0, v1, s[74:75]\n\t" \
  "s_nop 1\n\t" \
  "s_branch AY_%=\n\t" \
  "AE_%=:\n\t" \
  "s_nop 1\n\t"
#define N2_CLOB "v0", "v1", "v2", "v3", "v4", "v5", "v6", "v7", "v8", "v9", "v10", "v11", "v12", "v13", "v14", "v15", "v16", "v17", "v18", "v19", "v20", "v21", "v22", "v23", "v24", "v25", "v26", "v27", "v28", "v29", "v30", "v31", "v32", "v33", "v34", "v35", "v36", "v37", "v38", "v39", "v40", "v41", "v42", "v43", "v44", "v45", "v46", "v47", "v48", "v49", "v50", "v51", "v52", "v53", "v54", "v55", "v60", "v61", "v64", "v65", "v66", "v67", "v68", "v69", "v70", "v71", "v72", "v73", "v74", "v75", "v76", "v77", "v78", "v79", "v80", "v81", "v82", "v83", "v84", "v85", "v86", "v87", "v88", "v89", "v90", "v91", "v92", "v93", "v94", "v95", "v96", "v97", "v98", "v99", "v100", "v101", "v102", "v103", "v104", "v105", "v106", "v107", "v108", "v109", "v110", "v111", "v112", "v113", "v114", "v115", "v116", "v117", "v118", "v119", "v120", "v121", "v122", "v123", "v124", "v125", "v126", "v127", "v128", "v129", "v130", "v131", "v132", "v133", "v134", "v135", "v136", "v137", "v138", "v139", "v140", "v141", "v142", "v143", "v144", "v145", "v146", "v147", "v148", "v149", "v150", "v151", "v152", "v153", "v154", "v155", "v156", "v157", "v158", "v159", "v160", "v161", "v162", "v163", "v164", "v165", "v166", "v167", "s68", "s69", "s70", "s71", "s72", "s73", "s74", "s75", "s76", "s77", "s78", "s79", "s80", "s81", "s82", "s83", "s84", "s85"

#define CV_PRE 0
#define CV_Y 1
#define CV_ASM \
  "s_mov_b64 vcc, 1\n\t" \
  "ds_read_b64 v[0:1], v28 offset:0\n\t" \
  "ds_read_b64 v[2:3], v29 offset:0\n\t" \
  "ds_read_b64 v[4:5], v30 offset:1024\n\t" \
  "ds_read_b64 v[6:7], v31 offset:1024\n\t" \
  "ds_read_b64 v[8:9], v24 offset:2048\n\t" \
  "ds_read_b64 v[10:11], v25 offset:2048\n\t" \
  "ds_read_b64 v[12:13], v26 offset:3072\n\t" \
  "ds_read_b64 v[14:15], v27 offset:3072\n\t" \
  "ds_read_b64 v[16:17], v28 offset:4096\n\t" \
  "ds_read_b64 v[18:19], v29 offset:4096\n\t" \
  "ds_read_b64 v[20:21], v30 offset:5120\n\t" \
  "ds_read_b64 v[22:23], v31 offset:5120\n\t" \
  "s_waitcnt lgkmcnt(0)\n\t" \
  "v_mfma_f32_16x16x32_bf16 v[128:131], v[68:71], v[0:3], 0\n\t" \
  "v_mfma_f32_16x16x32_bf16 v[128:131], v[64:67], v[4:7], v[128:131]\n\t" \
  "v_mfma_f32_16x16x32_bf16 v[132:135], v[76:79], v[0:3], 0\n\t" \
  "v_mfma_f32_16x16x32_bf16 v[132:135], v[72:75], v[4:7], v[132:135]\n\t" \
  "v_mfma_f32_16x16x32_bf16 v[136:139], v[84:87], v[0:3], 0\n\t" \
  "v_mfma_f32_16x16x32_bf16 v[136:139], v[80:83], v[4:7], v[136:139]\n\t" \
  "v_mfma_f32_16x16x32_bf16 v[140:143], v[92:95], v[0:3], 0\n\t" \
  "v_mfma_f32_16x16x32_bf16 v[140:143], v[88:91], v[4:7], v[140:143]\n\t" \
  "v_mfma_f32_16x16x32_bf16 v[144:147], v[100:103], v[0:3], 0\n\t" \
  "v_mfma_f32_16x16x32_bf16 v[144:147], v[96:99], v[4:7], v[144:147]\n\t" \
  "ds_bpermute_b32 v34, v32, v131\n\t" \
  "v_mfma_f32_16x16x32_bf16 v[148:151], v[108:111], v[0:3], 0\n\t" \
  "v_mov_b32 v131, v130\n\t" \
  "v_mov_b32 v130, v129\n\t" \
  "v_mfma_f32_16x16x32_bf16 v[148:151], v[104:107], v[4:7], v[148:151]\n\t" \
  "v_mov_b32 v129, v128\n\t" \
  "v_mfma_f32_16x16x32_bf16 v[152:155], v[116:119], v[0:3], 0\n\t" \
  "v_mfma_f32_16x16x32_bf16 v[152:155], v[112:115], v[4:7], v[152:155]\n\t" \
  "v_mfma_f32_16x16x32_bf16 v[156:159], v[124:127], v[0:3], 0\n\t" \
  "ds_read_b64 v[0:1], v24 offset:6144\n\t" \
  "v_mfma_f32_16x16x32_bf16 v[156:159], v[120:123], v[4:7], v[156:159]\n\t" \
  "ds_read_b64 v[2:3], v25 offset:6144\n\t" \
  "v_mfma_f32_16x16x32_bf16 v[132:135], v[68:71], v[8:11], v[132:135]\n\t" \
  "ds_read_b64 v[4:5], v26 offset:7168\n\t" \
  "v_mfma_f32_16x16x32_bf16 v[132:135], v[64:67], v[12:15], v[132:135]\n\t" \
  "ds_read_b64 v[6:7], v27 offset:7168\n\t" \
  "v_mfma_f32_16x16x32_bf16 v[136:139], v[76:79], v[8:11], v[136:139]\n\t" \
  "v_mfma_f32_16x16x32_bf16 v[136:139], v[72:75], v[12:15], v[136:139]\n\t" \
  "v_mfma_f32_16x16x32_bf16 v[140:143], v[84:87], v[8:11], v[140:143]\n\t" \
  "s_waitcnt lgkmcnt(4)\n\t" \
  "v_cndmask_b32_e64 v128, v34, 0, vcc\n\t" \
  "v_mfma_f32_16x16x32_bf16 v[140:143], v[80:83], v[12:15], v[140:143]\n\t" \
  "v_mfma_f32_16x16x32_bf16 v[144:147], v[92:95], v[8:11], v[144:147]\n\t" \
  "ds_bpermute_b32 v35, v32, v135\n\t" \
  "v_mfma_f32_16x16x32_bf16 v[144:147], v[88:91], v[12:15], v[144:147]\n\t" \
  "v_mov_b32 v135, v134\n\t" \
  "v_mov_b32 v134, v133\n\t" \
  "v_mfma_f32_16x16x32_bf16 v[148:151], v[100:103], v[8:11], v[148:151]\n\t" \
  "v_mov_b32 v133, v132\n\t" \
  "v_mfma_f32_16x16x32_bf16 v[148:151], v[96:99], v[12:15], v[148:151]\n\t" \
  "v_mfma_f32_16x16x32_bf16 v[152:155], v[108:111], v[8:11], v[152:155]\n\t" \
  "v_mfma_f32_16x16x32_bf16 v[152:155], v[104:107], v[12:15], v[152:155]\n\t" \
  "v_mfma_f32_16x16x32_bf16 v[156:159], v[116:119], v[8:11], v[156:159]\n\t" \
  "ds_read_b64 v[8:9], v28 offset:8192\n\t" \
  "v_mfma_f32_16x16x32_bf16 v[156:159], v[112:115], v[12:15], v[156:159]\n\t" \
  "ds_read_b64 v[10:11], v29 offset:8192\n\t" \
  "v_mfma_f32_16x16x32_bf16 v[136:139], v[68:71], v[16:19], v[136:139]\n\t" \
  "ds_read_b64 v[12:13], v30 offset:9216\n\t" \
  "v_mfma_f32_16x16x32_bf16 v[136:139], v[64:67], v[20:23], v[136:139]\n\t" \
  "ds_read_b64 v[14:15], v31 offset:9216\n\t" \
  "v_mfma_f32_16x16x32_bf16 v[140:143], v[76:79], v[16:19], v[140:143]\n\t" \
  "v_mfma_f32_16x16x32_bf16 v[140:143], v[72:75], v[20:23], v[140:143]\n\t" \
  "s_waitcnt lgkmcnt(4)\n\t" \
  "v_cndmask_b32_e64 v132, v35, v34, vcc\n\t" \
  "v_mfma_f32_16x16x32_bf16 v[144:147], v[84:87], v[16:19], v[144:147]\n\t" \
  "v_mfma_f32_16x16x32_bf16 v[144:147], v[80:83], v[20:23], v[144:147]\n\t" \
  "v_mfma_f32_16x16x32_bf16 v[148:151], v[92:95], v[16:19], v[148:151]\n\t" \
  "ds_bpermute_b32 v36, v32, v139\n\t" \
  "v_mfma_f32_16x16x32_bf16 v[148:151], v[88:91], v[20:23], v[148:151]\n\t" \
  "v_mov_b32 v139, v138\n\t" \
  "v_mov_b32 v138, v137\n\t" \
  "v_mfma_f32_16x16x32_bf16 v[152:155], v[100:103], v[16:19], v[152:155]\n\t" \
  "v_mov_b32 v137, v136\n\t" \
  "v_mfma_f32_16x16x32_bf16 v[152:155], v[96:99], v[20:23], v[152:155]\n\t" \
  "v_mfma_f32_16x16x32_bf16 v[156:159], v[108:111], v[16:19], v[156:159]\n\t" \
  "ds_read_b64 v[16:17], v24 offset:10240\n\t" \
  "v_mfma_f32_16x16x32_bf16 v[156:159], v[104:107], v[20:23], v[156:159]\n\t" \
  "ds_read_b64 v[18:19], v25 offset:10240\n\t" \
  "v_mfma_f32_16x16x32_bf16 v[140:143], v[68:71], v[0:3], v[140:143]\n\t" \
  "ds_read_b64 v[20:21], v26 offset:11264\n\t" \
  "v_mfma_f32_16x16x32_bf16 v[140:143], v[64:67], v[4:7], v[140:143]\n\t" \
  "ds_read_b64 v[22:23], v27 offset:11264\n\t" \
  "v_mfma_f32_16x16x32_bf16 v[144:147], v[76:79], v[0:3], v[144:147]\n\t" \
  "v_mfma_f32_16x16x32_bf16 v[144:147], v[72:75], v[4:7], v[144:147]\n\t" \
  "v_mfma_f32_16x16x32_bf16 v[148:151], v[84:87], v[0:3], v[148:151]\n\t" \
  "v_mfma_f32_16x16x32_bf16 v[148:151], v[80:83], v[4:7], v[148:151]\n\t" \
  "s_waitcnt lgkmcnt(4)\n\t" \
  "v_cndmask_b32_e64 v136, v36, v35, vcc\n\t" \
  "v_mfma_f32_16x16x32_bf16 v[152:155], v[92:95], v[0:3], v[152:155]\n\t" \
  "ds_bpermute_b32 v37, v32, v143\n\t" \
  "v_mfma_f32_16x16x32_bf16 v[152:155], v[88:91], v[4:7], v[152:155]\n\t" \
  "v_mov_b32 v143, v142\n\t" \
  "v_mov_b32 v142, v141\n\t" \
  "v_mfma_f32_16x16x32_bf16 v[156:159], v[100:103], v[0:3], v[156:159]\n\t" \
  "ds_read_b64 v[0:1], v28 offset:12288\n\t" \
  "v_mov_b32 v141, v140\n\t" \
  "v_mfma_f32_16x16x32_bf16 v[156:159], v[96:99], v[4:7], v[156:159]\n\t" \
  "ds_read_b64 v[2:3], v29 offset:12288\n\t" \
  "v_mfma_f32_16x16x32_bf16 v[144:147], v[68:71], v[8:11], v[144:147]\n\t" \
  "ds_read_b64 v[4:5], v30 offset:13312\n\t" \
  "v_mfma_f32_16x16x32_bf16 v[144:147], v[64:67], v[12:15], v[144:147]\n\t" \
  "ds_read_b64 v[6:7], v31 offset:13312\n\t" \
  "v_mfma_f32_16x16x32_bf16 v[148:151], v[76:79], v[8:11], v[148:151]\n\t" \
  "v_mfma_f32_16x16x32_bf16 v[148:151], v[72:75], v[12:15], v[148:151]\n\t" \
  "v_mfma_f32_16x16x32_bf16 v[152:155], v[84:87], v[8:11], v[152:155]\n\t" \
  "v_mfma_f32_16x16x32_bf16 v[152:155], v[80:83], v[12:15], v[152:155]\n\t" \
  "v_mfma_f32_16x16x32_bf16 v[156:159], v[92:95], v[8:11], v[156:159]\n\t" \
  "ds_read_b64 v[8:9], v24 offset:14336\n\t" \
  "v_mfma_f32_16x16x32_bf16 v[156:159], v[88:91], v[12:15], v[156:159]\n\t" \
  "ds_read_b64 v[10:11], v25 offset:14336\n\t" \
  "s_waitcnt lgkmcnt(6)\n\t" \
  "v_mfma_f32_16x16x32_bf16 v[148:151], v[68:71], v[16:19], v[148:151]\n\t" \
  "ds_read_b64 v[12:13], v26 offset:15360\n\t" \
  "v_cndmask_b32_e64 v140, v37, v36, vcc\n\t" \
  "v_mfma_f32_16x16x32_bf16 v[148:151], v[64:67], v[20:23], v[148:151]\n\t" \
  "ds_read_b64 v[14:15], v27 offset:15360\n\t" \
  "v_mfma_f32_16x16x32_bf16 v[152:155], v[76:79], v[16:19], v[152:155]\n\t" \
  "ds_bpermute_b32 v38, v32, v147\n\t" \
  "v_mfma_f32_16x16x32_bf16 v[152:155], v[72:75], v[20:23], v[152:155]\n\t" \
  "v_mov_b32 v147, v146\n\t" \
  "v_mov_b32 v146, v145\n\t" \
  "v_mfma_f32_16x16x32_bf16 v[156:159], v[84:87], v[16:19], v[156:159]\n\t" \
  "ds_read_b64 v[16:17], v26 offset:0\n\t" \
  "ds_read_b64 v[18:19], v27 offset:0\n\t" \
  "v_mfma_f32_16x16x32_bf16 v[156:159], v[80:83], v[20:23], v[156:159]\n\t" \
  "ds_read_b64 v[20:21], v28 offset:1024\n\t" \
  "ds_read_b64 v[22:23], v29 offset:1024\n\t" \
  "s_waitcnt lgkmcnt(9)\n\t" \
  "v_mfma_f32_16x16x32_bf16 v[152:155], v[68:71], v[0:3], v[152:155]\n\t" \
  "ds_bpermute_b32 v39, v32, v151\n\t" \
  "v_mov_b32 v145, v144\n\t" \
  "v_mfma_f32_16x16x32_bf16 v[152:155], v[64:67], v[4:7], v[152:155]\n\t" \
  "v_mov_b32 v151, v150\n\t" \
  "v_mov_b32 v150, v149\n\t" \
  "v_mfma_f32_16x16x32_bf16 v[156:159], v[76:79], v[0:3], v[156:159]\n\t" \
  "ds_read_b64 v[0:1], v30 offset:2048\n\t" \
  "v_mov_b32 v149, v148\n\t" \
  "v_mfma_f32_16x16x32_bf16 v[156:159], v[72:75], v[4:7], v[156:159]\n\t" \
  "ds_read_b64 v[2:3], v31 offset:2048\n\t" \
  "s_waitcnt lgkmcnt(10)\n\t" \
  "v_mfma_f32_16x16x32_bf16 v[156:159], v[68:71], v[8:11], v[156:159]\n\t" \
  "ds_bpermute_b32 v40, v32, v155\n\t" \
  "s_waitcnt lgkmcnt(9)\n\t" \
  "v_mfma_f32_16x16x32_bf16 v[156:159], v[64:67], v[12:15], v[156:159]\n\t" \
  "ds_read_b64 v[4:5], v24 offset:3072\n\t" \
  "s_waitcnt lgkmcnt(7)\n\t" \
  "v_mfma_f32_16x16x32_bf16 v[128:131], v[68:71], v[16:19], v[128:131]\n\t" \
  "ds_read_b64 v[6:7], v25 offset:3072\n\t" \
  "s_waitcnt lgkmcnt(6)\n\t" \
  "v_mfma_f32_16x16x32_bf16 v[128:131], v[64:67], v[20:23], v[128:131]\n\t" \
  "v_cndmask_b32_e64 v144, v38, v37, vcc\n\t" \
  "v_mov_b32 v155, v154\n\t" \
  "v_mfma_f32_16x16x32_bf16 v[132:135], v[76:79], v[16:19], v[132:135]\n\t" \
  "ds_bpermute_b32 v41, v32, v159\n\t" \
  "v_mov_b32 v154, v153\n\t" \
  "v_mfma_f32_16x16x32_bf16 v[132:135], v[72:75], v[20:23], v[132:135]\n\t" \
  "global_load_dwordx4 v[44:47], v43, %[q0] offset:0\n\t" \
  "v_mfma_f32_16x16x32_bf16 v[136:139], v[84:87], v[16:19], v[136:139]\n\t" \
  "v_mov_b32 v153, v152\n\t" \
  "v_mov_b32 v159, v158\n\t" \
  "v_mfma_f32_16x16x32_bf16 v[136:139], v[80:83], v[20:23], v[136:139]\n\t" \
  "s_waitcnt lgkmcnt(6)\n\t" \
  "v_cndmask_b32_e64 v148, v39, v38, vcc\n\t" \
  "v_mfma_f32_16x16x32_bf16 v[140:143], v[92:95], v[16:19], v[140:143]\n\t" \
  "v_mov_b32 v158, v157\n\t" \
  "v_mov_b32 v157, v156\n\t" \
  "v_mfma_f32_16x16x32_bf16 v[140:143], v[88:91], v[20:23], v[140:143]\n\t" \
  "global_load_dwordx4 v[48:51], v43, %[q0] offset:2048\n\t" \
  "v_mfma_f32_16x16x32_bf16 v[144:147], v[100:103], v[16:19], v[144:147]\n\t" \
  "s_waitcnt lgkmcnt(3)\n\t" \
  "v_cndmask_b32_e64 v152, v40, v39, vcc\n\t" \
  "v_mfma_f32_16x16x32_bf16 v[144:147], v[96:99], v[20:23], v[144:147]\n\t" \
  "global_load_dwordx4 v[52:55], v43, %[q2] offset:0\n\t" \
  "v_mfma_f32_16x16x32_bf16 v[148:151], v[108:111], v[16:19], v[148:151]\n\t" \
  "s_waitcnt lgkmcnt(0)\n\t" \
  "v_cndmask_b32_e64 v156, v41, v40, vcc\n\t" \
  "v_mfma_f32_16x16x32_bf16 v[148:151], v[104:107], v[20:23], v[148:151]\n\t" \
  "ds_read_b64 v[8:9], v26 offset:4096\n\t" \
  "v_mfma_f32_16x16x32_bf16 v[152:155], v[116:119], v[16:19], v[152:155]\n\t" \
  "ds_read_b64 v[10:11], v27 offset:4096\n\t" \
  "v_mfma_f32_16x16x32_bf16 v[152:155], v[112:115], v[20:23], v[152:155]\n\t" \
  "global_load_dwordx4 v[56:59], v43, %[q2] offset:2048\n\t" \
  "v_mfma_f32_16x16x32_bf16 v[156:159], v[124:127], v[16:19], v[156:159]\n\t" \
  "ds_read_b64 v[12:13], v28 offset:5120\n\t" \
  "v_mfma_f32_16x16x32_bf16 v[156:159], v[120:123], v[20:23], v[156:159]\n\t" \
  "ds_read_b64 v[14:15], v29 offset:5120\n\t" \
  "v_mfma_f32_16x16x32_bf16 v[132:135], v[68:71], v[0:3], v[132:135]\n\t" \
  "ds_read_b64 v[16:17], v30 offset:6144\n\t" \
  "v_mfma_f32_16x16x32_bf16 v[132:135], v[64:67], v[4:7], v[132:135]\n\t" \
  "ds_read_b64 v[18:19], v31 offset:6144\n\t" \
  "v_mfma_f32_16x16x32_bf16 v[136:139], v[76:79], v[0:3], v[136:139]\n\t" \
  "ds_read_b64 v[20:21], v24 offset:7168\n\t" \
  "v_mfma_f32_16x16x32_bf16 v[136:139], v[72:75], v[4:7], v[136:139]\n\t" \
  "ds_read_b64 v[22:23], v25 offset:7168\n\t" \
  "v_mfma_f32_16x16x32_bf16 v[140:143], v[84:87], v[0:3], v[140:143]\n\t" \
  "ds_bpermute_b32 v34, v32, v131\n\t" \
  "v_mfma_f32_16x16x32_bf16 v[140:143], v[80:83], v[4:7], v[140:143]\n\t" \
  "ds_bpermute_b32 v35, v32, v135\n\t" \
  "v_mov_b32 v131, v130\n\t" \
  "v_mfma_f32_16x16x32_bf16 v[144:147], v[92:95], v[0:3], v[144:147]\n\t" \
  "v_mov_b32 v130, v129\n\t" \
  "v_mov_b32 v129, v128\n\t" \
  "v_mfma_f32_16x16x32_bf16 v[144:147], v[88:91], v[4:7], v[144:147]\n\t" \
  "v_mov_b32 v135, v134\n\t" \
  "v_mov_b32 v134, v133\n\t" \
  "v_mfma_f32_16x16x32_bf16 v[148:151], v[100:103], v[0:3], v[148:151]\n\t" \
  "v_mov_b32 v133, v132\n\t" \
  "v_mfma_f32_16x16x32_bf16 v[148:151], v[96:99], v[4:7], v[148:151]\n\t" \
  "v_mfma_f32_16x16x32_bf16 v[152:155], v[108:111], v[0:3], v[152:155]\n\t" \
  "v_mfma_f32_16x16x32_bf16 v[152:155], v[104:107], v[4:7], v[152:155]\n\t" \
  "v_mfma_f32_16x16x32_bf16 v[156:159], v[116:119], v[0:3], v[156:159]\n\t" \
  "ds_read_b64 v[0:1], v26 offset:8192\n\t" \
  "v_mfma_f32_16x16x32_bf16 v[156:159], v[112:115], v[4:7], v[156:159]\n\t" \
  "ds_read_b64 v[2:3], v27 offset:8192\n\t" \
  "s_waitcnt lgkmcnt(6)\n\t" \
  "v_mfma_f32_16x16x32_bf16 v[136:139], v[68:71], v[8:11], v[136:139]\n\t" \
  "ds_read_b64 v[4:5], v28 offset:9216\n\t" \
  "v_mfma_f32_16x16x32_bf16 v[136:139], v[64:67], v[12:15], v[136:139]\n\t" \
  "ds_read_b64 v[6:7], v29 offset:9216\n\t" \
  "v_mfma_f32_16x16x32_bf16 v[140:143], v[76:79], v[8:11], v[140:143]\n\t" \
  "s_waitcnt lgkmcnt(5)\n\t" \
  "v_cndmask_b32_e64 v128, v34, 0, vcc\n\t" \
  "v_mfma_f32_16x16x32_bf16 v[140:143], v[72:75], v[12:15], v[140:143]\n\t" \
  "s_waitcnt lgkmcnt(4)\n\t" \
  "v_cndmask_b32_e64 v132, v35, v34, vcc\n\t" \
  "v_mfma_f32_16x16x32_bf16 v[144:147], v[84:87], v[8:11], v[144:147]\n\t" \
  "ds_bpermute_b32 v36, v32, v139\n\t" \
  "v_mfma_f32_16x16x32_bf16 v[144:147], v[80:83], v[12:15], v[144:147]\n\t" \
  "v_mov_b32 v139, v138\n\t" \
  "v_mov_b32 v138, v137\n\t" \
  "v_mfma_f32_16x16x32_bf16 v[148:151], v[92:95], v[8:11], v[148:151]\n\t" \
  "v_mov_b32 v137, v136\n\t" \
  "v_mfma_f32_16x16x32_bf16 v[148:151], v[88:91], v[12:15], v[148:151]\n\t" \
  "v_mfma_f32_16x16x32_bf16 v[152:155], v[100:103], v[8:11], v[152:155]\n\t" \
  "v_mfma_f32_16x16x32_bf16 v[152:155], v[96:99], v[12:15], v[152:155]\n\t" \
  "v_mfma_f32_16x16x32_bf16 v[156:159], v[108:111], v[8:11], v[156:159]\n\t" \
  "ds_read_b64 v[8:9], v30 offset:10240\n\t" \
  "v_mfma_f32_16x16x32_bf16 v[156:159], v[104:107], v[12:15], v[156:159]\n\t" \
  "ds_read_b64 v[10:11], v31 offset:10240\n\t" \
  "v_mfma_f32_16x16x32_bf16 v[140:143], v[68:71], v[16:19], v[140:143]\n\t" \
  "ds_read_b64 v[12:13], v24 offset:11264\n\t" \
  "v_mfma_f32_16x16x32_bf16 v[140:143], v[64:67], v[20:23], v[140:143]\n\t" \
  "ds_read_b64 v[14:15], v25 offset:11264\n\t" \
  "v_mfma_f32_16x16x32_bf16 v[144:147], v[76:79], v[16:19], v[144:147]\n\t" \
  "v_mfma_f32_16x16x32_bf16 v[144:147], v[72:75], v[20:23], v[144:147]\n\t" \
  "s_waitcnt lgkmcnt(4)\n\t" \
  "v_cndmask_b32_e64 v136, v36, v35, vcc\n\t" \
  "v_mfma_f32_16x16x32_bf16 v[148:151], v[84:87], v[16:19], v[148:151]\n\t" \
  "v_mfma_f32_16x16x32_bf16 v[148:151], v[80:83], v[20:23], v[148:151]\n\t" \
  "v_mfma_f32_16x16x32_bf16 v[152:155], v[92:95], v[16:19], v[152:155]\n\t" \
  "ds_bpermute_b32 v37, v32, v143\n\t" \
  "v_mfma_f32_16x16x32_bf16 v[152:155], v[88:91], v[20:23], v[152:155]\n\t" \
  "v_mov_b32 v143, v142\n\t" \
  "v_mov_b32 v142, v141\n\t" \
  "v_mfma_f32_16x16x32_bf16 v[156:159], v[100:103], v[16:19], v[156:159]\n\t" \
  "ds_read_b64 v[16:17], v26 offset:12288\n\t" \
  "v_mov_b32 v141, v140\n\t" \
  "v_mfma_f32_16x16x32_bf16 v[156:159], v[96:99], v[20:23], v[156:159]\n\t" \
  "ds_read_b64 v[18:19], v27 offset:12288\n\t" \
  "v_mfma_f32_16x16x32_bf16 v[144:147], v[68:71], v[0:3], v[144:147]\n\t" \
  "ds_read_b64 v[20:21], v28 offset:13312\n\t" \
  "v_mfma_f32_16x16x32_bf16 v[144:147], v[64:67], v[4:7], v[144:147]\n\t" \
  "ds_read_b64 v[22:23], v29 offset:13312\n\t" \
  "v_mfma_f32_16x16x32_bf16 v[148:151], v[76:79], v[0:3], v[148:151]\n\t" \
  "v_mfma_f32_16x16x32_bf16 v[148:151], v[72:75], v[4:7], v[148:151]\n\t" \
  "v_mfma_f32_16x16x32_bf16 v[152:155], v[84:87], v[0:3], v[152:155]\n\t" \
  "v_mfma_f32_16x16x32_bf16 v[152:155], v[80:83], v[4:7], v[152:155]\n\t" \
  "v_mfma_f32_16x16x32_bf16 v[156:159], v[92:95], v[0:3], v[156:159]\n\t" \
  "ds_read_b64 v[0:1], v30 offset:14336\n\t" \
  "v_mfma_f32_16x16x32_bf16 v[156:159], v[88:91], v[4:7], v[156:159]\n\t" \
  "ds_read_b64 v[2:3], v31 offset:14336\n\t" \
  "s_waitcnt lgkmcnt(6)\n\t" \
  "v_mfma_f32_16x16x32_bf16 v[148:151], v[68:71], v[8:11], v[148:151]\n\t" \
  "ds_read_b64 v[4:5], v24 offset:15360\n\t" \
  "v_cndmask_b32_e64 v140, v37, v36, vcc\n\t" \
  "v_mfma_f32_16x16x32_bf16 v[148:151], v[64:67], v[12:15], v[148:151]\n\t" \
  "ds_read_b64 v[6:7], v25 offset:15360\n\t" \
  "v_mfma_f32_16x16x32_bf16 v[152:155], v[76:79], v[8:11], v[152:155]\n\t" \
  "ds_bpermute_b32 v38, v32, v147\n\t" \
  "v_mfma_f32_16x16x32_bf16 v[152:155], v[72:75], v[12:15], v[152:155]\n\t" \
  "v_mov_b32 v147, v146\n\t" \
  "v_mov_b32 v146, v145\n\t" \
  "v_mfma_f32_16x16x32_bf16 v[156:159], v[84:87], v[8:11], v[156:159]\n\t" \
  "ds_read_b64 v[8:9], v24 offset:0\n\t" \
  "ds_read_b64 v[10:11], v25 offset:0\n\t" \
  "v_mfma_f32_16x16x32_bf16 v[156:159], v[80:83], v[12:15], v[156:159]\n\t" \
  "ds_read_b64 v[12:13], v26 offset:1024\n\t" \
  "ds_read_b64 v[14:15], v27 offset:1024\n\t" \
  "s_waitcnt lgkmcnt(9)\n\t" \
  "v_mfma_f32_16x16x32_bf16 v[152:155], v[68:71], v[16:19], v[152:155]\n\t" \
  "ds_bpermute_b32 v39, v32, v151\n\t" \
  "v_mov_b32 v145, v144\n\t" \
  "v_mfma_f32_16x16x32_bf16 v[152:155], v[64:67], v[20:23], v[152:155]\n\t" \
  "v_mov_b32 v151, v150\n\t" \
  "v_mov_b32 v150, v149\n\t" \
  "v_mfma_f32_16x16x32_bf16 v[156:159], v[76:79], v[16:19], v[156:159]\n\t" \
  "ds_read_b64 v[16:17], v28 offset:2048\n\t" \
  "v_mov_b32 v149, v148\n\t" \
  "v_mfma_f32_16x16x32_bf16 v[156:159], v[72:75], v[20:23], v[156:159]\n\t" \
  "ds_read_b64 v[18:19], v29 offset:2048\n\t" \
  "s_waitcnt lgkmcnt(10)\n\t" \
  "v_mfma_f32_16x16x32_bf16 v[156:159], v[68:71], v[0:3], v[156:159]\n\t" \
  "ds_bpermute_b32 v40, v32, v155\n\t" \
  "s_waitcnt lgkmcnt(9)\n\t" \
  "v_mfma_f32_16x16x32_bf16 v[156:159], v[64:67], v[4:7], v[156:159]\n\t" \
  "ds_read_b64 v[20:21], v30 offset:3072\n\t" \
  "s_waitcnt lgkmcnt(7)\n\t" \
  "v_mfma_f32_16x16x32_bf16 v[128:131], v[68:71], v[8:11], v[128:131]\n\t" \
  "ds_read_b64 v[22:23], v31 offset:3072\n\t" \
  "s_waitcnt lgkmcnt(6)\n\t" \
  "v_mfma_f32_16x16x32_bf16 v[128:131], v[64:67], v[12:15], v[128:131]\n\t" \
  "v_cndmask_b32_e64 v144, v38, v37, vcc\n\t" \
  "v_mov_b32 v155, v154\n\t" \
  "v_mfma_f32_16x16x32_bf16 v[132:135], v[76:79], v[8:11], v[132:135]\n\t" \
  "ds_bpermute_b32 v41, v32, v159\n\t" \
  "v_mov_b32 v154, v153\n\t" \
  "v_mfma_f32_16x16x32_bf16 v[132:135], v[72:75], v[12:15], v[132:135]\n\t" \
  "v_mov_b32 v153, v152\n\t" \
  "v_mov_b32 v159, v158\n\t" \
  "v_mfma_f32_16x16x32_bf16 v[136:139], v[84:87], v[8:11], v[136:139]\n\t" \
  "v_mov_b32 v158, v157\n\t" \
  "v_mov_b32 v157, v156\n\t" \
  "v_mfma_f32_16x16x32_bf16 v[136:139], v[80:83], v[12:15], v[136:139]\n\t" \
  "s_waitcnt lgkmcnt(6)\n\t" \
  "v_cndmask_b32_e64 v148, v39, v38, vcc\n\t" \
  "v_mfma_f32_16x16x32_bf16 v[140:143], v[92:95], v[8:11], v[140:143]\n\t" \
  "ds_read_b64 v[0:1], v24 offset:4096\n\t" \
  "v_mfma_f32_16x16x32_bf16 v[140:143], v[88:91], v[12:15], v[140:143]\n\t" \
  "ds_read_b64 v[2:3], v25 offset:4096\n\t" \
  "v_mfma_f32_16x16x32_bf16 v[144:147], v[100:103], v[8:11], v[144:147]\n\t" \
  "s_waitcnt lgkmcnt(5)\n\t" \
  "v_cndmask_b32_e64 v152, v40, v39, vcc\n\t" \
  "v_mfma_f32_16x16x32_bf16 v[144:147], v[96:99], v[12:15], v[144:147]\n\t" \
  "ds_read_b64 v[4:5], v26 offset:5120\n\t" \
  "v_mfma_f32_16x16x32_bf16 v[148:151], v[108:111], v[8:11], v[148:151]\n\t" \
  "s_waitcnt lgkmcnt(3)\n\t" \
  "v_cndmask_b32_e64 v156, v41, v40, vcc\n\t" \
  "v_mfma_f32_16x16x32_bf16 v[148:151], v[104:107], v[12:15], v[148:151]\n\t" \
  "ds_read_b64 v[6:7], v27 offset:5120\n\t" \
  "v_mfma_f32_16x16x32_bf16 v[152:155], v[116:119], v[8:11], v[152:155]\n\t" \
  "ds_bpermute_b32 v34, v32, v131\n\t" \
  "v_mfma_f32_16x16x32_bf16 v[152:155], v[112:115], v[12:15], v[152:155]\n\t" \
  "v_mov_b32 v131, v130\n\t" \
  "v_mov_b32 v130, v129\n\t" \
  "v_mfma_f32_16x16x32_bf16 v[156:159], v[124:127], v[8:11], v[156:159]\n\t" \
  "ds_read_b64 v[8:9], v28 offset:6144\n\t" \
  "v_mov_b32 v129, v128\n\t" \
  "v_mfma_f32_16x16x32_bf16 v[156:159], v[120:123], v[12:15], v[156:159]\n\t" \
  "ds_read_b64 v[10:11], v29 offset:6144\n\t" \
  "v_mfma_f32_16x16x32_bf16 v[132:135], v[68:71], v[16:19], v[132:135]\n\t" \
  "ds_read_b64 v[12:13], v30 offset:7168\n\t" \
  "v_mfma_f32_16x16x32_bf16 v[132:135], v[64:67], v[20:23], v[132:135]\n\t" \
  "ds_read_b64 v[14:15], v31 offset:7168\n\t" \
  "v_mfma_f32_16x16x32_bf16 v[136:139], v[76:79], v[16:19], v[136:139]\n\t" \
  "v_mfma_f32_16x16x32_bf16 v[136:139], v[72:75], v[20:23], v[136:139]\n\t" \
  "v_mfma_f32_16x16x32_bf16 v[140:143], v[84:87], v[16:19], v[140:143]\n\t" \
  "v_mfma_f32_16x16x32_bf16 v[140:143], v[80:83], v[20:23], v[140:143]\n\t" \
  "v_mfma_f32_16x16x32_bf16 v[144:147], v[92:95], v[16:19], v[144:147]\n\t" \
  "v_mfma_f32_16x16x32_bf16 v[144:147], v[88:91], v[20:23], v[144:147]\n\t" \
  "s_waitcnt lgkmcnt(4)\n\t" \
  "v_cndmask_b32_e64 v128, v34, 0, vcc\n\t" \
  "v_mfma_f32_16x16x32_bf16 v[148:151], v[100:103], v[16:19], v[148:151]\n\t" \
  "ds_bpermute_b32 v35, v32, v135\n\t" \
  "v_mfma_f32_16x16x32_bf16 v[148:151], v[96:99], v[20:23], v[148:151]\n\t" \
  "v_mov_b32 v135, v134\n\t" \
  "v_mov_b32 v134, v133\n\t" \
  "v_mfma_f32_16x16x32_bf16 v[152:155], v[108:111], v[16:19], v[152:155]\n\t" \
  "v_mov_b32 v133, v132\n\t" \
  "v_mfma_f32_16x16x32_bf16 v[152:155], v[104:107], v[20:23], v[152:155]\n\t" \
  "v_mfma_f32_16x16x32_bf16 v[156:159], v[116:119], v[16:19], v[156:159]\n\t" \
  "ds_read_b64 v[16:17], v24 offset:8192\n\t" \
  "v_mfma_f32_16x16x32_bf16 v[156:159], v[112:115], v[20:23], v[156:159]\n\t" \
  "ds_read_b64 v[18:19], v25 offset:8192\n\t" \
  "v_mfma_f32_16x16x32_bf16 v[136:139], v[68:71], v[0:3], v[136:139]\n\t" \
  "ds_read_b64 v[20:21], v26 offset:9216\n\t" \
  "v_mfma_f32_16x16x32_bf16 v[136:139], v[64:67], v[4:7], v[136:139]\n\t" \
  "ds_read_b64 v[22:23], v27 offset:9216\n\t" \
  "v_mfma_f32_16x16x32_bf16 v[140:143], v[76:79], v[0:3], v[140:143]\n\t" \
  "v_mfma_f32_16x16x32_bf16 v[140:143], v[72:75], v[4:7], v[140:143]\n\t" \
  "v_mfma_f32_16x16x32_bf16 v[144:147], v[84:87], v[0:3], v[144:147]\n\t" \
  "v_mfma_f32_16x16x32_bf16 v[144:147], v[80:83], v[4:7], v[144:147]\n\t" \
  "s_waitcnt lgkmcnt(4)\n\t" \
  "v_cndmask_b32_e64 v132, v35, v34, vcc\n\t" \
  "v_mfma_f32_16x16x32_bf16 v[148:151], v[92:95], v[0:3], v[148:151]\n\t" \
  "ds_bpermute_b32 v36, v32, v139\n\t" \
  "v_mfma_f32_16x16x32_bf16 v[148:151], v[88:91], v[4:7], v[148:151]\n\t" \
  "v_mov_b32 v139, v138\n\t" \
  "v_mov_b32 v138, v137\n\t" \
  "v_mfma_f32_16x16x32_bf16 v[152:155], v[100:103], v[0:3], v[152:155]\n\t" \
  "v_mov_b32 v137, v136\n\t" \
  "v_mfma_f32_16x16x32_bf16 v[152:155], v[96:99], v[4:7], v[152:155]\n\t" \
  "v_mfma_f32_16x16x32_bf16 v[156:159], v[108:111], v[0:3], v[156:159]\n\t" \
  "ds_read_b64 v[0:1], v28 offset:10240\n\t" \
  "v_mfma_f32_16x16x32_bf16 v[156:159], v[104:107], v[4:7], v[156:159]\n\t" \
  "ds_read_b64 v[2:3], v29 offset:10240\n\t" \
  "v_mfma_f32_16x16x32_bf16 v[140:143], v[68:71], v[8:11], v[140:143]\n\t" \
  "ds_read_b64 v[4:5], v30 offset:11264\n\t" \
  "v_mfma_f32_16x16x32_bf16 v[140:143], v[64:67], v[12:15], v[140:143]\n\t" \
  "ds_read_b64 v[6:7], v31 offset:11264\n\t" \
  "v_mfma_f32_16x16x32_bf16 v[144:147], v[76:79], v[8:11], v[144:147]\n\t" \
  "v_mfma_f32_16x16x32_bf16 v[144:147], v[72:75], v[12:15], v[144:147]\n\t" \
  "v_mfma_f32_16x16x32_bf16 v[148:151], v[84:87], v[8:11], v[148:151]\n\t" \
  "v_mfma_f32_16x16x32_bf16 v[148:151], v[80:83], v[12:15], v[148:151]\n\t" \
  "s_waitcnt lgkmcnt(4)\n\t" \
  "v_cndmask_b32_e64 v136, v36, v35, vcc\n\t" \
  "v_mfma_f32_16x16x32_bf16 v[152:155], v[92:95], v[8:11], v[152:155]\n\t" \
  "ds_bpermute_b32 v37, v32, v143\n\t" \
  "v_mfma_f32_16x16x32_bf16 v[152:155], v[88:91], v[12:15], v[152:155]\n\t" \
  "v_mov_b32 v143, v142\n\t" \
  "v_mov_b32 v142, v141\n\t" \
  "v_mfma_f32_16x16x32_bf16 v[156:159], v[100:103], v[8:11], v[156:159]\n\t" \
  "ds_read_b64 v[8:9], v24 offset:12288\n\t" \
  "v_mov_b32 v141, v140\n\t" \
  "v_mfma_f32_16x16x32_bf16 v[156:159], v[96:99], v[12:15], v[156:159]\n\t" \
  "ds_read_b64 v[10:11], v25 offset:12288\n\t" \
  "v_mfma_f32_16x16x32_bf16 v[144:147], v[68:71], v[16:19], v[144:147]\n\t" \
  "ds_read_b64 v[12:13], v26 offset:13312\n\t" \
  "v_mfma_f32_16x16x32_bf16 v[144:147], v[64:67], v[20:23], v[144:147]\n\t" \
  "ds_read_b64 v[14:15], v27 offset:13312\n\t" \
  "v_mfma_f32_16x16x32_bf16 v[148:151], v[76:79], v[16:19], v[148:151]\n\t" \
  "v_mfma_f32_16x16x32_bf16 v[148:151], v[72:75], v[20:23], v[148:151]\n\t" \
  "v_mfma_f32_16x16x32_bf16 v[152:155], v[84:87], v[16:19], v[152:155]\n\t" \
  "v_mfma_f32_16x16x32_bf16 v[152:155], v[80:83], v[20:23], v[152:155]\n\t" \
  "v_mfma_f32_16x16x32_bf16 v[156:159], v[92:95], v[16:19], v[156:159]\n\t" \
  "ds_read_b64 v[16:17], v28 offset:14336\n\t" \
  "v_mfma_f32_16x16x32_bf16 v[156:159], v[88:91], v[20:23], v[156:159]\n\t" \
  "ds_read_b64 v[18:19], v29 offset:14336\n\t" \
  "s_waitcnt lgkmcnt(6)\n\t" \
  "v_mfma_f32_16x16x32_bf16 v[148:151], v[68:71], v[0:3], v[148:151]\n\t" \
  "ds_read_b64 v[20:21], v30 offset:15360\n\t" \
  "v_cndmask_b32_e64 v140, v37, v36, vcc\n\t" \
  "v_mfma_f32_16x16x32_bf16 v[148:151], v[64:67], v[4:7], v[148:151]\n\t" \
  "ds_read_b64 v[22:23], v31 offset:15360\n\t" \
  "v_mfma_f32_16x16x32_bf16 v[152:155], v[76:79], v[0:3], v[152:155]\n\t" \
  "ds_bpermute_b32 v38, v32, v147\n\t" \
  "v_mfma_f32_16x16x32_bf16 v[152:155], v[72:75], v[4:7], v[152:155]\n\t" \
  "v_mov_b32 v147, v146\n\t" \
  "v_mov_b32 v146, v145\n\t" \
  "v_mfma_f32_16x16x32_bf16 v[156:159], v[84:87], v[0:3], v[156:159]\n\t" \
  "ds_read_b64 v[0:1], v30 offset:0\n\t" \
  "ds_read_b64 v[2:3], v31 offset:0\n\t" \
  "v_mfma_f32_16x16x32_bf16 v[156:159], v[80:83], v[4:7], v[156:159]\n\t" \
  "ds_read_b64 v[4:5], v24 offset:1024\n\t" \
  "ds_read_b64 v[6:7], v25 offset:1024\n\t" \
  "s_waitcnt lgkmcnt(9)\n\t" \
  "v_mfma_f32_16x16x32_bf16 v[152:155], v[68:71], v[8:11], v[152:155]\n\t" \
  "ds_bpermute_b32 v39, v32, v151\n\t" \
  "v_mov_b32 v145, v144\n\t" \
  "v_mfma_f32_16x16x32_bf16 v[152:155], v[64:67], v[12:15], v[152:155]\n\t" \
  "v_mov_b32 v151, v150\n\t" \
  "v_mov_b32 v150, v149\n\t" \
  "v_mfma_f32_16x16x32_bf16 v[156:159], v[76:79], v[8:11], v[156:159]\n\t" \
  "ds_read_b64 v[8:9], v26 offset:2048\n\t" \
  "v_mov_b32 v149, v148\n\t" \
  "v_mfma_f32_16x16x32_bf16 v[156:159], v[72:75], v[12:15], v[156:159]\n\t" \
  "ds_read_b64 v[10:11], v27 offset:2048\n\t" \
  "s_waitcnt lgkmcnt(10)\n\t" \
  "v_mfma_f32_16x16x32_bf16 v[156:159], v[68:71], v[16:19], v[156:159]\n\t" \
  "ds_bpermute_b32 v40, v32, v155\n\t" \
  "s_waitcnt lgkmcnt(9)\n\t" \
  "v_mfma_f32_16x16x32_bf16 v[156:159], v[64:67], v[20:23], v[156:159]\n\t" \
  "ds_read_b64 v[12:13], v28 offset:3072\n\t" \
  "s_waitcnt lgkmcnt(7)\n\t" \
  "v_mfma_f32_16x16x32_bf16 v[128:131], v[68:71], v[0:3], v[128:131]\n\t" \
  "ds_read_b64 v[14:15], v29 offset:3072\n\t" \
  "s_waitcnt lgkmcnt(6)\n\t" \
  "v_mfma_f32_16x16x32_bf16 v[128:131], v[64:67], v[4:7], v[128:131]\n\t" \
  "v_cndmask_b32_e64 v144, v38, v37, vcc\n\t" \
  "v_mov_b32 v155, v154\n\t" \
  "v_mfma_f32_16x16x32_bf16 v[132:135], v[76:79], v[0:3], v[132:135]\n\t" \
  "ds_bpermute_b32 v41, v32, v159\n\t" \
  "v_mov_b32 v154, v153\n\t" \
  "v_mfma_f32_16x16x32_bf16 v[132:135], v[72:75], v[4:7], v[132:135]\n\t" \
  "v_mov_b32 v153, v152\n\t" \
  "v_mov_b32 v159, v158\n\t" \
  "v_mfma_f32_16x16x32_bf16 v[136:139], v[84:87], v[0:3], v[136:139]\n\t" \
  "v_mov_b32 v158, v157\n\t" \
  "v_mov_b32 v157, v156\n\t" \
  "v_mfma_f32_16x16x32_bf16 v[136:139], v[80:83], v[4:7], v[136:139]\n\t" \
  "s_waitcnt lgkmcnt(6)\n\t" \
  "v_cndmask_b32_e64 v148, v39, v38, vcc\n\t" \
  "v_mfma_f32_16x16x32_bf16 v[140:143], v[92:95], v[0:3], v[140:143]\n\t" \
  "ds_read_b64 v[16:17], v30 offset:4096\n\t" \
  "v_mfma_f32_16x16x32_bf16 v[140:143], v[88:91], v[4:7], v[140:143]\n\t" \
  "ds_read_b64 v[18:19], v31 offset:4096\n\t" \
  "v_mfma_f32_16x16x32_bf16 v[144:147], v[100:103], v[0:3], v[144:147]\n\t" \
  "s_waitcnt lgkmcnt(5)\n\t" \
  "v_cndmask_b32_e64 v152, v40, v39, vcc\n\t" \
  "v_mfma_f32_16x16x32_bf16 v[144:147], v[96:99], v[4:7], v[144:147]\n\t" \
  "ds_read_b64 v[20:21], v24 offset:5120\n\t" \
  "v_mfma_f32_16x16x32_bf16 v[148:151], v[108:111], v[0:3], v[148:151]\n\t" \
  "s_waitcnt lgkmcnt(3)\n\t" \
  "v_cndmask_b32_e64 v156, v41, v40, vcc\n\t" \
  "v_mfma_f32_16x16x32_bf16 v[148:151], v[104:107], v[4:7], v[148:151]\n\t" \
  "v_cvt_pk_bf16_f32 v34, v128, v129\n\t" \
  "v_cvt_pk_bf16_f32 v35, v130, v131\n\t" \
  "v_mfma_f32_16x16x32_bf16 v[152:155], v[116:119], v[0:3], v[152:155]\n\t" \
  "ds_read_b64 v[22:23], v25 offset:5120\n\t" \
  "v_mfma_f32_16x16x32_bf16 v[152:155], v[112:115], v[4:7], v[152:155]\n\t" \
  "v_mfma_f32_16x16x32_bf16 v[156:159], v[124:127], v[0:3], v[156:159]\n\t" \
  "ds_read_b64 v[0:1], v26 offset:6144\n\t" \
  "v_mfma_f32_16x16x32_bf16 v[156:159], v[120:123], v[4:7], v[156:159]\n\t" \
  "ds_read_b64 v[2:3], v27 offset:6144\n\t" \
  "v_mfma_f32_16x16x32_bf16 v[132:135], v[68:71], v[8:11], v[132:135]\n\t" \
  "ds_read_b64 v[4:5], v28 offset:7168\n\t" \
  "v_mfma_f32_16x16x32_bf16 v[132:135], v[64:67], v[12:15], v[132:135]\n\t" \
  "ds_read_b64 v[6:7], v29 offset:7168\n\t" \
  "v_mfma_f32_16x16x32_bf16 v[136:139], v[76:79], v[8:11], v[136:139]\n\t" \
  "v_mfma_f32_16x16x32_bf16 v[136:139], v[72:75], v[12:15], v[136:139]\n\t" \
  "v_mfma_f32_16x16x32_bf16 v[140:143], v[84:87], v[8:11], v[140:143]\n\t" \
  "v_mfma_f32_16x16x32_bf16 v[140:143], v[80:83], v[12:15], v[140:143]\n\t" \
  "v_mfma_f32_16x16x32_bf16 v[144:147], v[92:95], v[8:11], v[144:147]\n\t" \
  "v_mfma_f32_16x16x32_bf16 v[144:147], v[88:91], v[12:15], v[144:147]\n\t" \
  "v_mfma_f32_16x16x32_bf16 v[148:151], v[100:103], v[8:11], v[148:151]\n\t" \
  "v_cvt_pk_bf16_f32 v36, v132, v133\n\t" \
  "v_cvt_pk_bf16_f32 v37, v134, v135\n\t" \
  "v_mfma_f32_16x16x32_bf16 v[148:151], v[96:99], v[12:15], v[148:151]\n\t" \
  "s_mov_b64 exec, 1\n\t" \
  "ds_write_b32 v33, v33\n\t" \
  "s_mov_b64 exec, -1\n\t" \
  "v_mfma_f32_16x16x32_bf16 v[152:155], v[108:111], v[8:11], v[152:155]\n\t" \
  "v_permlane16_swap_b32 v34, v36\n\t" \
  "v_permlane16_swap_b32 v35, v37\n\t" \
  "v_mfma_f32_16x16x32_bf16 v[152:155], v[104:107], v[12:15], v[152:155]\n\t" \
  "global_store_dwordx4 v42, v[34:37], %[yb] offset:0 sc1\n\t" \
  "v_mfma_f32_16x16x32_bf16 v[156:159], v[116:119], v[8:11], v[156:159]\n\t" \
  "ds_read_b64 v[8:9], v30 offset:8192\n\t" \
  "v_mfma_f32_16x16x32_bf16 v[156:159], v[112:115], v[12:15], v[156:159]\n\t" \
  "ds_read_b64 v[10:11], v31 offset:8192\n\t" \
  "s_waitcnt lgkmcnt(3)\n\t" \
  "v_mfma_f32_16x16x32_bf16 v[136:139], v[68:71], v[16:19], v[136:139]\n\t" \
  "ds_read_b64 v[12:13], v24 offset:9216\n\t" \
  "v_mfma_f32_16x16x32_bf16 v[136:139], v[64:67], v[20:23], v[136:139]\n\t" \
  "ds_read_b64 v[14:15], v25 offset:9216\n\t" \
  "v_mfma_f32_16x16x32_bf16 v[140:143], v[76:79], v[16:19], v[140:143]\n\t" \
  "v_mfma_f32_16x16x32_bf16 v[140:143], v[72:75], v[20:23], v[140:143]\n\t" \
  "v_mfma_f32_16x16x32_bf16 v[144:147], v[84:87], v[16:19], v[144:147]\n\t" \
  "v_mfma_f32_16x16x32_bf16 v[144:147], v[80:83], v[20:23], v[144:147]\n\t" \
  "v_mfma_f32_16x16x32_bf16 v[148:151], v[92:95], v[16:19], v[148:151]\n\t" \
  "v_mfma_f32_16x16x32_bf16 v[148:151], v[88:91], v[20:23], v[148:151]\n\t" \
  "v_mfma_f32_16x16x32_bf16 v[152:155], v[100:103], v[16:19], v[152:155]\n\t" \
  "v_cvt_pk_bf16_f32 v38, v136, v137\n\t" \
  "v_cvt_pk_bf16_f32 v39, v138, v139\n\t" \
  "v_mfma_f32_16x16x32_bf16 v[152:155], v[96:99], v[20:23], v[152:155]\n\t" \
  "v_mfma_f32_16x16x32_bf16 v[156:159], v[108:111], v[16:19], v[156:159]\n\t" \
  "ds_read_b64 v[16:17], v26 offset:10240\n\t" \
  "v_mfma_f32_16x16x32_bf16 v[156:159], v[104:107], v[20:23], v[156:159]\n\t" \
  "ds_read_b64 v[18:19], v27 offset:10240\n\t" \
  "v_mfma_f32_16x16x32_bf16 v[140:143], v[68:71], v[0:3], v[140:143]\n\t" \
  "ds_read_b64 v[20:21], v28 offset:11264\n\t" \
  "v_mfma_f32_16x16x32_bf16 v[140:143], v[64:67], v[4:7], v[140:143]\n\t" \
  "ds_read_b64 v[22:23], v29 offset:11264\n\t" \
  "v_mfma_f32_16x16x32_bf16 v[144:147], v[76:79], v[0:3], v[144:147]\n\t" \
  "v_mfma_f32_16x16x32_bf16 v[144:147], v[72:75], v[4:7], v[144:147]\n\t" \
  "v_mfma_f32_16x16x32_bf16 v[148:151], v[84:87], v[0:3], v[148:151]\n\t" \
  "v_mfma_f32_16x16x32_bf16 v[148:151], v[80:83], v[4:7], v[148:151]\n\t" \
  "v_mfma_f32_16x16x32_bf16 v[152:155], v[92:95], v[0:3], v[152:155]\n\t" \
  "v_mfma_f32_16x16x32_bf16 v[152:155], v[88:91], v[4:7], v[152:155]\n\t" \
  "v_mfma_f32_16x16x32_bf16 v[156:159], v[100:103], v[0:3], v[156:159]\n\t" \
  "v_cvt_pk_bf16_f32 v40, v140, v141\n\t" \
  "v_cvt_pk_bf16_f32 v41, v142, v143\n\t" \
  "v_mfma_f32_16x16x32_bf16 v[156:159], v[96:99], v[4:7], v[156:159]\n\t" \
  "ds_read_b64 v[0:1], v30 offset:12288\n\t" \
  "s_waitcnt lgkmcnt(5)\n\t" \
  "v_mfma_f32_16x16x32_bf16 v[144:147], v[68:71], v[8:11], v[144:147]\n\t" \
  "v_permlane16_swap_b32 v38, v40\n\t" \
  "ds_read_b64 v[2:3], v31 offset:12288\n\t" \
  "v_mfma_f32_16x16x32_bf16 v[144:147], v[64:67], v[12:15], v[144:147]\n\t" \
  "v_permlane16_swap_b32 v39, v41\n\t" \
  "ds_read_b64 v[4:5], v24 offset:13312\n\t" \
  "v_mfma_f32_16x16x32_bf16 v[148:151], v[76:79], v[8:11], v[148:151]\n\t" \
  "ds_read_b64 v[6:7], v25 offset:13312\n\t" \
  "v_mfma_f32_16x16x32_bf16 v[148:151], v[72:75], v[12:15], v[148:151]\n\t" \
  "global_store_dwordx4 v42, v[38:41], %[yb] offset:1024 sc1\n\t" \
  "v_mfma_f32_16x16x32_bf16 v[152:155], v[84:87], v[8:11], v[152:155]\n\t" \
  "v_mfma_f32_16x16x32_bf16 v[152:155], v[80:83], v[12:15], v[152:155]\n\t" \
  "v_cvt_pk_bf16_f32 v34, v144, v145\n\t" \
  "v_cvt_pk_bf16_f32 v35, v146, v147\n\t" \
  "v_mfma_f32_16x16x32_bf16 v[156:159], v[92:95], v[8:11], v[156:159]\n\t" \
  "ds_read_b64 v[8:9], v26 offset:14336\n\t" \
  "v_mfma_f32_16x16x32_bf16 v[156:159], v[88:91], v[12:15], v[156:159]\n\t" \
  "ds_read_b64 v[10:11], v27 offset:14336\n\t" \
  "s_waitcnt lgkmcnt(6)\n\t" \
  "v_mfma_f32_16x16x32_bf16 v[148:151], v[68:71], v[16:19], v[148:151]\n\t" \
  "ds_read_b64 v[12:13], v28 offset:15360\n\t" \
  "v_mfma_f32_16x16x32_bf16 v[148:151], v[64:67], v[20:23], v[148:151]\n\t" \
  "ds_read_b64 v[14:15], v29 offset:15360\n\t" \
  "v_mfma_f32_16x16x32_bf16 v[152:155], v[76:79], v[16:19], v[152:155]\n\t" \
  "v_mfma_f32_16x16x32_bf16 v[152:155], v[72:75], v[20:23], v[152:155]\n\t" \
  "v_mfma_f32_16x16x32_bf16 v[156:159], v[84:87], v[16:19], v[156:159]\n\t" \
  "v_mfma_f32_16x16x32_bf16 v[156:159], v[80:83], v[20:23], v[156:159]\n\t" \
  "s_waitcnt lgkmcnt(4)\n\t" \
  "v_mfma_f32_16x16x32_bf16 v[152:155], v[68:71], v[0:3], v[152:155]\n\t" \
  "v_mfma_f32_16x16x32_bf16 v[152:155], v[64:67], v[4:7], v[152:155]\n\t" \
  "v_cvt_pk_bf16_f32 v36, v148, v149\n\t" \
  "v_cvt_pk_bf16_f32 v37, v150, v151\n\t" \
  "v_mfma_f32_16x16x32_bf16 v[156:159], v[76:79], v[0:3], v[156:159]\n\t" \
  "v_mfma_f32_16x16x32_bf16 v[156:159], v[72:75], v[4:7], v[156:159]\n\t" \
  "v_permlane16_swap_b32 v34, v36\n\t" \
  "s_waitcnt lgkmcnt(2)\n\t" \
  "v_mfma_f32_16x16x32_bf16 v[156:159], v[68:71], v[8:11], v[156:159]\n\t" \
  "v_permlane16_swap_b32 v35, v37\n\t" \
  "s_waitcnt lgkmcnt(0)\n\t" \
  "v_mfma_f32_16x16x32_bf16 v[156:159], v[64:67], v[12:15], v[156:159]\n\t" \
  "global_store_dwordx4 v42, v[34:37], %[yb] offset:2048 sc1\n\t" \
  "s_waitcnt lgkmcnt(0)\n\t" \
  "s_nop 4\n\t" \
  "v_cvt_pk_bf16_f32 v38, v152, v153\n\t" \
  "v_cvt_pk_bf16_f32 v39, v154, v155\n\t" \
  "v_cvt_pk_bf16_f32 v40, v156, v157\n\t" \
  "v_cvt_pk_bf16_f32 v41, v158, v159\n\t" \
  "s_nop 1\n\t" \
  "v_permlane16_swap_b32 v38, v40\n\t" \
  "v_permlane16_swap_b32 v39, v41\n\t" \
  "global_store_dwordx4 v42, v[38:41], %[yb] offset:3072 sc1\n\t" \
  "s_nop 1\n\t"
#define CV_Q 1
#define CV_CLOB "vcc", "v0", "v1", "v2", "v3", "v4", "v5", "v6", "v7", "v8", "v9", "v10", "v11", "v12", "v13", "v14", "v15", "v16", "v17", "v18", "v19", "v20", "v21", "v22", "v23", "v34", "v35", "v36", "v37", "v38", "v39", "v40", "v41"

#define G3_ASM \
  "v_readlane_b32 s64, v96, 0\n\t" \
  "v_readlane_b32 s65, v96, 1\n\t" \
  "v_readlane_b32 s66, v96, 2\n\t" \
  "v_readlane_b32 s67, v96, 3\n\t" \
  "v_readlane_b32 s68, v96, 4\n\t" \
  "v_readlane_b32 s69, v96, 5\n\t" \
  "v_readlane_b32 s70, v96, 6\n\t" \
  "v_readlane_b32 s71, v96, 7\n\t" \
  "v_readlane_b32 s72, v96, 8\n\t" \
  "v_readlane_b32 s73, v96, 9\n\t" \
  "v_readlane_b32 s74, v96, 10\n\t" \
  "v_readlane_b32 s75, v96, 11\n\t" \
  "v_readlane_b32 s76, v96, 12\n\t" \
  "v_readlane_b32 s77, v96, 13\n\t" \
  "v_readlane_b32 s78, v96, 14\n\t" \
  "v_readlane_b32 s79, v96, 15\n\t" \
  "v_add_u32 v97, %[go], v165\n\t" \
  "s_branch QG_%=\n\t" \
  "QA14_%=:\n\t" \
  "v_mov_b32 v165, %[sy]\n\t" \
  "s_sleep 1\n\t" \
  "ds_read_b32 v165, v165\n\t" \
  "s_addk_i32 %[cn], 1\n\t" \
  "s_cmp_gt_u32 %[cn], 0x400000\n\t" \
  "s_cbranch_scc1 QC14_%=\n\t" \
  "s_waitcnt lgkmcnt(0)\n\t" \
  "v_cmp_lt_u32 vcc, 15, v165\n\t" \
  "s_cbranch_vccz QA14_%=\n\t" \
  "QC14_%=:\n\t" \
  "s_waitcnt lgkmcnt(0)\n\t" \
  "s_branch QB14_%=\n\t" \
  "QS_%=:\n\t" \
  "s_sleep 1\n\t" \
  "QG_%=:\n\t" \
  "ds_read_b32 v98, v165\n\t" \
  "ds_read_b32 v99, v97\n\t" \
  "s_addk_i32 %[cn], 1\n\t" \
  "s_cmp_gt_u32 %[cn], 0x400000\n\t" \
  "s_cselect_b32 %[t2], 16, 0\n\t" \
  "s_waitcnt lgkmcnt(0)\n\t" \
  "v_readfirstlane_b32 %[t0], v98\n\t" \
  "v_readfirstlane_b32 %[t1], v99\n\t" \
  "s_max_u32 %[t0], %[t0], %[t2]\n\t" \
  "s_or_b32 %[t1], %[t1], %[t2]\n\t" \
  "s_min_u32 %[t2], %[t0], 6\n\t" \
  "s_cmp_ge_u32 %[iss], %[t2]\n\t" \
  "s_cbranch_scc1 QD_%=\n\t" \
  "s_cmp_eq_u32 %[iss], 1\n\t" \
  "s_cbranch_scc1 QL1_%=\n\t" \
  "s_cmp_eq_u32 %[iss], 2\n\t" \
  "s_cbranch_scc1 QL2_%=\n\t" \
  "s_cmp_eq_u32 %[iss], 3\n\t" \
  "s_cbranch_scc1 QL3_%=\n\t" \
  "s_cmp_eq_u32 %[iss], 4\n\t" \
  "s_cbranch_scc1 QL4_%=\n\t" \
  "s_cmp_eq_u32 %[iss], 5\n\t" \
  "s_cbranch_scc1 QL5_%=\n\t" \
  "QL0_%=:\n\t" \
  "buffer_load_dwordx4 v[0:3], v164, %[ob], s64 offen offset:0 sc1\n\t" \
  "buffer_load_dwordx4 v[4:7], v164, %[ob], s64 offen offset:1024 sc1\n\t" \
  "buffer_load_dwordx4 v[8:11], v164, %[ob], s64 offen offset:2048 sc1\n\t" \
  "buffer_load_dwordx4 v[12:15], v164, %[ob], s64 offen offset:3072 sc1\n\t" \
  "s_mov_b32 %[iss], 1\n\t" \
  "s_cmp_le_u32 %[t2], 1\n\t" \
  "s_cbranch_scc1 QD_%=\n\t" \
  "QL1_%=:\n\t" \
  "buffer_load_dwordx4 v[16:19], v164, %[ob], s65 offen offset:0 sc1\n\t" \
  "buffer_load_dwordx4 v[20:23], v164, %[ob], s65 offen offset:1024 sc1\n\t" \
  "buffer_load_dwordx4 v[24:27], v164, %[ob], s65 offen offset:2048 sc1\n\t" \
  "buffer_load_dwordx4 v[28:31], v164, %[ob], s65 offen offset:3072 sc1\n\t" \
  "s_mov_b32 %[iss], 2\n\t" \
  "s_cmp_le_u32 %[t2], 2\n\t" \
  "s_cbranch_scc1 QD_%=\n\t" \
  "QL2_%=:\n\t" \
  "buffer_load_dwordx4 v[32:35], v164, %[ob], s66 offen offset:0 sc1\n\t" \
  "buffer_load_dwordx4 v[36:39], v164, %[ob], s66 offen offset:1024 sc1\n\t" \
  "buffer_load_dwordx4 v[40:43], v164, %[ob], s66 offen offset:2048 sc1\n\t" \
  "buffer_load_dwordx4 v[44:47], v164, %[ob], s66 offen offset:3072 sc1\n\t" \
  "s_mov_b32 %[iss], 3\n\t" \
  "s_cmp_le_u32 %[t2], 3\n\t" \
  "s_cbranch_scc1 QD_%=\n\t" \
  "QL3_%=:\n\t" \
  "buffer_load_dwordx4 v[48:51], v164, %[ob], s67 offen offset:0 sc1\n\t" \
  "buffer_load_dwordx4 v[52:55], v164, %[ob], s67 offen offset:1024 sc1\n\t" \
  "buffer_load_dwordx4 v[56:59], v164, %[ob], s67 offen offset:2048 sc1\n\t" \
  "buffer_load_dwordx4 v[60:63], v164, %[ob], s67 offen offset:3072 sc1\n\t" \
  "s_mov_b32 %[iss], 4\n\t" \
  "s_cmp_le_u32 %[t2], 4\n\t" \
  "s_cbranch_scc1 QD_%=\n\t" \
  "QL4_%=:\n\t" \
  "buffer_load_dwordx4 v[64:67], v164, %[ob], s68 offen offset:0 sc1\n\t" \
  "buffer_load_dwordx4 v[68:71], v164, %[ob], s68 offen offset:1024 sc1\n\t" \
  "buffer_load_dwordx4 v[72:75], v164, %[ob], s68 offen offset:2048 sc1\n\t" \
  "buffer_load_dwordx4 v[76:79], v164, %[ob], s68 offen offset:3072 sc1\n\t" \
  "s_mov_b32 %[iss], 5\n\t" \
  "s_cmp_le_u32 %[t2], 5\n\t" \
  "s_cbranch_scc1 QD_%=\n\t" \
  "QL5_%=:\n\t" \
  "buffer_load_dwordx4 v[80:83], v164, %[ob], s69 offen offset:0 sc1\n\t" \
  "buffer_load_dwordx4 v[84:87], v164, %[ob], s69 offen offset:1024 sc1\n\t" \
  "buffer_load_dwordx4 v[88:91], v164, %[ob], s69 offen offset:2048 sc1\n\t" \
  "buffer_load_dwordx4 v[92:95], v164, %[ob], s69 offen offset:3072 sc1\n\t" \
  "s_mov_b32 %[iss], 6\n\t" \
  "QD_%=:\n\t" \
  "s_cmp_lt_u32 %[t0], 14\n\t" \
  "s_cbranch_scc1 QS_%=\n\t" \
  "s_cmp_eq_u32 %[t1], 0\n\t" \
  "s_cbranch_scc1 QS_%=\n\t" \
  "s_setprio 0\n\t" \
  "ds_read_b64_tr_b16 v[96:97], v160 offset:0\n\t" \
  "ds_read_b64_tr_b16 v[98:99], v160 offset:2048\n\t" \
  "ds_read_b64_tr_b16 v[100:101], v161 offset:0\n\t" \
  "ds_read_b64_tr_b16 v[102:103], v161 offset:2048\n\t" \
  "ds_read_b64_tr_b16 v[104:105], v162 offset:0\n\t" \
  "ds_read_b64_tr_b16 v[106:107], v162 offset:2048\n\t" \
  "ds_read_b64_tr_b16 v[108:109], v163 offset:0\n\t" \
  "ds_read_b64_tr_b16 v[110:111], v163 offset:2048\n\t" \
  "ds_read_b64_tr_b16 v[112:113], v160 offset:4096\n\t" \
  "ds_read_b64_tr_b16 v[114:115], v160 offset:6144\n\t" \
  "ds_read_b64_tr_b16 v[116:117], v161 offset:4096\n\t" \
  "ds_read_b64_tr_b16 v[118:119], v161 offset:6144\n\t" \
  "ds_read_b64_tr_b16 v[120:121], v162 offset:4096\n\t" \
  "ds_read_b64_tr_b16 v[122:123], v162 offset:6144\n\t" \
  "s_waitcnt vmcnt(22) lgkmcnt(10)\n\t" \
  "v_mfma_f32_16x16x32_bf16 v[128:131], v[96:99], v[0:3], 0\n\t" \
  "ds_read_b64_tr_b16 v[124:125], v163 offset:4096\n\t" \
  "v_mfma_f32_16x16x32_bf16 v[144:147], v[96:99], v[4:7], 0\n\t" \
  "ds_read_b64_tr_b16 v[126:127], v163 offset:6144\n\t" \
  "v_mfma_f32_16x16x32_bf16 v[132:135], v[100:103], v[0:3], 0\n\t" \
  "ds_read_b64_tr_b16 v[96:97], v160 offset:8192\n\t" \
  "v_mfma_f32_16x16x32_bf16 v[148:151], v[100:103], v[4:7], 0\n\t" \
  "ds_read_b64_tr_b16 v[98:99], v160 offset:10240\n\t" \
  "s_waitcnt lgkmcnt(10)\n\t" \
  "v_mfma_f32_16x16x32_bf16 v[136:139], v[104:107], v[0:3], 0\n\t" \
  "ds_read_b64_tr_b16 v[100:101], v161 offset:8192\n\t" \
  "v_mfma_f32_16x16x32_bf16 v[152:155], v[104:107], v[4:7], 0\n\t" \
  "ds_read_b64_tr_b16 v[102:103], v161 offset:10240\n\t" \
  "v_mfma_f32_16x16x32_bf16 v[140:143], v[108:111], v[0:3], 0\n\t" \
  "ds_read_b64_tr_b16 v[104:105], v162 offset:8192\n\t" \
  "v_mfma_f32_16x16x32_bf16 v[156:159], v[108:111], v[4:7], 0\n\t" \
  "ds_read_b64_tr_b16 v[106:107], v162 offset:10240\n\t" \
  "s_waitcnt vmcnt(20) lgkmcnt(10)\n\t" \
  "v_mfma_f32_16x16x32_bf16 v[128:131], v[112:115], v[8:11], v[128:131]\n\t" \
  "ds_read_b64_tr_b16 v[108:109], v163 offset:8192\n\t" \
  "buffer_load_dwordx4 v[0:3], v164, %[ob], s70 offen offset:0 sc1\n\t" \
  "v_mfma_f32_16x16x32_bf16 v[144:147], v[112:115], v[12:15], v[144:147]\n\t" \
  "ds_read_b64_tr_b16 v[110:111], v163 offset:10240\n\t" \
  "v_mfma_f32_16x16x32_bf16 v[132:135], v[116:119], v[8:11], v[132:135]\n\t" \
  "ds_read_b64_tr_b16 v[112:113], v160 offset:12288\n\t" \
  "v_mfma_f32_16x16x32_bf16 v[148:151], v[116:119], v[12:15], v[148:151]\n\t" \
  "ds_read_b64_tr_b16 v[114:115], v160 offset:14336\n\t" \
  "s_waitcnt lgkmcnt(10)\n\t" \
  "v_mfma_f32_16x16x32_bf16 v[136:139], v[120:123], v[8:11], v[136:139]\n\t" \
  "ds_read_b64_tr_b16 v[116:117], v161 offset:12288\n\t" \
  "buffer_load_dwordx4 v[4:7], v164, %[ob], s70 offen offset:1024 sc1\n\t" \
  "v_mfma_f32_16x16x32_bf16 v[152:155], v[120:123], v[12:15], v[152:155]\n\t" \
  "ds_read_b64_tr_b16 v[118:119], v161 offset:14336\n\t" \
  "v_mfma_f32_16x16x32_bf16 v[140:143], v[124:127], v[8:11], v[140:143]\n\t" \
  "ds_read_b64_tr_b16 v[120:121], v162 offset:12288\n\t" \
  "v_mfma_f32_16x16x32_bf16 v[156:159], v[124:127], v[12:15], v[156:159]\n\t" \
  "ds_read_b64_tr_b16 v[122:123], v162 offset:14336\n\t" \
  "s_waitcnt vmcnt(20) lgkmcnt(10)\n\t" \
  "v_mfma_f32_16x16x32_bf16 v[128:131], v[96:99], v[16:19], v[128:131]\n\t" \
  "ds_read_b64_tr_b16 v[124:125], v163 offset:12288\n\t" \
  "v_mfma_f32_16x16x32_bf16 v[144:147], v[96:99], v[20:23], v[144:147]\n\t" \
  "ds_read_b64_tr_b16 v[126:127], v163 offset:14336\n\t" \
  "buffer_load_dwordx4 v[8:11], v164, %[ob], s70 offen offset:2048 sc1\n\t" \
  "v_mfma_f32_16x16x32_bf16 v[132:135], v[100:103], v[16:19], v[132:135]\n\t" \
  "ds_read_b64_tr_b16 v[96:97], v160 offset:16384\n\t" \
  "v_mfma_f32_16x16x32_bf16 v[148:151], v[100:103], v[20:23], v[148:151]\n\t" \
  "ds_read_b64_tr_b16 v[98:99], v160 offset:18432\n\t" \
  "s_waitcnt lgkmcnt(10)\n\t" \
  "v_mfma_f32_16x16x32_bf16 v[136:139], v[104:107], v[16:19], v[136:139]\n\t" \
  "ds_read_b64_tr_b16 v[100:101], v161 offset:16384\n\t" \
  "v_mfma_f32_16x16x32_bf16 v[152:155], v[104:107], v[20:23], v[152:155]\n\t" \
  "ds_read_b64_tr_b16 v[102:103], v161 offset:18432\n\t" \
  "v_mfma_f32_16x16x32_bf16 v[140:143], v[108:111], v[16:19], v[140:143]\n\t" \
  "ds_read_b64_tr_b16 v[104:105], v162 offset:16384\n\t" \
  "buffer_load_dwordx4 v[12:15], v164, %[ob], s70 offen offset:3072 sc1\n\t" \
  "v_mfma_f32_16x16x32_bf16 v[156:159], v[108:111], v[20:23], v[156:159]\n\t" \
  "ds_read_b64_tr_b16 v[106:107], v162 offset:18432\n\t" \
  "s_waitcnt vmcnt(20) lgkmcnt(10)\n\t" \
  "v_mfma_f32_16x16x32_bf16 v[128:131], v[112:115], v[24:27], v[128:131]\n\t" \
  "ds_read_b64_tr_b16 v[108:109], v163 offset:16384\n\t" \
  "v_mfma_f32_16x16x32_bf16 v[144:147], v[112:115], v[28:31], v[144:147]\n\t" \
  "ds_read_b64_tr_b16 v[110:111], v163 offset:18432\n\t" \
  "v_mfma_f32_16x16x32_bf16 v[132:135], v[116:119], v[24:27], v[132:135]\n\t" \
  "ds_read_b64_tr_b16 v[112:113], v160 offset:20480\n\t" \
  "v_mfma_f32_16x16x32_bf16 v[148:151], v[116:119], v[28:31], v[148:151]\n\t" \
  "ds_read_b64_tr_b16 v[114:115], v160 offset:22528\n\t" \
  "s_waitcnt lgkmcnt(10)\n\t" \
  "v_mfma_f32_16x16x32_bf16 v[136:139], v[120:123], v[24:27], v[136:139]\n\t" \
  "ds_read_b64_tr_b16 v[116:117], v161 offset:20480\n\t" \
  "buffer_load_dwordx4 v[16:19], v164, %[ob], s71 offen offset:0 sc1\n\t" \
  "v_mfma_f32_16x16x32_bf16 v[152:155], v[120:123], v[28:31], v[152:155]\n\t" \
  "ds_read_b64_tr_b16 v[118:119], v161 offset:22528\n\t" \
  "v_mfma_f32_16x16x32_bf16 v[140:143], v[124:127], v[24:27], v[140:143]\n\t" \
  "ds_read_b64_tr_b16 v[120:121], v162 offset:20480\n\t" \
  "v_mfma_f32_16x16x32_bf16 v[156:159], v[124:127], v[28:31], v[156:159]\n\t" \
  "ds_read_b64_tr_b16 v[122:123], v162 offset:22528\n\t" \
  "s_waitcnt vmcnt(19) lgkmcnt(10)\n\t" \
  "v_mfma_f32_16x16x32_bf16 v[128:131], v[96:99], v[32:35], v[128:131]\n\t" \
  "ds_read_b64_tr_b16 v[124:125], v163 offset:20480\n\t" \
  "buffer_load_dwordx4 v[20:23], v164, %[ob], s71 offen offset:1024 sc1\n\t" \
  "v_mfma_f32_16x16x32_bf16 v[144:147], v[96:99], v[36:39], v[144:147]\n\t" \
  "ds_read_b64_tr_b16 v[126:127], v163 offset:22528\n\t" \
  "v_mfma_f32_16x16x32_bf16 v[132:135], v[100:103], v[32:35], v[132:135]\n\t" \
  "ds_read_b64_tr_b16 v[96:97], v160 offset:24576\n\t" \
  "v_mfma_f32_16x16x32_bf16 v[148:151], v[100:103], v[36:39], v[148:151]\n\t" \
  "ds_read_b64_tr_b16 v[98:99], v160 offset:26624\n\t" \
  "s_waitcnt lgkmcnt(10)\n\t" \
  "v_mfma_f32_16x16x32_bf16 v[136:139], v[104:107], v[32:35], v[136:139]\n\t" \
  "ds_read_b64_tr_b16 v[100:101], v161 offset:24576\n\t" \
  "v_mfma_f32_16x16x32_bf16 v[152:155], v[104:107], v[36:39], v[152:155]\n\t" \
  "ds_read_b64_tr_b16 v[102:103], v161 offset:26624\n\t" \
  "buffer_load_dwordx4 v[24:27], v164, %[ob], s71 offen offset:2048 sc1\n\t" \
  "v_mfma_f32_16x16x32_bf16 v[140:143], v[108:111], v[32:35], v[140:143]\n\t" \
  "ds_read_b64_tr_b16 v[104:105], v162 offset:24576\n\t" \
  "v_mfma_f32_16x16x32_bf16 v[156:159], v[108:111], v[36:39], v[156:159]\n\t" \
  "ds_read_b64_tr_b16 v[106:107], v162 offset:26624\n\t" \
  "s_waitcnt vmcnt(19) lgkmcnt(10)\n\t" \
  "v_mfma_f32_16x16x32_bf16 v[128:131], v[112:115], v[40:43], v[128:131]\n\t" \
  "ds_read_b64_tr_b16 v[108:109], v163 offset:24576\n\t" \
  "v_mfma_f32_16x16x32_bf16 v[144:147], v[112:115], v[44:47], v[144:147]\n\t" \
  "ds_read_b64_tr_b16 v[110:111], v163 offset:26624\n\t" \
  "v_mfma_f32_16x16x32_bf16 v[132:135], v[116:119], v[40:43], v[132:135]\n\t" \
  "ds_read_b64_tr_b16 v[112:113], v160 offset:28672\n\t" \
  "buffer_load_dwordx4 v[28:31], v164, %[ob], s71 offen offset:3072 sc1\n\t" \
  "v_mfma_f32_16x16x32_bf16 v[148:151], v[116:119], v[44:47], v[148:151]\n\t" \
  "ds_read_b64_tr_b16 v[114:115], v160 offset:30720\n\t" \
  "s_waitcnt lgkmcnt(10)\n\t" \
  "v_mfma_f32_16x16x32_bf16 v[136:139], v[120:123], v[40:43], v[136:139]\n\t" \
  "ds_read_b64_tr_b16 v[116:117], v161 offset:28672\n\t" \
  "v_mfma_f32_16x16x32_bf16 v[152:155], v[120:123], v[44:47], v[152:155]\n\t" \
  "ds_read_b64_tr_b16 v[118:119], v161 offset:30720\n\t" \
  "v_mfma_f32_16x16x32_bf16 v[140:143], v[124:127], v[40:43], v[140:143]\n\t" \
  "ds_read_b64_tr_b16 v[120:121], v162 offset:28672\n\t" \
  "v_mfma_f32_16x16x32_bf16 v[156:159], v[124:127], v[44:47], v[156:159]\n\t" \
  "ds_read_b64_tr_b16 v[122:123], v162 offset:30720\n\t" \
  "s_waitcnt vmcnt(18) lgkmcnt(10)\n\t" \
  "v_mfma_f32_16x16x32_bf16 v[128:131], v[96:99], v[48:51], v[128:131]\n\t" \
  "ds_read_b64_tr_b16 v[124:125], v163 offset:28672\n\t" \
  "buffer_load_dwordx4 v[32:35], v164, %[ob], s72 offen offset:0 sc1\n\t" \
  "v_mfma_f32_16x16x32_bf16 v[144:147], v[96:99], v[52:55], v[144:147]\n\t" \
  "ds_read_b64_tr_b16 v[126:127], v163 offset:30720\n\t" \
  "v_mfma_f32_16x16x32_bf16 v[132:135], v[100:103], v[48:51], v[132:135]\n\t" \
  "ds_read_b64_tr_b16 v[96:97], v160 offset:32768\n\t" \
  "v_mfma_f32_16x16x32_bf16 v[148:151], v[100:103], v[52:55], v[148:151]\n\t" \
  "ds_read_b64_tr_b16 v[98:99], v160 offset:34816\n\t" \
  "s_waitcnt lgkmcnt(10)\n\t" \
  "v_mfma_f32_16x16x32_bf16 v[136:139], v[104:107], v[48:51], v[136:139]\n\t" \
  "ds_read_b64_tr_b16 v[100:101], v161 offset:32768\n\t" \
  "buffer_load_dwordx4 v[36:39], v164, %[ob], s72 offen offset:1024 sc1\n\t" \
  "v_mfma_f32_16x16x32_bf16 v[152:155], v[104:107], v[52:55], v[152:155]\n\t" \
  "ds_read_b64_tr_b16 v[102:103], v161 offset:34816\n\t" \
  "v_mfma_f32_16x16x32_bf16 v[140:143], v[108:111], v[48:51], v[140:143]\n\t" \
  "ds_read_b64_tr_b16 v[104:105], v162 offset:32768\n\t" \
  "v_mfma_f32_16x16x32_bf16 v[156:159], v[108:111], v[52:55], v[156:159]\n\t" \
  "ds_read_b64_tr_b16 v[106:107], v162 offset:34816\n\t" \
  "s_waitcnt vmcnt(18) lgkmcnt(10)\n\t" \
  "v_mfma_f32_16x16x32_bf16 v[128:131], v[112:115], v[56:59], v[128:131]\n\t" \
  "ds_read_b64_tr_b16 v[108:109], v163 offset:32768\n\t" \
  "v_mfma_f32_16x16x32_bf16 v[144:147], v[112:115], v[60:63], v[144:147]\n\t" \
  "ds_read_b64_tr_b16 v[110:111], v163 offset:34816\n\t" \
  "buffer_load_dwordx4 v[40:43], v164, %[ob], s72 offen offset:2048 sc1\n\t" \
  "v_mfma_f32_16x16x32_bf16 v[132:135], v[116:119], v[56:59], v[132:135]\n\t" \
  "ds_read_b64_tr_b16 v[112:113], v160 offset:36864\n\t" \
  "v_mfma_f32_16x16x32_bf16 v[148:151], v[116:119], v[60:63], v[148:151]\n\t" \
  "ds_read_b64_tr_b16 v[114:115], v160 offset:38912\n\t" \
  "s_waitcnt lgkmcnt(10)\n\t" \
  "v_mfma_f32_16x16x32_bf16 v[136:139], v[120:123], v[56:59], v[136:139]\n\t" \
  "ds_read_b64_tr_b16 v[116:117], v161 offset:36864\n\t" \
  "v_mfma_f32_16x16x32_bf16 v[152:155], v[120:123], v[60:63], v[152:155]\n\t" \
  "ds_read_b64_tr_b16 v[118:119], v161 offset:38912\n\t" \
  "v_mfma_f32_16x16x32_bf16 v[140:143], v[124:127], v[56:59], v[140:143]\n\t" \
  "ds_read_b64_tr_b16 v[120:121], v162 offset:36864\n\t" \
  "buffer_load_dwordx4 v[44:47], v164, %[ob], s72 offen offset:3072 sc1\n\t" \
  "v_mfma_f32_16x16x32_bf16 v[156:159], v[124:127], v[60:63], v[156:159]\n\t" \
  "ds_read_b64_tr_b16 v[122:123], v162 offset:38912\n\t" \
  "s_waitcnt vmcnt(18) lgkmcnt(10)\n\t" \
  "v_mfma_f32_16x16x32_bf16 v[128:131], v[96:99], v[64:67], v[128:131]\n\t" \
  "ds_read_b64_tr_b16 v[124:125], v163 offset:36864\n\t" \
  "v_mfma_f32_16x16x32_bf16 v[144:147], v[96:99], v[68:71], v[144:147]\n\t" \
  "ds_read_b64_tr_b16 v[126:127], v163 offset:38912\n\t" \
  "v_mfma_f32_16x16x32_bf16 v[132:135], v[100:103], v[64:67], v[132:135]\n\t" \
  "ds_read_b64_tr_b16 v[96:97], v160 offset:40960\n\t" \
  "v_mfma_f32_16x16x32_bf16 v[148:151], v[100:103], v[68:71], v[148:151]\n\t" \
  "ds_read_b64_tr_b16 v[98:99], v160 offset:43008\n\t" \
  "s_waitcnt lgkmcnt(10)\n\t" \
  "v_mfma_f32_16x16x32_bf16 v[136:139], v[104:107], v[64:67], v[136:139]\n\t" \
  "ds_read_b64_tr_b16 v[100:101], v161 offset:40960\n\t" \
  "buffer_load_dwordx4 v[48:51], v164, %[ob], s73 offen offset:0 sc1\n\t" \
  "v_mfma_f32_16x16x32_bf16 v[152:155], v[104:107], v[68:71], v[152:155]\n\t" \
  "ds_read_b64_tr_b16 v[102:103], v161 offset:43008\n\t" \
  "v_mfma_f32_16x16x32_bf16 v[140:143], v[108:111], v[64:67], v[140:143]\n\t" \
  "ds_read_b64_tr_b16 v[104:105], v162 offset:40960\n\t" \
  "v_mfma_f32_16x16x32_bf16 v[156:159], v[108:111], v[68:71], v[156:159]\n\t" \
  "ds_read_b64_tr_b16 v[106:107], v162 offset:43008\n\t" \
  "s_waitcnt vmcnt(17) lgkmcnt(10)\n\t" \
  "v_mfma_f32_16x16x32_bf16 v[128:131], v[112:115], v[72:75], v[128:131]\n\t" \
  "ds_read_b64_tr_b16 v[108:109], v163 offset:40960\n\t" \
  "buffer_load_dwordx4 v[52:55], v164, %[ob], s73 offen offset:1024 sc1\n\t" \
  "v_mfma_f32_16x16x32_bf16 v[144:147], v[112:115], v[76:79], v[144:147]\n\t" \
  "ds_read_b64_tr_b16 v[110:111], v163 offset:43008\n\t" \
  "v_mfma_f32_16x16x32_bf16 v[132:135], v[116:119], v[72:75], v[132:135]\n\t" \
  "ds_read_b64_tr_b16 v[112:113], v160 offset:45056\n\t" \
  "v_mfma_f32_16x16x32_bf16 v[148:151], v[116:119], v[76:79], v[148:151]\n\t" \
  "ds_read_b64_tr_b16 v[114:115], v160 offset:47104\n\t" \
  "s_waitcnt lgkmcnt(10)\n\t" \
  "v_mfma_f32_16x16x32_bf16 v[136:139], v[120:123], v[72:75], v[136:139]\n\t" \
  "ds_read_b64_tr_b16 v[116:117], v161 offset:45056\n\t" \
  "buffer_load_dwordx4 v[56:59], v164, %[ob], s73 offen offset:2048 sc1\n\t" \
  "v_mfma_f32_16x16x32_bf16 v[152:155], v[120:123], v[76:79], v[152:155]\n\t" \
  "ds_read_b64_tr_b16 v[118:119], v161 offset:47104\n\t" \
  "v_mfma_f32_16x16x32_bf16 v[140:143], v[124:127], v[72:75], v[140:143]\n\t" \
  "ds_read_b64_tr_b16 v[120:121], v162 offset:45056\n\t" \
  "v_mfma_f32_16x16x32_bf16 v[156:159], v[124:127], v[76:79], v[156:159]\n\t" \
  "ds_read_b64_tr_b16 v[122:123], v162 offset:47104\n\t" \
  "s_waitcnt vmcnt(17) lgkmcnt(10)\n\t" \
  "v_mfma_f32_16x16x32_bf16 v[128:131], v[96:99], v[80:83], v[128:131]\n\t" \
  "ds_read_b64_tr_b16 v[124:125], v163 offset:45056\n\t" \
  "v_mfma_f32_16x16x32_bf16 v[144:147], v[96:99], v[84:87], v[144:147]\n\t" \
  "ds_read_b64_tr_b16 v[126:127], v163 offset:47104\n\t" \
  "buffer_load_dwordx4 v[60:63], v164, %[ob], s73 offen offset:3072 sc1\n\t" \
  "v_mfma_f32_16x16x32_bf16 v[132:135], v[100:103], v[80:83], v[132:135]\n\t" \
  "ds_read_b64_tr_b16 v[96:97], v160 offset:49152\n\t" \
  "v_mfma_f32_16x16x32_bf16 v[148:151], v[100:103], v[84:87], v[148:151]\n\t" \
  "ds_read_b64_tr_b16 v[98:99], v160 offset:51200\n\t" \
  "s_waitcnt lgkmcnt(10)\n\t" \
  "v_mfma_f32_16x16x32_bf16 v[136:139], v[104:107], v[80:83], v[136:139]\n\t" \
  "ds_read_b64_tr_b16 v[100:101], v161 offset:49152\n\t" \
  "v_mfma_f32_16x16x32_bf16 v[152:155], v[104:107], v[84:87], v[152:155]\n\t" \
  "ds_read_b64_tr_b16 v[102:103], v161 offset:51200\n\t" \
  "v_mfma_f32_16x16x32_bf16 v[140:143], v[108:111], v[80:83], v[140:143]\n\t" \
  "ds_read_b64_tr_b16 v[104:105], v162 offset:49152\n\t" \
  "buffer_load_dwordx4 v[64:67], v164, %[ob], s74 offen offset:0 sc1\n\t" \
  "v_mfma_f32_16x16x32_bf16 v[156:159], v[108:111], v[84:87], v[156:159]\n\t" \
  "ds_read_b64_tr_b16 v[106:107], v162 offset:51200\n\t" \
  "s_waitcnt vmcnt(17) lgkmcnt(10)\n\t" \
  "v_mfma_f32_16x16x32_bf16 v[128:131], v[112:115], v[88:91], v[128:131]\n\t" \
  "ds_read_b64_tr_b16 v[108:109], v163 offset:49152\n\t" \
  "v_mfma_f32_16x16x32_bf16 v[144:147], v[112:115], v[92:95], v[144:147]\n\t" \
  "ds_read_b64_tr_b16 v[110:111], v163 offset:51200\n\t" \
  "v_mfma_f32_16x16x32_bf16 v[132:135], v[116:119], v[88:91], v[132:135]\n\t" \
  "ds_read_b64_tr_b16 v[112:113], v160 offset:53248\n\t" \
  "s_mov_b64 exec, 1\n\t" \
  "ds_write_b32 v165, v165 offset:76\n\t" \
  "s_mov_b64 exec, -1\n\t" \
  "v_mfma_f32_16x16x32_bf16 v[148:151], v[116:119], v[92:95], v[148:151]\n\t" \
  "ds_read_b64_tr_b16 v[114:115], v160 offset:55296\n\t" \
  "s_waitcnt lgkmcnt(11)\n\t" \
  "v_mfma_f32_16x16x32_bf16 v[136:139], v[120:123], v[88:91], v[136:139]\n\t" \
  "ds_read_b64_tr_b16 v[116:117], v161 offset:53248\n\t" \
  "buffer_load_dwordx4 v[68:71], v164, %[ob], s74 offen offset:1024 sc1\n\t" \
  "v_mfma_f32_16x16x32_bf16 v[152:155], v[120:123], v[92:95], v[152:155]\n\t" \
  "ds_read_b64_tr_b16 v[118:119], v161 offset:55296\n\t" \
  "v_mfma_f32_16x16x32_bf16 v[140:143], v[124:127], v[88:91], v[140:143]\n\t" \
  "ds_read_b64_tr_b16 v[120:121], v162 offset:53248\n\t" \
  "v_mfma_f32_16x16x32_bf16 v[156:159], v[124:127], v[92:95], v[156:159]\n\t" \
  "ds_read_b64_tr_b16 v[122:123], v162 offset:55296\n\t" \
  "s_waitcnt vmcnt(16) lgkmcnt(11)\n\t" \
  "v_mfma_f32_16x16x32_bf16 v[128:131], v[96:99], v[0:3], v[128:131]\n\t" \
  "ds_read_b64_tr_b16 v[124:125], v163 offset:53248\n\t" \
  "buffer_load_dwordx4 v[72:75], v164, %[ob], s74 offen offset:2048 sc1\n\t" \
  "v_mfma_f32_16x16x32_bf16 v[144:147], v[96:99], v[4:7], v[144:147]\n\t" \
  "ds_read_b64_tr_b16 v[126:127], v163 offset:55296\n\t" \
  "v_mfma_f32_16x16x32_bf16 v[132:135], v[100:103], v[0:3], v[132:135]\n\t" \
  "ds_read_b64_tr_b16 v[96:97], v160 offset:57344\n\t" \
  "v_mfma_f32_16x16x32_bf16 v[148:151], v[100:103], v[4:7], v[148:151]\n\t" \
  "ds_read_b64_tr_b16 v[98:99], v160 offset:59392\n\t" \
  "s_waitcnt lgkmcnt(11)\n\t" \
  "v_mfma_f32_16x16x32_bf16 v[136:139], v[104:107], v[0:3], v[136:139]\n\t" \
  "ds_read_b64_tr_b16 v[100:101], v161 offset:57344\n\t" \
  "v_mfma_f32_16x16x32_bf16 v[152:155], v[104:107], v[4:7], v[152:155]\n\t" \
  "ds_read_b64_tr_b16 v[102:103], v161 offset:59392\n\t" \
  "buffer_load_dwordx4 v[76:79], v164, %[ob], s74 offen offset:3072 sc1\n\t" \
  "v_mfma_f32_16x16x32_bf16 v[140:143], v[108:111], v[0:3], v[140:143]\n\t" \
  "ds_read_b64_tr_b16 v[104:105], v162 offset:57344\n\t" \
  "v_mfma_f32_16x16x32_bf16 v[156:159], v[108:111], v[4:7], v[156:159]\n\t" \
  "ds_read_b64_tr_b16 v[106:107], v162 offset:59392\n\t" \
  "s_waitcnt vmcnt(16) lgkmcnt(10)\n\t" \
  "v_mfma_f32_16x16x32_bf16 v[128:131], v[112:115], v[8:11], v[128:131]\n\t" \
  "ds_read_b64_tr_b16 v[108:109], v163 offset:57344\n\t" \
  "v_mfma_f32_16x16x32_bf16 v[144:147], v[112:115], v[12:15], v[144:147]\n\t" \
  "ds_read_b64_tr_b16 v[110:111], v163 offset:59392\n\t" \
  "v_mfma_f32_16x16x32_bf16 v[132:135], v[116:119], v[8:11], v[132:135]\n\t" \
  "ds_read_b64_tr_b16 v[112:113], v160 offset:61440\n\t" \
  "buffer_load_dwordx4 v[80:83], v164, %[ob], s75 offen offset:0 sc1\n\t" \
  "v_mfma_f32_16x16x32_bf16 v[148:151], v[116:119], v[12:15], v[148:151]\n\t" \
  "ds_read_b64_tr_b16 v[114:115], v160 offset:63488\n\t" \
  "s_waitcnt lgkmcnt(10)\n\t" \
  "v_mfma_f32_16x16x32_bf16 v[136:139], v[120:123], v[8:11], v[136:139]\n\t" \
  "ds_read_b64_tr_b16 v[116:117], v161 offset:61440\n\t" \
  "v_mfma_f32_16x16x32_bf16 v[152:155], v[120:123], v[12:15], v[152:155]\n\t" \
  "ds_read_b64_tr_b16 v[118:119], v161 offset:63488\n\t" \
  "v_mfma_f32_16x16x32_bf16 v[140:143], v[124:127], v[8:11], v[140:143]\n\t" \
  "ds_read_b64_tr_b16 v[120:121], v162 offset:61440\n\t" \
  "v_mfma_f32_16x16x32_bf16 v[156:159], v[124:127], v[12:15], v[156:159]\n\t" \
  "ds_read_b64_tr_b16 v[122:123], v162 offset:63488\n\t" \
  "s_waitcnt vmcnt(15) lgkmcnt(10)\n\t" \
  "v_mfma_f32_16x16x32_bf16 v[128:131], v[96:99], v[16:19], v[128:131]\n\t" \
  "ds_read_b64_tr_b16 v[124:125], v163 offset:61440\n\t" \
  "buffer_load_dwordx4 v[84:87], v164, %[ob], s75 offen offset:1024 sc1\n\t" \
  "v_mfma_f32_16x16x32_bf16 v[144:147], v[96:99], v[20:23], v[144:147]\n\t" \
  "ds_read_b64_tr_b16 v[126:127], v163 offset:63488\n\t" \
  "v_mfma_f32_16x16x32_bf16 v[132:135], v[100:103], v[16:19], v[132:135]\n\t" \
  "v_add_u32 v160, 0x10000, v160\n\t" \
  "ds_read_b64_tr_b16 v[96:97], v160 offset:0\n\t" \
  "v_mfma_f32_16x16x32_bf16 v[148:151], v[100:103], v[20:23], v[148:151]\n\t" \
  "ds_read_b64_tr_b16 v[98:99], v160 offset:2048\n\t" \
  "s_waitcnt lgkmcnt(10)\n\t" \
  "v_mfma_f32_16x16x32_bf16 v[136:139], v[104:107], v[16:19], v[136:139]\n\t" \
  "v_add_u32 v161, 0x10000, v161\n\t" \
  "ds_read_b64_tr_b16 v[100:101], v161 offset:0\n\t" \
  "buffer_load_dwordx4 v[88:91], v164, %[ob], s75 offen offset:2048 sc1\n\t" \
  "v_mfma_f32_16x16x32_bf16 v[152:155], v[104:107], v[20:23], v[152:155]\n\t" \
  "ds_read_b64_tr_b16 v[102:103], v161 offset:2048\n\t" \
  "v_mfma_f32_16x16x32_bf16 v[140:143], v[108:111], v[16:19], v[140:143]\n\t" \
  "v_add_u32 v162, 0x10000, v162\n\t" \
  "ds_read_b64_tr_b16 v[104:105], v162 offset:0\n\t" \
  "v_mfma_f32_16x16x32_bf16 v[156:159], v[108:111], v[20:23], v[156:159]\n\t" \
  "ds_read_b64_tr_b16 v[106:107], v162 offset:2048\n\t" \
  "s_waitcnt vmcnt(15) lgkmcnt(10)\n\t" \
  "v_mfma_f32_16x16x32_bf16 v[128:131], v[112:115], v[24:27], v[128:131]\n\t" \
  "v_add_u32 v163, 0x10000, v163\n\t" \
  "ds_read_b64_tr_b16 v[108:109], v163 offset:0\n\t" \
  "v_mfma_f32_16x16x32_bf16 v[144:147], v[112:115], v[28:31], v[144:147]\n\t" \
  "ds_read_b64_tr_b16 v[110:111], v163 offset:2048\n\t" \
  "buffer_load_dwordx4 v[92:95], v164, %[ob], s75 offen offset:3072 sc1\n\t" \
  "v_mfma_f32_16x16x32_bf16 v[132:135], v[116:119], v[24:27], v[132:135]\n\t" \
  "ds_read_b64_tr_b16 v[112:113], v160 offset:4096\n\t" \
  "v_mfma_f32_16x16x32_bf16 v[148:151], v[116:119], v[28:31], v[148:151]\n\t" \
  "ds_read_b64_tr_b16 v[114:115], v160 offset:6144\n\t" \
  "s_waitcnt lgkmcnt(10)\n\t" \
  "v_mfma_f32_16x16x32_bf16 v[136:139], v[120:123], v[24:27], v[136:139]\n\t" \
  "ds_read_b64_tr_b16 v[116:117], v161 offset:4096\n\t" \
  "v_mfma_f32_16x16x32_bf16 v[152:155], v[120:123], v[28:31], v[152:155]\n\t" \
  "ds_read_b64_tr_b16 v[118:119], v161 offset:6144\n\t" \
  "v_mfma_f32_16x16x32_bf16 v[140:143], v[124:127], v[24:27], v[140:143]\n\t" \
  "ds_read_b64_tr_b16 v[120:121], v162 offset:4096\n\t" \
  "buffer_load_dwordx4 v[0:3], v164, %[ob], s76 offen offset:0 sc1\n\t" \
  "v_mfma_f32_16x16x32_bf16 v[156:159], v[124:127], v[28:31], v[156:159]\n\t" \
  "ds_read_b64_tr_b16 v[122:123], v162 offset:6144\n\t" \
  "s_waitcnt vmcnt(15) lgkmcnt(10)\n\t" \
  "v_mfma_f32_16x16x32_bf16 v[128:131], v[96:99], v[32:35], v[128:131]\n\t" \
  "ds_read_b64_tr_b16 v[124:125], v163 offset:4096\n\t" \
  "v_mfma_f32_16x16x32_bf16 v[144:147], v[96:99], v[36:39], v[144:147]\n\t" \
  "ds_read_b64_tr_b16 v[126:127], v163 offset:6144\n\t" \
  "v_mfma_f32_16x16x32_bf16 v[132:135], v[100:103], v[32:35], v[132:135]\n\t" \
  "ds_read_b64_tr_b16 v[96:97], v160 offset:8192\n\t" \
  "v_mfma_f32_16x16x32_bf16 v[148:151], v[100:103], v[36:39], v[148:151]\n\t" \
  "ds_read_b64_tr_b16 v[98:99], v160 offset:10240\n\t" \
  "s_waitcnt lgkmcnt(10)\n\t" \
  "v_mfma_f32_16x16x32_bf16 v[136:139], v[104:107], v[32:35], v[136:139]\n\t" \
  "ds_read_b64_tr_b16 v[100:101], v161 offset:8192\n\t" \
  "buffer_load_dwordx4 v[4:7], v164, %[ob], s76 offen offset:1024 sc1\n\t" \
  "v_mfma_f32_16x16x32_bf16 v[152:155], v[104:107], v[36:39], v[152:155]\n\t" \
  "ds_read_b64_tr_b16 v[102:103], v161 offset:10240\n\t" \
  "v_mfma_f32_16x16x32_bf16 v[140:143], v[108:111], v[32:35], v[140:143]\n\t" \
  "ds_read_b64_tr_b16 v[104:105], v162 offset:8192\n\t" \
  "v_mfma_f32_16x16x32_bf16 v[156:159], v[108:111], v[36:39], v[156:159]\n\t" \
  "ds_read_b64_tr_b16 v[106:107], v162 offset:10240\n\t" \
  "s_waitcnt vmcnt(14) lgkmcnt(10)\n\t" \
  "v_mfma_f32_16x16x32_bf16 v[128:131], v[112:115], v[40:43], v[128:131]\n\t" \
  "ds_read_b64_tr_b16 v[108:109], v163 offset:8192\n\t" \
  "buffer_load_dwordx4 v[8:11], v164, %[ob], s76 offen offset:2048 sc1\n\t" \
  "v_mfma_f32_16x16x32_bf16 v[144:147], v[112:115], v[44:47], v[144:147]\n\t" \
  "ds_read_b64_tr_b16 v[110:111], v163 offset:10240\n\t" \
  "v_mfma_f32_16x16x32_bf16 v[132:135], v[116:119], v[40:43], v[132:135]\n\t" \
  "ds_read_b64_tr_b16 v[112:113], v160 offset:12288\n\t" \
  "v_mfma_f32_16x16x32_bf16 v[148:151], v[116:119], v[44:47], v[148:151]\n\t" \
  "ds_read_b64_tr_b16 v[114:115], v160 offset:14336\n\t" \
  "s_waitcnt lgkmcnt(10)\n\t" \
  "v_mfma_f32_16x16x32_bf16 v[136:139], v[120:123], v[40:43], v[136:139]\n\t" \
  "ds_read_b64_tr_b16 v[116:117], v161 offset:12288\n\t" \
  "buffer_load_dwordx4 v[12:15], v164, %[ob], s76 offen offset:3072 sc1\n\t" \
  "v_mfma_f32_16x16x32_bf16 v[152:155], v[120:123], v[44:47], v[152:155]\n\t" \
  "ds_read_b64_tr_b16 v[118:119], v161 offset:14336\n\t" \
  "v_mfma_f32_16x16x32_bf16 v[140:143], v[124:127], v[40:43], v[140:143]\n\t" \
  "ds_read_b64_tr_b16 v[120:121], v162 offset:12288\n\t" \
  "v_mfma_f32_16x16x32_bf16 v[156:159], v[124:127], v[44:47], v[156:159]\n\t" \
  "ds_read_b64_tr_b16 v[122:123], v162 offset:14336\n\t" \
  "s_waitcnt vmcnt(14) lgkmcnt(10)\n\t" \
  "v_mfma_f32_16x16x32_bf16 v[128:131], v[96:99], v[48:51], v[128:131]\n\t" \
  "ds_read_b64_tr_b16 v[124:125], v163 offset:12288\n\t" \
  "v_mfma_f32_16x16x32_bf16 v[144:147], v[96:99], v[52:55], v[144:147]\n\t" \
  "ds_read_b64_tr_b16 v[126:127], v163 offset:14336\n\t" \
  "buffer_load_dwordx4 v[16:19], v164, %[ob], s77 offen offset:0 sc1\n\t" \
  "v_mfma_f32_16x16x32_bf16 v[132:135], v[100:103], v[48:51], v[132:135]\n\t" \
  "ds_read_b64_tr_b16 v[96:97], v160 offset:16384\n\t" \
  "v_mfma_f32_16x16x32_bf16 v[148:151], v[100:103], v[52:55], v[148:151]\n\t" \
  "ds_read_b64_tr_b16 v[98:99], v160 offset:18432\n\t" \
  "s_waitcnt lgkmcnt(10)\n\t" \
  "v_mfma_f32_16x16x32_bf16 v[136:139], v[104:107], v[48:51], v[136:139]\n\t" \
  "ds_read_b64_tr_b16 v[100:101], v161 offset:16384\n\t" \
  "v_mfma_f32_16x16x32_bf16 v[152:155], v[104:107], v[52:55], v[152:155]\n\t" \
  "ds_read_b64_tr_b16 v[102:103], v161 offset:18432\n\t" \
  "v_mfma_f32_16x16x32_bf16 v[140:143], v[108:111], v[48:51], v[140:143]\n\t" \
  "ds_read_b64_tr_b16 v[104:105], v162 offset:16384\n\t" \
  "buffer_load_dwordx4 v[20:23], v164, %[ob], s77 offen offset:1024 sc1\n\t" \
  "v_mfma_f32_16x16x32_bf16 v[156:159], v[108:111], v[52:55], v[156:159]\n\t" \
  "ds_read_b64_tr_b16 v[106:107], v162 offset:18432\n\t" \
  "s_waitcnt vmcnt(14) lgkmcnt(10)\n\t" \
  "v_mfma_f32_16x16x32_bf16 v[128:131], v[112:115], v[56:59], v[128:131]\n\t" \
  "ds_read_b64_tr_b16 v[108:109], v163 offset:16384\n\t" \
  "v_mfma_f32_16x16x32_bf16 v[144:147], v[112:115], v[60:63], v[144:147]\n\t" \
  "ds_read_b64_tr_b16 v[110:111], v163 offset:18432\n\t" \
  "ds_read_b32 v165, v165\n\t" \
  "v_mfma_f32_16x16x32_bf16 v[132:135], v[116:119], v[56:59], v[132:135]\n\t" \
  "ds_read_b64_tr_b16 v[112:113], v160 offset:20480\n\t" \
  "v_mfma_f32_16x16x32_bf16 v[148:151], v[116:119], v[60:63], v[148:151]\n\t" \
  "ds_read_b64_tr_b16 v[114:115], v160 offset:22528\n\t" \
  "s_waitcnt lgkmcnt(11)\n\t" \
  "v_mfma_f32_16x16x32_bf16 v[136:139], v[120:123], v[56:59], v[136:139]\n\t" \
  "ds_read_b64_tr_b16 v[116:117], v161 offset:20480\n\t" \
  "buffer_load_dwordx4 v[24:27], v164, %[ob], s77 offen offset:2048 sc1\n\t" \
  "v_mfma_f32_16x16x32_bf16 v[152:155], v[120:123], v[60:63], v[152:155]\n\t" \
  "ds_read_b64_tr_b16 v[118:119], v161 offset:22528\n\t" \
  "v_mfma_f32_16x16x32_bf16 v[140:143], v[124:127], v[56:59], v[140:143]\n\t" \
  "ds_read_b64_tr_b16 v[120:121], v162 offset:20480\n\t" \
  "v_mfma_f32_16x16x32_bf16 v[156:159], v[124:127], v[60:63], v[156:159]\n\t" \
  "ds_read_b64_tr_b16 v[122:123], v162 offset:22528\n\t" \
  "s_waitcnt vmcnt(13) lgkmcnt(11)\n\t" \
  "v_mfma_f32_16x16x32_bf16 v[128:131], v[96:99], v[64:67], v[128:131]\n\t" \
  "ds_read_b64_tr_b16 v[124:125], v163 offset:20480\n\t" \
  "buffer_load_dwordx4 v[28:31], v164, %[ob], s77 offen offset:3072 sc1\n\t" \
  "v_mfma_f32_16x16x32_bf16 v[144:147], v[96:99], v[68:71], v[144:147]\n\t" \
  "ds_read_b64_tr_b16 v[126:127], v163 offset:22528\n\t" \
  "v_mfma_f32_16x16x32_bf16 v[132:135], v[100:103], v[64:67], v[132:135]\n\t" \
  "ds_read_b64_tr_b16 v[96:97], v160 offset:24576\n\t" \
  "s_waitcnt lgkmcnt(9)\n\t" \
  "v_mfma_f32_16x16x32_bf16 v[148:151], v[100:103], v[68:71], v[148:151]\n\t" \
  "ds_read_b64_tr_b16 v[98:99], v160 offset:26624\n\t" \
  "v_cmp_lt_u32 vcc, 15, v165\n\t" \
  "v_mfma_f32_16x16x32_bf16 v[136:139], v[104:107], v[64:67], v[136:139]\n\t" \
  "ds_read_b64_tr_b16 v[100:101], v161 offset:24576\n\t" \
  "s_cbranch_vccz QA14_%=\n\t" \
  "QB14_%=:\n\t" \
  "v_mfma_f32_16x16x32_bf16 v[152:155], v[104:107], v[68:71], v[152:155]\n\t" \
  "ds_read_b64_tr_b16 v[102:103], v161 offset:26624\n\t" \
  "buffer_load_dwordx4 v[32:35], v164, %[ob], s78 offen offset:0 sc1\n\t" \
  "v_mfma_f32_16x16x32_bf16 v[140:143], v[108:111], v[64:67], v[140:143]\n\t" \
  "ds_read_b64_tr_b16 v[104:105], v162 offset:24576\n\t" \
  "v_mfma_f32_16x16x32_bf16 v[156:159], v[108:111], v[68:71], v[156:159]\n\t" \
  "ds_read_b64_tr_b16 v[106:107], v162 offset:26624\n\t" \
  "s_waitcnt vmcnt(13) lgkmcnt(10)\n\t" \
  "v_mfma_f32_16x16x32_bf16 v[128:131], v[112:115], v[72:75], v[128:131]\n\t" \
  "ds_read_b64_tr_b16 v[108:109], v163 offset:24576\n\t" \
  "v_mfma_f32_16x16x32_bf16 v[144:147], v[112:115], v[76:79], v[144:147]\n\t" \
  "ds_read_b64_tr_b16 v[110:111], v163 offset:26624\n\t" \
  "v_mfma_f32_16x16x32_bf16 v[132:135], v[116:119], v[72:75], v[132:135]\n\t" \
  "ds_read_b64_tr_b16 v[112:113], v160 offset:28672\n\t" \
  "buffer_load_dwordx4 v[36:39], v164, %[ob], s78 offen offset:1024 sc1\n\t" \
  "v_mfma_f32_16x16x32_bf16 v[148:151], v[116:119], v[76:79], v[148:151]\n\t" \
  "ds_read_b64_tr_b16 v[114:115], v160 offset:30720\n\t" \
  "s_waitcnt lgkmcnt(10)\n\t" \
  "v_mfma_f32_16x16x32_bf16 v[136:139], v[120:123], v[72:75], v[136:139]\n\t" \
  "ds_read_b64_tr_b16 v[116:117], v161 offset:28672\n\t" \
  "v_mfma_f32_16x16x32_bf16 v[152:155], v[120:123], v[76:79], v[152:155]\n\t" \
  "ds_read_b64_tr_b16 v[118:119], v161 offset:30720\n\t" \
  "v_mfma_f32_16x16x32_bf16 v[140:143], v[124:127], v[72:75], v[140:143]\n\t" \
  "ds_read_b64_tr_b16 v[120:121], v162 offset:28672\n\t" \
  "v_mfma_f32_16x16x32_bf16 v[156:159], v[124:127], v[76:79], v[156:159]\n\t" \
  "ds_read_b64_tr_b16 v[122:123], v162 offset:30720\n\t" \
  "s_waitcnt vmcnt(12) lgkmcnt(10)\n\t" \
  "v_mfma_f32_16x16x32_bf16 v[128:131], v[96:99], v[80:83], v[128:131]\n\t" \
  "ds_read_b64_tr_b16 v[124:125], v163 offset:28672\n\t" \
  "buffer_load_dwordx4 v[40:43], v164, %[ob], s78 offen offset:2048 sc1\n\t" \
  "v_mfma_f32_16x16x32_bf16 v[144:147], v[96:99], v[84:87], v[144:147]\n\t" \
  "ds_read_b64_tr_b16 v[126:127], v163 offset:30720\n\t" \
  "v_mfma_f32_16x16x32_bf16 v[132:135], v[100:103], v[80:83], v[132:135]\n\t" \
  "ds_read_b64_tr_b16 v[96:97], v160 offset:32768\n\t" \
  "v_mfma_f32_16x16x32_bf16 v[148:151], v[100:103], v[84:87], v[148:151]\n\t" \
  "ds_read_b64_tr_b16 v[98:99], v160 offset:34816\n\t" \
  "s_waitcnt lgkmcnt(10)\n\t" \
  "v_mfma_f32_16x16x32_bf16 v[136:139], v[104:107], v[80:83], v[136:139]\n\t" \
  "ds_read_b64_tr_b16 v[100:101], v161 offset:32768\n\t" \
  "buffer_load_dwordx4 v[44:47], v164, %[ob], s78 offen offset:3072 sc1\n\t" \
  "v_mfma_f32_16x16x32_bf16 v[152:155], v[104:107], v[84:87], v[152:155]\n\t" \
  "ds_read_b64_tr_b16 v[102:103], v161 offset:34816\n\t" \
  "v_mfma_f32_16x16x32_bf16 v[140:143], v[108:111], v[80:83], v[140:143]\n\t" \
  "ds_read_b64_tr_b16 v[104:105], v162 offset:32768\n\t" \
  "v_mfma_f32_16x16x32_bf16 v[156:159], v[108:111], v[84:87], v[156:159]\n\t" \
  "ds_read_b64_tr_b16 v[106:107], v162 offset:34816\n\t" \
  "s_waitcnt vmcnt(12) lgkmcnt(10)\n\t" \
  "v_mfma_f32_16x16x32_bf16 v[128:131], v[112:115], v[88:91], v[128:131]\n\t" \
  "ds_read_b64_tr_b16 v[108:109], v163 offset:32768\n\t" \
  "v_mfma_f32_16x16x32_bf16 v[144:147], v[112:115], v[92:95], v[144:147]\n\t" \
  "ds_read_b64_tr_b16 v[110:111], v163 offset:34816\n\t" \
  "buffer_load_dwordx4 v[48:51], v164, %[ob], s79 offen offset:0 sc1\n\t" \
  "v_mfma_f32_16x16x32_bf16 v[132:135], v[116:119], v[88:91], v[132:135]\n\t" \
  "ds_read_b64_tr_b16 v[112:113], v160 offset:36864\n\t" \
  "v_mfma_f32_16x16x32_bf16 v[148:151], v[116:119], v[92:95], v[148:151]\n\t" \
  "ds_read_b64_tr_b16 v[114:115], v160 offset:38912\n\t" \
  "s_waitcnt lgkmcnt(10)\n\t" \
  "v_mfma_f32_16x16x32_bf16 v[136:139], v[120:123], v[88:91], v[136:139]\n\t" \
  "ds_read_b64_tr_b16 v[116:117], v161 offset:36864\n\t" \
  "v_mfma_f32_16x16x32_bf16 v[152:155], v[120:123], v[92:95], v[152:155]\n\t" \
  "ds_read_b64_tr_b16 v[118:119], v161 offset:38912\n\t" \
  "v_mfma_f32_16x16x32_bf16 v[140:143], v[124:127], v[88:91], v[140:143]\n\t" \
  "ds_read_b64_tr_b16 v[120:121], v162 offset:36864\n\t" \
  "buffer_load_dwordx4 v[52:55], v164, %[ob], s79 offen offset:1024 sc1\n\t" \
  "v_mfma_f32_16x16x32_bf16 v[156:159], v[124:127], v[92:95], v[156:159]\n\t" \
  "ds_read_b64_tr_b16 v[122:123], v162 offset:38912\n\t" \
  "s_waitcnt vmcnt(12) lgkmcnt(10)\n\t" \
  "v_mfma_f32_16x16x32_bf16 v[128:131], v[96:99], v[0:3], v[128:131]\n\t" \
  "ds_read_b64_tr_b16 v[124:125], v163 offset:36864\n\t" \
  "v_mfma_f32_16x16x32_bf16 v[144:147], v[96:99], v[4:7], v[144:147]\n\t" \
  "ds_read_b64_tr_b16 v[126:127], v163 offset:38912\n\t" \
  "v_mfma_f32_16x16x32_bf16 v[132:135], v[100:103], v[0:3], v[132:135]\n\t" \
  "ds_read_b64_tr_b16 v[96:97], v160 offset:40960\n\t" \
  "v_mfma_f32_16x16x32_bf16 v[148:151], v[100:103], v[4:7], v[148:151]\n\t" \
  "ds_read_b64_tr_b16 v[98:99], v160 offset:43008\n\t" \
  "s_waitcnt lgkmcnt(10)\n\t" \
  "v_mfma_f32_16x16x32_bf16 v[136:139], v[104:107], v[0:3], v[136:139]\n\t" \
  "ds_read_b64_tr_b16 v[100:101], v161 offset:40960\n\t" \
  "buffer_load_dwordx4 v[56:59], v164, %[ob], s79 offen offset:2048 sc1\n\t" \
  "v_mfma_f32_16x16x32_bf16 v[152:155], v[104:107], v[4:7], v[152:155]\n\t" \
  "ds_read_b64_tr_b16 v[102:103], v161 offset:43008\n\t" \
  "v_mfma_f32_16x16x32_bf16 v[140:143], v[108:111], v[0:3], v[140:143]\n\t" \
  "ds_read_b64_tr_b16 v[104:105], v162 offset:40960\n\t" \
  "v_mfma_f32_16x16x32_bf16 v[156:159], v[108:111], v[4:7], v[156:159]\n\t" \
  "ds_read_b64_tr_b16 v[106:107], v162 offset:43008\n\t" \
  "s_waitcnt vmcnt(11) lgkmcnt(10)\n\t" \
  "v_mfma_f32_16x16x32_bf16 v[128:131], v[112:115], v[8:11], v[128:131]\n\t" \
  "ds_read_b64_tr_b16 v[108:109], v163 offset:40960\n\t" \
  "buffer_load_dwordx4 v[60:63], v164, %[ob], s79 offen offset:3072 sc1\n\t" \
  "v_mfma_f32_16x16x32_bf16 v[144:147], v[112:115], v[12:15], v[144:147]\n\t" \
  "ds_read_b64_tr_b16 v[110:111], v163 offset:43008\n\t" \
  "v_mfma_f32_16x16x32_bf16 v[132:135], v[116:119], v[8:11], v[132:135]\n\t" \
  "ds_read_b64_tr_b16 v[112:113], v160 offset:45056\n\t" \
  "v_mfma_f32_16x16x32_bf16 v[148:151], v[116:119], v[12:15], v[148:151]\n\t" \
  "ds_read_b64_tr_b16 v[114:115], v160 offset:47104\n\t" \
  "s_waitcnt lgkmcnt(10)\n\t" \
  "v_mfma_f32_16x16x32_bf16 v[136:139], v[120:123], v[8:11], v[136:139]\n\t" \
  "ds_read_b64_tr_b16 v[116:117], v161 offset:45056\n\t" \
  "v_mfma_f32_16x16x32_bf16 v[152:155], v[120:123], v[12:15], v[152:155]\n\t" \
  "ds_read_b64_tr_b16 v[118:119], v161 offset:47104\n\t" \
  "v_mfma_f32_16x16x32_bf16 v[140:143], v[124:127], v[8:11], v[140:143]\n\t" \
  "ds_read_b64_tr_b16 v[120:121], v162 offset:45056\n\t" \
  "v_mfma_f32_16x16x32_bf16 v[156:159], v[124:127], v[12:15], v[156:159]\n\t" \
  "ds_read_b64_tr_b16 v[122:123], v162 offset:47104\n\t" \
  "s_waitcnt vmcnt(10) lgkmcnt(10)\n\t" \
  "v_mfma_f32_16x16x32_bf16 v[128:131], v[96:99], v[16:19], v[128:131]\n\t" \
  "ds_read_b64_tr_b16 v[124:125], v163 offset:45056\n\t" \
  "v_mfma_f32_16x16x32_bf16 v[144:147], v[96:99], v[20:23], v[144:147]\n\t" \
  "ds_read_b64_tr_b16 v[126:127], v163 offset:47104\n\t" \
  "v_mfma_f32_16x16x32_bf16 v[132:135], v[100:103], v[16:19], v[132:135]\n\t" \
  "ds_read_b64_tr_b16 v[96:97], v160 offset:49152\n\t" \
  "v_mfma_f32_16x16x32_bf16 v[148:151], v[100:103], v[20:23], v[148:151]\n\t" \
  "ds_read_b64_tr_b16 v[98:99], v160 offset:51200\n\t" \
  "s_waitcnt lgkmcnt(10)\n\t" \
  "v_mfma_f32_16x16x32_bf16 v[136:139], v[104:107], v[16:19], v[136:139]\n\t" \
  "ds_read_b64_tr_b16 v[100:101], v161 offset:49152\n\t" \
  "v_xor_b32 v0, 32, v167\n\t" \
  "v_mfma_f32_16x16x32_bf16 v[152:155], v[104:107], v[20:23], v[152:155]\n\t" \
  "ds_read_b64_tr_b16 v[102:103], v161 offset:51200\n\t" \
  "v_xor_b32 v1, 64, v167\n\t" \
  "v_mfma_f32_16x16x32_bf16 v[140:143], v[108:111], v[16:19], v[140:143]\n\t" \
  "ds_read_b64_tr_b16 v[104:105], v162 offset:49152\n\t" \
  "v_xor_b32 v2, 0x60, v167\n\t" \
  "v_mfma_f32_16x16x32_bf16 v[156:159], v[108:111], v[20:23], v[156:159]\n\t" \
  "ds_read_b64_tr_b16 v[106:107], v162 offset:51200\n\t" \
  "s_waitcnt vmcnt(8) lgkmcnt(10)\n\t" \
  "v_mfma_f32_16x16x32_bf16 v[128:131], v[112:115], v[24:27], v[128:131]\n\t" \
  "ds_read_b64_tr_b16 v[108:109], v163 offset:49152\n\t" \
  "v_bfe_u32 v5, v164, 7, 3\n\t" \
  "v_mfma_f32_16x16x32_bf16 v[144:147], v[112:115], v[28:31], v[144:147]\n\t" \
  "ds_read_b64_tr_b16 v[110:111], v163 offset:51200\n\t" \
  "v_and_b32 v4, 0x70, v164\n\t" \
  "v_mfma_f32_16x16x32_bf16 v[132:135], v[116:119], v[24:27], v[132:135]\n\t" \
  "ds_read_b64_tr_b16 v[112:113], v160 offset:53248\n\t" \
  "v_lshl_or_b32 v4, v5, 12, v4\n\t" \
  "v_mfma_f32_16x16x32_bf16 v[148:151], v[116:119], v[28:31], v[148:151]\n\t" \
  "ds_read_b64_tr_b16 v[114:115], v160 offset:55296\n\t" \
  "s_waitcnt lgkmcnt(10)\n\t" \
  "v_mfma_f32_16x16x32_bf16 v[136:139], v[120:123], v[24:27], v[136:139]\n\t" \
  "ds_read_b64_tr_b16 v[116:117], v161 offset:53248\n\t" \
  "v_add_u32 v5, 0x8000, v4\n\t" \
  "v_mfma_f32_16x16x32_bf16 v[152:155], v[120:123], v[28:31], v[152:155]\n\t" \
  "ds_read_b64_tr_b16 v[118:119], v161 offset:55296\n\t" \
  "v_add_u32 v6, 0x10000, v4\n\t" \
  "v_mfma_f32_16x16x32_bf16 v[140:143], v[124:127], v[24:27], v[140:143]\n\t" \
  "ds_read_b64_tr_b16 v[120:121], v162 offset:53248\n\t" \
  "v_add_u32 v7, 0x18000, v4\n\t" \
  "v_mfma_f32_16x16x32_bf16 v[156:159], v[124:127], v[28:31], v[156:159]\n\t" \
  "ds_read_b64_tr_b16 v[122:123], v162 offset:55296\n\t" \
  "s_waitcnt vmcnt(6) lgkmcnt(10)\n\t" \
  "v_mfma_f32_16x16x32_bf16 v[128:131], v[96:99], v[32:35], v[128:131]\n\t" \
  "ds_read_b64_tr_b16 v[124:125], v163 offset:53248\n\t" \
  "v_mfma_f32_16x16x32_bf16 v[144:147], v[96:99], v[36:39], v[144:147]\n\t" \
  "ds_read_b64_tr_b16 v[126:127], v163 offset:55296\n\t" \
  "v_mfma_f32_16x16x32_bf16 v[132:135], v[100:103], v[32:35], v[132:135]\n\t" \
  "ds_read_b64_tr_b16 v[96:97], v160 offset:57344\n\t" \
  "v_mfma_f32_16x16x32_bf16 v[148:151], v[100:103], v[36:39], v[148:151]\n\t" \
  "ds_read_b64_tr_b16 v[98:99], v160 offset:59392\n\t" \
  "s_waitcnt lgkmcnt(10)\n\t" \
  "v_mfma_f32_16x16x32_bf16 v[136:139], v[104:107], v[32:35], v[136:139]\n\t" \
  "ds_read_b64_tr_b16 v[100:101], v161 offset:57344\n\t" \
  "v_mfma_f32_16x16x32_bf16 v[152:155], v[104:107], v[36:39], v[152:155]\n\t" \
  "ds_read_b64_tr_b16 v[102:103], v161 offset:59392\n\t" \
  "v_mfma_f32_16x16x32_bf16 v[140:143], v[108:111], v[32:35], v[140:143]\n\t" \
  "ds_read_b64_tr_b16 v[104:105], v162 offset:57344\n\t" \
  "v_mfma_f32_16x16x32_bf16 v[156:159], v[108:111], v[36:39], v[156:159]\n\t" \
  "ds_read_b64_tr_b16 v[106:107], v162 offset:59392\n\t" \
  "s_waitcnt vmcnt(4) lgkmcnt(10)\n\t" \
  "v_mfma_f32_16x16x32_bf16 v[128:131], v[112:115], v[40:43], v[128:131]\n\t" \
  "ds_read_b64_tr_b16 v[108:109], v163 offset:57344\n\t" \
  "v_mfma_f32_16x16x32_bf16 v[144:147], v[112:115], v[44:47], v[144:147]\n\t" \
  "ds_read_b64_tr_b16 v[110:111], v163 offset:59392\n\t" \
  "v_mfma_f32_16x16x32_bf16 v[132:135], v[116:119], v[40:43], v[132:135]\n\t" \
  "ds_read_b64_tr_b16 v[112:113], v160 offset:61440\n\t" \
  "v_mfma_f32_16x16x32_bf16 v[148:151], v[116:119], v[44:47], v[148:151]\n\t" \
  "ds_read_b64_tr_b16 v[114:115], v160 offset:63488\n\t" \
  "s_waitcnt lgkmcnt(10)\n\t" \
  "v_mfma_f32_16x16x32_bf16 v[136:139], v[120:123], v[40:43], v[136:139]\n\t" \
  "ds_read_b64_tr_b16 v[116:117], v161 offset:61440\n\t" \
  "v_mfma_f32_16x16x32_bf16 v[152:155], v[120:123], v[44:47], v[152:155]\n\t" \
  "ds_read_b64_tr_b16 v[118:119], v161 offset:63488\n\t" \
  "v_mfma_f32_16x16x32_bf16 v[140:143], v[124:127], v[40:43], v[140:143]\n\t" \
  "ds_read_b64_tr_b16 v[120:121], v162 offset:61440\n\t" \
  "v_mfma_f32_16x16x32_bf16 v[156:159], v[124:127], v[44:47], v[156:159]\n\t" \
  "ds_read_b64_tr_b16 v[122:123], v162 offset:63488\n\t" \
  "s_waitcnt vmcnt(0) lgkmcnt(10)\n\t" \
  "v_mfma_f32_16x16x32_bf16 v[128:131], v[96:99], v[48:51], v[128:131]\n\t" \
  "ds_read_b64_tr_b16 v[124:125], v163 offset:61440\n\t" \
  "v_mfma_f32_16x16x32_bf16 v[132:135], v[100:103], v[48:51], v[132:135]\n\t" \
  "ds_read_b64_tr_b16 v[126:127], v163 offset:63488\n\t" \
  "s_waitcnt lgkmcnt(6)\n\t" \
  "v_mfma_f32_16x16x32_bf16 v[136:139], v[104:107], v[48:51], v[136:139]\n\t" \
  "v_mfma_f32_16x16x32_bf16 v[140:143], v[108:111], v[48:51], v[140:143]\n\t" \
  "v_mfma_f32_16x16x32_bf16 v[128:131], v[112:115], v[56:59], v[128:131]\n\t" \
  "s_waitcnt lgkmcnt(2)\n\t" \
  "v_mfma_f32_16x16x32_bf16 v[132:135], v[116:119], v[56:59], v[132:135]\n\t" \
  "v_mfma_f32_16x16x32_bf16 v[136:139], v[120:123], v[56:59], v[136:139]\n\t" \
  "v_mfma_f32_16x16x32_bf16 v[144:147], v[96:99], v[52:55], v[144:147]\n\t" \
  "s_waitcnt lgkmcnt(0)\n\t" \
  "v_mfma_f32_16x16x32_bf16 v[140:143], v[124:127], v[56:59], v[140:143]\n\t" \
  "v_mfma_f32_16x16x32_bf16 v[144:147], v[112:115], v[60:63], v[144:147]\n\t" \
  "s_nop 0\n\t" \
  "v_cvt_pk_bf16_f32 v16, v128, v129\n\t" \
  "v_mfma_f32_16x16x32_bf16 v[148:151], v[100:103], v[52:55], v[148:151]\n\t" \
  "v_cvt_pk_bf16_f32 v17, v130, v131\n\t" \
  "v_cvt_pk_bf16_f32 v18, v132, v133\n\t" \
  "v_mfma_f32_16x16x32_bf16 v[148:151], v[116:119], v[60:63], v[148:151]\n\t" \
  "v_cvt_pk_bf16_f32 v19, v134, v135\n\t" \
  "ds_write_b64 v167, v[16:17]\n\t" \
  "v_mfma_f32_16x16x32_bf16 v[152:155], v[104:107], v[52:55], v[152:155]\n\t" \
  "v_cvt_pk_bf16_f32 v20, v136, v137\n\t" \
  "ds_write_b64 v0, v[18:19]\n\t" \
  "v_mfma_f32_16x16x32_bf16 v[152:155], v[120:123], v[60:63], v[152:155]\n\t" \
  "v_cvt_pk_bf16_f32 v21, v138, v139\n\t" \
  "v_cvt_pk_bf16_f32 v22, v140, v141\n\t" \
  "v_mfma_f32_16x16x32_bf16 v[156:159], v[108:111], v[52:55], v[156:159]\n\t" \
  "v_cvt_pk_bf16_f32 v23, v142, v143\n\t" \
  "ds_write_b64 v1, v[20:21]\n\t" \
  "v_mfma_f32_16x16x32_bf16 v[156:159], v[124:127], v[60:63], v[156:159]\n\t" \
  "v_cvt_pk_bf16_f32 v24, v144, v145\n\t" \
  "ds_write_b64 v2, v[22:23]\n\t" \
  "v_cvt_pk_bf16_f32 v25, v146, v147\n\t" \
  "ds_read_b128 v[64:67], v166 offset:0\n\t" \
  "v_cvt_pk_bf16_f32 v26, v148, v149\n\t" \
  "ds_read_b128 v[68:71], v166 offset:1024\n\t" \
  "v_cvt_pk_bf16_f32 v27, v150, v151\n\t" \
  "ds_write_b64 v167, v[24:25]\n\t" \
  "v_cvt_pk_bf16_f32 v28, v152, v153\n\t" \
  "v_cvt_pk_bf16_f32 v29, v154, v155\n\t" \
  "ds_write_b64 v0, v[26:27]\n\t" \
  "v_cvt_pk_bf16_f32 v30, v156, v157\n\t" \
  "v_cvt_pk_bf16_f32 v31, v158, v159\n\t" \
  "ds_write_b64 v1, v[28:29]\n\t" \
  "ds_write_b64 v2, v[30:31]\n\t" \
  "ds_read_b128 v[72:75], v166 offset:0\n\t" \
  "ds_read_b128 v[76:79], v166 offset:1024\n\t" \
  "s_waitcnt lgkmcnt(7)\n\t" \
  "global_store_dwordx4 v4, v[64:67], %[op] sc1\n\t" \
  "s_waitcnt lgkmcnt(6)\n\t" \
  "global_store_dwordx4 v5, v[68:71], %[op] sc1\n\t" \
  "s_waitcnt lgkmcnt(1)\n\t" \
  "global_store_dwordx4 v6, v[72:75], %[op] sc1\n\t" \
  "s_waitcnt lgkmcnt(0)\n\t" \
  "global_store_dwordx4 v7, v[76:79], %[op] sc1\n\t" \
  "s_nop 1\n\t"
#define G3_EP 2
#define G3_CLOB "v0", "v1", "v2", "v3", "v4", "v5", "v6", "v7", "v8", "v9", "v10", "v11", "v12", "v13", "v14", "v15", "v16", "v17", "v18", "v19", "v20", "v21", "v22", "v23", "v24", "v25", "v26", "v27", "v28", "v29", "v30", "v31", "v32", "v33", "v34", "v35", "v36", "v37", "v38", "v39", "v40", "v41", "v42", "v43", "v44", "v45", "v46", "v47", "v48", "v49", "v50", "v51", "v52", "v53", "v54", "v55", "v56", "v57", "v58", "v59", "v60", "v61", "v62", "v63", "v64", "v65", "v66", "v67", "v68", "v69", "v70", "v71", "v72", "v73", "v74", "v75", "v76", "v77", "v78", "v79", "v80", "v81", "v82", "v83", "v84", "v85", "v86", "v87", "v88", "v89", "v90", "v91", "v92", "v93", "v94", "v95", "v97", "v98", "v99", "v100", "v101", "v102", "v103", "v104", "v105", "v106", "v107", "v108", "v109", "v110", "v111", "v112", "v113", "v114", "v115", "v116", "v117", "v118", "v119", "v120", "v121", "v122", "v123", "v124", "v125", "v126", "v127", "v128", "v129", "v130", "v131", "v132", "v133", "v134", "v135", "v136", "v137", "v138", "v139", "v140", "v141", "v142", "v143", "v144", "v145", "v146", "v147", "v148", "v149", "v150", "v151", "v152", "v153", "v154", "v155", "v156", "v157", "v158", "v159", "s64", "s65", "s66", "s67", "s68", "s69", "s70", "s71", "s72", "s73", "s74", "s75", "s76", "s77", "s78", "s79"

#define PP_ASM \
  "v_mov_b32 v60, 0\n\t" \
  "s_mov_b32 %[t0], %[n0]\n\t" \
  "s_cmp_eq_u32 %[t0], 0\n\t" \
  "s_cbranch_scc1 PAx_%=\n\t" \
  "PA_%=:\n\t" \
  "s_barrier\n\t" \
  "s_sub_u32 %[t0], %[t0], 1\n\t" \
  "s_cmp_lg_u32 %[t0], 0\n\t" \
  "s_cbranch_scc1 PA_%=\n\t" \
  "PAx_%=:\n\t" \
  "s_mov_b32 m0, %[shd]\n\t" \
  "global_load_dword v160, v60, %[fl] sc1\n\t" \
  "global_load_lds_dwordx4 v163, %[shp]\n\t" \
  "s_mov_b32 m0, %[tgd]\n\t" \
  "s_mov_b64 exec, %[em]\n\t" \
  "global_load_lds_dword v60, %[fl] sc1\n\t" \
  "s_mov_b64 exec, -1\n\t" \
  "global_load_ushort v161, v60, %[hp]\n\t" \
  "global_load_ushort v162, v60, %[dp]\n\t" \
  "global_load_dwordx4 v[0:3], v152, %[kb0]\n\t" \
  "global_load_dwordx4 v[4:7], v153, %[kb0]\n\t" \
  "global_load_dwordx4 v[8:11], v154, %[kb0]\n\t" \
  "global_load_dwordx4 v[12:15], v155, %[kb3] offset:0\n\t" \
  "global_load_dwordx4 v[28:31], v155, %[vb0] offset:0\n\t" \
  "s_setprio 2\n\t" \
  "s_setprio 3\n\t" \
  "s_barrier\n\t" \
  "global_load_dwordx4 v[16:19], v155, %[kb3] offset:2048\n\t" \
  "global_load_dwordx4 v[32:35], v155, %[vb0] offset:2048\n\t" \
  "global_load_dwordx4 v[20:23], v155, %[kb5] offset:0\n\t" \
  "s_setprio 2\n\t" \
  "s_waitcnt vmcnt(8)\n\t" \
  "ds_read_b128 v[128:131], v156 offset:0\n\t" \
  "ds_read_b128 v[132:135], v156 offset:16\n\t" \
  "ds_read_b128 v[136:139], v156 offset:32\n\t" \
  "ds_read_b128 v[140:143], v156 offset:48\n\t" \
  "s_waitcnt lgkmcnt(0)\n\t" \
  "v_lshlrev_b32 v64, 16, v128\n\t" \
  "v_and_b32 v65, 0xffff0000, v128\n\t" \
  "v_lshlrev_b32 v66, 16, v129\n\t" \
  "v_and_b32 v67, 0xffff0000, v129\n\t" \
  "v_lshlrev_b32 v68, 16, v130\n\t" \
  "v_and_b32 v69, 0xffff0000, v130\n\t" \
  "v_lshlrev_b32 v70, 16, v131\n\t" \
  "v_and_b32 v71, 0xffff0000, v131\n\t" \
  "v_lshlrev_b32 v72, 16, v132\n\t" \
  "v_and_b32 v73, 0xffff0000, v132\n\t" \
  "v_lshlrev_b32 v74, 16, v133\n\t" \
  "v_and_b32 v75, 0xffff0000, v133\n\t" \
  "v_lshlrev_b32 v76, 16, v134\n\t" \
  "v_and_b32 v77, 0xffff0000, v134\n\t" \
  "v_lshlrev_b32 v78, 16, v135\n\t" \
  "v_and_b32 v79, 0xffff0000, v135\n\t" \
  "v_lshlrev_b32 v80, 16, v136\n\t" \
  "v_and_b32 v81, 0xffff0000, v136\n\t" \
  "v_lshlrev_b32 v82, 16, v137\n\t" \
  "v_and_b32 v83, 0xffff0000, v137\n\t" \
  "v_lshlrev_b32 v84, 16, v138\n\t" \
  "v_and_b32 v85, 0xffff0000, v138\n\t" \
  "v_lshlrev_b32 v86, 16, v139\n\t" \
  "v_and_b32 v87, 0xffff0000, v139\n\t" \
  "v_lshlrev_b32 v88, 16, v140\n\t" \
  "v_and_b32 v89, 0xffff0000, v140\n\t" \
  "v_lshlrev_b32 v90, 16, v141\n\t" \
  "v_and_b32 v91, 0xffff0000, v141\n\t" \
  "v_lshlrev_b32 v92, 16, v142\n\t" \
  "v_and_b32 v93, 0xffff0000, v142\n\t" \
  "v_lshlrev_b32 v94, 16, v143\n\t" \
  "v_and_b32 v95, 0xffff0000, v143\n\t" \
  "s_waitcnt vmcnt(7)\n\t" \
  "s_mov_b64 exec, %[zm]\n\t" \
  "v_mov_b32 v0, 0\n\t" \
  "v_mov_b32 v1, 0\n\t" \
  "v_mov_b32 v2, 0\n\t" \
  "v_mov_b32 v3, 0\n\t" \
  "s_mov_b64 exec, -1\n\t" \
  "v_lshlrev_b32 v128, 16, v0\n\t" \
  "v_and_b32 v129, 0xffff0000, v0\n\t" \
  "v_lshlrev_b32 v130, 16, v1\n\t" \
  "v_and_b32 v131, 0xffff0000, v1\n\t" \
  "v_lshlrev_b32 v132, 16, v2\n\t" \
  "v_and_b32 v133, 0xffff0000, v2\n\t" \
  "v_lshlrev_b32 v134, 16, v3\n\t" \
  "v_and_b32 v135, 0xffff0000, v3\n\t" \
  "v_mul_f32 v96, v128, v67\n\t" \
  "v_mul_f32 v97, v129, v71\n\t" \
  "v_mul_f32 v98, v130, v75\n\t" \
  "v_mul_f32 v99, v131, v79\n\t" \
  "v_mul_f32 v100, v132, v83\n\t" \
  "v_mul_f32 v101, v133, v87\n\t" \
  "v_mul_f32 v102, v134, v91\n\t" \
  "v_mul_f32 v103, v135, v95\n\t" \
  "s_waitcnt vmcnt(6)\n\t" \
  "s_mov_b64 exec, %[zm]\n\t" \
  "v_mov_b32 v4, 0\n\t" \
  "v_mov_b32 v5, 0\n\t" \
  "v_mov_b32 v6, 0\n\t" \
  "v_mov_b32 v7, 0\n\t" \
  "s_mov_b64 exec, -1\n\t" \
  "v_lshlrev_b32 v128, 16, v4\n\t" \
  "v_and_b32 v129, 0xffff0000, v4\n\t" \
  "v_lshlrev_b32 v130, 16, v5\n\t" \
  "v_and_b32 v131, 0xffff0000, v5\n\t" \
  "v_lshlrev_b32 v132, 16, v6\n\t" \
  "v_and_b32 v133, 0xffff0000, v6\n\t" \
  "v_lshlrev_b32 v134, 16, v7\n\t" \
  "v_and_b32 v135, 0xffff0000, v7\n\t" \
  "v_fmac_f32 v96, v128, v66\n\t" \
  "v_fmac_f32 v97, v129, v70\n\t" \
  "v_fmac_f32 v98, v130, v74\n\t" \
  "v_fmac_f32 v99, v131, v78\n\t" \
  "v_fmac_f32 v100, v132, v82\n\t" \
  "v_fmac_f32 v101, v133, v86\n\t" \
  "v_fmac_f32 v102, v134, v90\n\t" \
  "v_fmac_f32 v103, v135, v94\n\t" \
  "v_mul_f32 v104, v128, v67\n\t" \
  "v_mul_f32 v105, v129, v71\n\t" \
  "v_mul_f32 v106, v130, v75\n\t" \
  "v_mul_f32 v107, v131, v79\n\t" \
  "v_mul_f32 v108, v132, v83\n\t" \
  "v_mul_f32 v109, v133, v87\n\t" \
  "v_mul_f32 v110, v134, v91\n\t" \
  "v_mul_f32 v111, v135, v95\n\t" \
  "s_waitcnt vmcnt(5)\n\t" \
  "s_mov_b64 exec, %[zm]\n\t" \
  "v_mov_b32 v8, 0\n\t" \
  "v_mov_b32 v9, 0\n\t" \
  "v_mov_b32 v10, 0\n\t" \
  "v_mov_b32 v11, 0\n\t" \
  "s_mov_b64 exec, -1\n\t" \
  "s_mov_b32 s85, 0x300000\n\t" \
  "s_load_dword s84, %[ph], s85\n\t" \
  "s_mov_b32 s85, 0x300800\n\t" \
  "s_load_dword s84, %[ph], s85\n\t" \
  "s_mov_b32 s85, 0x301000\n\t" \
  "s_load_dword s84, %[ph], s85\n\t" \
  "s_mov_b32 s85, 0x360000\n\t" \
  "s_load_dword s84, %[ph], s85\n\t" \
  "s_setprio 3\n\t" \
  "s_barrier\n\t" \
  "global_load_dwordx4 v[36:39], v155, %[vb2] offset:0\n\t" \
  "global_load_dwordx4 v[24:27], v155, %[kb5] offset:2048\n\t" \
  "global_load_dwordx4 v[40:43], v155, %[vb2] offset:2048\n\t" \
  "s_setprio 2\n\t" \
  "v_lshlrev_b32 v128, 16, v8\n\t" \
  "v_and_b32 v129, 0xffff0000, v8\n\t" \
  "v_lshlrev_b32 v130, 16, v9\n\t" \
  "v_and_b32 v131, 0xffff0000, v9\n\t" \
  "v_lshlrev_b32 v132, 16, v10\n\t" \
  "v_and_b32 v133, 0xffff0000, v10\n\t" \
  "v_lshlrev_b32 v134, 16, v11\n\t" \
  "v_and_b32 v135, 0xffff0000, v11\n\t" \
  "v_fmac_f32 v96, v128, v65\n\t" \
  "v_fmac_f32 v97, v129, v69\n\t" \
  "v_fmac_f32 v98, v130, v73\n\t" \
  "v_fmac_f32 v99, v131, v77\n\t" \
  "v_fmac_f32 v100, v132, v81\n\t" \
  "v_fmac_f32 v101, v133, v85\n\t" \
  "v_fmac_f32 v102, v134, v89\n\t" \
  "v_fmac_f32 v103, v135, v93\n\t" \
  "v_fmac_f32 v104, v128, v66\n\t" \
  "v_fmac_f32 v105, v129, v70\n\t" \
  "v_fmac_f32 v106, v130, v74\n\t" \
  "v_fmac_f32 v107, v131, v78\n\t" \
  "v_fmac_f32 v108, v132, v82\n\t" \
  "v_fmac_f32 v109, v133, v86\n\t" \
  "v_fmac_f32 v110, v134, v90\n\t" \
  "v_fmac_f32 v111, v135, v94\n\t" \
  "v_mul_f32 v112, v128, v67\n\t" \
  "v_mul_f32 v113, v129, v71\n\t" \
  "v_mul_f32 v114, v130, v75\n\t" \
  "v_mul_f32 v115, v131, v79\n\t" \
  "v_mul_f32 v116, v132, v83\n\t" \
  "v_mul_f32 v117, v133, v87\n\t" \
  "v_mul_f32 v118, v134, v91\n\t" \
  "v_mul_f32 v119, v135, v95\n\t" \
  "s_waitcnt vmcnt(7)\n\t" \
  "v_lshlrev_b32 v128, 16, v12\n\t" \
  "v_and_b32 v129, 0xffff0000, v12\n\t" \
  "v_lshlrev_b32 v130, 16, v13\n\t" \
  "v_and_b32 v131, 0xffff0000, v13\n\t" \
  "v_lshlrev_b32 v132, 16, v14\n\t" \
  "v_and_b32 v133, 0xffff0000, v14\n\t" \
  "v_lshlrev_b32 v134, 16, v15\n\t" \
  "v_and_b32 v135, 0xffff0000, v15\n\t" \
  "v_fmac_f32 v96, v128, v64\n\t" \
  "v_fmac_f32 v97, v129, v68\n\t" \
  "v_fmac_f32 v98, v130, v72\n\t" \
  "v_fmac_f32 v99, v131, v76\n\t" \
  "v_fmac_f32 v100, v132, v80\n\t" \
  "v_fmac_f32 v101, v133, v84\n\t" \
  "v_fmac_f32 v102, v134, v88\n\t" \
  "v_fmac_f32 v103, v135, v92\n\t" \
  "v_fmac_f32 v104, v128, v65\n\t" \
  "v_fmac_f32 v105, v129, v69\n\t" \
  "v_fmac_f32 v106, v130, v73\n\t" \
  "v_fmac_f32 v107, v131, v77\n\t" \
  "v_fmac_f32 v108, v132, v81\n\t" \
  "v_fmac_f32 v109, v133, v85\n\t" \
  "v_fmac_f32 v110, v134, v89\n\t" \
  "v_fmac_f32 v111, v135, v93\n\t" \
  "v_fmac_f32 v112, v128, v66\n\t" \
  "v_fmac_f32 v113, v129, v70\n\t" \
  "v_fmac_f32 v114, v130, v74\n\t" \
  "v_fmac_f32 v115, v131, v78\n\t" \
  "v_fmac_f32 v116, v132, v82\n\t" \
  "v_fmac_f32 v117, v133, v86\n\t" \
  "v_fmac_f32 v118, v134, v90\n\t" \
  "v_fmac_f32 v119, v135, v94\n\t" \
  "v_mul_f32 v120, v128, v67\n\t" \
  "v_mul_f32 v121, v129, v71\n\t" \
  "v_mul_f32 v122, v130, v75\n\t" \
  "v_mul_f32 v123, v131, v79\n\t" \
  "v_mul_f32 v124, v132, v83\n\t" \
  "v_mul_f32 v125, v133, v87\n\t" \
  "v_mul_f32 v126, v134, v91\n\t" \
  "v_mul_f32 v127, v135, v95\n\t" \
  "s_waitcnt vmcnt(6)\n\t" \
  "v_lshlrev_b32 v128, 16, v28\n\t" \
  "v_and_b32 v129, 0xffff0000, v28\n\t" \
  "v_lshlrev_b32 v130, 16, v29\n\t" \
  "v_and_b32 v131, 0xffff0000, v29\n\t" \
  "v_lshlrev_b32 v132, 16, v30\n\t" \
  "v_and_b32 v133, 0xffff0000, v30\n\t" \
  "v_lshlrev_b32 v134, 16, v31\n\t" \
  "v_and_b32 v135, 0xffff0000, v31\n\t" \
  "s_mov_b32 s85, 0x360800\n\t" \
  "s_load_dword s84, %[ph], s85\n\t" \
  "s_mov_b32 s85, 0x361000\n\t" \
  "s_load_dword s84, %[ph], s85\n\t" \
  "s_mov_b32 s85, 0x3c0000\n\t" \
  "s_load_dword s84, %[ph], s85\n\t" \
  "s_mov_b32 s85, 0x3c0800\n\t" \
  "s_load_dword s84, %[ph], s85\n\t" \
  "s_barrier\n\t" \
  "v_mul_f32 v96, v96, v128\n\t" \
  "v_mul_f32 v97, v97, v129\n\t" \
  "v_mul_f32 v98, v98, v130\n\t" \
  "v_mul_f32 v99, v99, v131\n\t" \
  "v_mul_f32 v100, v100, v132\n\t" \
  "v_mul_f32 v101, v101, v133\n\t" \
  "v_mul_f32 v102, v102, v134\n\t" \
  "v_mul_f32 v103, v103, v135\n\t" \
  "v_cvt_pk_bf16_f32 v136, v96, v97\n\t" \
  "v_cvt_pk_bf16_f32 v137, v98, v99\n\t" \
  "v_cvt_pk_bf16_f32 v138, v100, v101\n\t" \
  "v_cvt_pk_bf16_f32 v139, v102, v103\n\t" \
  "v_mov_b32_dpp v128, v136 row_ror:8 row_mask:0xf bank_mask:0xf\n\t" \
  "v_mov_b32_dpp v129, v137 row_ror:8 row_mask:0xf bank_mask:0xf\n\t" \
  "v_mov_b32_dpp v130, v138 row_ror:8 row_mask:0xf bank_mask:0xf\n\t" \
  "v_mov_b32_dpp v131, v139 row_ror:8 row_mask:0xf bank_mask:0xf\n\t" \
  "v_perm_b32 v136, v128, v136, v157\n\t" \
  "v_perm_b32 v137, v129, v137, v157\n\t" \
  "v_perm_b32 v138, v130, v138, v157\n\t" \
  "v_perm_b32 v139, v131, v139, v157\n\t" \
  "v_permlane16_swap_b32 v136, v137\n\t" \
  "s_nop 0\n\t" \
  "v_permlane16_swap_b32 v138, v139\n\t" \
  "s_nop 1\n\t" \
  "v_permlane32_swap_b32 v136, v138\n\t" \
  "v_permlane32_swap_b32 v137, v139\n\t" \
  "s_waitcnt vmcnt(5)\n\t" \
  "v_lshlrev_b32 v128, 16, v16\n\t" \
  "v_and_b32 v129, 0xffff0000, v16\n\t" \
  "v_lshlrev_b32 v130, 16, v17\n\t" \
  "v_and_b32 v131, 0xffff0000, v17\n\t" \
  "v_lshlrev_b32 v132, 16, v18\n\t" \
  "v_and_b32 v133, 0xffff0000, v18\n\t" \
  "v_lshlrev_b32 v134, 16, v19\n\t" \
  "v_and_b32 v135, 0xffff0000, v19\n\t" \
  "v_fmac_f32 v104, v128, v64\n\t" \
  "v_fmac_f32 v105, v129, v68\n\t" \
  "v_fmac_f32 v106, v130, v72\n\t" \
  "v_fmac_f32 v107, v131, v76\n\t" \
  "v_fmac_f32 v108, v132, v80\n\t" \
  "v_fmac_f32 v109, v133, v84\n\t" \
  "v_fmac_f32 v110, v134, v88\n\t" \
  "v_fmac_f32 v111, v135, v92\n\t" \
  "v_fmac_f32 v112, v128, v65\n\t" \
  "v_fmac_f32 v113, v129, v69\n\t" \
  "v_fmac_f32 v114, v130, v73\n\t" \
  "v_fmac_f32 v115, v131, v77\n\t" \
  "v_fmac_f32 v116, v132, v81\n\t" \
  "v_fmac_f32 v117, v133, v85\n\t" \
  "v_fmac_f32 v118, v134, v89\n\t" \
  "v_fmac_f32 v119, v135, v93\n\t" \
  "v_fmac_f32 v120, v128, v66\n\t" \
  "v_fmac_f32 v121, v129, v70\n\t" \
  "v_fmac_f32 v122, v130, v74\n\t" \
  "v_fmac_f32 v123, v131, v78\n\t" \
  "v_fmac_f32 v124, v132, v82\n\t" \
  "v_fmac_f32 v125, v133, v86\n\t" \
  "v_fmac_f32 v126, v134, v90\n\t" \
  "v_fmac_f32 v127, v135, v94\n\t" \
  "s_waitcnt vmcnt(4)\n\t" \
  "v_lshlrev_b32 v128, 16, v32\n\t" \
  "v_and_b32 v129, 0xffff0000, v32\n\t" \
  "v_lshlrev_b32 v130, 16, v33\n\t" \
  "v_and_b32 v131, 0xffff0000, v33\n\t" \
  "v_lshlrev_b32 v132, 16, v34\n\t" \
  "v_and_b32 v133, 0xffff0000, v34\n\t" \
  "v_lshlrev_b32 v134, 16, v35\n\t" \
  "v_and_b32 v135, 0xffff0000, v35\n\t" \
  "v_mul_f32 v104, v104, v128\n\t" \
  "v_mul_f32 v105, v105, v129\n\t" \
  "v_mul_f32 v106, v106, v130\n\t" \
  "v_mul_f32 v107, v107, v131\n\t" \
  "v_mul_f32 v108, v108, v132\n\t" \
  "v_mul_f32 v109, v109, v133\n\t" \
  "v_mul_f32 v110, v110, v134\n\t" \
  "v_mul_f32 v111, v111, v135\n\t" \
  "v_cvt_pk_bf16_f32 v140, v104, v105\n\t" \
  "v_cvt_pk_bf16_f32 v141, v106, v107\n\t" \
  "v_cvt_pk_bf16_f32 v142, v108, v109\n\t" \
  "v_cvt_pk_bf16_f32 v143, v110, v111\n\t" \
  "v_mov_b32_dpp v128, v140 row_ror:8 row_mask:0xf bank_mask:0xf\n\t" \
  "v_mov_b32_dpp v129, v141 row_ror:8 row_mask:0xf bank_mask:0xf\n\t" \
  "s_mov_b32 s85, 0x3c1000\n\t" \
  "s_load_dword s84, %[ph], s85\n\t" \
  "s_mov_b32 s85, 0x420000\n\t" \
  "s_load_dword s84, %[ph], s85\n\t" \
  "s_mov_b32 s85, 0x420800\n\t" \
  "s_load_dword s84, %[ph], s85\n\t" \
  "s_mov_b32 s85, 0x421000\n\t" \
  "s_load_dword s84, %[ph], s85\n\t" \
  "s_setprio 3\n\t" \
  "s_barrier\n\t" \
  "global_store_dwordx4 v159, v[136:139], %[sb] sc1\n\t" \
  "v_add_u32 v158, 0x80, v159\n\t" \
  "v_bfi_b32 v159, %[mk], v158, v159\n\t" \
  "s_setprio 2\n\t" \
  "v_mov_b32_dpp v130, v142 row_ror:8 row_mask:0xf bank_mask:0xf\n\t" \
  "v_mov_b32_dpp v131, v143 row_ror:8 row_mask:0xf bank_mask:0xf\n\t" \
  "v_perm_b32 v140, v128, v140, v157\n\t" \
  "v_perm_b32 v141, v129, v141, v157\n\t" \
  "v_perm_b32 v142, v130, v142, v157\n\t" \
  "v_perm_b32 v143, v131, v143, v157\n\t" \
  "v_permlane16_swap_b32 v140, v141\n\t" \
  "s_nop 0\n\t" \
  "v_permlane16_swap_b32 v142, v143\n\t" \
  "s_nop 1\n\t" \
  "v_permlane32_swap_b32 v140, v142\n\t" \
  "v_permlane32_swap_b32 v141, v143\n\t" \
  "s_waitcnt vmcnt(4)\n\t" \
  "v_lshlrev_b32 v128, 16, v20\n\t" \
  "v_and_b32 v129, 0xffff0000, v20\n\t" \
  "v_lshlrev_b32 v130, 16, v21\n\t" \
  "v_and_b32 v131, 0xffff0000, v21\n\t" \
  "v_lshlrev_b32 v132, 16, v22\n\t" \
  "v_and_b32 v133, 0xffff0000, v22\n\t" \
  "v_lshlrev_b32 v134, 16, v23\n\t" \
  "v_and_b32 v135, 0xffff0000, v23\n\t" \
  "v_fmac_f32 v112, v128, v64\n\t" \
  "v_fmac_f32 v113, v129, v68\n\t" \
  "v_fmac_f32 v114, v130, v72\n\t" \
  "v_fmac_f32 v115, v131, v76\n\t" \
  "v_fmac_f32 v116, v132, v80\n\t" \
  "v_fmac_f32 v117, v133, v84\n\t" \
  "v_fmac_f32 v118, v134, v88\n\t" \
  "v_fmac_f32 v119, v135, v92\n\t" \
  "v_fmac_f32 v120, v128, v65\n\t" \
  "v_fmac_f32 v121, v129, v69\n\t" \
  "v_fmac_f32 v122, v130, v73\n\t" \
  "v_fmac_f32 v123, v131, v77\n\t" \
  "v_fmac_f32 v124, v132, v81\n\t" \
  "v_fmac_f32 v125, v133, v85\n\t" \
  "v_fmac_f32 v126, v134, v89\n\t" \
  "v_fmac_f32 v127, v135, v93\n\t" \
  "s_waitcnt vmcnt(3)\n\t" \
  "v_lshlrev_b32 v128, 16, v36\n\t" \
  "v_and_b32 v129, 0xffff0000, v36\n\t" \
  "v_lshlrev_b32 v130, 16, v37\n\t" \
  "v_and_b32 v131, 0xffff0000, v37\n\t" \
  "v_lshlrev_b32 v132, 16, v38\n\t" \
  "v_and_b32 v133, 0xffff0000, v38\n\t" \
  "v_lshlrev_b32 v134, 16, v39\n\t" \
  "v_and_b32 v135, 0xffff0000, v39\n\t" \
  "v_mul_f32 v112, v112, v128\n\t" \
  "v_mul_f32 v113, v113, v129\n\t" \
  "v_mul_f32 v114, v114, v130\n\t" \
  "v_mul_f32 v115, v115, v131\n\t" \
  "v_mul_f32 v116, v116, v132\n\t" \
  "v_mul_f32 v117, v117, v133\n\t" \
  "v_mul_f32 v118, v118, v134\n\t" \
  "v_mul_f32 v119, v119, v135\n\t" \
  "v_cvt_pk_bf16_f32 v144, v112, v113\n\t" \
  "v_cvt_pk_bf16_f32 v145, v114, v115\n\t" \
  "v_cvt_pk_bf16_f32 v146, v116, v117\n\t" \
  "v_cvt_pk_bf16_f32 v147, v118, v119\n\t" \
  "v_mov_b32_dpp v128, v144 row_ror:8 row_mask:0xf bank_mask:0xf\n\t" \
  "v_mov_b32_dpp v129, v145 row_ror:8 row_mask:0xf bank_mask:0xf\n\t" \
  "v_mov_b32_dpp v130, v146 row_ror:8 row_mask:0xf bank_mask:0xf\n\t" \
  "v_mov_b32_dpp v131, v147 row_ror:8 row_mask:0xf bank_mask:0xf\n\t" \
  "v_perm_b32 v144, v128, v144, v157\n\t" \
  "v_perm_b32 v145, v129, v145, v157\n\t" \
  "v_perm_b32 v146, v130, v146, v157\n\t" \
  "v_perm_b32 v147, v131, v147, v157\n\t" \
  "v_permlane16_swap_b32 v144, v145\n\t" \
  "s_nop 0\n\t" \
  "v_permlane16_swap_b32 v146, v147\n\t" \
  "s_nop 1\n\t" \
  "v_permlane32_swap_b32 v144, v146\n\t" \
  "v_permlane32_swap_b32 v145, v147\n\t" \
  "s_waitcnt vmcnt(2)\n\t" \
  "v_lshlrev_b32 v128, 16, v24\n\t" \
  "v_and_b32 v129, 0xffff0000, v24\n\t" \
  "v_lshlrev_b32 v130, 16, v25\n\t" \
  "v_and_b32 v131, 0xffff0000, v25\n\t" \
  "v_lshlrev_b32 v132, 16, v26\n\t" \
  "v_and_b32 v133, 0xffff0000, v26\n\t" \
  "v_lshlrev_b32 v134, 16, v27\n\t" \
  "v_and_b32 v135, 0xffff0000, v27\n\t" \
  "v_fmac_f32 v120, v128, v64\n\t" \
  "v_fmac_f32 v121, v129, v68\n\t" \
  "s_mov_b32 s85, 0x480000\n\t" \
  "s_load_dword s84, %[ph], s85\n\t" \
  "s_mov_b32 s85, 0x480800\n\t" \
  "s_load_dword s84, %[ph], s85\n\t" \
  "s_mov_b32 s85, 0x481000\n\t" \
  "s_load_dword s84, %[ph], s85\n\t" \
  "s_mov_b32 s85, 0x4e0000\n\t" \
  "s_load_dword s84, %[ph], s85\n\t" \
  "s_setprio 3\n\t" \
  "s_barrier\n\t" \
  "global_store_dwordx4 v159, v[140:143], %[sb] sc1\n\t" \
  "v_add_u32 v158, 0x80, v159\n\t" \
  "v_bfi_b32 v159, %[mk], v158, v159\n\t" \
  "global_store_dwordx4 v159, v[144:147], %[sb] sc1\n\t" \
  "v_add_u32 v158, 0x80, v159\n\t" \
  "v_bfi_b32 v159, %[mk], v158, v159\n\t" \
  "s_mov_b32 m0, %[hd]\n\t" \
  "s_nop 0\n\t" \
  "global_load_lds_dwordx4 v163, %[hp] offset:0\n\t" \
  "global_load_lds_dwordx4 v163, %[hp] offset:1024\n\t" \
  "global_load_lds_dwordx4 v163, %[hp] offset:2048\n\t" \
  "global_load_lds_dwordx4 v163, %[hp] offset:3072\n\t" \
  "s_setprio 2\n\t" \
  "v_fmac_f32 v122, v130, v72\n\t" \
  "v_fmac_f32 v123, v131, v76\n\t" \
  "v_fmac_f32 v124, v132, v80\n\t" \
  "v_fmac_f32 v125, v133, v84\n\t" \
  "v_fmac_f32 v126, v134, v88\n\t" \
  "v_fmac_f32 v127, v135, v92\n\t" \
  "s_waitcnt vmcnt(7)\n\t" \
  "v_lshlrev_b32 v128, 16, v40\n\t" \
  "v_and_b32 v129, 0xffff0000, v40\n\t" \
  "v_lshlrev_b32 v130, 16, v41\n\t" \
  "v_and_b32 v131, 0xffff0000, v41\n\t" \
  "v_lshlrev_b32 v132, 16, v42\n\t" \
  "v_and_b32 v133, 0xffff0000, v42\n\t" \
  "v_lshlrev_b32 v134, 16, v43\n\t" \
  "v_and_b32 v135, 0xffff0000, v43\n\t" \
  "v_mul_f32 v120, v120, v128\n\t" \
  "v_mul_f32 v121, v121, v129\n\t" \
  "v_mul_f32 v122, v122, v130\n\t" \
  "v_mul_f32 v123, v123, v131\n\t" \
  "v_mul_f32 v124, v124, v132\n\t" \
  "v_mul_f32 v125, v125, v133\n\t" \
  "v_mul_f32 v126, v126, v134\n\t" \
  "v_mul_f32 v127, v127, v135\n\t" \
  "v_cvt_pk_bf16_f32 v148, v120, v121\n\t" \
  "v_cvt_pk_bf16_f32 v149, v122, v123\n\t" \
  "v_cvt_pk_bf16_f32 v150, v124, v125\n\t" \
  "v_cvt_pk_bf16_f32 v151, v126, v127\n\t" \
  "v_mov_b32_dpp v128, v148 row_ror:8 row_mask:0xf bank_mask:0xf\n\t" \
  "v_mov_b32_dpp v129, v149 row_ror:8 row_mask:0xf bank_mask:0xf\n\t" \
  "v_mov_b32_dpp v130, v150 row_ror:8 row_mask:0xf bank_mask:0xf\n\t" \
  "v_mov_b32_dpp v131, v151 row_ror:8 row_mask:0xf bank_mask:0xf\n\t" \
  "v_perm_b32 v148, v128, v148, v157\n\t" \
  "v_perm_b32 v149, v129, v149, v157\n\t" \
  "v_perm_b32 v150, v130, v150, v157\n\t" \
  "v_perm_b32 v151, v131, v151, v157\n\t" \
  "v_permlane16_swap_b32 v148, v149\n\t" \
  "s_nop 0\n\t" \
  "v_permlane16_swap_b32 v150, v151\n\t" \
  "s_nop 1\n\t" \
  "v_permlane32_swap_b32 v148, v150\n\t" \
  "v_permlane32_swap_b32 v149, v151\n\t" \
  "s_mov_b32 s85, 0x4e0800\n\t" \
  "s_load_dword s84, %[ph], s85\n\t" \
  "s_mov_b32 s85, 0x4e1000\n\t" \
  "s_load_dword s84, %[ph], s85\n\t" \
  "s_mov_b32 s85, 0x540000\n\t" \
  "s_load_dword s84, %[ph], s85\n\t" \
  "s_mov_b32 s85, 0x540800\n\t" \
  "s_load_dword s84, %[ph], s85\n\t" \
  "s_setprio 3\n\t" \
  "s_barrier\n\t" \
  "global_store_dwordx4 v159, v[148:151], %[sb] sc1\n\t" \
  "s_setprio 2\n\t" \
  "s_waitcnt vmcnt(1)\n\t" \
  "ds_read_b64_tr_b16 v[64:65], v165 offset:32\n\t" \
  "ds_read_b64_tr_b16 v[66:67], v165 offset:0\n\t" \
  "ds_read_b64_tr_b16 v[68:69], v165 offset:288\n\t" \
  "ds_read_b64_tr_b16 v[70:71], v165 offset:256\n\t" \
  "ds_read_b64_tr_b16 v[72:73], v165 offset:544\n\t" \
  "ds_read_b64_tr_b16 v[74:75], v165 offset:512\n\t" \
  "ds_read_b64_tr_b16 v[76:77], v165 offset:800\n\t" \
  "ds_read_b64_tr_b16 v[78:79], v165 offset:768\n\t" \
  "ds_read_b64_tr_b16 v[80:81], v165 offset:1056\n\t" \
  "ds_read_b64_tr_b16 v[82:83], v165 offset:1024\n\t" \
  "ds_read_b64_tr_b16 v[84:85], v165 offset:1312\n\t" \
  "ds_read_b64_tr_b16 v[86:87], v165 offset:1280\n\t" \
  "ds_read_b64_tr_b16 v[88:89], v165 offset:1568\n\t" \
  "ds_read_b64_tr_b16 v[90:91], v165 offset:1536\n\t" \
  "ds_read_b64_tr_b16 v[92:93], v165 offset:1824\n\t" \
  "ds_read_b64_tr_b16 v[94:95], v165 offset:1792\n\t" \
  "v_mov_b32 v24, v166\n\t" \
  "v_mov_b32 v25, v167\n\t" \
  "v_mov_b32 v63, 0xc0\n\t" \
  "v_add_u32 v158, 64, v24\n\t" \
  "v_bfi_b32 v26, v63, v158, v24\n\t" \
  "v_add_u32 v158, 64, v25\n\t" \
  "v_bfi_b32 v27, v63, v158, v25\n\t" \
  "v_add_u32 v158, 64, v26\n\t" \
  "v_bfi_b32 v28, v63, v158, v26\n\t" \
  "v_add_u32 v158, 64, v27\n\t" \
  "v_bfi_b32 v29, v63, v158, v27\n\t" \
  "v_add_u32 v158, 64, v28\n\t" \
  "v_bfi_b32 v30, v63, v158, v28\n\t" \
  "v_add_u32 v158, 64, v29\n\t" \
  "v_bfi_b32 v31, v63, v158, v29\n\t" \
  "s_mov_b32 s85, 0x541000\n\t" \
  "s_load_dword s84, %[ph], s85\n\t" \
  "s_mov_b32 s85, 0x5a0000\n\t" \
  "s_load_dword s84, %[ph], s85\n\t" \
  "s_mov_b32 s85, 0x5a0800\n\t" \
  "s_load_dword s84, %[ph], s85\n\t" \
  "s_mov_b32 s85, 0x5a1000\n\t" \
  "s_load_dword s84, %[ph], s85\n\t" \
  "s_setprio 3\n\t" \
  "s_barrier\n\t" \
  "s_waitcnt vmcnt(0)\n\t" \
  "v_add_u32 v61, 1, v160\n\t" \
  "s_mov_b64 exec, 1\n\t" \
  "global_store_dword v60, v61, %[ff] sc1\n\t" \
  "s_mov_b64 exec, -1\n\t" \
  "s_setprio 2\n\t" \
  "ds_read_b64_tr_b16 v[96:97], v165 offset:2080\n\t" \
  "ds_read_b64_tr_b16 v[98:99], v165 offset:2048\n\t" \
  "ds_read_b64_tr_b16 v[100:101], v165 offset:2336\n\t" \
  "ds_read_b64_tr_b16 v[102:103], v165 offset:2304\n\t" \
  "ds_read_b64_tr_b16 v[104:105], v165 offset:2592\n\t" \
  "ds_read_b64_tr_b16 v[106:107], v165 offset:2560\n\t" \
  "ds_read_b64_tr_b16 v[108:109], v165 offset:2848\n\t" \
  "ds_read_b64_tr_b16 v[110:111], v165 offset:2816\n\t" \
  "ds_read_b64_tr_b16 v[112:113], v165 offset:3104\n\t" \
  "ds_read_b64_tr_b16 v[114:115], v165 offset:3072\n\t" \
  "ds_read_b64_tr_b16 v[116:117], v165 offset:3360\n\t" \
  "ds_read_b64_tr_b16 v[118:119], v165 offset:3328\n\t" \
  "ds_read_b64_tr_b16 v[120:121], v165 offset:3616\n\t" \
  "ds_read_b64_tr_b16 v[122:123], v165 offset:3584\n\t" \
  "ds_read_b64_tr_b16 v[124:125], v165 offset:3872\n\t" \
  "ds_read_b64_tr_b16 v[126:127], v165 offset:3840\n\t" \
  "s_barrier\n\t" \
  "s_waitcnt lgkmcnt(0)\n\t" \
  "v_lshlrev_b32 v61, 16, v161\n\t" \
  "v_lshlrev_b32 v62, 16, v162\n\t" \
  "v_add_f32 v61, v61, v62\n\t" \
  "v_mov_b32 v62, 0xffff\n\t" \
  "v_cvt_pk_bf16_f32 v61, v61, v61\n\t" \
  "s_mov_b64 exec, 1\n\t" \
  "v_bfi_b32 v66, v62, v61, v66\n\t" \
  "s_mov_b64 exec, 15\n\t" \
  "v_and_b32 v66, v62, v66\n\t" \
  "s_mov_b64 exec, 16\n\t" \
  "v_bfi_b32 v66, v62, v66, v61\n\t" \
  "s_mov_b64 exec, 0xff\n\t" \
  "v_mov_b32 v67, 0\n\t" \
  "s_mov_b64 exec, 0x100\n\t" \
  "v_mov_b32 v67, v61\n\t" \
  "s_mov_b64 exec, 0xf00\n\t" \
  "v_and_b32 v67, v62, v67\n\t" \
  "s_mov_b64 exec, 0x1000\n\t" \
  "v_bfi_b32 v67, v62, v67, v61\n\t" \
  "s_mov_b64 exec, -1\n\t" \
  "s_waitcnt lgkmcnt(0)\n\t"
#define PP_NB 8
#define PP_Q 0
#define PP_CLOB "v0", "v1", "v2", "v3", "v4", "v5", "v6", "v7", "v8", "v9", "v10", "v11", "v12", "v13", "v14", "v15", "v16", "v17", "v18", "v19", "v20", "v21", "v22", "v23", "v32", "v33", "v34", "v35", "v36", "v37", "v38", "v39", "v40", "v41", "v42", "v43", "v60", "v61", "v62", "v63", "v128", "v129", "v130", "v131", "v132", "v133", "v134", "v135", "v136", "v137", "v138", "v139", "v140", "v141", "v142", "v143", "v144", "v145", "v146", "v147", "v148", "v149", "v150", "v151", "v158", "v161", "v162", "s84", "s85"

#define LD_ASM1 \
  "s_add_u32 m0, s39, 49152\n\t" \
  "s_mov_b32 s68, 0x0\n\t" \
  "buffer_load_dwordx4 v5, s[44:47], s68 offen offset:0 lds\n\t" \
  "buffer_load_dwordx4 v6, s[44:47], s68 offen offset:1024 lds\n\t" \
  "buffer_load_dwordx4 v7, s[44:47], s68 offen offset:2048 lds\n\t" \
  "s_add_u32 m0, s38, 36864\n\t" \
  "s_mov_b32 s68, 0x0\n\t" \
  "buffer_load_dwordx4 v0, s[40:43], s68 offen offset:0 lds\n\t" \
  "buffer_load_dwordx4 v1, s[40:43], s68 offen offset:1024 lds\n\t" \
  "s_mov_b64 exec, 0xffff\n\t" \
  "buffer_load_dwordx4 v4, s[40:43], s68 offen offset:2048 lds\n\t" \
  "s_mov_b64 exec, -1\n\t" \
  "s_add_u32 m0, s38, 46080\n\t" \
  "s_mov_b32 s68, 0x24000\n\t" \
  "buffer_load_dwordx4 v2, s[40:43], s68 offen offset:0 lds\n\t" \
  "buffer_load_dwordx4 v3, s[40:43], s68 offen offset:1024 lds\n\t" \
  "s_mov_b64 exec, 0xffff\n\t" \
  "buffer_load_dwordx4 v4, s[40:43], s68 offen offset:2048 lds\n\t" \
  "s_mov_b64 exec, -1\n\t" \
  "s_add_u32 m0, s39, 61440\n\t" \
  "s_mov_b32 s68, 0x30000\n\t" \
  "buffer_load_dwordx4 v5, s[44:47], s68 offen offset:0 lds\n\t" \
  "buffer_load_dwordx4 v6, s[44:47], s68 offen offset:1024 lds\n\t" \
  "buffer_load_dwordx4 v7, s[44:47], s68 offen offset:2048 lds\n\t" \
  "s_mov_b32 m0, %[sk]\n\t" \
  "s_mov_b32 exec_hi, 0\n\t" \
  "buffer_load_dword v9, s[40:43], 0 offen lds\n\t" \
  "s_mov_b32 exec_lo, %[wm]\n\t" \
  "buffer_load_dword v10, s[44:47], 0 offen lds\n\t" \
  "s_mov_b64 exec, -1\n\t"
#define LD_ASM2 \
  "s_add_u32 m0, s38, 0\n\t" \
  "s_mov_b32 s68, 0x80\n\t" \
  "buffer_load_dwordx4 v0, s[40:43], s68 offen offset:0 lds\n\t" \
  "s_mov_b32 s85, 0x180000\n\t" \
  "s_load_dword s84, %[pw], s85\n\t" \
  "buffer_load_dwordx4 v1, s[40:43], s68 offen offset:1024 lds\n\t" \
  "s_mov_b32 s85, 0x180800\n\t" \
  "s_load_dword s84, %[pw], s85\n\t" \
  "s_mov_b64 exec, 0xffff\n\t" \
  "buffer_load_dwordx4 v4, s[40:43], s68 offen offset:2048 lds\n\t" \
  "s_mov_b64 exec, -1\n\t" \
  "s_add_u32 m0, s38, 9216\n\t" \
  "s_mov_b32 s68, 0x24080\n\t" \
  "buffer_load_dwordx4 v2, s[40:43], s68 offen offset:0 lds\n\t" \
  "s_mov_b32 s85, 0x181000\n\t" \
  "s_load_dword s84, %[pw], s85\n\t" \
  "buffer_load_dwordx4 v3, s[40:43], s68 offen offset:1024 lds\n\t" \
  "s_mov_b32 s85, 0x1b0000\n\t" \
  "s_load_dword s84, %[pw], s85\n\t" \
  "s_mov_b64 exec, 0xffff\n\t" \
  "buffer_load_dwordx4 v4, s[40:43], s68 offen offset:2048 lds\n\t" \
  "s_mov_b64 exec, -1\n\t" \
  "s_mov_b32 s85, 0x1b0800\n\t" \
  "s_load_dword s84, %[pw], s85\n\t" \
  "s_mov_b32 s85, 0x1b1000\n\t" \
  "s_load_dword s84, %[pw], s85\n\t" \
  "s_mov_b32 s85, 0x1e0000\n\t" \
  "s_load_dword s84, %[pw], s85\n\t" \
  "s_mov_b32 s85, 0x1e0800\n\t" \
  "s_load_dword s84, %[pw], s85\n\t" \
  "s_mov_b32 m0, %[tsk]\n\t" \
  "s_mov_b32 s85, 0x0\n\t" \
  "s_mov_b64 exec, %[hm]\n\t" \
  "buffer_load_dword v16, %[sh], s85 offen lds\n\t" \
  "s_mov_b64 exec, -1\n\t" \
  "s_mov_b32 s68, 0x60000\n\t" \
  "s_mov_b32 s69, 0x90000\n\t" \
  "s_mov_b32 s70, 0x100\n\t" \
  "s_mov_b32 s71, 0x24100\n\t" \
  "s_mov_b32 s72, 0xc0000\n\t" \
  "s_mov_b32 s73, 0xf0000\n\t" \
  "s_add_u32 m0, s39, 0\n\t" \
  "s_waitcnt vmcnt(9)\n\t" \
  "s_barrier\n\t" \
  "buffer_load_dwordx4 v5, s[44:47], s68 offen offset:0 lds\n\t" \
  "s_mov_b32 s85, 0x1e1000\n\t" \
  "s_load_dword s84, %[pw], s85\n\t" \
  "buffer_load_dwordx4 v6, s[44:47], s68 offen offset:1024 lds\n\t" \
  "s_mov_b32 s85, 0x210000\n\t" \
  "s_load_dword s84, %[pw], s85\n\t" \
  "buffer_load_dwordx4 v7, s[44:47], s68 offen offset:2048 lds\n\t" \
  "s_mov_b32 s85, 0x210800\n\t" \
  "s_load_dword s84, %[pw], s85\n\t" \
  "s_add_u32 m0, s39, 12288\n\t" \
  "s_nop 0\n\t" \
  "buffer_load_dwordx4 v5, s[44:47], s69 offen offset:0 lds\n\t" \
  "s_mov_b32 s85, 0x211000\n\t" \
  "s_load_dword s84, %[pw], s85\n\t" \
  "buffer_load_dwordx4 v6, s[44:47], s69 offen offset:1024 lds\n\t" \
  "s_mov_b32 s85, 0x240000\n\t" \
  "s_load_dword s84, %[pw], s85\n\t" \
  "buffer_load_dwordx4 v7, s[44:47], s69 offen offset:2048 lds\n\t" \
  "s_mov_b32 s85, 0x240800\n\t" \
  "s_load_dword s84, %[pw], s85\n\t" \
  "s_add_u32 m0, s38, 18432\n\t" \
  "s_nop 0\n\t" \
  "buffer_load_dwordx4 v0, s[40:43], s70 offen offset:0 lds\n\t" \
  "buffer_load_dwordx4 v1, s[40:43], s70 offen offset:1024 lds\n\t" \
  "s_mov_b64 exec, 0xffff\n\t" \
  "buffer_load_dwordx4 v4, s[40:43], s70 offen offset:2048 lds\n\t" \
  "s_add_u32 m0, s38, 27648\n\t" \
  "s_mov_b64 exec, -1\n\t" \
  "buffer_load_dwordx4 v2, s[40:43], s71 offen offset:0 lds\n\t" \
  "buffer_load_dwordx4 v3, s[40:43], s71 offen offset:1024 lds\n\t" \
  "s_mov_b64 exec, 0xffff\n\t" \
  "buffer_load_dwordx4 v4, s[40:43], s71 offen offset:2048 lds\n\t" \
  "s_add_u32 m0, s39, 24576\n\t" \
  "s_mov_b64 exec, -1\n\t" \
  "buffer_load_dwordx4 v5, s[44:47], s72 offen offset:0 lds\n\t" \
  "buffer_load_dwordx4 v6, s[44:47], s72 offen offset:1024 lds\n\t" \
  "buffer_load_dwordx4 v7, s[44:47], s72 offen offset:2048 lds\n\t" \
  "s_add_u32 m0, s39, 36864\n\t" \
  "s_nop 0\n\t" \
  "buffer_load_dwordx4 v5, s[44:47], s73 offen offset:0 lds\n\t" \
  "buffer_load_dwordx4 v6, s[44:47], s73 offen offset:1024 lds\n\t" \
  "buffer_load_dwordx4 v7, s[44:47], s73 offen offset:2048 lds\n\t" \
  "s_mov_b32 m0, %[tsk]\n\t" \
  "s_mov_b32 s85, 0x180000\n\t" \
  "s_mov_b64 exec, %[hm]\n\t" \
  "buffer_load_dword v16, %[sh], s85 offen lds\n\t" \
  "s_mov_b64 exec, -1\n\t"
#define LD_ASM3 \
  "s_mov_b32 s68, 0x180\n\t" \
  "s_mov_b32 s69, 0x120000\n\t" \
  "s_mov_b32 s70, 0x24180\n\t" \
  "s_mov_b32 s71, 0x150000\n\t" \
  "s_add_u32 m0, s38, 36864\n\t" \
  "s_waitcnt vmcnt(13)\n\t" \
  "s_barrier\n\t" \
  "buffer_load_dwordx4 v0, s[40:43], s68 offen offset:0 lds\n\t" \
  "s_mov_b32 s85, 0x241000\n\t" \
  "s_load_dword s84, %[pw], s85\n\t" \
  "buffer_load_dwordx4 v1, s[40:43], s68 offen offset:1024 lds\n\t" \
  "s_mov_b32 s85, 0x270000\n\t" \
  "s_load_dword s84, %[pw], s85\n\t" \
  "s_mov_b64 exec, 0xffff\n\t" \
  "buffer_load_dwordx4 v4, s[40:43], s68 offen offset:2048 lds\n\t" \
  "s_add_u32 m0, s39, 49152\n\t" \
  "s_mov_b64 exec, -1\n\t" \
  "buffer_load_dwordx4 v5, s[44:47], s69 offen offset:0 lds\n\t" \
  "s_mov_b32 s85, 0x270800\n\t" \
  "s_load_dword s84, %[pw], s85\n\t" \
  "buffer_load_dwordx4 v6, s[44:47], s69 offen offset:1024 lds\n\t" \
  "s_mov_b32 s85, 0x271000\n\t" \
  "s_load_dword s84, %[pw], s85\n\t" \
  "buffer_load_dwordx4 v7, s[44:47], s69 offen offset:2048 lds\n\t" \
  "s_mov_b32 s85, 0x2a0000\n\t" \
  "s_load_dword s84, %[pw], s85\n\t" \
  "s_add_u32 m0, s38, 46080\n\t" \
  "s_nop 0\n\t" \
  "buffer_load_dwordx4 v2, s[40:43], s70 offen offset:0 lds\n\t" \
  "s_mov_b32 s85, 0x2a0800\n\t" \
  "s_load_dword s84, %[pw], s85\n\t" \
  "buffer_load_dwordx4 v3, s[40:43], s70 offen offset:1024 lds\n\t" \
  "s_mov_b64 exec, 0xffff\n\t" \
  "buffer_load_dwordx4 v4, s[40:43], s70 offen offset:2048 lds\n\t" \
  "s_add_u32 m0, s39, 61440\n\t" \
  "s_mov_b64 exec, -1\n\t" \
  "buffer_load_dwordx4 v5, s[44:47], s71 offen offset:0 lds\n\t" \
  "buffer_load_dwordx4 v6, s[44:47], s71 offen offset:1024 lds\n\t" \
  "buffer_load_dwordx4 v7, s[44:47], s71 offen offset:2048 lds\n\t" \
  "s_mov_b32 s68, 0x200\n\t" \
  "s_mov_b32 s69, 0x180000\n\t" \
  "s_mov_b32 s70, 0x24200\n\t" \
  "s_mov_b32 s71, 0x1b0000\n\t" \
  "s_add_u32 m0, s38, 0\n\t" \
  "s_waitcnt vmcnt(13)\n\t" \
  "s_barrier\n\t" \
  "buffer_load_dwordx4 v0, s[40:43], s68 offen offset:0 lds\n\t" \
  "s_mov_b32 s85, 0x2a1000\n\t" \
  "s_load_dword s84, %[pw], s85\n\t" \
  "buffer_load_dwordx4 v1, s[40:43], s68 offen offset:1024 lds\n\t" \
  "s_mov_b32 s85, 0x2d0000\n\t" \
  "s_load_dword s84, %[pw], s85\n\t" \
  "s_mov_b64 exec, 0xffff\n\t" \
  "buffer_load_dwordx4 v4, s[40:43], s68 offen offset:2048 lds\n\t" \
  "s_add_u32 m0, s39, 0\n\t" \
  "s_mov_b64 exec, -1\n\t" \
  "buffer_load_dwordx4 v5, s[44:47], s69 offen offset:0 lds\n\t" \
  "s_mov_b32 s85, 0x2d0800\n\t" \
  "s_load_dword s84, %[pw], s85\n\t" \
  "buffer_load_dwordx4 v6, s[44:47], s69 offen offset:1024 lds\n\t" \
  "s_mov_b32 s85, 0x2d1000\n\t" \
  "s_load_dword s84, %[pw], s85\n\t" \
  "buffer_load_dwordx4 v7, s[44:47], s69 offen offset:2048 lds\n\t" \
  "s_mov_b32 s85, 0x300000\n\t" \
  "s_load_dword s84, %[pw], s85\n\t" \
  "s_add_u32 m0, s38, 9216\n\t" \
  "s_nop 0\n\t" \
  "buffer_load_dwordx4 v2, s[40:43], s70 offen offset:0 lds\n\t" \
  "s_mov_b32 s85, 0x300800\n\t" \
  "s_load_dword s84, %[pw], s85\n\t" \
  "buffer_load_dwordx4 v3, s[40:43], s70 offen offset:1024 lds\n\t" \
  "s_mov_b64 exec, 0xffff\n\t" \
  "buffer_load_dwordx4 v4, s[40:43], s70 offen offset:2048 lds\n\t" \
  "s_add_u32 m0, s39, 12288\n\t" \
  "s_mov_b64 exec, -1\n\t" \
  "buffer_load_dwordx4 v5, s[44:47], s71 offen offset:0 lds\n\t" \
  "buffer_load_dwordx4 v6, s[44:47], s71 offen offset:1024 lds\n\t" \
  "buffer_load_dwordx4 v7, s[44:47], s71 offen offset:2048 lds\n\t" \
  "s_mov_b32 s68, 0x280\n\t" \
  "s_mov_b32 s69, 0x1e0000\n\t" \
  "s_mov_b32 s70, 0x24280\n\t" \
  "s_mov_b32 s71, 0x210000\n\t" \
  "s_add_u32 m0, s38, 18432\n\t" \
  "s_waitcnt vmcnt(12)\n\t" \
  "s_barrier\n\t" \
  "buffer_load_dwordx4 v0, s[40:43], s68 offen offset:0 lds\n\t" \
  "s_mov_b32 s85, 0x301000\n\t" \
  "s_load_dword s84, %[pw], s85\n\t" \
  "buffer_load_dwordx4 v1, s[40:43], s68 offen offset:1024 lds\n\t" \
  "s_mov_b32 s85, 0x330000\n\t" \
  "s_load_dword s84, %[pw], s85\n\t" \
  "s_mov_b64 exec, 0xffff\n\t" \
  "buffer_load_dwordx4 v4, s[40:43], s68 offen offset:2048 lds\n\t" \
  "s_add_u32 m0, s39, 24576\n\t" \
  "s_mov_b64 exec, -1\n\t" \
  "buffer_load_dwordx4 v5, s[44:47], s69 offen offset:0 lds\n\t" \
  "s_mov_b32 s85, 0x330800\n\t" \
  "s_load_dword s84, %[pw], s85\n\t" \
  "buffer_load_dwordx4 v6, s[44:47], s69 offen offset:1024 lds\n\t" \
  "s_mov_b32 s85, 0x331000\n\t" \
  "s_load_dword s84, %[pw], s85\n\t" \
  "buffer_load_dwordx4 v7, s[44:47], s69 offen offset:2048 lds\n\t" \
  "s_mov_b32 s85, 0x360000\n\t" \
  "s_load_dword s84, %[pw], s85\n\t" \
  "s_add_u32 m0, s38, 27648\n\t" \
  "s_nop 0\n\t" \
  "buffer_load_dwordx4 v2, s[40:43], s70 offen offset:0 lds\n\t" \
  "s_mov_b32 s85, 0x360800\n\t" \
  "s_load_dword s84, %[pw], s85\n\t" \
  "buffer_load_dwordx4 v3, s[40:43], s70 offen offset:1024 lds\n\t" \
  "s_mov_b64 exec, 0xffff\n\t" \
  "buffer_load_dwordx4 v4, s[40:43], s70 offen offset:2048 lds\n\t" \
  "s_add_u32 m0, s39, 36864\n\t" \
  "s_mov_b64 exec, -1\n\t" \
  "buffer_load_dwordx4 v5, s[44:47], s71 offen offset:0 lds\n\t" \
  "buffer_load_dwordx4 v6, s[44:47], s71 offen offset:1024 lds\n\t" \
  "buffer_load_dwordx4 v7, s[44:47], s71 offen offset:2048 lds\n\t" \
  "s_mov_b32 s68, 0x300\n\t" \
  "s_mov_b32 s69, 0x240000\n\t" \
  "s_mov_b32 s70, 0x24300\n\t" \
  "s_mov_b32 s71, 0x270000\n\t" \
  "s_add_u32 m0, s38, 36864\n\t" \
  "s_waitcnt vmcnt(12)\n\t" \
  "s_barrier\n\t" \
  "buffer_load_dwordx4 v0, s[40:43], s68 offen offset:0 lds\n\t" \
  "s_mov_b32 s85, 0x361000\n\t" \
  "s_load_dword s84, %[pw], s85\n\t" \
  "buffer_load_dwordx4 v1, s[40:43], s68 offen offset:1024 lds\n\t" \
  "s_mov_b32 s85, 0x390000\n\t" \
  "s_load_dword s84, %[pw], s85\n\t" \
  "s_mov_b64 exec, 0xffff\n\t" \
  "buffer_load_dwordx4 v4, s[40:43], s68 offen offset:2048 lds\n\t" \
  "s_add_u32 m0, s39, 49152\n\t" \
  "s_mov_b64 exec, -1\n\t" \
  "buffer_load_dwordx4 v5, s[44:47], s69 offen offset:0 lds\n\t" \
  "s_mov_b32 s85, 0x390800\n\t" \
  "s_load_dword s84, %[pw], s85\n\t" \
  "buffer_load_dwordx4 v6, s[44:47], s69 offen offset:1024 lds\n\t" \
  "s_mov_b32 s85, 0x391000\n\t" \
  "s_load_dword s84, %[pw], s85\n\t" \
  "buffer_load_dwordx4 v7, s[44:47], s69 offen offset:2048 lds\n\t" \
  "s_mov_b32 s85, 0x3c0000\n\t" \
  "s_load_dword s84, %[pw], s85\n\t" \
  "s_add_u32 m0, s38, 46080\n\t" \
  "s_nop 0\n\t" \
  "buffer_load_dwordx4 v2, s[40:43], s70 offen offset:0 lds\n\t" \
  "s_mov_b32 s85, 0x3c0800\n\t" \
  "s_load_dword s84, %[pw], s85\n\t" \
  "buffer_load_dwordx4 v3, s[40:43], s70 offen offset:1024 lds\n\t" \
  "s_mov_b64 exec, 0xffff\n\t" \
  "buffer_load_dwordx4 v4, s[40:43], s70 offen offset:2048 lds\n\t" \
  "s_add_u32 m0, s39, 61440\n\t" \
  "s_mov_b64 exec, -1\n\t" \
  "buffer_load_dwordx4 v5, s[44:47], s71 offen offset:0 lds\n\t" \
  "buffer_load_dwordx4 v6, s[44:47], s71 offen offset:1024 lds\n\t" \
  "buffer_load_dwordx4 v7, s[44:47], s71 offen offset:2048 lds\n\t" \
  "s_mov_b32 s68, 0x380\n\t" \
  "s_mov_b32 s69, 0x2a0000\n\t" \
  "s_mov_b32 s70, 0x24380\n\t" \
  "s_mov_b32 s71, 0x2d0000\n\t" \
  "s_add_u32 m0, s38, 0\n\t" \
  "s_waitcnt vmcnt(12)\n\t" \
  "s_barrier\n\t" \
  "buffer_load_dwordx4 v0, s[40:43], s68 offen offset:0 lds\n\t" \
  "s_mov_b32 s85, 0x3c1000\n\t" \
  "s_load_dword s84, %[pw], s85\n\t" \
  "buffer_load_dwordx4 v1, s[40:43], s68 offen offset:1024 lds\n\t" \
  "s_mov_b32 s85, 0x3f0000\n\t" \
  "s_load_dword s84, %[pw], s85\n\t" \
  "s_mov_b64 exec, 0xffff\n\t" \
  "buffer_load_dwordx4 v4, s[40:43], s68 offen offset:2048 lds\n\t" \
  "s_add_u32 m0, s39, 0\n\t" \
  "s_mov_b64 exec, -1\n\t" \
  "buffer_load_dwordx4 v5, s[44:47], s69 offen offset:0 lds\n\t" \
  "s_mov_b32 s85, 0x3f0800\n\t" \
  "s_load_dword s84, %[pw], s85\n\t" \
  "buffer_load_dwordx4 v6, s[44:47], s69 offen offset:1024 lds\n\t" \
  "s_mov_b32 s85, 0x3f1000\n\t" \
  "s_load_dword s84, %[pw], s85\n\t" \
  "buffer_load_dwordx4 v7, s[44:47], s69 offen offset:2048 lds\n\t" \
  "s_mov_b32 s85, 0x420000\n\t" \
  "s_load_dword s84, %[pw], s85\n\t" \
  "s_add_u32 m0, s38, 9216\n\t" \
  "s_nop 0\n\t" \
  "buffer_load_dwordx4 v2, s[40:43], s70 offen offset:0 lds\n\t" \
  "s_mov_b32 s85, 0x420800\n\t" \
  "s_load_dword s84, %[pw], s85\n\t" \
  "buffer_load_dwordx4 v3, s[40:43], s70 offen offset:1024 lds\n\t" \
  "s_mov_b64 exec, 0xffff\n\t" \
  "buffer_load_dwordx4 v4, s[40:43], s70 offen offset:2048 lds\n\t" \
  "s_add_u32 m0, s39, 12288\n\t" \
  "s_mov_b64 exec, -1\n\t" \
  "buffer_load_dwordx4 v5, s[44:47], s71 offen offset:0 lds\n\t" \
  "buffer_load_dwordx4 v6, s[44:47], s71 offen offset:1024 lds\n\t" \
  "buffer_load_dwordx4 v7, s[44:47], s71 offen offset:2048 lds\n\t" \
  "s_mov_b32 s68, 0x400\n\t" \
  "s_mov_b32 s69, 0x300000\n\t" \
  "s_mov_b32 s70, 0x24400\n\t" \
  "s_mov_b32 s71, 0x330000\n\t" \
  "s_add_u32 m0, s38, 18432\n\t" \
  "s_waitcnt vmcnt(12)\n\t" \
  "s_barrier\n\t" \
  "buffer_load_dwordx4 v0, s[40:43], s68 offen offset:0 lds\n\t" \
  "s_mov_b32 s85, 0x421000\n\t" \
  "s_load_dword s84, %[pw], s85\n\t" \
  "buffer_load_dwordx4 v1, s[40:43], s68 offen offset:1024 lds\n\t" \
  "s_mov_b32 s85, 0x450000\n\t" \
  "s_load_dword s84, %[pw], s85\n\t" \
  "s_mov_b64 exec, 0xffff\n\t" \
  "buffer_load_dwordx4 v4, s[40:43], s68 offen offset:2048 lds\n\t" \
  "s_add_u32 m0, s39, 24576\n\t" \
  "s_mov_b64 exec, -1\n\t" \
  "buffer_load_dwordx4 v5, s[44:47], s69 offen offset:0 lds\n\t" \
  "s_mov_b32 s85, 0x450800\n\t" \
  "s_load_dword s84, %[pw], s85\n\t" \
  "buffer_load_dwordx4 v6, s[44:47], s69 offen offset:1024 lds\n\t" \
  "s_mov_b32 s85, 0x451000\n\t" \
  "s_load_dword s84, %[pw], s85\n\t" \
  "buffer_load_dwordx4 v7, s[44:47], s69 offen offset:2048 lds\n\t" \
  "s_mov_b32 s85, 0x480000\n\t" \
  "s_load_dword s84, %[pw], s85\n\t" \
  "s_add_u32 m0, s38, 27648\n\t" \
  "s_nop 0\n\t" \
  "buffer_load_dwordx4 v2, s[40:43], s70 offen offset:0 lds\n\t" \
  "s_mov_b32 s85, 0x480800\n\t" \
  "s_load_dword s84, %[pw], s85\n\t" \
  "buffer_load_dwordx4 v3, s[40:43], s70 offen offset:1024 lds\n\t" \
  "s_mov_b64 exec, 0xffff\n\t" \
  "buffer_load_dwordx4 v4, s[40:43], s70 offen offset:2048 lds\n\t" \
  "s_add_u32 m0, s39, 36864\n\t" \
  "s_mov_b64 exec, -1\n\t" \
  "buffer_load_dwordx4 v5, s[44:47], s71 offen offset:0 lds\n\t" \
  "buffer_load_dwordx4 v6, s[44:47], s71 offen offset:1024 lds\n\t" \
  "buffer_load_dwordx4 v7, s[44:47], s71 offen offset:2048 lds\n\t" \
  "s_mov_b32 s68, 0x480\n\t" \
  "s_mov_b32 s69, 0x360000\n\t" \
  "s_mov_b32 s70, 0x24480\n\t" \
  "s_mov_b32 s71, 0x390000\n\t" \
  "s_add_u32 m0, s38, 36864\n\t" \
  "s_waitcnt vmcnt(12)\n\t" \
  "s_barrier\n\t" \
  "buffer_load_dwordx4 v0, s[40:43], s68 offen offset:0 lds\n\t" \
  "s_mov_b32 s85, 0x481000\n\t" \
  "s_load_dword s84, %[pw], s85\n\t" \
  "buffer_load_dwordx4 v1, s[40:43], s68 offen offset:1024 lds\n\t" \
  "s_mov_b32 s85, 0x4b0000\n\t" \
  "s_load_dword s84, %[pw], s85\n\t" \
  "s_mov_b64 exec, 0xffff\n\t" \
  "buffer_load_dwordx4 v4, s[40:43], s68 offen offset:2048 lds\n\t" \
  "s_add_u32 m0, s39, 49152\n\t" \
  "s_mov_b64 exec, -1\n\t" \
  "buffer_load_dwordx4 v5, s[44:47], s69 offen offset:0 lds\n\t" \
  "s_mov_b32 s85, 0x4b0800\n\t" \
  "s_load_dword s84, %[pw], s85\n\t" \
  "buffer_load_dwordx4 v6, s[44:47], s69 offen offset:1024 lds\n\t" \
  "s_mov_b32 s85, 0x4b1000\n\t" \
  "s_load_dword s84, %[pw], s85\n\t" \
  "buffer_load_dwordx4 v7, s[44:47], s69 offen offset:2048 lds\n\t" \
  "s_mov_b32 s85, 0x4e0000\n\t" \
  "s_load_dword s84, %[pw], s85\n\t" \
  "s_add_u32 m0, s38, 46080\n\t" \
  "s_nop 0\n\t" \
  "buffer_load_dwordx4 v2, s[40:43], s70 offen offset:0 lds\n\t" \
  "s_mov_b32 s85, 0x4e0800\n\t" \
  "s_load_dword s84, %[pw], s85\n\t" \
  "buffer_load_dwordx4 v3, s[40:43], s70 offen offset:1024 lds\n\t" \
  "s_mov_b64 exec, 0xffff\n\t" \
  "buffer_load_dwordx4 v4, s[40:43], s70 offen offset:2048 lds\n\t" \
  "s_add_u32 m0, s39, 61440\n\t" \
  "s_mov_b64 exec, -1\n\t" \
  "buffer_load_dwordx4 v5, s[44:47], s71 offen offset:0 lds\n\t" \
  "buffer_load_dwordx4 v6, s[44:47], s71 offen offset:1024 lds\n\t" \
  "buffer_load_dwordx4 v7, s[44:47], s71 offen offset:2048 lds\n\t" \
  "s_mov_b32 s68, 0x500\n\t" \
  "s_mov_b32 s69, 0x3c0000\n\t" \
  "s_mov_b32 s70, 0x24500\n\t" \
  "s_mov_b32 s71, 0x3f0000\n\t" \
  "s_add_u32 m0, s38, 0\n\t" \
  "s_waitcnt vmcnt(12)\n\t" \
  "s_barrier\n\t" \
  "buffer_load_dwordx4 v0, s[40:43], s68 offen offset:0 lds\n\t" \
  "s_mov_b32 s85, 0x4e1000\n\t" \
  "s_load_dword s84, %[pw], s85\n\t" \
  "buffer_load_dwordx4 v1, s[40:43], s68 offen offset:1024 lds\n\t" \
  "s_mov_b32 s85, 0x510000\n\t" \
  "s_load_dword s84, %[pw], s85\n\t" \
  "s_mov_b64 exec, 0xffff\n\t" \
  "buffer_load_dwordx4 v4, s[40:43], s68 offen offset:2048 lds\n\t" \
  "s_add_u32 m0, s39, 0\n\t" \
  "s_mov_b64 exec, -1\n\t" \
  "buffer_load_dwordx4 v5, s[44:47], s69 offen offset:0 lds\n\t" \
  "s_mov_b32 s85, 0x510800\n\t" \
  "s_load_dword s84, %[pw], s85\n\t" \
  "buffer_load_dwordx4 v6, s[44:47], s69 offen offset:1024 lds\n\t" \
  "s_mov_b32 s85, 0x511000\n\t" \
  "s_load_dword s84, %[pw], s85\n\t" \
  "buffer_load_dwordx4 v7, s[44:47], s69 offen offset:2048 lds\n\t" \
  "s_mov_b32 s85, 0x540000\n\t" \
  "s_load_dword s84, %[pw], s85\n\t" \
  "s_add_u32 m0, s38, 9216\n\t" \
  "s_nop 0\n\t" \
  "buffer_load_dwordx4 v2, s[40:43], s70 offen offset:0 lds\n\t" \
  "s_mov_b32 s85, 0x540800\n\t" \
  "s_load_dword s84, %[pw], s85\n\t" \
  "buffer_load_dwordx4 v3, s[40:43], s70 offen offset:1024 lds\n\t" \
  "s_mov_b64 exec, 0xffff\n\t" \
  "buffer_load_dwordx4 v4, s[40:43], s70 offen offset:2048 lds\n\t" \
  "s_add_u32 m0, s39, 12288\n\t" \
  "s_mov_b64 exec, -1\n\t" \
  "buffer_load_dwordx4 v5, s[44:47], s71 offen offset:0 lds\n\t" \
  "buffer_load_dwordx4 v6, s[44:47], s71 offen offset:1024 lds\n\t" \
  "buffer_load_dwordx4 v7, s[44:47], s71 offen offset:2048 lds\n\t" \
  "s_mov_b32 s68, 0x580\n\t" \
  "s_mov_b32 s69, 0x420000\n\t" \
  "s_mov_b32 s70, 0x24580\n\t" \
  "s_mov_b32 s71, 0x450000\n\t" \
  "s_add_u32 m0, s38, 18432\n\t" \
  "s_waitcnt vmcnt(12)\n\t" \
  "s_barrier\n\t" \
  "buffer_load_dwordx4 v0, s[40:43], s68 offen offset:0 lds\n\t" \
  "s_load_dword s84, %[pq], 0x0\n\t" \
  "buffer_load_dwordx4 v1, s[40:43], s68 offen offset:1024 lds\n\t" \
  "s_load_dword s84, %[pq], 0x40\n\t" \
  "s_mov_b64 exec, 0xffff\n\t" \
  "buffer_load_dwordx4 v4, s[40:43], s68 offen offset:2048 lds\n\t" \
  "s_add_u32 m0, s39, 24576\n\t" \
  "s_mov_b64 exec, -1\n\t" \
  "buffer_load_dwordx4 v5, s[44:47], s69 offen offset:0 lds\n\t" \
  "s_load_dword s84, %[pq], 0x800\n\t" \
  "buffer_load_dwordx4 v6, s[44:47], s69 offen offset:1024 lds\n\t" \
  "s_load_dword s84, %[pq], 0x840\n\t" \
  "buffer_load_dwordx4 v7, s[44:47], s69 offen offset:2048 lds\n\t" \
  "s_mov_b32 s85, 0x541000\n\t" \
  "s_load_dword s84, %[pw], s85\n\t" \
  "s_add_u32 m0, s38, 27648\n\t" \
  "s_nop 0\n\t" \
  "buffer_load_dwordx4 v2, s[40:43], s70 offen offset:0 lds\n\t" \
  "s_mov_b32 s85, 0x570000\n\t" \
  "s_load_dword s84, %[pw], s85\n\t" \
  "buffer_load_dwordx4 v3, s[40:43], s70 offen offset:1024 lds\n\t" \
  "s_mov_b32 s85, 0x570800\n\t" \
  "s_load_dword s84, %[pw], s85\n\t" \
  "s_mov_b64 exec, 0xffff\n\t" \
  "buffer_load_dwordx4 v4, s[40:43], s70 offen offset:2048 lds\n\t" \
  "s_add_u32 m0, s39, 36864\n\t" \
  "s_mov_b64 exec, -1\n\t" \
  "buffer_load_dwordx4 v5, s[44:47], s71 offen offset:0 lds\n\t" \
  "s_mov_b32 s85, 0x571000\n\t" \
  "s_load_dword s84, %[pw], s85\n\t" \
  "buffer_load_dwordx4 v6, s[44:47], s71 offen offset:1024 lds\n\t" \
  "s_mov_b32 s85, 0x5a0000\n\t" \
  "s_load_dword s84, %[pw], s85\n\t" \
  "buffer_load_dwordx4 v7, s[44:47], s71 offen offset:2048 lds\n\t" \
  "s_mov_b32 s85, 0x5a0800\n\t" \
  "s_load_dword s84, %[pw], s85\n\t" \
  "s_mov_b32 s68, 0x600\n\t" \
  "s_mov_b32 s69, 0x480000\n\t" \
  "s_mov_b32 s70, 0x24600\n\t" \
  "s_mov_b32 s71, 0x4b0000\n\t" \
  "s_add_u32 m0, s38, 36864\n\t" \
  "s_waitcnt vmcnt(12)\n\t" \
  "s_barrier\n\t" \
  "buffer_load_dwordx4 v0, s[40:43], s68 offen offset:0 lds\n\t" \
  "s_load_dword s84, %[pq], 0x1000\n\t" \
  "buffer_load_dwordx4 v1, s[40:43], s68 offen offset:1024 lds\n\t" \
  "s_load_dword s84, %[pq], 0x1040\n\t" \
  "s_mov_b64 exec, 0xffff\n\t" \
  "buffer_load_dwordx4 v4, s[40:43], s68 offen offset:2048 lds\n\t" \
  "s_add_u32 m0, s39, 49152\n\t" \
  "s_mov_b64 exec, -1\n\t" \
  "buffer_load_dwordx4 v5, s[44:47], s69 offen offset:0 lds\n\t" \
  "s_load_dword s84, %[pq], 0x1800\n\t" \
  "buffer_load_dwordx4 v6, s[44:47], s69 offen offset:1024 lds\n\t" \
  "s_load_dword s84, %[pq], 0x1840\n\t" \
  "buffer_load_dwordx4 v7, s[44:47], s69 offen offset:2048 lds\n\t" \
  "s_mov_b32 s85, 0x5a1000\n\t" \
  "s_load_dword s84, %[pw], s85\n\t" \
  "s_add_u32 m0, s38, 46080\n\t" \
  "s_nop 0\n\t" \
  "buffer_load_dwordx4 v2, s[40:43], s70 offen offset:0 lds\n\t" \
  "s_mov_b32 s85, 0x5d0000\n\t" \
  "s_load_dword s84, %[pw], s85\n\t" \
  "buffer_load_dwordx4 v3, s[40:43], s70 offen offset:1024 lds\n\t" \
  "s_mov_b32 s85, 0x5d0800\n\t" \
  "s_load_dword s84, %[pw], s85\n\t" \
  "s_mov_b64 exec, 0xffff\n\t" \
  "buffer_load_dwordx4 v4, s[40:43], s70 offen offset:2048 lds\n\t" \
  "s_add_u32 m0, s39, 61440\n\t" \
  "s_mov_b64 exec, -1\n\t" \
  "buffer_load_dwordx4 v5, s[44:47], s71 offen offset:0 lds\n\t" \
  "s_mov_b32 s85, 0x5d1000\n\t" \
  "s_load_dword s84, %[pw], s85\n\t" \
  "buffer_load_dwordx4 v6, s[44:47], s71 offen offset:1024 lds\n\t" \
  "buffer_load_dwordx4 v7, s[44:47], s71 offen offset:2048 lds\n\t" \
  "s_mov_b32 s68, 0x680\n\t" \
  "s_mov_b32 s69, 0x4e0000\n\t" \
  "s_mov_b32 s70, 0x24680\n\t" \
  "s_mov_b32 s71, 0x510000\n\t" \
  "s_add_u32 m0, s38, 0\n\t" \
  "s_waitcnt vmcnt(12)\n\t" \
  "s_barrier\n\t" \
  "buffer_load_dwordx4 v0, s[40:43], s68 offen offset:0 lds\n\t" \
  "s_load_dword s84, %[pq], 0x2000\n\t" \
  "buffer_load_dwordx4 v1, s[40:43], s68 offen offset:1024 lds\n\t" \
  "s_load_dword s84, %[pq], 0x2040\n\t" \
  "s_mov_b64 exec, 0xffff\n\t" \
  "buffer_load_dwordx4 v4, s[40:43], s68 offen offset:2048 lds\n\t" \
  "s_add_u32 m0, s39, 0\n\t" \
  "s_mov_b64 exec, -1\n\t" \
  "buffer_load_dwordx4 v5, s[44:47], s69 offen offset:0 lds\n\t" \
  "s_load_dword s84, %[pq], 0x2800\n\t" \
  "buffer_load_dwordx4 v6, s[44:47], s69 offen offset:1024 lds\n\t" \
  "s_load_dword s84, %[pq], 0x2840\n\t" \
  "buffer_load_dwordx4 v7, s[44:47], s69 offen offset:2048 lds\n\t" \
  "s_add_u32 m0, s38, 9216\n\t" \
  "s_nop 0\n\t" \
  "buffer_load_dwordx4 v2, s[40:43], s70 offen offset:0 lds\n\t" \
  "buffer_load_dwordx4 v3, s[40:43], s70 offen offset:1024 lds\n\t" \
  "s_mov_b64 exec, 0xffff\n\t" \
  "buffer_load_dwordx4 v4, s[40:43], s70 offen offset:2048 lds\n\t" \
  "s_add_u32 m0, s39, 12288\n\t" \
  "s_mov_b64 exec, -1\n\t" \
  "buffer_load_dwordx4 v5, s[44:47], s71 offen offset:0 lds\n\t" \
  "buffer_load_dwordx4 v6, s[44:47], s71 offen offset:1024 lds\n\t" \
  "buffer_load_dwordx4 v7, s[44:47], s71 offen offset:2048 lds\n\t" \
  "s_mov_b32 s68, 0x700\n\t" \
  "s_mov_b32 s69, 0x540000\n\t" \
  "s_mov_b32 s70, 0x24700\n\t" \
  "s_mov_b32 s71, 0x570000\n\t" \
  "s_add_u32 m0, s38, 18432\n\t" \
  "s_waitcnt vmcnt(12)\n\t" \
  "s_barrier\n\t" \
  "buffer_load_dwordx4 v0, s[40:43], s68 offen offset:0 lds\n\t" \
  "s_load_dword s84, %[pq], 0x3000\n\t" \
  "buffer_load_dwordx4 v1, s[40:43], s68 offen offset:1024 lds\n\t" \
  "s_load_dword s84, %[pq], 0x3040\n\t" \
  "s_mov_b64 exec, 0xffff\n\t" \
  "buffer_load_dwordx4 v4, s[40:43], s68 offen offset:2048 lds\n\t" \
  "s_add_u32 m0, s39, 24576\n\t" \
  "s_mov_b64 exec, -1\n\t" \
  "buffer_load_dwordx4 v5, s[44:47], s69 offen offset:0 lds\n\t" \
  "s_load_dword s84, %[pq], 0x3800\n\t" \
  "buffer_load_dwordx4 v6, s[44:47], s69 offen offset:1024 lds\n\t" \
  "s_load_dword s84, %[pq], 0x3840\n\t" \
  "buffer_load_dwordx4 v7, s[44:47], s69 offen offset:2048 lds\n\t" \
  "s_add_u32 m0, s38, 27648\n\t" \
  "s_nop 0\n\t" \
  "buffer_load_dwordx4 v2, s[40:43], s70 offen offset:0 lds\n\t" \
  "buffer_load_dwordx4 v3, s[40:43], s70 offen offset:1024 lds\n\t" \
  "s_mov_b64 exec, 0xffff\n\t" \
  "buffer_load_dwordx4 v4, s[40:43], s70 offen offset:2048 lds\n\t" \
  "s_add_u32 m0, s39, 36864\n\t" \
  "s_mov_b64 exec, -1\n\t" \
  "buffer_load_dwordx4 v5, s[44:47], s71 offen offset:0 lds\n\t" \
  "buffer_load_dwordx4 v6, s[44:47], s71 offen offset:1024 lds\n\t" \
  "buffer_load_dwordx4 v7, s[44:47], s71 offen offset:2048 lds\n\t" \
  "s_mov_b32 s68, 0x780\n\t" \
  "s_mov_b32 s69, 0x5a0000\n\t" \
  "s_mov_b32 s70, 0x24780\n\t" \
  "s_mov_b32 s71, 0x5d0000\n\t" \
  "s_add_u32 m0, s38, 36864\n\t" \
  "s_waitcnt vmcnt(12)\n\t" \
  "s_barrier\n\t" \
  "buffer_load_dwordx4 v0, s[40:43], s68 offen offset:0 lds\n\t" \
  "buffer_load_dwordx4 v1, s[40:43], s68 offen offset:1024 lds\n\t" \
  "s_mov_b64 exec, 0xffff\n\t" \
  "buffer_load_dwordx4 v4, s[40:43], s68 offen offset:2048 lds\n\t" \
  "s_add_u32 m0, s39, 49152\n\t" \
  "s_mov_b64 exec, -1\n\t" \
  "buffer_load_dwordx4 v5, s[44:47], s69 offen offset:0 lds\n\t" \
  "buffer_load_dwordx4 v6, s[44:47], s69 offen offset:1024 lds\n\t" \
  "buffer_load_dwordx4 v7, s[44:47], s69 offen offset:2048 lds\n\t" \
  "s_add_u32 m0, s38, 46080\n\t" \
  "s_nop 0\n\t" \
  "buffer_load_dwordx4 v2, s[40:43], s70 offen offset:0 lds\n\t" \
  "buffer_load_dwordx4 v3, s[40:43], s70 offen offset:1024 lds\n\t" \
  "s_mov_b64 exec, 0xffff\n\t" \
  "buffer_load_dwordx4 v4, s[40:43], s70 offen offset:2048 lds\n\t" \
  "s_add_u32 m0, s39, 61440\n\t" \
  "s_mov_b64 exec, -1\n\t" \
  "buffer_load_dwordx4 v5, s[44:47], s71 offen offset:0 lds\n\t" \
  "buffer_load_dwordx4 v6, s[44:47], s71 offen offset:1024 lds\n\t" \
  "buffer_load_dwordx4 v7, s[44:47], s71 offen offset:2048 lds\n\t" \
  "v_readlane_b32 s68, v9, 18\n\t" \
  "v_readlane_b32 s69, v9, 19\n\t" \
  "v_readlane_b32 s70, v9, 20\n\t" \
  "v_readlane_b32 s71, v9, 21\n\t" \
  "v_readlane_b32 s72, v9, 0\n\t" \
  "v_readlane_b32 s73, v9, 1\n\t" \
  "v_readlane_b32 s74, v9, 2\n\t" \
  "v_readlane_b32 s75, v9, 3\n\t" \
  "v_readlane_b32 s76, v9, 4\n\t" \
  "v_readlane_b32 s77, v9, 5\n\t" \
  "s_add_u32 m0, %[ob], 73728\n\t" \
  "s_waitcnt vmcnt(12)\n\t" \
  "s_barrier\n\t" \
  "s_nop 2\n\t" \
  "buffer_load_dwordx4 v8, %[so], s68 offen lds\n\t" \
  "s_add_u32 m0, %[ob], 77824\n\t" \
  "s_nop 0\n\t" \
  "buffer_load_dwordx4 v8, %[so], s69 offen lds\n\t" \
  "s_add_u32 m0, %[ob], 81920\n\t" \
  "s_nop 0\n\t" \
  "buffer_load_dwordx4 v8, %[so], s70 offen lds\n\t" \
  "s_add_u32 m0, %[ob], 86016\n\t" \
  "s_nop 0\n\t" \
  "buffer_load_dwordx4 v8, %[so], s71 offen lds\n\t" \
  "s_add_u32 m0, %[ob], 0\n\t" \
  "s_nop 0\n\t" \
  "buffer_load_dwordx4 v8, %[so], s72 offen lds\n\t" \
  "s_add_u32 m0, %[ob], 4096\n\t" \
  "s_nop 0\n\t" \
  "buffer_load_dwordx4 v8, %[so], s73 offen lds\n\t" \
  "s_add_u32 m0, %[ob], 8192\n\t" \
  "s_nop 0\n\t" \
  "buffer_load_dwordx4 v8, %[so], s74 offen lds\n\t" \
  "s_add_u32 m0, %[ob], 12288\n\t" \
  "s_nop 0\n\t" \
  "buffer_load_dwordx4 v8, %[so], s75 offen lds\n\t" \
  "s_add_u32 m0, %[ob], 16384\n\t" \
  "s_nop 0\n\t" \
  "buffer_load_dwordx4 v8, %[so], s76 offen lds\n\t" \
  "s_add_u32 m0, %[ob], 20480\n\t" \
  "s_nop 0\n\t" \
  "buffer_load_dwordx4 v8, %[so], s77 offen lds\n\t" \
  "s_waitcnt vmcnt(10)\n\t" \
  "s_barrier\n\t" \
  "s_nop 2\n\t" \
  "s_add_u32 m0, %[ob], 90112\n\t" \
  "v_readlane_b32 s68, v9, 22\n\t" \
  "s_nop 4\n\t" \
  "buffer_load_dwordx4 v8, %[so], s68 offen lds\n\t" \
  "s_add_u32 m0, %[ob], 94208\n\t" \
  "v_readlane_b32 s68, v9, 23\n\t" \
  "s_nop 4\n\t" \
  "buffer_load_dwordx4 v8, %[so], s68 offen lds\n\t" \
  "s_add_u32 m0, %[ob], 98304\n\t" \
  "v_readlane_b32 s68, v9, 24\n\t" \
  "s_nop 4\n\t" \
  "buffer_load_dwordx4 v8, %[so], s68 offen lds\n\t" \
  "s_add_u32 m0, %[ob], 102400\n\t" \
  "v_readlane_b32 s68, v9, 25\n\t" \
  "s_nop 4\n\t" \
  "buffer_load_dwordx4 v8, %[so], s68 offen lds\n\t" \
  "s_add_u32 m0, %[ob], 106496\n\t" \
  "v_readlane_b32 s68, v9, 26\n\t" \
  "s_nop 4\n\t" \
  "buffer_load_dwordx4 v8, %[so], s68 offen lds\n\t" \
  "s_add_u32 m0, %[ob], 24576\n\t" \
  "v_readlane_b32 s68, v9, 6\n\t" \
  "s_nop 4\n\t" \
  "buffer_load_dwordx4 v8, %[so], s68 offen lds\n\t" \
  "s_add_u32 m0, %[ob], 28672\n\t" \
  "v_readlane_b32 s68, v9, 7\n\t" \
  "s_nop 4\n\t" \
  "buffer_load_dwordx4 v8, %[so], s68 offen lds\n\t" \
  "s_add_u32 m0, %[ob], 32768\n\t" \
  "v_readlane_b32 s68, v9, 8\n\t" \
  "s_nop 4\n\t" \
  "buffer_load_dwordx4 v8, %[so], s68 offen lds\n\t" \
  "s_add_u32 m0, %[ob], 36864\n\t" \
  "v_readlane_b32 s68, v9, 9\n\t" \
  "s_nop 4\n\t" \
  "buffer_load_dwordx4 v8, %[so], s68 offen lds\n\t" \
  "s_add_u32 m0, %[ob], 40960\n\t" \
  "v_readlane_b32 s68, v9, 10\n\t" \
  "s_nop 4\n\t" \
  "buffer_load_dwordx4 v8, %[so], s68 offen lds\n\t" \
  "s_add_u32 m0, %[ob], 45056\n\t" \
  "v_readlane_b32 s68, v9, 11\n\t" \
  "s_nop 4\n\t" \
  "buffer_load_dwordx4 v8, %[so], s68 offen lds\n\t" \
  "s_nop 3\n\t" \
  "s_barrier\n\t" \
  "s_nop 2\n\t" \
  "s_add_u32 m0, %[ob], 110592\n\t" \
  "v_readlane_b32 s68, v9, 27\n\t" \
  "s_nop 4\n\t" \
  "buffer_load_dwordx4 v8, %[so], s68 offen lds\n\t" \
  "s_add_u32 m0, %[ob], 114688\n\t" \
  "v_readlane_b32 s68, v9, 28\n\t" \
  "s_nop 4\n\t" \
  "buffer_load_dwordx4 v8, %[so], s68 offen lds\n\t" \
  "s_add_u32 m0, %[ob], 118784\n\t" \
  "v_readlane_b32 s68, v9, 29\n\t" \
  "s_nop 4\n\t" \
  "buffer_load_dwordx4 v8, %[so], s68 offen lds\n\t" \
  "s_add_u32 m0, %[ob], 122880\n\t" \
  "v_readlane_b32 s68, v9, 30\n\t" \
  "s_nop 4\n\t" \
  "buffer_load_dwordx4 v8, %[so], s68 offen lds\n\t" \
  "s_add_u32 m0, %[ob], 126976\n\t" \
  "v_readlane_b32 s68, v9, 31\n\t" \
  "s_nop 4\n\t" \
  "buffer_load_dwordx4 v8, %[so], s68 offen lds\n\t" \
  "s_add_u32 m0, %[ob], 49152\n\t" \
  "v_readlane_b32 s68, v9, 12\n\t" \
  "s_nop 4\n\t" \
  "buffer_load_dwordx4 v8, %[so], s68 offen lds\n\t" \
  "s_add_u32 m0, %[ob], 53248\n\t" \
  "v_readlane_b32 s68, v9, 13\n\t" \
  "s_nop 4\n\t" \
  "buffer_load_dwordx4 v8, %[so], s68 offen lds\n\t" \
  "s_add_u32 m0, %[ob], 57344\n\t" \
  "v_readlane_b32 s68, v9, 14\n\t" \
  "s_nop 4\n\t" \
  "buffer_load_dwordx4 v8, %[so], s68 offen lds\n\t" \
  "s_add_u32 m0, %[ob], 61440\n\t" \
  "v_readlane_b32 s68, v9, 15\n\t" \
  "s_nop 4\n\t" \
  "buffer_load_dwordx4 v8, %[so], s68 offen lds\n\t" \
  "s_add_u32 m0, %[ob], 65536\n\t" \
  "v_readlane_b32 s68, v9, 16\n\t" \
  "s_nop 4\n\t" \
  "buffer_load_dwordx4 v8, %[so], s68 offen lds\n\t" \
  "s_add_u32 m0, %[ob], 69632\n\t" \
  "v_readlane_b32 s68, v9, 17\n\t" \
  "s_nop 4\n\t" \
  "buffer_load_dwordx4 v8, %[so], s68 offen lds\n\t" \
  "s_waitcnt vmcnt(0) lgkmcnt(0)\n\t"
#define LD_NBAR 17
#define LD_CO 0
#define LD_HT 8
#define LD_TP 3
#define LD_CLOB "v11", "s68", "s69", "s70", "s71", "s72", "s73", "s74", "s75", "s76", "s77", "s78", "s79", "s80", "s81", "s82", "s83", "s85"

#define ENDW
#define TS(i)
#define TSS(i)
#define TOUT(n)

#define SPIN(cond,slp) do{ int sp_=0; while(cond){ __builtin_amdgcn_s_sleep(slp); if(++sp_>(1<<22)) break; } }while(0)
__device__ __forceinline__ unsigned rd1(const char* p){ return __builtin_amdgcn_readfirstlane(*(volatile LDS_AS unsigned*)(LDS_AS char*)p); }
__device__ __forceinline__ unsigned lds_inc(const char* p, int l){ unsigned r=0; if(l==0) r=__hip_atomic_fetch_add((LDS_AS unsigned*)(LDS_AS char*)p, 1u, __ATOMIC_RELAXED, __HIP_MEMORY_SCOPE_WORKGROUP); return __builtin_amdgcn_readfirstlane(r); }
__device__ __forceinline__ void lds_add(unsigned adr){ asm volatile("s_mov_b64 exec, 1\n\tds_add_u32 %0, %1\n\ts_mov_b64 exec, -1" :: "v"(adr), "v"(1u) : "memory"); }
__device__ __forceinline__ void publish(unsigned cnt, const unsigned* base, unsigned voff, unsigned tag){
  unsigned t;
  asm volatile("s_waitcnt vmcnt(0)\n\ts_mov_b64 exec, 1\n\tds_add_rtn_u32 %0, %1, %2\n\ts_waitcnt lgkmcnt(0)\n\tv_cmp_eq_u32 vcc, 3, %0\n\ts_cbranch_vccz PB_%=\n\t"
    "s_mov_b64 exec, 0xffff\n\tglobal_store_dword %3, %4, %5 sc1\n\tPB_%=:\n\ts_mov_b64 exec, -1"
    : "=&v"(t) : "v"(cnt), "v"(1u), "v"(voff), "v"(tag), "s"(base) : "memory", "vcc");
}
__device__ __forceinline__ void bars(int n){
  asm volatile("BL_%=:\n\ts_barrier\n\ts_sub_u32 %0, %0, 1\n\ts_cmp_lg_u32 %0, 0\n\ts_cbranch_scc1 BL_%=" : "+s"(n) :: "memory", "scc");
}
#define barc(rem) barc_(rem)
#define CTRA
#define CTRP
__device__ __forceinline__ void barc_(unsigned& rem){
  asm volatile("s_cmp_eq_u32 %0, 0\n\ts_cbranch_scc1 BC_%=\n\ts_barrier\n\ts_sub_u32 %0, %0, 1\n\tBC_%=:" : "+s"(rem) :: "memory", "scc");
}
template<int SLP> __device__ __forceinline__ void wait_flag_b(const unsigned* p, unsigned voff, unsigned tag, unsigned& rem CTRA){
  int spins=0;
  while(true){
    unsigned v; asm volatile("global_load_dword %0, %1, %2 sc1" : "=v"(v) : "v"(voff), "s"(p) : "memory");
    barc(rem); if(PB2) barc(rem);
    asm volatile("s_waitcnt vmcnt(0)" : "+v"(v) :: "memory");
    if(__all(v==tag) || ++spins>(1<<21)) break;
    if(rem==0) __builtin_amdgcn_s_sleep(SLP);
  }
}
__device__ __forceinline__ void stout(void* sbase, unsigned voff, u32x4 v){
  st16cs(sbase,voff,v);
}

typedef unsigned u32x8 __attribute__((ext_vector_type(8)));
typedef unsigned u32x16 __attribute__((ext_vector_type(16)));
__device__ __forceinline__ char* ptr64(unsigned lo, unsigned hi){ return (char*)(((unsigned long long)hi<<32)|lo); }
__device__ __forceinline__ u32x4 srd(const char* p, unsigned bytes){ return u32x4{(unsigned)(size_t)p,(unsigned)((size_t)p>>32)&0xffffu,bytes,0x20000u}; }
constexpr size_t WS_O=0, WS_KV=(size_t)S*DM*2, WS_Y=(size_t)S*DM*4, WS_FL=(size_t)S*DM*12;

__global__ __launch_bounds__(768,3) void fused_kernel(const u16* x_, const u16* wqkv_, const u16* wo_, char* ws_, u16* out_,
    const u16* kin_, const u16* vin_, const u16* shk_, const u16* hk_, const u16* qin_, const u16* sD_)
{
  __shared__ __attribute__((aligned(128))) char smem[ALL_LDS];
  TS(0);
  int wv;
  asm volatile(
      "s_load_dwordx16 s[48:63], %[ka], 0x0\n\ts_load_dwordx4 s[64:67], %[ka], 0x40\n\ts_load_dwordx2 s[78:79], %[ka], 0x50\n\ts_getpc_b64 s[72:73]\n\tv_readfirstlane_b32 s75, v0\n\ts_lshr_b32 s74, %[b], 3\n\ts_mul_i32 s74, s74, 12\n\ts_lshr_b32 s75, s75, 6\n\t"
      "s_add_u32 s74, s74, s75\n\ts_min_u32 s74, s74, %[cl]\n\ts_lshl_b32 s74, s74, 7\n\ts_mov_b32 m0, %[sk]\n\tv_mov_b32 v1, s74\n\ts_mov_b64 exec, 1\n\tglobal_load_lds_dword v1, s[72:73]\n\ts_mov_b64 exec, -1\n\t"
      "s_mov_b32 s76, %[b]\n\ts_cmp_lt_u32 s75, 4\n\ts_cbranch_scc1 FK_G\n\ts_cmp_lt_u32 s75, 8\n\ts_cbranch_scc0 FK_C\n\t"
      "v_and_b32 v9, 7, v0\n\tv_and_b32 v10, 63, v0\n\ts_lshl_b32 s74, %[b], 19\n\ts_lshl_b32 s73, %[b], 11\n\ts_or_b32 s74, s74, s73\n\ts_and_b32 s74, s74, 0x3fc000\n\ts_lshl_b32 s73, s75, 9\n\t"
      "s_add_u32 s74, s74, s73\n\ts_add_u32 s74, s74, 0x7800\n\tv_lshl_add_u32 v9, v9, 11, s74\n\tv_and_b32 v1, 24, v0\n\ts_lshl2_add_u32 s74, %[b], s75\n\ts_mul_i32 s74, s74, %[nw]\n\tv_lshl_add_u32 v9, v1, 4, v9\n\t"
      "s_add_u32 s74, s74, %[w0]\n\tv_add_lshl_u32 v10, v10, s74, 7\n\t"
      "s_setprio %[lp]\n\ts_and_b32 s74, s75, 3\n\tv_lshrrev_b32 v1, 3, v0\n\tv_lshrrev_b32 v3, 4, v0\n\ts_and_b32 s72, %[b], 1\n\ts_and_b32 s73, %[b], 0xe0\n\ts_lshl_b32 s72, s72, 10\n\ts_lshl2_add_u32 s72, s73, s72\n\t"
      "v_and_b32 v4, 2, v1\n\tv_and_or_b32 v3, v3, 4, v4\n\tv_and_b32 v4, 31, v1\n\tv_xor_b32 v3, v3, v0\n\tv_mul_u32_u24 v4, 0x1800, v4\n\tv_and_b32 v3, 7, v3\n\tv_lshl_add_u32 v3, v3, 4, s72\n\t"
      "s_mul_i32 s39, s74, 0xc00\n\ts_mul_i32 s38, s74, 0x900\n\ts_mul_i32 s74, s74, 18\n\tv_add_u32 v5, v3, v4\n\tv_and_b32 v1, 7, v1\n\tv_add_u32 v6, 0x400, v5\n\tv_add_u32 v1, s74, v1\n\tv_add_u32 v7, 0x800, v5\n\t"
      "s_and_b32 s72, %[b], 6\n\ts_and_b32 s73, %[b], 24\n\ts_lshl4_add_u32 s72, s72, s73\n\tv_xor_b32 v3, v1, v0\n\ts_lshl_b32 s72, s72, 15\n\tv_and_b32 v3, 7, v3\n\tv_lshl_add_u32 v1, v1, 11, s72\n\t"
      "s_mov_b32 s47, 0x20000\n\ts_mov_b32 s46, 0x600000\n\tv_lshl_add_u32 v2, v3, 4, v1\n\ts_mov_b32 s43, s47\n\ts_mov_b32 s42, 0x408000\n\ts_movk_i32 s73, 0x3c00\n\t"
      "v_add_u32 v3, s73, v2\n\ts_add_u32 s38, s38, %[xf]\n\tv_max_u32 v0, 0x8000, v2\n\tv_add_u32 v4, s73, v3\n\tv_max_u32 v1, 0x8000, v3\n\t"
      "s_waitcnt lgkmcnt(0)\n\ts_mov_b64 s[44:45], s[50:51]\n\ts_sub_u32 s40, s48, 0x8000\n\ts_subb_u32 s41, s49, 0\n\t"
      LD_ASM1
      "s_branch FK_L"
      : "=s"(wv)
      : "{v0}"(threadIdx.x), [ka]"s"(__builtin_amdgcn_kernarg_segment_ptr()), [b]"s"(blockIdx.x), [cl]"n"(CODEL-1), [sk]"n"(SINK+256), [lp]"n"(LPR), [xf]"n"(X_OFF), [nw]"n"(3*TPW), [w0]"n"(TW0*3072-12*TPW), [wm]"n"((1<<(3*TPW))-1)
      : "memory", "scc", "s48", "s49", "s50", "s51", "s52", "s53", "s54", "s55", "s72", "s73", "s74", "s75", "s76", "s78", "s79", "v1", "v2", "v3", "v4", "v5", "v6", "v7", "v9", "v10",
        "s40", "s41", "s42", "s43", "s44", "s45", "s46", "s47", "s38", "s39", "s56", "s57", "s58", "s59", "s60", "s61", "s62", "s63", "s64", "s65", "s66", "s67", "s68");
  #define ENTER(lbl) int w8, bid; u32x8 a0, a1; u32x4 a2; u32x2 a3; asm volatile(lbl ":" : "={s75}"(w8), "={s76}"(bid), "={s[48:55]}"(a0), "={s[56:63]}"(a1), "={s[64:67]}"(a2), "={s[78:79]}"(a3) :: "memory");
  #define ARGW asm volatile("s_waitcnt lgkmcnt(0)" : "+{s[48:55]}"(a0), "+{s[56:63]}"(a1), "+{s[64:67]}"(a2), "+{s[78:79]}"(a3));
  #define ROLE const int w=w8&3, xcd=bid&7, idx=bid>>3, rb=(xcd>>1)*4+(idx&3), hp=(xcd&1)*8+(idx>>2), s0=rb*128; (void)s0; (void)hp;
  #define LANEVARS int l; asm volatile("v_mbcnt_lo_u32_b32 %0, -1, 0\n\tv_mbcnt_hi_u32_b32 %0, -1, %0" : "=v"(l)); const int g=l>>4, fr=l&15, tq=(l&15)>>2, tp=l&3; (void)g; (void)fr; (void)tq; (void)tp;
  #define ARGS0 const char* x=ptr64(a0[0],a0[1]); const char* wqkv=ptr64(a0[2],a0[3]); const char* wo=ptr64(a0[4],a0[5]); char* ws=ptr64(a0[6],a0[7]); \
    unsigned* flags=(unsigned*)(ws+WS_FL); const int hot0=(3-(int)(a0[2]>>7))&7; (void)x; (void)wqkv; (void)wo; (void)flags; (void)hot0;
  #define OCOL ((hp&8)|((hp+(int)(a0[2]>>7)-(int)(a0[4]>>7)+OSH)&7))
  #define ORA ((unsigned)(hp-hot0-1)&7u)
  #define ORE(c) (ORA==7u? (c)-1u : (c))
  #define ORO(c) (ORE(c)>=NF && ORE(c)<NF+7u)
  #define ORD(c) (((((ORE(c)<14u? ((ORA==7u? 0u:ORA)+(ORO(c)? ORE(c)-NF : (ORE(c)<NF? ORE(c) : ORE(c)-7u)))%7u : 7u)+hot0+1)&7u))+8u*(((hp>>3)^(ORO(c) || (c)==15u))&1u))
  const unsigned sb0=__builtin_amdgcn_readfirstlane(lds_addr(smem));
  const char* syn=smem+SYN;

  if(__builtin_expect(wv==1,1)){
    ENTER("FK_L")
    u32x4 sx, sw; unsigned xl, wl, xoA, xoB, xo, xo1, xo2, wof[3];
    asm volatile("; descriptors and LDS bases" : "={s[40:43]}"(sx), "={s[44:47]}"(sw), "={s38}"(xl), "={s39}"(wl));
    asm volatile("; lane offsets" : "={v0}"(xoA), "={v1}"(xoB), "={v2}"(xo), "={v3}"(xo1), "={v4}"(xo2), "={v5}"(wof[0]), "={v6}"(wof[1]), "={v7}"(wof[2]));
    unsigned ltr=0, ltr2=0, ltr3=0, sd; (void)ltr; (void)ltr2; (void)ltr3;
    const char* pw;
    {
      ROLE
      LANEVARS
      ARGS0
      const int hg=((idx>>2)-hot0)&7, hi=hot0*4+(idx&3);
      const unsigned long long hm= hg==0? 0ull : hg==HGR? 0xfffull : 0x3full;
      const unsigned hto=(unsigned)(w*393216+(l<6? l*2048+idx*12288 : (l-6)*2048+hi*12288));
      pw=wqkv+(idx*6144+(xcd&1)*1024+(xcd>>1)*256+w*64);
      asm volatile(LD_ASM2
        : "=&{s84}"(sd), "+s"(w8), "+s"(bid), "+s"(a0)
        : "{v0}"(xoA), "{v1}"(xoB), "{v2}"(xo), "{v3}"(xo1), "{v4}"(xo2), "{v5}"(wof[0]), "{v6}"(wof[1]), "{v7}"(wof[2]),
          "{v16}"(hto), [sh]"s"(srd(wqkv+((xcd&1)*8+hot0)*128,6291456u-1920u)), [hm]"s"(hm),
          "{s[40:43]}"(sx), "{s[44:47]}"(sw), "{s38}"(xl), "{s39}"(wl), [pw]"s"(pw), [tsk]"n"(SINK+256)
        : "memory", "scc", "vcc", LD_CLOB);
    }
    ROLE
    LANEVARS
    ARGS0
    const unsigned wd=OCOL*128;
    {
      const unsigned wof2=(unsigned)(w*16384+(l>>3)*2048+((((l&7)>>1)^((l>>4)&3))*32)+(l&1)*16);
      const unsigned to=ORD((unsigned)(l>>1)&15u);
      const unsigned tab=to*131072u+(l&1)*65536;
      asm volatile(LD_ASM3
        : "+{s84}"(sd)
        : "{v0}"(xoA), "{v1}"(xoB), "{v2}"(xo), "{v3}"(xo1), "{v4}"(xo2), "{v5}"(wof[0]), "{v6}"(wof[1]), "{v7}"(wof[2]), "{v8}"(wof2), "{v9}"(tab),
          "{s[40:43]}"(sx), "{s[44:47]}"(sw), [so]"s"(srd(wo+wd,2097152u-wd)), "{s38}"(xl), "{s39}"(wl), [ob]"s"(sb0+w*1024), [pw]"s"(pw),
          [pq]"s"(wo+((xcd&1)*1024+((3-(int)(a0[4]>>7))&7)*128+(idx*32+w*8)*2048))
        : "memory", "scc", "vcc", LD_CLOB);
    }
    const unsigned tag=rd1(syn+64)+1u;
    lds_add(sb0+SYN+12);
    TS(2);
    if(w>=4-NPOL){
      const unsigned* fp=flags+F_O+(rb*16+hp)*16; const unsigned fo=ORD((unsigned)l&15u)*4u;
      const unsigned madr=sb0+(l==0? SYN+20 : SINK+l*4);
      SPIN(rd1(syn+12)<4u || rd1(syn+16)<4u,1);
      if(w==3) PUBM(madr,1u);
      if(PD0) __builtin_amdgcn_s_sleep(PD0);
      if(w==2) __builtin_amdgcn_s_sleep(PD1); if(w==1) __builtin_amdgcn_s_sleep(2*PD1); if(w==0) __builtin_amdgcn_s_sleep(3*PD1);
      unsigned rdy=1; int spins=0;
      while(rdy!=0xFFFFu){
        unsigned v;
        asm volatile("s_mov_b64 exec, 0xffff\n\tglobal_atomic_or %0, %1, %3, %2 sc0 sc1\n\ts_mov_b64 exec, -1\n\ts_waitcnt vmcnt(0)" : "=&v"(v) : "v"(fo), "s"(fp), "v"(0u) : "memory");
        unsigned r=((unsigned)__ballot(v==tag)&0xFFFFu)|1u;
        if(++spins>(1<<21)) r=0xFFFFu;
        if(r!=rdy){ PUBM(madr,(unsigned)__builtin_ctz(~r)); rdy=r; if(w<3 && (r&0x3FFFu)==0x3FFFu) break; }
        else __builtin_amdgcn_s_sleep(OSLP);
      }
      TS(3);
      if(bid==0 && w==3){
        int sp=0;
        while(true){
          u32x4 v; asm volatile("global_load_dwordx4 %0, %1, %2 sc1\n\ts_waitcnt vmcnt(0)" : "=&v"(v) : "v"(l*16), "s"(flags+F_Y) : "memory");
          if(__all(v[0]==tag && v[1]==tag && v[2]==tag && v[3]==tag) || ++sp>(1<<20)) break;
          __builtin_amdgcn_s_sleep(12);
        }
        if(l==0) stflag(flags, tag);
      }
    } else { TS(3); }
    TOUT(4);
    __builtin_amdgcn_endpgm();
  }

  if(wv==0){
    ENTER("FK_G")
    ROLE
    __builtin_amdgcn_s_setprio(GPR);
    const int h=w&1, r=w>>1;
    LANEVARS
    TSS(3);
    ARGW
    ARGS0
    if(PGT&1){ if(w==1 && l<7) touch(ws, l<6? l*2097152+PGO : (unsigned)WS_FL+2048, sb0+SINK+512); }
    if(PGT&2){ if(w==2 && l<4) touch(ptr64(a1[0],a1[1]), l*2097152+PGO, sb0+SINK+512); }
    if(PGT&4){ if(w==3 && l<1) touch(wo, PGO, sb0+SINK+512); if(w==0 && l<2) touch(wqkv, l*2097152+2097152, sb0+SINK+512); }
    TSS(4);
    {
      unsigned lx0=sb0+X_OFF+fr*128+((g^(fr&7))*16), lx1=sb0+X_OFF+fr*128+(((4+g)^(fr&7))*16);
      const int sw=wswz(8*g+tq);
      unsigned lw0=sb0+g*3072+tq*128+(unsigned)((((2*h)^sw)*32)+8*tp), lw1=sb0+g*3072+tq*128+(unsigned)((((2*h+1)^sw)*32)+8*tp);
      char* ot=ws+WS_O+(rb*16+hp)*16384+r*8192+h*2048;
      unsigned it; unsigned long long rm; asm("s_cmp_lg_u32 %1, 0\n\ts_cselect_b64 %0, -1, 0" : "=s"(rm) : "s"(r) : "scc");
      asm volatile(N2_ASM
        : [it]"=&s"(it), "+{v56}"(lx0), "+{v57}"(lx1), "+{v58}"(lw0), "+{v59}"(lw1)
        : "{v62}"(l*16), "{v63}"(sb0+EX_OFF+h*3072+l*16), [r]"s"(r), [ot]"s"(ot), [ou]"s"(ot+4096), [fa]"s"(sb0+SYN+32+w*4), [fp]"s"(sb0+SYN+32+(w^2)*4),
          [vo]"s"(2048-1024*r), [rm]"s"(rm), [z]"s"(rb|r)
        : "memory", "scc", "vcc", N2_CLOB);
    }
    TS(3);
    unsigned tb;
    {
      LANEVARS
      const unsigned tag=rd1(syn+64)+1u;
      const unsigned ra=sb0+(4*g+tq)*128+8*tp; const int sw=(2*g+(tq>>1))&3;
      unsigned b0=ra+((0^sw)*32), b1=ra+((1^sw)*32), b2=ra+((2^sw)*32), b3=ra+((3^sw)*32), vo=(unsigned)(w*4096+l*16), sy=sb0+SYN+20;
      unsigned sr=sb0+EX_OFF+w*2048+(l>>3)*128+(((l&7)^(l>>3))*16), sg=sb0+EX_OFF+w*2048+fr*128+(((g>>1)^(fr&7))*16)+(g&1)*8;
      tb=ORD((unsigned)l&15u)*16384u;
      asm volatile("" : "+{v160}"(b0), "+{v161}"(b1), "+{v162}"(b2), "+{v163}"(b3), "+{v164}"(vo), "+{v165}"(sy), "+{v166}"(sr), "+{v167}"(sg), "+{v96}"(tb));
      publish(sb0+SYN+16, flags+F_O+rb*256+hp, l*64, tag);
      TS(4);
      __builtin_amdgcn_s_setprio(G2P);
      unsigned iss=0, q0, q1, q2, cn=0;
      asm volatile(G3_ASM
        : [iss]"+&s"(iss), [t0]"=&s"(q0), [t1]"=&s"(q1), [t2]"=&s"(q2), [cn]"+&s"(cn),
          "+{v160}"(b0), "+{v161}"(b1), "+{v162}"(b2), "+{v163}"(b3), "+{v165}"(sy), "+{v96}"(tb)
        : "{v164}"(vo), "{v166}"(sr), "{v167}"(sg), [go]"s"(60+w*4), [sy]"n"(SYN+20), [ob]"s"(srd(ws+WS_O+rb*262144,262144u)), [op]"s"(ptr64(a1[0],a1[1])+((s0+w*32)*4096+2048+OCOL*128))
        : "memory", "scc", "vcc", G3_CLOB);
    }
    {
      ENDW TS(6); TOUT(7);
    }
    __builtin_amdgcn_endpgm();
  }

  {
    ENTER("FK_C")
    ROLE
    char* cr=smem+C_OFF+2048+w*4096;
    const unsigned crs=sb0+C_OFF+2048+w*4096;
    const int sblk=bid>>3, td0=(bid&7)*128+(w&1)*64;
    const int vb=(LOCA&1)? xcd*32+(idx&16)+(idx&1)*8+((idx>>1)&7) : bid, fb=(LOCA&2)? (idx&15)*16+xcd*2+(idx>>4) : bid;
    const int d=(vb>>4)*64+(((vb>>3)&1)*4+w)*8+(vb&7);
    int kv0=KV0+((sblk>>KSH)&KVS);
    static_assert(KV0+KVS+PP_NB<=LD_NBAR && KV0>=EB, "barrier budget");
    unsigned tagv, ctr; f32x8 RB;
    const int fd0=(fb&15)*64, fs0=(fb>>4)*128+w*32;
    u32x4 qv[4];
    f32x16 A0,A1,A2,A3;
    const int rw=sblk*64+(w>>1)*32, j=rw>>5;
    unsigned k0,k1,k2,ko,ta,se,so,ha,l16,rb0[2];
    {
      LANEVARS
      if(w==0 && l<25 && l!=16) ((unsigned*)(smem+SYN))[l]=0u;
      if(l<32) *(u32x4*)(smem+C_OFF+w*512+l*16)=u32x4{0,0,0,0};
      asm volatile("" ::: "memory");
      __builtin_amdgcn_s_setprio(CPR);
      const int dg=l&7, rg=l>>3;
      ko=(unsigned)(rg*8192+dg*16);
      const bool top=(rw==0 && rg==0);
      k0=top? ko+6144:ko; k1=top? ko+6144:ko+2048; k2=top? ko+6144:ko+4096;
      ta=sb0+EX_OFF+w*1024+(w&1)*512+dg*64; se=(l&8)? 0x03020706u : 0x05040100u; ha=crs+(32*g-4*tq+4*tp)*2; l16=l*16;
      so=(unsigned)(rg*32768+((j>>2)&3)*128+((dg&4)|((dg+j)&3))*16);
      #pragma unroll
      for(int k=0;k<2;k++){ const int q=fr-1-2*g+k; rb0[k]=sb0+C_OFF+(4+(q>>1))*256+((q>>3)&3)*64+((w+(q>>1))&3)*16+(q&1)*8; }
      asm volatile("" : "+{v152}"(k0), "+{v153}"(k1), "+{v154}"(k2), "+{v155}"(ko), "+{v156}"(ta), "+{v157}"(se), "+{v159}"(so), "+{v163}"(l16), "+{v165}"(ha), "+{v166}"(rb0[0]), "+{v167}"(rb0[1]));
    }
    TSS(3);
    ARGW
    if(EB==1) asm volatile("s_barrier" : "+s"(a1), "+s"(a2), "+s"(a3), "+s"(a0) :: "memory");
    if(EB==2) asm volatile("s_barrier\n\ts_barrier" : "+s"(a1), "+s"(a2), "+s"(a3), "+s"(a0) :: "memory");
    ARGS0
    char* out=ptr64(a1[0],a1[1]); const char* qp=ptr64(a2[2],a2[3])+(fs0*DM+fd0)*2;
    {
      const char* kb=ptr64(a1[2],a1[3])+(rw*DM+td0)*2; const char* vb=ptr64(a1[4],a1[5])+(rw*DM+td0)*2;
      unsigned long long zm; asm("s_cmp_eq_u32 %1, 0\n\ts_cselect_b64 %0, 0xff, 0" : "=s"(zm) : "s"(rw) : "scc");
      unsigned t0;
      if(PGT&8){ const char* tp= w==0? kb : w==1? vb : w==2? qp : ptr64(a2[0],a2[1])+d*4096;
        asm volatile("s_mov_b32 m0, %1\n\ts_mov_b64 exec, 1\n\tglobal_load_lds_dword v163, %0\n\ts_mov_b64 exec, -1" :: "s"(tp), "s"(sb0+SINK+512), "{v163}"(l16) : "memory"); }
      TSS(5);
      asm volatile(PP_ASM
        : [t0]"=&s"(t0), "=&{v164}"(ctr), "+{v159}"(so), "=&{v160}"(tagv), "=&{v[24:31]}"(RB), "=&{v[44:47]}"(qv[0]), "=&{v[48:51]}"(qv[1]), "=&{v[52:55]}"(qv[2]), "=&{v[56:59]}"(qv[3]),
          "=&{v[64:79]}"(A0), "=&{v[80:95]}"(A1), "=&{v[96:111]}"(A2), "=&{v[112:127]}"(A3)
        : "{v152}"(k0), "{v153}"(k1), "{v154}"(k2), "{v155}"(ko), "{v156}"(ta), "{v157}"(se), "{v166}"(rb0[0]), "{v167}"(rb0[1]), "{v165}"(ha), "{v163}"(l16),
          [qb0]"s"(qp), [qb2]"s"(qp+4096), [mk]"s"(0x180u), [em]"s"(1ull|(512ull<<w)), [hd]"s"(crs),
          [kb0]"s"(kb-6144), [kb3]"s"(kb), [kb5]"s"(kb+4096), [vb0]"s"(vb), [vb2]"s"(vb+4096), [sb]"s"(ws+WS_KV+td0*4096+j*512),
          [n0]"s"(kv0-EB-(HH==2)), [ff]"s"(flags+F_PP+(td0>>6)*64+j), [zm]"s"(zm), [shd]"s"(sb0+EX_OFF+w*1024), [tgd]"s"(sb0+SYN+64), [shp]"s"(ptr64(a1[6],a1[7])+(bid&7)*1024), [fl]"s"(flags),
          [hp]"s"(ptr64(a2[0],a2[1])+d*4096), [dp]"s"(ptr64(a3[0],a3[1])+d*2),
          [ph]"s"(wqkv+(((xcd&1)*8+hot0)*128+idx*6144+(w>>1)*196608+(w&1)*64))
        : "memory", "scc", PP_CLOB);
    }
    LANEVARS
    const unsigned tag=__builtin_amdgcn_readfirstlane(tagv)+1u;
    unsigned rem=LD_NBAR-PP_NB-kv0;
    barc(rem);
    const unsigned* pf=flags+F_PP+(d>>6)*64;
    const char* kvp=ws+WS_KV+((vb>>4)*8+(vb&7))*32768+((vb>>3)&1)*64+w*8192;
    const unsigned kvo=(unsigned)((l>>2)*128+(l&3)*16);
    int bidx=((l>=16)? l-16 : (l==0? 63 : l+47))*4;
    asm volatile("" : "+{v32}"(bidx));
    __builtin_amdgcn_s_setprio(3);
    while(rem>PREM) barc(rem);
    wait_flag_b<8>(pf, l*4, tag, rem CTRP);
    #pragma unroll
    for(int i=0;i<4;i++){ if(i%KTH==0 && i) barc(rem); gldsc(kvp+i*2048, kvo, crs+i*1024); }
    barc(rem); if(PB2) barc(rem);
    WAITV(0);
    asm volatile("s_mov_b32 m0, %2\n\ts_mov_b64 exec, 1\n\tglobal_load_lds_dword %0, %1 sc1\n\ts_mov_b64 exec, -1" :: "v"(0u), "s"(pf), "s"(sb0+SYN+100+w*4) : "memory");
    while(rem) barc(rem);
    { int sp=0; while(!__all(*(volatile LDS_AS unsigned*)(LDS_AS char*)(syn+100+(l&3)*4)==tag) && ++sp<(1<<22)) __builtin_amdgcn_s_sleep(1); }
    SPIN(rd1(syn+32+w*4)==0u,1); __builtin_amdgcn_s_sleep(CGATE);
    __builtin_amdgcn_s_setprio(CVP);
    TS(3);
    f32x16 ac0, ac1;
    {
      asm volatile("s_nop 1\n\t" CV_ASM
        : "=&{v[128:143]}"(ac0), "=&{v[144:159]}"(ac1)
          , "+{v[44:47]}"(qv[0]), "+{v[48:51]}"(qv[1]), "+{v[52:55]}"(qv[2]), "+{v[56:59]}"(qv[3])
        : "{v[64:79]}"(A0), "{v[80:95]}"(A1), "{v[96:111]}"(A2), "{v[112:127]}"(A3),
          "{v[24:31]}"(RB), "{v32}"(bidx), "{v33}"(sb0+SYN+80+w*4)
          , "{v42}"((256*(g&1)+16*fr+4*(g&2))*2), [yb]"s"(ws+WS_Y+d*4096)
          , "{v43}"((l>>3)*8192+(l&7)*16), [q0]"s"(qp), [q2]"s"(qp+4096)
        : "memory", "scc", CV_CLOB);
    }
    TS(4);
    __builtin_amdgcn_s_setprio(3);
    publish(sb0+SYN+24, flags+F_Y+vb, l*1024, tag);
    asm volatile("" : "+v"(qv[0]), "+v"(qv[1]), "+v"(qv[2]), "+v"(qv[3]));
    TS(5);
    float qf[32];
    #pragma unroll
    for(int i=0;i<16;i++){ qf[2*i]=bflo(qv[i>>2][i&3]); qf[2*i+1]=bfhi(qv[i>>2][i&3]); }
    unsigned ya=crs+(g*8+tq)*64+tp*8, wa0=crs+fr*128+((g^(fr&7))*16), wa1=crs+fr*128+(((4+g)^(fr&7))*16), ra[4], oo=(unsigned)((l>>3)*16384+(l&7)*16), yo2=(l>>2)*4096+(l&3)*16;
    #pragma unroll
    for(int i=0;i<4;i++){ const int row=4*(l>>3)+i; ra[i]=crs+row*128+(((l&7)^(row&7))*16); }
    char* op=out+fs0*4096+fd0*2; char* op2=op+8192; const char* yg=ws+WS_Y+fd0*4096+fs0*2;
    #pragma unroll
    for(int i=0;i<32;i++) asm volatile("" : "+v"(qf[i]));
    asm volatile("" : "+v"(ya), "+v"(wa0), "+v"(wa1), "+v"(ra[0]), "+v"(ra[1]), "+v"(ra[2]), "+v"(ra[3]), "+v"(oo), "+v"(yo2));
    asm volatile("" : "+s"(op), "+s"(op2), "+s"(yg));
    if(w==0){
      SPIN(rd1(syn+24)<4u,1);
      SPIN(rd1(syn+96)==0u,2);
      { int sp=0;
        while(true){
          unsigned v=tag; asm volatile("s_mov_b64 exec, 0xffff\n\tglobal_atomic_or %0, %1, %3, %2 sc0 sc1\n\ts_mov_b64 exec, -1\n\ts_waitcnt vmcnt(0)" : "+&v"(v) : "v"(l*4), "s"(flags+F_Y+fb*16), "v"(0u) : "memory");
          if(__all(v==tag) || ++sp>(1<<21)) break;
          __builtin_amdgcn_s_sleep(YSLP);
        } }
      PUB(sb0+(l==0? SYN+28 : SINK+l*4), 1u);
    } else SPIN(rd1(syn+28)==0u,YS2);
    #pragma unroll
    for(int i=0;i<4;i++) gldsc(yg+i*65536, yo2, crs+i*1024);
    {
      u32x4 y4[4];
      asm volatile("s_waitcnt vmcnt(0)\n\t"
        "ds_read_b64_tr_b16 v[0:1], %4\n\tds_read_b64_tr_b16 v[2:3], %4 offset:256\n\tds_read_b64_tr_b16 v[4:5], %4 offset:32\n\tds_read_b64_tr_b16 v[6:7], %4 offset:288\n\t"
        "ds_read_b64_tr_b16 v[8:9], %4 offset:2048\n\tds_read_b64_tr_b16 v[10:11], %4 offset:2304\n\tds_read_b64_tr_b16 v[12:13], %4 offset:2080\n\tds_read_b64_tr_b16 v[14:15], %4 offset:2336\n\t"
        "s_waitcnt lgkmcnt(6)\n\tds_write_b128 %5, v[0:3]\n\ts_waitcnt lgkmcnt(5)\n\tds_write_b128 %5, v[4:7] offset:2048\n\t"
        "s_waitcnt lgkmcnt(4)\n\tds_write_b128 %6, v[8:11]\n\ts_waitcnt lgkmcnt(3)\n\tds_write_b128 %6, v[12:15] offset:2048\n\t"
        "ds_read_b128 %0, %7\n\tds_read_b128 %1, %8\n\tds_read_b128 %2, %9\n\tds_read_b128 %3, %10\n\ts_waitcnt lgkmcnt(0)"
        : "=&v"(y4[0]), "=&v"(y4[1]), "=&v"(y4[2]), "=&v"(y4[3])
        : "v"(ya), "v"(wa0), "v"(wa1), "v"(ra[0]), "v"(ra[1]), "v"(ra[2]), "v"(ra[3])
        : "memory", "v0", "v1", "v2", "v3", "v4", "v5", "v6", "v7", "v8", "v9", "v10", "v11", "v12", "v13", "v14", "v15");
      #pragma unroll
      for(int i=0;i<4;i++){
        u32x4 ov;
        #pragma unroll
        for(int e=0;e<4;e++) ov[e]=pack2(mul1(qf[8*i+2*e],bflo(y4[i][e])), mul1(qf[8*i+2*e+1],bfhi(y4[i][e])));
        stout((i&2? op2:op)+(i&1)*4096, oo, ov);
      }
    }
    ENDW TS(6); TOUT(7);
    __builtin_amdgcn_endpgm();
  }
}

extern "C" void kernel_launch(void* const* d_in, const int* in_sizes, int n_in, void* d_out, int out_size, void* d_ws, size_t ws_size, hipStream_t stream){
  const u16* q=(const u16*)d_in[0]; const u16* k=(const u16*)d_in[1]; const u16* v=(const u16*)d_in[2];
  const u16* shk=(const u16*)d_in[3]; const u16* hk=(const u16*)d_in[4]; const u16* sD=(const u16*)d_in[5];
  const u16* x=(const u16*)d_in[6]; const u16* wqkv=(const u16*)d_in[7]; const u16* wo=(const u16*)d_in[8];
  fused_kernel<<<256,768,0,stream>>>(x,wqkv,wo,(char*)d_ws,(u16*)d_out,k,v,shk,hk,q,sD);
}
```

```cpp
#include <hip/hip_runtime.h>
#include <hip/hip_bf16.h>
#include <stdint.h>

typedef unsigned short u16;
typedef short bf16x8 __attribute__((ext_vector_type(8)));
typedef short s16x4 __attribute__((ext_vector_type(4)));
typedef float f32x2 __attribute__((ext_vector_type(2)));
typedef float f32x4 __attribute__((ext_vector_type(4)));
typedef float f32x8 __attribute__((ext_vector_type(8)));
typedef float f32x16 __attribute__((ext_vector_type(16)));
typedef unsigned u32x4 __attribute__((ext_vector_type(4)));
typedef unsigned u32x2 __attribute__((ext_vector_type(2)));
typedef __bf16 bf16x2_t __attribute__((ext_vector_type(2)));
#define LDS_AS __attribute__((address_space(3)))

__device__ __forceinline__ float bflo(unsigned x){ return __uint_as_float(x<<16); }
__device__ __forceinline__ float bfhi(unsigned x){ return __uint_as_float(x&0xffff0000u); }
__device__ __forceinline__ unsigned pack2(float lo, float hi){ return __builtin_bit_cast(unsigned, __builtin_convertvector(f32x2{lo,hi}, bf16x2_t)); }
__device__ __forceinline__ float mul1(float a, float b){ float r; asm("v_mul_f32 %0, %1, %2" : "=v"(r) : "v"(a), "v"(b)); return r; }
__device__ __forceinline__ float fma1(float a, float b, float c){ float r; asm("v_fma_f32 %0, %1, %2, %3" : "=v"(r) : "v"(a), "v"(b), "v"(c)); return r; }

__device__ __forceinline__ void glds(const void* sbase, unsigned voff, unsigned ldsaddr){
  asm volatile("s_mov_b32 m0, %2\n\ts_nop 0\n\tglobal_load_lds_dwordx4 %0, %1" :: "v"(voff), "s"(sbase), "s"(ldsaddr) : "memory");
}
__device__ __forceinline__ void gldsc(const void* sbase, unsigned voff, unsigned ldsaddr){
  asm volatile("s_mov_b32 m0, %2\n\ts_nop 0\n\tglobal_load_lds_dwordx4 %0, %1 sc1" :: "v"(voff), "s"(sbase), "s"(ldsaddr) : "memory");
}
__device__ __forceinline__ void touch(const void* sbase, unsigned voff, unsigned ldsaddr){
  asm volatile("s_mov_b32 m0, %2\n\ts_nop 0\n\tglobal_load_lds_dword %0, %1" :: "v"(voff), "s"(sbase), "s"(ldsaddr) : "memory");
}
__device__ __forceinline__ void st16c(void* p, u32x4 v){ asm volatile("global_store_dwordx4 %0, %1, off sc1\n\ts_nop 1" :: "v"(p), "v"(v) : "memory"); }
__device__ __forceinline__ void st16cs(void* sbase, unsigned voff, u32x4 v){ asm volatile("global_store_dwordx4 %0, %1, %2 sc1\n\ts_nop 1" :: "v"(voff), "v"(v), "s"(sbase) : "memory"); }
#define GLB_AS __attribute__((address_space(1)))
__device__ __forceinline__ unsigned ldflag(const unsigned* p){ return __hip_atomic_load((const GLB_AS unsigned*)p, __ATOMIC_RELAXED, __HIP_MEMORY_SCOPE_AGENT); }
__device__ __forceinline__ void stflag(unsigned* p, unsigned v){ __hip_atomic_store((GLB_AS unsigned*)p, v, __ATOMIC_RELAXED, __HIP_MEMORY_SCOPE_AGENT); }
template<int SLP> __device__ __forceinline__ void wait_flag(const unsigned* p, unsigned tag){
  int spins=0;
  while(true){ unsigned v=ldflag(p); if(__all(v==tag)) break; __builtin_amdgcn_s_sleep(SLP); if(++spins>(1<<21)) break; }
}
__device__ __forceinline__ unsigned lds_addr(const void* p){ return (unsigned)(size_t)(const LDS_AS void*)p; }
#define WAITV(n) asm volatile("s_waitcnt vmcnt(" #n ")":::"memory")
#define WAITL() asm volatile("s_waitcnt lgkmcnt(0)":::"memory")
#define SBAR() asm volatile("s_barrier":::"memory")
#define PUBM(addr,val) asm volatile("ds_max_u32 %0, %1" :: "v"(addr), "v"(val) : "memory")
#define PUB(addr,val) asm volatile("ds_write_b32 %0, %1" :: "v"(addr), "v"(val) : "memory")
__device__ __forceinline__ s16x4 trread(const char* p){ return __builtin_amdgcn_ds_read_tr16_b64_v4i16((LDS_AS s16x4*)p); }
__device__ __forceinline__ bf16x8 cat44(s16x4 a, s16x4 b){ return __builtin_shufflevector(a,b,0,1,2,3,4,5,6,7); }
__device__ __forceinline__ int wswz(int row){ return ((row>>1)&1) | (((row>>3)&1)<<1); }
__device__ __forceinline__ u32x4 widen(u32x2 a, u32x2 b){
  auto r0=__builtin_amdgcn_permlane16_swap(a[0],b[0],false,false);
  auto r1=__builtin_amdgcn_permlane16_swap(a[1],b[1],false,false);
  return u32x4{r0[0],r1[0],r0[1],r1[1]};
}

constexpr int S=2048, DM=1024;
constexpr int WS=12288, XS=18432, X_OFF=6*WS;
constexpr int CREG=4608, C_OFF=131072;
constexpr int EX_OFF=C_OFF+4*CREG;
constexpr int SYN=EX_OFF+8192;
constexpr int SINK=SYN+256;
constexpr int ALL_LDS=SINK+768;
constexpr int F_PP=1024, F_O=4096, F_Y=8192;
#define KV0 1
#define KVS 1
#define LPR 2
#define CPR 2
#define GPR 1
#define CGATE 2
#define OSLP 2
#define YSLP 8
#define TPX 16
#define TPW 2
#define TWO 0
#define CVP 1
#define G2P 3
#define OUTC 1
#define ROT 0
#define WTL 2
#define PB2 0
#define PREM 5
#define KTH 2
#define YDEF 3
#define OPOL 1
#define CODEL 270
#define MIX 0
#define LOCA 3
#define XLOC 1
#define PGT 0
#define PGO 0
#define QREM 0
#define EB 0
#define TW0 1
#define ORDM 1
#define NF 7u
#define NPOL 1
#define PD0 0
#define PD1 4
#define HH 0
#define HW 3
#define HKV 4
#define YPOL 1
#define YS2 2
#define OSH 4
#define HSK 3
#define HGR 2
#define KSH 0
#define EPI 0
#define YFL 0
#define HLAST 1

#define N2_ASM \
  "v_mov_b64 v[64:65], 0\n\t" \
  "v_mov_b64 v[66:67], 0\n\t" \
  "v_mov_b64 v[68:69], 0\n\t" \
  "v_mov_b64 v[70:71], 0\n\t" \
  "v_mov_b64 v[72:73], 0\n\t" \
  "v_mov_b64 v[74:75], 0\n\t" \
  "v_mov_b64 v[76:77], 0\n\t" \
  "v_mov_b64 v[78:79], 0\n\t" \
  "v_mov_b64 v[80:81], 0\n\t" \
  "v_mov_b64 v[82:83], 0\n\t" \
  "v_mov_b64 v[84:85], 0\n\t" \
  "v_mov_b64 v[86:87], 0\n\t" \
  "v_mov_b64 v[88:89], 0\n\t" \
  "v_mov_b64 v[90:91], 0\n\t" \
  "v_mov_b64 v[92:93], 0\n\t" \
  "v_mov_b64 v[94:95], 0\n\t" \
  "v_mov_b64 v[96:97], 0\n\t" \
  "v_mov_b64 v[98:99], 0\n\t" \
  "v_mov_b64 v[100:101], 0\n\t" \
  "v_mov_b64 v[102:103], 0\n\t" \
  "v_mov_b64 v[104:105], 0\n\t" \
  "v_mov_b64 v[106:107], 0\n\t" \
  "v_mov_b64 v[108:109], 0\n\t" \
  "v_mov_b64 v[110:111], 0\n\t" \
  "v_mov_b64 v[112:113], 0\n\t" \
  "v_mov_b64 v[114:115], 0\n\t" \
  "v_mov_b64 v[116:117], 0\n\t" \
  "v_mov_b64 v[118:119], 0\n\t" \
  "v_mov_b64 v[120:121], 0\n\t" \
  "v_mov_b64 v[122:123], 0\n\t" \
  "v_mov_b64 v[124:125], 0\n\t" \
  "v_mov_b64 v[126:127], 0\n\t" \
  "v_mov_b64 v[128:129], 0\n\t" \
  "v_mov_b64 v[130:131], 0\n\t" \
  "v_mov_b64 v[132:133], 0\n\t" \
  "v_mov_b64 v[134:135], 0\n\t" \
  "v_mov_b64 v[136:137], 0\n\t" \
  "v_mov_b64 v[138:139], 0\n\t" \
  "v_mov_b64 v[140:141], 0\n\t" \
  "v_mov_b64 v[142:143], 0\n\t" \
  "v_mov_b64 v[144:145], 0\n\t" \
  "v_mov_b64 v[146:147], 0\n\t" \
  "v_mov_b64 v[148:149], 0\n\t" \
  "v_mov_b64 v[150:151], 0\n\t" \
  "v_mov_b64 v[152:153], 0\n\t" \
  "v_mov_b64 v[154:155], 0\n\t" \
  "v_mov_b64 v[156:157], 0\n\t" \
  "v_mov_b64 v[158:159], 0\n\t" \
  "v_mov_b64 v[160:161], 0\n\t" \
  "v_mov_b64 v[162:163], 0\n\t" \
  "v_mov_b64 v[164:165], 0\n\t" \
  "v_mov_b64 v[166:167], 0\n\t" \
  "v_add_u32 v60, 36864, v58\n\t" \
  "v_add_u32 v61, 36864, v59\n\t" \
  "s_mov_b32 %[it], 0\n\t" \
  "s_cmp_lg_u32 %[r], 0\n\t" \
  "s_barrier\n\t" \
  "s_cbranch_scc1 NB_%=\n\t" \
  "ds_read_b64_tr_b16 v[48:49], v60 offset:14336\n\t" \
  "ds_read_b64_tr_b16 v[50:51], v60 offset:14848\n\t" \
  "ds_read_b128 v[0:3], v56 offset:38912\n\t" \
  "ds_read_b64_tr_b16 v[52:53], v60 offset:26624\n\t" \
  "ds_read_b64_tr_b16 v[54:55], v60 offset:27136\n\t" \
  "ds_read_b128 v[16:19], v57 offset:38912\n\t" \
  "ds_read_b128 v[4:7], v56 offset:40960\n\t" \
  "ds_read_b128 v[20:23], v57 offset:40960\n\t" \
  "ds_read_b128 v[8:11], v56 offset:43008\n\t" \
  "ds_read_b128 v[24:27], v57 offset:43008\n\t" \
  "ds_read_b128 v[12:15], v56 offset:45056\n\t" \
  "ds_read_b128 v[28:31], v57 offset:45056\n\t" \
  "ds_read_b64_tr_b16 v[32:33], v60 offset:13312\n\t" \
  "ds_read_b64_tr_b16 v[34:35], v60 offset:13824\n\t" \
  "s_mov_b32 s68, 0x50000\n\t" \
  "s_mov_b32 s69, 0x5550055\n\t" \
  "s_mov_b32 s70, 0xa0000\n\t" \
  "s_mov_b32 s71, 0xaaa00aa\n\t" \
  "s_mov_b32 s72, 0x150001\n\t" \
  "s_mov_b32 s73, 0x15550155\n\t" \
  "s_mov_b32 s74, 0x2a0002\n\t" \
  "s_mov_b32 s75, 0x2aaa02aa\n\t" \
  "s_mov_b32 s76, 0x55505555\n\t" \
  "s_mov_b32 s77, 0x50005500\n\t" \
  "s_mov_b32 s78, 0xaaa0aaaa\n\t" \
  "s_mov_b32 s79, 0xa000aa00\n\t" \
  "s_mov_b32 s80, 0x55405554\n\t" \
  "s_mov_b32 s81, 0x40005400\n\t" \
  "s_mov_b32 s82, 0xaa80aaa8\n\t" \
  "s_mov_b32 s83, 0x8000a800\n\t" \
  "s_mov_b32 s84, 0xaaaaaaaa\n\t" \
  "s_mov_b32 s85, 0xaaaaaaaa\n\t" \
  "s_waitcnt lgkmcnt(0)\n\t" \
  "s_branch N2a_%=\n\t" \
  "N0a_%=:\n\t" \
  "s_waitcnt lgkmcnt(10)\n\t" \
  "v_mfma_f32_16x16x32_bf16 v[128:131], v[0:3], v[32:35], v[128:131]\n\t" \
  "ds_read_b64_tr_b16 v[44:45], v58 offset:13312\n\t" \
  "s_waitcnt lgkmcnt(7)\n\t" \
  "v_mfma_f32_16x16x32_bf16 v[128:131], v[16:19], v[36:39], v[128:131]\n\t" \
  "ds_read_b64_tr_b16 v[46:47], v58 offset:13824\n\t" \
  "v_mfma_f32_16x16x32_bf16 v[136:139], v[4:7], v[32:35], v[136:139]\n\t" \
  "ds_read_b128 v[48:51], v56 offset:0\n\t" \
  "v_mfma_f32_16x16x32_bf16 v[136:139], v[20:23], v[36:39], v[136:139]\n\t" \
  "ds_read_b128 v[52:55], v57 offset:0\n\t" \
  "s_waitcnt lgkmcnt(8)\n\t" \
  "v_mfma_f32_16x16x32_bf16 v[144:147], v[8:11], v[32:35], v[144:147]\n\t" \
  "v_mfma_f32_16x16x32_bf16 v[144:147], v[24:27], v[36:39], v[144:147]\n\t" \
  "s_waitcnt lgkmcnt(4)\n\t" \
  "v_mfma_f32_16x16x32_bf16 v[152:155], v[12:15], v[32:35], v[152:155]\n\t" \
  "v_mfma_f32_16x16x32_bf16 v[152:155], v[28:31], v[36:39], v[152:155]\n\t" \
  "ds_read_b64_tr_b16 v[32:33], v59 offset:1024\n\t" \
  "v_mfma_f32_16x16x32_bf16 v[96:99], v[40:43], v[0:3], v[96:99]\n\t" \
  "ds_read_b64_tr_b16 v[34:35], v59 offset:1536\n\t" \
  "s_waitcnt lgkmcnt(4)\n\t" \
  "v_mfma_f32_16x16x32_bf16 v[96:99], v[44:47], v[16:19], v[96:99]\n\t" \
  "ds_read_b64_tr_b16 v[36:37], v59 offset:13312\n\t" \
  "v_mfma_f32_16x16x32_bf16 v[104:107], v[40:43], v[4:7], v[104:107]\n\t" \
  "v_mfma_f32_16x16x32_bf16 v[104:107], v[44:47], v[20:23], v[104:107]\n\t" \
  "ds_read_b64_tr_b16 v[38:39], v59 offset:13824\n\t" \
  "v_mfma_f32_16x16x32_bf16 v[112:115], v[40:43], v[8:11], v[112:115]\n\t" \
  "v_mfma_f32_16x16x32_bf16 v[112:115], v[44:47], v[24:27], v[112:115]\n\t" \
  "v_mfma_f32_16x16x32_bf16 v[120:123], v[40:43], v[12:15], v[120:123]\n\t" \
  "v_mfma_f32_16x16x32_bf16 v[120:123], v[44:47], v[28:31], v[120:123]\n\t" \
  "s_waitcnt lgkmcnt(4)\n\t" \
  "v_mfma_f32_16x16x32_bf16 v[160:163], v[40:43], v[48:51], v[160:163]\n\t" \
  "v_mfma_f32_16x16x32_bf16 v[160:163], v[44:47], v[52:55], v[160:163]\n\t" \
  "ds_read_b64_tr_b16 v[40:41], v59 offset:2048\n\t" \
  "s_waitcnt lgkmcnt(1)\n\t" \
  "v_mfma_f32_16x16x32_bf16 v[164:167], v[32:35], v[48:51], v[164:167]\n\t" \
  "ds_read_b64_tr_b16 v[42:43], v59 offset:2560\n\t" \
  "v_mfma_f32_16x16x32_bf16 v[164:167], v[36:39], v[52:55], v[164:167]\n\t" \
  "ds_read_b64_tr_b16 v[44:45], v59 offset:14336\n\t" \
  "v_mfma_f32_16x16x32_bf16 v[100:103], v[32:35], v[0:3], v[100:103]\n\t" \
  "ds_read_b64_tr_b16 v[46:47], v59 offset:14848\n\t" \
  "v_mfma_f32_16x16x32_bf16 v[100:103], v[36:39], v[16:19], v[100:103]\n\t" \
  "ds_read_b64_tr_b16 v[48:49], v58 offset:0\n\t" \
  "v_mfma_f32_16x16x32_bf16 v[108:111], v[32:35], v[4:7], v[108:111]\n\t" \
  "ds_read_b64_tr_b16 v[50:51], v58 offset:512\n\t" \
  "v_mfma_f32_16x16x32_bf16 v[108:111], v[36:39], v[20:23], v[108:111]\n\t" \
  "ds_read_b64_tr_b16 v[52:53], v58 offset:12288\n\t" \
  "v_mfma_f32_16x16x32_bf16 v[116:119], v[32:35], v[8:11], v[116:119]\n\t" \
  "ds_read_b64_tr_b16 v[54:55], v58 offset:12800\n\t" \
  "v_mfma_f32_16x16x32_bf16 v[116:119], v[36:39], v[24:27], v[116:119]\n\t" \
  "v_mfma_f32_16x16x32_bf16 v[124:127], v[32:35], v[12:15], v[124:127]\n\t" \
  "ds_read_b64_tr_b16 v[32:33], v59 offset:0\n\t" \
  "v_mfma_f32_16x16x32_bf16 v[124:127], v[36:39], v[28:31], v[124:127]\n\t" \
  "ds_read_b64_tr_b16 v[34:35], v59 offset:512\n\t" \
  "s_waitcnt lgkmcnt(6)\n\t" \
  "v_mfma_f32_16x16x32_bf16 v[132:135], v[0:3], v[40:43], v[132:135]\n\t" \
  "ds_read_b64_tr_b16 v[36:37], v59 offset:12288\n\t" \
  "v_mfma_f32_16x16x32_bf16 v[132:135], v[16:19], v[44:47], v[132:135]\n\t" \
  "ds_read_b64_tr_b16 v[38:39], v59 offset:12800\n\t" \
  "v_mfma_f32_16x16x32_bf16 v[140:143], v[4:7], v[40:43], v[140:143]\n\t" \
  "v_mfma_f32_16x16x32_bf16 v[140:143], v[20:23], v[44:47], v[140:143]\n\t" \
  "v_mfma_f32_16x16x32_bf16 v[148:151], v[8:11], v[40:43], v[148:151]\n\t" \
  "v_mfma_f32_16x16x32_bf16 v[148:151], v[24:27], v[44:47], v[148:151]\n\t" \
  "v_mfma_f32_16x16x32_bf16 v[156:159], v[12:15], v[40:43], v[156:159]\n\t" \
  "v_mfma_f32_16x16x32_bf16 v[156:159], v[28:31], v[44:47], v[156:159]\n\t" \
  "s_waitcnt lgkmcnt(0)\n\t" \
  "s_barrier\n\t" \
  "v_mfma_f32_16x16x32_bf16 v[64:67], v[48:51], v[0:3], v[64:67]\n\t" \
  "v_mfma_f32_16x16x32_bf16 v[64:67], v[52:55], v[16:19], v[64:67]\n\t" \
  "ds_read_b64_tr_b16 v[40:41], v58 offset:26624\n\t" \
  "v_mfma_f32_16x16x32_bf16 v[68:71], v[32:35], v[0:3], v[68:71]\n\t" \
  "ds_read_b128 v[0:3], v56 offset:20480\n\t" \
  "v_mfma_f32_16x16x32_bf16 v[68:71], v[36:39], v[16:19], v[68:71]\n\t" \
  "ds_read_b128 v[16:19], v57 offset:20480\n\t" \
  "v_mfma_f32_16x16x32_bf16 v[72:75], v[48:51], v[4:7], v[72:75]\n\t" \
  "ds_read_b64_tr_b16 v[42:43], v58 offset:27136\n\t" \
  "v_mfma_f32_16x16x32_bf16 v[72:75], v[52:55], v[20:23], v[72:75]\n\t" \
  "ds_read_b64_tr_b16 v[44:45], v60 offset:2048\n\t" \
  "v_mfma_f32_16x16x32_bf16 v[76:79], v[32:35], v[4:7], v[76:79]\n\t" \
  "ds_read_b128 v[4:7], v56 offset:22528\n\t" \
  "v_mfma_f32_16x16x32_bf16 v[76:79], v[36:39], v[20:23], v[76:79]\n\t" \
  "ds_read_b128 v[20:23], v57 offset:22528\n\t" \
  "v_mfma_f32_16x16x32_bf16 v[80:83], v[48:51], v[8:11], v[80:83]\n\t" \
  "ds_read_b64_tr_b16 v[46:47], v60 offset:2560\n\t" \
  "v_mfma_f32_16x16x32_bf16 v[80:83], v[52:55], v[24:27], v[80:83]\n\t" \
  "v_mfma_f32_16x16x32_bf16 v[84:87], v[32:35], v[8:11], v[84:87]\n\t" \
  "ds_read_b128 v[8:11], v56 offset:24576\n\t" \
  "v_mfma_f32_16x16x32_bf16 v[84:87], v[36:39], v[24:27], v[84:87]\n\t" \
  "ds_read_b128 v[24:27], v57 offset:24576\n\t" \
  "v_mfma_f32_16x16x32_bf16 v[88:91], v[48:51], v[12:15], v[88:91]\n\t" \
  "ds_read_b64_tr_b16 v[48:49], v58 offset:25600\n\t" \
  "v_mfma_f32_16x16x32_bf16 v[88:91], v[52:55], v[28:31], v[88:91]\n\t" \
  "ds_read_b64_tr_b16 v[50:51], v58 offset:26112\n\t" \
  "v_mfma_f32_16x16x32_bf16 v[92:95], v[32:35], v[12:15], v[92:95]\n\t" \
  "ds_read_b128 v[12:15], v56 offset:26624\n\t" \
  "v_mfma_f32_16x16x32_bf16 v[92:95], v[36:39], v[28:31], v[92:95]\n\t" \
  "ds_read_b128 v[28:31], v57 offset:26624\n\t" \
  "N1a_%=:\n\t" \
  "s_waitcnt lgkmcnt(10)\n\t" \
  "v_mfma_f32_16x16x32_bf16 v[128:131], v[0:3], v[40:43], v[128:131]\n\t" \
  "ds_read_b64_tr_b16 v[52:53], v60 offset:1024\n\t" \
  "s_waitcnt lgkmcnt(7)\n\t" \
  "v_mfma_f32_16x16x32_bf16 v[128:131], v[16:19], v[44:47], v[128:131]\n\t" \
  "ds_read_b64_tr_b16 v[54:55], v60 offset:1536\n\t" \
  "v_mfma_f32_16x16x32_bf16 v[136:139], v[4:7], v[40:43], v[136:139]\n\t" \
  "ds_read_b128 v[32:35], v56 offset:18432\n\t" \
  "v_mfma_f32_16x16x32_bf16 v[136:139], v[20:23], v[44:47], v[136:139]\n\t" \
  "ds_read_b128 v[36:39], v57 offset:18432\n\t" \
  "s_waitcnt lgkmcnt(8)\n\t" \
  "v_mfma_f32_16x16x32_bf16 v[144:147], v[8:11], v[40:43], v[144:147]\n\t" \
  "v_mfma_f32_16x16x32_bf16 v[144:147], v[24:27], v[44:47], v[144:147]\n\t" \
  "s_waitcnt lgkmcnt(4)\n\t" \
  "v_mfma_f32_16x16x32_bf16 v[152:155], v[12:15], v[40:43], v[152:155]\n\t" \
  "v_mfma_f32_16x16x32_bf16 v[152:155], v[28:31], v[44:47], v[152:155]\n\t" \
  "ds_read_b64_tr_b16 v[40:41], v59 offset:25600\n\t" \
  "v_mfma_f32_16x16x32_bf16 v[96:99], v[48:51], v[0:3], v[96:99]\n\t" \
  "ds_read_b64_tr_b16 v[42:43], v59 offset:26112\n\t" \
  "s_waitcnt lgkmcnt(4)\n\t" \
  "v_mfma_f32_16x16x32_bf16 v[96:99], v[52:55], v[16:19], v[96:99]\n\t" \
  "ds_read_b64_tr_b16 v[44:45], v61 offset:1024\n\t" \
  "v_mfma_f32_16x16x32_bf16 v[104:107], v[48:51], v[4:7], v[104:107]\n\t" \
  "v_mfma_f32_16x16x32_bf16 v[104:107], v[52:55], v[20:23], v[104:107]\n\t" \
  "ds_read_b64_tr_b16 v[46:47], v61 offset:1536\n\t" \
  "v_mfma_f32_16x16x32_bf16 v[112:115], v[48:51], v[8:11], v[112:115]\n\t" \
  "v_mfma_f32_16x16x32_bf16 v[112:115], v[52:55], v[24:27], v[112:115]\n\t" \
  "v_mfma_f32_16x16x32_bf16 v[120:123], v[48:51], v[12:15], v[120:123]\n\t" \
  "v_mfma_f32_16x16x32_bf16 v[120:123], v[52:55], v[28:31], v[120:123]\n\t" \
  "s_waitcnt lgkmcnt(4)\n\t" \
  "v_mfma_f32_16x16x32_bf16 v[160:163], v[48:51], v[32:35], v[160:163]\n\t" \
  "v_mfma_f32_16x16x32_bf16 v[160:163], v[52:55], v[36:39], v[160:163]\n\t" \
  "ds_read_b64_tr_b16 v[48:49], v59 offset:26624\n\t" \
  "s_waitcnt lgkmcnt(1)\n\t" \
  "v_mfma_f32_16x16x32_bf16 v[164:167], v[40:43], v[32:35], v[164:167]\n\t" \
  "ds_read_b64_tr_b16 v[50:51], v59 offset:27136\n\t" \
  "v_mfma_f32_16x16x32_bf16 v[164:167], v[44:47], v[36:39], v[164:167]\n\t" \
  "ds_read_b64_tr_b16 v[52:53], v61 offset:2048\n\t" \
  "v_mfma_f32_16x16x32_bf16 v[100:103], v[40:43], v[0:3], v[100:103]\n\t" \
  "ds_read_b64_tr_b16 v[54:55], v61 offset:2560\n\t" \
  "v_mfma_f32_16x16x32_bf16 v[100:103], v[44:47], v[16:19], v[100:103]\n\t" \
  "ds_read_b64_tr_b16 v[32:33], v58 offset:24576\n\t" \
  "v_mfma_f32_16x16x32_bf16 v[108:111], v[40:43], v[4:7], v[108:111]\n\t" \
  "ds_read_b64_tr_b16 v[34:35], v58 offset:25088\n\t" \
  "v_mfma_f32_16x16x32_bf16 v[108:111], v[44:47], v[20:23], v[108:111]\n\t" \
  "ds_read_b64_tr_b16 v[36:37], v60 offset:0\n\t" \
  "v_mfma_f32_16x16x32_bf16 v[116:119], v[40:43], v[8:11], v[116:119]\n\t" \
  "ds_read_b64_tr_b16 v[38:39], v60 offset:512\n\t" \
  "v_mfma_f32_16x16x32_bf16 v[116:119], v[44:47], v[24:27], v[116:119]\n\t" \
  "v_mfma_f32_16x16x32_bf16 v[124:127], v[40:43], v[12:15], v[124:127]\n\t" \
  "ds_read_b64_tr_b16 v[40:41], v59 offset:24576\n\t" \
  "v_mfma_f32_16x16x32_bf16 v[124:127], v[44:47], v[28:31], v[124:127]\n\t" \
  "ds_read_b64_tr_b16 v[42:43], v59 offset:25088\n\t" \
  "s_waitcnt lgkmcnt(6)\n\t" \
  "v_mfma_f32_16x16x32_bf16 v[132:135], v[0:3], v[48:51], v[132:135]\n\t" \
  "ds_read_b64_tr_b16 v[44:45], v61 offset:0\n\t" \
  "v_mfma_f32_16x16x32_bf16 v[132:135], v[16:19], v[52:55], v[132:135]\n\t" \
  "ds_read_b64_tr_b16 v[46:47], v61 offset:512\n\t" \
  "v_mfma_f32_16x16x32_bf16 v[140:143], v[4:7], v[48:51], v[140:143]\n\t" \
  "v_mfma_f32_16x16x32_bf16 v[140:143], v[20:23], v[52:55], v[140:143]\n\t" \
  "v_mfma_f32_16x16x32_bf16 v[148:151], v[8:11], v[48:51], v[148:151]\n\t" \
  "v_mfma_f32_16x16x32_bf16 v[148:151], v[24:27], v[52:55], v[148:151]\n\t" \
  "v_mfma_f32_16x16x32_bf16 v[156:159], v[12:15], v[48:51], v[156:159]\n\t" \
  "v_mfma_f32_16x16x32_bf16 v[156:159], v[28:31], v[52:55], v[156:159]\n\t" \
  "s_waitcnt lgkmcnt(0)\n\t" \
  "s_barrier\n\t" \
  "v_mfma_f32_16x16x32_bf16 v[64:67], v[32:35], v[0:3], v[64:67]\n\t" \
  "v_mfma_f32_16x16x32_bf16 v[64:67], v[36:39], v[16:19], v[64:67]\n\t" \
  "ds_read_b64_tr_b16 v[48:49], v60 offset:14336\n\t" \
  "v_mfma_f32_16x16x32_bf16 v[68:71], v[40:43], v[0:3], v[68:71]\n\t" \
  "ds_read_b128 v[0:3], v56 offset:38912\n\t" \
  "v_mfma_f32_16x16x32_bf16 v[68:71], v[44:47], v[16:19], v[68:71]\n\t" \
  "ds_read_b128 v[16:19], v57 offset:38912\n\t" \
  "v_mfma_f32_16x16x32_bf16 v[72:75], v[32:35], v[4:7], v[72:75]\n\t" \
  "ds_read_b64_tr_b16 v[50:51], v60 offset:14848\n\t" \
  "v_mfma_f32_16x16x32_bf16 v[72:75], v[36:39], v[20:23], v[72:75]\n\t" \
  "ds_read_b64_tr_b16 v[52:53], v60 offset:26624\n\t" \
  "v_mfma_f32_16x16x32_bf16 v[76:79], v[40:43], v[4:7], v[76:79]\n\t" \
  "ds_read_b128 v[4:7], v56 offset:40960\n\t" \
  "v_mfma_f32_16x16x32_bf16 v[76:79], v[44:47], v[20:23], v[76:79]\n\t" \
  "ds_read_b128 v[20:23], v57 offset:40960\n\t" \
  "v_mfma_f32_16x16x32_bf16 v[80:83], v[32:35], v[8:11], v[80:83]\n\t" \
  "ds_read_b64_tr_b16 v[54:55], v60 offset:27136\n\t" \
  "v_mfma_f32_16x16x32_bf16 v[80:83], v[36:39], v[24:27], v[80:83]\n\t" \
  "v_mfma_f32_16x16x32_bf16 v[84:87], v[40:43], v[8:11], v[84:87]\n\t" \
  "ds_read_b128 v[8:11], v56 offset:43008\n\t" \
  "v_mfma_f32_16x16x32_bf16 v[84:87], v[44:47], v[24:27], v[84:87]\n\t" \
  "ds_read_b128 v[24:27], v57 offset:43008\n\t" \
  "v_mfma_f32_16x16x32_bf16 v[88:91], v[32:35], v[12:15], v[88:91]\n\t" \
  "ds_read_b64_tr_b16 v[32:33], v60 offset:13312\n\t" \
  "v_mfma_f32_16x16x32_bf16 v[88:91], v[36:39], v[28:31], v[88:91]\n\t" \
  "ds_read_b64_tr_b16 v[34:35], v60 offset:13824\n\t" \
  "v_mfma_f32_16x16x32_bf16 v[92:95], v[40:43], v[12:15], v[92:95]\n\t" \
  "ds_read_b128 v[12:15], v56 offset:45056\n\t" \
  "v_mfma_f32_16x16x32_bf16 v[92:95], v[44:47], v[28:31], v[92:95]\n\t" \
  "ds_read_b128 v[28:31], v57 offset:45056\n\t" \
  "N2a_%=:\n\t" \
  "s_waitcnt lgkmcnt(10)\n\t" \
  "v_mfma_f32_16x16x32_bf16 v[128:131], v[0:3], v[48:51], v[128:131]\n\t" \
  "ds_read_b64_tr_b16 v[36:37], v60 offset:25600\n\t" \
  "s_waitcnt lgkmcnt(7)\n\t" \
  "v_mfma_f32_16x16x32_bf16 v[128:131], v[16:19], v[52:55], v[128:131]\n\t" \
  "ds_read_b64_tr_b16 v[38:39], v60 offset:26112\n\t" \
  "v_mfma_f32_16x16x32_bf16 v[136:139], v[4:7], v[48:51], v[136:139]\n\t" \
  "ds_read_b128 v[40:43], v56 offset:36864\n\t" \
  "v_mfma_f32_16x16x32_bf16 v[136:139], v[20:23], v[52:55], v[136:139]\n\t" \
  "ds_read_b128 v[44:47], v57 offset:36864\n\t" \
  "s_waitcnt lgkmcnt(8)\n\t" \
  "v_mfma_f32_16x16x32_bf16 v[144:147], v[8:11], v[48:51], v[144:147]\n\t" \
  "v_mfma_f32_16x16x32_bf16 v[144:147], v[24:27], v[52:55], v[144:147]\n\t" \
  "s_waitcnt lgkmcnt(4)\n\t" \
  "v_mfma_f32_16x16x32_bf16 v[152:155], v[12:15], v[48:51], v[152:155]\n\t" \
  "v_mfma_f32_16x16x32_bf16 v[152:155], v[28:31], v[52:55], v[152:155]\n\t" \
  "ds_read_b64_tr_b16 v[48:49], v61 offset:13312\n\t" \
  "v_mfma_f32_16x16x32_bf16 v[96:99], v[32:35], v[0:3], v[96:99]\n\t" \
  "ds_read_b64_tr_b16 v[50:51], v61 offset:13824\n\t" \
  "s_waitcnt lgkmcnt(4)\n\t" \
  "v_mfma_f32_16x16x32_bf16 v[96:99], v[36:39], v[16:19], v[96:99]\n\t" \
  "ds_read_b64_tr_b16 v[52:53], v61 offset:25600\n\t" \
  "v_mfma_f32_16x16x32_bf16 v[104:107], v[32:35], v[4:7], v[104:107]\n\t" \
  "v_mfma_f32_16x16x32_bf16 v[104:107], v[36:39], v[20:23], v[104:107]\n\t" \
  "ds_read_b64_tr_b16 v[54:55], v61 offset:26112\n\t" \
  "v_mfma_f32_16x16x32_bf16 v[112:115], v[32:35], v[8:11], v[112:115]\n\t" \
  "v_mfma_f32_16x16x32_bf16 v[112:115], v[36:39], v[24:27], v[112:115]\n\t" \
  "v_mfma_f32_16x16x32_bf16 v[120:123], v[32:35], v[12:15], v[120:123]\n\t" \
  "v_mfma_f32_16x16x32_bf16 v[120:123], v[36:39], v[28:31], v[120:123]\n\t" \
  "s_waitcnt lgkmcnt(4)\n\t" \
  "v_mfma_f32_16x16x32_bf16 v[160:163], v[32:35], v[40:43], v[160:163]\n\t" \
  "v_mfma_f32_16x16x32_bf16 v[160:163], v[36:39], v[44:47], v[160:163]\n\t" \
  "ds_read_b64_tr_b16 v[32:33], v61 offset:14336\n\t" \
  "s_waitcnt lgkmcnt(1)\n\t" \
  "v_mfma_f32_16x16x32_bf16 v[164:167], v[48:51], v[40:43], v[164:167]\n\t" \
  "ds_read_b64_tr_b16 v[34:35], v61 offset:14848\n\t" \
  "v_mfma_f32_16x16x32_bf16 v[164:167], v[52:55], v[44:47], v[164:167]\n\t" \
  "ds_read_b64_tr_b16 v[36:37], v61 offset:26624\n\t" \
  "v_mfma_f32_16x16x32_bf16 v[100:103], v[48:51], v[0:3], v[100:103]\n\t" \
  "ds_read_b64_tr_b16 v[38:39], v61 offset:27136\n\t" \
  "v_mfma_f32_16x16x32_bf16 v[100:103], v[52:55], v[16:19], v[100:103]\n\t" \
  "ds_read_b64_tr_b16 v[40:41], v60 offset:12288\n\t" \
  "v_mfma_f32_16x16x32_bf16 v[108:111], v[48:51], v[4:7], v[108:111]\n\t" \
  "ds_read_b64_tr_b16 v[42:43], v60 offset:12800\n\t" \
  "v_mfma_f32_16x16x32_bf16 v[108:111], v[52:55], v[20:23], v[108:111]\n\t" \
  "ds_read_b64_tr_b16 v[44:45], v60 offset:24576\n\t" \
  "v_mfma_f32_16x16x32_bf16 v[116:119], v[48:51], v[8:11], v[116:119]\n\t" \
  "ds_read_b64_tr_b16 v[46:47], v60 offset:25088\n\t" \
  "v_mfma_f32_16x16x32_bf16 v[116:119], v[52:55], v[24:27], v[116:119]\n\t" \
  "v_mfma_f32_16x16x32_bf16 v[124:127], v[48:51], v[12:15], v[124:127]\n\t" \
  "ds_read_b64_tr_b16 v[48:49], v61 offset:12288\n\t" \
  "v_mfma_f32_16x16x32_bf16 v[124:127], v[52:55], v[28:31], v[124:127]\n\t" \
  "ds_read_b64_tr_b16 v[50:51], v61 offset:12800\n\t" \
  "s_waitcnt lgkmcnt(6)\n\t" \
  "v_mfma_f32_16x16x32_bf16 v[132:135], v[0:3], v[32:35], v[132:135]\n\t" \
  "ds_read_b64_tr_b16 v[52:53], v61 offset:24576\n\t" \
  "v_mfma_f32_16x16x32_bf16 v[132:135], v[16:19], v[36:39], v[132:135]\n\t" \
  "ds_read_b64_tr_b16 v[54:55], v61 offset:25088\n\t" \
  "v_mfma_f32_16x16x32_bf16 v[140:143], v[4:7], v[32:35], v[140:143]\n\t" \
  "v_mfma_f32_16x16x32_bf16 v[140:143], v[20:23], v[36:39], v[140:143]\n\t" \
  "s_addk_i32 %[it], 1\n\t" \
  "v_mfma_f32_16x16x32_bf16 v[148:151], v[8:11], v[32:35], v[148:151]\n\t" \
  "s_cmp_lt_u32 %[it], 6\n\t" \
  "v_mfma_f32_16x16x32_bf16 v[148:151], v[24:27], v[36:39], v[148:151]\n\t" \
  "v_mfma_f32_16x16x32_bf16 v[156:159], v[12:15], v[32:35], v[156:159]\n\t" \
  "v_mfma_f32_16x16x32_bf16 v[156:159], v[28:31], v[36:39], v[156:159]\n\t" \
  "s_waitcnt lgkmcnt(0)\n\t" \
  "s_barrier\n\t" \
  "s_cbranch_scc0 NTa_%=\n\t" \
  "v_mfma_f32_16x16x32_bf16 v[64:67], v[40:43], v[0:3], v[64:67]\n\t" \
  "v_mfma_f32_16x16x32_bf16 v[64:67], v[44:47], v[16:19], v[64:67]\n\t" \
  "ds_read_b64_tr_b16 v[32:33], v58 offset:2048\n\t" \
  "v_mfma_f32_16x16x32_bf16 v[68:71], v[48:51], v[0:3], v[68:71]\n\t" \
  "ds_read_b128 v[0:3], v56 offset:2048\n\t" \
  "v_mfma_f32_16x16x32_bf16 v[68:71], v[52:55], v[16:19], v[68:71]\n\t" \
  "ds_read_b128 v[16:19], v57 offset:2048\n\t" \
  "v_mfma_f32_16x16x32_bf16 v[72:75], v[40:43], v[4:7], v[72:75]\n\t" \
  "ds_read_b64_tr_b16 v[34:35], v58 offset:2560\n\t" \
  "v_mfma_f32_16x16x32_bf16 v[72:75], v[44:47], v[20:23], v[72:75]\n\t" \
  "ds_read_b64_tr_b16 v[36:37], v58 offset:14336\n\t" \
  "v_mfma_f32_16x16x32_bf16 v[76:79], v[48:51], v[4:7], v[76:79]\n\t" \
  "ds_read_b128 v[4:7], v56 offset:4096\n\t" \
  "v_mfma_f32_16x16x32_bf16 v[76:79], v[52:55], v[20:23], v[76:79]\n\t" \
  "ds_read_b128 v[20:23], v57 offset:4096\n\t" \
  "v_mfma_f32_16x16x32_bf16 v[80:83], v[40:43], v[8:11], v[80:83]\n\t" \
  "ds_read_b64_tr_b16 v[38:39], v58 offset:14848\n\t" \
  "v_mfma_f32_16x16x32_bf16 v[80:83], v[44:47], v[24:27], v[80:83]\n\t" \
  "v_mfma_f32_16x16x32_bf16 v[84:87], v[48:51], v[8:11], v[84:87]\n\t" \
  "ds_read_b128 v[8:11], v56 offset:6144\n\t" \
  "v_mfma_f32_16x16x32_bf16 v[84:87], v[52:55], v[24:27], v[84:87]\n\t" \
  "ds_read_b128 v[24:27], v57 offset:6144\n\t" \
  "v_mfma_f32_16x16x32_bf16 v[88:91], v[40:43], v[12:15], v[88:91]\n\t" \
  "ds_read_b64_tr_b16 v[40:41], v58 offset:1024\n\t" \
  "v_mfma_f32_16x16x32_bf16 v[88:91], v[44:47], v[28:31], v[88:91]\n\t" \
  "ds_read_b64_tr_b16 v[42:43], v58 offset:1536\n\t" \
  "v_mfma_f32_16x16x32_bf16 v[92:95], v[48:51], v[12:15], v[92:95]\n\t" \
  "ds_read_b128 v[12:15], v56 offset:8192\n\t" \
  "v_mfma_f32_16x16x32_bf16 v[92:95], v[52:55], v[28:31], v[92:95]\n\t" \
  "ds_read_b128 v[28:31], v57 offset:8192\n\t" \
  "s_branch N0a_%=\n\t" \
  "NTa_%=:\n\t" \
  "s_setprio 3\n\t" \
  "v_mfma_f32_16x16x32_bf16 v[88:91], v[40:43], v[12:15], v[88:91]\n\t" \
  "v_cvt_pk_bf16_f32 v120, v120, v121\n\t" \
  "v_cvt_pk_bf16_f32 v121, v122, v123\n\t" \
  "v_mfma_f32_16x16x32_bf16 v[88:91], v[44:47], v[28:31], v[88:91]\n\t" \
  "v_cvt_pk_bf16_f32 v122, v124, v125\n\t" \
  "v_cvt_pk_bf16_f32 v123, v126, v127\n\t" \
  "v_mfma_f32_16x16x32_bf16 v[92:95], v[48:51], v[12:15], v[92:95]\n\t" \
  "v_mov_b32 v57, %[fa]\n\t" \
  "v_mov_b32 v58, 1\n\t" \
  "v_mfma_f32_16x16x32_bf16 v[92:95], v[52:55], v[28:31], v[92:95]\n\t" \
  "ds_write_b128 v63, v[120:123]\n\t" \
  "v_cvt_pk_bf16_f32 v38, v156, v157\n\t" \
  "v_mfma_f32_16x16x32_bf16 v[80:83], v[40:43], v[8:11], v[80:83]\n\t" \
  "v_cvt_pk_bf16_f32 v39, v158, v159\n\t" \
  "v_cvt_pk_bf16_f32 v28, v152, v153\n\t" \
  "v_mfma_f32_16x16x32_bf16 v[80:83], v[44:47], v[24:27], v[80:83]\n\t" \
  "v_cvt_pk_bf16_f32 v29, v154, v155\n\t" \
  "v_mov_b32 v60, 0xff61b1e6\n\t" \
  "v_mfma_f32_16x16x32_bf16 v[84:87], v[48:51], v[8:11], v[84:87]\n\t" \
  "ds_write2_b64 v63, v[28:29], v[38:39] offset0:128 offset1:129\n\t" \
  "s_mov_b64 exec, 1\n\t" \
  "ds_write_b32 v57, v58\n\t" \
  "s_mov_b64 exec, -1\n\t" \
  "v_mfma_f32_16x16x32_bf16 v[84:87], v[52:55], v[24:27], v[84:87]\n\t" \
  "v_cvt_pk_bf16_f32 v160, v160, v161\n\t" \
  "v_cvt_pk_bf16_f32 v161, v162, v163\n\t" \
  "v_mfma_f32_16x16x32_bf16 v[72:75], v[40:43], v[4:7], v[72:75]\n\t" \
  "v_cvt_pk_bf16_f32 v162, v164, v165\n\t" \
  "v_cvt_pk_bf16_f32 v163, v166, v167\n\t" \
  "v_mfma_f32_16x16x32_bf16 v[72:75], v[44:47], v[20:23], v[72:75]\n\t" \
  "v_cvt_pk_bf16_f32 v112, v112, v113\n\t" \
  "v_cvt_pk_bf16_f32 v113, v114, v115\n\t" \
  "v_mfma_f32_16x16x32_bf16 v[76:79], v[48:51], v[4:7], v[76:79]\n\t" \
  "v_cvt_pk_bf16_f32 v114, v116, v117\n\t" \
  "v_cvt_pk_bf16_f32 v115, v118, v119\n\t" \
  "v_mfma_f32_16x16x32_bf16 v[76:79], v[52:55], v[20:23], v[76:79]\n\t" \
  "v_cndmask_b32_e64 v12, v60, 0, s[76:77]\n\t" \
  "v_cndmask_b32_e64 v13, v60, 0, s[78:79]\n\t" \
  "v_mfma_f32_16x16x32_bf16 v[64:67], v[40:43], v[0:3], v[64:67]\n\t" \
  "v_cndmask_b32_e64 v14, v60, 0, s[80:81]\n\t" \
  "v_cndmask_b32_e64 v15, v60, 0, s[82:83]\n\t" \
  "v_mfma_f32_16x16x32_bf16 v[64:67], v[44:47], v[16:19], v[64:67]\n\t" \
  "v_cndmask_b32_e64 v56, v60, 0, s[68:69]\n\t" \
  "v_cndmask_b32_e64 v57, v60, 0, s[70:71]\n\t" \
  "v_mfma_f32_16x16x32_bf16 v[68:71], v[48:51], v[0:3], v[68:71]\n\t" \
  "v_cndmask_b32_e64 v58, v60, 0, s[72:73]\n\t" \
  "v_cndmask_b32_e64 v59, v60, 0, s[74:75]\n\t" \
  "v_mfma_f32_16x16x32_bf16 v[68:71], v[52:55], v[16:19], v[68:71]\n\t" \
  "s_branch NX_%=\n\t" \
  "NB_%=:\n\t" \
  "ds_read_b64_tr_b16 v[48:49], v60 offset:13312\n\t" \
  "ds_read_b64_tr_b16 v[50:51], v60 offset:13824\n\t" \
  "ds_read_b128 v[0:3], v56 offset:47104\n\t" \
  "ds_read_b64_tr_b16 v[52:53], v60 offset:25600\n\t" \
  "ds_read_b64_tr_b16 v[54:55], v60 offset:26112\n\t" \
  "ds_read_b128 v[16:19], v57 offset:47104\n\t" \
  "ds_read_b128 v[4:7], v56 offset:49152\n\t" \
  "ds_read_b128 v[20:23], v57 offset:49152\n\t" \
  "ds_read_b128 v[8:11], v56 offset:51200\n\t" \
  "ds_read_b128 v[24:27], v57 offset:51200\n\t" \
  "ds_read_b128 v[12:15], v56 offset:53248\n\t" \
  "ds_read_b128 v[28:31], v57 offset:53248\n\t" \
  "ds_read_b64_tr_b16 v[32:33], v60 offset:14336\n\t" \
  "ds_read_b64_tr_b16 v[34:35], v60 offset:14848\n\t" \
  "s_mov_b32 s68, 0x50000\n\t" \
  "s_mov_b32 s69, 0x5550055\n\t" \
  "s_mov_b32 s70, 0xa0000\n\t" \
  "s_mov_b32 s71, 0xaaa00aa\n\t" \
  "s_mov_b32 s72, 0x150001\n\t" \
  "s_mov_b32 s73, 0x15550155\n\t" \
  "s_mov_b32 s74, 0x2a0002\n\t" \
  "s_mov_b32 s75, 0x2aaa02aa\n\t" \
  "s_mov_b32 s76, 0x55505555\n\t" \
  "s_mov_b32 s77, 0x50005500\n\t" \
  "s_mov_b32 s78, 0xaaa0aaaa\n\t" \
  "s_mov_b32 s79, 0xa000aa00\n\t" \
  "s_mov_b32 s80, 0x55405554\n\t" \
  "s_mov_b32 s81, 0x40005400\n\t" \
  "s_mov_b32 s82, 0xaa80aaa8\n\t" \
  "s_mov_b32 s83, 0x8000a800\n\t" \
  "s_mov_b32 s84, 0xaaaaaaaa\n\t" \
  "s_mov_b32 s85, 0xaaaaaaaa\n\t" \
  "s_waitcnt lgkmcnt(0)\n\t" \
  "s_branch N2b_%=\n\t" \
  "N0b_%=:\n\t" \
  "s_waitcnt lgkmcnt(10)\n\t" \
  "v_mfma_f32_16x16x32_bf16 v[96:99], v[32:35], v[0:3], v[96:99]\n\t" \
  "ds_read_b64_tr_b16 v[44:45], v58 offset:14336\n\t" \
  "s_waitcnt lgkmcnt(7)\n\t" \
  "v_mfma_f32_16x16x32_bf16 v[96:99], v[36:39], v[16:19], v[96:99]\n\t" \
  "ds_read_b64_tr_b16 v[46:47], v58 offset:14848\n\t" \
  "v_mfma_f32_16x16x32_bf16 v[104:107], v[32:35], v[4:7], v[104:107]\n\t" \
  "ds_read_b128 v[48:51], v56 offset:0\n\t" \
  "v_mfma_f32_16x16x32_bf16 v[104:107], v[36:39], v[20:23], v[104:107]\n\t" \
  "ds_read_b128 v[52:55], v57 offset:0\n\t" \
  "s_waitcnt lgkmcnt(8)\n\t" \
  "v_mfma_f32_16x16x32_bf16 v[112:115], v[32:35], v[8:11], v[112:115]\n\t" \
  "v_mfma_f32_16x16x32_bf16 v[112:115], v[36:39], v[24:27], v[112:115]\n\t" \
  "s_waitcnt lgkmcnt(4)\n\t" \
  "v_mfma_f32_16x16x32_bf16 v[120:123], v[32:35], v[12:15], v[120:123]\n\t" \
  "v_mfma_f32_16x16x32_bf16 v[120:123], v[36:39], v[28:31], v[120:123]\n\t" \
  "ds_read_b64_tr_b16 v[32:33], v59 offset:2048\n\t" \
  "v_mfma_f32_16x16x32_bf16 v[128:131], v[0:3], v[40:43], v[128:131]\n\t" \
  "ds_read_b64_tr_b16 v[34:35], v59 offset:2560\n\t" \
  "s_waitcnt lgkmcnt(4)\n\t" \
  "v_mfma_f32_16x16x32_bf16 v[128:131], v[16:19], v[44:47], v[128:131]\n\t" \
  "ds_read_b64_tr_b16 v[36:37], v59 offset:14336\n\t" \
  "v_mfma_f32_16x16x32_bf16 v[136:139], v[4:7], v[40:43], v[136:139]\n\t" \
  "v_mfma_f32_16x16x32_bf16 v[136:139], v[20:23], v[44:47], v[136:139]\n\t" \
  "ds_read_b64_tr_b16 v[38:39], v59 offset:14848\n\t" \
  "v_mfma_f32_16x16x32_bf16 v[144:147], v[8:11], v[40:43], v[144:147]\n\t" \
  "v_mfma_f32_16x16x32_bf16 v[144:147], v[24:27], v[44:47], v[144:147]\n\t" \
  "v_mfma_f32_16x16x32_bf16 v[152:155], v[12:15], v[40:43], v[152:155]\n\t" \
  "v_mfma_f32_16x16x32_bf16 v[152:155], v[28:31], v[44:47], v[152:155]\n\t" \
  "s_waitcnt lgkmcnt(4)\n\t" \
  "v_mfma_f32_16x16x32_bf16 v[160:163], v[48:51], v[40:43], v[160:163]\n\t" \
  "v_mfma_f32_16x16x32_bf16 v[160:163], v[52:55], v[44:47], v[160:163]\n\t" \
  "ds_read_b64_tr_b16 v[40:41], v59 offset:1024\n\t" \
  "s_waitcnt lgkmcnt(1)\n\t" \
  "v_mfma_f32_16x16x32_bf16 v[164:167], v[48:51], v[32:35], v[164:167]\n\t" \
  "ds_read_b64_tr_b16 v[42:43], v59 offset:1536\n\t" \
  "v_mfma_f32_16x16x32_bf16 v[164:167], v[52:55], v[36:39], v[164:167]\n\t" \
  "ds_read_b64_tr_b16 v[44:45], v59 offset:13312\n\t" \
  "v_mfma_f32_16x16x32_bf16 v[132:135], v[0:3], v[32:35], v[132:135]\n\t" \
  "ds_read_b64_tr_b16 v[46:47], v59 offset:13824\n\t" \
  "v_mfma_f32_16x16x32_bf16 v[132:135], v[16:19], v[36:39], v[132:135]\n\t" \
  "ds_read_b64_tr_b16 v[48:49], v58 offset:0\n\t" \
  "v_mfma_f32_16x16x32_bf16 v[140:143], v[4:7], v[32:35], v[140:143]\n\t" \
  "ds_read_b64_tr_b16 v[50:51], v58 offset:512\n\t" \
  "v_mfma_f32_16x16x32_bf16 v[140:143], v[20:23], v[36:39], v[140:143]\n\t" \
  "ds_read_b64_tr_b16 v[52:53], v58 offset:12288\n\t" \
  "v_mfma_f32_16x16x32_bf16 v[148:151], v[8:11], v[32:35], v[148:151]\n\t" \
  "ds_read_b64_tr_b16 v[54:55], v58 offset:12800\n\t" \
  "v_mfma_f32_16x16x32_bf16 v[148:151], v[24:27], v[36:39], v[148:151]\n\t" \
  "v_mfma_f32_16x16x32_bf16 v[156:159], v[12:15], v[32:35], v[156:159]\n\t" \
  "ds_read_b64_tr_b16 v[32:33], v59 offset:0\n\t" \
  "v_mfma_f32_16x16x32_bf16 v[156:159], v[28:31], v[36:39], v[156:159]\n\t" \
  "ds_read_b64_tr_b16 v[34:35], v59 offset:512\n\t" \
  "s_waitcnt lgkmcnt(6)\n\t" \
  "v_mfma_f32_16x16x32_bf16 v[100:103], v[40:43], v[0:3], v[100:103]\n\t" \
  "ds_read_b64_tr_b16 v[36:37], v59 offset:12288\n\t" \
  "v_mfma_f32_16x16x32_bf16 v[100:103], v[44:47], v[16:19], v[100:103]\n\t" \
  "ds_read_b64_tr_b16 v[38:39], v59 offset:12800\n\t" \
  "v_mfma_f32_16x16x32_bf16 v[108:111], v[40:43], v[4:7], v[108:111]\n\t" \
  "v_mfma_f32_16x16x32_bf16 v[108:111], v[44:47], v[20:23], v[108:111]\n\t" \
  "v_mfma_f32_16x16x32_bf16 v[116:119], v[40:43], v[8:11], v[116:119]\n\t" \
  "v_mfma_f32_16x16x32_bf16 v[116:119], v[44:47], v[24:27], v[116:119]\n\t" \
  "v_mfma_f32_16x16x32_bf16 v[124:127], v[40:43], v[12:15], v[124:127]\n\t" \
  "v_mfma_f32_16x16x32_bf16 v[124:127], v[44:47], v[28:31], v[124:127]\n\t" \
  "s_waitcnt lgkmcnt(0)\n\t" \
  "s_barrier\n\t" \
  "v_mfma_f32_16x16x32_bf16 v[64:67], v[48:51], v[0:3], v[64:67]\n\t" \
  "v_mfma_f32_16x16x32_bf16 v[64:67], v[52:55], v[16:19], v[64:67]\n\t" \
  "ds_read_b64_tr_b16 v[40:41], v58 offset:25600\n\t" \
  "v_mfma_f32_16x16x32_bf16 v[68:71], v[32:35], v[0:3], v[68:71]\n\t" \
  "ds_read_b128 v[0:3], v56 offset:28672\n\t" \
  "v_mfma_f32_16x16x32_bf16 v[68:71], v[36:39], v[16:19], v[68:71]\n\t" \
  "ds_read_b128 v[16:19], v57 offset:28672\n\t" \
  "v_mfma_f32_16x16x32_bf16 v[72:75], v[48:51], v[4:7], v[72:75]\n\t" \
  "ds_read_b64_tr_b16 v[42:43], v58 offset:26112\n\t" \
  "v_mfma_f32_16x16x32_bf16 v[72:75], v[52:55], v[20:23], v[72:75]\n\t" \
  "ds_read_b64_tr_b16 v[44:45], v60 offset:1024\n\t" \
  "v_mfma_f32_16x16x32_bf16 v[76:79], v[32:35], v[4:7], v[76:79]\n\t" \
  "ds_read_b128 v[4:7], v56 offset:30720\n\t" \
  "v_mfma_f32_16x16x32_bf16 v[76:79], v[36:39], v[20:23], v[76:79]\n\t" \
  "ds_read_b128 v[20:23], v57 offset:30720\n\t" \
  "v_mfma_f32_16x16x32_bf16 v[80:83], v[48:51], v[8:11], v[80:83]\n\t" \
  "ds_read_b64_tr_b16 v[46:47], v60 offset:1536\n\t" \
  "v_mfma_f32_16x16x32_bf16 v[80:83], v[52:55], v[24:27], v[80:83]\n\t" \
  "v_mfma_f32_16x16x32_bf16 v[84:87], v[32:35], v[8:11], v[84:87]\n\t" \
  "ds_read_b128 v[8:11], v56 offset:32768\n\t" \
  "v_mfma_f32_16x16x32_bf16 v[84:87], v[36:39], v[24:27], v[84:87]\n\t" \
  "ds_read_b128 v[24:27], v57 offset:32768\n\t" \
  "v_mfma_f32_16x16x32_bf16 v[88:91], v[48:51], v[12:15], v[88:91]\n\t" \
  "ds_read_b64_tr_b16 v[48:49], v58 offset:26624\n\t" \
  "v_mfma_f32_16x16x32_bf16 v[88:91], v[52:55], v[28:31], v[88:91]\n\t" \
  "ds_read_b64_tr_b16 v[50:51], v58 offset:27136\n\t" \
  "v_mfma_f32_16x16x32_bf16 v[92:95], v[32:35], v[12:15], v[92:95]\n\t" \
  "ds_read_b128 v[12:15], v56 offset:34816\n\t" \
  "v_mfma_f32_16x16x32_bf16 v[92:95], v[36:39], v[28:31], v[92:95]\n\t" \
  "ds_read_b128 v[28:31], v57 offset:34816\n\t" \
  "N1b_%=:\n\t" \
  "s_waitcnt lgkmcnt(10)\n\t" \
  "v_mfma_f32_16x16x32_bf16 v[96:99], v[40:43], v[0:3], v[96:99]\n\t" \
  "ds_read_b64_tr_b16 v[52:53], v60 offset:2048\n\t" \
  "s_waitcnt lgkmcnt(7)\n\t" \
  "v_mfma_f32_16x16x32_bf16 v[96:99], v[44:47], v[16:19], v[96:99]\n\t" \
  "ds_read_b64_tr_b16 v[54:55], v60 offset:2560\n\t" \
  "v_mfma_f32_16x16x32_bf16 v[104:107], v[40:43], v[4:7], v[104:107]\n\t" \
  "ds_read_b128 v[32:35], v56 offset:18432\n\t" \
  "v_mfma_f32_16x16x32_bf16 v[104:107], v[44:47], v[20:23], v[104:107]\n\t" \
  "ds_read_b128 v[36:39], v57 offset:18432\n\t" \
  "s_waitcnt lgkmcnt(8)\n\t" \
  "v_mfma_f32_16x16x32_bf16 v[112:115], v[40:43], v[8:11], v[112:115]\n\t" \
  "v_mfma_f32_16x16x32_bf16 v[112:115], v[44:47], v[24:27], v[112:115]\n\t" \
  "s_waitcnt lgkmcnt(4)\n\t" \
  "v_mfma_f32_16x16x32_bf16 v[120:123], v[40:43], v[12:15], v[120:123]\n\t" \
  "v_mfma_f32_16x16x32_bf16 v[120:123], v[44:47], v[28:31], v[120:123]\n\t" \
  "ds_read_b64_tr_b16 v[40:41], v59 offset:26624\n\t" \
  "v_mfma_f32_16x16x32_bf16 v[128:131], v[0:3], v[48:51], v[128:131]\n\t" \
  "ds_read_b64_tr_b16 v[42:43], v59 offset:27136\n\t" \
  "s_waitcnt lgkmcnt(4)\n\t" \
  "v_mfma_f32_16x16x32_bf16 v[128:131], v[16:19], v[52:55], v[128:131]\n\t" \
  "ds_read_b64_tr_b16 v[44:45], v61 offset:2048\n\t" \
  "v_mfma_f32_16x16x32_bf16 v[136:139], v[4:7], v[48:51], v[136:139]\n\t" \
  "v_mfma_f32_16x16x32_bf16 v[136:139], v[20:23], v[52:55], v[136:139]\n\t" \
  "ds_read_b64_tr_b16 v[46:47], v61 offset:2560\n\t" \
  "v_mfma_f32_16x16x32_bf16 v[144:147], v[8:11], v[48:51], v[144:147]\n\t" \
  "v_mfma_f32_16x16x32_bf16 v[144:147], v[24:27], v[52:55], v[144:147]\n\t" \
  "v_mfma_f32_16x16x32_bf16 v[152:155], v[12:15], v[48:51], v[152:155]\n\t" \
  "v_mfma_f32_16x16x32_bf16 v[152:155], v[28:31], v[52:55], v[152:155]\n\t" \
  "s_waitcnt lgkmcnt(4)\n\t" \
  "v_mfma_f32_16x16x32_bf16 v[160:163], v[32:35], v[48:51], v[160:163]\n\t" \
  "v_mfma_f32_16x16x32_bf16 v[160:163], v[36:39], v[52:55], v[160:163]\n\t" \
  "ds_read_b64_tr_b16 v[48:49], v59 offset:25600\n\t" \
  "s_waitcnt lgkmcnt(1)\n\t" \
  "v_mfma_f32_16x16x32_bf16 v[164:167], v[32:35], v[40:43], v[164:167]\n\t" \
  "ds_read_b64_tr_b16 v[50:51], v59 offset:26112\n\t" \
  "v_mfma_f32_16x16x32_bf16 v[164:167], v[36:39], v[44:47], v[164:167]\n\t" \
  "ds_read_b64_tr_b16 v[52:53], v61 offset:1024\n\t" \
  "v_mfma_f32_16x16x32_bf16 v[132:135], v[0:3], v[40:43], v[132:135]\n\t" \
  "ds_read_b64_tr_b16 v[54:55], v61 offset:1536\n\t" \
  "v_mfma_f32_16x16x32_bf16 v[132:135], v[16:19], v[44:47], v[132:135]\n\t" \
  "ds_read_b64_tr_b16 v[32:33], v58 offset:24576\n\t" \
  "v_mfma_f32_16x16x32_bf16 v[140:143], v[4:7], v[40:43], v[140:143]\n\t" \
  "ds_read_b64_tr_b16 v[34:35], v58 offset:25088\n\t" \
  "v_mfma_f32_16x16x32_bf16 v[140:143], v[20:23], v[44:47], v[140:143]\n\t" \
  "ds_read_b64_tr_b16 v[36:37], v60 offset:0\n\t" \
  "v_mfma_f32_16x16x32_bf16 v[148:151], v[8:11], v[40:43], v[148:151]\n\t" \
  "ds_read_b64_tr_b16 v[38:39], v60 offset:512\n\t" \
  "v_mfma_f32_16x16x32_bf16 v[148:151], v[24:27], v[44:47], v[148:151]\n\t" \
  "v_mfma_f32_16x16x32_bf16 v[156:159], v[12:15], v[40:43], v[156:159]\n\t" \
  "ds_read_b64_tr_b16 v[40:41], v59 offset:24576\n\t" \
  "v_mfma_f32_16x16x32_bf16 v[156:159], v[28:31], v[44:47], v[156:159]\n\t" \
  "ds_read_b64_tr_b16 v[42:43], v59 offset:25088\n\t" \
  "s_waitcnt lgkmcnt(6)\n\t" \
  "v_mfma_f32_16x16x32_bf16 v[100:103], v[48:51], v[0:3], v[100:103]\n\t" \
  "ds_read_b64_tr_b16 v[44:45], v61 offset:0\n\t" \
  "v_mfma_f32_16x16x32_bf16 v[100:103], v[52:55], v[16:19], v[100:103]\n\t" \
  "ds_read_b64_tr_b16 v[46:47], v61 offset:512\n\t" \
  "v_mfma_f32_16x16x32_bf16 v[108:111], v[48:51], v[4:7], v[108:111]\n\t" \
  "v_mfma_f32_16x16x32_bf16 v[108:111], v[52:55], v[20:23], v[108:111]\n\t" \
  "v_mfma_f32_16x16x32_bf16 v[116:119], v[48:51], v[8:11], v[116:119]\n\t" \
  "v_mfma_f32_16x16x32_bf16 v[116:119], v[52:55], v[24:27], v[116:119]\n\t" \
  "v_mfma_f32_16x16x32_bf16 v[124:127], v[48:51], v[12:15], v[124:127]\n\t" \
  "v_mfma_f32_16x16x32_bf16 v[124:127], v[52:55], v[28:31], v[124:127]\n\t" \
  "s_waitcnt lgkmcnt(0)\n\t" \
  "s_barrier\n\t" \
  "v_mfma_f32_16x16x32_bf16 v[64:67], v[32:35], v[0:3], v[64:67]\n\t" \
  "v_mfma_f32_16x16x32_bf16 v[64:67], v[36:39], v[16:19], v[64:67]\n\t" \
  "ds_read_b64_tr_b16 v[48:49], v60 offset:13312\n\t" \
  "v_mfma_f32_16x16x32_bf16 v[68:71], v[40:43], v[0:3], v[68:71]\n\t" \
  "ds_read_b128 v[0:3], v56 offset:47104\n\t" \
  "v_mfma_f32_16x16x32_bf16 v[68:71], v[44:47], v[16:19], v[68:71]\n\t" \
  "ds_read_b128 v[16:19], v57 offset:47104\n\t" \
  "v_mfma_f32_16x16x32_bf16 v[72:75], v[32:35], v[4:7], v[72:75]\n\t" \
  "ds_read_b64_tr_b16 v[50:51], v60 offset:13824\n\t" \
  "v_mfma_f32_16x16x32_bf16 v[72:75], v[36:39], v[20:23], v[72:75]\n\t" \
  "ds_read_b64_tr_b16 v[52:53], v60 offset:25600\n\t" \
  "v_mfma_f32_16x16x32_bf16 v[76:79], v[40:43], v[4:7], v[76:79]\n\t" \
  "ds_read_b128 v[4:7], v56 offset:49152\n\t" \
  "v_mfma_f32_16x16x32_bf16 v[76:79], v[44:47], v[20:23], v[76:79]\n\t" \
  "ds_read_b128 v[20:23], v57 offset:49152\n\t" \
  "v_mfma_f32_16x16x32_bf16 v[80:83], v[32:35], v[8:11], v[80:83]\n\t" \
  "ds_read_b64_tr_b16 v[54:55], v60 offset:26112\n\t" \
  "v_mfma_f32_16x16x32_bf16 v[80:83], v[36:39], v[24:27], v[80:83]\n\t" \
  "v_mfma_f32_16x16x32_bf16 v[84:87], v[40:43], v[8:11], v[84:87]\n\t" \
  "ds_read_b128 v[8:11], v56 offset:51200\n\t" \
  "v_mfma_f32_16x16x32_bf16 v[84:87], v[44:47], v[24:27], v[84:87]\n\t" \
  "ds_read_b128 v[24:27], v57 offset:51200\n\t" \
  "v_mfma_f32_16x16x32_bf16 v[88:91], v[32:35], v[12:15], v[88:91]\n\t" \
  "ds_read_b64_tr_b16 v[32:33], v60 offset:14336\n\t" \
  "v_mfma_f32_16x16x32_bf16 v[88:91], v[36:39], v[28:31], v[88:91]\n\t" \
  "ds_read_b64_tr_b16 v[34:35], v60 offset:14848\n\t" \
  "v_mfma_f32_16x16x32_bf16 v[92:95], v[40:43], v[12:15], v[92:95]\n\t" \
  "ds_read_b128 v[12:15], v56 offset:53248\n\t" \
  "v_mfma_f32_16x16x32_bf16 v[92:95], v[44:47], v[28:31], v[92:95]\n\t" \
  "ds_read_b128 v[28:31], v57 offset:53248\n\t" \
  "N2b_%=:\n\t" \
  "s_waitcnt lgkmcnt(10)\n\t" \
  "v_mfma_f32_16x16x32_bf16 v[96:99], v[48:51], v[0:3], v[96:99]\n\t" \
  "ds_read_b64_tr_b16 v[36:37], v60 offset:26624\n\t" \
  "s_waitcnt lgkmcnt(7)\n\t" \
  "v_mfma_f32_16x16x32_bf16 v[96:99], v[52:55], v[16:19], v[96:99]\n\t" \
  "ds_read_b64_tr_b16 v[38:39], v60 offset:27136\n\t" \
  "v_mfma_f32_16x16x32_bf16 v[104:107], v[48:51], v[4:7], v[104:107]\n\t" \
  "ds_read_b128 v[40:43], v56 offset:36864\n\t" \
  "v_mfma_f32_16x16x32_bf16 v[104:107], v[52:55], v[20:23], v[104:107]\n\t" \
  "ds_read_b128 v[44:47], v57 offset:36864\n\t" \
  "s_waitcnt lgkmcnt(8)\n\t" \
  "v_mfma_f32_16x16x32_bf16 v[112:115], v[48:51], v[8:11], v[112:115]\n\t" \
  "v_mfma_f32_16x16x32_bf16 v[112:115], v[52:55], v[24:27], v[112:115]\n\t" \
  "s_waitcnt lgkmcnt(4)\n\t" \
  "v_mfma_f32_16x16x32_bf16 v[120:123], v[48:51], v[12:15], v[120:123]\n\t" \
  "v_mfma_f32_16x16x32_bf16 v[120:123], v[52:55], v[28:31], v[120:123]\n\t" \
  "ds_read_b64_tr_b16 v[48:49], v61 offset:14336\n\t" \
  "v_mfma_f32_16x16x32_bf16 v[128:131], v[0:3], v[32:35], v[128:131]\n\t" \
  "ds_read_b64_tr_b16 v[50:51], v61 offset:14848\n\t" \
  "s_waitcnt lgkmcnt(4)\n\t" \
  "v_mfma_f32_16x16x32_bf16 v[128:131], v[16:19], v[36:39], v[128:131]\n\t" \
  "ds_read_b64_tr_b16 v[52:53], v61 offset:26624\n\t" \
  "v_mfma_f32_16x16x32_bf16 v[136:139], v[4:7], v[32:35], v[136:139]\n\t" \
  "v_mfma_f32_16x16x32_bf16 v[136:139], v[20:23], v[36:39], v[136:139]\n\t" \
  "ds_read_b64_tr_b16 v[54:55], v61 offset:27136\n\t" \
  "v_mfma_f32_16x16x32_bf16 v[144:147], v[8:11], v[32:35], v[144:147]\n\t" \
  "v_mfma_f32_16x16x32_bf16 v[144:147], v[24:27], v[36:39], v[144:147]\n\t" \
  "v_mfma_f32_16x16x32_bf16 v[152:155], v[12:15], v[32:35], v[152:155]\n\t" \
  "v_mfma_f32_16x16x32_bf16 v[152:155], v[28:31], v[36:39], v[152:155]\n\t" \
  "s_waitcnt lgkmcnt(4)\n\t" \
  "v_mfma_f32_16x16x32_bf16 v[160:163], v[40:43], v[32:35], v[160:163]\n\t" \
  "v_mfma_f32_16x16x32_bf16 v[160:163], v[44:47], v[36:39], v[160:163]\n\t" \
  "ds_read_b64_tr_b16 v[32:33], v61 offset:13312\n\t" \
  "s_waitcnt lgkmcnt(1)\n\t" \
  "v_mfma_f32_16x16x32_bf16 v[164:167], v[40:43], v[48:51], v[164:167]\n\t" \
  "ds_read_b64_tr_b16 v[34:35], v61 offset:13824\n\t" \
  "v_mfma_f32_16x16x32_bf16 v[164:167], v[44:47], v[52:55], v[164:167]\n\t" \
  "ds_read_b64_tr_b16 v[36:37], v61 offset:25600\n\t" \
  "v_mfma_f32_16x16x32_bf16 v[132:135], v[0:3], v[48:51], v[132:135]\n\t" \
  "ds_read_b64_tr_b16 v[38:39], v61 offset:26112\n\t" \
  "v_mfma_f32_16x16x32_bf16 v[132:135], v[16:19], v[52:55], v[132:135]\n\t" \
  "ds_read_b64_tr_b16 v[40:41], v60 offset:12288\n\t" \
  "v_mfma_f32_16x16x32_bf16 v[140:143], v[4:7], v[48:51], v[140:143]\n\t" \
  "ds_read_b64_tr_b16 v[42:43], v60 offset:12800\n\t" \
  "v_mfma_f32_16x16x32_bf16 v[140:143], v[20:23], v[52:55], v[140:143]\n\t" \
  "ds_read_b64_tr_b16 v[44:45], v60 offset:24576\n\t" \
  "v_mfma_f32_16x16x32_bf16 v[148:151], v[8:11], v[48:51], v[148:151]\n\t" \
  "ds_read_b64_tr_b16 v[46:47], v60 offset:25088\n\t" \
  "v_mfma_f32_16x16x32_bf16 v[148:151], v[24:27], v[52:55], v[148:151]\n\t" \
  "v_mfma_f32_16x16x32_bf16 v[156:159], v[12:15], v[48:51], v[156:159]\n\t" \
  "ds_read_b64_tr_b16 v[48:49], v61 offset:12288\n\t" \
  "v_mfma_f32_16x16x32_bf16 v[156:159], v[28:31], v[52:55], v[156:159]\n\t" \
  "ds_read_b64_tr_b16 v[50:51], v61 offset:12800\n\t" \
  "s_waitcnt lgkmcnt(6)\n\t" \
  "v_mfma_f32_16x16x32_bf16 v[100:103], v[32:35], v[0:3], v[100:103]\n\t" \
  "ds_read_b64_tr_b16 v[52:53], v61 offset:24576\n\t" \
  "v_mfma_f32_16x16x32_bf16 v[100:103], v[36:39], v[16:19], v[100:103]\n\t" \
  "ds_read_b64_tr_b16 v[54:55], v61 offset:25088\n\t" \
  "v_mfma_f32_16x16x32_bf16 v[108:111], v[32:35], v[4:7], v[108:111]\n\t" \
  "v_mfma_f32_16x16x32_bf16 v[108:111], v[36:39], v[20:23], v[108:111]\n\t" \
  "s_addk_i32 %[it], 1\n\t" \
  "v_mfma_f32_16x16x32_bf16 v[116:119], v[32:35], v[8:11], v[116:119]\n\t" \
  "s_cmp_lt_u32 %[it], 6\n\t" \
  "v_mfma_f32_16x16x32_bf16 v[116:119], v[36:39], v[24:27], v[116:119]\n\t" \
  "v_mfma_f32_16x16x32_bf16 v[124:127], v[32:35], v[12:15], v[124:127]\n\t" \
  "v_mfma_f32_16x16x32_bf16 v[124:127], v[36:39], v[28:31], v[124:127]\n\t" \
  "s_waitcnt lgkmcnt(0)\n\t" \
  "s_barrier\n\t" \
  "s_cbranch_scc0 NTb_%=\n\t" \
  "v_mfma_f32_16x16x32_bf16 v[64:67], v[40:43], v[0:3], v[64:67]\n\t" \
  "v_mfma_f32_16x16x32_bf16 v[64:67], v[44:47], v[16:19], v[64:67]\n\t" \
  "ds_read_b64_tr_b16 v[32:33], v58 offset:1024\n\t" \
  "v_mfma_f32_16x16x32_bf16 v[68:71], v[48:51], v[0:3], v[68:71]\n\t" \
  "ds_read_b128 v[0:3], v56 offset:10240\n\t" \
  "v_mfma_f32_16x16x32_bf16 v[68:71], v[52:55], v[16:19], v[68:71]\n\t" \
  "ds_read_b128 v[16:19], v57 offset:10240\n\t" \
  "v_mfma_f32_16x16x32_bf16 v[72:75], v[40:43], v[4:7], v[72:75]\n\t" \
  "ds_read_b64_tr_b16 v[34:35], v58 offset:1536\n\t" \
  "v_mfma_f32_16x16x32_bf16 v[72:75], v[44:47], v[20:23], v[72:75]\n\t" \
  "ds_read_b64_tr_b16 v[36:37], v58 offset:13312\n\t" \
  "v_mfma_f32_16x16x32_bf16 v[76:79], v[48:51], v[4:7], v[76:79]\n\t" \
  "ds_read_b128 v[4:7], v56 offset:12288\n\t" \
  "v_mfma_f32_16x16x32_bf16 v[76:79], v[52:55], v[20:23], v[76:79]\n\t" \
  "ds_read_b128 v[20:23], v57 offset:12288\n\t" \
  "v_mfma_f32_16x16x32_bf16 v[80:83], v[40:43], v[8:11], v[80:83]\n\t" \
  "ds_read_b64_tr_b16 v[38:39], v58 offset:13824\n\t" \
  "v_mfma_f32_16x16x32_bf16 v[80:83], v[44:47], v[24:27], v[80:83]\n\t" \
  "v_mfma_f32_16x16x32_bf16 v[84:87], v[48:51], v[8:11], v[84:87]\n\t" \
  "ds_read_b128 v[8:11], v56 offset:14336\n\t" \
  "v_mfma_f32_16x16x32_bf16 v[84:87], v[52:55], v[24:27], v[84:87]\n\t" \
  "ds_read_b128 v[24:27], v57 offset:14336\n\t" \
  "v_mfma_f32_16x16x32_bf16 v[88:91], v[40:43], v[12:15], v[88:91]\n\t" \
  "ds_read_b64_tr_b16 v[40:41], v58 offset:2048\n\t" \
  "v_mfma_f32_16x16x32_bf16 v[88:91], v[44:47], v[28:31], v[88:91]\n\t" \
  "ds_read_b64_tr_b16 v[42:43], v58 offset:2560\n\t" \
  "v_mfma_f32_16x16x32_bf16 v[92:95], v[48:51], v[12:15], v[92:95]\n\t" \
  "ds_read_b128 v[12:15], v56 offset:16384\n\t" \
  "v_mfma_f32_16x16x32_bf16 v[92:95], v[52:55], v[28:31], v[92:95]\n\t" \
  "ds_read_b128 v[28:31], v57 offset:16384\n\t" \
  "s_branch N0b_%=\n\t" \
  "NTb_%=:\n\t" \
  "s_setprio 3\n\t" \
  "v_mfma_f32_16x16x32_bf16 v[88:91], v[40:43], v[12:15], v[88:91]\n\t" \
  "v_cvt_pk_bf16_f32 v160, v160, v161\n\t" \
  "v_cvt_pk_bf16_f32 v161, v162, v163\n\t" \
  "v_mfma_f32_16x16x32_bf16 v[88:91], v[44:47], v[28:31], v[88:91]\n\t" \
  "v_cvt_pk_bf16_f32 v162, v164, v165\n\t" \
  "v_cvt_pk_bf16_f32 v163, v166, v167\n\t" \
  "v_mfma_f32_16x16x32_bf16 v[92:95], v[48:51], v[12:15], v[92:95]\n\t" \
  "v_mov_b32 v57, %[fa]\n\t" \
  "v_mov_b32 v58, 1\n\t" \
  "v_mfma_f32_16x16x32_bf16 v[92:95], v[52:55], v[28:31], v[92:95]\n\t" \
  "ds_write_b128 v63, v[160:163] offset:2048\n\t" \
  "s_mov_b64 exec, 1\n\t" \
  "ds_write_b32 v57, v58\n\t" \
  "s_mov_b64 exec, -1\n\t" \
  "v_mfma_f32_16x16x32_bf16 v[80:83], v[40:43], v[8:11], v[80:83]\n\t" \
  "v_mov_b32 v60, 0xff61b1e6\n\t" \
  "v_cvt_pk_bf16_f32 v120, v120, v121\n\t" \
  "v_mfma_f32_16x16x32_bf16 v[80:83], v[44:47], v[24:27], v[80:83]\n\t" \
  "v_cvt_pk_bf16_f32 v121, v122, v123\n\t" \
  "v_cvt_pk_bf16_f32 v122, v124, v125\n\t" \
  "v_mfma_f32_16x16x32_bf16 v[84:87], v[48:51], v[8:11], v[84:87]\n\t" \
  "v_cvt_pk_bf16_f32 v123, v126, v127\n\t" \
  "v_cvt_pk_bf16_f32 v28, v152, v153\n\t" \
  "v_mfma_f32_16x16x32_bf16 v[84:87], v[52:55], v[24:27], v[84:87]\n\t" \
  "v_cvt_pk_bf16_f32 v29, v154, v155\n\t" \
  "v_cvt_pk_bf16_f32 v38, v156, v157\n\t" \
  "v_mfma_f32_16x16x32_bf16 v[72:75], v[40:43], v[4:7], v[72:75]\n\t" \
  "v_cvt_pk_bf16_f32 v39, v158, v159\n\t" \
  "v_cvt_pk_bf16_f32 v112, v112, v113\n\t" \
  "v_mfma_f32_16x16x32_bf16 v[72:75], v[44:47], v[20:23], v[72:75]\n\t" \
  "v_cvt_pk_bf16_f32 v113, v114, v115\n\t" \
  "v_cvt_pk_bf16_f32 v114, v116, v117\n\t" \
  "v_mfma_f32_16x16x32_bf16 v[76:79], v[48:51], v[4:7], v[76:79]\n\t" \
  "v_cvt_pk_bf16_f32 v115, v118, v119\n\t" \
  "v_cndmask_b32_e64 v12, v60, 0, s[76:77]\n\t" \
  "v_mfma_f32_16x16x32_bf16 v[76:79], v[52:55], v[20:23], v[76:79]\n\t" \
  "v_cndmask_b32_e64 v13, v60, 0, s[78:79]\n\t" \
  "v_cndmask_b32_e64 v14, v60, 0, s[80:81]\n\t" \
  "v_mfma_f32_16x16x32_bf16 v[64:67], v[40:43], v[0:3], v[64:67]\n\t" \
  "v_cndmask_b32_e64 v15, v60, 0, s[82:83]\n\t" \
  "v_cndmask_b32_e64 v56, v60, 0, s[68:69]\n\t" \
  "v_mfma_f32_16x16x32_bf16 v[64:67], v[44:47], v[16:19], v[64:67]\n\t" \
  "v_cndmask_b32_e64 v57, v60, 0, s[70:71]\n\t" \
  "v_cndmask_b32_e64 v58, v60, 0, s[72:73]\n\t" \
  "v_mfma_f32_16x16x32_bf16 v[68:71], v[48:51], v[0:3], v[68:71]\n\t" \
  "v_cndmask_b32_e64 v59, v60, 0, s[74:75]\n\t" \
  "v_mfma_f32_16x16x32_bf16 v[68:71], v[52:55], v[16:19], v[68:71]\n\t" \
  "NX_%=:\n\t" \
  "v_cvt_pk_bf16_f32 v88, v88, v89\n\t" \
  "v_cvt_pk_bf16_f32 v89, v90, v91\n\t" \
  "v_cvt_pk_bf16_f32 v90, v92, v93\n\t" \
  "v_cvt_pk_bf16_f32 v91, v94, v95\n\t" \
  "v_mov_b32 v61, %[fp]\n\t" \
  "v_add_u32 v60, %[vo], v63\n\t" \
  "v_mfma_f32_16x16x32_bf16 v[152:155], v[112:115], v[88:91], v[56:59]\n\t" \
  "v_cvt_pk_bf16_f32 v26, v144, v145\n\t" \
  "v_cvt_pk_bf16_f32 v27, v146, v147\n\t" \
  "v_mfma_f32_16x16x32_bf16 v[156:159], v[120:123], v[88:91], v[12:15]\n\t" \
  "v_cvt_pk_bf16_f32 v36, v148, v149\n\t" \
  "v_cvt_pk_bf16_f32 v37, v150, v151\n\t" \
  "v_cvt_pk_bf16_f32 v80, v80, v81\n\t" \
  "v_cvt_pk_bf16_f32 v81, v82, v83\n\t" \
  "v_cvt_pk_bf16_f32 v82, v84, v85\n\t" \
  "v_cvt_pk_bf16_f32 v83, v86, v87\n\t" \
  "v_cvt_pk_bf16_f32 v104, v104, v105\n\t" \
  "v_cvt_pk_bf16_f32 v105, v106, v107\n\t" \
  "v_mfma_f32_16x16x32_bf16 v[148:151], v[112:115], v[80:83], v[12:15]\n\t" \
  "v_cvt_pk_bf16_f32 v106, v108, v109\n\t" \
  "v_cvt_pk_bf16_f32 v107, v110, v111\n\t" \
  "ds_read_b32 v0, v61\n\t" \
  "ds_read_b64 v[20:21], v60\n\t" \
  "ds_read_b64 v[30:31], v60 offset:8\n\t" \
  "s_mov_b64 exec, %[rm]\n\t" \
  "ds_read_b128 v[160:163], v63\n\t" \
  "s_mov_b64 exec, -1\n\t" \
  "v_mfma_f32_16x16x32_bf16 v[144:147], v[104:107], v[80:83], v[56:59]\n\t" \
  "v_cvt_pk_bf16_f32 v24, v136, v137\n\t" \
  "v_cvt_pk_bf16_f32 v25, v138, v139\n\t" \
  "v_cvt_pk_bf16_f32 v34, v140, v141\n\t" \
  "v_cvt_pk_bf16_f32 v35, v142, v143\n\t" \
  "v_cvt_pk_bf16_f32 v72, v72, v73\n\t" \
  "v_cvt_pk_bf16_f32 v73, v74, v75\n\t" \
  "v_cvt_pk_bf16_f32 v74, v76, v77\n\t" \
  "v_cvt_pk_bf16_f32 v75, v78, v79\n\t" \
  "v_cvt_pk_bf16_f32 v96, v96, v97\n\t" \
  "v_cvt_pk_bf16_f32 v97, v98, v99\n\t" \
  "v_mfma_f32_16x16x32_bf16 v[140:143], v[104:107], v[72:75], v[12:15]\n\t" \
  "v_cvt_pk_bf16_f32 v98, v100, v101\n\t" \
  "v_cvt_pk_bf16_f32 v99, v102, v103\n\t" \
  "v_cvt_pk_bf16_f32 v22, v128, v129\n\t" \
  "v_cvt_pk_bf16_f32 v23, v130, v131\n\t" \
  "v_mfma_f32_16x16x32_bf16 v[136:139], v[96:99], v[72:75], v[56:59]\n\t" \
  "v_cvt_pk_bf16_f32 v32, v132, v133\n\t" \
  "v_cvt_pk_bf16_f32 v33, v134, v135\n\t" \
  "v_cvt_pk_bf16_f32 v64, v64, v65\n\t" \
  "v_cvt_pk_bf16_f32 v65, v66, v67\n\t" \
  "v_cvt_pk_bf16_f32 v66, v68, v69\n\t" \
  "v_cvt_pk_bf16_f32 v67, v70, v71\n\t" \
  "v_max_f32 v152, v152, v153\n\t" \
  "v_max_f32 v154, v154, v155\n\t" \
  "v_mfma_f32_16x16x32_bf16 v[132:135], v[96:99], v[64:67], v[12:15]\n\t" \
  "AW_%=:\n\t" \
  "s_waitcnt lgkmcnt(0)\n\t" \
  "v_cmp_eq_u32 vcc, 0, v0\n\t" \
  "v_mov_b32 v8, 0xc0c0100\n\t" \
  "v_mov_b32 v9, 0x1000c0c\n\t" \
  "s_cbranch_vccnz AR_%=\n\t" \
  "s_cmp_eq_u32 %[z], 0\n\t" \
  "s_cbranch_scc1 AZ_%=\n\t" \
  "AY_%=:\n\t" \
  "v_mfma_f32_16x16x32_bf16 v[128:131], v[160:163], v[64:67], v[56:59]\n\t" \
  "v_max_f32 v156, v156, v157\n\t" \
  "v_max_f32 v158, v158, v159\n\t" \
  "v_max3_f32 v7, v152, v154, v156\n\t" \
  "v_max_f32 v7, v7, v158\n\t" \
  "v_max_f32 v144, v144, v145\n\t" \
  "v_max_f32 v146, v146, v147\n\t" \
  "v_max_f32 v148, v148, v149\n\t" \
  "v_max_f32 v150, v150, v151\n\t" \
  "v_max3_f32 v6, v144, v146, v148\n\t" \
  "v_max_f32 v6, v6, v150\n\t" \
  "v_max_f32 v136, v136, v137\n\t" \
  "v_max_f32 v138, v138, v139\n\t" \
  "v_max_f32 v140, v140, v141\n\t" \
  "v_max_f32 v142, v142, v143\n\t" \
  "v_max3_f32 v5, v136, v138, v140\n\t" \
  "v_max_f32 v5, v5, v142\n\t" \
  "v_max_f32 v128, v128, v129\n\t" \
  "v_max_f32 v130, v130, v131\n\t" \
  "v_max_f32 v132, v132, v133\n\t" \
  "v_max_f32 v134, v134, v135\n\t" \
  "v_max3_f32 v4, v128, v130, v132\n\t" \
  "v_max_f32 v4, v4, v134\n\t" \
  "v_cndmask_b32_e64 v60, v8, v9, s[84:85]\n\t" \
  "v_mov_b32 v8, 0xc0c0302\n\t" \
  "v_permlane16_swap_b32 v6, v7\n\t" \
  "v_permlane16_swap_b32 v4, v5\n\t" \
  "v_max_f32 v6, v6, v7\n\t" \
  "v_max_f32 v4, v4, v5\n\t" \
  "v_mov_b32 v9, 0x3020c0c\n\t" \
  "v_cndmask_b32_e64 v61, v8, v9, s[84:85]\n\t" \
  "v_permlane32_swap_b32 v4, v6\n\t" \
  "v_max_f32 v4, v4, v6\n\t" \
  "v_mul_f32 v6, 0xbe8293ee, v4\n\t" \
  "v_mul_f32 v4, 0xbe8293ee, v4\n\t" \
  "s_nop 1\n\t" \
  "v_permlane32_swap_b32 v4, v6\n\t" \
  "v_mov_b32 v5, v4\n\t" \
  "v_mov_b32 v7, v6\n\t" \
  "s_nop 0\n\t" \
  "v_permlane16_swap_b32 v4, v5\n\t" \
  "v_permlane16_swap_b32 v6, v7\n\t" \
  "v_fmamk_f32 v128, v128, 0x3e8293ee, v4\n\t" \
  "v_fmamk_f32 v130, v130, 0x3e8293ee, v4\n\t" \
  "v_fmamk_f32 v132, v132, 0x3e8293ee, v4\n\t" \
  "v_fmamk_f32 v134, v134, 0x3e8293ee, v4\n\t" \
  "v_fmamk_f32 v136, v136, 0x3e8293ee, v5\n\t" \
  "v_fmamk_f32 v138, v138, 0x3e8293ee, v5\n\t" \
  "v_fmamk_f32 v140, v140, 0x3e8293ee, v5\n\t" \
  "v_fmamk_f32 v142, v142, 0x3e8293ee, v5\n\t" \
  "v_fmamk_f32 v144, v144, 0x3e8293ee, v6\n\t" \
  "v_fmamk_f32 v146, v146, 0x3e8293ee, v6\n\t" \
  "v_fmamk_f32 v148, v148, 0x3e8293ee, v6\n\t" \
  "v_fmamk_f32 v150, v150, 0x3e8293ee, v6\n\t" \
  "v_fmamk_f32 v152, v152, 0x3e8293ee, v7\n\t" \
  "v_fmamk_f32 v154, v154, 0x3e8293ee, v7\n\t" \
  "v_fmamk_f32 v156, v156, 0x3e8293ee, v7\n\t" \
  "v_fmamk_f32 v158, v158, 0x3e8293ee, v7\n\t" \
  "v_exp_f32 v128, v128\n\t" \
  "v_exp_f32 v130, v130\n\t" \
  "v_exp_f32 v132, v132\n\t" \
  "v_exp_f32 v134, v134\n\t" \
  "v_exp_f32 v136, v136\n\t" \
  "v_exp_f32 v138, v138\n\t" \
  "v_exp_f32 v140, v140\n\t" \
  "v_exp_f32 v142, v142\n\t" \
  "v_exp_f32 v144, v144\n\t" \
  "v_exp_f32 v146, v146\n\t" \
  "v_exp_f32 v148, v148\n\t" \
  "v_exp_f32 v150, v150\n\t" \
  "v_exp_f32 v152, v152\n\t" \
  "v_exp_f32 v154, v154\n\t" \
  "v_exp_f32 v156, v156\n\t" \
  "v_exp_f32 v158, v158\n\t" \
  "v_add_f32 v4, v128, v130\n\t" \
  "v_add_f32 v8, v132, v134\n\t" \
  "v_add_f32 v5, v136, v138\n\t" \
  "v_add_f32 v9, v140, v142\n\t" \
  "v_add_f32 v6, v144, v146\n\t" \
  "v_add_f32 v10, v148, v150\n\t" \
  "v_add_f32 v7, v152, v154\n\t" \
  "v_add_f32 v11, v156, v158\n\t" \
  "v_add_f32 v4, v4, v8\n\t" \
  "v_add_f32 v5, v5, v9\n\t" \
  "v_add_f32 v6, v6, v10\n\t" \
  "v_add_f32 v7, v7, v11\n\t" \
  "v_permlane16_swap_b32 v4, v5\n\t" \
  "s_nop 0\n\t" \
  "v_permlane16_swap_b32 v6, v7\n\t" \
  "v_add_f32 v4, v4, v5\n\t" \
  "v_add_f32 v6, v6, v7\n\t" \
  "s_nop 1\n\t" \
  "v_permlane32_swap_b32 v4, v6\n\t" \
  "v_add_f32 v4, v4, v6\n\t" \
  "v_rcp_f32 v4, v4\n\t" \
  "s_nop 0\n\t" \
  "v_mov_b32 v6, v4\n\t" \
  "s_nop 1\n\t" \
  "v_permlane32_swap_b32 v4, v6\n\t" \
  "v_mov_b32 v5, v4\n\t" \
  "v_mov_b32 v7, v6\n\t" \
  "s_nop 0\n\t" \
  "v_permlane16_swap_b32 v4, v5\n\t" \
  "v_permlane16_swap_b32 v6, v7\n\t" \
  "v_mul_f32 v128, v128, v4\n\t" \
  "v_mul_f32 v130, v130, v4\n\t" \
  "v_mul_f32 v132, v132, v4\n\t" \
  "v_mul_f32 v134, v134, v4\n\t" \
  "v_cvt_pk_bf16_f32 v128, v128, v130\n\t" \
  "v_cvt_pk_bf16_f32 v132, v132, v134\n\t" \
  "v_perm_b32 v68, 0, v128, v60\n\t" \
  "v_perm_b32 v69, 0, v128, v61\n\t" \
  "v_perm_b32 v70, 0, v132, v60\n\t" \
  "v_perm_b32 v71, 0, v132, v61\n\t" \
  "v_mul_f32 v136, v136, v5\n\t" \
  "v_mul_f32 v138, v138, v5\n\t" \
  "v_mfma_f32_16x16x32_bf16 v[128:131], v[20:23], v[68:71], 0\n\t" \
  "v_mul_f32 v140, v140, v5\n\t" \
  "v_mul_f32 v142, v142, v5\n\t" \
  "v_cvt_pk_bf16_f32 v136, v136, v138\n\t" \
  "v_mfma_f32_16x16x32_bf16 v[132:135], v[30:33], v[68:71], 0\n\t" \
  "v_cvt_pk_bf16_f32 v140, v140, v142\n\t" \
  "v_perm_b32 v76, 0, v136, v60\n\t" \
  "v_perm_b32 v77, 0, v136, v61\n\t" \
  "v_perm_b32 v78, 0, v140, v60\n\t" \
  "v_perm_b32 v79, 0, v140, v61\n\t" \
  "v_mul_f32 v144, v144, v6\n\t" \
  "v_mul_f32 v146, v146, v6\n\t" \
  "v_mfma_f32_16x16x32_bf16 v[136:139], v[22:25], v[76:79], 0\n\t" \
  "v_mul_f32 v148, v148, v6\n\t" \
  "v_mul_f32 v150, v150, v6\n\t" \
  "v_cvt_pk_bf16_f32 v144, v144, v146\n\t" \
  "v_mfma_f32_16x16x32_bf16 v[140:143], v[32:35], v[76:79], 0\n\t" \
  "v_cvt_pk_bf16_f32 v148, v148, v150\n\t" \
  "v_perm_b32 v84, 0, v144, v60\n\t" \
  "v_perm_b32 v85, 0, v144, v61\n\t" \
  "v_perm_b32 v86, 0, v148, v60\n\t" \
  "v_perm_b32 v87, 0, v148, v61\n\t" \
  "v_cvt_pk_bf16_f32 v100, v128, v129\n\t" \
  "v_cvt_pk_bf16_f32 v101, v130, v131\n\t" \
  "v_mfma_f32_16x16x32_bf16 v[144:147], v[24:27], v[84:87], 0\n\t" \
  "v_cvt_pk_bf16_f32 v102, v132, v133\n\t" \
  "v_cvt_pk_bf16_f32 v103, v134, v135\n\t" \
  "global_store_dwordx4 v62, v[100:103], %[ot] offset:0 sc1\n\t" \
  "v_mfma_f32_16x16x32_bf16 v[148:151], v[34:37], v[84:87], 0\n\t" \
  "v_mul_f32 v152, v152, v7\n\t" \
  "v_mul_f32 v154, v154, v7\n\t" \
  "v_mul_f32 v156, v156, v7\n\t" \
  "v_mul_f32 v158, v158, v7\n\t" \
  "v_cvt_pk_bf16_f32 v152, v152, v154\n\t" \
  "v_cvt_pk_bf16_f32 v156, v156, v158\n\t" \
  "v_perm_b32 v92, 0, v152, v60\n\t" \
  "v_perm_b32 v93, 0, v152, v61\n\t" \
  "v_perm_b32 v94, 0, v156, v60\n\t" \
  "v_perm_b32 v95, 0, v156, v61\n\t" \
  "v_cvt_pk_bf16_f32 v108, v136, v137\n\t" \
  "v_cvt_pk_bf16_f32 v109, v138, v139\n\t" \
  "v_mfma_f32_16x16x32_bf16 v[152:155], v[26:29], v[92:95], 0\n\t" \
  "v_cvt_pk_bf16_f32 v110, v140, v141\n\t" \
  "v_cvt_pk_bf16_f32 v111, v142, v143\n\t" \
  "global_store_dwordx4 v62, v[108:111], %[ot] offset:1024 sc1\n\t" \
  "v_mfma_f32_16x16x32_bf16 v[156:159], v[36:39], v[92:95], 0\n\t" \
  "v_cvt_pk_bf16_f32 v116, v144, v145\n\t" \
  "v_cvt_pk_bf16_f32 v117, v146, v147\n\t" \
  "v_cvt_pk_bf16_f32 v118, v148, v149\n\t" \
  "v_cvt_pk_bf16_f32 v119, v150, v151\n\t" \
  "global_store_dwordx4 v62, v[116:119], %[ou] offset:0 sc1\n\t" \
  "v_cvt_pk_bf16_f32 v124, v152, v153\n\t" \
  "v_cvt_pk_bf16_f32 v125, v154, v155\n\t" \
  "s_nop 0\n\t" \
  "v_cvt_pk_bf16_f32 v126, v156, v157\n\t" \
  "v_cvt_pk_bf16_f32 v127, v158, v159\n\t" \
  "global_store_dwordx4 v62, v[124:127], %[ou] offset:1024 sc1\n\t" \
  "s_branch AE_%=\n\t" \
  "AR_%=:\n\t" \
  "s_addk_i32 %[it], 1\n\t" \
  "s_cmp_gt_u32 %[it], 0x400000\n\t" \
  "s_cbranch_scc1 AY_%=\n\t" \
  "ds_read_b32 v0, v61\n\t" \
  "ds_read_b64 v[20:21], v60\n\t" \
  "ds_read_b64 v[30:31], v60 offset:8\n\t" \
  "s_mov_b64 exec, %[rm]\n\t" \
  "ds_read_b128 v[160:163], v63\n\t" \
  "s_mov_b64 exec, -1\n\t" \
  "s_branch AW_%=\n\t" \
  "AZ_%=:\n\t" \
  "v_mov_b32 v0, 0xff61b1e6\n\t" \
  "v_mov_b32 v1, 0xcfa8965f\n\t" \
  "s_nop 3\n\t" \
  "v_cndmask_b32_e64 v56, v0, v1, s[68:69]\n\t" \
  "v_cndmask_b32_e64 v57, v0, v1, s[70:71]\n\t" \
  "v_cndmask_b32_e64 v58, v0, v1, s[72:73]\n\t" \
  "v_cndmask_b32_e64 v59, v0, v1, s[74:75]\n\t" \
  "s_nop 1\n\t" \
  "s_branch AY_%=\n\t" \
  "AE_%=:\n\t" \
  "s_nop 1\n\t"
#define N2_CLOB "v0", "v1", "v2", "v3", "v4", "v5", "v6", "v7", "v8", "v9", "v10", "v11", "v12", "v13", "v14", "v15", "v16", "v17", "v18", "v19", "v20", "v21", "v22", "v23", "v24", "v25", "v26", "v27", "v28", "v29", "v30", "v31", "v32", "v33", "v34", "v35", "v36", "v37", "v38", "v39", "v40", "v41", "v42", "v43", "v44", "v45", "v46", "v47", "v48", "v49", "v50", "v51", "v52", "v53", "v54", "v55", "v60", "v61", "v64", "v65", "v66", "v67", "v68", "v69", "v70", "v71", "v72", "v73", "v74", "v75", "v76", "v77", "v78", "v79", "v80", "v81", "v82", "v83", "v84", "v85", "v86", "v87", "v88", "v89", "v90", "v91", "v92", "v93", "v94", "v95", "v96", "v97", "v98", "v99", "v100", "v101", "v102", "v103", "v104", "v105", "v106", "v107", "v108", "v109", "v110", "v111", "v112", "v113", "v114", "v115", "v116", "v117", "v118", "v119", "v120", "v121", "v122", "v123", "v124", "v125", "v126", "v127", "v128", "v129", "v130", "v131", "v132", "v133", "v134", "v135", "v136", "v137", "v138", "v139", "v140", "v141", "v142", "v143", "v144", "v145", "v146", "v147", "v148", "v149", "v150", "v151", "v152", "v153", "v154", "v155", "v156", "v157", "v158", "v159", "v160", "v161", "v162", "v163", "v164", "v165", "v166", "v167", "s68", "s69", "s70", "s71", "s72", "s73", "s74", "s75", "s76", "s77", "s78", "s79", "s80", "s81", "s82", "s83", "s84", "s85"

#define CV_PRE 0
#define CV_Y 1
#define CV_ASM \
  "s_mov_b64 vcc, 1\n\t" \
  "ds_read_b64 v[0:1], v28 offset:0\n\t" \
  "ds_read_b64 v[2:3], v29 offset:0\n\t" \
  "ds_read_b64 v[4:5], v30 offset:1024\n\t" \
  "ds_read_b64 v[6:7], v31 offset:1024\n\t" \
  "ds_read_b64 v[8:9], v24 offset:2048\n\t" \
  "ds_read_b64 v[10:11], v25 offset:2048\n\t" \
  "ds_read_b64 v[12:13], v26 offset:3072\n\t" \
  "ds_read_b64 v[14:15], v27 offset:3072\n\t" \
  "ds_read_b64 v[16:17], v28 offset:4096\n\t" \
  "ds_read_b64 v[18:19], v29 offset:4096\n\t" \
  "ds_read_b64 v[20:21], v30 offset:5120\n\t" \
  "ds_read_b64 v[22:23], v31 offset:5120\n\t" \
  "s_waitcnt lgkmcnt(0)\n\t" \
  "v_mfma_f32_16x16x32_bf16 v[128:131], v[68:71], v[0:3], 0\n\t" \
  "v_mfma_f32_16x16x32_bf16 v[128:131], v[64:67], v[4:7], v[128:131]\n\t" \
  "v_mfma_f32_16x16x32_bf16 v[132:135], v[76:79], v[0:3], 0\n\t" \
  "v_mfma_f32_16x16x32_bf16 v[132:135], v[72:75], v[4:7], v[132:135]\n\t" \
  "v_mfma_f32_16x16x32_bf16 v[136:139], v[84:87], v[0:3], 0\n\t" \
  "v_mfma_f32_16x16x32_bf16 v[136:139], v[80:83], v[4:7], v[136:139]\n\t" \
  "v_mfma_f32_16x16x32_bf16 v[140:143], v[92:95], v[0:3], 0\n\t" \
  "v_mfma_f32_16x16x32_bf16 v[140:143], v[88:91], v[4:7], v[140:143]\n\t" \
  "v_mfma_f32_16x16x32_bf16 v[144:147], v[100:103], v[0:3], 0\n\t" \
  "v_mfma_f32_16x16x32_bf16 v[144:147], v[96:99], v[4:7], v[144:147]\n\t" \
  "ds_bpermute_b32 v34, v32, v131\n\t" \
  "v_mfma_f32_16x16x32_bf16 v[148:151], v[108:111], v[0:3], 0\n\t" \
  "v_mov_b32 v131, v130\n\t" \
  "v_mov_b32 v130, v129\n\t" \
  "v_mfma_f32_16x16x32_bf16 v[148:151], v[104:107], v[4:7], v[148:151]\n\t" \
  "v_mov_b32 v129, v128\n\t" \
  "v_mfma_f32_16x16x32_bf16 v[152:155], v[116:119], v[0:3], 0\n\t" \
  "v_mfma_f32_16x16x32_bf16 v[152:155], v[112:115], v[4:7], v[152:155]\n\t" \
  "v_mfma_f32_16x16x32_bf16 v[156:159], v[124:127], v[0:3], 0\n\t" \
  "ds_read_b64 v[0:1], v24 offset:6144\n\t" \
  "v_mfma_f32_16x16x32_bf16 v[156:159], v[120:123], v[4:7], v[156:159]\n\t" \
  "ds_read_b64 v[2:3], v25 offset:6144\n\t" \
  "v_mfma_f32_16x16x32_bf16 v[132:135], v[68:71], v[8:11], v[132:135]\n\t" \
  "ds_read_b64 v[4:5], v26 offset:7168\n\t" \
  "v_mfma_f32_16x16x32_bf16 v[132:135], v[64:67], v[12:15], v[132:135]\n\t" \
  "ds_read_b64 v[6:7], v27 offset:7168\n\t" \
  "v_mfma_f32_16x16x32_bf16 v[136:139], v[76:79], v[8:11], v[136:139]\n\t" \
  "v_mfma_f32_16x16x32_bf16 v[136:139], v[72:75], v[12:15], v[136:139]\n\t" \
  "v_mfma_f32_16x16x32_bf16 v[140:143], v[84:87], v[8:11], v[140:143]\n\t" \
  "s_waitcnt lgkmcnt(4)\n\t" \
  "v_cndmask_b32_e64 v128, v34, 0, vcc\n\t" \
  "v_mfma_f32_16x16x32_bf16 v[140:143], v[80:83], v[12:15], v[140:143]\n\t" \
  "v_mfma_f32_16x16x32_bf16 v[144:147], v[92:95], v[8:11], v[144:147]\n\t" \
  "ds_bpermute_b32 v35, v32, v135\n\t" \
  "v_mfma_f32_16x16x32_bf16 v[144:147], v[88:91], v[12:15], v[144:147]\n\t" \
  "v_mov_b32 v135, v134\n\t" \
  "v_mov_b32 v134, v133\n\t" \
  "v_mfma_f32_16x16x32_bf16 v[148:151], v[100:103], v[8:11], v[148:151]\n\t" \
  "v_mov_b32 v133, v132\n\t" \
  "v_mfma_f32_16x16x32_bf16 v[148:151], v[96:99], v[12:15], v[148:151]\n\t" \
  "v_mfma_f32_16x16x32_bf16 v[152:155], v[108:111], v[8:11], v[152:155]\n\t" \
  "v_mfma_f32_16x16x32_bf16 v[152:155], v[104:107], v[12:15], v[152:155]\n\t" \
  "v_mfma_f32_16x16x32_bf16 v[156:159], v[116:119], v[8:11], v[156:159]\n\t" \
  "ds_read_b64 v[8:9], v28 offset:8192\n\t" \
  "v_mfma_f32_16x16x32_bf16 v[156:159], v[112:115], v[12:15], v[156:159]\n\t" \
  "ds_read_b64 v[10:11], v29 offset:8192\n\t" \
  "v_mfma_f32_16x16x32_bf16 v[136:139], v[68:71], v[16:19], v[136:139]\n\t" \
  "ds_read_b64 v[12:13], v30 offset:9216\n\t" \
  "v_mfma_f32_16x16x32_bf16 v[136:139], v[64:67], v[20:23], v[136:139]\n\t" \
  "ds_read_b64 v[14:15], v31 offset:9216\n\t" \
  "v_mfma_f32_16x16x32_bf16 v[140:143], v[76:79], v[16:19], v[140:143]\n\t" \
  "v_mfma_f32_16x16x32_bf16 v[140:143], v[72:75], v[20:23], v[140:143]\n\t" \
  "s_waitcnt lgkmcnt(4)\n\t" \
  "v_cndmask_b32_e64 v132, v35, v34, vcc\n\t" \
  "v_mfma_f32_16x16x32_bf16 v[144:147], v[84:87], v[16:19], v[144:147]\n\t" \
  "v_mfma_f32_16x16x32_bf16 v[144:147], v[80:83], v[20:23], v[144:147]\n\t" \
  "v_mfma_f32_16x16x32_bf16 v[148:151], v[92:95], v[16:19], v[148:151]\n\t" \
  "ds_bpermute_b32 v36, v32, v139\n\t" \
  "v_mfma_f32_16x16x32_bf16 v[148:151], v[88:91], v[20:23], v[148:151]\n\t" \
  "v_mov_b32 v139, v138\n\t" \
  "v_mov_b32 v138, v137\n\t" \
  "v_mfma_f32_16x16x32_bf16 v[152:155], v[100:103], v[16:19], v[152:155]\n\t" \
  "v_mov_b32 v137, v136\n\t" \
  "v_mfma_f32_16x16x32_bf16 v[152:155], v[96:99], v[20:23], v[152:155]\n\t" \
  "v_mfma_f32_16x16x32_bf16 v[156:159], v[108:111], v[16:19], v[156:159]\n\t" \
  "ds_read_b64 v[16:17], v24 offset:10240\n\t" \
  "v_mfma_f32_16x16x32_bf16 v[156:159], v[104:107], v[20:23], v[156:159]\n\t" \
  "ds_read_b64 v[18:19], v25 offset:10240\n\t" \
  "v_mfma_f32_16x16x32_bf16 v[140:143], v[68:71], v[0:3], v[140:143]\n\t" \
  "ds_read_b64 v[20:21], v26 offset:11264\n\t" \
  "v_mfma_f32_16x16x32_bf16 v[140:143], v[64:67], v[4:7], v[140:143]\n\t" \
  "ds_read_b64 v[22:23], v27 offset:11264\n\t" \
  "v_mfma_f32_16x16x32_bf16 v[144:147], v[76:79], v[0:3], v[144:147]\n\t" \
  "v_mfma_f32_16x16x32_bf16 v[144:147], v[72:75], v[4:7], v[144:147]\n\t" \
  "v_mfma_f32_16x16x32_bf16 v[148:151], v[84:87], v[0:3], v[148:151]\n\t" \
  "v_mfma_f32_16x16x32_bf16 v[148:151], v[80:83], v[4:7], v[148:151]\n\t" \
  "s_waitcnt lgkmcnt(4)\n\t" \
  "v_cndmask_b32_e64 v136, v36, v35, vcc\n\t" \
  "v_mfma_f32_16x16x32_bf16 v[152:155], v[92:95], v[0:3], v[152:155]\n\t" \
  "ds_bpermute_b32 v37, v32, v143\n\t" \
  "v_mfma_f32_16x16x32_bf16 v[152:155], v[88:91], v[4:7], v[152:155]\n\t" \
  "v_mov_b32 v143, v142\n\t" \
  "v_mov_b32 v142, v141\n\t" \
  "v_mfma_f32_16x16x32_bf16 v[156:159], v[100:103], v[0:3], v[156:159]\n\t" \
  "ds_read_b64 v[0:1], v28 offset:12288\n\t" \
  "v_mov_b32 v141, v140\n\t" \
  "v_mfma_f32_16x16x32_bf16 v[156:159], v[96:99], v[4:7], v[156:159]\n\t" \
  "ds_read_b64 v[2:3], v29 offset:12288\n\t" \
  "v_mfma_f32_16x16x32_bf16 v[144:147], v[68:71], v[8:11], v[144:147]\n\t" \
  "ds_read_b64 v[4:5], v30 offset:13312\n\t" \
  "v_mfma_f32_16x16x32_bf16 v[144:147], v[64:67], v[12:15], v[144:147]\n\t" \
  "ds_read_b64 v[6:7], v31 offset:13312\n\t" \
  "v_mfma_f32_16x16x32_bf16 v[148:151], v[76:79], v[8:11], v[148:151]\n\t" \
  "v_mfma_f32_16x16x32_bf16 v[148:151], v[72:75], v[12:15], v[148:151]\n\t" \
  "v_mfma_f32_16x16x32_bf16 v[152:155], v[84:87], v[8:11], v[152:155]\n\t" \
  "v_mfma_f32_16x16x32_bf16 v[152:155], v[80:83], v[12:15], v[152:155]\n\t" \
  "v_mfma_f32_16x16x32_bf16 v[156:159], v[92:95], v[8:11], v[156:159]\n\t" \
  "ds_read_b64 v[8:9], v24 offset:14336\n\t" \
  "v_mfma_f32_16x16x32_bf16 v[156:159], v[88:91], v[12:15], v[156:159]\n\t" \
  "ds_read_b64 v[10:11], v25 offset:14336\n\t" \
  "s_waitcnt lgkmcnt(6)\n\t" \
  "v_mfma_f32_16x16x32_bf16 v[148:151], v[68:71], v[16:19], v[148:151]\n\t" \
  "ds_read_b64 v[12:13], v26 offset:15360\n\t" \
  "v_cndmask_b32_e64 v140, v37, v36, vcc\n\t" \
  "v_mfma_f32_16x16x32_bf16 v[148:151], v[64:67], v[20:23], v[148:151]\n\t" \
  "ds_read_b64 v[14:15], v27 offset:15360\n\t" \
  "v_mfma_f32_16x16x32_bf16 v[152:155], v[76:79], v[16:19], v[152:155]\n\t" \
  "ds_bpermute_b32 v38, v32, v147\n\t" \
  "v_mfma_f32_16x16x32_bf16 v[152:155], v[72:75], v[20:23], v[152:155]\n\t" \
  "global_load_dwordx4 v[44:47], v43, %[q0] offset:0\n\t" \
  "v_mfma_f32_16x16x32_bf16 v[156:159], v[84:87], v[16:19], v[156:159]\n\t" \
  "ds_read_b64 v[16:17], v26 offset:0\n\t" \
  "ds_read_b64 v[18:19], v27 offset:0\n\t" \
  "v_mfma_f32_16x16x32_bf16 v[156:159], v[80:83], v[20:23], v[156:159]\n\t" \
  "ds_read_b64 v[20:21], v28 offset:1024\n\t" \
  "ds_read_b64 v[22:23], v29 offset:1024\n\t" \
  "s_waitcnt lgkmcnt(9)\n\t" \
  "v_mfma_f32_16x16x32_bf16 v[152:155], v[68:71], v[0:3], v[152:155]\n\t" \
  "ds_bpermute_b32 v39, v32, v151\n\t" \
  "v_mov_b32 v147, v146\n\t" \
  "v_mfma_f32_16x16x32_bf16 v[152:155], v[64:67], v[4:7], v[152:155]\n\t" \
  "global_load_dwordx4 v[48:51], v43, %[q0] offset:2048\n\t" \
  "v_mfma_f32_16x16x32_bf16 v[156:159], v[76:79], v[0:3], v[156:159]\n\t" \
  "v_mov_b32 v146, v145\n\t" \
  "ds_read_b64 v[0:1], v30 offset:2048\n\t" \
  "v_mfma_f32_16x16x32_bf16 v[156:159], v[72:75], v[4:7], v[156:159]\n\t" \
  "ds_read_b64 v[2:3], v31 offset:2048\n\t" \
  "s_waitcnt lgkmcnt(10)\n\t" \
  "v_mfma_f32_16x16x32_bf16 v[156:159], v[68:71], v[8:11], v[156:159]\n\t" \
  "ds_bpermute_b32 v40, v32, v155\n\t" \
  "s_waitcnt lgkmcnt(9)\n\t" \
  "v_mfma_f32_16x16x32_bf16 v[156:159], v[64:67], v[12:15], v[156:159]\n\t" \
  "v_mov_b32 v145, v144\n\t" \
  "s_waitcnt lgkmcnt(6)\n\t" \
  "v_mfma_f32_16x16x32_bf16 v[128:131], v[68:71], v[16:19], v[128:131]\n\t" \
  "v_mov_b32 v151, v150\n\t" \
  "s_waitcnt lgkmcnt(4)\n\t" \
  "v_mfma_f32_16x16x32_bf16 v[128:131], v[64:67], v[20:23], v[128:131]\n\t" \
  "global_load_dwordx4 v[52:55], v43, %[q2] offset:0\n\t" \
  "v_mfma_f32_16x16x32_bf16 v[132:135], v[76:79], v[16:19], v[132:135]\n\t" \
  "ds_bpermute_b32 v41, v32, v159\n\t" \
  "v_cndmask_b32_e64 v144, v38, v37, vcc\n\t" \
  "v_mfma_f32_16x16x32_bf16 v[132:135], v[72:75], v[20:23], v[132:135]\n\t" \
  "ds_read_b64 v[4:5], v24 offset:3072\n\t" \
  "v_mov_b32 v150, v149\n\t" \
  "v_mfma_f32_16x16x32_bf16 v[136:139], v[84:87], v[16:19], v[136:139]\n\t" \
  "ds_read_b64 v[6:7], v25 offset:3072\n\t" \
  "v_mov_b32 v149, v148\n\t" \
  "v_mfma_f32_16x16x32_bf16 v[136:139], v[80:83], v[20:23], v[136:139]\n\t" \
  "v_mov_b32 v155, v154\n\t" \
  "v_mov_b32 v154, v153\n\t" \
  "v_mfma_f32_16x16x32_bf16 v[140:143], v[92:95], v[16:19], v[140:143]\n\t" \
  "s_waitcnt lgkmcnt(6)\n\t" \
  "v_cndmask_b32_e64 v148, v39, v38, vcc\n\t" \
  "v_mfma_f32_16x16x32_bf16 v[140:143], v[88:91], v[20:23], v[140:143]\n\t" \
  "v_mov_b32 v153, v152\n\t" \
  "v_mov_b32 v159, v158\n\t" \
  "v_mfma_f32_16x16x32_bf16 v[144:147], v[100:103], v[16:19], v[144:147]\n\t" \
  "global_load_dwordx4 v[56:59], v43, %[q2] offset:2048\n\t" \
  "v_mfma_f32_16x16x32_bf16 v[144:147], v[96:99], v[20:23], v[144:147]\n\t" \
  "s_waitcnt lgkmcnt(3)\n\t" \
  "v_cndmask_b32_e64 v152, v40, v39, vcc\n\t" \
  "v_mfma_f32_16x16x32_bf16 v[148:151], v[108:111], v[16:19], v[148:151]\n\t" \
  "v_mov_b32 v158, v157\n\t" \
  "v_mov_b32 v157, v156\n\t" \
  "v_mfma_f32_16x16x32_bf16 v[148:151], v[104:107], v[20:23], v[148:151]\n\t" \
  "s_waitcnt lgkmcnt(2)\n\t" \
  "v_cndmask_b32_e64 v156, v41, v40, vcc\n\t" \
  "v_mfma_f32_16x16x32_bf16 v[152:155], v[116:119], v[16:19], v[152:155]\n\t" \
  "ds_read_b64 v[8:9], v26 offset:4096\n\t" \
  "v_mfma_f32_16x16x32_bf16 v[152:155], v[112:115], v[20:23], v[152:155]\n\t" \
  "ds_read_b64 v[10:11], v27 offset:4096\n\t" \
  "v_mfma_f32_16x16x32_bf16 v[156:159], v[124:127], v[16:19], v[156:159]\n\t" \
  "ds_read_b64 v[12:13], v28 offset:5120\n\t" \
  "v_mfma_f32_16x16x32_bf16 v[156:159], v[120:123], v[20:23], v[156:159]\n\t" \
  "ds_read_b64 v[14:15], v29 offset:5120\n\t" \
  "v_mfma_f32_16x16x32_bf16 v[132:135], v[68:71], v[0:3], v[132:135]\n\t" \
  "ds_read_b64 v[16:17], v30 offset:6144\n\t" \
  "s_waitcnt lgkmcnt(5)\n\t" \
  "v_mfma_f32_16x16x32_bf16 v[132:135], v[64:67], v[4:7], v[132:135]\n\t" \
  "ds_read_b64 v[18:19], v31 offset:6144\n\t" \
  "v_mfma_f32_16x16x32_bf16 v[136:139], v[76:79], v[0:3], v[136:139]\n\t" \
  "ds_read_b64 v[20:21], v24 offset:7168\n\t" \
  "v_mfma_f32_16x16x32_bf16 v[136:139], v[72:75], v[4:7], v[136:139]\n\t" \
  "ds_read_b64 v[22:23], v25 offset:7168\n\t" \
  "v_mfma_f32_16x16x32_bf16 v[140:143], v[84:87], v[0:3], v[140:143]\n\t" \
  "ds_bpermute_b32 v34, v32, v131\n\t" \
  "v_mfma_f32_16x16x32_bf16 v[140:143], v[80:83], v[4:7], v[140:143]\n\t" \
  "ds_bpermute_b32 v35, v32, v135\n\t" \
  "v_mov_b32 v131, v130\n\t" \
  "v_mfma_f32_16x16x32_bf16 v[144:147], v[92:95], v[0:3], v[144:147]\n\t" \
  "v_mov_b32 v130, v129\n\t" \
  "v_mov_b32 v129, v128\n\t" \
  "v_mfma_f32_16x16x32_bf16 v[144:147], v[88:91], v[4:7], v[144:147]\n\t" \
  "v_mov_b32 v135, v134\n\t" \
  "v_mov_b32 v134, v133\n\t" \
  "v_mfma_f32_16x16x32_bf16 v[148:151], v[100:103], v[0:3], v[148:151]\n\t" \
  "v_mov_b32 v133, v132\n\t" \
  "v_mfma_f32_16x16x32_bf16 v[148:151], v[96:99], v[4:7], v[148:151]\n\t" \
  "v_mfma_f32_16x16x32_bf16 v[152:155], v[108:111], v[0:3], v[152:155]\n\t" \
  "v_mfma_f32_16x16x32_bf16 v[152:155], v[104:107], v[4:7], v[152:155]\n\t" \
  "v_mfma_f32_16x16x32_bf16 v[156:159], v[116:119], v[0:3], v[156:159]\n\t" \
  "ds_read_b64 v[0:1], v26 offset:8192\n\t" \
  "v_mfma_f32_16x16x32_bf16 v[156:159], v[112:115], v[4:7], v[156:159]\n\t" \
  "ds_read_b64 v[2:3], v27 offset:8192\n\t" \
  "s_waitcnt lgkmcnt(6)\n\t" \
  "v_mfma_f32_16x16x32_bf16 v[136:139], v[68:71], v[8:11], v[136:139]\n\t" \
  "ds_read_b64 v[4:5], v28 offset:9216\n\t" \
  "v_mfma_f32_16x16x32_bf16 v[136:139], v[64:67], v[12:15], v[136:139]\n\t" \
  "ds_read_b64 v[6:7], v29 offset:9216\n\t" \
  "v_mfma_f32_16x16x32_bf16 v[140:143], v[76:79], v[8:11], v[140:143]\n\t" \
  "s_waitcnt lgkmcnt(5)\n\t" \
  "v_cndmask_b32_e64 v128, v34, 0, vcc\n\t" \
  "v_mfma_f32_16x16x32_bf16 v[140:143], v[72:75], v[12:15], v[140:143]\n\t" \
  "s_waitcnt lgkmcnt(4)\n\t" \
  "v_cndmask_b32_e64 v132, v35, v34, vcc\n\t" \
  "v_mfma_f32_16x16x32_bf16 v[144:147], v[84:87], v[8:11], v[144:147]\n\t" \
  "ds_bpermute_b32 v36, v32, v139\n\t" \
  "v_mfma_f32_16x16x32_bf16 v[144:147], v[80:83], v[12:15], v[144:147]\n\t" \
  "v_mov_b32 v139, v138\n\t" \
  "v_mov_b32 v138, v137\n\t" \
  "v_mfma_f32_16x16x32_bf16 v[148:151], v[92:95], v[8:11], v[148:151]\n\t" \
  "v_mov_b32 v137, v136\n\t" \
  "v_mfma_f32_16x16x32_bf16 v[148:151], v[88:91], v[12:15], v[148:151]\n\t" \
  "v_mfma_f32_16x16x32_bf16 v[152:155], v[100:103], v[8:11], v[152:155]\n\t" \
  "v_mfma_f32_16x16x32_bf16 v[152:155], v[96:99], v[12:15], v[152:155]\n\t" \
  "v_mfma_f32_16x16x32_bf16 v[156:159], v[108:111], v[8:11], v[156:159]\n\t" \
  "ds_read_b64 v[8:9], v30 offset:10240\n\t" \
  "v_mfma_f32_16x16x32_bf16 v[156:159], v[104:107], v[12:15], v[156:159]\n\t" \
  "ds_read_b64 v[10:11], v31 offset:10240\n\t" \
  "v_mfma_f32_16x16x32_bf16 v[140:143], v[68:71], v[16:19], v[140:143]\n\t" \
  "ds_read_b64 v[12:13], v24 offset:11264\n\t" \
  "v_mfma_f32_16x16x32_bf16 v[140:143], v[64:67], v[20:23], v[140:143]\n\t" \
  "ds_read_b64 v[14:15], v25 offset:11264\n\t" \
  "v_mfma_f32_16x16x32_bf16 v[144:147], v[76:79], v[16:19], v[144:147]\n\t" \
  "v_mfma_f32_16x16x32_bf16 v[144:147], v[72:75], v[20:23], v[144:147]\n\t" \
  "s_waitcnt lgkmcnt(4)\n\t" \
  "v_cndmask_b32_e64 v136, v36, v35, vcc\n\t" \
  "v_mfma_f32_16x16x32_bf16 v[148:151], v[84:87], v[16:19], v[148:151]\n\t" \
  "v_mfma_f32_16x16x32_bf16 v[148:151], v[80:83], v[20:23], v[148:151]\n\t" \
  "v_mfma_f32_16x16x32_bf16 v[152:155], v[92:95], v[16:19], v[152:155]\n\t" \
  "ds_bpermute_b32 v37, v32, v143\n\t" \
  "v_mfma_f32_16x16x32_bf16 v[152:155], v[88:91], v[20:23], v[152:155]\n\t" \
  "v_mov_b32 v143, v142\n\t" \
  "v_mov_b32 v142, v141\n\t" \
  "v_mfma_f32_16x16x32_bf16 v[156:159], v[100:103], v[16:19], v[156:159]\n\t" \
  "ds_read_b64 v[16:17], v26 offset:12288\n\t" \
  "v_mov_b32 v141, v140\n\t" \
  "v_mfma_f32_16x16x32_bf16 v[156:159], v[96:99], v[20:23], v[156:159]\n\t" \
  "ds_read_b64 v[18:19], v27 offset:12288\n\t" \
  "v_mfma_f32_16x16x32_bf16 v[144:147], v[68:71], v[0:3], v[144:147]\n\t" \
  "ds_read_b64 v[20:21], v28 offset:13312\n\t" \
  "v_mfma_f32_16x16x32_bf16 v[144:147], v[64:67], v[4:7], v[144:147]\n\t" \
  "ds_read_b64 v[22:23], v29 offset:13312\n\t" \
  "v_mfma_f32_16x16x32_bf16 v[148:151], v[76:79], v[0:3], v[148:151]\n\t" \
  "v_mfma_f32_16x16x32_bf16 v[148:151], v[72:75], v[4:7], v[148:151]\n\t" \
  "v_mfma_f32_16x16x32_bf16 v[152:155], v[84:87], v[0:3], v[152:155]\n\t" \
  "v_mfma_f32_16x16x32_bf16 v[152:155], v[80:83], v[4:7], v[152:155]\n\t" \
  "v_mfma_f32_16x16x32_bf16 v[156:159], v[92:95], v[0:3], v[156:159]\n\t" \
  "ds_read_b64 v[0:1], v30 offset:14336\n\t" \
  "v_mfma_f32_16x16x32_bf16 v[156:159], v[88:91], v[4:7], v[156:159]\n\t" \
  "ds_read_b64 v[2:3], v31 offset:14336\n\t" \
  "s_waitcnt lgkmcnt(6)\n\t" \
  "v_mfma_f32_16x16x32_bf16 v[148:151], v[68:71], v[8:11], v[148:151]\n\t" \
  "ds_read_b64 v[4:5], v24 offset:15360\n\t" \
  "v_cndmask_b32_e64 v140, v37, v36, vcc\n\t" \
  "v_mfma_f32_16x16x32_bf16 v[148:151], v[64:67], v[12:15], v[148:151]\n\t" \
  "ds_read_b64 v[6:7], v25 offset:15360\n\t" \
  "v_mfma_f32_16x16x32_bf16 v[152:155], v[76:79], v[8:11], v[152:155]\n\t" \
  "ds_bpermute_b32 v38, v32, v147\n\t" \
  "v_mfma_f32_16x16x32_bf16 v[152:155], v[72:75], v[12:15], v[152:155]\n\t" \
  "v_mov_b32 v147, v146\n\t" \
  "v_mov_b32 v146, v145\n\t" \
  "v_mfma_f32_16x16x32_bf16 v[156:159], v[84:87], v[8:11], v[156:159]\n\t" \
  "ds_read_b64 v[8:9], v24 offset:0\n\t" \
  "ds_read_b64 v[10:11], v25 offset:0\n\t" \
  "v_mfma_f32_16x16x32_bf16 v[156:159], v[80:83], v[12:15], v[156:159]\n\t" \
  "ds_read_b64 v[12:13], v26 offset:1024\n\t" \
  "ds_read_b64 v[14:15], v27 offset:1024\n\t" \
  "s_waitcnt lgkmcnt(9)\n\t" \
  "v_mfma_f32_16x16x32_bf16 v[152:155], v[68:71], v[16:19], v[152:155]\n\t" \
  "ds_bpermute_b32 v39, v32, v151\n\t" \
  "v_mov_b32 v145, v144\n\t" \
  "v_mfma_f32_16x16x32_bf16 v[152:155], v[64:67], v[20:23], v[152:155]\n\t" \
  "v_mov_b32 v151, v150\n\t" \
  "v_mov_b32 v150, v149\n\t" \
  "v_mfma_f32_16x16x32_bf16 v[156:159], v[76:79], v[16:19], v[156:159]\n\t" \
  "ds_read_b64 v[16:17], v28 offset:2048\n\t" \
  "v_mov_b32 v149, v148\n\t" \
  "v_mfma_f32_16x16x32_bf16 v[156:159], v[72:75], v[20:23], v[156:159]\n\t" \
  "ds_read_b64 v[18:19], v29 offset:2048\n\t" \
  "s_waitcnt lgkmcnt(10)\n\t" \
  "v_mfma_f32_16x16x32_bf16 v[156:159], v[68:71], v[0:3], v[156:159]\n\t" \
  "ds_bpermute_b32 v40, v32, v155\n\t" \
  "s_waitcnt lgkmcnt(9)\n\t" \
  "v_mfma_f32_16x16x32_bf16 v[156:159], v[64:67], v[4:7], v[156:159]\n\t" \
  "ds_read_b64 v[20:21], v30 offset:3072\n\t" \
  "s_waitcnt lgkmcnt(7)\n\t" \
  "v_mfma_f32_16x16x32_bf16 v[128:131], v[68:71], v[8:11], v[128:131]\n\t" \
  "ds_read_b64 v[22:23], v31 offset:3072\n\t" \
  "s_waitcnt lgkmcnt(6)\n\t" \
  "v_mfma_f32_16x16x32_bf16 v[128:131], v[64:67], v[12:15], v[128:131]\n\t" \
  "v_cndmask_b32_e64 v144, v38, v37, vcc\n\t" \
  "v_mov_b32 v155, v154\n\t" \
  "v_mfma_f32_16x16x32_bf16 v[132:135], v[76:79], v[8:11], v[132:135]\n\t" \
  "ds_bpermute_b32 v41, v32, v159\n\t" \
  "v_mov_b32 v154, v153\n\t" \
  "v_mfma_f32_16x16x32_bf16 v[132:135], v[72:75], v[12:15], v[132:135]\n\t" \
  "v_mov_b32 v153, v152\n\t" \
  "v_mov_b32 v159, v158\n\t" \
  "v_mfma_f32_16x16x32_bf16 v[136:139], v[84:87], v[8:11], v[136:139]\n\t" \
  "v_mov_b32 v158, v157\n\t" \
  "v_mov_b32 v157, v156\n\t" \
  "v_mfma_f32_16x16x32_bf16 v[136:139], v[80:83], v[12:15], v[136:139]\n\t" \
  "s_waitcnt lgkmcnt(6)\n\t" \
  "v_cndmask_b32_e64 v148, v39, v38, vcc\n\t" \
  "v_mfma_f32_16x16x32_bf16 v[140:143], v[92:95], v[8:11], v[140:143]\n\t" \
  "ds_read_b64 v[0:1], v24 offset:4096\n\t" \
  "v_mfma_f32_16x16x32_bf16 v[140:143], v[88:91], v[12:15], v[140:143]\n\t" \
  "ds_read_b64 v[2:3], v25 offset:4096\n\t" \
  "v_mfma_f32_16x16x32_bf16 v[144:147], v[100:103], v[8:11], v[144:147]\n\t" \
  "s_waitcnt lgkmcnt(5)\n\t" \
  "v_cndmask_b32_e64 v152, v40, v39, vcc\n\t" \
  "v_mfma_f32_16x16x32_bf16 v[144:147], v[96:99], v[12:15], v[144:147]\n\t" \
  "ds_read_b64 v[4:5], v26 offset:5120\n\t" \
  "v_mfma_f32_16x16x32_bf16 v[148:151], v[108:111], v[8:11], v[148:151]\n\t" \
  "s_waitcnt lgkmcnt(3)\n\t" \
  "v_cndmask_b32_e64 v156, v41, v40, vcc\n\t" \
  "v_mfma_f32_16x16x32_bf16 v[148:151], v[104:107], v[12:15], v[148:151]\n\t" \
  "ds_read_b64 v[6:7], v27 offset:5120\n\t" \
  "v_mfma_f32_16x16x32_bf16 v[152:155], v[116:119], v[8:11], v[152:155]\n\t" \
  "ds_bpermute_b32 v34, v32, v131\n\t" \
  "v_mfma_f32_16x16x32_bf16 v[152:155], v[112:115], v[12:15], v[152:155]\n\t" \
  "v_mov_b32 v131, v130\n\t" \
  "v_mov_b32 v130, v129\n\t" \
  "v_mfma_f32_16x16x32_bf16 v[156:159], v[124:127], v[8:11], v[156:159]\n\t" \
  "ds_read_b64 v[8:9], v28 offset:6144\n\t" \
  "v_mov_b32 v129, v128\n\t" \
  "v_mfma_f32_16x16x32_bf16 v[156:159], v[120:123], v[12:15], v[156:159]\n\t" \
  "ds_read_b64 v[10:11], v29 offset:6144\n\t" \
  "v_mfma_f32_16x16x32_bf16 v[132:135], v[68:71], v[16:19], v[132:135]\n\t" \
  "ds_read_b64 v[12:13], v30 offset:7168\n\t" \
  "v_mfma_f32_16x16x32_bf16 v[132:135], v[64:67], v[20:23], v[132:135]\n\t" \
  "ds_read_b64 v[14:15], v31 offset:7168\n\t" \
  "v_mfma_f32_16x16x32_bf16 v[136:139], v[76:79], v[16:19], v[136:139]\n\t" \
  "v_mfma_f32_16x16x32_bf16 v[136:139], v[72:75], v[20:23], v[136:139]\n\t" \
  "v_mfma_f32_16x16x32_bf16 v[140:143], v[84:87], v[16:19], v[140:143]\n\t" \
  "v_mfma_f32_16x16x32_bf16 v[140:143], v[80:83], v[20:23], v[140:143]\n\t" \
  "v_mfma_f32_16x16x32_bf16 v[144:147], v[92:95], v[16:19], v[144:147]\n\t" \
  "v_mfma_f32_16x16x32_bf16 v[144:147], v[88:91], v[20:23], v[144:147]\n\t" \
  "s_waitcnt lgkmcnt(4)\n\t" \
  "v_cndmask_b32_e64 v128, v34, 0, vcc\n\t" \
  "v_mfma_f32_16x16x32_bf16 v[148:151], v[100:103], v[16:19], v[148:151]\n\t" \
  "ds_bpermute_b32 v35, v32, v135\n\t" \
  "v_mfma_f32_16x16x32_bf16 v[148:151], v[96:99], v[20:23], v[148:151]\n\t" \
  "v_mov_b32 v135, v134\n\t" \
  "v_mov_b32 v134, v133\n\t" \
  "v_mfma_f32_16x16x32_bf16 v[152:155], v[108:111], v[16:19], v[152:155]\n\t" \
  "v_mov_b32 v133, v132\n\t" \
  "v_mfma_f32_16x16x32_bf16 v[152:155], v[104:107], v[20:23], v[152:155]\n\t" \
  "v_mfma_f32_16x16x32_bf16 v[156:159], v[116:119], v[16:19], v[156:159]\n\t" \
  "ds_read_b64 v[16:17], v24 offset:8192\n\t" \
  "v_mfma_f32_16x16x32_bf16 v[156:159], v[112:115], v[20:23], v[156:159]\n\t" \
  "ds_read_b64 v[18:19], v25 offset:8192\n\t" \
  "v_mfma_f32_16x16x32_bf16 v[136:139], v[68:71], v[0:3], v[136:139]\n\t" \
  "ds_read_b64 v[20:21], v26 offset:9216\n\t" \
  "v_mfma_f32_16x16x32_bf16 v[136:139], v[64:67], v[4:7], v[136:139]\n\t" \
  "ds_read_b64 v[22:23], v27 offset:9216\n\t" \
  "v_mfma_f32_16x16x32_bf16 v[140:143], v[76:79], v[0:3], v[140:143]\n\t" \
  "v_mfma_f32_16x16x32_bf16 v[140:143], v[72:75], v[4:7], v[140:143]\n\t" \
  "v_mfma_f32_16x16x32_bf16 v[144:147], v[84:87], v[0:3], v[144:147]\n\t" \
  "v_mfma_f32_16x16x32_bf16 v[144:147], v[80:83], v[4:7], v[144:147]\n\t" \
  "s_waitcnt lgkmcnt(4)\n\t" \
  "v_cndmask_b32_e64 v132, v35, v34, vcc\n\t" \
  "v_mfma_f32_16x16x32_bf16 v[148:151], v[92:95], v[0:3], v[148:151]\n\t" \
  "ds_bpermute_b32 v36, v32, v139\n\t" \
  "v_mfma_f32_16x16x32_bf16 v[148:151], v[88:91], v[4:7], v[148:151]\n\t" \
  "v_mov_b32 v139, v138\n\t" \
  "v_mov_b32 v138, v137\n\t" \
  "v_mfma_f32_16x16x32_bf16 v[152:155], v[100:103], v[0:3], v[152:155]\n\t" \
  "v_mov_b32 v137, v136\n\t" \
  "v_mfma_f32_16x16x32_bf16 v[152:155], v[96:99], v[4:7], v[152:155]\n\t" \
  "v_mfma_f32_16x16x32_bf16 v[156:159], v[108:111], v[0:3], v[156:159]\n\t" \
  "ds_read_b64 v[0:1], v28 offset:10240\n\t" \
  "v_mfma_f32_16x16x32_bf16 v[156:159], v[104:107], v[4:7], v[156:159]\n\t" \
  "ds_read_b64 v[2:3], v29 offset:10240\n\t" \
  "v_mfma_f32_16x16x32_bf16 v[140:143], v[68:71], v[8:11], v[140:143]\n\t" \
  "ds_read_b64 v[4:5], v30 offset:11264\n\t" \
  "v_mfma_f32_16x16x32_bf16 v[140:143], v[64:67], v[12:15], v[140:143]\n\t" \
  "ds_read_b64 v[6:7], v31 offset:11264\n\t" \
  "v_mfma_f32_16x16x32_bf16 v[144:147], v[76:79], v[8:11], v[144:147]\n\t" \
  "v_mfma_f32_16x16x32_bf16 v[144:147], v[72:75], v[12:15], v[144:147]\n\t" \
  "v_mfma_f32_16x16x32_bf16 v[148:151], v[84:87], v[8:11], v[148:151]\n\t" \
  "v_mfma_f32_16x16x32_bf16 v[148:151], v[80:83], v[12:15], v[148:151]\n\t" \
  "s_waitcnt lgkmcnt(4)\n\t" \
  "v_cndmask_b32_e64 v136, v36, v35, vcc\n\t" \
  "v_mfma_f32_16x16x32_bf16 v[152:155], v[92:95], v[8:11], v[152:155]\n\t" \
  "ds_bpermute_b32 v37, v32, v143\n\t" \
  "v_mfma_f32_16x16x32_bf16 v[152:155], v[88:91], v[12:15], v[152:155]\n\t" \
  "v_mov_b32 v143, v142\n\t" \
  "v_mov_b32 v142, v141\n\t" \
  "v_mfma_f32_16x16x32_bf16 v[156:159], v[100:103], v[8:11], v[156:159]\n\t" \
  "ds_read_b64 v[8:9], v24 offset:12288\n\t" \
  "v_mov_b32 v141, v140\n\t" \
  "v_mfma_f32_16x16x32_bf16 v[156:159], v[96:99], v[12:15], v[156:159]\n\t" \
  "ds_read_b64 v[10:11], v25 offset:12288\n\t" \
  "v_mfma_f32_16x16x32_bf16 v[144:147], v[68:71], v[16:19], v[144:147]\n\t" \
  "ds_read_b64 v[12:13], v26 offset:13312\n\t" \
  "v_mfma_f32_16x16x32_bf16 v[144:147], v[64:67], v[20:23], v[144:147]\n\t" \
  "ds_read_b64 v[14:15], v27 offset:13312\n\t" \
  "v_mfma_f32_16x16x32_bf16 v[148:151], v[76:79], v[16:19], v[148:151]\n\t" \
  "v_mfma_f32_16x16x32_bf16 v[148:151], v[72:75], v[20:23], v[148:151]\n\t" \
  "v_mfma_f32_16x16x32_bf16 v[152:155], v[84:87], v[16:19], v[152:155]\n\t" \
  "v_mfma_f32_16x16x32_bf16 v[152:155], v[80:83], v[20:23], v[152:155]\n\t" \
  "v_mfma_f32_16x16x32_bf16 v[156:159], v[92:95], v[16:19], v[156:159]\n\t" \
  "ds_read_b64 v[16:17], v28 offset:14336\n\t" \
  "v_mfma_f32_16x16x32_bf16 v[156:159], v[88:91], v[20:23], v[156:159]\n\t" \
  "ds_read_b64 v[18:19], v29 offset:14336\n\t" \
  "s_waitcnt lgkmcnt(6)\n\t" \
  "v_mfma_f32_16x16x32_bf16 v[148:151], v[68:71], v[0:3], v[148:151]\n\t" \
  "ds_read_b64 v[20:21], v30 offset:15360\n\t" \
  "v_cndmask_b32_e64 v140, v37, v36, vcc\n\t" \
  "v_mfma_f32_16x16x32_bf16 v[148:151], v[64:67], v[4:7], v[148:151]\n\t" \
  "ds_read_b64 v[22:23], v31 offset:15360\n\t" \
  "v_mfma_f32_16x16x32_bf16 v[152:155], v[76:79], v[0:3], v[152:155]\n\t" \
  "ds_bpermute_b32 v38, v32, v147\n\t" \
  "v_mfma_f32_16x16x32_bf16 v[152:155], v[72:75], v[4:7], v[152:155]\n\t" \
  "v_mov_b32 v147, v146\n\t" \
  "v_mov_b32 v146, v145\n\t" \
  "v_mfma_f32_16x16x32_bf16 v[156:159], v[84:87], v[0:3], v[156:159]\n\t" \
  "ds_read_b64 v[0:1], v30 offset:0\n\t" \
  "ds_read_b64 v[2:3], v31 offset:0\n\t" \
  "v_mfma_f32_16x16x32_bf16 v[156:159], v[80:83], v[4:7], v[156:159]\n\t" \
  "ds_read_b64 v[4:5], v24 offset:1024\n\t" \
  "ds_read_b64 v[6:7], v25 offset:1024\n\t" \
  "s_waitcnt lgkmcnt(9)\n\t" \
  "v_mfma_f32_16x16x32_bf16 v[152:155], v[68:71], v[8:11], v[152:155]\n\t" \
  "ds_bpermute_b32 v39, v32, v151\n\t" \
  "v_mov_b32 v145, v144\n\t" \
  "v_mfma_f32_16x16x32_bf16 v[152:155], v[64:67], v[12:15], v[152:155]\n\t" \
  "v_mov_b32 v151, v150\n\t" \
  "v_mov_b32 v150, v149\n\t" \
  "v_mfma_f32_16x16x32_bf16 v[156:159], v[76:79], v[8:11], v[156:159]\n\t" \
  "ds_read_b64 v[8:9], v26 offset:2048\n\t" \
  "v_mov_b32 v149, v148\n\t" \
  "v_mfma_f32_16x16x32_bf16 v[156:159], v[72:75], v[12:15], v[156:159]\n\t" \
  "ds_read_b64 v[10:11], v27 offset:2048\n\t" \
  "s_waitcnt lgkmcnt(10)\n\t" \
  "v_mfma_f32_16x16x32_bf16 v[156:159], v[68:71], v[16:19], v[156:159]\n\t" \
  "ds_bpermute_b32 v40, v32, v155\n\t" \
  "s_waitcnt lgkmcnt(9)\n\t" \
  "v_mfma_f32_16x16x32_bf16 v[156:159], v[64:67], v[20:23], v[156:159]\n\t" \
  "ds_read_b64 v[12:13], v28 offset:3072\n\t" \
  "s_waitcnt lgkmcnt(7)\n\t" \
  "v_mfma_f32_16x16x32_bf16 v[128:131], v[68:71], v[0:3], v[128:131]\n\t" \
  "ds_read_b64 v[14:15], v29 offset:3072\n\t" \
  "s_waitcnt lgkmcnt(6)\n\t" \
  "v_mfma_f32_16x16x32_bf16 v[128:131], v[64:67], v[4:7], v[128:131]\n\t" \
  "v_cndmask_b32_e64 v144, v38, v37, vcc\n\t" \
  "v_mov_b32 v155, v154\n\t" \
  "v_mfma_f32_16x16x32_bf16 v[132:135], v[76:79], v[0:3], v[132:135]\n\t" \
  "ds_bpermute_b32 v41, v32, v159\n\t" \
  "v_mov_b32 v154, v153\n\t" \
  "v_mfma_f32_16x16x32_bf16 v[132:135], v[72:75], v[4:7], v[132:135]\n\t" \
  "v_mov_b32 v153, v152\n\t" \
  "v_mov_b32 v159, v158\n\t" \
  "v_mfma_f32_16x16x32_bf16 v[136:139], v[84:87], v[0:3], v[136:139]\n\t" \
  "v_mov_b32 v158, v157\n\t" \
  "v_mov_b32 v157, v156\n\t" \
  "v_mfma_f32_16x16x32_bf16 v[136:139], v[80:83], v[4:7], v[136:139]\n\t" \
  "s_waitcnt lgkmcnt(6)\n\t" \
  "v_cndmask_b32_e64 v148, v39, v38, vcc\n\t" \
  "v_mfma_f32_16x16x32_bf16 v[140:143], v[92:95], v[0:3], v[140:143]\n\t" \
  "ds_read_b64 v[16:17], v30 offset:4096\n\t" \
  "v_mfma_f32_16x16x32_bf16 v[140:143], v[88:91], v[4:7], v[140:143]\n\t" \
  "ds_read_b64 v[18:19], v31 offset:4096\n\t" \
  "v_mfma_f32_16x16x32_bf16 v[144:147], v[100:103], v[0:3], v[144:147]\n\t" \
  "s_waitcnt lgkmcnt(5)\n\t" \
  "v_cndmask_b32_e64 v152, v40, v39, vcc\n\t" \
  "v_mfma_f32_16x16x32_bf16 v[144:147], v[96:99], v[4:7], v[144:147]\n\t" \
  "ds_read_b64 v[20:21], v24 offset:5120\n\t" \
  "v_mfma_f32_16x16x32_bf16 v[148:151], v[108:111], v[0:3], v[148:151]\n\t" \
  "s_waitcnt lgkmcnt(3)\n\t" \
  "v_cndmask_b32_e64 v156, v41, v40, vcc\n\t" \
  "v_mfma_f32_16x16x32_bf16 v[148:151], v[104:107], v[4:7], v[148:151]\n\t" \
  "v_cvt_pk_bf16_f32 v34, v128, v129\n\t" \
  "v_cvt_pk_bf16_f32 v35, v130, v131\n\t" \
  "v_mfma_f32_16x16x32_bf16 v[152:155], v[116:119], v[0:3], v[152:155]\n\t" \
  "ds_read_b64 v[22:23], v25 offset:5120\n\t" \
  "v_mfma_f32_16x16x32_bf16 v[152:155], v[112:115], v[4:7], v[152:155]\n\t" \
  "v_mfma_f32_16x16x32_bf16 v[156:159], v[124:127], v[0:3], v[156:159]\n\t" \
  "ds_read_b64 v[0:1], v26 offset:6144\n\t" \
  "v_mfma_f32_16x16x32_bf16 v[156:159], v[120:123], v[4:7], v[156:159]\n\t" \
  "ds_read_b64 v[2:3], v27 offset:6144\n\t" \
  "v_mfma_f32_16x16x32_bf16 v[132:135], v[68:71], v[8:11], v[132:135]\n\t" \
  "ds_read_b64 v[4:5], v28 offset:7168\n\t" \
  "v_mfma_f32_16x16x32_bf16 v[132:135], v[64:67], v[12:15], v[132:135]\n\t" \
  "ds_read_b64 v[6:7], v29 offset:7168\n\t" \
  "v_mfma_f32_16x16x32_bf16 v[136:139], v[76:79], v[8:11], v[136:139]\n\t" \
  "v_mfma_f32_16x16x32_bf16 v[136:139], v[72:75], v[12:15], v[136:139]\n\t" \
  "v_mfma_f32_16x16x32_bf16 v[140:143], v[84:87], v[8:11], v[140:143]\n\t" \
  "v_mfma_f32_16x16x32_bf16 v[140:143], v[80:83], v[12:15], v[140:143]\n\t" \
  "v_mfma_f32_16x16x32_bf16 v[144:147], v[92:95], v[8:11], v[144:147]\n\t" \
  "v_mfma_f32_16x16x32_bf16 v[144:147], v[88:91], v[12:15], v[144:147]\n\t" \
  "v_mfma_f32_16x16x32_bf16 v[148:151], v[100:103], v[8:11], v[148:151]\n\t" \
  "v_cvt_pk_bf16_f32 v36, v132, v133\n\t" \
  "v_cvt_pk_bf16_f32 v37, v134, v135\n\t" \
  "v_mfma_f32_16x16x32_bf16 v[148:151], v[96:99], v[12:15], v[148:151]\n\t" \
  "s_mov_b64 exec, 1\n\t" \
  "ds_write_b32 v33, v33\n\t" \
  "s_mov_b64 exec, -1\n\t" \
  "v_mfma_f32_16x16x32_bf16 v[152:155], v[108:111], v[8:11], v[152:155]\n\t" \
  "v_permlane16_swap_b32 v34, v36\n\t" \
  "v_permlane16_swap_b32 v35, v37\n\t" \
  "v_mfma_f32_16x16x32_bf16 v[152:155], v[104:107], v[12:15], v[152:155]\n\t" \
  "global_store_dwordx4 v42, v[34:37], %[yb] offset:0 sc1\n\t" \
  "v_mfma_f32_16x16x32_bf16 v[156:159], v[116:119], v[8:11], v[156:159]\n\t" \
  "ds_read_b64 v[8:9], v30 offset:8192\n\t" \
  "v_mfma_f32_16x16x32_bf16 v[156:159], v[112:115], v[12:15], v[156:159]\n\t" \
  "ds_read_b64 v[10:11], v31 offset:8192\n\t" \
  "s_waitcnt lgkmcnt(3)\n\t" \
  "v_mfma_f32_16x16x32_bf16 v[136:139], v[68:71], v[16:19], v[136:139]\n\t" \
  "ds_read_b64 v[12:13], v24 offset:9216\n\t" \
  "v_mfma_f32_16x16x32_bf16 v[136:139], v[64:67], v[20:23], v[136:139]\n\t" \
  "ds_read_b64 v[14:15], v25 offset:9216\n\t" \
  "v_mfma_f32_16x16x32_bf16 v[140:143], v[76:79], v[16:19], v[140:143]\n\t" \
  "v_mfma_f32_16x16x32_bf16 v[140:143], v[72:75], v[20:23], v[140:143]\n\t" \
  "v_mfma_f32_16x16x32_bf16 v[144:147], v[84:87], v[16:19], v[144:147]\n\t" \
  "v_mfma_f32_16x16x32_bf16 v[144:147], v[80:83], v[20:23], v[144:147]\n\t" \
  "v_mfma_f32_16x16x32_bf16 v[148:151], v[92:95], v[16:19], v[148:151]\n\t" \
  "v_mfma_f32_16x16x32_bf16 v[148:151], v[88:91], v[20:23], v[148:151]\n\t" \
  "v_mfma_f32_16x16x32_bf16 v[152:155], v[100:103], v[16:19], v[152:155]\n\t" \
  "v_cvt_pk_bf16_f32 v38, v136, v137\n\t" \
  "v_cvt_pk_bf16_f32 v39, v138, v139\n\t" \
  "v_mfma_f32_16x16x32_bf16 v[152:155], v[96:99], v[20:23], v[152:155]\n\t" \
  "v_mfma_f32_16x16x32_bf16 v[156:159], v[108:111], v[16:19], v[156:159]\n\t" \
  "ds_read_b64 v[16:17], v26 offset:10240\n\t" \
  "v_mfma_f32_16x16x32_bf16 v[156:159], v[104:107], v[20:23], v[156:159]\n\t" \
  "ds_read_b64 v[18:19], v27 offset:10240\n\t" \
  "v_mfma_f32_16x16x32_bf16 v[140:143], v[68:71], v[0:3], v[140:143]\n\t" \
  "ds_read_b64 v[20:21], v28 offset:11264\n\t" \
  "v_mfma_f32_16x16x32_bf16 v[140:143], v[64:67], v[4:7], v[140:143]\n\t" \
  "ds_read_b64 v[22:23], v29 offset:11264\n\t" \
  "v_mfma_f32_16x16x32_bf16 v[144:147], v[76:79], v[0:3], v[144:147]\n\t" \
  "v_mfma_f32_16x16x32_bf16 v[144:147], v[72:75], v[4:7], v[144:147]\n\t" \
  "v_mfma_f32_16x16x32_bf16 v[148:151], v[84:87], v[0:3], v[148:151]\n\t" \
  "v_mfma_f32_16x16x32_bf16 v[148:151], v[80:83], v[4:7], v[148:151]\n\t" \
  "v_mfma_f32_16x16x32_bf16 v[152:155], v[92:95], v[0:3], v[152:155]\n\t" \
  "v_mfma_f32_16x16x32_bf16 v[152:155], v[88:91], v[4:7], v[152:155]\n\t" \
  "v_mfma_f32_16x16x32_bf16 v[156:159], v[100:103], v[0:3], v[156:159]\n\t" \
  "v_cvt_pk_bf16_f32 v40, v140, v141\n\t" \
  "v_cvt_pk_bf16_f32 v41, v142, v143\n\t" \
  "v_mfma_f32_16x16x32_bf16 v[156:159], v[96:99], v[4:7], v[156:159]\n\t" \
  "ds_read_b64 v[0:1], v30 offset:12288\n\t" \
  "s_waitcnt lgkmcnt(5)\n\t" \
  "v_mfma_f32_16x16x32_bf16 v[144:147], v[68:71], v[8:11], v[144:147]\n\t" \
  "v_permlane16_swap_b32 v38, v40\n\t" \
  "ds_read_b64 v[2:3], v31 offset:12288\n\t" \
  "v_mfma_f32_16x16x32_bf16 v[144:147], v[64:67], v[12:15], v[144:147]\n\t" \
  "v_permlane16_swap_b32 v39, v41\n\t" \
  "ds_read_b64 v[4:5], v24 offset:13312\n\t" \
  "v_mfma_f32_16x16x32_bf16 v[148:151], v[76:79], v[8:11], v[148:151]\n\t" \
  "ds_read_b64 v[6:7], v25 offset:13312\n\t" \
  "v_mfma_f32_16x16x32_bf16 v[148:151], v[72:75], v[12:15], v[148:151]\n\t" \
  "global_store_dwordx4 v42, v[38:41], %[yb] offset:1024 sc1\n\t" \
  "v_mfma_f32_16x16x32_bf16 v[152:155], v[84:87], v[8:11], v[152:155]\n\t" \
  "v_mfma_f32_16x16x32_bf16 v[152:155], v[80:83], v[12:15], v[152:155]\n\t" \
  "v_cvt_pk_bf16_f32 v34, v144, v145\n\t" \
  "v_cvt_pk_bf16_f32 v35, v146, v147\n\t" \
  "v_mfma_f32_16x16x32_bf16 v[156:159], v[92:95], v[8:11], v[156:159]\n\t" \
  "ds_read_b64 v[8:9], v26 offset:14336\n\t" \
  "v_mfma_f32_16x16x32_bf16 v[156:159], v[88:91], v[12:15], v[156:159]\n\t" \
  "ds_read_b64 v[10:11], v27 offset:14336\n\t" \
  "s_waitcnt lgkmcnt(6)\n\t" \
  "v_mfma_f32_16x16x32_bf16 v[148:151], v[68:71], v[16:19], v[148:151]\n\t" \
  "ds_read_b64 v[12:13], v28 offset:15360\n\t" \
  "v_mfma_f32_16x16x32_bf16 v[148:151], v[64:67], v[20:23], v[148:151]\n\t" \
  "ds_read_b64 v[14:15], v29 offset:15360\n\t" \
  "v_mfma_f32_16x16x32_bf16 v[152:155], v[76:79], v[16:19], v[152:155]\n\t" \
  "v_mfma_f32_16x16x32_bf16 v[152:155], v[72:75], v[20:23], v[152:155]\n\t" \
  "v_mfma_f32_16x16x32_bf16 v[156:159], v[84:87], v[16:19], v[156:159]\n\t" \
  "v_mfma_f32_16x16x32_bf16 v[156:159], v[80:83], v[20:23], v[156:159]\n\t" \
  "s_waitcnt lgkmcnt(4)\n\t" \
  "v_mfma_f32_16x16x32_bf16 v[152:155], v[68:71], v[0:3], v[152:155]\n\t" \
  "v_mfma_f32_16x16x32_bf16 v[152:155], v[64:67], v[4:7], v[152:155]\n\t" \
  "v_cvt_pk_bf16_f32 v36, v148, v149\n\t" \
  "v_cvt_pk_bf16_f32 v37, v150, v151\n\t" \
  "v_mfma_f32_16x16x32_bf16 v[156:159], v[76:79], v[0:3], v[156:159]\n\t" \
  "v_mfma_f32_16x16x32_bf16 v[156:159], v[72:75], v[4:7], v[156:159]\n\t" \
  "v_permlane16_swap_b32 v34, v36\n\t" \
  "s_waitcnt lgkmcnt(2)\n\t" \
  "v_mfma_f32_16x16x32_bf16 v[156:159], v[68:71], v[8:11], v[156:159]\n\t" \
  "v_permlane16_swap_b32 v35, v37\n\t" \
  "s_waitcnt lgkmcnt(0)\n\t" \
  "v_mfma_f32_16x16x32_bf16 v[156:159], v[64:67], v[12:15], v[156:159]\n\t" \
  "global_store_dwordx4 v42, v[34:37], %[yb] offset:2048 sc1\n\t" \
  "s_waitcnt lgkmcnt(0)\n\t" \
  "s_nop 4\n\t" \
  "v_cvt_pk_bf16_f32 v38, v152, v153\n\t" \
  "v_cvt_pk_bf16_f32 v39, v154, v155\n\t" \
  "v_cvt_pk_bf16_f32 v40, v156, v157\n\t" \
  "v_cvt_pk_bf16_f32 v41, v158, v159\n\t" \
  "s_nop 1\n\t" \
  "v_permlane16_swap_b32 v38, v40\n\t" \
  "v_permlane16_swap_b32 v39, v41\n\t" \
  "global_store_dwordx4 v42, v[38:41], %[yb] offset:3072 sc1\n\t" \
  "s_nop 1\n\t"
#define CV_Q 1
#define CV_CLOB "vcc", "v0", "v1", "v2", "v3", "v4", "v5", "v6", "v7", "v8", "v9", "v10", "v11", "v12", "v13", "v14", "v15", "v16", "v17", "v18", "v19", "v20", "v21", "v22", "v23", "v34", "v35", "v36", "v37", "v38", "v39", "v40", "v41"

#define G3_ASM \
  "v_readlane_b32 s64, v96, 0\n\t" \
  "v_readlane_b32 s65, v96, 1\n\t" \
  "v_readlane_b32 s66, v96, 2\n\t" \
  "v_readlane_b32 s67, v96, 3\n\t" \
  "v_readlane_b32 s68, v96, 4\n\t" \
  "v_readlane_b32 s69, v96, 5\n\t" \
  "v_readlane_b32 s70, v96, 6\n\t" \
  "v_readlane_b32 s71, v96, 7\n\t" \
  "v_readlane_b32 s72, v96, 8\n\t" \
  "v_readlane_b32 s73, v96, 9\n\t" \
  "v_readlane_b32 s74, v96, 10\n\t" \
  "v_readlane_b32 s75, v96, 11\n\t" \
  "v_readlane_b32 s76, v96, 12\n\t" \
  "v_readlane_b32 s77, v96, 13\n\t" \
  "v_readlane_b32 s78, v96, 14\n\t" \
  "v_readlane_b32 s79, v96, 15\n\t" \
  "v_add_u32 v97, %[go], v165\n\t" \
  "s_branch QG_%=\n\t" \
  "QA14_%=:\n\t" \
  "v_mov_b32 v165, %[sy]\n\t" \
  "s_sleep 1\n\t" \
  "ds_read_b32 v165, v165\n\t" \
  "s_addk_i32 %[cn], 1\n\t" \
  "s_cmp_gt_u32 %[cn], 0x400000\n\t" \
  "s_cbranch_scc1 QC14_%=\n\t" \
  "s_waitcnt lgkmcnt(0)\n\t" \
  "v_cmp_lt_u32 vcc, 15, v165\n\t" \
  "s_cbranch_vccz QA14_%=\n\t" \
  "QC14_%=:\n\t" \
  "s_waitcnt lgkmcnt(0)\n\t" \
  "s_branch QB14_%=\n\t" \
  "QS_%=:\n\t" \
  "s_sleep 1\n\t" \
  "QG_%=:\n\t" \
  "ds_read_b32 v98, v165\n\t" \
  "ds_read_b32 v99, v97\n\t" \
  "s_addk_i32 %[cn], 1\n\t" \
  "s_cmp_gt_u32 %[cn], 0x400000\n\t" \
  "s_cselect_b32 %[t2], 16, 0\n\t" \
  "s_waitcnt lgkmcnt(0)\n\t" \
  "v_readfirstlane_b32 %[t0], v98\n\t" \
  "v_readfirstlane_b32 %[t1], v99\n\t" \
  "s_max_u32 %[t0], %[t0], %[t2]\n\t" \
  "s_or_b32 %[t1], %[t1], %[t2]\n\t" \
  "s_min_u32 %[t2], %[t0], 6\n\t" \
  "s_cmp_ge_u32 %[iss], %[t2]\n\t" \
  "s_cbranch_scc1 QD_%=\n\t" \
  "s_cmp_eq_u32 %[iss], 1\n\t" \
  "s_cbranch_scc1 QL1_%=\n\t" \
  "s_cmp_eq_u32 %[iss], 2\n\t" \
  "s_cbranch_scc1 QL2_%=\n\t" \
  "s_cmp_eq_u32 %[iss], 3\n\t" \
  "s_cbranch_scc1 QL3_%=\n\t" \
  "s_cmp_eq_u32 %[iss], 4\n\t" \
  "s_cbranch_scc1 QL4_%=\n\t" \
  "s_cmp_eq_u32 %[iss], 5\n\t" \
  "s_cbranch_scc1 QL5_%=\n\t" \
  "QL0_%=:\n\t" \
  "buffer_load_dwordx4 v[0:3], v164, %[ob], s64 offen offset:0 sc1\n\t" \
  "buffer_load_dwordx4 v[4:7], v164, %[ob], s64 offen offset:1024 sc1\n\t" \
  "buffer_load_dwordx4 v[8:11], v164, %[ob], s64 offen offset:2048 sc1\n\t" \
  "buffer_load_dwordx4 v[12:15], v164, %[ob], s64 offen offset:3072 sc1\n\t" \
  "s_mov_b32 %[iss], 1\n\t" \
  "s_cmp_le_u32 %[t2], 1\n\t" \
  "s_cbranch_scc1 QD_%=\n\t" \
  "QL1_%=:\n\t" \
  "buffer_load_dwordx4 v[16:19], v164, %[ob], s65 offen offset:0 sc1\n\t" \
  "buffer_load_dwordx4 v[20:23], v164, %[ob], s65 offen offset:1024 sc1\n\t" \
  "buffer_load_dwordx4 v[24:27], v164, %[ob], s65 offen offset:2048 sc1\n\t" \
  "buffer_load_dwordx4 v[28:31], v164, %[ob], s65 offen offset:3072 sc1\n\t" \
  "s_mov_b32 %[iss], 2\n\t" \
  "s_cmp_le_u32 %[t2], 2\n\t" \
  "s_cbranch_scc1 QD_%=\n\t" \
  "QL2_%=:\n\t" \
  "buffer_load_dwordx4 v[32:35], v164, %[ob], s66 offen offset:0 sc1\n\t" \
  "buffer_load_dwordx4 v[36:39], v164, %[ob], s66 offen offset:1024 sc1\n\t" \
  "buffer_load_dwordx4 v[40:43], v164, %[ob], s66 offen offset:2048 sc1\n\t" \
  "buffer_load_dwordx4 v[44:47], v164, %[ob], s66 offen offset:3072 sc1\n\t" \
  "s_mov_b32 %[iss], 3\n\t" \
  "s_cmp_le_u32 %[t2], 3\n\t" \
  "s_cbranch_scc1 QD_%=\n\t" \
  "QL3_%=:\n\t" \
  "buffer_load_dwordx4 v[48:51], v164, %[ob], s67 offen offset:0 sc1\n\t" \
  "buffer_load_dwordx4 v[52:55], v164, %[ob], s67 offen offset:1024 sc1\n\t" \
  "buffer_load_dwordx4 v[56:59], v164, %[ob], s67 offen offset:2048 sc1\n\t" \
  "buffer_load_dwordx4 v[60:63], v164, %[ob], s67 offen offset:3072 sc1\n\t" \
  "s_mov_b32 %[iss], 4\n\t" \
  "s_cmp_le_u32 %[t2], 4\n\t" \
  "s_cbranch_scc1 QD_%=\n\t" \
  "QL4_%=:\n\t" \
  "buffer_load_dwordx4 v[64:67], v164, %[ob], s68 offen offset:0 sc1\n\t" \
  "buffer_load_dwordx4 v[68:71], v164, %[ob], s68 offen offset:1024 sc1\n\t" \
  "buffer_load_dwordx4 v[72:75], v164, %[ob], s68 offen offset:2048 sc1\n\t" \
  "buffer_load_dwordx4 v[76:79], v164, %[ob], s68 offen offset:3072 sc1\n\t" \
  "s_mov_b32 %[iss], 5\n\t" \
  "s_cmp_le_u32 %[t2], 5\n\t" \
  "s_cbranch_scc1 QD_%=\n\t" \
  "QL5_%=:\n\t" \
  "buffer_load_dwordx4 v[80:83], v164, %[ob], s69 offen offset:0 sc1\n\t" \
  "buffer_load_dwordx4 v[84:87], v164, %[ob], s69 offen offset:1024 sc1\n\t" \
  "buffer_load_dwordx4 v[88:91], v164, %[ob], s69 offen offset:2048 sc1\n\t" \
  "buffer_load_dwordx4 v[92:95], v164, %[ob], s69 offen offset:3072 sc1\n\t" \
  "s_mov_b32 %[iss], 6\n\t" \
  "QD_%=:\n\t" \
  "s_cmp_lt_u32 %[t0], 14\n\t" \
  "s_cbranch_scc1 QS_%=\n\t" \
  "s_cmp_eq_u32 %[t1], 0\n\t" \
  "s_cbranch_scc1 QS_%=\n\t" \
  "s_setprio 0\n\t" \
  "ds_read_b64_tr_b16 v[96:97], v160 offset:0\n\t" \
  "ds_read_b64_tr_b16 v[98:99], v160 offset:2048\n\t" \
  "ds_read_b64_tr_b16 v[100:101], v161 offset:0\n\t" \
  "ds_read_b64_tr_b16 v[102:103], v161 offset:2048\n\t" \
  "ds_read_b64_tr_b16 v[104:105], v162 offset:0\n\t" \
  "ds_read_b64_tr_b16 v[106:107], v162 offset:2048\n\t" \
  "ds_read_b64_tr_b16 v[108:109], v163 offset:0\n\t" \
  "ds_read_b64_tr_b16 v[110:111], v163 offset:2048\n\t" \
  "ds_read_b64_tr_b16 v[112:113], v160 offset:4096\n\t" \
  "ds_read_b64_tr_b16 v[114:115], v160 offset:6144\n\t" \
  "ds_read_b64_tr_b16 v[116:117], v161 offset:4096\n\t" \
  "ds_read_b64_tr_b16 v[118:119], v161 offset:6144\n\t" \
  "ds_read_b64_tr_b16 v[120:121], v162 offset:4096\n\t" \
  "ds_read_b64_tr_b16 v[122:123], v162 offset:6144\n\t" \
  "s_waitcnt vmcnt(22) lgkmcnt(10)\n\t" \
  "v_mfma_f32_16x16x32_bf16 v[128:131], v[96:99], v[0:3], 0\n\t" \
  "ds_read_b64_tr_b16 v[124:125], v163 offset:4096\n\t" \
  "v_mfma_f32_16x16x32_bf16 v[144:147], v[96:99], v[4:7], 0\n\t" \
  "ds_read_b64_tr_b16 v[126:127], v163 offset:6144\n\t" \
  "v_mfma_f32_16x16x32_bf16 v[132:135], v[100:103], v[0:3], 0\n\t" \
  "ds_read_b64_tr_b16 v[96:97], v160 offset:8192\n\t" \
  "v_mfma_f32_16x16x32_bf16 v[148:151], v[100:103], v[4:7], 0\n\t" \
  "ds_read_b64_tr_b16 v[98:99], v160 offset:10240\n\t" \
  "s_waitcnt lgkmcnt(10)\n\t" \
  "v_mfma_f32_16x16x32_bf16 v[136:139], v[104:107], v[0:3], 0\n\t" \
  "ds_read_b64_tr_b16 v[100:101], v161 offset:8192\n\t" \
  "v_mfma_f32_16x16x32_bf16 v[152:155], v[104:107], v[4:7], 0\n\t" \
  "ds_read_b64_tr_b16 v[102:103], v161 offset:10240\n\t" \
  "v_mfma_f32_16x16x32_bf16 v[140:143], v[108:111], v[0:3], 0\n\t" \
  "ds_read_b64_tr_b16 v[104:105], v162 offset:8192\n\t" \
  "v_mfma_f32_16x16x32_bf16 v[156:159], v[108:111], v[4:7], 0\n\t" \
  "ds_read_b64_tr_b16 v[106:107], v162 offset:10240\n\t" \
  "s_waitcnt vmcnt(20) lgkmcnt(10)\n\t" \
  "v_mfma_f32_16x16x32_bf16 v[128:131], v[112:115], v[8:11], v[128:131]\n\t" \
  "ds_read_b64_tr_b16 v[108:109], v163 offset:8192\n\t" \
  "buffer_load_dwordx4 v[0:3], v164, %[ob], s70 offen offset:0 sc1\n\t" \
  "v_mfma_f32_16x16x32_bf16 v[144:147], v[112:115], v[12:15], v[144:147]\n\t" \
  "ds_read_b64_tr_b16 v[110:111], v163 offset:10240\n\t" \
  "v_mfma_f32_16x16x32_bf16 v[132:135], v[116:119], v[8:11], v[132:135]\n\t" \
  "ds_read_b64_tr_b16 v[112:113], v160 offset:12288\n\t" \
  "v_mfma_f32_16x16x32_bf16 v[148:151], v[116:119], v[12:15], v[148:151]\n\t" \
  "ds_read_b64_tr_b16 v[114:115], v160 offset:14336\n\t" \
  "s_waitcnt lgkmcnt(10)\n\t" \
  "v_mfma_f32_16x16x32_bf16 v[136:139], v[120:123], v[8:11], v[136:139]\n\t" \
  "ds_read_b64_tr_b16 v[116:117], v161 offset:12288\n\t" \
  "buffer_load_dwordx4 v[4:7], v164, %[ob], s70 offen offset:1024 sc1\n\t" \
  "v_mfma_f32_16x16x32_bf16 v[152:155], v[120:123], v[12:15], v[152:155]\n\t" \
  "ds_read_b64_tr_b16 v[118:119], v161 offset:14336\n\t" \
  "v_mfma_f32_16x16x32_bf16 v[140:143], v[124:127], v[8:11], v[140:143]\n\t" \
  "ds_read_b64_tr_b16 v[120:121], v162 offset:12288\n\t" \
  "v_mfma_f32_16x16x32_bf16 v[156:159], v[124:127], v[12:15], v[156:159]\n\t" \
  "ds_read_b64_tr_b16 v[122:123], v162 offset:14336\n\t" \
  "s_waitcnt vmcnt(20) lgkmcnt(10)\n\t" \
  "v_mfma_f32_16x16x32_bf16 v[128:131], v[96:99], v[16:19], v[128:131]\n\t" \
  "ds_read_b64_tr_b16 v[124:125], v163 offset:12288\n\t" \
  "v_mfma_f32_16x16x32_bf16 v[144:147], v[96:99], v[20:23], v[144:147]\n\t" \
  "ds_read_b64_tr_b16 v[126:127], v163 offset:14336\n\t" \
  "buffer_load_dwordx4 v[8:11], v164, %[ob], s70 offen offset:2048 sc1\n\t" \
  "v_mfma_f32_16x16x32_bf16 v[132:135], v[100:103], v[16:19], v[132:135]\n\t" \
  "ds_read_b64_tr_b16 v[96:97], v160 offset:16384\n\t" \
  "v_mfma_f32_16x16x32_bf16 v[148:151], v[100:103], v[20:23], v[148:151]\n\t" \
  "ds_read_b64_tr_b16 v[98:99], v160 offset:18432\n\t" \
  "s_waitcnt lgkmcnt(10)\n\t" \
  "v_mfma_f32_16x16x32_bf16 v[136:139], v[104:107], v[16:19], v[136:139]\n\t" \
  "ds_read_b64_tr_b16 v[100:101], v161 offset:16384\n\t" \
  "v_mfma_f32_16x16x32_bf16 v[152:155], v[104:107], v[20:23], v[152:155]\n\t" \
  "ds_read_b64_tr_b16 v[102:103], v161 offset:18432\n\t" \
  "v_mfma_f32_16x16x32_bf16 v[140:143], v[108:111], v[16:19], v[140:143]\n\t" \
  "ds_read_b64_tr_b16 v[104:105], v162 offset:16384\n\t" \
  "buffer_load_dwordx4 v[12:15], v164, %[ob], s70 offen offset:3072 sc1\n\t" \
  "v_mfma_f32_16x16x32_bf16 v[156:159], v[108:111], v[20:23], v[156:159]\n\t" \
  "ds_read_b64_tr_b16 v[106:107], v162 offset:18432\n\t" \
  "s_waitcnt vmcnt(20) lgkmcnt(10)\n\t" \
  "v_mfma_f32_16x16x32_bf16 v[128:131], v[112:115], v[24:27], v[128:131]\n\t" \
  "ds_read_b64_tr_b16 v[108:109], v163 offset:16384\n\t" \
  "v_mfma_f32_16x16x32_bf16 v[144:147], v[112:115], v[28:31], v[144:147]\n\t" \
  "ds_read_b64_tr_b16 v[110:111], v163 offset:18432\n\t" \
  "v_mfma_f32_16x16x32_bf16 v[132:135], v[116:119], v[24:27], v[132:135]\n\t" \
  "ds_read_b64_tr_b16 v[112:113], v160 offset:20480\n\t" \
  "v_mfma_f32_16x16x32_bf16 v[148:151], v[116:119], v[28:31], v[148:151]\n\t" \
  "ds_read_b64_tr_b16 v[114:115], v160 offset:22528\n\t" \
  "s_waitcnt lgkmcnt(10)\n\t" \
  "v_mfma_f32_16x16x32_bf16 v[136:139], v[120:123], v[24:27], v[136:139]\n\t" \
  "ds_read_b64_tr_b16 v[116:117], v161 offset:20480\n\t" \
  "buffer_load_dwordx4 v[16:19], v164, %[ob], s71 offen offset:0 sc1\n\t" \
  "v_mfma_f32_16x16x32_bf16 v[152:155], v[120:123], v[28:31], v[152:155]\n\t" \
  "ds_read_b64_tr_b16 v[118:119], v161 offset:22528\n\t" \
  "v_mfma_f32_16x16x32_bf16 v[140:143], v[124:127], v[24:27], v[140:143]\n\t" \
  "ds_read_b64_tr_b16 v[120:121], v162 offset:20480\n\t" \
  "v_mfma_f32_16x16x32_bf16 v[156:159], v[124:127], v[28:31], v[156:159]\n\t" \
  "ds_read_b64_tr_b16 v[122:123], v162 offset:22528\n\t" \
  "s_waitcnt vmcnt(19) lgkmcnt(10)\n\t" \
  "v_mfma_f32_16x16x32_bf16 v[128:131], v[96:99], v[32:35], v[128:131]\n\t" \
  "ds_read_b64_tr_b16 v[124:125], v163 offset:20480\n\t" \
  "buffer_load_dwordx4 v[20:23], v164, %[ob], s71 offen offset:1024 sc1\n\t" \
  "v_mfma_f32_16x16x32_bf16 v[144:147], v[96:99], v[36:39], v[144:147]\n\t" \
  "ds_read_b64_tr_b16 v[126:127], v163 offset:22528\n\t" \
  "v_mfma_f32_16x16x32_bf16 v[132:135], v[100:103], v[32:35], v[132:135]\n\t" \
  "ds_read_b64_tr_b16 v[96:97], v160 offset:24576\n\t" \
  "v_mfma_f32_16x16x32_bf16 v[148:151], v[100:103], v[36:39], v[148:151]\n\t" \
  "ds_read_b64_tr_b16 v[98:99], v160 offset:26624\n\t" \
  "s_waitcnt lgkmcnt(10)\n\t" \
  "v_mfma_f32_16x16x32_bf16 v[136:139], v[104:107], v[32:35], v[136:139]\n\t" \
  "ds_read_b64_tr_b16 v[100:101], v161 offset:24576\n\t" \
  "v_mfma_f32_16x16x32_bf16 v[152:155], v[104:107], v[36:39], v[152:155]\n\t" \
  "ds_read_b64_tr_b16 v[102:103], v161 offset:26624\n\t" \
  "buffer_load_dwordx4 v[24:27], v164, %[ob], s71 offen offset:2048 sc1\n\t" \
  "v_mfma_f32_16x16x32_bf16 v[140:143], v[108:111], v[32:35], v[140:143]\n\t" \
  "ds_read_b64_tr_b16 v[104:105], v162 offset:24576\n\t" \
  "v_mfma_f32_16x16x32_bf16 v[156:159], v[108:111], v[36:39], v[156:159]\n\t" \
  "ds_read_b64_tr_b16 v[106:107], v162 offset:26624\n\t" \
  "s_waitcnt vmcnt(19) lgkmcnt(10)\n\t" \
  "v_mfma_f32_16x16x32_bf16 v[128:131], v[112:115], v[40:43], v[128:131]\n\t" \
  "ds_read_b64_tr_b16 v[108:109], v163 offset:24576\n\t" \
  "v_mfma_f32_16x16x32_bf16 v[144:147], v[112:115], v[44:47], v[144:147]\n\t" \
  "ds_read_b64_tr_b16 v[110:111], v163 offset:26624\n\t" \
  "v_mfma_f32_16x16x32_bf16 v[132:135], v[116:119], v[40:43], v[132:135]\n\t" \
  "ds_read_b64_tr_b16 v[112:113], v160 offset:28672\n\t" \
  "buffer_load_dwordx4 v[28:31], v164, %[ob], s71 offen offset:3072 sc1\n\t" \
  "v_mfma_f32_16x16x32_bf16 v[148:151], v[116:119], v[44:47], v[148:151]\n\t" \
  "ds_read_b64_tr_b16 v[114:115], v160 offset:30720\n\t" \
  "s_waitcnt lgkmcnt(10)\n\t" \
  "v_mfma_f32_16x16x32_bf16 v[136:139], v[120:123], v[40:43], v[136:139]\n\t" \
  "ds_read_b64_tr_b16 v[116:117], v161 offset:28672\n\t" \
  "v_mfma_f32_16x16x32_bf16 v[152:155], v[120:123], v[44:47], v[152:155]\n\t" \
  "ds_read_b64_tr_b16 v[118:119], v161 offset:30720\n\t" \
  "v_mfma_f32_16x16x32_bf16 v[140:143], v[124:127], v[40:43], v[140:143]\n\t" \
  "ds_read_b64_tr_b16 v[120:121], v162 offset:28672\n\t" \
  "v_mfma_f32_16x16x32_bf16 v[156:159], v[124:127], v[44:47], v[156:159]\n\t" \
  "ds_read_b64_tr_b16 v[122:123], v162 offset:30720\n\t" \
  "s_waitcnt vmcnt(18) lgkmcnt(10)\n\t" \
  "v_mfma_f32_16x16x32_bf16 v[128:131], v[96:99], v[48:51], v[128:131]\n\t" \
  "ds_read_b64_tr_b16 v[124:125], v163 offset:28672\n\t" \
  "buffer_load_dwordx4 v[32:35], v164, %[ob], s72 offen offset:0 sc1\n\t" \
  "v_mfma_f32_16x16x32_bf16 v[144:147], v[96:99], v[52:55], v[144:147]\n\t" \
  "ds_read_b64_tr_b16 v[126:127], v163 offset:30720\n\t" \
  "v_mfma_f32_16x16x32_bf16 v[132:135], v[100:103], v[48:51], v[132:135]\n\t" \
  "ds_read_b64_tr_b16 v[96:97], v160 offset:32768\n\t" \
  "v_mfma_f32_16x16x32_bf16 v[148:151], v[100:103], v[52:55], v[148:151]\n\t" \
  "ds_read_b64_tr_b16 v[98:99], v160 offset:34816\n\t" \
  "s_waitcnt lgkmcnt(10)\n\t" \
  "v_mfma_f32_16x16x32_bf16 v[136:139], v[104:107], v[48:51], v[136:139]\n\t" \
  "ds_read_b64_tr_b16 v[100:101], v161 offset:32768\n\t" \
  "buffer_load_dwordx4 v[36:39], v164, %[ob], s72 offen offset:1024 sc1\n\t" \
  "v_mfma_f32_16x16x32_bf16 v[152:155], v[104:107], v[52:55], v[152:155]\n\t" \
  "ds_read_b64_tr_b16 v[102:103], v161 offset:34816\n\t" \
  "v_mfma_f32_16x16x32_bf16 v[140:143], v[108:111], v[48:51], v[140:143]\n\t" \
  "ds_read_b64_tr_b16 v[104:105], v162 offset:32768\n\t" \
  "v_mfma_f32_16x16x32_bf16 v[156:159], v[108:111], v[52:55], v[156:159]\n\t" \
  "ds_read_b64_tr_b16 v[106:107], v162 offset:34816\n\t" \
  "s_waitcnt vmcnt(18) lgkmcnt(10)\n\t" \
  "v_mfma_f32_16x16x32_bf16 v[128:131], v[112:115], v[56:59], v[128:131]\n\t" \
  "ds_read_b64_tr_b16 v[108:109], v163 offset:32768\n\t" \
  "v_mfma_f32_16x16x32_bf16 v[144:147], v[112:115], v[60:63], v[144:147]\n\t" \
  "ds_read_b64_tr_b16 v[110:111], v163 offset:34816\n\t" \
  "buffer_load_dwordx4 v[40:43], v164, %[ob], s72 offen offset:2048 sc1\n\t" \
  "v_mfma_f32_16x16x32_bf16 v[132:135], v[116:119], v[56:59], v[132:135]\n\t" \
  "ds_read_b64_tr_b16 v[112:113], v160 offset:36864\n\t" \
  "v_mfma_f32_16x16x32_bf16 v[148:151], v[116:119], v[60:63], v[148:151]\n\t" \
  "ds_read_b64_tr_b16 v[114:115], v160 offset:38912\n\t" \
  "s_waitcnt lgkmcnt(10)\n\t" \
  "v_mfma_f32_16x16x32_bf16 v[136:139], v[120:123], v[56:59], v[136:139]\n\t" \
  "ds_read_b64_tr_b16 v[116:117], v161 offset:36864\n\t" \
  "v_mfma_f32_16x16x32_bf16 v[152:155], v[120:123], v[60:63], v[152:155]\n\t" \
  "ds_read_b64_tr_b16 v[118:119], v161 offset:38912\n\t" \
  "v_mfma_f32_16x16x32_bf16 v[140:143], v[124:127], v[56:59], v[140:143]\n\t" \
  "ds_read_b64_tr_b16 v[120:121], v162 offset:36864\n\t" \
  "buffer_load_dwordx4 v[44:47], v164, %[ob], s72 offen offset:3072 sc1\n\t" \
  "v_mfma_f32_16x16x32_bf16 v[156:159], v[124:127], v[60:63], v[156:159]\n\t" \
  "ds_read_b64_tr_b16 v[122:123], v162 offset:38912\n\t" \
  "s_waitcnt vmcnt(18) lgkmcnt(10)\n\t" \
  "v_mfma_f32_16x16x32_bf16 v[128:131], v[96:99], v[64:67], v[128:131]\n\t" \
  "ds_read_b64_tr_b16 v[124:125], v163 offset:36864\n\t" \
  "v_mfma_f32_16x16x32_bf16 v[144:147], v[96:99], v[68:71], v[144:147]\n\t" \
  "ds_read_b64_tr_b16 v[126:127], v163 offset:38912\n\t" \
  "v_mfma_f32_16x16x32_bf16 v[132:135], v[100:103], v[64:67], v[132:135]\n\t" \
  "ds_read_b64_tr_b16 v[96:97], v160 offset:40960\n\t" \
  "v_mfma_f32_16x16x32_bf16 v[148:151], v[100:103], v[68:71], v[148:151]\n\t" \
  "ds_read_b64_tr_b16 v[98:99], v160 offset:43008\n\t" \
  "s_waitcnt lgkmcnt(10)\n\t" \
  "v_mfma_f32_16x16x32_bf16 v[136:139], v[104:107], v[64:67], v[136:139]\n\t" \
  "ds_read_b64_tr_b16 v[100:101], v161 offset:40960\n\t" \
  "buffer_load_dwordx4 v[48:51], v164, %[ob], s73 offen offset:0 sc1\n\t" \
  "v_mfma_f32_16x16x32_bf16 v[152:155], v[104:107], v[68:71], v[152:155]\n\t" \
  "ds_read_b64_tr_b16 v[102:103], v161 offset:43008\n\t" \
  "v_mfma_f32_16x16x32_bf16 v[140:143], v[108:111], v[64:67], v[140:143]\n\t" \
  "ds_read_b64_tr_b16 v[104:105], v162 offset:40960\n\t" \
  "v_mfma_f32_16x16x32_bf16 v[156:159], v[108:111], v[68:71], v[156:159]\n\t" \
  "ds_read_b64_tr_b16 v[106:107], v162 offset:43008\n\t" \
  "s_waitcnt vmcnt(17) lgkmcnt(10)\n\t" \
  "v_mfma_f32_16x16x32_bf16 v[128:131], v[112:115], v[72:75], v[128:131]\n\t" \
  "ds_read_b64_tr_b16 v[108:109], v163 offset:40960\n\t" \
  "buffer_load_dwordx4 v[52:55], v164, %[ob], s73 offen offset:1024 sc1\n\t" \
  "v_mfma_f32_16x16x32_bf16 v[144:147], v[112:115], v[76:79], v[144:147]\n\t" \
  "ds_read_b64_tr_b16 v[110:111], v163 offset:43008\n\t" \
  "v_mfma_f32_16x16x32_bf16 v[132:135], v[116:119], v[72:75], v[132:135]\n\t" \
  "ds_read_b64_tr_b16 v[112:113], v160 offset:45056\n\t" \
  "v_mfma_f32_16x16x32_bf16 v[148:151], v[116:119], v[76:79], v[148:151]\n\t" \
  "ds_read_b64_tr_b16 v[114:115], v160 offset:47104\n\t" \
  "s_waitcnt lgkmcnt(10)\n\t" \
  "v_mfma_f32_16x16x32_bf16 v[136:139], v[120:123], v[72:75], v[136:139]\n\t" \
  "ds_read_b64_tr_b16 v[116:117], v161 offset:45056\n\t" \
  "buffer_load_dwordx4 v[56:59], v164, %[ob], s73 offen offset:2048 sc1\n\t" \
  "v_mfma_f32_16x16x32_bf16 v[152:155], v[120:123], v[76:79], v[152:155]\n\t" \
  "ds_read_b64_tr_b16 v[118:119], v161 offset:47104\n\t" \
  "v_mfma_f32_16x16x32_bf16 v[140:143], v[124:127], v[72:75], v[140:143]\n\t" \
  "ds_read_b64_tr_b16 v[120:121], v162 offset:45056\n\t" \
  "v_mfma_f32_16x16x32_bf16 v[156:159], v[124:127], v[76:79], v[156:159]\n\t" \
  "ds_read_b64_tr_b16 v[122:123], v162 offset:47104\n\t" \
  "s_waitcnt vmcnt(17) lgkmcnt(10)\n\t" \
  "v_mfma_f32_16x16x32_bf16 v[128:131], v[96:99], v[80:83], v[128:131]\n\t" \
  "ds_read_b64_tr_b16 v[124:125], v163 offset:45056\n\t" \
  "v_mfma_f32_16x16x32_bf16 v[144:147], v[96:99], v[84:87], v[144:147]\n\t" \
  "ds_read_b64_tr_b16 v[126:127], v163 offset:47104\n\t" \
  "buffer_load_dwordx4 v[60:63], v164, %[ob], s73 offen offset:3072 sc1\n\t" \
  "v_mfma_f32_16x16x32_bf16 v[132:135], v[100:103], v[80:83], v[132:135]\n\t" \
  "ds_read_b64_tr_b16 v[96:97], v160 offset:49152\n\t" \
  "v_mfma_f32_16x16x32_bf16 v[148:151], v[100:103], v[84:87], v[148:151]\n\t" \
  "ds_read_b64_tr_b16 v[98:99], v160 offset:51200\n\t" \
  "s_waitcnt lgkmcnt(10)\n\t" \
  "v_mfma_f32_16x16x32_bf16 v[136:139], v[104:107], v[80:83], v[136:139]\n\t" \
  "ds_read_b64_tr_b16 v[100:101], v161 offset:49152\n\t" \
  "v_mfma_f32_16x16x32_bf16 v[152:155], v[104:107], v[84:87], v[152:155]\n\t" \
  "ds_read_b64_tr_b16 v[102:103], v161 offset:51200\n\t" \
  "v_mfma_f32_16x16x32_bf16 v[140:143], v[108:111], v[80:83], v[140:143]\n\t" \
  "ds_read_b64_tr_b16 v[104:105], v162 offset:49152\n\t" \
  "buffer_load_dwordx4 v[64:67], v164, %[ob], s74 offen offset:0 sc1\n\t" \
  "v_mfma_f32_16x16x32_bf16 v[156:159], v[108:111], v[84:87], v[156:159]\n\t" \
  "ds_read_b64_tr_b16 v[106:107], v162 offset:51200\n\t" \
  "s_waitcnt vmcnt(17) lgkmcnt(10)\n\t" \
  "v_mfma_f32_16x16x32_bf16 v[128:131], v[112:115], v[88:91], v[128:131]\n\t" \
  "ds_read_b64_tr_b16 v[108:109], v163 offset:49152\n\t" \
  "v_mfma_f32_16x16x32_bf16 v[144:147], v[112:115], v[92:95], v[144:147]\n\t" \
  "ds_read_b64_tr_b16 v[110:111], v163 offset:51200\n\t" \
  "v_mfma_f32_16x16x32_bf16 v[132:135], v[116:119], v[88:91], v[132:135]\n\t" \
  "ds_read_b64_tr_b16 v[112:113], v160 offset:53248\n\t" \
  "s_mov_b64 exec, 1\n\t" \
  "ds_write_b32 v165, v165 offset:76\n\t" \
  "s_mov_b64 exec, -1\n\t" \
  "v_mfma_f32_16x16x32_bf16 v[148:151], v[116:119], v[92:95], v[148:151]\n\t" \
  "ds_read_b64_tr_b16 v[114:115], v160 offset:55296\n\t" \
  "s_waitcnt lgkmcnt(11)\n\t" \
  "v_mfma_f32_16x16x32_bf16 v[136:139], v[120:123], v[88:91], v[136:139]\n\t" \
  "ds_read_b64_tr_b16 v[116:117], v161 offset:53248\n\t" \
  "buffer_load_dwordx4 v[68:71], v164, %[ob], s74 offen offset:1024 sc1\n\t" \
  "v_mfma_f32_16x16x32_bf16 v[152:155], v[120:123], v[92:95], v[152:155]\n\t" \
  "ds_read_b64_tr_b16 v[118:119], v161 offset:55296\n\t" \
  "v_mfma_f32_16x16x32_bf16 v[140:143], v[124:127], v[88:91], v[140:143]\n\t" \
  "ds_read_b64_tr_b16 v[120:121], v162 offset:53248\n\t" \
  "v_mfma_f32_16x16x32_bf16 v[156:159], v[124:127], v[92:95], v[156:159]\n\t" \
  "ds_read_b64_tr_b16 v[122:123], v162 offset:55296\n\t" \
  "s_waitcnt vmcnt(16) lgkmcnt(11)\n\t" \
  "v_mfma_f32_16x16x32_bf16 v[128:131], v[96:99], v[0:3], v[128:131]\n\t" \
  "ds_read_b64_tr_b16 v[124:125], v163 offset:53248\n\t" \
  "buffer_load_dwordx4 v[72:75], v164, %[ob], s74 offen offset:2048 sc1\n\t" \
  "v_mfma_f32_16x16x32_bf16 v[144:147], v[96:99], v[4:7], v[144:147]\n\t" \
  "ds_read_b64_tr_b16 v[126:127], v163 offset:55296\n\t" \
  "v_mfma_f32_16x16x32_bf16 v[132:135], v[100:103], v[0:3], v[132:135]\n\t" \
  "ds_read_b64_tr_b16 v[96:97], v160 offset:57344\n\t" \
  "v_mfma_f32_16x16x32_bf16 v[148:151], v[100:103], v[4:7], v[148:151]\n\t" \
  "ds_read_b64_tr_b16 v[98:99], v160 offset:59392\n\t" \
  "s_waitcnt lgkmcnt(11)\n\t" \
  "v_mfma_f32_16x16x32_bf16 v[136:139], v[104:107], v[0:3], v[136:139]\n\t" \
  "ds_read_b64_tr_b16 v[100:101], v161 offset:57344\n\t" \
  "v_mfma_f32_16x16x32_bf16 v[152:155], v[104:107], v[4:7], v[152:155]\n\t" \
  "ds_read_b64_tr_b16 v[102:103], v161 offset:59392\n\t" \
  "buffer_load_dwordx4 v[76:79], v164, %[ob], s74 offen offset:3072 sc1\n\t" \
  "v_mfma_f32_16x16x32_bf16 v[140:143], v[108:111], v[0:3], v[140:143]\n\t" \
  "ds_read_b64_tr_b16 v[104:105], v162 offset:57344\n\t" \
  "v_mfma_f32_16x16x32_bf16 v[156:159], v[108:111], v[4:7], v[156:159]\n\t" \
  "ds_read_b64_tr_b16 v[106:107], v162 offset:59392\n\t" \
  "s_waitcnt vmcnt(16) lgkmcnt(10)\n\t" \
  "v_mfma_f32_16x16x32_bf16 v[128:131], v[112:115], v[8:11], v[128:131]\n\t" \
  "ds_read_b64_tr_b16 v[108:109], v163 offset:57344\n\t" \
  "v_mfma_f32_16x16x32_bf16 v[144:147], v[112:115], v[12:15], v[144:147]\n\t" \
  "ds_read_b64_tr_b16 v[110:111], v163 offset:59392\n\t" \
  "v_mfma_f32_16x16x32_bf16 v[132:135], v[116:119], v[8:11], v[132:135]\n\t" \
  "ds_read_b64_tr_b16 v[112:113], v160 offset:61440\n\t" \
  "buffer_load_dwordx4 v[80:83], v164, %[ob], s75 offen offset:0 sc1\n\t" \
  "v_mfma_f32_16x16x32_bf16 v[148:151], v[116:119], v[12:15], v[148:151]\n\t" \
  "ds_read_b64_tr_b16 v[114:115], v160 offset:63488\n\t" \
  "s_waitcnt lgkmcnt(10)\n\t" \
  "v_mfma_f32_16x16x32_bf16 v[136:139], v[120:123], v[8:11], v[136:139]\n\t" \
  "ds_read_b64_tr_b16 v[116:117], v161 offset:61440\n\t" \
  "v_mfma_f32_16x16x32_bf16 v[152:155], v[120:123], v[12:15], v[152:155]\n\t" \
  "ds_read_b64_tr_b16 v[118:119], v161 offset:63488\n\t" \
  "v_mfma_f32_16x16x32_bf16 v[140:143], v[124:127], v[8:11], v[140:143]\n\t" \
  "ds_read_b64_tr_b16 v[120:121], v162 offset:61440\n\t" \
  "v_mfma_f32_16x16x32_bf16 v[156:159], v[124:127], v[12:15], v[156:159]\n\t" \
  "ds_read_b64_tr_b16 v[122:123], v162 offset:63488\n\t" \
  "s_waitcnt vmcnt(15) lgkmcnt(10)\n\t" \
  "v_mfma_f32_16x16x32_bf16 v[128:131], v[96:99], v[16:19], v[128:131]\n\t" \
  "ds_read_b64_tr_b16 v[124:125], v163 offset:61440\n\t" \
  "buffer_load_dwordx4 v[84:87], v164, %[ob], s75 offen offset:1024 sc1\n\t" \
  "v_mfma_f32_16x16x32_bf16 v[144:147], v[96:99], v[20:23], v[144:147]\n\t" \
  "ds_read_b64_tr_b16 v[126:127], v163 offset:63488\n\t" \
  "v_mfma_f32_16x16x32_bf16 v[132:135], v[100:103], v[16:19], v[132:135]\n\t" \
  "v_add_u32 v160, 0x10000, v160\n\t" \
  "ds_read_b64_tr_b16 v[96:97], v160 offset:0\n\t" \
  "v_mfma_f32_16x16x32_bf16 v[148:151], v[100:103], v[20:23], v[148:151]\n\t" \
  "ds_read_b64_tr_b16 v[98:99], v160 offset:2048\n\t" \
  "s_waitcnt lgkmcnt(10)\n\t" \
  "v_mfma_f32_16x16x32_bf16 v[136:139], v[104:107], v[16:19], v[136:139]\n\t" \
  "v_add_u32 v161, 0x10000, v161\n\t" \
  "ds_read_b64_tr_b16 v[100:101], v161 offset:0\n\t" \
  "buffer_load_dwordx4 v[88:91], v164, %[ob], s75 offen offset:2048 sc1\n\t" \
  "v_mfma_f32_16x16x32_bf16 v[152:155], v[104:107], v[20:23], v[152:155]\n\t" \
  "ds_read_b64_tr_b16 v[102:103], v161 offset:2048\n\t" \
  "v_mfma_f32_16x16x32_bf16 v[140:143], v[108:111], v[16:19], v[140:143]\n\t" \
  "v_add_u32 v162, 0x10000, v162\n\t" \
  "ds_read_b64_tr_b16 v[104:105], v162 offset:0\n\t" \
  "v_mfma_f32_16x16x32_bf16 v[156:159], v[108:111], v[20:23], v[156:159]\n\t" \
  "ds_read_b64_tr_b16 v[106:107], v162 offset:2048\n\t" \
  "s_waitcnt vmcnt(15) lgkmcnt(10)\n\t" \
  "v_mfma_f32_16x16x32_bf16 v[128:131], v[112:115], v[24:27], v[128:131]\n\t" \
  "v_add_u32 v163, 0x10000, v163\n\t" \
  "ds_read_b64_tr_b16 v[108:109], v163 offset:0\n\t" \
  "v_mfma_f32_16x16x32_bf16 v[144:147], v[112:115], v[28:31], v[144:147]\n\t" \
  "ds_read_b64_tr_b16 v[110:111], v163 offset:2048\n\t" \
  "buffer_load_dwordx4 v[92:95], v164, %[ob], s75 offen offset:3072 sc1\n\t" \
  "v_mfma_f32_16x16x32_bf16 v[132:135], v[116:119], v[24:27], v[132:135]\n\t" \
  "ds_read_b64_tr_b16 v[112:113], v160 offset:4096\n\t" \
  "v_mfma_f32_16x16x32_bf16 v[148:151], v[116:119], v[28:31], v[148:151]\n\t" \
  "ds_read_b64_tr_b16 v[114:115], v160 offset:6144\n\t" \
  "s_waitcnt lgkmcnt(10)\n\t" \
  "v_mfma_f32_16x16x32_bf16 v[136:139], v[120:123], v[24:27], v[136:139]\n\t" \
  "ds_read_b64_tr_b16 v[116:117], v161 offset:4096\n\t" \
  "v_mfma_f32_16x16x32_bf16 v[152:155], v[120:123], v[28:31], v[152:155]\n\t" \
  "ds_read_b64_tr_b16 v[118:119], v161 offset:6144\n\t" \
  "v_mfma_f32_16x16x32_bf16 v[140:143], v[124:127], v[24:27], v[140:143]\n\t" \
  "ds_read_b64_tr_b16 v[120:121], v162 offset:4096\n\t" \
  "buffer_load_dwordx4 v[0:3], v164, %[ob], s76 offen offset:0 sc1\n\t" \
  "v_mfma_f32_16x16x32_bf16 v[156:159], v[124:127], v[28:31], v[156:159]\n\t" \
  "ds_read_b64_tr_b16 v[122:123], v162 offset:6144\n\t" \
  "s_waitcnt vmcnt(15) lgkmcnt(10)\n\t" \
  "v_mfma_f32_16x16x32_bf16 v[128:131], v[96:99], v[32:35], v[128:131]\n\t" \
  "ds_read_b64_tr_b16 v[124:125], v163 offset:4096\n\t" \
  "v_mfma_f32_16x16x32_bf16 v[144:147], v[96:99], v[36:39], v[144:147]\n\t" \
  "ds_read_b64_tr_b16 v[126:127], v163 offset:6144\n\t" \
  "v_mfma_f32_16x16x32_bf16 v[132:135], v[100:103], v[32:35], v[132:135]\n\t" \
  "ds_read_b64_tr_b16 v[96:97], v160 offset:8192\n\t" \
  "v_mfma_f32_16x16x32_bf16 v[148:151], v[100:103], v[36:39], v[148:151]\n\t" \
  "ds_read_b64_tr_b16 v[98:99], v160 offset:10240\n\t" \
  "s_waitcnt lgkmcnt(10)\n\t" \
  "v_mfma_f32_16x16x32_bf16 v[136:139], v[104:107], v[32:35], v[136:139]\n\t" \
  "ds_read_b64_tr_b16 v[100:101], v161 offset:8192\n\t" \
  "buffer_load_dwordx4 v[4:7], v164, %[ob], s76 offen offset:1024 sc1\n\t" \
  "v_mfma_f32_16x16x32_bf16 v[152:155], v[104:107], v[36:39], v[152:155]\n\t" \
  "ds_read_b64_tr_b16 v[102:103], v161 offset:10240\n\t" \
  "v_mfma_f32_16x16x32_bf16 v[140:143], v[108:111], v[32:35], v[140:143]\n\t" \
  "ds_read_b64_tr_b16 v[104:105], v162 offset:8192\n\t" \
  "v_mfma_f32_16x16x32_bf16 v[156:159], v[108:111], v[36:39], v[156:159]\n\t" \
  "ds_read_b64_tr_b16 v[106:107], v162 offset:10240\n\t" \
  "s_waitcnt vmcnt(14) lgkmcnt(10)\n\t" \
  "v_mfma_f32_16x16x32_bf16 v[128:131], v[112:115], v[40:43], v[128:131]\n\t" \
  "ds_read_b64_tr_b16 v[108:109], v163 offset:8192\n\t" \
  "buffer_load_dwordx4 v[8:11], v164, %[ob], s76 offen offset:2048 sc1\n\t" \
  "v_mfma_f32_16x16x32_bf16 v[144:147], v[112:115], v[44:47], v[144:147]\n\t" \
  "ds_read_b64_tr_b16 v[110:111], v163 offset:10240\n\t" \
  "v_mfma_f32_16x16x32_bf16 v[132:135], v[116:119], v[40:43], v[132:135]\n\t" \
  "ds_read_b64_tr_b16 v[112:113], v160 offset:12288\n\t" \
  "v_mfma_f32_16x16x32_bf16 v[148:151], v[116:119], v[44:47], v[148:151]\n\t" \
  "ds_read_b64_tr_b16 v[114:115], v160 offset:14336\n\t" \
  "s_waitcnt lgkmcnt(10)\n\t" \
  "v_mfma_f32_16x16x32_bf16 v[136:139], v[120:123], v[40:43], v[136:139]\n\t" \
  "ds_read_b64_tr_b16 v[116:117], v161 offset:12288\n\t" \
  "buffer_load_dwordx4 v[12:15], v164, %[ob], s76 offen offset:3072 sc1\n\t" \
  "v_mfma_f32_16x16x32_bf16 v[152:155], v[120:123], v[44:47], v[152:155]\n\t" \
  "ds_read_b64_tr_b16 v[118:119], v161 offset:14336\n\t" \
  "v_mfma_f32_16x16x32_bf16 v[140:143], v[124:127], v[40:43], v[140:143]\n\t" \
  "ds_read_b64_tr_b16 v[120:121], v162 offset:12288\n\t" \
  "v_mfma_f32_16x16x32_bf16 v[156:159], v[124:127], v[44:47], v[156:159]\n\t" \
  "ds_read_b64_tr_b16 v[122:123], v162 offset:14336\n\t" \
  "s_waitcnt vmcnt(14) lgkmcnt(10)\n\t" \
  "v_mfma_f32_16x16x32_bf16 v[128:131], v[96:99], v[48:51], v[128:131]\n\t" \
  "ds_read_b64_tr_b16 v[124:125], v163 offset:12288\n\t" \
  "v_mfma_f32_16x16x32_bf16 v[144:147], v[96:99], v[52:55], v[144:147]\n\t" \
  "ds_read_b64_tr_b16 v[126:127], v163 offset:14336\n\t" \
  "buffer_load_dwordx4 v[16:19], v164, %[ob], s77 offen offset:0 sc1\n\t" \
  "v_mfma_f32_16x16x32_bf16 v[132:135], v[100:103], v[48:51], v[132:135]\n\t" \
  "ds_read_b64_tr_b16 v[96:97], v160 offset:16384\n\t" \
  "v_mfma_f32_16x16x32_bf16 v[148:151], v[100:103], v[52:55], v[148:151]\n\t" \
  "ds_read_b64_tr_b16 v[98:99], v160 offset:18432\n\t" \
  "s_waitcnt lgkmcnt(10)\n\t" \
  "v_mfma_f32_16x16x32_bf16 v[136:139], v[104:107], v[48:51], v[136:139]\n\t" \
  "ds_read_b64_tr_b16 v[100:101], v161 offset:16384\n\t" \
  "v_mfma_f32_16x16x32_bf16 v[152:155], v[104:107], v[52:55], v[152:155]\n\t" \
  "ds_read_b64_tr_b16 v[102:103], v161 offset:18432\n\t" \
  "v_mfma_f32_16x16x32_bf16 v[140:143], v[108:111], v[48:51], v[140:143]\n\t" \
  "ds_read_b64_tr_b16 v[104:105], v162 offset:16384\n\t" \
  "buffer_load_dwordx4 v[20:23], v164, %[ob], s77 offen offset:1024 sc1\n\t" \
  "v_mfma_f32_16x16x32_bf16 v[156:159], v[108:111], v[52:55], v[156:159]\n\t" \
  "ds_read_b64_tr_b16 v[106:107], v162 offset:18432\n\t" \
  "s_waitcnt vmcnt(14) lgkmcnt(10)\n\t" \
  "v_mfma_f32_16x16x32_bf16 v[128:131], v[112:115], v[56:59], v[128:131]\n\t" \
  "ds_read_b64_tr_b16 v[108:109], v163 offset:16384\n\t" \
  "v_mfma_f32_16x16x32_bf16 v[144:147], v[112:115], v[60:63], v[144:147]\n\t" \
  "ds_read_b64_tr_b16 v[110:111], v163 offset:18432\n\t" \
  "ds_read_b32 v165, v165\n\t" \
  "v_mfma_f32_16x16x32_bf16 v[132:135], v[116:119], v[56:59], v[132:135]\n\t" \
  "ds_read_b64_tr_b16 v[112:113], v160 offset:20480\n\t" \
  "v_mfma_f32_16x16x32_bf16 v[148:151], v[116:119], v[60:63], v[148:151]\n\t" \
  "ds_read_b64_tr_b16 v[114:115], v160 offset:22528\n\t" \
  "s_waitcnt lgkmcnt(11)\n\t" \
  "v_mfma_f32_16x16x32_bf16 v[136:139], v[120:123], v[56:59], v[136:139]\n\t" \
  "ds_read_b64_tr_b16 v[116:117], v161 offset:20480\n\t" \
  "buffer_load_dwordx4 v[24:27], v164, %[ob], s77 offen offset:2048 sc1\n\t" \
  "v_mfma_f32_16x16x32_bf16 v[152:155], v[120:123], v[60:63], v[152:155]\n\t" \
  "ds_read_b64_tr_b16 v[118:119], v161 offset:22528\n\t" \
  "v_mfma_f32_16x16x32_bf16 v[140:143], v[124:127], v[56:59], v[140:143]\n\t" \
  "ds_read_b64_tr_b16 v[120:121], v162 offset:20480\n\t" \
  "v_mfma_f32_16x16x32_bf16 v[156:159], v[124:127], v[60:63], v[156:159]\n\t" \
  "ds_read_b64_tr_b16 v[122:123], v162 offset:22528\n\t" \
  "s_waitcnt vmcnt(13) lgkmcnt(11)\n\t" \
  "v_mfma_f32_16x16x32_bf16 v[128:131], v[96:99], v[64:67], v[128:131]\n\t" \
  "ds_read_b64_tr_b16 v[124:125], v163 offset:20480\n\t" \
  "buffer_load_dwordx4 v[28:31], v164, %[ob], s77 offen offset:3072 sc1\n\t" \
  "v_mfma_f32_16x16x32_bf16 v[144:147], v[96:99], v[68:71], v[144:147]\n\t" \
  "ds_read_b64_tr_b16 v[126:127], v163 offset:22528\n\t" \
  "v_mfma_f32_16x16x32_bf16 v[132:135], v[100:103], v[64:67], v[132:135]\n\t" \
  "ds_read_b64_tr_b16 v[96:97], v160 offset:24576\n\t" \
  "s_waitcnt lgkmcnt(9)\n\t" \
  "v_mfma_f32_16x16x32_bf16 v[148:151], v[100:103], v[68:71], v[148:151]\n\t" \
  "ds_read_b64_tr_b16 v[98:99], v160 offset:26624\n\t" \
  "v_cmp_lt_u32 vcc, 15, v165\n\t" \
  "v_mfma_f32_16x16x32_bf16 v[136:139], v[104:107], v[64:67], v[136:139]\n\t" \
  "ds_read_b64_tr_b16 v[100:101], v161 offset:24576\n\t" \
  "s_cbranch_vccz QA14_%=\n\t" \
  "QB14_%=:\n\t" \
  "v_mfma_f32_16x16x32_bf16 v[152:155], v[104:107], v[68:71], v[152:155]\n\t" \
  "ds_read_b64_tr_b16 v[102:103], v161 offset:26624\n\t" \
  "buffer_load_dwordx4 v[32:35], v164, %[ob], s78 offen offset:0 sc1\n\t" \
  "v_mfma_f32_16x16x32_bf16 v[140:143], v[108:111], v[64:67], v[140:143]\n\t" \
  "ds_read_b64_tr_b16 v[104:105], v162 offset:24576\n\t" \
  "v_mfma_f32_16x16x32_bf16 v[156:159], v[108:111], v[68:71], v[156:159]\n\t" \
  "ds_read_b64_tr_b16 v[106:107], v162 offset:26624\n\t" \
  "s_waitcnt vmcnt(13) lgkmcnt(10)\n\t" \
  "v_mfma_f32_16x16x32_bf16 v[128:131], v[112:115], v[72:75], v[128:131]\n\t" \
  "ds_read_b64_tr_b16 v[108:109], v163 offset:24576\n\t" \
  "v_mfma_f32_16x16x32_bf16 v[144:147], v[112:115], v[76:79], v[144:147]\n\t" \
  "ds_read_b64_tr_b16 v[110:111], v163 offset:26624\n\t" \
  "v_mfma_f32_16x16x32_bf16 v[132:135], v[116:119], v[72:75], v[132:135]\n\t" \
  "ds_read_b64_tr_b16 v[112:113], v160 offset:28672\n\t" \
  "buffer_load_dwordx4 v[36:39], v164, %[ob], s78 offen offset:1024 sc1\n\t" \
  "v_mfma_f32_16x16x32_bf16 v[148:151], v[116:119], v[76:79], v[148:151]\n\t" \
  "ds_read_b64_tr_b16 v[114:115], v160 offset:30720\n\t" \
  "s_waitcnt lgkmcnt(10)\n\t" \
  "v_mfma_f32_16x16x32_bf16 v[136:139], v[120:123], v[72:75], v[136:139]\n\t" \
  "ds_read_b64_tr_b16 v[116:117], v161 offset:28672\n\t" \
  "v_mfma_f32_16x16x32_bf16 v[152:155], v[120:123], v[76:79], v[152:155]\n\t" \
  "ds_read_b64_tr_b16 v[118:119], v161 offset:30720\n\t" \
  "v_mfma_f32_16x16x32_bf16 v[140:143], v[124:127], v[72:75], v[140:143]\n\t" \
  "ds_read_b64_tr_b16 v[120:121], v162 offset:28672\n\t" \
  "v_mfma_f32_16x16x32_bf16 v[156:159], v[124:127], v[76:79], v[156:159]\n\t" \
  "ds_read_b64_tr_b16 v[122:123], v162 offset:30720\n\t" \
  "s_waitcnt vmcnt(12) lgkmcnt(10)\n\t" \
  "v_mfma_f32_16x16x32_bf16 v[128:131], v[96:99], v[80:83], v[128:131]\n\t" \
  "ds_read_b64_tr_b16 v[124:125], v163 offset:28672\n\t" \
  "buffer_load_dwordx4 v[40:43], v164, %[ob], s78 offen offset:2048 sc1\n\t" \
  "v_mfma_f32_16x16x32_bf16 v[144:147], v[96:99], v[84:87], v[144:147]\n\t" \
  "ds_read_b64_tr_b16 v[126:127], v163 offset:30720\n\t" \
  "v_mfma_f32_16x16x32_bf16 v[132:135], v[100:103], v[80:83], v[132:135]\n\t" \
  "ds_read_b64_tr_b16 v[96:97], v160 offset:32768\n\t" \
  "v_mfma_f32_16x16x32_bf16 v[148:151], v[100:103], v[84:87], v[148:151]\n\t" \
  "ds_read_b64_tr_b16 v[98:99], v160 offset:34816\n\t" \
  "s_waitcnt lgkmcnt(10)\n\t" \
  "v_mfma_f32_16x16x32_bf16 v[136:139], v[104:107], v[80:83], v[136:139]\n\t" \
  "ds_read_b64_tr_b16 v[100:101], v161 offset:32768\n\t" \
  "buffer_load_dwordx4 v[44:47], v164, %[ob], s78 offen offset:3072 sc1\n\t" \
  "v_mfma_f32_16x16x32_bf16 v[152:155], v[104:107], v[84:87], v[152:155]\n\t" \
  "ds_read_b64_tr_b16 v[102:103], v161 offset:34816\n\t" \
  "v_mfma_f32_16x16x32_bf16 v[140:143], v[108:111], v[80:83], v[140:143]\n\t" \
  "ds_read_b64_tr_b16 v[104:105], v162 offset:32768\n\t" \
  "v_mfma_f32_16x16x32_bf16 v[156:159], v[108:111], v[84:87], v[156:159]\n\t" \
  "ds_read_b64_tr_b16 v[106:107], v162 offset:34816\n\t" \
  "s_waitcnt vmcnt(12) lgkmcnt(10)\n\t" \
  "v_mfma_f32_16x16x32_bf16 v[128:131], v[112:115], v[88:91], v[128:131]\n\t" \
  "ds_read_b64_tr_b16 v[108:109], v163 offset:32768\n\t" \
  "v_mfma_f32_16x16x32_bf16 v[144:147], v[112:115], v[92:95], v[144:147]\n\t" \
  "ds_read_b64_tr_b16 v[110:111], v163 offset:34816\n\t" \
  "buffer_load_dwordx4 v[48:51], v164, %[ob], s79 offen offset:0 sc1\n\t" \
  "v_mfma_f32_16x16x32_bf16 v[132:135], v[116:119], v[88:91], v[132:135]\n\t" \
  "ds_read_b64_tr_b16 v[112:113], v160 offset:36864\n\t" \
  "v_mfma_f32_16x16x32_bf16 v[148:151], v[116:119], v[92:95], v[148:151]\n\t" \
  "ds_read_b64_tr_b16 v[114:115], v160 offset:38912\n\t" \
  "s_waitcnt lgkmcnt(10)\n\t" \
  "v_mfma_f32_16x16x32_bf16 v[136:139], v[120:123], v[88:91], v[136:139]\n\t" \
  "ds_read_b64_tr_b16 v[116:117], v161 offset:36864\n\t" \
  "v_mfma_f32_16x16x32_bf16 v[152:155], v[120:123], v[92:95], v[152:155]\n\t" \
  "ds_read_b64_tr_b16 v[118:119], v161 offset:38912\n\t" \
  "v_mfma_f32_16x16x32_bf16 v[140:143], v[124:127], v[88:91], v[140:143]\n\t" \
  "ds_read_b64_tr_b16 v[120:121], v162 offset:36864\n\t" \
  "buffer_load_dwordx4 v[52:55], v164, %[ob], s79 offen offset:1024 sc1\n\t" \
  "v_mfma_f32_16x16x32_bf16 v[156:159], v[124:127], v[92:95], v[156:159]\n\t" \
  "ds_read_b64_tr_b16 v[122:123], v162 offset:38912\n\t" \
  "s_waitcnt vmcnt(12) lgkmcnt(10)\n\t" \
  "v_mfma_f32_16x16x32_bf16 v[128:131], v[96:99], v[0:3], v[128:131]\n\t" \
  "ds_read_b64_tr_b16 v[124:125], v163 offset:36864\n\t" \
  "v_mfma_f32_16x16x32_bf16 v[144:147], v[96:99], v[4:7], v[144:147]\n\t" \
  "ds_read_b64_tr_b16 v[126:127], v163 offset:38912\n\t" \
  "v_mfma_f32_16x16x32_bf16 v[132:135], v[100:103], v[0:3], v[132:135]\n\t" \
  "ds_read_b64_tr_b16 v[96:97], v160 offset:40960\n\t" \
  "v_mfma_f32_16x16x32_bf16 v[148:151], v[100:103], v[4:7], v[148:151]\n\t" \
  "ds_read_b64_tr_b16 v[98:99], v160 offset:43008\n\t" \
  "s_waitcnt lgkmcnt(10)\n\t" \
  "v_mfma_f32_16x16x32_bf16 v[136:139], v[104:107], v[0:3], v[136:139]\n\t" \
  "ds_read_b64_tr_b16 v[100:101], v161 offset:40960\n\t" \
  "buffer_load_dwordx4 v[56:59], v164, %[ob], s79 offen offset:2048 sc1\n\t" \
  "v_mfma_f32_16x16x32_bf16 v[152:155], v[104:107], v[4:7], v[152:155]\n\t" \
  "ds_read_b64_tr_b16 v[102:103], v161 offset:43008\n\t" \
  "v_mfma_f32_16x16x32_bf16 v[140:143], v[108:111], v[0:3], v[140:143]\n\t" \
  "ds_read_b64_tr_b16 v[104:105], v162 offset:40960\n\t" \
  "v_mfma_f32_16x16x32_bf16 v[156:159], v[108:111], v[4:7], v[156:159]\n\t" \
  "ds_read_b64_tr_b16 v[106:107], v162 offset:43008\n\t" \
  "s_waitcnt vmcnt(11) lgkmcnt(10)\n\t" \
  "v_mfma_f32_16x16x32_bf16 v[128:131], v[112:115], v[8:11], v[128:131]\n\t" \
  "ds_read_b64_tr_b16 v[108:109], v163 offset:40960\n\t" \
  "buffer_load_dwordx4 v[60:63], v164, %[ob], s79 offen offset:3072 sc1\n\t" \
  "v_mfma_f32_16x16x32_bf16 v[144:147], v[112:115], v[12:15], v[144:147]\n\t" \
  "ds_read_b64_tr_b16 v[110:111], v163 offset:43008\n\t" \
  "v_mfma_f32_16x16x32_bf16 v[132:135], v[116:119], v[8:11], v[132:135]\n\t" \
  "ds_read_b64_tr_b16 v[112:113], v160 offset:45056\n\t" \
  "v_mfma_f32_16x16x32_bf16 v[148:151], v[116:119], v[12:15], v[148:151]\n\t" \
  "ds_read_b64_tr_b16 v[114:115], v160 offset:47104\n\t" \
  "s_waitcnt lgkmcnt(10)\n\t" \
  "v_mfma_f32_16x16x32_bf16 v[136:139], v[120:123], v[8:11], v[136:139]\n\t" \
  "ds_read_b64_tr_b16 v[116:117], v161 offset:45056\n\t" \
  "v_mfma_f32_16x16x32_bf16 v[152:155], v[120:123], v[12:15], v[152:155]\n\t" \
  "ds_read_b64_tr_b16 v[118:119], v161 offset:47104\n\t" \
  "v_mfma_f32_16x16x32_bf16 v[140:143], v[124:127], v[8:11], v[140:143]\n\t" \
  "ds_read_b64_tr_b16 v[120:121], v162 offset:45056\n\t" \
  "v_mfma_f32_16x16x32_bf16 v[156:159], v[124:127], v[12:15], v[156:159]\n\t" \
  "ds_read_b64_tr_b16 v[122:123], v162 offset:47104\n\t" \
  "s_waitcnt vmcnt(10) lgkmcnt(10)\n\t" \
  "v_mfma_f32_16x16x32_bf16 v[128:131], v[96:99], v[16:19], v[128:131]\n\t" \
  "ds_read_b64_tr_b16 v[124:125], v163 offset:45056\n\t" \
  "v_mfma_f32_16x16x32_bf16 v[144:147], v[96:99], v[20:23], v[144:147]\n\t" \
  "ds_read_b64_tr_b16 v[126:127], v163 offset:47104\n\t" \
  "v_mfma_f32_16x16x32_bf16 v[132:135], v[100:103], v[16:19], v[132:135]\n\t" \
  "ds_read_b64_tr_b16 v[96:97], v160 offset:49152\n\t" \
  "v_mfma_f32_16x16x32_bf16 v[148:151], v[100:103], v[20:23], v[148:151]\n\t" \
  "ds_read_b64_tr_b16 v[98:99], v160 offset:51200\n\t" \
  "s_waitcnt lgkmcnt(10)\n\t" \
  "v_mfma_f32_16x16x32_bf16 v[136:139], v[104:107], v[16:19], v[136:139]\n\t" \
  "ds_read_b64_tr_b16 v[100:101], v161 offset:49152\n\t" \
  "v_xor_b32 v0, 32, v167\n\t" \
  "v_mfma_f32_16x16x32_bf16 v[152:155], v[104:107], v[20:23], v[152:155]\n\t" \
  "ds_read_b64_tr_b16 v[102:103], v161 offset:51200\n\t" \
  "v_xor_b32 v1, 64, v167\n\t" \
  "v_mfma_f32_16x16x32_bf16 v[140:143], v[108:111], v[16:19], v[140:143]\n\t" \
  "ds_read_b64_tr_b16 v[104:105], v162 offset:49152\n\t" \
  "v_xor_b32 v2, 0x60, v167\n\t" \
  "v_mfma_f32_16x16x32_bf16 v[156:159], v[108:111], v[20:23], v[156:159]\n\t" \
  "ds_read_b64_tr_b16 v[106:107], v162 offset:51200\n\t" \
  "s_waitcnt vmcnt(8) lgkmcnt(10)\n\t" \
  "v_mfma_f32_16x16x32_bf16 v[128:131], v[112:115], v[24:27], v[128:131]\n\t" \
  "ds_read_b64_tr_b16 v[108:109], v163 offset:49152\n\t" \
  "v_bfe_u32 v5, v164, 7, 3\n\t" \
  "v_mfma_f32_16x16x32_bf16 v[144:147], v[112:115], v[28:31], v[144:147]\n\t" \
  "ds_read_b64_tr_b16 v[110:111], v163 offset:51200\n\t" \
  "v_and_b32 v4, 0x70, v164\n\t" \
  "v_mfma_f32_16x16x32_bf16 v[132:135], v[116:119], v[24:27], v[132:135]\n\t" \
  "ds_read_b64_tr_b16 v[112:113], v160 offset:53248\n\t" \
  "v_lshl_or_b32 v4, v5, 12, v4\n\t" \
  "v_mfma_f32_16x16x32_bf16 v[148:151], v[116:119], v[28:31], v[148:151]\n\t" \
  "ds_read_b64_tr_b16 v[114:115], v160 offset:55296\n\t" \
  "s_waitcnt lgkmcnt(10)\n\t" \
  "v_mfma_f32_16x16x32_bf16 v[136:139], v[120:123], v[24:27], v[136:139]\n\t" \
  "ds_read_b64_tr_b16 v[116:117], v161 offset:53248\n\t" \
  "v_add_u32 v5, 0x8000, v4\n\t" \
  "v_mfma_f32_16x16x32_bf16 v[152:155], v[120:123], v[28:31], v[152:155]\n\t" \
  "ds_read_b64_tr_b16 v[118:119], v161 offset:55296\n\t" \
  "v_add_u32 v6, 0x10000, v4\n\t" \
  "v_mfma_f32_16x16x32_bf16 v[140:143], v[124:127], v[24:27], v[140:143]\n\t" \
  "ds_read_b64_tr_b16 v[120:121], v162 offset:53248\n\t" \
  "v_add_u32 v7, 0x18000, v4\n\t" \
  "v_mfma_f32_16x16x32_bf16 v[156:159], v[124:127], v[28:31], v[156:159]\n\t" \
  "ds_read_b64_tr_b16 v[122:123], v162 offset:55296\n\t" \
  "s_waitcnt vmcnt(6) lgkmcnt(10)\n\t" \
  "v_mfma_f32_16x16x32_bf16 v[128:131], v[96:99], v[32:35], v[128:131]\n\t" \
  "ds_read_b64_tr_b16 v[124:125], v163 offset:53248\n\t" \
  "v_mfma_f32_16x16x32_bf16 v[144:147], v[96:99], v[36:39], v[144:147]\n\t" \
  "ds_read_b64_tr_b16 v[126:127], v163 offset:55296\n\t" \
  "v_mfma_f32_16x16x32_bf16 v[132:135], v[100:103], v[32:35], v[132:135]\n\t" \
  "ds_read_b64_tr_b16 v[96:97], v160 offset:57344\n\t" \
  "v_mfma_f32_16x16x32_bf16 v[148:151], v[100:103], v[36:39], v[148:151]\n\t" \
  "ds_read_b64_tr_b16 v[98:99], v160 offset:59392\n\t" \
  "s_waitcnt lgkmcnt(10)\n\t" \
  "v_mfma_f32_16x16x32_bf16 v[136:139], v[104:107], v[32:35], v[136:139]\n\t" \
  "ds_read_b64_tr_b16 v[100:101], v161 offset:57344\n\t" \
  "v_mfma_f32_16x16x32_bf16 v[152:155], v[104:107], v[36:39], v[152:155]\n\t" \
  "ds_read_b64_tr_b16 v[102:103], v161 offset:59392\n\t" \
  "v_mfma_f32_16x16x32_bf16 v[140:143], v[108:111], v[32:35], v[140:143]\n\t" \
  "ds_read_b64_tr_b16 v[104:105], v162 offset:57344\n\t" \
  "v_mfma_f32_16x16x32_bf16 v[156:159], v[108:111], v[36:39], v[156:159]\n\t" \
  "ds_read_b64_tr_b16 v[106:107], v162 offset:59392\n\t" \
  "s_waitcnt vmcnt(4) lgkmcnt(10)\n\t" \
  "v_mfma_f32_16x16x32_bf16 v[128:131], v[112:115], v[40:43], v[128:131]\n\t" \
  "ds_read_b64_tr_b16 v[108:109], v163 offset:57344\n\t" \
  "v_mfma_f32_16x16x32_bf16 v[144:147], v[112:115], v[44:47], v[144:147]\n\t" \
  "ds_read_b64_tr_b16 v[110:111], v163 offset:59392\n\t" \
  "v_mfma_f32_16x16x32_bf16 v[132:135], v[116:119], v[40:43], v[132:135]\n\t" \
  "ds_read_b64_tr_b16 v[112:113], v160 offset:61440\n\t" \
  "v_mfma_f32_16x16x32_bf16 v[148:151], v[116:119], v[44:47], v[148:151]\n\t" \
  "ds_read_b64_tr_b16 v[114:115], v160 offset:63488\n\t" \
  "s_waitcnt lgkmcnt(10)\n\t" \
  "v_mfma_f32_16x16x32_bf16 v[136:139], v[120:123], v[40:43], v[136:139]\n\t" \
  "ds_read_b64_tr_b16 v[116:117], v161 offset:61440\n\t" \
  "v_mfma_f32_16x16x32_bf16 v[152:155], v[120:123], v[44:47], v[152:155]\n\t" \
  "ds_read_b64_tr_b16 v[118:119], v161 offset:63488\n\t" \
  "v_mfma_f32_16x16x32_bf16 v[140:143], v[124:127], v[40:43], v[140:143]\n\t" \
  "ds_read_b64_tr_b16 v[120:121], v162 offset:61440\n\t" \
  "v_mfma_f32_16x16x32_bf16 v[156:159], v[124:127], v[44:47], v[156:159]\n\t" \
  "ds_read_b64_tr_b16 v[122:123], v162 offset:63488\n\t" \
  "s_waitcnt vmcnt(0) lgkmcnt(10)\n\t" \
  "v_mfma_f32_16x16x32_bf16 v[128:131], v[96:99], v[48:51], v[128:131]\n\t" \
  "ds_read_b64_tr_b16 v[124:125], v163 offset:61440\n\t" \
  "v_mfma_f32_16x16x32_bf16 v[132:135], v[100:103], v[48:51], v[132:135]\n\t" \
  "ds_read_b64_tr_b16 v[126:127], v163 offset:63488\n\t" \
  "s_waitcnt lgkmcnt(6)\n\t" \
  "v_mfma_f32_16x16x32_bf16 v[136:139], v[104:107], v[48:51], v[136:139]\n\t" \
  "v_mfma_f32_16x16x32_bf16 v[140:143], v[108:111], v[48:51], v[140:143]\n\t" \
  "v_mfma_f32_16x16x32_bf16 v[128:131], v[112:115], v[56:59], v[128:131]\n\t" \
  "s_waitcnt lgkmcnt(2)\n\t" \
  "v_mfma_f32_16x16x32_bf16 v[132:135], v[116:119], v[56:59], v[132:135]\n\t" \
  "v_mfma_f32_16x16x32_bf16 v[136:139], v[120:123], v[56:59], v[136:139]\n\t" \
  "v_mfma_f32_16x16x32_bf16 v[144:147], v[96:99], v[52:55], v[144:147]\n\t" \
  "s_waitcnt lgkmcnt(0)\n\t" \
  "v_mfma_f32_16x16x32_bf16 v[140:143], v[124:127], v[56:59], v[140:143]\n\t" \
  "v_mfma_f32_16x16x32_bf16 v[144:147], v[112:115], v[60:63], v[144:147]\n\t" \
  "s_nop 0\n\t" \
  "v_cvt_pk_bf16_f32 v16, v128, v129\n\t" \
  "v_mfma_f32_16x16x32_bf16 v[148:151], v[100:103], v[52:55], v[148:151]\n\t" \
  "v_cvt_pk_bf16_f32 v17, v130, v131\n\t" \
  "v_cvt_pk_bf16_f32 v18, v132, v133\n\t" \
  "v_mfma_f32_16x16x32_bf16 v[148:151], v[116:119], v[60:63], v[148:151]\n\t" \
  "v_cvt_pk_bf16_f32 v19, v134, v135\n\t" \
  "ds_write_b64 v167, v[16:17]\n\t" \
  "v_mfma_f32_16x16x32_bf16 v[152:155], v[104:107], v[52:55], v[152:155]\n\t" \
  "v_cvt_pk_bf16_f32 v20, v136, v137\n\t" \
  "ds_write_b64 v0, v[18:19]\n\t" \
  "v_mfma_f32_16x16x32_bf16 v[152:155], v[120:123], v[60:63], v[152:155]\n\t" \
  "v_cvt_pk_bf16_f32 v21, v138, v139\n\t" \
  "v_cvt_pk_bf16_f32 v22, v140, v141\n\t" \
  "v_mfma_f32_16x16x32_bf16 v[156:159], v[108:111], v[52:55], v[156:159]\n\t" \
  "v_cvt_pk_bf16_f32 v23, v142, v143\n\t" \
  "ds_write_b64 v1, v[20:21]\n\t" \
  "v_mfma_f32_16x16x32_bf16 v[156:159], v[124:127], v[60:63], v[156:159]\n\t" \
  "v_cvt_pk_bf16_f32 v24, v144, v145\n\t" \
  "ds_write_b64 v2, v[22:23]\n\t" \
  "v_cvt_pk_bf16_f32 v25, v146, v147\n\t" \
  "ds_read_b128 v[64:67], v166 offset:0\n\t" \
  "v_cvt_pk_bf16_f32 v26, v148, v149\n\t" \
  "ds_read_b128 v[68:71], v166 offset:1024\n\t" \
  "v_cvt_pk_bf16_f32 v27, v150, v151\n\t" \
  "ds_write_b64 v167, v[24:25]\n\t" \
  "v_cvt_pk_bf16_f32 v28, v152, v153\n\t" \
  "v_cvt_pk_bf16_f32 v29, v154, v155\n\t" \
  "ds_write_b64 v0, v[26:27]\n\t" \
  "v_cvt_pk_bf16_f32 v30, v156, v157\n\t" \
  "v_cvt_pk_bf16_f32 v31, v158, v159\n\t" \
  "ds_write_b64 v1, v[28:29]\n\t" \
  "ds_write_b64 v2, v[30:31]\n\t" \
  "ds_read_b128 v[72:75], v166 offset:0\n\t" \
  "ds_read_b128 v[76:79], v166 offset:1024\n\t" \
  "s_waitcnt lgkmcnt(7)\n\t" \
  "global_store_dwordx4 v4, v[64:67], %[op] sc1\n\t" \
  "s_waitcnt lgkmcnt(6)\n\t" \
  "global_store_dwordx4 v5, v[68:71], %[op] sc1\n\t" \
  "s_waitcnt lgkmcnt(1)\n\t" \
  "global_store_dwordx4 v6, v[72:75], %[op] sc1\n\t" \
  "s_waitcnt lgkmcnt(0)\n\t" \
  "global_store_dwordx4 v7, v[76:79], %[op] sc1\n\t" \
  "s_nop 1\n\t"
#define G3_EP 2
#define G3_CLOB "v0", "v1", "v2", "v3", "v4", "v5", "v6", "v7", "v8", "v9", "v10", "v11", "v12", "v13", "v14", "v15", "v16", "v17", "v18", "v19", "v20", "v21", "v22", "v23", "v24", "v25", "v26", "v27", "v28", "v29", "v30", "v31", "v32", "v33", "v34", "v35", "v36", "v37", "v38", "v39", "v40", "v41", "v42", "v43", "v44", "v45", "v46", "v47", "v48", "v49", "v50", "v51", "v52", "v53", "v54", "v55", "v56", "v57", "v58", "v59", "v60", "v61", "v62", "v63", "v64", "v65", "v66", "v67", "v68", "v69", "v70", "v71", "v72", "v73", "v74", "v75", "v76", "v77", "v78", "v79", "v80", "v81", "v82", "v83", "v84", "v85", "v86", "v87", "v88", "v89", "v90", "v91", "v92", "v93", "v94", "v95", "v97", "v98", "v99", "v100", "v101", "v102", "v103", "v104", "v105", "v106", "v107", "v108", "v109", "v110", "v111", "v112", "v113", "v114", "v115", "v116", "v117", "v118", "v119", "v120", "v121", "v122", "v123", "v124", "v125", "v126", "v127", "v128", "v129", "v130", "v131", "v132", "v133", "v134", "v135", "v136", "v137", "v138", "v139", "v140", "v141", "v142", "v143", "v144", "v145", "v146", "v147", "v148", "v149", "v150", "v151", "v152", "v153", "v154", "v155", "v156", "v157", "v158", "v159", "s64", "s65", "s66", "s67", "s68", "s69", "s70", "s71", "s72", "s73", "s74", "s75", "s76", "s77", "s78", "s79"

#define PP_ASM \
  "v_mov_b32 v60, 0\n\t" \
  "s_mov_b32 %[t0], %[n0]\n\t" \
  "s_cmp_eq_u32 %[t0], 0\n\t" \
  "s_cbranch_scc1 PAx_%=\n\t" \
  "PA_%=:\n\t" \
  "s_barrier\n\t" \
  "s_sub_u32 %[t0], %[t0], 1\n\t" \
  "s_cmp_lg_u32 %[t0], 0\n\t" \
  "s_cbranch_scc1 PA_%=\n\t" \
  "PAx_%=:\n\t" \
  "s_mov_b32 m0, %[shd]\n\t" \
  "global_load_dword v160, v60, %[fl] sc1\n\t" \
  "global_load_lds_dwordx4 v163, %[shp]\n\t" \
  "s_mov_b32 m0, %[tgd]\n\t" \
  "s_mov_b64 exec, %[em]\n\t" \
  "global_load_lds_dword v60, %[fl] sc1\n\t" \
  "s_mov_b64 exec, -1\n\t" \
  "global_load_ushort v161, v60, %[hp]\n\t" \
  "global_load_ushort v162, v60, %[dp]\n\t" \
  "global_load_dwordx4 v[0:3], v152, %[kb0]\n\t" \
  "global_load_dwordx4 v[4:7], v153, %[kb0]\n\t" \
  "global_load_dwordx4 v[8:11], v154, %[kb0]\n\t" \
  "global_load_dwordx4 v[12:15], v155, %[kb3] offset:0\n\t" \
  "global_load_dwordx4 v[28:31], v155, %[vb0] offset:0\n\t" \
  "s_setprio 2\n\t" \
  "s_setprio 3\n\t" \
  "s_barrier\n\t" \
  "global_load_dwordx4 v[16:19], v155, %[kb3] offset:2048\n\t" \
  "global_load_dwordx4 v[32:35], v155, %[vb0] offset:2048\n\t" \
  "global_load_dwordx4 v[20:23], v155, %[kb5] offset:0\n\t" \
  "s_setprio 2\n\t" \
  "s_waitcnt vmcnt(8)\n\t" \
  "ds_read_b128 v[128:131], v156 offset:0\n\t" \
  "ds_read_b128 v[132:135], v156 offset:16\n\t" \
  "ds_read_b128 v[136:139], v156 offset:32\n\t" \
  "ds_read_b128 v[140:143], v156 offset:48\n\t" \
  "s_waitcnt lgkmcnt(0)\n\t" \
  "v_lshlrev_b32 v64, 16, v128\n\t" \
  "v_and_b32 v65, 0xffff0000, v128\n\t" \
  "v_lshlrev_b32 v66, 16, v129\n\t" \
  "v_and_b32 v67, 0xffff0000, v129\n\t" \
  "v_lshlrev_b32 v68, 16, v130\n\t" \
  "v_and_b32 v69, 0xffff0000, v130\n\t" \
  "v_lshlrev_b32 v70, 16, v131\n\t" \
  "v_and_b32 v71, 0xffff0000, v131\n\t" \
  "v_lshlrev_b32 v72, 16, v132\n\t" \
  "v_and_b32 v73, 0xffff0000, v132\n\t" \
  "v_lshlrev_b32 v74, 16, v133\n\t" \
  "v_and_b32 v75, 0xffff0000, v133\n\t" \
  "v_lshlrev_b32 v76, 16, v134\n\t" \
  "v_and_b32 v77, 0xffff0000, v134\n\t" \
  "v_lshlrev_b32 v78, 16, v135\n\t" \
  "v_and_b32 v79, 0xffff0000, v135\n\t" \
  "v_lshlrev_b32 v80, 16, v136\n\t" \
  "v_and_b32 v81, 0xffff0000, v136\n\t" \
  "v_lshlrev_b32 v82, 16, v137\n\t" \
  "v_and_b32 v83, 0xffff0000, v137\n\t" \
  "v_lshlrev_b32 v84, 16, v138\n\t" \
  "v_and_b32 v85, 0xffff0000, v138\n\t" \
  "v_lshlrev_b32 v86, 16, v139\n\t" \
  "v_and_b32 v87, 0xffff0000, v139\n\t" \
  "v_lshlrev_b32 v88, 16, v140\n\t" \
  "v_and_b32 v89, 0xffff0000, v140\n\t" \
  "v_lshlrev_b32 v90, 16, v141\n\t" \
  "v_and_b32 v91, 0xffff0000, v141\n\t" \
  "v_lshlrev_b32 v92, 16, v142\n\t" \
  "v_and_b32 v93, 0xffff0000, v142\n\t" \
  "v_lshlrev_b32 v94, 16, v143\n\t" \
  "v_and_b32 v95, 0xffff0000, v143\n\t" \
  "s_waitcnt vmcnt(7)\n\t" \
  "s_mov_b64 exec, %[zm]\n\t" \
  "v_mov_b32 v0, 0\n\t" \
  "v_mov_b32 v1, 0\n\t" \
  "v_mov_b32 v2, 0\n\t" \
  "v_mov_b32 v3, 0\n\t" \
  "s_mov_b64 exec, -1\n\t" \
  "v_lshlrev_b32 v128, 16, v0\n\t" \
  "v_and_b32 v129, 0xffff0000, v0\n\t" \
  "v_lshlrev_b32 v130, 16, v1\n\t" \
  "v_and_b32 v131, 0xffff0000, v1\n\t" \
  "v_lshlrev_b32 v132, 16, v2\n\t" \
  "v_and_b32 v133, 0xffff0000, v2\n\t" \
  "v_lshlrev_b32 v134, 16, v3\n\t" \
  "v_and_b32 v135, 0xffff0000, v3\n\t" \
  "v_mul_f32 v96, v128, v67\n\t" \
  "v_mul_f32 v97, v129, v71\n\t" \
  "v_mul_f32 v98, v130, v75\n\t" \
  "v_mul_f32 v99, v131, v79\n\t" \
  "v_mul_f32 v100, v132, v83\n\t" \
  "v_mul_f32 v101, v133, v87\n\t" \
  "v_mul_f32 v102, v134, v91\n\t" \
  "v_mul_f32 v103, v135, v95\n\t" \
  "s_waitcnt vmcnt(6)\n\t" \
  "s_mov_b64 exec, %[zm]\n\t" \
  "v_mov_b32 v4, 0\n\t" \
  "v_mov_b32 v5, 0\n\t" \
  "v_mov_b32 v6, 0\n\t" \
  "v_mov_b32 v7, 0\n\t" \
  "s_mov_b64 exec, -1\n\t" \
  "v_lshlrev_b32 v128, 16, v4\n\t" \
  "v_and_b32 v129, 0xffff0000, v4\n\t" \
  "v_lshlrev_b32 v130, 16, v5\n\t" \
  "v_and_b32 v131, 0xffff0000, v5\n\t" \
  "v_lshlrev_b32 v132, 16, v6\n\t" \
  "v_and_b32 v133, 0xffff0000, v6\n\t" \
  "v_lshlrev_b32 v134, 16, v7\n\t" \
  "v_and_b32 v135, 0xffff0000, v7\n\t" \
  "v_fmac_f32 v96, v128, v66\n\t" \
  "v_fmac_f32 v97, v129, v70\n\t" \
  "v_fmac_f32 v98, v130, v74\n\t" \
  "v_fmac_f32 v99, v131, v78\n\t" \
  "v_fmac_f32 v100, v132, v82\n\t" \
  "v_fmac_f32 v101, v133, v86\n\t" \
  "v_fmac_f32 v102, v134, v90\n\t" \
  "v_fmac_f32 v103, v135, v94\n\t" \
  "v_mul_f32 v104, v128, v67\n\t" \
  "v_mul_f32 v105, v129, v71\n\t" \
  "v_mul_f32 v106, v130, v75\n\t" \
  "v_mul_f32 v107, v131, v79\n\t" \
  "v_mul_f32 v108, v132, v83\n\t" \
  "v_mul_f32 v109, v133, v87\n\t" \
  "v_mul_f32 v110, v134, v91\n\t" \
  "v_mul_f32 v111, v135, v95\n\t" \
  "s_waitcnt vmcnt(5)\n\t" \
  "s_mov_b64 exec, %[zm]\n\t" \
  "v_mov_b32 v8, 0\n\t" \
  "v_mov_b32 v9, 0\n\t" \
  "v_mov_b32 v10, 0\n\t" \
  "v_mov_b32 v11, 0\n\t" \
  "s_mov_b64 exec, -1\n\t" \
  "s_mov_b32 s85, 0x300000\n\t" \
  "s_load_dword s84, %[ph], s85\n\t" \
  "s_mov_b32 s85, 0x300800\n\t" \
  "s_load_dword s84, %[ph], s85\n\t" \
  "s_mov_b32 s85, 0x301000\n\t" \
  "s_load_dword s84, %[ph], s85\n\t" \
  "s_mov_b32 s85, 0x360000\n\t" \
  "s_load_dword s84, %[ph], s85\n\t" \
  "s_setprio 3\n\t" \
  "s_barrier\n\t" \
  "global_load_dwordx4 v[36:39], v155, %[vb2] offset:0\n\t" \
  "global_load_dwordx4 v[24:27], v155, %[kb5] offset:2048\n\t" \
  "global_load_dwordx4 v[40:43], v155, %[vb2] offset:2048\n\t" \
  "s_setprio 2\n\t" \
  "v_lshlrev_b32 v128, 16, v8\n\t" \
  "v_and_b32 v129, 0xffff0000, v8\n\t" \
  "v_lshlrev_b32 v130, 16, v9\n\t" \
  "v_and_b32 v131, 0xffff0000, v9\n\t" \
  "v_lshlrev_b32 v132, 16, v10\n\t" \
  "v_and_b32 v133, 0xffff0000, v10\n\t" \
  "v_lshlrev_b32 v134, 16, v11\n\t" \
  "v_and_b32 v135, 0xffff0000, v11\n\t" \
  "v_fmac_f32 v96, v128, v65\n\t" \
  "v_fmac_f32 v97, v129, v69\n\t" \
  "v_fmac_f32 v98, v130, v73\n\t" \
  "v_fmac_f32 v99, v131, v77\n\t" \
  "v_fmac_f32 v100, v132, v81\n\t" \
  "v_fmac_f32 v101, v133, v85\n\t" \
  "v_fmac_f32 v102, v134, v89\n\t" \
  "v_fmac_f32 v103, v135, v93\n\t" \
  "v_fmac_f32 v104, v128, v66\n\t" \
  "v_fmac_f32 v105, v129, v70\n\t" \
  "v_fmac_f32 v106, v130, v74\n\t" \
  "v_fmac_f32 v107, v131, v78\n\t" \
  "v_fmac_f32 v108, v132, v82\n\t" \
  "v_fmac_f32 v109, v133, v86\n\t" \
  "v_fmac_f32 v110, v134, v90\n\t" \
  "v_fmac_f32 v111, v135, v94\n\t" \
  "v_mul_f32 v112, v128, v67\n\t" \
  "v_mul_f32 v113, v129, v71\n\t" \
  "v_mul_f32 v114, v130, v75\n\t" \
  "v_mul_f32 v115, v131, v79\n\t" \
  "v_mul_f32 v116, v132, v83\n\t" \
  "v_mul_f32 v117, v133, v87\n\t" \
  "v_mul_f32 v118, v134, v91\n\t" \
  "v_mul_f32 v119, v135, v95\n\t" \
  "s_waitcnt vmcnt(7)\n\t" \
  "v_lshlrev_b32 v128, 16, v12\n\t" \
  "v_and_b32 v129, 0xffff0000, v12\n\t" \
  "v_lshlrev_b32 v130, 16, v13\n\t" \
  "v_and_b32 v131, 0xffff0000, v13\n\t" \
  "v_lshlrev_b32 v132, 16, v14\n\t" \
  "v_and_b32 v133, 0xffff0000, v14\n\t" \
  "v_lshlrev_b32 v134, 16, v15\n\t" \
  "v_and_b32 v135, 0xffff0000, v15\n\t" \
  "v_fmac_f32 v96, v128, v64\n\t" \
  "v_fmac_f32 v97, v129, v68\n\t" \
  "v_fmac_f32 v98, v130, v72\n\t" \
  "v_fmac_f32 v99, v131, v76\n\t" \
  "v_fmac_f32 v100, v132, v80\n\t" \
  "v_fmac_f32 v101, v133, v84\n\t" \
  "v_fmac_f32 v102, v134, v88\n\t" \
  "v_fmac_f32 v103, v135, v92\n\t" \
  "v_fmac_f32 v104, v128, v65\n\t" \
  "v_fmac_f32 v105, v129, v69\n\t" \
  "v_fmac_f32 v106, v130, v73\n\t" \
  "v_fmac_f32 v107, v131, v77\n\t" \
  "v_fmac_f32 v108, v132, v81\n\t" \
  "v_fmac_f32 v109, v133, v85\n\t" \
  "v_fmac_f32 v110, v134, v89\n\t" \
  "v_fmac_f32 v111, v135, v93\n\t" \
  "v_fmac_f32 v112, v128, v66\n\t" \
  "v_fmac_f32 v113, v129, v70\n\t" \
  "v_fmac_f32 v114, v130, v74\n\t" \
  "v_fmac_f32 v115, v131, v78\n\t" \
  "v_fmac_f32 v116, v132, v82\n\t" \
  "v_fmac_f32 v117, v133, v86\n\t" \
  "v_fmac_f32 v118, v134, v90\n\t" \
  "v_fmac_f32 v119, v135, v94\n\t" \
  "v_mul_f32 v120, v128, v67\n\t" \
  "v_mul_f32 v121, v129, v71\n\t" \
  "v_mul_f32 v122, v130, v75\n\t" \
  "v_mul_f32 v123, v131, v79\n\t" \
  "v_mul_f32 v124, v132, v83\n\t" \
  "v_mul_f32 v125, v133, v87\n\t" \
  "v_mul_f32 v126, v134, v91\n\t" \
  "v_mul_f32 v127, v135, v95\n\t" \
  "s_waitcnt vmcnt(6)\n\t" \
  "v_lshlrev_b32 v128, 16, v28\n\t" \
  "v_and_b32 v129, 0xffff0000, v28\n\t" \
  "v_lshlrev_b32 v130, 16, v29\n\t" \
  "v_and_b32 v131, 0xffff0000, v29\n\t" \
  "v_lshlrev_b32 v132, 16, v30\n\t" \
  "v_and_b32 v133, 0xffff0000, v30\n\t" \
  "v_lshlrev_b32 v134, 16, v31\n\t" \
  "v_and_b32 v135, 0xffff0000, v31\n\t" \
  "s_mov_b32 s85, 0x360800\n\t" \
  "s_load_dword s84, %[ph], s85\n\t" \
  "s_mov_b32 s85, 0x361000\n\t" \
  "s_load_dword s84, %[ph], s85\n\t" \
  "s_mov_b32 s85, 0x3c0000\n\t" \
  "s_load_dword s84, %[ph], s85\n\t" \
  "s_mov_b32 s85, 0x3c0800\n\t" \
  "s_load_dword s84, %[ph], s85\n\t" \
  "s_barrier\n\t" \
  "v_mul_f32 v96, v96, v128\n\t" \
  "v_mul_f32 v97, v97, v129\n\t" \
  "v_mul_f32 v98, v98, v130\n\t" \
  "v_mul_f32 v99, v99, v131\n\t" \
  "v_mul_f32 v100, v100, v132\n\t" \
  "v_mul_f32 v101, v101, v133\n\t" \
  "v_mul_f32 v102, v102, v134\n\t" \
  "v_mul_f32 v103, v103, v135\n\t" \
  "v_cvt_pk_bf16_f32 v136, v96, v97\n\t" \
  "v_cvt_pk_bf16_f32 v137, v98, v99\n\t" \
  "v_cvt_pk_bf16_f32 v138, v100, v101\n\t" \
  "v_cvt_pk_bf16_f32 v139, v102, v103\n\t" \
  "v_mov_b32_dpp v128, v136 row_ror:8 row_mask:0xf bank_mask:0xf\n\t" \
  "v_mov_b32_dpp v129, v137 row_ror:8 row_mask:0xf bank_mask:0xf\n\t" \
  "v_mov_b32_dpp v130, v138 row_ror:8 row_mask:0xf bank_mask:0xf\n\t" \
  "v_mov_b32_dpp v131, v139 row_ror:8 row_mask:0xf bank_mask:0xf\n\t" \
  "v_perm_b32 v136, v128, v136, v157\n\t" \
  "v_perm_b32 v137, v129, v137, v157\n\t" \
  "v_perm_b32 v138, v130, v138, v157\n\t" \
  "v_perm_b32 v139, v131, v139, v157\n\t" \
  "v_permlane16_swap_b32 v136, v137\n\t" \
  "s_nop 0\n\t" \
  "v_permlane16_swap_b32 v138, v139\n\t" \
  "s_nop 1\n\t" \
  "v_permlane32_swap_b32 v136, v138\n\t" \
  "v_permlane32_swap_b32 v137, v139\n\t" \
  "s_waitcnt vmcnt(5)\n\t" \
  "v_lshlrev_b32 v128, 16, v16\n\t" \
  "v_and_b32 v129, 0xffff0000, v16\n\t" \
  "v_lshlrev_b32 v130, 16, v17\n\t" \
  "v_and_b32 v131, 0xffff0000, v17\n\t" \
  "v_lshlrev_b32 v132, 16, v18\n\t" \
  "v_and_b32 v133, 0xffff0000, v18\n\t" \
  "v_lshlrev_b32 v134, 16, v19\n\t" \
  "v_and_b32 v135, 0xffff0000, v19\n\t" \
  "v_fmac_f32 v104, v128, v64\n\t" \
  "v_fmac_f32 v105, v129, v68\n\t" \
  "v_fmac_f32 v106, v130, v72\n\t" \
  "v_fmac_f32 v107, v131, v76\n\t" \
  "v_fmac_f32 v108, v132, v80\n\t" \
  "v_fmac_f32 v109, v133, v84\n\t" \
  "v_fmac_f32 v110, v134, v88\n\t" \
  "v_fmac_f32 v111, v135, v92\n\t" \
  "v_fmac_f32 v112, v128, v65\n\t" \
  "v_fmac_f32 v113, v129, v69\n\t" \
  "v_fmac_f32 v114, v130, v73\n\t" \
  "v_fmac_f32 v115, v131, v77\n\t" \
  "v_fmac_f32 v116, v132, v81\n\t" \
  "v_fmac_f32 v117, v133, v85\n\t" \
  "v_fmac_f32 v118, v134, v89\n\t" \
  "v_fmac_f32 v119, v135, v93\n\t" \
  "v_fmac_f32 v120, v128, v66\n\t" \
  "v_fmac_f32 v121, v129, v70\n\t" \
  "v_fmac_f32 v122, v130, v74\n\t" \
  "v_fmac_f32 v123, v131, v78\n\t" \
  "v_fmac_f32 v124, v132, v82\n\t" \
  "v_fmac_f32 v125, v133, v86\n\t" \
  "v_fmac_f32 v126, v134, v90\n\t" \
  "v_fmac_f32 v127, v135, v94\n\t" \
  "s_waitcnt vmcnt(4)\n\t" \
  "v_lshlrev_b32 v128, 16, v32\n\t" \
  "v_and_b32 v129, 0xffff0000, v32\n\t" \
  "v_lshlrev_b32 v130, 16, v33\n\t" \
  "v_and_b32 v131, 0xffff0000, v33\n\t" \
  "v_lshlrev_b32 v132, 16, v34\n\t" \
  "v_and_b32 v133, 0xffff0000, v34\n\t" \
  "v_lshlrev_b32 v134, 16, v35\n\t" \
  "v_and_b32 v135, 0xffff0000, v35\n\t" \
  "v_mul_f32 v104, v104, v128\n\t" \
  "v_mul_f32 v105, v105, v129\n\t" \
  "v_mul_f32 v106, v106, v130\n\t" \
  "v_mul_f32 v107, v107, v131\n\t" \
  "v_mul_f32 v108, v108, v132\n\t" \
  "v_mul_f32 v109, v109, v133\n\t" \
  "v_mul_f32 v110, v110, v134\n\t" \
  "v_mul_f32 v111, v111, v135\n\t" \
  "v_cvt_pk_bf16_f32 v140, v104, v105\n\t" \
  "v_cvt_pk_bf16_f32 v141, v106, v107\n\t" \
  "v_cvt_pk_bf16_f32 v142, v108, v109\n\t" \
  "v_cvt_pk_bf16_f32 v143, v110, v111\n\t" \
  "v_mov_b32_dpp v128, v140 row_ror:8 row_mask:0xf bank_mask:0xf\n\t" \
  "v_mov_b32_dpp v129, v141 row_ror:8 row_mask:0xf bank_mask:0xf\n\t" \
  "s_mov_b32 s85, 0x3c1000\n\t" \
  "s_load_dword s84, %[ph], s85\n\t" \
  "s_mov_b32 s85, 0x420000\n\t" \
  "s_load_dword s84, %[ph], s85\n\t" \
  "s_mov_b32 s85, 0x420800\n\t" \
  "s_load_dword s84, %[ph], s85\n\t" \
  "s_mov_b32 s85, 0x421000\n\t" \
  "s_load_dword s84, %[ph], s85\n\t" \
  "s_setprio 3\n\t" \
  "s_barrier\n\t" \
  "global_store_dwordx4 v159, v[136:139], %[sb] sc1\n\t" \
  "v_add_u32 v158, 0x80, v159\n\t" \
  "v_bfi_b32 v159, %[mk], v158, v159\n\t" \
  "s_setprio 2\n\t" \
  "v_mov_b32_dpp v130, v142 row_ror:8 row_mask:0xf bank_mask:0xf\n\t" \
  "v_mov_b32_dpp v131, v143 row_ror:8 row_mask:0xf bank_mask:0xf\n\t" \
  "v_perm_b32 v140, v128, v140, v157\n\t" \
  "v_perm_b32 v141, v129, v141, v157\n\t" \
  "v_perm_b32 v142, v130, v142, v157\n\t" \
  "v_perm_b32 v143, v131, v143, v157\n\t" \
  "v_permlane16_swap_b32 v140, v141\n\t" \
  "s_nop 0\n\t" \
  "v_permlane16_swap_b32 v142, v143\n\t" \
  "s_nop 1\n\t" \
  "v_permlane32_swap_b32 v140, v142\n\t" \
  "v_permlane32_swap_b32 v141, v143\n\t" \
  "s_waitcnt vmcnt(4)\n\t" \
  "v_lshlrev_b32 v128, 16, v20\n\t" \
  "v_and_b32 v129, 0xffff0000, v20\n\t" \
  "v_lshlrev_b32 v130, 16, v21\n\t" \
  "v_and_b32 v131, 0xffff0000, v21\n\t" \
  "v_lshlrev_b32 v132, 16, v22\n\t" \
  "v_and_b32 v133, 0xffff0000, v22\n\t" \
  "v_lshlrev_b32 v134, 16, v23\n\t" \
  "v_and_b32 v135, 0xffff0000, v23\n\t" \
  "v_fmac_f32 v112, v128, v64\n\t" \
  "v_fmac_f32 v113, v129, v68\n\t" \
  "v_fmac_f32 v114, v130, v72\n\t" \
  "v_fmac_f32 v115, v131, v76\n\t" \
  "v_fmac_f32 v116, v132, v80\n\t" \
  "v_fmac_f32 v117, v133, v84\n\t" \
  "v_fmac_f32 v118, v134, v88\n\t" \
  "v_fmac_f32 v119, v135, v92\n\t" \
  "v_fmac_f32 v120, v128, v65\n\t" \
  "v_fmac_f32 v121, v129, v69\n\t" \
  "v_fmac_f32 v122, v130, v73\n\t" \
  "v_fmac_f32 v123, v131, v77\n\t" \
  "v_fmac_f32 v124, v132, v81\n\t" \
  "v_fmac_f32 v125, v133, v85\n\t" \
  "v_fmac_f32 v126, v134, v89\n\t" \
  "v_fmac_f32 v127, v135, v93\n\t" \
  "s_waitcnt vmcnt(3)\n\t" \
  "v_lshlrev_b32 v128, 16, v36\n\t" \
  "v_and_b32 v129, 0xffff0000, v36\n\t" \
  "v_lshlrev_b32 v130, 16, v37\n\t" \
  "v_and_b32 v131, 0xffff0000, v37\n\t" \
  "v_lshlrev_b32 v132, 16, v38\n\t" \
  "v_and_b32 v133, 0xffff0000, v38\n\t" \
  "v_lshlrev_b32 v134, 16, v39\n\t" \
  "v_and_b32 v135, 0xffff0000, v39\n\t" \
  "v_mul_f32 v112, v112, v128\n\t" \
  "v_mul_f32 v113, v113, v129\n\t" \
  "v_mul_f32 v114, v114, v130\n\t" \
  "v_mul_f32 v115, v115, v131\n\t" \
  "v_mul_f32 v116, v116, v132\n\t" \
  "v_mul_f32 v117, v117, v133\n\t" \
  "v_mul_f32 v118, v118, v134\n\t" \
  "v_mul_f32 v119, v119, v135\n\t" \
  "v_cvt_pk_bf16_f32 v144, v112, v113\n\t" \
  "v_cvt_pk_bf16_f32 v145, v114, v115\n\t" \
  "v_cvt_pk_bf16_f32 v146, v116, v117\n\t" \
  "v_cvt_pk_bf16_f32 v147, v118, v119\n\t" \
  "v_mov_b32_dpp v128, v144 row_ror:8 row_mask:0xf bank_mask:0xf\n\t" \
  "v_mov_b32_dpp v129, v145 row_ror:8 row_mask:0xf bank_mask:0xf\n\t" \
  "v_mov_b32_dpp v130, v146 row_ror:8 row_mask:0xf bank_mask:0xf\n\t" \
  "v_mov_b32_dpp v131, v147 row_ror:8 row_mask:0xf bank_mask:0xf\n\t" \
  "v_perm_b32 v144, v128, v144, v157\n\t" \
  "v_perm_b32 v145, v129, v145, v157\n\t" \
  "v_perm_b32 v146, v130, v146, v157\n\t" \
  "v_perm_b32 v147, v131, v147, v157\n\t" \
  "v_permlane16_swap_b32 v144, v145\n\t" \
  "s_nop 0\n\t" \
  "v_permlane16_swap_b32 v146, v147\n\t" \
  "s_nop 1\n\t" \
  "v_permlane32_swap_b32 v144, v146\n\t" \
  "v_permlane32_swap_b32 v145, v147\n\t" \
  "s_waitcnt vmcnt(2)\n\t" \
  "v_lshlrev_b32 v128, 16, v24\n\t" \
  "v_and_b32 v129, 0xffff0000, v24\n\t" \
  "v_lshlrev_b32 v130, 16, v25\n\t" \
  "v_and_b32 v131, 0xffff0000, v25\n\t" \
  "v_lshlrev_b32 v132, 16, v26\n\t" \
  "v_and_b32 v133, 0xffff0000, v26\n\t" \
  "v_lshlrev_b32 v134, 16, v27\n\t" \
  "v_and_b32 v135, 0xffff0000, v27\n\t" \
  "v_fmac_f32 v120, v128, v64\n\t" \
  "v_fmac_f32 v121, v129, v68\n\t" \
  "s_mov_b32 s85, 0x480000\n\t" \
  "s_load_dword s84, %[ph], s85\n\t" \
  "s_mov_b32 s85, 0x480800\n\t" \
  "s_load_dword s84, %[ph], s85\n\t" \
  "s_mov_b32 s85, 0x481000\n\t" \
  "s_load_dword s84, %[ph], s85\n\t" \
  "s_mov_b32 s85, 0x4e0000\n\t" \
  "s_load_dword s84, %[ph], s85\n\t" \
  "s_setprio 3\n\t" \
  "s_barrier\n\t" \
  "global_store_dwordx4 v159, v[140:143], %[sb] sc1\n\t" \
  "v_add_u32 v158, 0x80, v159\n\t" \
  "v_bfi_b32 v159, %[mk], v158, v159\n\t" \
  "global_store_dwordx4 v159, v[144:147], %[sb] sc1\n\t" \
  "v_add_u32 v158, 0x80, v159\n\t" \
  "v_bfi_b32 v159, %[mk], v158, v159\n\t" \
  "s_mov_b32 m0, %[hd]\n\t" \
  "s_nop 0\n\t" \
  "global_load_lds_dwordx4 v163, %[hp] offset:0\n\t" \
  "global_load_lds_dwordx4 v163, %[hp] offset:1024\n\t" \
  "global_load_lds_dwordx4 v163, %[hp] offset:2048\n\t" \
  "global_load_lds_dwordx4 v163, %[hp] offset:3072\n\t" \
  "s_setprio 2\n\t" \
  "v_fmac_f32 v122, v130, v72\n\t" \
  "v_fmac_f32 v123, v131, v76\n\t" \
  "v_fmac_f32 v124, v132, v80\n\t" \
  "v_fmac_f32 v125, v133, v84\n\t" \
  "v_fmac_f32 v126, v134, v88\n\t" \
  "v_fmac_f32 v127, v135, v92\n\t" \
  "s_waitcnt vmcnt(7)\n\t" \
  "v_lshlrev_b32 v128, 16, v40\n\t" \
  "v_and_b32 v129, 0xffff0000, v40\n\t" \
  "v_lshlrev_b32 v130, 16, v41\n\t" \
  "v_and_b32 v131, 0xffff0000, v41\n\t" \
  "v_lshlrev_b32 v132, 16, v42\n\t" \
  "v_and_b32 v133, 0xffff0000, v42\n\t" \
  "v_lshlrev_b32 v134, 16, v43\n\t" \
  "v_and_b32 v135, 0xffff0000, v43\n\t" \
  "v_mul_f32 v120, v120, v128\n\t" \
  "v_mul_f32 v121, v121, v129\n\t" \
  "v_mul_f32 v122, v122, v130\n\t" \
  "v_mul_f32 v123, v123, v131\n\t" \
  "v_mul_f32 v124, v124, v132\n\t" \
  "v_mul_f32 v125, v125, v133\n\t" \
  "v_mul_f32 v126, v126, v134\n\t" \
  "v_mul_f32 v127, v127, v135\n\t" \
  "v_cvt_pk_bf16_f32 v148, v120, v121\n\t" \
  "v_cvt_pk_bf16_f32 v149, v122, v123\n\t" \
  "v_cvt_pk_bf16_f32 v150, v124, v125\n\t" \
  "v_cvt_pk_bf16_f32 v151, v126, v127\n\t" \
  "v_mov_b32_dpp v128, v148 row_ror:8 row_mask:0xf bank_mask:0xf\n\t" \
  "v_mov_b32_dpp v129, v149 row_ror:8 row_mask:0xf bank_mask:0xf\n\t" \
  "v_mov_b32_dpp v130, v150 row_ror:8 row_mask:0xf bank_mask:0xf\n\t" \
  "v_mov_b32_dpp v131, v151 row_ror:8 row_mask:0xf bank_mask:0xf\n\t" \
  "v_perm_b32 v148, v128, v148, v157\n\t" \
  "v_perm_b32 v149, v129, v149, v157\n\t" \
  "v_perm_b32 v150, v130, v150, v157\n\t" \
  "v_perm_b32 v151, v131, v151, v157\n\t" \
  "v_permlane16_swap_b32 v148, v149\n\t" \
  "s_nop 0\n\t" \
  "v_permlane16_swap_b32 v150, v151\n\t" \
  "s_nop 1\n\t" \
  "v_permlane32_swap_b32 v148, v150\n\t" \
  "v_permlane32_swap_b32 v149, v151\n\t" \
  "s_mov_b32 s85, 0x4e0800\n\t" \
  "s_load_dword s84, %[ph], s85\n\t" \
  "s_mov_b32 s85, 0x4e1000\n\t" \
  "s_load_dword s84, %[ph], s85\n\t" \
  "s_mov_b32 s85, 0x540000\n\t" \
  "s_load_dword s84, %[ph], s85\n\t" \
  "s_mov_b32 s85, 0x540800\n\t" \
  "s_load_dword s84, %[ph], s85\n\t" \
  "s_setprio 3\n\t" \
  "s_barrier\n\t" \
  "global_store_dwordx4 v159, v[148:151], %[sb] sc1\n\t" \
  "s_setprio 2\n\t" \
  "s_waitcnt vmcnt(1)\n\t" \
  "ds_read_b64_tr_b16 v[64:65], v165 offset:32\n\t" \
  "ds_read_b64_tr_b16 v[66:67], v165 offset:0\n\t" \
  "ds_read_b64_tr_b16 v[68:69], v165 offset:288\n\t" \
  "ds_read_b64_tr_b16 v[70:71], v165 offset:256\n\t" \
  "ds_read_b64_tr_b16 v[72:73], v165 offset:544\n\t" \
  "ds_read_b64_tr_b16 v[74:75], v165 offset:512\n\t" \
  "ds_read_b64_tr_b16 v[76:77], v165 offset:800\n\t" \
  "ds_read_b64_tr_b16 v[78:79], v165 offset:768\n\t" \
  "ds_read_b64_tr_b16 v[80:81], v165 offset:1056\n\t" \
  "ds_read_b64_tr_b16 v[82:83], v165 offset:1024\n\t" \
  "ds_read_b64_tr_b16 v[84:85], v165 offset:1312\n\t" \
  "ds_read_b64_tr_b16 v[86:87], v165 offset:1280\n\t" \
  "ds_read_b64_tr_b16 v[88:89], v165 offset:1568\n\t" \
  "ds_read_b64_tr_b16 v[90:91], v165 offset:1536\n\t" \
  "ds_read_b64_tr_b16 v[92:93], v165 offset:1824\n\t" \
  "ds_read_b64_tr_b16 v[94:95], v165 offset:1792\n\t" \
  "v_mov_b32 v24, v166\n\t" \
  "v_mov_b32 v25, v167\n\t" \
  "v_mov_b32 v63, 0xc0\n\t" \
  "v_add_u32 v158, 64, v24\n\t" \
  "v_bfi_b32 v26, v63, v158, v24\n\t" \
  "v_add_u32 v158, 64, v25\n\t" \
  "v_bfi_b32 v27, v63, v158, v25\n\t" \
  "v_add_u32 v158, 64, v26\n\t" \
  "v_bfi_b32 v28, v63, v158, v26\n\t" \
  "v_add_u32 v158, 64, v27\n\t" \
  "v_bfi_b32 v29, v63, v158, v27\n\t" \
  "v_add_u32 v158, 64, v28\n\t" \
  "v_bfi_b32 v30, v63, v158, v28\n\t" \
  "v_add_u32 v158, 64, v29\n\t" \
  "v_bfi_b32 v31, v63, v158, v29\n\t" \
  "s_mov_b32 s85, 0x541000\n\t" \
  "s_load_dword s84, %[ph], s85\n\t" \
  "s_mov_b32 s85, 0x5a0000\n\t" \
  "s_load_dword s84, %[ph], s85\n\t" \
  "s_mov_b32 s85, 0x5a0800\n\t" \
  "s_load_dword s84, %[ph], s85\n\t" \
  "s_mov_b32 s85, 0x5a1000\n\t" \
  "s_load_dword s84, %[ph], s85\n\t" \
  "s_setprio 3\n\t" \
  "s_barrier\n\t" \
  "s_waitcnt vmcnt(0)\n\t" \
  "v_add_u32 v61, 1, v160\n\t" \
  "s_mov_b64 exec, 1\n\t" \
  "global_store_dword v60, v61, %[ff] sc1\n\t" \
  "s_mov_b64 exec, -1\n\t" \
  "s_setprio 2\n\t" \
  "ds_read_b64_tr_b16 v[96:97], v165 offset:2080\n\t" \
  "ds_read_b64_tr_b16 v[98:99], v165 offset:2048\n\t" \
  "ds_read_b64_tr_b16 v[100:101], v165 offset:2336\n\t" \
  "ds_read_b64_tr_b16 v[102:103], v165 offset:2304\n\t" \
  "ds_read_b64_tr_b16 v[104:105], v165 offset:2592\n\t" \
  "ds_read_b64_tr_b16 v[106:107], v165 offset:2560\n\t" \
  "ds_read_b64_tr_b16 v[108:109], v165 offset:2848\n\t" \
  "ds_read_b64_tr_b16 v[110:111], v165 offset:2816\n\t" \
  "ds_read_b64_tr_b16 v[112:113], v165 offset:3104\n\t" \
  "ds_read_b64_tr_b16 v[114:115], v165 offset:3072\n\t" \
  "ds_read_b64_tr_b16 v[116:117], v165 offset:3360\n\t" \
  "ds_read_b64_tr_b16 v[118:119], v165 offset:3328\n\t" \
  "ds_read_b64_tr_b16 v[120:121], v165 offset:3616\n\t" \
  "ds_read_b64_tr_b16 v[122:123], v165 offset:3584\n\t" \
  "ds_read_b64_tr_b16 v[124:125], v165 offset:3872\n\t" \
  "ds_read_b64_tr_b16 v[126:127], v165 offset:3840\n\t" \
  "s_barrier\n\t" \
  "s_waitcnt lgkmcnt(0)\n\t" \
  "v_lshlrev_b32 v61, 16, v161\n\t" \
  "v_lshlrev_b32 v62, 16, v162\n\t" \
  "v_add_f32 v61, v61, v62\n\t" \
  "v_mov_b32 v62, 0xffff\n\t" \
  "v_cvt_pk_bf16_f32 v61, v61, v61\n\t" \
  "s_mov_b64 exec, 1\n\t" \
  "v_bfi_b32 v66, v62, v61, v66\n\t" \
  "s_mov_b64 exec, 15\n\t" \
  "v_and_b32 v66, v62, v66\n\t" \
  "s_mov_b64 exec, 16\n\t" \
  "v_bfi_b32 v66, v62, v66, v61\n\t" \
  "s_mov_b64 exec, 0xff\n\t" \
  "v_mov_b32 v67, 0\n\t" \
  "s_mov_b64 exec, 0x100\n\t" \
  "v_mov_b32 v67, v61\n\t" \
  "s_mov_b64 exec, 0xf00\n\t" \
  "v_and_b32 v67, v62, v67\n\t" \
  "s_mov_b64 exec, 0x1000\n\t" \
  "v_bfi_b32 v67, v62, v67, v61\n\t" \
  "s_mov_b64 exec, -1\n\t" \
  "s_waitcnt lgkmcnt(0)\n\t"
#define PP_NB 8
#define PP_Q 0
#define PP_CLOB "v0", "v1", "v2", "v3", "v4", "v5", "v6", "v7", "v8", "v9", "v10", "v11", "v12", "v13", "v14", "v15", "v16", "v17", "v18", "v19", "v20", "v21", "v22", "v23", "v32", "v33", "v34", "v35", "v36", "v37", "v38", "v39", "v40", "v41", "v42", "v43", "v60", "v61", "v62", "v63", "v128", "v129", "v130", "v131", "v132", "v133", "v134", "v135", "v136", "v137", "v138", "v139", "v140", "v141", "v142", "v143", "v144", "v145", "v146", "v147", "v148", "v149", "v150", "v151", "v158", "v161", "v162", "s84", "s85"

#define LD_ASM1 \
  "s_add_u32 m0, s39, 49152\n\t" \
  "s_mov_b32 s68, 0x0\n\t" \
  "buffer_load_dwordx4 v5, s[44:47], s68 offen offset:0 lds\n\t" \
  "buffer_load_dwordx4 v6, s[44:47], s68 offen offset:1024 lds\n\t" \
  "buffer_load_dwordx4 v7, s[44:47], s68 offen offset:2048 lds\n\t" \
  "s_add_u32 m0, s38, 36864\n\t" \
  "s_mov_b32 s68, 0x0\n\t" \
  "buffer_load_dwordx4 v0, s[40:43], s68 offen offset:0 lds\n\t" \
  "buffer_load_dwordx4 v1, s[40:43], s68 offen offset:1024 lds\n\t" \
  "s_mov_b64 exec, 0xffff\n\t" \
  "buffer_load_dwordx4 v4, s[40:43], s68 offen offset:2048 lds\n\t" \
  "s_mov_b64 exec, -1\n\t" \
  "s_add_u32 m0, s38, 46080\n\t" \
  "s_mov_b32 s68, 0x24000\n\t" \
  "buffer_load_dwordx4 v2, s[40:43], s68 offen offset:0 lds\n\t" \
  "buffer_load_dwordx4 v3, s[40:43], s68 offen offset:1024 lds\n\t" \
  "s_mov_b64 exec, 0xffff\n\t" \
  "buffer_load_dwordx4 v4, s[40:43], s68 offen offset:2048 lds\n\t" \
  "s_mov_b64 exec, -1\n\t" \
  "s_add_u32 m0, s39, 61440\n\t" \
  "s_mov_b32 s68, 0x30000\n\t" \
  "buffer_load_dwordx4 v5, s[44:47], s68 offen offset:0 lds\n\t" \
  "buffer_load_dwordx4 v6, s[44:47], s68 offen offset:1024 lds\n\t" \
  "buffer_load_dwordx4 v7, s[44:47], s68 offen offset:2048 lds\n\t" \
  "s_mov_b32 m0, %[sk]\n\t" \
  "s_mov_b32 exec_hi, 0\n\t" \
  "buffer_load_dword v9, s[40:43], 0 offen lds\n\t" \
  "s_mov_b32 exec_lo, %[wm]\n\t" \
  "buffer_load_dword v10, s[44:47], 0 offen lds\n\t" \
  "s_mov_b64 exec, -1\n\t"
#define LD_ASM2 \
  "s_add_u32 m0, s38, 0\n\t" \
  "s_mov_b32 s68, 0x80\n\t" \
  "buffer_load_dwordx4 v0, s[40:43], s68 offen offset:0 lds\n\t" \
  "s_mov_b32 s85, 0x180000\n\t" \
  "s_load_dword s84, %[pw], s85\n\t" \
  "buffer_load_dwordx4 v1, s[40:43], s68 offen offset:1024 lds\n\t" \
  "s_mov_b32 s85, 0x180800\n\t" \
  "s_load_dword s84, %[pw], s85\n\t" \
  "s_mov_b64 exec, 0xffff\n\t" \
  "buffer_load_dwordx4 v4, s[40:43], s68 offen offset:2048 lds\n\t" \
  "s_mov_b64 exec, -1\n\t" \
  "s_add_u32 m0, s38, 9216\n\t" \
  "s_mov_b32 s68, 0x24080\n\t" \
  "buffer_load_dwordx4 v2, s[40:43], s68 offen offset:0 lds\n\t" \
  "s_mov_b32 s85, 0x181000\n\t" \
  "s_load_dword s84, %[pw], s85\n\t" \
  "buffer_load_dwordx4 v3, s[40:43], s68 offen offset:1024 lds\n\t" \
  "s_mov_b32 s85, 0x1b0000\n\t" \
  "s_load_dword s84, %[pw], s85\n\t" \
  "s_mov_b64 exec, 0xffff\n\t" \
  "buffer_load_dwordx4 v4, s[40:43], s68 offen offset:2048 lds\n\t" \
  "s_mov_b64 exec, -1\n\t" \
  "s_mov_b32 s85, 0x1b0800\n\t" \
  "s_load_dword s84, %[pw], s85\n\t" \
  "s_mov_b32 s85, 0x1b1000\n\t" \
  "s_load_dword s84, %[pw], s85\n\t" \
  "s_mov_b32 s85, 0x1e0000\n\t" \
  "s_load_dword s84, %[pw], s85\n\t" \
  "s_mov_b32 s85, 0x1e0800\n\t" \
  "s_load_dword s84, %[pw], s85\n\t" \
  "s_mov_b32 m0, %[tsk]\n\t" \
  "s_mov_b32 s85, 0x0\n\t" \
  "s_mov_b64 exec, %[hm]\n\t" \
  "buffer_load_dword v16, %[sh], s85 offen lds\n\t" \
  "s_mov_b64 exec, -1\n\t" \
  "s_mov_b32 s68, 0x60000\n\t" \
  "s_mov_b32 s69, 0x90000\n\t" \
  "s_mov_b32 s70, 0x100\n\t" \
  "s_mov_b32 s71, 0x24100\n\t" \
  "s_mov_b32 s72, 0xc0000\n\t" \
  "s_mov_b32 s73, 0xf0000\n\t" \
  "s_add_u32 m0, s39, 0\n\t" \
  "s_waitcnt vmcnt(9)\n\t" \
  "s_barrier\n\t" \
  "buffer_load_dwordx4 v5, s[44:47], s68 offen offset:0 lds\n\t" \
  "s_mov_b32 s85, 0x1e1000\n\t" \
  "s_load_dword s84, %[pw], s85\n\t" \
  "buffer_load_dwordx4 v6, s[44:47], s68 offen offset:1024 lds\n\t" \
  "s_mov_b32 s85, 0x210000\n\t" \
  "s_load_dword s84, %[pw], s85\n\t" \
  "buffer_load_dwordx4 v7, s[44:47], s68 offen offset:2048 lds\n\t" \
  "s_mov_b32 s85, 0x210800\n\t" \
  "s_load_dword s84, %[pw], s85\n\t" \
  "s_add_u32 m0, s39, 12288\n\t" \
  "s_nop 0\n\t" \
  "buffer_load_dwordx4 v5, s[44:47], s69 offen offset:0 lds\n\t" \
  "s_mov_b32 s85, 0x211000\n\t" \
  "s_load_dword s84, %[pw], s85\n\t" \
  "buffer_load_dwordx4 v6, s[44:47], s69 offen offset:1024 lds\n\t" \
  "s_mov_b32 s85, 0x240000\n\t" \
  "s_load_dword s84, %[pw], s85\n\t" \
  "buffer_load_dwordx4 v7, s[44:47], s69 offen offset:2048 lds\n\t" \
  "s_mov_b32 s85, 0x240800\n\t" \
  "s_load_dword s84, %[pw], s85\n\t" \
  "s_add_u32 m0, s38, 18432\n\t" \
  "s_nop 0\n\t" \
  "buffer_load_dwordx4 v0, s[40:43], s70 offen offset:0 lds\n\t" \
  "buffer_load_dwordx4 v1, s[40:43], s70 offen offset:1024 lds\n\t" \
  "s_mov_b64 exec, 0xffff\n\t" \
  "buffer_load_dwordx4 v4, s[40:43], s70 offen offset:2048 lds\n\t" \
  "s_add_u32 m0, s38, 27648\n\t" \
  "s_mov_b64 exec, -1\n\t" \
  "buffer_load_dwordx4 v2, s[40:43], s71 offen offset:0 lds\n\t" \
  "buffer_load_dwordx4 v3, s[40:43], s71 offen offset:1024 lds\n\t" \
  "s_mov_b64 exec, 0xffff\n\t" \
  "buffer_load_dwordx4 v4, s[40:43], s71 offen offset:2048 lds\n\t" \
  "s_add_u32 m0, s39, 24576\n\t" \
  "s_mov_b64 exec, -1\n\t" \
  "buffer_load_dwordx4 v5, s[44:47], s72 offen offset:0 lds\n\t" \
  "buffer_load_dwordx4 v6, s[44:47], s72 offen offset:1024 lds\n\t" \
  "buffer_load_dwordx4 v7, s[44:47], s72 offen offset:2048 lds\n\t" \
  "s_add_u32 m0, s39, 36864\n\t" \
  "s_nop 0\n\t" \
  "buffer_load_dwordx4 v5, s[44:47], s73 offen offset:0 lds\n\t" \
  "buffer_load_dwordx4 v6, s[44:47], s73 offen offset:1024 lds\n\t" \
  "buffer_load_dwordx4 v7, s[44:47], s73 offen offset:2048 lds\n\t" \
  "s_mov_b32 m0, %[tsk]\n\t" \
  "s_mov_b32 s85, 0x180000\n\t" \
  "s_mov_b64 exec, %[hm]\n\t" \
  "buffer_load_dword v16, %[sh], s85 offen lds\n\t" \
  "s_mov_b64 exec, -1\n\t"
#define LD_ASM3 \
  "s_mov_b32 s68, 0x180\n\t" \
  "s_mov_b32 s69, 0x120000\n\t" \
  "s_mov_b32 s70, 0x24180\n\t" \
  "s_mov_b32 s71, 0x150000\n\t" \
  "s_add_u32 m0, s38, 36864\n\t" \
  "s_waitcnt vmcnt(13)\n\t" \
  "s_barrier\n\t" \
  "buffer_load_dwordx4 v0, s[40:43], s68 offen offset:0 lds\n\t" \
  "s_mov_b32 s85, 0x241000\n\t" \
  "s_load_dword s84, %[pw], s85\n\t" \
  "buffer_load_dwordx4 v1, s[40:43], s68 offen offset:1024 lds\n\t" \
  "s_mov_b32 s85, 0x270000\n\t" \
  "s_load_dword s84, %[pw], s85\n\t" \
  "s_mov_b64 exec, 0xffff\n\t" \
  "buffer_load_dwordx4 v4, s[40:43], s68 offen offset:2048 lds\n\t" \
  "s_add_u32 m0, s39, 49152\n\t" \
  "s_mov_b64 exec, -1\n\t" \
  "buffer_load_dwordx4 v5, s[44:47], s69 offen offset:0 lds\n\t" \
  "s_mov_b32 s85, 0x270800\n\t" \
  "s_load_dword s84, %[pw], s85\n\t" \
  "buffer_load_dwordx4 v6, s[44:47], s69 offen offset:1024 lds\n\t" \
  "s_mov_b32 s85, 0x271000\n\t" \
  "s_load_dword s84, %[pw], s85\n\t" \
  "buffer_load_dwordx4 v7, s[44:47], s69 offen offset:2048 lds\n\t" \
  "s_mov_b32 s85, 0x2a0000\n\t" \
  "s_load_dword s84, %[pw], s85\n\t" \
  "s_add_u32 m0, s38, 46080\n\t" \
  "s_nop 0\n\t" \
  "buffer_load_dwordx4 v2, s[40:43], s70 offen offset:0 lds\n\t" \
  "s_mov_b32 s85, 0x2a0800\n\t" \
  "s_load_dword s84, %[pw], s85\n\t" \
  "buffer_load_dwordx4 v3, s[40:43], s70 offen offset:1024 lds\n\t" \
  "s_mov_b64 exec, 0xffff\n\t" \
  "buffer_load_dwordx4 v4, s[40:43], s70 offen offset:2048 lds\n\t" \
  "s_add_u32 m0, s39, 61440\n\t" \
  "s_mov_b64 exec, -1\n\t" \
  "buffer_load_dwordx4 v5, s[44:47], s71 offen offset:0 lds\n\t" \
  "buffer_load_dwordx4 v6, s[44:47], s71 offen offset:1024 lds\n\t" \
  "buffer_load_dwordx4 v7, s[44:47], s71 offen offset:2048 lds\n\t" \
  "s_mov_b32 s68, 0x200\n\t" \
  "s_mov_b32 s69, 0x180000\n\t" \
  "s_mov_b32 s70, 0x24200\n\t" \
  "s_mov_b32 s71, 0x1b0000\n\t" \
  "s_add_u32 m0, s38, 0\n\t" \
  "s_waitcnt vmcnt(13)\n\t" \
  "s_barrier\n\t" \
  "buffer_load_dwordx4 v0, s[40:43], s68 offen offset:0 lds\n\t" \
  "s_mov_b32 s85, 0x2a1000\n\t" \
  "s_load_dword s84, %[pw], s85\n\t" \
  "buffer_load_dwordx4 v1, s[40:43], s68 offen offset:1024 lds\n\t" \
  "s_mov_b32 s85, 0x2d0000\n\t" \
  "s_load_dword s84, %[pw], s85\n\t" \
  "s_mov_b64 exec, 0xffff\n\t" \
  "buffer_load_dwordx4 v4, s[40:43], s68 offen offset:2048 lds\n\t" \
  "s_add_u32 m0, s39, 0\n\t" \
  "s_mov_b64 exec, -1\n\t" \
  "buffer_load_dwordx4 v5, s[44:47], s69 offen offset:0 lds\n\t" \
  "s_mov_b32 s85, 0x2d0800\n\t" \
  "s_load_dword s84, %[pw], s85\n\t" \
  "buffer_load_dwordx4 v6, s[44:47], s69 offen offset:1024 lds\n\t" \
  "s_mov_b32 s85, 0x2d1000\n\t" \
  "s_load_dword s84, %[pw], s85\n\t" \
  "buffer_load_dwordx4 v7, s[44:47], s69 offen offset:2048 lds\n\t" \
  "s_mov_b32 s85, 0x300000\n\t" \
  "s_load_dword s84, %[pw], s85\n\t" \
  "s_add_u32 m0, s38, 9216\n\t" \
  "s_nop 0\n\t" \
  "buffer_load_dwordx4 v2, s[40:43], s70 offen offset:0 lds\n\t" \
  "s_mov_b32 s85, 0x300800\n\t" \
  "s_load_dword s84, %[pw], s85\n\t" \
  "buffer_load_dwordx4 v3, s[40:43], s70 offen offset:1024 lds\n\t" \
  "s_mov_b64 exec, 0xffff\n\t" \
  "buffer_load_dwordx4 v4, s[40:43], s70 offen offset:2048 lds\n\t" \
  "s_add_u32 m0, s39, 12288\n\t" \
  "s_mov_b64 exec, -1\n\t" \
  "buffer_load_dwordx4 v5, s[44:47], s71 offen offset:0 lds\n\t" \
  "buffer_load_dwordx4 v6, s[44:47], s71 offen offset:1024 lds\n\t" \
  "buffer_load_dwordx4 v7, s[44:47], s71 offen offset:2048 lds\n\t" \
  "s_mov_b32 s68, 0x280\n\t" \
  "s_mov_b32 s69, 0x1e0000\n\t" \
  "s_mov_b32 s70, 0x24280\n\t" \
  "s_mov_b32 s71, 0x210000\n\t" \
  "s_add_u32 m0, s38, 18432\n\t" \
  "s_waitcnt vmcnt(12)\n\t" \
  "s_barrier\n\t" \
  "buffer_load_dwordx4 v0, s[40:43], s68 offen offset:0 lds\n\t" \
  "s_mov_b32 s85, 0x301000\n\t" \
  "s_load_dword s84, %[pw], s85\n\t" \
  "buffer_load_dwordx4 v1, s[40:43], s68 offen offset:1024 lds\n\t" \
  "s_mov_b32 s85, 0x330000\n\t" \
  "s_load_dword s84, %[pw], s85\n\t" \
  "s_mov_b64 exec, 0xffff\n\t" \
  "buffer_load_dwordx4 v4, s[40:43], s68 offen offset:2048 lds\n\t" \
  "s_add_u32 m0, s39, 24576\n\t" \
  "s_mov_b64 exec, -1\n\t" \
  "buffer_load_dwordx4 v5, s[44:47], s69 offen offset:0 lds\n\t" \
  "s_mov_b32 s85, 0x330800\n\t" \
  "s_load_dword s84, %[pw], s85\n\t" \
  "buffer_load_dwordx4 v6, s[44:47], s69 offen offset:1024 lds\n\t" \
  "s_mov_b32 s85, 0x331000\n\t" \
  "s_load_dword s84, %[pw], s85\n\t" \
  "buffer_load_dwordx4 v7, s[44:47], s69 offen offset:2048 lds\n\t" \
  "s_mov_b32 s85, 0x360000\n\t" \
  "s_load_dword s84, %[pw], s85\n\t" \
  "s_add_u32 m0, s38, 27648\n\t" \
  "s_nop 0\n\t" \
  "buffer_load_dwordx4 v2, s[40:43], s70 offen offset:0 lds\n\t" \
  "s_mov_b32 s85, 0x360800\n\t" \
  "s_load_dword s84, %[pw], s85\n\t" \
  "buffer_load_dwordx4 v3, s[40:43], s70 offen offset:1024 lds\n\t" \
  "s_mov_b64 exec, 0xffff\n\t" \
  "buffer_load_dwordx4 v4, s[40:43], s70 offen offset:2048 lds\n\t" \
  "s_add_u32 m0, s39, 36864\n\t" \
  "s_mov_b64 exec, -1\n\t" \
  "buffer_load_dwordx4 v5, s[44:47], s71 offen offset:0 lds\n\t" \
  "buffer_load_dwordx4 v6, s[44:47], s71 offen offset:1024 lds\n\t" \
  "buffer_load_dwordx4 v7, s[44:47], s71 offen offset:2048 lds\n\t" \
  "s_mov_b32 s68, 0x300\n\t" \
  "s_mov_b32 s69, 0x240000\n\t" \
  "s_mov_b32 s70, 0x24300\n\t" \
  "s_mov_b32 s71, 0x270000\n\t" \
  "s_add_u32 m0, s38, 36864\n\t" \
  "s_waitcnt vmcnt(12)\n\t" \
  "s_barrier\n\t" \
  "buffer_load_dwordx4 v0, s[40:43], s68 offen offset:0 lds\n\t" \
  "s_mov_b32 s85, 0x361000\n\t" \
  "s_load_dword s84, %[pw], s85\n\t" \
  "buffer_load_dwordx4 v1, s[40:43], s68 offen offset:1024 lds\n\t" \
  "s_mov_b32 s85, 0x390000\n\t" \
  "s_load_dword s84, %[pw], s85\n\t" \
  "s_mov_b64 exec, 0xffff\n\t" \
  "buffer_load_dwordx4 v4, s[40:43], s68 offen offset:2048 lds\n\t" \
  "s_add_u32 m0, s39, 49152\n\t" \
  "s_mov_b64 exec, -1\n\t" \
  "buffer_load_dwordx4 v5, s[44:47], s69 offen offset:0 lds\n\t" \
  "s_mov_b32 s85, 0x390800\n\t" \
  "s_load_dword s84, %[pw], s85\n\t" \
  "buffer_load_dwordx4 v6, s[44:47], s69 offen offset:1024 lds\n\t" \
  "s_mov_b32 s85, 0x391000\n\t" \
  "s_load_dword s84, %[pw], s85\n\t" \
  "buffer_load_dwordx4 v7, s[44:47], s69 offen offset:2048 lds\n\t" \
  "s_mov_b32 s85, 0x3c0000\n\t" \
  "s_load_dword s84, %[pw], s85\n\t" \
  "s_add_u32 m0, s38, 46080\n\t" \
  "s_nop 0\n\t" \
  "buffer_load_dwordx4 v2, s[40:43], s70 offen offset:0 lds\n\t" \
  "s_mov_b32 s85, 0x3c0800\n\t" \
  "s_load_dword s84, %[pw], s85\n\t" \
  "buffer_load_dwordx4 v3, s[40:43], s70 offen offset:1024 lds\n\t" \
  "s_mov_b64 exec, 0xffff\n\t" \
  "buffer_load_dwordx4 v4, s[40:43], s70 offen offset:2048 lds\n\t" \
  "s_add_u32 m0, s39, 61440\n\t" \
  "s_mov_b64 exec, -1\n\t" \
  "buffer_load_dwordx4 v5, s[44:47], s71 offen offset:0 lds\n\t" \
  "buffer_load_dwordx4 v6, s[44:47], s71 offen offset:1024 lds\n\t" \
  "buffer_load_dwordx4 v7, s[44:47], s71 offen offset:2048 lds\n\t" \
  "s_mov_b32 s68, 0x380\n\t" \
  "s_mov_b32 s69, 0x2a0000\n\t" \
  "s_mov_b32 s70, 0x24380\n\t" \
  "s_mov_b32 s71, 0x2d0000\n\t" \
  "s_add_u32 m0, s38, 0\n\t" \
  "s_waitcnt vmcnt(12)\n\t" \
  "s_barrier\n\t" \
  "buffer_load_dwordx4 v0, s[40:43], s68 offen offset:0 lds\n\t" \
  "s_mov_b32 s85, 0x3c1000\n\t" \
  "s_load_dword s84, %[pw], s85\n\t" \
  "buffer_load_dwordx4 v1, s[40:43], s68 offen offset:1024 lds\n\t" \
  "s_mov_b32 s85, 0x3f0000\n\t" \
  "s_load_dword s84, %[pw], s85\n\t" \
  "s_mov_b64 exec, 0xffff\n\t" \
  "buffer_load_dwordx4 v4, s[40:43], s68 offen offset:2048 lds\n\t" \
  "s_add_u32 m0, s39, 0\n\t" \
  "s_mov_b64 exec, -1\n\t" \
  "buffer_load_dwordx4 v5, s[44:47], s69 offen offset:0 lds\n\t" \
  "s_mov_b32 s85, 0x3f0800\n\t" \
  "s_load_dword s84, %[pw], s85\n\t" \
  "buffer_load_dwordx4 v6, s[44:47], s69 offen offset:1024 lds\n\t" \
  "s_mov_b32 s85, 0x3f1000\n\t" \
  "s_load_dword s84, %[pw], s85\n\t" \
  "buffer_load_dwordx4 v7, s[44:47], s69 offen offset:2048 lds\n\t" \
  "s_mov_b32 s85, 0x420000\n\t" \
  "s_load_dword s84, %[pw], s85\n\t" \
  "s_add_u32 m0, s38, 9216\n\t" \
  "s_nop 0\n\t" \
  "buffer_load_dwordx4 v2, s[40:43], s70 offen offset:0 lds\n\t" \
  "s_mov_b32 s85, 0x420800\n\t" \
  "s_load_dword s84, %[pw], s85\n\t" \
  "buffer_load_dwordx4 v3, s[40:43], s70 offen offset:1024 lds\n\t" \
  "s_mov_b64 exec, 0xffff\n\t" \
  "buffer_load_dwordx4 v4, s[40:43], s70 offen offset:2048 lds\n\t" \
  "s_add_u32 m0, s39, 12288\n\t" \
  "s_mov_b64 exec, -1\n\t" \
  "buffer_load_dwordx4 v5, s[44:47], s71 offen offset:0 lds\n\t" \
  "buffer_load_dwordx4 v6, s[44:47], s71 offen offset:1024 lds\n\t" \
  "buffer_load_dwordx4 v7, s[44:47], s71 offen offset:2048 lds\n\t" \
  "s_mov_b32 s68, 0x400\n\t" \
  "s_mov_b32 s69, 0x300000\n\t" \
  "s_mov_b32 s70, 0x24400\n\t" \
  "s_mov_b32 s71, 0x330000\n\t" \
  "s_add_u32 m0, s38, 18432\n\t" \
  "s_waitcnt vmcnt(12)\n\t" \
  "s_barrier\n\t" \
  "buffer_load_dwordx4 v0, s[40:43], s68 offen offset:0 lds\n\t" \
  "s_mov_b32 s85, 0x421000\n\t" \
  "s_load_dword s84, %[pw], s85\n\t" \
  "buffer_load_dwordx4 v1, s[40:43], s68 offen offset:1024 lds\n\t" \
  "s_mov_b32 s85, 0x450000\n\t" \
  "s_load_dword s84, %[pw], s85\n\t" \
  "s_mov_b64 exec, 0xffff\n\t" \
  "buffer_load_dwordx4 v4, s[40:43], s68 offen offset:2048 lds\n\t" \
  "s_add_u32 m0, s39, 24576\n\t" \
  "s_mov_b64 exec, -1\n\t" \
  "buffer_load_dwordx4 v5, s[44:47], s69 offen offset:0 lds\n\t" \
  "s_mov_b32 s85, 0x450800\n\t" \
  "s_load_dword s84, %[pw], s85\n\t" \
  "buffer_load_dwordx4 v6, s[44:47], s69 offen offset:1024 lds\n\t" \
  "s_mov_b32 s85, 0x451000\n\t" \
  "s_load_dword s84, %[pw], s85\n\t" \
  "buffer_load_dwordx4 v7, s[44:47], s69 offen offset:2048 lds\n\t" \
  "s_mov_b32 s85, 0x480000\n\t" \
  "s_load_dword s84, %[pw], s85\n\t" \
  "s_add_u32 m0, s38, 27648\n\t" \
  "s_nop 0\n\t" \
  "buffer_load_dwordx4 v2, s[40:43], s70 offen offset:0 lds\n\t" \
  "s_mov_b32 s85, 0x480800\n\t" \
  "s_load_dword s84, %[pw], s85\n\t" \
  "buffer_load_dwordx4 v3, s[40:43], s70 offen offset:1024 lds\n\t" \
  "s_mov_b64 exec, 0xffff\n\t" \
  "buffer_load_dwordx4 v4, s[40:43], s70 offen offset:2048 lds\n\t" \
  "s_add_u32 m0, s39, 36864\n\t" \
  "s_mov_b64 exec, -1\n\t" \
  "buffer_load_dwordx4 v5, s[44:47], s71 offen offset:0 lds\n\t" \
  "buffer_load_dwordx4 v6, s[44:47], s71 offen offset:1024 lds\n\t" \
  "buffer_load_dwordx4 v7, s[44:47], s71 offen offset:2048 lds\n\t" \
  "s_mov_b32 s68, 0x480\n\t" \
  "s_mov_b32 s69, 0x360000\n\t" \
  "s_mov_b32 s70, 0x24480\n\t" \
  "s_mov_b32 s71, 0x390000\n\t" \
  "s_add_u32 m0, s38, 36864\n\t" \
  "s_waitcnt vmcnt(12)\n\t" \
  "s_barrier\n\t" \
  "buffer_load_dwordx4 v0, s[40:43], s68 offen offset:0 lds\n\t" \
  "s_mov_b32 s85, 0x481000\n\t" \
  "s_load_dword s84, %[pw], s85\n\t" \
  "buffer_load_dwordx4 v1, s[40:43], s68 offen offset:1024 lds\n\t" \
  "s_mov_b32 s85, 0x4b0000\n\t" \
  "s_load_dword s84, %[pw], s85\n\t" \
  "s_mov_b64 exec, 0xffff\n\t" \
  "buffer_load_dwordx4 v4, s[40:43], s68 offen offset:2048 lds\n\t" \
  "s_add_u32 m0, s39, 49152\n\t" \
  "s_mov_b64 exec, -1\n\t" \
  "buffer_load_dwordx4 v5, s[44:47], s69 offen offset:0 lds\n\t" \
  "s_mov_b32 s85, 0x4b0800\n\t" \
  "s_load_dword s84, %[pw], s85\n\t" \
  "buffer_load_dwordx4 v6, s[44:47], s69 offen offset:1024 lds\n\t" \
  "s_mov_b32 s85, 0x4b1000\n\t" \
  "s_load_dword s84, %[pw], s85\n\t" \
  "buffer_load_dwordx4 v7, s[44:47], s69 offen offset:2048 lds\n\t" \
  "s_mov_b32 s85, 0x4e0000\n\t" \
  "s_load_dword s84, %[pw], s85\n\t" \
  "s_add_u32 m0, s38, 46080\n\t" \
  "s_nop 0\n\t" \
  "buffer_load_dwordx4 v2, s[40:43], s70 offen offset:0 lds\n\t" \
  "s_mov_b32 s85, 0x4e0800\n\t" \
  "s_load_dword s84, %[pw], s85\n\t" \
  "buffer_load_dwordx4 v3, s[40:43], s70 offen offset:1024 lds\n\t" \
  "s_mov_b64 exec, 0xffff\n\t" \
  "buffer_load_dwordx4 v4, s[40:43], s70 offen offset:2048 lds\n\t" \
  "s_add_u32 m0, s39, 61440\n\t" \
  "s_mov_b64 exec, -1\n\t" \
  "buffer_load_dwordx4 v5, s[44:47], s71 offen offset:0 lds\n\t" \
  "buffer_load_dwordx4 v6, s[44:47], s71 offen offset:1024 lds\n\t" \
  "buffer_load_dwordx4 v7, s[44:47], s71 offen offset:2048 lds\n\t" \
  "s_mov_b32 s68, 0x500\n\t" \
  "s_mov_b32 s69, 0x3c0000\n\t" \
  "s_mov_b32 s70, 0x24500\n\t" \
  "s_mov_b32 s71, 0x3f0000\n\t" \
  "s_add_u32 m0, s38, 0\n\t" \
  "s_waitcnt vmcnt(12)\n\t" \
  "s_barrier\n\t" \
  "buffer_load_dwordx4 v0, s[40:43], s68 offen offset:0 lds\n\t" \
  "s_mov_b32 s85, 0x4e1000\n\t" \
  "s_load_dword s84, %[pw], s85\n\t" \
  "buffer_load_dwordx4 v1, s[40:43], s68 offen offset:1024 lds\n\t" \
  "s_mov_b32 s85, 0x510000\n\t" \
  "s_load_dword s84, %[pw], s85\n\t" \
  "s_mov_b64 exec, 0xffff\n\t" \
  "buffer_load_dwordx4 v4, s[40:43], s68 offen offset:2048 lds\n\t" \
  "s_add_u32 m0, s39, 0\n\t" \
  "s_mov_b64 exec, -1\n\t" \
  "buffer_load_dwordx4 v5, s[44:47], s69 offen offset:0 lds\n\t" \
  "s_mov_b32 s85, 0x510800\n\t" \
  "s_load_dword s84, %[pw], s85\n\t" \
  "buffer_load_dwordx4 v6, s[44:47], s69 offen offset:1024 lds\n\t" \
  "s_mov_b32 s85, 0x511000\n\t" \
  "s_load_dword s84, %[pw], s85\n\t" \
  "buffer_load_dwordx4 v7, s[44:47], s69 offen offset:2048 lds\n\t" \
  "s_mov_b32 s85, 0x540000\n\t" \
  "s_load_dword s84, %[pw], s85\n\t" \
  "s_add_u32 m0, s38, 9216\n\t" \
  "s_nop 0\n\t" \
  "buffer_load_dwordx4 v2, s[40:43], s70 offen offset:0 lds\n\t" \
  "s_mov_b32 s85, 0x540800\n\t" \
  "s_load_dword s84, %[pw], s85\n\t" \
  "buffer_load_dwordx4 v3, s[40:43], s70 offen offset:1024 lds\n\t" \
  "s_mov_b64 exec, 0xffff\n\t" \
  "buffer_load_dwordx4 v4, s[40:43], s70 offen offset:2048 lds\n\t" \
  "s_add_u32 m0, s39, 12288\n\t" \
  "s_mov_b64 exec, -1\n\t" \
  "buffer_load_dwordx4 v5, s[44:47], s71 offen offset:0 lds\n\t" \
  "buffer_load_dwordx4 v6, s[44:47], s71 offen offset:1024 lds\n\t" \
  "buffer_load_dwordx4 v7, s[44:47], s71 offen offset:2048 lds\n\t" \
  "s_mov_b32 s68, 0x580\n\t" \
  "s_mov_b32 s69, 0x420000\n\t" \
  "s_mov_b32 s70, 0x24580\n\t" \
  "s_mov_b32 s71, 0x450000\n\t" \
  "s_add_u32 m0, s38, 18432\n\t" \
  "s_waitcnt vmcnt(12)\n\t" \
  "s_barrier\n\t" \
  "buffer_load_dwordx4 v0, s[40:43], s68 offen offset:0 lds\n\t" \
  "s_load_dword s84, %[pq], 0x0\n\t" \
  "buffer_load_dwordx4 v1, s[40:43], s68 offen offset:1024 lds\n\t" \
  "s_load_dword s84, %[pq], 0x40\n\t" \
  "s_mov_b64 exec, 0xffff\n\t" \
  "buffer_load_dwordx4 v4, s[40:43], s68 offen offset:2048 lds\n\t" \
  "s_add_u32 m0, s39, 24576\n\t" \
  "s_mov_b64 exec, -1\n\t" \
  "buffer_load_dwordx4 v5, s[44:47], s69 offen offset:0 lds\n\t" \
  "s_load_dword s84, %[pq], 0x800\n\t" \
  "buffer_load_dwordx4 v6, s[44:47], s69 offen offset:1024 lds\n\t" \
  "s_load_dword s84, %[pq], 0x840\n\t" \
  "buffer_load_dwordx4 v7, s[44:47], s69 offen offset:2048 lds\n\t" \
  "s_mov_b32 s85, 0x541000\n\t" \
  "s_load_dword s84, %[pw], s85\n\t" \
  "s_add_u32 m0, s38, 27648\n\t" \
  "s_nop 0\n\t" \
  "buffer_load_dwordx4 v2, s[40:43], s70 offen offset:0 lds\n\t" \
  "s_mov_b32 s85, 0x570000\n\t" \
  "s_load_dword s84, %[pw], s85\n\t" \
  "buffer_load_dwordx4 v3, s[40:43], s70 offen offset:1024 lds\n\t" \
  "s_mov_b32 s85, 0x570800\n\t" \
  "s_load_dword s84, %[pw], s85\n\t" \
  "s_mov_b64 exec, 0xffff\n\t" \
  "buffer_load_dwordx4 v4, s[40:43], s70 offen offset:2048 lds\n\t" \
  "s_add_u32 m0, s39, 36864\n\t" \
  "s_mov_b64 exec, -1\n\t" \
  "buffer_load_dwordx4 v5, s[44:47], s71 offen offset:0 lds\n\t" \
  "s_mov_b32 s85, 0x571000\n\t" \
  "s_load_dword s84, %[pw], s85\n\t" \
  "buffer_load_dwordx4 v6, s[44:47], s71 offen offset:1024 lds\n\t" \
  "s_mov_b32 s85, 0x5a0000\n\t" \
  "s_load_dword s84, %[pw], s85\n\t" \
  "buffer_load_dwordx4 v7, s[44:47], s71 offen offset:2048 lds\n\t" \
  "s_mov_b32 s85, 0x5a0800\n\t" \
  "s_load_dword s84, %[pw], s85\n\t" \
  "s_mov_b32 s68, 0x600\n\t" \
  "s_mov_b32 s69, 0x480000\n\t" \
  "s_mov_b32 s70, 0x24600\n\t" \
  "s_mov_b32 s71, 0x4b0000\n\t" \
  "s_add_u32 m0, s38, 36864\n\t" \
  "s_waitcnt vmcnt(12)\n\t" \
  "s_barrier\n\t" \
  "buffer_load_dwordx4 v0, s[40:43], s68 offen offset:0 lds\n\t" \
  "s_load_dword s84, %[pq], 0x1000\n\t" \
  "buffer_load_dwordx4 v1, s[40:43], s68 offen offset:1024 lds\n\t" \
  "s_load_dword s84, %[pq], 0x1040\n\t" \
  "s_mov_b64 exec, 0xffff\n\t" \
  "buffer_load_dwordx4 v4, s[40:43], s68 offen offset:2048 lds\n\t" \
  "s_add_u32 m0, s39, 49152\n\t" \
  "s_mov_b64 exec, -1\n\t" \
  "buffer_load_dwordx4 v5, s[44:47], s69 offen offset:0 lds\n\t" \
  "s_load_dword s84, %[pq], 0x1800\n\t" \
  "buffer_load_dwordx4 v6, s[44:47], s69 offen offset:1024 lds\n\t" \
  "s_load_dword s84, %[pq], 0x1840\n\t" \
  "buffer_load_dwordx4 v7, s[44:47], s69 offen offset:2048 lds\n\t" \
  "s_mov_b32 s85, 0x5a1000\n\t" \
  "s_load_dword s84, %[pw], s85\n\t" \
  "s_add_u32 m0, s38, 46080\n\t" \
  "s_nop 0\n\t" \
  "buffer_load_dwordx4 v2, s[40:43], s70 offen offset:0 lds\n\t" \
  "s_mov_b32 s85, 0x5d0000\n\t" \
  "s_load_dword s84, %[pw], s85\n\t" \
  "buffer_load_dwordx4 v3, s[40:43], s70 offen offset:1024 lds\n\t" \
  "s_mov_b32 s85, 0x5d0800\n\t" \
  "s_load_dword s84, %[pw], s85\n\t" \
  "s_mov_b64 exec, 0xffff\n\t" \
  "buffer_load_dwordx4 v4, s[40:43], s70 offen offset:2048 lds\n\t" \
  "s_add_u32 m0, s39, 61440\n\t" \
  "s_mov_b64 exec, -1\n\t" \
  "buffer_load_dwordx4 v5, s[44:47], s71 offen offset:0 lds\n\t" \
  "s_mov_b32 s85, 0x5d1000\n\t" \
  "s_load_dword s84, %[pw], s85\n\t" \
  "buffer_load_dwordx4 v6, s[44:47], s71 offen offset:1024 lds\n\t" \
  "buffer_load_dwordx4 v7, s[44:47], s71 offen offset:2048 lds\n\t" \
  "s_mov_b32 s68, 0x680\n\t" \
  "s_mov_b32 s69, 0x4e0000\n\t" \
  "s_mov_b32 s70, 0x24680\n\t" \
  "s_mov_b32 s71, 0x510000\n\t" \
  "s_add_u32 m0, s38, 0\n\t" \
  "s_waitcnt vmcnt(12)\n\t" \
  "s_barrier\n\t" \
  "buffer_load_dwordx4 v0, s[40:43], s68 offen offset:0 lds\n\t" \
  "s_load_dword s84, %[pq], 0x2000\n\t" \
  "buffer_load_dwordx4 v1, s[40:43], s68 offen offset:1024 lds\n\t" \
  "s_load_dword s84, %[pq], 0x2040\n\t" \
  "s_mov_b64 exec, 0xffff\n\t" \
  "buffer_load_dwordx4 v4, s[40:43], s68 offen offset:2048 lds\n\t" \
  "s_add_u32 m0, s39, 0\n\t" \
  "s_mov_b64 exec, -1\n\t" \
  "buffer_load_dwordx4 v5, s[44:47], s69 offen offset:0 lds\n\t" \
  "s_load_dword s84, %[pq], 0x2800\n\t" \
  "buffer_load_dwordx4 v6, s[44:47], s69 offen offset:1024 lds\n\t" \
  "s_load_dword s84, %[pq], 0x2840\n\t" \
  "buffer_load_dwordx4 v7, s[44:47], s69 offen offset:2048 lds\n\t" \
  "s_add_u32 m0, s38, 9216\n\t" \
  "s_nop 0\n\t" \
  "buffer_load_dwordx4 v2, s[40:43], s70 offen offset:0 lds\n\t" \
  "buffer_load_dwordx4 v3, s[40:43], s70 offen offset:1024 lds\n\t" \
  "s_mov_b64 exec, 0xffff\n\t" \
  "buffer_load_dwordx4 v4, s[40:43], s70 offen offset:2048 lds\n\t" \
  "s_add_u32 m0, s39, 12288\n\t" \
  "s_mov_b64 exec, -1\n\t" \
  "buffer_load_dwordx4 v5, s[44:47], s71 offen offset:0 lds\n\t" \
  "buffer_load_dwordx4 v6, s[44:47], s71 offen offset:1024 lds\n\t" \
  "buffer_load_dwordx4 v7, s[44:47], s71 offen offset:2048 lds\n\t" \
  "s_mov_b32 s68, 0x700\n\t" \
  "s_mov_b32 s69, 0x540000\n\t" \
  "s_mov_b32 s70, 0x24700\n\t" \
  "s_mov_b32 s71, 0x570000\n\t" \
  "s_add_u32 m0, s38, 18432\n\t" \
  "s_waitcnt vmcnt(12)\n\t" \
  "s_barrier\n\t" \
  "buffer_load_dwordx4 v0, s[40:43], s68 offen offset:0 lds\n\t" \
  "s_load_dword s84, %[pq], 0x3000\n\t" \
  "buffer_load_dwordx4 v1, s[40:43], s68 offen offset:1024 lds\n\t" \
  "s_load_dword s84, %[pq], 0x3040\n\t" \
  "s_mov_b64 exec, 0xffff\n\t" \
  "buffer_load_dwordx4 v4, s[40:43], s68 offen offset:2048 lds\n\t" \
  "s_add_u32 m0, s39, 24576\n\t" \
  "s_mov_b64 exec, -1\n\t" \
  "buffer_load_dwordx4 v5, s[44:47], s69 offen offset:0 lds\n\t" \
  "s_load_dword s84, %[pq], 0x3800\n\t" \
  "buffer_load_dwordx4 v6, s[44:47], s69 offen offset:1024 lds\n\t" \
  "s_load_dword s84, %[pq], 0x3840\n\t" \
  "buffer_load_dwordx4 v7, s[44:47], s69 offen offset:2048 lds\n\t" \
  "s_add_u32 m0, s38, 27648\n\t" \
  "s_nop 0\n\t" \
  "buffer_load_dwordx4 v2, s[40:43], s70 offen offset:0 lds\n\t" \
  "buffer_load_dwordx4 v3, s[40:43], s70 offen offset:1024 lds\n\t" \
  "s_mov_b64 exec, 0xffff\n\t" \
  "buffer_load_dwordx4 v4, s[40:43], s70 offen offset:2048 lds\n\t" \
  "s_add_u32 m0, s39, 36864\n\t" \
  "s_mov_b64 exec, -1\n\t" \
  "buffer_load_dwordx4 v5, s[44:47], s71 offen offset:0 lds\n\t" \
  "buffer_load_dwordx4 v6, s[44:47], s71 offen offset:1024 lds\n\t" \
  "buffer_load_dwordx4 v7, s[44:47], s71 offen offset:2048 lds\n\t" \
  "s_mov_b32 s68, 0x780\n\t" \
  "s_mov_b32 s69, 0x5a0000\n\t" \
  "s_mov_b32 s70, 0x24780\n\t" \
  "s_mov_b32 s71, 0x5d0000\n\t" \
  "s_add_u32 m0, s38, 36864\n\t" \
  "s_waitcnt vmcnt(12)\n\t" \
  "s_barrier\n\t" \
  "buffer_load_dwordx4 v0, s[40:43], s68 offen offset:0 lds\n\t" \
  "buffer_load_dwordx4 v1, s[40:43], s68 offen offset:1024 lds\n\t" \
  "s_mov_b64 exec, 0xffff\n\t" \
  "buffer_load_dwordx4 v4, s[40:43], s68 offen offset:2048 lds\n\t" \
  "s_add_u32 m0, s39, 49152\n\t" \
  "s_mov_b64 exec, -1\n\t" \
  "buffer_load_dwordx4 v5, s[44:47], s69 offen offset:0 lds\n\t" \
  "buffer_load_dwordx4 v6, s[44:47], s69 offen offset:1024 lds\n\t" \
  "buffer_load_dwordx4 v7, s[44:47], s69 offen offset:2048 lds\n\t" \
  "s_add_u32 m0, s38, 46080\n\t" \
  "s_nop 0\n\t" \
  "buffer_load_dwordx4 v2, s[40:43], s70 offen offset:0 lds\n\t" \
  "buffer_load_dwordx4 v3, s[40:43], s70 offen offset:1024 lds\n\t" \
  "s_mov_b64 exec, 0xffff\n\t" \
  "buffer_load_dwordx4 v4, s[40:43], s70 offen offset:2048 lds\n\t" \
  "s_add_u32 m0, s39, 61440\n\t" \
  "s_mov_b64 exec, -1\n\t" \
  "buffer_load_dwordx4 v5, s[44:47], s71 offen offset:0 lds\n\t" \
  "buffer_load_dwordx4 v6, s[44:47], s71 offen offset:1024 lds\n\t" \
  "buffer_load_dwordx4 v7, s[44:47], s71 offen offset:2048 lds\n\t" \
  "v_readlane_b32 s68, v9, 18\n\t" \
  "v_readlane_b32 s69, v9, 19\n\t" \
  "v_readlane_b32 s70, v9, 20\n\t" \
  "v_readlane_b32 s71, v9, 21\n\t" \
  "v_readlane_b32 s72, v9, 0\n\t" \
  "v_readlane_b32 s73, v9, 1\n\t" \
  "v_readlane_b32 s74, v9, 2\n\t" \
  "v_readlane_b32 s75, v9, 3\n\t" \
  "v_readlane_b32 s76, v9, 4\n\t" \
  "v_readlane_b32 s77, v9, 5\n\t" \
  "s_add_u32 m0, %[ob], 73728\n\t" \
  "s_waitcnt vmcnt(12)\n\t" \
  "s_barrier\n\t" \
  "s_nop 2\n\t" \
  "buffer_load_dwordx4 v8, %[so], s68 offen lds\n\t" \
  "s_add_u32 m0, %[ob], 77824\n\t" \
  "s_nop 0\n\t" \
  "buffer_load_dwordx4 v8, %[so], s69 offen lds\n\t" \
  "s_add_u32 m0, %[ob], 81920\n\t" \
  "s_nop 0\n\t" \
  "buffer_load_dwordx4 v8, %[so], s70 offen lds\n\t" \
  "s_add_u32 m0, %[ob], 86016\n\t" \
  "s_nop 0\n\t" \
  "buffer_load_dwordx4 v8, %[so], s71 offen lds\n\t" \
  "s_add_u32 m0, %[ob], 0\n\t" \
  "s_nop 0\n\t" \
  "buffer_load_dwordx4 v8, %[so], s72 offen lds\n\t" \
  "s_add_u32 m0, %[ob], 4096\n\t" \
  "s_nop 0\n\t" \
  "buffer_load_dwordx4 v8, %[so], s73 offen lds\n\t" \
  "s_add_u32 m0, %[ob], 8192\n\t" \
  "s_nop 0\n\t" \
  "buffer_load_dwordx4 v8, %[so], s74 offen lds\n\t" \
  "s_add_u32 m0, %[ob], 12288\n\t" \
  "s_nop 0\n\t" \
  "buffer_load_dwordx4 v8, %[so], s75 offen lds\n\t" \
  "s_add_u32 m0, %[ob], 16384\n\t" \
  "s_nop 0\n\t" \
  "buffer_load_dwordx4 v8, %[so], s76 offen lds\n\t" \
  "s_add_u32 m0, %[ob], 20480\n\t" \
  "s_nop 0\n\t" \
  "buffer_load_dwordx4 v8, %[so], s77 offen lds\n\t" \
  "s_waitcnt vmcnt(10)\n\t" \
  "s_barrier\n\t" \
  "s_nop 2\n\t" \
  "s_add_u32 m0, %[ob], 90112\n\t" \
  "v_readlane_b32 s68, v9, 22\n\t" \
  "s_nop 4\n\t" \
  "buffer_load_dwordx4 v8, %[so], s68 offen lds\n\t" \
  "s_add_u32 m0, %[ob], 94208\n\t" \
  "v_readlane_b32 s68, v9, 23\n\t" \
  "s_nop 4\n\t" \
  "buffer_load_dwordx4 v8, %[so], s68 offen lds\n\t" \
  "s_add_u32 m0, %[ob], 98304\n\t" \
  "v_readlane_b32 s68, v9, 24\n\t" \
  "s_nop 4\n\t" \
  "buffer_load_dwordx4 v8, %[so], s68 offen lds\n\t" \
  "s_add_u32 m0, %[ob], 102400\n\t" \
  "v_readlane_b32 s68, v9, 25\n\t" \
  "s_nop 4\n\t" \
  "buffer_load_dwordx4 v8, %[so], s68 offen lds\n\t" \
  "s_add_u32 m0, %[ob], 106496\n\t" \
  "v_readlane_b32 s68, v9, 26\n\t" \
  "s_nop 4\n\t" \
  "buffer_load_dwordx4 v8, %[so], s68 offen lds\n\t" \
  "s_add_u32 m0, %[ob], 24576\n\t" \
  "v_readlane_b32 s68, v9, 6\n\t" \
  "s_nop 4\n\t" \
  "buffer_load_dwordx4 v8, %[so], s68 offen lds\n\t" \
  "s_add_u32 m0, %[ob], 28672\n\t" \
  "v_readlane_b32 s68, v9, 7\n\t" \
  "s_nop 4\n\t" \
  "buffer_load_dwordx4 v8, %[so], s68 offen lds\n\t" \
  "s_add_u32 m0, %[ob], 32768\n\t" \
  "v_readlane_b32 s68, v9, 8\n\t" \
  "s_nop 4\n\t" \
  "buffer_load_dwordx4 v8, %[so], s68 offen lds\n\t" \
  "s_add_u32 m0, %[ob], 36864\n\t" \
  "v_readlane_b32 s68, v9, 9\n\t" \
  "s_nop 4\n\t" \
  "buffer_load_dwordx4 v8, %[so], s68 offen lds\n\t" \
  "s_add_u32 m0, %[ob], 40960\n\t" \
  "v_readlane_b32 s68, v9, 10\n\t" \
  "s_nop 4\n\t" \
  "buffer_load_dwordx4 v8, %[so], s68 offen lds\n\t" \
  "s_add_u32 m0, %[ob], 45056\n\t" \
  "v_readlane_b32 s68, v9, 11\n\t" \
  "s_nop 4\n\t" \
  "buffer_load_dwordx4 v8, %[so], s68 offen lds\n\t" \
  "s_nop 3\n\t" \
  "s_barrier\n\t" \
  "s_nop 2\n\t" \
  "s_add_u32 m0, %[ob], 110592\n\t" \
  "v_readlane_b32 s68, v9, 27\n\t" \
  "s_nop 4\n\t" \
  "buffer_load_dwordx4 v8, %[so], s68 offen lds\n\t" \
  "s_add_u32 m0, %[ob], 114688\n\t" \
  "v_readlane_b32 s68, v9, 28\n\t" \
  "s_nop 4\n\t" \
  "buffer_load_dwordx4 v8, %[so], s68 offen lds\n\t" \
  "s_add_u32 m0, %[ob], 118784\n\t" \
  "v_readlane_b32 s68, v9, 29\n\t" \
  "s_nop 4\n\t" \
  "buffer_load_dwordx4 v8, %[so], s68 offen lds\n\t" \
  "s_add_u32 m0, %[ob], 122880\n\t" \
  "v_readlane_b32 s68, v9, 30\n\t" \
  "s_nop 4\n\t" \
  "buffer_load_dwordx4 v8, %[so], s68 offen lds\n\t" \
  "s_add_u32 m0, %[ob], 126976\n\t" \
  "v_readlane_b32 s68, v9, 31\n\t" \
  "s_nop 4\n\t" \
  "buffer_load_dwordx4 v8, %[so], s68 offen lds\n\t" \
  "s_add_u32 m0, %[ob], 49152\n\t" \
  "v_readlane_b32 s68, v9, 12\n\t" \
  "s_nop 4\n\t" \
  "buffer_load_dwordx4 v8, %[so], s68 offen lds\n\t" \
  "s_add_u32 m0, %[ob], 53248\n\t" \
  "v_readlane_b32 s68, v9, 13\n\t" \
  "s_nop 4\n\t" \
  "buffer_load_dwordx4 v8, %[so], s68 offen lds\n\t" \
  "s_add_u32 m0, %[ob], 57344\n\t" \
  "v_readlane_b32 s68, v9, 14\n\t" \
  "s_nop 4\n\t" \
  "buffer_load_dwordx4 v8, %[so], s68 offen lds\n\t" \
  "s_add_u32 m0, %[ob], 61440\n\t" \
  "v_readlane_b32 s68, v9, 15\n\t" \
  "s_nop 4\n\t" \
  "buffer_load_dwordx4 v8, %[so], s68 offen lds\n\t" \
  "s_add_u32 m0, %[ob], 65536\n\t" \
  "v_readlane_b32 s68, v9, 16\n\t" \
  "s_nop 4\n\t" \
  "buffer_load_dwordx4 v8, %[so], s68 offen lds\n\t" \
  "s_add_u32 m0, %[ob], 69632\n\t" \
  "v_readlane_b32 s68, v9, 17\n\t" \
  "s_nop 4\n\t" \
  "buffer_load_dwordx4 v8, %[so], s68 offen lds\n\t" \
  "s_waitcnt vmcnt(0) lgkmcnt(0)\n\t"
#define LD_NBAR 17
#define LD_CO 0
#define LD_HT 8
#define LD_TP 3
#define LD_CLOB "v11", "s68", "s69", "s70", "s71", "s72", "s73", "s74", "s75", "s76", "s77", "s78", "s79", "s80", "s81", "s82", "s83", "s85"

#define ENDW
#define TS(i)
#define TSS(i)
#define TOUT(n)

#define SPIN(cond,slp) do{ int sp_=0; while(cond){ __builtin_amdgcn_s_sleep(slp); if(++sp_>(1<<22)) break; } }while(0)
__device__ __forceinline__ unsigned rd1(const char* p){ return __builtin_amdgcn_readfirstlane(*(volatile LDS_AS unsigned*)(LDS_AS char*)p); }
__device__ __forceinline__ unsigned lds_inc(const char* p, int l){ unsigned r=0; if(l==0) r=__hip_atomic_fetch_add((LDS_AS unsigned*)(LDS_AS char*)p, 1u, __ATOMIC_RELAXED, __HIP_MEMORY_SCOPE_WORKGROUP); return __builtin_amdgcn_readfirstlane(r); }
__device__ __forceinline__ void lds_add(unsigned adr){ asm volatile("s_mov_b64 exec, 1\n\tds_add_u32 %0, %1\n\ts_mov_b64 exec, -1" :: "v"(adr), "v"(1u) : "memory"); }
__device__ __forceinline__ void publish(unsigned cnt, const unsigned* base, unsigned voff, unsigned tag){
  unsigned t;
  asm volatile("s_waitcnt vmcnt(0)\n\ts_mov_b64 exec, 1\n\tds_add_rtn_u32 %0, %1, %2\n\ts_waitcnt lgkmcnt(0)\n\tv_cmp_eq_u32 vcc, 3, %0\n\ts_cbranch_vccz PB_%=\n\t"
    "s_mov_b64 exec, 0xffff\n\tglobal_store_dword %3, %4, %5 sc1\n\tPB_%=:\n\ts_mov_b64 exec, -1"
    : "=&v"(t) : "v"(cnt), "v"(1u), "v"(voff), "v"(tag), "s"(base) : "memory", "vcc");
}
__device__ __forceinline__ void bars(int n){
  asm volatile("BL_%=:\n\ts_barrier\n\ts_sub_u32 %0, %0, 1\n\ts_cmp_lg_u32 %0, 0\n\ts_cbranch_scc1 BL_%=" : "+s"(n) :: "memory", "scc");
}
#define barc(rem) barc_(rem)
#define CTRA
#define CTRP
__device__ __forceinline__ void barc_(unsigned& rem){
  asm volatile("s_cmp_eq_u32 %0, 0\n\ts_cbranch_scc1 BC_%=\n\ts_barrier\n\ts_sub_u32 %0, %0, 1\n\tBC_%=:" : "+s"(rem) :: "memory", "scc");
}
template<int SLP> __device__ __forceinline__ void wait_flag_b(const unsigned* p, unsigned voff, unsigned tag, unsigned& rem CTRA){
  int spins=0;
  while(true){
    unsigned v; asm volatile("global_load_dword %0, %1, %2 sc1" : "=v"(v) : "v"(voff), "s"(p) : "memory");
    barc(rem); if(PB2) barc(rem);
    asm volatile("s_waitcnt vmcnt(0)" : "+v"(v) :: "memory");
    if(__all(v==tag) || ++spins>(1<<21)) break;
    if(rem==0) __builtin_amdgcn_s_sleep(SLP);
  }
}
__device__ __forceinline__ void stout(void* sbase, unsigned voff, u32x4 v){
  st16cs(sbase,voff,v);
}

typedef unsigned u32x8 __attribute__((ext_vector_type(8)));
typedef unsigned u32x16 __attribute__((ext_vector_type(16)));
__device__ __forceinline__ char* ptr64(unsigned lo, unsigned hi){ return (char*)(((unsigned long long)hi<<32)|lo); }
__device__ __forceinline__ u32x4 srd(const char* p, unsigned bytes){ return u32x4{(unsigned)(size_t)p,(unsigned)((size_t)p>>32)&0xffffu,bytes,0x20000u}; }
constexpr size_t WS_O=0, WS_KV=(size_t)S*DM*2, WS_Y=(size_t)S*DM*4, WS_FL=(size_t)S*DM*12;

__global__ __launch_bounds__(768,3) void fused_kernel(const u16* x_, const u16* wqkv_, const u16* wo_, char* ws_, u16* out_,
    const u16* kin_, const u16* vin_, const u16* shk_, const u16* hk_, const u16* qin_, const u16* sD_)
{
  __shared__ __attribute__((aligned(128))) char smem[ALL_LDS];
  TS(0);
  int wv;
  asm volatile(
      "s_load_dwordx16 s[48:63], %[ka], 0x0\n\ts_load_dwordx4 s[64:67], %[ka], 0x40\n\ts_load_dwordx2 s[78:79], %[ka], 0x50\n\ts_getpc_b64 s[72:73]\n\tv_readfirstlane_b32 s75, v0\n\ts_lshr_b32 s74, %[b], 3\n\ts_mul_i32 s74, s74, 12\n\ts_lshr_b32 s75, s75, 6\n\t"
      "s_add_u32 s74, s74, s75\n\ts_min_u32 s74, s74, %[cl]\n\ts_lshl_b32 s74, s74, 7\n\ts_mov_b32 m0, %[sk]\n\tv_mov_b32 v1, s74\n\ts_mov_b64 exec, 1\n\tglobal_load_lds_dword v1, s[72:73]\n\ts_mov_b64 exec, -1\n\t"
      "s_mov_b32 s76, %[b]\n\ts_cmp_lt_u32 s75, 4\n\ts_cbranch_scc1 FK_G\n\ts_cmp_lt_u32 s75, 8\n\ts_cbranch_scc0 FK_C\n\t"
      "v_and_b32 v9, 7, v0\n\tv_and_b32 v10, 63, v0\n\ts_lshl_b32 s74, %[b], 19\n\ts_lshl_b32 s73, %[b], 11\n\ts_or_b32 s74, s74, s73\n\ts_and_b32 s74, s74, 0x3fc000\n\ts_lshl_b32 s73, s75, 9\n\t"
      "s_add_u32 s74, s74, s73\n\ts_add_u32 s74, s74, 0x7800\n\tv_lshl_add_u32 v9, v9, 11, s74\n\tv_and_b32 v1, 24, v0\n\ts_lshl2_add_u32 s74, %[b], s75\n\ts_mul_i32 s74, s74, %[nw]\n\tv_lshl_add_u32 v9, v1, 4, v9\n\t"
      "s_add_u32 s74, s74, %[w0]\n\tv_add_lshl_u32 v10, v10, s74, 7\n\t"
      "s_setprio %[lp]\n\ts_and_b32 s74, s75, 3\n\tv_lshrrev_b32 v1, 3, v0\n\tv_lshrrev_b32 v3, 4, v0\n\ts_and_b32 s72, %[b], 1\n\ts_and_b32 s73, %[b], 0xe0\n\ts_lshl_b32 s72, s72, 10\n\ts_lshl2_add_u32 s72, s73, s72\n\t"
      "v_and_b32 v4, 2, v1\n\tv_and_or_b32 v3, v3, 4, v4\n\tv_and_b32 v4, 31, v1\n\tv_xor_b32 v3, v3, v0\n\tv_mul_u32_u24 v4, 0x1800, v4\n\tv_and_b32 v3, 7, v3\n\tv_lshl_add_u32 v3, v3, 4, s72\n\t"
      "s_mul_i32 s39, s74, 0xc00\n\ts_mul_i32 s38, s74, 0x900\n\ts_mul_i32 s74, s74, 18\n\tv_add_u32 v5, v3, v4\n\tv_and_b32 v1, 7, v1\n\tv_add_u32 v6, 0x400, v5\n\tv_add_u32 v1, s74, v1\n\tv_add_u32 v7, 0x800, v5\n\t"
      "s_and_b32 s72, %[b], 6\n\ts_and_b32 s73, %[b], 24\n\ts_lshl4_add_u32 s72, s72, s73\n\tv_xor_b32 v3, v1, v0\n\ts_lshl_b32 s72, s72, 15\n\tv_and_b32 v3, 7, v3\n\tv_lshl_add_u32 v1, v1, 11, s72\n\t"
      "s_mov_b32 s47, 0x20000\n\ts_mov_b32 s46, 0x600000\n\tv_lshl_add_u32 v2, v3, 4, v1\n\ts_mov_b32 s43, s47\n\ts_mov_b32 s42, 0x408000\n\ts_movk_i32 s73, 0x3c00\n\t"
      "v_add_u32 v3, s73, v2\n\ts_add_u32 s38, s38, %[xf]\n\tv_max_u32 v0, 0x8000, v2\n\tv_add_u32 v4, s73, v3\n\tv_max_u32 v1, 0x8000, v3\n\t"
      "s_waitcnt lgkmcnt(0)\n\ts_mov_b64 s[44:45], s[50:51]\n\ts_sub_u32 s40, s48, 0x8000\n\ts_subb_u32 s41, s49, 0\n\t"
      LD_ASM1
      "s_branch FK_L"
      : "=s"(wv)
      : "{v0}"(threadIdx.x), [ka]"s"(__builtin_amdgcn_kernarg_segment_ptr()), [b]"s"(blockIdx.x), [cl]"n"(CODEL-1), [sk]"n"(SINK+256), [lp]"n"(LPR), [xf]"n"(X_OFF), [nw]"n"(3*TPW), [w0]"n"(TW0*3072-12*TPW), [wm]"n"((1<<(3*TPW))-1)
      : "memory", "scc", "s48", "s49", "s50", "s51", "s52", "s53", "s54", "s55", "s72", "s73", "s74", "s75", "s76", "s78", "s79", "v1", "v2", "v3", "v4", "v5", "v6", "v7", "v9", "v10",
        "s40", "s41", "s42", "s43", "s44", "s45", "s46", "s47", "s38", "s39", "s56", "s57", "s58", "s59", "s60", "s61", "s62", "s63", "s64", "s65", "s66", "s67", "s68");
  #define ENTER(lbl) int w8, bid; u32x8 a0, a1; u32x4 a2; u32x2 a3; asm volatile(lbl ":" : "={s75}"(w8), "={s76}"(bid), "={s[48:55]}"(a0), "={s[56:63]}"(a1), "={s[64:67]}"(a2), "={s[78:79]}"(a3) :: "memory");
  #define ARGW asm volatile("s_waitcnt lgkmcnt(0)" : "+{s[48:55]}"(a0), "+{s[56:63]}"(a1), "+{s[64:67]}"(a2), "+{s[78:79]}"(a3));
  #define ROLE const int w=w8&3, xcd=bid&7, idx=bid>>3, rb=(xcd>>1)*4+(idx&3), hp=(xcd&1)*8+(idx>>2), s0=rb*128; (void)s0; (void)hp;
  #define LANEVARS int l; asm volatile("v_mbcnt_lo_u32_b32 %0, -1, 0\n\tv_mbcnt_hi_u32_b32 %0, -1, %0" : "=v"(l)); const int g=l>>4, fr=l&15, tq=(l&15)>>2, tp=l&3; (void)g; (void)fr; (void)tq; (void)tp;
  #define ARGS0 const char* x=ptr64(a0[0],a0[1]); const char* wqkv=ptr64(a0[2],a0[3]); const char* wo=ptr64(a0[4],a0[5]); char* ws=ptr64(a0[6],a0[7]); \
    unsigned* flags=(unsigned*)(ws+WS_FL); const int hot0=(3-(int)(a0[2]>>7))&7; (void)x; (void)wqkv; (void)wo; (void)flags; (void)hot0;
  #define OCOL ((hp&8)|((hp+(int)(a0[2]>>7)-(int)(a0[4]>>7)+OSH)&7))
  #define ORA ((unsigned)(hp-hot0-1)&7u)
  #define ORE(c) (ORA==7u? (c)-1u : (c))
  #define ORO(c) (ORE(c)>=NF && ORE(c)<NF+7u)
  #define ORD(c) (((((ORE(c)<14u? ((ORA==7u? 0u:ORA)+(ORO(c)? ORE(c)-NF : (ORE(c)<NF? ORE(c) : ORE(c)-7u)))%7u : 7u)+hot0+1)&7u))+8u*(((hp>>3)^(ORO(c) || (c)==15u))&1u))
  const unsigned sb0=__builtin_amdgcn_readfirstlane(lds_addr(smem));
  const char* syn=smem+SYN;

  if(__builtin_expect(wv==1,1)){
    ENTER("FK_L")
    u32x4 sx, sw; unsigned xl, wl, xoA, xoB, xo, xo1, xo2, wof[3];
    asm volatile("; descriptors and LDS bases" : "={s[40:43]}"(sx), "={s[44:47]}"(sw), "={s38}"(xl), "={s39}"(wl));
    asm volatile("; lane offsets" : "={v0}"(xoA), "={v1}"(xoB), "={v2}"(xo), "={v3}"(xo1), "={v4}"(xo2), "={v5}"(wof[0]), "={v6}"(wof[1]), "={v7}"(wof[2]));
    unsigned ltr=0, ltr2=0, ltr3=0, sd; (void)ltr; (void)ltr2; (void)ltr3;
    const char* pw;
    {
      ROLE
      LANEVARS
      ARGS0
      const int hg=((idx>>2)-hot0)&7, hi=hot0*4+(idx&3);
      const unsigned long long hm= hg==0? 0ull : hg==HGR? 0xfffull : 0x3full;
      const unsigned hto=(unsigned)(w*393216+(l<6? l*2048+idx*12288 : (l-6)*2048+hi*12288));
      pw=wqkv+(idx*6144+(xcd&1)*1024+(xcd>>1)*256+w*64);
      asm volatile(LD_ASM2
        : "=&{s84}"(sd), "+s"(w8), "+s"(bid), "+s"(a0)
        : "{v0}"(xoA), "{v1}"(xoB), "{v2}"(xo), "{v3}"(xo1), "{v4}"(xo2), "{v5}"(wof[0]), "{v6}"(wof[1]), "{v7}"(wof[2]),
          "{v16}"(hto), [sh]"s"(srd(wqkv+((xcd&1)*8+hot0)*128,6291456u-1920u)), [hm]"s"(hm),
          "{s[40:43]}"(sx), "{s[44:47]}"(sw), "{s38}"(xl), "{s39}"(wl), [pw]"s"(pw), [tsk]"n"(SINK+256)
        : "memory", "scc", "vcc", LD_CLOB);
    }
    ROLE
    LANEVARS
    ARGS0
    const unsigned wd=OCOL*128;
    {
      const unsigned wof2=(unsigned)(w*16384+(l>>3)*2048+((((l&7)>>1)^((l>>4)&3))*32)+(l&1)*16);
      const unsigned to=ORD((unsigned)(l>>1)&15u);
      const unsigned tab=to*131072u+(l&1)*65536;
      asm volatile(LD_ASM3
        : "+{s84}"(sd)
        : "{v0}"(xoA), "{v1}"(xoB), "{v2}"(xo), "{v3}"(xo1), "{v4}"(xo2), "{v5}"(wof[0]), "{v6}"(wof[1]), "{v7}"(wof[2]), "{v8}"(wof2), "{v9}"(tab),
          "{s[40:43]}"(sx), "{s[44:47]}"(sw), [so]"s"(srd(wo+wd,2097152u-wd)), "{s38}"(xl), "{s39}"(wl), [ob]"s"(sb0+w*1024), [pw]"s"(pw),
          [pq]"s"(wo+((xcd&1)*1024+((3-(int)(a0[4]>>7))&7)*128+(idx*32+w*8)*2048))
        : "memory", "scc", "vcc", LD_CLOB);
    }
    const unsigned tag=rd1(syn+64)+1u;
    lds_add(sb0+SYN+12);
    TS(2);
    if(w>=4-NPOL){
      const unsigned* fp=flags+F_O+(rb*16+hp)*16; const unsigned fo=ORD((unsigned)l&15u)*4u;
      const unsigned madr=sb0+(l==0? SYN+20 : SINK+l*4);
      SPIN(rd1(syn+12)<4u || rd1(syn+16)<4u,1);
      if(w==3) PUBM(madr,1u);
      if(PD0) __builtin_amdgcn_s_sleep(PD0);
      if(w==2) __builtin_amdgcn_s_sleep(PD1); if(w==1) __builtin_amdgcn_s_sleep(2*PD1); if(w==0) __builtin_amdgcn_s_sleep(3*PD1);
      unsigned rdy=1; int spins=0;
      while(rdy!=0xFFFFu){
        unsigned v;
        asm volatile("s_mov_b64 exec, 0xffff\n\tglobal_atomic_or %0, %1, %3, %2 sc0 sc1\n\ts_mov_b64 exec, -1\n\ts_waitcnt vmcnt(0)" : "=&v"(v) : "v"(fo), "s"(fp), "v"(0u) : "memory");
        unsigned r=((unsigned)__ballot(v==tag)&0xFFFFu)|1u;
        if(++spins>(1<<21)) r=0xFFFFu;
        if(r!=rdy){ PUBM(madr,(unsigned)__builtin_ctz(~r)); rdy=r; if(w<3 && (r&0x3FFFu)==0x3FFFu) break; }
        else __builtin_amdgcn_s_sleep(OSLP);
      }
      TS(3);
      if(bid==0 && w==3){
        int sp=0;
        while(true){
          u32x4 v; asm volatile("global_load_dwordx4 %0, %1, %2 sc1\n\ts_waitcnt vmcnt(0)" : "=&v"(v) : "v"(l*16), "s"(flags+F_Y) : "memory");
          if(__all(v[0]==tag && v[1]==tag && v[2]==tag && v[3]==tag) || ++sp>(1<<20)) break;
          __builtin_amdgcn_s_sleep(12);
        }
        if(l==0) stflag(flags, tag);
      }
    } else { TS(3); }
    TOUT(4);
    __builtin_amdgcn_endpgm();
  }

  if(wv==0){
    ENTER("FK_G")
    ROLE
    __builtin_amdgcn_s_setprio(GPR);
    const int h=w&1, r=w>>1;
    LANEVARS
    TSS(3);
    ARGW
    ARGS0
    if(PGT&1){ if(w==1 && l<7) touch(ws, l<6? l*2097152+PGO : (unsigned)WS_FL+2048, sb0+SINK+512); }
    if(PGT&2){ if(w==2 && l<4) touch(ptr64(a1[0],a1[1]), l*2097152+PGO, sb0+SINK+512); }
    if(PGT&4){ if(w==3 && l<1) touch(wo, PGO, sb0+SINK+512); if(w==0 && l<2) touch(wqkv, l*2097152+2097152, sb0+SINK+512); }
    TSS(4);
    {
      unsigned lx0=sb0+X_OFF+fr*128+((g^(fr&7))*16), lx1=sb0+X_OFF+fr*128+(((4+g)^(fr&7))*16);
      const int sw=wswz(8*g+tq);
      unsigned lw0=sb0+g*3072+tq*128+(unsigned)((((2*h)^sw)*32)+8*tp), lw1=sb0+g*3072+tq*128+(unsigned)((((2*h+1)^sw)*32)+8*tp);
      char* ot=ws+WS_O+(rb*16+hp)*16384+r*8192+h*2048;
      unsigned it; unsigned long long rm; asm("s_cmp_lg_u32 %1, 0\n\ts_cselect_b64 %0, -1, 0" : "=s"(rm) : "s"(r) : "scc");
      asm volatile(N2_ASM
        : [it]"=&s"(it), "+{v56}"(lx0), "+{v57}"(lx1), "+{v58}"(lw0), "+{v59}"(lw1)
        : "{v62}"(l*16), "{v63}"(sb0+EX_OFF+h*3072+l*16), [r]"s"(r), [ot]"s"(ot), [ou]"s"(ot+4096), [fa]"s"(sb0+SYN+32+w*4), [fp]"s"(sb0+SYN+32+(w^2)*4),
          [vo]"s"(2048-1024*r), [rm]"s"(rm), [z]"s"(rb|r)
        : "memory", "scc", "vcc", N2_CLOB);
    }
    TS(3);
    unsigned tb;
    {
      LANEVARS
      const unsigned tag=rd1(syn+64)+1u;
      const unsigned ra=sb0+(4*g+tq)*128+8*tp; const int sw=(2*g+(tq>>1))&3;
      unsigned b0=ra+((0^sw)*32), b1=ra+((1^sw)*32), b2=ra+((2^sw)*32), b3=ra+((3^sw)*32), vo=(unsigned)(w*4096+l*16), sy=sb0+SYN+20;
      unsigned sr=sb0+EX_OFF+w*2048+(l>>3)*128+(((l&7)^(l>>3))*16), sg=sb0+EX_OFF+w*2048+fr*128+(((g>>1)^(fr&7))*16)+(g&1)*8;
      tb=ORD((unsigned)l&15u)*16384u;
      asm volatile("" : "+{v160}"(b0), "+{v161}"(b1), "+{v162}"(b2), "+{v163}"(b3), "+{v164}"(vo), "+{v165}"(sy), "+{v166}"(sr), "+{v167}"(sg), "+{v96}"(tb));
      publish(sb0+SYN+16, flags+F_O+rb*256+hp, l*64, tag);
      TS(4);
      __builtin_amdgcn_s_setprio(G2P);
      unsigned iss=0, q0, q1, q2, cn=0;
      asm volatile(G3_ASM
        : [iss]"+&s"(iss), [t0]"=&s"(q0), [t1]"=&s"(q1), [t2]"=&s"(q2), [cn]"+&s"(cn),
          "+{v160}"(b0), "+{v161}"(b1), "+{v162}"(b2), "+{v163}"(b3), "+{v165}"(sy), "+{v96}"(tb)
        : "{v164}"(vo), "{v166}"(sr), "{v167}"(sg), [go]"s"(60+w*4), [sy]"n"(SYN+20), [ob]"s"(srd(ws+WS_O+rb*262144,262144u)), [op]"s"(ptr64(a1[0],a1[1])+((s0+w*32)*4096+2048+OCOL*128))
        : "memory", "scc", "vcc", G3_CLOB);
    }
    {
      ENDW TS(6); TOUT(7);
    }
    __builtin_amdgcn_endpgm();
  }

  {
    ENTER("FK_C")
    ROLE
    char* cr=smem+C_OFF+2048+w*4096;
    const unsigned crs=sb0+C_OFF+2048+w*4096;
    const int sblk=bid>>3, td0=(bid&7)*128+(w&1)*64;
    const int vb=(LOCA&1)? xcd*32+(idx&16)+(idx&1)*8+((idx>>1)&7) : bid, fb=(LOCA&2)? (idx&15)*16+xcd*2+(idx>>4) : bid;
    const int d=(vb>>4)*64+(((vb>>3)&1)*4+w)*8+(vb&7);
    int kv0=KV0+((sblk>>KSH)&KVS);
    static_assert(KV0+KVS+PP_NB<=LD_NBAR && KV0>=EB, "barrier budget");
    unsigned tagv, ctr; f32x8 RB;
    const int fd0=(fb&15)*64, fs0=(fb>>4)*128+w*32;
    u32x4 qv[4];
    f32x16 A0,A1,A2,A3;
    const int rw=sblk*64+(w>>1)*32, j=rw>>5;
    unsigned k0,k1,k2,ko,ta,se,so,ha,l16,rb0[2];
    {
      LANEVARS
      if(w==0 && l<25 && l!=16) ((unsigned*)(smem+SYN))[l]=0u;
      if(l<32) *(u32x4*)(smem+C_OFF+w*512+l*16)=u32x4{0,0,0,0};
      asm volatile("" ::: "memory");
      __builtin_amdgcn_s_setprio(CPR);
      const int dg=l&7, rg=l>>3;
      ko=(unsigned)(rg*8192+dg*16);
      const bool top=(rw==0 && rg==0);
      k0=top? ko+6144:ko; k1=top? ko+6144:ko+2048; k2=top? ko+6144:ko+4096;
      ta=sb0+EX_OFF+w*1024+(w&1)*512+dg*64; se=(l&8)? 0x03020706u : 0x05040100u; ha=crs+(32*g-4*tq+4*tp)*2; l16=l*16;
      so=(unsigned)(rg*32768+((j>>2)&3)*128+((dg&4)|((dg+j)&3))*16);
      #pragma unroll
      for(int k=0;k<2;k++){ const int q=fr-1-2*g+k; rb0[k]=sb0+C_OFF+(4+(q>>1))*256+((q>>3)&3)*64+((w+(q>>1))&3)*16+(q&1)*8; }
      asm volatile("" : "+{v152}"(k0), "+{v153}"(k1), "+{v154}"(k2), "+{v155}"(ko), "+{v156}"(ta), "+{v157}"(se), "+{v159}"(so), "+{v163}"(l16), "+{v165}"(ha), "+{v166}"(rb0[0]), "+{v167}"(rb0[1]));
    }
    TSS(3);
    ARGW
    if(EB==1) asm volatile("s_barrier" : "+s"(a1), "+s"(a2), "+s"(a3), "+s"(a0) :: "memory");
    if(EB==2) asm volatile("s_barrier\n\ts_barrier" : "+s"(a1), "+s"(a2), "+s"(a3), "+s"(a0) :: "memory");
    ARGS0
    char* out=ptr64(a1[0],a1[1]); const char* qp=ptr64(a2[2],a2[3])+(fs0*DM+fd0)*2;
    {
      const char* kb=ptr64(a1[2],a1[3])+(rw*DM+td0)*2; const char* vb=ptr64(a1[4],a1[5])+(rw*DM+td0)*2;
      unsigned long long zm; asm("s_cmp_eq_u32 %1, 0\n\ts_cselect_b64 %0, 0xff, 0" : "=s"(zm) : "s"(rw) : "scc");
      unsigned t0;
      if(PGT&8){ const char* tp= w==0? kb : w==1? vb : w==2? qp : ptr64(a2[0],a2[1])+d*4096;
        asm volatile("s_mov_b32 m0, %1\n\ts_mov_b64 exec, 1\n\tglobal_load_lds_dword v163, %0\n\ts_mov_b64 exec, -1" :: "s"(tp), "s"(sb0+SINK+512), "{v163}"(l16) : "memory"); }
      TSS(5);
      asm volatile(PP_ASM
        : [t0]"=&s"(t0), "=&{v164}"(ctr), "+{v159}"(so), "=&{v160}"(tagv), "=&{v[24:31]}"(RB), "=&{v[44:47]}"(qv[0]), "=&{v[48:51]}"(qv[1]), "=&{v[52:55]}"(qv[2]), "=&{v[56:59]}"(qv[3]),
          "=&{v[64:79]}"(A0), "=&{v[80:95]}"(A1), "=&{v[96:111]}"(A2), "=&{v[112:127]}"(A3)
        : "{v152}"(k0), "{v153}"(k1), "{v154}"(k2), "{v155}"(ko), "{v156}"(ta), "{v157}"(se), "{v166}"(rb0[0]), "{v167}"(rb0[1]), "{v165}"(ha), "{v163}"(l16),
          [qb0]"s"(qp), [qb2]"s"(qp+4096), [mk]"s"(0x180u), [em]"s"(1ull|(512ull<<w)), [hd]"s"(crs),
          [kb0]"s"(kb-6144), [kb3]"s"(kb), [kb5]"s"(kb+4096), [vb0]"s"(vb), [vb2]"s"(vb+4096), [sb]"s"(ws+WS_KV+td0*4096+j*512),
          [n0]"s"(kv0-EB-(HH==2)), [ff]"s"(flags+F_PP+(td0>>6)*64+j), [zm]"s"(zm), [shd]"s"(sb0+EX_OFF+w*1024), [tgd]"s"(sb0+SYN+64), [shp]"s"(ptr64(a1[6],a1[7])+(bid&7)*1024), [fl]"s"(flags),
          [hp]"s"(ptr64(a2[0],a2[1])+d*4096), [dp]"s"(ptr64(a3[0],a3[1])+d*2),
          [ph]"s"(wqkv+(((xcd&1)*8+hot0)*128+idx*6144+(w>>1)*196608+(w&1)*64))
        : "memory", "scc", PP_CLOB);
    }
    LANEVARS
    const unsigned tag=__builtin_amdgcn_readfirstlane(tagv)+1u;
    unsigned rem=LD_NBAR-PP_NB-kv0;
    barc(rem);
    const unsigned* pf=flags+F_PP+(d>>6)*64;
    const char* kvp=ws+WS_KV+((vb>>4)*8+(vb&7))*32768+((vb>>3)&1)*64+w*8192;
    const unsigned kvo=(unsigned)((l>>2)*128+(l&3)*16);
    int bidx=((l>=16)? l-16 : (l==0? 63 : l+47))*4;
    asm volatile("" : "+{v32}"(bidx));
    __builtin_amdgcn_s_setprio(3);
    while(rem>PREM) barc(rem);
    wait_flag_b<8>(pf, l*4, tag, rem CTRP);
    #pragma unroll
    for(int i=0;i<4;i++){ if(i%KTH==0 && i) barc(rem); gldsc(kvp+i*2048, kvo, crs+i*1024); }
    barc(rem); if(PB2) barc(rem);
    WAITV(0);
    asm volatile("s_mov_b32 m0, %2\n\ts_mov_b64 exec, 1\n\tglobal_load_lds_dword %0, %1 sc1\n\ts_mov_b64 exec, -1" :: "v"(0u), "s"(pf), "s"(sb0+SYN+100+w*4) : "memory");
    while(rem) barc(rem);
    { int sp=0; while(!__all(*(volatile LDS_AS unsigned*)(LDS_AS char*)(syn+100+(l&3)*4)==tag) && ++sp<(1<<22)) __builtin_amdgcn_s_sleep(1); }
    SPIN(rd1(syn+32+w*4)==0u,1); __builtin_amdgcn_s_sleep(CGATE);
    __builtin_amdgcn_s_setprio(CVP);
    TS(3);
    f32x16 ac0, ac1;
    {
      asm volatile("s_nop 1\n\t" CV_ASM
        : "=&{v[128:143]}"(ac0), "=&{v[144:159]}"(ac1)
          , "+{v[44:47]}"(qv[0]), "+{v[48:51]}"(qv[1]), "+{v[52:55]}"(qv[2]), "+{v[56:59]}"(qv[3])
        : "{v[64:79]}"(A0), "{v[80:95]}"(A1), "{v[96:111]}"(A2), "{v[112:127]}"(A3),
          "{v[24:31]}"(RB), "{v32}"(bidx), "{v33}"(sb0+SYN+80+w*4)
          , "{v42}"((256*(g&1)+16*fr+4*(g&2))*2), [yb]"s"(ws+WS_Y+d*4096)
          , "{v43}"((l>>3)*8192+(l&7)*16), [q0]"s"(qp), [q2]"s"(qp+4096)
        : "memory", "scc", CV_CLOB);
    }
    TS(4);
    __builtin_amdgcn_s_setprio(3);
    publish(sb0+SYN+24, flags+F_Y+vb, l*1024, tag);
    asm volatile("" : "+v"(qv[0]), "+v"(qv[1]), "+v"(qv[2]), "+v"(qv[3]));
    TS(5);
    float qf[32];
    #pragma unroll
    for(int i=0;i<16;i++){ qf[2*i]=bflo(qv[i>>2][i&3]); qf[2*i+1]=bfhi(qv[i>>2][i&3]); }
    unsigned ya=crs+(g*8+tq)*64+tp*8, wa0=crs+fr*128+((g^(fr&7))*16), wa1=crs+fr*128+(((4+g)^(fr&7))*16), ra[4], oo=(unsigned)((l>>3)*16384+(l&7)*16), yo2=(l>>2)*4096+(l&3)*16;
    #pragma unroll
    for(int i=0;i<4;i++){ const int row=4*(l>>3)+i; ra[i]=crs+row*128+(((l&7)^(row&7))*16); }
    char* op=out+fs0*4096+fd0*2; char* op2=op+8192; const char* yg=ws+WS_Y+fd0*4096+fs0*2;
    #pragma unroll
    for(int i=0;i<32;i++) asm volatile("" : "+v"(qf[i]));
    asm volatile("" : "+v"(ya), "+v"(wa0), "+v"(wa1), "+v"(ra[0]), "+v"(ra[1]), "+v"(ra[2]), "+v"(ra[3]), "+v"(oo), "+v"(yo2));
    asm volatile("" : "+s"(op), "+s"(op2), "+s"(yg));
    if(w==0){
      SPIN(rd1(syn+24)<4u,1);
      SPIN(rd1(syn+96)==0u,2);
      { int sp=0;
        while(true){
          unsigned v=tag; asm volatile("s_mov_b64 exec, 0xffff\n\tglobal_atomic_or %0, %1, %3, %2 sc0 sc1\n\ts_mov_b64 exec, -1\n\ts_waitcnt vmcnt(0)" : "+&v"(v) : "v"(l*4), "s"(flags+F_Y+fb*16), "v"(0u) : "memory");
          if(__all(v==tag) || ++sp>(1<<21)) break;
          __builtin_amdgcn_s_sleep(YSLP);
        } }
      PUB(sb0+(l==0? SYN+28 : SINK+l*4), 1u);
    } else SPIN(rd1(syn+28)==0u,YS2);
    #pragma unroll
    for(int i=0;i<4;i++) gldsc(yg+i*65536, yo2, crs+i*1024);
    {
      u32x4 y4[4];
      asm volatile("s_waitcnt vmcnt(0)\n\t"
        "ds_read_b64_tr_b16 v[0:1], %4\n\tds_read_b64_tr_b16 v[2:3], %4 offset:256\n\tds_read_b64_tr_b16 v[4:5], %4 offset:32\n\tds_read_b64_tr_b16 v[6:7], %4 offset:288\n\t"
        "ds_read_b64_tr_b16 v[8:9], %4 offset:2048\n\tds_read_b64_tr_b16 v[10:11], %4 offset:2304\n\tds_read_b64_tr_b16 v[12:13], %4 offset:2080\n\tds_read_b64_tr_b16 v[14:15], %4 offset:2336\n\t"
        "s_waitcnt lgkmcnt(6)\n\tds_write_b128 %5, v[0:3]\n\ts_waitcnt lgkmcnt(5)\n\tds_write_b128 %5, v[4:7] offset:2048\n\t"
        "s_waitcnt lgkmcnt(4)\n\tds_write_b128 %6, v[8:11]\n\ts_waitcnt lgkmcnt(3)\n\tds_write_b128 %6, v[12:15] offset:2048\n\t"
        "ds_read_b128 %0, %7\n\tds_read_b128 %1, %8\n\tds_read_b128 %2, %9\n\tds_read_b128 %3, %10\n\ts_waitcnt lgkmcnt(0)"
        : "=&v"(y4[0]), "=&v"(y4[1]), "=&v"(y4[2]), "=&v"(y4[3])
        : "v"(ya), "v"(wa0), "v"(wa1), "v"(ra[0]), "v"(ra[1]), "v"(ra[2]), "v"(ra[3])
        : "memory", "v0", "v1", "v2", "v3", "v4", "v5", "v6", "v7", "v8", "v9", "v10", "v11", "v12", "v13", "v14", "v15");
      #pragma unroll
      for(int i=0;i<4;i++){
        u32x4 ov;
        #pragma unroll
        for(int e=0;e<4;e++) ov[e]=pack2(mul1(qf[8*i+2*e],bflo(y4[i][e])), mul1(qf[8*i+2*e+1],bfhi(y4[i][e])));
        stout((i&2? op2:op)+(i&1)*4096, oo, ov);
      }
    }
    ENDW TS(6); TOUT(7);
    __builtin_amdgcn_endpgm();
  }
}

extern "C" void kernel_launch(void* const* d_in, const int* in_sizes, int n_in, void* d_out, int out_size, void* d_ws, size_t ws_size, hipStream_t stream){
  const u16* q=(const u16*)d_in[0]; const u16* k=(const u16*)d_in[1]; const u16* v=(const u16*)d_in[2];
  const u16* shk=(const u16*)d_in[3]; const u16* hk=(const u16*)d_in[4]; const u16* sD=(const u16*)d_in[5];
  const u16* x=(const u16*)d_in[6]; const u16* wqkv=(const u16*)d_in[7]; const u16* wo=(const u16*)d_in[8];
  fused_kernel<<<256,768,0,stream>>>(x,wqkv,wo,(char*)d_ws,(u16*)d_out,k,v,shk,hk,q,sD);
}
```

```cpp
#include <hip/hip_runtime.h>
#include <hip/hip_bf16.h>
#include <stdint.h>

typedef unsigned short u16;
typedef short bf16x8 __attribute__((ext_vector_type(8)));
typedef short s16x4 __attribute__((ext_vector_type(4)));
typedef float f32x2 __attribute__((ext_vector_type(2)));
typedef float f32x4 __attribute__((ext_vector_type(4)));
typedef float f32x8 __attribute__((ext_vector_type(8)));
typedef float f32x16 __attribute__((ext_vector_type(16)));
typedef unsigned u32x4 __attribute__((ext_vector_type(4)));
typedef unsigned u32x2 __attribute__((ext_vector_type(2)));
typedef __bf16 bf16x2_t __attribute__((ext_vector_type(2)));
#define LDS_AS __attribute__((address_space(3)))

__device__ __forceinline__ float bflo(unsigned x){ return __uint_as_float(x<<16); }
__device__ __forceinline__ float bfhi(unsigned x){ return __uint_as_float(x&0xffff0000u); }
__device__ __forceinline__ unsigned pack2(float lo, float hi){ return __builtin_bit_cast(unsigned, __builtin_convertvector(f32x2{lo,hi}, bf16x2_t)); }
__device__ __forceinline__ float mul1(float a, float b){ float r; asm("v_mul_f32 %0, %1, %2" : "=v"(r) : "v"(a), "v"(b)); return r; }
__device__ __forceinline__ float fma1(float a, float b, float c){ float r; asm("v_fma_f32 %0, %1, %2, %3" : "=v"(r) : "v"(a), "v"(b), "v"(c)); return r; }

__device__ __forceinline__ void glds(const void* sbase, unsigned voff, unsigned ldsaddr){
  asm volatile("s_mov_b32 m0, %2\n\ts_nop 0\n\tglobal_load_lds_dwordx4 %0, %1" :: "v"(voff), "s"(sbase), "s"(ldsaddr) : "memory");
}
__device__ __forceinline__ void gldsc(const void* sbase, unsigned voff, unsigned ldsaddr){
  asm volatile("s_mov_b32 m0, %2\n\ts_nop 0\n\tglobal_load_lds_dwordx4 %0, %1 sc1" :: "v"(voff), "s"(sbase), "s"(ldsaddr) : "memory");
}
__device__ __forceinline__ void touch(const void* sbase, unsigned voff, unsigned ldsaddr){
  asm volatile("s_mov_b32 m0, %2\n\ts_nop 0\n\tglobal_load_lds_dword %0, %1" :: "v"(voff), "s"(sbase), "s"(ldsaddr) : "memory");
}
__device__ __forceinline__ void st16c(void* p, u32x4 v){ asm volatile("global_store_dwordx4 %0, %1, off sc1\n\ts_nop 1" :: "v"(p), "v"(v) : "memory"); }
__device__ __forceinline__ void st16cs(void* sbase, unsigned voff, u32x4 v){ asm volatile("global_store_dwordx4 %0, %1, %2 sc1\n\ts_nop 1" :: "v"(voff), "v"(v), "s"(sbase) : "memory"); }
#define GLB_AS __attribute__((address_space(1)))
__device__ __forceinline__ unsigned ldflag(const unsigned* p){ return __hip_atomic_load((const GLB_AS unsigned*)p, __ATOMIC_RELAXED, __HIP_MEMORY_SCOPE_AGENT); }
__device__ __forceinline__ void stflag(unsigned* p, unsigned v){ __hip_atomic_store((GLB_AS unsigned*)p, v, __ATOMIC_RELAXED, __HIP_MEMORY_SCOPE_AGENT); }
template<int SLP> __device__ __forceinline__ void wait_flag(const unsigned* p, unsigned tag){
  int spins=0;
  while(true){ unsigned v=ldflag(p); if(__all(v==tag)) break; __builtin_amdgcn_s_sleep(SLP); if(++spins>(1<<21)) break; }
}
__device__ __forceinline__ unsigned lds_addr(const void* p){ return (unsigned)(size_t)(const LDS_AS void*)p; }
#define WAITV(n) asm volatile("s_waitcnt vmcnt(" #n ")":::"memory")
#define WAITL() asm volatile("s_waitcnt lgkmcnt(0)":::"memory")
#define SBAR() asm volatile("s_barrier":::"memory")
#define PUBM(addr,val) asm volatile("ds_max_u32 %0, %1" :: "v"(addr), "v"(val) : "memory")
#define PUB(addr,val) asm volatile("ds_write_b32 %0, %1" :: "v"(addr), "v"(val) : "memory")
__device__ __forceinline__ s16x4 trread(const char* p){ return __builtin_amdgcn_ds_read_tr16_b64_v4i16((LDS_AS s16x4*)p); }
__device__ __forceinline__ bf16x8 cat44(s16x4 a, s16x4 b){ return __builtin_shufflevector(a,b,0,1,2,3,4,5,6,7); }
__device__ __forceinline__ int wswz(int row){ return ((row>>1)&1) | (((row>>3)&1)<<1); }
__device__ __forceinline__ u32x4 widen(u32x2 a, u32x2 b){
  auto r0=__builtin_amdgcn_permlane16_swap(a[0],b[0],false,false);
  auto r1=__builtin_amdgcn_permlane16_swap(a[1],b[1],false,false);
  return u32x4{r0[0],r1[0],r0[1],r1[1]};
}

constexpr int S=2048, DM=1024;
constexpr int WS=12288, XS=18432, X_OFF=6*WS;
constexpr int CREG=4608, C_OFF=131072;
constexpr int EX_OFF=C_OFF+4*CREG;
constexpr int SYN=EX_OFF+8192;
constexpr int SINK=SYN+256;
constexpr int CAR=SINK+768;
constexpr int ALL_LDS=CAR+4*1280;
constexpr int F_PP=1024, F_O=4096, F_Y=8192;
#define KV0 1
#define KVS 1
#define LPR 2
#define CPR 2
#define GPR 1
#define CGATE 2
#define OSLP 2
#define YSLP 2
#define TPX 16
#define TPW 2
#define TWO 0
#define CVP 1
#define G2P 3
#define OUTC 1
#define ROT 0
#define WTL 2
#define PB2 0
#define PREM 5
#define KTH 2
#define YDEF 0
#define OPOL 1
#define CODEL 264
#define MIX 0
#define LOCA 3
#define XLOC 1
#define PGT 0
#define PGO 0
#define QREM 0
#define EB 0
#define TW0 1
#define ORDM 1
#define NF 7u
#define NPOL 1
#define PD0 0
#define PD1 4
#define HH 0
#define HW 3
#define HKV 4
#define YPOL 1
#define YS2 2
#define OSH 4
#define HSK 3
#define HGR 2
#define KSH 0
#define EPI 0
#define YFL 0
#define CODEX 0
#define HLAST 1

#define N2_ASM \
  "v_mov_b64 v[64:65], 0\n\t" \
  "v_mov_b64 v[66:67], 0\n\t" \
  "v_mov_b64 v[68:69], 0\n\t" \
  "v_mov_b64 v[70:71], 0\n\t" \
  "v_mov_b64 v[72:73], 0\n\t" \
  "v_mov_b64 v[74:75], 0\n\t" \
  "v_mov_b64 v[76:77], 0\n\t" \
  "v_mov_b64 v[78:79], 0\n\t" \
  "v_mov_b64 v[80:81], 0\n\t" \
  "v_mov_b64 v[82:83], 0\n\t" \
  "v_mov_b64 v[84:85], 0\n\t" \
  "v_mov_b64 v[86:87], 0\n\t" \
  "v_mov_b64 v[88:89], 0\n\t" \
  "v_mov_b64 v[90:91], 0\n\t" \
  "v_mov_b64 v[92:93], 0\n\t" \
  "v_mov_b64 v[94:95], 0\n\t" \
  "v_mov_b64 v[96:97], 0\n\t" \
  "v_mov_b64 v[98:99], 0\n\t" \
  "v_mov_b64 v[100:101], 0\n\t" \
  "v_mov_b64 v[102:103], 0\n\t" \
  "v_mov_b64 v[104:105], 0\n\t" \
  "v_mov_b64 v[106:107], 0\n\t" \
  "v_mov_b64 v[108:109], 0\n\t" \
  "v_mov_b64 v[110:111], 0\n\t" \
  "v_mov_b64 v[112:113], 0\n\t" \
  "v_mov_b64 v[114:115], 0\n\t" \
  "v_mov_b64 v[116:117], 0\n\t" \
  "v_mov_b64 v[118:119], 0\n\t" \
  "v_mov_b64 v[120:121], 0\n\t" \
  "v_mov_b64 v[122:123], 0\n\t" \
  "v_mov_b64 v[124:125], 0\n\t" \
  "v_mov_b64 v[126:127], 0\n\t" \
  "v_mov_b64 v[128:129], 0\n\t" \
  "v_mov_b64 v[130:131], 0\n\t" \
  "v_mov_b64 v[132:133], 0\n\t" \
  "v_mov_b64 v[134:135], 0\n\t" \
  "v_mov_b64 v[136:137], 0\n\t" \
  "v_mov_b64 v[138:139], 0\n\t" \
  "v_mov_b64 v[140:141], 0\n\t" \
  "v_mov_b64 v[142:143], 0\n\t" \
  "v_mov_b64 v[144:145], 0\n\t" \
  "v_mov_b64 v[146:147], 0\n\t" \
  "v_mov_b64 v[148:149], 0\n\t" \
  "v_mov_b64 v[150:151], 0\n\t" \
  "v_mov_b64 v[152:153], 0\n\t" \
  "v_mov_b64 v[154:155], 0\n\t" \
  "v_mov_b64 v[156:157], 0\n\t" \
  "v_mov_b64 v[158:159], 0\n\t" \
  "v_mov_b64 v[160:161], 0\n\t" \
  "v_mov_b64 v[162:163], 0\n\t" \
  "v_mov_b64 v[164:165], 0\n\t" \
  "v_mov_b64 v[166:167], 0\n\t" \
  "v_add_u32 v60, 36864, v58\n\t" \
  "v_add_u32 v61, 36864, v59\n\t" \
  "s_mov_b32 %[it], 0\n\t" \
  "s_cmp_lg_u32 %[r], 0\n\t" \
  "s_barrier\n\t" \
  "s_cbranch_scc1 NB_%=\n\t" \
  "ds_read_b64_tr_b16 v[48:49], v60 offset:14336\n\t" \
  "ds_read_b64_tr_b16 v[50:51], v60 offset:14848\n\t" \
  "ds_read_b128 v[0:3], v56 offset:38912\n\t" \
  "ds_read_b64_tr_b16 v[52:53], v60 offset:26624\n\t" \
  "ds_read_b64_tr_b16 v[54:55], v60 offset:27136\n\t" \
  "ds_read_b128 v[16:19], v57 offset:38912\n\t" \
  "ds_read_b128 v[4:7], v56 offset:40960\n\t" \
  "ds_read_b128 v[20:23], v57 offset:40960\n\t" \
  "ds_read_b128 v[8:11], v56 offset:43008\n\t" \
  "ds_read_b128 v[24:27], v57 offset:43008\n\t" \
  "ds_read_b128 v[12:15], v56 offset:45056\n\t" \
  "ds_read_b128 v[28:31], v57 offset:45056\n\t" \
  "ds_read_b64_tr_b16 v[32:33], v60 offset:13312\n\t" \
  "ds_read_b64_tr_b16 v[34:35], v60 offset:13824\n\t" \
  "s_mov_b32 s68, 0x50000\n\t" \
  "s_mov_b32 s69, 0x5550055\n\t" \
  "s_mov_b32 s70, 0xa0000\n\t" \
  "s_mov_b32 s71, 0xaaa00aa\n\t" \
  "s_mov_b32 s72, 0x150001\n\t" \
  "s_mov_b32 s73, 0x15550155\n\t" \
  "s_mov_b32 s74, 0x2a0002\n\t" \
  "s_mov_b32 s75, 0x2aaa02aa\n\t" \
  "s_mov_b32 s76, 0x55505555\n\t" \
  "s_mov_b32 s77, 0x50005500\n\t" \
  "s_mov_b32 s78, 0xaaa0aaaa\n\t" \
  "s_mov_b32 s79, 0xa000aa00\n\t" \
  "s_mov_b32 s80, 0x55405554\n\t" \
  "s_mov_b32 s81, 0x40005400\n\t" \
  "s_mov_b32 s82, 0xaa80aaa8\n\t" \
  "s_mov_b32 s83, 0x8000a800\n\t" \
  "s_mov_b32 s84, 0xaaaaaaaa\n\t" \
  "s_mov_b32 s85, 0xaaaaaaaa\n\t" \
  "s_waitcnt lgkmcnt(0)\n\t" \
  "s_branch N2a_%=\n\t" \
  "N0a_%=:\n\t" \
  "s_waitcnt lgkmcnt(10)\n\t" \
  "v_mfma_f32_16x16x32_bf16 v[128:131], v[0:3], v[32:35], v[128:131]\n\t" \
  "ds_read_b64_tr_b16 v[44:45], v58 offset:13312\n\t" \
  "s_waitcnt lgkmcnt(7)\n\t" \
  "v_mfma_f32_16x16x32_bf16 v[128:131], v[16:19], v[36:39], v[128:131]\n\t" \
  "ds_read_b64_tr_b16 v[46:47], v58 offset:13824\n\t" \
  "v_mfma_f32_16x16x32_bf16 v[136:139], v[4:7], v[32:35], v[136:139]\n\t" \
  "ds_read_b128 v[48:51], v56 offset:0\n\t" \
  "v_mfma_f32_16x16x32_bf16 v[136:139], v[20:23], v[36:39], v[136:139]\n\t" \
  "ds_read_b128 v[52:55], v57 offset:0\n\t" \
  "s_waitcnt lgkmcnt(8)\n\t" \
  "v_mfma_f32_16x16x32_bf16 v[144:147], v[8:11], v[32:35], v[144:147]\n\t" \
  "v_mfma_f32_16x16x32_bf16 v[144:147], v[24:27], v[36:39], v[144:147]\n\t" \
  "s_waitcnt lgkmcnt(4)\n\t" \
  "v_mfma_f32_16x16x32_bf16 v[152:155], v[12:15], v[32:35], v[152:155]\n\t" \
  "v_mfma_f32_16x16x32_bf16 v[152:155], v[28:31], v[36:39], v[152:155]\n\t" \
  "ds_read_b64_tr_b16 v[32:33], v59 offset:1024\n\t" \
  "v_mfma_f32_16x16x32_bf16 v[96:99], v[40:43], v[0:3], v[96:99]\n\t" \
  "ds_read_b64_tr_b16 v[34:35], v59 offset:1536\n\t" \
  "s_waitcnt lgkmcnt(4)\n\t" \
  "v_mfma_f32_16x16x32_bf16 v[96:99], v[44:47], v[16:19], v[96:99]\n\t" \
  "ds_read_b64_tr_b16 v[36:37], v59 offset:13312\n\t" \
  "v_mfma_f32_16x16x32_bf16 v[104:107], v[40:43], v[4:7], v[104:107]\n\t" \
  "v_mfma_f32_16x16x32_bf16 v[104:107], v[44:47], v[20:23], v[104:107]\n\t" \
  "ds_read_b64_tr_b16 v[38:39], v59 offset:13824\n\t" \
  "v_mfma_f32_16x16x32_bf16 v[112:115], v[40:43], v[8:11], v[112:115]\n\t" \
  "v_mfma_f32_16x16x32_bf16 v[112:115], v[44:47], v[24:27], v[112:115]\n\t" \
  "v_mfma_f32_16x16x32_bf16 v[120:123], v[40:43], v[12:15], v[120:123]\n\t" \
  "v_mfma_f32_16x16x32_bf16 v[120:123], v[44:47], v[28:31], v[120:123]\n\t" \
  "s_waitcnt lgkmcnt(4)\n\t" \
  "v_mfma_f32_16x16x32_bf16 v[160:163], v[40:43], v[48:51], v[160:163]\n\t" \
  "v_mfma_f32_16x16x32_bf16 v[160:163], v[44:47], v[52:55], v[160:163]\n\t" \
  "ds_read_b64_tr_b16 v[40:41], v59 offset:2048\n\t" \
  "s_waitcnt lgkmcnt(1)\n\t" \
  "v_mfma_f32_16x16x32_bf16 v[164:167], v[32:35], v[48:51], v[164:167]\n\t" \
  "ds_read_b64_tr_b16 v[42:43], v59 offset:2560\n\t" \
  "v_mfma_f32_16x16x32_bf16 v[164:167], v[36:39], v[52:55], v[164:167]\n\t" \
  "ds_read_b64_tr_b16 v[44:45], v59 offset:14336\n\t" \
  "v_mfma_f32_16x16x32_bf16 v[100:103], v[32:35], v[0:3], v[100:103]\n\t" \
  "ds_read_b64_tr_b16 v[46:47], v59 offset:14848\n\t" \
  "v_mfma_f32_16x16x32_bf16 v[100:103], v[36:39], v[16:19], v[100:103]\n\t" \
  "ds_read_b64_tr_b16 v[48:49], v58 offset:0\n\t" \
  "v_mfma_f32_16x16x32_bf16 v[108:111], v[32:35], v[4:7], v[108:111]\n\t" \
  "ds_read_b64_tr_b16 v[50:51], v58 offset:512\n\t" \
  "v_mfma_f32_16x16x32_bf16 v[108:111], v[36:39], v[20:23], v[108:111]\n\t" \
  "ds_read_b64_tr_b16 v[52:53], v58 offset:12288\n\t" \
  "v_mfma_f32_16x16x32_bf16 v[116:119], v[32:35], v[8:11], v[116:119]\n\t" \
  "ds_read_b64_tr_b16 v[54:55], v58 offset:12800\n\t" \
  "v_mfma_f32_16x16x32_bf16 v[116:119], v[36:39], v[24:27], v[116:119]\n\t" \
  "v_mfma_f32_16x16x32_bf16 v[124:127], v[32:35], v[12:15], v[124:127]\n\t" \
  "ds_read_b64_tr_b16 v[32:33], v59 offset:0\n\t" \
  "v_mfma_f32_16x16x32_bf16 v[124:127], v[36:39], v[28:31], v[124:127]\n\t" \
  "ds_read_b64_tr_b16 v[34:35], v59 offset:512\n\t" \
  "s_waitcnt lgkmcnt(6)\n\t" \
  "v_mfma_f32_16x16x32_bf16 v[132:135], v[0:3], v[40:43], v[132:135]\n\t" \
  "ds_read_b64_tr_b16 v[36:37], v59 offset:12288\n\t" \
  "v_mfma_f32_16x16x32_bf16 v[132:135], v[16:19], v[44:47], v[132:135]\n\t" \
  "ds_read_b64_tr_b16 v[38:39], v59 offset:12800\n\t" \
  "v_mfma_f32_16x16x32_bf16 v[140:143], v[4:7], v[40:43], v[140:143]\n\t" \
  "v_mfma_f32_16x16x32_bf16 v[140:143], v[20:23], v[44:47], v[140:143]\n\t" \
  "v_mfma_f32_16x16x32_bf16 v[148:151], v[8:11], v[40:43], v[148:151]\n\t" \
  "v_mfma_f32_16x16x32_bf16 v[148:151], v[24:27], v[44:47], v[148:151]\n\t" \
  "v_mfma_f32_16x16x32_bf16 v[156:159], v[12:15], v[40:43], v[156:159]\n\t" \
  "v_mfma_f32_16x16x32_bf16 v[156:159], v[28:31], v[44:47], v[156:159]\n\t" \
  "s_waitcnt lgkmcnt(0)\n\t" \
  "s_barrier\n\t" \
  "v_mfma_f32_16x16x32_bf16 v[64:67], v[48:51], v[0:3], v[64:67]\n\t" \
  "v_mfma_f32_16x16x32_bf16 v[64:67], v[52:55], v[16:19], v[64:67]\n\t" \
  "ds_read_b64_tr_b16 v[40:41], v58 offset:26624\n\t" \
  "v_mfma_f32_16x16x32_bf16 v[68:71], v[32:35], v[0:3], v[68:71]\n\t" \
  "ds_read_b128 v[0:3], v56 offset:20480\n\t" \
  "v_mfma_f32_16x16x32_bf16 v[68:71], v[36:39], v[16:19], v[68:71]\n\t" \
  "ds_read_b128 v[16:19], v57 offset:20480\n\t" \
  "v_mfma_f32_16x16x32_bf16 v[72:75], v[48:51], v[4:7], v[72:75]\n\t" \
  "ds_read_b64_tr_b16 v[42:43], v58 offset:27136\n\t" \
  "v_mfma_f32_16x16x32_bf16 v[72:75], v[52:55], v[20:23], v[72:75]\n\t" \
  "ds_read_b64_tr_b16 v[44:45], v60 offset:2048\n\t" \
  "v_mfma_f32_16x16x32_bf16 v[76:79], v[32:35], v[4:7], v[76:79]\n\t" \
  "ds_read_b128 v[4:7], v56 offset:22528\n\t" \
  "v_mfma_f32_16x16x32_bf16 v[76:79], v[36:39], v[20:23], v[76:79]\n\t" \
  "ds_read_b128 v[20:23], v57 offset:22528\n\t" \
  "v_mfma_f32_16x16x32_bf16 v[80:83], v[48:51], v[8:11], v[80:83]\n\t" \
  "ds_read_b64_tr_b16 v[46:47], v60 offset:2560\n\t" \
  "v_mfma_f32_16x16x32_bf16 v[80:83], v[52:55], v[24:27], v[80:83]\n\t" \
  "v_mfma_f32_16x16x32_bf16 v[84:87], v[32:35], v[8:11], v[84:87]\n\t" \
  "ds_read_b128 v[8:11], v56 offset:24576\n\t" \
  "v_mfma_f32_16x16x32_bf16 v[84:87], v[36:39], v[24:27], v[84:87]\n\t" \
  "ds_read_b128 v[24:27], v57 offset:24576\n\t" \
  "v_mfma_f32_16x16x32_bf16 v[88:91], v[48:51], v[12:15], v[88:91]\n\t" \
  "ds_read_b64_tr_b16 v[48:49], v58 offset:25600\n\t" \
  "v_mfma_f32_16x16x32_bf16 v[88:91], v[52:55], v[28:31], v[88:91]\n\t" \
  "ds_read_b64_tr_b16 v[50:51], v58 offset:26112\n\t" \
  "v_mfma_f32_16x16x32_bf16 v[92:95], v[32:35], v[12:15], v[92:95]\n\t" \
  "ds_read_b128 v[12:15], v56 offset:26624\n\t" \
  "v_mfma_f32_16x16x32_bf16 v[92:95], v[36:39], v[28:31], v[92:95]\n\t" \
  "ds_read_b128 v[28:31], v57 offset:26624\n\t" \
  "N1a_%=:\n\t" \
  "s_waitcnt lgkmcnt(10)\n\t" \
  "v_mfma_f32_16x16x32_bf16 v[128:131], v[0:3], v[40:43], v[128:131]\n\t" \
  "ds_read_b64_tr_b16 v[52:53], v60 offset:1024\n\t" \
  "s_waitcnt lgkmcnt(7)\n\t" \
  "v_mfma_f32_16x16x32_bf16 v[128:131], v[16:19], v[44:47], v[128:131]\n\t" \
  "ds_read_b64_tr_b16 v[54:55], v60 offset:1536\n\t" \
  "v_mfma_f32_16x16x32_bf16 v[136:139], v[4:7], v[40:43], v[136:139]\n\t" \
  "ds_read_b128 v[32:35], v56 offset:18432\n\t" \
  "v_mfma_f32_16x16x32_bf16 v[136:139], v[20:23], v[44:47], v[136:139]\n\t" \
  "ds_read_b128 v[36:39], v57 offset:18432\n\t" \
  "s_waitcnt lgkmcnt(8)\n\t" \
  "v_mfma_f32_16x16x32_bf16 v[144:147], v[8:11], v[40:43], v[144:147]\n\t" \
  "v_mfma_f32_16x16x32_bf16 v[144:147], v[24:27], v[44:47], v[144:147]\n\t" \
  "s_waitcnt lgkmcnt(4)\n\t" \
  "v_mfma_f32_16x16x32_bf16 v[152:155], v[12:15], v[40:43], v[152:155]\n\t" \
  "v_mfma_f32_16x16x32_bf16 v[152:155], v[28:31], v[44:47], v[152:155]\n\t" \
  "ds_read_b64_tr_b16 v[40:41], v59 offset:25600\n\t" \
  "v_mfma_f32_16x16x32_bf16 v[96:99], v[48:51], v[0:3], v[96:99]\n\t" \
  "ds_read_b64_tr_b16 v[42:43], v59 offset:26112\n\t" \
  "s_waitcnt lgkmcnt(4)\n\t" \
  "v_mfma_f32_16x16x32_bf16 v[96:99], v[52:55], v[16:19], v[96:99]\n\t" \
  "ds_read_b64_tr_b16 v[44:45], v61 offset:1024\n\t" \
  "v_mfma_f32_16x16x32_bf16 v[104:107], v[48:51], v[4:7], v[104:107]\n\t" \
  "v_mfma_f32_16x16x32_bf16 v[104:107], v[52:55], v[20:23], v[104:107]\n\t" \
  "ds_read_b64_tr_b16 v[46:47], v61 offset:1536\n\t" \
  "v_mfma_f32_16x16x32_bf16 v[112:115], v[48:51], v[8:11], v[112:115]\n\t" \
  "v_mfma_f32_16x16x32_bf16 v[112:115], v[52:55], v[24:27], v[112:115]\n\t" \
  "v_mfma_f32_16x16x32_bf16 v[120:123], v[48:51], v[12:15], v[120:123]\n\t" \
  "v_mfma_f32_16x16x32_bf16 v[120:123], v[52:55], v[28:31], v[120:123]\n\t" \
  "s_waitcnt lgkmcnt(4)\n\t" \
  "v_mfma_f32_16x16x32_bf16 v[160:163], v[48:51], v[32:35], v[160:163]\n\t" \
  "v_mfma_f32_16x16x32_bf16 v[160:163], v[52:55], v[36:39], v[160:163]\n\t" \
  "ds_read_b64_tr_b16 v[48:49], v59 offset:26624\n\t" \
  "s_waitcnt lgkmcnt(1)\n\t" \
  "v_mfma_f32_16x16x32_bf16 v[164:167], v[40:43], v[32:35], v[164:167]\n\t" \
  "ds_read_b64_tr_b16 v[50:51], v59 offset:27136\n\t" \
  "v_mfma_f32_16x16x32_bf16 v[164:167], v[44:47], v[36:39], v[164:167]\n\t" \
  "ds_read_b64_tr_b16 v[52:53], v61 offset:2048\n\t" \
  "v_mfma_f32_16x16x32_bf16 v[100:103], v[40:43], v[0:3], v[100:103]\n\t" \
  "ds_read_b64_tr_b16 v[54:55], v61 offset:2560\n\t" \
  "v_mfma_f32_16x16x32_bf16 v[100:103], v[44:47], v[16:19], v[100:103]\n\t" \
  "ds_read_b64_tr_b16 v[32:33], v58 offset:24576\n\t" \
  "v_mfma_f32_16x16x32_bf16 v[108:111], v[40:43], v[4:7], v[108:111]\n\t" \
  "ds_read_b64_tr_b16 v[34:35], v58 offset:25088\n\t" \
  "v_mfma_f32_16x16x32_bf16 v[108:111], v[44:47], v[20:23], v[108:111]\n\t" \
  "ds_read_b64_tr_b16 v[36:37], v60 offset:0\n\t" \
  "v_mfma_f32_16x16x32_bf16 v[116:119], v[40:43], v[8:11], v[116:119]\n\t" \
  "ds_read_b64_tr_b16 v[38:39], v60 offset:512\n\t" \
  "v_mfma_f32_16x16x32_bf16 v[116:119], v[44:47], v[24:27], v[116:119]\n\t" \
  "v_mfma_f32_16x16x32_bf16 v[124:127], v[40:43], v[12:15], v[124:127]\n\t" \
  "ds_read_b64_tr_b16 v[40:41], v59 offset:24576\n\t" \
  "v_mfma_f32_16x16x32_bf16 v[124:127], v[44:47], v[28:31], v[124:127]\n\t" \
  "ds_read_b64_tr_b16 v[42:43], v59 offset:25088\n\t" \
  "s_waitcnt lgkmcnt(6)\n\t" \
  "v_mfma_f32_16x16x32_bf16 v[132:135], v[0:3], v[48:51], v[132:135]\n\t" \
  "ds_read_b64_tr_b16 v[44:45], v61 offset:0\n\t" \
  "v_mfma_f32_16x16x32_bf16 v[132:135], v[16:19], v[52:55], v[132:135]\n\t" \
  "ds_read_b64_tr_b16 v[46:47], v61 offset:512\n\t" \
  "v_mfma_f32_16x16x32_bf16 v[140:143], v[4:7], v[48:51], v[140:143]\n\t" \
  "v_mfma_f32_16x16x32_bf16 v[140:143], v[20:23], v[52:55], v[140:143]\n\t" \
  "v_mfma_f32_16x16x32_bf16 v[148:151], v[8:11], v[48:51], v[148:151]\n\t" \
  "v_mfma_f32_16x16x32_bf16 v[148:151], v[24:27], v[52:55], v[148:151]\n\t" \
  "v_mfma_f32_16x16x32_bf16 v[156:159], v[12:15], v[48:51], v[156:159]\n\t" \
  "v_mfma_f32_16x16x32_bf16 v[156:159], v[28:31], v[52:55], v[156:159]\n\t" \
  "s_waitcnt lgkmcnt(0)\n\t" \
  "s_barrier\n\t" \
  "v_mfma_f32_16x16x32_bf16 v[64:67], v[32:35], v[0:3], v[64:67]\n\t" \
  "v_mfma_f32_16x16x32_bf16 v[64:67], v[36:39], v[16:19], v[64:67]\n\t" \
  "ds_read_b64_tr_b16 v[48:49], v60 offset:14336\n\t" \
  "v_mfma_f32_16x16x32_bf16 v[68:71], v[40:43], v[0:3], v[68:71]\n\t" \
  "ds_read_b128 v[0:3], v56 offset:38912\n\t" \
  "v_mfma_f32_16x16x32_bf16 v[68:71], v[44:47], v[16:19], v[68:71]\n\t" \
  "ds_read_b128 v[16:19], v57 offset:38912\n\t" \
  "v_mfma_f32_16x16x32_bf16 v[72:75], v[32:35], v[4:7], v[72:75]\n\t" \
  "ds_read_b64_tr_b16 v[50:51], v60 offset:14848\n\t" \
  "v_mfma_f32_16x16x32_bf16 v[72:75], v[36:39], v[20:23], v[72:75]\n\t" \
  "ds_read_b64_tr_b16 v[52:53], v60 offset:26624\n\t" \
  "v_mfma_f32_16x16x32_bf16 v[76:79], v[40:43], v[4:7], v[76:79]\n\t" \
  "ds_read_b128 v[4:7], v56 offset:40960\n\t" \
  "v_mfma_f32_16x16x32_bf16 v[76:79], v[44:47], v[20:23], v[76:79]\n\t" \
  "ds_read_b128 v[20:23], v57 offset:40960\n\t" \
  "v_mfma_f32_16x16x32_bf16 v[80:83], v[32:35], v[8:11], v[80:83]\n\t" \
  "ds_read_b64_tr_b16 v[54:55], v60 offset:27136\n\t" \
  "v_mfma_f32_16x16x32_bf16 v[80:83], v[36:39], v[24:27], v[80:83]\n\t" \
  "v_mfma_f32_16x16x32_bf16 v[84:87], v[40:43], v[8:11], v[84:87]\n\t" \
  "ds_read_b128 v[8:11], v56 offset:43008\n\t" \
  "v_mfma_f32_16x16x32_bf16 v[84:87], v[44:47], v[24:27], v[84:87]\n\t" \
  "ds_read_b128 v[24:27], v57 offset:43008\n\t" \
  "v_mfma_f32_16x16x32_bf16 v[88:91], v[32:35], v[12:15], v[88:91]\n\t" \
  "ds_read_b64_tr_b16 v[32:33], v60 offset:13312\n\t" \
  "v_mfma_f32_16x16x32_bf16 v[88:91], v[36:39], v[28:31], v[88:91]\n\t" \
  "ds_read_b64_tr_b16 v[34:35], v60 offset:13824\n\t" \
  "v_mfma_f32_16x16x32_bf16 v[92:95], v[40:43], v[12:15], v[92:95]\n\t" \
  "ds_read_b128 v[12:15], v56 offset:45056\n\t" \
  "v_mfma_f32_16x16x32_bf16 v[92:95], v[44:47], v[28:31], v[92:95]\n\t" \
  "ds_read_b128 v[28:31], v57 offset:45056\n\t" \
  "N2a_%=:\n\t" \
  "s_waitcnt lgkmcnt(10)\n\t" \
  "v_mfma_f32_16x16x32_bf16 v[128:131], v[0:3], v[48:51], v[128:131]\n\t" \
  "ds_read_b64_tr_b16 v[36:37], v60 offset:25600\n\t" \
  "s_waitcnt lgkmcnt(7)\n\t" \
  "v_mfma_f32_16x16x32_bf16 v[128:131], v[16:19], v[52:55], v[128:131]\n\t" \
  "ds_read_b64_tr_b16 v[38:39], v60 offset:26112\n\t" \
  "v_mfma_f32_16x16x32_bf16 v[136:139], v[4:7], v[48:51], v[136:139]\n\t" \
  "ds_read_b128 v[40:43], v56 offset:36864\n\t" \
  "v_mfma_f32_16x16x32_bf16 v[136:139], v[20:23], v[52:55], v[136:139]\n\t" \
  "ds_read_b128 v[44:47], v57 offset:36864\n\t" \
  "s_waitcnt lgkmcnt(8)\n\t" \
  "v_mfma_f32_16x16x32_bf16 v[144:147], v[8:11], v[48:51], v[144:147]\n\t" \
  "v_mfma_f32_16x16x32_bf16 v[144:147], v[24:27], v[52:55], v[144:147]\n\t" \
  "s_waitcnt lgkmcnt(4)\n\t" \
  "v_mfma_f32_16x16x32_bf16 v[152:155], v[12:15], v[48:51], v[152:155]\n\t" \
  "v_mfma_f32_16x16x32_bf16 v[152:155], v[28:31], v[52:55], v[152:155]\n\t" \
  "ds_read_b64_tr_b16 v[48:49], v61 offset:13312\n\t" \
  "v_mfma_f32_16x16x32_bf16 v[96:99], v[32:35], v[0:3], v[96:99]\n\t" \
  "ds_read_b64_tr_b16 v[50:51], v61 offset:13824\n\t" \
  "s_waitcnt lgkmcnt(4)\n\t" \
  "v_mfma_f32_16x16x32_bf16 v[96:99], v[36:39], v[16:19], v[96:99]\n\t" \
  "ds_read_b64_tr_b16 v[52:53], v61 offset:25600\n\t" \
  "v_mfma_f32_16x16x32_bf16 v[104:107], v[32:35], v[4:7], v[104:107]\n\t" \
  "v_mfma_f32_16x16x32_bf16 v[104:107], v[36:39], v[20:23], v[104:107]\n\t" \
  "ds_read_b64_tr_b16 v[54:55], v61 offset:26112\n\t" \
  "v_mfma_f32_16x16x32_bf16 v[112:115], v[32:35], v[8:11], v[112:115]\n\t" \
  "v_mfma_f32_16x16x32_bf16 v[112:115], v[36:39], v[24:27], v[112:115]\n\t" \
  "v_mfma_f32_16x16x32_bf16 v[120:123], v[32:35], v[12:15], v[120:123]\n\t" \
  "v_mfma_f32_16x16x32_bf16 v[120:123], v[36:39], v[28:31], v[120:123]\n\t" \
  "s_waitcnt lgkmcnt(4)\n\t" \
  "v_mfma_f32_16x16x32_bf16 v[160:163], v[32:35], v[40:43], v[160:163]\n\t" \
  "v_mfma_f32_16x16x32_bf16 v[160:163], v[36:39], v[44:47], v[160:163]\n\t" \
  "ds_read_b64_tr_b16 v[32:33], v61 offset:14336\n\t" \
  "s_waitcnt lgkmcnt(1)\n\t" \
  "v_mfma_f32_16x16x32_bf16 v[164:167], v[48:51], v[40:43], v[164:167]\n\t" \
  "ds_read_b64_tr_b16 v[34:35], v61 offset:14848\n\t" \
  "v_mfma_f32_16x16x32_bf16 v[164:167], v[52:55], v[44:47], v[164:167]\n\t" \
  "ds_read_b64_tr_b16 v[36:37], v61 offset:26624\n\t" \
  "v_mfma_f32_16x16x32_bf16 v[100:103], v[48:51], v[0:3], v[100:103]\n\t" \
  "ds_read_b64_tr_b16 v[38:39], v61 offset:27136\n\t" \
  "v_mfma_f32_16x16x32_bf16 v[100:103], v[52:55], v[16:19], v[100:103]\n\t" \
  "ds_read_b64_tr_b16 v[40:41], v60 offset:12288\n\t" \
  "v_mfma_f32_16x16x32_bf16 v[108:111], v[48:51], v[4:7], v[108:111]\n\t" \
  "ds_read_b64_tr_b16 v[42:43], v60 offset:12800\n\t" \
  "v_mfma_f32_16x16x32_bf16 v[108:111], v[52:55], v[20:23], v[108:111]\n\t" \
  "ds_read_b64_tr_b16 v[44:45], v60 offset:24576\n\t" \
  "v_mfma_f32_16x16x32_bf16 v[116:119], v[48:51], v[8:11], v[116:119]\n\t" \
  "ds_read_b64_tr_b16 v[46:47], v60 offset:25088\n\t" \
  "v_mfma_f32_16x16x32_bf16 v[116:119], v[52:55], v[24:27], v[116:119]\n\t" \
  "v_mfma_f32_16x16x32_bf16 v[124:127], v[48:51], v[12:15], v[124:127]\n\t" \
  "ds_read_b64_tr_b16 v[48:49], v61 offset:12288\n\t" \
  "v_mfma_f32_16x16x32_bf16 v[124:127], v[52:55], v[28:31], v[124:127]\n\t" \
  "ds_read_b64_tr_b16 v[50:51], v61 offset:12800\n\t" \
  "s_waitcnt lgkmcnt(6)\n\t" \
  "v_mfma_f32_16x16x32_bf16 v[132:135], v[0:3], v[32:35], v[132:135]\n\t" \
  "ds_read_b64_tr_b16 v[52:53], v61 offset:24576\n\t" \
  "v_mfma_f32_16x16x32_bf16 v[132:135], v[16:19], v[36:39], v[132:135]\n\t" \
  "ds_read_b64_tr_b16 v[54:55], v61 offset:25088\n\t" \
  "v_mfma_f32_16x16x32_bf16 v[140:143], v[4:7], v[32:35], v[140:143]\n\t" \
  "v_mfma_f32_16x16x32_bf16 v[140:143], v[20:23], v[36:39], v[140:143]\n\t" \
  "s_addk_i32 %[it], 1\n\t" \
  "v_mfma_f32_16x16x32_bf16 v[148:151], v[8:11], v[32:35], v[148:151]\n\t" \
  "s_cmp_lt_u32 %[it], 6\n\t" \
  "v_mfma_f32_16x16x32_bf16 v[148:151], v[24:27], v[36:39], v[148:151]\n\t" \
  "v_mfma_f32_16x16x32_bf16 v[156:159], v[12:15], v[32:35], v[156:159]\n\t" \
  "v_mfma_f32_16x16x32_bf16 v[156:159], v[28:31], v[36:39], v[156:159]\n\t" \
  "s_waitcnt lgkmcnt(0)\n\t" \
  "s_barrier\n\t" \
  "s_cbranch_scc0 NTa_%=\n\t" \
  "v_mfma_f32_16x16x32_bf16 v[64:67], v[40:43], v[0:3], v[64:67]\n\t" \
  "v_mfma_f32_16x16x32_bf16 v[64:67], v[44:47], v[16:19], v[64:67]\n\t" \
  "ds_read_b64_tr_b16 v[32:33], v58 offset:2048\n\t" \
  "v_mfma_f32_16x16x32_bf16 v[68:71], v[48:51], v[0:3], v[68:71]\n\t" \
  "ds_read_b128 v[0:3], v56 offset:2048\n\t" \
  "v_mfma_f32_16x16x32_bf16 v[68:71], v[52:55], v[16:19], v[68:71]\n\t" \
  "ds_read_b128 v[16:19], v57 offset:2048\n\t" \
  "v_mfma_f32_16x16x32_bf16 v[72:75], v[40:43], v[4:7], v[72:75]\n\t" \
  "ds_read_b64_tr_b16 v[34:35], v58 offset:2560\n\t" \
  "v_mfma_f32_16x16x32_bf16 v[72:75], v[44:47], v[20:23], v[72:75]\n\t" \
  "ds_read_b64_tr_b16 v[36:37], v58 offset:14336\n\t" \
  "v_mfma_f32_16x16x32_bf16 v[76:79], v[48:51], v[4:7], v[76:79]\n\t" \
  "ds_read_b128 v[4:7], v56 offset:4096\n\t" \
  "v_mfma_f32_16x16x32_bf16 v[76:79], v[52:55], v[20:23], v[76:79]\n\t" \
  "ds_read_b128 v[20:23], v57 offset:4096\n\t" \
  "v_mfma_f32_16x16x32_bf16 v[80:83], v[40:43], v[8:11], v[80:83]\n\t" \
  "ds_read_b64_tr_b16 v[38:39], v58 offset:14848\n\t" \
  "v_mfma_f32_16x16x32_bf16 v[80:83], v[44:47], v[24:27], v[80:83]\n\t" \
  "v_mfma_f32_16x16x32_bf16 v[84:87], v[48:51], v[8:11], v[84:87]\n\t" \
  "ds_read_b128 v[8:11], v56 offset:6144\n\t" \
  "v_mfma_f32_16x16x32_bf16 v[84:87], v[52:55], v[24:27], v[84:87]\n\t" \
  "ds_read_b128 v[24:27], v57 offset:6144\n\t" \
  "v_mfma_f32_16x16x32_bf16 v[88:91], v[40:43], v[12:15], v[88:91]\n\t" \
  "ds_read_b64_tr_b16 v[40:41], v58 offset:1024\n\t" \
  "v_mfma_f32_16x16x32_bf16 v[88:91], v[44:47], v[28:31], v[88:91]\n\t" \
  "ds_read_b64_tr_b16 v[42:43], v58 offset:1536\n\t" \
  "v_mfma_f32_16x16x32_bf16 v[92:95], v[48:51], v[12:15], v[92:95]\n\t" \
  "ds_read_b128 v[12:15], v56 offset:8192\n\t" \
  "v_mfma_f32_16x16x32_bf16 v[92:95], v[52:55], v[28:31], v[92:95]\n\t" \
  "ds_read_b128 v[28:31], v57 offset:8192\n\t" \
  "s_branch N0a_%=\n\t" \
  "NTa_%=:\n\t" \
  "s_setprio 3\n\t" \
  "v_mfma_f32_16x16x32_bf16 v[88:91], v[40:43], v[12:15], v[88:91]\n\t" \
  "v_cvt_pk_bf16_f32 v120, v120, v121\n\t" \
  "v_cvt_pk_bf16_f32 v121, v122, v123\n\t" \
  "v_mfma_f32_16x16x32_bf16 v[88:91], v[44:47], v[28:31], v[88:91]\n\t" \
  "v_cvt_pk_bf16_f32 v122, v124, v125\n\t" \
  "v_cvt_pk_bf16_f32 v123, v126, v127\n\t" \
  "v_mfma_f32_16x16x32_bf16 v[92:95], v[48:51], v[12:15], v[92:95]\n\t" \
  "v_mov_b32 v57, %[fa]\n\t" \
  "v_mov_b32 v58, 1\n\t" \
  "v_mfma_f32_16x16x32_bf16 v[92:95], v[52:55], v[28:31], v[92:95]\n\t" \
  "ds_write_b128 v63, v[120:123]\n\t" \
  "v_cvt_pk_bf16_f32 v38, v156, v157\n\t" \
  "v_mfma_f32_16x16x32_bf16 v[80:83], v[40:43], v[8:11], v[80:83]\n\t" \
  "v_cvt_pk_bf16_f32 v39, v158, v159\n\t" \
  "v_cvt_pk_bf16_f32 v28, v152, v153\n\t" \
  "v_mfma_f32_16x16x32_bf16 v[80:83], v[44:47], v[24:27], v[80:83]\n\t" \
  "v_cvt_pk_bf16_f32 v29, v154, v155\n\t" \
  "v_mov_b32 v60, 0xff61b1e6\n\t" \
  "v_mfma_f32_16x16x32_bf16 v[84:87], v[48:51], v[8:11], v[84:87]\n\t" \
  "ds_write2_b64 v63, v[28:29], v[38:39] offset0:128 offset1:129\n\t" \
  "s_mov_b64 exec, 1\n\t" \
  "ds_write_b32 v57, v58\n\t" \
  "s_mov_b64 exec, -1\n\t" \
  "v_mfma_f32_16x16x32_bf16 v[84:87], v[52:55], v[24:27], v[84:87]\n\t" \
  "v_cvt_pk_bf16_f32 v160, v160, v161\n\t" \
  "v_cvt_pk_bf16_f32 v161, v162, v163\n\t" \
  "v_mfma_f32_16x16x32_bf16 v[72:75], v[40:43], v[4:7], v[72:75]\n\t" \
  "v_cvt_pk_bf16_f32 v162, v164, v165\n\t" \
  "v_cvt_pk_bf16_f32 v163, v166, v167\n\t" \
  "v_mfma_f32_16x16x32_bf16 v[72:75], v[44:47], v[20:23], v[72:75]\n\t" \
  "v_cvt_pk_bf16_f32 v112, v112, v113\n\t" \
  "v_cvt_pk_bf16_f32 v113, v114, v115\n\t" \
  "v_mfma_f32_16x16x32_bf16 v[76:79], v[48:51], v[4:7], v[76:79]\n\t" \
  "v_cvt_pk_bf16_f32 v114, v116, v117\n\t" \
  "v_cvt_pk_bf16_f32 v115, v118, v119\n\t" \
  "v_mfma_f32_16x16x32_bf16 v[76:79], v[52:55], v[20:23], v[76:79]\n\t" \
  "v_cndmask_b32_e64 v12, v60, 0, s[76:77]\n\t" \
  "v_cndmask_b32_e64 v13, v60, 0, s[78:79]\n\t" \
  "v_mfma_f32_16x16x32_bf16 v[64:67], v[40:43], v[0:3], v[64:67]\n\t" \
  "v_cndmask_b32_e64 v14, v60, 0, s[80:81]\n\t" \
  "v_cndmask_b32_e64 v15, v60, 0, s[82:83]\n\t" \
  "v_mfma_f32_16x16x32_bf16 v[64:67], v[44:47], v[16:19], v[64:67]\n\t" \
  "v_cndmask_b32_e64 v56, v60, 0, s[68:69]\n\t" \
  "v_cndmask_b32_e64 v57, v60, 0, s[70:71]\n\t" \
  "v_mfma_f32_16x16x32_bf16 v[68:71], v[48:51], v[0:3], v[68:71]\n\t" \
  "v_cndmask_b32_e64 v58, v60, 0, s[72:73]\n\t" \
  "v_cndmask_b32_e64 v59, v60, 0, s[74:75]\n\t" \
  "v_mfma_f32_16x16x32_bf16 v[68:71], v[52:55], v[16:19], v[68:71]\n\t" \
  "s_branch NX_%=\n\t" \
  "NB_%=:\n\t" \
  "ds_read_b64_tr_b16 v[48:49], v60 offset:13312\n\t" \
  "ds_read_b64_tr_b16 v[50:51], v60 offset:13824\n\t" \
  "ds_read_b128 v[0:3], v56 offset:47104\n\t" \
  "ds_read_b64_tr_b16 v[52:53], v60 offset:25600\n\t" \
  "ds_read_b64_tr_b16 v[54:55], v60 offset:26112\n\t" \
  "ds_read_b128 v[16:19], v57 offset:47104\n\t" \
  "ds_read_b128 v[4:7], v56 offset:49152\n\t" \
  "ds_read_b128 v[20:23], v57 offset:49152\n\t" \
  "ds_read_b128 v[8:11], v56 offset:51200\n\t" \
  "ds_read_b128 v[24:27], v57 offset:51200\n\t" \
  "ds_read_b128 v[12:15], v56 offset:53248\n\t" \
  "ds_read_b128 v[28:31], v57 offset:53248\n\t" \
  "ds_read_b64_tr_b16 v[32:33], v60 offset:14336\n\t" \
  "ds_read_b64_tr_b16 v[34:35], v60 offset:14848\n\t" \
  "s_mov_b32 s68, 0x50000\n\t" \
  "s_mov_b32 s69, 0x5550055\n\t" \
  "s_mov_b32 s70, 0xa0000\n\t" \
  "s_mov_b32 s71, 0xaaa00aa\n\t" \
  "s_mov_b32 s72, 0x150001\n\t" \
  "s_mov_b32 s73, 0x15550155\n\t" \
  "s_mov_b32 s74, 0x2a0002\n\t" \
  "s_mov_b32 s75, 0x2aaa02aa\n\t" \
  "s_mov_b32 s76, 0x55505555\n\t" \
  "s_mov_b32 s77, 0x50005500\n\t" \
  "s_mov_b32 s78, 0xaaa0aaaa\n\t" \
  "s_mov_b32 s79, 0xa000aa00\n\t" \
  "s_mov_b32 s80, 0x55405554\n\t" \
  "s_mov_b32 s81, 0x40005400\n\t" \
  "s_mov_b32 s82, 0xaa80aaa8\n\t" \
  "s_mov_b32 s83, 0x8000a800\n\t" \
  "s_mov_b32 s84, 0xaaaaaaaa\n\t" \
  "s_mov_b32 s85, 0xaaaaaaaa\n\t" \
  "s_waitcnt lgkmcnt(0)\n\t" \
  "s_branch N2b_%=\n\t" \
  "N0b_%=:\n\t" \
  "s_waitcnt lgkmcnt(10)\n\t" \
  "v_mfma_f32_16x16x32_bf16 v[96:99], v[32:35], v[0:3], v[96:99]\n\t" \
  "ds_read_b64_tr_b16 v[44:45], v58 offset:14336\n\t" \
  "s_waitcnt lgkmcnt(7)\n\t" \
  "v_mfma_f32_16x16x32_bf16 v[96:99], v[36:39], v[16:19], v[96:99]\n\t" \
  "ds_read_b64_tr_b16 v[46:47], v58 offset:14848\n\t" \
  "v_mfma_f32_16x16x32_bf16 v[104:107], v[32:35], v[4:7], v[104:107]\n\t" \
  "ds_read_b128 v[48:51], v56 offset:0\n\t" \
  "v_mfma_f32_16x16x32_bf16 v[104:107], v[36:39], v[20:23], v[104:107]\n\t" \
  "ds_read_b128 v[52:55], v57 offset:0\n\t" \
  "s_waitcnt lgkmcnt(8)\n\t" \
  "v_mfma_f32_16x16x32_bf16 v[112:115], v[32:35], v[8:11], v[112:115]\n\t" \
  "v_mfma_f32_16x16x32_bf16 v[112:115], v[36:39], v[24:27], v[112:115]\n\t" \
  "s_waitcnt lgkmcnt(4)\n\t" \
  "v_mfma_f32_16x16x32_bf16 v[120:123], v[32:35], v[12:15], v[120:123]\n\t" \
  "v_mfma_f32_16x16x32_bf16 v[120:123], v[36:39], v[28:31], v[120:123]\n\t" \
  "ds_read_b64_tr_b16 v[32:33], v59 offset:2048\n\t" \
  "v_mfma_f32_16x16x32_bf16 v[128:131], v[0:3], v[40:43], v[128:131]\n\t" \
  "ds_read_b64_tr_b16 v[34:35], v59 offset:2560\n\t" \
  "s_waitcnt lgkmcnt(4)\n\t" \
  "v_mfma_f32_16x16x32_bf16 v[128:131], v[16:19], v[44:47], v[128:131]\n\t" \
  "ds_read_b64_tr_b16 v[36:37], v59 offset:14336\n\t" \
  "v_mfma_f32_16x16x32_bf16 v[136:139], v[4:7], v[40:43], v[136:139]\n\t" \
  "v_mfma_f32_16x16x32_bf16 v[136:139], v[20:23], v[44:47], v[136:139]\n\t" \
  "ds_read_b64_tr_b16 v[38:39], v59 offset:14848\n\t" \
  "v_mfma_f32_16x16x32_bf16 v[144:147], v[8:11], v[40:43], v[144:147]\n\t" \
  "v_mfma_f32_16x16x32_bf16 v[144:147], v[24:27], v[44:47], v[144:147]\n\t" \
  "v_mfma_f32_16x16x32_bf16 v[152:155], v[12:15], v[40:43], v[152:155]\n\t" \
  "v_mfma_f32_16x16x32_bf16 v[152:155], v[28:31], v[44:47], v[152:155]\n\t" \
  "s_waitcnt lgkmcnt(4)\n\t" \
  "v_mfma_f32_16x16x32_bf16 v[160:163], v[48:51], v[40:43], v[160:163]\n\t" \
  "v_mfma_f32_16x16x32_bf16 v[160:163], v[52:55], v[44:47], v[160:163]\n\t" \
  "ds_read_b64_tr_b16 v[40:41], v59 offset:1024\n\t" \
  "s_waitcnt lgkmcnt(1)\n\t" \
  "v_mfma_f32_16x16x32_bf16 v[164:167], v[48:51], v[32:35], v[164:167]\n\t" \
  "ds_read_b64_tr_b16 v[42:43], v59 offset:1536\n\t" \
  "v_mfma_f32_16x16x32_bf16 v[164:167], v[52:55], v[36:39], v[164:167]\n\t" \
  "ds_read_b64_tr_b16 v[44:45], v59 offset:13312\n\t" \
  "v_mfma_f32_16x16x32_bf16 v[132:135], v[0:3], v[32:35], v[132:135]\n\t" \
  "ds_read_b64_tr_b16 v[46:47], v59 offset:13824\n\t" \
  "v_mfma_f32_16x16x32_bf16 v[132:135], v[16:19], v[36:39], v[132:135]\n\t" \
  "ds_read_b64_tr_b16 v[48:49], v58 offset:0\n\t" \
  "v_mfma_f32_16x16x32_bf16 v[140:143], v[4:7], v[32:35], v[140:143]\n\t" \
  "ds_read_b64_tr_b16 v[50:51], v58 offset:512\n\t" \
  "v_mfma_f32_16x16x32_bf16 v[140:143], v[20:23], v[36:39], v[140:143]\n\t" \
  "ds_read_b64_tr_b16 v[52:53], v58 offset:12288\n\t" \
  "v_mfma_f32_16x16x32_bf16 v[148:151], v[8:11], v[32:35], v[148:151]\n\t" \
  "ds_read_b64_tr_b16 v[54:55], v58 offset:12800\n\t" \
  "v_mfma_f32_16x16x32_bf16 v[148:151], v[24:27], v[36:39], v[148:151]\n\t" \
  "v_mfma_f32_16x16x32_bf16 v[156:159], v[12:15], v[32:35], v[156:159]\n\t" \
  "ds_read_b64_tr_b16 v[32:33], v59 offset:0\n\t" \
  "v_mfma_f32_16x16x32_bf16 v[156:159], v[28:31], v[36:39], v[156:159]\n\t" \
  "ds_read_b64_tr_b16 v[34:35], v59 offset:512\n\t" \
  "s_waitcnt lgkmcnt(6)\n\t" \
  "v_mfma_f32_16x16x32_bf16 v[100:103], v[40:43], v[0:3], v[100:103]\n\t" \
  "ds_read_b64_tr_b16 v[36:37], v59 offset:12288\n\t" \
  "v_mfma_f32_16x16x32_bf16 v[100:103], v[44:47], v[16:19], v[100:103]\n\t" \
  "ds_read_b64_tr_b16 v[38:39], v59 offset:12800\n\t" \
  "v_mfma_f32_16x16x32_bf16 v[108:111], v[40:43], v[4:7], v[108:111]\n\t" \
  "v_mfma_f32_16x16x32_bf16 v[108:111], v[44:47], v[20:23], v[108:111]\n\t" \
  "v_mfma_f32_16x16x32_bf16 v[116:119], v[40:43], v[8:11], v[116:119]\n\t" \
  "v_mfma_f32_16x16x32_bf16 v[116:119], v[44:47], v[24:27], v[116:119]\n\t" \
  "v_mfma_f32_16x16x32_bf16 v[124:127], v[40:43], v[12:15], v[124:127]\n\t" \
  "v_mfma_f32_16x16x32_bf16 v[124:127], v[44:47], v[28:31], v[124:127]\n\t" \
  "s_waitcnt lgkmcnt(0)\n\t" \
  "s_barrier\n\t" \
  "v_mfma_f32_16x16x32_bf16 v[64:67], v[48:51], v[0:3], v[64:67]\n\t" \
  "v_mfma_f32_16x16x32_bf16 v[64:67], v[52:55], v[16:19], v[64:67]\n\t" \
  "ds_read_b64_tr_b16 v[40:41], v58 offset:25600\n\t" \
  "v_mfma_f32_16x16x32_bf16 v[68:71], v[32:35], v[0:3], v[68:71]\n\t" \
  "ds_read_b128 v[0:3], v56 offset:28672\n\t" \
  "v_mfma_f32_16x16x32_bf16 v[68:71], v[36:39], v[16:19], v[68:71]\n\t" \
  "ds_read_b128 v[16:19], v57 offset:28672\n\t" \
  "v_mfma_f32_16x16x32_bf16 v[72:75], v[48:51], v[4:7], v[72:75]\n\t" \
  "ds_read_b64_tr_b16 v[42:43], v58 offset:26112\n\t" \
  "v_mfma_f32_16x16x32_bf16 v[72:75], v[52:55], v[20:23], v[72:75]\n\t" \
  "ds_read_b64_tr_b16 v[44:45], v60 offset:1024\n\t" \
  "v_mfma_f32_16x16x32_bf16 v[76:79], v[32:35], v[4:7], v[76:79]\n\t" \
  "ds_read_b128 v[4:7], v56 offset:30720\n\t" \
  "v_mfma_f32_16x16x32_bf16 v[76:79], v[36:39], v[20:23], v[76:79]\n\t" \
  "ds_read_b128 v[20:23], v57 offset:30720\n\t" \
  "v_mfma_f32_16x16x32_bf16 v[80:83], v[48:51], v[8:11], v[80:83]\n\t" \
  "ds_read_b64_tr_b16 v[46:47], v60 offset:1536\n\t" \
  "v_mfma_f32_16x16x32_bf16 v[80:83], v[52:55], v[24:27], v[80:83]\n\t" \
  "v_mfma_f32_16x16x32_bf16 v[84:87], v[32:35], v[8:11], v[84:87]\n\t" \
  "ds_read_b128 v[8:11], v56 offset:32768\n\t" \
  "v_mfma_f32_16x16x32_bf16 v[84:87], v[36:39], v[24:27], v[84:87]\n\t" \
  "ds_read_b128 v[24:27], v57 offset:32768\n\t" \
  "v_mfma_f32_16x16x32_bf16 v[88:91], v[48:51], v[12:15], v[88:91]\n\t" \
  "ds_read_b64_tr_b16 v[48:49], v58 offset:26624\n\t" \
  "v_mfma_f32_16x16x32_bf16 v[88:91], v[52:55], v[28:31], v[88:91]\n\t" \
  "ds_read_b64_tr_b16 v[50:51], v58 offset:27136\n\t" \
  "v_mfma_f32_16x16x32_bf16 v[92:95], v[32:35], v[12:15], v[92:95]\n\t" \
  "ds_read_b128 v[12:15], v56 offset:34816\n\t" \
  "v_mfma_f32_16x16x32_bf16 v[92:95], v[36:39], v[28:31], v[92:95]\n\t" \
  "ds_read_b128 v[28:31], v57 offset:34816\n\t" \
  "N1b_%=:\n\t" \
  "s_waitcnt lgkmcnt(10)\n\t" \
  "v_mfma_f32_16x16x32_bf16 v[96:99], v[40:43], v[0:3], v[96:99]\n\t" \
  "ds_read_b64_tr_b16 v[52:53], v60 offset:2048\n\t" \
  "s_waitcnt lgkmcnt(7)\n\t" \
  "v_mfma_f32_16x16x32_bf16 v[96:99], v[44:47], v[16:19], v[96:99]\n\t" \
  "ds_read_b64_tr_b16 v[54:55], v60 offset:2560\n\t" \
  "v_mfma_f32_16x16x32_bf16 v[104:107], v[40:43], v[4:7], v[104:107]\n\t" \
  "ds_read_b128 v[32:35], v56 offset:18432\n\t" \
  "v_mfma_f32_16x16x32_bf16 v[104:107], v[44:47], v[20:23], v[104:107]\n\t" \
  "ds_read_b128 v[36:39], v57 offset:18432\n\t" \
  "s_waitcnt lgkmcnt(8)\n\t" \
  "v_mfma_f32_16x16x32_bf16 v[112:115], v[40:43], v[8:11], v[112:115]\n\t" \
  "v_mfma_f32_16x16x32_bf16 v[112:115], v[44:47], v[24:27], v[112:115]\n\t" \
  "s_waitcnt lgkmcnt(4)\n\t" \
  "v_mfma_f32_16x16x32_bf16 v[120:123], v[40:43], v[12:15], v[120:123]\n\t" \
  "v_mfma_f32_16x16x32_bf16 v[120:123], v[44:47], v[28:31], v[120:123]\n\t" \
  "ds_read_b64_tr_b16 v[40:41], v59 offset:26624\n\t" \
  "v_mfma_f32_16x16x32_bf16 v[128:131], v[0:3], v[48:51], v[128:131]\n\t" \
  "ds_read_b64_tr_b16 v[42:43], v59 offset:27136\n\t" \
  "s_waitcnt lgkmcnt(4)\n\t" \
  "v_mfma_f32_16x16x32_bf16 v[128:131], v[16:19], v[52:55], v[128:131]\n\t" \
  "ds_read_b64_tr_b16 v[44:45], v61 offset:2048\n\t" \
  "v_mfma_f32_16x16x32_bf16 v[136:139], v[4:7], v[48:51], v[136:139]\n\t" \
  "v_mfma_f32_16x16x32_bf16 v[136:139], v[20:23], v[52:55], v[136:139]\n\t" \
  "ds_read_b64_tr_b16 v[46:47], v61 offset:2560\n\t" \
  "v_mfma_f32_16x16x32_bf16 v[144:147], v[8:11], v[48:51], v[144:147]\n\t" \
  "v_mfma_f32_16x16x32_bf16 v[144:147], v[24:27], v[52:55], v[144:147]\n\t" \
  "v_mfma_f32_16x16x32_bf16 v[152:155], v[12:15], v[48:51], v[152:155]\n\t" \
  "v_mfma_f32_16x16x32_bf16 v[152:155], v[28:31], v[52:55], v[152:155]\n\t" \
  "s_waitcnt lgkmcnt(4)\n\t" \
  "v_mfma_f32_16x16x32_bf16 v[160:163], v[32:35], v[48:51], v[160:163]\n\t" \
  "v_mfma_f32_16x16x32_bf16 v[160:163], v[36:39], v[52:55], v[160:163]\n\t" \
  "ds_read_b64_tr_b16 v[48:49], v59 offset:25600\n\t" \
  "s_waitcnt lgkmcnt(1)\n\t" \
  "v_mfma_f32_16x16x32_bf16 v[164:167], v[32:35], v[40:43], v[164:167]\n\t" \
  "ds_read_b64_tr_b16 v[50:51], v59 offset:26112\n\t" \
  "v_mfma_f32_16x16x32_bf16 v[164:167], v[36:39], v[44:47], v[164:167]\n\t" \
  "ds_read_b64_tr_b16 v[52:53], v61 offset:1024\n\t" \
  "v_mfma_f32_16x16x32_bf16 v[132:135], v[0:3], v[40:43], v[132:135]\n\t" \
  "ds_read_b64_tr_b16 v[54:55], v61 offset:1536\n\t" \
  "v_mfma_f32_16x16x32_bf16 v[132:135], v[16:19], v[44:47], v[132:135]\n\t" \
  "ds_read_b64_tr_b16 v[32:33], v58 offset:24576\n\t" \
  "v_mfma_f32_16x16x32_bf16 v[140:143], v[4:7], v[40:43], v[140:143]\n\t" \
  "ds_read_b64_tr_b16 v[34:35], v58 offset:25088\n\t" \
  "v_mfma_f32_16x16x32_bf16 v[140:143], v[20:23], v[44:47], v[140:143]\n\t" \
  "ds_read_b64_tr_b16 v[36:37], v60 offset:0\n\t" \
  "v_mfma_f32_16x16x32_bf16 v[148:151], v[8:11], v[40:43], v[148:151]\n\t" \
  "ds_read_b64_tr_b16 v[38:39], v60 offset:512\n\t" \
  "v_mfma_f32_16x16x32_bf16 v[148:151], v[24:27], v[44:47], v[148:151]\n\t" \
  "v_mfma_f32_16x16x32_bf16 v[156:159], v[12:15], v[40:43], v[156:159]\n\t" \
  "ds_read_b64_tr_b16 v[40:41], v59 offset:24576\n\t" \
  "v_mfma_f32_16x16x32_bf16 v[156:159], v[28:31], v[44:47], v[156:159]\n\t" \
  "ds_read_b64_tr_b16 v[42:43], v59 offset:25088\n\t" \
  "s_waitcnt lgkmcnt(6)\n\t" \
  "v_mfma_f32_16x16x32_bf16 v[100:103], v[48:51], v[0:3], v[100:103]\n\t" \
  "ds_read_b64_tr_b16 v[44:45], v61 offset:0\n\t" \
  "v_mfma_f32_16x16x32_bf16 v[100:103], v[52:55], v[16:19], v[100:103]\n\t" \
  "ds_read_b64_tr_b16 v[46:47], v61 offset:512\n\t" \
  "v_mfma_f32_16x16x32_bf16 v[108:111], v[48:51], v[4:7], v[108:111]\n\t" \
  "v_mfma_f32_16x16x32_bf16 v[108:111], v[52:55], v[20:23], v[108:111]\n\t" \
  "v_mfma_f32_16x16x32_bf16 v[116:119], v[48:51], v[8:11], v[116:119]\n\t" \
  "v_mfma_f32_16x16x32_bf16 v[116:119], v[52:55], v[24:27], v[116:119]\n\t" \
  "v_mfma_f32_16x16x32_bf16 v[124:127], v[48:51], v[12:15], v[124:127]\n\t" \
  "v_mfma_f32_16x16x32_bf16 v[124:127], v[52:55], v[28:31], v[124:127]\n\t" \
  "s_waitcnt lgkmcnt(0)\n\t" \
  "s_barrier\n\t" \
  "v_mfma_f32_16x16x32_bf16 v[64:67], v[32:35], v[0:3], v[64:67]\n\t" \
  "v_mfma_f32_16x16x32_bf16 v[64:67], v[36:39], v[16:19], v[64:67]\n\t" \
  "ds_read_b64_tr_b16 v[48:49], v60 offset:13312\n\t" \
  "v_mfma_f32_16x16x32_bf16 v[68:71], v[40:43], v[0:3], v[68:71]\n\t" \
  "ds_read_b128 v[0:3], v56 offset:47104\n\t" \
  "v_mfma_f32_16x16x32_bf16 v[68:71], v[44:47], v[16:19], v[68:71]\n\t" \
  "ds_read_b128 v[16:19], v57 offset:47104\n\t" \
  "v_mfma_f32_16x16x32_bf16 v[72:75], v[32:35], v[4:7], v[72:75]\n\t" \
  "ds_read_b64_tr_b16 v[50:51], v60 offset:13824\n\t" \
  "v_mfma_f32_16x16x32_bf16 v[72:75], v[36:39], v[20:23], v[72:75]\n\t" \
  "ds_read_b64_tr_b16 v[52:53], v60 offset:25600\n\t" \
  "v_mfma_f32_16x16x32_bf16 v[76:79], v[40:43], v[4:7], v[76:79]\n\t" \
  "ds_read_b128 v[4:7], v56 offset:49152\n\t" \
  "v_mfma_f32_16x16x32_bf16 v[76:79], v[44:47], v[20:23], v[76:79]\n\t" \
  "ds_read_b128 v[20:23], v57 offset:49152\n\t" \
  "v_mfma_f32_16x16x32_bf16 v[80:83], v[32:35], v[8:11], v[80:83]\n\t" \
  "ds_read_b64_tr_b16 v[54:55], v60 offset:26112\n\t" \
  "v_mfma_f32_16x16x32_bf16 v[80:83], v[36:39], v[24:27], v[80:83]\n\t" \
  "v_mfma_f32_16x16x32_bf16 v[84:87], v[40:43], v[8:11], v[84:87]\n\t" \
  "ds_read_b128 v[8:11], v56 offset:51200\n\t" \
  "v_mfma_f32_16x16x32_bf16 v[84:87], v[44:47], v[24:27], v[84:87]\n\t" \
  "ds_read_b128 v[24:27], v57 offset:51200\n\t" \
  "v_mfma_f32_16x16x32_bf16 v[88:91], v[32:35], v[12:15], v[88:91]\n\t" \
  "ds_read_b64_tr_b16 v[32:33], v60 offset:14336\n\t" \
  "v_mfma_f32_16x16x32_bf16 v[88:91], v[36:39], v[28:31], v[88:91]\n\t" \
  "ds_read_b64_tr_b16 v[34:35], v60 offset:14848\n\t" \
  "v_mfma_f32_16x16x32_bf16 v[92:95], v[40:43], v[12:15], v[92:95]\n\t" \
  "ds_read_b128 v[12:15], v56 offset:53248\n\t" \
  "v_mfma_f32_16x16x32_bf16 v[92:95], v[44:47], v[28:31], v[92:95]\n\t" \
  "ds_read_b128 v[28:31], v57 offset:53248\n\t" \
  "N2b_%=:\n\t" \
  "s_waitcnt lgkmcnt(10)\n\t" \
  "v_mfma_f32_16x16x32_bf16 v[96:99], v[48:51], v[0:3], v[96:99]\n\t" \
  "ds_read_b64_tr_b16 v[36:37], v60 offset:26624\n\t" \
  "s_waitcnt lgkmcnt(7)\n\t" \
  "v_mfma_f32_16x16x32_bf16 v[96:99], v[52:55], v[16:19], v[96:99]\n\t" \
  "ds_read_b64_tr_b16 v[38:39], v60 offset:27136\n\t" \
  "v_mfma_f32_16x16x32_bf16 v[104:107], v[48:51], v[4:7], v[104:107]\n\t" \
  "ds_read_b128 v[40:43], v56 offset:36864\n\t" \
  "v_mfma_f32_16x16x32_bf16 v[104:107], v[52:55], v[20:23], v[104:107]\n\t" \
  "ds_read_b128 v[44:47], v57 offset:36864\n\t" \
  "s_waitcnt lgkmcnt(8)\n\t" \
  "v_mfma_f32_16x16x32_bf16 v[112:115], v[48:51], v[8:11], v[112:115]\n\t" \
  "v_mfma_f32_16x16x32_bf16 v[112:115], v[52:55], v[24:27], v[112:115]\n\t" \
  "s_waitcnt lgkmcnt(4)\n\t" \
  "v_mfma_f32_16x16x32_bf16 v[120:123], v[48:51], v[12:15], v[120:123]\n\t" \
  "v_mfma_f32_16x16x32_bf16 v[120:123], v[52:55], v[28:31], v[120:123]\n\t" \
  "ds_read_b64_tr_b16 v[48:49], v61 offset:14336\n\t" \
  "v_mfma_f32_16x16x32_bf16 v[128:131], v[0:3], v[32:35], v[128:131]\n\t" \
  "ds_read_b64_tr_b16 v[50:51], v61 offset:14848\n\t" \
  "s_waitcnt lgkmcnt(4)\n\t" \
  "v_mfma_f32_16x16x32_bf16 v[128:131], v[16:19], v[36:39], v[128:131]\n\t" \
  "ds_read_b64_tr_b16 v[52:53], v61 offset:26624\n\t" \
  "v_mfma_f32_16x16x32_bf16 v[136:139], v[4:7], v[32:35], v[136:139]\n\t" \
  "v_mfma_f32_16x16x32_bf16 v[136:139], v[20:23], v[36:39], v[136:139]\n\t" \
  "ds_read_b64_tr_b16 v[54:55], v61 offset:27136\n\t" \
  "v_mfma_f32_16x16x32_bf16 v[144:147], v[8:11], v[32:35], v[144:147]\n\t" \
  "v_mfma_f32_16x16x32_bf16 v[144:147], v[24:27], v[36:39], v[144:147]\n\t" \
  "v_mfma_f32_16x16x32_bf16 v[152:155], v[12:15], v[32:35], v[152:155]\n\t" \
  "v_mfma_f32_16x16x32_bf16 v[152:155], v[28:31], v[36:39], v[152:155]\n\t" \
  "s_waitcnt lgkmcnt(4)\n\t" \
  "v_mfma_f32_16x16x32_bf16 v[160:163], v[40:43], v[32:35], v[160:163]\n\t" \
  "v_mfma_f32_16x16x32_bf16 v[160:163], v[44:47], v[36:39], v[160:163]\n\t" \
  "ds_read_b64_tr_b16 v[32:33], v61 offset:13312\n\t" \
  "s_waitcnt lgkmcnt(1)\n\t" \
  "v_mfma_f32_16x16x32_bf16 v[164:167], v[40:43], v[48:51], v[164:167]\n\t" \
  "ds_read_b64_tr_b16 v[34:35], v61 offset:13824\n\t" \
  "v_mfma_f32_16x16x32_bf16 v[164:167], v[44:47], v[52:55], v[164:167]\n\t" \
  "ds_read_b64_tr_b16 v[36:37], v61 offset:25600\n\t" \
  "v_mfma_f32_16x16x32_bf16 v[132:135], v[0:3], v[48:51], v[132:135]\n\t" \
  "ds_read_b64_tr_b16 v[38:39], v61 offset:26112\n\t" \
  "v_mfma_f32_16x16x32_bf16 v[132:135], v[16:19], v[52:55], v[132:135]\n\t" \
  "ds_read_b64_tr_b16 v[40:41], v60 offset:12288\n\t" \
  "v_mfma_f32_16x16x32_bf16 v[140:143], v[4:7], v[48:51], v[140:143]\n\t" \
  "ds_read_b64_tr_b16 v[42:43], v60 offset:12800\n\t" \
  "v_mfma_f32_16x16x32_bf16 v[140:143], v[20:23], v[52:55], v[140:143]\n\t" \
  "ds_read_b64_tr_b16 v[44:45], v60 offset:24576\n\t" \
  "v_mfma_f32_16x16x32_bf16 v[148:151], v[8:11], v[48:51], v[148:151]\n\t" \
  "ds_read_b64_tr_b16 v[46:47], v60 offset:25088\n\t" \
  "v_mfma_f32_16x16x32_bf16 v[148:151], v[24:27], v[52:55], v[148:151]\n\t" \
  "v_mfma_f32_16x16x32_bf16 v[156:159], v[12:15], v[48:51], v[156:159]\n\t" \
  "ds_read_b64_tr_b16 v[48:49], v61 offset:12288\n\t" \
  "v_mfma_f32_16x16x32_bf16 v[156:159], v[28:31], v[52:55], v[156:159]\n\t" \
  "ds_read_b64_tr_b16 v[50:51], v61 offset:12800\n\t" \
  "s_waitcnt lgkmcnt(6)\n\t" \
  "v_mfma_f32_16x16x32_bf16 v[100:103], v[32:35], v[0:3], v[100:103]\n\t" \
  "ds_read_b64_tr_b16 v[52:53], v61 offset:24576\n\t" \
  "v_mfma_f32_16x16x32_bf16 v[100:103], v[36:39], v[16:19], v[100:103]\n\t" \
  "ds_read_b64_tr_b16 v[54:55], v61 offset:25088\n\t" \
  "v_mfma_f32_16x16x32_bf16 v[108:111], v[32:35], v[4:7], v[108:111]\n\t" \
  "v_mfma_f32_16x16x32_bf16 v[108:111], v[36:39], v[20:23], v[108:111]\n\t" \
  "s_addk_i32 %[it], 1\n\t" \
  "v_mfma_f32_16x16x32_bf16 v[116:119], v[32:35], v[8:11], v[116:119]\n\t" \
  "s_cmp_lt_u32 %[it], 6\n\t" \
  "v_mfma_f32_16x16x32_bf16 v[116:119], v[36:39], v[24:27], v[116:119]\n\t" \
  "v_mfma_f32_16x16x32_bf16 v[124:127], v[32:35], v[12:15], v[124:127]\n\t" \
  "v_mfma_f32_16x16x32_bf16 v[124:127], v[36:39], v[28:31], v[124:127]\n\t" \
  "s_waitcnt lgkmcnt(0)\n\t" \
  "s_barrier\n\t" \
  "s_cbranch_scc0 NTb_%=\n\t" \
  "v_mfma_f32_16x16x32_bf16 v[64:67], v[40:43], v[0:3], v[64:67]\n\t" \
  "v_mfma_f32_16x16x32_bf16 v[64:67], v[44:47], v[16:19], v[64:67]\n\t" \
  "ds_read_b64_tr_b16 v[32:33], v58 offset:1024\n\t" \
  "v_mfma_f32_16x16x32_bf16 v[68:71], v[48:51], v[0:3], v[68:71]\n\t" \
  "ds_read_b128 v[0:3], v56 offset:10240\n\t" \
  "v_mfma_f32_16x16x32_bf16 v[68:71], v[52:55], v[16:19], v[68:71]\n\t" \
  "ds_read_b128 v[16:19], v57 offset:10240\n\t" \
  "v_mfma_f32_16x16x32_bf16 v[72:75], v[40:43], v[4:7], v[72:75]\n\t" \
  "ds_read_b64_tr_b16 v[34:35], v58 offset:1536\n\t" \
  "v_mfma_f32_16x16x32_bf16 v[72:75], v[44:47], v[20:23], v[72:75]\n\t" \
  "ds_read_b64_tr_b16 v[36:37], v58 offset:13312\n\t" \
  "v_mfma_f32_16x16x32_bf16 v[76:79], v[48:51], v[4:7], v[76:79]\n\t" \
  "ds_read_b128 v[4:7], v56 offset:12288\n\t" \
  "v_mfma_f32_16x16x32_bf16 v[76:79], v[52:55], v[20:23], v[76:79]\n\t" \
  "ds_read_b128 v[20:23], v57 offset:12288\n\t" \
  "v_mfma_f32_16x16x32_bf16 v[80:83], v[40:43], v[8:11], v[80:83]\n\t" \
  "ds_read_b64_tr_b16 v[38:39], v58 offset:13824\n\t" \
  "v_mfma_f32_16x16x32_bf16 v[80:83], v[44:47], v[24:27], v[80:83]\n\t" \
  "v_mfma_f32_16x16x32_bf16 v[84:87], v[48:51], v[8:11], v[84:87]\n\t" \
  "ds_read_b128 v[8:11], v56 offset:14336\n\t" \
  "v_mfma_f32_16x16x32_bf16 v[84:87], v[52:55], v[24:27], v[84:87]\n\t" \
  "ds_read_b128 v[24:27], v57 offset:14336\n\t" \
  "v_mfma_f32_16x16x32_bf16 v[88:91], v[40:43], v[12:15], v[88:91]\n\t" \
  "ds_read_b64_tr_b16 v[40:41], v58 offset:2048\n\t" \
  "v_mfma_f32_16x16x32_bf16 v[88:91], v[44:47], v[28:31], v[88:91]\n\t" \
  "ds_read_b64_tr_b16 v[42:43], v58 offset:2560\n\t" \
  "v_mfma_f32_16x16x32_bf16 v[92:95], v[48:51], v[12:15], v[92:95]\n\t" \
  "ds_read_b128 v[12:15], v56 offset:16384\n\t" \
  "v_mfma_f32_16x16x32_bf16 v[92:95], v[52:55], v[28:31], v[92:95]\n\t" \
  "ds_read_b128 v[28:31], v57 offset:16384\n\t" \
  "s_branch N0b_%=\n\t" \
  "NTb_%=:\n\t" \
  "s_setprio 3\n\t" \
  "v_mfma_f32_16x16x32_bf16 v[88:91], v[40:43], v[12:15], v[88:91]\n\t" \
  "v_cvt_pk_bf16_f32 v160, v160, v161\n\t" \
  "v_cvt_pk_bf16_f32 v161, v162, v163\n\t" \
  "v_mfma_f32_16x16x32_bf16 v[88:91], v[44:47], v[28:31], v[88:91]\n\t" \
  "v_cvt_pk_bf16_f32 v162, v164, v165\n\t" \
  "v_cvt_pk_bf16_f32 v163, v166, v167\n\t" \
  "v_mfma_f32_16x16x32_bf16 v[92:95], v[48:51], v[12:15], v[92:95]\n\t" \
  "v_mov_b32 v57, %[fa]\n\t" \
  "v_mov_b32 v58, 1\n\t" \
  "v_mfma_f32_16x16x32_bf16 v[92:95], v[52:55], v[28:31], v[92:95]\n\t" \
  "ds_write_b128 v63, v[160:163] offset:2048\n\t" \
  "s_mov_b64 exec, 1\n\t" \
  "ds_write_b32 v57, v58\n\t" \
  "s_mov_b64 exec, -1\n\t" \
  "v_mfma_f32_16x16x32_bf16 v[80:83], v[40:43], v[8:11], v[80:83]\n\t" \
  "v_mov_b32 v60, 0xff61b1e6\n\t" \
  "v_cvt_pk_bf16_f32 v120, v120, v121\n\t" \
  "v_mfma_f32_16x16x32_bf16 v[80:83], v[44:47], v[24:27], v[80:83]\n\t" \
  "v_cvt_pk_bf16_f32 v121, v122, v123\n\t" \
  "v_cvt_pk_bf16_f32 v122, v124, v125\n\t" \
  "v_mfma_f32_16x16x32_bf16 v[84:87], v[48:51], v[8:11], v[84:87]\n\t" \
  "v_cvt_pk_bf16_f32 v123, v126, v127\n\t" \
  "v_cvt_pk_bf16_f32 v28, v152, v153\n\t" \
  "v_mfma_f32_16x16x32_bf16 v[84:87], v[52:55], v[24:27], v[84:87]\n\t" \
  "v_cvt_pk_bf16_f32 v29, v154, v155\n\t" \
  "v_cvt_pk_bf16_f32 v38, v156, v157\n\t" \
  "v_mfma_f32_16x16x32_bf16 v[72:75], v[40:43], v[4:7], v[72:75]\n\t" \
  "v_cvt_pk_bf16_f32 v39, v158, v159\n\t" \
  "v_cvt_pk_bf16_f32 v112, v112, v113\n\t" \
  "v_mfma_f32_16x16x32_bf16 v[72:75], v[44:47], v[20:23], v[72:75]\n\t" \
  "v_cvt_pk_bf16_f32 v113, v114, v115\n\t" \
  "v_cvt_pk_bf16_f32 v114, v116, v117\n\t" \
  "v_mfma_f32_16x16x32_bf16 v[76:79], v[48:51], v[4:7], v[76:79]\n\t" \
  "v_cvt_pk_bf16_f32 v115, v118, v119\n\t" \
  "v_cndmask_b32_e64 v12, v60, 0, s[76:77]\n\t" \
  "v_mfma_f32_16x16x32_bf16 v[76:79], v[52:55], v[20:23], v[76:79]\n\t" \
  "v_cndmask_b32_e64 v13, v60, 0, s[78:79]\n\t" \
  "v_cndmask_b32_e64 v14, v60, 0, s[80:81]\n\t" \
  "v_mfma_f32_16x16x32_bf16 v[64:67], v[40:43], v[0:3], v[64:67]\n\t" \
  "v_cndmask_b32_e64 v15, v60, 0, s[82:83]\n\t" \
  "v_cndmask_b32_e64 v56, v60, 0, s[68:69]\n\t" \
  "v_mfma_f32_16x16x32_bf16 v[64:67], v[44:47], v[16:19], v[64:67]\n\t" \
  "v_cndmask_b32_e64 v57, v60, 0, s[70:71]\n\t" \
  "v_cndmask_b32_e64 v58, v60, 0, s[72:73]\n\t" \
  "v_mfma_f32_16x16x32_bf16 v[68:71], v[48:51], v[0:3], v[68:71]\n\t" \
  "v_cndmask_b32_e64 v59, v60, 0, s[74:75]\n\t" \
  "v_mfma_f32_16x16x32_bf16 v[68:71], v[52:55], v[16:19], v[68:71]\n\t" \
  "NX_%=:\n\t" \
  "v_cvt_pk_bf16_f32 v88, v88, v89\n\t" \
  "v_cvt_pk_bf16_f32 v89, v90, v91\n\t" \
  "v_cvt_pk_bf16_f32 v90, v92, v93\n\t" \
  "v_cvt_pk_bf16_f32 v91, v94, v95\n\t" \
  "v_mov_b32 v61, %[fp]\n\t" \
  "v_add_u32 v60, %[vo], v63\n\t" \
  "v_mfma_f32_16x16x32_bf16 v[152:155], v[112:115], v[88:91], v[56:59]\n\t" \
  "v_cvt_pk_bf16_f32 v26, v144, v145\n\t" \
  "v_cvt_pk_bf16_f32 v27, v146, v147\n\t" \
  "v_mfma_f32_16x16x32_bf16 v[156:159], v[120:123], v[88:91], v[12:15]\n\t" \
  "v_cvt_pk_bf16_f32 v36, v148, v149\n\t" \
  "v_cvt_pk_bf16_f32 v37, v150, v151\n\t" \
  "v_cvt_pk_bf16_f32 v80, v80, v81\n\t" \
  "v_cvt_pk_bf16_f32 v81, v82, v83\n\t" \
  "v_cvt_pk_bf16_f32 v82, v84, v85\n\t" \
  "v_cvt_pk_bf16_f32 v83, v86, v87\n\t" \
  "v_cvt_pk_bf16_f32 v104, v104, v105\n\t" \
  "v_cvt_pk_bf16_f32 v105, v106, v107\n\t" \
  "v_mfma_f32_16x16x32_bf16 v[148:151], v[112:115], v[80:83], v[12:15]\n\t" \
  "v_cvt_pk_bf16_f32 v106, v108, v109\n\t" \
  "v_cvt_pk_bf16_f32 v107, v110, v111\n\t" \
  "ds_read_b32 v0, v61\n\t" \
  "ds_read_b64 v[20:21], v60\n\t" \
  "ds_read_b64 v[30:31], v60 offset:8\n\t" \
  "s_mov_b64 exec, %[rm]\n\t" \
  "ds_read_b128 v[160:163], v63\n\t" \
  "s_mov_b64 exec, -1\n\t" \
  "v_mfma_f32_16x16x32_bf16 v[144:147], v[104:107], v[80:83], v[56:59]\n\t" \
  "v_cvt_pk_bf16_f32 v24, v136, v137\n\t" \
  "v_cvt_pk_bf16_f32 v25, v138, v139\n\t" \
  "v_cvt_pk_bf16_f32 v34, v140, v141\n\t" \
  "v_cvt_pk_bf16_f32 v35, v142, v143\n\t" \
  "v_cvt_pk_bf16_f32 v72, v72, v73\n\t" \
  "v_cvt_pk_bf16_f32 v73, v74, v75\n\t" \
  "v_cvt_pk_bf16_f32 v74, v76, v77\n\t" \
  "v_cvt_pk_bf16_f32 v75, v78, v79\n\t" \
  "v_cvt_pk_bf16_f32 v96, v96, v97\n\t" \
  "v_cvt_pk_bf16_f32 v97, v98, v99\n\t" \
  "v_mfma_f32_16x16x32_bf16 v[140:143], v[104:107], v[72:75], v[12:15]\n\t" \
  "v_cvt_pk_bf16_f32 v98, v100, v101\n\t" \
  "v_cvt_pk_bf16_f32 v99, v102, v103\n\t" \
  "v_cvt_pk_bf16_f32 v22, v128, v129\n\t" \
  "v_cvt_pk_bf16_f32 v23, v130, v131\n\t" \
  "v_mfma_f32_16x16x32_bf16 v[136:139], v[96:99], v[72:75], v[56:59]\n\t" \
  "v_cvt_pk_bf16_f32 v32, v132, v133\n\t" \
  "v_cvt_pk_bf16_f32 v33, v134, v135\n\t" \
  "v_cvt_pk_bf16_f32 v64, v64, v65\n\t" \
  "v_cvt_pk_bf16_f32 v65, v66, v67\n\t" \
  "v_cvt_pk_bf16_f32 v66, v68, v69\n\t" \
  "v_cvt_pk_bf16_f32 v67, v70, v71\n\t" \
  "v_max_f32 v152, v152, v153\n\t" \
  "v_max_f32 v154, v154, v155\n\t" \
  "v_mfma_f32_16x16x32_bf16 v[132:135], v[96:99], v[64:67], v[12:15]\n\t" \
  "AW_%=:\n\t" \
  "s_waitcnt lgkmcnt(0)\n\t" \
  "v_cmp_eq_u32 vcc, 0, v0\n\t" \
  "v_mov_b32 v8, 0xc0c0100\n\t" \
  "v_mov_b32 v9, 0x1000c0c\n\t" \
  "s_cbranch_vccnz AR_%=\n\t" \
  "s_cmp_eq_u32 %[z], 0\n\t" \
  "s_cbranch_scc1 AZ_%=\n\t" \
  "AY_%=:\n\t" \
  "v_mfma_f32_16x16x32_bf16 v[128:131], v[160:163], v[64:67], v[56:59]\n\t" \
  "v_max_f32 v156, v156, v157\n\t" \
  "v_max_f32 v158, v158, v159\n\t" \
  "v_max3_f32 v7, v152, v154, v156\n\t" \
  "v_max_f32 v7, v7, v158\n\t" \
  "v_max_f32 v144, v144, v145\n\t" \
  "v_max_f32 v146, v146, v147\n\t" \
  "v_max_f32 v148, v148, v149\n\t" \
  "v_max_f32 v150, v150, v151\n\t" \
  "v_max3_f32 v6, v144, v146, v148\n\t" \
  "v_max_f32 v6, v6, v150\n\t" \
  "v_max_f32 v136, v136, v137\n\t" \
  "v_max_f32 v138, v138, v139\n\t" \
  "v_max_f32 v140, v140, v141\n\t" \
  "v_max_f32 v142, v142, v143\n\t" \
  "v_max3_f32 v5, v136, v138, v140\n\t" \
  "v_max_f32 v5, v5, v142\n\t" \
  "v_max_f32 v128, v128, v129\n\t" \
  "v_max_f32 v130, v130, v131\n\t" \
  "v_max_f32 v132, v132, v133\n\t" \
  "v_max_f32 v134, v134, v135\n\t" \
  "v_max3_f32 v4, v128, v130, v132\n\t" \
  "v_max_f32 v4, v4, v134\n\t" \
  "v_cndmask_b32_e64 v60, v8, v9, s[84:85]\n\t" \
  "v_mov_b32 v8, 0xc0c0302\n\t" \
  "v_permlane16_swap_b32 v6, v7\n\t" \
  "v_permlane16_swap_b32 v4, v5\n\t" \
  "v_max_f32 v6, v6, v7\n\t" \
  "v_max_f32 v4, v4, v5\n\t" \
  "v_mov_b32 v9, 0x3020c0c\n\t" \
  "v_cndmask_b32_e64 v61, v8, v9, s[84:85]\n\t" \
  "v_permlane32_swap_b32 v4, v6\n\t" \
  "v_max_f32 v4, v4, v6\n\t" \
  "v_mul_f32 v6, 0xbe8293ee, v4\n\t" \
  "v_mul_f32 v4, 0xbe8293ee, v4\n\t" \
  "s_nop 1\n\t" \
  "v_permlane32_swap_b32 v4, v6\n\t" \
  "v_mov_b32 v5, v4\n\t" \
  "v_mov_b32 v7, v6\n\t" \
  "s_nop 0\n\t" \
  "v_permlane16_swap_b32 v4, v5\n\t" \
  "v_permlane16_swap_b32 v6, v7\n\t" \
  "v_fmamk_f32 v128, v128, 0x3e8293ee, v4\n\t" \
  "v_fmamk_f32 v130, v130, 0x3e8293ee, v4\n\t" \
  "v_fmamk_f32 v132, v132, 0x3e8293ee, v4\n\t" \
  "v_fmamk_f32 v134, v134, 0x3e8293ee, v4\n\t" \
  "v_fmamk_f32 v136, v136, 0x3e8293ee, v5\n\t" \
  "v_fmamk_f32 v138, v138, 0x3e8293ee, v5\n\t" \
  "v_fmamk_f32 v140, v140, 0x3e8293ee, v5\n\t" \
  "v_fmamk_f32 v142, v142, 0x3e8293ee, v5\n\t" \
  "v_fmamk_f32 v144, v144, 0x3e8293ee, v6\n\t" \
  "v_fmamk_f32 v146, v146, 0x3e8293ee, v6\n\t" \
  "v_fmamk_f32 v148, v148, 0x3e8293ee, v6\n\t" \
  "v_fmamk_f32 v150, v150, 0x3e8293ee, v6\n\t" \
  "v_fmamk_f32 v152, v152, 0x3e8293ee, v7\n\t" \
  "v_fmamk_f32 v154, v154, 0x3e8293ee, v7\n\t" \
  "v_fmamk_f32 v156, v156, 0x3e8293ee, v7\n\t" \
  "v_fmamk_f32 v158, v158, 0x3e8293ee, v7\n\t" \
  "v_exp_f32 v128, v128\n\t" \
  "v_exp_f32 v130, v130\n\t" \
  "v_exp_f32 v132, v132\n\t" \
  "v_exp_f32 v134, v134\n\t" \
  "v_exp_f32 v136, v136\n\t" \
  "v_exp_f32 v138, v138\n\t" \
  "v_exp_f32 v140, v140\n\t" \
  "v_exp_f32 v142, v142\n\t" \
  "v_exp_f32 v144, v144\n\t" \
  "v_exp_f32 v146, v146\n\t" \
  "v_exp_f32 v148, v148\n\t" \
  "v_exp_f32 v150, v150\n\t" \
  "v_exp_f32 v152, v152\n\t" \
  "v_exp_f32 v154, v154\n\t" \
  "v_exp_f32 v156, v156\n\t" \
  "v_exp_f32 v158, v158\n\t" \
  "v_add_f32 v4, v128, v130\n\t" \
  "v_add_f32 v8, v132, v134\n\t" \
  "v_add_f32 v5, v136, v138\n\t" \
  "v_add_f32 v9, v140, v142\n\t" \
  "v_add_f32 v6, v144, v146\n\t" \
  "v_add_f32 v10, v148, v150\n\t" \
  "v_add_f32 v7, v152, v154\n\t" \
  "v_add_f32 v11, v156, v158\n\t" \
  "v_add_f32 v4, v4, v8\n\t" \
  "v_add_f32 v5, v5, v9\n\t" \
  "v_add_f32 v6, v6, v10\n\t" \
  "v_add_f32 v7, v7, v11\n\t" \
  "v_permlane16_swap_b32 v4, v5\n\t" \
  "s_nop 0\n\t" \
  "v_permlane16_swap_b32 v6, v7\n\t" \
  "v_add_f32 v4, v4, v5\n\t" \
  "v_add_f32 v6, v6, v7\n\t" \
  "s_nop 1\n\t" \
  "v_permlane32_swap_b32 v4, v6\n\t" \
  "v_add_f32 v4, v4, v6\n\t" \
  "v_rcp_f32 v4, v4\n\t" \
  "s_nop 0\n\t" \
  "v_mov_b32 v6, v4\n\t" \
  "s_nop 1\n\t" \
  "v_permlane32_swap_b32 v4, v6\n\t" \
  "v_mov_b32 v5, v4\n\t" \
  "v_mov_b32 v7, v6\n\t" \
  "s_nop 0\n\t" \
  "v_permlane16_swap_b32 v4, v5\n\t" \
  "v_permlane16_swap_b32 v6, v7\n\t" \
  "v_mul_f32 v128, v128, v4\n\t" \
  "v_mul_f32 v130, v130, v4\n\t" \
  "v_mul_f32 v132, v132, v4\n\t" \
  "v_mul_f32 v134, v134, v4\n\t" \
  "v_cvt_pk_bf16_f32 v128, v128, v130\n\t" \
  "v_cvt_pk_bf16_f32 v132, v132, v134\n\t" \
  "v_perm_b32 v68, 0, v128, v60\n\t" \
  "v_perm_b32 v69, 0, v128, v61\n\t" \
  "v_perm_b32 v70, 0, v132, v60\n\t" \
  "v_perm_b32 v71, 0, v132, v61\n\t" \
  "v_mul_f32 v136, v136, v5\n\t" \
  "v_mul_f32 v138, v138, v5\n\t" \
  "v_mfma_f32_16x16x32_bf16 v[128:131], v[20:23], v[68:71], 0\n\t" \
  "v_mul_f32 v140, v140, v5\n\t" \
  "v_mul_f32 v142, v142, v5\n\t" \
  "v_cvt_pk_bf16_f32 v136, v136, v138\n\t" \
  "v_mfma_f32_16x16x32_bf16 v[132:135], v[30:33], v[68:71], 0\n\t" \
  "v_cvt_pk_bf16_f32 v140, v140, v142\n\t" \
  "v_perm_b32 v76, 0, v136, v60\n\t" \
  "v_perm_b32 v77, 0, v136, v61\n\t" \
  "v_perm_b32 v78, 0, v140, v60\n\t" \
  "v_perm_b32 v79, 0, v140, v61\n\t" \
  "v_mul_f32 v144, v144, v6\n\t" \
  "v_mul_f32 v146, v146, v6\n\t" \
  "v_mfma_f32_16x16x32_bf16 v[136:139], v[22:25], v[76:79], 0\n\t" \
  "v_mul_f32 v148, v148, v6\n\t" \
  "v_mul_f32 v150, v150, v6\n\t" \
  "v_cvt_pk_bf16_f32 v144, v144, v146\n\t" \
  "v_mfma_f32_16x16x32_bf16 v[140:143], v[32:35], v[76:79], 0\n\t" \
  "v_cvt_pk_bf16_f32 v148, v148, v150\n\t" \
  "v_perm_b32 v84, 0, v144, v60\n\t" \
  "v_perm_b32 v85, 0, v144, v61\n\t" \
  "v_perm_b32 v86, 0, v148, v60\n\t" \
  "v_perm_b32 v87, 0, v148, v61\n\t" \
  "v_cvt_pk_bf16_f32 v100, v128, v129\n\t" \
  "v_cvt_pk_bf16_f32 v101, v130, v131\n\t" \
  "v_mfma_f32_16x16x32_bf16 v[144:147], v[24:27], v[84:87], 0\n\t" \
  "v_cvt_pk_bf16_f32 v102, v132, v133\n\t" \
  "v_cvt_pk_bf16_f32 v103, v134, v135\n\t" \
  "global_store_dwordx4 v62, v[100:103], %[ot] offset:0 sc1\n\t" \
  "v_mfma_f32_16x16x32_bf16 v[148:151], v[34:37], v[84:87], 0\n\t" \
  "v_mul_f32 v152, v152, v7\n\t" \
  "v_mul_f32 v154, v154, v7\n\t" \
  "v_mul_f32 v156, v156, v7\n\t" \
  "v_mul_f32 v158, v158, v7\n\t" \
  "v_cvt_pk_bf16_f32 v152, v152, v154\n\t" \
  "v_cvt_pk_bf16_f32 v156, v156, v158\n\t" \
  "v_perm_b32 v92, 0, v152, v60\n\t" \
  "v_perm_b32 v93, 0, v152, v61\n\t" \
  "v_perm_b32 v94, 0, v156, v60\n\t" \
  "v_perm_b32 v95, 0, v156, v61\n\t" \
  "v_cvt_pk_bf16_f32 v108, v136, v137\n\t" \
  "v_cvt_pk_bf16_f32 v109, v138, v139\n\t" \
  "v_mfma_f32_16x16x32_bf16 v[152:155], v[26:29], v[92:95], 0\n\t" \
  "v_cvt_pk_bf16_f32 v110, v140, v141\n\t" \
  "v_cvt_pk_bf16_f32 v111, v142, v143\n\t" \
  "global_store_dwordx4 v62, v[108:111], %[ot] offset:1024 sc1\n\t" \
  "v_mfma_f32_16x16x32_bf16 v[156:159], v[36:39], v[92:95], 0\n\t" \
  "v_cvt_pk_bf16_f32 v116, v144, v145\n\t" \
  "v_cvt_pk_bf16_f32 v117, v146, v147\n\t" \
  "v_cvt_pk_bf16_f32 v118, v148, v149\n\t" \
  "v_cvt_pk_bf16_f32 v119, v150, v151\n\t" \
  "global_store_dwordx4 v62, v[116:119], %[ou] offset:0 sc1\n\t" \
  "v_cvt_pk_bf16_f32 v124, v152, v153\n\t" \
  "v_cvt_pk_bf16_f32 v125, v154, v155\n\t" \
  "s_nop 0\n\t" \
  "v_cvt_pk_bf16_f32 v126, v156, v157\n\t" \
  "v_cvt_pk_bf16_f32 v127, v158, v159\n\t" \
  "global_store_dwordx4 v62, v[124:127], %[ou] offset:1024 sc1\n\t" \
  "s_branch AE_%=\n\t" \
  "AR_%=:\n\t" \
  "s_addk_i32 %[it], 1\n\t" \
  "s_cmp_gt_u32 %[it], 0x400000\n\t" \
  "s_cbranch_scc1 AY_%=\n\t" \
  "ds_read_b32 v0, v61\n\t" \
  "ds_read_b64 v[20:21], v60\n\t" \
  "ds_read_b64 v[30:31], v60 offset:8\n\t" \
  "s_mov_b64 exec, %[rm]\n\t" \
  "ds_read_b128 v[160:163], v63\n\t" \
  "s_mov_b64 exec, -1\n\t" \
  "s_branch AW_%=\n\t" \
  "AZ_%=:\n\t" \
  "v_mov_b32 v0, 0xff61b1e6\n\t" \
  "v_mov_b32 v1, 0xcfa8965f\n\t" \
  "s_nop 3\n\t" \
  "v_cndmask_b32_e64 v56, v0, v1, s[68:69]\n\t" \
  "v_cndmask_b32_e64 v57, v0, v1, s[70:71]\n\t" \
  "v_cndmask_b32_e64 v58, v0, v1, s[72:73]\n\t" \
  "v_cndmask_b32_e64 v59, v0, v1, s[74:75]\n\t" \
  "s_nop 1\n\t" \
  "s_branch AY_%=\n\t" \
  "AE_%=:\n\t" \
  "s_nop 1\n\t"
#define N2_CLOB "v0", "v1", "v2", "v3", "v4", "v5", "v6", "v7", "v8", "v9", "v10", "v11", "v12", "v13", "v14", "v15", "v16", "v17", "v18", "v19", "v20", "v21", "v22", "v23", "v24", "v25", "v26", "v27", "v28", "v29", "v30", "v31", "v32", "v33", "v34", "v35", "v36", "v37", "v38", "v39", "v40", "v41", "v42", "v43", "v44", "v45", "v46", "v47", "v48", "v49", "v50", "v51", "v52", "v53", "v54", "v55", "v60", "v61", "v64", "v65", "v66", "v67", "v68", "v69", "v70", "v71", "v72", "v73", "v74", "v75", "v76", "v77", "v78", "v79", "v80", "v81", "v82", "v83", "v84", "v85", "v86", "v87", "v88", "v89", "v90", "v91", "v92", "v93", "v94", "v95", "v96", "v97", "v98", "v99", "v100", "v101", "v102", "v103", "v104", "v105", "v106", "v107", "v108", "v109", "v110", "v111", "v112", "v113", "v114", "v115", "v116", "v117", "v118", "v119", "v120", "v121", "v122", "v123", "v124", "v125", "v126", "v127", "v128", "v129", "v130", "v131", "v132", "v133", "v134", "v135", "v136", "v137", "v138", "v139", "v140", "v141", "v142", "v143", "v144", "v145", "v146", "v147", "v148", "v149", "v150", "v151", "v152", "v153", "v154", "v155", "v156", "v157", "v158", "v159", "v160", "v161", "v162", "v163", "v164", "v165", "v166", "v167", "s68", "s69", "s70", "s71", "s72", "s73", "s74", "s75", "s76", "s77", "s78", "s79", "s80", "s81", "s82", "s83", "s84", "s85"

#define CV_PRE 1
#define CV_Y 1
#define CV_ASM \
  "CS_%=:\n\t" \
  "ds_read_b32 v34, v61\n\t" \
  "s_waitcnt lgkmcnt(0)\n\t" \
  "v_cmp_ne_u32 vcc, %[tg], v34\n\t" \
  "s_cbranch_vccz CR_%=\n\t" \
  "s_cmp_eq_u32 %[rem], 0\n\t" \
  "s_cbranch_scc1 CT_%=\n\t" \
  "s_barrier\n\t" \
  "s_sub_u32 %[rem], %[rem], 1\n\t" \
  "s_branch CS_%=\n\t" \
  "CT_%=:\n\t" \
  "s_addk_i32 %[cn], 1\n\t" \
  "s_cmp_gt_u32 %[cn], 0x400000\n\t" \
  "s_cbranch_scc1 CR_%=\n\t" \
  "s_sleep 1\n\t" \
  "s_branch CS_%=\n\t" \
  "CR_%=:\n\t" \
  "ds_read_b64 v[0:1], v28 offset:0\n\t" \
  "ds_read_b64 v[2:3], v29 offset:0\n\t" \
  "ds_read_b64 v[4:5], v30 offset:1024\n\t" \
  "ds_read_b64 v[6:7], v31 offset:1024\n\t" \
  "ds_read_b64 v[8:9], v24 offset:2048\n\t" \
  "ds_read_b64 v[10:11], v25 offset:2048\n\t" \
  "ds_read_b64 v[12:13], v26 offset:3072\n\t" \
  "ds_read_b64 v[14:15], v27 offset:3072\n\t" \
  "ds_read_b64 v[16:17], v28 offset:4096\n\t" \
  "ds_read_b64 v[18:19], v29 offset:4096\n\t" \
  "ds_read_b64 v[20:21], v30 offset:5120\n\t" \
  "ds_read_b64 v[22:23], v31 offset:5120\n\t" \
  "s_mov_b64 vcc, 1\n\t" \
  "s_setprio 0\n\t" \
  "s_cmp_eq_u32 %[rem], 0\n\t" \
  "s_cbranch_scc1 CM_%=\n\t" \
  "CB_%=:\n\t" \
  "s_barrier\n\t" \
  "s_sub_u32 %[rem], %[rem], 1\n\t" \
  "s_cmp_lg_u32 %[rem], 0\n\t" \
  "s_cbranch_scc1 CB_%=\n\t" \
  "CM_%=:\n\t" \
  "s_waitcnt lgkmcnt(0)\n\t" \
  "v_mfma_f32_16x16x32_bf16 v[128:131], v[68:71], v[0:3], 0\n\t" \
  "v_mfma_f32_16x16x32_bf16 v[128:131], v[64:67], v[4:7], v[128:131]\n\t" \
  "v_mfma_f32_16x16x32_bf16 v[132:135], v[76:79], v[0:3], 0\n\t" \
  "v_mfma_f32_16x16x32_bf16 v[132:135], v[72:75], v[4:7], v[132:135]\n\t" \
  "v_mfma_f32_16x16x32_bf16 v[136:139], v[84:87], v[0:3], 0\n\t" \
  "v_mfma_f32_16x16x32_bf16 v[136:139], v[80:83], v[4:7], v[136:139]\n\t" \
  "v_mfma_f32_16x16x32_bf16 v[140:143], v[92:95], v[0:3], 0\n\t" \
  "s_setprio 1\n\t" \
  "v_mfma_f32_16x16x32_bf16 v[140:143], v[88:91], v[4:7], v[140:143]\n\t" \
  "v_mfma_f32_16x16x32_bf16 v[144:147], v[100:103], v[0:3], 0\n\t" \
  "ds_bpermute_b32 v34, v32, v131\n\t" \
  "v_mfma_f32_16x16x32_bf16 v[144:147], v[96:99], v[4:7], v[144:147]\n\t" \
  "v_mov_b32 v131, v130\n\t" \
  "v_mov_b32 v130, v129\n\t" \
  "v_mfma_f32_16x16x32_bf16 v[148:151], v[108:111], v[0:3], 0\n\t" \
  "v_mov_b32 v129, v128\n\t" \
  "v_mfma_f32_16x16x32_bf16 v[148:151], v[104:107], v[4:7], v[148:151]\n\t" \
  "v_mfma_f32_16x16x32_bf16 v[152:155], v[116:119], v[0:3], 0\n\t" \
  "v_mfma_f32_16x16x32_bf16 v[152:155], v[112:115], v[4:7], v[152:155]\n\t" \
  "v_mfma_f32_16x16x32_bf16 v[156:159], v[124:127], v[0:3], 0\n\t" \
  "ds_read_b64 v[0:1], v24 offset:6144\n\t" \
  "v_mfma_f32_16x16x32_bf16 v[156:159], v[120:123], v[4:7], v[156:159]\n\t" \
  "ds_read_b64 v[2:3], v25 offset:6144\n\t" \
  "v_mfma_f32_16x16x32_bf16 v[132:135], v[68:71], v[8:11], v[132:135]\n\t" \
  "ds_read_b64 v[4:5], v26 offset:7168\n\t" \
  "v_mfma_f32_16x16x32_bf16 v[132:135], v[64:67], v[12:15], v[132:135]\n\t" \
  "ds_read_b64 v[6:7], v27 offset:7168\n\t" \
  "v_mfma_f32_16x16x32_bf16 v[136:139], v[76:79], v[8:11], v[136:139]\n\t" \
  "v_mfma_f32_16x16x32_bf16 v[136:139], v[72:75], v[12:15], v[136:139]\n\t" \
  "s_waitcnt lgkmcnt(4)\n\t" \
  "v_cndmask_b32_e64 v128, v34, 0, vcc\n\t" \
  "v_mfma_f32_16x16x32_bf16 v[140:143], v[84:87], v[8:11], v[140:143]\n\t" \
  "v_mfma_f32_16x16x32_bf16 v[140:143], v[80:83], v[12:15], v[140:143]\n\t" \
  "v_mfma_f32_16x16x32_bf16 v[144:147], v[92:95], v[8:11], v[144:147]\n\t" \
  "ds_bpermute_b32 v35, v32, v135\n\t" \
  "v_mfma_f32_16x16x32_bf16 v[144:147], v[88:91], v[12:15], v[144:147]\n\t" \
  "v_mov_b32 v135, v134\n\t" \
  "v_mov_b32 v134, v133\n\t" \
  "v_mfma_f32_16x16x32_bf16 v[148:151], v[100:103], v[8:11], v[148:151]\n\t" \
  "v_mov_b32 v133, v132\n\t" \
  "v_mfma_f32_16x16x32_bf16 v[148:151], v[96:99], v[12:15], v[148:151]\n\t" \
  "v_mfma_f32_16x16x32_bf16 v[152:155], v[108:111], v[8:11], v[152:155]\n\t" \
  "v_mfma_f32_16x16x32_bf16 v[152:155], v[104:107], v[12:15], v[152:155]\n\t" \
  "v_mfma_f32_16x16x32_bf16 v[156:159], v[116:119], v[8:11], v[156:159]\n\t" \
  "ds_read_b64 v[8:9], v28 offset:8192\n\t" \
  "v_mfma_f32_16x16x32_bf16 v[156:159], v[112:115], v[12:15], v[156:159]\n\t" \
  "ds_read_b64 v[10:11], v29 offset:8192\n\t" \
  "v_mfma_f32_16x16x32_bf16 v[136:139], v[68:71], v[16:19], v[136:139]\n\t" \
  "ds_read_b64 v[12:13], v30 offset:9216\n\t" \
  "v_mfma_f32_16x16x32_bf16 v[136:139], v[64:67], v[20:23], v[136:139]\n\t" \
  "ds_read_b64 v[14:15], v31 offset:9216\n\t" \
  "v_mfma_f32_16x16x32_bf16 v[140:143], v[76:79], v[16:19], v[140:143]\n\t" \
  "v_mfma_f32_16x16x32_bf16 v[140:143], v[72:75], v[20:23], v[140:143]\n\t" \
  "s_waitcnt lgkmcnt(4)\n\t" \
  "v_cndmask_b32_e64 v132, v35, v34, vcc\n\t" \
  "v_mfma_f32_16x16x32_bf16 v[144:147], v[84:87], v[16:19], v[144:147]\n\t" \
  "v_mfma_f32_16x16x32_bf16 v[144:147], v[80:83], v[20:23], v[144:147]\n\t" \
  "v_mfma_f32_16x16x32_bf16 v[148:151], v[92:95], v[16:19], v[148:151]\n\t" \
  "ds_bpermute_b32 v36, v32, v139\n\t" \
  "v_mfma_f32_16x16x32_bf16 v[148:151], v[88:91], v[20:23], v[148:151]\n\t" \
  "v_mov_b32 v139, v138\n\t" \
  "v_mov_b32 v138, v137\n\t" \
  "v_mfma_f32_16x16x32_bf16 v[152:155], v[100:103], v[16:19], v[152:155]\n\t" \
  "v_mov_b32 v137, v136\n\t" \
  "v_mfma_f32_16x16x32_bf16 v[152:155], v[96:99], v[20:23], v[152:155]\n\t" \
  "v_mfma_f32_16x16x32_bf16 v[156:159], v[108:111], v[16:19], v[156:159]\n\t" \
  "ds_read_b64 v[16:17], v24 offset:10240\n\t" \
  "v_mfma_f32_16x16x32_bf16 v[156:159], v[104:107], v[20:23], v[156:159]\n\t" \
  "ds_read_b64 v[18:19], v25 offset:10240\n\t" \
  "v_mfma_f32_16x16x32_bf16 v[140:143], v[68:71], v[0:3], v[140:143]\n\t" \
  "ds_read_b64 v[20:21], v26 offset:11264\n\t" \
  "v_mfma_f32_16x16x32_bf16 v[140:143], v[64:67], v[4:7], v[140:143]\n\t" \
  "ds_read_b64 v[22:23], v27 offset:11264\n\t" \
  "v_mfma_f32_16x16x32_bf16 v[144:147], v[76:79], v[0:3], v[144:147]\n\t" \
  "v_mfma_f32_16x16x32_bf16 v[144:147], v[72:75], v[4:7], v[144:147]\n\t" \
  "v_mfma_f32_16x16x32_bf16 v[148:151], v[84:87], v[0:3], v[148:151]\n\t" \
  "v_mfma_f32_16x16x32_bf16 v[148:151], v[80:83], v[4:7], v[148:151]\n\t" \
  "s_waitcnt lgkmcnt(4)\n\t" \
  "v_cndmask_b32_e64 v136, v36, v35, vcc\n\t" \
  "v_mfma_f32_16x16x32_bf16 v[152:155], v[92:95], v[0:3], v[152:155]\n\t" \
  "ds_bpermute_b32 v37, v32, v143\n\t" \
  "v_mfma_f32_16x16x32_bf16 v[152:155], v[88:91], v[4:7], v[152:155]\n\t" \
  "v_mov_b32 v143, v142\n\t" \
  "v_mov_b32 v142, v141\n\t" \
  "v_mfma_f32_16x16x32_bf16 v[156:159], v[100:103], v[0:3], v[156:159]\n\t" \
  "ds_read_b64 v[0:1], v28 offset:12288\n\t" \
  "v_mov_b32 v141, v140\n\t" \
  "v_mfma_f32_16x16x32_bf16 v[156:159], v[96:99], v[4:7], v[156:159]\n\t" \
  "ds_read_b64 v[2:3], v29 offset:12288\n\t" \
  "v_mfma_f32_16x16x32_bf16 v[144:147], v[68:71], v[8:11], v[144:147]\n\t" \
  "ds_read_b64 v[4:5], v30 offset:13312\n\t" \
  "v_mfma_f32_16x16x32_bf16 v[144:147], v[64:67], v[12:15], v[144:147]\n\t" \
  "ds_read_b64 v[6:7], v31 offset:13312\n\t" \
  "v_mfma_f32_16x16x32_bf16 v[148:151], v[76:79], v[8:11], v[148:151]\n\t" \
  "v_mfma_f32_16x16x32_bf16 v[148:151], v[72:75], v[12:15], v[148:151]\n\t" \
  "v_mfma_f32_16x16x32_bf16 v[152:155], v[84:87], v[8:11], v[152:155]\n\t" \
  "v_mfma_f32_16x16x32_bf16 v[152:155], v[80:83], v[12:15], v[152:155]\n\t" \
  "v_mfma_f32_16x16x32_bf16 v[156:159], v[92:95], v[8:11], v[156:159]\n\t" \
  "ds_read_b64 v[8:9], v24 offset:14336\n\t" \
  "v_mfma_f32_16x16x32_bf16 v[156:159], v[88:91], v[12:15], v[156:159]\n\t" \
  "ds_read_b64 v[10:11], v25 offset:14336\n\t" \
  "s_waitcnt lgkmcnt(6)\n\t" \
  "v_mfma_f32_16x16x32_bf16 v[148:151], v[68:71], v[16:19], v[148:151]\n\t" \
  "ds_read_b64 v[12:13], v26 offset:15360\n\t" \
  "v_cndmask_b32_e64 v140, v37, v36, vcc\n\t" \
  "v_mfma_f32_16x16x32_bf16 v[148:151], v[64:67], v[20:23], v[148:151]\n\t" \
  "ds_read_b64 v[14:15], v27 offset:15360\n\t" \
  "v_mfma_f32_16x16x32_bf16 v[152:155], v[76:79], v[16:19], v[152:155]\n\t" \
  "ds_bpermute_b32 v38, v32, v147\n\t" \
  "v_mfma_f32_16x16x32_bf16 v[152:155], v[72:75], v[20:23], v[152:155]\n\t" \
  "v_mov_b32 v147, v146\n\t" \
  "v_mov_b32 v146, v145\n\t" \
  "v_mfma_f32_16x16x32_bf16 v[156:159], v[84:87], v[16:19], v[156:159]\n\t" \
  "ds_read_b64 v[16:17], v26 offset:0\n\t" \
  "ds_read_b64 v[18:19], v27 offset:0\n\t" \
  "v_mfma_f32_16x16x32_bf16 v[156:159], v[80:83], v[20:23], v[156:159]\n\t" \
  "ds_read_b64 v[20:21], v28 offset:1024\n\t" \
  "ds_read_b64 v[22:23], v29 offset:1024\n\t" \
  "s_waitcnt lgkmcnt(9)\n\t" \
  "v_mfma_f32_16x16x32_bf16 v[152:155], v[68:71], v[0:3], v[152:155]\n\t" \
  "ds_bpermute_b32 v39, v32, v151\n\t" \
  "v_mov_b32 v145, v144\n\t" \
  "v_mfma_f32_16x16x32_bf16 v[152:155], v[64:67], v[4:7], v[152:155]\n\t" \
  "v_mov_b32 v151, v150\n\t" \
  "v_mov_b32 v150, v149\n\t" \
  "v_mfma_f32_16x16x32_bf16 v[156:159], v[76:79], v[0:3], v[156:159]\n\t" \
  "ds_read_b64 v[0:1], v30 offset:2048\n\t" \
  "v_mov_b32 v149, v148\n\t" \
  "v_mfma_f32_16x16x32_bf16 v[156:159], v[72:75], v[4:7], v[156:159]\n\t" \
  "ds_read_b64 v[2:3], v31 offset:2048\n\t" \
  "s_waitcnt lgkmcnt(10)\n\t" \
  "v_mfma_f32_16x16x32_bf16 v[156:159], v[68:71], v[8:11], v[156:159]\n\t" \
  "ds_bpermute_b32 v40, v32, v155\n\t" \
  "s_waitcnt lgkmcnt(9)\n\t" \
  "v_mfma_f32_16x16x32_bf16 v[156:159], v[64:67], v[12:15], v[156:159]\n\t" \
  "ds_read_b64 v[4:5], v24 offset:3072\n\t" \
  "s_waitcnt lgkmcnt(7)\n\t" \
  "v_mfma_f32_16x16x32_bf16 v[128:131], v[68:71], v[16:19], v[128:131]\n\t" \
  "ds_read_b64 v[6:7], v25 offset:3072\n\t" \
  "s_waitcnt lgkmcnt(6)\n\t" \
  "v_mfma_f32_16x16x32_bf16 v[128:131], v[64:67], v[20:23], v[128:131]\n\t" \
  "v_cndmask_b32_e64 v144, v38, v37, vcc\n\t" \
  "v_mov_b32 v155, v154\n\t" \
  "v_mfma_f32_16x16x32_bf16 v[132:135], v[76:79], v[16:19], v[132:135]\n\t" \
  "ds_bpermute_b32 v41, v32, v159\n\t" \
  "v_mov_b32 v154, v153\n\t" \
  "v_mfma_f32_16x16x32_bf16 v[132:135], v[72:75], v[20:23], v[132:135]\n\t" \
  "v_mov_b32 v153, v152\n\t" \
  "v_mov_b32 v159, v158\n\t" \
  "v_mfma_f32_16x16x32_bf16 v[136:139], v[84:87], v[16:19], v[136:139]\n\t" \
  "v_mov_b32 v158, v157\n\t" \
  "v_mov_b32 v157, v156\n\t" \
  "v_mfma_f32_16x16x32_bf16 v[136:139], v[80:83], v[20:23], v[136:139]\n\t" \
  "s_waitcnt lgkmcnt(6)\n\t" \
  "v_cndmask_b32_e64 v148, v39, v38, vcc\n\t" \
  "v_mfma_f32_16x16x32_bf16 v[140:143], v[92:95], v[16:19], v[140:143]\n\t" \
  "ds_read_b64 v[8:9], v26 offset:4096\n\t" \
  "v_mfma_f32_16x16x32_bf16 v[140:143], v[88:91], v[20:23], v[140:143]\n\t" \
  "ds_read_b64 v[10:11], v27 offset:4096\n\t" \
  "v_mfma_f32_16x16x32_bf16 v[144:147], v[100:103], v[16:19], v[144:147]\n\t" \
  "s_waitcnt lgkmcnt(5)\n\t" \
  "v_cndmask_b32_e64 v152, v40, v39, vcc\n\t" \
  "v_mfma_f32_16x16x32_bf16 v[144:147], v[96:99], v[20:23], v[144:147]\n\t" \
  "global_load_dwordx4 v[44:47], v43, %[q0] offset:0\n\t" \
  "v_mfma_f32_16x16x32_bf16 v[148:151], v[108:111], v[16:19], v[148:151]\n\t" \
  "s_waitcnt lgkmcnt(2)\n\t" \
  "v_cndmask_b32_e64 v156, v41, v40, vcc\n\t" \
  "v_mfma_f32_16x16x32_bf16 v[148:151], v[104:107], v[20:23], v[148:151]\n\t" \
  "ds_read_b64 v[12:13], v28 offset:5120\n\t" \
  "v_mfma_f32_16x16x32_bf16 v[152:155], v[116:119], v[16:19], v[152:155]\n\t" \
  "ds_read_b64 v[14:15], v29 offset:5120\n\t" \
  "v_mfma_f32_16x16x32_bf16 v[152:155], v[112:115], v[20:23], v[152:155]\n\t" \
  "global_load_dwordx4 v[48:51], v43, %[q0] offset:2048\n\t" \
  "v_mfma_f32_16x16x32_bf16 v[156:159], v[124:127], v[16:19], v[156:159]\n\t" \
  "ds_read_b64 v[16:17], v30 offset:6144\n\t" \
  "v_mfma_f32_16x16x32_bf16 v[156:159], v[120:123], v[20:23], v[156:159]\n\t" \
  "ds_read_b64 v[18:19], v31 offset:6144\n\t" \
  "v_mfma_f32_16x16x32_bf16 v[132:135], v[68:71], v[0:3], v[132:135]\n\t" \
  "global_load_dwordx4 v[52:55], v43, %[q2] offset:0\n\t" \
  "v_mfma_f32_16x16x32_bf16 v[132:135], v[64:67], v[4:7], v[132:135]\n\t" \
  "ds_read_b64 v[20:21], v24 offset:7168\n\t" \
  "v_mfma_f32_16x16x32_bf16 v[136:139], v[76:79], v[0:3], v[136:139]\n\t" \
  "ds_read_b64 v[22:23], v25 offset:7168\n\t" \
  "v_mfma_f32_16x16x32_bf16 v[136:139], v[72:75], v[4:7], v[136:139]\n\t" \
  "ds_bpermute_b32 v34, v32, v131\n\t" \
  "v_mfma_f32_16x16x32_bf16 v[140:143], v[84:87], v[0:3], v[140:143]\n\t" \
  "global_load_dwordx4 v[56:59], v43, %[q2] offset:2048\n\t" \
  "v_mfma_f32_16x16x32_bf16 v[140:143], v[80:83], v[4:7], v[140:143]\n\t" \
  "ds_bpermute_b32 v35, v32, v135\n\t" \
  "v_mov_b32 v131, v130\n\t" \
  "v_mfma_f32_16x16x32_bf16 v[144:147], v[92:95], v[0:3], v[144:147]\n\t" \
  "v_mov_b32 v130, v129\n\t" \
  "v_mov_b32 v129, v128\n\t" \
  "v_mfma_f32_16x16x32_bf16 v[144:147], v[88:91], v[4:7], v[144:147]\n\t" \
  "v_mov_b32 v135, v134\n\t" \
  "v_mov_b32 v134, v133\n\t" \
  "v_mfma_f32_16x16x32_bf16 v[148:151], v[100:103], v[0:3], v[148:151]\n\t" \
  "v_mov_b32 v133, v132\n\t" \
  "v_mfma_f32_16x16x32_bf16 v[148:151], v[96:99], v[4:7], v[148:151]\n\t" \
  "v_mfma_f32_16x16x32_bf16 v[152:155], v[108:111], v[0:3], v[152:155]\n\t" \
  "v_mfma_f32_16x16x32_bf16 v[152:155], v[104:107], v[4:7], v[152:155]\n\t" \
  "v_mfma_f32_16x16x32_bf16 v[156:159], v[116:119], v[0:3], v[156:159]\n\t" \
  "ds_read_b64 v[0:1], v26 offset:8192\n\t" \
  "v_mfma_f32_16x16x32_bf16 v[156:159], v[112:115], v[4:7], v[156:159]\n\t" \
  "ds_read_b64 v[2:3], v27 offset:8192\n\t" \
  "s_waitcnt lgkmcnt(4)\n\t" \
  "v_mfma_f32_16x16x32_bf16 v[136:139], v[68:71], v[8:11], v[136:139]\n\t" \
  "ds_read_b64 v[4:5], v28 offset:9216\n\t" \
  "v_mfma_f32_16x16x32_bf16 v[136:139], v[64:67], v[12:15], v[136:139]\n\t" \
  "ds_read_b64 v[6:7], v29 offset:9216\n\t" \
  "v_mfma_f32_16x16x32_bf16 v[140:143], v[76:79], v[8:11], v[140:143]\n\t" \
  "s_waitcnt lgkmcnt(5)\n\t" \
  "v_cndmask_b32_e64 v128, v34, 0, vcc\n\t" \
  "v_mfma_f32_16x16x32_bf16 v[140:143], v[72:75], v[12:15], v[140:143]\n\t" \
  "s_waitcnt lgkmcnt(4)\n\t" \
  "v_cndmask_b32_e64 v132, v35, v34, vcc\n\t" \
  "v_mfma_f32_16x16x32_bf16 v[144:147], v[84:87], v[8:11], v[144:147]\n\t" \
  "ds_bpermute_b32 v36, v32, v139\n\t" \
  "v_mfma_f32_16x16x32_bf16 v[144:147], v[80:83], v[12:15], v[144:147]\n\t" \
  "v_mov_b32 v139, v138\n\t" \
  "v_mov_b32 v138, v137\n\t" \
  "v_mfma_f32_16x16x32_bf16 v[148:151], v[92:95], v[8:11], v[148:151]\n\t" \
  "v_mov_b32 v137, v136\n\t" \
  "v_mfma_f32_16x16x32_bf16 v[148:151], v[88:91], v[12:15], v[148:151]\n\t" \
  "v_mfma_f32_16x16x32_bf16 v[152:155], v[100:103], v[8:11], v[152:155]\n\t" \
  "v_mfma_f32_16x16x32_bf16 v[152:155], v[96:99], v[12:15], v[152:155]\n\t" \
  "v_mfma_f32_16x16x32_bf16 v[156:159], v[108:111], v[8:11], v[156:159]\n\t" \
  "ds_read_b64 v[8:9], v30 offset:10240\n\t" \
  "v_mfma_f32_16x16x32_bf16 v[156:159], v[104:107], v[12:15], v[156:159]\n\t" \
  "ds_read_b64 v[10:11], v31 offset:10240\n\t" \
  "v_mfma_f32_16x16x32_bf16 v[140:143], v[68:71], v[16:19], v[140:143]\n\t" \
  "ds_read_b64 v[12:13], v24 offset:11264\n\t" \
  "v_mfma_f32_16x16x32_bf16 v[140:143], v[64:67], v[20:23], v[140:143]\n\t" \
  "ds_read_b64 v[14:15], v25 offset:11264\n\t" \
  "v_mfma_f32_16x16x32_bf16 v[144:147], v[76:79], v[16:19], v[144:147]\n\t" \
  "v_mfma_f32_16x16x32_bf16 v[144:147], v[72:75], v[20:23], v[144:147]\n\t" \
  "s_waitcnt lgkmcnt(4)\n\t" \
  "v_cndmask_b32_e64 v136, v36, v35, vcc\n\t" \
  "v_mfma_f32_16x16x32_bf16 v[148:151], v[84:87], v[16:19], v[148:151]\n\t" \
  "v_mfma_f32_16x16x32_bf16 v[148:151], v[80:83], v[20:23], v[148:151]\n\t" \
  "v_mfma_f32_16x16x32_bf16 v[152:155], v[92:95], v[16:19], v[152:155]\n\t" \
  "ds_bpermute_b32 v37, v32, v143\n\t" \
  "v_mfma_f32_16x16x32_bf16 v[152:155], v[88:91], v[20:23], v[152:155]\n\t" \
  "v_mov_b32 v143, v142\n\t" \
  "v_mov_b32 v142, v141\n\t" \
  "v_mfma_f32_16x16x32_bf16 v[156:159], v[100:103], v[16:19], v[156:159]\n\t" \
  "ds_read_b64 v[16:17], v26 offset:12288\n\t" \
  "v_mov_b32 v141, v140\n\t" \
  "v_mfma_f32_16x16x32_bf16 v[156:159], v[96:99], v[20:23], v[156:159]\n\t" \
  "ds_read_b64 v[18:19], v27 offset:12288\n\t" \
  "v_mfma_f32_16x16x32_bf16 v[144:147], v[68:71], v[0:3], v[144:147]\n\t" \
  "ds_read_b64 v[20:21], v28 offset:13312\n\t" \
  "v_mfma_f32_16x16x32_bf16 v[144:147], v[64:67], v[4:7], v[144:147]\n\t" \
  "ds_read_b64 v[22:23], v29 offset:13312\n\t" \
  "v_mfma_f32_16x16x32_bf16 v[148:151], v[76:79], v[0:3], v[148:151]\n\t" \
  "v_mfma_f32_16x16x32_bf16 v[148:151], v[72:75], v[4:7], v[148:151]\n\t" \
  "v_mfma_f32_16x16x32_bf16 v[152:155], v[84:87], v[0:3], v[152:155]\n\t" \
  "v_mfma_f32_16x16x32_bf16 v[152:155], v[80:83], v[4:7], v[152:155]\n\t" \
  "v_mfma_f32_16x16x32_bf16 v[156:159], v[92:95], v[0:3], v[156:159]\n\t" \
  "ds_read_b64 v[0:1], v30 offset:14336\n\t" \
  "v_mfma_f32_16x16x32_bf16 v[156:159], v[88:91], v[4:7], v[156:159]\n\t" \
  "ds_read_b64 v[2:3], v31 offset:14336\n\t" \
  "s_waitcnt lgkmcnt(6)\n\t" \
  "v_mfma_f32_16x16x32_bf16 v[148:151], v[68:71], v[8:11], v[148:151]\n\t" \
  "ds_read_b64 v[4:5], v24 offset:15360\n\t" \
  "v_cndmask_b32_e64 v140, v37, v36, vcc\n\t" \
  "v_mfma_f32_16x16x32_bf16 v[148:151], v[64:67], v[12:15], v[148:151]\n\t" \
  "ds_read_b64 v[6:7], v25 offset:15360\n\t" \
  "v_mfma_f32_16x16x32_bf16 v[152:155], v[76:79], v[8:11], v[152:155]\n\t" \
  "ds_bpermute_b32 v38, v32, v147\n\t" \
  "v_mfma_f32_16x16x32_bf16 v[152:155], v[72:75], v[12:15], v[152:155]\n\t" \
  "v_mov_b32 v147, v146\n\t" \
  "v_mov_b32 v146, v145\n\t" \
  "v_mfma_f32_16x16x32_bf16 v[156:159], v[84:87], v[8:11], v[156:159]\n\t" \
  "ds_read_b64 v[8:9], v24 offset:0\n\t" \
  "ds_read_b64 v[10:11], v25 offset:0\n\t" \
  "v_mfma_f32_16x16x32_bf16 v[156:159], v[80:83], v[12:15], v[156:159]\n\t" \
  "ds_read_b64 v[12:13], v26 offset:1024\n\t" \
  "ds_read_b64 v[14:15], v27 offset:1024\n\t" \
  "s_waitcnt lgkmcnt(9)\n\t" \
  "v_mfma_f32_16x16x32_bf16 v[152:155], v[68:71], v[16:19], v[152:155]\n\t" \
  "ds_bpermute_b32 v39, v32, v151\n\t" \
  "v_mov_b32 v145, v144\n\t" \
  "v_mfma_f32_16x16x32_bf16 v[152:155], v[64:67], v[20:23], v[152:155]\n\t" \
  "v_mov_b32 v151, v150\n\t" \
  "v_mov_b32 v150, v149\n\t" \
  "v_mfma_f32_16x16x32_bf16 v[156:159], v[76:79], v[16:19], v[156:159]\n\t" \
  "ds_read_b64 v[16:17], v28 offset:2048\n\t" \
  "v_mov_b32 v149, v148\n\t" \
  "v_mfma_f32_16x16x32_bf16 v[156:159], v[72:75], v[20:23], v[156:159]\n\t" \
  "ds_read_b64 v[18:19], v29 offset:2048\n\t" \
  "s_waitcnt lgkmcnt(10)\n\t" \
  "v_mfma_f32_16x16x32_bf16 v[156:159], v[68:71], v[0:3], v[156:159]\n\t" \
  "ds_bpermute_b32 v40, v32, v155\n\t" \
  "s_waitcnt lgkmcnt(9)\n\t" \
  "v_mfma_f32_16x16x32_bf16 v[156:159], v[64:67], v[4:7], v[156:159]\n\t" \
  "ds_read_b64 v[20:21], v30 offset:3072\n\t" \
  "s_waitcnt lgkmcnt(7)\n\t" \
  "v_mfma_f32_16x16x32_bf16 v[128:131], v[68:71], v[8:11], v[128:131]\n\t" \
  "ds_read_b64 v[22:23], v31 offset:3072\n\t" \
  "s_waitcnt lgkmcnt(6)\n\t" \
  "v_mfma_f32_16x16x32_bf16 v[128:131], v[64:67], v[12:15], v[128:131]\n\t" \
  "v_cndmask_b32_e64 v144, v38, v37, vcc\n\t" \
  "v_mov_b32 v155, v154\n\t" \
  "v_mfma_f32_16x16x32_bf16 v[132:135], v[76:79], v[8:11], v[132:135]\n\t" \
  "ds_bpermute_b32 v41, v32, v159\n\t" \
  "v_mov_b32 v154, v153\n\t" \
  "v_mfma_f32_16x16x32_bf16 v[132:135], v[72:75], v[12:15], v[132:135]\n\t" \
  "v_mov_b32 v153, v152\n\t" \
  "v_mov_b32 v159, v158\n\t" \
  "v_mfma_f32_16x16x32_bf16 v[136:139], v[84:87], v[8:11], v[136:139]\n\t" \
  "v_mov_b32 v158, v157\n\t" \
  "v_mov_b32 v157, v156\n\t" \
  "v_mfma_f32_16x16x32_bf16 v[136:139], v[80:83], v[12:15], v[136:139]\n\t" \
  "s_waitcnt lgkmcnt(6)\n\t" \
  "v_cndmask_b32_e64 v148, v39, v38, vcc\n\t" \
  "v_mfma_f32_16x16x32_bf16 v[140:143], v[92:95], v[8:11], v[140:143]\n\t" \
  "ds_read_b64 v[0:1], v24 offset:4096\n\t" \
  "v_mfma_f32_16x16x32_bf16 v[140:143], v[88:91], v[12:15], v[140:143]\n\t" \
  "ds_read_b64 v[2:3], v25 offset:4096\n\t" \
  "v_mfma_f32_16x16x32_bf16 v[144:147], v[100:103], v[8:11], v[144:147]\n\t" \
  "s_waitcnt lgkmcnt(5)\n\t" \
  "v_cndmask_b32_e64 v152, v40, v39, vcc\n\t" \
  "v_mfma_f32_16x16x32_bf16 v[144:147], v[96:99], v[12:15], v[144:147]\n\t" \
  "ds_read_b64 v[4:5], v26 offset:5120\n\t" \
  "v_mfma_f32_16x16x32_bf16 v[148:151], v[108:111], v[8:11], v[148:151]\n\t" \
  "s_waitcnt lgkmcnt(3)\n\t" \
  "v_cndmask_b32_e64 v156, v41, v40, vcc\n\t" \
  "v_mfma_f32_16x16x32_bf16 v[148:151], v[104:107], v[12:15], v[148:151]\n\t" \
  "ds_read_b64 v[6:7], v27 offset:5120\n\t" \
  "v_mfma_f32_16x16x32_bf16 v[152:155], v[116:119], v[8:11], v[152:155]\n\t" \
  "ds_bpermute_b32 v34, v32, v131\n\t" \
  "v_mfma_f32_16x16x32_bf16 v[152:155], v[112:115], v[12:15], v[152:155]\n\t" \
  "v_mov_b32 v131, v130\n\t" \
  "v_mov_b32 v130, v129\n\t" \
  "v_mfma_f32_16x16x32_bf16 v[156:159], v[124:127], v[8:11], v[156:159]\n\t" \
  "ds_read_b64 v[8:9], v28 offset:6144\n\t" \
  "v_mov_b32 v129, v128\n\t" \
  "v_mfma_f32_16x16x32_bf16 v[156:159], v[120:123], v[12:15], v[156:159]\n\t" \
  "ds_read_b64 v[10:11], v29 offset:6144\n\t" \
  "v_mfma_f32_16x16x32_bf16 v[132:135], v[68:71], v[16:19], v[132:135]\n\t" \
  "ds_read_b64 v[12:13], v30 offset:7168\n\t" \
  "v_mfma_f32_16x16x32_bf16 v[132:135], v[64:67], v[20:23], v[132:135]\n\t" \
  "ds_read_b64 v[14:15], v31 offset:7168\n\t" \
  "v_mfma_f32_16x16x32_bf16 v[136:139], v[76:79], v[16:19], v[136:139]\n\t" \
  "v_mfma_f32_16x16x32_bf16 v[136:139], v[72:75], v[20:23], v[136:139]\n\t" \
  "v_mfma_f32_16x16x32_bf16 v[140:143], v[84:87], v[16:19], v[140:143]\n\t" \
  "v_mfma_f32_16x16x32_bf16 v[140:143], v[80:83], v[20:23], v[140:143]\n\t" \
  "v_mfma_f32_16x16x32_bf16 v[144:147], v[92:95], v[16:19], v[144:147]\n\t" \
  "v_mfma_f32_16x16x32_bf16 v[144:147], v[88:91], v[20:23], v[144:147]\n\t" \
  "s_waitcnt lgkmcnt(4)\n\t" \
  "v_cndmask_b32_e64 v128, v34, 0, vcc\n\t" \
  "v_mfma_f32_16x16x32_bf16 v[148:151], v[100:103], v[16:19], v[148:151]\n\t" \
  "ds_bpermute_b32 v35, v32, v135\n\t" \
  "v_mfma_f32_16x16x32_bf16 v[148:151], v[96:99], v[20:23], v[148:151]\n\t" \
  "v_mov_b32 v135, v134\n\t" \
  "v_mov_b32 v134, v133\n\t" \
  "v_mfma_f32_16x16x32_bf16 v[152:155], v[108:111], v[16:19], v[152:155]\n\t" \
  "v_mov_b32 v133, v132\n\t" \
  "v_mfma_f32_16x16x32_bf16 v[152:155], v[104:107], v[20:23], v[152:155]\n\t" \
  "v_mfma_f32_16x16x32_bf16 v[156:159], v[116:119], v[16:19], v[156:159]\n\t" \
  "ds_read_b64 v[16:17], v24 offset:8192\n\t" \
  "v_mfma_f32_16x16x32_bf16 v[156:159], v[112:115], v[20:23], v[156:159]\n\t" \
  "ds_read_b64 v[18:19], v25 offset:8192\n\t" \
  "v_mfma_f32_16x16x32_bf16 v[136:139], v[68:71], v[0:3], v[136:139]\n\t" \
  "ds_read_b64 v[20:21], v26 offset:9216\n\t" \
  "v_mfma_f32_16x16x32_bf16 v[136:139], v[64:67], v[4:7], v[136:139]\n\t" \
  "ds_read_b64 v[22:23], v27 offset:9216\n\t" \
  "v_mfma_f32_16x16x32_bf16 v[140:143], v[76:79], v[0:3], v[140:143]\n\t" \
  "v_mfma_f32_16x16x32_bf16 v[140:143], v[72:75], v[4:7], v[140:143]\n\t" \
  "v_mfma_f32_16x16x32_bf16 v[144:147], v[84:87], v[0:3], v[144:147]\n\t" \
  "v_mfma_f32_16x16x32_bf16 v[144:147], v[80:83], v[4:7], v[144:147]\n\t" \
  "s_waitcnt lgkmcnt(4)\n\t" \
  "v_cndmask_b32_e64 v132, v35, v34, vcc\n\t" \
  "v_mfma_f32_16x16x32_bf16 v[148:151], v[92:95], v[0:3], v[148:151]\n\t" \
  "ds_bpermute_b32 v36, v32, v139\n\t" \
  "v_mfma_f32_16x16x32_bf16 v[148:151], v[88:91], v[4:7], v[148:151]\n\t" \
  "v_mov_b32 v139, v138\n\t" \
  "v_mov_b32 v138, v137\n\t" \
  "v_mfma_f32_16x16x32_bf16 v[152:155], v[100:103], v[0:3], v[152:155]\n\t" \
  "v_mov_b32 v137, v136\n\t" \
  "v_mfma_f32_16x16x32_bf16 v[152:155], v[96:99], v[4:7], v[152:155]\n\t" \
  "v_mfma_f32_16x16x32_bf16 v[156:159], v[108:111], v[0:3], v[156:159]\n\t" \
  "ds_read_b64 v[0:1], v28 offset:10240\n\t" \
  "v_mfma_f32_16x16x32_bf16 v[156:159], v[104:107], v[4:7], v[156:159]\n\t" \
  "ds_read_b64 v[2:3], v29 offset:10240\n\t" \
  "v_mfma_f32_16x16x32_bf16 v[140:143], v[68:71], v[8:11], v[140:143]\n\t" \
  "ds_read_b64 v[4:5], v30 offset:11264\n\t" \
  "v_mfma_f32_16x16x32_bf16 v[140:143], v[64:67], v[12:15], v[140:143]\n\t" \
  "ds_read_b64 v[6:7], v31 offset:11264\n\t" \
  "v_mfma_f32_16x16x32_bf16 v[144:147], v[76:79], v[8:11], v[144:147]\n\t" \
  "v_mfma_f32_16x16x32_bf16 v[144:147], v[72:75], v[12:15], v[144:147]\n\t" \
  "v_mfma_f32_16x16x32_bf16 v[148:151], v[84:87], v[8:11], v[148:151]\n\t" \
  "v_mfma_f32_16x16x32_bf16 v[148:151], v[80:83], v[12:15], v[148:151]\n\t" \
  "s_waitcnt lgkmcnt(4)\n\t" \
  "v_cndmask_b32_e64 v136, v36, v35, vcc\n\t" \
  "v_mfma_f32_16x16x32_bf16 v[152:155], v[92:95], v[8:11], v[152:155]\n\t" \
  "ds_bpermute_b32 v37, v32, v143\n\t" \
  "v_mfma_f32_16x16x32_bf16 v[152:155], v[88:91], v[12:15], v[152:155]\n\t" \
  "v_mov_b32 v143, v142\n\t" \
  "v_mov_b32 v142, v141\n\t" \
  "v_mfma_f32_16x16x32_bf16 v[156:159], v[100:103], v[8:11], v[156:159]\n\t" \
  "ds_read_b64 v[8:9], v24 offset:12288\n\t" \
  "v_mov_b32 v141, v140\n\t" \
  "v_mfma_f32_16x16x32_bf16 v[156:159], v[96:99], v[12:15], v[156:159]\n\t" \
  "ds_read_b64 v[10:11], v25 offset:12288\n\t" \
  "v_mfma_f32_16x16x32_bf16 v[144:147], v[68:71], v[16:19], v[144:147]\n\t" \
  "ds_read_b64 v[12:13], v26 offset:13312\n\t" \
  "v_mfma_f32_16x16x32_bf16 v[144:147], v[64:67], v[20:23], v[144:147]\n\t" \
  "ds_read_b64 v[14:15], v27 offset:13312\n\t" \
  "v_mfma_f32_16x16x32_bf16 v[148:151], v[76:79], v[16:19], v[148:151]\n\t" \
  "v_mfma_f32_16x16x32_bf16 v[148:151], v[72:75], v[20:23], v[148:151]\n\t" \
  "v_mfma_f32_16x16x32_bf16 v[152:155], v[84:87], v[16:19], v[152:155]\n\t" \
  "v_mfma_f32_16x16x32_bf16 v[152:155], v[80:83], v[20:23], v[152:155]\n\t" \
  "v_mfma_f32_16x16x32_bf16 v[156:159], v[92:95], v[16:19], v[156:159]\n\t" \
  "ds_read_b64 v[16:17], v28 offset:14336\n\t" \
  "v_mfma_f32_16x16x32_bf16 v[156:159], v[88:91], v[20:23], v[156:159]\n\t" \
  "ds_read_b64 v[18:19], v29 offset:14336\n\t" \
  "s_waitcnt lgkmcnt(6)\n\t" \
  "v_mfma_f32_16x16x32_bf16 v[148:151], v[68:71], v[0:3], v[148:151]\n\t" \
  "ds_read_b64 v[20:21], v30 offset:15360\n\t" \
  "v_cndmask_b32_e64 v140, v37, v36, vcc\n\t" \
  "v_mfma_f32_16x16x32_bf16 v[148:151], v[64:67], v[4:7], v[148:151]\n\t" \
  "ds_read_b64 v[22:23], v31 offset:15360\n\t" \
  "v_mfma_f32_16x16x32_bf16 v[152:155], v[76:79], v[0:3], v[152:155]\n\t" \
  "ds_bpermute_b32 v38, v32, v147\n\t" \
  "v_mfma_f32_16x16x32_bf16 v[152:155], v[72:75], v[4:7], v[152:155]\n\t" \
  "v_mov_b32 v147, v146\n\t" \
  "v_mov_b32 v146, v145\n\t" \
  "v_mfma_f32_16x16x32_bf16 v[156:159], v[84:87], v[0:3], v[156:159]\n\t" \
  "ds_read_b64 v[0:1], v30 offset:0\n\t" \
  "ds_read_b64 v[2:3], v31 offset:0\n\t" \
  "v_mfma_f32_16x16x32_bf16 v[156:159], v[80:83], v[4:7], v[156:159]\n\t" \
  "ds_read_b64 v[4:5], v24 offset:1024\n\t" \
  "ds_read_b64 v[6:7], v25 offset:1024\n\t" \
  "s_waitcnt lgkmcnt(9)\n\t" \
  "v_mfma_f32_16x16x32_bf16 v[152:155], v[68:71], v[8:11], v[152:155]\n\t" \
  "ds_bpermute_b32 v39, v32, v151\n\t" \
  "v_mov_b32 v145, v144\n\t" \
  "v_mfma_f32_16x16x32_bf16 v[152:155], v[64:67], v[12:15], v[152:155]\n\t" \
  "v_mov_b32 v151, v150\n\t" \
  "v_mov_b32 v150, v149\n\t" \
  "v_mfma_f32_16x16x32_bf16 v[156:159], v[76:79], v[8:11], v[156:159]\n\t" \
  "ds_read_b64 v[8:9], v26 offset:2048\n\t" \
  "v_mov_b32 v149, v148\n\t" \
  "v_mfma_f32_16x16x32_bf16 v[156:159], v[72:75], v[12:15], v[156:159]\n\t" \
  "ds_read_b64 v[10:11], v27 offset:2048\n\t" \
  "s_waitcnt lgkmcnt(10)\n\t" \
  "v_mfma_f32_16x16x32_bf16 v[156:159], v[68:71], v[16:19], v[156:159]\n\t" \
  "ds_bpermute_b32 v40, v32, v155\n\t" \
  "s_waitcnt lgkmcnt(9)\n\t" \
  "v_mfma_f32_16x16x32_bf16 v[156:159], v[64:67], v[20:23], v[156:159]\n\t" \
  "ds_read_b64 v[12:13], v28 offset:3072\n\t" \
  "s_waitcnt lgkmcnt(7)\n\t" \
  "v_mfma_f32_16x16x32_bf16 v[128:131], v[68:71], v[0:3], v[128:131]\n\t" \
  "ds_read_b64 v[14:15], v29 offset:3072\n\t" \
  "s_waitcnt lgkmcnt(6)\n\t" \
  "v_mfma_f32_16x16x32_bf16 v[128:131], v[64:67], v[4:7], v[128:131]\n\t" \
  "v_cndmask_b32_e64 v144, v38, v37, vcc\n\t" \
  "v_mov_b32 v155, v154\n\t" \
  "v_mfma_f32_16x16x32_bf16 v[132:135], v[76:79], v[0:3], v[132:135]\n\t" \
  "ds_bpermute_b32 v41, v32, v159\n\t" \
  "v_mov_b32 v154, v153\n\t" \
  "v_mfma_f32_16x16x32_bf16 v[132:135], v[72:75], v[4:7], v[132:135]\n\t" \
  "v_mov_b32 v153, v152\n\t" \
  "v_mov_b32 v159, v158\n\t" \
  "v_mfma_f32_16x16x32_bf16 v[136:139], v[84:87], v[0:3], v[136:139]\n\t" \
  "v_mov_b32 v158, v157\n\t" \
  "v_mov_b32 v157, v156\n\t" \
  "v_mfma_f32_16x16x32_bf16 v[136:139], v[80:83], v[4:7], v[136:139]\n\t" \
  "s_waitcnt lgkmcnt(6)\n\t" \
  "v_cndmask_b32_e64 v148, v39, v38, vcc\n\t" \
  "v_mfma_f32_16x16x32_bf16 v[140:143], v[92:95], v[0:3], v[140:143]\n\t" \
  "ds_read_b64 v[16:17], v30 offset:4096\n\t" \
  "v_mfma_f32_16x16x32_bf16 v[140:143], v[88:91], v[4:7], v[140:143]\n\t" \
  "ds_read_b64 v[18:19], v31 offset:4096\n\t" \
  "v_mfma_f32_16x16x32_bf16 v[144:147], v[100:103], v[0:3], v[144:147]\n\t" \
  "s_waitcnt lgkmcnt(5)\n\t" \
  "v_cndmask_b32_e64 v152, v40, v39, vcc\n\t" \
  "v_mfma_f32_16x16x32_bf16 v[144:147], v[96:99], v[4:7], v[144:147]\n\t" \
  "ds_read_b64 v[20:21], v24 offset:5120\n\t" \
  "v_mfma_f32_16x16x32_bf16 v[148:151], v[108:111], v[0:3], v[148:151]\n\t" \
  "s_waitcnt lgkmcnt(3)\n\t" \
  "v_cndmask_b32_e64 v156, v41, v40, vcc\n\t" \
  "v_mfma_f32_16x16x32_bf16 v[148:151], v[104:107], v[4:7], v[148:151]\n\t" \
  "v_cvt_pk_bf16_f32 v34, v128, v129\n\t" \
  "v_cvt_pk_bf16_f32 v35, v130, v131\n\t" \
  "v_mfma_f32_16x16x32_bf16 v[152:155], v[116:119], v[0:3], v[152:155]\n\t" \
  "ds_read_b64 v[22:23], v25 offset:5120\n\t" \
  "v_mfma_f32_16x16x32_bf16 v[152:155], v[112:115], v[4:7], v[152:155]\n\t" \
  "v_mfma_f32_16x16x32_bf16 v[156:159], v[124:127], v[0:3], v[156:159]\n\t" \
  "ds_read_b64 v[0:1], v26 offset:6144\n\t" \
  "v_mfma_f32_16x16x32_bf16 v[156:159], v[120:123], v[4:7], v[156:159]\n\t" \
  "ds_read_b64 v[2:3], v27 offset:6144\n\t" \
  "v_mfma_f32_16x16x32_bf16 v[132:135], v[68:71], v[8:11], v[132:135]\n\t" \
  "ds_read_b64 v[4:5], v28 offset:7168\n\t" \
  "v_mfma_f32_16x16x32_bf16 v[132:135], v[64:67], v[12:15], v[132:135]\n\t" \
  "ds_read_b64 v[6:7], v29 offset:7168\n\t" \
  "v_mfma_f32_16x16x32_bf16 v[136:139], v[76:79], v[8:11], v[136:139]\n\t" \
  "v_mfma_f32_16x16x32_bf16 v[136:139], v[72:75], v[12:15], v[136:139]\n\t" \
  "v_mfma_f32_16x16x32_bf16 v[140:143], v[84:87], v[8:11], v[140:143]\n\t" \
  "v_mfma_f32_16x16x32_bf16 v[140:143], v[80:83], v[12:15], v[140:143]\n\t" \
  "v_mfma_f32_16x16x32_bf16 v[144:147], v[92:95], v[8:11], v[144:147]\n\t" \
  "v_mfma_f32_16x16x32_bf16 v[144:147], v[88:91], v[12:15], v[144:147]\n\t" \
  "v_mfma_f32_16x16x32_bf16 v[148:151], v[100:103], v[8:11], v[148:151]\n\t" \
  "v_cvt_pk_bf16_f32 v36, v132, v133\n\t" \
  "v_cvt_pk_bf16_f32 v37, v134, v135\n\t" \
  "v_mfma_f32_16x16x32_bf16 v[148:151], v[96:99], v[12:15], v[148:151]\n\t" \
  "v_mfma_f32_16x16x32_bf16 v[152:155], v[108:111], v[8:11], v[152:155]\n\t" \
  "v_permlane16_swap_b32 v34, v36\n\t" \
  "v_permlane16_swap_b32 v35, v37\n\t" \
  "v_mfma_f32_16x16x32_bf16 v[152:155], v[104:107], v[12:15], v[152:155]\n\t" \
  "global_store_dwordx4 v42, v[34:37], %[yb] offset:0 sc1\n\t" \
  "v_mfma_f32_16x16x32_bf16 v[156:159], v[116:119], v[8:11], v[156:159]\n\t" \
  "ds_read_b64 v[8:9], v30 offset:8192\n\t" \
  "v_mfma_f32_16x16x32_bf16 v[156:159], v[112:115], v[12:15], v[156:159]\n\t" \
  "ds_read_b64 v[10:11], v31 offset:8192\n\t" \
  "s_waitcnt lgkmcnt(2)\n\t" \
  "v_mfma_f32_16x16x32_bf16 v[136:139], v[68:71], v[16:19], v[136:139]\n\t" \
  "ds_read_b64 v[12:13], v24 offset:9216\n\t" \
  "v_mfma_f32_16x16x32_bf16 v[136:139], v[64:67], v[20:23], v[136:139]\n\t" \
  "ds_read_b64 v[14:15], v25 offset:9216\n\t" \
  "v_mfma_f32_16x16x32_bf16 v[140:143], v[76:79], v[16:19], v[140:143]\n\t" \
  "v_mfma_f32_16x16x32_bf16 v[140:143], v[72:75], v[20:23], v[140:143]\n\t" \
  "v_mfma_f32_16x16x32_bf16 v[144:147], v[84:87], v[16:19], v[144:147]\n\t" \
  "s_mov_b64 exec, 1\n\t" \
  "ds_write_b32 v33, v33\n\t" \
  "s_mov_b64 exec, -1\n\t" \
  "v_mfma_f32_16x16x32_bf16 v[144:147], v[80:83], v[20:23], v[144:147]\n\t" \
  "v_cvt_pk_bf16_f32 v38, v136, v137\n\t" \
  "v_cvt_pk_bf16_f32 v39, v138, v139\n\t" \
  "v_mfma_f32_16x16x32_bf16 v[148:151], v[92:95], v[16:19], v[148:151]\n\t" \
  "v_mfma_f32_16x16x32_bf16 v[148:151], v[88:91], v[20:23], v[148:151]\n\t" \
  "v_mfma_f32_16x16x32_bf16 v[152:155], v[100:103], v[16:19], v[152:155]\n\t" \
  "v_mfma_f32_16x16x32_bf16 v[152:155], v[96:99], v[20:23], v[152:155]\n\t" \
  "v_mfma_f32_16x16x32_bf16 v[156:159], v[108:111], v[16:19], v[156:159]\n\t" \
  "ds_read_b64 v[16:17], v26 offset:10240\n\t" \
  "v_mfma_f32_16x16x32_bf16 v[156:159], v[104:107], v[20:23], v[156:159]\n\t" \
  "ds_read_b64 v[18:19], v27 offset:10240\n\t" \
  "v_mfma_f32_16x16x32_bf16 v[140:143], v[68:71], v[0:3], v[140:143]\n\t" \
  "ds_read_b64 v[20:21], v28 offset:11264\n\t" \
  "v_mfma_f32_16x16x32_bf16 v[140:143], v[64:67], v[4:7], v[140:143]\n\t" \
  "ds_read_b64 v[22:23], v29 offset:11264\n\t" \
  "v_mfma_f32_16x16x32_bf16 v[144:147], v[76:79], v[0:3], v[144:147]\n\t" \
  "v_mfma_f32_16x16x32_bf16 v[144:147], v[72:75], v[4:7], v[144:147]\n\t" \
  "v_mfma_f32_16x16x32_bf16 v[148:151], v[84:87], v[0:3], v[148:151]\n\t" \
  "v_mfma_f32_16x16x32_bf16 v[148:151], v[80:83], v[4:7], v[148:151]\n\t" \
  "v_mfma_f32_16x16x32_bf16 v[152:155], v[92:95], v[0:3], v[152:155]\n\t" \
  "v_mfma_f32_16x16x32_bf16 v[152:155], v[88:91], v[4:7], v[152:155]\n\t" \
  "v_mfma_f32_16x16x32_bf16 v[156:159], v[100:103], v[0:3], v[156:159]\n\t" \
  "v_cvt_pk_bf16_f32 v40, v140, v141\n\t" \
  "v_cvt_pk_bf16_f32 v41, v142, v143\n\t" \
  "v_mfma_f32_16x16x32_bf16 v[156:159], v[96:99], v[4:7], v[156:159]\n\t" \
  "ds_read_b64 v[0:1], v30 offset:12288\n\t" \
  "s_waitcnt lgkmcnt(6)\n\t" \
  "v_mfma_f32_16x16x32_bf16 v[144:147], v[68:71], v[8:11], v[144:147]\n\t" \
  "v_permlane16_swap_b32 v38, v40\n\t" \
  "ds_read_b64 v[2:3], v31 offset:12288\n\t" \
  "v_mfma_f32_16x16x32_bf16 v[144:147], v[64:67], v[12:15], v[144:147]\n\t" \
  "v_permlane16_swap_b32 v39, v41\n\t" \
  "ds_read_b64 v[4:5], v24 offset:13312\n\t" \
  "v_mfma_f32_16x16x32_bf16 v[148:151], v[76:79], v[8:11], v[148:151]\n\t" \
  "ds_read_b64 v[6:7], v25 offset:13312\n\t" \
  "v_mfma_f32_16x16x32_bf16 v[148:151], v[72:75], v[12:15], v[148:151]\n\t" \
  "global_store_dwordx4 v42, v[38:41], %[yb] offset:1024 sc1\n\t" \
  "v_mfma_f32_16x16x32_bf16 v[152:155], v[84:87], v[8:11], v[152:155]\n\t" \
  "v_mfma_f32_16x16x32_bf16 v[152:155], v[80:83], v[12:15], v[152:155]\n\t" \
  "v_cvt_pk_bf16_f32 v34, v144, v145\n\t" \
  "v_cvt_pk_bf16_f32 v35, v146, v147\n\t" \
  "v_mfma_f32_16x16x32_bf16 v[156:159], v[92:95], v[8:11], v[156:159]\n\t" \
  "ds_read_b64 v[8:9], v26 offset:14336\n\t" \
  "v_mfma_f32_16x16x32_bf16 v[156:159], v[88:91], v[12:15], v[156:159]\n\t" \
  "ds_read_b64 v[10:11], v27 offset:14336\n\t" \
  "s_waitcnt lgkmcnt(6)\n\t" \
  "v_mfma_f32_16x16x32_bf16 v[148:151], v[68:71], v[16:19], v[148:151]\n\t" \
  "ds_read_b64 v[12:13], v28 offset:15360\n\t" \
  "v_mfma_f32_16x16x32_bf16 v[148:151], v[64:67], v[20:23], v[148:151]\n\t" \
  "ds_read_b64 v[14:15], v29 offset:15360\n\t" \
  "v_mfma_f32_16x16x32_bf16 v[152:155], v[76:79], v[16:19], v[152:155]\n\t" \
  "v_mfma_f32_16x16x32_bf16 v[152:155], v[72:75], v[20:23], v[152:155]\n\t" \
  "v_mfma_f32_16x16x32_bf16 v[156:159], v[84:87], v[16:19], v[156:159]\n\t" \
  "v_mfma_f32_16x16x32_bf16 v[156:159], v[80:83], v[20:23], v[156:159]\n\t" \
  "s_waitcnt lgkmcnt(4)\n\t" \
  "v_mfma_f32_16x16x32_bf16 v[152:155], v[68:71], v[0:3], v[152:155]\n\t" \
  "v_mfma_f32_16x16x32_bf16 v[152:155], v[64:67], v[4:7], v[152:155]\n\t" \
  "v_cvt_pk_bf16_f32 v36, v148, v149\n\t" \
  "v_cvt_pk_bf16_f32 v37, v150, v151\n\t" \
  "v_mfma_f32_16x16x32_bf16 v[156:159], v[76:79], v[0:3], v[156:159]\n\t" \
  "v_mfma_f32_16x16x32_bf16 v[156:159], v[72:75], v[4:7], v[156:159]\n\t" \
  "v_permlane16_swap_b32 v34, v36\n\t" \
  "s_waitcnt lgkmcnt(2)\n\t" \
  "v_mfma_f32_16x16x32_bf16 v[156:159], v[68:71], v[8:11], v[156:159]\n\t" \
  "v_permlane16_swap_b32 v35, v37\n\t" \
  "s_waitcnt lgkmcnt(0)\n\t" \
  "v_mfma_f32_16x16x32_bf16 v[156:159], v[64:67], v[12:15], v[156:159]\n\t" \
  "global_store_dwordx4 v42, v[34:37], %[yb] offset:2048 sc1\n\t" \
  "s_waitcnt lgkmcnt(0)\n\t" \
  "s_nop 4\n\t" \
  "v_cvt_pk_bf16_f32 v38, v152, v153\n\t" \
  "v_cvt_pk_bf16_f32 v39, v154, v155\n\t" \
  "v_cvt_pk_bf16_f32 v40, v156, v157\n\t" \
  "v_cvt_pk_bf16_f32 v41, v158, v159\n\t" \
  "s_nop 1\n\t" \
  "v_permlane16_swap_b32 v38, v40\n\t" \
  "v_permlane16_swap_b32 v39, v41\n\t" \
  "global_store_dwordx4 v42, v[38:41], %[yb] offset:3072 sc1\n\t" \
  "s_nop 1\n\t"
#define CV_Q 1
#define CV_CLOB "vcc", "v0", "v1", "v2", "v3", "v4", "v5", "v6", "v7", "v8", "v9", "v10", "v11", "v12", "v13", "v14", "v15", "v16", "v17", "v18", "v19", "v20", "v21", "v22", "v23", "v34", "v35", "v36", "v37", "v38", "v39"

#define G3_ASM \
  "v_readlane_b32 s64, v96, 0\n\t" \
  "v_readlane_b32 s65, v96, 1\n\t" \
  "v_readlane_b32 s66, v96, 2\n\t" \
  "v_readlane_b32 s67, v96, 3\n\t" \
  "v_readlane_b32 s68, v96, 4\n\t" \
  "v_readlane_b32 s69, v96, 5\n\t" \
  "v_readlane_b32 s70, v96, 6\n\t" \
  "v_readlane_b32 s71, v96, 7\n\t" \
  "v_readlane_b32 s72, v96, 8\n\t" \
  "v_readlane_b32 s73, v96, 9\n\t" \
  "v_readlane_b32 s74, v96, 10\n\t" \
  "v_readlane_b32 s75, v96, 11\n\t" \
  "v_readlane_b32 s76, v96, 12\n\t" \
  "v_readlane_b32 s77, v96, 13\n\t" \
  "v_readlane_b32 s78, v96, 14\n\t" \
  "v_readlane_b32 s79, v96, 15\n\t" \
  "v_add_u32 v97, %[go], v165\n\t" \
  "s_branch QG_%=\n\t" \
  "QA14_%=:\n\t" \
  "v_mov_b32 v165, %[sy]\n\t" \
  "s_sleep 1\n\t" \
  "ds_read_b32 v165, v165\n\t" \
  "s_addk_i32 %[cn], 1\n\t" \
  "s_cmp_gt_u32 %[cn], 0x400000\n\t" \
  "s_cbranch_scc1 QC14_%=\n\t" \
  "s_waitcnt lgkmcnt(0)\n\t" \
  "v_cmp_lt_u32 vcc, 15, v165\n\t" \
  "s_cbranch_vccz QA14_%=\n\t" \
  "QC14_%=:\n\t" \
  "s_waitcnt lgkmcnt(0)\n\t" \
  "s_branch QB14_%=\n\t" \
  "QA7_%=:\n\t" \
  "v_mov_b32 v165, %[sy]\n\t" \
  "s_sleep 1\n\t" \
  "ds_read_b32 v165, v165\n\t" \
  "s_addk_i32 %[cn], 1\n\t" \
  "s_cmp_gt_u32 %[cn], 0x400000\n\t" \
  "s_cbranch_scc1 QC7_%=\n\t" \
  "s_waitcnt lgkmcnt(0)\n\t" \
  "v_cmp_lt_u32 vcc, 13, v165\n\t" \
  "s_cbranch_vccz QA7_%=\n\t" \
  "QC7_%=:\n\t" \
  "s_waitcnt lgkmcnt(0)\n\t" \
  "s_branch QB7_%=\n\t" \
  "QS_%=:\n\t" \
  "s_sleep 1\n\t" \
  "QG_%=:\n\t" \
  "ds_read_b32 v98, v165\n\t" \
  "ds_read_b32 v99, v97\n\t" \
  "s_addk_i32 %[cn], 1\n\t" \
  "s_cmp_gt_u32 %[cn], 0x400000\n\t" \
  "s_cselect_b32 %[t2], 16, 0\n\t" \
  "s_waitcnt lgkmcnt(0)\n\t" \
  "v_readfirstlane_b32 %[t0], v98\n\t" \
  "v_readfirstlane_b32 %[t1], v99\n\t" \
  "s_max_u32 %[t0], %[t0], %[t2]\n\t" \
  "s_or_b32 %[t1], %[t1], %[t2]\n\t" \
  "s_min_u32 %[t2], %[t0], 5\n\t" \
  "s_cmp_ge_u32 %[iss], %[t2]\n\t" \
  "s_cbranch_scc1 QD_%=\n\t" \
  "s_cmp_eq_u32 %[iss], 1\n\t" \
  "s_cbranch_scc1 QL1_%=\n\t" \
  "s_cmp_eq_u32 %[iss], 2\n\t" \
  "s_cbranch_scc1 QL2_%=\n\t" \
  "s_cmp_eq_u32 %[iss], 3\n\t" \
  "s_cbranch_scc1 QL3_%=\n\t" \
  "s_cmp_eq_u32 %[iss], 4\n\t" \
  "s_cbranch_scc1 QL4_%=\n\t" \
  "QL0_%=:\n\t" \
  "buffer_load_dwordx4 v[0:3], v164, %[ob], s64 offen offset:0 sc1\n\t" \
  "buffer_load_dwordx4 v[4:7], v164, %[ob], s64 offen offset:1024 sc1\n\t" \
  "buffer_load_dwordx4 v[8:11], v164, %[ob], s64 offen offset:2048 sc1\n\t" \
  "buffer_load_dwordx4 v[12:15], v164, %[ob], s64 offen offset:3072 sc1\n\t" \
  "s_mov_b32 %[iss], 1\n\t" \
  "s_cmp_le_u32 %[t2], 1\n\t" \
  "s_cbranch_scc1 QD_%=\n\t" \
  "QL1_%=:\n\t" \
  "buffer_load_dwordx4 v[16:19], v164, %[ob], s65 offen offset:0 sc1\n\t" \
  "buffer_load_dwordx4 v[20:23], v164, %[ob], s65 offen offset:1024 sc1\n\t" \
  "buffer_load_dwordx4 v[24:27], v164, %[ob], s65 offen offset:2048 sc1\n\t" \
  "buffer_load_dwordx4 v[28:31], v164, %[ob], s65 offen offset:3072 sc1\n\t" \
  "s_mov_b32 %[iss], 2\n\t" \
  "s_cmp_le_u32 %[t2], 2\n\t" \
  "s_cbranch_scc1 QD_%=\n\t" \
  "QL2_%=:\n\t" \
  "buffer_load_dwordx4 v[32:35], v164, %[ob], s66 offen offset:0 sc1\n\t" \
  "buffer_load_dwordx4 v[36:39], v164, %[ob], s66 offen offset:1024 sc1\n\t" \
  "buffer_load_dwordx4 v[40:43], v164, %[ob], s66 offen offset:2048 sc1\n\t" \
  "buffer_load_dwordx4 v[44:47], v164, %[ob], s66 offen offset:3072 sc1\n\t" \
  "s_mov_b32 %[iss], 3\n\t" \
  "s_cmp_le_u32 %[t2], 3\n\t" \
  "s_cbranch_scc1 QD_%=\n\t" \
  "QL3_%=:\n\t" \
  "buffer_load_dwordx4 v[48:51], v164, %[ob], s67 offen offset:0 sc1\n\t" \
  "buffer_load_dwordx4 v[52:55], v164, %[ob], s67 offen offset:1024 sc1\n\t" \
  "buffer_load_dwordx4 v[56:59], v164, %[ob], s67 offen offset:2048 sc1\n\t" \
  "buffer_load_dwordx4 v[60:63], v164, %[ob], s67 offen offset:3072 sc1\n\t" \
  "s_mov_b32 %[iss], 4\n\t" \
  "s_cmp_le_u32 %[t2], 4\n\t" \
  "s_cbranch_scc1 QD_%=\n\t" \
  "QL4_%=:\n\t" \
  "buffer_load_dwordx4 v[64:67], v164, %[ob], s68 offen offset:0 sc1\n\t" \
  "buffer_load_dwordx4 v[68:71], v164, %[ob], s68 offen offset:1024 sc1\n\t" \
  "buffer_load_dwordx4 v[72:75], v164, %[ob], s68 offen offset:2048 sc1\n\t" \
  "buffer_load_dwordx4 v[76:79], v164, %[ob], s68 offen offset:3072 sc1\n\t" \
  "s_mov_b32 %[iss], 5\n\t" \
  "QD_%=:\n\t" \
  "s_cmp_lt_u32 %[t0], 7\n\t" \
  "s_cbranch_scc1 QS_%=\n\t" \
  "s_cmp_eq_u32 %[t1], 0\n\t" \
  "s_cbranch_scc1 QS_%=\n\t" \
  "s_setprio 0\n\t" \
  "ds_read_b64_tr_b16 v[96:97], v160 offset:0\n\t" \
  "ds_read_b64_tr_b16 v[98:99], v160 offset:2048\n\t" \
  "ds_read_b64_tr_b16 v[100:101], v161 offset:0\n\t" \
  "ds_read_b64_tr_b16 v[102:103], v161 offset:2048\n\t" \
  "ds_read_b64_tr_b16 v[104:105], v162 offset:0\n\t" \
  "ds_read_b64_tr_b16 v[106:107], v162 offset:2048\n\t" \
  "ds_read_b64_tr_b16 v[108:109], v163 offset:0\n\t" \
  "ds_read_b64_tr_b16 v[110:111], v163 offset:2048\n\t" \
  "ds_read_b64_tr_b16 v[112:113], v160 offset:4096\n\t" \
  "ds_read_b64_tr_b16 v[114:115], v160 offset:6144\n\t" \
  "ds_read_b64_tr_b16 v[116:117], v161 offset:4096\n\t" \
  "ds_read_b64_tr_b16 v[118:119], v161 offset:6144\n\t" \
  "ds_read_b64_tr_b16 v[120:121], v162 offset:4096\n\t" \
  "ds_read_b64_tr_b16 v[122:123], v162 offset:6144\n\t" \
  "s_waitcnt vmcnt(18) lgkmcnt(10)\n\t" \
  "v_mfma_f32_16x16x32_bf16 v[128:131], v[96:99], v[0:3], 0\n\t" \
  "ds_read_b64_tr_b16 v[124:125], v163 offset:4096\n\t" \
  "v_mfma_f32_16x16x32_bf16 v[144:147], v[96:99], v[4:7], 0\n\t" \
  "ds_read_b64_tr_b16 v[126:127], v163 offset:6144\n\t" \
  "v_mfma_f32_16x16x32_bf16 v[132:135], v[100:103], v[0:3], 0\n\t" \
  "ds_read_b64_tr_b16 v[96:97], v160 offset:8192\n\t" \
  "v_mfma_f32_16x16x32_bf16 v[148:151], v[100:103], v[4:7], 0\n\t" \
  "ds_read_b64_tr_b16 v[98:99], v160 offset:10240\n\t" \
  "s_waitcnt lgkmcnt(10)\n\t" \
  "v_mfma_f32_16x16x32_bf16 v[136:139], v[104:107], v[0:3], 0\n\t" \
  "ds_read_b64_tr_b16 v[100:101], v161 offset:8192\n\t" \
  "v_mfma_f32_16x16x32_bf16 v[152:155], v[104:107], v[4:7], 0\n\t" \
  "ds_read_b64_tr_b16 v[102:103], v161 offset:10240\n\t" \
  "v_mfma_f32_16x16x32_bf16 v[140:143], v[108:111], v[0:3], 0\n\t" \
  "ds_read_b64_tr_b16 v[104:105], v162 offset:8192\n\t" \
  "v_mfma_f32_16x16x32_bf16 v[156:159], v[108:111], v[4:7], 0\n\t" \
  "ds_read_b64_tr_b16 v[106:107], v162 offset:10240\n\t" \
  "s_waitcnt vmcnt(16) lgkmcnt(10)\n\t" \
  "v_mfma_f32_16x16x32_bf16 v[128:131], v[112:115], v[8:11], v[128:131]\n\t" \
  "ds_read_b64_tr_b16 v[108:109], v163 offset:8192\n\t" \
  "buffer_load_dwordx4 v[80:83], v164, %[ob], s69 offen offset:0 sc1\n\t" \
  "v_mfma_f32_16x16x32_bf16 v[144:147], v[112:115], v[12:15], v[144:147]\n\t" \
  "ds_read_b64_tr_b16 v[110:111], v163 offset:10240\n\t" \
  "v_mfma_f32_16x16x32_bf16 v[132:135], v[116:119], v[8:11], v[132:135]\n\t" \
  "ds_read_b64_tr_b16 v[112:113], v160 offset:12288\n\t" \
  "v_mfma_f32_16x16x32_bf16 v[148:151], v[116:119], v[12:15], v[148:151]\n\t" \
  "ds_read_b64_tr_b16 v[114:115], v160 offset:14336\n\t" \
  "s_waitcnt lgkmcnt(10)\n\t" \
  "v_mfma_f32_16x16x32_bf16 v[136:139], v[120:123], v[8:11], v[136:139]\n\t" \
  "ds_read_b64_tr_b16 v[116:117], v161 offset:12288\n\t" \
  "buffer_load_dwordx4 v[84:87], v164, %[ob], s69 offen offset:1024 sc1\n\t" \
  "v_mfma_f32_16x16x32_bf16 v[152:155], v[120:123], v[12:15], v[152:155]\n\t" \
  "ds_read_b64_tr_b16 v[118:119], v161 offset:14336\n\t" \
  "v_mfma_f32_16x16x32_bf16 v[140:143], v[124:127], v[8:11], v[140:143]\n\t" \
  "ds_read_b64_tr_b16 v[120:121], v162 offset:12288\n\t" \
  "v_mfma_f32_16x16x32_bf16 v[156:159], v[124:127], v[12:15], v[156:159]\n\t" \
  "ds_read_b64_tr_b16 v[122:123], v162 offset:14336\n\t" \
  "s_waitcnt vmcnt(16) lgkmcnt(10)\n\t" \
  "v_mfma_f32_16x16x32_bf16 v[128:131], v[96:99], v[16:19], v[128:131]\n\t" \
  "ds_read_b64_tr_b16 v[124:125], v163 offset:12288\n\t" \
  "buffer_load_dwordx4 v[88:91], v164, %[ob], s69 offen offset:2048 sc1\n\t" \
  "v_mfma_f32_16x16x32_bf16 v[144:147], v[96:99], v[20:23], v[144:147]\n\t" \
  "ds_read_b64_tr_b16 v[126:127], v163 offset:14336\n\t" \
  "v_mfma_f32_16x16x32_bf16 v[132:135], v[100:103], v[16:19], v[132:135]\n\t" \
  "ds_read_b64_tr_b16 v[96:97], v160 offset:16384\n\t" \
  "v_mfma_f32_16x16x32_bf16 v[148:151], v[100:103], v[20:23], v[148:151]\n\t" \
  "ds_read_b64_tr_b16 v[98:99], v160 offset:18432\n\t" \
  "s_waitcnt lgkmcnt(10)\n\t" \
  "v_mfma_f32_16x16x32_bf16 v[136:139], v[104:107], v[16:19], v[136:139]\n\t" \
  "ds_read_b64_tr_b16 v[100:101], v161 offset:16384\n\t" \
  "v_mfma_f32_16x16x32_bf16 v[152:155], v[104:107], v[20:23], v[152:155]\n\t" \
  "ds_read_b64_tr_b16 v[102:103], v161 offset:18432\n\t" \
  "buffer_load_dwordx4 v[92:95], v164, %[ob], s69 offen offset:3072 sc1\n\t" \
  "v_mfma_f32_16x16x32_bf16 v[140:143], v[108:111], v[16:19], v[140:143]\n\t" \
  "ds_read_b64_tr_b16 v[104:105], v162 offset:16384\n\t" \
  "v_mfma_f32_16x16x32_bf16 v[156:159], v[108:111], v[20:23], v[156:159]\n\t" \
  "ds_read_b64_tr_b16 v[106:107], v162 offset:18432\n\t" \
  "s_waitcnt vmcnt(16) lgkmcnt(10)\n\t" \
  "v_mfma_f32_16x16x32_bf16 v[128:131], v[112:115], v[24:27], v[128:131]\n\t" \
  "ds_read_b64_tr_b16 v[108:109], v163 offset:16384\n\t" \
  "v_mfma_f32_16x16x32_bf16 v[144:147], v[112:115], v[28:31], v[144:147]\n\t" \
  "ds_read_b64_tr_b16 v[110:111], v163 offset:18432\n\t" \
  "buffer_load_dwordx4 v[0:3], v164, %[ob], s70 offen offset:0 sc1\n\t" \
  "v_mfma_f32_16x16x32_bf16 v[132:135], v[116:119], v[24:27], v[132:135]\n\t" \
  "ds_read_b64_tr_b16 v[112:113], v160 offset:20480\n\t" \
  "v_mfma_f32_16x16x32_bf16 v[148:151], v[116:119], v[28:31], v[148:151]\n\t" \
  "ds_read_b64_tr_b16 v[114:115], v160 offset:22528\n\t" \
  "s_waitcnt lgkmcnt(10)\n\t" \
  "v_mfma_f32_16x16x32_bf16 v[136:139], v[120:123], v[24:27], v[136:139]\n\t" \
  "ds_read_b64_tr_b16 v[116:117], v161 offset:20480\n\t" \
  "v_mfma_f32_16x16x32_bf16 v[152:155], v[120:123], v[28:31], v[152:155]\n\t" \
  "ds_read_b64_tr_b16 v[118:119], v161 offset:22528\n\t" \
  "v_mfma_f32_16x16x32_bf16 v[140:143], v[124:127], v[24:27], v[140:143]\n\t" \
  "ds_read_b64_tr_b16 v[120:121], v162 offset:20480\n\t" \
  "buffer_load_dwordx4 v[4:7], v164, %[ob], s70 offen offset:1024 sc1\n\t" \
  "v_mfma_f32_16x16x32_bf16 v[156:159], v[124:127], v[28:31], v[156:159]\n\t" \
  "ds_read_b64_tr_b16 v[122:123], v162 offset:22528\n\t" \
  "s_waitcnt vmcnt(16) lgkmcnt(10)\n\t" \
  "v_mfma_f32_16x16x32_bf16 v[128:131], v[96:99], v[32:35], v[128:131]\n\t" \
  "ds_read_b64_tr_b16 v[124:125], v163 offset:20480\n\t" \
  "ds_read_b32 v165, v165\n\t" \
  "v_mfma_f32_16x16x32_bf16 v[144:147], v[96:99], v[36:39], v[144:147]\n\t" \
  "ds_read_b64_tr_b16 v[126:127], v163 offset:22528\n\t" \
  "v_mfma_f32_16x16x32_bf16 v[132:135], v[100:103], v[32:35], v[132:135]\n\t" \
  "ds_read_b64_tr_b16 v[96:97], v160 offset:24576\n\t" \
  "buffer_load_dwordx4 v[8:11], v164, %[ob], s70 offen offset:2048 sc1\n\t" \
  "v_mfma_f32_16x16x32_bf16 v[148:151], v[100:103], v[36:39], v[148:151]\n\t" \
  "ds_read_b64_tr_b16 v[98:99], v160 offset:26624\n\t" \
  "s_waitcnt lgkmcnt(11)\n\t" \
  "v_mfma_f32_16x16x32_bf16 v[136:139], v[104:107], v[32:35], v[136:139]\n\t" \
  "ds_read_b64_tr_b16 v[100:101], v161 offset:24576\n\t" \
  "v_mfma_f32_16x16x32_bf16 v[152:155], v[104:107], v[36:39], v[152:155]\n\t" \
  "ds_read_b64_tr_b16 v[102:103], v161 offset:26624\n\t" \
  "v_mfma_f32_16x16x32_bf16 v[140:143], v[108:111], v[32:35], v[140:143]\n\t" \
  "ds_read_b64_tr_b16 v[104:105], v162 offset:24576\n\t" \
  "v_mfma_f32_16x16x32_bf16 v[156:159], v[108:111], v[36:39], v[156:159]\n\t" \
  "ds_read_b64_tr_b16 v[106:107], v162 offset:26624\n\t" \
  "s_waitcnt vmcnt(15) lgkmcnt(11)\n\t" \
  "v_mfma_f32_16x16x32_bf16 v[128:131], v[112:115], v[40:43], v[128:131]\n\t" \
  "ds_read_b64_tr_b16 v[108:109], v163 offset:24576\n\t" \
  "buffer_load_dwordx4 v[12:15], v164, %[ob], s70 offen offset:3072 sc1\n\t" \
  "v_mfma_f32_16x16x32_bf16 v[144:147], v[112:115], v[44:47], v[144:147]\n\t" \
  "ds_read_b64_tr_b16 v[110:111], v163 offset:26624\n\t" \
  "s_waitcnt lgkmcnt(9)\n\t" \
  "v_mfma_f32_16x16x32_bf16 v[132:135], v[116:119], v[40:43], v[132:135]\n\t" \
  "ds_read_b64_tr_b16 v[112:113], v160 offset:28672\n\t" \
  "v_cmp_lt_u32 vcc, 13, v165\n\t" \
  "v_mfma_f32_16x16x32_bf16 v[148:151], v[116:119], v[44:47], v[148:151]\n\t" \
  "ds_read_b64_tr_b16 v[114:115], v160 offset:30720\n\t" \
  "s_cbranch_vccz QA7_%=\n\t" \
  "QB7_%=:\n\t" \
  "s_waitcnt lgkmcnt(10)\n\t" \
  "v_mfma_f32_16x16x32_bf16 v[136:139], v[120:123], v[40:43], v[136:139]\n\t" \
  "ds_read_b64_tr_b16 v[116:117], v161 offset:28672\n\t" \
  "buffer_load_dwordx4 v[16:19], v164, %[ob], s71 offen offset:0 sc1\n\t" \
  "v_mfma_f32_16x16x32_bf16 v[152:155], v[120:123], v[44:47], v[152:155]\n\t" \
  "ds_read_b64_tr_b16 v[118:119], v161 offset:30720\n\t" \
  "v_mov_b32 v165, %[sy]\n\t" \
  "v_mfma_f32_16x16x32_bf16 v[140:143], v[124:127], v[40:43], v[140:143]\n\t" \
  "ds_read_b64_tr_b16 v[120:121], v162 offset:28672\n\t" \
  "v_mfma_f32_16x16x32_bf16 v[156:159], v[124:127], v[44:47], v[156:159]\n\t" \
  "ds_read_b64_tr_b16 v[122:123], v162 offset:30720\n\t" \
  "s_waitcnt vmcnt(15) lgkmcnt(10)\n\t" \
  "v_mfma_f32_16x16x32_bf16 v[128:131], v[96:99], v[48:51], v[128:131]\n\t" \
  "ds_read_b64_tr_b16 v[124:125], v163 offset:28672\n\t" \
  "buffer_load_dwordx4 v[20:23], v164, %[ob], s71 offen offset:1024 sc1\n\t" \
  "v_mfma_f32_16x16x32_bf16 v[144:147], v[96:99], v[52:55], v[144:147]\n\t" \
  "ds_read_b64_tr_b16 v[126:127], v163 offset:30720\n\t" \
  "v_mfma_f32_16x16x32_bf16 v[132:135], v[100:103], v[48:51], v[132:135]\n\t" \
  "ds_read_b64_tr_b16 v[96:97], v160 offset:32768\n\t" \
  "v_mfma_f32_16x16x32_bf16 v[148:151], v[100:103], v[52:55], v[148:151]\n\t" \
  "ds_read_b64_tr_b16 v[98:99], v160 offset:34816\n\t" \
  "s_waitcnt lgkmcnt(10)\n\t" \
  "v_mfma_f32_16x16x32_bf16 v[136:139], v[104:107], v[48:51], v[136:139]\n\t" \
  "ds_read_b64_tr_b16 v[100:101], v161 offset:32768\n\t" \
  "buffer_load_dwordx4 v[24:27], v164, %[ob], s71 offen offset:2048 sc1\n\t" \
  "v_mfma_f32_16x16x32_bf16 v[152:155], v[104:107], v[52:55], v[152:155]\n\t" \
  "ds_read_b64_tr_b16 v[102:103], v161 offset:34816\n\t" \
  "v_mfma_f32_16x16x32_bf16 v[140:143], v[108:111], v[48:51], v[140:143]\n\t" \
  "ds_read_b64_tr_b16 v[104:105], v162 offset:32768\n\t" \
  "v_mfma_f32_16x16x32_bf16 v[156:159], v[108:111], v[52:55], v[156:159]\n\t" \
  "ds_read_b64_tr_b16 v[106:107], v162 offset:34816\n\t" \
  "s_waitcnt vmcnt(15) lgkmcnt(10)\n\t" \
  "v_mfma_f32_16x16x32_bf16 v[128:131], v[112:115], v[56:59], v[128:131]\n\t" \
  "ds_read_b64_tr_b16 v[108:109], v163 offset:32768\n\t" \
  "v_mfma_f32_16x16x32_bf16 v[144:147], v[112:115], v[60:63], v[144:147]\n\t" \
  "ds_read_b64_tr_b16 v[110:111], v163 offset:34816\n\t" \
  "buffer_load_dwordx4 v[28:31], v164, %[ob], s71 offen offset:3072 sc1\n\t" \
  "v_mfma_f32_16x16x32_bf16 v[132:135], v[116:119], v[56:59], v[132:135]\n\t" \
  "ds_read_b64_tr_b16 v[112:113], v160 offset:36864\n\t" \
  "v_mfma_f32_16x16x32_bf16 v[148:151], v[116:119], v[60:63], v[148:151]\n\t" \
  "ds_read_b64_tr_b16 v[114:115], v160 offset:38912\n\t" \
  "s_waitcnt lgkmcnt(10)\n\t" \
  "v_mfma_f32_16x16x32_bf16 v[136:139], v[120:123], v[56:59], v[136:139]\n\t" \
  "ds_read_b64_tr_b16 v[116:117], v161 offset:36864\n\t" \
  "v_mfma_f32_16x16x32_bf16 v[152:155], v[120:123], v[60:63], v[152:155]\n\t" \
  "ds_read_b64_tr_b16 v[118:119], v161 offset:38912\n\t" \
  "buffer_load_dwordx4 v[32:35], v164, %[ob], s72 offen offset:0 sc1\n\t" \
  "v_mfma_f32_16x16x32_bf16 v[140:143], v[124:127], v[56:59], v[140:143]\n\t" \
  "ds_read_b64_tr_b16 v[120:121], v162 offset:36864\n\t" \
  "v_mfma_f32_16x16x32_bf16 v[156:159], v[124:127], v[60:63], v[156:159]\n\t" \
  "ds_read_b64_tr_b16 v[122:123], v162 offset:38912\n\t" \
  "s_waitcnt vmcnt(15) lgkmcnt(10)\n\t" \
  "v_mfma_f32_16x16x32_bf16 v[128:131], v[96:99], v[64:67], v[128:131]\n\t" \
  "ds_read_b64_tr_b16 v[124:125], v163 offset:36864\n\t" \
  "v_mfma_f32_16x16x32_bf16 v[144:147], v[96:99], v[68:71], v[144:147]\n\t" \
  "ds_read_b64_tr_b16 v[126:127], v163 offset:38912\n\t" \
  "v_mfma_f32_16x16x32_bf16 v[132:135], v[100:103], v[64:67], v[132:135]\n\t" \
  "ds_read_b64_tr_b16 v[96:97], v160 offset:40960\n\t" \
  "buffer_load_dwordx4 v[36:39], v164, %[ob], s72 offen offset:1024 sc1\n\t" \
  "v_mfma_f32_16x16x32_bf16 v[148:151], v[100:103], v[68:71], v[148:151]\n\t" \
  "ds_read_b64_tr_b16 v[98:99], v160 offset:43008\n\t" \
  "s_waitcnt lgkmcnt(10)\n\t" \
  "v_mfma_f32_16x16x32_bf16 v[136:139], v[104:107], v[64:67], v[136:139]\n\t" \
  "ds_read_b64_tr_b16 v[100:101], v161 offset:40960\n\t" \
  "v_mfma_f32_16x16x32_bf16 v[152:155], v[104:107], v[68:71], v[152:155]\n\t" \
  "ds_read_b64_tr_b16 v[102:103], v161 offset:43008\n\t" \
  "v_mfma_f32_16x16x32_bf16 v[140:143], v[108:111], v[64:67], v[140:143]\n\t" \
  "ds_read_b64_tr_b16 v[104:105], v162 offset:40960\n\t" \
  "buffer_load_dwordx4 v[40:43], v164, %[ob], s72 offen offset:2048 sc1\n\t" \
  "v_mfma_f32_16x16x32_bf16 v[156:159], v[108:111], v[68:71], v[156:159]\n\t" \
  "ds_read_b64_tr_b16 v[106:107], v162 offset:43008\n\t" \
  "s_waitcnt vmcnt(15) lgkmcnt(10)\n\t" \
  "v_mfma_f32_16x16x32_bf16 v[128:131], v[112:115], v[72:75], v[128:131]\n\t" \
  "ds_read_b64_tr_b16 v[108:109], v163 offset:40960\n\t" \
  "v_mfma_f32_16x16x32_bf16 v[144:147], v[112:115], v[76:79], v[144:147]\n\t" \
  "ds_read_b64_tr_b16 v[110:111], v163 offset:43008\n\t" \
  "v_mfma_f32_16x16x32_bf16 v[132:135], v[116:119], v[72:75], v[132:135]\n\t" \
  "ds_read_b64_tr_b16 v[112:113], v160 offset:45056\n\t" \
  "buffer_load_dwordx4 v[44:47], v164, %[ob], s72 offen offset:3072 sc1\n\t" \
  "v_mfma_f32_16x16x32_bf16 v[148:151], v[116:119], v[76:79], v[148:151]\n\t" \
  "ds_read_b64_tr_b16 v[114:115], v160 offset:47104\n\t" \
  "s_waitcnt lgkmcnt(10)\n\t" \
  "v_mfma_f32_16x16x32_bf16 v[136:139], v[120:123], v[72:75], v[136:139]\n\t" \
  "ds_read_b64_tr_b16 v[116:117], v161 offset:45056\n\t" \
  "v_mfma_f32_16x16x32_bf16 v[152:155], v[120:123], v[76:79], v[152:155]\n\t" \
  "ds_read_b64_tr_b16 v[118:119], v161 offset:47104\n\t" \
  "v_mfma_f32_16x16x32_bf16 v[140:143], v[124:127], v[72:75], v[140:143]\n\t" \
  "ds_read_b64_tr_b16 v[120:121], v162 offset:45056\n\t" \
  "v_mfma_f32_16x16x32_bf16 v[156:159], v[124:127], v[76:79], v[156:159]\n\t" \
  "ds_read_b64_tr_b16 v[122:123], v162 offset:47104\n\t" \
  "s_waitcnt vmcnt(14) lgkmcnt(10)\n\t" \
  "v_mfma_f32_16x16x32_bf16 v[128:131], v[96:99], v[80:83], v[128:131]\n\t" \
  "ds_read_b64_tr_b16 v[124:125], v163 offset:45056\n\t" \
  "buffer_load_dwordx4 v[48:51], v164, %[ob], s73 offen offset:0 sc1\n\t" \
  "v_mfma_f32_16x16x32_bf16 v[144:147], v[96:99], v[84:87], v[144:147]\n\t" \
  "ds_read_b64_tr_b16 v[126:127], v163 offset:47104\n\t" \
  "v_mfma_f32_16x16x32_bf16 v[132:135], v[100:103], v[80:83], v[132:135]\n\t" \
  "ds_read_b64_tr_b16 v[96:97], v160 offset:49152\n\t" \
  "v_mfma_f32_16x16x32_bf16 v[148:151], v[100:103], v[84:87], v[148:151]\n\t" \
  "ds_read_b64_tr_b16 v[98:99], v160 offset:51200\n\t" \
  "s_waitcnt lgkmcnt(10)\n\t" \
  "v_mfma_f32_16x16x32_bf16 v[136:139], v[104:107], v[80:83], v[136:139]\n\t" \
  "ds_read_b64_tr_b16 v[100:101], v161 offset:49152\n\t" \
  "buffer_load_dwordx4 v[52:55], v164, %[ob], s73 offen offset:1024 sc1\n\t" \
  "v_mfma_f32_16x16x32_bf16 v[152:155], v[104:107], v[84:87], v[152:155]\n\t" \
  "ds_read_b64_tr_b16 v[102:103], v161 offset:51200\n\t" \
  "v_mfma_f32_16x16x32_bf16 v[140:143], v[108:111], v[80:83], v[140:143]\n\t" \
  "ds_read_b64_tr_b16 v[104:105], v162 offset:49152\n\t" \
  "v_mfma_f32_16x16x32_bf16 v[156:159], v[108:111], v[84:87], v[156:159]\n\t" \
  "ds_read_b64_tr_b16 v[106:107], v162 offset:51200\n\t" \
  "s_waitcnt vmcnt(14) lgkmcnt(10)\n\t" \
  "v_mfma_f32_16x16x32_bf16 v[128:131], v[112:115], v[88:91], v[128:131]\n\t" \
  "ds_read_b64_tr_b16 v[108:109], v163 offset:49152\n\t" \
  "buffer_load_dwordx4 v[56:59], v164, %[ob], s73 offen offset:2048 sc1\n\t" \
  "v_mfma_f32_16x16x32_bf16 v[144:147], v[112:115], v[92:95], v[144:147]\n\t" \
  "ds_read_b64_tr_b16 v[110:111], v163 offset:51200\n\t" \
  "v_mfma_f32_16x16x32_bf16 v[132:135], v[116:119], v[88:91], v[132:135]\n\t" \
  "ds_read_b64_tr_b16 v[112:113], v160 offset:53248\n\t" \
  "v_mfma_f32_16x16x32_bf16 v[148:151], v[116:119], v[92:95], v[148:151]\n\t" \
  "ds_read_b64_tr_b16 v[114:115], v160 offset:55296\n\t" \
  "s_waitcnt lgkmcnt(10)\n\t" \
  "v_mfma_f32_16x16x32_bf16 v[136:139], v[120:123], v[88:91], v[136:139]\n\t" \
  "ds_read_b64_tr_b16 v[116:117], v161 offset:53248\n\t" \
  "buffer_load_dwordx4 v[60:63], v164, %[ob], s73 offen offset:3072 sc1\n\t" \
  "v_mfma_f32_16x16x32_bf16 v[152:155], v[120:123], v[92:95], v[152:155]\n\t" \
  "ds_read_b64_tr_b16 v[118:119], v161 offset:55296\n\t" \
  "v_mfma_f32_16x16x32_bf16 v[140:143], v[124:127], v[88:91], v[140:143]\n\t" \
  "ds_read_b64_tr_b16 v[120:121], v162 offset:53248\n\t" \
  "v_mfma_f32_16x16x32_bf16 v[156:159], v[124:127], v[92:95], v[156:159]\n\t" \
  "ds_read_b64_tr_b16 v[122:123], v162 offset:55296\n\t" \
  "s_waitcnt vmcnt(14) lgkmcnt(10)\n\t" \
  "v_mfma_f32_16x16x32_bf16 v[128:131], v[96:99], v[0:3], v[128:131]\n\t" \
  "ds_read_b64_tr_b16 v[124:125], v163 offset:53248\n\t" \
  "v_mfma_f32_16x16x32_bf16 v[144:147], v[96:99], v[4:7], v[144:147]\n\t" \
  "ds_read_b64_tr_b16 v[126:127], v163 offset:55296\n\t" \
  "buffer_load_dwordx4 v[64:67], v164, %[ob], s74 offen offset:0 sc1\n\t" \
  "v_mfma_f32_16x16x32_bf16 v[132:135], v[100:103], v[0:3], v[132:135]\n\t" \
  "ds_read_b64_tr_b16 v[96:97], v160 offset:57344\n\t" \
  "v_mfma_f32_16x16x32_bf16 v[148:151], v[100:103], v[4:7], v[148:151]\n\t" \
  "ds_read_b64_tr_b16 v[98:99], v160 offset:59392\n\t" \
  "s_waitcnt lgkmcnt(10)\n\t" \
  "v_mfma_f32_16x16x32_bf16 v[136:139], v[104:107], v[0:3], v[136:139]\n\t" \
  "ds_read_b64_tr_b16 v[100:101], v161 offset:57344\n\t" \
  "v_mfma_f32_16x16x32_bf16 v[152:155], v[104:107], v[4:7], v[152:155]\n\t" \
  "ds_read_b64_tr_b16 v[102:103], v161 offset:59392\n\t" \
  "buffer_load_dwordx4 v[68:71], v164, %[ob], s74 offen offset:1024 sc1\n\t" \
  "v_mfma_f32_16x16x32_bf16 v[140:143], v[108:111], v[0:3], v[140:143]\n\t" \
  "ds_read_b64_tr_b16 v[104:105], v162 offset:57344\n\t" \
  "v_mfma_f32_16x16x32_bf16 v[156:159], v[108:111], v[4:7], v[156:159]\n\t" \
  "ds_read_b64_tr_b16 v[106:107], v162 offset:59392\n\t" \
  "s_waitcnt vmcnt(14) lgkmcnt(10)\n\t" \
  "v_mfma_f32_16x16x32_bf16 v[128:131], v[112:115], v[8:11], v[128:131]\n\t" \
  "ds_read_b64_tr_b16 v[108:109], v163 offset:57344\n\t" \
  "v_mfma_f32_16x16x32_bf16 v[144:147], v[112:115], v[12:15], v[144:147]\n\t" \
  "ds_read_b64_tr_b16 v[110:111], v163 offset:59392\n\t" \
  "v_mfma_f32_16x16x32_bf16 v[132:135], v[116:119], v[8:11], v[132:135]\n\t" \
  "ds_read_b64_tr_b16 v[112:113], v160 offset:61440\n\t" \
  "buffer_load_dwordx4 v[72:75], v164, %[ob], s74 offen offset:2048 sc1\n\t" \
  "v_mfma_f32_16x16x32_bf16 v[148:151], v[116:119], v[12:15], v[148:151]\n\t" \
  "ds_read_b64_tr_b16 v[114:115], v160 offset:63488\n\t" \
  "s_waitcnt lgkmcnt(10)\n\t" \
  "v_mfma_f32_16x16x32_bf16 v[136:139], v[120:123], v[8:11], v[136:139]\n\t" \
  "ds_read_b64_tr_b16 v[116:117], v161 offset:61440\n\t" \
  "v_mfma_f32_16x16x32_bf16 v[152:155], v[120:123], v[12:15], v[152:155]\n\t" \
  "ds_read_b64_tr_b16 v[118:119], v161 offset:63488\n\t" \
  "v_mfma_f32_16x16x32_bf16 v[140:143], v[124:127], v[8:11], v[140:143]\n\t" \
  "ds_read_b64_tr_b16 v[120:121], v162 offset:61440\n\t" \
  "buffer_load_dwordx4 v[76:79], v164, %[ob], s74 offen offset:3072 sc1\n\t" \
  "v_mfma_f32_16x16x32_bf16 v[156:159], v[124:127], v[12:15], v[156:159]\n\t" \
  "ds_read_b64_tr_b16 v[122:123], v162 offset:63488\n\t" \
  "s_waitcnt vmcnt(14) lgkmcnt(10)\n\t" \
  "v_mfma_f32_16x16x32_bf16 v[128:131], v[96:99], v[16:19], v[128:131]\n\t" \
  "ds_read_b64_tr_b16 v[124:125], v163 offset:61440\n\t" \
  "v_mfma_f32_16x16x32_bf16 v[144:147], v[96:99], v[20:23], v[144:147]\n\t" \
  "ds_read_b64_tr_b16 v[126:127], v163 offset:63488\n\t" \
  "v_mfma_f32_16x16x32_bf16 v[132:135], v[100:103], v[16:19], v[132:135]\n\t" \
  "v_add_u32 v160, 0x10000, v160\n\t" \
  "ds_read_b64_tr_b16 v[96:97], v160 offset:0\n\t" \
  "v_mfma_f32_16x16x32_bf16 v[148:151], v[100:103], v[20:23], v[148:151]\n\t" \
  "ds_read_b64_tr_b16 v[98:99], v160 offset:2048\n\t" \
  "s_waitcnt lgkmcnt(10)\n\t" \
  "v_mfma_f32_16x16x32_bf16 v[136:139], v[104:107], v[16:19], v[136:139]\n\t" \
  "v_add_u32 v161, 0x10000, v161\n\t" \
  "ds_read_b64_tr_b16 v[100:101], v161 offset:0\n\t" \
  "buffer_load_dwordx4 v[80:83], v164, %[ob], s75 offen offset:0 sc1\n\t" \
  "v_mfma_f32_16x16x32_bf16 v[152:155], v[104:107], v[20:23], v[152:155]\n\t" \
  "ds_read_b64_tr_b16 v[102:103], v161 offset:2048\n\t" \
  "v_mfma_f32_16x16x32_bf16 v[140:143], v[108:111], v[16:19], v[140:143]\n\t" \
  "v_add_u32 v162, 0x10000, v162\n\t" \
  "ds_read_b64_tr_b16 v[104:105], v162 offset:0\n\t" \
  "v_mfma_f32_16x16x32_bf16 v[156:159], v[108:111], v[20:23], v[156:159]\n\t" \
  "ds_read_b64_tr_b16 v[106:107], v162 offset:2048\n\t" \
  "s_waitcnt vmcnt(13) lgkmcnt(10)\n\t" \
  "v_mfma_f32_16x16x32_bf16 v[128:131], v[112:115], v[24:27], v[128:131]\n\t" \
  "v_add_u32 v163, 0x10000, v163\n\t" \
  "ds_read_b64_tr_b16 v[108:109], v163 offset:0\n\t" \
  "buffer_load_dwordx4 v[84:87], v164, %[ob], s75 offen offset:1024 sc1\n\t" \
  "v_mfma_f32_16x16x32_bf16 v[144:147], v[112:115], v[28:31], v[144:147]\n\t" \
  "ds_read_b64_tr_b16 v[110:111], v163 offset:2048\n\t" \
  "v_mfma_f32_16x16x32_bf16 v[132:135], v[116:119], v[24:27], v[132:135]\n\t" \
  "ds_read_b64_tr_b16 v[112:113], v160 offset:4096\n\t" \
  "v_mfma_f32_16x16x32_bf16 v[148:151], v[116:119], v[28:31], v[148:151]\n\t" \
  "ds_read_b64_tr_b16 v[114:115], v160 offset:6144\n\t" \
  "s_waitcnt lgkmcnt(10)\n\t" \
  "v_mfma_f32_16x16x32_bf16 v[136:139], v[120:123], v[24:27], v[136:139]\n\t" \
  "ds_read_b64_tr_b16 v[116:117], v161 offset:4096\n\t" \
  "buffer_load_dwordx4 v[88:91], v164, %[ob], s75 offen offset:2048 sc1\n\t" \
  "v_mfma_f32_16x16x32_bf16 v[152:155], v[120:123], v[28:31], v[152:155]\n\t" \
  "ds_read_b64_tr_b16 v[118:119], v161 offset:6144\n\t" \
  "v_mfma_f32_16x16x32_bf16 v[140:143], v[124:127], v[24:27], v[140:143]\n\t" \
  "ds_read_b64_tr_b16 v[120:121], v162 offset:4096\n\t" \
  "v_mfma_f32_16x16x32_bf16 v[156:159], v[124:127], v[28:31], v[156:159]\n\t" \
  "ds_read_b64_tr_b16 v[122:123], v162 offset:6144\n\t" \
  "s_waitcnt vmcnt(13) lgkmcnt(10)\n\t" \
  "v_mfma_f32_16x16x32_bf16 v[128:131], v[96:99], v[32:35], v[128:131]\n\t" \
  "ds_read_b64_tr_b16 v[124:125], v163 offset:4096\n\t" \
  "buffer_load_dwordx4 v[92:95], v164, %[ob], s75 offen offset:3072 sc1\n\t" \
  "v_mfma_f32_16x16x32_bf16 v[144:147], v[96:99], v[36:39], v[144:147]\n\t" \
  "ds_read_b64_tr_b16 v[126:127], v163 offset:6144\n\t" \
  "v_mfma_f32_16x16x32_bf16 v[132:135], v[100:103], v[32:35], v[132:135]\n\t" \
  "ds_read_b64_tr_b16 v[96:97], v160 offset:8192\n\t" \
  "v_mfma_f32_16x16x32_bf16 v[148:151], v[100:103], v[36:39], v[148:151]\n\t" \
  "ds_read_b64_tr_b16 v[98:99], v160 offset:10240\n\t" \
  "s_waitcnt lgkmcnt(10)\n\t" \
  "v_mfma_f32_16x16x32_bf16 v[136:139], v[104:107], v[32:35], v[136:139]\n\t" \
  "ds_read_b64_tr_b16 v[100:101], v161 offset:8192\n\t" \
  "v_mfma_f32_16x16x32_bf16 v[152:155], v[104:107], v[36:39], v[152:155]\n\t" \
  "ds_read_b64_tr_b16 v[102:103], v161 offset:10240\n\t" \
  "buffer_load_dwordx4 v[0:3], v164, %[ob], s76 offen offset:0 sc1\n\t" \
  "v_mfma_f32_16x16x32_bf16 v[140:143], v[108:111], v[32:35], v[140:143]\n\t" \
  "ds_read_b64_tr_b16 v[104:105], v162 offset:8192\n\t" \
  "v_mfma_f32_16x16x32_bf16 v[156:159], v[108:111], v[36:39], v[156:159]\n\t" \
  "ds_read_b64_tr_b16 v[106:107], v162 offset:10240\n\t" \
  "s_waitcnt vmcnt(13) lgkmcnt(10)\n\t" \
  "v_mfma_f32_16x16x32_bf16 v[128:131], v[112:115], v[40:43], v[128:131]\n\t" \
  "ds_read_b64_tr_b16 v[108:109], v163 offset:8192\n\t" \
  "v_mfma_f32_16x16x32_bf16 v[144:147], v[112:115], v[44:47], v[144:147]\n\t" \
  "ds_read_b64_tr_b16 v[110:111], v163 offset:10240\n\t" \
  "buffer_load_dwordx4 v[4:7], v164, %[ob], s76 offen offset:1024 sc1\n\t" \
  "v_mfma_f32_16x16x32_bf16 v[132:135], v[116:119], v[40:43], v[132:135]\n\t" \
  "ds_read_b64_tr_b16 v[112:113], v160 offset:12288\n\t" \
  "v_mfma_f32_16x16x32_bf16 v[148:151], v[116:119], v[44:47], v[148:151]\n\t" \
  "ds_read_b64_tr_b16 v[114:115], v160 offset:14336\n\t" \
  "s_waitcnt lgkmcnt(10)\n\t" \
  "v_mfma_f32_16x16x32_bf16 v[136:139], v[120:123], v[40:43], v[136:139]\n\t" \
  "ds_read_b64_tr_b16 v[116:117], v161 offset:12288\n\t" \
  "v_mfma_f32_16x16x32_bf16 v[152:155], v[120:123], v[44:47], v[152:155]\n\t" \
  "ds_read_b64_tr_b16 v[118:119], v161 offset:14336\n\t" \
  "buffer_load_dwordx4 v[8:11], v164, %[ob], s76 offen offset:2048 sc1\n\t" \
  "v_mfma_f32_16x16x32_bf16 v[140:143], v[124:127], v[40:43], v[140:143]\n\t" \
  "ds_read_b64_tr_b16 v[120:121], v162 offset:12288\n\t" \
  "v_mfma_f32_16x16x32_bf16 v[156:159], v[124:127], v[44:47], v[156:159]\n\t" \
  "ds_read_b64_tr_b16 v[122:123], v162 offset:14336\n\t" \
  "s_waitcnt vmcnt(13) lgkmcnt(10)\n\t" \
  "v_mfma_f32_16x16x32_bf16 v[128:131], v[96:99], v[48:51], v[128:131]\n\t" \
  "ds_read_b64_tr_b16 v[124:125], v163 offset:12288\n\t" \
  "v_mfma_f32_16x16x32_bf16 v[144:147], v[96:99], v[52:55], v[144:147]\n\t" \
  "ds_read_b64_tr_b16 v[126:127], v163 offset:14336\n\t" \
  "v_mfma_f32_16x16x32_bf16 v[132:135], v[100:103], v[48:51], v[132:135]\n\t" \
  "ds_read_b64_tr_b16 v[96:97], v160 offset:16384\n\t" \
  "buffer_load_dwordx4 v[12:15], v164, %[ob], s76 offen offset:3072 sc1\n\t" \
  "v_mfma_f32_16x16x32_bf16 v[148:151], v[100:103], v[52:55], v[148:151]\n\t" \
  "ds_read_b64_tr_b16 v[98:99], v160 offset:18432\n\t" \
  "s_waitcnt lgkmcnt(10)\n\t" \
  "v_mfma_f32_16x16x32_bf16 v[136:139], v[104:107], v[48:51], v[136:139]\n\t" \
  "ds_read_b64_tr_b16 v[100:101], v161 offset:16384\n\t" \
  "v_mfma_f32_16x16x32_bf16 v[152:155], v[104:107], v[52:55], v[152:155]\n\t" \
  "ds_read_b64_tr_b16 v[102:103], v161 offset:18432\n\t" \
  "v_mfma_f32_16x16x32_bf16 v[140:143], v[108:111], v[48:51], v[140:143]\n\t" \
  "ds_read_b64_tr_b16 v[104:105], v162 offset:16384\n\t" \
  "buffer_load_dwordx4 v[16:19], v164, %[ob], s77 offen offset:0 sc1\n\t" \
  "v_mfma_f32_16x16x32_bf16 v[156:159], v[108:111], v[52:55], v[156:159]\n\t" \
  "ds_read_b64_tr_b16 v[106:107], v162 offset:18432\n\t" \
  "s_waitcnt vmcnt(13) lgkmcnt(10)\n\t" \
  "v_mfma_f32_16x16x32_bf16 v[128:131], v[112:115], v[56:59], v[128:131]\n\t" \
  "ds_read_b64_tr_b16 v[108:109], v163 offset:16384\n\t" \
  "v_mfma_f32_16x16x32_bf16 v[144:147], v[112:115], v[60:63], v[144:147]\n\t" \
  "ds_read_b64_tr_b16 v[110:111], v163 offset:18432\n\t" \
  "v_mfma_f32_16x16x32_bf16 v[132:135], v[116:119], v[56:59], v[132:135]\n\t" \
  "ds_read_b64_tr_b16 v[112:113], v160 offset:20480\n\t" \
  "v_mfma_f32_16x16x32_bf16 v[148:151], v[116:119], v[60:63], v[148:151]\n\t" \
  "ds_read_b64_tr_b16 v[114:115], v160 offset:22528\n\t" \
  "s_waitcnt lgkmcnt(10)\n\t" \
  "v_mfma_f32_16x16x32_bf16 v[136:139], v[120:123], v[56:59], v[136:139]\n\t" \
  "ds_read_b64_tr_b16 v[116:117], v161 offset:20480\n\t" \
  "buffer_load_dwordx4 v[20:23], v164, %[ob], s77 offen offset:1024 sc1\n\t" \
  "ds_read_b32 v165, v165\n\t" \
  "v_mfma_f32_16x16x32_bf16 v[152:155], v[120:123], v[60:63], v[152:155]\n\t" \
  "ds_read_b64_tr_b16 v[118:119], v161 offset:22528\n\t" \
  "v_mfma_f32_16x16x32_bf16 v[140:143], v[124:127], v[56:59], v[140:143]\n\t" \
  "ds_read_b64_tr_b16 v[120:121], v162 offset:20480\n\t" \
  "v_mfma_f32_16x16x32_bf16 v[156:159], v[124:127], v[60:63], v[156:159]\n\t" \
  "ds_read_b64_tr_b16 v[122:123], v162 offset:22528\n\t" \
  "s_waitcnt vmcnt(12) lgkmcnt(11)\n\t" \
  "v_mfma_f32_16x16x32_bf16 v[128:131], v[96:99], v[64:67], v[128:131]\n\t" \
  "ds_read_b64_tr_b16 v[124:125], v163 offset:20480\n\t" \
  "buffer_load_dwordx4 v[24:27], v164, %[ob], s77 offen offset:2048 sc1\n\t" \
  "v_mfma_f32_16x16x32_bf16 v[144:147], v[96:99], v[68:71], v[144:147]\n\t" \
  "ds_read_b64_tr_b16 v[126:127], v163 offset:22528\n\t" \
  "v_mfma_f32_16x16x32_bf16 v[132:135], v[100:103], v[64:67], v[132:135]\n\t" \
  "ds_read_b64_tr_b16 v[96:97], v160 offset:24576\n\t" \
  "v_mfma_f32_16x16x32_bf16 v[148:151], v[100:103], v[68:71], v[148:151]\n\t" \
  "ds_read_b64_tr_b16 v[98:99], v160 offset:26624\n\t" \
  "s_waitcnt lgkmcnt(11)\n\t" \
  "v_mfma_f32_16x16x32_bf16 v[136:139], v[104:107], v[64:67], v[136:139]\n\t" \
  "ds_read_b64_tr_b16 v[100:101], v161 offset:24576\n\t" \
  "buffer_load_dwordx4 v[28:31], v164, %[ob], s77 offen offset:3072 sc1\n\t" \
  "v_mfma_f32_16x16x32_bf16 v[152:155], v[104:107], v[68:71], v[152:155]\n\t" \
  "ds_read_b64_tr_b16 v[102:103], v161 offset:26624\n\t" \
  "s_waitcnt lgkmcnt(9)\n\t" \
  "v_mfma_f32_16x16x32_bf16 v[140:143], v[108:111], v[64:67], v[140:143]\n\t" \
  "ds_read_b64_tr_b16 v[104:105], v162 offset:24576\n\t" \
  "v_cmp_lt_u32 vcc, 15, v165\n\t" \
  "v_mfma_f32_16x16x32_bf16 v[156:159], v[108:111], v[68:71], v[156:159]\n\t" \
  "ds_read_b64_tr_b16 v[106:107], v162 offset:26624\n\t" \
  "s_cbranch_vccz QA14_%=\n\t" \
  "QB14_%=:\n\t" \
  "s_waitcnt vmcnt(12) lgkmcnt(10)\n\t" \
  "v_mfma_f32_16x16x32_bf16 v[128:131], v[112:115], v[72:75], v[128:131]\n\t" \
  "ds_read_b64_tr_b16 v[108:109], v163 offset:24576\n\t" \
  "buffer_load_dwordx4 v[32:35], v164, %[ob], s78 offen offset:0 sc1\n\t" \
  "v_mfma_f32_16x16x32_bf16 v[144:147], v[112:115], v[76:79], v[144:147]\n\t" \
  "ds_read_b64_tr_b16 v[110:111], v163 offset:26624\n\t" \
  "v_mfma_f32_16x16x32_bf16 v[132:135], v[116:119], v[72:75], v[132:135]\n\t" \
  "ds_read_b64_tr_b16 v[112:113], v160 offset:28672\n\t" \
  "v_mfma_f32_16x16x32_bf16 v[148:151], v[116:119], v[76:79], v[148:151]\n\t" \
  "ds_read_b64_tr_b16 v[114:115], v160 offset:30720\n\t" \
  "s_waitcnt lgkmcnt(10)\n\t" \
  "v_mfma_f32_16x16x32_bf16 v[136:139], v[120:123], v[72:75], v[136:139]\n\t" \
  "ds_read_b64_tr_b16 v[116:117], v161 offset:28672\n\t" \
  "v_mfma_f32_16x16x32_bf16 v[152:155], v[120:123], v[76:79], v[152:155]\n\t" \
  "ds_read_b64_tr_b16 v[118:119], v161 offset:30720\n\t" \
  "buffer_load_dwordx4 v[36:39], v164, %[ob], s78 offen offset:1024 sc1\n\t" \
  "v_mfma_f32_16x16x32_bf16 v[140:143], v[124:127], v[72:75], v[140:143]\n\t" \
  "ds_read_b64_tr_b16 v[120:121], v162 offset:28672\n\t" \
  "v_mfma_f32_16x16x32_bf16 v[156:159], v[124:127], v[76:79], v[156:159]\n\t" \
  "ds_read_b64_tr_b16 v[122:123], v162 offset:30720\n\t" \
  "s_waitcnt vmcnt(12) lgkmcnt(10)\n\t" \
  "v_mfma_f32_16x16x32_bf16 v[128:131], v[96:99], v[80:83], v[128:131]\n\t" \
  "ds_read_b64_tr_b16 v[124:125], v163 offset:28672\n\t" \
  "v_mfma_f32_16x16x32_bf16 v[144:147], v[96:99], v[84:87], v[144:147]\n\t" \
  "ds_read_b64_tr_b16 v[126:127], v163 offset:30720\n\t" \
  "buffer_load_dwordx4 v[40:43], v164, %[ob], s78 offen offset:2048 sc1\n\t" \
  "v_mfma_f32_16x16x32_bf16 v[132:135], v[100:103], v[80:83], v[132:135]\n\t" \
  "ds_read_b64_tr_b16 v[96:97], v160 offset:32768\n\t" \
  "v_mfma_f32_16x16x32_bf16 v[148:151], v[100:103], v[84:87], v[148:151]\n\t" \
  "ds_read_b64_tr_b16 v[98:99], v160 offset:34816\n\t" \
  "s_waitcnt lgkmcnt(10)\n\t" \
  "v_mfma_f32_16x16x32_bf16 v[136:139], v[104:107], v[80:83], v[136:139]\n\t" \
  "ds_read_b64_tr_b16 v[100:101], v161 offset:32768\n\t" \
  "v_mfma_f32_16x16x32_bf16 v[152:155], v[104:107], v[84:87], v[152:155]\n\t" \
  "ds_read_b64_tr_b16 v[102:103], v161 offset:34816\n\t" \
  "v_mfma_f32_16x16x32_bf16 v[140:143], v[108:111], v[80:83], v[140:143]\n\t" \
  "ds_read_b64_tr_b16 v[104:105], v162 offset:32768\n\t" \
  "buffer_load_dwordx4 v[44:47], v164, %[ob], s78 offen offset:3072 sc1\n\t" \
  "v_mfma_f32_16x16x32_bf16 v[156:159], v[108:111], v[84:87], v[156:159]\n\t" \
  "ds_read_b64_tr_b16 v[106:107], v162 offset:34816\n\t" \
  "s_waitcnt vmcnt(12) lgkmcnt(10)\n\t" \
  "v_mfma_f32_16x16x32_bf16 v[128:131], v[112:115], v[88:91], v[128:131]\n\t" \
  "ds_read_b64_tr_b16 v[108:109], v163 offset:32768\n\t" \
  "v_mfma_f32_16x16x32_bf16 v[144:147], v[112:115], v[92:95], v[144:147]\n\t" \
  "ds_read_b64_tr_b16 v[110:111], v163 offset:34816\n\t" \
  "v_mfma_f32_16x16x32_bf16 v[132:135], v[116:119], v[88:91], v[132:135]\n\t" \
  "ds_read_b64_tr_b16 v[112:113], v160 offset:36864\n\t" \
  "buffer_load_dwordx4 v[48:51], v164, %[ob], s79 offen offset:0 sc1\n\t" \
  "v_mfma_f32_16x16x32_bf16 v[148:151], v[116:119], v[92:95], v[148:151]\n\t" \
  "ds_read_b64_tr_b16 v[114:115], v160 offset:38912\n\t" \
  "s_waitcnt lgkmcnt(10)\n\t" \
  "v_mfma_f32_16x16x32_bf16 v[136:139], v[120:123], v[88:91], v[136:139]\n\t" \
  "ds_read_b64_tr_b16 v[116:117], v161 offset:36864\n\t" \
  "v_mfma_f32_16x16x32_bf16 v[152:155], v[120:123], v[92:95], v[152:155]\n\t" \
  "ds_read_b64_tr_b16 v[118:119], v161 offset:38912\n\t" \
  "v_mfma_f32_16x16x32_bf16 v[140:143], v[124:127], v[88:91], v[140:143]\n\t" \
  "ds_read_b64_tr_b16 v[120:121], v162 offset:36864\n\t" \
  "v_mfma_f32_16x16x32_bf16 v[156:159], v[124:127], v[92:95], v[156:159]\n\t" \
  "ds_read_b64_tr_b16 v[122:123], v162 offset:38912\n\t" \
  "s_waitcnt vmcnt(11) lgkmcnt(10)\n\t" \
  "v_mfma_f32_16x16x32_bf16 v[128:131], v[96:99], v[0:3], v[128:131]\n\t" \
  "ds_read_b64_tr_b16 v[124:125], v163 offset:36864\n\t" \
  "buffer_load_dwordx4 v[52:55], v164, %[ob], s79 offen offset:1024 sc1\n\t" \
  "v_mfma_f32_16x16x32_bf16 v[144:147], v[96:99], v[4:7], v[144:147]\n\t" \
  "ds_read_b64_tr_b16 v[126:127], v163 offset:38912\n\t" \
  "v_mfma_f32_16x16x32_bf16 v[132:135], v[100:103], v[0:3], v[132:135]\n\t" \
  "ds_read_b64_tr_b16 v[96:97], v160 offset:40960\n\t" \
  "v_mfma_f32_16x16x32_bf16 v[148:151], v[100:103], v[4:7], v[148:151]\n\t" \
  "ds_read_b64_tr_b16 v[98:99], v160 offset:43008\n\t" \
  "s_waitcnt lgkmcnt(10)\n\t" \
  "v_mfma_f32_16x16x32_bf16 v[136:139], v[104:107], v[0:3], v[136:139]\n\t" \
  "ds_read_b64_tr_b16 v[100:101], v161 offset:40960\n\t" \
  "buffer_load_dwordx4 v[56:59], v164, %[ob], s79 offen offset:2048 sc1\n\t" \
  "v_mfma_f32_16x16x32_bf16 v[152:155], v[104:107], v[4:7], v[152:155]\n\t" \
  "ds_read_b64_tr_b16 v[102:103], v161 offset:43008\n\t" \
  "v_mfma_f32_16x16x32_bf16 v[140:143], v[108:111], v[0:3], v[140:143]\n\t" \
  "ds_read_b64_tr_b16 v[104:105], v162 offset:40960\n\t" \
  "v_mfma_f32_16x16x32_bf16 v[156:159], v[108:111], v[4:7], v[156:159]\n\t" \
  "ds_read_b64_tr_b16 v[106:107], v162 offset:43008\n\t" \
  "s_waitcnt vmcnt(11) lgkmcnt(10)\n\t" \
  "v_mfma_f32_16x16x32_bf16 v[128:131], v[112:115], v[8:11], v[128:131]\n\t" \
  "ds_read_b64_tr_b16 v[108:109], v163 offset:40960\n\t" \
  "buffer_load_dwordx4 v[60:63], v164, %[ob], s79 offen offset:3072 sc1\n\t" \
  "v_mfma_f32_16x16x32_bf16 v[144:147], v[112:115], v[12:15], v[144:147]\n\t" \
  "ds_read_b64_tr_b16 v[110:111], v163 offset:43008\n\t" \
  "v_mfma_f32_16x16x32_bf16 v[132:135], v[116:119], v[8:11], v[132:135]\n\t" \
  "ds_read_b64_tr_b16 v[112:113], v160 offset:45056\n\t" \
  "v_mfma_f32_16x16x32_bf16 v[148:151], v[116:119], v[12:15], v[148:151]\n\t" \
  "ds_read_b64_tr_b16 v[114:115], v160 offset:47104\n\t" \
  "s_waitcnt lgkmcnt(10)\n\t" \
  "v_mfma_f32_16x16x32_bf16 v[136:139], v[120:123], v[8:11], v[136:139]\n\t" \
  "ds_read_b64_tr_b16 v[116:117], v161 offset:45056\n\t" \
  "v_mfma_f32_16x16x32_bf16 v[152:155], v[120:123], v[12:15], v[152:155]\n\t" \
  "ds_read_b64_tr_b16 v[118:119], v161 offset:47104\n\t" \
  "v_mfma_f32_16x16x32_bf16 v[140:143], v[124:127], v[8:11], v[140:143]\n\t" \
  "ds_read_b64_tr_b16 v[120:121], v162 offset:45056\n\t" \
  "v_mfma_f32_16x16x32_bf16 v[156:159], v[124:127], v[12:15], v[156:159]\n\t" \
  "ds_read_b64_tr_b16 v[122:123], v162 offset:47104\n\t" \
  "s_waitcnt vmcnt(10) lgkmcnt(10)\n\t" \
  "v_mfma_f32_16x16x32_bf16 v[128:131], v[96:99], v[16:19], v[128:131]\n\t" \
  "ds_read_b64_tr_b16 v[124:125], v163 offset:45056\n\t" \
  "v_mfma_f32_16x16x32_bf16 v[144:147], v[96:99], v[20:23], v[144:147]\n\t" \
  "ds_read_b64_tr_b16 v[126:127], v163 offset:47104\n\t" \
  "v_mfma_f32_16x16x32_bf16 v[132:135], v[100:103], v[16:19], v[132:135]\n\t" \
  "ds_read_b64_tr_b16 v[96:97], v160 offset:49152\n\t" \
  "v_mfma_f32_16x16x32_bf16 v[148:151], v[100:103], v[20:23], v[148:151]\n\t" \
  "ds_read_b64_tr_b16 v[98:99], v160 offset:51200\n\t" \
  "s_waitcnt lgkmcnt(10)\n\t" \
  "v_mfma_f32_16x16x32_bf16 v[136:139], v[104:107], v[16:19], v[136:139]\n\t" \
  "ds_read_b64_tr_b16 v[100:101], v161 offset:49152\n\t" \
  "v_xor_b32 v0, 32, v167\n\t" \
  "v_mfma_f32_16x16x32_bf16 v[152:155], v[104:107], v[20:23], v[152:155]\n\t" \
  "ds_read_b64_tr_b16 v[102:103], v161 offset:51200\n\t" \
  "v_xor_b32 v1, 64, v167\n\t" \
  "v_mfma_f32_16x16x32_bf16 v[140:143], v[108:111], v[16:19], v[140:143]\n\t" \
  "ds_read_b64_tr_b16 v[104:105], v162 offset:49152\n\t" \
  "v_xor_b32 v2, 0x60, v167\n\t" \
  "v_mfma_f32_16x16x32_bf16 v[156:159], v[108:111], v[20:23], v[156:159]\n\t" \
  "ds_read_b64_tr_b16 v[106:107], v162 offset:51200\n\t" \
  "s_waitcnt vmcnt(8) lgkmcnt(10)\n\t" \
  "v_mfma_f32_16x16x32_bf16 v[128:131], v[112:115], v[24:27], v[128:131]\n\t" \
  "ds_read_b64_tr_b16 v[108:109], v163 offset:49152\n\t" \
  "v_bfe_u32 v5, v164, 7, 3\n\t" \
  "v_mfma_f32_16x16x32_bf16 v[144:147], v[112:115], v[28:31], v[144:147]\n\t" \
  "ds_read_b64_tr_b16 v[110:111], v163 offset:51200\n\t" \
  "v_and_b32 v4, 0x70, v164\n\t" \
  "v_mfma_f32_16x16x32_bf16 v[132:135], v[116:119], v[24:27], v[132:135]\n\t" \
  "ds_read_b64_tr_b16 v[112:113], v160 offset:53248\n\t" \
  "v_lshl_or_b32 v4, v5, 12, v4\n\t" \
  "v_mfma_f32_16x16x32_bf16 v[148:151], v[116:119], v[28:31], v[148:151]\n\t" \
  "ds_read_b64_tr_b16 v[114:115], v160 offset:55296\n\t" \
  "s_waitcnt lgkmcnt(10)\n\t" \
  "v_mfma_f32_16x16x32_bf16 v[136:139], v[120:123], v[24:27], v[136:139]\n\t" \
  "ds_read_b64_tr_b16 v[116:117], v161 offset:53248\n\t" \
  "v_add_u32 v5, 0x8000, v4\n\t" \
  "v_mfma_f32_16x16x32_bf16 v[152:155], v[120:123], v[28:31], v[152:155]\n\t" \
  "ds_read_b64_tr_b16 v[118:119], v161 offset:55296\n\t" \
  "v_add_u32 v6, 0x10000, v4\n\t" \
  "v_mfma_f32_16x16x32_bf16 v[140:143], v[124:127], v[24:27], v[140:143]\n\t" \
  "ds_read_b64_tr_b16 v[120:121], v162 offset:53248\n\t" \
  "v_add_u32 v7, 0x18000, v4\n\t" \
  "v_mfma_f32_16x16x32_bf16 v[156:159], v[124:127], v[28:31], v[156:159]\n\t" \
  "ds_read_b64_tr_b16 v[122:123], v162 offset:55296\n\t" \
  "s_waitcnt vmcnt(6) lgkmcnt(10)\n\t" \
  "v_mfma_f32_16x16x32_bf16 v[128:131], v[96:99], v[32:35], v[128:131]\n\t" \
  "ds_read_b64_tr_b16 v[124:125], v163 offset:53248\n\t" \
  "v_mfma_f32_16x16x32_bf16 v[144:147], v[96:99], v[36:39], v[144:147]\n\t" \
  "ds_read_b64_tr_b16 v[126:127], v163 offset:55296\n\t" \
  "v_mfma_f32_16x16x32_bf16 v[132:135], v[100:103], v[32:35], v[132:135]\n\t" \
  "ds_read_b64_tr_b16 v[96:97], v160 offset:57344\n\t" \
  "v_mfma_f32_16x16x32_bf16 v[148:151], v[100:103], v[36:39], v[148:151]\n\t" \
  "ds_read_b64_tr_b16 v[98:99], v160 offset:59392\n\t" \
  "s_waitcnt lgkmcnt(10)\n\t" \
  "v_mfma_f32_16x16x32_bf16 v[136:139], v[104:107], v[32:35], v[136:139]\n\t" \
  "ds_read_b64_tr_b16 v[100:101], v161 offset:57344\n\t" \
  "v_mfma_f32_16x16x32_bf16 v[152:155], v[104:107], v[36:39], v[152:155]\n\t" \
  "ds_read_b64_tr_b16 v[102:103], v161 offset:59392\n\t" \
  "v_mfma_f32_16x16x32_bf16 v[140:143], v[108:111], v[32:35], v[140:143]\n\t" \
  "ds_read_b64_tr_b16 v[104:105], v162 offset:57344\n\t" \
  "v_mfma_f32_16x16x32_bf16 v[156:159], v[108:111], v[36:39], v[156:159]\n\t" \
  "ds_read_b64_tr_b16 v[106:107], v162 offset:59392\n\t" \
  "s_waitcnt vmcnt(4) lgkmcnt(10)\n\t" \
  "v_mfma_f32_16x16x32_bf16 v[128:131], v[112:115], v[40:43], v[128:131]\n\t" \
  "ds_read_b64_tr_b16 v[108:109], v163 offset:57344\n\t" \
  "v_mfma_f32_16x16x32_bf16 v[144:147], v[112:115], v[44:47], v[144:147]\n\t" \
  "ds_read_b64_tr_b16 v[110:111], v163 offset:59392\n\t" \
  "v_mfma_f32_16x16x32_bf16 v[132:135], v[116:119], v[40:43], v[132:135]\n\t" \
  "ds_read_b64_tr_b16 v[112:113], v160 offset:61440\n\t" \
  "v_mfma_f32_16x16x32_bf16 v[148:151], v[116:119], v[44:47], v[148:151]\n\t" \
  "ds_read_b64_tr_b16 v[114:115], v160 offset:63488\n\t" \
  "s_waitcnt lgkmcnt(10)\n\t" \
  "v_mfma_f32_16x16x32_bf16 v[136:139], v[120:123], v[40:43], v[136:139]\n\t" \
  "ds_read_b64_tr_b16 v[116:117], v161 offset:61440\n\t" \
  "v_mfma_f32_16x16x32_bf16 v[152:155], v[120:123], v[44:47], v[152:155]\n\t" \
  "ds_read_b64_tr_b16 v[118:119], v161 offset:63488\n\t" \
  "v_mfma_f32_16x16x32_bf16 v[140:143], v[124:127], v[40:43], v[140:143]\n\t" \
  "ds_read_b64_tr_b16 v[120:121], v162 offset:61440\n\t" \
  "v_mfma_f32_16x16x32_bf16 v[156:159], v[124:127], v[44:47], v[156:159]\n\t" \
  "ds_read_b64_tr_b16 v[122:123], v162 offset:63488\n\t" \
  "s_waitcnt vmcnt(0) lgkmcnt(10)\n\t" \
  "v_mfma_f32_16x16x32_bf16 v[128:131], v[96:99], v[48:51], v[128:131]\n\t" \
  "ds_read_b64_tr_b16 v[124:125], v163 offset:61440\n\t" \
  "v_mfma_f32_16x16x32_bf16 v[132:135], v[100:103], v[48:51], v[132:135]\n\t" \
  "ds_read_b64_tr_b16 v[126:127], v163 offset:63488\n\t" \
  "s_waitcnt lgkmcnt(6)\n\t" \
  "v_mfma_f32_16x16x32_bf16 v[136:139], v[104:107], v[48:51], v[136:139]\n\t" \
  "v_mfma_f32_16x16x32_bf16 v[140:143], v[108:111], v[48:51], v[140:143]\n\t" \
  "v_mfma_f32_16x16x32_bf16 v[128:131], v[112:115], v[56:59], v[128:131]\n\t" \
  "s_waitcnt lgkmcnt(2)\n\t" \
  "v_mfma_f32_16x16x32_bf16 v[132:135], v[116:119], v[56:59], v[132:135]\n\t" \
  "v_mfma_f32_16x16x32_bf16 v[136:139], v[120:123], v[56:59], v[136:139]\n\t" \
  "v_mfma_f32_16x16x32_bf16 v[144:147], v[96:99], v[52:55], v[144:147]\n\t" \
  "s_waitcnt lgkmcnt(0)\n\t" \
  "v_mfma_f32_16x16x32_bf16 v[140:143], v[124:127], v[56:59], v[140:143]\n\t" \
  "v_mfma_f32_16x16x32_bf16 v[144:147], v[112:115], v[60:63], v[144:147]\n\t" \
  "s_nop 0\n\t" \
  "v_cvt_pk_bf16_f32 v16, v128, v129\n\t" \
  "v_mfma_f32_16x16x32_bf16 v[148:151], v[100:103], v[52:55], v[148:151]\n\t" \
  "v_cvt_pk_bf16_f32 v17, v130, v131\n\t" \
  "v_cvt_pk_bf16_f32 v18, v132, v133\n\t" \
  "v_mfma_f32_16x16x32_bf16 v[148:151], v[116:119], v[60:63], v[148:151]\n\t" \
  "v_cvt_pk_bf16_f32 v19, v134, v135\n\t" \
  "ds_write_b64 v167, v[16:17]\n\t" \
  "v_mfma_f32_16x16x32_bf16 v[152:155], v[104:107], v[52:55], v[152:155]\n\t" \
  "v_cvt_pk_bf16_f32 v20, v136, v137\n\t" \
  "ds_write_b64 v0, v[18:19]\n\t" \
  "v_mfma_f32_16x16x32_bf16 v[152:155], v[120:123], v[60:63], v[152:155]\n\t" \
  "v_cvt_pk_bf16_f32 v21, v138, v139\n\t" \
  "v_cvt_pk_bf16_f32 v22, v140, v141\n\t" \
  "v_mfma_f32_16x16x32_bf16 v[156:159], v[108:111], v[52:55], v[156:159]\n\t" \
  "v_cvt_pk_bf16_f32 v23, v142, v143\n\t" \
  "ds_write_b64 v1, v[20:21]\n\t" \
  "v_mfma_f32_16x16x32_bf16 v[156:159], v[124:127], v[60:63], v[156:159]\n\t" \
  "v_cvt_pk_bf16_f32 v24, v144, v145\n\t" \
  "ds_write_b64 v2, v[22:23]\n\t" \
  "v_cvt_pk_bf16_f32 v25, v146, v147\n\t" \
  "ds_read_b128 v[64:67], v166 offset:0\n\t" \
  "v_cvt_pk_bf16_f32 v26, v148, v149\n\t" \
  "ds_read_b128 v[68:71], v166 offset:1024\n\t" \
  "v_cvt_pk_bf16_f32 v27, v150, v151\n\t" \
  "ds_write_b64 v167, v[24:25]\n\t" \
  "v_cvt_pk_bf16_f32 v28, v152, v153\n\t" \
  "v_cvt_pk_bf16_f32 v29, v154, v155\n\t" \
  "ds_write_b64 v0, v[26:27]\n\t" \
  "v_cvt_pk_bf16_f32 v30, v156, v157\n\t" \
  "v_cvt_pk_bf16_f32 v31, v158, v159\n\t" \
  "ds_write_b64 v1, v[28:29]\n\t" \
  "ds_write_b64 v2, v[30:31]\n\t" \
  "ds_read_b128 v[72:75], v166 offset:0\n\t" \
  "ds_read_b128 v[76:79], v166 offset:1024\n\t" \
  "s_waitcnt lgkmcnt(7)\n\t" \
  "global_store_dwordx4 v4, v[64:67], %[op] sc1\n\t" \
  "s_waitcnt lgkmcnt(6)\n\t" \
  "global_store_dwordx4 v5, v[68:71], %[op] sc1\n\t" \
  "s_waitcnt lgkmcnt(1)\n\t" \
  "global_store_dwordx4 v6, v[72:75], %[op] sc1\n\t" \
  "s_waitcnt lgkmcnt(0)\n\t" \
  "global_store_dwordx4 v7, v[76:79], %[op] sc1\n\t" \
  "s_nop 1\n\t"
#define G3_EP 2
#define G3_CLOB "v0", "v1", "v2", "v3", "v4", "v5", "v6", "v7", "v8", "v9", "v10", "v11", "v12", "v13", "v14", "v15", "v16", "v17", "v18", "v19", "v20", "v21", "v22", "v23", "v24", "v25", "v26", "v27", "v28", "v29", "v30", "v31", "v32", "v33", "v34", "v35", "v36", "v37", "v38", "v39", "v40", "v41", "v42", "v43", "v44", "v45", "v46", "v47", "v48", "v49", "v50", "v51", "v52", "v53", "v54", "v55", "v56", "v57", "v58", "v59", "v60", "v61", "v62", "v63", "v64", "v65", "v66", "v67", "v68", "v69", "v70", "v71", "v72", "v73", "v74", "v75", "v76", "v77", "v78", "v79", "v80", "v81", "v82", "v83", "v84", "v85", "v86", "v87", "v88", "v89", "v90", "v91", "v92", "v93", "v94", "v95", "v97", "v98", "v99", "v100", "v101", "v102", "v103", "v104", "v105", "v106", "v107", "v108", "v109", "v110", "v111", "v112", "v113", "v114", "v115", "v116", "v117", "v118", "v119", "v120", "v121", "v122", "v123", "v124", "v125", "v126", "v127", "v128", "v129", "v130", "v131", "v132", "v133", "v134", "v135", "v136", "v137", "v138", "v139", "v140", "v141", "v142", "v143", "v144", "v145", "v146", "v147", "v148", "v149", "v150", "v151", "v152", "v153", "v154", "v155", "v156", "v157", "v158", "v159", "s64", "s65", "s66", "s67", "s68", "s69", "s70", "s71", "s72", "s73", "s74", "s75", "s76", "s77", "s78", "s79"

#define PP_ASM \
  "v_mov_b32 v60, 0\n\t" \
  "s_mov_b32 %[t0], %[n0]\n\t" \
  "s_cmp_eq_u32 %[t0], 0\n\t" \
  "s_cbranch_scc1 PAx_%=\n\t" \
  "PA_%=:\n\t" \
  "s_barrier\n\t" \
  "s_sub_u32 %[t0], %[t0], 1\n\t" \
  "s_cmp_lg_u32 %[t0], 0\n\t" \
  "s_cbranch_scc1 PA_%=\n\t" \
  "PAx_%=:\n\t" \
  "s_mov_b32 m0, %[shd]\n\t" \
  "global_load_dword v160, v60, %[fl] sc1\n\t" \
  "global_load_lds_dwordx4 v163, %[shp]\n\t" \
  "s_mov_b32 m0, %[tgd]\n\t" \
  "s_mov_b64 exec, %[em]\n\t" \
  "global_load_lds_dword v60, %[fl] sc1\n\t" \
  "s_mov_b64 exec, -1\n\t" \
  "global_load_ushort v161, v60, %[hp]\n\t" \
  "global_load_ushort v162, v60, %[dp]\n\t" \
  "global_load_dwordx4 v[0:3], v152, %[kb0]\n\t" \
  "global_load_dwordx4 v[4:7], v153, %[kb0]\n\t" \
  "global_load_dwordx4 v[8:11], v154, %[kb0]\n\t" \
  "global_load_dwordx4 v[12:15], v155, %[kb3] offset:0\n\t" \
  "global_load_dwordx4 v[28:31], v155, %[vb0] offset:0\n\t" \
  "s_setprio 2\n\t" \
  "s_setprio 3\n\t" \
  "s_barrier\n\t" \
  "global_load_dwordx4 v[16:19], v155, %[kb3] offset:2048\n\t" \
  "global_load_dwordx4 v[32:35], v155, %[vb0] offset:2048\n\t" \
  "global_load_dwordx4 v[20:23], v155, %[kb5] offset:0\n\t" \
  "s_setprio 2\n\t" \
  "s_waitcnt vmcnt(8)\n\t" \
  "ds_read_b128 v[128:131], v156 offset:0\n\t" \
  "ds_read_b128 v[132:135], v156 offset:16\n\t" \
  "ds_read_b128 v[136:139], v156 offset:32\n\t" \
  "ds_read_b128 v[140:143], v156 offset:48\n\t" \
  "s_waitcnt lgkmcnt(0)\n\t" \
  "v_lshlrev_b32 v64, 16, v128\n\t" \
  "v_and_b32 v65, 0xffff0000, v128\n\t" \
  "v_lshlrev_b32 v66, 16, v129\n\t" \
  "v_and_b32 v67, 0xffff0000, v129\n\t" \
  "v_lshlrev_b32 v68, 16, v130\n\t" \
  "v_and_b32 v69, 0xffff0000, v130\n\t" \
  "v_lshlrev_b32 v70, 16, v131\n\t" \
  "v_and_b32 v71, 0xffff0000, v131\n\t" \
  "v_lshlrev_b32 v72, 16, v132\n\t" \
  "v_and_b32 v73, 0xffff0000, v132\n\t" \
  "v_lshlrev_b32 v74, 16, v133\n\t" \
  "v_and_b32 v75, 0xffff0000, v133\n\t" \
  "v_lshlrev_b32 v76, 16, v134\n\t" \
  "v_and_b32 v77, 0xffff0000, v134\n\t" \
  "v_lshlrev_b32 v78, 16, v135\n\t" \
  "v_and_b32 v79, 0xffff0000, v135\n\t" \
  "v_lshlrev_b32 v80, 16, v136\n\t" \
  "v_and_b32 v81, 0xffff0000, v136\n\t" \
  "v_lshlrev_b32 v82, 16, v137\n\t" \
  "v_and_b32 v83, 0xffff0000, v137\n\t" \
  "v_lshlrev_b32 v84, 16, v138\n\t" \
  "v_and_b32 v85, 0xffff0000, v138\n\t" \
  "v_lshlrev_b32 v86, 16, v139\n\t" \
  "v_and_b32 v87, 0xffff0000, v139\n\t" \
  "v_lshlrev_b32 v88, 16, v140\n\t" \
  "v_and_b32 v89, 0xffff0000, v140\n\t" \
  "v_lshlrev_b32 v90, 16, v141\n\t" \
  "v_and_b32 v91, 0xffff0000, v141\n\t" \
  "v_lshlrev_b32 v92, 16, v142\n\t" \
  "v_and_b32 v93, 0xffff0000, v142\n\t" \
  "v_lshlrev_b32 v94, 16, v143\n\t" \
  "v_and_b32 v95, 0xffff0000, v143\n\t" \
  "s_waitcnt vmcnt(7)\n\t" \
  "s_mov_b64 exec, %[zm]\n\t" \
  "v_mov_b32 v0, 0\n\t" \
  "v_mov_b32 v1, 0\n\t" \
  "v_mov_b32 v2, 0\n\t" \
  "v_mov_b32 v3, 0\n\t" \
  "s_mov_b64 exec, -1\n\t" \
  "v_lshlrev_b32 v128, 16, v0\n\t" \
  "v_and_b32 v129, 0xffff0000, v0\n\t" \
  "v_lshlrev_b32 v130, 16, v1\n\t" \
  "v_and_b32 v131, 0xffff0000, v1\n\t" \
  "v_lshlrev_b32 v132, 16, v2\n\t" \
  "v_and_b32 v133, 0xffff0000, v2\n\t" \
  "v_lshlrev_b32 v134, 16, v3\n\t" \
  "v_and_b32 v135, 0xffff0000, v3\n\t" \
  "v_mul_f32 v96, v128, v67\n\t" \
  "v_mul_f32 v97, v129, v71\n\t" \
  "v_mul_f32 v98, v130, v75\n\t" \
  "v_mul_f32 v99, v131, v79\n\t" \
  "v_mul_f32 v100, v132, v83\n\t" \
  "v_mul_f32 v101, v133, v87\n\t" \
  "v_mul_f32 v102, v134, v91\n\t" \
  "v_mul_f32 v103, v135, v95\n\t" \
  "s_waitcnt vmcnt(6)\n\t" \
  "s_mov_b64 exec, %[zm]\n\t" \
  "v_mov_b32 v4, 0\n\t" \
  "v_mov_b32 v5, 0\n\t" \
  "v_mov_b32 v6, 0\n\t" \
  "v_mov_b32 v7, 0\n\t" \
  "s_mov_b64 exec, -1\n\t" \
  "v_lshlrev_b32 v128, 16, v4\n\t" \
  "v_and_b32 v129, 0xffff0000, v4\n\t" \
  "v_lshlrev_b32 v130, 16, v5\n\t" \
  "v_and_b32 v131, 0xffff0000, v5\n\t" \
  "v_lshlrev_b32 v132, 16, v6\n\t" \
  "v_and_b32 v133, 0xffff0000, v6\n\t" \
  "v_lshlrev_b32 v134, 16, v7\n\t" \
  "v_and_b32 v135, 0xffff0000, v7\n\t" \
  "v_fmac_f32 v96, v128, v66\n\t" \
  "v_fmac_f32 v97, v129, v70\n\t" \
  "v_fmac_f32 v98, v130, v74\n\t" \
  "v_fmac_f32 v99, v131, v78\n\t" \
  "v_fmac_f32 v100, v132, v82\n\t" \
  "v_fmac_f32 v101, v133, v86\n\t" \
  "v_fmac_f32 v102, v134, v90\n\t" \
  "v_fmac_f32 v103, v135, v94\n\t" \
  "v_mul_f32 v104, v128, v67\n\t" \
  "v_mul_f32 v105, v129, v71\n\t" \
  "v_mul_f32 v106, v130, v75\n\t" \
  "v_mul_f32 v107, v131, v79\n\t" \
  "v_mul_f32 v108, v132, v83\n\t" \
  "v_mul_f32 v109, v133, v87\n\t" \
  "v_mul_f32 v110, v134, v91\n\t" \
  "v_mul_f32 v111, v135, v95\n\t" \
  "s_waitcnt vmcnt(5)\n\t" \
  "s_mov_b64 exec, %[zm]\n\t" \
  "v_mov_b32 v8, 0\n\t" \
  "v_mov_b32 v9, 0\n\t" \
  "v_mov_b32 v10, 0\n\t" \
  "v_mov_b32 v11, 0\n\t" \
  "s_mov_b64 exec, -1\n\t" \
  "s_mov_b32 s85, 0x300000\n\t" \
  "s_load_dword s84, %[ph], s85\n\t" \
  "s_mov_b32 s85, 0x300800\n\t" \
  "s_load_dword s84, %[ph], s85\n\t" \
  "s_mov_b32 s85, 0x301000\n\t" \
  "s_load_dword s84, %[ph], s85\n\t" \
  "s_mov_b32 s85, 0x360000\n\t" \
  "s_load_dword s84, %[ph], s85\n\t" \
  "s_setprio 3\n\t" \
  "s_barrier\n\t" \
  "global_load_dwordx4 v[36:39], v155, %[vb2] offset:0\n\t" \
  "global_load_dwordx4 v[24:27], v155, %[kb5] offset:2048\n\t" \
  "global_load_dwordx4 v[40:43], v155, %[vb2] offset:2048\n\t" \
  "s_setprio 2\n\t" \
  "v_lshlrev_b32 v128, 16, v8\n\t" \
  "v_and_b32 v129, 0xffff0000, v8\n\t" \
  "v_lshlrev_b32 v130, 16, v9\n\t" \
  "v_and_b32 v131, 0xffff0000, v9\n\t" \
  "v_lshlrev_b32 v132, 16, v10\n\t" \
  "v_and_b32 v133, 0xffff0000, v10\n\t" \
  "v_lshlrev_b32 v134, 16, v11\n\t" \
  "v_and_b32 v135, 0xffff0000, v11\n\t" \
  "v_fmac_f32 v96, v128, v65\n\t" \
  "v_fmac_f32 v97, v129, v69\n\t" \
  "v_fmac_f32 v98, v130, v73\n\t" \
  "v_fmac_f32 v99, v131, v77\n\t" \
  "v_fmac_f32 v100, v132, v81\n\t" \
  "v_fmac_f32 v101, v133, v85\n\t" \
  "v_fmac_f32 v102, v134, v89\n\t" \
  "v_fmac_f32 v103, v135, v93\n\t" \
  "v_fmac_f32 v104, v128, v66\n\t" \
  "v_fmac_f32 v105, v129, v70\n\t" \
  "v_fmac_f32 v106, v130, v74\n\t" \
  "v_fmac_f32 v107, v131, v78\n\t" \
  "v_fmac_f32 v108, v132, v82\n\t" \
  "v_fmac_f32 v109, v133, v86\n\t" \
  "v_fmac_f32 v110, v134, v90\n\t" \
  "v_fmac_f32 v111, v135, v94\n\t" \
  "v_mul_f32 v112, v128, v67\n\t" \
  "v_mul_f32 v113, v129, v71\n\t" \
  "v_mul_f32 v114, v130, v75\n\t" \
  "v_mul_f32 v115, v131, v79\n\t" \
  "v_mul_f32 v116, v132, v83\n\t" \
  "v_mul_f32 v117, v133, v87\n\t" \
  "v_mul_f32 v118, v134, v91\n\t" \
  "v_mul_f32 v119, v135, v95\n\t" \
  "s_waitcnt vmcnt(7)\n\t" \
  "v_lshlrev_b32 v128, 16, v12\n\t" \
  "v_and_b32 v129, 0xffff0000, v12\n\t" \
  "v_lshlrev_b32 v130, 16, v13\n\t" \
  "v_and_b32 v131, 0xffff0000, v13\n\t" \
  "v_lshlrev_b32 v132, 16, v14\n\t" \
  "v_and_b32 v133, 0xffff0000, v14\n\t" \
  "v_lshlrev_b32 v134, 16, v15\n\t" \
  "v_and_b32 v135, 0xffff0000, v15\n\t" \
  "v_fmac_f32 v96, v128, v64\n\t" \
  "v_fmac_f32 v97, v129, v68\n\t" \
  "v_fmac_f32 v98, v130, v72\n\t" \
  "v_fmac_f32 v99, v131, v76\n\t" \
  "v_fmac_f32 v100, v132, v80\n\t" \
  "v_fmac_f32 v101, v133, v84\n\t" \
  "v_fmac_f32 v102, v134, v88\n\t" \
  "v_fmac_f32 v103, v135, v92\n\t" \
  "v_fmac_f32 v104, v128, v65\n\t" \
  "v_fmac_f32 v105, v129, v69\n\t" \
  "v_fmac_f32 v106, v130, v73\n\t" \
  "v_fmac_f32 v107, v131, v77\n\t" \
  "v_fmac_f32 v108, v132, v81\n\t" \
  "v_fmac_f32 v109, v133, v85\n\t" \
  "v_fmac_f32 v110, v134, v89\n\t" \
  "v_fmac_f32 v111, v135, v93\n\t" \
  "v_fmac_f32 v112, v128, v66\n\t" \
  "v_fmac_f32 v113, v129, v70\n\t" \
  "v_fmac_f32 v114, v130, v74\n\t" \
  "v_fmac_f32 v115, v131, v78\n\t" \
  "v_fmac_f32 v116, v132, v82\n\t" \
  "v_fmac_f32 v117, v133, v86\n\t" \
  "v_fmac_f32 v118, v134, v90\n\t" \
  "v_fmac_f32 v119, v135, v94\n\t" \
  "v_mul_f32 v120, v128, v67\n\t" \
  "v_mul_f32 v121, v129, v71\n\t" \
  "v_mul_f32 v122, v130, v75\n\t" \
  "v_mul_f32 v123, v131, v79\n\t" \
  "v_mul_f32 v124, v132, v83\n\t" \
  "v_mul_f32 v125, v133, v87\n\t" \
  "v_mul_f32 v126, v134, v91\n\t" \
  "v_mul_f32 v127, v135, v95\n\t" \
  "s_waitcnt vmcnt(6)\n\t" \
  "v_lshlrev_b32 v128, 16, v28\n\t" \
  "v_and_b32 v129, 0xffff0000, v28\n\t" \
  "v_lshlrev_b32 v130, 16, v29\n\t" \
  "v_and_b32 v131, 0xffff0000, v29\n\t" \
  "v_lshlrev_b32 v132, 16, v30\n\t" \
  "v_and_b32 v133, 0xffff0000, v30\n\t" \
  "v_lshlrev_b32 v134, 16, v31\n\t" \
  "v_and_b32 v135, 0xffff0000, v31\n\t" \
  "s_mov_b32 s85, 0x360800\n\t" \
  "s_load_dword s84, %[ph], s85\n\t" \
  "s_mov_b32 s85, 0x361000\n\t" \
  "s_load_dword s84, %[ph], s85\n\t" \
  "s_mov_b32 s85, 0x3c0000\n\t" \
  "s_load_dword s84, %[ph], s85\n\t" \
  "s_mov_b32 s85, 0x3c0800\n\t" \
  "s_load_dword s84, %[ph], s85\n\t" \
  "s_barrier\n\t" \
  "v_mul_f32 v96, v96, v128\n\t" \
  "v_mul_f32 v97, v97, v129\n\t" \
  "v_mul_f32 v98, v98, v130\n\t" \
  "v_mul_f32 v99, v99, v131\n\t" \
  "v_mul_f32 v100, v100, v132\n\t" \
  "v_mul_f32 v101, v101, v133\n\t" \
  "v_mul_f32 v102, v102, v134\n\t" \
  "v_mul_f32 v103, v103, v135\n\t" \
  "v_cvt_pk_bf16_f32 v136, v96, v97\n\t" \
  "v_cvt_pk_bf16_f32 v137, v98, v99\n\t" \
  "v_cvt_pk_bf16_f32 v138, v100, v101\n\t" \
  "v_cvt_pk_bf16_f32 v139, v102, v103\n\t" \
  "v_mov_b32_dpp v128, v136 row_ror:8 row_mask:0xf bank_mask:0xf\n\t" \
  "v_mov_b32_dpp v129, v137 row_ror:8 row_mask:0xf bank_mask:0xf\n\t" \
  "v_mov_b32_dpp v130, v138 row_ror:8 row_mask:0xf bank_mask:0xf\n\t" \
  "v_mov_b32_dpp v131, v139 row_ror:8 row_mask:0xf bank_mask:0xf\n\t" \
  "v_perm_b32 v136, v128, v136, v157\n\t" \
  "v_perm_b32 v137, v129, v137, v157\n\t" \
  "v_perm_b32 v138, v130, v138, v157\n\t" \
  "v_perm_b32 v139, v131, v139, v157\n\t" \
  "v_permlane16_swap_b32 v136, v137\n\t" \
  "s_nop 0\n\t" \
  "v_permlane16_swap_b32 v138, v139\n\t" \
  "s_nop 1\n\t" \
  "v_permlane32_swap_b32 v136, v138\n\t" \
  "v_permlane32_swap_b32 v137, v139\n\t" \
  "s_waitcnt vmcnt(5)\n\t" \
  "v_lshlrev_b32 v128, 16, v16\n\t" \
  "v_and_b32 v129, 0xffff0000, v16\n\t" \
  "v_lshlrev_b32 v130, 16, v17\n\t" \
  "v_and_b32 v131, 0xffff0000, v17\n\t" \
  "v_lshlrev_b32 v132, 16, v18\n\t" \
  "v_and_b32 v133, 0xffff0000, v18\n\t" \
  "v_lshlrev_b32 v134, 16, v19\n\t" \
  "v_and_b32 v135, 0xffff0000, v19\n\t" \
  "v_fmac_f32 v104, v128, v64\n\t" \
  "v_fmac_f32 v105, v129, v68\n\t" \
  "v_fmac_f32 v106, v130, v72\n\t" \
  "v_fmac_f32 v107, v131, v76\n\t" \
  "v_fmac_f32 v108, v132, v80\n\t" \
  "v_fmac_f32 v109, v133, v84\n\t" \
  "v_fmac_f32 v110, v134, v88\n\t" \
  "v_fmac_f32 v111, v135, v92\n\t" \
  "v_fmac_f32 v112, v128, v65\n\t" \
  "v_fmac_f32 v113, v129, v69\n\t" \
  "v_fmac_f32 v114, v130, v73\n\t" \
  "v_fmac_f32 v115, v131, v77\n\t" \
  "v_fmac_f32 v116, v132, v81\n\t" \
  "v_fmac_f32 v117, v133, v85\n\t" \
  "v_fmac_f32 v118, v134, v89\n\t" \
  "v_fmac_f32 v119, v135, v93\n\t" \
  "v_fmac_f32 v120, v128, v66\n\t" \
  "v_fmac_f32 v121, v129, v70\n\t" \
  "v_fmac_f32 v122, v130, v74\n\t" \
  "v_fmac_f32 v123, v131, v78\n\t" \
  "v_fmac_f32 v124, v132, v82\n\t" \
  "v_fmac_f32 v125, v133, v86\n\t" \
  "v_fmac_f32 v126, v134, v90\n\t" \
  "v_fmac_f32 v127, v135, v94\n\t" \
  "s_waitcnt vmcnt(4)\n\t" \
  "v_lshlrev_b32 v128, 16, v32\n\t" \
  "v_and_b32 v129, 0xffff0000, v32\n\t" \
  "v_lshlrev_b32 v130, 16, v33\n\t" \
  "v_and_b32 v131, 0xffff0000, v33\n\t" \
  "v_lshlrev_b32 v132, 16, v34\n\t" \
  "v_and_b32 v133, 0xffff0000, v34\n\t" \
  "v_lshlrev_b32 v134, 16, v35\n\t" \
  "v_and_b32 v135, 0xffff0000, v35\n\t" \
  "v_mul_f32 v104, v104, v128\n\t" \
  "v_mul_f32 v105, v105, v129\n\t" \
  "v_mul_f32 v106, v106, v130\n\t" \
  "v_mul_f32 v107, v107, v131\n\t" \
  "v_mul_f32 v108, v108, v132\n\t" \
  "v_mul_f32 v109, v109, v133\n\t" \
  "v_mul_f32 v110, v110, v134\n\t" \
  "v_mul_f32 v111, v111, v135\n\t" \
  "v_cvt_pk_bf16_f32 v140, v104, v105\n\t" \
  "v_cvt_pk_bf16_f32 v141, v106, v107\n\t" \
  "v_cvt_pk_bf16_f32 v142, v108, v109\n\t" \
  "v_cvt_pk_bf16_f32 v143, v110, v111\n\t" \
  "v_mov_b32_dpp v128, v140 row_ror:8 row_mask:0xf bank_mask:0xf\n\t" \
  "v_mov_b32_dpp v129, v141 row_ror:8 row_mask:0xf bank_mask:0xf\n\t" \
  "s_mov_b32 s85, 0x3c1000\n\t" \
  "s_load_dword s84, %[ph], s85\n\t" \
  "s_mov_b32 s85, 0x420000\n\t" \
  "s_load_dword s84, %[ph], s85\n\t" \
  "s_mov_b32 s85, 0x420800\n\t" \
  "s_load_dword s84, %[ph], s85\n\t" \
  "s_mov_b32 s85, 0x421000\n\t" \
  "s_load_dword s84, %[ph], s85\n\t" \
  "s_setprio 3\n\t" \
  "s_barrier\n\t" \
  "global_store_dwordx4 v159, v[136:139], %[sb] sc1\n\t" \
  "v_add_u32 v158, 0x80, v159\n\t" \
  "v_bfi_b32 v159, %[mk], v158, v159\n\t" \
  "s_setprio 2\n\t" \
  "v_mov_b32_dpp v130, v142 row_ror:8 row_mask:0xf bank_mask:0xf\n\t" \
  "v_mov_b32_dpp v131, v143 row_ror:8 row_mask:0xf bank_mask:0xf\n\t" \
  "v_perm_b32 v140, v128, v140, v157\n\t" \
  "v_perm_b32 v141, v129, v141, v157\n\t" \
  "v_perm_b32 v142, v130, v142, v157\n\t" \
  "v_perm_b32 v143, v131, v143, v157\n\t" \
  "v_permlane16_swap_b32 v140, v141\n\t" \
  "s_nop 0\n\t" \
  "v_permlane16_swap_b32 v142, v143\n\t" \
  "s_nop 1\n\t" \
  "v_permlane32_swap_b32 v140, v142\n\t" \
  "v_permlane32_swap_b32 v141, v143\n\t" \
  "s_waitcnt vmcnt(4)\n\t" \
  "v_lshlrev_b32 v128, 16, v20\n\t" \
  "v_and_b32 v129, 0xffff0000, v20\n\t" \
  "v_lshlrev_b32 v130, 16, v21\n\t" \
  "v_and_b32 v131, 0xffff0000, v21\n\t" \
  "v_lshlrev_b32 v132, 16, v22\n\t" \
  "v_and_b32 v133, 0xffff0000, v22\n\t" \
  "v_lshlrev_b32 v134, 16, v23\n\t" \
  "v_and_b32 v135, 0xffff0000, v23\n\t" \
  "v_fmac_f32 v112, v128, v64\n\t" \
  "v_fmac_f32 v113, v129, v68\n\t" \
  "v_fmac_f32 v114, v130, v72\n\t" \
  "v_fmac_f32 v115, v131, v76\n\t" \
  "v_fmac_f32 v116, v132, v80\n\t" \
  "v_fmac_f32 v117, v133, v84\n\t" \
  "v_fmac_f32 v118, v134, v88\n\t" \
  "v_fmac_f32 v119, v135, v92\n\t" \
  "v_fmac_f32 v120, v128, v65\n\t" \
  "v_fmac_f32 v121, v129, v69\n\t" \
  "v_fmac_f32 v122, v130, v73\n\t" \
  "v_fmac_f32 v123, v131, v77\n\t" \
  "v_fmac_f32 v124, v132, v81\n\t" \
  "v_fmac_f32 v125, v133, v85\n\t" \
  "v_fmac_f32 v126, v134, v89\n\t" \
  "v_fmac_f32 v127, v135, v93\n\t" \
  "s_waitcnt vmcnt(3)\n\t" \
  "v_lshlrev_b32 v128, 16, v36\n\t" \
  "v_and_b32 v129, 0xffff0000, v36\n\t" \
  "v_lshlrev_b32 v130, 16, v37\n\t" \
  "v_and_b32 v131, 0xffff0000, v37\n\t" \
  "v_lshlrev_b32 v132, 16, v38\n\t" \
  "v_and_b32 v133, 0xffff0000, v38\n\t" \
  "v_lshlrev_b32 v134, 16, v39\n\t" \
  "v_and_b32 v135, 0xffff0000, v39\n\t" \
  "v_mul_f32 v112, v112, v128\n\t" \
  "v_mul_f32 v113, v113, v129\n\t" \
  "v_mul_f32 v114, v114, v130\n\t" \
  "v_mul_f32 v115, v115, v131\n\t" \
  "v_mul_f32 v116, v116, v132\n\t" \
  "v_mul_f32 v117, v117, v133\n\t" \
  "v_mul_f32 v118, v118, v134\n\t" \
  "v_mul_f32 v119, v119, v135\n\t" \
  "v_cvt_pk_bf16_f32 v144, v112, v113\n\t" \
  "v_cvt_pk_bf16_f32 v145, v114, v115\n\t" \
  "v_cvt_pk_bf16_f32 v146, v116, v117\n\t" \
  "v_cvt_pk_bf16_f32 v147, v118, v119\n\t" \
  "v_mov_b32_dpp v128, v144 row_ror:8 row_mask:0xf bank_mask:0xf\n\t" \
  "v_mov_b32_dpp v129, v145 row_ror:8 row_mask:0xf bank_mask:0xf\n\t" \
  "v_mov_b32_dpp v130, v146 row_ror:8 row_mask:0xf bank_mask:0xf\n\t" \
  "v_mov_b32_dpp v131, v147 row_ror:8 row_mask:0xf bank_mask:0xf\n\t" \
  "v_perm_b32 v144, v128, v144, v157\n\t" \
  "v_perm_b32 v145, v129, v145, v157\n\t" \
  "v_perm_b32 v146, v130, v146, v157\n\t" \
  "v_perm_b32 v147, v131, v147, v157\n\t" \
  "v_permlane16_swap_b32 v144, v145\n\t" \
  "s_nop 0\n\t" \
  "v_permlane16_swap_b32 v146, v147\n\t" \
  "s_nop 1\n\t" \
  "v_permlane32_swap_b32 v144, v146\n\t" \
  "v_permlane32_swap_b32 v145, v147\n\t" \
  "s_waitcnt vmcnt(2)\n\t" \
  "v_lshlrev_b32 v128, 16, v24\n\t" \
  "v_and_b32 v129, 0xffff0000, v24\n\t" \
  "v_lshlrev_b32 v130, 16, v25\n\t" \
  "v_and_b32 v131, 0xffff0000, v25\n\t" \
  "v_lshlrev_b32 v132, 16, v26\n\t" \
  "v_and_b32 v133, 0xffff0000, v26\n\t" \
  "v_lshlrev_b32 v134, 16, v27\n\t" \
  "v_and_b32 v135, 0xffff0000, v27\n\t" \
  "v_fmac_f32 v120, v128, v64\n\t" \
  "v_fmac_f32 v121, v129, v68\n\t" \
  "s_mov_b32 s85, 0x480000\n\t" \
  "s_load_dword s84, %[ph], s85\n\t" \
  "s_mov_b32 s85, 0x480800\n\t" \
  "s_load_dword s84, %[ph], s85\n\t" \
  "s_mov_b32 s85, 0x481000\n\t" \
  "s_load_dword s84, %[ph], s85\n\t" \
  "s_mov_b32 s85, 0x4e0000\n\t" \
  "s_load_dword s84, %[ph], s85\n\t" \
  "s_setprio 3\n\t" \
  "s_barrier\n\t" \
  "global_store_dwordx4 v159, v[140:143], %[sb] sc1\n\t" \
  "v_add_u32 v158, 0x80, v159\n\t" \
  "v_bfi_b32 v159, %[mk], v158, v159\n\t" \
  "global_store_dwordx4 v159, v[144:147], %[sb] sc1\n\t" \
  "v_add_u32 v158, 0x80, v159\n\t" \
  "v_bfi_b32 v159, %[mk], v158, v159\n\t" \
  "s_mov_b32 m0, %[hd]\n\t" \
  "s_nop 0\n\t" \
  "global_load_lds_dwordx4 v163, %[hp] offset:0\n\t" \
  "global_load_lds_dwordx4 v163, %[hp] offset:1024\n\t" \
  "global_load_lds_dwordx4 v163, %[hp] offset:2048\n\t" \
  "global_load_lds_dwordx4 v163, %[hp] offset:3072\n\t" \
  "s_setprio 2\n\t" \
  "v_fmac_f32 v122, v130, v72\n\t" \
  "v_fmac_f32 v123, v131, v76\n\t" \
  "v_fmac_f32 v124, v132, v80\n\t" \
  "v_fmac_f32 v125, v133, v84\n\t" \
  "v_fmac_f32 v126, v134, v88\n\t" \
  "v_fmac_f32 v127, v135, v92\n\t" \
  "s_waitcnt vmcnt(7)\n\t" \
  "v_lshlrev_b32 v128, 16, v40\n\t" \
  "v_and_b32 v129, 0xffff0000, v40\n\t" \
  "v_lshlrev_b32 v130, 16, v41\n\t" \
  "v_and_b32 v131, 0xffff0000, v41\n\t" \
  "v_lshlrev_b32 v132, 16, v42\n\t" \
  "v_and_b32 v133, 0xffff0000, v42\n\t" \
  "v_lshlrev_b32 v134, 16, v43\n\t" \
  "v_and_b32 v135, 0xffff0000, v43\n\t" \
  "v_mul_f32 v120, v120, v128\n\t" \
  "v_mul_f32 v121, v121, v129\n\t" \
  "v_mul_f32 v122, v122, v130\n\t" \
  "v_mul_f32 v123, v123, v131\n\t" \
  "v_mul_f32 v124, v124, v132\n\t" \
  "v_mul_f32 v125, v125, v133\n\t" \
  "v_mul_f32 v126, v126, v134\n\t" \
  "v_mul_f32 v127, v127, v135\n\t" \
  "v_cvt_pk_bf16_f32 v148, v120, v121\n\t" \
  "v_cvt_pk_bf16_f32 v149, v122, v123\n\t" \
  "v_cvt_pk_bf16_f32 v150, v124, v125\n\t" \
  "v_cvt_pk_bf16_f32 v151, v126, v127\n\t" \
  "v_mov_b32_dpp v128, v148 row_ror:8 row_mask:0xf bank_mask:0xf\n\t" \
  "v_mov_b32_dpp v129, v149 row_ror:8 row_mask:0xf bank_mask:0xf\n\t" \
  "v_mov_b32_dpp v130, v150 row_ror:8 row_mask:0xf bank_mask:0xf\n\t" \
  "v_mov_b32_dpp v131, v151 row_ror:8 row_mask:0xf bank_mask:0xf\n\t" \
  "v_perm_b32 v148, v128, v148, v157\n\t" \
  "v_perm_b32 v149, v129, v149, v157\n\t" \
  "v_perm_b32 v150, v130, v150, v157\n\t" \
  "v_perm_b32 v151, v131, v151, v157\n\t" \
  "v_permlane16_swap_b32 v148, v149\n\t" \
  "s_nop 0\n\t" \
  "v_permlane16_swap_b32 v150, v151\n\t" \
  "s_nop 1\n\t" \
  "v_permlane32_swap_b32 v148, v150\n\t" \
  "v_permlane32_swap_b32 v149, v151\n\t" \
  "s_mov_b32 s85, 0x4e0800\n\t" \
  "s_load_dword s84, %[ph], s85\n\t" \
  "s_mov_b32 s85, 0x4e1000\n\t" \
  "s_load_dword s84, %[ph], s85\n\t" \
  "s_mov_b32 s85, 0x540000\n\t" \
  "s_load_dword s84, %[ph], s85\n\t" \
  "s_mov_b32 s85, 0x540800\n\t" \
  "s_load_dword s84, %[ph], s85\n\t" \
  "s_setprio 3\n\t" \
  "s_barrier\n\t" \
  "global_store_dwordx4 v159, v[148:151], %[sb] sc1\n\t" \
  "s_setprio 2\n\t" \
  "s_waitcnt vmcnt(1)\n\t" \
  "ds_read_b64_tr_b16 v[64:65], v165 offset:32\n\t" \
  "ds_read_b64_tr_b16 v[66:67], v165 offset:0\n\t" \
  "ds_read_b64_tr_b16 v[68:69], v165 offset:288\n\t" \
  "ds_read_b64_tr_b16 v[70:71], v165 offset:256\n\t" \
  "ds_read_b64_tr_b16 v[72:73], v165 offset:544\n\t" \
  "ds_read_b64_tr_b16 v[74:75], v165 offset:512\n\t" \
  "ds_read_b64_tr_b16 v[76:77], v165 offset:800\n\t" \
  "ds_read_b64_tr_b16 v[78:79], v165 offset:768\n\t" \
  "ds_read_b64_tr_b16 v[80:81], v165 offset:1056\n\t" \
  "ds_read_b64_tr_b16 v[82:83], v165 offset:1024\n\t" \
  "ds_read_b64_tr_b16 v[84:85], v165 offset:1312\n\t" \
  "ds_read_b64_tr_b16 v[86:87], v165 offset:1280\n\t" \
  "ds_read_b64_tr_b16 v[88:89], v165 offset:1568\n\t" \
  "ds_read_b64_tr_b16 v[90:91], v165 offset:1536\n\t" \
  "ds_read_b64_tr_b16 v[92:93], v165 offset:1824\n\t" \
  "ds_read_b64_tr_b16 v[94:95], v165 offset:1792\n\t" \
  "v_mov_b32 v24, v166\n\t" \
  "v_mov_b32 v25, v167\n\t" \
  "v_mov_b32 v63, 0xc0\n\t" \
  "v_add_u32 v158, 64, v24\n\t" \
  "v_bfi_b32 v26, v63, v158, v24\n\t" \
  "v_add_u32 v158, 64, v25\n\t" \
  "v_bfi_b32 v27, v63, v158, v25\n\t" \
  "v_add_u32 v158, 64, v26\n\t" \
  "v_bfi_b32 v28, v63, v158, v26\n\t" \
  "v_add_u32 v158, 64, v27\n\t" \
  "v_bfi_b32 v29, v63, v158, v27\n\t" \
  "v_add_u32 v158, 64, v28\n\t" \
  "v_bfi_b32 v30, v63, v158, v28\n\t" \
  "v_add_u32 v158, 64, v29\n\t" \
  "v_bfi_b32 v31, v63, v158, v29\n\t" \
  "s_mov_b32 s85, 0x541000\n\t" \
  "s_load_dword s84, %[ph], s85\n\t" \
  "s_mov_b32 s85, 0x5a0000\n\t" \
  "s_load_dword s84, %[ph], s85\n\t" \
  "s_mov_b32 s85, 0x5a0800\n\t" \
  "s_load_dword s84, %[ph], s85\n\t" \
  "s_mov_b32 s85, 0x5a1000\n\t" \
  "s_load_dword s84, %[ph], s85\n\t" \
  "s_setprio 3\n\t" \
  "s_barrier\n\t" \
  "s_waitcnt vmcnt(0)\n\t" \
  "v_add_u32 v61, 1, v160\n\t" \
  "s_mov_b64 exec, 1\n\t" \
  "global_store_dword v60, v61, %[ff] sc1\n\t" \
  "s_mov_b64 exec, -1\n\t" \
  "s_setprio 2\n\t" \
  "ds_read_b64_tr_b16 v[96:97], v165 offset:2080\n\t" \
  "ds_read_b64_tr_b16 v[98:99], v165 offset:2048\n\t" \
  "ds_read_b64_tr_b16 v[100:101], v165 offset:2336\n\t" \
  "ds_read_b64_tr_b16 v[102:103], v165 offset:2304\n\t" \
  "ds_read_b64_tr_b16 v[104:105], v165 offset:2592\n\t" \
  "ds_read_b64_tr_b16 v[106:107], v165 offset:2560\n\t" \
  "ds_read_b64_tr_b16 v[108:109], v165 offset:2848\n\t" \
  "ds_read_b64_tr_b16 v[110:111], v165 offset:2816\n\t" \
  "ds_read_b64_tr_b16 v[112:113], v165 offset:3104\n\t" \
  "ds_read_b64_tr_b16 v[114:115], v165 offset:3072\n\t" \
  "ds_read_b64_tr_b16 v[116:117], v165 offset:3360\n\t" \
  "ds_read_b64_tr_b16 v[118:119], v165 offset:3328\n\t" \
  "ds_read_b64_tr_b16 v[120:121], v165 offset:3616\n\t" \
  "ds_read_b64_tr_b16 v[122:123], v165 offset:3584\n\t" \
  "ds_read_b64_tr_b16 v[124:125], v165 offset:3872\n\t" \
  "ds_read_b64_tr_b16 v[126:127], v165 offset:3840\n\t" \
  "s_barrier\n\t" \
  "s_waitcnt lgkmcnt(0)\n\t" \
  "v_lshlrev_b32 v61, 16, v161\n\t" \
  "v_lshlrev_b32 v62, 16, v162\n\t" \
  "v_add_f32 v61, v61, v62\n\t" \
  "v_mov_b32 v62, 0xffff\n\t" \
  "v_cvt_pk_bf16_f32 v61, v61, v61\n\t" \
  "s_mov_b64 exec, 1\n\t" \
  "v_bfi_b32 v66, v62, v61, v66\n\t" \
  "s_mov_b64 exec, 15\n\t" \
  "v_and_b32 v66, v62, v66\n\t" \
  "s_mov_b64 exec, 16\n\t" \
  "v_bfi_b32 v66, v62, v66, v61\n\t" \
  "s_mov_b64 exec, 0xff\n\t" \
  "v_mov_b32 v67, 0\n\t" \
  "s_mov_b64 exec, 0x100\n\t" \
  "v_mov_b32 v67, v61\n\t" \
  "s_mov_b64 exec, 0xf00\n\t" \
  "v_and_b32 v67, v62, v67\n\t" \
  "s_mov_b64 exec, 0x1000\n\t" \
  "v_bfi_b32 v67, v62, v67, v61\n\t" \
  "s_mov_b64 exec, -1\n\t" \
  "s_waitcnt lgkmcnt(0)\n\t"
#define PP_NB 8
#define PP_Q 0
#define PP_CLOB "v0", "v1", "v2", "v3", "v4", "v5", "v6", "v7", "v8", "v9", "v10", "v11", "v12", "v13", "v14", "v15", "v16", "v17", "v18", "v19", "v20", "v21", "v22", "v23", "v32", "v33", "v34", "v35", "v36", "v37", "v38", "v39", "v40", "v41", "v42", "v43", "v60", "v61", "v62", "v63", "v128", "v129", "v130", "v131", "v132", "v133", "v134", "v135", "v136", "v137", "v138", "v139", "v140", "v141", "v142", "v143", "v144", "v145", "v146", "v147", "v148", "v149", "v150", "v151", "v158", "v161", "v162", "s84", "s85"

#define LD_ASM1 \
  "s_add_u32 m0, s39, 49152\n\t" \
  "s_mov_b32 s68, 0x0\n\t" \
  "buffer_load_dwordx4 v5, s[44:47], s68 offen offset:0 lds\n\t" \
  "buffer_load_dwordx4 v6, s[44:47], s68 offen offset:1024 lds\n\t" \
  "buffer_load_dwordx4 v7, s[44:47], s68 offen offset:2048 lds\n\t" \
  "s_add_u32 m0, s38, 36864\n\t" \
  "s_mov_b32 s68, 0x0\n\t" \
  "buffer_load_dwordx4 v0, s[40:43], s68 offen offset:0 lds\n\t" \
  "buffer_load_dwordx4 v1, s[40:43], s68 offen offset:1024 lds\n\t" \
  "s_mov_b64 exec, 0xffff\n\t" \
  "buffer_load_dwordx4 v4, s[40:43], s68 offen offset:2048 lds\n\t" \
  "s_mov_b64 exec, -1\n\t" \
  "s_add_u32 m0, s38, 46080\n\t" \
  "s_mov_b32 s68, 0x24000\n\t" \
  "buffer_load_dwordx4 v2, s[40:43], s68 offen offset:0 lds\n\t" \
  "buffer_load_dwordx4 v3, s[40:43], s68 offen offset:1024 lds\n\t" \
  "s_mov_b64 exec, 0xffff\n\t" \
  "buffer_load_dwordx4 v4, s[40:43], s68 offen offset:2048 lds\n\t" \
  "s_mov_b64 exec, -1\n\t" \
  "s_add_u32 m0, s39, 61440\n\t" \
  "s_mov_b32 s68, 0x30000\n\t" \
  "buffer_load_dwordx4 v5, s[44:47], s68 offen offset:0 lds\n\t" \
  "buffer_load_dwordx4 v6, s[44:47], s68 offen offset:1024 lds\n\t" \
  "buffer_load_dwordx4 v7, s[44:47], s68 offen offset:2048 lds\n\t" \
  "s_mov_b32 m0, %[sk]\n\t" \
  "s_mov_b32 exec_hi, 0\n\t" \
  "buffer_load_dword v9, s[40:43], 0 offen lds\n\t" \
  "s_mov_b32 exec_lo, %[wm]\n\t" \
  "buffer_load_dword v10, s[44:47], 0 offen lds\n\t" \
  "s_mov_b64 exec, -1\n\t"
#define LD_ASM2 \
  "s_add_u32 m0, s38, 0\n\t" \
  "s_mov_b32 s68, 0x80\n\t" \
  "buffer_load_dwordx4 v0, s[40:43], s68 offen offset:0 lds\n\t" \
  "s_mov_b32 s85, 0x180000\n\t" \
  "s_load_dword s84, %[pw], s85\n\t" \
  "buffer_load_dwordx4 v1, s[40:43], s68 offen offset:1024 lds\n\t" \
  "s_mov_b32 s85, 0x180800\n\t" \
  "s_load_dword s84, %[pw], s85\n\t" \
  "s_mov_b64 exec, 0xffff\n\t" \
  "buffer_load_dwordx4 v4, s[40:43], s68 offen offset:2048 lds\n\t" \
  "s_mov_b64 exec, -1\n\t" \
  "s_add_u32 m0, s38, 9216\n\t" \
  "s_mov_b32 s68, 0x24080\n\t" \
  "buffer_load_dwordx4 v2, s[40:43], s68 offen offset:0 lds\n\t" \
  "s_mov_b32 s85, 0x181000\n\t" \
  "s_load_dword s84, %[pw], s85\n\t" \
  "buffer_load_dwordx4 v3, s[40:43], s68 offen offset:1024 lds\n\t" \
  "s_mov_b32 s85, 0x1b0000\n\t" \
  "s_load_dword s84, %[pw], s85\n\t" \
  "s_mov_b64 exec, 0xffff\n\t" \
  "buffer_load_dwordx4 v4, s[40:43], s68 offen offset:2048 lds\n\t" \
  "s_mov_b64 exec, -1\n\t" \
  "s_mov_b32 s85, 0x1b0800\n\t" \
  "s_load_dword s84, %[pw], s85\n\t" \
  "s_mov_b32 s85, 0x1b1000\n\t" \
  "s_load_dword s84, %[pw], s85\n\t" \
  "s_mov_b32 s85, 0x1e0000\n\t" \
  "s_load_dword s84, %[pw], s85\n\t" \
  "s_mov_b32 s85, 0x1e0800\n\t" \
  "s_load_dword s84, %[pw], s85\n\t" \
  "s_mov_b32 m0, %[tsk]\n\t" \
  "s_mov_b32 s85, 0x0\n\t" \
  "s_mov_b64 exec, %[hm]\n\t" \
  "buffer_load_dword v16, %[sh], s85 offen lds\n\t" \
  "s_mov_b64 exec, -1\n\t" \
  "s_mov_b32 s68, 0x60000\n\t" \
  "s_mov_b32 s69, 0x90000\n\t" \
  "s_mov_b32 s70, 0x100\n\t" \
  "s_mov_b32 s71, 0x24100\n\t" \
  "s_mov_b32 s72, 0xc0000\n\t" \
  "s_mov_b32 s73, 0xf0000\n\t" \
  "s_add_u32 m0, s39, 0\n\t" \
  "s_waitcnt vmcnt(9)\n\t" \
  "s_barrier\n\t" \
  "buffer_load_dwordx4 v5, s[44:47], s68 offen offset:0 lds\n\t" \
  "s_mov_b32 s85, 0x1e1000\n\t" \
  "s_load_dword s84, %[pw], s85\n\t" \
  "buffer_load_dwordx4 v6, s[44:47], s68 offen offset:1024 lds\n\t" \
  "s_mov_b32 s85, 0x210000\n\t" \
  "s_load_dword s84, %[pw], s85\n\t" \
  "buffer_load_dwordx4 v7, s[44:47], s68 offen offset:2048 lds\n\t" \
  "s_mov_b32 s85, 0x210800\n\t" \
  "s_load_dword s84, %[pw], s85\n\t" \
  "s_add_u32 m0, s39, 12288\n\t" \
  "s_nop 0\n\t" \
  "buffer_load_dwordx4 v5, s[44:47], s69 offen offset:0 lds\n\t" \
  "s_mov_b32 s85, 0x211000\n\t" \
  "s_load_dword s84, %[pw], s85\n\t" \
  "buffer_load_dwordx4 v6, s[44:47], s69 offen offset:1024 lds\n\t" \
  "s_mov_b32 s85, 0x240000\n\t" \
  "s_load_dword s84, %[pw], s85\n\t" \
  "buffer_load_dwordx4 v7, s[44:47], s69 offen offset:2048 lds\n\t" \
  "s_mov_b32 s85, 0x240800\n\t" \
  "s_load_dword s84, %[pw], s85\n\t" \
  "s_add_u32 m0, s38, 18432\n\t" \
  "s_nop 0\n\t" \
  "buffer_load_dwordx4 v0, s[40:43], s70 offen offset:0 lds\n\t" \
  "buffer_load_dwordx4 v1, s[40:43], s70 offen offset:1024 lds\n\t" \
  "s_mov_b64 exec, 0xffff\n\t" \
  "buffer_load_dwordx4 v4, s[40:43], s70 offen offset:2048 lds\n\t" \
  "s_add_u32 m0, s38, 27648\n\t" \
  "s_mov_b64 exec, -1\n\t" \
  "buffer_load_dwordx4 v2, s[40:43], s71 offen offset:0 lds\n\t" \
  "buffer_load_dwordx4 v3, s[40:43], s71 offen offset:1024 lds\n\t" \
  "s_mov_b64 exec, 0xffff\n\t" \
  "buffer_load_dwordx4 v4, s[40:43], s71 offen offset:2048 lds\n\t" \
  "s_add_u32 m0, s39, 24576\n\t" \
  "s_mov_b64 exec, -1\n\t" \
  "buffer_load_dwordx4 v5, s[44:47], s72 offen offset:0 lds\n\t" \
  "buffer_load_dwordx4 v6, s[44:47], s72 offen offset:1024 lds\n\t" \
  "buffer_load_dwordx4 v7, s[44:47], s72 offen offset:2048 lds\n\t" \
  "s_add_u32 m0, s39, 36864\n\t" \
  "s_nop 0\n\t" \
  "buffer_load_dwordx4 v5, s[44:47], s73 offen offset:0 lds\n\t" \
  "buffer_load_dwordx4 v6, s[44:47], s73 offen offset:1024 lds\n\t" \
  "buffer_load_dwordx4 v7, s[44:47], s73 offen offset:2048 lds\n\t" \
  "s_mov_b32 m0, %[tsk]\n\t" \
  "s_mov_b32 s85, 0x180000\n\t" \
  "s_mov_b64 exec, %[hm]\n\t" \
  "buffer_load_dword v16, %[sh], s85 offen lds\n\t" \
  "s_mov_b64 exec, -1\n\t"
#define LD_ASM3 \
  "s_mov_b32 s68, 0x180\n\t" \
  "s_mov_b32 s69, 0x120000\n\t" \
  "s_mov_b32 s70, 0x24180\n\t" \
  "s_mov_b32 s71, 0x150000\n\t" \
  "s_add_u32 m0, s38, 36864\n\t" \
  "s_waitcnt vmcnt(13)\n\t" \
  "s_barrier\n\t" \
  "buffer_load_dwordx4 v0, s[40:43], s68 offen offset:0 lds\n\t" \
  "s_mov_b32 s85, 0x241000\n\t" \
  "s_load_dword s84, %[pw], s85\n\t" \
  "buffer_load_dwordx4 v1, s[40:43], s68 offen offset:1024 lds\n\t" \
  "s_mov_b32 s85, 0x270000\n\t" \
  "s_load_dword s84, %[pw], s85\n\t" \
  "s_mov_b64 exec, 0xffff\n\t" \
  "buffer_load_dwordx4 v4, s[40:43], s68 offen offset:2048 lds\n\t" \
  "s_add_u32 m0, s39, 49152\n\t" \
  "s_mov_b64 exec, -1\n\t" \
  "buffer_load_dwordx4 v5, s[44:47], s69 offen offset:0 lds\n\t" \
  "s_mov_b32 s85, 0x270800\n\t" \
  "s_load_dword s84, %[pw], s85\n\t" \
  "buffer_load_dwordx4 v6, s[44:47], s69 offen offset:1024 lds\n\t" \
  "s_mov_b32 s85, 0x271000\n\t" \
  "s_load_dword s84, %[pw], s85\n\t" \
  "buffer_load_dwordx4 v7, s[44:47], s69 offen offset:2048 lds\n\t" \
  "s_mov_b32 s85, 0x2a0000\n\t" \
  "s_load_dword s84, %[pw], s85\n\t" \
  "s_add_u32 m0, s38, 46080\n\t" \
  "s_nop 0\n\t" \
  "buffer_load_dwordx4 v2, s[40:43], s70 offen offset:0 lds\n\t" \
  "s_mov_b32 s85, 0x2a0800\n\t" \
  "s_load_dword s84, %[pw], s85\n\t" \
  "buffer_load_dwordx4 v3, s[40:43], s70 offen offset:1024 lds\n\t" \
  "s_mov_b64 exec, 0xffff\n\t" \
  "buffer_load_dwordx4 v4, s[40:43], s70 offen offset:2048 lds\n\t" \
  "s_add_u32 m0, s39, 61440\n\t" \
  "s_mov_b64 exec, -1\n\t" \
  "buffer_load_dwordx4 v5, s[44:47], s71 offen offset:0 lds\n\t" \
  "buffer_load_dwordx4 v6, s[44:47], s71 offen offset:1024 lds\n\t" \
  "buffer_load_dwordx4 v7, s[44:47], s71 offen offset:2048 lds\n\t" \
  "s_mov_b32 s68, 0x200\n\t" \
  "s_mov_b32 s69, 0x180000\n\t" \
  "s_mov_b32 s70, 0x24200\n\t" \
  "s_mov_b32 s71, 0x1b0000\n\t" \
  "s_add_u32 m0, s38, 0\n\t" \
  "s_waitcnt vmcnt(13)\n\t" \
  "s_barrier\n\t" \
  "buffer_load_dwordx4 v0, s[40:43], s68 offen offset:0 lds\n\t" \
  "s_mov_b32 s85, 0x2a1000\n\t" \
  "s_load_dword s84, %[pw], s85\n\t" \
  "buffer_load_dwordx4 v1, s[40:43], s68 offen offset:1024 lds\n\t" \
  "s_mov_b32 s85, 0x2d0000\n\t" \
  "s_load_dword s84, %[pw], s85\n\t" \
  "s_mov_b64 exec, 0xffff\n\t" \
  "buffer_load_dwordx4 v4, s[40:43], s68 offen offset:2048 lds\n\t" \
  "s_add_u32 m0, s39, 0\n\t" \
  "s_mov_b64 exec, -1\n\t" \
  "buffer_load_dwordx4 v5, s[44:47], s69 offen offset:0 lds\n\t" \
  "s_mov_b32 s85, 0x2d0800\n\t" \
  "s_load_dword s84, %[pw], s85\n\t" \
  "buffer_load_dwordx4 v6, s[44:47], s69 offen offset:1024 lds\n\t" \
  "s_mov_b32 s85, 0x2d1000\n\t" \
  "s_load_dword s84, %[pw], s85\n\t" \
  "buffer_load_dwordx4 v7, s[44:47], s69 offen offset:2048 lds\n\t" \
  "s_mov_b32 s85, 0x300000\n\t" \
  "s_load_dword s84, %[pw], s85\n\t" \
  "s_add_u32 m0, s38, 9216\n\t" \
  "s_nop 0\n\t" \
  "buffer_load_dwordx4 v2, s[40:43], s70 offen offset:0 lds\n\t" \
  "s_mov_b32 s85, 0x300800\n\t" \
  "s_load_dword s84, %[pw], s85\n\t" \
  "buffer_load_dwordx4 v3, s[40:43], s70 offen offset:1024 lds\n\t" \
  "s_mov_b64 exec, 0xffff\n\t" \
  "buffer_load_dwordx4 v4, s[40:43], s70 offen offset:2048 lds\n\t" \
  "s_add_u32 m0, s39, 12288\n\t" \
  "s_mov_b64 exec, -1\n\t" \
  "buffer_load_dwordx4 v5, s[44:47], s71 offen offset:0 lds\n\t" \
  "buffer_load_dwordx4 v6, s[44:47], s71 offen offset:1024 lds\n\t" \
  "buffer_load_dwordx4 v7, s[44:47], s71 offen offset:2048 lds\n\t" \
  "s_mov_b32 s68, 0x280\n\t" \
  "s_mov_b32 s69, 0x1e0000\n\t" \
  "s_mov_b32 s70, 0x24280\n\t" \
  "s_mov_b32 s71, 0x210000\n\t" \
  "s_add_u32 m0, s38, 18432\n\t" \
  "s_waitcnt vmcnt(12)\n\t" \
  "s_barrier\n\t" \
  "buffer_load_dwordx4 v0, s[40:43], s68 offen offset:0 lds\n\t" \
  "s_mov_b32 s85, 0x301000\n\t" \
  "s_load_dword s84, %[pw], s85\n\t" \
  "buffer_load_dwordx4 v1, s[40:43], s68 offen offset:1024 lds\n\t" \
  "s_mov_b32 s85, 0x330000\n\t" \
  "s_load_dword s84, %[pw], s85\n\t" \
  "s_mov_b64 exec, 0xffff\n\t" \
  "buffer_load_dwordx4 v4, s[40:43], s68 offen offset:2048 lds\n\t" \
  "s_add_u32 m0, s39, 24576\n\t" \
  "s_mov_b64 exec, -1\n\t" \
  "buffer_load_dwordx4 v5, s[44:47], s69 offen offset:0 lds\n\t" \
  "s_mov_b32 s85, 0x330800\n\t" \
  "s_load_dword s84, %[pw], s85\n\t" \
  "buffer_load_dwordx4 v6, s[44:47], s69 offen offset:1024 lds\n\t" \
  "s_mov_b32 s85, 0x331000\n\t" \
  "s_load_dword s84, %[pw], s85\n\t" \
  "buffer_load_dwordx4 v7, s[44:47], s69 offen offset:2048 lds\n\t" \
  "s_mov_b32 s85, 0x360000\n\t" \
  "s_load_dword s84, %[pw], s85\n\t" \
  "s_add_u32 m0, s38, 27648\n\t" \
  "s_nop 0\n\t" \
  "buffer_load_dwordx4 v2, s[40:43], s70 offen offset:0 lds\n\t" \
  "s_mov_b32 s85, 0x360800\n\t" \
  "s_load_dword s84, %[pw], s85\n\t" \
  "buffer_load_dwordx4 v3, s[40:43], s70 offen offset:1024 lds\n\t" \
  "s_mov_b64 exec, 0xffff\n\t" \
  "buffer_load_dwordx4 v4, s[40:43], s70 offen offset:2048 lds\n\t" \
  "s_add_u32 m0, s39, 36864\n\t" \
  "s_mov_b64 exec, -1\n\t" \
  "buffer_load_dwordx4 v5, s[44:47], s71 offen offset:0 lds\n\t" \
  "buffer_load_dwordx4 v6, s[44:47], s71 offen offset:1024 lds\n\t" \
  "buffer_load_dwordx4 v7, s[44:47], s71 offen offset:2048 lds\n\t" \
  "s_mov_b32 s68, 0x300\n\t" \
  "s_mov_b32 s69, 0x240000\n\t" \
  "s_mov_b32 s70, 0x24300\n\t" \
  "s_mov_b32 s71, 0x270000\n\t" \
  "s_add_u32 m0, s38, 36864\n\t" \
  "s_waitcnt vmcnt(12)\n\t" \
  "s_barrier\n\t" \
  "buffer_load_dwordx4 v0, s[40:43], s68 offen offset:0 lds\n\t" \
  "s_mov_b32 s85, 0x361000\n\t" \
  "s_load_dword s84, %[pw], s85\n\t" \
  "buffer_load_dwordx4 v1, s[40:43], s68 offen offset:1024 lds\n\t" \
  "s_mov_b32 s85, 0x390000\n\t" \
  "s_load_dword s84, %[pw], s85\n\t" \
  "s_mov_b64 exec, 0xffff\n\t" \
  "buffer_load_dwordx4 v4, s[40:43], s68 offen offset:2048 lds\n\t" \
  "s_add_u32 m0, s39, 49152\n\t" \
  "s_mov_b64 exec, -1\n\t" \
  "buffer_load_dwordx4 v5, s[44:47], s69 offen offset:0 lds\n\t" \
  "s_mov_b32 s85, 0x390800\n\t" \
  "s_load_dword s84, %[pw], s85\n\t" \
  "buffer_load_dwordx4 v6, s[44:47], s69 offen offset:1024 lds\n\t" \
  "s_mov_b32 s85, 0x391000\n\t" \
  "s_load_dword s84, %[pw], s85\n\t" \
  "buffer_load_dwordx4 v7, s[44:47], s69 offen offset:2048 lds\n\t" \
  "s_mov_b32 s85, 0x3c0000\n\t" \
  "s_load_dword s84, %[pw], s85\n\t" \
  "s_add_u32 m0, s38, 46080\n\t" \
  "s_nop 0\n\t" \
  "buffer_load_dwordx4 v2, s[40:43], s70 offen offset:0 lds\n\t" \
  "s_mov_b32 s85, 0x3c0800\n\t" \
  "s_load_dword s84, %[pw], s85\n\t" \
  "buffer_load_dwordx4 v3, s[40:43], s70 offen offset:1024 lds\n\t" \
  "s_mov_b64 exec, 0xffff\n\t" \
  "buffer_load_dwordx4 v4, s[40:43], s70 offen offset:2048 lds\n\t" \
  "s_add_u32 m0, s39, 61440\n\t" \
  "s_mov_b64 exec, -1\n\t" \
  "buffer_load_dwordx4 v5, s[44:47], s71 offen offset:0 lds\n\t" \
  "buffer_load_dwordx4 v6, s[44:47], s71 offen offset:1024 lds\n\t" \
  "buffer_load_dwordx4 v7, s[44:47], s71 offen offset:2048 lds\n\t" \
  "s_mov_b32 s68, 0x380\n\t" \
  "s_mov_b32 s69, 0x2a0000\n\t" \
  "s_mov_b32 s70, 0x24380\n\t" \
  "s_mov_b32 s71, 0x2d0000\n\t" \
  "s_add_u32 m0, s38, 0\n\t" \
  "s_waitcnt vmcnt(12)\n\t" \
  "s_barrier\n\t" \
  "buffer_load_dwordx4 v0, s[40:43], s68 offen offset:0 lds\n\t" \
  "s_mov_b32 s85, 0x3c1000\n\t" \
  "s_load_dword s84, %[pw], s85\n\t" \
  "buffer_load_dwordx4 v1, s[40:43], s68 offen offset:1024 lds\n\t" \
  "s_mov_b32 s85, 0x3f0000\n\t" \
  "s_load_dword s84, %[pw], s85\n\t" \
  "s_mov_b64 exec, 0xffff\n\t" \
  "buffer_load_dwordx4 v4, s[40:43], s68 offen offset:2048 lds\n\t" \
  "s_add_u32 m0, s39, 0\n\t" \
  "s_mov_b64 exec, -1\n\t" \
  "buffer_load_dwordx4 v5, s[44:47], s69 offen offset:0 lds\n\t" \
  "s_mov_b32 s85, 0x3f0800\n\t" \
  "s_load_dword s84, %[pw], s85\n\t" \
  "buffer_load_dwordx4 v6, s[44:47], s69 offen offset:1024 lds\n\t" \
  "s_mov_b32 s85, 0x3f1000\n\t" \
  "s_load_dword s84, %[pw], s85\n\t" \
  "buffer_load_dwordx4 v7, s[44:47], s69 offen offset:2048 lds\n\t" \
  "s_mov_b32 s85, 0x420000\n\t" \
  "s_load_dword s84, %[pw], s85\n\t" \
  "s_add_u32 m0, s38, 9216\n\t" \
  "s_nop 0\n\t" \
  "buffer_load_dwordx4 v2, s[40:43], s70 offen offset:0 lds\n\t" \
  "s_mov_b32 s85, 0x420800\n\t" \
  "s_load_dword s84, %[pw], s85\n\t" \
  "buffer_load_dwordx4 v3, s[40:43], s70 offen offset:1024 lds\n\t" \
  "s_mov_b64 exec, 0xffff\n\t" \
  "buffer_load_dwordx4 v4, s[40:43], s70 offen offset:2048 lds\n\t" \
  "s_add_u32 m0, s39, 12288\n\t" \
  "s_mov_b64 exec, -1\n\t" \
  "buffer_load_dwordx4 v5, s[44:47], s71 offen offset:0 lds\n\t" \
  "buffer_load_dwordx4 v6, s[44:47], s71 offen offset:1024 lds\n\t" \
  "buffer_load_dwordx4 v7, s[44:47], s71 offen offset:2048 lds\n\t" \
  "s_mov_b32 s68, 0x400\n\t" \
  "s_mov_b32 s69, 0x300000\n\t" \
  "s_mov_b32 s70, 0x24400\n\t" \
  "s_mov_b32 s71, 0x330000\n\t" \
  "s_add_u32 m0, s38, 18432\n\t" \
  "s_waitcnt vmcnt(12)\n\t" \
  "s_barrier\n\t" \
  "buffer_load_dwordx4 v0, s[40:43], s68 offen offset:0 lds\n\t" \
  "s_mov_b32 s85, 0x421000\n\t" \
  "s_load_dword s84, %[pw], s85\n\t" \
  "buffer_load_dwordx4 v1, s[40:43], s68 offen offset:1024 lds\n\t" \
  "s_mov_b32 s85, 0x450000\n\t" \
  "s_load_dword s84, %[pw], s85\n\t" \
  "s_mov_b64 exec, 0xffff\n\t" \
  "buffer_load_dwordx4 v4, s[40:43], s68 offen offset:2048 lds\n\t" \
  "s_add_u32 m0, s39, 24576\n\t" \
  "s_mov_b64 exec, -1\n\t" \
  "buffer_load_dwordx4 v5, s[44:47], s69 offen offset:0 lds\n\t" \
  "s_mov_b32 s85, 0x450800\n\t" \
  "s_load_dword s84, %[pw], s85\n\t" \
  "buffer_load_dwordx4 v6, s[44:47], s69 offen offset:1024 lds\n\t" \
  "s_mov_b32 s85, 0x451000\n\t" \
  "s_load_dword s84, %[pw], s85\n\t" \
  "buffer_load_dwordx4 v7, s[44:47], s69 offen offset:2048 lds\n\t" \
  "s_mov_b32 s85, 0x480000\n\t" \
  "s_load_dword s84, %[pw], s85\n\t" \
  "s_add_u32 m0, s38, 27648\n\t" \
  "s_nop 0\n\t" \
  "buffer_load_dwordx4 v2, s[40:43], s70 offen offset:0 lds\n\t" \
  "s_mov_b32 s85, 0x480800\n\t" \
  "s_load_dword s84, %[pw], s85\n\t" \
  "buffer_load_dwordx4 v3, s[40:43], s70 offen offset:1024 lds\n\t" \
  "s_mov_b64 exec, 0xffff\n\t" \
  "buffer_load_dwordx4 v4, s[40:43], s70 offen offset:2048 lds\n\t" \
  "s_add_u32 m0, s39, 36864\n\t" \
  "s_mov_b64 exec, -1\n\t" \
  "buffer_load_dwordx4 v5, s[44:47], s71 offen offset:0 lds\n\t" \
  "buffer_load_dwordx4 v6, s[44:47], s71 offen offset:1024 lds\n\t" \
  "buffer_load_dwordx4 v7, s[44:47], s71 offen offset:2048 lds\n\t" \
  "s_mov_b32 s68, 0x480\n\t" \
  "s_mov_b32 s69, 0x360000\n\t" \
  "s_mov_b32 s70, 0x24480\n\t" \
  "s_mov_b32 s71, 0x390000\n\t" \
  "s_add_u32 m0, s38, 36864\n\t" \
  "s_waitcnt vmcnt(12)\n\t" \
  "s_barrier\n\t" \
  "buffer_load_dwordx4 v0, s[40:43], s68 offen offset:0 lds\n\t" \
  "s_mov_b32 s85, 0x481000\n\t" \
  "s_load_dword s84, %[pw], s85\n\t" \
  "buffer_load_dwordx4 v1, s[40:43], s68 offen offset:1024 lds\n\t" \
  "s_mov_b32 s85, 0x4b0000\n\t" \
  "s_load_dword s84, %[pw], s85\n\t" \
  "s_mov_b64 exec, 0xffff\n\t" \
  "buffer_load_dwordx4 v4, s[40:43], s68 offen offset:2048 lds\n\t" \
  "s_add_u32 m0, s39, 49152\n\t" \
  "s_mov_b64 exec, -1\n\t" \
  "buffer_load_dwordx4 v5, s[44:47], s69 offen offset:0 lds\n\t" \
  "s_mov_b32 s85, 0x4b0800\n\t" \
  "s_load_dword s84, %[pw], s85\n\t" \
  "buffer_load_dwordx4 v6, s[44:47], s69 offen offset:1024 lds\n\t" \
  "s_mov_b32 s85, 0x4b1000\n\t" \
  "s_load_dword s84, %[pw], s85\n\t" \
  "buffer_load_dwordx4 v7, s[44:47], s69 offen offset:2048 lds\n\t" \
  "s_mov_b32 s85, 0x4e0000\n\t" \
  "s_load_dword s84, %[pw], s85\n\t" \
  "s_add_u32 m0, s38, 46080\n\t" \
  "s_nop 0\n\t" \
  "buffer_load_dwordx4 v2, s[40:43], s70 offen offset:0 lds\n\t" \
  "s_mov_b32 s85, 0x4e0800\n\t" \
  "s_load_dword s84, %[pw], s85\n\t" \
  "buffer_load_dwordx4 v3, s[40:43], s70 offen offset:1024 lds\n\t" \
  "s_mov_b64 exec, 0xffff\n\t" \
  "buffer_load_dwordx4 v4, s[40:43], s70 offen offset:2048 lds\n\t" \
  "s_add_u32 m0, s39, 61440\n\t" \
  "s_mov_b64 exec, -1\n\t" \
  "buffer_load_dwordx4 v5, s[44:47], s71 offen offset:0 lds\n\t" \
  "buffer_load_dwordx4 v6, s[44:47], s71 offen offset:1024 lds\n\t" \
  "buffer_load_dwordx4 v7, s[44:47], s71 offen offset:2048 lds\n\t" \
  "s_mov_b32 s68, 0x500\n\t" \
  "s_mov_b32 s69, 0x3c0000\n\t" \
  "s_mov_b32 s70, 0x24500\n\t" \
  "s_mov_b32 s71, 0x3f0000\n\t" \
  "s_add_u32 m0, s38, 0\n\t" \
  "s_waitcnt vmcnt(12)\n\t" \
  "s_barrier\n\t" \
  "buffer_load_dwordx4 v0, s[40:43], s68 offen offset:0 lds\n\t" \
  "s_mov_b32 s85, 0x4e1000\n\t" \
  "s_load_dword s84, %[pw], s85\n\t" \
  "buffer_load_dwordx4 v1, s[40:43], s68 offen offset:1024 lds\n\t" \
  "s_mov_b32 s85, 0x510000\n\t" \
  "s_load_dword s84, %[pw], s85\n\t" \
  "s_mov_b64 exec, 0xffff\n\t" \
  "buffer_load_dwordx4 v4, s[40:43], s68 offen offset:2048 lds\n\t" \
  "s_add_u32 m0, s39, 0\n\t" \
  "s_mov_b64 exec, -1\n\t" \
  "buffer_load_dwordx4 v5, s[44:47], s69 offen offset:0 lds\n\t" \
  "s_mov_b32 s85, 0x510800\n\t" \
  "s_load_dword s84, %[pw], s85\n\t" \
  "buffer_load_dwordx4 v6, s[44:47], s69 offen offset:1024 lds\n\t" \
  "s_mov_b32 s85, 0x511000\n\t" \
  "s_load_dword s84, %[pw], s85\n\t" \
  "buffer_load_dwordx4 v7, s[44:47], s69 offen offset:2048 lds\n\t" \
  "s_mov_b32 s85, 0x540000\n\t" \
  "s_load_dword s84, %[pw], s85\n\t" \
  "s_add_u32 m0, s38, 9216\n\t" \
  "s_nop 0\n\t" \
  "buffer_load_dwordx4 v2, s[40:43], s70 offen offset:0 lds\n\t" \
  "s_mov_b32 s85, 0x540800\n\t" \
  "s_load_dword s84, %[pw], s85\n\t" \
  "buffer_load_dwordx4 v3, s[40:43], s70 offen offset:1024 lds\n\t" \
  "s_mov_b64 exec, 0xffff\n\t" \
  "buffer_load_dwordx4 v4, s[40:43], s70 offen offset:2048 lds\n\t" \
  "s_add_u32 m0, s39, 12288\n\t" \
  "s_mov_b64 exec, -1\n\t" \
  "buffer_load_dwordx4 v5, s[44:47], s71 offen offset:0 lds\n\t" \
  "buffer_load_dwordx4 v6, s[44:47], s71 offen offset:1024 lds\n\t" \
  "buffer_load_dwordx4 v7, s[44:47], s71 offen offset:2048 lds\n\t" \
  "s_mov_b32 s68, 0x580\n\t" \
  "s_mov_b32 s69, 0x420000\n\t" \
  "s_mov_b32 s70, 0x24580\n\t" \
  "s_mov_b32 s71, 0x450000\n\t" \
  "s_add_u32 m0, s38, 18432\n\t" \
  "s_waitcnt vmcnt(12)\n\t" \
  "s_barrier\n\t" \
  "buffer_load_dwordx4 v0, s[40:43], s68 offen offset:0 lds\n\t" \
  "s_load_dword s84, %[pq], 0x0\n\t" \
  "buffer_load_dwordx4 v1, s[40:43], s68 offen offset:1024 lds\n\t" \
  "s_load_dword s84, %[pq], 0x40\n\t" \
  "s_mov_b64 exec, 0xffff\n\t" \
  "buffer_load_dwordx4 v4, s[40:43], s68 offen offset:2048 lds\n\t" \
  "s_add_u32 m0, s39, 24576\n\t" \
  "s_mov_b64 exec, -1\n\t" \
  "buffer_load_dwordx4 v5, s[44:47], s69 offen offset:0 lds\n\t" \
  "s_load_dword s84, %[pq], 0x800\n\t" \
  "buffer_load_dwordx4 v6, s[44:47], s69 offen offset:1024 lds\n\t" \
  "s_load_dword s84, %[pq], 0x840\n\t" \
  "buffer_load_dwordx4 v7, s[44:47], s69 offen offset:2048 lds\n\t" \
  "s_mov_b32 s85, 0x541000\n\t" \
  "s_load_dword s84, %[pw], s85\n\t" \
  "s_add_u32 m0, s38, 27648\n\t" \
  "s_nop 0\n\t" \
  "buffer_load_dwordx4 v2, s[40:43], s70 offen offset:0 lds\n\t" \
  "s_mov_b32 s85, 0x570000\n\t" \
  "s_load_dword s84, %[pw], s85\n\t" \
  "buffer_load_dwordx4 v3, s[40:43], s70 offen offset:1024 lds\n\t" \
  "s_mov_b32 s85, 0x570800\n\t" \
  "s_load_dword s84, %[pw], s85\n\t" \
  "s_mov_b64 exec, 0xffff\n\t" \
  "buffer_load_dwordx4 v4, s[40:43], s70 offen offset:2048 lds\n\t" \
  "s_add_u32 m0, s39, 36864\n\t" \
  "s_mov_b64 exec, -1\n\t" \
  "buffer_load_dwordx4 v5, s[44:47], s71 offen offset:0 lds\n\t" \
  "s_mov_b32 s85, 0x571000\n\t" \
  "s_load_dword s84, %[pw], s85\n\t" \
  "buffer_load_dwordx4 v6, s[44:47], s71 offen offset:1024 lds\n\t" \
  "s_mov_b32 s85, 0x5a0000\n\t" \
  "s_load_dword s84, %[pw], s85\n\t" \
  "buffer_load_dwordx4 v7, s[44:47], s71 offen offset:2048 lds\n\t" \
  "s_mov_b32 s85, 0x5a0800\n\t" \
  "s_load_dword s84, %[pw], s85\n\t" \
  "s_mov_b32 s68, 0x600\n\t" \
  "s_mov_b32 s69, 0x480000\n\t" \
  "s_mov_b32 s70, 0x24600\n\t" \
  "s_mov_b32 s71, 0x4b0000\n\t" \
  "s_add_u32 m0, s38, 36864\n\t" \
  "s_waitcnt vmcnt(12)\n\t" \
  "s_barrier\n\t" \
  "buffer_load_dwordx4 v0, s[40:43], s68 offen offset:0 lds\n\t" \
  "s_load_dword s84, %[pq], 0x1000\n\t" \
  "buffer_load_dwordx4 v1, s[40:43], s68 offen offset:1024 lds\n\t" \
  "s_load_dword s84, %[pq], 0x1040\n\t" \
  "s_mov_b64 exec, 0xffff\n\t" \
  "buffer_load_dwordx4 v4, s[40:43], s68 offen offset:2048 lds\n\t" \
  "s_add_u32 m0, s39, 49152\n\t" \
  "s_mov_b64 exec, -1\n\t" \
  "buffer_load_dwordx4 v5, s[44:47], s69 offen offset:0 lds\n\t" \
  "s_load_dword s84, %[pq], 0x1800\n\t" \
  "buffer_load_dwordx4 v6, s[44:47], s69 offen offset:1024 lds\n\t" \
  "s_load_dword s84, %[pq], 0x1840\n\t" \
  "buffer_load_dwordx4 v7, s[44:47], s69 offen offset:2048 lds\n\t" \
  "s_mov_b32 s85, 0x5a1000\n\t" \
  "s_load_dword s84, %[pw], s85\n\t" \
  "s_add_u32 m0, s38, 46080\n\t" \
  "s_nop 0\n\t" \
  "buffer_load_dwordx4 v2, s[40:43], s70 offen offset:0 lds\n\t" \
  "s_mov_b32 s85, 0x5d0000\n\t" \
  "s_load_dword s84, %[pw], s85\n\t" \
  "buffer_load_dwordx4 v3, s[40:43], s70 offen offset:1024 lds\n\t" \
  "s_mov_b32 s85, 0x5d0800\n\t" \
  "s_load_dword s84, %[pw], s85\n\t" \
  "s_mov_b64 exec, 0xffff\n\t" \
  "buffer_load_dwordx4 v4, s[40:43], s70 offen offset:2048 lds\n\t" \
  "s_add_u32 m0, s39, 61440\n\t" \
  "s_mov_b64 exec, -1\n\t" \
  "buffer_load_dwordx4 v5, s[44:47], s71 offen offset:0 lds\n\t" \
  "s_mov_b32 s85, 0x5d1000\n\t" \
  "s_load_dword s84, %[pw], s85\n\t" \
  "buffer_load_dwordx4 v6, s[44:47], s71 offen offset:1024 lds\n\t" \
  "buffer_load_dwordx4 v7, s[44:47], s71 offen offset:2048 lds\n\t" \
  "s_mov_b32 s68, 0x680\n\t" \
  "s_mov_b32 s69, 0x4e0000\n\t" \
  "s_mov_b32 s70, 0x24680\n\t" \
  "s_mov_b32 s71, 0x510000\n\t" \
  "s_add_u32 m0, s38, 0\n\t" \
  "s_waitcnt vmcnt(12)\n\t" \
  "s_barrier\n\t" \
  "buffer_load_dwordx4 v0, s[40:43], s68 offen offset:0 lds\n\t" \
  "s_load_dword s84, %[pq], 0x2000\n\t" \
  "buffer_load_dwordx4 v1, s[40:43], s68 offen offset:1024 lds\n\t" \
  "s_load_dword s84, %[pq], 0x2040\n\t" \
  "s_mov_b64 exec, 0xffff\n\t" \
  "buffer_load_dwordx4 v4, s[40:43], s68 offen offset:2048 lds\n\t" \
  "s_add_u32 m0, s39, 0\n\t" \
  "s_mov_b64 exec, -1\n\t" \
  "buffer_load_dwordx4 v5, s[44:47], s69 offen offset:0 lds\n\t" \
  "s_load_dword s84, %[pq], 0x2800\n\t" \
  "buffer_load_dwordx4 v6, s[44:47], s69 offen offset:1024 lds\n\t" \
  "s_load_dword s84, %[pq], 0x2840\n\t" \
  "buffer_load_dwordx4 v7, s[44:47], s69 offen offset:2048 lds\n\t" \
  "s_add_u32 m0, s38, 9216\n\t" \
  "s_nop 0\n\t" \
  "buffer_load_dwordx4 v2, s[40:43], s70 offen offset:0 lds\n\t" \
  "buffer_load_dwordx4 v3, s[40:43], s70 offen offset:1024 lds\n\t" \
  "s_mov_b64 exec, 0xffff\n\t" \
  "buffer_load_dwordx4 v4, s[40:43], s70 offen offset:2048 lds\n\t" \
  "s_add_u32 m0, s39, 12288\n\t" \
  "s_mov_b64 exec, -1\n\t" \
  "buffer_load_dwordx4 v5, s[44:47], s71 offen offset:0 lds\n\t" \
  "buffer_load_dwordx4 v6, s[44:47], s71 offen offset:1024 lds\n\t" \
  "buffer_load_dwordx4 v7, s[44:47], s71 offen offset:2048 lds\n\t" \
  "s_mov_b32 s68, 0x700\n\t" \
  "s_mov_b32 s69, 0x540000\n\t" \
  "s_mov_b32 s70, 0x24700\n\t" \
  "s_mov_b32 s71, 0x570000\n\t" \
  "s_add_u32 m0, s38, 18432\n\t" \
  "s_waitcnt vmcnt(12)\n\t" \
  "s_barrier\n\t" \
  "buffer_load_dwordx4 v0, s[40:43], s68 offen offset:0 lds\n\t" \
  "s_load_dword s84, %[pq], 0x3000\n\t" \
  "buffer_load_dwordx4 v1, s[40:43], s68 offen offset:1024 lds\n\t" \
  "s_load_dword s84, %[pq], 0x3040\n\t" \
  "s_mov_b64 exec, 0xffff\n\t" \
  "buffer_load_dwordx4 v4, s[40:43], s68 offen offset:2048 lds\n\t" \
  "s_add_u32 m0, s39, 24576\n\t" \
  "s_mov_b64 exec, -1\n\t" \
  "buffer_load_dwordx4 v5, s[44:47], s69 offen offset:0 lds\n\t" \
  "s_load_dword s84, %[pq], 0x3800\n\t" \
  "buffer_load_dwordx4 v6, s[44:47], s69 offen offset:1024 lds\n\t" \
  "s_load_dword s84, %[pq], 0x3840\n\t" \
  "buffer_load_dwordx4 v7, s[44:47], s69 offen offset:2048 lds\n\t" \
  "s_add_u32 m0, s38, 27648\n\t" \
  "s_nop 0\n\t" \
  "buffer_load_dwordx4 v2, s[40:43], s70 offen offset:0 lds\n\t" \
  "buffer_load_dwordx4 v3, s[40:43], s70 offen offset:1024 lds\n\t" \
  "s_mov_b64 exec, 0xffff\n\t" \
  "buffer_load_dwordx4 v4, s[40:43], s70 offen offset:2048 lds\n\t" \
  "s_add_u32 m0, s39, 36864\n\t" \
  "s_mov_b64 exec, -1\n\t" \
  "buffer_load_dwordx4 v5, s[44:47], s71 offen offset:0 lds\n\t" \
  "buffer_load_dwordx4 v6, s[44:47], s71 offen offset:1024 lds\n\t" \
  "buffer_load_dwordx4 v7, s[44:47], s71 offen offset:2048 lds\n\t" \
  "s_mov_b32 s68, 0x780\n\t" \
  "s_mov_b32 s69, 0x5a0000\n\t" \
  "s_mov_b32 s70, 0x24780\n\t" \
  "s_mov_b32 s71, 0x5d0000\n\t" \
  "s_add_u32 m0, s38, 36864\n\t" \
  "s_waitcnt vmcnt(12)\n\t" \
  "s_barrier\n\t" \
  "buffer_load_dwordx4 v0, s[40:43], s68 offen offset:0 lds\n\t" \
  "buffer_load_dwordx4 v1, s[40:43], s68 offen offset:1024 lds\n\t" \
  "s_mov_b64 exec, 0xffff\n\t" \
  "buffer_load_dwordx4 v4, s[40:43], s68 offen offset:2048 lds\n\t" \
  "s_add_u32 m0, s39, 49152\n\t" \
  "s_mov_b64 exec, -1\n\t" \
  "buffer_load_dwordx4 v5, s[44:47], s69 offen offset:0 lds\n\t" \
  "buffer_load_dwordx4 v6, s[44:47], s69 offen offset:1024 lds\n\t" \
  "buffer_load_dwordx4 v7, s[44:47], s69 offen offset:2048 lds\n\t" \
  "s_add_u32 m0, s38, 46080\n\t" \
  "s_nop 0\n\t" \
  "buffer_load_dwordx4 v2, s[40:43], s70 offen offset:0 lds\n\t" \
  "buffer_load_dwordx4 v3, s[40:43], s70 offen offset:1024 lds\n\t" \
  "s_mov_b64 exec, 0xffff\n\t" \
  "buffer_load_dwordx4 v4, s[40:43], s70 offen offset:2048 lds\n\t" \
  "s_add_u32 m0, s39, 61440\n\t" \
  "s_mov_b64 exec, -1\n\t" \
  "buffer_load_dwordx4 v5, s[44:47], s71 offen offset:0 lds\n\t" \
  "buffer_load_dwordx4 v6, s[44:47], s71 offen offset:1024 lds\n\t" \
  "buffer_load_dwordx4 v7, s[44:47], s71 offen offset:2048 lds\n\t" \
  "v_readlane_b32 s68, v9, 18\n\t" \
  "v_readlane_b32 s69, v9, 19\n\t" \
  "v_readlane_b32 s70, v9, 20\n\t" \
  "v_readlane_b32 s71, v9, 21\n\t" \
  "v_readlane_b32 s72, v9, 0\n\t" \
  "v_readlane_b32 s73, v9, 1\n\t" \
  "v_readlane_b32 s74, v9, 2\n\t" \
  "v_readlane_b32 s75, v9, 3\n\t" \
  "v_readlane_b32 s76, v9, 4\n\t" \
  "v_readlane_b32 s77, v9, 5\n\t" \
  "s_add_u32 m0, %[ob], 73728\n\t" \
  "s_waitcnt vmcnt(12)\n\t" \
  "s_barrier\n\t" \
  "s_nop 2\n\t" \
  "buffer_load_dwordx4 v8, %[so], s68 offen lds\n\t" \
  "s_add_u32 m0, %[ob], 77824\n\t" \
  "s_nop 0\n\t" \
  "buffer_load_dwordx4 v8, %[so], s69 offen lds\n\t" \
  "s_add_u32 m0, %[ob], 81920\n\t" \
  "s_nop 0\n\t" \
  "buffer_load_dwordx4 v8, %[so], s70 offen lds\n\t" \
  "s_add_u32 m0, %[ob], 86016\n\t" \
  "s_nop 0\n\t" \
  "buffer_load_dwordx4 v8, %[so], s71 offen lds\n\t" \
  "s_add_u32 m0, %[ob], 0\n\t" \
  "s_nop 0\n\t" \
  "buffer_load_dwordx4 v8, %[so], s72 offen lds\n\t" \
  "s_add_u32 m0, %[ob], 4096\n\t" \
  "s_nop 0\n\t" \
  "buffer_load_dwordx4 v8, %[so], s73 offen lds\n\t" \
  "s_add_u32 m0, %[ob], 8192\n\t" \
  "s_nop 0\n\t" \
  "buffer_load_dwordx4 v8, %[so], s74 offen lds\n\t" \
  "s_add_u32 m0, %[ob], 12288\n\t" \
  "s_nop 0\n\t" \
  "buffer_load_dwordx4 v8, %[so], s75 offen lds\n\t" \
  "s_add_u32 m0, %[ob], 16384\n\t" \
  "s_nop 0\n\t" \
  "buffer_load_dwordx4 v8, %[so], s76 offen lds\n\t" \
  "s_add_u32 m0, %[ob], 20480\n\t" \
  "s_nop 0\n\t" \
  "buffer_load_dwordx4 v8, %[so], s77 offen lds\n\t" \
  "s_waitcnt vmcnt(10)\n\t" \
  "s_barrier\n\t" \
  "s_nop 2\n\t" \
  "s_add_u32 m0, %[ob], 90112\n\t" \
  "v_readlane_b32 s68, v9, 22\n\t" \
  "s_nop 4\n\t" \
  "buffer_load_dwordx4 v8, %[so], s68 offen lds\n\t" \
  "s_add_u32 m0, %[ob], 94208\n\t" \
  "v_readlane_b32 s68, v9, 23\n\t" \
  "s_nop 4\n\t" \
  "buffer_load_dwordx4 v8, %[so], s68 offen lds\n\t" \
  "s_add_u32 m0, %[ob], 98304\n\t" \
  "v_readlane_b32 s68, v9, 24\n\t" \
  "s_nop 4\n\t" \
  "buffer_load_dwordx4 v8, %[so], s68 offen lds\n\t" \
  "s_add_u32 m0, %[ob], 102400\n\t" \
  "v_readlane_b32 s68, v9, 25\n\t" \
  "s_nop 4\n\t" \
  "buffer_load_dwordx4 v8, %[so], s68 offen lds\n\t" \
  "s_add_u32 m0, %[ob], 106496\n\t" \
  "v_readlane_b32 s68, v9, 26\n\t" \
  "s_nop 4\n\t" \
  "buffer_load_dwordx4 v8, %[so], s68 offen lds\n\t" \
  "s_add_u32 m0, %[ob], 24576\n\t" \
  "v_readlane_b32 s68, v9, 6\n\t" \
  "s_nop 4\n\t" \
  "buffer_load_dwordx4 v8, %[so], s68 offen lds\n\t" \
  "s_add_u32 m0, %[ob], 28672\n\t" \
  "v_readlane_b32 s68, v9, 7\n\t" \
  "s_nop 4\n\t" \
  "buffer_load_dwordx4 v8, %[so], s68 offen lds\n\t" \
  "s_add_u32 m0, %[ob], 32768\n\t" \
  "v_readlane_b32 s68, v9, 8\n\t" \
  "s_nop 4\n\t" \
  "buffer_load_dwordx4 v8, %[so], s68 offen lds\n\t" \
  "s_add_u32 m0, %[ob], 36864\n\t" \
  "v_readlane_b32 s68, v9, 9\n\t" \
  "s_nop 4\n\t" \
  "buffer_load_dwordx4 v8, %[so], s68 offen lds\n\t" \
  "s_add_u32 m0, %[ob], 40960\n\t" \
  "v_readlane_b32 s68, v9, 10\n\t" \
  "s_nop 4\n\t" \
  "buffer_load_dwordx4 v8, %[so], s68 offen lds\n\t" \
  "s_add_u32 m0, %[ob], 45056\n\t" \
  "v_readlane_b32 s68, v9, 11\n\t" \
  "s_nop 4\n\t" \
  "buffer_load_dwordx4 v8, %[so], s68 offen lds\n\t" \
  "s_nop 3\n\t" \
  "s_barrier\n\t" \
  "s_nop 2\n\t" \
  "s_add_u32 m0, %[ob], 110592\n\t" \
  "v_readlane_b32 s68, v9, 27\n\t" \
  "s_nop 4\n\t" \
  "buffer_load_dwordx4 v8, %[so], s68 offen lds\n\t" \
  "s_add_u32 m0, %[ob], 114688\n\t" \
  "v_readlane_b32 s68, v9, 28\n\t" \
  "s_nop 4\n\t" \
  "buffer_load_dwordx4 v8, %[so], s68 offen lds\n\t" \
  "s_add_u32 m0, %[ob], 118784\n\t" \
  "v_readlane_b32 s68, v9, 29\n\t" \
  "s_nop 4\n\t" \
  "buffer_load_dwordx4 v8, %[so], s68 offen lds\n\t" \
  "s_add_u32 m0, %[ob], 122880\n\t" \
  "v_readlane_b32 s68, v9, 30\n\t" \
  "s_nop 4\n\t" \
  "buffer_load_dwordx4 v8, %[so], s68 offen lds\n\t" \
  "s_add_u32 m0, %[ob], 126976\n\t" \
  "v_readlane_b32 s68, v9, 31\n\t" \
  "s_nop 4\n\t" \
  "buffer_load_dwordx4 v8, %[so], s68 offen lds\n\t" \
  "s_add_u32 m0, %[ob], 49152\n\t" \
  "v_readlane_b32 s68, v9, 12\n\t" \
  "s_nop 4\n\t" \
  "buffer_load_dwordx4 v8, %[so], s68 offen lds\n\t" \
  "s_add_u32 m0, %[ob], 53248\n\t" \
  "v_readlane_b32 s68, v9, 13\n\t" \
  "s_nop 4\n\t" \
  "buffer_load_dwordx4 v8, %[so], s68 offen lds\n\t" \
  "s_add_u32 m0, %[ob], 57344\n\t" \
  "v_readlane_b32 s68, v9, 14\n\t" \
  "s_nop 4\n\t" \
  "buffer_load_dwordx4 v8, %[so], s68 offen lds\n\t" \
  "s_add_u32 m0, %[ob], 61440\n\t" \
  "v_readlane_b32 s68, v9, 15\n\t" \
  "s_nop 4\n\t" \
  "buffer_load_dwordx4 v8, %[so], s68 offen lds\n\t" \
  "s_add_u32 m0, %[ob], 65536\n\t" \
  "v_readlane_b32 s68, v9, 16\n\t" \
  "s_nop 4\n\t" \
  "buffer_load_dwordx4 v8, %[so], s68 offen lds\n\t" \
  "s_add_u32 m0, %[ob], 69632\n\t" \
  "v_readlane_b32 s68, v9, 17\n\t" \
  "s_nop 4\n\t" \
  "buffer_load_dwordx4 v8, %[so], s68 offen lds\n\t" \
  "s_waitcnt vmcnt(0) lgkmcnt(0)\n\t"
#define LD_NBAR 17
#define LD_CO 0
#define LD_HT 8
#define LD_TP 3
#define LD_CLOB "v11", "s68", "s69", "s70", "s71", "s72", "s73", "s74", "s75", "s76", "s77", "s78", "s79", "s80", "s81", "s82", "s83", "s85"

#define ENDW
#define TS(i)
#define TSS(i)
#define TOUT(n)

#define SPIN(cond,slp) do{ int sp_=0; while(cond){ __builtin_amdgcn_s_sleep(slp); if(++sp_>(1<<22)) break; } }while(0)
__device__ __forceinline__ unsigned rd1(const char* p){ return __builtin_amdgcn_readfirstlane(*(volatile LDS_AS unsigned*)(LDS_AS char*)p); }
__device__ __forceinline__ unsigned lds_inc(const char* p, int l){ unsigned r=0; if(l==0) r=__hip_atomic_fetch_add((LDS_AS unsigned*)(LDS_AS char*)p, 1u, __ATOMIC_RELAXED, __HIP_MEMORY_SCOPE_WORKGROUP); return __builtin_amdgcn_readfirstlane(r); }
__device__ __forceinline__ void lds_add(unsigned adr){ asm volatile("s_mov_b64 exec, 1\n\tds_add_u32 %0, %1\n\ts_mov_b64 exec, -1" :: "v"(adr), "v"(1u) : "memory"); }
__device__ __forceinline__ void publish(unsigned cnt, const unsigned* base, unsigned voff, unsigned tag){
  unsigned t;
  asm volatile("s_waitcnt vmcnt(0)\n\ts_mov_b64 exec, 1\n\tds_add_rtn_u32 %0, %1, %2\n\ts_waitcnt lgkmcnt(0)\n\tv_cmp_eq_u32 vcc, 3, %0\n\ts_cbranch_vccz PB_%=\n\t"
    "s_mov_b64 exec, 0xffff\n\tglobal_store_dword %3, %4, %5 sc1\n\tPB_%=:\n\ts_mov_b64 exec, -1"
    : "=&v"(t) : "v"(cnt), "v"(1u), "v"(voff), "v"(tag), "s"(base) : "memory", "vcc");
}
__device__ __forceinline__ void bars(int n){
  asm volatile("BL_%=:\n\ts_barrier\n\ts_sub_u32 %0, %0, 1\n\ts_cmp_lg_u32 %0, 0\n\ts_cbranch_scc1 BL_%=" : "+s"(n) :: "memory", "scc");
}
#define barc(rem) barc_(rem)
#define CTRA
#define CTRP
__device__ __forceinline__ void barc_(unsigned& rem){
  asm volatile("s_cmp_eq_u32 %0, 0\n\ts_cbranch_scc1 BC_%=\n\ts_barrier\n\ts_sub_u32 %0, %0, 1\n\tBC_%=:" : "+s"(rem) :: "memory", "scc");
}
template<int SLP> __device__ __forceinline__ void wait_flag_b(const unsigned* p, unsigned voff, unsigned tag, unsigned& rem CTRA){
  int spins=0;
  while(true){
    unsigned v; asm volatile("global_load_dword %0, %1, %2 sc1" : "=v"(v) : "v"(voff), "s"(p) : "memory");
    barc(rem); if(PB2) barc(rem);
    asm volatile("s_waitcnt vmcnt(0)" : "+v"(v) :: "memory");
    if(__all(v==tag) || ++spins>(1<<21)) break;
    if(rem==0) __builtin_amdgcn_s_sleep(SLP);
  }
}
__device__ __forceinline__ void stout(void* sbase, unsigned voff, u32x4 v){
  st16cs(sbase,voff,v);
}

typedef unsigned u32x8 __attribute__((ext_vector_type(8)));
typedef unsigned u32x16 __attribute__((ext_vector_type(16)));
__device__ __forceinline__ char* ptr64(unsigned lo, unsigned hi){ return (char*)(((unsigned long long)hi<<32)|lo); }
__device__ __forceinline__ u32x4 srd(const char* p, unsigned bytes){ return u32x4{(unsigned)(size_t)p,(unsigned)((size_t)p>>32)&0xffffu,bytes,0x20000u}; }
constexpr size_t WS_O=0, WS_KV=(size_t)S*DM*2, WS_Y=(size_t)S*DM*4, WS_FL=(size_t)S*DM*12;

__global__ __launch_bounds__(768,3) void fused_kernel(const u16* x_, const u16* wqkv_, const u16* wo_, char* ws_, u16* out_,
    const u16* kin_, const u16* vin_, const u16* shk_, const u16* hk_, const u16* qin_, const u16* sD_)
{
  __shared__ __attribute__((aligned(128))) char smem[ALL_LDS];
  TS(0);
  int wv;
  asm volatile(
      "s_load_dwordx16 s[48:63], %[ka], 0x0\n\ts_load_dwordx4 s[64:67], %[ka], 0x40\n\ts_load_dwordx2 s[78:79], %[ka], 0x50\n\ts_getpc_b64 s[72:73]\n\tv_readfirstlane_b32 s75, v0\n\ts_lshr_b32 s74, %[b], 3\n\ts_mul_i32 s74, s74, 12\n\ts_lshr_b32 s75, s75, 6\n\t"
      "s_add_u32 s74, s74, s75\n\ts_min_u32 s74, s74, %[cl]\n\ts_lshl_b32 s74, s74, 7\n\ts_mov_b32 m0, %[sk]\n\tv_mov_b32 v1, s74\n\ts_mov_b64 exec, 1\n\tglobal_load_lds_dword v1, s[72:73]\n\ts_mov_b64 exec, -1\n\t"
      "s_mov_b32 s76, %[b]\n\ts_cmp_lt_u32 s75, 4\n\ts_cbranch_scc1 FK_G\n\ts_cmp_lt_u32 s75, 8\n\ts_cbranch_scc0 FK_C\n\t"
      "v_and_b32 v9, 7, v0\n\tv_and_b32 v10, 63, v0\n\ts_lshl_b32 s74, %[b], 19\n\ts_lshl_b32 s73, %[b], 11\n\ts_or_b32 s74, s74, s73\n\ts_and_b32 s74, s74, 0x3fc000\n\ts_lshl_b32 s73, s75, 9\n\t"
      "s_add_u32 s74, s74, s73\n\ts_add_u32 s74, s74, 0x7800\n\tv_lshl_add_u32 v9, v9, 11, s74\n\tv_and_b32 v1, 24, v0\n\ts_lshl2_add_u32 s74, %[b], s75\n\ts_mul_i32 s74, s74, %[nw]\n\tv_lshl_add_u32 v9, v1, 4, v9\n\t"
      "s_add_u32 s74, s74, %[w0]\n\tv_add_lshl_u32 v10, v10, s74, 7\n\t"
      "s_setprio %[lp]\n\ts_and_b32 s74, s75, 3\n\tv_lshrrev_b32 v1, 3, v0\n\tv_lshrrev_b32 v3, 4, v0\n\ts_and_b32 s72, %[b], 1\n\ts_and_b32 s73, %[b], 0xe0\n\ts_lshl_b32 s72, s72, 10\n\ts_lshl2_add_u32 s72, s73, s72\n\t"
      "v_and_b32 v4, 2, v1\n\tv_and_or_b32 v3, v3, 4, v4\n\tv_and_b32 v4, 31, v1\n\tv_xor_b32 v3, v3, v0\n\tv_mul_u32_u24 v4, 0x1800, v4\n\tv_and_b32 v3, 7, v3\n\tv_lshl_add_u32 v3, v3, 4, s72\n\t"
      "s_mul_i32 s39, s74, 0xc00\n\ts_mul_i32 s38, s74, 0x900\n\ts_mul_i32 s74, s74, 18\n\tv_add_u32 v5, v3, v4\n\tv_and_b32 v1, 7, v1\n\tv_add_u32 v6, 0x400, v5\n\tv_add_u32 v1, s74, v1\n\tv_add_u32 v7, 0x800, v5\n\t"
      "s_and_b32 s72, %[b], 6\n\ts_and_b32 s73, %[b], 24\n\ts_lshl4_add_u32 s72, s72, s73\n\tv_xor_b32 v3, v1, v0\n\ts_lshl_b32 s72, s72, 15\n\tv_and_b32 v3, 7, v3\n\tv_lshl_add_u32 v1, v1, 11, s72\n\t"
      "s_mov_b32 s47, 0x20000\n\ts_mov_b32 s46, 0x600000\n\tv_lshl_add_u32 v2, v3, 4, v1\n\ts_mov_b32 s43, s47\n\ts_mov_b32 s42, 0x408000\n\ts_movk_i32 s73, 0x3c00\n\t"
      "v_add_u32 v3, s73, v2\n\ts_add_u32 s38, s38, %[xf]\n\tv_max_u32 v0, 0x8000, v2\n\tv_add_u32 v4, s73, v3\n\tv_max_u32 v1, 0x8000, v3\n\t"
      "s_waitcnt lgkmcnt(0)\n\ts_mov_b64 s[44:45], s[50:51]\n\ts_sub_u32 s40, s48, 0x8000\n\ts_subb_u32 s41, s49, 0\n\t"
      LD_ASM1
      "s_branch FK_L"
      : "=s"(wv)
      : "{v0}"(threadIdx.x), [ka]"s"(__builtin_amdgcn_kernarg_segment_ptr()), [b]"s"(blockIdx.x), [cl]"n"(CODEL-1+CODEX), [sk]"n"(SINK+256), [lp]"n"(LPR), [xf]"n"(X_OFF), [nw]"n"(3*TPW), [w0]"n"(TW0*3072-12*TPW), [wm]"n"((1<<(3*TPW))-1)
      : "memory", "scc", "s48", "s49", "s50", "s51", "s52", "s53", "s54", "s55", "s72", "s73", "s74", "s75", "s76", "s78", "s79", "v1", "v2", "v3", "v4", "v5", "v6", "v7", "v9", "v10",
        "s40", "s41", "s42", "s43", "s44", "s45", "s46", "s47", "s38", "s39", "s56", "s57", "s58", "s59", "s60", "s61", "s62", "s63", "s64", "s65", "s66", "s67", "s68");
  #define ENTER(lbl) int w8, bid; u32x8 a0, a1; u32x4 a2; u32x2 a3; asm volatile(lbl ":" : "={s75}"(w8), "={s76}"(bid), "={s[48:55]}"(a0), "={s[56:63]}"(a1), "={s[64:67]}"(a2), "={s[78:79]}"(a3) :: "memory");
  #define ARGW asm volatile("s_waitcnt lgkmcnt(0)" : "+{s[48:55]}"(a0), "+{s[56:63]}"(a1), "+{s[64:67]}"(a2), "+{s[78:79]}"(a3));
  #define ROLE const int w=w8&3, xcd=bid&7, idx=bid>>3, rb=(xcd>>1)*4+(idx&3), hp=(xcd&1)*8+(idx>>2), s0=rb*128; (void)s0; (void)hp;
  #define LANEVARS int l; asm volatile("v_mbcnt_lo_u32_b32 %0, -1, 0\n\tv_mbcnt_hi_u32_b32 %0, -1, %0" : "=v"(l)); const int g=l>>4, fr=l&15, tq=(l&15)>>2, tp=l&3; (void)g; (void)fr; (void)tq; (void)tp;
  #define ARGS0 const char* x=ptr64(a0[0],a0[1]); const char* wqkv=ptr64(a0[2],a0[3]); const char* wo=ptr64(a0[4],a0[5]); char* ws=ptr64(a0[6],a0[7]); \
    unsigned* flags=(unsigned*)(ws+WS_FL); const int hot0=(3-(int)(a0[2]>>7))&7; (void)x; (void)wqkv; (void)wo; (void)flags; (void)hot0;
  #define OCOL ((hp&8)|((hp+(int)(a0[2]>>7)-(int)(a0[4]>>7)+OSH)&7))
  #define ORA ((unsigned)(hp-hot0-1)&7u)
  #define ORE(c) (ORA==7u? (c)-1u : (c))
  #define ORO(c) (ORE(c)>=NF && ORE(c)<NF+7u)
  #define ORD(c) (((((ORE(c)<14u? ((ORA==7u? 0u:ORA)+(ORO(c)? ORE(c)-NF : (ORE(c)<NF? ORE(c) : ORE(c)-7u)))%7u : 7u)+hot0+1)&7u))+8u*(((hp>>3)^(ORO(c) || (c)==15u))&1u))
  const unsigned sb0=__builtin_amdgcn_readfirstlane(lds_addr(smem));
  const char* syn=smem+SYN;

  if(__builtin_expect(wv==1,1)){
    ENTER("FK_L")
    u32x4 sx, sw; unsigned xl, wl, xoA, xoB, xo, xo1, xo2, wof[3];
    asm volatile("; descriptors and LDS bases" : "={s[40:43]}"(sx), "={s[44:47]}"(sw), "={s38}"(xl), "={s39}"(wl));
    asm volatile("; lane offsets" : "={v0}"(xoA), "={v1}"(xoB), "={v2}"(xo), "={v3}"(xo1), "={v4}"(xo2), "={v5}"(wof[0]), "={v6}"(wof[1]), "={v7}"(wof[2]));
    unsigned ltr=0, ltr2=0, ltr3=0, sd; (void)ltr; (void)ltr2; (void)ltr3;
    const char* pw;
    {
      ROLE
      LANEVARS
      ARGS0
      const int hg=((idx>>2)-hot0)&7, hi=hot0*4+(idx&3);
      const unsigned long long hm= hg==0? 0ull : hg==HGR? 0xfffull : 0x3full;
      const unsigned hto=(unsigned)(w*393216+(l<6? l*2048+idx*12288 : (l-6)*2048+hi*12288));
      pw=wqkv+(idx*6144+(xcd&1)*1024+(xcd>>1)*256+w*64);
      asm volatile(LD_ASM2
        : "=&{s84}"(sd), "+s"(w8), "+s"(bid), "+s"(a0)
        : "{v0}"(xoA), "{v1}"(xoB), "{v2}"(xo), "{v3}"(xo1), "{v4}"(xo2), "{v5}"(wof[0]), "{v6}"(wof[1]), "{v7}"(wof[2]),
          "{v16}"(hto), [sh]"s"(srd(wqkv+((xcd&1)*8+hot0)*128,6291456u-1920u)), [hm]"s"(hm),
          "{s[40:43]}"(sx), "{s[44:47]}"(sw), "{s38}"(xl), "{s39}"(wl), [pw]"s"(pw), [tsk]"n"(SINK+256)
        : "memory", "scc", "vcc", LD_CLOB);
    }
    ROLE
    LANEVARS
    ARGS0
    const unsigned wd=OCOL*128;
    {
      const unsigned wof2=(unsigned)(w*16384+(l>>3)*2048+((((l&7)>>1)^((l>>4)&3))*32)+(l&1)*16);
      const unsigned to=ORD((unsigned)(l>>1)&15u);
      const unsigned tab=to*131072u+(l&1)*65536;
      asm volatile(LD_ASM3
        : "+{s84}"(sd)
        : "{v0}"(xoA), "{v1}"(xoB), "{v2}"(xo), "{v3}"(xo1), "{v4}"(xo2), "{v5}"(wof[0]), "{v6}"(wof[1]), "{v7}"(wof[2]), "{v8}"(wof2), "{v9}"(tab),
          "{s[40:43]}"(sx), "{s[44:47]}"(sw), [so]"s"(srd(wo+wd,2097152u-wd)), "{s38}"(xl), "{s39}"(wl), [ob]"s"(sb0+w*1024), [pw]"s"(pw),
          [pq]"s"(wo+((xcd&1)*1024+((3-(int)(a0[4]>>7))&7)*128+(idx*32+w*8)*2048))
        : "memory", "scc", "vcc", LD_CLOB);
    }
    const unsigned tag=rd1(syn+64)+1u;
    lds_add(sb0+SYN+12);
    TS(2);
    if(w>=4-NPOL){
      const unsigned* fp=flags+F_O+(rb*16+hp)*16; const unsigned fo=ORD((unsigned)l&15u)*4u;
      const unsigned madr=sb0+(l==0? SYN+20 : SINK+l*4);
      SPIN(rd1(syn+12)<4u || rd1(syn+16)<4u,1);
      if(w==3) PUBM(madr,1u);
      if(PD0) __builtin_amdgcn_s_sleep(PD0);
      if(w==2) __builtin_amdgcn_s_sleep(PD1); if(w==1) __builtin_amdgcn_s_sleep(2*PD1); if(w==0) __builtin_amdgcn_s_sleep(3*PD1);
      unsigned rdy=1; int spins=0;
      while(rdy!=0xFFFFu){
        unsigned v;
        asm volatile("s_mov_b64 exec, 0xffff\n\tglobal_atomic_or %0, %1, %3, %2 sc0 sc1\n\ts_mov_b64 exec, -1\n\ts_waitcnt vmcnt(0)" : "=&v"(v) : "v"(fo), "s"(fp), "v"(0u) : "memory");
        unsigned r=((unsigned)__ballot(v==tag)&0xFFFFu)|1u;
        if(++spins>(1<<21)) r=0xFFFFu;
        if(r!=rdy){ PUBM(madr,(unsigned)__builtin_ctz(~r)); rdy=r; if(w<3 && (r&0x3FFFu)==0x3FFFu) break; }
        else __builtin_amdgcn_s_sleep(OSLP);
      }
      TS(3);
      if(bid==0 && w==3){
        int sp=0;
        while(true){
          u32x4 v; asm volatile("global_load_dwordx4 %0, %1, %2 sc1\n\ts_waitcnt vmcnt(0)" : "=&v"(v) : "v"(l*16), "s"(flags+F_Y) : "memory");
          if(__all(v[0]==tag && v[1]==tag && v[2]==tag && v[3]==tag) || ++sp>(1<<20)) break;
          __builtin_amdgcn_s_sleep(12);
        }
        if(l==0) stflag(flags, tag);
      }
    } else { TS(3); }
    TOUT(4);
    __builtin_amdgcn_endpgm();
  }

  if(wv==0){
    ENTER("FK_G")
    ROLE
    __builtin_amdgcn_s_setprio(GPR);
    const int h=w&1, r=w>>1;
    LANEVARS
    TSS(3);
    ARGW
    ARGS0
    if(PGT&1){ if(w==1 && l<7) touch(ws, l<6? l*2097152+PGO : (unsigned)WS_FL+2048, sb0+SINK+512); }
    if(PGT&2){ if(w==2 && l<4) touch(ptr64(a1[0],a1[1]), l*2097152+PGO, sb0+SINK+512); }
    if(PGT&4){ if(w==3 && l<1) touch(wo, PGO, sb0+SINK+512); if(w==0 && l<2) touch(wqkv, l*2097152+2097152, sb0+SINK+512); }
    TSS(4);
    {
      unsigned lx0=sb0+X_OFF+fr*128+((g^(fr&7))*16), lx1=sb0+X_OFF+fr*128+(((4+g)^(fr&7))*16);
      const int sw=wswz(8*g+tq);
      unsigned lw0=sb0+g*3072+tq*128+(unsigned)((((2*h)^sw)*32)+8*tp), lw1=sb0+g*3072+tq*128+(unsigned)((((2*h+1)^sw)*32)+8*tp);
      char* ot=ws+WS_O+(rb*16+hp)*16384+r*8192+h*2048;
      unsigned it; unsigned long long rm; asm("s_cmp_lg_u32 %1, 0\n\ts_cselect_b64 %0, -1, 0" : "=s"(rm) : "s"(r) : "scc");
      asm volatile(N2_ASM
        : [it]"=&s"(it), "+{v56}"(lx0), "+{v57}"(lx1), "+{v58}"(lw0), "+{v59}"(lw1)
        : "{v62}"(l*16), "{v63}"(sb0+EX_OFF+h*3072+l*16), [r]"s"(r), [ot]"s"(ot), [ou]"s"(ot+4096), [fa]"s"(sb0+SYN+32+w*4), [fp]"s"(sb0+SYN+32+(w^2)*4),
          [vo]"s"(2048-1024*r), [rm]"s"(rm), [z]"s"(rb|r)
        : "memory", "scc", "vcc", N2_CLOB);
    }
    TS(3);
    unsigned tb;
    {
      LANEVARS
      const unsigned tag=rd1(syn+64)+1u;
      const unsigned ra=sb0+(4*g+tq)*128+8*tp; const int sw=(2*g+(tq>>1))&3;
      unsigned b0=ra+((0^sw)*32), b1=ra+((1^sw)*32), b2=ra+((2^sw)*32), b3=ra+((3^sw)*32), vo=(unsigned)(w*4096+l*16), sy=sb0+SYN+20;
      unsigned sr=sb0+EX_OFF+w*2048+(l>>3)*128+(((l&7)^(l>>3))*16), sg=sb0+EX_OFF+w*2048+fr*128+(((g>>1)^(fr&7))*16)+(g&1)*8;
      tb=ORD((unsigned)l&15u)*16384u;
      asm volatile("" : "+{v160}"(b0), "+{v161}"(b1), "+{v162}"(b2), "+{v163}"(b3), "+{v164}"(vo), "+{v165}"(sy), "+{v166}"(sr), "+{v167}"(sg), "+{v96}"(tb));
      publish(sb0+SYN+16, flags+F_O+rb*256+hp, l*64, tag);
      TS(4);
      __builtin_amdgcn_s_setprio(G2P);
      unsigned iss=0, q0, q1, q2, cn=0;
      asm volatile(G3_ASM
        : [iss]"+&s"(iss), [t0]"=&s"(q0), [t1]"=&s"(q1), [t2]"=&s"(q2), [cn]"+&s"(cn),
          "+{v160}"(b0), "+{v161}"(b1), "+{v162}"(b2), "+{v163}"(b3), "+{v165}"(sy), "+{v96}"(tb)
        : "{v164}"(vo), "{v166}"(sr), "{v167}"(sg), [go]"s"(60+w*4), [sy]"n"(SYN+20), [ob]"s"(srd(ws+WS_O+rb*262144,262144u)), [op]"s"(ptr64(a1[0],a1[1])+((s0+w*32)*4096+2048+OCOL*128))
        : "memory", "scc", "vcc", G3_CLOB);
    }
    {
      ENDW TS(6); TOUT(7);
    }
    __builtin_amdgcn_endpgm();
  }

  {
    ENTER("FK_C")
    ROLE
    char* cr=smem+C_OFF+2048+w*4096;
    const unsigned crs=sb0+C_OFF+2048+w*4096;
    const int sblk=bid>>3, td0=(bid&7)*128+(w&1)*64;
    const int vb=(LOCA&1)? xcd*32+(idx&16)+(idx&1)*8+((idx>>1)&7) : bid, fb=(LOCA&2)? (idx&15)*16+xcd*2+(idx>>4) : bid;
    const int d=(vb>>4)*64+(((vb>>3)&1)*4+w)*8+(vb&7);
    int kv0=KV0+((sblk>>KSH)&KVS);
    static_assert(KV0+KVS+PP_NB<=LD_NBAR && KV0>=EB, "barrier budget");
    unsigned tagv, ctr; f32x8 RB;
    const int fd0=(fb&15)*64, fs0=(fb>>4)*128+w*32;
    u32x4 qv[4];
    f32x16 A0,A1,A2,A3;
    const int rw=sblk*64+(w>>1)*32, j=rw>>5;
    unsigned k0,k1,k2,ko,ta,se,so,ha,l16,rb0[2];
    {
      LANEVARS
      if(w==0 && l<25 && l!=16) ((unsigned*)(smem+SYN))[l]=0u;
      if(l<32) *(u32x4*)(smem+C_OFF+w*512+l*16)=u32x4{0,0,0,0};
      asm volatile("" ::: "memory");
      __builtin_amdgcn_s_setprio(CPR);
      const int dg=l&7, rg=l>>3;
      ko=(unsigned)(rg*8192+dg*16);
      const bool top=(rw==0 && rg==0);
      k0=top? ko+6144:ko; k1=top? ko+6144:ko+2048; k2=top? ko+6144:ko+4096;
      ta=sb0+EX_OFF+w*1024+(w&1)*512+dg*64; se=(l&8)? 0x03020706u : 0x05040100u; ha=crs+(32*g-4*tq+4*tp)*2; l16=l*16;
      so=(unsigned)(rg*32768+((j>>2)&3)*128+((dg&4)|((dg+j)&3))*16);
      #pragma unroll
      for(int k=0;k<2;k++){ const int q=fr-1-2*g+k; rb0[k]=sb0+C_OFF+(4+(q>>1))*256+((q>>3)&3)*64+((w+(q>>1))&3)*16+(q&1)*8; }
      asm volatile("" : "+{v152}"(k0), "+{v153}"(k1), "+{v154}"(k2), "+{v155}"(ko), "+{v156}"(ta), "+{v157}"(se), "+{v159}"(so), "+{v163}"(l16), "+{v165}"(ha), "+{v166}"(rb0[0]), "+{v167}"(rb0[1]));
    }
    TSS(3);
    ARGW
    if(EB==1) asm volatile("s_barrier" : "+s"(a1), "+s"(a2), "+s"(a3), "+s"(a0) :: "memory");
    if(EB==2) asm volatile("s_barrier\n\ts_barrier" : "+s"(a1), "+s"(a2), "+s"(a3), "+s"(a0) :: "memory");
    ARGS0
    char* out=ptr64(a1[0],a1[1]); const char* qp=ptr64(a2[2],a2[3])+(fs0*DM+fd0)*2;
    {
      const char* kb=ptr64(a1[2],a1[3])+(rw*DM+td0)*2; const char* vb=ptr64(a1[4],a1[5])+(rw*DM+td0)*2;
      unsigned long long zm; asm("s_cmp_eq_u32 %1, 0\n\ts_cselect_b64 %0, 0xff, 0" : "=s"(zm) : "s"(rw) : "scc");
      unsigned t0;
      if(PGT&8){ const char* tp= w==0? kb : w==1? vb : w==2? qp : ptr64(a2[0],a2[1])+d*4096;
        asm volatile("s_mov_b32 m0, %1\n\ts_mov_b64 exec, 1\n\tglobal_load_lds_dword v163, %0\n\ts_mov_b64 exec, -1" :: "s"(tp), "s"(sb0+SINK+512), "{v163}"(l16) : "memory"); }
      TSS(5);
      asm volatile(PP_ASM
        : [t0]"=&s"(t0), "=&{v164}"(ctr), "+{v159}"(so), "=&{v160}"(tagv), "=&{v[24:31]}"(RB), "=&{v[44:47]}"(qv[0]), "=&{v[48:51]}"(qv[1]), "=&{v[52:55]}"(qv[2]), "=&{v[56:59]}"(qv[3]),
          "=&{v[64:79]}"(A0), "=&{v[80:95]}"(A1), "=&{v[96:111]}"(A2), "=&{v[112:127]}"(A3)
        : "{v152}"(k0), "{v153}"(k1), "{v154}"(k2), "{v155}"(ko), "{v156}"(ta), "{v157}"(se), "{v166}"(rb0[0]), "{v167}"(rb0[1]), "{v165}"(ha), "{v163}"(l16),
          [qb0]"s"(qp), [qb2]"s"(qp+4096), [mk]"s"(0x180u), [em]"s"(1ull|(512ull<<w)), [hd]"s"(crs),
          [kb0]"s"(kb-6144), [kb3]"s"(kb), [kb5]"s"(kb+4096), [vb0]"s"(vb), [vb2]"s"(vb+4096), [sb]"s"(ws+WS_KV+td0*4096+j*512),
          [n0]"s"(kv0-EB-(HH==2)), [ff]"s"(flags+F_PP+(td0>>6)*64+j), [zm]"s"(zm), [shd]"s"(sb0+EX_OFF+w*1024), [tgd]"s"(sb0+SYN+64), [shp]"s"(ptr64(a1[6],a1[7])+(bid&7)*1024), [fl]"s"(flags),
          [hp]"s"(ptr64(a2[0],a2[1])+d*4096), [dp]"s"(ptr64(a3[0],a3[1])+d*2),
          [ph]"s"(wqkv+(((xcd&1)*8+hot0)*128+idx*6144+(w>>1)*196608+(w&1)*64))
        : "memory", "scc", PP_CLOB);
    }
    LANEVARS
    const unsigned tag=__builtin_amdgcn_readfirstlane(tagv)+1u;
    unsigned rem=LD_NBAR-PP_NB-kv0;
    barc(rem);
    const unsigned* pf=flags+F_PP+(d>>6)*64;
    const char* kvp=ws+WS_KV+((vb>>4)*8+(vb&7))*32768+((vb>>3)&1)*64+w*8192;
    const unsigned kvo=(unsigned)((l>>2)*128+(l&3)*16);
    int bidx=((l>=16)? l-16 : (l==0? 63 : l+47))*4;
    asm volatile("" : "+{v32}"(bidx));
    __builtin_amdgcn_s_setprio(3);
    while(rem>PREM) barc(rem);
    wait_flag_b<8>(pf, l*4, tag, rem CTRP);
    #pragma unroll
    for(int i=0;i<4;i++){ if(i%KTH==0 && i) barc(rem); gldsc(kvp+i*2048, kvo, crs+i*1024); }
    barc(rem); if(PB2) barc(rem);
    WAITV(0);
    asm volatile("s_mov_b32 m0, %2\n\ts_mov_b64 exec, 1\n\tglobal_load_lds_dword %0, %1 sc1\n\ts_mov_b64 exec, -1" :: "v"(0u), "s"(pf), "s"(sb0+SYN+100+w*4) : "memory");
    f32x16 ac0, ac1; unsigned vtr, cn=0; (void)vtr;
    unsigned ca=sb0+CAR+w*1280+(fr*4+g)*4, cz= l? ca : sb0+C_OFF;
    asm volatile("s_nop 1\n\t" CV_ASM
      : "=&{v[128:143]}"(ac0), "=&{v[144:159]}"(ac1), [rem]"+&s"(rem), [cn]"+&s"(cn), "+{v40}"(ca), "+{v41}"(cz)
        , "+{v[44:47]}"(qv[0]), "+{v[48:51]}"(qv[1]), "+{v[52:55]}"(qv[2]), "+{v[56:59]}"(qv[3])
      : "{v[64:79]}"(A0), "{v[80:95]}"(A1), "{v[96:111]}"(A2), "{v[112:127]}"(A3),
        "{v[24:31]}"(RB), "{v32}"(bidx), "{v33}"(sb0+SYN+80+w*4), "{v61}"(sb0+SYN+100+(l&3)*4), [tg]"s"(tag),
        "{v42}"((256*(g&1)+16*fr+4*(g&2))*2), [yb]"s"(ws+WS_Y+d*4096), "{v43}"((l>>3)*8192+(l&7)*16), [q0]"s"(qp), [q2]"s"(qp+4096)
      : "memory", "scc", CV_CLOB);
    TS(4);
    __builtin_amdgcn_s_setprio(3);
    publish(sb0+SYN+24, flags+F_Y+vb, l*1024, tag);
    asm volatile("" : "+v"(qv[0]), "+v"(qv[1]), "+v"(qv[2]), "+v"(qv[3]));
    TS(5);
    float qf[32];
    #pragma unroll
    for(int i=0;i<16;i++){ qf[2*i]=bflo(qv[i>>2][i&3]); qf[2*i+1]=bfhi(qv[i>>2][i&3]); }
    unsigned ya=crs+(g*8+tq)*64+tp*8, wa0=crs+fr*128+((g^(fr&7))*16), wa1=crs+fr*128+(((4+g)^(fr&7))*16), ra[4], oo=(unsigned)((l>>3)*16384+(l&7)*16), yo2=(l>>2)*4096+(l&3)*16;
    #pragma unroll
    for(int i=0;i<4;i++){ const int row=4*(l>>3)+i; ra[i]=crs+row*128+(((l&7)^(row&7))*16); }
    char* op=out+fs0*4096+fd0*2; char* op2=op+8192; const char* yg=ws+WS_Y+fd0*4096+fs0*2;
    #pragma unroll
    for(int i=0;i<32;i++) asm volatile("" : "+v"(qf[i]));
    asm volatile("" : "+v"(ya), "+v"(wa0), "+v"(wa1), "+v"(ra[0]), "+v"(ra[1]), "+v"(ra[2]), "+v"(ra[3]), "+v"(oo), "+v"(yo2));
    asm volatile("" : "+s"(op), "+s"(op2), "+s"(yg));
    if(w==0){
      SPIN(rd1(syn+24)<4u,1);
      { int sp=0;
        while(true){
          unsigned v=tag; asm volatile("s_mov_b64 exec, 0xffff\n\tglobal_atomic_or %0, %1, %3, %2 sc0 sc1\n\ts_mov_b64 exec, -1\n\ts_waitcnt vmcnt(0)" : "+&v"(v) : "v"(l*4), "s"(flags+F_Y+fb*16), "v"(0u) : "memory");
          if(__all(v==tag) || ++sp>(1<<21)) break;
          __builtin_amdgcn_s_sleep(YSLP);
        } }
      PUB(sb0+(l==0? SYN+28 : SINK+l*4), 1u);
    } else SPIN(rd1(syn+28)==0u,YS2);
    #pragma unroll
    for(int i=0;i<4;i++) gldsc(yg+i*65536, yo2, crs+i*1024);
    {
      u32x4 y4[4];
      asm volatile("s_waitcnt vmcnt(0)\n\t"
        "ds_read_b64_tr_b16 v[0:1], %4\n\tds_read_b64_tr_b16 v[2:3], %4 offset:256\n\tds_read_b64_tr_b16 v[4:5], %4 offset:32\n\tds_read_b64_tr_b16 v[6:7], %4 offset:288\n\t"
        "ds_read_b64_tr_b16 v[8:9], %4 offset:2048\n\tds_read_b64_tr_b16 v[10:11], %4 offset:2304\n\tds_read_b64_tr_b16 v[12:13], %4 offset:2080\n\tds_read_b64_tr_b16 v[14:15], %4 offset:2336\n\t"
        "s_waitcnt lgkmcnt(6)\n\tds_write_b128 %5, v[0:3]\n\ts_waitcnt lgkmcnt(5)\n\tds_write_b128 %5, v[4:7] offset:2048\n\t"
        "s_waitcnt lgkmcnt(4)\n\tds_write_b128 %6, v[8:11]\n\ts_waitcnt lgkmcnt(3)\n\tds_write_b128 %6, v[12:15] offset:2048\n\t"
        "ds_read_b128 %0, %7\n\tds_read_b128 %1, %8\n\tds_read_b128 %2, %9\n\tds_read_b128 %3, %10\n\ts_waitcnt lgkmcnt(0)"
        : "=&v"(y4[0]), "=&v"(y4[1]), "=&v"(y4[2]), "=&v"(y4[3])
        : "v"(ya), "v"(wa0), "v"(wa1), "v"(ra[0]), "v"(ra[1]), "v"(ra[2]), "v"(ra[3])
        : "memory", "v0", "v1", "v2", "v3", "v4", "v5", "v6", "v7", "v8", "v9", "v10", "v11", "v12", "v13", "v14", "v15");
      #pragma unroll
      for(int i=0;i<4;i++){
        u32x4 ov;
        #pragma unroll
        for(int e=0;e<4;e++) ov[e]=pack2(mul1(qf[8*i+2*e],bflo(y4[i][e])), mul1(qf[8*i+2*e+1],bfhi(y4[i][e])));
        stout((i&2? op2:op)+(i&1)*4096, oo, ov);
      }
    }
    ENDW TS(6); TOUT(7);
    __builtin_amdgcn_endpgm();
  }
}

extern "C" void kernel_launch(void* const* d_in, const int* in_sizes, int n_in, void* d_out, int out_size, void* d_ws, size_t ws_size, hipStream_t stream){
  const u16* q=(const u16*)d_in[0]; const u16* k=(const u16*)d_in[1]; const u16* v=(const u16*)d_in[2];
  const u16* shk=(const u16*)d_in[3]; const u16* hk=(const u16*)d_in[4]; const u16* sD=(const u16*)d_in[5];
  const u16* x=(const u16*)d_in[6]; const u16* wqkv=(const u16*)d_in[7]; const u16* wo=(const u16*)d_in[8];
  fused_kernel<<<256,768,0,stream>>>(x,wqkv,wo,(char*)d_ws,(u16*)d_out,k,v,shk,hk,q,sD);
}
```

```cpp
#include <hip/hip_runtime.h>
#include <hip/hip_bf16.h>
#include <stdint.h>

typedef unsigned short u16;
typedef short bf16x8 __attribute__((ext_vector_type(8)));
typedef short s16x4 __attribute__((ext_vector_type(4)));
typedef float f32x2 __attribute__((ext_vector_type(2)));
typedef float f32x4 __attribute__((ext_vector_type(4)));
typedef float f32x8 __attribute__((ext_vector_type(8)));
typedef float f32x16 __attribute__((ext_vector_type(16)));
typedef unsigned u32x4 __attribute__((ext_vector_type(4)));
typedef unsigned u32x2 __attribute__((ext_vector_type(2)));
typedef __bf16 bf16x2_t __attribute__((ext_vector_type(2)));
#define LDS_AS __attribute__((address_space(3)))

__device__ __forceinline__ float bflo(unsigned x){ return __uint_as_float(x<<16); }
__device__ __forceinline__ float bfhi(unsigned x){ return __uint_as_float(x&0xffff0000u); }
__device__ __forceinline__ unsigned pack2(float lo, float hi){ return __builtin_bit_cast(unsigned, __builtin_convertvector(f32x2{lo,hi}, bf16x2_t)); }
__device__ __forceinline__ float mul1(float a, float b){ float r; asm("v_mul_f32 %0, %1, %2" : "=v"(r) : "v"(a), "v"(b)); return r; }
__device__ __forceinline__ float fma1(float a, float b, float c){ float r; asm("v_fma_f32 %0, %1, %2, %3" : "=v"(r) : "v"(a), "v"(b), "v"(c)); return r; }

__device__ __forceinline__ void glds(const void* sbase, unsigned voff, unsigned ldsaddr){
  asm volatile("s_mov_b32 m0, %2\n\ts_nop 0\n\tglobal_load_lds_dwordx4 %0, %1" :: "v"(voff), "s"(sbase), "s"(ldsaddr) : "memory");
}
__device__ __forceinline__ void gldsc(const void* sbase, unsigned voff, unsigned ldsaddr){
  asm volatile("s_mov_b32 m0, %2\n\ts_nop 0\n\tglobal_load_lds_dwordx4 %0, %1 sc1" :: "v"(voff), "s"(sbase), "s"(ldsaddr) : "memory");
}
__device__ __forceinline__ void touch(const void* sbase, unsigned voff, unsigned ldsaddr){
  asm volatile("s_mov_b32 m0, %2\n\ts_nop 0\n\tglobal_load_lds_dword %0, %1" :: "v"(voff), "s"(sbase), "s"(ldsaddr) : "memory");
}
__device__ __forceinline__ void st16c(void* p, u32x4 v){ asm volatile("global_store_dwordx4 %0, %1, off sc1\n\ts_nop 1" :: "v"(p), "v"(v) : "memory"); }
__device__ __forceinline__ void st16cs(void* sbase, unsigned voff, u32x4 v){ asm volatile("global_store_dwordx4 %0, %1, %2 sc1\n\ts_nop 1" :: "v"(voff), "v"(v), "s"(sbase) : "memory"); }
#define GLB_AS __attribute__((address_space(1)))
__device__ __forceinline__ unsigned ldflag(const unsigned* p){ return __hip_atomic_load((const GLB_AS unsigned*)p, __ATOMIC_RELAXED, __HIP_MEMORY_SCOPE_AGENT); }
__device__ __forceinline__ void stflag(unsigned* p, unsigned v){ __hip_atomic_store((GLB_AS unsigned*)p, v, __ATOMIC_RELAXED, __HIP_MEMORY_SCOPE_AGENT); }
template<int SLP> __device__ __forceinline__ void wait_flag(const unsigned* p, unsigned tag){
  int spins=0;
  while(true){ unsigned v=ldflag(p); if(__all(v==tag)) break; __builtin_amdgcn_s_sleep(SLP); if(++spins>(1<<21)) break; }
}
__device__ __forceinline__ unsigned lds_addr(const void* p){ return (unsigned)(size_t)(const LDS_AS void*)p; }
#define WAITV(n) asm volatile("s_waitcnt vmcnt(" #n ")":::"memory")
#define WAITL() asm volatile("s_waitcnt lgkmcnt(0)":::"memory")
#define SBAR() asm volatile("s_barrier":::"memory")
#define PUBM(addr,val) asm volatile("ds_max_u32 %0, %1" :: "v"(addr), "v"(val) : "memory")
#define PUB(addr,val) asm volatile("ds_write_b32 %0, %1" :: "v"(addr), "v"(val) : "memory")
__device__ __forceinline__ s16x4 trread(const char* p){ return __builtin_amdgcn_ds_read_tr16_b64_v4i16((LDS_AS s16x4*)p); }
__device__ __forceinline__ bf16x8 cat44(s16x4 a, s16x4 b){ return __builtin_shufflevector(a,b,0,1,2,3,4,5,6,7); }
__device__ __forceinline__ int wswz(int row){ return ((row>>1)&1) | (((row>>3)&1)<<1); }
__device__ __forceinline__ u32x4 widen(u32x2 a, u32x2 b){
  auto r0=__builtin_amdgcn_permlane16_swap(a[0],b[0],false,false);
  auto r1=__builtin_amdgcn_permlane16_swap(a[1],b[1],false,false);
  return u32x4{r0[0],r1[0],r0[1],r1[1]};
}

constexpr int S=2048, DM=1024;
constexpr int WS=12288, XS=18432, X_OFF=6*WS;
constexpr int CREG=4608, C_OFF=131072;
constexpr int EX_OFF=C_OFF+4*CREG;
constexpr int SYN=EX_OFF+8192;
constexpr int SINK=SYN+256;
constexpr int CAR=SINK+768;
constexpr int ALL_LDS=CAR+4*1280;
constexpr int F_PP=1024, F_O=4096, F_Y=8192;
#define KV0 1
#define KVS 1
#define LPR 2
#define CPR 2
#define GPR 1
#define CGATE 2
#define OSLP 2
#define YSLP 2
#define TPX 16
#define TPW 2
#define TWO 0
#define CVP 1
#define G2P 3
#define OUTC 1
#define ROT 0
#define WTL 2
#define PB2 0
#define PREM 5
#define KTH 2
#define YDEF 0
#define OPOL 1
#define CODEL 260
#define MIX 0
#define LOCA 3
#define XLOC 1
#define PGT 0
#define PGO 0
#define QREM 0
#define EB 0
#define TW0 1
#define ORDM 1
#define NF 7u
#define NPOL 1
#define PD0 0
#define PD1 4
#define HH 0
#define HW 3
#define HKV 4
#define YPOL 1
#define YS2 2
#define OSH 4
#define HSK 3
#define HGR 2
#define KSH 0
#define EPI 0
#define YFL 0
#define CODEX 0
#define YPM 1
#define YPD 4
#define KPOL 0
#define HLAST 1

#define N2_ASM \
  "v_mov_b64 v[64:65], 0\n\t" \
  "v_mov_b64 v[66:67], 0\n\t" \
  "v_mov_b64 v[68:69], 0\n\t" \
  "v_mov_b64 v[70:71], 0\n\t" \
  "v_mov_b64 v[72:73], 0\n\t" \
  "v_mov_b64 v[74:75], 0\n\t" \
  "v_mov_b64 v[76:77], 0\n\t" \
  "v_mov_b64 v[78:79], 0\n\t" \
  "v_mov_b64 v[80:81], 0\n\t" \
  "v_mov_b64 v[82:83], 0\n\t" \
  "v_mov_b64 v[84:85], 0\n\t" \
  "v_mov_b64 v[86:87], 0\n\t" \
  "v_mov_b64 v[88:89], 0\n\t" \
  "v_mov_b64 v[90:91], 0\n\t" \
  "v_mov_b64 v[92:93], 0\n\t" \
  "v_mov_b64 v[94:95], 0\n\t" \
  "v_mov_b64 v[96:97], 0\n\t" \
  "v_mov_b64 v[98:99], 0\n\t" \
  "v_mov_b64 v[100:101], 0\n\t" \
  "v_mov_b64 v[102:103], 0\n\t" \
  "v_mov_b64 v[104:105], 0\n\t" \
  "v_mov_b64 v[106:107], 0\n\t" \
  "v_mov_b64 v[108:109], 0\n\t" \
  "v_mov_b64 v[110:111], 0\n\t" \
  "v_mov_b64 v[112:113], 0\n\t" \
  "v_mov_b64 v[114:115], 0\n\t" \
  "v_mov_b64 v[116:117], 0\n\t" \
  "v_mov_b64 v[118:119], 0\n\t" \
  "v_mov_b64 v[120:121], 0\n\t" \
  "v_mov_b64 v[122:123], 0\n\t" \
  "v_mov_b64 v[124:125], 0\n\t" \
  "v_mov_b64 v[126:127], 0\n\t" \
  "v_mov_b64 v[128:129], 0\n\t" \
  "v_mov_b64 v[130:131], 0\n\t" \
  "v_mov_b64 v[132:133], 0\n\t" \
  "v_mov_b64 v[134:135], 0\n\t" \
  "v_mov_b64 v[136:137], 0\n\t" \
  "v_mov_b64 v[138:139], 0\n\t" \
  "v_mov_b64 v[140:141], 0\n\t" \
  "v_mov_b64 v[142:143], 0\n\t" \
  "v_mov_b64 v[144:145], 0\n\t" \
  "v_mov_b64 v[146:147], 0\n\t" \
  "v_mov_b64 v[148:149], 0\n\t" \
  "v_mov_b64 v[150:151], 0\n\t" \
  "v_mov_b64 v[152:153], 0\n\t" \
  "v_mov_b64 v[154:155], 0\n\t" \
  "v_mov_b64 v[156:157], 0\n\t" \
  "v_mov_b64 v[158:159], 0\n\t" \
  "v_mov_b64 v[160:161], 0\n\t" \
  "v_mov_b64 v[162:163], 0\n\t" \
  "v_mov_b64 v[164:165], 0\n\t" \
  "v_mov_b64 v[166:167], 0\n\t" \
  "v_add_u32 v60, 36864, v58\n\t" \
  "v_add_u32 v61, 36864, v59\n\t" \
  "s_mov_b32 %[it], 0\n\t" \
  "s_cmp_lg_u32 %[r], 0\n\t" \
  "s_barrier\n\t" \
  "s_cbranch_scc1 NB_%=\n\t" \
  "ds_read_b64_tr_b16 v[48:49], v60 offset:14336\n\t" \
  "ds_read_b64_tr_b16 v[50:51], v60 offset:14848\n\t" \
  "ds_read_b128 v[0:3], v56 offset:38912\n\t" \
  "ds_read_b64_tr_b16 v[52:53], v60 offset:26624\n\t" \
  "ds_read_b64_tr_b16 v[54:55], v60 offset:27136\n\t" \
  "ds_read_b128 v[16:19], v57 offset:38912\n\t" \
  "ds_read_b128 v[4:7], v56 offset:40960\n\t" \
  "ds_read_b128 v[20:23], v57 offset:40960\n\t" \
  "ds_read_b128 v[8:11], v56 offset:43008\n\t" \
  "ds_read_b128 v[24:27], v57 offset:43008\n\t" \
  "ds_read_b128 v[12:15], v56 offset:45056\n\t" \
  "ds_read_b128 v[28:31], v57 offset:45056\n\t" \
  "ds_read_b64_tr_b16 v[32:33], v60 offset:13312\n\t" \
  "ds_read_b64_tr_b16 v[34:35], v60 offset:13824\n\t" \
  "s_mov_b32 s68, 0x50000\n\t" \
  "s_mov_b32 s69, 0x5550055\n\t" \
  "s_mov_b32 s70, 0xa0000\n\t" \
  "s_mov_b32 s71, 0xaaa00aa\n\t" \
  "s_mov_b32 s72, 0x150001\n\t" \
  "s_mov_b32 s73, 0x15550155\n\t" \
  "s_mov_b32 s74, 0x2a0002\n\t" \
  "s_mov_b32 s75, 0x2aaa02aa\n\t" \
  "s_mov_b32 s76, 0x55505555\n\t" \
  "s_mov_b32 s77, 0x50005500\n\t" \
  "s_mov_b32 s78, 0xaaa0aaaa\n\t" \
  "s_mov_b32 s79, 0xa000aa00\n\t" \
  "s_mov_b32 s80, 0x55405554\n\t" \
  "s_mov_b32 s81, 0x40005400\n\t" \
  "s_mov_b32 s82, 0xaa80aaa8\n\t" \
  "s_mov_b32 s83, 0x8000a800\n\t" \
  "s_mov_b32 s84, 0xaaaaaaaa\n\t" \
  "s_mov_b32 s85, 0xaaaaaaaa\n\t" \
  "s_waitcnt lgkmcnt(0)\n\t" \
  "s_branch N2a_%=\n\t" \
  "N0a_%=:\n\t" \
  "s_waitcnt lgkmcnt(10)\n\t" \
  "v_mfma_f32_16x16x32_bf16 v[128:131], v[0:3], v[32:35], v[128:131]\n\t" \
  "ds_read_b64_tr_b16 v[44:45], v58 offset:13312\n\t" \
  "s_waitcnt lgkmcnt(7)\n\t" \
  "v_mfma_f32_16x16x32_bf16 v[128:131], v[16:19], v[36:39], v[128:131]\n\t" \
  "ds_read_b64_tr_b16 v[46:47], v58 offset:13824\n\t" \
  "v_mfma_f32_16x16x32_bf16 v[136:139], v[4:7], v[32:35], v[136:139]\n\t" \
  "ds_read_b128 v[48:51], v56 offset:0\n\t" \
  "v_mfma_f32_16x16x32_bf16 v[136:139], v[20:23], v[36:39], v[136:139]\n\t" \
  "ds_read_b128 v[52:55], v57 offset:0\n\t" \
  "s_waitcnt lgkmcnt(8)\n\t" \
  "v_mfma_f32_16x16x32_bf16 v[144:147], v[8:11], v[32:35], v[144:147]\n\t" \
  "v_mfma_f32_16x16x32_bf16 v[144:147], v[24:27], v[36:39], v[144:147]\n\t" \
  "s_waitcnt lgkmcnt(4)\n\t" \
  "v_mfma_f32_16x16x32_bf16 v[152:155], v[12:15], v[32:35], v[152:155]\n\t" \
  "v_mfma_f32_16x16x32_bf16 v[152:155], v[28:31], v[36:39], v[152:155]\n\t" \
  "ds_read_b64_tr_b16 v[32:33], v59 offset:1024\n\t" \
  "v_mfma_f32_16x16x32_bf16 v[96:99], v[40:43], v[0:3], v[96:99]\n\t" \
  "ds_read_b64_tr_b16 v[34:35], v59 offset:1536\n\t" \
  "s_waitcnt lgkmcnt(4)\n\t" \
  "v_mfma_f32_16x16x32_bf16 v[96:99], v[44:47], v[16:19], v[96:99]\n\t" \
  "ds_read_b64_tr_b16 v[36:37], v59 offset:13312\n\t" \
  "v_mfma_f32_16x16x32_bf16 v[104:107], v[40:43], v[4:7], v[104:107]\n\t" \
  "v_mfma_f32_16x16x32_bf16 v[104:107], v[44:47], v[20:23], v[104:107]\n\t" \
  "ds_read_b64_tr_b16 v[38:39], v59 offset:13824\n\t" \
  "v_mfma_f32_16x16x32_bf16 v[112:115], v[40:43], v[8:11], v[112:115]\n\t" \
  "v_mfma_f32_16x16x32_bf16 v[112:115], v[44:47], v[24:27], v[112:115]\n\t" \
  "v_mfma_f32_16x16x32_bf16 v[120:123], v[40:43], v[12:15], v[120:123]\n\t" \
  "v_mfma_f32_16x16x32_bf16 v[120:123], v[44:47], v[28:31], v[120:123]\n\t" \
  "s_waitcnt lgkmcnt(4)\n\t" \
  "v_mfma_f32_16x16x32_bf16 v[160:163], v[40:43], v[48:51], v[160:163]\n\t" \
  "v_mfma_f32_16x16x32_bf16 v[160:163], v[44:47], v[52:55], v[160:163]\n\t" \
  "ds_read_b64_tr_b16 v[40:41], v59 offset:2048\n\t" \
  "s_waitcnt lgkmcnt(1)\n\t" \
  "v_mfma_f32_16x16x32_bf16 v[164:167], v[32:35], v[48:51], v[164:167]\n\t" \
  "ds_read_b64_tr_b16 v[42:43], v59 offset:2560\n\t" \
  "v_mfma_f32_16x16x32_bf16 v[164:167], v[36:39], v[52:55], v[164:167]\n\t" \
  "ds_read_b64_tr_b16 v[44:45], v59 offset:14336\n\t" \
  "v_mfma_f32_16x16x32_bf16 v[100:103], v[32:35], v[0:3], v[100:103]\n\t" \
  "ds_read_b64_tr_b16 v[46:47], v59 offset:14848\n\t" \
  "v_mfma_f32_16x16x32_bf16 v[100:103], v[36:39], v[16:19], v[100:103]\n\t" \
  "ds_read_b64_tr_b16 v[48:49], v58 offset:0\n\t" \
  "v_mfma_f32_16x16x32_bf16 v[108:111], v[32:35], v[4:7], v[108:111]\n\t" \
  "ds_read_b64_tr_b16 v[50:51], v58 offset:512\n\t" \
  "v_mfma_f32_16x16x32_bf16 v[108:111], v[36:39], v[20:23], v[108:111]\n\t" \
  "ds_read_b64_tr_b16 v[52:53], v58 offset:12288\n\t" \
  "v_mfma_f32_16x16x32_bf16 v[116:119], v[32:35], v[8:11], v[116:119]\n\t" \
  "ds_read_b64_tr_b16 v[54:55], v58 offset:12800\n\t" \
  "v_mfma_f32_16x16x32_bf16 v[116:119], v[36:39], v[24:27], v[116:119]\n\t" \
  "v_mfma_f32_16x16x32_bf16 v[124:127], v[32:35], v[12:15], v[124:127]\n\t" \
  "ds_read_b64_tr_b16 v[32:33], v59 offset:0\n\t" \
  "v_mfma_f32_16x16x32_bf16 v[124:127], v[36:39], v[28:31], v[124:127]\n\t" \
  "ds_read_b64_tr_b16 v[34:35], v59 offset:512\n\t" \
  "s_waitcnt lgkmcnt(6)\n\t" \
  "v_mfma_f32_16x16x32_bf16 v[132:135], v[0:3], v[40:43], v[132:135]\n\t" \
  "ds_read_b64_tr_b16 v[36:37], v59 offset:12288\n\t" \
  "v_mfma_f32_16x16x32_bf16 v[132:135], v[16:19], v[44:47], v[132:135]\n\t" \
  "ds_read_b64_tr_b16 v[38:39], v59 offset:12800\n\t" \
  "v_mfma_f32_16x16x32_bf16 v[140:143], v[4:7], v[40:43], v[140:143]\n\t" \
  "v_mfma_f32_16x16x32_bf16 v[140:143], v[20:23], v[44:47], v[140:143]\n\t" \
  "v_mfma_f32_16x16x32_bf16 v[148:151], v[8:11], v[40:43], v[148:151]\n\t" \
  "v_mfma_f32_16x16x32_bf16 v[148:151], v[24:27], v[44:47], v[148:151]\n\t" \
  "v_mfma_f32_16x16x32_bf16 v[156:159], v[12:15], v[40:43], v[156:159]\n\t" \
  "v_mfma_f32_16x16x32_bf16 v[156:159], v[28:31], v[44:47], v[156:159]\n\t" \
  "s_waitcnt lgkmcnt(0)\n\t" \
  "s_barrier\n\t" \
  "v_mfma_f32_16x16x32_bf16 v[64:67], v[48:51], v[0:3], v[64:67]\n\t" \
  "v_mfma_f32_16x16x32_bf16 v[64:67], v[52:55], v[16:19], v[64:67]\n\t" \
  "ds_read_b64_tr_b16 v[40:41], v58 offset:26624\n\t" \
  "v_mfma_f32_16x16x32_bf16 v[68:71], v[32:35], v[0:3], v[68:71]\n\t" \
  "ds_read_b128 v[0:3], v56 offset:20480\n\t" \
  "v_mfma_f32_16x16x32_bf16 v[68:71], v[36:39], v[16:19], v[68:71]\n\t" \
  "ds_read_b128 v[16:19], v57 offset:20480\n\t" \
  "v_mfma_f32_16x16x32_bf16 v[72:75], v[48:51], v[4:7], v[72:75]\n\t" \
  "ds_read_b64_tr_b16 v[42:43], v58 offset:27136\n\t" \
  "v_mfma_f32_16x16x32_bf16 v[72:75], v[52:55], v[20:23], v[72:75]\n\t" \
  "ds_read_b64_tr_b16 v[44:45], v60 offset:2048\n\t" \
  "v_mfma_f32_16x16x32_bf16 v[76:79], v[32:35], v[4:7], v[76:79]\n\t" \
  "ds_read_b128 v[4:7], v56 offset:22528\n\t" \
  "v_mfma_f32_16x16x32_bf16 v[76:79], v[36:39], v[20:23], v[76:79]\n\t" \
  "ds_read_b128 v[20:23], v57 offset:22528\n\t" \
  "v_mfma_f32_16x16x32_bf16 v[80:83], v[48:51], v[8:11], v[80:83]\n\t" \
  "ds_read_b64_tr_b16 v[46:47], v60 offset:2560\n\t" \
  "v_mfma_f32_16x16x32_bf16 v[80:83], v[52:55], v[24:27], v[80:83]\n\t" \
  "v_mfma_f32_16x16x32_bf16 v[84:87], v[32:35], v[8:11], v[84:87]\n\t" \
  "ds_read_b128 v[8:11], v56 offset:24576\n\t" \
  "v_mfma_f32_16x16x32_bf16 v[84:87], v[36:39], v[24:27], v[84:87]\n\t" \
  "ds_read_b128 v[24:27], v57 offset:24576\n\t" \
  "v_mfma_f32_16x16x32_bf16 v[88:91], v[48:51], v[12:15], v[88:91]\n\t" \
  "ds_read_b64_tr_b16 v[48:49], v58 offset:25600\n\t" \
  "v_mfma_f32_16x16x32_bf16 v[88:91], v[52:55], v[28:31], v[88:91]\n\t" \
  "ds_read_b64_tr_b16 v[50:51], v58 offset:26112\n\t" \
  "v_mfma_f32_16x16x32_bf16 v[92:95], v[32:35], v[12:15], v[92:95]\n\t" \
  "ds_read_b128 v[12:15], v56 offset:26624\n\t" \
  "v_mfma_f32_16x16x32_bf16 v[92:95], v[36:39], v[28:31], v[92:95]\n\t" \
  "ds_read_b128 v[28:31], v57 offset:26624\n\t" \
  "N1a_%=:\n\t" \
  "s_waitcnt lgkmcnt(10)\n\t" \
  "v_mfma_f32_16x16x32_bf16 v[128:131], v[0:3], v[40:43], v[128:131]\n\t" \
  "ds_read_b64_tr_b16 v[52:53], v60 offset:1024\n\t" \
  "s_waitcnt lgkmcnt(7)\n\t" \
  "v_mfma_f32_16x16x32_bf16 v[128:131], v[16:19], v[44:47], v[128:131]\n\t" \
  "ds_read_b64_tr_b16 v[54:55], v60 offset:1536\n\t" \
  "v_mfma_f32_16x16x32_bf16 v[136:139], v[4:7], v[40:43], v[136:139]\n\t" \
  "ds_read_b128 v[32:35], v56 offset:18432\n\t" \
  "v_mfma_f32_16x16x32_bf16 v[136:139], v[20:23], v[44:47], v[136:139]\n\t" \
  "ds_read_b128 v[36:39], v57 offset:18432\n\t" \
  "s_waitcnt lgkmcnt(8)\n\t" \
  "v_mfma_f32_16x16x32_bf16 v[144:147], v[8:11], v[40:43], v[144:147]\n\t" \
  "v_mfma_f32_16x16x32_bf16 v[144:147], v[24:27], v[44:47], v[144:147]\n\t" \
  "s_waitcnt lgkmcnt(4)\n\t" \
  "v_mfma_f32_16x16x32_bf16 v[152:155], v[12:15], v[40:43], v[152:155]\n\t" \
  "v_mfma_f32_16x16x32_bf16 v[152:155], v[28:31], v[44:47], v[152:155]\n\t" \
  "ds_read_b64_tr_b16 v[40:41], v59 offset:25600\n\t" \
  "v_mfma_f32_16x16x32_bf16 v[96:99], v[48:51], v[0:3], v[96:99]\n\t" \
  "ds_read_b64_tr_b16 v[42:43], v59 offset:26112\n\t" \
  "s_waitcnt lgkmcnt(4)\n\t" \
  "v_mfma_f32_16x16x32_bf16 v[96:99], v[52:55], v[16:19], v[96:99]\n\t" \
  "ds_read_b64_tr_b16 v[44:45], v61 offset:1024\n\t" \
  "v_mfma_f32_16x16x32_bf16 v[104:107], v[48:51], v[4:7], v[104:107]\n\t" \
  "v_mfma_f32_16x16x32_bf16 v[104:107], v[52:55], v[20:23], v[104:107]\n\t" \
  "ds_read_b64_tr_b16 v[46:47], v61 offset:1536\n\t" \
  "v_mfma_f32_16x16x32_bf16 v[112:115], v[48:51], v[8:11], v[112:115]\n\t" \
  "v_mfma_f32_16x16x32_bf16 v[112:115], v[52:55], v[24:27], v[112:115]\n\t" \
  "v_mfma_f32_16x16x32_bf16 v[120:123], v[48:51], v[12:15], v[120:123]\n\t" \
  "v_mfma_f32_16x16x32_bf16 v[120:123], v[52:55], v[28:31], v[120:123]\n\t" \
  "s_waitcnt lgkmcnt(4)\n\t" \
  "v_mfma_f32_16x16x32_bf16 v[160:163], v[48:51], v[32:35], v[160:163]\n\t" \
  "v_mfma_f32_16x16x32_bf16 v[160:163], v[52:55], v[36:39], v[160:163]\n\t" \
  "ds_read_b64_tr_b16 v[48:49], v59 offset:26624\n\t" \
  "s_waitcnt lgkmcnt(1)\n\t" \
  "v_mfma_f32_16x16x32_bf16 v[164:167], v[40:43], v[32:35], v[164:167]\n\t" \
  "ds_read_b64_tr_b16 v[50:51], v59 offset:27136\n\t" \
  "v_mfma_f32_16x16x32_bf16 v[164:167], v[44:47], v[36:39], v[164:167]\n\t" \
  "ds_read_b64_tr_b16 v[52:53], v61 offset:2048\n\t" \
  "v_mfma_f32_16x16x32_bf16 v[100:103], v[40:43], v[0:3], v[100:103]\n\t" \
  "ds_read_b64_tr_b16 v[54:55], v61 offset:2560\n\t" \
  "v_mfma_f32_16x16x32_bf16 v[100:103], v[44:47], v[16:19], v[100:103]\n\t" \
  "ds_read_b64_tr_b16 v[32:33], v58 offset:24576\n\t" \
  "v_mfma_f32_16x16x32_bf16 v[108:111], v[40:43], v[4:7], v[108:111]\n\t" \
  "ds_read_b64_tr_b16 v[34:35], v58 offset:25088\n\t" \
  "v_mfma_f32_16x16x32_bf16 v[108:111], v[44:47], v[20:23], v[108:111]\n\t" \
  "ds_read_b64_tr_b16 v[36:37], v60 offset:0\n\t" \
  "v_mfma_f32_16x16x32_bf16 v[116:119], v[40:43], v[8:11], v[116:119]\n\t" \
  "ds_read_b64_tr_b16 v[38:39], v60 offset:512\n\t" \
  "v_mfma_f32_16x16x32_bf16 v[116:119], v[44:47], v[24:27], v[116:119]\n\t" \
  "v_mfma_f32_16x16x32_bf16 v[124:127], v[40:43], v[12:15], v[124:127]\n\t" \
  "ds_read_b64_tr_b16 v[40:41], v59 offset:24576\n\t" \
  "v_mfma_f32_16x16x32_bf16 v[124:127], v[44:47], v[28:31], v[124:127]\n\t" \
  "ds_read_b64_tr_b16 v[42:43], v59 offset:25088\n\t" \
  "s_waitcnt lgkmcnt(6)\n\t" \
  "v_mfma_f32_16x16x32_bf16 v[132:135], v[0:3], v[48:51], v[132:135]\n\t" \
  "ds_read_b64_tr_b16 v[44:45], v61 offset:0\n\t" \
  "v_mfma_f32_16x16x32_bf16 v[132:135], v[16:19], v[52:55], v[132:135]\n\t" \
  "ds_read_b64_tr_b16 v[46:47], v61 offset:512\n\t" \
  "v_mfma_f32_16x16x32_bf16 v[140:143], v[4:7], v[48:51], v[140:143]\n\t" \
  "v_mfma_f32_16x16x32_bf16 v[140:143], v[20:23], v[52:55], v[140:143]\n\t" \
  "v_mfma_f32_16x16x32_bf16 v[148:151], v[8:11], v[48:51], v[148:151]\n\t" \
  "v_mfma_f32_16x16x32_bf16 v[148:151], v[24:27], v[52:55], v[148:151]\n\t" \
  "v_mfma_f32_16x16x32_bf16 v[156:159], v[12:15], v[48:51], v[156:159]\n\t" \
  "v_mfma_f32_16x16x32_bf16 v[156:159], v[28:31], v[52:55], v[156:159]\n\t" \
  "s_waitcnt lgkmcnt(0)\n\t" \
  "s_barrier\n\t" \
  "v_mfma_f32_16x16x32_bf16 v[64:67], v[32:35], v[0:3], v[64:67]\n\t" \
  "v_mfma_f32_16x16x32_bf16 v[64:67], v[36:39], v[16:19], v[64:67]\n\t" \
  "ds_read_b64_tr_b16 v[48:49], v60 offset:14336\n\t" \
  "v_mfma_f32_16x16x32_bf16 v[68:71], v[40:43], v[0:3], v[68:71]\n\t" \
  "ds_read_b128 v[0:3], v56 offset:38912\n\t" \
  "v_mfma_f32_16x16x32_bf16 v[68:71], v[44:47], v[16:19], v[68:71]\n\t" \
  "ds_read_b128 v[16:19], v57 offset:38912\n\t" \
  "v_mfma_f32_16x16x32_bf16 v[72:75], v[32:35], v[4:7], v[72:75]\n\t" \
  "ds_read_b64_tr_b16 v[50:51], v60 offset:14848\n\t" \
  "v_mfma_f32_16x16x32_bf16 v[72:75], v[36:39], v[20:23], v[72:75]\n\t" \
  "ds_read_b64_tr_b16 v[52:53], v60 offset:26624\n\t" \
  "v_mfma_f32_16x16x32_bf16 v[76:79], v[40:43], v[4:7], v[76:79]\n\t" \
  "ds_read_b128 v[4:7], v56 offset:40960\n\t" \
  "v_mfma_f32_16x16x32_bf16 v[76:79], v[44:47], v[20:23], v[76:79]\n\t" \
  "ds_read_b128 v[20:23], v57 offset:40960\n\t" \
  "v_mfma_f32_16x16x32_bf16 v[80:83], v[32:35], v[8:11], v[80:83]\n\t" \
  "ds_read_b64_tr_b16 v[54:55], v60 offset:27136\n\t" \
  "v_mfma_f32_16x16x32_bf16 v[80:83], v[36:39], v[24:27], v[80:83]\n\t" \
  "v_mfma_f32_16x16x32_bf16 v[84:87], v[40:43], v[8:11], v[84:87]\n\t" \
  "ds_read_b128 v[8:11], v56 offset:43008\n\t" \
  "v_mfma_f32_16x16x32_bf16 v[84:87], v[44:47], v[24:27], v[84:87]\n\t" \
  "ds_read_b128 v[24:27], v57 offset:43008\n\t" \
  "v_mfma_f32_16x16x32_bf16 v[88:91], v[32:35], v[12:15], v[88:91]\n\t" \
  "ds_read_b64_tr_b16 v[32:33], v60 offset:13312\n\t" \
  "v_mfma_f32_16x16x32_bf16 v[88:91], v[36:39], v[28:31], v[88:91]\n\t" \
  "ds_read_b64_tr_b16 v[34:35], v60 offset:13824\n\t" \
  "v_mfma_f32_16x16x32_bf16 v[92:95], v[40:43], v[12:15], v[92:95]\n\t" \
  "ds_read_b128 v[12:15], v56 offset:45056\n\t" \
  "v_mfma_f32_16x16x32_bf16 v[92:95], v[44:47], v[28:31], v[92:95]\n\t" \
  "ds_read_b128 v[28:31], v57 offset:45056\n\t" \
  "N2a_%=:\n\t" \
  "s_waitcnt lgkmcnt(10)\n\t" \
  "v_mfma_f32_16x16x32_bf16 v[128:131], v[0:3], v[48:51], v[128:131]\n\t" \
  "ds_read_b64_tr_b16 v[36:37], v60 offset:25600\n\t" \
  "s_waitcnt lgkmcnt(7)\n\t" \
  "v_mfma_f32_16x16x32_bf16 v[128:131], v[16:19], v[52:55], v[128:131]\n\t" \
  "ds_read_b64_tr_b16 v[38:39], v60 offset:26112\n\t" \
  "v_mfma_f32_16x16x32_bf16 v[136:139], v[4:7], v[48:51], v[136:139]\n\t" \
  "ds_read_b128 v[40:43], v56 offset:36864\n\t" \
  "v_mfma_f32_16x16x32_bf16 v[136:139], v[20:23], v[52:55], v[136:139]\n\t" \
  "ds_read_b128 v[44:47], v57 offset:36864\n\t" \
  "s_waitcnt lgkmcnt(8)\n\t" \
  "v_mfma_f32_16x16x32_bf16 v[144:147], v[8:11], v[48:51], v[144:147]\n\t" \
  "v_mfma_f32_16x16x32_bf16 v[144:147], v[24:27], v[52:55], v[144:147]\n\t" \
  "s_waitcnt lgkmcnt(4)\n\t" \
  "v_mfma_f32_16x16x32_bf16 v[152:155], v[12:15], v[48:51], v[152:155]\n\t" \
  "v_mfma_f32_16x16x32_bf16 v[152:155], v[28:31], v[52:55], v[152:155]\n\t" \
  "ds_read_b64_tr_b16 v[48:49], v61 offset:13312\n\t" \
  "v_mfma_f32_16x16x32_bf16 v[96:99], v[32:35], v[0:3], v[96:99]\n\t" \
  "ds_read_b64_tr_b16 v[50:51], v61 offset:13824\n\t" \
  "s_waitcnt lgkmcnt(4)\n\t" \
  "v_mfma_f32_16x16x32_bf16 v[96:99], v[36:39], v[16:19], v[96:99]\n\t" \
  "ds_read_b64_tr_b16 v[52:53], v61 offset:25600\n\t" \
  "v_mfma_f32_16x16x32_bf16 v[104:107], v[32:35], v[4:7], v[104:107]\n\t" \
  "v_mfma_f32_16x16x32_bf16 v[104:107], v[36:39], v[20:23], v[104:107]\n\t" \
  "ds_read_b64_tr_b16 v[54:55], v61 offset:26112\n\t" \
  "v_mfma_f32_16x16x32_bf16 v[112:115], v[32:35], v[8:11], v[112:115]\n\t" \
  "v_mfma_f32_16x16x32_bf16 v[112:115], v[36:39], v[24:27], v[112:115]\n\t" \
  "v_mfma_f32_16x16x32_bf16 v[120:123], v[32:35], v[12:15], v[120:123]\n\t" \
  "v_mfma_f32_16x16x32_bf16 v[120:123], v[36:39], v[28:31], v[120:123]\n\t" \
  "s_waitcnt lgkmcnt(4)\n\t" \
  "v_mfma_f32_16x16x32_bf16 v[160:163], v[32:35], v[40:43], v[160:163]\n\t" \
  "v_mfma_f32_16x16x32_bf16 v[160:163], v[36:39], v[44:47], v[160:163]\n\t" \
  "ds_read_b64_tr_b16 v[32:33], v61 offset:14336\n\t" \
  "s_waitcnt lgkmcnt(1)\n\t" \
  "v_mfma_f32_16x16x32_bf16 v[164:167], v[48:51], v[40:43], v[164:167]\n\t" \
  "ds_read_b64_tr_b16 v[34:35], v61 offset:14848\n\t" \
  "v_mfma_f32_16x16x32_bf16 v[164:167], v[52:55], v[44:47], v[164:167]\n\t" \
  "ds_read_b64_tr_b16 v[36:37], v61 offset:26624\n\t" \
  "v_mfma_f32_16x16x32_bf16 v[100:103], v[48:51], v[0:3], v[100:103]\n\t" \
  "ds_read_b64_tr_b16 v[38:39], v61 offset:27136\n\t" \
  "v_mfma_f32_16x16x32_bf16 v[100:103], v[52:55], v[16:19], v[100:103]\n\t" \
  "ds_read_b64_tr_b16 v[40:41], v60 offset:12288\n\t" \
  "v_mfma_f32_16x16x32_bf16 v[108:111], v[48:51], v[4:7], v[108:111]\n\t" \
  "ds_read_b64_tr_b16 v[42:43], v60 offset:12800\n\t" \
  "v_mfma_f32_16x16x32_bf16 v[108:111], v[52:55], v[20:23], v[108:111]\n\t" \
  "ds_read_b64_tr_b16 v[44:45], v60 offset:24576\n\t" \
  "v_mfma_f32_16x16x32_bf16 v[116:119], v[48:51], v[8:11], v[116:119]\n\t" \
  "ds_read_b64_tr_b16 v[46:47], v60 offset:25088\n\t" \
  "v_mfma_f32_16x16x32_bf16 v[116:119], v[52:55], v[24:27], v[116:119]\n\t" \
  "v_mfma_f32_16x16x32_bf16 v[124:127], v[48:51], v[12:15], v[124:127]\n\t" \
  "ds_read_b64_tr_b16 v[48:49], v61 offset:12288\n\t" \
  "v_mfma_f32_16x16x32_bf16 v[124:127], v[52:55], v[28:31], v[124:127]\n\t" \
  "ds_read_b64_tr_b16 v[50:51], v61 offset:12800\n\t" \
  "s_waitcnt lgkmcnt(6)\n\t" \
  "v_mfma_f32_16x16x32_bf16 v[132:135], v[0:3], v[32:35], v[132:135]\n\t" \
  "ds_read_b64_tr_b16 v[52:53], v61 offset:24576\n\t" \
  "v_mfma_f32_16x16x32_bf16 v[132:135], v[16:19], v[36:39], v[132:135]\n\t" \
  "ds_read_b64_tr_b16 v[54:55], v61 offset:25088\n\t" \
  "v_mfma_f32_16x16x32_bf16 v[140:143], v[4:7], v[32:35], v[140:143]\n\t" \
  "v_mfma_f32_16x16x32_bf16 v[140:143], v[20:23], v[36:39], v[140:143]\n\t" \
  "s_addk_i32 %[it], 1\n\t" \
  "v_mfma_f32_16x16x32_bf16 v[148:151], v[8:11], v[32:35], v[148:151]\n\t" \
  "s_cmp_lt_u32 %[it], 6\n\t" \
  "v_mfma_f32_16x16x32_bf16 v[148:151], v[24:27], v[36:39], v[148:151]\n\t" \
  "v_mfma_f32_16x16x32_bf16 v[156:159], v[12:15], v[32:35], v[156:159]\n\t" \
  "v_mfma_f32_16x16x32_bf16 v[156:159], v[28:31], v[36:39], v[156:159]\n\t" \
  "s_waitcnt lgkmcnt(0)\n\t" \
  "s_barrier\n\t" \
  "s_cbranch_scc0 NTa_%=\n\t" \
  "v_mfma_f32_16x16x32_bf16 v[64:67], v[40:43], v[0:3], v[64:67]\n\t" \
  "v_mfma_f32_16x16x32_bf16 v[64:67], v[44:47], v[16:19], v[64:67]\n\t" \
  "ds_read_b64_tr_b16 v[32:33], v58 offset:2048\n\t" \
  "v_mfma_f32_16x16x32_bf16 v[68:71], v[48:51], v[0:3], v[68:71]\n\t" \
  "ds_read_b128 v[0:3], v56 offset:2048\n\t" \
  "v_mfma_f32_16x16x32_bf16 v[68:71], v[52:55], v[16:19], v[68:71]\n\t" \
  "ds_read_b128 v[16:19], v57 offset:2048\n\t" \
  "v_mfma_f32_16x16x32_bf16 v[72:75], v[40:43], v[4:7], v[72:75]\n\t" \
  "ds_read_b64_tr_b16 v[34:35], v58 offset:2560\n\t" \
  "v_mfma_f32_16x16x32_bf16 v[72:75], v[44:47], v[20:23], v[72:75]\n\t" \
  "ds_read_b64_tr_b16 v[36:37], v58 offset:14336\n\t" \
  "v_mfma_f32_16x16x32_bf16 v[76:79], v[48:51], v[4:7], v[76:79]\n\t" \
  "ds_read_b128 v[4:7], v56 offset:4096\n\t" \
  "v_mfma_f32_16x16x32_bf16 v[76:79], v[52:55], v[20:23], v[76:79]\n\t" \
  "ds_read_b128 v[20:23], v57 offset:4096\n\t" \
  "v_mfma_f32_16x16x32_bf16 v[80:83], v[40:43], v[8:11], v[80:83]\n\t" \
  "ds_read_b64_tr_b16 v[38:39], v58 offset:14848\n\t" \
  "v_mfma_f32_16x16x32_bf16 v[80:83], v[44:47], v[24:27], v[80:83]\n\t" \
  "v_mfma_f32_16x16x32_bf16 v[84:87], v[48:51], v[8:11], v[84:87]\n\t" \
  "ds_read_b128 v[8:11], v56 offset:6144\n\t" \
  "v_mfma_f32_16x16x32_bf16 v[84:87], v[52:55], v[24:27], v[84:87]\n\t" \
  "ds_read_b128 v[24:27], v57 offset:6144\n\t" \
  "v_mfma_f32_16x16x32_bf16 v[88:91], v[40:43], v[12:15], v[88:91]\n\t" \
  "ds_read_b64_tr_b16 v[40:41], v58 offset:1024\n\t" \
  "v_mfma_f32_16x16x32_bf16 v[88:91], v[44:47], v[28:31], v[88:91]\n\t" \
  "ds_read_b64_tr_b16 v[42:43], v58 offset:1536\n\t" \
  "v_mfma_f32_16x16x32_bf16 v[92:95], v[48:51], v[12:15], v[92:95]\n\t" \
  "ds_read_b128 v[12:15], v56 offset:8192\n\t" \
  "v_mfma_f32_16x16x32_bf16 v[92:95], v[52:55], v[28:31], v[92:95]\n\t" \
  "ds_read_b128 v[28:31], v57 offset:8192\n\t" \
  "s_branch N0a_%=\n\t" \
  "NTa_%=:\n\t" \
  "s_setprio 3\n\t" \
  "v_mfma_f32_16x16x32_bf16 v[88:91], v[40:43], v[12:15], v[88:91]\n\t" \
  "v_cvt_pk_bf16_f32 v120, v120, v121\n\t" \
  "v_cvt_pk_bf16_f32 v121, v122, v123\n\t" \
  "v_mfma_f32_16x16x32_bf16 v[88:91], v[44:47], v[28:31], v[88:91]\n\t" \
  "v_cvt_pk_bf16_f32 v122, v124, v125\n\t" \
  "v_cvt_pk_bf16_f32 v123, v126, v127\n\t" \
  "v_mfma_f32_16x16x32_bf16 v[92:95], v[48:51], v[12:15], v[92:95]\n\t" \
  "v_mov_b32 v57, %[fa]\n\t" \
  "v_mov_b32 v58, 1\n\t" \
  "v_mfma_f32_16x16x32_bf16 v[92:95], v[52:55], v[28:31], v[92:95]\n\t" \
  "ds_write_b128 v63, v[120:123]\n\t" \
  "v_cvt_pk_bf16_f32 v38, v156, v157\n\t" \
  "v_mfma_f32_16x16x32_bf16 v[80:83], v[40:43], v[8:11], v[80:83]\n\t" \
  "v_cvt_pk_bf16_f32 v39, v158, v159\n\t" \
  "v_cvt_pk_bf16_f32 v28, v152, v153\n\t" \
  "v_mfma_f32_16x16x32_bf16 v[80:83], v[44:47], v[24:27], v[80:83]\n\t" \
  "v_cvt_pk_bf16_f32 v29, v154, v155\n\t" \
  "v_mov_b32 v60, 0xff61b1e6\n\t" \
  "v_mfma_f32_16x16x32_bf16 v[84:87], v[48:51], v[8:11], v[84:87]\n\t" \
  "ds_write2_b64 v63, v[28:29], v[38:39] offset0:128 offset1:129\n\t" \
  "s_mov_b64 exec, 1\n\t" \
  "ds_write_b32 v57, v58\n\t" \
  "s_mov_b64 exec, -1\n\t" \
  "v_mfma_f32_16x16x32_bf16 v[84:87], v[52:55], v[24:27], v[84:87]\n\t" \
  "v_cvt_pk_bf16_f32 v160, v160, v161\n\t" \
  "v_cvt_pk_bf16_f32 v161, v162, v163\n\t" \
  "v_mfma_f32_16x16x32_bf16 v[72:75], v[40:43], v[4:7], v[72:75]\n\t" \
  "v_cvt_pk_bf16_f32 v162, v164, v165\n\t" \
  "v_cvt_pk_bf16_f32 v163, v166, v167\n\t" \
  "v_mfma_f32_16x16x32_bf16 v[72:75], v[44:47], v[20:23], v[72:75]\n\t" \
  "v_cvt_pk_bf16_f32 v112, v112, v113\n\t" \
  "v_cvt_pk_bf16_f32 v113, v114, v115\n\t" \
  "v_mfma_f32_16x16x32_bf16 v[76:79], v[48:51], v[4:7], v[76:79]\n\t" \
  "v_cvt_pk_bf16_f32 v114, v116, v117\n\t" \
  "v_cvt_pk_bf16_f32 v115, v118, v119\n\t" \
  "v_mfma_f32_16x16x32_bf16 v[76:79], v[52:55], v[20:23], v[76:79]\n\t" \
  "v_cndmask_b32_e64 v12, v60, 0, s[76:77]\n\t" \
  "v_cndmask_b32_e64 v13, v60, 0, s[78:79]\n\t" \
  "v_mfma_f32_16x16x32_bf16 v[64:67], v[40:43], v[0:3], v[64:67]\n\t" \
  "v_cndmask_b32_e64 v14, v60, 0, s[80:81]\n\t" \
  "v_cndmask_b32_e64 v15, v60, 0, s[82:83]\n\t" \
  "v_mfma_f32_16x16x32_bf16 v[64:67], v[44:47], v[16:19], v[64:67]\n\t" \
  "v_cndmask_b32_e64 v56, v60, 0, s[68:69]\n\t" \
  "v_cndmask_b32_e64 v57, v60, 0, s[70:71]\n\t" \
  "v_mfma_f32_16x16x32_bf16 v[68:71], v[48:51], v[0:3], v[68:71]\n\t" \
  "v_cndmask_b32_e64 v58, v60, 0, s[72:73]\n\t" \
  "v_cndmask_b32_e64 v59, v60, 0, s[74:75]\n\t" \
  "v_mfma_f32_16x16x32_bf16 v[68:71], v[52:55], v[16:19], v[68:71]\n\t" \
  "s_branch NX_%=\n\t" \
  "NB_%=:\n\t" \
  "ds_read_b64_tr_b16 v[48:49], v60 offset:13312\n\t" \
  "ds_read_b64_tr_b16 v[50:51], v60 offset:13824\n\t" \
  "ds_read_b128 v[0:3], v56 offset:47104\n\t" \
  "ds_read_b64_tr_b16 v[52:53], v60 offset:25600\n\t" \
  "ds_read_b64_tr_b16 v[54:55], v60 offset:26112\n\t" \
  "ds_read_b128 v[16:19], v57 offset:47104\n\t" \
  "ds_read_b128 v[4:7], v56 offset:49152\n\t" \
  "ds_read_b128 v[20:23], v57 offset:49152\n\t" \
  "ds_read_b128 v[8:11], v56 offset:51200\n\t" \
  "ds_read_b128 v[24:27], v57 offset:51200\n\t" \
  "ds_read_b128 v[12:15], v56 offset:53248\n\t" \
  "ds_read_b128 v[28:31], v57 offset:53248\n\t" \
  "ds_read_b64_tr_b16 v[32:33], v60 offset:14336\n\t" \
  "ds_read_b64_tr_b16 v[34:35], v60 offset:14848\n\t" \
  "s_mov_b32 s68, 0x50000\n\t" \
  "s_mov_b32 s69, 0x5550055\n\t" \
  "s_mov_b32 s70, 0xa0000\n\t" \
  "s_mov_b32 s71, 0xaaa00aa\n\t" \
  "s_mov_b32 s72, 0x150001\n\t" \
  "s_mov_b32 s73, 0x15550155\n\t" \
  "s_mov_b32 s74, 0x2a0002\n\t" \
  "s_mov_b32 s75, 0x2aaa02aa\n\t" \
  "s_mov_b32 s76, 0x55505555\n\t" \
  "s_mov_b32 s77, 0x50005500\n\t" \
  "s_mov_b32 s78, 0xaaa0aaaa\n\t" \
  "s_mov_b32 s79, 0xa000aa00\n\t" \
  "s_mov_b32 s80, 0x55405554\n\t" \
  "s_mov_b32 s81, 0x40005400\n\t" \
  "s_mov_b32 s82, 0xaa80aaa8\n\t" \
  "s_mov_b32 s83, 0x8000a800\n\t" \
  "s_mov_b32 s84, 0xaaaaaaaa\n\t" \
  "s_mov_b32 s85, 0xaaaaaaaa\n\t" \
  "s_waitcnt lgkmcnt(0)\n\t" \
  "s_branch N2b_%=\n\t" \
  "N0b_%=:\n\t" \
  "s_waitcnt lgkmcnt(10)\n\t" \
  "v_mfma_f32_16x16x32_bf16 v[96:99], v[32:35], v[0:3], v[96:99]\n\t" \
  "ds_read_b64_tr_b16 v[44:45], v58 offset:14336\n\t" \
  "s_waitcnt lgkmcnt(7)\n\t" \
  "v_mfma_f32_16x16x32_bf16 v[96:99], v[36:39], v[16:19], v[96:99]\n\t" \
  "ds_read_b64_tr_b16 v[46:47], v58 offset:14848\n\t" \
  "v_mfma_f32_16x16x32_bf16 v[104:107], v[32:35], v[4:7], v[104:107]\n\t" \
  "ds_read_b128 v[48:51], v56 offset:0\n\t" \
  "v_mfma_f32_16x16x32_bf16 v[104:107], v[36:39], v[20:23], v[104:107]\n\t" \
  "ds_read_b128 v[52:55], v57 offset:0\n\t" \
  "s_waitcnt lgkmcnt(8)\n\t" \
  "v_mfma_f32_16x16x32_bf16 v[112:115], v[32:35], v[8:11], v[112:115]\n\t" \
  "v_mfma_f32_16x16x32_bf16 v[112:115], v[36:39], v[24:27], v[112:115]\n\t" \
  "s_waitcnt lgkmcnt(4)\n\t" \
  "v_mfma_f32_16x16x32_bf16 v[120:123], v[32:35], v[12:15], v[120:123]\n\t" \
  "v_mfma_f32_16x16x32_bf16 v[120:123], v[36:39], v[28:31], v[120:123]\n\t" \
  "ds_read_b64_tr_b16 v[32:33], v59 offset:2048\n\t" \
  "v_mfma_f32_16x16x32_bf16 v[128:131], v[0:3], v[40:43], v[128:131]\n\t" \
  "ds_read_b64_tr_b16 v[34:35], v59 offset:2560\n\t" \
  "s_waitcnt lgkmcnt(4)\n\t" \
  "v_mfma_f32_16x16x32_bf16 v[128:131], v[16:19], v[44:47], v[128:131]\n\t" \
  "ds_read_b64_tr_b16 v[36:37], v59 offset:14336\n\t" \
  "v_mfma_f32_16x16x32_bf16 v[136:139], v[4:7], v[40:43], v[136:139]\n\t" \
  "v_mfma_f32_16x16x32_bf16 v[136:139], v[20:23], v[44:47], v[136:139]\n\t" \
  "ds_read_b64_tr_b16 v[38:39], v59 offset:14848\n\t" \
  "v_mfma_f32_16x16x32_bf16 v[144:147], v[8:11], v[40:43], v[144:147]\n\t" \
  "v_mfma_f32_16x16x32_bf16 v[144:147], v[24:27], v[44:47], v[144:147]\n\t" \
  "v_mfma_f32_16x16x32_bf16 v[152:155], v[12:15], v[40:43], v[152:155]\n\t" \
  "v_mfma_f32_16x16x32_bf16 v[152:155], v[28:31], v[44:47], v[152:155]\n\t" \
  "s_waitcnt lgkmcnt(4)\n\t" \
  "v_mfma_f32_16x16x32_bf16 v[160:163], v[48:51], v[40:43], v[160:163]\n\t" \
  "v_mfma_f32_16x16x32_bf16 v[160:163], v[52:55], v[44:47], v[160:163]\n\t" \
  "ds_read_b64_tr_b16 v[40:41], v59 offset:1024\n\t" \
  "s_waitcnt lgkmcnt(1)\n\t" \
  "v_mfma_f32_16x16x32_bf16 v[164:167], v[48:51], v[32:35], v[164:167]\n\t" \
  "ds_read_b64_tr_b16 v[42:43], v59 offset:1536\n\t" \
  "v_mfma_f32_16x16x32_bf16 v[164:167], v[52:55], v[36:39], v[164:167]\n\t" \
  "ds_read_b64_tr_b16 v[44:45], v59 offset:13312\n\t" \
  "v_mfma_f32_16x16x32_bf16 v[132:135], v[0:3], v[32:35], v[132:135]\n\t" \
  "ds_read_b64_tr_b16 v[46:47], v59 offset:13824\n\t" \
  "v_mfma_f32_16x16x32_bf16 v[132:135], v[16:19], v[36:39], v[132:135]\n\t" \
  "ds_read_b64_tr_b16 v[48:49], v58 offset:0\n\t" \
  "v_mfma_f32_16x16x32_bf16 v[140:143], v[4:7], v[32:35], v[140:143]\n\t" \
  "ds_read_b64_tr_b16 v[50:51], v58 offset:512\n\t" \
  "v_mfma_f32_16x16x32_bf16 v[140:143], v[20:23], v[36:39], v[140:143]\n\t" \
  "ds_read_b64_tr_b16 v[52:53], v58 offset:12288\n\t" \
  "v_mfma_f32_16x16x32_bf16 v[148:151], v[8:11], v[32:35], v[148:151]\n\t" \
  "ds_read_b64_tr_b16 v[54:55], v58 offset:12800\n\t" \
  "v_mfma_f32_16x16x32_bf16 v[148:151], v[24:27], v[36:39], v[148:151]\n\t" \
  "v_mfma_f32_16x16x32_bf16 v[156:159], v[12:15], v[32:35], v[156:159]\n\t" \
  "ds_read_b64_tr_b16 v[32:33], v59 offset:0\n\t" \
  "v_mfma_f32_16x16x32_bf16 v[156:159], v[28:31], v[36:39], v[156:159]\n\t" \
  "ds_read_b64_tr_b16 v[34:35], v59 offset:512\n\t" \
  "s_waitcnt lgkmcnt(6)\n\t" \
  "v_mfma_f32_16x16x32_bf16 v[100:103], v[40:43], v[0:3], v[100:103]\n\t" \
  "ds_read_b64_tr_b16 v[36:37], v59 offset:12288\n\t" \
  "v_mfma_f32_16x16x32_bf16 v[100:103], v[44:47], v[16:19], v[100:103]\n\t" \
  "ds_read_b64_tr_b16 v[38:39], v59 offset:12800\n\t" \
  "v_mfma_f32_16x16x32_bf16 v[108:111], v[40:43], v[4:7], v[108:111]\n\t" \
  "v_mfma_f32_16x16x32_bf16 v[108:111], v[44:47], v[20:23], v[108:111]\n\t" \
  "v_mfma_f32_16x16x32_bf16 v[116:119], v[40:43], v[8:11], v[116:119]\n\t" \
  "v_mfma_f32_16x16x32_bf16 v[116:119], v[44:47], v[24:27], v[116:119]\n\t" \
  "v_mfma_f32_16x16x32_bf16 v[124:127], v[40:43], v[12:15], v[124:127]\n\t" \
  "v_mfma_f32_16x16x32_bf16 v[124:127], v[44:47], v[28:31], v[124:127]\n\t" \
  "s_waitcnt lgkmcnt(0)\n\t" \
  "s_barrier\n\t" \
  "v_mfma_f32_16x16x32_bf16 v[64:67], v[48:51], v[0:3], v[64:67]\n\t" \
  "v_mfma_f32_16x16x32_bf16 v[64:67], v[52:55], v[16:19], v[64:67]\n\t" \
  "ds_read_b64_tr_b16 v[40:41], v58 offset:25600\n\t" \
  "v_mfma_f32_16x16x32_bf16 v[68:71], v[32:35], v[0:3], v[68:71]\n\t" \
  "ds_read_b128 v[0:3], v56 offset:28672\n\t" \
  "v_mfma_f32_16x16x32_bf16 v[68:71], v[36:39], v[16:19], v[68:71]\n\t" \
  "ds_read_b128 v[16:19], v57 offset:28672\n\t" \
  "v_mfma_f32_16x16x32_bf16 v[72:75], v[48:51], v[4:7], v[72:75]\n\t" \
  "ds_read_b64_tr_b16 v[42:43], v58 offset:26112\n\t" \
  "v_mfma_f32_16x16x32_bf16 v[72:75], v[52:55], v[20:23], v[72:75]\n\t" \
  "ds_read_b64_tr_b16 v[44:45], v60 offset:1024\n\t" \
  "v_mfma_f32_16x16x32_bf16 v[76:79], v[32:35], v[4:7], v[76:79]\n\t" \
  "ds_read_b128 v[4:7], v56 offset:30720\n\t" \
  "v_mfma_f32_16x16x32_bf16 v[76:79], v[36:39], v[20:23], v[76:79]\n\t" \
  "ds_read_b128 v[20:23], v57 offset:30720\n\t" \
  "v_mfma_f32_16x16x32_bf16 v[80:83], v[48:51], v[8:11], v[80:83]\n\t" \
  "ds_read_b64_tr_b16 v[46:47], v60 offset:1536\n\t" \
  "v_mfma_f32_16x16x32_bf16 v[80:83], v[52:55], v[24:27], v[80:83]\n\t" \
  "v_mfma_f32_16x16x32_bf16 v[84:87], v[32:35], v[8:11], v[84:87]\n\t" \
  "ds_read_b128 v[8:11], v56 offset:32768\n\t" \
  "v_mfma_f32_16x16x32_bf16 v[84:87], v[36:39], v[24:27], v[84:87]\n\t" \
  "ds_read_b128 v[24:27], v57 offset:32768\n\t" \
  "v_mfma_f32_16x16x32_bf16 v[88:91], v[48:51], v[12:15], v[88:91]\n\t" \
  "ds_read_b64_tr_b16 v[48:49], v58 offset:26624\n\t" \
  "v_mfma_f32_16x16x32_bf16 v[88:91], v[52:55], v[28:31], v[88:91]\n\t" \
  "ds_read_b64_tr_b16 v[50:51], v58 offset:27136\n\t" \
  "v_mfma_f32_16x16x32_bf16 v[92:95], v[32:35], v[12:15], v[92:95]\n\t" \
  "ds_read_b128 v[12:15], v56 offset:34816\n\t" \
  "v_mfma_f32_16x16x32_bf16 v[92:95], v[36:39], v[28:31], v[92:95]\n\t" \
  "ds_read_b128 v[28:31], v57 offset:34816\n\t" \
  "N1b_%=:\n\t" \
  "s_waitcnt lgkmcnt(10)\n\t" \
  "v_mfma_f32_16x16x32_bf16 v[96:99], v[40:43], v[0:3], v[96:99]\n\t" \
  "ds_read_b64_tr_b16 v[52:53], v60 offset:2048\n\t" \
  "s_waitcnt lgkmcnt(7)\n\t" \
  "v_mfma_f32_16x16x32_bf16 v[96:99], v[44:47], v[16:19], v[96:99]\n\t" \
  "ds_read_b64_tr_b16 v[54:55], v60 offset:2560\n\t" \
  "v_mfma_f32_16x16x32_bf16 v[104:107], v[40:43], v[4:7], v[104:107]\n\t" \
  "ds_read_b128 v[32:35], v56 offset:18432\n\t" \
  "v_mfma_f32_16x16x32_bf16 v[104:107], v[44:47], v[20:23], v[104:107]\n\t" \
  "ds_read_b128 v[36:39], v57 offset:18432\n\t" \
  "s_waitcnt lgkmcnt(8)\n\t" \
  "v_mfma_f32_16x16x32_bf16 v[112:115], v[40:43], v[8:11], v[112:115]\n\t" \
  "v_mfma_f32_16x16x32_bf16 v[112:115], v[44:47], v[24:27], v[112:115]\n\t" \
  "s_waitcnt lgkmcnt(4)\n\t" \
  "v_mfma_f32_16x16x32_bf16 v[120:123], v[40:43], v[12:15], v[120:123]\n\t" \
  "v_mfma_f32_16x16x32_bf16 v[120:123], v[44:47], v[28:31], v[120:123]\n\t" \
  "ds_read_b64_tr_b16 v[40:41], v59 offset:26624\n\t" \
  "v_mfma_f32_16x16x32_bf16 v[128:131], v[0:3], v[48:51], v[128:131]\n\t" \
  "ds_read_b64_tr_b16 v[42:43], v59 offset:27136\n\t" \
  "s_waitcnt lgkmcnt(4)\n\t" \
  "v_mfma_f32_16x16x32_bf16 v[128:131], v[16:19], v[52:55], v[128:131]\n\t" \
  "ds_read_b64_tr_b16 v[44:45], v61 offset:2048\n\t" \
  "v_mfma_f32_16x16x32_bf16 v[136:139], v[4:7], v[48:51], v[136:139]\n\t" \
  "v_mfma_f32_16x16x32_bf16 v[136:139], v[20:23], v[52:55], v[136:139]\n\t" \
  "ds_read_b64_tr_b16 v[46:47], v61 offset:2560\n\t" \
  "v_mfma_f32_16x16x32_bf16 v[144:147], v[8:11], v[48:51], v[144:147]\n\t" \
  "v_mfma_f32_16x16x32_bf16 v[144:147], v[24:27], v[52:55], v[144:147]\n\t" \
  "v_mfma_f32_16x16x32_bf16 v[152:155], v[12:15], v[48:51], v[152:155]\n\t" \
  "v_mfma_f32_16x16x32_bf16 v[152:155], v[28:31], v[52:55], v[152:155]\n\t" \
  "s_waitcnt lgkmcnt(4)\n\t" \
  "v_mfma_f32_16x16x32_bf16 v[160:163], v[32:35], v[48:51], v[160:163]\n\t" \
  "v_mfma_f32_16x16x32_bf16 v[160:163], v[36:39], v[52:55], v[160:163]\n\t" \
  "ds_read_b64_tr_b16 v[48:49], v59 offset:25600\n\t" \
  "s_waitcnt lgkmcnt(1)\n\t" \
  "v_mfma_f32_16x16x32_bf16 v[164:167], v[32:35], v[40:43], v[164:167]\n\t" \
  "ds_read_b64_tr_b16 v[50:51], v59 offset:26112\n\t" \
  "v_mfma_f32_16x16x32_bf16 v[164:167], v[36:39], v[44:47], v[164:167]\n\t" \
  "ds_read_b64_tr_b16 v[52:53], v61 offset:1024\n\t" \
  "v_mfma_f32_16x16x32_bf16 v[132:135], v[0:3], v[40:43], v[132:135]\n\t" \
  "ds_read_b64_tr_b16 v[54:55], v61 offset:1536\n\t" \
  "v_mfma_f32_16x16x32_bf16 v[132:135], v[16:19], v[44:47], v[132:135]\n\t" \
  "ds_read_b64_tr_b16 v[32:33], v58 offset:24576\n\t" \
  "v_mfma_f32_16x16x32_bf16 v[140:143], v[4:7], v[40:43], v[140:143]\n\t" \
  "ds_read_b64_tr_b16 v[34:35], v58 offset:25088\n\t" \
  "v_mfma_f32_16x16x32_bf16 v[140:143], v[20:23], v[44:47], v[140:143]\n\t" \
  "ds_read_b64_tr_b16 v[36:37], v60 offset:0\n\t" \
  "v_mfma_f32_16x16x32_bf16 v[148:151], v[8:11], v[40:43], v[148:151]\n\t" \
  "ds_read_b64_tr_b16 v[38:39], v60 offset:512\n\t" \
  "v_mfma_f32_16x16x32_bf16 v[148:151], v[24:27], v[44:47], v[148:151]\n\t" \
  "v_mfma_f32_16x16x32_bf16 v[156:159], v[12:15], v[40:43], v[156:159]\n\t" \
  "ds_read_b64_tr_b16 v[40:41], v59 offset:24576\n\t" \
  "v_mfma_f32_16x16x32_bf16 v[156:159], v[28:31], v[44:47], v[156:159]\n\t" \
  "ds_read_b64_tr_b16 v[42:43], v59 offset:25088\n\t" \
  "s_waitcnt lgkmcnt(6)\n\t" \
  "v_mfma_f32_16x16x32_bf16 v[100:103], v[48:51], v[0:3], v[100:103]\n\t" \
  "ds_read_b64_tr_b16 v[44:45], v61 offset:0\n\t" \
  "v_mfma_f32_16x16x32_bf16 v[100:103], v[52:55], v[16:19], v[100:103]\n\t" \
  "ds_read_b64_tr_b16 v[46:47], v61 offset:512\n\t" \
  "v_mfma_f32_16x16x32_bf16 v[108:111], v[48:51], v[4:7], v[108:111]\n\t" \
  "v_mfma_f32_16x16x32_bf16 v[108:111], v[52:55], v[20:23], v[108:111]\n\t" \
  "v_mfma_f32_16x16x32_bf16 v[116:119], v[48:51], v[8:11], v[116:119]\n\t" \
  "v_mfma_f32_16x16x32_bf16 v[116:119], v[52:55], v[24:27], v[116:119]\n\t" \
  "v_mfma_f32_16x16x32_bf16 v[124:127], v[48:51], v[12:15], v[124:127]\n\t" \
  "v_mfma_f32_16x16x32_bf16 v[124:127], v[52:55], v[28:31], v[124:127]\n\t" \
  "s_waitcnt lgkmcnt(0)\n\t" \
  "s_barrier\n\t" \
  "v_mfma_f32_16x16x32_bf16 v[64:67], v[32:35], v[0:3], v[64:67]\n\t" \
  "v_mfma_f32_16x16x32_bf16 v[64:67], v[36:39], v[16:19], v[64:67]\n\t" \
  "ds_read_b64_tr_b16 v[48:49], v60 offset:13312\n\t" \
  "v_mfma_f32_16x16x32_bf16 v[68:71], v[40:43], v[0:3], v[68:71]\n\t" \
  "ds_read_b128 v[0:3], v56 offset:47104\n\t" \
  "v_mfma_f32_16x16x32_bf16 v[68:71], v[44:47], v[16:19], v[68:71]\n\t" \
  "ds_read_b128 v[16:19], v57 offset:47104\n\t" \
  "v_mfma_f32_16x16x32_bf16 v[72:75], v[32:35], v[4:7], v[72:75]\n\t" \
  "ds_read_b64_tr_b16 v[50:51], v60 offset:13824\n\t" \
  "v_mfma_f32_16x16x32_bf16 v[72:75], v[36:39], v[20:23], v[72:75]\n\t" \
  "ds_read_b64_tr_b16 v[52:53], v60 offset:25600\n\t" \
  "v_mfma_f32_16x16x32_bf16 v[76:79], v[40:43], v[4:7], v[76:79]\n\t" \
  "ds_read_b128 v[4:7], v56 offset:49152\n\t" \
  "v_mfma_f32_16x16x32_bf16 v[76:79], v[44:47], v[20:23], v[76:79]\n\t" \
  "ds_read_b128 v[20:23], v57 offset:49152\n\t" \
  "v_mfma_f32_16x16x32_bf16 v[80:83], v[32:35], v[8:11], v[80:83]\n\t" \
  "ds_read_b64_tr_b16 v[54:55], v60 offset:26112\n\t" \
  "v_mfma_f32_16x16x32_bf16 v[80:83], v[36:39], v[24:27], v[80:83]\n\t" \
  "v_mfma_f32_16x16x32_bf16 v[84:87], v[40:43], v[8:11], v[84:87]\n\t" \
  "ds_read_b128 v[8:11], v56 offset:51200\n\t" \
  "v_mfma_f32_16x16x32_bf16 v[84:87], v[44:47], v[24:27], v[84:87]\n\t" \
  "ds_read_b128 v[24:27], v57 offset:51200\n\t" \
  "v_mfma_f32_16x16x32_bf16 v[88:91], v[32:35], v[12:15], v[88:91]\n\t" \
  "ds_read_b64_tr_b16 v[32:33], v60 offset:14336\n\t" \
  "v_mfma_f32_16x16x32_bf16 v[88:91], v[36:39], v[28:31], v[88:91]\n\t" \
  "ds_read_b64_tr_b16 v[34:35], v60 offset:14848\n\t" \
  "v_mfma_f32_16x16x32_bf16 v[92:95], v[40:43], v[12:15], v[92:95]\n\t" \
  "ds_read_b128 v[12:15], v56 offset:53248\n\t" \
  "v_mfma_f32_16x16x32_bf16 v[92:95], v[44:47], v[28:31], v[92:95]\n\t" \
  "ds_read_b128 v[28:31], v57 offset:53248\n\t" \
  "N2b_%=:\n\t" \
  "s_waitcnt lgkmcnt(10)\n\t" \
  "v_mfma_f32_16x16x32_bf16 v[96:99], v[48:51], v[0:3], v[96:99]\n\t" \
  "ds_read_b64_tr_b16 v[36:37], v60 offset:26624\n\t" \
  "s_waitcnt lgkmcnt(7)\n\t" \
  "v_mfma_f32_16x16x32_bf16 v[96:99], v[52:55], v[16:19], v[96:99]\n\t" \
  "ds_read_b64_tr_b16 v[38:39], v60 offset:27136\n\t" \
  "v_mfma_f32_16x16x32_bf16 v[104:107], v[48:51], v[4:7], v[104:107]\n\t" \
  "ds_read_b128 v[40:43], v56 offset:36864\n\t" \
  "v_mfma_f32_16x16x32_bf16 v[104:107], v[52:55], v[20:23], v[104:107]\n\t" \
  "ds_read_b128 v[44:47], v57 offset:36864\n\t" \
  "s_waitcnt lgkmcnt(8)\n\t" \
  "v_mfma_f32_16x16x32_bf16 v[112:115], v[48:51], v[8:11], v[112:115]\n\t" \
  "v_mfma_f32_16x16x32_bf16 v[112:115], v[52:55], v[24:27], v[112:115]\n\t" \
  "s_waitcnt lgkmcnt(4)\n\t" \
  "v_mfma_f32_16x16x32_bf16 v[120:123], v[48:51], v[12:15], v[120:123]\n\t" \
  "v_mfma_f32_16x16x32_bf16 v[120:123], v[52:55], v[28:31], v[120:123]\n\t" \
  "ds_read_b64_tr_b16 v[48:49], v61 offset:14336\n\t" \
  "v_mfma_f32_16x16x32_bf16 v[128:131], v[0:3], v[32:35], v[128:131]\n\t" \
  "ds_read_b64_tr_b16 v[50:51], v61 offset:14848\n\t" \
  "s_waitcnt lgkmcnt(4)\n\t" \
  "v_mfma_f32_16x16x32_bf16 v[128:131], v[16:19], v[36:39], v[128:131]\n\t" \
  "ds_read_b64_tr_b16 v[52:53], v61 offset:26624\n\t" \
  "v_mfma_f32_16x16x32_bf16 v[136:139], v[4:7], v[32:35], v[136:139]\n\t" \
  "v_mfma_f32_16x16x32_bf16 v[136:139], v[20:23], v[36:39], v[136:139]\n\t" \
  "ds_read_b64_tr_b16 v[54:55], v61 offset:27136\n\t" \
  "v_mfma_f32_16x16x32_bf16 v[144:147], v[8:11], v[32:35], v[144:147]\n\t" \
  "v_mfma_f32_16x16x32_bf16 v[144:147], v[24:27], v[36:39], v[144:147]\n\t" \
  "v_mfma_f32_16x16x32_bf16 v[152:155], v[12:15], v[32:35], v[152:155]\n\t" \
  "v_mfma_f32_16x16x32_bf16 v[152:155], v[28:31], v[36:39], v[152:155]\n\t" \
  "s_waitcnt lgkmcnt(4)\n\t" \
  "v_mfma_f32_16x16x32_bf16 v[160:163], v[40:43], v[32:35], v[160:163]\n\t" \
  "v_mfma_f32_16x16x32_bf16 v[160:163], v[44:47], v[36:39], v[160:163]\n\t" \
  "ds_read_b64_tr_b16 v[32:33], v61 offset:13312\n\t" \
  "s_waitcnt lgkmcnt(1)\n\t" \
  "v_mfma_f32_16x16x32_bf16 v[164:167], v[40:43], v[48:51], v[164:167]\n\t" \
  "ds_read_b64_tr_b16 v[34:35], v61 offset:13824\n\t" \
  "v_mfma_f32_16x16x32_bf16 v[164:167], v[44:47], v[52:55], v[164:167]\n\t" \
  "ds_read_b64_tr_b16 v[36:37], v61 offset:25600\n\t" \
  "v_mfma_f32_16x16x32_bf16 v[132:135], v[0:3], v[48:51], v[132:135]\n\t" \
  "ds_read_b64_tr_b16 v[38:39], v61 offset:26112\n\t" \
  "v_mfma_f32_16x16x32_bf16 v[132:135], v[16:19], v[52:55], v[132:135]\n\t" \
  "ds_read_b64_tr_b16 v[40:41], v60 offset:12288\n\t" \
  "v_mfma_f32_16x16x32_bf16 v[140:143], v[4:7], v[48:51], v[140:143]\n\t" \
  "ds_read_b64_tr_b16 v[42:43], v60 offset:12800\n\t" \
  "v_mfma_f32_16x16x32_bf16 v[140:143], v[20:23], v[52:55], v[140:143]\n\t" \
  "ds_read_b64_tr_b16 v[44:45], v60 offset:24576\n\t" \
  "v_mfma_f32_16x16x32_bf16 v[148:151], v[8:11], v[48:51], v[148:151]\n\t" \
  "ds_read_b64_tr_b16 v[46:47], v60 offset:25088\n\t" \
  "v_mfma_f32_16x16x32_bf16 v[148:151], v[24:27], v[52:55], v[148:151]\n\t" \
  "v_mfma_f32_16x16x32_bf16 v[156:159], v[12:15], v[48:51], v[156:159]\n\t" \
  "ds_read_b64_tr_b16 v[48:49], v61 offset:12288\n\t" \
  "v_mfma_f32_16x16x32_bf16 v[156:159], v[28:31], v[52:55], v[156:159]\n\t" \
  "ds_read_b64_tr_b16 v[50:51], v61 offset:12800\n\t" \
  "s_waitcnt lgkmcnt(6)\n\t" \
  "v_mfma_f32_16x16x32_bf16 v[100:103], v[32:35], v[0:3], v[100:103]\n\t" \
  "ds_read_b64_tr_b16 v[52:53], v61 offset:24576\n\t" \
  "v_mfma_f32_16x16x32_bf16 v[100:103], v[36:39], v[16:19], v[100:103]\n\t" \
  "ds_read_b64_tr_b16 v[54:55], v61 offset:25088\n\t" \
  "v_mfma_f32_16x16x32_bf16 v[108:111], v[32:35], v[4:7], v[108:111]\n\t" \
  "v_mfma_f32_16x16x32_bf16 v[108:111], v[36:39], v[20:23], v[108:111]\n\t" \
  "s_addk_i32 %[it], 1\n\t" \
  "v_mfma_f32_16x16x32_bf16 v[116:119], v[32:35], v[8:11], v[116:119]\n\t" \
  "s_cmp_lt_u32 %[it], 6\n\t" \
  "v_mfma_f32_16x16x32_bf16 v[116:119], v[36:39], v[24:27], v[116:119]\n\t" \
  "v_mfma_f32_16x16x32_bf16 v[124:127], v[32:35], v[12:15], v[124:127]\n\t" \
  "v_mfma_f32_16x16x32_bf16 v[124:127], v[36:39], v[28:31], v[124:127]\n\t" \
  "s_waitcnt lgkmcnt(0)\n\t" \
  "s_barrier\n\t" \
  "s_cbranch_scc0 NTb_%=\n\t" \
  "v_mfma_f32_16x16x32_bf16 v[64:67], v[40:43], v[0:3], v[64:67]\n\t" \
  "v_mfma_f32_16x16x32_bf16 v[64:67], v[44:47], v[16:19], v[64:67]\n\t" \
  "ds_read_b64_tr_b16 v[32:33], v58 offset:1024\n\t" \
  "v_mfma_f32_16x16x32_bf16 v[68:71], v[48:51], v[0:3], v[68:71]\n\t" \
  "ds_read_b128 v[0:3], v56 offset:10240\n\t" \
  "v_mfma_f32_16x16x32_bf16 v[68:71], v[52:55], v[16:19], v[68:71]\n\t" \
  "ds_read_b128 v[16:19], v57 offset:10240\n\t" \
  "v_mfma_f32_16x16x32_bf16 v[72:75], v[40:43], v[4:7], v[72:75]\n\t" \
  "ds_read_b64_tr_b16 v[34:35], v58 offset:1536\n\t" \
  "v_mfma_f32_16x16x32_bf16 v[72:75], v[44:47], v[20:23], v[72:75]\n\t" \
  "ds_read_b64_tr_b16 v[36:37], v58 offset:13312\n\t" \
  "v_mfma_f32_16x16x32_bf16 v[76:79], v[48:51], v[4:7], v[76:79]\n\t" \
  "ds_read_b128 v[4:7], v56 offset:12288\n\t" \
  "v_mfma_f32_16x16x32_bf16 v[76:79], v[52:55], v[20:23], v[76:79]\n\t" \
  "ds_read_b128 v[20:23], v57 offset:12288\n\t" \
  "v_mfma_f32_16x16x32_bf16 v[80:83], v[40:43], v[8:11], v[80:83]\n\t" \
  "ds_read_b64_tr_b16 v[38:39], v58 offset:13824\n\t" \
  "v_mfma_f32_16x16x32_bf16 v[80:83], v[44:47], v[24:27], v[80:83]\n\t" \
  "v_mfma_f32_16x16x32_bf16 v[84:87], v[48:51], v[8:11], v[84:87]\n\t" \
  "ds_read_b128 v[8:11], v56 offset:14336\n\t" \
  "v_mfma_f32_16x16x32_bf16 v[84:87], v[52:55], v[24:27], v[84:87]\n\t" \
  "ds_read_b128 v[24:27], v57 offset:14336\n\t" \
  "v_mfma_f32_16x16x32_bf16 v[88:91], v[40:43], v[12:15], v[88:91]\n\t" \
  "ds_read_b64_tr_b16 v[40:41], v58 offset:2048\n\t" \
  "v_mfma_f32_16x16x32_bf16 v[88:91], v[44:47], v[28:31], v[88:91]\n\t" \
  "ds_read_b64_tr_b16 v[42:43], v58 offset:2560\n\t" \
  "v_mfma_f32_16x16x32_bf16 v[92:95], v[48:51], v[12:15], v[92:95]\n\t" \
  "ds_read_b128 v[12:15], v56 offset:16384\n\t" \
  "v_mfma_f32_16x16x32_bf16 v[92:95], v[52:55], v[28:31], v[92:95]\n\t" \
  "ds_read_b128 v[28:31], v57 offset:16384\n\t" \
  "s_branch N0b_%=\n\t" \
  "NTb_%=:\n\t" \
  "s_setprio 3\n\t" \
  "v_mfma_f32_16x16x32_bf16 v[88:91], v[40:43], v[12:15], v[88:91]\n\t" \
  "v_cvt_pk_bf16_f32 v160, v160, v161\n\t" \
  "v_cvt_pk_bf16_f32 v161, v162, v163\n\t" \
  "v_mfma_f32_16x16x32_bf16 v[88:91], v[44:47], v[28:31], v[88:91]\n\t" \
  "v_cvt_pk_bf16_f32 v162, v164, v165\n\t" \
  "v_cvt_pk_bf16_f32 v163, v166, v167\n\t" \
  "v_mfma_f32_16x16x32_bf16 v[92:95], v[48:51], v[12:15], v[92:95]\n\t" \
  "v_mov_b32 v57, %[fa]\n\t" \
  "v_mov_b32 v58, 1\n\t" \
  "v_mfma_f32_16x16x32_bf16 v[92:95], v[52:55], v[28:31], v[92:95]\n\t" \
  "ds_write_b128 v63, v[160:163] offset:2048\n\t" \
  "s_mov_b64 exec, 1\n\t" \
  "ds_write_b32 v57, v58\n\t" \
  "s_mov_b64 exec, -1\n\t" \
  "v_mfma_f32_16x16x32_bf16 v[80:83], v[40:43], v[8:11], v[80:83]\n\t" \
  "v_mov_b32 v60, 0xff61b1e6\n\t" \
  "v_cvt_pk_bf16_f32 v120, v120, v121\n\t" \
  "v_mfma_f32_16x16x32_bf16 v[80:83], v[44:47], v[24:27], v[80:83]\n\t" \
  "v_cvt_pk_bf16_f32 v121, v122, v123\n\t" \
  "v_cvt_pk_bf16_f32 v122, v124, v125\n\t" \
  "v_mfma_f32_16x16x32_bf16 v[84:87], v[48:51], v[8:11], v[84:87]\n\t" \
  "v_cvt_pk_bf16_f32 v123, v126, v127\n\t" \
  "v_cvt_pk_bf16_f32 v28, v152, v153\n\t" \
  "v_mfma_f32_16x16x32_bf16 v[84:87], v[52:55], v[24:27], v[84:87]\n\t" \
  "v_cvt_pk_bf16_f32 v29, v154, v155\n\t" \
  "v_cvt_pk_bf16_f32 v38, v156, v157\n\t" \
  "v_mfma_f32_16x16x32_bf16 v[72:75], v[40:43], v[4:7], v[72:75]\n\t" \
  "v_cvt_pk_bf16_f32 v39, v158, v159\n\t" \
  "v_cvt_pk_bf16_f32 v112, v112, v113\n\t" \
  "v_mfma_f32_16x16x32_bf16 v[72:75], v[44:47], v[20:23], v[72:75]\n\t" \
  "v_cvt_pk_bf16_f32 v113, v114, v115\n\t" \
  "v_cvt_pk_bf16_f32 v114, v116, v117\n\t" \
  "v_mfma_f32_16x16x32_bf16 v[76:79], v[48:51], v[4:7], v[76:79]\n\t" \
  "v_cvt_pk_bf16_f32 v115, v118, v119\n\t" \
  "v_cndmask_b32_e64 v12, v60, 0, s[76:77]\n\t" \
  "v_mfma_f32_16x16x32_bf16 v[76:79], v[52:55], v[20:23], v[76:79]\n\t" \
  "v_cndmask_b32_e64 v13, v60, 0, s[78:79]\n\t" \
  "v_cndmask_b32_e64 v14, v60, 0, s[80:81]\n\t" \
  "v_mfma_f32_16x16x32_bf16 v[64:67], v[40:43], v[0:3], v[64:67]\n\t" \
  "v_cndmask_b32_e64 v15, v60, 0, s[82:83]\n\t" \
  "v_cndmask_b32_e64 v56, v60, 0, s[68:69]\n\t" \
  "v_mfma_f32_16x16x32_bf16 v[64:67], v[44:47], v[16:19], v[64:67]\n\t" \
  "v_cndmask_b32_e64 v57, v60, 0, s[70:71]\n\t" \
  "v_cndmask_b32_e64 v58, v60, 0, s[72:73]\n\t" \
  "v_mfma_f32_16x16x32_bf16 v[68:71], v[48:51], v[0:3], v[68:71]\n\t" \
  "v_cndmask_b32_e64 v59, v60, 0, s[74:75]\n\t" \
  "v_mfma_f32_16x16x32_bf16 v[68:71], v[52:55], v[16:19], v[68:71]\n\t" \
  "NX_%=:\n\t" \
  "v_cvt_pk_bf16_f32 v88, v88, v89\n\t" \
  "v_cvt_pk_bf16_f32 v89, v90, v91\n\t" \
  "v_cvt_pk_bf16_f32 v90, v92, v93\n\t" \
  "v_cvt_pk_bf16_f32 v91, v94, v95\n\t" \
  "v_mov_b32 v61, %[fp]\n\t" \
  "v_add_u32 v60, %[vo], v63\n\t" \
  "v_mfma_f32_16x16x32_bf16 v[152:155], v[112:115], v[88:91], v[56:59]\n\t" \
  "v_cvt_pk_bf16_f32 v26, v144, v145\n\t" \
  "v_cvt_pk_bf16_f32 v27, v146, v147\n\t" \
  "v_mfma_f32_16x16x32_bf16 v[156:159], v[120:123], v[88:91], v[12:15]\n\t" \
  "v_cvt_pk_bf16_f32 v36, v148, v149\n\t" \
  "v_cvt_pk_bf16_f32 v37, v150, v151\n\t" \
  "v_cvt_pk_bf16_f32 v80, v80, v81\n\t" \
  "v_cvt_pk_bf16_f32 v81, v82, v83\n\t" \
  "v_cvt_pk_bf16_f32 v82, v84, v85\n\t" \
  "v_cvt_pk_bf16_f32 v83, v86, v87\n\t" \
  "v_cvt_pk_bf16_f32 v104, v104, v105\n\t" \
  "v_cvt_pk_bf16_f32 v105, v106, v107\n\t" \
  "v_mfma_f32_16x16x32_bf16 v[148:151], v[112:115], v[80:83], v[12:15]\n\t" \
  "v_cvt_pk_bf16_f32 v106, v108, v109\n\t" \
  "v_cvt_pk_bf16_f32 v107, v110, v111\n\t" \
  "ds_read_b32 v0, v61\n\t" \
  "ds_read_b64 v[20:21], v60\n\t" \
  "ds_read_b64 v[30:31], v60 offset:8\n\t" \
  "s_mov_b64 exec, %[rm]\n\t" \
  "ds_read_b128 v[160:163], v63\n\t" \
  "s_mov_b64 exec, -1\n\t" \
  "v_mfma_f32_16x16x32_bf16 v[144:147], v[104:107], v[80:83], v[56:59]\n\t" \
  "v_cvt_pk_bf16_f32 v24, v136, v137\n\t" \
  "v_cvt_pk_bf16_f32 v25, v138, v139\n\t" \
  "v_cvt_pk_bf16_f32 v34, v140, v141\n\t" \
  "v_cvt_pk_bf16_f32 v35, v142, v143\n\t" \
  "v_cvt_pk_bf16_f32 v72, v72, v73\n\t" \
  "v_cvt_pk_bf16_f32 v73, v74, v75\n\t" \
  "v_cvt_pk_bf16_f32 v74, v76, v77\n\t" \
  "v_cvt_pk_bf16_f32 v75, v78, v79\n\t" \
  "v_cvt_pk_bf16_f32 v96, v96, v97\n\t" \
  "v_cvt_pk_bf16_f32 v97, v98, v99\n\t" \
  "v_mfma_f32_16x16x32_bf16 v[140:143], v[104:107], v[72:75], v[12:15]\n\t" \
  "v_cvt_pk_bf16_f32 v98, v100, v101\n\t" \
  "v_cvt_pk_bf16_f32 v99, v102, v103\n\t" \
  "v_cvt_pk_bf16_f32 v22, v128, v129\n\t" \
  "v_cvt_pk_bf16_f32 v23, v130, v131\n\t" \
  "v_mfma_f32_16x16x32_bf16 v[136:139], v[96:99], v[72:75], v[56:59]\n\t" \
  "v_cvt_pk_bf16_f32 v32, v132, v133\n\t" \
  "v_cvt_pk_bf16_f32 v33, v134, v135\n\t" \
  "v_cvt_pk_bf16_f32 v64, v64, v65\n\t" \
  "v_cvt_pk_bf16_f32 v65, v66, v67\n\t" \
  "v_cvt_pk_bf16_f32 v66, v68, v69\n\t" \
  "v_cvt_pk_bf16_f32 v67, v70, v71\n\t" \
  "v_max_f32 v152, v152, v153\n\t" \
  "v_max_f32 v154, v154, v155\n\t" \
  "v_mfma_f32_16x16x32_bf16 v[132:135], v[96:99], v[64:67], v[12:15]\n\t" \
  "AW_%=:\n\t" \
  "s_waitcnt lgkmcnt(0)\n\t" \
  "v_cmp_eq_u32 vcc, 0, v0\n\t" \
  "v_mov_b32 v8, 0xc0c0100\n\t" \
  "v_mov_b32 v9, 0x1000c0c\n\t" \
  "s_cbranch_vccnz AR_%=\n\t" \
  "s_cmp_eq_u32 %[z], 0\n\t" \
  "s_cbranch_scc1 AZ_%=\n\t" \
  "AY_%=:\n\t" \
  "v_mfma_f32_16x16x32_bf16 v[128:131], v[160:163], v[64:67], v[56:59]\n\t" \
  "v_max_f32 v156, v156, v157\n\t" \
  "v_max_f32 v158, v158, v159\n\t" \
  "v_max3_f32 v7, v152, v154, v156\n\t" \
  "v_max_f32 v7, v7, v158\n\t" \
  "v_max_f32 v144, v144, v145\n\t" \
  "v_max_f32 v146, v146, v147\n\t" \
  "v_max_f32 v148, v148, v149\n\t" \
  "v_max_f32 v150, v150, v151\n\t" \
  "v_max3_f32 v6, v144, v146, v148\n\t" \
  "v_max_f32 v6, v6, v150\n\t" \
  "v_max_f32 v136, v136, v137\n\t" \
  "v_max_f32 v138, v138, v139\n\t" \
  "v_max_f32 v140, v140, v141\n\t" \
  "v_max_f32 v142, v142, v143\n\t" \
  "v_max3_f32 v5, v136, v138, v140\n\t" \
  "v_max_f32 v5, v5, v142\n\t" \
  "v_max_f32 v128, v128, v129\n\t" \
  "v_max_f32 v130, v130, v131\n\t" \
  "v_max_f32 v132, v132, v133\n\t" \
  "v_max_f32 v134, v134, v135\n\t" \
  "v_max3_f32 v4, v128, v130, v132\n\t" \
  "v_max_f32 v4, v4, v134\n\t" \
  "v_cndmask_b32_e64 v60, v8, v9, s[84:85]\n\t" \
  "v_mov_b32 v8, 0xc0c0302\n\t" \
  "v_permlane16_swap_b32 v6, v7\n\t" \
  "v_permlane16_swap_b32 v4, v5\n\t" \
  "v_max_f32 v6, v6, v7\n\t" \
  "v_max_f32 v4, v4, v5\n\t" \
  "v_mov_b32 v9, 0x3020c0c\n\t" \
  "v_cndmask_b32_e64 v61, v8, v9, s[84:85]\n\t" \
  "v_permlane32_swap_b32 v4, v6\n\t" \
  "v_max_f32 v4, v4, v6\n\t" \
  "v_mul_f32 v6, 0xbe8293ee, v4\n\t" \
  "v_mul_f32 v4, 0xbe8293ee, v4\n\t" \
  "s_nop 1\n\t" \
  "v_permlane32_swap_b32 v4, v6\n\t" \
  "v_mov_b32 v5, v4\n\t" \
  "v_mov_b32 v7, v6\n\t" \
  "s_nop 0\n\t" \
  "v_permlane16_swap_b32 v4, v5\n\t" \
  "v_permlane16_swap_b32 v6, v7\n\t" \
  "v_fmamk_f32 v128, v128, 0x3e8293ee, v4\n\t" \
  "v_fmamk_f32 v130, v130, 0x3e8293ee, v4\n\t" \
  "v_fmamk_f32 v132, v132, 0x3e8293ee, v4\n\t" \
  "v_fmamk_f32 v134, v134, 0x3e8293ee, v4\n\t" \
  "v_fmamk_f32 v136, v136, 0x3e8293ee, v5\n\t" \
  "v_fmamk_f32 v138, v138, 0x3e8293ee, v5\n\t" \
  "v_fmamk_f32 v140, v140, 0x3e8293ee, v5\n\t" \
  "v_fmamk_f32 v142, v142, 0x3e8293ee, v5\n\t" \
  "v_fmamk_f32 v144, v144, 0x3e8293ee, v6\n\t" \
  "v_fmamk_f32 v146, v146, 0x3e8293ee, v6\n\t" \
  "v_fmamk_f32 v148, v148, 0x3e8293ee, v6\n\t" \
  "v_fmamk_f32 v150, v150, 0x3e8293ee, v6\n\t" \
  "v_fmamk_f32 v152, v152, 0x3e8293ee, v7\n\t" \
  "v_fmamk_f32 v154, v154, 0x3e8293ee, v7\n\t" \
  "v_fmamk_f32 v156, v156, 0x3e8293ee, v7\n\t" \
  "v_fmamk_f32 v158, v158, 0x3e8293ee, v7\n\t" \
  "v_exp_f32 v128, v128\n\t" \
  "v_exp_f32 v130, v130\n\t" \
  "v_exp_f32 v132, v132\n\t" \
  "v_exp_f32 v134, v134\n\t" \
  "v_exp_f32 v136, v136\n\t" \
  "v_exp_f32 v138, v138\n\t" \
  "v_exp_f32 v140, v140\n\t" \
  "v_exp_f32 v142, v142\n\t" \
  "v_exp_f32 v144, v144\n\t" \
  "v_exp_f32 v146, v146\n\t" \
  "v_exp_f32 v148, v148\n\t" \
  "v_exp_f32 v150, v150\n\t" \
  "v_exp_f32 v152, v152\n\t" \
  "v_exp_f32 v154, v154\n\t" \
  "v_exp_f32 v156, v156\n\t" \
  "v_exp_f32 v158, v158\n\t" \
  "v_add_f32 v4, v128, v130\n\t" \
  "v_add_f32 v8, v132, v134\n\t" \
  "v_add_f32 v5, v136, v138\n\t" \
  "v_add_f32 v9, v140, v142\n\t" \
  "v_add_f32 v6, v144, v146\n\t" \
  "v_add_f32 v10, v148, v150\n\t" \
  "v_add_f32 v7, v152, v154\n\t" \
  "v_add_f32 v11, v156, v158\n\t" \
  "v_add_f32 v4, v4, v8\n\t" \
  "v_add_f32 v5, v5, v9\n\t" \
  "v_add_f32 v6, v6, v10\n\t" \
  "v_add_f32 v7, v7, v11\n\t" \
  "v_permlane16_swap_b32 v4, v5\n\t" \
  "s_nop 0\n\t" \
  "v_permlane16_swap_b32 v6, v7\n\t" \
  "v_add_f32 v4, v4, v5\n\t" \
  "v_add_f32 v6, v6, v7\n\t" \
  "s_nop 1\n\t" \
  "v_permlane32_swap_b32 v4, v6\n\t" \
  "v_add_f32 v4, v4, v6\n\t" \
  "v_rcp_f32 v4, v4\n\t" \
  "s_nop 0\n\t" \
  "v_mov_b32 v6, v4\n\t" \
  "s_nop 1\n\t" \
  "v_permlane32_swap_b32 v4, v6\n\t" \
  "v_mov_b32 v5, v4\n\t" \
  "v_mov_b32 v7, v6\n\t" \
  "s_nop 0\n\t" \
  "v_permlane16_swap_b32 v4, v5\n\t" \
  "v_permlane16_swap_b32 v6, v7\n\t" \
  "v_mul_f32 v128, v128, v4\n\t" \
  "v_mul_f32 v130, v130, v4\n\t" \
  "v_mul_f32 v132, v132, v4\n\t" \
  "v_mul_f32 v134, v134, v4\n\t" \
  "v_cvt_pk_bf16_f32 v128, v128, v130\n\t" \
  "v_cvt_pk_bf16_f32 v132, v132, v134\n\t" \
  "v_perm_b32 v68, 0, v128, v60\n\t" \
  "v_perm_b32 v69, 0, v128, v61\n\t" \
  "v_perm_b32 v70, 0, v132, v60\n\t" \
  "v_perm_b32 v71, 0, v132, v61\n\t" \
  "v_mul_f32 v136, v136, v5\n\t" \
  "v_mul_f32 v138, v138, v5\n\t" \
  "v_mfma_f32_16x16x32_bf16 v[128:131], v[20:23], v[68:71], 0\n\t" \
  "v_mul_f32 v140, v140, v5\n\t" \
  "v_mul_f32 v142, v142, v5\n\t" \
  "v_cvt_pk_bf16_f32 v136, v136, v138\n\t" \
  "v_mfma_f32_16x16x32_bf16 v[132:135], v[30:33], v[68:71], 0\n\t" \
  "v_cvt_pk_bf16_f32 v140, v140, v142\n\t" \
  "v_perm_b32 v76, 0, v136, v60\n\t" \
  "v_perm_b32 v77, 0, v136, v61\n\t" \
  "v_perm_b32 v78, 0, v140, v60\n\t" \
  "v_perm_b32 v79, 0, v140, v61\n\t" \
  "v_mul_f32 v144, v144, v6\n\t" \
  "v_mul_f32 v146, v146, v6\n\t" \
  "v_mfma_f32_16x16x32_bf16 v[136:139], v[22:25], v[76:79], 0\n\t" \
  "v_mul_f32 v148, v148, v6\n\t" \
  "v_mul_f32 v150, v150, v6\n\t" \
  "v_cvt_pk_bf16_f32 v144, v144, v146\n\t" \
  "v_mfma_f32_16x16x32_bf16 v[140:143], v[32:35], v[76:79], 0\n\t" \
  "v_cvt_pk_bf16_f32 v148, v148, v150\n\t" \
  "v_perm_b32 v84, 0, v144, v60\n\t" \
  "v_perm_b32 v85, 0, v144, v61\n\t" \
  "v_perm_b32 v86, 0, v148, v60\n\t" \
  "v_perm_b32 v87, 0, v148, v61\n\t" \
  "v_cvt_pk_bf16_f32 v100, v128, v129\n\t" \
  "v_cvt_pk_bf16_f32 v101, v130, v131\n\t" \
  "v_mfma_f32_16x16x32_bf16 v[144:147], v[24:27], v[84:87], 0\n\t" \
  "v_cvt_pk_bf16_f32 v102, v132, v133\n\t" \
  "v_cvt_pk_bf16_f32 v103, v134, v135\n\t" \
  "global_store_dwordx4 v62, v[100:103], %[ot] offset:0 sc1\n\t" \
  "v_mfma_f32_16x16x32_bf16 v[148:151], v[34:37], v[84:87], 0\n\t" \
  "v_mul_f32 v152, v152, v7\n\t" \
  "v_mul_f32 v154, v154, v7\n\t" \
  "v_mul_f32 v156, v156, v7\n\t" \
  "v_mul_f32 v158, v158, v7\n\t" \
  "v_cvt_pk_bf16_f32 v152, v152, v154\n\t" \
  "v_cvt_pk_bf16_f32 v156, v156, v158\n\t" \
  "v_perm_b32 v92, 0, v152, v60\n\t" \
  "v_perm_b32 v93, 0, v152, v61\n\t" \
  "v_perm_b32 v94, 0, v156, v60\n\t" \
  "v_perm_b32 v95, 0, v156, v61\n\t" \
  "v_cvt_pk_bf16_f32 v108, v136, v137\n\t" \
  "v_cvt_pk_bf16_f32 v109, v138, v139\n\t" \
  "v_mfma_f32_16x16x32_bf16 v[152:155], v[26:29], v[92:95], 0\n\t" \
  "v_cvt_pk_bf16_f32 v110, v140, v141\n\t" \
  "v_cvt_pk_bf16_f32 v111, v142, v143\n\t" \
  "global_store_dwordx4 v62, v[108:111], %[ot] offset:1024 sc1\n\t" \
  "v_mfma_f32_16x16x32_bf16 v[156:159], v[36:39], v[92:95], 0\n\t" \
  "v_cvt_pk_bf16_f32 v116, v144, v145\n\t" \
  "v_cvt_pk_bf16_f32 v117, v146, v147\n\t" \
  "v_cvt_pk_bf16_f32 v118, v148, v149\n\t" \
  "v_cvt_pk_bf16_f32 v119, v150, v151\n\t" \
  "global_store_dwordx4 v62, v[116:119], %[ou] offset:0 sc1\n\t" \
  "v_cvt_pk_bf16_f32 v124, v152, v153\n\t" \
  "v_cvt_pk_bf16_f32 v125, v154, v155\n\t" \
  "s_nop 0\n\t" \
  "v_cvt_pk_bf16_f32 v126, v156, v157\n\t" \
  "v_cvt_pk_bf16_f32 v127, v158, v159\n\t" \
  "global_store_dwordx4 v62, v[124:127], %[ou] offset:1024 sc1\n\t" \
  "s_branch AE_%=\n\t" \
  "AR_%=:\n\t" \
  "s_addk_i32 %[it], 1\n\t" \
  "s_cmp_gt_u32 %[it], 0x400000\n\t" \
  "s_cbranch_scc1 AY_%=\n\t" \
  "ds_read_b32 v0, v61\n\t" \
  "ds_read_b64 v[20:21], v60\n\t" \
  "ds_read_b64 v[30:31], v60 offset:8\n\t" \
  "s_mov_b64 exec, %[rm]\n\t" \
  "ds_read_b128 v[160:163], v63\n\t" \
  "s_mov_b64 exec, -1\n\t" \
  "s_branch AW_%=\n\t" \
  "AZ_%=:\n\t" \
  "v_mov_b32 v0, 0xff61b1e6\n\t" \
  "v_mov_b32 v1, 0xcfa8965f\n\t" \
  "s_nop 3\n\t" \
  "v_cndmask_b32_e64 v56, v0, v1, s[68:69]\n\t" \
  "v_cndmask_b32_e64 v57, v0, v1, s[70:71]\n\t" \
  "v_cndmask_b32_e64 v58, v0, v1, s[72:73]\n\t" \
  "v_cndmask_b32_e64 v59, v0, v1, s[74:75]\n\t" \
  "s_nop 1\n\t" \
  "s_branch AY_%=\n\t" \
  "AE_%=:\n\t" \
  "s_nop 1\n\t"
#define N2_CLOB "v0", "v1", "v2", "v3", "v4", "v5", "v6", "v7", "v8", "v9", "v10", "v11", "v12", "v13", "v14", "v15", "v16", "v17", "v18", "v19", "v20", "v21", "v22", "v23", "v24", "v25", "v26", "v27", "v28", "v29", "v30", "v31", "v32", "v33", "v34", "v35", "v36", "v37", "v38", "v39", "v40", "v41", "v42", "v43", "v44", "v45", "v46", "v47", "v48", "v49", "v50", "v51", "v52", "v53", "v54", "v55", "v60", "v61", "v64", "v65", "v66", "v67", "v68", "v69", "v70", "v71", "v72", "v73", "v74", "v75", "v76", "v77", "v78", "v79", "v80", "v81", "v82", "v83", "v84", "v85", "v86", "v87", "v88", "v89", "v90", "v91", "v92", "v93", "v94", "v95", "v96", "v97", "v98", "v99", "v100", "v101", "v102", "v103", "v104", "v105", "v106", "v107", "v108", "v109", "v110", "v111", "v112", "v113", "v114", "v115", "v116", "v117", "v118", "v119", "v120", "v121", "v122", "v123", "v124", "v125", "v126", "v127", "v128", "v129", "v130", "v131", "v132", "v133", "v134", "v135", "v136", "v137", "v138", "v139", "v140", "v141", "v142", "v143", "v144", "v145", "v146", "v147", "v148", "v149", "v150", "v151", "v152", "v153", "v154", "v155", "v156", "v157", "v158", "v159", "v160", "v161", "v162", "v163", "v164", "v165", "v166", "v167", "s68", "s69", "s70", "s71", "s72", "s73", "s74", "s75", "s76", "s77", "s78", "s79", "s80", "s81", "s82", "s83", "s84", "s85"

#define CV_PRE 1
#define CV_Y 1
#define CV_ASM \
  "CS_%=:\n\t" \
  "ds_read_b32 v34, v61\n\t" \
  "s_waitcnt lgkmcnt(0)\n\t" \
  "v_cmp_ne_u32 vcc, %[tg], v34\n\t" \
  "s_cbranch_vccz CR_%=\n\t" \
  "s_cmp_eq_u32 %[rem], 0\n\t" \
  "s_cbranch_scc1 CT_%=\n\t" \
  "s_barrier\n\t" \
  "s_sub_u32 %[rem], %[rem], 1\n\t" \
  "s_branch CS_%=\n\t" \
  "CT_%=:\n\t" \
  "s_addk_i32 %[cn], 1\n\t" \
  "s_cmp_gt_u32 %[cn], 0x400000\n\t" \
  "s_cbranch_scc1 CR_%=\n\t" \
  "s_sleep 1\n\t" \
  "s_branch CS_%=\n\t" \
  "CR_%=:\n\t" \
  "ds_read_b64 v[0:1], v28 offset:0\n\t" \
  "ds_read_b64 v[2:3], v29 offset:0\n\t" \
  "ds_read_b64 v[4:5], v30 offset:1024\n\t" \
  "ds_read_b64 v[6:7], v31 offset:1024\n\t" \
  "ds_read_b64 v[8:9], v24 offset:2048\n\t" \
  "ds_read_b64 v[10:11], v25 offset:2048\n\t" \
  "ds_read_b64 v[12:13], v26 offset:3072\n\t" \
  "ds_read_b64 v[14:15], v27 offset:3072\n\t" \
  "ds_read_b64 v[16:17], v28 offset:4096\n\t" \
  "ds_read_b64 v[18:19], v29 offset:4096\n\t" \
  "ds_read_b64 v[20:21], v30 offset:5120\n\t" \
  "ds_read_b64 v[22:23], v31 offset:5120\n\t" \
  "s_mov_b64 vcc, 1\n\t" \
  "s_setprio 0\n\t" \
  "s_cmp_eq_u32 %[rem], 0\n\t" \
  "s_cbranch_scc1 CM_%=\n\t" \
  "CB_%=:\n\t" \
  "s_barrier\n\t" \
  "s_sub_u32 %[rem], %[rem], 1\n\t" \
  "s_cmp_lg_u32 %[rem], 0\n\t" \
  "s_cbranch_scc1 CB_%=\n\t" \
  "CM_%=:\n\t" \
  "s_waitcnt lgkmcnt(0)\n\t" \
  "v_mfma_f32_16x16x32_bf16 v[128:131], v[68:71], v[0:3], 0\n\t" \
  "v_mfma_f32_16x16x32_bf16 v[128:131], v[64:67], v[4:7], v[128:131]\n\t" \
  "v_mfma_f32_16x16x32_bf16 v[132:135], v[76:79], v[0:3], 0\n\t" \
  "v_mfma_f32_16x16x32_bf16 v[132:135], v[72:75], v[4:7], v[132:135]\n\t" \
  "v_mfma_f32_16x16x32_bf16 v[136:139], v[84:87], v[0:3], 0\n\t" \
  "v_mfma_f32_16x16x32_bf16 v[136:139], v[80:83], v[4:7], v[136:139]\n\t" \
  "v_mfma_f32_16x16x32_bf16 v[140:143], v[92:95], v[0:3], 0\n\t" \
  "s_setprio 1\n\t" \
  "v_mfma_f32_16x16x32_bf16 v[140:143], v[88:91], v[4:7], v[140:143]\n\t" \
  "v_mfma_f32_16x16x32_bf16 v[144:147], v[100:103], v[0:3], 0\n\t" \
  "ds_bpermute_b32 v34, v32, v131\n\t" \
  "v_mfma_f32_16x16x32_bf16 v[144:147], v[96:99], v[4:7], v[144:147]\n\t" \
  "v_mov_b32 v131, v130\n\t" \
  "v_mov_b32 v130, v129\n\t" \
  "v_mfma_f32_16x16x32_bf16 v[148:151], v[108:111], v[0:3], 0\n\t" \
  "v_mov_b32 v129, v128\n\t" \
  "v_mfma_f32_16x16x32_bf16 v[148:151], v[104:107], v[4:7], v[148:151]\n\t" \
  "v_mfma_f32_16x16x32_bf16 v[152:155], v[116:119], v[0:3], 0\n\t" \
  "v_mfma_f32_16x16x32_bf16 v[152:155], v[112:115], v[4:7], v[152:155]\n\t" \
  "v_mfma_f32_16x16x32_bf16 v[156:159], v[124:127], v[0:3], 0\n\t" \
  "ds_read_b64 v[0:1], v24 offset:6144\n\t" \
  "v_mfma_f32_16x16x32_bf16 v[156:159], v[120:123], v[4:7], v[156:159]\n\t" \
  "ds_read_b64 v[2:3], v25 offset:6144\n\t" \
  "v_mfma_f32_16x16x32_bf16 v[132:135], v[68:71], v[8:11], v[132:135]\n\t" \
  "ds_read_b64 v[4:5], v26 offset:7168\n\t" \
  "v_mfma_f32_16x16x32_bf16 v[132:135], v[64:67], v[12:15], v[132:135]\n\t" \
  "ds_read_b64 v[6:7], v27 offset:7168\n\t" \
  "v_mfma_f32_16x16x32_bf16 v[136:139], v[76:79], v[8:11], v[136:139]\n\t" \
  "v_mfma_f32_16x16x32_bf16 v[136:139], v[72:75], v[12:15], v[136:139]\n\t" \
  "s_waitcnt lgkmcnt(4)\n\t" \
  "v_cndmask_b32_e64 v128, v34, 0, vcc\n\t" \
  "v_mfma_f32_16x16x32_bf16 v[140:143], v[84:87], v[8:11], v[140:143]\n\t" \
  "v_mfma_f32_16x16x32_bf16 v[140:143], v[80:83], v[12:15], v[140:143]\n\t" \
  "v_mfma_f32_16x16x32_bf16 v[144:147], v[92:95], v[8:11], v[144:147]\n\t" \
  "ds_bpermute_b32 v35, v32, v135\n\t" \
  "v_mfma_f32_16x16x32_bf16 v[144:147], v[88:91], v[12:15], v[144:147]\n\t" \
  "v_mov_b32 v135, v134\n\t" \
  "v_mov_b32 v134, v133\n\t" \
  "v_mfma_f32_16x16x32_bf16 v[148:151], v[100:103], v[8:11], v[148:151]\n\t" \
  "v_mov_b32 v133, v132\n\t" \
  "v_mfma_f32_16x16x32_bf16 v[148:151], v[96:99], v[12:15], v[148:151]\n\t" \
  "v_mfma_f32_16x16x32_bf16 v[152:155], v[108:111], v[8:11], v[152:155]\n\t" \
  "v_mfma_f32_16x16x32_bf16 v[152:155], v[104:107], v[12:15], v[152:155]\n\t" \
  "v_mfma_f32_16x16x32_bf16 v[156:159], v[116:119], v[8:11], v[156:159]\n\t" \
  "ds_read_b64 v[8:9], v28 offset:8192\n\t" \
  "v_mfma_f32_16x16x32_bf16 v[156:159], v[112:115], v[12:15], v[156:159]\n\t" \
  "ds_read_b64 v[10:11], v29 offset:8192\n\t" \
  "v_mfma_f32_16x16x32_bf16 v[136:139], v[68:71], v[16:19], v[136:139]\n\t" \
  "ds_read_b64 v[12:13], v30 offset:9216\n\t" \
  "v_mfma_f32_16x16x32_bf16 v[136:139], v[64:67], v[20:23], v[136:139]\n\t" \
  "ds_read_b64 v[14:15], v31 offset:9216\n\t" \
  "v_mfma_f32_16x16x32_bf16 v[140:143], v[76:79], v[16:19], v[140:143]\n\t" \
  "v_mfma_f32_16x16x32_bf16 v[140:143], v[72:75], v[20:23], v[140:143]\n\t" \
  "s_waitcnt lgkmcnt(4)\n\t" \
  "v_cndmask_b32_e64 v132, v35, v34, vcc\n\t" \
  "v_mfma_f32_16x16x32_bf16 v[144:147], v[84:87], v[16:19], v[144:147]\n\t" \
  "v_mfma_f32_16x16x32_bf16 v[144:147], v[80:83], v[20:23], v[144:147]\n\t" \
  "v_mfma_f32_16x16x32_bf16 v[148:151], v[92:95], v[16:19], v[148:151]\n\t" \
  "ds_bpermute_b32 v36, v32, v139\n\t" \
  "v_mfma_f32_16x16x32_bf16 v[148:151], v[88:91], v[20:23], v[148:151]\n\t" \
  "v_mov_b32 v139, v138\n\t" \
  "v_mov_b32 v138, v137\n\t" \
  "v_mfma_f32_16x16x32_bf16 v[152:155], v[100:103], v[16:19], v[152:155]\n\t" \
  "v_mov_b32 v137, v136\n\t" \
  "v_mfma_f32_16x16x32_bf16 v[152:155], v[96:99], v[20:23], v[152:155]\n\t" \
  "v_mfma_f32_16x16x32_bf16 v[156:159], v[108:111], v[16:19], v[156:159]\n\t" \
  "ds_read_b64 v[16:17], v24 offset:10240\n\t" \
  "v_mfma_f32_16x16x32_bf16 v[156:159], v[104:107], v[20:23], v[156:159]\n\t" \
  "ds_read_b64 v[18:19], v25 offset:10240\n\t" \
  "v_mfma_f32_16x16x32_bf16 v[140:143], v[68:71], v[0:3], v[140:143]\n\t" \
  "ds_read_b64 v[20:21], v26 offset:11264\n\t" \
  "v_mfma_f32_16x16x32_bf16 v[140:143], v[64:67], v[4:7], v[140:143]\n\t" \
  "ds_read_b64 v[22:23], v27 offset:11264\n\t" \
  "v_mfma_f32_16x16x32_bf16 v[144:147], v[76:79], v[0:3], v[144:147]\n\t" \
  "v_mfma_f32_16x16x32_bf16 v[144:147], v[72:75], v[4:7], v[144:147]\n\t" \
  "v_mfma_f32_16x16x32_bf16 v[148:151], v[84:87], v[0:3], v[148:151]\n\t" \
  "v_mfma_f32_16x16x32_bf16 v[148:151], v[80:83], v[4:7], v[148:151]\n\t" \
  "s_waitcnt lgkmcnt(4)\n\t" \
  "v_cndmask_b32_e64 v136, v36, v35, vcc\n\t" \
  "v_mfma_f32_16x16x32_bf16 v[152:155], v[92:95], v[0:3], v[152:155]\n\t" \
  "ds_bpermute_b32 v37, v32, v143\n\t" \
  "v_mfma_f32_16x16x32_bf16 v[152:155], v[88:91], v[4:7], v[152:155]\n\t" \
  "v_mov_b32 v143, v142\n\t" \
  "v_mov_b32 v142, v141\n\t" \
  "v_mfma_f32_16x16x32_bf16 v[156:159], v[100:103], v[0:3], v[156:159]\n\t" \
  "ds_read_b64 v[0:1], v28 offset:12288\n\t" \
  "v_mov_b32 v141, v140\n\t" \
  "v_mfma_f32_16x16x32_bf16 v[156:159], v[96:99], v[4:7], v[156:159]\n\t" \
  "ds_read_b64 v[2:3], v29 offset:12288\n\t" \
  "v_mfma_f32_16x16x32_bf16 v[144:147], v[68:71], v[8:11], v[144:147]\n\t" \
  "ds_read_b64 v[4:5], v30 offset:13312\n\t" \
  "v_mfma_f32_16x16x32_bf16 v[144:147], v[64:67], v[12:15], v[144:147]\n\t" \
  "ds_read_b64 v[6:7], v31 offset:13312\n\t" \
  "v_mfma_f32_16x16x32_bf16 v[148:151], v[76:79], v[8:11], v[148:151]\n\t" \
  "v_mfma_f32_16x16x32_bf16 v[148:151], v[72:75], v[12:15], v[148:151]\n\t" \
  "v_mfma_f32_16x16x32_bf16 v[152:155], v[84:87], v[8:11], v[152:155]\n\t" \
  "v_mfma_f32_16x16x32_bf16 v[152:155], v[80:83], v[12:15], v[152:155]\n\t" \
  "v_mfma_f32_16x16x32_bf16 v[156:159], v[92:95], v[8:11], v[156:159]\n\t" \
  "ds_read_b64 v[8:9], v24 offset:14336\n\t" \
  "v_mfma_f32_16x16x32_bf16 v[156:159], v[88:91], v[12:15], v[156:159]\n\t" \
  "ds_read_b64 v[10:11], v25 offset:14336\n\t" \
  "s_waitcnt lgkmcnt(6)\n\t" \
  "v_mfma_f32_16x16x32_bf16 v[148:151], v[68:71], v[16:19], v[148:151]\n\t" \
  "ds_read_b64 v[12:13], v26 offset:15360\n\t" \
  "v_cndmask_b32_e64 v140, v37, v36, vcc\n\t" \
  "v_mfma_f32_16x16x32_bf16 v[148:151], v[64:67], v[20:23], v[148:151]\n\t" \
  "ds_read_b64 v[14:15], v27 offset:15360\n\t" \
  "v_mfma_f32_16x16x32_bf16 v[152:155], v[76:79], v[16:19], v[152:155]\n\t" \
  "ds_bpermute_b32 v38, v32, v147\n\t" \
  "v_mfma_f32_16x16x32_bf16 v[152:155], v[72:75], v[20:23], v[152:155]\n\t" \
  "v_mov_b32 v147, v146\n\t" \
  "v_mov_b32 v146, v145\n\t" \
  "v_mfma_f32_16x16x32_bf16 v[156:159], v[84:87], v[16:19], v[156:159]\n\t" \
  "ds_read_b64 v[16:17], v26 offset:0\n\t" \
  "ds_read_b64 v[18:19], v27 offset:0\n\t" \
  "v_mfma_f32_16x16x32_bf16 v[156:159], v[80:83], v[20:23], v[156:159]\n\t" \
  "ds_read_b64 v[20:21], v28 offset:1024\n\t" \
  "ds_read_b64 v[22:23], v29 offset:1024\n\t" \
  "s_waitcnt lgkmcnt(9)\n\t" \
  "v_mfma_f32_16x16x32_bf16 v[152:155], v[68:71], v[0:3], v[152:155]\n\t" \
  "ds_bpermute_b32 v39, v32, v151\n\t" \
  "v_mov_b32 v145, v144\n\t" \
  "v_mfma_f32_16x16x32_bf16 v[152:155], v[64:67], v[4:7], v[152:155]\n\t" \
  "v_mov_b32 v151, v150\n\t" \
  "v_mov_b32 v150, v149\n\t" \
  "v_mfma_f32_16x16x32_bf16 v[156:159], v[76:79], v[0:3], v[156:159]\n\t" \
  "ds_read_b64 v[0:1], v30 offset:2048\n\t" \
  "v_mov_b32 v149, v148\n\t" \
  "v_mfma_f32_16x16x32_bf16 v[156:159], v[72:75], v[4:7], v[156:159]\n\t" \
  "ds_read_b64 v[2:3], v31 offset:2048\n\t" \
  "s_waitcnt lgkmcnt(10)\n\t" \
  "v_mfma_f32_16x16x32_bf16 v[156:159], v[68:71], v[8:11], v[156:159]\n\t" \
  "ds_bpermute_b32 v40, v32, v155\n\t" \
  "s_waitcnt lgkmcnt(9)\n\t" \
  "v_mfma_f32_16x16x32_bf16 v[156:159], v[64:67], v[12:15], v[156:159]\n\t" \
  "ds_read_b64 v[4:5], v24 offset:3072\n\t" \
  "s_waitcnt lgkmcnt(7)\n\t" \
  "v_mfma_f32_16x16x32_bf16 v[128:131], v[68:71], v[16:19], v[128:131]\n\t" \
  "ds_read_b64 v[6:7], v25 offset:3072\n\t" \
  "s_waitcnt lgkmcnt(6)\n\t" \
  "v_mfma_f32_16x16x32_bf16 v[128:131], v[64:67], v[20:23], v[128:131]\n\t" \
  "v_cndmask_b32_e64 v144, v38, v37, vcc\n\t" \
  "v_mov_b32 v155, v154\n\t" \
  "v_mfma_f32_16x16x32_bf16 v[132:135], v[76:79], v[16:19], v[132:135]\n\t" \
  "ds_bpermute_b32 v41, v32, v159\n\t" \
  "v_mov_b32 v154, v153\n\t" \
  "v_mfma_f32_16x16x32_bf16 v[132:135], v[72:75], v[20:23], v[132:135]\n\t" \
  "v_mov_b32 v153, v152\n\t" \
  "v_mov_b32 v159, v158\n\t" \
  "v_mfma_f32_16x16x32_bf16 v[136:139], v[84:87], v[16:19], v[136:139]\n\t" \
  "v_mov_b32 v158, v157\n\t" \
  "v_mov_b32 v157, v156\n\t" \
  "v_mfma_f32_16x16x32_bf16 v[136:139], v[80:83], v[20:23], v[136:139]\n\t" \
  "s_waitcnt lgkmcnt(6)\n\t" \
  "v_cndmask_b32_e64 v148, v39, v38, vcc\n\t" \
  "v_mfma_f32_16x16x32_bf16 v[140:143], v[92:95], v[16:19], v[140:143]\n\t" \
  "ds_read_b64 v[8:9], v26 offset:4096\n\t" \
  "v_mfma_f32_16x16x32_bf16 v[140:143], v[88:91], v[20:23], v[140:143]\n\t" \
  "ds_read_b64 v[10:11], v27 offset:4096\n\t" \
  "v_mfma_f32_16x16x32_bf16 v[144:147], v[100:103], v[16:19], v[144:147]\n\t" \
  "s_waitcnt lgkmcnt(5)\n\t" \
  "v_cndmask_b32_e64 v152, v40, v39, vcc\n\t" \
  "v_mfma_f32_16x16x32_bf16 v[144:147], v[96:99], v[20:23], v[144:147]\n\t" \
  "global_load_dwordx4 v[44:47], v43, %[q0] offset:0\n\t" \
  "v_mfma_f32_16x16x32_bf16 v[148:151], v[108:111], v[16:19], v[148:151]\n\t" \
  "s_waitcnt lgkmcnt(2)\n\t" \
  "v_cndmask_b32_e64 v156, v41, v40, vcc\n\t" \
  "v_mfma_f32_16x16x32_bf16 v[148:151], v[104:107], v[20:23], v[148:151]\n\t" \
  "ds_read_b64 v[12:13], v28 offset:5120\n\t" \
  "v_mfma_f32_16x16x32_bf16 v[152:155], v[116:119], v[16:19], v[152:155]\n\t" \
  "ds_read_b64 v[14:15], v29 offset:5120\n\t" \
  "v_mfma_f32_16x16x32_bf16 v[152:155], v[112:115], v[20:23], v[152:155]\n\t" \
  "global_load_dwordx4 v[48:51], v43, %[q0] offset:2048\n\t" \
  "v_mfma_f32_16x16x32_bf16 v[156:159], v[124:127], v[16:19], v[156:159]\n\t" \
  "ds_read_b64 v[16:17], v30 offset:6144\n\t" \
  "v_mfma_f32_16x16x32_bf16 v[156:159], v[120:123], v[20:23], v[156:159]\n\t" \
  "ds_read_b64 v[18:19], v31 offset:6144\n\t" \
  "v_mfma_f32_16x16x32_bf16 v[132:135], v[68:71], v[0:3], v[132:135]\n\t" \
  "global_load_dwordx4 v[52:55], v43, %[q2] offset:0\n\t" \
  "v_mfma_f32_16x16x32_bf16 v[132:135], v[64:67], v[4:7], v[132:135]\n\t" \
  "ds_read_b64 v[20:21], v24 offset:7168\n\t" \
  "v_mfma_f32_16x16x32_bf16 v[136:139], v[76:79], v[0:3], v[136:139]\n\t" \
  "ds_read_b64 v[22:23], v25 offset:7168\n\t" \
  "v_mfma_f32_16x16x32_bf16 v[136:139], v[72:75], v[4:7], v[136:139]\n\t" \
  "ds_bpermute_b32 v34, v32, v131\n\t" \
  "v_mfma_f32_16x16x32_bf16 v[140:143], v[84:87], v[0:3], v[140:143]\n\t" \
  "global_load_dwordx4 v[56:59], v43, %[q2] offset:2048\n\t" \
  "v_mfma_f32_16x16x32_bf16 v[140:143], v[80:83], v[4:7], v[140:143]\n\t" \
  "ds_bpermute_b32 v35, v32, v135\n\t" \
  "v_mov_b32 v131, v130\n\t" \
  "v_mfma_f32_16x16x32_bf16 v[144:147], v[92:95], v[0:3], v[144:147]\n\t" \
  "v_mov_b32 v130, v129\n\t" \
  "v_mov_b32 v129, v128\n\t" \
  "v_mfma_f32_16x16x32_bf16 v[144:147], v[88:91], v[4:7], v[144:147]\n\t" \
  "v_mov_b32 v135, v134\n\t" \
  "v_mov_b32 v134, v133\n\t" \
  "v_mfma_f32_16x16x32_bf16 v[148:151], v[100:103], v[0:3], v[148:151]\n\t" \
  "v_mov_b32 v133, v132\n\t" \
  "v_mfma_f32_16x16x32_bf16 v[148:151], v[96:99], v[4:7], v[148:151]\n\t" \
  "v_mfma_f32_16x16x32_bf16 v[152:155], v[108:111], v[0:3], v[152:155]\n\t" \
  "v_mfma_f32_16x16x32_bf16 v[152:155], v[104:107], v[4:7], v[152:155]\n\t" \
  "v_mfma_f32_16x16x32_bf16 v[156:159], v[116:119], v[0:3], v[156:159]\n\t" \
  "ds_read_b64 v[0:1], v26 offset:8192\n\t" \
  "v_mfma_f32_16x16x32_bf16 v[156:159], v[112:115], v[4:7], v[156:159]\n\t" \
  "ds_read_b64 v[2:3], v27 offset:8192\n\t" \
  "s_waitcnt lgkmcnt(4)\n\t" \
  "v_mfma_f32_16x16x32_bf16 v[136:139], v[68:71], v[8:11], v[136:139]\n\t" \
  "ds_read_b64 v[4:5], v28 offset:9216\n\t" \
  "v_mfma_f32_16x16x32_bf16 v[136:139], v[64:67], v[12:15], v[136:139]\n\t" \
  "ds_read_b64 v[6:7], v29 offset:9216\n\t" \
  "v_mfma_f32_16x16x32_bf16 v[140:143], v[76:79], v[8:11], v[140:143]\n\t" \
  "s_waitcnt lgkmcnt(5)\n\t" \
  "v_cndmask_b32_e64 v128, v34, 0, vcc\n\t" \
  "v_mfma_f32_16x16x32_bf16 v[140:143], v[72:75], v[12:15], v[140:143]\n\t" \
  "s_waitcnt lgkmcnt(4)\n\t" \
  "v_cndmask_b32_e64 v132, v35, v34, vcc\n\t" \
  "v_mfma_f32_16x16x32_bf16 v[144:147], v[84:87], v[8:11], v[144:147]\n\t" \
  "ds_bpermute_b32 v36, v32, v139\n\t" \
  "v_mfma_f32_16x16x32_bf16 v[144:147], v[80:83], v[12:15], v[144:147]\n\t" \
  "v_mov_b32 v139, v138\n\t" \
  "v_mov_b32 v138, v137\n\t" \
  "v_mfma_f32_16x16x32_bf16 v[148:151], v[92:95], v[8:11], v[148:151]\n\t" \
  "v_mov_b32 v137, v136\n\t" \
  "v_mfma_f32_16x16x32_bf16 v[148:151], v[88:91], v[12:15], v[148:151]\n\t" \
  "v_mfma_f32_16x16x32_bf16 v[152:155], v[100:103], v[8:11], v[152:155]\n\t" \
  "v_mfma_f32_16x16x32_bf16 v[152:155], v[96:99], v[12:15], v[152:155]\n\t" \
  "v_mfma_f32_16x16x32_bf16 v[156:159], v[108:111], v[8:11], v[156:159]\n\t" \
  "ds_read_b64 v[8:9], v30 offset:10240\n\t" \
  "v_mfma_f32_16x16x32_bf16 v[156:159], v[104:107], v[12:15], v[156:159]\n\t" \
  "ds_read_b64 v[10:11], v31 offset:10240\n\t" \
  "v_mfma_f32_16x16x32_bf16 v[140:143], v[68:71], v[16:19], v[140:143]\n\t" \
  "ds_read_b64 v[12:13], v24 offset:11264\n\t" \
  "v_mfma_f32_16x16x32_bf16 v[140:143], v[64:67], v[20:23], v[140:143]\n\t" \
  "ds_read_b64 v[14:15], v25 offset:11264\n\t" \
  "v_mfma_f32_16x16x32_bf16 v[144:147], v[76:79], v[16:19], v[144:147]\n\t" \
  "v_mfma_f32_16x16x32_bf16 v[144:147], v[72:75], v[20:23], v[144:147]\n\t" \
  "s_waitcnt lgkmcnt(4)\n\t" \
  "v_cndmask_b32_e64 v136, v36, v35, vcc\n\t" \
  "v_mfma_f32_16x16x32_bf16 v[148:151], v[84:87], v[16:19], v[148:151]\n\t" \
  "v_mfma_f32_16x16x32_bf16 v[148:151], v[80:83], v[20:23], v[148:151]\n\t" \
  "v_mfma_f32_16x16x32_bf16 v[152:155], v[92:95], v[16:19], v[152:155]\n\t" \
  "ds_bpermute_b32 v37, v32, v143\n\t" \
  "v_mfma_f32_16x16x32_bf16 v[152:155], v[88:91], v[20:23], v[152:155]\n\t" \
  "v_mov_b32 v143, v142\n\t" \
  "v_mov_b32 v142, v141\n\t" \
  "v_mfma_f32_16x16x32_bf16 v[156:159], v[100:103], v[16:19], v[156:159]\n\t" \
  "ds_read_b64 v[16:17], v26 offset:12288\n\t" \
  "v_mov_b32 v141, v140\n\t" \
  "v_mfma_f32_16x16x32_bf16 v[156:159], v[96:99], v[20:23], v[156:159]\n\t" \
  "ds_read_b64 v[18:19], v27 offset:12288\n\t" \
  "v_mfma_f32_16x16x32_bf16 v[144:147], v[68:71], v[0:3], v[144:147]\n\t" \
  "ds_read_b64 v[20:21], v28 offset:13312\n\t" \
  "v_mfma_f32_16x16x32_bf16 v[144:147], v[64:67], v[4:7], v[144:147]\n\t" \
  "ds_read_b64 v[22:23], v29 offset:13312\n\t" \
  "v_mfma_f32_16x16x32_bf16 v[148:151], v[76:79], v[0:3], v[148:151]\n\t" \
  "v_mfma_f32_16x16x32_bf16 v[148:151], v[72:75], v[4:7], v[148:151]\n\t" \
  "v_mfma_f32_16x16x32_bf16 v[152:155], v[84:87], v[0:3], v[152:155]\n\t" \
  "v_mfma_f32_16x16x32_bf16 v[152:155], v[80:83], v[4:7], v[152:155]\n\t" \
  "v_mfma_f32_16x16x32_bf16 v[156:159], v[92:95], v[0:3], v[156:159]\n\t" \
  "ds_read_b64 v[0:1], v30 offset:14336\n\t" \
  "v_mfma_f32_16x16x32_bf16 v[156:159], v[88:91], v[4:7], v[156:159]\n\t" \
  "ds_read_b64 v[2:3], v31 offset:14336\n\t" \
  "s_waitcnt lgkmcnt(6)\n\t" \
  "v_mfma_f32_16x16x32_bf16 v[148:151], v[68:71], v[8:11], v[148:151]\n\t" \
  "ds_read_b64 v[4:5], v24 offset:15360\n\t" \
  "v_cndmask_b32_e64 v140, v37, v36, vcc\n\t" \
  "v_mfma_f32_16x16x32_bf16 v[148:151], v[64:67], v[12:15], v[148:151]\n\t" \
  "ds_read_b64 v[6:7], v25 offset:15360\n\t" \
  "v_mfma_f32_16x16x32_bf16 v[152:155], v[76:79], v[8:11], v[152:155]\n\t" \
  "ds_bpermute_b32 v38, v32, v147\n\t" \
  "v_mfma_f32_16x16x32_bf16 v[152:155], v[72:75], v[12:15], v[152:155]\n\t" \
  "v_mov_b32 v147, v146\n\t" \
  "v_mov_b32 v146, v145\n\t" \
  "v_mfma_f32_16x16x32_bf16 v[156:159], v[84:87], v[8:11], v[156:159]\n\t" \
  "ds_read_b64 v[8:9], v24 offset:0\n\t" \
  "ds_read_b64 v[10:11], v25 offset:0\n\t" \
  "v_mfma_f32_16x16x32_bf16 v[156:159], v[80:83], v[12:15], v[156:159]\n\t" \
  "ds_read_b64 v[12:13], v26 offset:1024\n\t" \
  "ds_read_b64 v[14:15], v27 offset:1024\n\t" \
  "s_waitcnt lgkmcnt(9)\n\t" \
  "v_mfma_f32_16x16x32_bf16 v[152:155], v[68:71], v[16:19], v[152:155]\n\t" \
  "ds_bpermute_b32 v39, v32, v151\n\t" \
  "v_mov_b32 v145, v144\n\t" \
  "v_mfma_f32_16x16x32_bf16 v[152:155], v[64:67], v[20:23], v[152:155]\n\t" \
  "v_mov_b32 v151, v150\n\t" \
  "v_mov_b32 v150, v149\n\t" \
  "v_mfma_f32_16x16x32_bf16 v[156:159], v[76:79], v[16:19], v[156:159]\n\t" \
  "ds_read_b64 v[16:17], v28 offset:2048\n\t" \
  "v_mov_b32 v149, v148\n\t" \
  "v_mfma_f32_16x16x32_bf16 v[156:159], v[72:75], v[20:23], v[156:159]\n\t" \
  "ds_read_b64 v[18:19], v29 offset:2048\n\t" \
  "s_waitcnt lgkmcnt(10)\n\t" \
  "v_mfma_f32_16x16x32_bf16 v[156:159], v[68:71], v[0:3], v[156:159]\n\t" \
  "ds_bpermute_b32 v40, v32, v155\n\t" \
  "s_waitcnt lgkmcnt(9)\n\t" \
  "v_mfma_f32_16x16x32_bf16 v[156:159], v[64:67], v[4:7], v[156:159]\n\t" \
  "ds_read_b64 v[20:21], v30 offset:3072\n\t" \
  "s_waitcnt lgkmcnt(7)\n\t" \
  "v_mfma_f32_16x16x32_bf16 v[128:131], v[68:71], v[8:11], v[128:131]\n\t" \
  "ds_read_b64 v[22:23], v31 offset:3072\n\t" \
  "s_waitcnt lgkmcnt(6)\n\t" \
  "v_mfma_f32_16x16x32_bf16 v[128:131], v[64:67], v[12:15], v[128:131]\n\t" \
  "v_cndmask_b32_e64 v144, v38, v37, vcc\n\t" \
  "v_mov_b32 v155, v154\n\t" \
  "v_mfma_f32_16x16x32_bf16 v[132:135], v[76:79], v[8:11], v[132:135]\n\t" \
  "ds_bpermute_b32 v41, v32, v159\n\t" \
  "v_mov_b32 v154, v153\n\t" \
  "v_mfma_f32_16x16x32_bf16 v[132:135], v[72:75], v[12:15], v[132:135]\n\t" \
  "v_mov_b32 v153, v152\n\t" \
  "v_mov_b32 v159, v158\n\t" \
  "v_mfma_f32_16x16x32_bf16 v[136:139], v[84:87], v[8:11], v[136:139]\n\t" \
  "v_mov_b32 v158, v157\n\t" \
  "v_mov_b32 v157, v156\n\t" \
  "v_mfma_f32_16x16x32_bf16 v[136:139], v[80:83], v[12:15], v[136:139]\n\t" \
  "s_waitcnt lgkmcnt(6)\n\t" \
  "v_cndmask_b32_e64 v148, v39, v38, vcc\n\t" \
  "v_mfma_f32_16x16x32_bf16 v[140:143], v[92:95], v[8:11], v[140:143]\n\t" \
  "ds_read_b64 v[0:1], v24 offset:4096\n\t" \
  "v_mfma_f32_16x16x32_bf16 v[140:143], v[88:91], v[12:15], v[140:143]\n\t" \
  "ds_read_b64 v[2:3], v25 offset:4096\n\t" \
  "v_mfma_f32_16x16x32_bf16 v[144:147], v[100:103], v[8:11], v[144:147]\n\t" \
  "s_waitcnt lgkmcnt(5)\n\t" \
  "v_cndmask_b32_e64 v152, v40, v39, vcc\n\t" \
  "v_mfma_f32_16x16x32_bf16 v[144:147], v[96:99], v[12:15], v[144:147]\n\t" \
  "ds_read_b64 v[4:5], v26 offset:5120\n\t" \
  "v_mfma_f32_16x16x32_bf16 v[148:151], v[108:111], v[8:11], v[148:151]\n\t" \
  "s_waitcnt lgkmcnt(3)\n\t" \
  "v_cndmask_b32_e64 v156, v41, v40, vcc\n\t" \
  "v_mfma_f32_16x16x32_bf16 v[148:151], v[104:107], v[12:15], v[148:151]\n\t" \
  "ds_read_b64 v[6:7], v27 offset:5120\n\t" \
  "v_mfma_f32_16x16x32_bf16 v[152:155], v[116:119], v[8:11], v[152:155]\n\t" \
  "ds_bpermute_b32 v34, v32, v131\n\t" \
  "v_mfma_f32_16x16x32_bf16 v[152:155], v[112:115], v[12:15], v[152:155]\n\t" \
  "v_mov_b32 v131, v130\n\t" \
  "v_mov_b32 v130, v129\n\t" \
  "v_mfma_f32_16x16x32_bf16 v[156:159], v[124:127], v[8:11], v[156:159]\n\t" \
  "ds_read_b64 v[8:9], v28 offset:6144\n\t" \
  "v_mov_b32 v129, v128\n\t" \
  "v_mfma_f32_16x16x32_bf16 v[156:159], v[120:123], v[12:15], v[156:159]\n\t" \
  "ds_read_b64 v[10:11], v29 offset:6144\n\t" \
  "v_mfma_f32_16x16x32_bf16 v[132:135], v[68:71], v[16:19], v[132:135]\n\t" \
  "ds_read_b64 v[12:13], v30 offset:7168\n\t" \
  "v_mfma_f32_16x16x32_bf16 v[132:135], v[64:67], v[20:23], v[132:135]\n\t" \
  "ds_read_b64 v[14:15], v31 offset:7168\n\t" \
  "v_mfma_f32_16x16x32_bf16 v[136:139], v[76:79], v[16:19], v[136:139]\n\t" \
  "v_mfma_f32_16x16x32_bf16 v[136:139], v[72:75], v[20:23], v[136:139]\n\t" \
  "v_mfma_f32_16x16x32_bf16 v[140:143], v[84:87], v[16:19], v[140:143]\n\t" \
  "v_mfma_f32_16x16x32_bf16 v[140:143], v[80:83], v[20:23], v[140:143]\n\t" \
  "v_mfma_f32_16x16x32_bf16 v[144:147], v[92:95], v[16:19], v[144:147]\n\t" \
  "v_mfma_f32_16x16x32_bf16 v[144:147], v[88:91], v[20:23], v[144:147]\n\t" \
  "s_waitcnt lgkmcnt(4)\n\t" \
  "v_cndmask_b32_e64 v128, v34, 0, vcc\n\t" \
  "v_mfma_f32_16x16x32_bf16 v[148:151], v[100:103], v[16:19], v[148:151]\n\t" \
  "ds_bpermute_b32 v35, v32, v135\n\t" \
  "v_mfma_f32_16x16x32_bf16 v[148:151], v[96:99], v[20:23], v[148:151]\n\t" \
  "v_mov_b32 v135, v134\n\t" \
  "v_mov_b32 v134, v133\n\t" \
  "v_mfma_f32_16x16x32_bf16 v[152:155], v[108:111], v[16:19], v[152:155]\n\t" \
  "v_mov_b32 v133, v132\n\t" \
  "v_mfma_f32_16x16x32_bf16 v[152:155], v[104:107], v[20:23], v[152:155]\n\t" \
  "v_mfma_f32_16x16x32_bf16 v[156:159], v[116:119], v[16:19], v[156:159]\n\t" \
  "ds_read_b64 v[16:17], v24 offset:8192\n\t" \
  "v_mfma_f32_16x16x32_bf16 v[156:159], v[112:115], v[20:23], v[156:159]\n\t" \
  "ds_read_b64 v[18:19], v25 offset:8192\n\t" \
  "v_mfma_f32_16x16x32_bf16 v[136:139], v[68:71], v[0:3], v[136:139]\n\t" \
  "ds_read_b64 v[20:21], v26 offset:9216\n\t" \
  "v_mfma_f32_16x16x32_bf16 v[136:139], v[64:67], v[4:7], v[136:139]\n\t" \
  "ds_read_b64 v[22:23], v27 offset:9216\n\t" \
  "v_mfma_f32_16x16x32_bf16 v[140:143], v[76:79], v[0:3], v[140:143]\n\t" \
  "v_mfma_f32_16x16x32_bf16 v[140:143], v[72:75], v[4:7], v[140:143]\n\t" \
  "v_mfma_f32_16x16x32_bf16 v[144:147], v[84:87], v[0:3], v[144:147]\n\t" \
  "v_mfma_f32_16x16x32_bf16 v[144:147], v[80:83], v[4:7], v[144:147]\n\t" \
  "s_waitcnt lgkmcnt(4)\n\t" \
  "v_cndmask_b32_e64 v132, v35, v34, vcc\n\t" \
  "v_mfma_f32_16x16x32_bf16 v[148:151], v[92:95], v[0:3], v[148:151]\n\t" \
  "ds_bpermute_b32 v36, v32, v139\n\t" \
  "v_mfma_f32_16x16x32_bf16 v[148:151], v[88:91], v[4:7], v[148:151]\n\t" \
  "v_mov_b32 v139, v138\n\t" \
  "v_mov_b32 v138, v137\n\t" \
  "v_mfma_f32_16x16x32_bf16 v[152:155], v[100:103], v[0:3], v[152:155]\n\t" \
  "v_mov_b32 v137, v136\n\t" \
  "v_mfma_f32_16x16x32_bf16 v[152:155], v[96:99], v[4:7], v[152:155]\n\t" \
  "v_mfma_f32_16x16x32_bf16 v[156:159], v[108:111], v[0:3], v[156:159]\n\t" \
  "ds_read_b64 v[0:1], v28 offset:10240\n\t" \
  "v_mfma_f32_16x16x32_bf16 v[156:159], v[104:107], v[4:7], v[156:159]\n\t" \
  "ds_read_b64 v[2:3], v29 offset:10240\n\t" \
  "v_mfma_f32_16x16x32_bf16 v[140:143], v[68:71], v[8:11], v[140:143]\n\t" \
  "ds_read_b64 v[4:5], v30 offset:11264\n\t" \
  "v_mfma_f32_16x16x32_bf16 v[140:143], v[64:67], v[12:15], v[140:143]\n\t" \
  "ds_read_b64 v[6:7], v31 offset:11264\n\t" \
  "v_mfma_f32_16x16x32_bf16 v[144:147], v[76:79], v[8:11], v[144:147]\n\t" \
  "v_mfma_f32_16x16x32_bf16 v[144:147], v[72:75], v[12:15], v[144:147]\n\t" \
  "v_mfma_f32_16x16x32_bf16 v[148:151], v[84:87], v[8:11], v[148:151]\n\t" \
  "v_mfma_f32_16x16x32_bf16 v[148:151], v[80:83], v[12:15], v[148:151]\n\t" \
  "s_waitcnt lgkmcnt(4)\n\t" \
  "v_cndmask_b32_e64 v136, v36, v35, vcc\n\t" \
  "v_mfma_f32_16x16x32_bf16 v[152:155], v[92:95], v[8:11], v[152:155]\n\t" \
  "ds_bpermute_b32 v37, v32, v143\n\t" \
  "v_mfma_f32_16x16x32_bf16 v[152:155], v[88:91], v[12:15], v[152:155]\n\t" \
  "v_mov_b32 v143, v142\n\t" \
  "v_mov_b32 v142, v141\n\t" \
  "v_mfma_f32_16x16x32_bf16 v[156:159], v[100:103], v[8:11], v[156:159]\n\t" \
  "ds_read_b64 v[8:9], v24 offset:12288\n\t" \
  "v_mov_b32 v141, v140\n\t" \
  "v_mfma_f32_16x16x32_bf16 v[156:159], v[96:99], v[12:15], v[156:159]\n\t" \
  "ds_read_b64 v[10:11], v25 offset:12288\n\t" \
  "v_mfma_f32_16x16x32_bf16 v[144:147], v[68:71], v[16:19], v[144:147]\n\t" \
  "ds_read_b64 v[12:13], v26 offset:13312\n\t" \
  "v_mfma_f32_16x16x32_bf16 v[144:147], v[64:67], v[20:23], v[144:147]\n\t" \
  "ds_read_b64 v[14:15], v27 offset:13312\n\t" \
  "v_mfma_f32_16x16x32_bf16 v[148:151], v[76:79], v[16:19], v[148:151]\n\t" \
  "v_mfma_f32_16x16x32_bf16 v[148:151], v[72:75], v[20:23], v[148:151]\n\t" \
  "v_mfma_f32_16x16x32_bf16 v[152:155], v[84:87], v[16:19], v[152:155]\n\t" \
  "v_mfma_f32_16x16x32_bf16 v[152:155], v[80:83], v[20:23], v[152:155]\n\t" \
  "v_mfma_f32_16x16x32_bf16 v[156:159], v[92:95], v[16:19], v[156:159]\n\t" \
  "ds_read_b64 v[16:17], v28 offset:14336\n\t" \
  "v_mfma_f32_16x16x32_bf16 v[156:159], v[88:91], v[20:23], v[156:159]\n\t" \
  "ds_read_b64 v[18:19], v29 offset:14336\n\t" \
  "s_waitcnt lgkmcnt(6)\n\t" \
  "v_mfma_f32_16x16x32_bf16 v[148:151], v[68:71], v[0:3], v[148:151]\n\t" \
  "ds_read_b64 v[20:21], v30 offset:15360\n\t" \
  "v_cndmask_b32_e64 v140, v37, v36, vcc\n\t" \
  "v_mfma_f32_16x16x32_bf16 v[148:151], v[64:67], v[4:7], v[148:151]\n\t" \
  "ds_read_b64 v[22:23], v31 offset:15360\n\t" \
  "v_mfma_f32_16x16x32_bf16 v[152:155], v[76:79], v[0:3], v[152:155]\n\t" \
  "ds_bpermute_b32 v38, v32, v147\n\t" \
  "v_mfma_f32_16x16x32_bf16 v[152:155], v[72:75], v[4:7], v[152:155]\n\t" \
  "v_mov_b32 v147, v146\n\t" \
  "v_mov_b32 v146, v145\n\t" \
  "v_mfma_f32_16x16x32_bf16 v[156:159], v[84:87], v[0:3], v[156:159]\n\t" \
  "ds_read_b64 v[0:1], v30 offset:0\n\t" \
  "ds_read_b64 v[2:3], v31 offset:0\n\t" \
  "v_mfma_f32_16x16x32_bf16 v[156:159], v[80:83], v[4:7], v[156:159]\n\t" \
  "ds_read_b64 v[4:5], v24 offset:1024\n\t" \
  "ds_read_b64 v[6:7], v25 offset:1024\n\t" \
  "s_waitcnt lgkmcnt(9)\n\t" \
  "v_mfma_f32_16x16x32_bf16 v[152:155], v[68:71], v[8:11], v[152:155]\n\t" \
  "ds_bpermute_b32 v39, v32, v151\n\t" \
  "v_mov_b32 v145, v144\n\t" \
  "v_mfma_f32_16x16x32_bf16 v[152:155], v[64:67], v[12:15], v[152:155]\n\t" \
  "v_mov_b32 v151, v150\n\t" \
  "v_mov_b32 v150, v149\n\t" \
  "v_mfma_f32_16x16x32_bf16 v[156:159], v[76:79], v[8:11], v[156:159]\n\t" \
  "ds_read_b64 v[8:9], v26 offset:2048\n\t" \
  "v_mov_b32 v149, v148\n\t" \
  "v_mfma_f32_16x16x32_bf16 v[156:159], v[72:75], v[12:15], v[156:159]\n\t" \
  "ds_read_b64 v[10:11], v27 offset:2048\n\t" \
  "s_waitcnt lgkmcnt(10)\n\t" \
  "v_mfma_f32_16x16x32_bf16 v[156:159], v[68:71], v[16:19], v[156:159]\n\t" \
  "ds_bpermute_b32 v40, v32, v155\n\t" \
  "s_waitcnt lgkmcnt(9)\n\t" \
  "v_mfma_f32_16x16x32_bf16 v[156:159], v[64:67], v[20:23], v[156:159]\n\t" \
  "ds_read_b64 v[12:13], v28 offset:3072\n\t" \
  "s_waitcnt lgkmcnt(7)\n\t" \
  "v_mfma_f32_16x16x32_bf16 v[128:131], v[68:71], v[0:3], v[128:131]\n\t" \
  "ds_read_b64 v[14:15], v29 offset:3072\n\t" \
  "s_waitcnt lgkmcnt(6)\n\t" \
  "v_mfma_f32_16x16x32_bf16 v[128:131], v[64:67], v[4:7], v[128:131]\n\t" \
  "v_cndmask_b32_e64 v144, v38, v37, vcc\n\t" \
  "v_mov_b32 v155, v154\n\t" \
  "v_mfma_f32_16x16x32_bf16 v[132:135], v[76:79], v[0:3], v[132:135]\n\t" \
  "ds_bpermute_b32 v41, v32, v159\n\t" \
  "v_mov_b32 v154, v153\n\t" \
  "v_mfma_f32_16x16x32_bf16 v[132:135], v[72:75], v[4:7], v[132:135]\n\t" \
  "v_mov_b32 v153, v152\n\t" \
  "v_mov_b32 v159, v158\n\t" \
  "v_mfma_f32_16x16x32_bf16 v[136:139], v[84:87], v[0:3], v[136:139]\n\t" \
  "v_mov_b32 v158, v157\n\t" \
  "v_mov_b32 v157, v156\n\t" \
  "v_mfma_f32_16x16x32_bf16 v[136:139], v[80:83], v[4:7], v[136:139]\n\t" \
  "s_waitcnt lgkmcnt(6)\n\t" \
  "v_cndmask_b32_e64 v148, v39, v38, vcc\n\t" \
  "v_mfma_f32_16x16x32_bf16 v[140:143], v[92:95], v[0:3], v[140:143]\n\t" \
  "ds_read_b64 v[16:17], v30 offset:4096\n\t" \
  "v_mfma_f32_16x16x32_bf16 v[140:143], v[88:91], v[4:7], v[140:143]\n\t" \
  "ds_read_b64 v[18:19], v31 offset:4096\n\t" \
  "v_mfma_f32_16x16x32_bf16 v[144:147], v[100:103], v[0:3], v[144:147]\n\t" \
  "s_waitcnt lgkmcnt(5)\n\t" \
  "v_cndmask_b32_e64 v152, v40, v39, vcc\n\t" \
  "v_mfma_f32_16x16x32_bf16 v[144:147], v[96:99], v[4:7], v[144:147]\n\t" \
  "ds_read_b64 v[20:21], v24 offset:5120\n\t" \
  "v_mfma_f32_16x16x32_bf16 v[148:151], v[108:111], v[0:3], v[148:151]\n\t" \
  "s_waitcnt lgkmcnt(3)\n\t" \
  "v_cndmask_b32_e64 v156, v41, v40, vcc\n\t" \
  "v_mfma_f32_16x16x32_bf16 v[148:151], v[104:107], v[4:7], v[148:151]\n\t" \
  "v_cvt_pk_bf16_f32 v34, v128, v129\n\t" \
  "v_cvt_pk_bf16_f32 v35, v130, v131\n\t" \
  "v_mfma_f32_16x16x32_bf16 v[152:155], v[116:119], v[0:3], v[152:155]\n\t" \
  "ds_read_b64 v[22:23], v25 offset:5120\n\t" \
  "v_mfma_f32_16x16x32_bf16 v[152:155], v[112:115], v[4:7], v[152:155]\n\t" \
  "v_mfma_f32_16x16x32_bf16 v[156:159], v[124:127], v[0:3], v[156:159]\n\t" \
  "ds_read_b64 v[0:1], v26 offset:6144\n\t" \
  "v_mfma_f32_16x16x32_bf16 v[156:159], v[120:123], v[4:7], v[156:159]\n\t" \
  "ds_read_b64 v[2:3], v27 offset:6144\n\t" \
  "v_mfma_f32_16x16x32_bf16 v[132:135], v[68:71], v[8:11], v[132:135]\n\t" \
  "ds_read_b64 v[4:5], v28 offset:7168\n\t" \
  "v_mfma_f32_16x16x32_bf16 v[132:135], v[64:67], v[12:15], v[132:135]\n\t" \
  "ds_read_b64 v[6:7], v29 offset:7168\n\t" \
  "v_mfma_f32_16x16x32_bf16 v[136:139], v[76:79], v[8:11], v[136:139]\n\t" \
  "v_mfma_f32_16x16x32_bf16 v[136:139], v[72:75], v[12:15], v[136:139]\n\t" \
  "v_mfma_f32_16x16x32_bf16 v[140:143], v[84:87], v[8:11], v[140:143]\n\t" \
  "v_mfma_f32_16x16x32_bf16 v[140:143], v[80:83], v[12:15], v[140:143]\n\t" \
  "v_mfma_f32_16x16x32_bf16 v[144:147], v[92:95], v[8:11], v[144:147]\n\t" \
  "v_mfma_f32_16x16x32_bf16 v[144:147], v[88:91], v[12:15], v[144:147]\n\t" \
  "v_mfma_f32_16x16x32_bf16 v[148:151], v[100:103], v[8:11], v[148:151]\n\t" \
  "v_cvt_pk_bf16_f32 v36, v132, v133\n\t" \
  "v_cvt_pk_bf16_f32 v37, v134, v135\n\t" \
  "v_mfma_f32_16x16x32_bf16 v[148:151], v[96:99], v[12:15], v[148:151]\n\t" \
  "v_mfma_f32_16x16x32_bf16 v[152:155], v[108:111], v[8:11], v[152:155]\n\t" \
  "v_permlane16_swap_b32 v34, v36\n\t" \
  "v_permlane16_swap_b32 v35, v37\n\t" \
  "v_mfma_f32_16x16x32_bf16 v[152:155], v[104:107], v[12:15], v[152:155]\n\t" \
  "global_store_dwordx4 v42, v[34:37], %[yb] offset:0 sc1\n\t" \
  "v_mfma_f32_16x16x32_bf16 v[156:159], v[116:119], v[8:11], v[156:159]\n\t" \
  "ds_read_b64 v[8:9], v30 offset:8192\n\t" \
  "v_mfma_f32_16x16x32_bf16 v[156:159], v[112:115], v[12:15], v[156:159]\n\t" \
  "ds_read_b64 v[10:11], v31 offset:8192\n\t" \
  "s_waitcnt lgkmcnt(2)\n\t" \
  "v_mfma_f32_16x16x32_bf16 v[136:139], v[68:71], v[16:19], v[136:139]\n\t" \
  "ds_read_b64 v[12:13], v24 offset:9216\n\t" \
  "v_mfma_f32_16x16x32_bf16 v[136:139], v[64:67], v[20:23], v[136:139]\n\t" \
  "ds_read_b64 v[14:15], v25 offset:9216\n\t" \
  "v_mfma_f32_16x16x32_bf16 v[140:143], v[76:79], v[16:19], v[140:143]\n\t" \
  "v_mfma_f32_16x16x32_bf16 v[140:143], v[72:75], v[20:23], v[140:143]\n\t" \
  "v_mfma_f32_16x16x32_bf16 v[144:147], v[84:87], v[16:19], v[144:147]\n\t" \
  "s_mov_b64 exec, 1\n\t" \
  "ds_write_b32 v33, v33\n\t" \
  "s_mov_b64 exec, -1\n\t" \
  "v_mfma_f32_16x16x32_bf16 v[144:147], v[80:83], v[20:23], v[144:147]\n\t" \
  "v_cvt_pk_bf16_f32 v38, v136, v137\n\t" \
  "v_cvt_pk_bf16_f32 v39, v138, v139\n\t" \
  "v_mfma_f32_16x16x32_bf16 v[148:151], v[92:95], v[16:19], v[148:151]\n\t" \
  "v_mfma_f32_16x16x32_bf16 v[148:151], v[88:91], v[20:23], v[148:151]\n\t" \
  "v_mfma_f32_16x16x32_bf16 v[152:155], v[100:103], v[16:19], v[152:155]\n\t" \
  "v_mfma_f32_16x16x32_bf16 v[152:155], v[96:99], v[20:23], v[152:155]\n\t" \
  "v_mfma_f32_16x16x32_bf16 v[156:159], v[108:111], v[16:19], v[156:159]\n\t" \
  "ds_read_b64 v[16:17], v26 offset:10240\n\t" \
  "v_mfma_f32_16x16x32_bf16 v[156:159], v[104:107], v[20:23], v[156:159]\n\t" \
  "ds_read_b64 v[18:19], v27 offset:10240\n\t" \
  "v_mfma_f32_16x16x32_bf16 v[140:143], v[68:71], v[0:3], v[140:143]\n\t" \
  "ds_read_b64 v[20:21], v28 offset:11264\n\t" \
  "v_mfma_f32_16x16x32_bf16 v[140:143], v[64:67], v[4:7], v[140:143]\n\t" \
  "ds_read_b64 v[22:23], v29 offset:11264\n\t" \
  "v_mfma_f32_16x16x32_bf16 v[144:147], v[76:79], v[0:3], v[144:147]\n\t" \
  "v_mfma_f32_16x16x32_bf16 v[144:147], v[72:75], v[4:7], v[144:147]\n\t" \
  "v_mfma_f32_16x16x32_bf16 v[148:151], v[84:87], v[0:3], v[148:151]\n\t" \
  "v_mfma_f32_16x16x32_bf16 v[148:151], v[80:83], v[4:7], v[148:151]\n\t" \
  "v_mfma_f32_16x16x32_bf16 v[152:155], v[92:95], v[0:3], v[152:155]\n\t" \
  "v_mfma_f32_16x16x32_bf16 v[152:155], v[88:91], v[4:7], v[152:155]\n\t" \
  "v_mfma_f32_16x16x32_bf16 v[156:159], v[100:103], v[0:3], v[156:159]\n\t" \
  "v_cvt_pk_bf16_f32 v40, v140, v141\n\t" \
  "v_cvt_pk_bf16_f32 v41, v142, v143\n\t" \
  "v_mfma_f32_16x16x32_bf16 v[156:159], v[96:99], v[4:7], v[156:159]\n\t" \
  "ds_read_b64 v[0:1], v30 offset:12288\n\t" \
  "s_waitcnt lgkmcnt(6)\n\t" \
  "v_mfma_f32_16x16x32_bf16 v[144:147], v[68:71], v[8:11], v[144:147]\n\t" \
  "v_permlane16_swap_b32 v38, v40\n\t" \
  "ds_read_b64 v[2:3], v31 offset:12288\n\t" \
  "v_mfma_f32_16x16x32_bf16 v[144:147], v[64:67], v[12:15], v[144:147]\n\t" \
  "v_permlane16_swap_b32 v39, v41\n\t" \
  "ds_read_b64 v[4:5], v24 offset:13312\n\t" \
  "v_mfma_f32_16x16x32_bf16 v[148:151], v[76:79], v[8:11], v[148:151]\n\t" \
  "ds_read_b64 v[6:7], v25 offset:13312\n\t" \
  "v_mfma_f32_16x16x32_bf16 v[148:151], v[72:75], v[12:15], v[148:151]\n\t" \
  "global_store_dwordx4 v42, v[38:41], %[yb] offset:1024 sc1\n\t" \
  "v_mfma_f32_16x16x32_bf16 v[152:155], v[84:87], v[8:11], v[152:155]\n\t" \
  "v_mfma_f32_16x16x32_bf16 v[152:155], v[80:83], v[12:15], v[152:155]\n\t" \
  "v_cvt_pk_bf16_f32 v34, v144, v145\n\t" \
  "v_cvt_pk_bf16_f32 v35, v146, v147\n\t" \
  "v_mfma_f32_16x16x32_bf16 v[156:159], v[92:95], v[8:11], v[156:159]\n\t" \
  "ds_read_b64 v[8:9], v26 offset:14336\n\t" \
  "v_mfma_f32_16x16x32_bf16 v[156:159], v[88:91], v[12:15], v[156:159]\n\t" \
  "ds_read_b64 v[10:11], v27 offset:14336\n\t" \
  "s_waitcnt lgkmcnt(6)\n\t" \
  "v_mfma_f32_16x16x32_bf16 v[148:151], v[68:71], v[16:19], v[148:151]\n\t" \
  "ds_read_b64 v[12:13], v28 offset:15360\n\t" \
  "v_mfma_f32_16x16x32_bf16 v[148:151], v[64:67], v[20:23], v[148:151]\n\t" \
  "ds_read_b64 v[14:15], v29 offset:15360\n\t" \
  "v_mfma_f32_16x16x32_bf16 v[152:155], v[76:79], v[16:19], v[152:155]\n\t" \
  "v_mfma_f32_16x16x32_bf16 v[152:155], v[72:75], v[20:23], v[152:155]\n\t" \
  "v_mfma_f32_16x16x32_bf16 v[156:159], v[84:87], v[16:19], v[156:159]\n\t" \
  "v_mfma_f32_16x16x32_bf16 v[156:159], v[80:83], v[20:23], v[156:159]\n\t" \
  "s_waitcnt lgkmcnt(4)\n\t" \
  "v_mfma_f32_16x16x32_bf16 v[152:155], v[68:71], v[0:3], v[152:155]\n\t" \
  "v_mfma_f32_16x16x32_bf16 v[152:155], v[64:67], v[4:7], v[152:155]\n\t" \
  "v_cvt_pk_bf16_f32 v36, v148, v149\n\t" \
  "v_cvt_pk_bf16_f32 v37, v150, v151\n\t" \
  "v_mfma_f32_16x16x32_bf16 v[156:159], v[76:79], v[0:3], v[156:159]\n\t" \
  "v_mfma_f32_16x16x32_bf16 v[156:159], v[72:75], v[4:7], v[156:159]\n\t" \
  "v_permlane16_swap_b32 v34, v36\n\t" \
  "s_waitcnt lgkmcnt(2)\n\t" \
  "v_mfma_f32_16x16x32_bf16 v[156:159], v[68:71], v[8:11], v[156:159]\n\t" \
  "v_permlane16_swap_b32 v35, v37\n\t" \
  "s_waitcnt lgkmcnt(0)\n\t" \
  "v_mfma_f32_16x16x32_bf16 v[156:159], v[64:67], v[12:15], v[156:159]\n\t" \
  "global_store_dwordx4 v42, v[34:37], %[yb] offset:2048 sc1\n\t" \
  "s_waitcnt lgkmcnt(0)\n\t" \
  "s_nop 4\n\t" \
  "v_cvt_pk_bf16_f32 v38, v152, v153\n\t" \
  "v_cvt_pk_bf16_f32 v39, v154, v155\n\t" \
  "v_cvt_pk_bf16_f32 v40, v156, v157\n\t" \
  "v_cvt_pk_bf16_f32 v41, v158, v159\n\t" \
  "s_nop 1\n\t" \
  "v_permlane16_swap_b32 v38, v40\n\t" \
  "v_permlane16_swap_b32 v39, v41\n\t" \
  "global_store_dwordx4 v42, v[38:41], %[yb] offset:3072 sc1\n\t" \
  "s_nop 1\n\t"
#define CV_Q 1
#define CV_CLOB "vcc", "v0", "v1", "v2", "v3", "v4", "v5", "v6", "v7", "v8", "v9", "v10", "v11", "v12", "v13", "v14", "v15", "v16", "v17", "v18", "v19", "v20", "v21", "v22", "v23", "v34", "v35", "v36", "v37", "v38", "v39"

#define G3_ASM \
  "v_readlane_b32 s64, v96, 0\n\t" \
  "v_readlane_b32 s65, v96, 1\n\t" \
  "v_readlane_b32 s66, v96, 2\n\t" \
  "v_readlane_b32 s67, v96, 3\n\t" \
  "v_readlane_b32 s68, v96, 4\n\t" \
  "v_readlane_b32 s69, v96, 5\n\t" \
  "v_readlane_b32 s70, v96, 6\n\t" \
  "v_readlane_b32 s71, v96, 7\n\t" \
  "v_readlane_b32 s72, v96, 8\n\t" \
  "v_readlane_b32 s73, v96, 9\n\t" \
  "v_readlane_b32 s74, v96, 10\n\t" \
  "v_readlane_b32 s75, v96, 11\n\t" \
  "v_readlane_b32 s76, v96, 12\n\t" \
  "v_readlane_b32 s77, v96, 13\n\t" \
  "v_readlane_b32 s78, v96, 14\n\t" \
  "v_readlane_b32 s79, v96, 15\n\t" \
  "v_add_u32 v97, %[go], v165\n\t" \
  "s_branch QG_%=\n\t" \
  "QA14_%=:\n\t" \
  "v_mov_b32 v165, %[sy]\n\t" \
  "s_sleep 1\n\t" \
  "ds_read_b32 v165, v165\n\t" \
  "s_addk_i32 %[cn], 1\n\t" \
  "s_cmp_gt_u32 %[cn], 0x400000\n\t" \
  "s_cbranch_scc1 QC14_%=\n\t" \
  "s_waitcnt lgkmcnt(0)\n\t" \
  "v_cmp_lt_u32 vcc, 15, v165\n\t" \
  "s_cbranch_vccz QA14_%=\n\t" \
  "QC14_%=:\n\t" \
  "s_waitcnt lgkmcnt(0)\n\t" \
  "s_branch QB14_%=\n\t" \
  "QA7_%=:\n\t" \
  "v_mov_b32 v165, %[sy]\n\t" \
  "s_sleep 1\n\t" \
  "ds_read_b32 v165, v165\n\t" \
  "s_addk_i32 %[cn], 1\n\t" \
  "s_cmp_gt_u32 %[cn], 0x400000\n\t" \
  "s_cbranch_scc1 QC7_%=\n\t" \
  "s_waitcnt lgkmcnt(0)\n\t" \
  "v_cmp_lt_u32 vcc, 13, v165\n\t" \
  "s_cbranch_vccz QA7_%=\n\t" \
  "QC7_%=:\n\t" \
  "s_waitcnt lgkmcnt(0)\n\t" \
  "s_branch QB7_%=\n\t" \
  "QS_%=:\n\t" \
  "s_sleep 1\n\t" \
  "QG_%=:\n\t" \
  "ds_read_b32 v98, v165\n\t" \
  "ds_read_b32 v99, v97\n\t" \
  "s_addk_i32 %[cn], 1\n\t" \
  "s_cmp_gt_u32 %[cn], 0x400000\n\t" \
  "s_cselect_b32 %[t2], 16, 0\n\t" \
  "s_waitcnt lgkmcnt(0)\n\t" \
  "v_readfirstlane_b32 %[t0], v98\n\t" \
  "v_readfirstlane_b32 %[t1], v99\n\t" \
  "s_max_u32 %[t0], %[t0], %[t2]\n\t" \
  "s_or_b32 %[t1], %[t1], %[t2]\n\t" \
  "s_min_u32 %[t2], %[t0], 5\n\t" \
  "s_cmp_ge_u32 %[iss], %[t2]\n\t" \
  "s_cbranch_scc1 QD_%=\n\t" \
  "s_cmp_eq_u32 %[iss], 1\n\t" \
  "s_cbranch_scc1 QL1_%=\n\t" \
  "s_cmp_eq_u32 %[iss], 2\n\t" \
  "s_cbranch_scc1 QL2_%=\n\t" \
  "s_cmp_eq_u32 %[iss], 3\n\t" \
  "s_cbranch_scc1 QL3_%=\n\t" \
  "s_cmp_eq_u32 %[iss], 4\n\t" \
  "s_cbranch_scc1 QL4_%=\n\t" \
  "QL0_%=:\n\t" \
  "buffer_load_dwordx4 v[0:3], v164, %[ob], s64 offen offset:0 sc1\n\t" \
  "buffer_load_dwordx4 v[4:7], v164, %[ob], s64 offen offset:1024 sc1\n\t" \
  "buffer_load_dwordx4 v[8:11], v164, %[ob], s64 offen offset:2048 sc1\n\t" \
  "buffer_load_dwordx4 v[12:15], v164, %[ob], s64 offen offset:3072 sc1\n\t" \
  "s_mov_b32 %[iss], 1\n\t" \
  "s_cmp_le_u32 %[t2], 1\n\t" \
  "s_cbranch_scc1 QD_%=\n\t" \
  "QL1_%=:\n\t" \
  "buffer_load_dwordx4 v[16:19], v164, %[ob], s65 offen offset:0 sc1\n\t" \
  "buffer_load_dwordx4 v[20:23], v164, %[ob], s65 offen offset:1024 sc1\n\t" \
  "buffer_load_dwordx4 v[24:27], v164, %[ob], s65 offen offset:2048 sc1\n\t" \
  "buffer_load_dwordx4 v[28:31], v164, %[ob], s65 offen offset:3072 sc1\n\t" \
  "s_mov_b32 %[iss], 2\n\t" \
  "s_cmp_le_u32 %[t2], 2\n\t" \
  "s_cbranch_scc1 QD_%=\n\t" \
  "QL2_%=:\n\t" \
  "buffer_load_dwordx4 v[32:35], v164, %[ob], s66 offen offset:0 sc1\n\t" \
  "buffer_load_dwordx4 v[36:39], v164, %[ob], s66 offen offset:1024 sc1\n\t" \
  "buffer_load_dwordx4 v[40:43], v164, %[ob], s66 offen offset:2048 sc1\n\t" \
  "buffer_load_dwordx4 v[44:47], v164, %[ob], s66 offen offset:3072 sc1\n\t" \
  "s_mov_b32 %[iss], 3\n\t" \
  "s_cmp_le_u32 %[t2], 3\n\t" \
  "s_cbranch_scc1 QD_%=\n\t" \
  "QL3_%=:\n\t" \
  "buffer_load_dwordx4 v[48:51], v164, %[ob], s67 offen offset:0 sc1\n\t" \
  "buffer_load_dwordx4 v[52:55], v164, %[ob], s67 offen offset:1024 sc1\n\t" \
  "buffer_load_dwordx4 v[56:59], v164, %[ob], s67 offen offset:2048 sc1\n\t" \
  "buffer_load_dwordx4 v[60:63], v164, %[ob], s67 offen offset:3072 sc1\n\t" \
  "s_mov_b32 %[iss], 4\n\t" \
  "s_cmp_le_u32 %[t2], 4\n\t" \
  "s_cbranch_scc1 QD_%=\n\t" \
  "QL4_%=:\n\t" \
  "buffer_load_dwordx4 v[64:67], v164, %[ob], s68 offen offset:0 sc1\n\t" \
  "buffer_load_dwordx4 v[68:71], v164, %[ob], s68 offen offset:1024 sc1\n\t" \
  "buffer_load_dwordx4 v[72:75], v164, %[ob], s68 offen offset:2048 sc1\n\t" \
  "buffer_load_dwordx4 v[76:79], v164, %[ob], s68 offen offset:3072 sc1\n\t" \
  "s_mov_b32 %[iss], 5\n\t" \
  "QD_%=:\n\t" \
  "s_cmp_lt_u32 %[t0], 7\n\t" \
  "s_cbranch_scc1 QS_%=\n\t" \
  "s_cmp_eq_u32 %[t1], 0\n\t" \
  "s_cbranch_scc1 QS_%=\n\t" \
  "s_setprio 0\n\t" \
  "ds_read_b64_tr_b16 v[96:97], v160 offset:0\n\t" \
  "ds_read_b64_tr_b16 v[98:99], v160 offset:2048\n\t" \
  "ds_read_b64_tr_b16 v[100:101], v161 offset:0\n\t" \
  "ds_read_b64_tr_b16 v[102:103], v161 offset:2048\n\t" \
  "ds_read_b64_tr_b16 v[104:105], v162 offset:0\n\t" \
  "ds_read_b64_tr_b16 v[106:107], v162 offset:2048\n\t" \
  "ds_read_b64_tr_b16 v[108:109], v163 offset:0\n\t" \
  "ds_read_b64_tr_b16 v[110:111], v163 offset:2048\n\t" \
  "ds_read_b64_tr_b16 v[112:113], v160 offset:4096\n\t" \
  "ds_read_b64_tr_b16 v[114:115], v160 offset:6144\n\t" \
  "ds_read_b64_tr_b16 v[116:117], v161 offset:4096\n\t" \
  "ds_read_b64_tr_b16 v[118:119], v161 offset:6144\n\t" \
  "ds_read_b64_tr_b16 v[120:121], v162 offset:4096\n\t" \
  "ds_read_b64_tr_b16 v[122:123], v162 offset:6144\n\t" \
  "s_waitcnt vmcnt(18) lgkmcnt(10)\n\t" \
  "v_mfma_f32_16x16x32_bf16 v[128:131], v[96:99], v[0:3], 0\n\t" \
  "ds_read_b64_tr_b16 v[124:125], v163 offset:4096\n\t" \
  "v_mfma_f32_16x16x32_bf16 v[144:147], v[96:99], v[4:7], 0\n\t" \
  "ds_read_b64_tr_b16 v[126:127], v163 offset:6144\n\t" \
  "v_mfma_f32_16x16x32_bf16 v[132:135], v[100:103], v[0:3], 0\n\t" \
  "ds_read_b64_tr_b16 v[96:97], v160 offset:8192\n\t" \
  "v_mfma_f32_16x16x32_bf16 v[148:151], v[100:103], v[4:7], 0\n\t" \
  "ds_read_b64_tr_b16 v[98:99], v160 offset:10240\n\t" \
  "s_waitcnt lgkmcnt(10)\n\t" \
  "v_mfma_f32_16x16x32_bf16 v[136:139], v[104:107], v[0:3], 0\n\t" \
  "ds_read_b64_tr_b16 v[100:101], v161 offset:8192\n\t" \
  "v_mfma_f32_16x16x32_bf16 v[152:155], v[104:107], v[4:7], 0\n\t" \
  "ds_read_b64_tr_b16 v[102:103], v161 offset:10240\n\t" \
  "v_mfma_f32_16x16x32_bf16 v[140:143], v[108:111], v[0:3], 0\n\t" \
  "ds_read_b64_tr_b16 v[104:105], v162 offset:8192\n\t" \
  "v_mfma_f32_16x16x32_bf16 v[156:159], v[108:111], v[4:7], 0\n\t" \
  "ds_read_b64_tr_b16 v[106:107], v162 offset:10240\n\t" \
  "s_waitcnt vmcnt(16) lgkmcnt(10)\n\t" \
  "v_mfma_f32_16x16x32_bf16 v[128:131], v[112:115], v[8:11], v[128:131]\n\t" \
  "ds_read_b64_tr_b16 v[108:109], v163 offset:8192\n\t" \
  "buffer_load_dwordx4 v[80:83], v164, %[ob], s69 offen offset:0 sc1\n\t" \
  "v_mfma_f32_16x16x32_bf16 v[144:147], v[112:115], v[12:15], v[144:147]\n\t" \
  "ds_read_b64_tr_b16 v[110:111], v163 offset:10240\n\t" \
  "v_mfma_f32_16x16x32_bf16 v[132:135], v[116:119], v[8:11], v[132:135]\n\t" \
  "ds_read_b64_tr_b16 v[112:113], v160 offset:12288\n\t" \
  "v_mfma_f32_16x16x32_bf16 v[148:151], v[116:119], v[12:15], v[148:151]\n\t" \
  "ds_read_b64_tr_b16 v[114:115], v160 offset:14336\n\t" \
  "s_waitcnt lgkmcnt(10)\n\t" \
  "v_mfma_f32_16x16x32_bf16 v[136:139], v[120:123], v[8:11], v[136:139]\n\t" \
  "ds_read_b64_tr_b16 v[116:117], v161 offset:12288\n\t" \
  "buffer_load_dwordx4 v[84:87], v164, %[ob], s69 offen offset:1024 sc1\n\t" \
  "v_mfma_f32_16x16x32_bf16 v[152:155], v[120:123], v[12:15], v[152:155]\n\t" \
  "ds_read_b64_tr_b16 v[118:119], v161 offset:14336\n\t" \
  "v_mfma_f32_16x16x32_bf16 v[140:143], v[124:127], v[8:11], v[140:143]\n\t" \
  "ds_read_b64_tr_b16 v[120:121], v162 offset:12288\n\t" \
  "v_mfma_f32_16x16x32_bf16 v[156:159], v[124:127], v[12:15], v[156:159]\n\t" \
  "ds_read_b64_tr_b16 v[122:123], v162 offset:14336\n\t" \
  "s_waitcnt vmcnt(16) lgkmcnt(10)\n\t" \
  "v_mfma_f32_16x16x32_bf16 v[128:131], v[96:99], v[16:19], v[128:131]\n\t" \
  "ds_read_b64_tr_b16 v[124:125], v163 offset:12288\n\t" \
  "buffer_load_dwordx4 v[88:91], v164, %[ob], s69 offen offset:2048 sc1\n\t" \
  "v_mfma_f32_16x16x32_bf16 v[144:147], v[96:99], v[20:23], v[144:147]\n\t" \
  "ds_read_b64_tr_b16 v[126:127], v163 offset:14336\n\t" \
  "v_mfma_f32_16x16x32_bf16 v[132:135], v[100:103], v[16:19], v[132:135]\n\t" \
  "ds_read_b64_tr_b16 v[96:97], v160 offset:16384\n\t" \
  "v_mfma_f32_16x16x32_bf16 v[148:151], v[100:103], v[20:23], v[148:151]\n\t" \
  "ds_read_b64_tr_b16 v[98:99], v160 offset:18432\n\t" \
  "s_waitcnt lgkmcnt(10)\n\t" \
  "v_mfma_f32_16x16x32_bf16 v[136:139], v[104:107], v[16:19], v[136:139]\n\t" \
  "ds_read_b64_tr_b16 v[100:101], v161 offset:16384\n\t" \
  "v_mfma_f32_16x16x32_bf16 v[152:155], v[104:107], v[20:23], v[152:155]\n\t" \
  "ds_read_b64_tr_b16 v[102:103], v161 offset:18432\n\t" \
  "buffer_load_dwordx4 v[92:95], v164, %[ob], s69 offen offset:3072 sc1\n\t" \
  "v_mfma_f32_16x16x32_bf16 v[140:143], v[108:111], v[16:19], v[140:143]\n\t" \
  "ds_read_b64_tr_b16 v[104:105], v162 offset:16384\n\t" \
  "v_mfma_f32_16x16x32_bf16 v[156:159], v[108:111], v[20:23], v[156:159]\n\t" \
  "ds_read_b64_tr_b16 v[106:107], v162 offset:18432\n\t" \
  "s_waitcnt vmcnt(16) lgkmcnt(10)\n\t" \
  "v_mfma_f32_16x16x32_bf16 v[128:131], v[112:115], v[24:27], v[128:131]\n\t" \
  "ds_read_b64_tr_b16 v[108:109], v163 offset:16384\n\t" \
  "v_mfma_f32_16x16x32_bf16 v[144:147], v[112:115], v[28:31], v[144:147]\n\t" \
  "ds_read_b64_tr_b16 v[110:111], v163 offset:18432\n\t" \
  "buffer_load_dwordx4 v[0:3], v164, %[ob], s70 offen offset:0 sc1\n\t" \
  "v_mfma_f32_16x16x32_bf16 v[132:135], v[116:119], v[24:27], v[132:135]\n\t" \
  "ds_read_b64_tr_b16 v[112:113], v160 offset:20480\n\t" \
  "v_mfma_f32_16x16x32_bf16 v[148:151], v[116:119], v[28:31], v[148:151]\n\t" \
  "ds_read_b64_tr_b16 v[114:115], v160 offset:22528\n\t" \
  "s_waitcnt lgkmcnt(10)\n\t" \
  "v_mfma_f32_16x16x32_bf16 v[136:139], v[120:123], v[24:27], v[136:139]\n\t" \
  "ds_read_b64_tr_b16 v[116:117], v161 offset:20480\n\t" \
  "v_mfma_f32_16x16x32_bf16 v[152:155], v[120:123], v[28:31], v[152:155]\n\t" \
  "ds_read_b64_tr_b16 v[118:119], v161 offset:22528\n\t" \
  "v_mfma_f32_16x16x32_bf16 v[140:143], v[124:127], v[24:27], v[140:143]\n\t" \
  "ds_read_b64_tr_b16 v[120:121], v162 offset:20480\n\t" \
  "buffer_load_dwordx4 v[4:7], v164, %[ob], s70 offen offset:1024 sc1\n\t" \
  "v_mfma_f32_16x16x32_bf16 v[156:159], v[124:127], v[28:31], v[156:159]\n\t" \
  "ds_read_b64_tr_b16 v[122:123], v162 offset:22528\n\t" \
  "s_waitcnt vmcnt(16) lgkmcnt(10)\n\t" \
  "v_mfma_f32_16x16x32_bf16 v[128:131], v[96:99], v[32:35], v[128:131]\n\t" \
  "ds_read_b64_tr_b16 v[124:125], v163 offset:20480\n\t" \
  "ds_read_b32 v165, v165\n\t" \
  "v_mfma_f32_16x16x32_bf16 v[144:147], v[96:99], v[36:39], v[144:147]\n\t" \
  "ds_read_b64_tr_b16 v[126:127], v163 offset:22528\n\t" \
  "v_mfma_f32_16x16x32_bf16 v[132:135], v[100:103], v[32:35], v[132:135]\n\t" \
  "ds_read_b64_tr_b16 v[96:97], v160 offset:24576\n\t" \
  "buffer_load_dwordx4 v[8:11], v164, %[ob], s70 offen offset:2048 sc1\n\t" \
  "v_mfma_f32_16x16x32_bf16 v[148:151], v[100:103], v[36:39], v[148:151]\n\t" \
  "ds_read_b64_tr_b16 v[98:99], v160 offset:26624\n\t" \
  "s_waitcnt lgkmcnt(11)\n\t" \
  "v_mfma_f32_16x16x32_bf16 v[136:139], v[104:107], v[32:35], v[136:139]\n\t" \
  "ds_read_b64_tr_b16 v[100:101], v161 offset:24576\n\t" \
  "v_mfma_f32_16x16x32_bf16 v[152:155], v[104:107], v[36:39], v[152:155]\n\t" \
  "ds_read_b64_tr_b16 v[102:103], v161 offset:26624\n\t" \
  "v_mfma_f32_16x16x32_bf16 v[140:143], v[108:111], v[32:35], v[140:143]\n\t" \
  "ds_read_b64_tr_b16 v[104:105], v162 offset:24576\n\t" \
  "v_mfma_f32_16x16x32_bf16 v[156:159], v[108:111], v[36:39], v[156:159]\n\t" \
  "ds_read_b64_tr_b16 v[106:107], v162 offset:26624\n\t" \
  "s_waitcnt vmcnt(15) lgkmcnt(11)\n\t" \
  "v_mfma_f32_16x16x32_bf16 v[128:131], v[112:115], v[40:43], v[128:131]\n\t" \
  "ds_read_b64_tr_b16 v[108:109], v163 offset:24576\n\t" \
  "buffer_load_dwordx4 v[12:15], v164, %[ob], s70 offen offset:3072 sc1\n\t" \
  "v_mfma_f32_16x16x32_bf16 v[144:147], v[112:115], v[44:47], v[144:147]\n\t" \
  "ds_read_b64_tr_b16 v[110:111], v163 offset:26624\n\t" \
  "s_waitcnt lgkmcnt(9)\n\t" \
  "v_mfma_f32_16x16x32_bf16 v[132:135], v[116:119], v[40:43], v[132:135]\n\t" \
  "ds_read_b64_tr_b16 v[112:113], v160 offset:28672\n\t" \
  "v_cmp_lt_u32 vcc, 13, v165\n\t" \
  "v_mfma_f32_16x16x32_bf16 v[148:151], v[116:119], v[44:47], v[148:151]\n\t" \
  "ds_read_b64_tr_b16 v[114:115], v160 offset:30720\n\t" \
  "s_cbranch_vccz QA7_%=\n\t" \
  "QB7_%=:\n\t" \
  "s_waitcnt lgkmcnt(10)\n\t" \
  "v_mfma_f32_16x16x32_bf16 v[136:139], v[120:123], v[40:43], v[136:139]\n\t" \
  "ds_read_b64_tr_b16 v[116:117], v161 offset:28672\n\t" \
  "buffer_load_dwordx4 v[16:19], v164, %[ob], s71 offen offset:0 sc1\n\t" \
  "v_mfma_f32_16x16x32_bf16 v[152:155], v[120:123], v[44:47], v[152:155]\n\t" \
  "ds_read_b64_tr_b16 v[118:119], v161 offset:30720\n\t" \
  "v_mov_b32 v165, %[sy]\n\t" \
  "v_mfma_f32_16x16x32_bf16 v[140:143], v[124:127], v[40:43], v[140:143]\n\t" \
  "ds_read_b64_tr_b16 v[120:121], v162 offset:28672\n\t" \
  "v_mfma_f32_16x16x32_bf16 v[156:159], v[124:127], v[44:47], v[156:159]\n\t" \
  "ds_read_b64_tr_b16 v[122:123], v162 offset:30720\n\t" \
  "s_waitcnt vmcnt(15) lgkmcnt(10)\n\t" \
  "v_mfma_f32_16x16x32_bf16 v[128:131], v[96:99], v[48:51], v[128:131]\n\t" \
  "ds_read_b64_tr_b16 v[124:125], v163 offset:28672\n\t" \
  "buffer_load_dwordx4 v[20:23], v164, %[ob], s71 offen offset:1024 sc1\n\t" \
  "v_mfma_f32_16x16x32_bf16 v[144:147], v[96:99], v[52:55], v[144:147]\n\t" \
  "ds_read_b64_tr_b16 v[126:127], v163 offset:30720\n\t" \
  "v_mfma_f32_16x16x32_bf16 v[132:135], v[100:103], v[48:51], v[132:135]\n\t" \
  "ds_read_b64_tr_b16 v[96:97], v160 offset:32768\n\t" \
  "v_mfma_f32_16x16x32_bf16 v[148:151], v[100:103], v[52:55], v[148:151]\n\t" \
  "ds_read_b64_tr_b16 v[98:99], v160 offset:34816\n\t" \
  "s_waitcnt lgkmcnt(10)\n\t" \
  "v_mfma_f32_16x16x32_bf16 v[136:139], v[104:107], v[48:51], v[136:139]\n\t" \
  "ds_read_b64_tr_b16 v[100:101], v161 offset:32768\n\t" \
  "buffer_load_dwordx4 v[24:27], v164, %[ob], s71 offen offset:2048 sc1\n\t" \
  "v_mfma_f32_16x16x32_bf16 v[152:155], v[104:107], v[52:55], v[152:155]\n\t" \
  "ds_read_b64_tr_b16 v[102:103], v161 offset:34816\n\t" \
  "v_mfma_f32_16x16x32_bf16 v[140:143], v[108:111], v[48:51], v[140:143]\n\t" \
  "ds_read_b64_tr_b16 v[104:105], v162 offset:32768\n\t" \
  "v_mfma_f32_16x16x32_bf16 v[156:159], v[108:111], v[52:55], v[156:159]\n\t" \
  "ds_read_b64_tr_b16 v[106:107], v162 offset:34816\n\t" \
  "s_waitcnt vmcnt(15) lgkmcnt(10)\n\t" \
  "v_mfma_f32_16x16x32_bf16 v[128:131], v[112:115], v[56:59], v[128:131]\n\t" \
  "ds_read_b64_tr_b16 v[108:109], v163 offset:32768\n\t" \
  "v_mfma_f32_16x16x32_bf16 v[144:147], v[112:115], v[60:63], v[144:147]\n\t" \
  "ds_read_b64_tr_b16 v[110:111], v163 offset:34816\n\t" \
  "buffer_load_dwordx4 v[28:31], v164, %[ob], s71 offen offset:3072 sc1\n\t" \
  "v_mfma_f32_16x16x32_bf16 v[132:135], v[116:119], v[56:59], v[132:135]\n\t" \
  "ds_read_b64_tr_b16 v[112:113], v160 offset:36864\n\t" \
  "v_mfma_f32_16x16x32_bf16 v[148:151], v[116:119], v[60:63], v[148:151]\n\t" \
  "ds_read_b64_tr_b16 v[114:115], v160 offset:38912\n\t" \
  "s_waitcnt lgkmcnt(10)\n\t" \
  "v_mfma_f32_16x16x32_bf16 v[136:139], v[120:123], v[56:59], v[136:139]\n\t" \
  "ds_read_b64_tr_b16 v[116:117], v161 offset:36864\n\t" \
  "v_mfma_f32_16x16x32_bf16 v[152:155], v[120:123], v[60:63], v[152:155]\n\t" \
  "ds_read_b64_tr_b16 v[118:119], v161 offset:38912\n\t" \
  "buffer_load_dwordx4 v[32:35], v164, %[ob], s72 offen offset:0 sc1\n\t" \
  "v_mfma_f32_16x16x32_bf16 v[140:143], v[124:127], v[56:59], v[140:143]\n\t" \
  "ds_read_b64_tr_b16 v[120:121], v162 offset:36864\n\t" \
  "v_mfma_f32_16x16x32_bf16 v[156:159], v[124:127], v[60:63], v[156:159]\n\t" \
  "ds_read_b64_tr_b16 v[122:123], v162 offset:38912\n\t" \
  "s_waitcnt vmcnt(15) lgkmcnt(10)\n\t" \
  "v_mfma_f32_16x16x32_bf16 v[128:131], v[96:99], v[64:67], v[128:131]\n\t" \
  "ds_read_b64_tr_b16 v[124:125], v163 offset:36864\n\t" \
  "v_mfma_f32_16x16x32_bf16 v[144:147], v[96:99], v[68:71], v[144:147]\n\t" \
  "ds_read_b64_tr_b16 v[126:127], v163 offset:38912\n\t" \
  "v_mfma_f32_16x16x32_bf16 v[132:135], v[100:103], v[64:67], v[132:135]\n\t" \
  "ds_read_b64_tr_b16 v[96:97], v160 offset:40960\n\t" \
  "buffer_load_dwordx4 v[36:39], v164, %[ob], s72 offen offset:1024 sc1\n\t" \
  "v_mfma_f32_16x16x32_bf16 v[148:151], v[100:103], v[68:71], v[148:151]\n\t" \
  "ds_read_b64_tr_b16 v[98:99], v160 offset:43008\n\t" \
  "s_waitcnt lgkmcnt(10)\n\t" \
  "v_mfma_f32_16x16x32_bf16 v[136:139], v[104:107], v[64:67], v[136:139]\n\t" \
  "ds_read_b64_tr_b16 v[100:101], v161 offset:40960\n\t" \
  "v_mfma_f32_16x16x32_bf16 v[152:155], v[104:107], v[68:71], v[152:155]\n\t" \
  "ds_read_b64_tr_b16 v[102:103], v161 offset:43008\n\t" \
  "v_mfma_f32_16x16x32_bf16 v[140:143], v[108:111], v[64:67], v[140:143]\n\t" \
  "ds_read_b64_tr_b16 v[104:105], v162 offset:40960\n\t" \
  "buffer_load_dwordx4 v[40:43], v164, %[ob], s72 offen offset:2048 sc1\n\t" \
  "v_mfma_f32_16x16x32_bf16 v[156:159], v[108:111], v[68:71], v[156:159]\n\t" \
  "ds_read_b64_tr_b16 v[106:107], v162 offset:43008\n\t" \
  "s_waitcnt vmcnt(15) lgkmcnt(10)\n\t" \
  "v_mfma_f32_16x16x32_bf16 v[128:131], v[112:115], v[72:75], v[128:131]\n\t" \
  "ds_read_b64_tr_b16 v[108:109], v163 offset:40960\n\t" \
  "v_mfma_f32_16x16x32_bf16 v[144:147], v[112:115], v[76:79], v[144:147]\n\t" \
  "ds_read_b64_tr_b16 v[110:111], v163 offset:43008\n\t" \
  "v_mfma_f32_16x16x32_bf16 v[132:135], v[116:119], v[72:75], v[132:135]\n\t" \
  "ds_read_b64_tr_b16 v[112:113], v160 offset:45056\n\t" \
  "buffer_load_dwordx4 v[44:47], v164, %[ob], s72 offen offset:3072 sc1\n\t" \
  "v_mfma_f32_16x16x32_bf16 v[148:151], v[116:119], v[76:79], v[148:151]\n\t" \
  "ds_read_b64_tr_b16 v[114:115], v160 offset:47104\n\t" \
  "s_waitcnt lgkmcnt(10)\n\t" \
  "v_mfma_f32_16x16x32_bf16 v[136:139], v[120:123], v[72:75], v[136:139]\n\t" \
  "ds_read_b64_tr_b16 v[116:117], v161 offset:45056\n\t" \
  "v_mfma_f32_16x16x32_bf16 v[152:155], v[120:123], v[76:79], v[152:155]\n\t" \
  "ds_read_b64_tr_b16 v[118:119], v161 offset:47104\n\t" \
  "v_mfma_f32_16x16x32_bf16 v[140:143], v[124:127], v[72:75], v[140:143]\n\t" \
  "ds_read_b64_tr_b16 v[120:121], v162 offset:45056\n\t" \
  "v_mfma_f32_16x16x32_bf16 v[156:159], v[124:127], v[76:79], v[156:159]\n\t" \
  "ds_read_b64_tr_b16 v[122:123], v162 offset:47104\n\t" \
  "s_waitcnt vmcnt(14) lgkmcnt(10)\n\t" \
  "v_mfma_f32_16x16x32_bf16 v[128:131], v[96:99], v[80:83], v[128:131]\n\t" \
  "ds_read_b64_tr_b16 v[124:125], v163 offset:45056\n\t" \
  "buffer_load_dwordx4 v[48:51], v164, %[ob], s73 offen offset:0 sc1\n\t" \
  "v_mfma_f32_16x16x32_bf16 v[144:147], v[96:99], v[84:87], v[144:147]\n\t" \
  "ds_read_b64_tr_b16 v[126:127], v163 offset:47104\n\t" \
  "v_mfma_f32_16x16x32_bf16 v[132:135], v[100:103], v[80:83], v[132:135]\n\t" \
  "ds_read_b64_tr_b16 v[96:97], v160 offset:49152\n\t" \
  "v_mfma_f32_16x16x32_bf16 v[148:151], v[100:103], v[84:87], v[148:151]\n\t" \
  "ds_read_b64_tr_b16 v[98:99], v160 offset:51200\n\t" \
  "s_waitcnt lgkmcnt(10)\n\t" \
  "v_mfma_f32_16x16x32_bf16 v[136:139], v[104:107], v[80:83], v[136:139]\n\t" \
  "ds_read_b64_tr_b16 v[100:101], v161 offset:49152\n\t" \
  "buffer_load_dwordx4 v[52:55], v164, %[ob], s73 offen offset:1024 sc1\n\t" \
  "v_mfma_f32_16x16x32_bf16 v[152:155], v[104:107], v[84:87], v[152:155]\n\t" \
  "ds_read_b64_tr_b16 v[102:103], v161 offset:51200\n\t" \
  "v_mfma_f32_16x16x32_bf16 v[140:143], v[108:111], v[80:83], v[140:143]\n\t" \
  "ds_read_b64_tr_b16 v[104:105], v162 offset:49152\n\t" \
  "v_mfma_f32_16x16x32_bf16 v[156:159], v[108:111], v[84:87], v[156:159]\n\t" \
  "ds_read_b64_tr_b16 v[106:107], v162 offset:51200\n\t" \
  "s_waitcnt vmcnt(14) lgkmcnt(10)\n\t" \
  "v_mfma_f32_16x16x32_bf16 v[128:131], v[112:115], v[88:91], v[128:131]\n\t" \
  "ds_read_b64_tr_b16 v[108:109], v163 offset:49152\n\t" \
  "buffer_load_dwordx4 v[56:59], v164, %[ob], s73 offen offset:2048 sc1\n\t" \
  "v_mfma_f32_16x16x32_bf16 v[144:147], v[112:115], v[92:95], v[144:147]\n\t" \
  "ds_read_b64_tr_b16 v[110:111], v163 offset:51200\n\t" \
  "v_mfma_f32_16x16x32_bf16 v[132:135], v[116:119], v[88:91], v[132:135]\n\t" \
  "ds_read_b64_tr_b16 v[112:113], v160 offset:53248\n\t" \
  "v_mfma_f32_16x16x32_bf16 v[148:151], v[116:119], v[92:95], v[148:151]\n\t" \
  "ds_read_b64_tr_b16 v[114:115], v160 offset:55296\n\t" \
  "s_waitcnt lgkmcnt(10)\n\t" \
  "v_mfma_f32_16x16x32_bf16 v[136:139], v[120:123], v[88:91], v[136:139]\n\t" \
  "ds_read_b64_tr_b16 v[116:117], v161 offset:53248\n\t" \
  "buffer_load_dwordx4 v[60:63], v164, %[ob], s73 offen offset:3072 sc1\n\t" \
  "v_mfma_f32_16x16x32_bf16 v[152:155], v[120:123], v[92:95], v[152:155]\n\t" \
  "ds_read_b64_tr_b16 v[118:119], v161 offset:55296\n\t" \
  "v_mfma_f32_16x16x32_bf16 v[140:143], v[124:127], v[88:91], v[140:143]\n\t" \
  "ds_read_b64_tr_b16 v[120:121], v162 offset:53248\n\t" \
  "v_mfma_f32_16x16x32_bf16 v[156:159], v[124:127], v[92:95], v[156:159]\n\t" \
  "ds_read_b64_tr_b16 v[122:123], v162 offset:55296\n\t" \
  "s_waitcnt vmcnt(14) lgkmcnt(10)\n\t" \
  "v_mfma_f32_16x16x32_bf16 v[128:131], v[96:99], v[0:3], v[128:131]\n\t" \
  "ds_read_b64_tr_b16 v[124:125], v163 offset:53248\n\t" \
  "v_mfma_f32_16x16x32_bf16 v[144:147], v[96:99], v[4:7], v[144:147]\n\t" \
  "ds_read_b64_tr_b16 v[126:127], v163 offset:55296\n\t" \
  "buffer_load_dwordx4 v[64:67], v164, %[ob], s74 offen offset:0 sc1\n\t" \
  "v_mfma_f32_16x16x32_bf16 v[132:135], v[100:103], v[0:3], v[132:135]\n\t" \
  "ds_read_b64_tr_b16 v[96:97], v160 offset:57344\n\t" \
  "v_mfma_f32_16x16x32_bf16 v[148:151], v[100:103], v[4:7], v[148:151]\n\t" \
  "ds_read_b64_tr_b16 v[98:99], v160 offset:59392\n\t" \
  "s_waitcnt lgkmcnt(10)\n\t" \
  "v_mfma_f32_16x16x32_bf16 v[136:139], v[104:107], v[0:3], v[136:139]\n\t" \
  "ds_read_b64_tr_b16 v[100:101], v161 offset:57344\n\t" \
  "v_mfma_f32_16x16x32_bf16 v[152:155], v[104:107], v[4:7], v[152:155]\n\t" \
  "ds_read_b64_tr_b16 v[102:103], v161 offset:59392\n\t" \
  "buffer_load_dwordx4 v[68:71], v164, %[ob], s74 offen offset:1024 sc1\n\t" \
  "v_mfma_f32_16x16x32_bf16 v[140:143], v[108:111], v[0:3], v[140:143]\n\t" \
  "ds_read_b64_tr_b16 v[104:105], v162 offset:57344\n\t" \
  "v_mfma_f32_16x16x32_bf16 v[156:159], v[108:111], v[4:7], v[156:159]\n\t" \
  "ds_read_b64_tr_b16 v[106:107], v162 offset:59392\n\t" \
  "s_waitcnt vmcnt(14) lgkmcnt(10)\n\t" \
  "v_mfma_f32_16x16x32_bf16 v[128:131], v[112:115], v[8:11], v[128:131]\n\t" \
  "ds_read_b64_tr_b16 v[108:109], v163 offset:57344\n\t" \
  "v_mfma_f32_16x16x32_bf16 v[144:147], v[112:115], v[12:15], v[144:147]\n\t" \
  "ds_read_b64_tr_b16 v[110:111], v163 offset:59392\n\t" \
  "v_mfma_f32_16x16x32_bf16 v[132:135], v[116:119], v[8:11], v[132:135]\n\t" \
  "ds_read_b64_tr_b16 v[112:113], v160 offset:61440\n\t" \
  "buffer_load_dwordx4 v[72:75], v164, %[ob], s74 offen offset:2048 sc1\n\t" \
  "v_mfma_f32_16x16x32_bf16 v[148:151], v[116:119], v[12:15], v[148:151]\n\t" \
  "ds_read_b64_tr_b16 v[114:115], v160 offset:63488\n\t" \
  "s_waitcnt lgkmcnt(10)\n\t" \
  "v_mfma_f32_16x16x32_bf16 v[136:139], v[120:123], v[8:11], v[136:139]\n\t" \
  "ds_read_b64_tr_b16 v[116:117], v161 offset:61440\n\t" \
  "v_mfma_f32_16x16x32_bf16 v[152:155], v[120:123], v[12:15], v[152:155]\n\t" \
  "ds_read_b64_tr_b16 v[118:119], v161 offset:63488\n\t" \
  "v_mfma_f32_16x16x32_bf16 v[140:143], v[124:127], v[8:11], v[140:143]\n\t" \
  "ds_read_b64_tr_b16 v[120:121], v162 offset:61440\n\t" \
  "buffer_load_dwordx4 v[76:79], v164, %[ob], s74 offen offset:3072 sc1\n\t" \
  "v_mfma_f32_16x16x32_bf16 v[156:159], v[124:127], v[12:15], v[156:159]\n\t" \
  "ds_read_b64_tr_b16 v[122:123], v162 offset:63488\n\t" \
  "s_waitcnt vmcnt(14) lgkmcnt(10)\n\t" \
  "v_mfma_f32_16x16x32_bf16 v[128:131], v[96:99], v[16:19], v[128:131]\n\t" \
  "ds_read_b64_tr_b16 v[124:125], v163 offset:61440\n\t" \
  "v_mfma_f32_16x16x32_bf16 v[144:147], v[96:99], v[20:23], v[144:147]\n\t" \
  "ds_read_b64_tr_b16 v[126:127], v163 offset:63488\n\t" \
  "v_mfma_f32_16x16x32_bf16 v[132:135], v[100:103], v[16:19], v[132:135]\n\t" \
  "v_add_u32 v160, 0x10000, v160\n\t" \
  "ds_read_b64_tr_b16 v[96:97], v160 offset:0\n\t" \
  "v_mfma_f32_16x16x32_bf16 v[148:151], v[100:103], v[20:23], v[148:151]\n\t" \
  "ds_read_b64_tr_b16 v[98:99], v160 offset:2048\n\t" \
  "s_waitcnt lgkmcnt(10)\n\t" \
  "v_mfma_f32_16x16x32_bf16 v[136:139], v[104:107], v[16:19], v[136:139]\n\t" \
  "v_add_u32 v161, 0x10000, v161\n\t" \
  "ds_read_b64_tr_b16 v[100:101], v161 offset:0\n\t" \
  "buffer_load_dwordx4 v[80:83], v164, %[ob], s75 offen offset:0 sc1\n\t" \
  "v_mfma_f32_16x16x32_bf16 v[152:155], v[104:107], v[20:23], v[152:155]\n\t" \
  "ds_read_b64_tr_b16 v[102:103], v161 offset:2048\n\t" \
  "v_mfma_f32_16x16x32_bf16 v[140:143], v[108:111], v[16:19], v[140:143]\n\t" \
  "v_add_u32 v162, 0x10000, v162\n\t" \
  "ds_read_b64_tr_b16 v[104:105], v162 offset:0\n\t" \
  "v_mfma_f32_16x16x32_bf16 v[156:159], v[108:111], v[20:23], v[156:159]\n\t" \
  "ds_read_b64_tr_b16 v[106:107], v162 offset:2048\n\t" \
  "s_waitcnt vmcnt(13) lgkmcnt(10)\n\t" \
  "v_mfma_f32_16x16x32_bf16 v[128:131], v[112:115], v[24:27], v[128:131]\n\t" \
  "v_add_u32 v163, 0x10000, v163\n\t" \
  "ds_read_b64_tr_b16 v[108:109], v163 offset:0\n\t" \
  "buffer_load_dwordx4 v[84:87], v164, %[ob], s75 offen offset:1024 sc1\n\t" \
  "v_mfma_f32_16x16x32_bf16 v[144:147], v[112:115], v[28:31], v[144:147]\n\t" \
  "ds_read_b64_tr_b16 v[110:111], v163 offset:2048\n\t" \
  "v_mfma_f32_16x16x32_bf16 v[132:135], v[116:119], v[24:27], v[132:135]\n\t" \
  "ds_read_b64_tr_b16 v[112:113], v160 offset:4096\n\t" \
  "v_mfma_f32_16x16x32_bf16 v[148:151], v[116:119], v[28:31], v[148:151]\n\t" \
  "ds_read_b64_tr_b16 v[114:115], v160 offset:6144\n\t" \
  "s_waitcnt lgkmcnt(10)\n\t" \
  "v_mfma_f32_16x16x32_bf16 v[136:139], v[120:123], v[24:27], v[136:139]\n\t" \
  "ds_read_b64_tr_b16 v[116:117], v161 offset:4096\n\t" \
  "buffer_load_dwordx4 v[88:91], v164, %[ob], s75 offen offset:2048 sc1\n\t" \
  "v_mfma_f32_16x16x32_bf16 v[152:155], v[120:123], v[28:31], v[152:155]\n\t" \
  "ds_read_b64_tr_b16 v[118:119], v161 offset:6144\n\t" \
  "v_mfma_f32_16x16x32_bf16 v[140:143], v[124:127], v[24:27], v[140:143]\n\t" \
  "ds_read_b64_tr_b16 v[120:121], v162 offset:4096\n\t" \
  "v_mfma_f32_16x16x32_bf16 v[156:159], v[124:127], v[28:31], v[156:159]\n\t" \
  "ds_read_b64_tr_b16 v[122:123], v162 offset:6144\n\t" \
  "s_waitcnt vmcnt(13) lgkmcnt(10)\n\t" \
  "v_mfma_f32_16x16x32_bf16 v[128:131], v[96:99], v[32:35], v[128:131]\n\t" \
  "ds_read_b64_tr_b16 v[124:125], v163 offset:4096\n\t" \
  "buffer_load_dwordx4 v[92:95], v164, %[ob], s75 offen offset:3072 sc1\n\t" \
  "v_mfma_f32_16x16x32_bf16 v[144:147], v[96:99], v[36:39], v[144:147]\n\t" \
  "ds_read_b64_tr_b16 v[126:127], v163 offset:6144\n\t" \
  "v_mfma_f32_16x16x32_bf16 v[132:135], v[100:103], v[32:35], v[132:135]\n\t" \
  "ds_read_b64_tr_b16 v[96:97], v160 offset:8192\n\t" \
  "v_mfma_f32_16x16x32_bf16 v[148:151], v[100:103], v[36:39], v[148:151]\n\t" \
  "ds_read_b64_tr_b16 v[98:99], v160 offset:10240\n\t" \
  "s_waitcnt lgkmcnt(10)\n\t" \
  "v_mfma_f32_16x16x32_bf16 v[136:139], v[104:107], v[32:35], v[136:139]\n\t" \
  "ds_read_b64_tr_b16 v[100:101], v161 offset:8192\n\t" \
  "v_mfma_f32_16x16x32_bf16 v[152:155], v[104:107], v[36:39], v[152:155]\n\t" \
  "ds_read_b64_tr_b16 v[102:103], v161 offset:10240\n\t" \
  "buffer_load_dwordx4 v[0:3], v164, %[ob], s76 offen offset:0 sc1\n\t" \
  "v_mfma_f32_16x16x32_bf16 v[140:143], v[108:111], v[32:35], v[140:143]\n\t" \
  "ds_read_b64_tr_b16 v[104:105], v162 offset:8192\n\t" \
  "v_mfma_f32_16x16x32_bf16 v[156:159], v[108:111], v[36:39], v[156:159]\n\t" \
  "ds_read_b64_tr_b16 v[106:107], v162 offset:10240\n\t" \
  "s_waitcnt vmcnt(13) lgkmcnt(10)\n\t" \
  "v_mfma_f32_16x16x32_bf16 v[128:131], v[112:115], v[40:43], v[128:131]\n\t" \
  "ds_read_b64_tr_b16 v[108:109], v163 offset:8192\n\t" \
  "v_mfma_f32_16x16x32_bf16 v[144:147], v[112:115], v[44:47], v[144:147]\n\t" \
  "ds_read_b64_tr_b16 v[110:111], v163 offset:10240\n\t" \
  "buffer_load_dwordx4 v[4:7], v164, %[ob], s76 offen offset:1024 sc1\n\t" \
  "v_mfma_f32_16x16x32_bf16 v[132:135], v[116:119], v[40:43], v[132:135]\n\t" \
  "ds_read_b64_tr_b16 v[112:113], v160 offset:12288\n\t" \
  "v_mfma_f32_16x16x32_bf16 v[148:151], v[116:119], v[44:47], v[148:151]\n\t" \
  "ds_read_b64_tr_b16 v[114:115], v160 offset:14336\n\t" \
  "s_waitcnt lgkmcnt(10)\n\t" \
  "v_mfma_f32_16x16x32_bf16 v[136:139], v[120:123], v[40:43], v[136:139]\n\t" \
  "ds_read_b64_tr_b16 v[116:117], v161 offset:12288\n\t" \
  "v_mfma_f32_16x16x32_bf16 v[152:155], v[120:123], v[44:47], v[152:155]\n\t" \
  "ds_read_b64_tr_b16 v[118:119], v161 offset:14336\n\t" \
  "buffer_load_dwordx4 v[8:11], v164, %[ob], s76 offen offset:2048 sc1\n\t" \
  "v_mfma_f32_16x16x32_bf16 v[140:143], v[124:127], v[40:43], v[140:143]\n\t" \
  "ds_read_b64_tr_b16 v[120:121], v162 offset:12288\n\t" \
  "v_mfma_f32_16x16x32_bf16 v[156:159], v[124:127], v[44:47], v[156:159]\n\t" \
  "ds_read_b64_tr_b16 v[122:123], v162 offset:14336\n\t" \
  "s_waitcnt vmcnt(13) lgkmcnt(10)\n\t" \
  "v_mfma_f32_16x16x32_bf16 v[128:131], v[96:99], v[48:51], v[128:131]\n\t" \
  "ds_read_b64_tr_b16 v[124:125], v163 offset:12288\n\t" \
  "v_mfma_f32_16x16x32_bf16 v[144:147], v[96:99], v[52:55], v[144:147]\n\t" \
  "ds_read_b64_tr_b16 v[126:127], v163 offset:14336\n\t" \
  "v_mfma_f32_16x16x32_bf16 v[132:135], v[100:103], v[48:51], v[132:135]\n\t" \
  "ds_read_b64_tr_b16 v[96:97], v160 offset:16384\n\t" \
  "buffer_load_dwordx4 v[12:15], v164, %[ob], s76 offen offset:3072 sc1\n\t" \
  "v_mfma_f32_16x16x32_bf16 v[148:151], v[100:103], v[52:55], v[148:151]\n\t" \
  "ds_read_b64_tr_b16 v[98:99], v160 offset:18432\n\t" \
  "s_waitcnt lgkmcnt(10)\n\t" \
  "v_mfma_f32_16x16x32_bf16 v[136:139], v[104:107], v[48:51], v[136:139]\n\t" \
  "ds_read_b64_tr_b16 v[100:101], v161 offset:16384\n\t" \
  "v_mfma_f32_16x16x32_bf16 v[152:155], v[104:107], v[52:55], v[152:155]\n\t" \
  "ds_read_b64_tr_b16 v[102:103], v161 offset:18432\n\t" \
  "v_mfma_f32_16x16x32_bf16 v[140:143], v[108:111], v[48:51], v[140:143]\n\t" \
  "ds_read_b64_tr_b16 v[104:105], v162 offset:16384\n\t" \
  "buffer_load_dwordx4 v[16:19], v164, %[ob], s77 offen offset:0 sc1\n\t" \
  "v_mfma_f32_16x16x32_bf16 v[156:159], v[108:111], v[52:55], v[156:159]\n\t" \
  "ds_read_b64_tr_b16 v[106:107], v162 offset:18432\n\t" \
  "s_waitcnt vmcnt(13) lgkmcnt(10)\n\t" \
  "v_mfma_f32_16x16x32_bf16 v[128:131], v[112:115], v[56:59], v[128:131]\n\t" \
  "ds_read_b64_tr_b16 v[108:109], v163 offset:16384\n\t" \
  "v_mfma_f32_16x16x32_bf16 v[144:147], v[112:115], v[60:63], v[144:147]\n\t" \
  "ds_read_b64_tr_b16 v[110:111], v163 offset:18432\n\t" \
  "v_mfma_f32_16x16x32_bf16 v[132:135], v[116:119], v[56:59], v[132:135]\n\t" \
  "ds_read_b64_tr_b16 v[112:113], v160 offset:20480\n\t" \
  "v_mfma_f32_16x16x32_bf16 v[148:151], v[116:119], v[60:63], v[148:151]\n\t" \
  "ds_read_b64_tr_b16 v[114:115], v160 offset:22528\n\t" \
  "s_waitcnt lgkmcnt(10)\n\t" \
  "v_mfma_f32_16x16x32_bf16 v[136:139], v[120:123], v[56:59], v[136:139]\n\t" \
  "ds_read_b64_tr_b16 v[116:117], v161 offset:20480\n\t" \
  "buffer_load_dwordx4 v[20:23], v164, %[ob], s77 offen offset:1024 sc1\n\t" \
  "ds_read_b32 v165, v165\n\t" \
  "v_mfma_f32_16x16x32_bf16 v[152:155], v[120:123], v[60:63], v[152:155]\n\t" \
  "ds_read_b64_tr_b16 v[118:119], v161 offset:22528\n\t" \
  "v_mfma_f32_16x16x32_bf16 v[140:143], v[124:127], v[56:59], v[140:143]\n\t" \
  "ds_read_b64_tr_b16 v[120:121], v162 offset:20480\n\t" \
  "v_mfma_f32_16x16x32_bf16 v[156:159], v[124:127], v[60:63], v[156:159]\n\t" \
  "ds_read_b64_tr_b16 v[122:123], v162 offset:22528\n\t" \
  "s_waitcnt vmcnt(12) lgkmcnt(11)\n\t" \
  "v_mfma_f32_16x16x32_bf16 v[128:131], v[96:99], v[64:67], v[128:131]\n\t" \
  "ds_read_b64_tr_b16 v[124:125], v163 offset:20480\n\t" \
  "buffer_load_dwordx4 v[24:27], v164, %[ob], s77 offen offset:2048 sc1\n\t" \
  "v_mfma_f32_16x16x32_bf16 v[144:147], v[96:99], v[68:71], v[144:147]\n\t" \
  "ds_read_b64_tr_b16 v[126:127], v163 offset:22528\n\t" \
  "v_mfma_f32_16x16x32_bf16 v[132:135], v[100:103], v[64:67], v[132:135]\n\t" \
  "ds_read_b64_tr_b16 v[96:97], v160 offset:24576\n\t" \
  "v_mfma_f32_16x16x32_bf16 v[148:151], v[100:103], v[68:71], v[148:151]\n\t" \
  "ds_read_b64_tr_b16 v[98:99], v160 offset:26624\n\t" \
  "s_waitcnt lgkmcnt(11)\n\t" \
  "v_mfma_f32_16x16x32_bf16 v[136:139], v[104:107], v[64:67], v[136:139]\n\t" \
  "ds_read_b64_tr_b16 v[100:101], v161 offset:24576\n\t" \
  "buffer_load_dwordx4 v[28:31], v164, %[ob], s77 offen offset:3072 sc1\n\t" \
  "v_mfma_f32_16x16x32_bf16 v[152:155], v[104:107], v[68:71], v[152:155]\n\t" \
  "ds_read_b64_tr_b16 v[102:103], v161 offset:26624\n\t" \
  "s_waitcnt lgkmcnt(9)\n\t" \
  "v_mfma_f32_16x16x32_bf16 v[140:143], v[108:111], v[64:67], v[140:143]\n\t" \
  "ds_read_b64_tr_b16 v[104:105], v162 offset:24576\n\t" \
  "v_cmp_lt_u32 vcc, 15, v165\n\t" \
  "v_mfma_f32_16x16x32_bf16 v[156:159], v[108:111], v[68:71], v[156:159]\n\t" \
  "ds_read_b64_tr_b16 v[106:107], v162 offset:26624\n\t" \
  "s_cbranch_vccz QA14_%=\n\t" \
  "QB14_%=:\n\t" \
  "s_waitcnt vmcnt(12) lgkmcnt(10)\n\t" \
  "v_mfma_f32_16x16x32_bf16 v[128:131], v[112:115], v[72:75], v[128:131]\n\t" \
  "ds_read_b64_tr_b16 v[108:109], v163 offset:24576\n\t" \
  "buffer_load_dwordx4 v[32:35], v164, %[ob], s78 offen offset:0 sc1\n\t" \
  "v_mfma_f32_16x16x32_bf16 v[144:147], v[112:115], v[76:79], v[144:147]\n\t" \
  "ds_read_b64_tr_b16 v[110:111], v163 offset:26624\n\t" \
  "v_mfma_f32_16x16x32_bf16 v[132:135], v[116:119], v[72:75], v[132:135]\n\t" \
  "ds_read_b64_tr_b16 v[112:113], v160 offset:28672\n\t" \
  "v_mfma_f32_16x16x32_bf16 v[148:151], v[116:119], v[76:79], v[148:151]\n\t" \
  "ds_read_b64_tr_b16 v[114:115], v160 offset:30720\n\t" \
  "s_waitcnt lgkmcnt(10)\n\t" \
  "v_mfma_f32_16x16x32_bf16 v[136:139], v[120:123], v[72:75], v[136:139]\n\t" \
  "ds_read_b64_tr_b16 v[116:117], v161 offset:28672\n\t" \
  "v_mfma_f32_16x16x32_bf16 v[152:155], v[120:123], v[76:79], v[152:155]\n\t" \
  "ds_read_b64_tr_b16 v[118:119], v161 offset:30720\n\t" \
  "buffer_load_dwordx4 v[36:39], v164, %[ob], s78 offen offset:1024 sc1\n\t" \
  "v_mfma_f32_16x16x32_bf16 v[140:143], v[124:127], v[72:75], v[140:143]\n\t" \
  "ds_read_b64_tr_b16 v[120:121], v162 offset:28672\n\t" \
  "v_mfma_f32_16x16x32_bf16 v[156:159], v[124:127], v[76:79], v[156:159]\n\t" \
  "ds_read_b64_tr_b16 v[122:123], v162 offset:30720\n\t" \
  "s_waitcnt vmcnt(12) lgkmcnt(10)\n\t" \
  "v_mfma_f32_16x16x32_bf16 v[128:131], v[96:99], v[80:83], v[128:131]\n\t" \
  "ds_read_b64_tr_b16 v[124:125], v163 offset:28672\n\t" \
  "v_mfma_f32_16x16x32_bf16 v[144:147], v[96:99], v[84:87], v[144:147]\n\t" \
  "ds_read_b64_tr_b16 v[126:127], v163 offset:30720\n\t" \
  "buffer_load_dwordx4 v[40:43], v164, %[ob], s78 offen offset:2048 sc1\n\t" \
  "v_mfma_f32_16x16x32_bf16 v[132:135], v[100:103], v[80:83], v[132:135]\n\t" \
  "ds_read_b64_tr_b16 v[96:97], v160 offset:32768\n\t" \
  "v_mfma_f32_16x16x32_bf16 v[148:151], v[100:103], v[84:87], v[148:151]\n\t" \
  "ds_read_b64_tr_b16 v[98:99], v160 offset:34816\n\t" \
  "s_waitcnt lgkmcnt(10)\n\t" \
  "v_mfma_f32_16x16x32_bf16 v[136:139], v[104:107], v[80:83], v[136:139]\n\t" \
  "ds_read_b64_tr_b16 v[100:101], v161 offset:32768\n\t" \
  "v_mfma_f32_16x16x32_bf16 v[152:155], v[104:107], v[84:87], v[152:155]\n\t" \
  "ds_read_b64_tr_b16 v[102:103], v161 offset:34816\n\t" \
  "v_mfma_f32_16x16x32_bf16 v[140:143], v[108:111], v[80:83], v[140:143]\n\t" \
  "ds_read_b64_tr_b16 v[104:105], v162 offset:32768\n\t" \
  "buffer_load_dwordx4 v[44:47], v164, %[ob], s78 offen offset:3072 sc1\n\t" \
  "v_mfma_f32_16x16x32_bf16 v[156:159], v[108:111], v[84:87], v[156:159]\n\t" \
  "ds_read_b64_tr_b16 v[106:107], v162 offset:34816\n\t" \
  "s_waitcnt vmcnt(12) lgkmcnt(10)\n\t" \
  "v_mfma_f32_16x16x32_bf16 v[128:131], v[112:115], v[88:91], v[128:131]\n\t" \
  "ds_read_b64_tr_b16 v[108:109], v163 offset:32768\n\t" \
  "v_mfma_f32_16x16x32_bf16 v[144:147], v[112:115], v[92:95], v[144:147]\n\t" \
  "ds_read_b64_tr_b16 v[110:111], v163 offset:34816\n\t" \
  "v_mfma_f32_16x16x32_bf16 v[132:135], v[116:119], v[88:91], v[132:135]\n\t" \
  "ds_read_b64_tr_b16 v[112:113], v160 offset:36864\n\t" \
  "buffer_load_dwordx4 v[48:51], v164, %[ob], s79 offen offset:0 sc1\n\t" \
  "v_mfma_f32_16x16x32_bf16 v[148:151], v[116:119], v[92:95], v[148:151]\n\t" \
  "ds_read_b64_tr_b16 v[114:115], v160 offset:38912\n\t" \
  "s_waitcnt lgkmcnt(10)\n\t" \
  "v_mfma_f32_16x16x32_bf16 v[136:139], v[120:123], v[88:91], v[136:139]\n\t" \
  "ds_read_b64_tr_b16 v[116:117], v161 offset:36864\n\t" \
  "v_mfma_f32_16x16x32_bf16 v[152:155], v[120:123], v[92:95], v[152:155]\n\t" \
  "ds_read_b64_tr_b16 v[118:119], v161 offset:38912\n\t" \
  "v_mfma_f32_16x16x32_bf16 v[140:143], v[124:127], v[88:91], v[140:143]\n\t" \
  "ds_read_b64_tr_b16 v[120:121], v162 offset:36864\n\t" \
  "v_mfma_f32_16x16x32_bf16 v[156:159], v[124:127], v[92:95], v[156:159]\n\t" \
  "ds_read_b64_tr_b16 v[122:123], v162 offset:38912\n\t" \
  "s_waitcnt vmcnt(11) lgkmcnt(10)\n\t" \
  "v_mfma_f32_16x16x32_bf16 v[128:131], v[96:99], v[0:3], v[128:131]\n\t" \
  "ds_read_b64_tr_b16 v[124:125], v163 offset:36864\n\t" \
  "buffer_load_dwordx4 v[52:55], v164, %[ob], s79 offen offset:1024 sc1\n\t" \
  "v_mfma_f32_16x16x32_bf16 v[144:147], v[96:99], v[4:7], v[144:147]\n\t" \
  "ds_read_b64_tr_b16 v[126:127], v163 offset:38912\n\t" \
  "v_mfma_f32_16x16x32_bf16 v[132:135], v[100:103], v[0:3], v[132:135]\n\t" \
  "ds_read_b64_tr_b16 v[96:97], v160 offset:40960\n\t" \
  "v_mfma_f32_16x16x32_bf16 v[148:151], v[100:103], v[4:7], v[148:151]\n\t" \
  "ds_read_b64_tr_b16 v[98:99], v160 offset:43008\n\t" \
  "s_waitcnt lgkmcnt(10)\n\t" \
  "v_mfma_f32_16x16x32_bf16 v[136:139], v[104:107], v[0:3], v[136:139]\n\t" \
  "ds_read_b64_tr_b16 v[100:101], v161 offset:40960\n\t" \
  "buffer_load_dwordx4 v[56:59], v164, %[ob], s79 offen offset:2048 sc1\n\t" \
  "v_mfma_f32_16x16x32_bf16 v[152:155], v[104:107], v[4:7], v[152:155]\n\t" \
  "ds_read_b64_tr_b16 v[102:103], v161 offset:43008\n\t" \
  "v_mfma_f32_16x16x32_bf16 v[140:143], v[108:111], v[0:3], v[140:143]\n\t" \
  "ds_read_b64_tr_b16 v[104:105], v162 offset:40960\n\t" \
  "v_mfma_f32_16x16x32_bf16 v[156:159], v[108:111], v[4:7], v[156:159]\n\t" \
  "ds_read_b64_tr_b16 v[106:107], v162 offset:43008\n\t" \
  "s_waitcnt vmcnt(11) lgkmcnt(10)\n\t" \
  "v_mfma_f32_16x16x32_bf16 v[128:131], v[112:115], v[8:11], v[128:131]\n\t" \
  "ds_read_b64_tr_b16 v[108:109], v163 offset:40960\n\t" \
  "buffer_load_dwordx4 v[60:63], v164, %[ob], s79 offen offset:3072 sc1\n\t" \
  "v_mfma_f32_16x16x32_bf16 v[144:147], v[112:115], v[12:15], v[144:147]\n\t" \
  "ds_read_b64_tr_b16 v[110:111], v163 offset:43008\n\t" \
  "v_mfma_f32_16x16x32_bf16 v[132:135], v[116:119], v[8:11], v[132:135]\n\t" \
  "ds_read_b64_tr_b16 v[112:113], v160 offset:45056\n\t" \
  "v_mfma_f32_16x16x32_bf16 v[148:151], v[116:119], v[12:15], v[148:151]\n\t" \
  "ds_read_b64_tr_b16 v[114:115], v160 offset:47104\n\t" \
  "s_waitcnt lgkmcnt(10)\n\t" \
  "v_mfma_f32_16x16x32_bf16 v[136:139], v[120:123], v[8:11], v[136:139]\n\t" \
  "ds_read_b64_tr_b16 v[116:117], v161 offset:45056\n\t" \
  "v_mfma_f32_16x16x32_bf16 v[152:155], v[120:123], v[12:15], v[152:155]\n\t" \
  "ds_read_b64_tr_b16 v[118:119], v161 offset:47104\n\t" \
  "v_mfma_f32_16x16x32_bf16 v[140:143], v[124:127], v[8:11], v[140:143]\n\t" \
  "ds_read_b64_tr_b16 v[120:121], v162 offset:45056\n\t" \
  "v_mfma_f32_16x16x32_bf16 v[156:159], v[124:127], v[12:15], v[156:159]\n\t" \
  "ds_read_b64_tr_b16 v[122:123], v162 offset:47104\n\t" \
  "s_waitcnt vmcnt(10) lgkmcnt(10)\n\t" \
  "v_mfma_f32_16x16x32_bf16 v[128:131], v[96:99], v[16:19], v[128:131]\n\t" \
  "ds_read_b64_tr_b16 v[124:125], v163 offset:45056\n\t" \
  "v_mfma_f32_16x16x32_bf16 v[144:147], v[96:99], v[20:23], v[144:147]\n\t" \
  "ds_read_b64_tr_b16 v[126:127], v163 offset:47104\n\t" \
  "v_mfma_f32_16x16x32_bf16 v[132:135], v[100:103], v[16:19], v[132:135]\n\t" \
  "ds_read_b64_tr_b16 v[96:97], v160 offset:49152\n\t" \
  "v_mfma_f32_16x16x32_bf16 v[148:151], v[100:103], v[20:23], v[148:151]\n\t" \
  "ds_read_b64_tr_b16 v[98:99], v160 offset:51200\n\t" \
  "s_waitcnt lgkmcnt(10)\n\t" \
  "v_mfma_f32_16x16x32_bf16 v[136:139], v[104:107], v[16:19], v[136:139]\n\t" \
  "ds_read_b64_tr_b16 v[100:101], v161 offset:49152\n\t" \
  "v_xor_b32 v0, 32, v167\n\t" \
  "v_mfma_f32_16x16x32_bf16 v[152:155], v[104:107], v[20:23], v[152:155]\n\t" \
  "ds_read_b64_tr_b16 v[102:103], v161 offset:51200\n\t" \
  "v_xor_b32 v1, 64, v167\n\t" \
  "v_mfma_f32_16x16x32_bf16 v[140:143], v[108:111], v[16:19], v[140:143]\n\t" \
  "ds_read_b64_tr_b16 v[104:105], v162 offset:49152\n\t" \
  "v_xor_b32 v2, 0x60, v167\n\t" \
  "v_mfma_f32_16x16x32_bf16 v[156:159], v[108:111], v[20:23], v[156:159]\n\t" \
  "ds_read_b64_tr_b16 v[106:107], v162 offset:51200\n\t" \
  "s_waitcnt vmcnt(8) lgkmcnt(10)\n\t" \
  "v_mfma_f32_16x16x32_bf16 v[128:131], v[112:115], v[24:27], v[128:131]\n\t" \
  "ds_read_b64_tr_b16 v[108:109], v163 offset:49152\n\t" \
  "v_bfe_u32 v5, v164, 7, 3\n\t" \
  "v_mfma_f32_16x16x32_bf16 v[144:147], v[112:115], v[28:31], v[144:147]\n\t" \
  "ds_read_b64_tr_b16 v[110:111], v163 offset:51200\n\t" \
  "v_and_b32 v4, 0x70, v164\n\t" \
  "v_mfma_f32_16x16x32_bf16 v[132:135], v[116:119], v[24:27], v[132:135]\n\t" \
  "ds_read_b64_tr_b16 v[112:113], v160 offset:53248\n\t" \
  "v_lshl_or_b32 v4, v5, 12, v4\n\t" \
  "v_mfma_f32_16x16x32_bf16 v[148:151], v[116:119], v[28:31], v[148:151]\n\t" \
  "ds_read_b64_tr_b16 v[114:115], v160 offset:55296\n\t" \
  "s_waitcnt lgkmcnt(10)\n\t" \
  "v_mfma_f32_16x16x32_bf16 v[136:139], v[120:123], v[24:27], v[136:139]\n\t" \
  "ds_read_b64_tr_b16 v[116:117], v161 offset:53248\n\t" \
  "v_add_u32 v5, 0x8000, v4\n\t" \
  "v_mfma_f32_16x16x32_bf16 v[152:155], v[120:123], v[28:31], v[152:155]\n\t" \
  "ds_read_b64_tr_b16 v[118:119], v161 offset:55296\n\t" \
  "v_add_u32 v6, 0x10000, v4\n\t" \
  "v_mfma_f32_16x16x32_bf16 v[140:143], v[124:127], v[24:27], v[140:143]\n\t" \
  "ds_read_b64_tr_b16 v[120:121], v162 offset:53248\n\t" \
  "v_add_u32 v7, 0x18000, v4\n\t" \
  "v_mfma_f32_16x16x32_bf16 v[156:159], v[124:127], v[28:31], v[156:159]\n\t" \
  "ds_read_b64_tr_b16 v[122:123], v162 offset:55296\n\t" \
  "s_waitcnt vmcnt(6) lgkmcnt(10)\n\t" \
  "v_mfma_f32_16x16x32_bf16 v[128:131], v[96:99], v[32:35], v[128:131]\n\t" \
  "ds_read_b64_tr_b16 v[124:125], v163 offset:53248\n\t" \
  "v_mfma_f32_16x16x32_bf16 v[144:147], v[96:99], v[36:39], v[144:147]\n\t" \
  "ds_read_b64_tr_b16 v[126:127], v163 offset:55296\n\t" \
  "v_mfma_f32_16x16x32_bf16 v[132:135], v[100:103], v[32:35], v[132:135]\n\t" \
  "ds_read_b64_tr_b16 v[96:97], v160 offset:57344\n\t" \
  "v_mfma_f32_16x16x32_bf16 v[148:151], v[100:103], v[36:39], v[148:151]\n\t" \
  "ds_read_b64_tr_b16 v[98:99], v160 offset:59392\n\t" \
  "s_waitcnt lgkmcnt(10)\n\t" \
  "v_mfma_f32_16x16x32_bf16 v[136:139], v[104:107], v[32:35], v[136:139]\n\t" \
  "ds_read_b64_tr_b16 v[100:101], v161 offset:57344\n\t" \
  "v_mfma_f32_16x16x32_bf16 v[152:155], v[104:107], v[36:39], v[152:155]\n\t" \
  "ds_read_b64_tr_b16 v[102:103], v161 offset:59392\n\t" \
  "v_mfma_f32_16x16x32_bf16 v[140:143], v[108:111], v[32:35], v[140:143]\n\t" \
  "ds_read_b64_tr_b16 v[104:105], v162 offset:57344\n\t" \
  "v_mfma_f32_16x16x32_bf16 v[156:159], v[108:111], v[36:39], v[156:159]\n\t" \
  "ds_read_b64_tr_b16 v[106:107], v162 offset:59392\n\t" \
  "s_waitcnt vmcnt(4) lgkmcnt(10)\n\t" \
  "v_mfma_f32_16x16x32_bf16 v[128:131], v[112:115], v[40:43], v[128:131]\n\t" \
  "ds_read_b64_tr_b16 v[108:109], v163 offset:57344\n\t" \
  "v_mfma_f32_16x16x32_bf16 v[144:147], v[112:115], v[44:47], v[144:147]\n\t" \
  "ds_read_b64_tr_b16 v[110:111], v163 offset:59392\n\t" \
  "v_mfma_f32_16x16x32_bf16 v[132:135], v[116:119], v[40:43], v[132:135]\n\t" \
  "ds_read_b64_tr_b16 v[112:113], v160 offset:61440\n\t" \
  "v_mfma_f32_16x16x32_bf16 v[148:151], v[116:119], v[44:47], v[148:151]\n\t" \
  "ds_read_b64_tr_b16 v[114:115], v160 offset:63488\n\t" \
  "s_waitcnt lgkmcnt(10)\n\t" \
  "v_mfma_f32_16x16x32_bf16 v[136:139], v[120:123], v[40:43], v[136:139]\n\t" \
  "ds_read_b64_tr_b16 v[116:117], v161 offset:61440\n\t" \
  "v_mfma_f32_16x16x32_bf16 v[152:155], v[120:123], v[44:47], v[152:155]\n\t" \
  "ds_read_b64_tr_b16 v[118:119], v161 offset:63488\n\t" \
  "v_mfma_f32_16x16x32_bf16 v[140:143], v[124:127], v[40:43], v[140:143]\n\t" \
  "ds_read_b64_tr_b16 v[120:121], v162 offset:61440\n\t" \
  "v_mfma_f32_16x16x32_bf16 v[156:159], v[124:127], v[44:47], v[156:159]\n\t" \
  "ds_read_b64_tr_b16 v[122:123], v162 offset:63488\n\t" \
  "s_waitcnt vmcnt(0) lgkmcnt(10)\n\t" \
  "v_mfma_f32_16x16x32_bf16 v[128:131], v[96:99], v[48:51], v[128:131]\n\t" \
  "ds_read_b64_tr_b16 v[124:125], v163 offset:61440\n\t" \
  "v_mfma_f32_16x16x32_bf16 v[132:135], v[100:103], v[48:51], v[132:135]\n\t" \
  "ds_read_b64_tr_b16 v[126:127], v163 offset:63488\n\t" \
  "s_waitcnt lgkmcnt(6)\n\t" \
  "v_mfma_f32_16x16x32_bf16 v[136:139], v[104:107], v[48:51], v[136:139]\n\t" \
  "v_mfma_f32_16x16x32_bf16 v[140:143], v[108:111], v[48:51], v[140:143]\n\t" \
  "v_mfma_f32_16x16x32_bf16 v[128:131], v[112:115], v[56:59], v[128:131]\n\t" \
  "s_waitcnt lgkmcnt(2)\n\t" \
  "v_mfma_f32_16x16x32_bf16 v[132:135], v[116:119], v[56:59], v[132:135]\n\t" \
  "v_mfma_f32_16x16x32_bf16 v[136:139], v[120:123], v[56:59], v[136:139]\n\t" \
  "v_mfma_f32_16x16x32_bf16 v[144:147], v[96:99], v[52:55], v[144:147]\n\t" \
  "s_waitcnt lgkmcnt(0)\n\t" \
  "v_mfma_f32_16x16x32_bf16 v[140:143], v[124:127], v[56:59], v[140:143]\n\t" \
  "v_mfma_f32_16x16x32_bf16 v[144:147], v[112:115], v[60:63], v[144:147]\n\t" \
  "s_nop 0\n\t" \
  "v_cvt_pk_bf16_f32 v16, v128, v129\n\t" \
  "v_mfma_f32_16x16x32_bf16 v[148:151], v[100:103], v[52:55], v[148:151]\n\t" \
  "v_cvt_pk_bf16_f32 v17, v130, v131\n\t" \
  "v_cvt_pk_bf16_f32 v18, v132, v133\n\t" \
  "v_mfma_f32_16x16x32_bf16 v[148:151], v[116:119], v[60:63], v[148:151]\n\t" \
  "v_cvt_pk_bf16_f32 v19, v134, v135\n\t" \
  "ds_write_b64 v167, v[16:17]\n\t" \
  "v_mfma_f32_16x16x32_bf16 v[152:155], v[104:107], v[52:55], v[152:155]\n\t" \
  "v_cvt_pk_bf16_f32 v20, v136, v137\n\t" \
  "ds_write_b64 v0, v[18:19]\n\t" \
  "v_mfma_f32_16x16x32_bf16 v[152:155], v[120:123], v[60:63], v[152:155]\n\t" \
  "v_cvt_pk_bf16_f32 v21, v138, v139\n\t" \
  "v_cvt_pk_bf16_f32 v22, v140, v141\n\t" \
  "v_mfma_f32_16x16x32_bf16 v[156:159], v[108:111], v[52:55], v[156:159]\n\t" \
  "v_cvt_pk_bf16_f32 v23, v142, v143\n\t" \
  "ds_write_b64 v1, v[20:21]\n\t" \
  "v_mfma_f32_16x16x32_bf16 v[156:159], v[124:127], v[60:63], v[156:159]\n\t" \
  "v_cvt_pk_bf16_f32 v24, v144, v145\n\t" \
  "ds_write_b64 v2, v[22:23]\n\t" \
  "v_cvt_pk_bf16_f32 v25, v146, v147\n\t" \
  "ds_read_b128 v[64:67], v166 offset:0\n\t" \
  "v_cvt_pk_bf16_f32 v26, v148, v149\n\t" \
  "ds_read_b128 v[68:71], v166 offset:1024\n\t" \
  "v_cvt_pk_bf16_f32 v27, v150, v151\n\t" \
  "ds_write_b64 v167, v[24:25]\n\t" \
  "v_cvt_pk_bf16_f32 v28, v152, v153\n\t" \
  "v_cvt_pk_bf16_f32 v29, v154, v155\n\t" \
  "ds_write_b64 v0, v[26:27]\n\t" \
  "v_cvt_pk_bf16_f32 v30, v156, v157\n\t" \
  "v_cvt_pk_bf16_f32 v31, v158, v159\n\t" \
  "ds_write_b64 v1, v[28:29]\n\t" \
  "ds_write_b64 v2, v[30:31]\n\t" \
  "ds_read_b128 v[72:75], v166 offset:0\n\t" \
  "ds_read_b128 v[76:79], v166 offset:1024\n\t" \
  "s_waitcnt lgkmcnt(7)\n\t" \
  "global_store_dwordx4 v4, v[64:67], %[op] sc1\n\t" \
  "s_waitcnt lgkmcnt(6)\n\t" \
  "global_store_dwordx4 v5, v[68:71], %[op] sc1\n\t" \
  "s_waitcnt lgkmcnt(1)\n\t" \
  "global_store_dwordx4 v6, v[72:75], %[op] sc1\n\t" \
  "s_waitcnt lgkmcnt(0)\n\t" \
  "global_store_dwordx4 v7, v[76:79], %[op] sc1\n\t" \
  "s_nop 1\n\t"
#define G3_EP 2
#define G3_CLOB "v0", "v1", "v2", "v3", "v4", "v5", "v6", "v7", "v8", "v9", "v10", "v11", "v12", "v13", "v14", "v15", "v16", "v17", "v18", "v19", "v20", "v21", "v22", "v23", "v24", "v25", "v26", "v27", "v28", "v29", "v30", "v31", "v32", "v33", "v34", "v35", "v36", "v37", "v38", "v39", "v40", "v41", "v42", "v43", "v44", "v45", "v46", "v47", "v48", "v49", "v50", "v51", "v52", "v53", "v54", "v55", "v56", "v57", "v58", "v59", "v60", "v61", "v62", "v63", "v64", "v65", "v66", "v67", "v68", "v69", "v70", "v71", "v72", "v73", "v74", "v75", "v76", "v77", "v78", "v79", "v80", "v81", "v82", "v83", "v84", "v85", "v86", "v87", "v88", "v89", "v90", "v91", "v92", "v93", "v94", "v95", "v97", "v98", "v99", "v100", "v101", "v102", "v103", "v104", "v105", "v106", "v107", "v108", "v109", "v110", "v111", "v112", "v113", "v114", "v115", "v116", "v117", "v118", "v119", "v120", "v121", "v122", "v123", "v124", "v125", "v126", "v127", "v128", "v129", "v130", "v131", "v132", "v133", "v134", "v135", "v136", "v137", "v138", "v139", "v140", "v141", "v142", "v143", "v144", "v145", "v146", "v147", "v148", "v149", "v150", "v151", "v152", "v153", "v154", "v155", "v156", "v157", "v158", "v159", "s64", "s65", "s66", "s67", "s68", "s69", "s70", "s71", "s72", "s73", "s74", "s75", "s76", "s77", "s78", "s79"

#define PP_ASM \
  "v_mov_b32 v60, 0\n\t" \
  "s_mov_b32 %[t0], %[n0]\n\t" \
  "s_cmp_eq_u32 %[t0], 0\n\t" \
  "s_cbranch_scc1 PAx_%=\n\t" \
  "PA_%=:\n\t" \
  "s_barrier\n\t" \
  "s_sub_u32 %[t0], %[t0], 1\n\t" \
  "s_cmp_lg_u32 %[t0], 0\n\t" \
  "s_cbranch_scc1 PA_%=\n\t" \
  "PAx_%=:\n\t" \
  "s_mov_b32 m0, %[shd]\n\t" \
  "global_load_dword v160, v60, %[fl] sc1\n\t" \
  "global_load_lds_dwordx4 v163, %[shp]\n\t" \
  "s_mov_b32 m0, %[tgd]\n\t" \
  "s_mov_b64 exec, %[em]\n\t" \
  "global_load_lds_dword v60, %[fl] sc1\n\t" \
  "s_mov_b64 exec, -1\n\t" \
  "global_load_ushort v161, v60, %[hp]\n\t" \
  "global_load_ushort v162, v60, %[dp]\n\t" \
  "global_load_dwordx4 v[0:3], v152, %[kb0]\n\t" \
  "global_load_dwordx4 v[4:7], v153, %[kb0]\n\t" \
  "global_load_dwordx4 v[8:11], v154, %[kb0]\n\t" \
  "global_load_dwordx4 v[12:15], v155, %[kb3] offset:0\n\t" \
  "global_load_dwordx4 v[28:31], v155, %[vb0] offset:0\n\t" \
  "s_setprio 2\n\t" \
  "s_setprio 3\n\t" \
  "s_barrier\n\t" \
  "global_load_dwordx4 v[16:19], v155, %[kb3] offset:2048\n\t" \
  "global_load_dwordx4 v[32:35], v155, %[vb0] offset:2048\n\t" \
  "global_load_dwordx4 v[20:23], v155, %[kb5] offset:0\n\t" \
  "s_setprio 2\n\t" \
  "s_waitcnt vmcnt(8)\n\t" \
  "ds_read_b128 v[128:131], v156 offset:0\n\t" \
  "ds_read_b128 v[132:135], v156 offset:16\n\t" \
  "ds_read_b128 v[136:139], v156 offset:32\n\t" \
  "ds_read_b128 v[140:143], v156 offset:48\n\t" \
  "s_waitcnt lgkmcnt(0)\n\t" \
  "v_lshlrev_b32 v64, 16, v128\n\t" \
  "v_and_b32 v65, 0xffff0000, v128\n\t" \
  "v_lshlrev_b32 v66, 16, v129\n\t" \
  "v_and_b32 v67, 0xffff0000, v129\n\t" \
  "v_lshlrev_b32 v68, 16, v130\n\t" \
  "v_and_b32 v69, 0xffff0000, v130\n\t" \
  "v_lshlrev_b32 v70, 16, v131\n\t" \
  "v_and_b32 v71, 0xffff0000, v131\n\t" \
  "v_lshlrev_b32 v72, 16, v132\n\t" \
  "v_and_b32 v73, 0xffff0000, v132\n\t" \
  "v_lshlrev_b32 v74, 16, v133\n\t" \
  "v_and_b32 v75, 0xffff0000, v133\n\t" \
  "v_lshlrev_b32 v76, 16, v134\n\t" \
  "v_and_b32 v77, 0xffff0000, v134\n\t" \
  "v_lshlrev_b32 v78, 16, v135\n\t" \
  "v_and_b32 v79, 0xffff0000, v135\n\t" \
  "v_lshlrev_b32 v80, 16, v136\n\t" \
  "v_and_b32 v81, 0xffff0000, v136\n\t" \
  "v_lshlrev_b32 v82, 16, v137\n\t" \
  "v_and_b32 v83, 0xffff0000, v137\n\t" \
  "v_lshlrev_b32 v84, 16, v138\n\t" \
  "v_and_b32 v85, 0xffff0000, v138\n\t" \
  "v_lshlrev_b32 v86, 16, v139\n\t" \
  "v_and_b32 v87, 0xffff0000, v139\n\t" \
  "v_lshlrev_b32 v88, 16, v140\n\t" \
  "v_and_b32 v89, 0xffff0000, v140\n\t" \
  "v_lshlrev_b32 v90, 16, v141\n\t" \
  "v_and_b32 v91, 0xffff0000, v141\n\t" \
  "v_lshlrev_b32 v92, 16, v142\n\t" \
  "v_and_b32 v93, 0xffff0000, v142\n\t" \
  "v_lshlrev_b32 v94, 16, v143\n\t" \
  "v_and_b32 v95, 0xffff0000, v143\n\t" \
  "s_waitcnt vmcnt(7)\n\t" \
  "s_mov_b64 exec, %[zm]\n\t" \
  "v_mov_b32 v0, 0\n\t" \
  "v_mov_b32 v1, 0\n\t" \
  "v_mov_b32 v2, 0\n\t" \
  "v_mov_b32 v3, 0\n\t" \
  "s_mov_b64 exec, -1\n\t" \
  "v_lshlrev_b32 v128, 16, v0\n\t" \
  "v_and_b32 v129, 0xffff0000, v0\n\t" \
  "v_lshlrev_b32 v130, 16, v1\n\t" \
  "v_and_b32 v131, 0xffff0000, v1\n\t" \
  "v_lshlrev_b32 v132, 16, v2\n\t" \
  "v_and_b32 v133, 0xffff0000, v2\n\t" \
  "v_lshlrev_b32 v134, 16, v3\n\t" \
  "v_and_b32 v135, 0xffff0000, v3\n\t" \
  "v_mul_f32 v96, v128, v67\n\t" \
  "v_mul_f32 v97, v129, v71\n\t" \
  "v_mul_f32 v98, v130, v75\n\t" \
  "v_mul_f32 v99, v131, v79\n\t" \
  "v_mul_f32 v100, v132, v83\n\t" \
  "v_mul_f32 v101, v133, v87\n\t" \
  "v_mul_f32 v102, v134, v91\n\t" \
  "v_mul_f32 v103, v135, v95\n\t" \
  "s_waitcnt vmcnt(6)\n\t" \
  "s_mov_b64 exec, %[zm]\n\t" \
  "v_mov_b32 v4, 0\n\t" \
  "v_mov_b32 v5, 0\n\t" \
  "v_mov_b32 v6, 0\n\t" \
  "v_mov_b32 v7, 0\n\t" \
  "s_mov_b64 exec, -1\n\t" \
  "v_lshlrev_b32 v128, 16, v4\n\t" \
  "v_and_b32 v129, 0xffff0000, v4\n\t" \
  "v_lshlrev_b32 v130, 16, v5\n\t" \
  "v_and_b32 v131, 0xffff0000, v5\n\t" \
  "v_lshlrev_b32 v132, 16, v6\n\t" \
  "v_and_b32 v133, 0xffff0000, v6\n\t" \
  "v_lshlrev_b32 v134, 16, v7\n\t" \
  "v_and_b32 v135, 0xffff0000, v7\n\t" \
  "v_fmac_f32 v96, v128, v66\n\t" \
  "v_fmac_f32 v97, v129, v70\n\t" \
  "v_fmac_f32 v98, v130, v74\n\t" \
  "v_fmac_f32 v99, v131, v78\n\t" \
  "v_fmac_f32 v100, v132, v82\n\t" \
  "v_fmac_f32 v101, v133, v86\n\t" \
  "v_fmac_f32 v102, v134, v90\n\t" \
  "v_fmac_f32 v103, v135, v94\n\t" \
  "v_mul_f32 v104, v128, v67\n\t" \
  "v_mul_f32 v105, v129, v71\n\t" \
  "v_mul_f32 v106, v130, v75\n\t" \
  "v_mul_f32 v107, v131, v79\n\t" \
  "v_mul_f32 v108, v132, v83\n\t" \
  "v_mul_f32 v109, v133, v87\n\t" \
  "v_mul_f32 v110, v134, v91\n\t" \
  "v_mul_f32 v111, v135, v95\n\t" \
  "s_waitcnt vmcnt(5)\n\t" \
  "s_mov_b64 exec, %[zm]\n\t" \
  "v_mov_b32 v8, 0\n\t" \
  "v_mov_b32 v9, 0\n\t" \
  "v_mov_b32 v10, 0\n\t" \
  "v_mov_b32 v11, 0\n\t" \
  "s_mov_b64 exec, -1\n\t" \
  "s_mov_b32 s85, 0x300000\n\t" \
  "s_load_dword s84, %[ph], s85\n\t" \
  "s_mov_b32 s85, 0x300800\n\t" \
  "s_load_dword s84, %[ph], s85\n\t" \
  "s_mov_b32 s85, 0x301000\n\t" \
  "s_load_dword s84, %[ph], s85\n\t" \
  "s_mov_b32 s85, 0x360000\n\t" \
  "s_load_dword s84, %[ph], s85\n\t" \
  "s_setprio 3\n\t" \
  "s_barrier\n\t" \
  "global_load_dwordx4 v[36:39], v155, %[vb2] offset:0\n\t" \
  "global_load_dwordx4 v[24:27], v155, %[kb5] offset:2048\n\t" \
  "global_load_dwordx4 v[40:43], v155, %[vb2] offset:2048\n\t" \
  "s_setprio 2\n\t" \
  "v_lshlrev_b32 v128, 16, v8\n\t" \
  "v_and_b32 v129, 0xffff0000, v8\n\t" \
  "v_lshlrev_b32 v130, 16, v9\n\t" \
  "v_and_b32 v131, 0xffff0000, v9\n\t" \
  "v_lshlrev_b32 v132, 16, v10\n\t" \
  "v_and_b32 v133, 0xffff0000, v10\n\t" \
  "v_lshlrev_b32 v134, 16, v11\n\t" \
  "v_and_b32 v135, 0xffff0000, v11\n\t" \
  "v_fmac_f32 v96, v128, v65\n\t" \
  "v_fmac_f32 v97, v129, v69\n\t" \
  "v_fmac_f32 v98, v130, v73\n\t" \
  "v_fmac_f32 v99, v131, v77\n\t" \
  "v_fmac_f32 v100, v132, v81\n\t" \
  "v_fmac_f32 v101, v133, v85\n\t" \
  "v_fmac_f32 v102, v134, v89\n\t" \
  "v_fmac_f32 v103, v135, v93\n\t" \
  "v_fmac_f32 v104, v128, v66\n\t" \
  "v_fmac_f32 v105, v129, v70\n\t" \
  "v_fmac_f32 v106, v130, v74\n\t" \
  "v_fmac_f32 v107, v131, v78\n\t" \
  "v_fmac_f32 v108, v132, v82\n\t" \
  "v_fmac_f32 v109, v133, v86\n\t" \
  "v_fmac_f32 v110, v134, v90\n\t" \
  "v_fmac_f32 v111, v135, v94\n\t" \
  "v_mul_f32 v112, v128, v67\n\t" \
  "v_mul_f32 v113, v129, v71\n\t" \
  "v_mul_f32 v114, v130, v75\n\t" \
  "v_mul_f32 v115, v131, v79\n\t" \
  "v_mul_f32 v116, v132, v83\n\t" \
  "v_mul_f32 v117, v133, v87\n\t" \
  "v_mul_f32 v118, v134, v91\n\t" \
  "v_mul_f32 v119, v135, v95\n\t" \
  "s_waitcnt vmcnt(7)\n\t" \
  "v_lshlrev_b32 v128, 16, v12\n\t" \
  "v_and_b32 v129, 0xffff0000, v12\n\t" \
  "v_lshlrev_b32 v130, 16, v13\n\t" \
  "v_and_b32 v131, 0xffff0000, v13\n\t" \
  "v_lshlrev_b32 v132, 16, v14\n\t" \
  "v_and_b32 v133, 0xffff0000, v14\n\t" \
  "v_lshlrev_b32 v134, 16, v15\n\t" \
  "v_and_b32 v135, 0xffff0000, v15\n\t" \
  "v_fmac_f32 v96, v128, v64\n\t" \
  "v_fmac_f32 v97, v129, v68\n\t" \
  "v_fmac_f32 v98, v130, v72\n\t" \
  "v_fmac_f32 v99, v131, v76\n\t" \
  "v_fmac_f32 v100, v132, v80\n\t" \
  "v_fmac_f32 v101, v133, v84\n\t" \
  "v_fmac_f32 v102, v134, v88\n\t" \
  "v_fmac_f32 v103, v135, v92\n\t" \
  "v_fmac_f32 v104, v128, v65\n\t" \
  "v_fmac_f32 v105, v129, v69\n\t" \
  "v_fmac_f32 v106, v130, v73\n\t" \
  "v_fmac_f32 v107, v131, v77\n\t" \
  "v_fmac_f32 v108, v132, v81\n\t" \
  "v_fmac_f32 v109, v133, v85\n\t" \
  "v_fmac_f32 v110, v134, v89\n\t" \
  "v_fmac_f32 v111, v135, v93\n\t" \
  "v_fmac_f32 v112, v128, v66\n\t" \
  "v_fmac_f32 v113, v129, v70\n\t" \
  "v_fmac_f32 v114, v130, v74\n\t" \
  "v_fmac_f32 v115, v131, v78\n\t" \
  "v_fmac_f32 v116, v132, v82\n\t" \
  "v_fmac_f32 v117, v133, v86\n\t" \
  "v_fmac_f32 v118, v134, v90\n\t" \
  "v_fmac_f32 v119, v135, v94\n\t" \
  "v_mul_f32 v120, v128, v67\n\t" \
  "v_mul_f32 v121, v129, v71\n\t" \
  "v_mul_f32 v122, v130, v75\n\t" \
  "v_mul_f32 v123, v131, v79\n\t" \
  "v_mul_f32 v124, v132, v83\n\t" \
  "v_mul_f32 v125, v133, v87\n\t" \
  "v_mul_f32 v126, v134, v91\n\t" \
  "v_mul_f32 v127, v135, v95\n\t" \
  "s_waitcnt vmcnt(6)\n\t" \
  "v_lshlrev_b32 v128, 16, v28\n\t" \
  "v_and_b32 v129, 0xffff0000, v28\n\t" \
  "v_lshlrev_b32 v130, 16, v29\n\t" \
  "v_and_b32 v131, 0xffff0000, v29\n\t" \
  "v_lshlrev_b32 v132, 16, v30\n\t" \
  "v_and_b32 v133, 0xffff0000, v30\n\t" \
  "v_lshlrev_b32 v134, 16, v31\n\t" \
  "v_and_b32 v135, 0xffff0000, v31\n\t" \
  "s_mov_b32 s85, 0x360800\n\t" \
  "s_load_dword s84, %[ph], s85\n\t" \
  "s_mov_b32 s85, 0x361000\n\t" \
  "s_load_dword s84, %[ph], s85\n\t" \
  "s_mov_b32 s85, 0x3c0000\n\t" \
  "s_load_dword s84, %[ph], s85\n\t" \
  "s_mov_b32 s85, 0x3c0800\n\t" \
  "s_load_dword s84, %[ph], s85\n\t" \
  "s_barrier\n\t" \
  "v_mul_f32 v96, v96, v128\n\t" \
  "v_mul_f32 v97, v97, v129\n\t" \
  "v_mul_f32 v98, v98, v130\n\t" \
  "v_mul_f32 v99, v99, v131\n\t" \
  "v_mul_f32 v100, v100, v132\n\t" \
  "v_mul_f32 v101, v101, v133\n\t" \
  "v_mul_f32 v102, v102, v134\n\t" \
  "v_mul_f32 v103, v103, v135\n\t" \
  "v_cvt_pk_bf16_f32 v136, v96, v97\n\t" \
  "v_cvt_pk_bf16_f32 v137, v98, v99\n\t" \
  "v_cvt_pk_bf16_f32 v138, v100, v101\n\t" \
  "v_cvt_pk_bf16_f32 v139, v102, v103\n\t" \
  "v_mov_b32_dpp v128, v136 row_ror:8 row_mask:0xf bank_mask:0xf\n\t" \
  "v_mov_b32_dpp v129, v137 row_ror:8 row_mask:0xf bank_mask:0xf\n\t" \
  "v_mov_b32_dpp v130, v138 row_ror:8 row_mask:0xf bank_mask:0xf\n\t" \
  "v_mov_b32_dpp v131, v139 row_ror:8 row_mask:0xf bank_mask:0xf\n\t" \
  "v_perm_b32 v136, v128, v136, v157\n\t" \
  "v_perm_b32 v137, v129, v137, v157\n\t" \
  "v_perm_b32 v138, v130, v138, v157\n\t" \
  "v_perm_b32 v139, v131, v139, v157\n\t" \
  "v_permlane16_swap_b32 v136, v137\n\t" \
  "s_nop 0\n\t" \
  "v_permlane16_swap_b32 v138, v139\n\t" \
  "s_nop 1\n\t" \
  "v_permlane32_swap_b32 v136, v138\n\t" \
  "v_permlane32_swap_b32 v137, v139\n\t" \
  "s_waitcnt vmcnt(5)\n\t" \
  "v_lshlrev_b32 v128, 16, v16\n\t" \
  "v_and_b32 v129, 0xffff0000, v16\n\t" \
  "v_lshlrev_b32 v130, 16, v17\n\t" \
  "v_and_b32 v131, 0xffff0000, v17\n\t" \
  "v_lshlrev_b32 v132, 16, v18\n\t" \
  "v_and_b32 v133, 0xffff0000, v18\n\t" \
  "v_lshlrev_b32 v134, 16, v19\n\t" \
  "v_and_b32 v135, 0xffff0000, v19\n\t" \
  "v_fmac_f32 v104, v128, v64\n\t" \
  "v_fmac_f32 v105, v129, v68\n\t" \
  "v_fmac_f32 v106, v130, v72\n\t" \
  "v_fmac_f32 v107, v131, v76\n\t" \
  "v_fmac_f32 v108, v132, v80\n\t" \
  "v_fmac_f32 v109, v133, v84\n\t" \
  "v_fmac_f32 v110, v134, v88\n\t" \
  "v_fmac_f32 v111, v135, v92\n\t" \
  "v_fmac_f32 v112, v128, v65\n\t" \
  "v_fmac_f32 v113, v129, v69\n\t" \
  "v_fmac_f32 v114, v130, v73\n\t" \
  "v_fmac_f32 v115, v131, v77\n\t" \
  "v_fmac_f32 v116, v132, v81\n\t" \
  "v_fmac_f32 v117, v133, v85\n\t" \
  "v_fmac_f32 v118, v134, v89\n\t" \
  "v_fmac_f32 v119, v135, v93\n\t" \
  "v_fmac_f32 v120, v128, v66\n\t" \
  "v_fmac_f32 v121, v129, v70\n\t" \
  "v_fmac_f32 v122, v130, v74\n\t" \
  "v_fmac_f32 v123, v131, v78\n\t" \
  "v_fmac_f32 v124, v132, v82\n\t" \
  "v_fmac_f32 v125, v133, v86\n\t" \
  "v_fmac_f32 v126, v134, v90\n\t" \
  "v_fmac_f32 v127, v135, v94\n\t" \
  "s_waitcnt vmcnt(4)\n\t" \
  "v_lshlrev_b32 v128, 16, v32\n\t" \
  "v_and_b32 v129, 0xffff0000, v32\n\t" \
  "v_lshlrev_b32 v130, 16, v33\n\t" \
  "v_and_b32 v131, 0xffff0000, v33\n\t" \
  "v_lshlrev_b32 v132, 16, v34\n\t" \
  "v_and_b32 v133, 0xffff0000, v34\n\t" \
  "v_lshlrev_b32 v134, 16, v35\n\t" \
  "v_and_b32 v135, 0xffff0000, v35\n\t" \
  "v_mul_f32 v104, v104, v128\n\t" \
  "v_mul_f32 v105, v105, v129\n\t" \
  "v_mul_f32 v106, v106, v130\n\t" \
  "v_mul_f32 v107, v107, v131\n\t" \
  "v_mul_f32 v108, v108, v132\n\t" \
  "v_mul_f32 v109, v109, v133\n\t" \
  "v_mul_f32 v110, v110, v134\n\t" \
  "v_mul_f32 v111, v111, v135\n\t" \
  "v_cvt_pk_bf16_f32 v140, v104, v105\n\t" \
  "v_cvt_pk_bf16_f32 v141, v106, v107\n\t" \
  "v_cvt_pk_bf16_f32 v142, v108, v109\n\t" \
  "v_cvt_pk_bf16_f32 v143, v110, v111\n\t" \
  "v_mov_b32_dpp v128, v140 row_ror:8 row_mask:0xf bank_mask:0xf\n\t" \
  "v_mov_b32_dpp v129, v141 row_ror:8 row_mask:0xf bank_mask:0xf\n\t" \
  "s_mov_b32 s85, 0x3c1000\n\t" \
  "s_load_dword s84, %[ph], s85\n\t" \
  "s_mov_b32 s85, 0x420000\n\t" \
  "s_load_dword s84, %[ph], s85\n\t" \
  "s_mov_b32 s85, 0x420800\n\t" \
  "s_load_dword s84, %[ph], s85\n\t" \
  "s_mov_b32 s85, 0x421000\n\t" \
  "s_load_dword s84, %[ph], s85\n\t" \
  "s_setprio 3\n\t" \
  "s_barrier\n\t" \
  "global_store_dwordx4 v159, v[136:139], %[sb] sc1\n\t" \
  "v_add_u32 v158, 0x80, v159\n\t" \
  "v_bfi_b32 v159, %[mk], v158, v159\n\t" \
  "s_setprio 2\n\t" \
  "v_mov_b32_dpp v130, v142 row_ror:8 row_mask:0xf bank_mask:0xf\n\t" \
  "v_mov_b32_dpp v131, v143 row_ror:8 row_mask:0xf bank_mask:0xf\n\t" \
  "v_perm_b32 v140, v128, v140, v157\n\t" \
  "v_perm_b32 v141, v129, v141, v157\n\t" \
  "v_perm_b32 v142, v130, v142, v157\n\t" \
  "v_perm_b32 v143, v131, v143, v157\n\t" \
  "v_permlane16_swap_b32 v140, v141\n\t" \
  "s_nop 0\n\t" \
  "v_permlane16_swap_b32 v142, v143\n\t" \
  "s_nop 1\n\t" \
  "v_permlane32_swap_b32 v140, v142\n\t" \
  "v_permlane32_swap_b32 v141, v143\n\t" \
  "s_waitcnt vmcnt(4)\n\t" \
  "v_lshlrev_b32 v128, 16, v20\n\t" \
  "v_and_b32 v129, 0xffff0000, v20\n\t" \
  "v_lshlrev_b32 v130, 16, v21\n\t" \
  "v_and_b32 v131, 0xffff0000, v21\n\t" \
  "v_lshlrev_b32 v132, 16, v22\n\t" \
  "v_and_b32 v133, 0xffff0000, v22\n\t" \
  "v_lshlrev_b32 v134, 16, v23\n\t" \
  "v_and_b32 v135, 0xffff0000, v23\n\t" \
  "v_fmac_f32 v112, v128, v64\n\t" \
  "v_fmac_f32 v113, v129, v68\n\t" \
  "v_fmac_f32 v114, v130, v72\n\t" \
  "v_fmac_f32 v115, v131, v76\n\t" \
  "v_fmac_f32 v116, v132, v80\n\t" \
  "v_fmac_f32 v117, v133, v84\n\t" \
  "v_fmac_f32 v118, v134, v88\n\t" \
  "v_fmac_f32 v119, v135, v92\n\t" \
  "v_fmac_f32 v120, v128, v65\n\t" \
  "v_fmac_f32 v121, v129, v69\n\t" \
  "v_fmac_f32 v122, v130, v73\n\t" \
  "v_fmac_f32 v123, v131, v77\n\t" \
  "v_fmac_f32 v124, v132, v81\n\t" \
  "v_fmac_f32 v125, v133, v85\n\t" \
  "v_fmac_f32 v126, v134, v89\n\t" \
  "v_fmac_f32 v127, v135, v93\n\t" \
  "s_waitcnt vmcnt(3)\n\t" \
  "v_lshlrev_b32 v128, 16, v36\n\t" \
  "v_and_b32 v129, 0xffff0000, v36\n\t" \
  "v_lshlrev_b32 v130, 16, v37\n\t" \
  "v_and_b32 v131, 0xffff0000, v37\n\t" \
  "v_lshlrev_b32 v132, 16, v38\n\t" \
  "v_and_b32 v133, 0xffff0000, v38\n\t" \
  "v_lshlrev_b32 v134, 16, v39\n\t" \
  "v_and_b32 v135, 0xffff0000, v39\n\t" \
  "v_mul_f32 v112, v112, v128\n\t" \
  "v_mul_f32 v113, v113, v129\n\t" \
  "v_mul_f32 v114, v114, v130\n\t" \
  "v_mul_f32 v115, v115, v131\n\t" \
  "v_mul_f32 v116, v116, v132\n\t" \
  "v_mul_f32 v117, v117, v133\n\t" \
  "v_mul_f32 v118, v118, v134\n\t" \
  "v_mul_f32 v119, v119, v135\n\t" \
  "v_cvt_pk_bf16_f32 v144, v112, v113\n\t" \
  "v_cvt_pk_bf16_f32 v145, v114, v115\n\t" \
  "v_cvt_pk_bf16_f32 v146, v116, v117\n\t" \
  "v_cvt_pk_bf16_f32 v147, v118, v119\n\t" \
  "v_mov_b32_dpp v128, v144 row_ror:8 row_mask:0xf bank_mask:0xf\n\t" \
  "v_mov_b32_dpp v129, v145 row_ror:8 row_mask:0xf bank_mask:0xf\n\t" \
  "v_mov_b32_dpp v130, v146 row_ror:8 row_mask:0xf bank_mask:0xf\n\t" \
  "v_mov_b32_dpp v131, v147 row_ror:8 row_mask:0xf bank_mask:0xf\n\t" \
  "v_perm_b32 v144, v128, v144, v157\n\t" \
  "v_perm_b32 v145, v129, v145, v157\n\t" \
  "v_perm_b32 v146, v130, v146, v157\n\t" \
  "v_perm_b32 v147, v131, v147, v157\n\t" \
  "v_permlane16_swap_b32 v144, v145\n\t" \
  "s_nop 0\n\t" \
  "v_permlane16_swap_b32 v146, v147\n\t" \
  "s_nop 1\n\t" \
  "v_permlane32_swap_b32 v144, v146\n\t" \
  "v_permlane32_swap_b32 v145, v147\n\t" \
  "s_waitcnt vmcnt(2)\n\t" \
  "v_lshlrev_b32 v128, 16, v24\n\t" \
  "v_and_b32 v129, 0xffff0000, v24\n\t" \
  "v_lshlrev_b32 v130, 16, v25\n\t" \
  "v_and_b32 v131, 0xffff0000, v25\n\t" \
  "v_lshlrev_b32 v132, 16, v26\n\t" \
  "v_and_b32 v133, 0xffff0000, v26\n\t" \
  "v_lshlrev_b32 v134, 16, v27\n\t" \
  "v_and_b32 v135, 0xffff0000, v27\n\t" \
  "v_fmac_f32 v120, v128, v64\n\t" \
  "v_fmac_f32 v121, v129, v68\n\t" \
  "s_mov_b32 s85, 0x480000\n\t" \
  "s_load_dword s84, %[ph], s85\n\t" \
  "s_mov_b32 s85, 0x480800\n\t" \
  "s_load_dword s84, %[ph], s85\n\t" \
  "s_mov_b32 s85, 0x481000\n\t" \
  "s_load_dword s84, %[ph], s85\n\t" \
  "s_mov_b32 s85, 0x4e0000\n\t" \
  "s_load_dword s84, %[ph], s85\n\t" \
  "s_setprio 3\n\t" \
  "s_barrier\n\t" \
  "global_store_dwordx4 v159, v[140:143], %[sb] sc1\n\t" \
  "v_add_u32 v158, 0x80, v159\n\t" \
  "v_bfi_b32 v159, %[mk], v158, v159\n\t" \
  "global_store_dwordx4 v159, v[144:147], %[sb] sc1\n\t" \
  "v_add_u32 v158, 0x80, v159\n\t" \
  "v_bfi_b32 v159, %[mk], v158, v159\n\t" \
  "s_mov_b32 m0, %[hd]\n\t" \
  "s_nop 0\n\t" \
  "global_load_lds_dwordx4 v163, %[hp] offset:0\n\t" \
  "global_load_lds_dwordx4 v163, %[hp] offset:1024\n\t" \
  "global_load_lds_dwordx4 v163, %[hp] offset:2048\n\t" \
  "global_load_lds_dwordx4 v163, %[hp] offset:3072\n\t" \
  "s_setprio 2\n\t" \
  "v_fmac_f32 v122, v130, v72\n\t" \
  "v_fmac_f32 v123, v131, v76\n\t" \
  "v_fmac_f32 v124, v132, v80\n\t" \
  "v_fmac_f32 v125, v133, v84\n\t" \
  "v_fmac_f32 v126, v134, v88\n\t" \
  "v_fmac_f32 v127, v135, v92\n\t" \
  "s_waitcnt vmcnt(7)\n\t" \
  "v_lshlrev_b32 v128, 16, v40\n\t" \
  "v_and_b32 v129, 0xffff0000, v40\n\t" \
  "v_lshlrev_b32 v130, 16, v41\n\t" \
  "v_and_b32 v131, 0xffff0000, v41\n\t" \
  "v_lshlrev_b32 v132, 16, v42\n\t" \
  "v_and_b32 v133, 0xffff0000, v42\n\t" \
  "v_lshlrev_b32 v134, 16, v43\n\t" \
  "v_and_b32 v135, 0xffff0000, v43\n\t" \
  "v_mul_f32 v120, v120, v128\n\t" \
  "v_mul_f32 v121, v121, v129\n\t" \
  "v_mul_f32 v122, v122, v130\n\t" \
  "v_mul_f32 v123, v123, v131\n\t" \
  "v_mul_f32 v124, v124, v132\n\t" \
  "v_mul_f32 v125, v125, v133\n\t" \
  "v_mul_f32 v126, v126, v134\n\t" \
  "v_mul_f32 v127, v127, v135\n\t" \
  "v_cvt_pk_bf16_f32 v148, v120, v121\n\t" \
  "v_cvt_pk_bf16_f32 v149, v122, v123\n\t" \
  "v_cvt_pk_bf16_f32 v150, v124, v125\n\t" \
  "v_cvt_pk_bf16_f32 v151, v126, v127\n\t" \
  "v_mov_b32_dpp v128, v148 row_ror:8 row_mask:0xf bank_mask:0xf\n\t" \
  "v_mov_b32_dpp v129, v149 row_ror:8 row_mask:0xf bank_mask:0xf\n\t" \
  "v_mov_b32_dpp v130, v150 row_ror:8 row_mask:0xf bank_mask:0xf\n\t" \
  "v_mov_b32_dpp v131, v151 row_ror:8 row_mask:0xf bank_mask:0xf\n\t" \
  "v_perm_b32 v148, v128, v148, v157\n\t" \
  "v_perm_b32 v149, v129, v149, v157\n\t" \
  "v_perm_b32 v150, v130, v150, v157\n\t" \
  "v_perm_b32 v151, v131, v151, v157\n\t" \
  "v_permlane16_swap_b32 v148, v149\n\t" \
  "s_nop 0\n\t" \
  "v_permlane16_swap_b32 v150, v151\n\t" \
  "s_nop 1\n\t" \
  "v_permlane32_swap_b32 v148, v150\n\t" \
  "v_permlane32_swap_b32 v149, v151\n\t" \
  "s_mov_b32 s85, 0x4e0800\n\t" \
  "s_load_dword s84, %[ph], s85\n\t" \
  "s_mov_b32 s85, 0x4e1000\n\t" \
  "s_load_dword s84, %[ph], s85\n\t" \
  "s_mov_b32 s85, 0x540000\n\t" \
  "s_load_dword s84, %[ph], s85\n\t" \
  "s_mov_b32 s85, 0x540800\n\t" \
  "s_load_dword s84, %[ph], s85\n\t" \
  "s_setprio 3\n\t" \
  "s_barrier\n\t" \
  "global_store_dwordx4 v159, v[148:151], %[sb] sc1\n\t" \
  "s_setprio 2\n\t" \
  "s_waitcnt vmcnt(1)\n\t" \
  "ds_read_b64_tr_b16 v[64:65], v165 offset:32\n\t" \
  "ds_read_b64_tr_b16 v[66:67], v165 offset:0\n\t" \
  "ds_read_b64_tr_b16 v[68:69], v165 offset:288\n\t" \
  "ds_read_b64_tr_b16 v[70:71], v165 offset:256\n\t" \
  "ds_read_b64_tr_b16 v[72:73], v165 offset:544\n\t" \
  "ds_read_b64_tr_b16 v[74:75], v165 offset:512\n\t" \
  "ds_read_b64_tr_b16 v[76:77], v165 offset:800\n\t" \
  "ds_read_b64_tr_b16 v[78:79], v165 offset:768\n\t" \
  "ds_read_b64_tr_b16 v[80:81], v165 offset:1056\n\t" \
  "ds_read_b64_tr_b16 v[82:83], v165 offset:1024\n\t" \
  "ds_read_b64_tr_b16 v[84:85], v165 offset:1312\n\t" \
  "ds_read_b64_tr_b16 v[86:87], v165 offset:1280\n\t" \
  "ds_read_b64_tr_b16 v[88:89], v165 offset:1568\n\t" \
  "ds_read_b64_tr_b16 v[90:91], v165 offset:1536\n\t" \
  "ds_read_b64_tr_b16 v[92:93], v165 offset:1824\n\t" \
  "ds_read_b64_tr_b16 v[94:95], v165 offset:1792\n\t" \
  "v_mov_b32 v24, v166\n\t" \
  "v_mov_b32 v25, v167\n\t" \
  "v_mov_b32 v63, 0xc0\n\t" \
  "v_add_u32 v158, 64, v24\n\t" \
  "v_bfi_b32 v26, v63, v158, v24\n\t" \
  "v_add_u32 v158, 64, v25\n\t" \
  "v_bfi_b32 v27, v63, v158, v25\n\t" \
  "v_add_u32 v158, 64, v26\n\t" \
  "v_bfi_b32 v28, v63, v158, v26\n\t" \
  "v_add_u32 v158, 64, v27\n\t" \
  "v_bfi_b32 v29, v63, v158, v27\n\t" \
  "v_add_u32 v158, 64, v28\n\t" \
  "v_bfi_b32 v30, v63, v158, v28\n\t" \
  "v_add_u32 v158, 64, v29\n\t" \
  "v_bfi_b32 v31, v63, v158, v29\n\t" \
  "s_mov_b32 s85, 0x541000\n\t" \
  "s_load_dword s84, %[ph], s85\n\t" \
  "s_mov_b32 s85, 0x5a0000\n\t" \
  "s_load_dword s84, %[ph], s85\n\t" \
  "s_mov_b32 s85, 0x5a0800\n\t" \
  "s_load_dword s84, %[ph], s85\n\t" \
  "s_mov_b32 s85, 0x5a1000\n\t" \
  "s_load_dword s84, %[ph], s85\n\t" \
  "s_setprio 3\n\t" \
  "s_barrier\n\t" \
  "s_waitcnt vmcnt(0)\n\t" \
  "v_add_u32 v61, 1, v160\n\t" \
  "s_mov_b64 exec, 1\n\t" \
  "global_store_dword v60, v61, %[ff] sc1\n\t" \
  "s_mov_b64 exec, -1\n\t" \
  "s_setprio 2\n\t" \
  "ds_read_b64_tr_b16 v[96:97], v165 offset:2080\n\t" \
  "ds_read_b64_tr_b16 v[98:99], v165 offset:2048\n\t" \
  "ds_read_b64_tr_b16 v[100:101], v165 offset:2336\n\t" \
  "ds_read_b64_tr_b16 v[102:103], v165 offset:2304\n\t" \
  "ds_read_b64_tr_b16 v[104:105], v165 offset:2592\n\t" \
  "ds_read_b64_tr_b16 v[106:107], v165 offset:2560\n\t" \
  "ds_read_b64_tr_b16 v[108:109], v165 offset:2848\n\t" \
  "ds_read_b64_tr_b16 v[110:111], v165 offset:2816\n\t" \
  "ds_read_b64_tr_b16 v[112:113], v165 offset:3104\n\t" \
  "ds_read_b64_tr_b16 v[114:115], v165 offset:3072\n\t" \
  "ds_read_b64_tr_b16 v[116:117], v165 offset:3360\n\t" \
  "ds_read_b64_tr_b16 v[118:119], v165 offset:3328\n\t" \
  "ds_read_b64_tr_b16 v[120:121], v165 offset:3616\n\t" \
  "ds_read_b64_tr_b16 v[122:123], v165 offset:3584\n\t" \
  "ds_read_b64_tr_b16 v[124:125], v165 offset:3872\n\t" \
  "ds_read_b64_tr_b16 v[126:127], v165 offset:3840\n\t" \
  "s_barrier\n\t" \
  "s_waitcnt lgkmcnt(0)\n\t" \
  "v_lshlrev_b32 v61, 16, v161\n\t" \
  "v_lshlrev_b32 v62, 16, v162\n\t" \
  "v_add_f32 v61, v61, v62\n\t" \
  "v_mov_b32 v62, 0xffff\n\t" \
  "v_cvt_pk_bf16_f32 v61, v61, v61\n\t" \
  "s_mov_b64 exec, 1\n\t" \
  "v_bfi_b32 v66, v62, v61, v66\n\t" \
  "s_mov_b64 exec, 15\n\t" \
  "v_and_b32 v66, v62, v66\n\t" \
  "s_mov_b64 exec, 16\n\t" \
  "v_bfi_b32 v66, v62, v66, v61\n\t" \
  "s_mov_b64 exec, 0xff\n\t" \
  "v_mov_b32 v67, 0\n\t" \
  "s_mov_b64 exec, 0x100\n\t" \
  "v_mov_b32 v67, v61\n\t" \
  "s_mov_b64 exec, 0xf00\n\t" \
  "v_and_b32 v67, v62, v67\n\t" \
  "s_mov_b64 exec, 0x1000\n\t" \
  "v_bfi_b32 v67, v62, v67, v61\n\t" \
  "s_mov_b64 exec, -1\n\t" \
  "s_waitcnt lgkmcnt(0)\n\t"
#define PP_NB 8
#define PP_Q 0
#define PP_CLOB "v0", "v1", "v2", "v3", "v4", "v5", "v6", "v7", "v8", "v9", "v10", "v11", "v12", "v13", "v14", "v15", "v16", "v17", "v18", "v19", "v20", "v21", "v22", "v23", "v32", "v33", "v34", "v35", "v36", "v37", "v38", "v39", "v40", "v41", "v42", "v43", "v60", "v61", "v62", "v63", "v128", "v129", "v130", "v131", "v132", "v133", "v134", "v135", "v136", "v137", "v138", "v139", "v140", "v141", "v142", "v143", "v144", "v145", "v146", "v147", "v148", "v149", "v150", "v151", "v158", "v161", "v162", "s84", "s85"

#define LD_ASM1 \
  "s_add_u32 m0, s39, 49152\n\t" \
  "s_mov_b32 s68, 0x0\n\t" \
  "buffer_load_dwordx4 v5, s[44:47], s68 offen offset:0 lds\n\t" \
  "buffer_load_dwordx4 v6, s[44:47], s68 offen offset:1024 lds\n\t" \
  "buffer_load_dwordx4 v7, s[44:47], s68 offen offset:2048 lds\n\t" \
  "s_add_u32 m0, s38, 36864\n\t" \
  "s_mov_b32 s68, 0x0\n\t" \
  "buffer_load_dwordx4 v0, s[40:43], s68 offen offset:0 lds\n\t" \
  "buffer_load_dwordx4 v1, s[40:43], s68 offen offset:1024 lds\n\t" \
  "buffer_load_dwordx4 v2, s[40:43], s68 offen offset:2048 lds\n\t" \
  "buffer_load_dwordx4 v3, s[40:43], s68 offen offset:3072 lds\n\t" \
  "s_add_u32 m0, s37, 36864\n\t" \
  "s_mov_b32 s68, 0x0\n\t" \
  "s_mov_b32 exec_hi, 0\n\t" \
  "buffer_load_dwordx4 v4, s[40:43], s68 offen offset:0 lds\n\t" \
  "s_mov_b32 exec_hi, -1\n\t" \
  "s_add_u32 m0, s39, 61440\n\t" \
  "s_mov_b32 s68, 0x30000\n\t" \
  "buffer_load_dwordx4 v5, s[44:47], s68 offen offset:0 lds\n\t" \
  "buffer_load_dwordx4 v6, s[44:47], s68 offen offset:1024 lds\n\t" \
  "buffer_load_dwordx4 v7, s[44:47], s68 offen offset:2048 lds\n\t" \
  "s_mov_b32 m0, %[sk]\n\t" \
  "s_mov_b32 exec_hi, 0\n\t" \
  "buffer_load_dword v9, s[40:43], 0 offen lds\n\t" \
  "s_mov_b32 exec_lo, %[wm]\n\t" \
  "buffer_load_dword v10, s[44:47], 0 offen lds\n\t" \
  "s_mov_b64 exec, -1\n\t"
#define LD_ASM2 \
  "s_add_u32 m0, s38, 0\n\t" \
  "s_mov_b32 s68, 0x80\n\t" \
  "buffer_load_dwordx4 v0, s[40:43], s68 offen offset:0 lds\n\t" \
  "s_mov_b32 s85, 0x180000\n\t" \
  "s_load_dword s84, %[pw], s85\n\t" \
  "buffer_load_dwordx4 v1, s[40:43], s68 offen offset:1024 lds\n\t" \
  "s_mov_b32 s85, 0x180800\n\t" \
  "s_load_dword s84, %[pw], s85\n\t" \
  "buffer_load_dwordx4 v2, s[40:43], s68 offen offset:2048 lds\n\t" \
  "s_mov_b32 s85, 0x181000\n\t" \
  "s_load_dword s84, %[pw], s85\n\t" \
  "buffer_load_dwordx4 v3, s[40:43], s68 offen offset:3072 lds\n\t" \
  "s_mov_b32 s85, 0x1b0000\n\t" \
  "s_load_dword s84, %[pw], s85\n\t" \
  "s_add_u32 m0, s37, 0\n\t" \
  "s_mov_b32 s68, 0x80\n\t" \
  "s_mov_b32 exec_hi, 0\n\t" \
  "buffer_load_dwordx4 v4, s[40:43], s68 offen offset:0 lds\n\t" \
  "s_mov_b32 exec_hi, -1\n\t" \
  "s_mov_b32 s85, 0x1b0800\n\t" \
  "s_load_dword s84, %[pw], s85\n\t" \
  "s_mov_b32 s85, 0x1b1000\n\t" \
  "s_load_dword s84, %[pw], s85\n\t" \
  "s_mov_b32 s85, 0x1e0000\n\t" \
  "s_load_dword s84, %[pw], s85\n\t" \
  "s_mov_b32 s85, 0x1e0800\n\t" \
  "s_load_dword s84, %[pw], s85\n\t" \
  "s_mov_b32 m0, %[tsk]\n\t" \
  "s_mov_b32 s85, 0x0\n\t" \
  "s_mov_b64 exec, %[hm]\n\t" \
  "buffer_load_dword v16, %[sh], s85 offen lds\n\t" \
  "s_mov_b64 exec, -1\n\t" \
  "s_mov_b32 s68, 0x60000\n\t" \
  "s_mov_b32 s69, 0x90000\n\t" \
  "s_mov_b32 s70, 0x100\n\t" \
  "s_mov_b32 s71, 0x100\n\t" \
  "s_mov_b32 s72, 0xc0000\n\t" \
  "s_mov_b32 s73, 0xf0000\n\t" \
  "s_add_u32 m0, s39, 0\n\t" \
  "s_waitcnt vmcnt(8)\n\t" \
  "s_barrier\n\t" \
  "buffer_load_dwordx4 v5, s[44:47], s68 offen offset:0 lds\n\t" \
  "s_mov_b32 s85, 0x1e1000\n\t" \
  "s_load_dword s84, %[pw], s85\n\t" \
  "buffer_load_dwordx4 v6, s[44:47], s68 offen offset:1024 lds\n\t" \
  "s_mov_b32 s85, 0x210000\n\t" \
  "s_load_dword s84, %[pw], s85\n\t" \
  "buffer_load_dwordx4 v7, s[44:47], s68 offen offset:2048 lds\n\t" \
  "s_mov_b32 s85, 0x210800\n\t" \
  "s_load_dword s84, %[pw], s85\n\t" \
  "s_add_u32 m0, s39, 12288\n\t" \
  "s_nop 0\n\t" \
  "buffer_load_dwordx4 v5, s[44:47], s69 offen offset:0 lds\n\t" \
  "s_mov_b32 s85, 0x211000\n\t" \
  "s_load_dword s84, %[pw], s85\n\t" \
  "buffer_load_dwordx4 v6, s[44:47], s69 offen offset:1024 lds\n\t" \
  "s_mov_b32 s85, 0x240000\n\t" \
  "s_load_dword s84, %[pw], s85\n\t" \
  "buffer_load_dwordx4 v7, s[44:47], s69 offen offset:2048 lds\n\t" \
  "s_mov_b32 s85, 0x240800\n\t" \
  "s_load_dword s84, %[pw], s85\n\t" \
  "s_add_u32 m0, s38, 18432\n\t" \
  "s_nop 0\n\t" \
  "buffer_load_dwordx4 v0, s[40:43], s70 offen offset:0 lds\n\t" \
  "buffer_load_dwordx4 v1, s[40:43], s70 offen offset:1024 lds\n\t" \
  "buffer_load_dwordx4 v2, s[40:43], s70 offen offset:2048 lds\n\t" \
  "buffer_load_dwordx4 v3, s[40:43], s70 offen offset:3072 lds\n\t" \
  "s_add_u32 m0, s37, 18432\n\t" \
  "s_nop 0\n\t" \
  "s_mov_b32 exec_hi, 0\n\t" \
  "buffer_load_dwordx4 v4, s[40:43], s71 offen offset:0 lds\n\t" \
  "s_add_u32 m0, s39, 24576\n\t" \
  "s_mov_b32 exec_hi, -1\n\t" \
  "buffer_load_dwordx4 v5, s[44:47], s72 offen offset:0 lds\n\t" \
  "buffer_load_dwordx4 v6, s[44:47], s72 offen offset:1024 lds\n\t" \
  "buffer_load_dwordx4 v7, s[44:47], s72 offen offset:2048 lds\n\t" \
  "s_add_u32 m0, s39, 36864\n\t" \
  "s_nop 0\n\t" \
  "buffer_load_dwordx4 v5, s[44:47], s73 offen offset:0 lds\n\t" \
  "buffer_load_dwordx4 v6, s[44:47], s73 offen offset:1024 lds\n\t" \
  "buffer_load_dwordx4 v7, s[44:47], s73 offen offset:2048 lds\n\t" \
  "s_mov_b32 m0, %[tsk]\n\t" \
  "s_mov_b32 s85, 0x180000\n\t" \
  "s_mov_b64 exec, %[hm]\n\t" \
  "buffer_load_dword v16, %[sh], s85 offen lds\n\t" \
  "s_mov_b64 exec, -1\n\t"
#define LD_ASM3 \
  "s_mov_b32 s68, 0x180\n\t" \
  "s_mov_b32 s69, 0x180\n\t" \
  "s_mov_b32 s70, 0x120000\n\t" \
  "s_mov_b32 s71, 0x150000\n\t" \
  "s_add_u32 m0, s38, 36864\n\t" \
  "s_waitcnt vmcnt(12)\n\t" \
  "s_barrier\n\t" \
  "buffer_load_dwordx4 v0, s[40:43], s68 offen offset:0 lds\n\t" \
  "s_mov_b32 s85, 0x241000\n\t" \
  "s_load_dword s84, %[pw], s85\n\t" \
  "buffer_load_dwordx4 v1, s[40:43], s68 offen offset:1024 lds\n\t" \
  "s_mov_b32 s85, 0x270000\n\t" \
  "s_load_dword s84, %[pw], s85\n\t" \
  "buffer_load_dwordx4 v2, s[40:43], s68 offen offset:2048 lds\n\t" \
  "s_mov_b32 s85, 0x270800\n\t" \
  "s_load_dword s84, %[pw], s85\n\t" \
  "buffer_load_dwordx4 v3, s[40:43], s68 offen offset:3072 lds\n\t" \
  "s_mov_b32 s85, 0x271000\n\t" \
  "s_load_dword s84, %[pw], s85\n\t" \
  "s_add_u32 m0, s37, 36864\n\t" \
  "s_nop 0\n\t" \
  "s_mov_b32 exec_hi, 0\n\t" \
  "buffer_load_dwordx4 v4, s[40:43], s69 offen offset:0 lds\n\t" \
  "s_add_u32 m0, s39, 49152\n\t" \
  "s_mov_b32 exec_hi, -1\n\t" \
  "buffer_load_dwordx4 v5, s[44:47], s70 offen offset:0 lds\n\t" \
  "s_mov_b32 s85, 0x2a0000\n\t" \
  "s_load_dword s84, %[pw], s85\n\t" \
  "buffer_load_dwordx4 v6, s[44:47], s70 offen offset:1024 lds\n\t" \
  "s_mov_b32 s85, 0x2a0800\n\t" \
  "s_load_dword s84, %[pw], s85\n\t" \
  "buffer_load_dwordx4 v7, s[44:47], s70 offen offset:2048 lds\n\t" \
  "s_add_u32 m0, s39, 61440\n\t" \
  "s_nop 0\n\t" \
  "buffer_load_dwordx4 v5, s[44:47], s71 offen offset:0 lds\n\t" \
  "buffer_load_dwordx4 v6, s[44:47], s71 offen offset:1024 lds\n\t" \
  "buffer_load_dwordx4 v7, s[44:47], s71 offen offset:2048 lds\n\t" \
  "s_mov_b32 s68, 0x200\n\t" \
  "s_mov_b32 s69, 0x200\n\t" \
  "s_mov_b32 s70, 0x180000\n\t" \
  "s_mov_b32 s71, 0x1b0000\n\t" \
  "s_add_u32 m0, s38, 0\n\t" \
  "s_waitcnt vmcnt(12)\n\t" \
  "s_barrier\n\t" \
  "buffer_load_dwordx4 v0, s[40:43], s68 offen offset:0 lds\n\t" \
  "s_mov_b32 s85, 0x2a1000\n\t" \
  "s_load_dword s84, %[pw], s85\n\t" \
  "buffer_load_dwordx4 v1, s[40:43], s68 offen offset:1024 lds\n\t" \
  "s_mov_b32 s85, 0x2d0000\n\t" \
  "s_load_dword s84, %[pw], s85\n\t" \
  "buffer_load_dwordx4 v2, s[40:43], s68 offen offset:2048 lds\n\t" \
  "s_mov_b32 s85, 0x2d0800\n\t" \
  "s_load_dword s84, %[pw], s85\n\t" \
  "buffer_load_dwordx4 v3, s[40:43], s68 offen offset:3072 lds\n\t" \
  "s_mov_b32 s85, 0x2d1000\n\t" \
  "s_load_dword s84, %[pw], s85\n\t" \
  "s_add_u32 m0, s37, 0\n\t" \
  "s_nop 0\n\t" \
  "s_mov_b32 exec_hi, 0\n\t" \
  "buffer_load_dwordx4 v4, s[40:43], s69 offen offset:0 lds\n\t" \
  "s_add_u32 m0, s39, 0\n\t" \
  "s_mov_b32 exec_hi, -1\n\t" \
  "buffer_load_dwordx4 v5, s[44:47], s70 offen offset:0 lds\n\t" \
  "s_mov_b32 s85, 0x300000\n\t" \
  "s_load_dword s84, %[pw], s85\n\t" \
  "buffer_load_dwordx4 v6, s[44:47], s70 offen offset:1024 lds\n\t" \
  "s_mov_b32 s85, 0x300800\n\t" \
  "s_load_dword s84, %[pw], s85\n\t" \
  "buffer_load_dwordx4 v7, s[44:47], s70 offen offset:2048 lds\n\t" \
  "s_add_u32 m0, s39, 12288\n\t" \
  "s_nop 0\n\t" \
  "buffer_load_dwordx4 v5, s[44:47], s71 offen offset:0 lds\n\t" \
  "buffer_load_dwordx4 v6, s[44:47], s71 offen offset:1024 lds\n\t" \
  "buffer_load_dwordx4 v7, s[44:47], s71 offen offset:2048 lds\n\t" \
  "s_mov_b32 s68, 0x280\n\t" \
  "s_mov_b32 s69, 0x280\n\t" \
  "s_mov_b32 s70, 0x1e0000\n\t" \
  "s_mov_b32 s71, 0x210000\n\t" \
  "s_add_u32 m0, s38, 18432\n\t" \
  "s_waitcnt vmcnt(11)\n\t" \
  "s_barrier\n\t" \
  "buffer_load_dwordx4 v0, s[40:43], s68 offen offset:0 lds\n\t" \
  "s_mov_b32 s85, 0x301000\n\t" \
  "s_load_dword s84, %[pw], s85\n\t" \
  "buffer_load_dwordx4 v1, s[40:43], s68 offen offset:1024 lds\n\t" \
  "s_mov_b32 s85, 0x330000\n\t" \
  "s_load_dword s84, %[pw], s85\n\t" \
  "buffer_load_dwordx4 v2, s[40:43], s68 offen offset:2048 lds\n\t" \
  "s_mov_b32 s85, 0x330800\n\t" \
  "s_load_dword s84, %[pw], s85\n\t" \
  "buffer_load_dwordx4 v3, s[40:43], s68 offen offset:3072 lds\n\t" \
  "s_mov_b32 s85, 0x331000\n\t" \
  "s_load_dword s84, %[pw], s85\n\t" \
  "s_add_u32 m0, s37, 18432\n\t" \
  "s_nop 0\n\t" \
  "s_mov_b32 exec_hi, 0\n\t" \
  "buffer_load_dwordx4 v4, s[40:43], s69 offen offset:0 lds\n\t" \
  "s_add_u32 m0, s39, 24576\n\t" \
  "s_mov_b32 exec_hi, -1\n\t" \
  "buffer_load_dwordx4 v5, s[44:47], s70 offen offset:0 lds\n\t" \
  "s_mov_b32 s85, 0x360000\n\t" \
  "s_load_dword s84, %[pw], s85\n\t" \
  "buffer_load_dwordx4 v6, s[44:47], s70 offen offset:1024 lds\n\t" \
  "s_mov_b32 s85, 0x360800\n\t" \
  "s_load_dword s84, %[pw], s85\n\t" \
  "buffer_load_dwordx4 v7, s[44:47], s70 offen offset:2048 lds\n\t" \
  "s_add_u32 m0, s39, 36864\n\t" \
  "s_nop 0\n\t" \
  "buffer_load_dwordx4 v5, s[44:47], s71 offen offset:0 lds\n\t" \
  "buffer_load_dwordx4 v6, s[44:47], s71 offen offset:1024 lds\n\t" \
  "buffer_load_dwordx4 v7, s[44:47], s71 offen offset:2048 lds\n\t" \
  "s_mov_b32 s68, 0x300\n\t" \
  "s_mov_b32 s69, 0x300\n\t" \
  "s_mov_b32 s70, 0x240000\n\t" \
  "s_mov_b32 s71, 0x270000\n\t" \
  "s_add_u32 m0, s38, 36864\n\t" \
  "s_waitcnt vmcnt(11)\n\t" \
  "s_barrier\n\t" \
  "buffer_load_dwordx4 v0, s[40:43], s68 offen offset:0 lds\n\t" \
  "s_mov_b32 s85, 0x361000\n\t" \
  "s_load_dword s84, %[pw], s85\n\t" \
  "buffer_load_dwordx4 v1, s[40:43], s68 offen offset:1024 lds\n\t" \
  "s_mov_b32 s85, 0x390000\n\t" \
  "s_load_dword s84, %[pw], s85\n\t" \
  "buffer_load_dwordx4 v2, s[40:43], s68 offen offset:2048 lds\n\t" \
  "s_mov_b32 s85, 0x390800\n\t" \
  "s_load_dword s84, %[pw], s85\n\t" \
  "buffer_load_dwordx4 v3, s[40:43], s68 offen offset:3072 lds\n\t" \
  "s_mov_b32 s85, 0x391000\n\t" \
  "s_load_dword s84, %[pw], s85\n\t" \
  "s_add_u32 m0, s37, 36864\n\t" \
  "s_nop 0\n\t" \
  "s_mov_b32 exec_hi, 0\n\t" \
  "buffer_load_dwordx4 v4, s[40:43], s69 offen offset:0 lds\n\t" \
  "s_add_u32 m0, s39, 49152\n\t" \
  "s_mov_b32 exec_hi, -1\n\t" \
  "buffer_load_dwordx4 v5, s[44:47], s70 offen offset:0 lds\n\t" \
  "s_mov_b32 s85, 0x3c0000\n\t" \
  "s_load_dword s84, %[pw], s85\n\t" \
  "buffer_load_dwordx4 v6, s[44:47], s70 offen offset:1024 lds\n\t" \
  "s_mov_b32 s85, 0x3c0800\n\t" \
  "s_load_dword s84, %[pw], s85\n\t" \
  "buffer_load_dwordx4 v7, s[44:47], s70 offen offset:2048 lds\n\t" \
  "s_add_u32 m0, s39, 61440\n\t" \
  "s_nop 0\n\t" \
  "buffer_load_dwordx4 v5, s[44:47], s71 offen offset:0 lds\n\t" \
  "buffer_load_dwordx4 v6, s[44:47], s71 offen offset:1024 lds\n\t" \
  "buffer_load_dwordx4 v7, s[44:47], s71 offen offset:2048 lds\n\t" \
  "s_mov_b32 s68, 0x380\n\t" \
  "s_mov_b32 s69, 0x380\n\t" \
  "s_mov_b32 s70, 0x2a0000\n\t" \
  "s_mov_b32 s71, 0x2d0000\n\t" \
  "s_add_u32 m0, s38, 0\n\t" \
  "s_waitcnt vmcnt(11)\n\t" \
  "s_barrier\n\t" \
  "buffer_load_dwordx4 v0, s[40:43], s68 offen offset:0 lds\n\t" \
  "s_mov_b32 s85, 0x3c1000\n\t" \
  "s_load_dword s84, %[pw], s85\n\t" \
  "buffer_load_dwordx4 v1, s[40:43], s68 offen offset:1024 lds\n\t" \
  "s_mov_b32 s85, 0x3f0000\n\t" \
  "s_load_dword s84, %[pw], s85\n\t" \
  "buffer_load_dwordx4 v2, s[40:43], s68 offen offset:2048 lds\n\t" \
  "s_mov_b32 s85, 0x3f0800\n\t" \
  "s_load_dword s84, %[pw], s85\n\t" \
  "buffer_load_dwordx4 v3, s[40:43], s68 offen offset:3072 lds\n\t" \
  "s_mov_b32 s85, 0x3f1000\n\t" \
  "s_load_dword s84, %[pw], s85\n\t" \
  "s_add_u32 m0, s37, 0\n\t" \
  "s_nop 0\n\t" \
  "s_mov_b32 exec_hi, 0\n\t" \
  "buffer_load_dwordx4 v4, s[40:43], s69 offen offset:0 lds\n\t" \
  "s_add_u32 m0, s39, 0\n\t" \
  "s_mov_b32 exec_hi, -1\n\t" \
  "buffer_load_dwordx4 v5, s[44:47], s70 offen offset:0 lds\n\t" \
  "s_mov_b32 s85, 0x420000\n\t" \
  "s_load_dword s84, %[pw], s85\n\t" \
  "buffer_load_dwordx4 v6, s[44:47], s70 offen offset:1024 lds\n\t" \
  "s_mov_b32 s85, 0x420800\n\t" \
  "s_load_dword s84, %[pw], s85\n\t" \
  "buffer_load_dwordx4 v7, s[44:47], s70 offen offset:2048 lds\n\t" \
  "s_add_u32 m0, s39, 12288\n\t" \
  "s_nop 0\n\t" \
  "buffer_load_dwordx4 v5, s[44:47], s71 offen offset:0 lds\n\t" \
  "buffer_load_dwordx4 v6, s[44:47], s71 offen offset:1024 lds\n\t" \
  "buffer_load_dwordx4 v7, s[44:47], s71 offen offset:2048 lds\n\t" \
  "s_mov_b32 s68, 0x400\n\t" \
  "s_mov_b32 s69, 0x400\n\t" \
  "s_mov_b32 s70, 0x300000\n\t" \
  "s_mov_b32 s71, 0x330000\n\t" \
  "s_add_u32 m0, s38, 18432\n\t" \
  "s_waitcnt vmcnt(11)\n\t" \
  "s_barrier\n\t" \
  "buffer_load_dwordx4 v0, s[40:43], s68 offen offset:0 lds\n\t" \
  "s_mov_b32 s85, 0x421000\n\t" \
  "s_load_dword s84, %[pw], s85\n\t" \
  "buffer_load_dwordx4 v1, s[40:43], s68 offen offset:1024 lds\n\t" \
  "s_mov_b32 s85, 0x450000\n\t" \
  "s_load_dword s84, %[pw], s85\n\t" \
  "buffer_load_dwordx4 v2, s[40:43], s68 offen offset:2048 lds\n\t" \
  "s_mov_b32 s85, 0x450800\n\t" \
  "s_load_dword s84, %[pw], s85\n\t" \
  "buffer_load_dwordx4 v3, s[40:43], s68 offen offset:3072 lds\n\t" \
  "s_mov_b32 s85, 0x451000\n\t" \
  "s_load_dword s84, %[pw], s85\n\t" \
  "s_add_u32 m0, s37, 18432\n\t" \
  "s_nop 0\n\t" \
  "s_mov_b32 exec_hi, 0\n\t" \
  "buffer_load_dwordx4 v4, s[40:43], s69 offen offset:0 lds\n\t" \
  "s_add_u32 m0, s39, 24576\n\t" \
  "s_mov_b32 exec_hi, -1\n\t" \
  "buffer_load_dwordx4 v5, s[44:47], s70 offen offset:0 lds\n\t" \
  "s_mov_b32 s85, 0x480000\n\t" \
  "s_load_dword s84, %[pw], s85\n\t" \
  "buffer_load_dwordx4 v6, s[44:47], s70 offen offset:1024 lds\n\t" \
  "s_mov_b32 s85, 0x480800\n\t" \
  "s_load_dword s84, %[pw], s85\n\t" \
  "buffer_load_dwordx4 v7, s[44:47], s70 offen offset:2048 lds\n\t" \
  "s_add_u32 m0, s39, 36864\n\t" \
  "s_nop 0\n\t" \
  "buffer_load_dwordx4 v5, s[44:47], s71 offen offset:0 lds\n\t" \
  "buffer_load_dwordx4 v6, s[44:47], s71 offen offset:1024 lds\n\t" \
  "buffer_load_dwordx4 v7, s[44:47], s71 offen offset:2048 lds\n\t" \
  "s_mov_b32 s68, 0x480\n\t" \
  "s_mov_b32 s69, 0x480\n\t" \
  "s_mov_b32 s70, 0x360000\n\t" \
  "s_mov_b32 s71, 0x390000\n\t" \
  "s_add_u32 m0, s38, 36864\n\t" \
  "s_waitcnt vmcnt(11)\n\t" \
  "s_barrier\n\t" \
  "buffer_load_dwordx4 v0, s[40:43], s68 offen offset:0 lds\n\t" \
  "s_mov_b32 s85, 0x481000\n\t" \
  "s_load_dword s84, %[pw], s85\n\t" \
  "buffer_load_dwordx4 v1, s[40:43], s68 offen offset:1024 lds\n\t" \
  "s_mov_b32 s85, 0x4b0000\n\t" \
  "s_load_dword s84, %[pw], s85\n\t" \
  "buffer_load_dwordx4 v2, s[40:43], s68 offen offset:2048 lds\n\t" \
  "s_mov_b32 s85, 0x4b0800\n\t" \
  "s_load_dword s84, %[pw], s85\n\t" \
  "buffer_load_dwordx4 v3, s[40:43], s68 offen offset:3072 lds\n\t" \
  "s_mov_b32 s85, 0x4b1000\n\t" \
  "s_load_dword s84, %[pw], s85\n\t" \
  "s_add_u32 m0, s37, 36864\n\t" \
  "s_nop 0\n\t" \
  "s_mov_b32 exec_hi, 0\n\t" \
  "buffer_load_dwordx4 v4, s[40:43], s69 offen offset:0 lds\n\t" \
  "s_add_u32 m0, s39, 49152\n\t" \
  "s_mov_b32 exec_hi, -1\n\t" \
  "buffer_load_dwordx4 v5, s[44:47], s70 offen offset:0 lds\n\t" \
  "s_mov_b32 s85, 0x4e0000\n\t" \
  "s_load_dword s84, %[pw], s85\n\t" \
  "buffer_load_dwordx4 v6, s[44:47], s70 offen offset:1024 lds\n\t" \
  "s_mov_b32 s85, 0x4e0800\n\t" \
  "s_load_dword s84, %[pw], s85\n\t" \
  "buffer_load_dwordx4 v7, s[44:47], s70 offen offset:2048 lds\n\t" \
  "s_add_u32 m0, s39, 61440\n\t" \
  "s_nop 0\n\t" \
  "buffer_load_dwordx4 v5, s[44:47], s71 offen offset:0 lds\n\t" \
  "buffer_load_dwordx4 v6, s[44:47], s71 offen offset:1024 lds\n\t" \
  "buffer_load_dwordx4 v7, s[44:47], s71 offen offset:2048 lds\n\t" \
  "s_mov_b32 s68, 0x500\n\t" \
  "s_mov_b32 s69, 0x500\n\t" \
  "s_mov_b32 s70, 0x3c0000\n\t" \
  "s_mov_b32 s71, 0x3f0000\n\t" \
  "s_add_u32 m0, s38, 0\n\t" \
  "s_waitcnt vmcnt(11)\n\t" \
  "s_barrier\n\t" \
  "buffer_load_dwordx4 v0, s[40:43], s68 offen offset:0 lds\n\t" \
  "s_mov_b32 s85, 0x4e1000\n\t" \
  "s_load_dword s84, %[pw], s85\n\t" \
  "buffer_load_dwordx4 v1, s[40:43], s68 offen offset:1024 lds\n\t" \
  "s_mov_b32 s85, 0x510000\n\t" \
  "s_load_dword s84, %[pw], s85\n\t" \
  "buffer_load_dwordx4 v2, s[40:43], s68 offen offset:2048 lds\n\t" \
  "s_mov_b32 s85, 0x510800\n\t" \
  "s_load_dword s84, %[pw], s85\n\t" \
  "buffer_load_dwordx4 v3, s[40:43], s68 offen offset:3072 lds\n\t" \
  "s_mov_b32 s85, 0x511000\n\t" \
  "s_load_dword s84, %[pw], s85\n\t" \
  "s_add_u32 m0, s37, 0\n\t" \
  "s_nop 0\n\t" \
  "s_mov_b32 exec_hi, 0\n\t" \
  "buffer_load_dwordx4 v4, s[40:43], s69 offen offset:0 lds\n\t" \
  "s_add_u32 m0, s39, 0\n\t" \
  "s_mov_b32 exec_hi, -1\n\t" \
  "buffer_load_dwordx4 v5, s[44:47], s70 offen offset:0 lds\n\t" \
  "s_mov_b32 s85, 0x540000\n\t" \
  "s_load_dword s84, %[pw], s85\n\t" \
  "buffer_load_dwordx4 v6, s[44:47], s70 offen offset:1024 lds\n\t" \
  "s_mov_b32 s85, 0x540800\n\t" \
  "s_load_dword s84, %[pw], s85\n\t" \
  "buffer_load_dwordx4 v7, s[44:47], s70 offen offset:2048 lds\n\t" \
  "s_add_u32 m0, s39, 12288\n\t" \
  "s_nop 0\n\t" \
  "buffer_load_dwordx4 v5, s[44:47], s71 offen offset:0 lds\n\t" \
  "buffer_load_dwordx4 v6, s[44:47], s71 offen offset:1024 lds\n\t" \
  "buffer_load_dwordx4 v7, s[44:47], s71 offen offset:2048 lds\n\t" \
  "s_mov_b32 s68, 0x580\n\t" \
  "s_mov_b32 s69, 0x580\n\t" \
  "s_mov_b32 s70, 0x420000\n\t" \
  "s_mov_b32 s71, 0x450000\n\t" \
  "s_add_u32 m0, s38, 18432\n\t" \
  "s_waitcnt vmcnt(11)\n\t" \
  "s_barrier\n\t" \
  "buffer_load_dwordx4 v0, s[40:43], s68 offen offset:0 lds\n\t" \
  "s_load_dword s84, %[pq], 0x0\n\t" \
  "buffer_load_dwordx4 v1, s[40:43], s68 offen offset:1024 lds\n\t" \
  "s_load_dword s84, %[pq], 0x40\n\t" \
  "buffer_load_dwordx4 v2, s[40:43], s68 offen offset:2048 lds\n\t" \
  "s_load_dword s84, %[pq], 0x800\n\t" \
  "buffer_load_dwordx4 v3, s[40:43], s68 offen offset:3072 lds\n\t" \
  "s_load_dword s84, %[pq], 0x840\n\t" \
  "s_add_u32 m0, s37, 18432\n\t" \
  "s_nop 0\n\t" \
  "s_mov_b32 exec_hi, 0\n\t" \
  "buffer_load_dwordx4 v4, s[40:43], s69 offen offset:0 lds\n\t" \
  "s_add_u32 m0, s39, 24576\n\t" \
  "s_mov_b32 exec_hi, -1\n\t" \
  "buffer_load_dwordx4 v5, s[44:47], s70 offen offset:0 lds\n\t" \
  "s_mov_b32 s85, 0x541000\n\t" \
  "s_load_dword s84, %[pw], s85\n\t" \
  "buffer_load_dwordx4 v6, s[44:47], s70 offen offset:1024 lds\n\t" \
  "s_mov_b32 s85, 0x570000\n\t" \
  "s_load_dword s84, %[pw], s85\n\t" \
  "buffer_load_dwordx4 v7, s[44:47], s70 offen offset:2048 lds\n\t" \
  "s_mov_b32 s85, 0x570800\n\t" \
  "s_load_dword s84, %[pw], s85\n\t" \
  "s_add_u32 m0, s39, 36864\n\t" \
  "s_nop 0\n\t" \
  "buffer_load_dwordx4 v5, s[44:47], s71 offen offset:0 lds\n\t" \
  "s_mov_b32 s85, 0x571000\n\t" \
  "s_load_dword s84, %[pw], s85\n\t" \
  "buffer_load_dwordx4 v6, s[44:47], s71 offen offset:1024 lds\n\t" \
  "s_mov_b32 s85, 0x5a0000\n\t" \
  "s_load_dword s84, %[pw], s85\n\t" \
  "buffer_load_dwordx4 v7, s[44:47], s71 offen offset:2048 lds\n\t" \
  "s_mov_b32 s85, 0x5a0800\n\t" \
  "s_load_dword s84, %[pw], s85\n\t" \
  "s_mov_b32 s68, 0x600\n\t" \
  "s_mov_b32 s69, 0x600\n\t" \
  "s_mov_b32 s70, 0x480000\n\t" \
  "s_mov_b32 s71, 0x4b0000\n\t" \
  "s_add_u32 m0, s38, 36864\n\t" \
  "s_waitcnt vmcnt(11)\n\t" \
  "s_barrier\n\t" \
  "buffer_load_dwordx4 v0, s[40:43], s68 offen offset:0 lds\n\t" \
  "s_load_dword s84, %[pq], 0x1000\n\t" \
  "buffer_load_dwordx4 v1, s[40:43], s68 offen offset:1024 lds\n\t" \
  "s_load_dword s84, %[pq], 0x1040\n\t" \
  "buffer_load_dwordx4 v2, s[40:43], s68 offen offset:2048 lds\n\t" \
  "s_load_dword s84, %[pq], 0x1800\n\t" \
  "buffer_load_dwordx4 v3, s[40:43], s68 offen offset:3072 lds\n\t" \
  "s_load_dword s84, %[pq], 0x1840\n\t" \
  "s_add_u32 m0, s37, 36864\n\t" \
  "s_nop 0\n\t" \
  "s_mov_b32 exec_hi, 0\n\t" \
  "buffer_load_dwordx4 v4, s[40:43], s69 offen offset:0 lds\n\t" \
  "s_add_u32 m0, s39, 49152\n\t" \
  "s_mov_b32 exec_hi, -1\n\t" \
  "buffer_load_dwordx4 v5, s[44:47], s70 offen offset:0 lds\n\t" \
  "s_mov_b32 s85, 0x5a1000\n\t" \
  "s_load_dword s84, %[pw], s85\n\t" \
  "buffer_load_dwordx4 v6, s[44:47], s70 offen offset:1024 lds\n\t" \
  "s_mov_b32 s85, 0x5d0000\n\t" \
  "s_load_dword s84, %[pw], s85\n\t" \
  "buffer_load_dwordx4 v7, s[44:47], s70 offen offset:2048 lds\n\t" \
  "s_mov_b32 s85, 0x5d0800\n\t" \
  "s_load_dword s84, %[pw], s85\n\t" \
  "s_add_u32 m0, s39, 61440\n\t" \
  "s_nop 0\n\t" \
  "buffer_load_dwordx4 v5, s[44:47], s71 offen offset:0 lds\n\t" \
  "s_mov_b32 s85, 0x5d1000\n\t" \
  "s_load_dword s84, %[pw], s85\n\t" \
  "buffer_load_dwordx4 v6, s[44:47], s71 offen offset:1024 lds\n\t" \
  "buffer_load_dwordx4 v7, s[44:47], s71 offen offset:2048 lds\n\t" \
  "s_mov_b32 s68, 0x680\n\t" \
  "s_mov_b32 s69, 0x680\n\t" \
  "s_mov_b32 s70, 0x4e0000\n\t" \
  "s_mov_b32 s71, 0x510000\n\t" \
  "s_add_u32 m0, s38, 0\n\t" \
  "s_waitcnt vmcnt(11)\n\t" \
  "s_barrier\n\t" \
  "buffer_load_dwordx4 v0, s[40:43], s68 offen offset:0 lds\n\t" \
  "s_load_dword s84, %[pq], 0x2000\n\t" \
  "buffer_load_dwordx4 v1, s[40:43], s68 offen offset:1024 lds\n\t" \
  "s_load_dword s84, %[pq], 0x2040\n\t" \
  "buffer_load_dwordx4 v2, s[40:43], s68 offen offset:2048 lds\n\t" \
  "s_load_dword s84, %[pq], 0x2800\n\t" \
  "buffer_load_dwordx4 v3, s[40:43], s68 offen offset:3072 lds\n\t" \
  "s_load_dword s84, %[pq], 0x2840\n\t" \
  "s_add_u32 m0, s37, 0\n\t" \
  "s_nop 0\n\t" \
  "s_mov_b32 exec_hi, 0\n\t" \
  "buffer_load_dwordx4 v4, s[40:43], s69 offen offset:0 lds\n\t" \
  "s_add_u32 m0, s39, 0\n\t" \
  "s_mov_b32 exec_hi, -1\n\t" \
  "buffer_load_dwordx4 v5, s[44:47], s70 offen offset:0 lds\n\t" \
  "buffer_load_dwordx4 v6, s[44:47], s70 offen offset:1024 lds\n\t" \
  "buffer_load_dwordx4 v7, s[44:47], s70 offen offset:2048 lds\n\t" \
  "s_add_u32 m0, s39, 12288\n\t" \
  "s_nop 0\n\t" \
  "buffer_load_dwordx4 v5, s[44:47], s71 offen offset:0 lds\n\t" \
  "buffer_load_dwordx4 v6, s[44:47], s71 offen offset:1024 lds\n\t" \
  "buffer_load_dwordx4 v7, s[44:47], s71 offen offset:2048 lds\n\t" \
  "s_mov_b32 s68, 0x700\n\t" \
  "s_mov_b32 s69, 0x700\n\t" \
  "s_mov_b32 s70, 0x540000\n\t" \
  "s_mov_b32 s71, 0x570000\n\t" \
  "s_add_u32 m0, s38, 18432\n\t" \
  "s_waitcnt vmcnt(11)\n\t" \
  "s_barrier\n\t" \
  "buffer_load_dwordx4 v0, s[40:43], s68 offen offset:0 lds\n\t" \
  "s_load_dword s84, %[pq], 0x3000\n\t" \
  "buffer_load_dwordx4 v1, s[40:43], s68 offen offset:1024 lds\n\t" \
  "s_load_dword s84, %[pq], 0x3040\n\t" \
  "buffer_load_dwordx4 v2, s[40:43], s68 offen offset:2048 lds\n\t" \
  "s_load_dword s84, %[pq], 0x3800\n\t" \
  "buffer_load_dwordx4 v3, s[40:43], s68 offen offset:3072 lds\n\t" \
  "s_load_dword s84, %[pq], 0x3840\n\t" \
  "s_add_u32 m0, s37, 18432\n\t" \
  "s_nop 0\n\t" \
  "s_mov_b32 exec_hi, 0\n\t" \
  "buffer_load_dwordx4 v4, s[40:43], s69 offen offset:0 lds\n\t" \
  "s_add_u32 m0, s39, 24576\n\t" \
  "s_mov_b32 exec_hi, -1\n\t" \
  "buffer_load_dwordx4 v5, s[44:47], s70 offen offset:0 lds\n\t" \
  "buffer_load_dwordx4 v6, s[44:47], s70 offen offset:1024 lds\n\t" \
  "buffer_load_dwordx4 v7, s[44:47], s70 offen offset:2048 lds\n\t" \
  "s_add_u32 m0, s39, 36864\n\t" \
  "s_nop 0\n\t" \
  "buffer_load_dwordx4 v5, s[44:47], s71 offen offset:0 lds\n\t" \
  "buffer_load_dwordx4 v6, s[44:47], s71 offen offset:1024 lds\n\t" \
  "buffer_load_dwordx4 v7, s[44:47], s71 offen offset:2048 lds\n\t" \
  "s_mov_b32 s68, 0x780\n\t" \
  "s_mov_b32 s69, 0x780\n\t" \
  "s_mov_b32 s70, 0x5a0000\n\t" \
  "s_mov_b32 s71, 0x5d0000\n\t" \
  "s_add_u32 m0, s38, 36864\n\t" \
  "s_waitcnt vmcnt(11)\n\t" \
  "s_barrier\n\t" \
  "buffer_load_dwordx4 v0, s[40:43], s68 offen offset:0 lds\n\t" \
  "buffer_load_dwordx4 v1, s[40:43], s68 offen offset:1024 lds\n\t" \
  "buffer_load_dwordx4 v2, s[40:43], s68 offen offset:2048 lds\n\t" \
  "buffer_load_dwordx4 v3, s[40:43], s68 offen offset:3072 lds\n\t" \
  "s_add_u32 m0, s37, 36864\n\t" \
  "s_nop 0\n\t" \
  "s_mov_b32 exec_hi, 0\n\t" \
  "buffer_load_dwordx4 v4, s[40:43], s69 offen offset:0 lds\n\t" \
  "s_add_u32 m0, s39, 49152\n\t" \
  "s_mov_b32 exec_hi, -1\n\t" \
  "buffer_load_dwordx4 v5, s[44:47], s70 offen offset:0 lds\n\t" \
  "buffer_load_dwordx4 v6, s[44:47], s70 offen offset:1024 lds\n\t" \
  "buffer_load_dwordx4 v7, s[44:47], s70 offen offset:2048 lds\n\t" \
  "s_add_u32 m0, s39, 61440\n\t" \
  "s_nop 0\n\t" \
  "buffer_load_dwordx4 v5, s[44:47], s71 offen offset:0 lds\n\t" \
  "buffer_load_dwordx4 v6, s[44:47], s71 offen offset:1024 lds\n\t" \
  "buffer_load_dwordx4 v7, s[44:47], s71 offen offset:2048 lds\n\t" \
  "v_readlane_b32 s68, v9, 18\n\t" \
  "v_readlane_b32 s69, v9, 19\n\t" \
  "v_readlane_b32 s70, v9, 20\n\t" \
  "v_readlane_b32 s71, v9, 21\n\t" \
  "v_readlane_b32 s72, v9, 0\n\t" \
  "v_readlane_b32 s73, v9, 1\n\t" \
  "v_readlane_b32 s74, v9, 2\n\t" \
  "v_readlane_b32 s75, v9, 3\n\t" \
  "v_readlane_b32 s76, v9, 4\n\t" \
  "v_readlane_b32 s77, v9, 5\n\t" \
  "s_add_u32 m0, %[ob], 73728\n\t" \
  "s_waitcnt vmcnt(11)\n\t" \
  "s_barrier\n\t" \
  "s_nop 2\n\t" \
  "buffer_load_dwordx4 v8, %[so], s68 offen lds\n\t" \
  "s_add_u32 m0, %[ob], 77824\n\t" \
  "s_nop 0\n\t" \
  "buffer_load_dwordx4 v8, %[so], s69 offen lds\n\t" \
  "s_add_u32 m0, %[ob], 81920\n\t" \
  "s_nop 0\n\t" \
  "buffer_load_dwordx4 v8, %[so], s70 offen lds\n\t" \
  "s_add_u32 m0, %[ob], 86016\n\t" \
  "s_nop 0\n\t" \
  "buffer_load_dwordx4 v8, %[so], s71 offen lds\n\t" \
  "s_add_u32 m0, %[ob], 0\n\t" \
  "s_nop 0\n\t" \
  "buffer_load_dwordx4 v8, %[so], s72 offen lds\n\t" \
  "s_add_u32 m0, %[ob], 4096\n\t" \
  "s_nop 0\n\t" \
  "buffer_load_dwordx4 v8, %[so], s73 offen lds\n\t" \
  "s_add_u32 m0, %[ob], 8192\n\t" \
  "s_nop 0\n\t" \
  "buffer_load_dwordx4 v8, %[so], s74 offen lds\n\t" \
  "s_add_u32 m0, %[ob], 12288\n\t" \
  "s_nop 0\n\t" \
  "buffer_load_dwordx4 v8, %[so], s75 offen lds\n\t" \
  "s_add_u32 m0, %[ob], 16384\n\t" \
  "s_nop 0\n\t" \
  "buffer_load_dwordx4 v8, %[so], s76 offen lds\n\t" \
  "s_add_u32 m0, %[ob], 20480\n\t" \
  "s_nop 0\n\t" \
  "buffer_load_dwordx4 v8, %[so], s77 offen lds\n\t" \
  "s_waitcnt vmcnt(10)\n\t" \
  "s_barrier\n\t" \
  "s_nop 2\n\t" \
  "s_add_u32 m0, %[ob], 90112\n\t" \
  "v_readlane_b32 s68, v9, 22\n\t" \
  "s_nop 4\n\t" \
  "buffer_load_dwordx4 v8, %[so], s68 offen lds\n\t" \
  "s_add_u32 m0, %[ob], 94208\n\t" \
  "v_readlane_b32 s68, v9, 23\n\t" \
  "s_nop 4\n\t" \
  "buffer_load_dwordx4 v8, %[so], s68 offen lds\n\t" \
  "s_add_u32 m0, %[ob], 98304\n\t" \
  "v_readlane_b32 s68, v9, 24\n\t" \
  "s_nop 4\n\t" \
  "buffer_load_dwordx4 v8, %[so], s68 offen lds\n\t" \
  "s_add_u32 m0, %[ob], 102400\n\t" \
  "v_readlane_b32 s68, v9, 25\n\t" \
  "s_nop 4\n\t" \
  "buffer_load_dwordx4 v8, %[so], s68 offen lds\n\t" \
  "s_add_u32 m0, %[ob], 106496\n\t" \
  "v_readlane_b32 s68, v9, 26\n\t" \
  "s_nop 4\n\t" \
  "buffer_load_dwordx4 v8, %[so], s68 offen lds\n\t" \
  "s_add_u32 m0, %[ob], 24576\n\t" \
  "v_readlane_b32 s68, v9, 6\n\t" \
  "s_nop 4\n\t" \
  "buffer_load_dwordx4 v8, %[so], s68 offen lds\n\t" \
  "s_add_u32 m0, %[ob], 28672\n\t" \
  "v_readlane_b32 s68, v9, 7\n\t" \
  "s_nop 4\n\t" \
  "buffer_load_dwordx4 v8, %[so], s68 offen lds\n\t" \
  "s_add_u32 m0, %[ob], 32768\n\t" \
  "v_readlane_b32 s68, v9, 8\n\t" \
  "s_nop 4\n\t" \
  "buffer_load_dwordx4 v8, %[so], s68 offen lds\n\t" \
  "s_add_u32 m0, %[ob], 36864\n\t" \
  "v_readlane_b32 s68, v9, 9\n\t" \
  "s_nop 4\n\t" \
  "buffer_load_dwordx4 v8, %[so], s68 offen lds\n\t" \
  "s_add_u32 m0, %[ob], 40960\n\t" \
  "v_readlane_b32 s68, v9, 10\n\t" \
  "s_nop 4\n\t" \
  "buffer_load_dwordx4 v8, %[so], s68 offen lds\n\t" \
  "s_add_u32 m0, %[ob], 45056\n\t" \
  "v_readlane_b32 s68, v9, 11\n\t" \
  "s_nop 4\n\t" \
  "buffer_load_dwordx4 v8, %[so], s68 offen lds\n\t" \
  "s_nop 3\n\t" \
  "s_barrier\n\t" \
  "s_nop 2\n\t" \
  "s_add_u32 m0, %[ob], 110592\n\t" \
  "v_readlane_b32 s68, v9, 27\n\t" \
  "s_nop 4\n\t" \
  "buffer_load_dwordx4 v8, %[so], s68 offen lds\n\t" \
  "s_add_u32 m0, %[ob], 114688\n\t" \
  "v_readlane_b32 s68, v9, 28\n\t" \
  "s_nop 4\n\t" \
  "buffer_load_dwordx4 v8, %[so], s68 offen lds\n\t" \
  "s_add_u32 m0, %[ob], 118784\n\t" \
  "v_readlane_b32 s68, v9, 29\n\t" \
  "s_nop 4\n\t" \
  "buffer_load_dwordx4 v8, %[so], s68 offen lds\n\t" \
  "s_add_u32 m0, %[ob], 122880\n\t" \
  "v_readlane_b32 s68, v9, 30\n\t" \
  "s_nop 4\n\t" \
  "buffer_load_dwordx4 v8, %[so], s68 offen lds\n\t" \
  "s_add_u32 m0, %[ob], 126976\n\t" \
  "v_readlane_b32 s68, v9, 31\n\t" \
  "s_nop 4\n\t" \
  "buffer_load_dwordx4 v8, %[so], s68 offen lds\n\t" \
  "s_add_u32 m0, %[ob], 49152\n\t" \
  "v_readlane_b32 s68, v9, 12\n\t" \
  "s_nop 4\n\t" \
  "buffer_load_dwordx4 v8, %[so], s68 offen lds\n\t" \
  "s_add_u32 m0, %[ob], 53248\n\t" \
  "v_readlane_b32 s68, v9, 13\n\t" \
  "s_nop 4\n\t" \
  "buffer_load_dwordx4 v8, %[so], s68 offen lds\n\t" \
  "s_add_u32 m0, %[ob], 57344\n\t" \
  "v_readlane_b32 s68, v9, 14\n\t" \
  "s_nop 4\n\t" \
  "buffer_load_dwordx4 v8, %[so], s68 offen lds\n\t" \
  "s_add_u32 m0, %[ob], 61440\n\t" \
  "v_readlane_b32 s68, v9, 15\n\t" \
  "s_nop 4\n\t" \
  "buffer_load_dwordx4 v8, %[so], s68 offen lds\n\t" \
  "s_add_u32 m0, %[ob], 65536\n\t" \
  "v_readlane_b32 s68, v9, 16\n\t" \
  "s_nop 4\n\t" \
  "buffer_load_dwordx4 v8, %[so], s68 offen lds\n\t" \
  "s_add_u32 m0, %[ob], 69632\n\t" \
  "v_readlane_b32 s68, v9, 17\n\t" \
  "s_nop 4\n\t" \
  "buffer_load_dwordx4 v8, %[so], s68 offen lds\n\t" \
  "s_waitcnt vmcnt(0) lgkmcnt(0)\n\t"
#define LD_NBAR 17
#define LD_CO 0
#define LD_X5 1
#define LD_HT 8
#define LD_TP 3
#define LD_CLOB "v11", "s68", "s69", "s70", "s71", "s72", "s73", "s74", "s75", "s76", "s77", "s78", "s79", "s80", "s81", "s82", "s83", "s85"

#define ENDW
#define TS(i)
#define TSS(i)
#define TOUT(n)

#define SPIN(cond,slp) do{ int sp_=0; while(cond){ __builtin_amdgcn_s_sleep(slp); if(++sp_>(1<<22)) break; } }while(0)
__device__ __forceinline__ unsigned rd1(const char* p){ return __builtin_amdgcn_readfirstlane(*(volatile LDS_AS unsigned*)(LDS_AS char*)p); }
__device__ __forceinline__ unsigned lds_inc(const char* p, int l){ unsigned r=0; if(l==0) r=__hip_atomic_fetch_add((LDS_AS unsigned*)(LDS_AS char*)p, 1u, __ATOMIC_RELAXED, __HIP_MEMORY_SCOPE_WORKGROUP); return __builtin_amdgcn_readfirstlane(r); }
__device__ __forceinline__ void lds_add(unsigned adr){ asm volatile("s_mov_b64 exec, 1\n\tds_add_u32 %0, %1\n\ts_mov_b64 exec, -1" :: "v"(adr), "v"(1u) : "memory"); }
__device__ __forceinline__ void publish(unsigned cnt, const unsigned* base, unsigned voff, unsigned tag){
  unsigned t;
  asm volatile("s_waitcnt vmcnt(0)\n\ts_mov_b64 exec, 1\n\tds_add_rtn_u32 %0, %1, %2\n\ts_waitcnt lgkmcnt(0)\n\tv_cmp_eq_u32 vcc, 3, %0\n\ts_cbranch_vccz PB_%=\n\t"
    "s_mov_b64 exec, 0xffff\n\tglobal_store_dword %3, %4, %5 sc1\n\tPB_%=:\n\ts_mov_b64 exec, -1"
    : "=&v"(t) : "v"(cnt), "v"(1u), "v"(voff), "v"(tag), "s"(base) : "memory", "vcc");
}
__device__ __forceinline__ void bars(int n){
  asm volatile("BL_%=:\n\ts_barrier\n\ts_sub_u32 %0, %0, 1\n\ts_cmp_lg_u32 %0, 0\n\ts_cbranch_scc1 BL_%=" : "+s"(n) :: "memory", "scc");
}
#define barc(rem) barc_(rem)
#define CTRA
#define CTRP
__device__ __forceinline__ void barc_(unsigned& rem){
  asm volatile("s_cmp_eq_u32 %0, 0\n\ts_cbranch_scc1 BC_%=\n\ts_barrier\n\ts_sub_u32 %0, %0, 1\n\tBC_%=:" : "+s"(rem) :: "memory", "scc");
}
template<int SLP> __device__ __forceinline__ void wait_flag_b(const unsigned* p, unsigned voff, unsigned tag, unsigned& rem CTRA){
  int spins=0;
  while(true){
    unsigned v; asm volatile("global_load_dword %0, %1, %2 sc1" : "=v"(v) : "v"(voff), "s"(p) : "memory");
    barc(rem); if(PB2) barc(rem);
    asm volatile("s_waitcnt vmcnt(0)" : "+v"(v) :: "memory");
    if(__all(v==tag) || ++spins>(1<<21)) break;
    if(rem==0) __builtin_amdgcn_s_sleep(SLP);
  }
}
__device__ __forceinline__ void stout(void* sbase, unsigned voff, u32x4 v){
  st16cs(sbase,voff,v);
}

typedef unsigned u32x8 __attribute__((ext_vector_type(8)));
typedef unsigned u32x16 __attribute__((ext_vector_type(16)));
__device__ __forceinline__ char* ptr64(unsigned lo, unsigned hi){ return (char*)(((unsigned long long)hi<<32)|lo); }
__device__ __forceinline__ u32x4 srd(const char* p, unsigned bytes){ return u32x4{(unsigned)(size_t)p,(unsigned)((size_t)p>>32)&0xffffu,bytes,0x20000u}; }
constexpr size_t WS_O=0, WS_KV=(size_t)S*DM*2, WS_Y=(size_t)S*DM*4, WS_FL=(size_t)S*DM*12;

__global__ __launch_bounds__(768,3) void fused_kernel(const u16* x_, const u16* wqkv_, const u16* wo_, char* ws_, u16* out_,
    const u16* kin_, const u16* vin_, const u16* shk_, const u16* hk_, const u16* qin_, const u16* sD_)
{
  __shared__ __attribute__((aligned(128))) char smem[ALL_LDS];
  TS(0);
  int wv;
  asm volatile(
      "s_load_dwordx16 s[48:63], %[ka], 0x0\n\ts_load_dwordx4 s[64:67], %[ka], 0x40\n\ts_load_dwordx2 s[78:79], %[ka], 0x50\n\ts_getpc_b64 s[72:73]\n\tv_readfirstlane_b32 s75, v0\n\ts_lshr_b32 s74, %[b], 3\n\ts_mul_i32 s74, s74, 12\n\ts_lshr_b32 s75, s75, 6\n\t"
      "s_add_u32 s74, s74, s75\n\ts_min_u32 s74, s74, %[cl]\n\ts_lshl_b32 s74, s74, 7\n\ts_mov_b32 m0, %[sk]\n\tv_mov_b32 v1, s74\n\ts_mov_b64 exec, 1\n\tglobal_load_lds_dword v1, s[72:73]\n\ts_mov_b64 exec, -1\n\t"
      "s_mov_b32 s76, %[b]\n\ts_cmp_lt_u32 s75, 4\n\ts_cbranch_scc1 FK_G\n\ts_cmp_lt_u32 s75, 8\n\ts_cbranch_scc0 FK_C\n\t"
      "v_and_b32 v9, 7, v0\n\tv_and_b32 v10, 63, v0\n\ts_lshl_b32 s74, %[b], 19\n\ts_lshl_b32 s73, %[b], 11\n\ts_or_b32 s74, s74, s73\n\ts_and_b32 s74, s74, 0x3fc000\n\ts_lshl_b32 s73, s75, 9\n\t"
      "s_add_u32 s74, s74, s73\n\ts_add_u32 s74, s74, 0x7800\n\tv_lshl_add_u32 v9, v9, 11, s74\n\tv_and_b32 v1, 24, v0\n\ts_lshl2_add_u32 s74, %[b], s75\n\ts_mul_i32 s74, s74, %[nw]\n\tv_lshl_add_u32 v9, v1, 4, v9\n\t"
      "s_add_u32 s74, s74, %[w0]\n\tv_add_lshl_u32 v10, v10, s74, 7\n\t"
      "s_setprio %[lp]\n\ts_and_b32 s74, s75, 3\n\tv_lshrrev_b32 v1, 3, v0\n\tv_lshrrev_b32 v3, 4, v0\n\ts_and_b32 s72, %[b], 1\n\ts_and_b32 s73, %[b], 0xe0\n\ts_lshl_b32 s72, s72, 10\n\ts_lshl2_add_u32 s72, s73, s72\n\t"
      "v_and_b32 v4, 2, v1\n\tv_and_or_b32 v3, v3, 4, v4\n\tv_and_b32 v4, 31, v1\n\tv_xor_b32 v3, v3, v0\n\tv_mul_u32_u24 v4, 0x1800, v4\n\tv_and_b32 v3, 7, v3\n\tv_lshl_add_u32 v3, v3, 4, s72\n\t"
      "s_mul_i32 s39, s74, 0xc00\n\ts_lshl_b32 s38, s74, 12\n\ts_lshl_b32 s37, s74, 9\n\tv_add_u32 v5, v3, v4\n\tv_and_b32 v1, 7, v1\n\tv_add_u32 v6, 0x400, v5\n\tv_add_u32 v7, 0x800, v5\n\t"
      "s_and_b32 s72, %[b], 6\n\ts_and_b32 s73, %[b], 24\n\ts_lshl4_add_u32 s72, s72, s73\n\tv_xor_b32 v3, v1, v0\n\ts_lshl_b32 s72, s72, 15\n\tv_and_b32 v3, 7, v3\n\tv_lshl_add_u32 v1, v1, 11, s72\n\t"
      "s_mov_b32 s47, 0x20000\n\ts_mov_b32 s46, 0x600000\n\tv_lshl_add_u32 v1, v3, 4, v1\n\ts_mov_b32 s43, s47\n\ts_mov_b32 s42, 0x408000\n\ts_lshl_b32 s72, s74, 6\n\ts_and_b32 s72, s72, 64\n\t"
      "v_xor_b32 v4, s72, v1\n\ts_lshl_b32 s72, s74, 13\n\ts_add_u32 s72, s72, 0x40000\n\tv_add_u32 v4, s72, v4\n\ts_lshl_b32 s72, s74, 16\n\tv_add_u32 v0, s72, v1\n\ts_movk_i32 s73, 0x3c00\n\t"
      "v_add_u32 v1, s73, v0\n\ts_add_u32 s38, s38, %[xf]\n\tv_add_u32 v2, s73, v1\n\ts_add_u32 s37, s37, %[xf]+16384\n\tv_add_u32 v3, s73, v2\n\tv_max_u32 v0, 0x8000, v0\n\tv_max_u32 v1, 0x8000, v1\n\t"
      "s_waitcnt lgkmcnt(0)\n\ts_mov_b64 s[44:45], s[50:51]\n\ts_sub_u32 s40, s48, 0x8000\n\ts_subb_u32 s41, s49, 0\n\t"
      LD_ASM1
      "s_branch FK_L"
      : "=s"(wv)
      : "{v0}"(threadIdx.x), [ka]"s"(__builtin_amdgcn_kernarg_segment_ptr()), [b]"s"(blockIdx.x), [cl]"n"(CODEL-1+CODEX), [sk]"n"(SINK+256), [lp]"n"(LPR), [xf]"n"(X_OFF), [nw]"n"(3*TPW), [w0]"n"(TW0*3072-12*TPW), [wm]"n"((1<<(3*TPW))-1)
      : "memory", "scc", "vcc", "s48", "s49", "s50", "s51", "s52", "s53", "s54", "s55", "s72", "s73", "s74", "s75", "s76", "s78", "s79", "v1", "v2", "v3", "v4", "v5", "v6", "v7", "v9", "v10",
        "s40", "s41", "s42", "s43", "s44", "s45", "s46", "s47", "s37", "s38", "s39", "s56", "s57", "s58", "s59", "s60", "s61", "s62", "s63", "s64", "s65", "s66", "s67", "s68");
  #define ENTER(lbl) int w8, bid; u32x8 a0, a1; u32x4 a2; u32x2 a3; asm volatile(lbl ":" : "={s75}"(w8), "={s76}"(bid), "={s[48:55]}"(a0), "={s[56:63]}"(a1), "={s[64:67]}"(a2), "={s[78:79]}"(a3) :: "memory");
  #define ARGW asm volatile("s_waitcnt lgkmcnt(0)" : "+{s[48:55]}"(a0), "+{s[56:63]}"(a1), "+{s[64:67]}"(a2), "+{s[78:79]}"(a3));
  #define ROLE const int w=w8&3, xcd=bid&7, idx=bid>>3, rb=(xcd>>1)*4+(idx&3), hp=(xcd&1)*8+(idx>>2), s0=rb*128; (void)s0; (void)hp;
  #define LANEVARS int l; asm volatile("v_mbcnt_lo_u32_b32 %0, -1, 0\n\tv_mbcnt_hi_u32_b32 %0, -1, %0" : "=v"(l)); const int g=l>>4, fr=l&15, tq=(l&15)>>2, tp=l&3; (void)g; (void)fr; (void)tq; (void)tp;
  #define ARGS0 const char* x=ptr64(a0[0],a0[1]); const char* wqkv=ptr64(a0[2],a0[3]); const char* wo=ptr64(a0[4],a0[5]); char* ws=ptr64(a0[6],a0[7]); \
    unsigned* flags=(unsigned*)(ws+WS_FL); const int hot0=(3-(int)(a0[2]>>7))&7; (void)x; (void)wqkv; (void)wo; (void)flags; (void)hot0;
  #define OCOL ((hp&8)|((hp+(int)(a0[2]>>7)-(int)(a0[4]>>7)+OSH)&7))
  #define ORA ((unsigned)(hp-hot0-1)&7u)
  #define ORE(c) (ORA==7u? (c)-1u : (c))
  #define ORO(c) (ORE(c)>=NF && ORE(c)<NF+7u)
  #define ORD(c) (((((ORE(c)<14u? ((ORA==7u? 0u:ORA)+(ORO(c)? ORE(c)-NF : (ORE(c)<NF? ORE(c) : ORE(c)-7u)))%7u : 7u)+hot0+1)&7u))+8u*(((hp>>3)^(ORO(c) || (c)==15u))&1u))
  const unsigned sb0=__builtin_amdgcn_readfirstlane(lds_addr(smem));
  const char* syn=smem+SYN;

  if(__builtin_expect(wv==1,1)){
    ENTER("FK_L")
    u32x4 sx, sw; unsigned xt, xl, wl, xoA, xoB, xo, xo1, xo2, wof[3];
    asm volatile("; descriptors and LDS bases" : "={s[40:43]}"(sx), "={s[44:47]}"(sw), "={s38}"(xl), "={s39}"(wl), "={s37}"(xt));
    asm volatile("; lane offsets" : "={v0}"(xoA), "={v1}"(xoB), "={v2}"(xo), "={v3}"(xo1), "={v4}"(xo2), "={v5}"(wof[0]), "={v6}"(wof[1]), "={v7}"(wof[2]));
    unsigned ltr=0, ltr2=0, ltr3=0, sd; (void)ltr; (void)ltr2; (void)ltr3;
    const char* pw;
    {
      ROLE
      LANEVARS
      ARGS0
      const int hg=((idx>>2)-hot0)&7, hi=hot0*4+(idx&3);
      const unsigned long long hm= hg==0? 1ull : hg==HGR? 0xfffull : 0x3full;
      const unsigned hto=(unsigned)((hg? w*393216 : 0x40000000)+(l<6? l*2048+idx*12288 : (l-6)*2048+hi*12288));
      pw=wqkv+(idx*6144+(xcd&1)*1024+(xcd>>1)*256+w*64);
      asm volatile(LD_ASM2
        : "=&{s84}"(sd), "+s"(w8), "+s"(bid), "+s"(a0)
        : "{v0}"(xoA), "{v1}"(xoB), "{v2}"(xo), "{v3}"(xo1), "{v4}"(xo2), "{v5}"(wof[0]), "{v6}"(wof[1]), "{v7}"(wof[2]),
          "{v16}"(hto), [sh]"s"(srd(wqkv+((xcd&1)*8+hot0)*128,6291456u-1920u)), [hm]"s"(hm),
          "{s[40:43]}"(sx), "{s[44:47]}"(sw), "{s37}"(xt), "{s38}"(xl), "{s39}"(wl), [pw]"s"(pw), [tsk]"n"(SINK+256)
        : "memory", "scc", "vcc", LD_CLOB);
    }
    ROLE
    LANEVARS
    ARGS0
    const unsigned wd=OCOL*128;
    {
      const unsigned wof2=(unsigned)(w*16384+(l>>3)*2048+((((l&7)>>1)^((l>>4)&3))*32)+(l&1)*16);
      const unsigned to=ORD((unsigned)(l>>1)&15u);
      const unsigned tab=to*131072u+(l&1)*65536;
      asm volatile(LD_ASM3
        : "+{s84}"(sd)
        : "{v0}"(xoA), "{v1}"(xoB), "{v2}"(xo), "{v3}"(xo1), "{v4}"(xo2), "{v5}"(wof[0]), "{v6}"(wof[1]), "{v7}"(wof[2]), "{v8}"(wof2), "{v9}"(tab),
          "{s[40:43]}"(sx), "{s[44:47]}"(sw), [so]"s"(srd(wo+wd,2097152u-wd)), "{s37}"(xt), "{s38}"(xl), "{s39}"(wl), [ob]"s"(sb0+w*1024), [pw]"s"(pw),
          [pq]"s"(wo+((xcd&1)*1024+((3-(int)(a0[4]>>7))&7)*128+(idx*32+w*8)*2048))
        : "memory", "scc", "vcc", LD_CLOB);
    }
    const unsigned tag=rd1(syn+64)+1u;
    lds_add(sb0+SYN+12);
    TS(2);
    if(w>=4-NPOL){
      const unsigned* fp=flags+F_O+(rb*16+hp)*16; const unsigned fo=ORD((unsigned)l&15u)*4u;
      const unsigned madr=sb0+(l==0? SYN+20 : SINK+l*4);
      SPIN(rd1(syn+12)<4u || rd1(syn+16)<4u,1);
      if(w==3) PUBM(madr,1u);
      if(PD0) __builtin_amdgcn_s_sleep(PD0);
      if(w==2) __builtin_amdgcn_s_sleep(PD1); if(w==1) __builtin_amdgcn_s_sleep(2*PD1); if(w==0) __builtin_amdgcn_s_sleep(3*PD1);
      unsigned rdy=1; int spins=0;
      while(rdy!=0xFFFFu){
        unsigned v;
        asm volatile("s_mov_b64 exec, 0xffff\n\tglobal_atomic_or %0, %1, %3, %2 sc0 sc1\n\ts_mov_b64 exec, -1\n\ts_waitcnt vmcnt(0)" : "=&v"(v) : "v"(fo), "s"(fp), "v"(0u) : "memory");
        unsigned r=((unsigned)__ballot(v==tag)&0xFFFFu)|1u;
        if(++spins>(1<<21)) r=0xFFFFu;
        if(r!=rdy){ PUBM(madr,(unsigned)__builtin_ctz(~r)); rdy=r; if(w<3 && (r&0x3FFFu)==0x3FFFu) break; }
        else __builtin_amdgcn_s_sleep(OSLP);
      }
      TS(3);
      if(bid==0 && w==3){
        int sp=0;
        while(true){
          u32x4 v; asm volatile("global_load_dwordx4 %0, %1, %2 sc1\n\ts_waitcnt vmcnt(0)" : "=&v"(v) : "v"(l*16), "s"(flags+F_Y) : "memory");
          if(__all(v[0]==tag && v[1]==tag && v[2]==tag && v[3]==tag) || ++sp>(1<<20)) break;
          __builtin_amdgcn_s_sleep(12);
        }
        if(l==0) stflag(flags, tag);
      }
    } else { TS(3); }
    TOUT(4);
    __builtin_amdgcn_endpgm();
  }

  if(wv==0){
    ENTER("FK_G")
    ROLE
    __builtin_amdgcn_s_setprio(GPR);
    const int h=w&1, r=w>>1;
    LANEVARS
    TSS(3);
    ARGW
    ARGS0
    if(PGT&1){ if(w==1 && l<7) touch(ws, l<6? l*2097152+PGO : (unsigned)WS_FL+2048, sb0+SINK+512); }
    if(PGT&2){ if(w==2 && l<4) touch(ptr64(a1[0],a1[1]), l*2097152+PGO, sb0+SINK+512); }
    if(PGT&4){ if(w==3 && l<1) touch(wo, PGO, sb0+SINK+512); if(w==0 && l<2) touch(wqkv, l*2097152+2097152, sb0+SINK+512); }
    TSS(4);
    {
      unsigned lx0=sb0+X_OFF+fr*128+((g^(fr&7))*16), lx1=sb0+X_OFF+fr*128+(((4+g)^(fr&7))*16);
      const int sw=wswz(8*g+tq);
      unsigned lw0=sb0+g*3072+tq*128+(unsigned)((((2*h)^sw)*32)+8*tp), lw1=sb0+g*3072+tq*128+(unsigned)((((2*h+1)^sw)*32)+8*tp);
      char* ot=ws+WS_O+(rb*16+hp)*16384+r*8192+h*2048;
      unsigned it; unsigned long long rm; asm("s_cmp_lg_u32 %1, 0\n\ts_cselect_b64 %0, -1, 0" : "=s"(rm) : "s"(r) : "scc");
      asm volatile(N2_ASM
        : [it]"=&s"(it), "+{v56}"(lx0), "+{v57}"(lx1), "+{v58}"(lw0), "+{v59}"(lw1)
        : "{v62}"(l*16), "{v63}"(sb0+EX_OFF+h*3072+l*16), [r]"s"(r), [ot]"s"(ot), [ou]"s"(ot+4096), [fa]"s"(sb0+SYN+32+w*4), [fp]"s"(sb0+SYN+32+(w^2)*4),
          [vo]"s"(2048-1024*r), [rm]"s"(rm), [z]"s"(rb|r)
        : "memory", "scc", "vcc", N2_CLOB);
    }
    TS(3);
    unsigned tb;
    {
      LANEVARS
      const unsigned tag=rd1(syn+64)+1u;
      const unsigned ra=sb0+(4*g+tq)*128+8*tp; const int sw=(2*g+(tq>>1))&3;
      unsigned b0=ra+((0^sw)*32), b1=ra+((1^sw)*32), b2=ra+((2^sw)*32), b3=ra+((3^sw)*32), vo=(unsigned)(w*4096+l*16), sy=sb0+SYN+20;
      unsigned sr=sb0+EX_OFF+w*2048+(l>>3)*128+(((l&7)^(l>>3))*16), sg=sb0+EX_OFF+w*2048+fr*128+(((g>>1)^(fr&7))*16)+(g&1)*8;
      tb=ORD((unsigned)l&15u)*16384u;
      asm volatile("" : "+{v160}"(b0), "+{v161}"(b1), "+{v162}"(b2), "+{v163}"(b3), "+{v164}"(vo), "+{v165}"(sy), "+{v166}"(sr), "+{v167}"(sg), "+{v96}"(tb));
      publish(sb0+SYN+16, flags+F_O+rb*256+hp, l*64, tag);
      TS(4);
      __builtin_amdgcn_s_setprio(G2P);
      unsigned iss=0, q0, q1, q2, cn=0;
      asm volatile(G3_ASM
        : [iss]"+&s"(iss), [t0]"=&s"(q0), [t1]"=&s"(q1), [t2]"=&s"(q2), [cn]"+&s"(cn),
          "+{v160}"(b0), "+{v161}"(b1), "+{v162}"(b2), "+{v163}"(b3), "+{v165}"(sy), "+{v96}"(tb)
        : "{v164}"(vo), "{v166}"(sr), "{v167}"(sg), [go]"s"(60+w*4), [sy]"n"(SYN+20), [ob]"s"(srd(ws+WS_O+rb*262144,262144u)), [op]"s"(ptr64(a1[0],a1[1])+((s0+w*32)*4096+2048+OCOL*128))
        : "memory", "scc", "vcc", G3_CLOB);
    }
    {
      ENDW TS(6); TOUT(7);
    }
    __builtin_amdgcn_endpgm();
  }

  {
    ENTER("FK_C")
    ROLE
    char* cr=smem+C_OFF+2048+w*4096;
    const unsigned crs=sb0+C_OFF+2048+w*4096;
    const int sblk=bid>>3, td0=(bid&7)*128+(w&1)*64;
    const int vb=(LOCA&1)? xcd*32+(idx&16)+(idx&1)*8+((idx>>1)&7) : bid, fb=(LOCA&2)? (idx&15)*16+xcd*2+(idx>>4) : bid;
    const int d=(vb>>4)*64+(((vb>>3)&1)*4+w)*8+(vb&7);
    int kv0=KV0+((sblk>>KSH)&KVS);
    static_assert(KV0+KVS+PP_NB<=LD_NBAR && KV0>=EB, "barrier budget");
    unsigned tagv, ctr; f32x8 RB;
    const int fd0=(fb&15)*64, fs0=(fb>>4)*128+w*32;
    u32x4 qv[4];
    f32x16 A0,A1,A2,A3;
    const int rw=sblk*64+(w>>1)*32, j=rw>>5;
    unsigned k0,k1,k2,ko,ta,se,so,ha,l16,rb0[2];
    {
      LANEVARS
      if(w==0 && l<25 && l!=16) ((unsigned*)(smem+SYN))[l]=0u;
      if(l<32) *(u32x4*)(smem+C_OFF+w*512+l*16)=u32x4{0,0,0,0};
      asm volatile("" ::: "memory");
      __builtin_amdgcn_s_setprio(CPR);
      const int dg=l&7, rg=l>>3;
      ko=(unsigned)(rg*8192+dg*16);
      const bool top=(rw==0 && rg==0);
      k0=top? ko+6144:ko; k1=top? ko+6144:ko+2048; k2=top? ko+6144:ko+4096;
      ta=sb0+EX_OFF+w*1024+(w&1)*512+dg*64; se=(l&8)? 0x03020706u : 0x05040100u; ha=crs+(32*g-4*tq+4*tp)*2; l16=l*16;
      so=(unsigned)(rg*32768+((j>>2)&3)*128+((dg&4)|((dg+j)&3))*16);
      #pragma unroll
      for(int k=0;k<2;k++){ const int q=fr-1-2*g+k; rb0[k]=sb0+C_OFF+(4+(q>>1))*256+((q>>3)&3)*64+((w+(q>>1))&3)*16+(q&1)*8; }
      asm volatile("" : "+{v152}"(k0), "+{v153}"(k1), "+{v154}"(k2), "+{v155}"(ko), "+{v156}"(ta), "+{v157}"(se), "+{v159}"(so), "+{v163}"(l16), "+{v165}"(ha), "+{v166}"(rb0[0]), "+{v167}"(rb0[1]));
    }
    TSS(3);
    ARGW
    if(EB==1) asm volatile("s_barrier" : "+s"(a1), "+s"(a2), "+s"(a3), "+s"(a0) :: "memory");
    if(EB==2) asm volatile("s_barrier\n\ts_barrier" : "+s"(a1), "+s"(a2), "+s"(a3), "+s"(a0) :: "memory");
    ARGS0
    char* out=ptr64(a1[0],a1[1]); const char* qp=ptr64(a2[2],a2[3])+(fs0*DM+fd0)*2;
    {
      const char* kb=ptr64(a1[2],a1[3])+(rw*DM+td0)*2; const char* vb=ptr64(a1[4],a1[5])+(rw*DM+td0)*2;
      unsigned long long zm; asm("s_cmp_eq_u32 %1, 0\n\ts_cselect_b64 %0, 0xff, 0" : "=s"(zm) : "s"(rw) : "scc");
      unsigned t0;
      if(PGT&8){ const char* tp= w==0? kb : w==1? vb : w==2? qp : ptr64(a2[0],a2[1])+d*4096;
        asm volatile("s_mov_b32 m0, %1\n\ts_mov_b64 exec, 1\n\tglobal_load_lds_dword v163, %0\n\ts_mov_b64 exec, -1" :: "s"(tp), "s"(sb0+SINK+512), "{v163}"(l16) : "memory"); }
      TSS(5);
      asm volatile(PP_ASM
        : [t0]"=&s"(t0), "=&{v164}"(ctr), "+{v159}"(so), "=&{v160}"(tagv), "=&{v[24:31]}"(RB), "=&{v[44:47]}"(qv[0]), "=&{v[48:51]}"(qv[1]), "=&{v[52:55]}"(qv[2]), "=&{v[56:59]}"(qv[3]),
          "=&{v[64:79]}"(A0), "=&{v[80:95]}"(A1), "=&{v[96:111]}"(A2), "=&{v[112:127]}"(A3)
        : "{v152}"(k0), "{v153}"(k1), "{v154}"(k2), "{v155}"(ko), "{v156}"(ta), "{v157}"(se), "{v166}"(rb0[0]), "{v167}"(rb0[1]), "{v165}"(ha), "{v163}"(l16),
          [qb0]"s"(qp), [qb2]"s"(qp+4096), [mk]"s"(0x180u), [em]"s"(1ull|(512ull<<w)), [hd]"s"(crs),
          [kb0]"s"(kb-6144), [kb3]"s"(kb), [kb5]"s"(kb+4096), [vb0]"s"(vb), [vb2]"s"(vb+4096), [sb]"s"(ws+WS_KV+td0*4096+j*512),
          [n0]"s"(kv0-EB-(HH==2)), [ff]"s"(flags+F_PP+(td0>>6)*64+j), [zm]"s"(zm), [shd]"s"(sb0+EX_OFF+w*1024), [tgd]"s"(sb0+SYN+64), [shp]"s"(ptr64(a1[6],a1[7])+(bid&7)*1024), [fl]"s"(flags),
          [hp]"s"(ptr64(a2[0],a2[1])+d*4096), [dp]"s"(ptr64(a3[0],a3[1])+d*2),
          [ph]"s"(wqkv+(((xcd&1)*8+hot0)*128+idx*6144+(w>>1)*196608+(w&1)*64))
        : "memory", "scc", PP_CLOB);
    }
    LANEVARS
    const unsigned tag=__builtin_amdgcn_readfirstlane(tagv)+1u;
    unsigned rem=LD_NBAR-PP_NB-kv0;
    barc(rem);
    const unsigned* pf=flags+F_PP+(d>>6)*64+(KPOL? w*16:0);
    const char* kvp=ws+WS_KV+((vb>>4)*8+(vb&7))*32768+((vb>>3)&1)*64+w*8192;
    const unsigned kvo=(unsigned)((l>>2)*128+(l&3)*16);
    int bidx=((l>=16)? l-16 : (l==0? 63 : l+47))*4;
    asm volatile("" : "+{v32}"(bidx));
    __builtin_amdgcn_s_setprio(3);
    while(rem>PREM) barc(rem);
    wait_flag_b<8>(pf, (KPOL? fr:l)*4, tag, rem CTRP);
    #pragma unroll
    for(int i=0;i<4;i++){ if(i%KTH==0 && i) barc(rem); gldsc(kvp+i*2048, kvo, crs+i*1024); }
    barc(rem); if(PB2) barc(rem);
    WAITV(0);
    asm volatile("s_mov_b32 m0, %2\n\ts_mov_b64 exec, 1\n\tglobal_load_lds_dword %0, %1 sc1\n\ts_mov_b64 exec, -1" :: "v"(0u), "s"(pf), "s"(sb0+SYN+100+w*4) : "memory");
    f32x16 ac0, ac1; unsigned vtr, cn=0; (void)vtr;
    unsigned ca=sb0+CAR+w*1280+(fr*4+g)*4, cz= l? ca : sb0+C_OFF;
    asm volatile("s_nop 1\n\t" CV_ASM
      : "=&{v[128:143]}"(ac0), "=&{v[144:159]}"(ac1), [rem]"+&s"(rem), [cn]"+&s"(cn), "+{v40}"(ca), "+{v41}"(cz)
        , "+{v[44:47]}"(qv[0]), "+{v[48:51]}"(qv[1]), "+{v[52:55]}"(qv[2]), "+{v[56:59]}"(qv[3])
      : "{v[64:79]}"(A0), "{v[80:95]}"(A1), "{v[96:111]}"(A2), "{v[112:127]}"(A3),
        "{v[24:31]}"(RB), "{v32}"(bidx), "{v33}"(sb0+SYN+80+w*4), "{v61}"(sb0+SYN+100+(l&3)*4), [tg]"s"(tag),
        "{v42}"((256*(g&1)+16*fr+4*(g&2))*2), [yb]"s"(ws+WS_Y+d*4096), "{v43}"((l>>3)*8192+(l&7)*16), [q0]"s"(qp), [q2]"s"(qp+4096)
      : "memory", "scc", CV_CLOB);
    TS(4);
    __builtin_amdgcn_s_setprio(3);
    publish(sb0+SYN+24, flags+F_Y+vb, l*1024, tag);
    asm volatile("" : "+v"(qv[0]), "+v"(qv[1]), "+v"(qv[2]), "+v"(qv[3]));
    TS(5);
    float qf[32];
    #pragma unroll
    for(int i=0;i<16;i++){ qf[2*i]=bflo(qv[i>>2][i&3]); qf[2*i+1]=bfhi(qv[i>>2][i&3]); }
    unsigned ya=crs+(g*8+tq)*64+tp*8, wa0=crs+fr*128+((g^(fr&7))*16), wa1=crs+fr*128+(((4+g)^(fr&7))*16), ra[4], oo=(unsigned)((l>>3)*16384+(l&7)*16), yo2=(l>>2)*4096+(l&3)*16;
    #pragma unroll
    for(int i=0;i<4;i++){ const int row=4*(l>>3)+i; ra[i]=crs+row*128+(((l&7)^(row&7))*16); }
    char* op=out+fs0*4096+fd0*2; char* op2=op+8192; const char* yg=ws+WS_Y+fd0*4096+fs0*2;
    #pragma unroll
    for(int i=0;i<32;i++) asm volatile("" : "+v"(qf[i]));
    asm volatile("" : "+v"(ya), "+v"(wa0), "+v"(wa1), "+v"(ra[0]), "+v"(ra[1]), "+v"(ra[2]), "+v"(ra[3]), "+v"(oo), "+v"(yo2));
    asm volatile("" : "+s"(op), "+s"(op2), "+s"(yg));
    if((YPM>>w)&1){
      SPIN(rd1(syn+24)<4u,1);
      if(YPM>1){ if(w==1) __builtin_amdgcn_s_sleep(YPD); if(w==2) __builtin_amdgcn_s_sleep(2*YPD); if(w==3) __builtin_amdgcn_s_sleep(3*YPD); }
      { int sp=0;
        while(true){
          unsigned v=tag; asm volatile("s_mov_b64 exec, 0xffff\n\tglobal_atomic_or %0, %1, %3, %2 sc0 sc1\n\ts_mov_b64 exec, -1\n\ts_waitcnt vmcnt(0)" : "+&v"(v) : "v"(l*4), "s"(flags+F_Y+fb*16), "v"(0u) : "memory");
          if(__all(v==tag) || ++sp>(1<<21)) break;
          if(YPM>1){ if(rd1(syn+28)) break; } else __builtin_amdgcn_s_sleep(YSLP);
        } }
      PUB(sb0+(l==0? SYN+28 : SINK+l*4), 1u);
    } else SPIN(rd1(syn+28)==0u,YS2);
    #pragma unroll
    for(int i=0;i<4;i++) gldsc(yg+i*65536, yo2, crs+i*1024);
    {
      u32x4 y4[4];
      asm volatile("s_waitcnt vmcnt(0)\n\t"
        "ds_read_b64_tr_b16 v[0:1], %4\n\tds_read_b64_tr_b16 v[2:3], %4 offset:256\n\tds_read_b64_tr_b16 v[4:5], %4 offset:32\n\tds_read_b64_tr_b16 v[6:7], %4 offset:288\n\t"
        "ds_read_b64_tr_b16 v[8:9], %4 offset:2048\n\tds_read_b64_tr_b16 v[10:11], %4 offset:2304\n\tds_read_b64_tr_b16 v[12:13], %4 offset:2080\n\tds_read_b64_tr_b16 v[14:15], %4 offset:2336\n\t"
        "s_waitcnt lgkmcnt(6)\n\tds_write_b128 %5, v[0:3]\n\ts_waitcnt lgkmcnt(5)\n\tds_write_b128 %5, v[4:7] offset:2048\n\t"
        "s_waitcnt lgkmcnt(4)\n\tds_write_b128 %6, v[8:11]\n\ts_waitcnt lgkmcnt(3)\n\tds_write_b128 %6, v[12:15] offset:2048\n\t"
        "ds_read_b128 %0, %7\n\tds_read_b128 %1, %8\n\tds_read_b128 %2, %9\n\tds_read_b128 %3, %10\n\ts_waitcnt lgkmcnt(0)"
        : "=&v"(y4[0]), "=&v"(y4[1]), "=&v"(y4[2]), "=&v"(y4[3])
        : "v"(ya), "v"(wa0), "v"(wa1), "v"(ra[0]), "v"(ra[1]), "v"(ra[2]), "v"(ra[3])
        : "memory", "v0", "v1", "v2", "v3", "v4", "v5", "v6", "v7", "v8", "v9", "v10", "v11", "v12", "v13", "v14", "v15");
      #pragma unroll
      for(int i=0;i<4;i++){
        u32x4 ov;
        #pragma unroll
        for(int e=0;e<4;e++) ov[e]=pack2(mul1(qf[8*i+2*e],bflo(y4[i][e])), mul1(qf[8*i+2*e+1],bfhi(y4[i][e])));
        stout((i&2? op2:op)+(i&1)*4096, oo, ov);
      }
    }
    ENDW TS(6); TOUT(7);
    __builtin_amdgcn_endpgm();
  }
}

extern "C" void kernel_launch(void* const* d_in, const int* in_sizes, int n_in, void* d_out, int out_size, void* d_ws, size_t ws_size, hipStream_t stream){
  const u16* q=(const u16*)d_in[0]; const u16* k=(const u16*)d_in[1]; const u16* v=(const u16*)d_in[2];
  const u16* shk=(const u16*)d_in[3]; const u16* hk=(const u16*)d_in[4]; const u16* sD=(const u16*)d_in[5];
  const u16* x=(const u16*)d_in[6]; const u16* wqkv=(const u16*)d_in[7]; const u16* wo=(const u16*)d_in[8];
  fused_kernel<<<256,768,0,stream>>>(x,wqkv,wo,(char*)d_ws,(u16*)d_out,k,v,shk,hk,q,sD);
}
```

```cpp
#include <hip/hip_runtime.h>
#include <hip/hip_bf16.h>
#include <stdint.h>

typedef unsigned short u16;
typedef short bf16x8 __attribute__((ext_vector_type(8)));
typedef short s16x4 __attribute__((ext_vector_type(4)));
typedef float f32x2 __attribute__((ext_vector_type(2)));
typedef float f32x4 __attribute__((ext_vector_type(4)));
typedef float f32x8 __attribute__((ext_vector_type(8)));
typedef float f32x16 __attribute__((ext_vector_type(16)));
typedef unsigned u32x4 __attribute__((ext_vector_type(4)));
typedef unsigned u32x2 __attribute__((ext_vector_type(2)));
typedef __bf16 bf16x2_t __attribute__((ext_vector_type(2)));
#define LDS_AS __attribute__((address_space(3)))

__device__ __forceinline__ float bflo(unsigned x){ return __uint_as_float(x<<16); }
__device__ __forceinline__ float bfhi(unsigned x){ return __uint_as_float(x&0xffff0000u); }
__device__ __forceinline__ unsigned pack2(float lo, float hi){ return __builtin_bit_cast(unsigned, __builtin_convertvector(f32x2{lo,hi}, bf16x2_t)); }
__device__ __forceinline__ float mul1(float a, float b){ float r; asm("v_mul_f32 %0, %1, %2" : "=v"(r) : "v"(a), "v"(b)); return r; }
__device__ __forceinline__ float fma1(float a, float b, float c){ float r; asm("v_fma_f32 %0, %1, %2, %3" : "=v"(r) : "v"(a), "v"(b), "v"(c)); return r; }

__device__ __forceinline__ void glds(const void* sbase, unsigned voff, unsigned ldsaddr){
  asm volatile("s_mov_b32 m0, %2\n\ts_nop 0\n\tglobal_load_lds_dwordx4 %0, %1" :: "v"(voff), "s"(sbase), "s"(ldsaddr) : "memory");
}
__device__ __forceinline__ void gldsc(const void* sbase, unsigned voff, unsigned ldsaddr){
  asm volatile("s_mov_b32 m0, %2\n\ts_nop 0\n\tglobal_load_lds_dwordx4 %0, %1 sc1" :: "v"(voff), "s"(sbase), "s"(ldsaddr) : "memory");
}
__device__ __forceinline__ void touch(const void* sbase, unsigned voff, unsigned ldsaddr){
  asm volatile("s_mov_b32 m0, %2\n\ts_nop 0\n\tglobal_load_lds_dword %0, %1" :: "v"(voff), "s"(sbase), "s"(ldsaddr) : "memory");
}
__device__ __forceinline__ void st16c(void* p, u32x4 v){ asm volatile("global_store_dwordx4 %0, %1, off sc1\n\ts_nop 1" :: "v"(p), "v"(v) : "memory"); }
__device__ __forceinline__ void st16cs(void* sbase, unsigned voff, u32x4 v){ asm volatile("global_store_dwordx4 %0, %1, %2 sc1\n\ts_nop 1" :: "v"(voff), "v"(v), "s"(sbase) : "memory"); }
#define GLB_AS __attribute__((address_space(1)))
__device__ __forceinline__ unsigned ldflag(const unsigned* p){ return __hip_atomic_load((const GLB_AS unsigned*)p, __ATOMIC_RELAXED, __HIP_MEMORY_SCOPE_AGENT); }
__device__ __forceinline__ void stflag(unsigned* p, unsigned v){ __hip_atomic_store((GLB_AS unsigned*)p, v, __ATOMIC_RELAXED, __HIP_MEMORY_SCOPE_AGENT); }
template<int SLP> __device__ __forceinline__ void wait_flag(const unsigned* p, unsigned tag){
  int spins=0;
  while(true){ unsigned v=ldflag(p); if(__all(v==tag)) break; __builtin_amdgcn_s_sleep(SLP); if(++spins>(1<<21)) break; }
}
__device__ __forceinline__ unsigned lds_addr(const void* p){ return (unsigned)(size_t)(const LDS_AS void*)p; }
#define WAITV(n) asm volatile("s_waitcnt vmcnt(" #n ")":::"memory")
#define WAITL() asm volatile("s_waitcnt lgkmcnt(0)":::"memory")
#define SBAR() asm volatile("s_barrier":::"memory")
#define PUBM(addr,val) asm volatile("ds_max_u32 %0, %1" :: "v"(addr), "v"(val) : "memory")
#define PUB(addr,val) asm volatile("ds_write_b32 %0, %1" :: "v"(addr), "v"(val) : "memory")
__device__ __forceinline__ s16x4 trread(const char* p){ return __builtin_amdgcn_ds_read_tr16_b64_v4i16((LDS_AS s16x4*)p); }
__device__ __forceinline__ bf16x8 cat44(s16x4 a, s16x4 b){ return __builtin_shufflevector(a,b,0,1,2,3,4,5,6,7); }
__device__ __forceinline__ int wswz(int row){ return ((row>>1)&1) | (((row>>3)&1)<<1); }
__device__ __forceinline__ u32x4 widen(u32x2 a, u32x2 b){
  auto r0=__builtin_amdgcn_permlane16_swap(a[0],b[0],false,false);
  auto r1=__builtin_amdgcn_permlane16_swap(a[1],b[1],false,false);
  return u32x4{r0[0],r1[0],r0[1],r1[1]};
}

constexpr int S=2048, DM=1024;
constexpr int WS=12288, XS=18432, X_OFF=6*WS;
constexpr int CREG=4608, C_OFF=131072;
constexpr int EX_OFF=C_OFF+4*CREG;
constexpr int SYN=EX_OFF+8192;
constexpr int SINK=SYN+256;
constexpr int CAR=SINK+768;
constexpr int ALL_LDS=CAR+4*1280;
constexpr int F_PP=1024, F_O=4096, F_Y=8192;
#define KV0 1
#define KVS 1
#define LPR 2
#define CPR 2
#define GPR 1
#define CGATE 2
#define OSLP 2
#define YSLP 2
#define TPX 16
#define TPW 2
#define TWO 0
#define CVP 1
#define G2P 3
#define OUTC 1
#define ROT 0
#define WTL 2
#define PB2 0
#define PREM 5
#define KTH 2
#define YDEF 0
#define OPOL 1
#define CODEL 260
#define MIX 0
#define LOCA 3
#define XLOC 1
#define PGT 0
#define PGO 0
#define QREM 0
#define EB 0
#define TW0 1
#define ORDM 1
#define NF 7u
#define NPOL 1
#define PD0 0
#define PD1 4
#define HH 0
#define HW 3
#define HKV 4
#define YPOL 1
#define YS2 2
#define OSH 4
#define HSK 3
#define HGR 2
#define KSH 0
#define EPI 0
#define YFL 0
#define CODEX 0
#define YPM 1
#define YPD 4
#define KPOL 0
#define GWD 16
#define HLAST 1

#define N2_ASM \
  "v_mov_b64 v[64:65], 0\n\t" \
  "v_mov_b64 v[66:67], 0\n\t" \
  "v_mov_b64 v[68:69], 0\n\t" \
  "v_mov_b64 v[70:71], 0\n\t" \
  "v_mov_b64 v[72:73], 0\n\t" \
  "v_mov_b64 v[74:75], 0\n\t" \
  "v_mov_b64 v[76:77], 0\n\t" \
  "v_mov_b64 v[78:79], 0\n\t" \
  "v_mov_b64 v[80:81], 0\n\t" \
  "v_mov_b64 v[82:83], 0\n\t" \
  "v_mov_b64 v[84:85], 0\n\t" \
  "v_mov_b64 v[86:87], 0\n\t" \
  "v_mov_b64 v[88:89], 0\n\t" \
  "v_mov_b64 v[90:91], 0\n\t" \
  "v_mov_b64 v[92:93], 0\n\t" \
  "v_mov_b64 v[94:95], 0\n\t" \
  "v_mov_b64 v[96:97], 0\n\t" \
  "v_mov_b64 v[98:99], 0\n\t" \
  "v_mov_b64 v[100:101], 0\n\t" \
  "v_mov_b64 v[102:103], 0\n\t" \
  "v_mov_b64 v[104:105], 0\n\t" \
  "v_mov_b64 v[106:107], 0\n\t" \
  "v_mov_b64 v[108:109], 0\n\t" \
  "v_mov_b64 v[110:111], 0\n\t" \
  "v_mov_b64 v[112:113], 0\n\t" \
  "v_mov_b64 v[114:115], 0\n\t" \
  "v_mov_b64 v[116:117], 0\n\t" \
  "v_mov_b64 v[118:119], 0\n\t" \
  "v_mov_b64 v[120:121], 0\n\t" \
  "v_mov_b64 v[122:123], 0\n\t" \
  "v_mov_b64 v[124:125], 0\n\t" \
  "v_mov_b64 v[126:127], 0\n\t" \
  "v_mov_b64 v[128:129], 0\n\t" \
  "v_mov_b64 v[130:131], 0\n\t" \
  "v_mov_b64 v[132:133], 0\n\t" \
  "v_mov_b64 v[134:135], 0\n\t" \
  "v_mov_b64 v[136:137], 0\n\t" \
  "v_mov_b64 v[138:139], 0\n\t" \
  "v_mov_b64 v[140:141], 0\n\t" \
  "v_mov_b64 v[142:143], 0\n\t" \
  "v_mov_b64 v[144:145], 0\n\t" \
  "v_mov_b64 v[146:147], 0\n\t" \
  "v_mov_b64 v[148:149], 0\n\t" \
  "v_mov_b64 v[150:151], 0\n\t" \
  "v_mov_b64 v[152:153], 0\n\t" \
  "v_mov_b64 v[154:155], 0\n\t" \
  "v_mov_b64 v[156:157], 0\n\t" \
  "v_mov_b64 v[158:159], 0\n\t" \
  "v_mov_b64 v[160:161], 0\n\t" \
  "v_mov_b64 v[162:163], 0\n\t" \
  "v_mov_b64 v[164:165], 0\n\t" \
  "v_mov_b64 v[166:167], 0\n\t" \
  "v_add_u32 v60, 36864, v58\n\t" \
  "v_add_u32 v61, 36864, v59\n\t" \
  "s_mov_b32 %[it], 0\n\t" \
  "s_cmp_lg_u32 %[r], 0\n\t" \
  "s_barrier\n\t" \
  "s_cbranch_scc1 NB_%=\n\t" \
  "ds_read_b64_tr_b16 v[48:49], v60 offset:14336\n\t" \
  "ds_read_b64_tr_b16 v[50:51], v60 offset:14848\n\t" \
  "ds_read_b128 v[0:3], v56 offset:38912\n\t" \
  "ds_read_b64_tr_b16 v[52:53], v60 offset:26624\n\t" \
  "ds_read_b64_tr_b16 v[54:55], v60 offset:27136\n\t" \
  "ds_read_b128 v[16:19], v57 offset:38912\n\t" \
  "ds_read_b128 v[4:7], v56 offset:40960\n\t" \
  "ds_read_b128 v[20:23], v57 offset:40960\n\t" \
  "ds_read_b128 v[8:11], v56 offset:43008\n\t" \
  "ds_read_b128 v[24:27], v57 offset:43008\n\t" \
  "ds_read_b128 v[12:15], v56 offset:45056\n\t" \
  "ds_read_b128 v[28:31], v57 offset:45056\n\t" \
  "ds_read_b64_tr_b16 v[32:33], v60 offset:13312\n\t" \
  "ds_read_b64_tr_b16 v[34:35], v60 offset:13824\n\t" \
  "s_mov_b32 s68, 0x50000\n\t" \
  "s_mov_b32 s69, 0x5550055\n\t" \
  "s_mov_b32 s70, 0xa0000\n\t" \
  "s_mov_b32 s71, 0xaaa00aa\n\t" \
  "s_mov_b32 s72, 0x150001\n\t" \
  "s_mov_b32 s73, 0x15550155\n\t" \
  "s_mov_b32 s74, 0x2a0002\n\t" \
  "s_mov_b32 s75, 0x2aaa02aa\n\t" \
  "s_mov_b32 s76, 0x55505555\n\t" \
  "s_mov_b32 s77, 0x50005500\n\t" \
  "s_mov_b32 s78, 0xaaa0aaaa\n\t" \
  "s_mov_b32 s79, 0xa000aa00\n\t" \
  "s_mov_b32 s80, 0x55405554\n\t" \
  "s_mov_b32 s81, 0x40005400\n\t" \
  "s_mov_b32 s82, 0xaa80aaa8\n\t" \
  "s_mov_b32 s83, 0x8000a800\n\t" \
  "s_mov_b32 s84, 0xaaaaaaaa\n\t" \
  "s_mov_b32 s85, 0xaaaaaaaa\n\t" \
  "s_waitcnt lgkmcnt(0)\n\t" \
  "s_branch N2a_%=\n\t" \
  "N0a_%=:\n\t" \
  "s_waitcnt lgkmcnt(10)\n\t" \
  "v_mfma_f32_16x16x32_bf16 v[128:131], v[0:3], v[32:35], v[128:131]\n\t" \
  "ds_read_b64_tr_b16 v[44:45], v58 offset:13312\n\t" \
  "s_waitcnt lgkmcnt(7)\n\t" \
  "v_mfma_f32_16x16x32_bf16 v[128:131], v[16:19], v[36:39], v[128:131]\n\t" \
  "ds_read_b64_tr_b16 v[46:47], v58 offset:13824\n\t" \
  "v_mfma_f32_16x16x32_bf16 v[136:139], v[4:7], v[32:35], v[136:139]\n\t" \
  "ds_read_b128 v[48:51], v56 offset:0\n\t" \
  "v_mfma_f32_16x16x32_bf16 v[136:139], v[20:23], v[36:39], v[136:139]\n\t" \
  "ds_read_b128 v[52:55], v57 offset:0\n\t" \
  "s_waitcnt lgkmcnt(8)\n\t" \
  "v_mfma_f32_16x16x32_bf16 v[144:147], v[8:11], v[32:35], v[144:147]\n\t" \
  "v_mfma_f32_16x16x32_bf16 v[144:147], v[24:27], v[36:39], v[144:147]\n\t" \
  "s_waitcnt lgkmcnt(4)\n\t" \
  "v_mfma_f32_16x16x32_bf16 v[152:155], v[12:15], v[32:35], v[152:155]\n\t" \
  "v_mfma_f32_16x16x32_bf16 v[152:155], v[28:31], v[36:39], v[152:155]\n\t" \
  "ds_read_b64_tr_b16 v[32:33], v59 offset:1024\n\t" \
  "v_mfma_f32_16x16x32_bf16 v[96:99], v[40:43], v[0:3], v[96:99]\n\t" \
  "ds_read_b64_tr_b16 v[34:35], v59 offset:1536\n\t" \
  "s_waitcnt lgkmcnt(4)\n\t" \
  "v_mfma_f32_16x16x32_bf16 v[96:99], v[44:47], v[16:19], v[96:99]\n\t" \
  "ds_read_b64_tr_b16 v[36:37], v59 offset:13312\n\t" \
  "v_mfma_f32_16x16x32_bf16 v[104:107], v[40:43], v[4:7], v[104:107]\n\t" \
  "v_mfma_f32_16x16x32_bf16 v[104:107], v[44:47], v[20:23], v[104:107]\n\t" \
  "ds_read_b64_tr_b16 v[38:39], v59 offset:13824\n\t" \
  "v_mfma_f32_16x16x32_bf16 v[112:115], v[40:43], v[8:11], v[112:115]\n\t" \
  "v_mfma_f32_16x16x32_bf16 v[112:115], v[44:47], v[24:27], v[112:115]\n\t" \
  "v_mfma_f32_16x16x32_bf16 v[120:123], v[40:43], v[12:15], v[120:123]\n\t" \
  "v_mfma_f32_16x16x32_bf16 v[120:123], v[44:47], v[28:31], v[120:123]\n\t" \
  "s_waitcnt lgkmcnt(4)\n\t" \
  "v_mfma_f32_16x16x32_bf16 v[160:163], v[40:43], v[48:51], v[160:163]\n\t" \
  "v_mfma_f32_16x16x32_bf16 v[160:163], v[44:47], v[52:55], v[160:163]\n\t" \
  "ds_read_b64_tr_b16 v[40:41], v59 offset:2048\n\t" \
  "s_waitcnt lgkmcnt(1)\n\t" \
  "v_mfma_f32_16x16x32_bf16 v[164:167], v[32:35], v[48:51], v[164:167]\n\t" \
  "ds_read_b64_tr_b16 v[42:43], v59 offset:2560\n\t" \
  "v_mfma_f32_16x16x32_bf16 v[164:167], v[36:39], v[52:55], v[164:167]\n\t" \
  "ds_read_b64_tr_b16 v[44:45], v59 offset:14336\n\t" \
  "v_mfma_f32_16x16x32_bf16 v[100:103], v[32:35], v[0:3], v[100:103]\n\t" \
  "ds_read_b64_tr_b16 v[46:47], v59 offset:14848\n\t" \
  "v_mfma_f32_16x16x32_bf16 v[100:103], v[36:39], v[16:19], v[100:103]\n\t" \
  "ds_read_b64_tr_b16 v[48:49], v58 offset:0\n\t" \
  "v_mfma_f32_16x16x32_bf16 v[108:111], v[32:35], v[4:7], v[108:111]\n\t" \
  "ds_read_b64_tr_b16 v[50:51], v58 offset:512\n\t" \
  "v_mfma_f32_16x16x32_bf16 v[108:111], v[36:39], v[20:23], v[108:111]\n\t" \
  "ds_read_b64_tr_b16 v[52:53], v58 offset:12288\n\t" \
  "v_mfma_f32_16x16x32_bf16 v[116:119], v[32:35], v[8:11], v[116:119]\n\t" \
  "ds_read_b64_tr_b16 v[54:55], v58 offset:12800\n\t" \
  "v_mfma_f32_16x16x32_bf16 v[116:119], v[36:39], v[24:27], v[116:119]\n\t" \
  "v_mfma_f32_16x16x32_bf16 v[124:127], v[32:35], v[12:15], v[124:127]\n\t" \
  "ds_read_b64_tr_b16 v[32:33], v59 offset:0\n\t" \
  "v_mfma_f32_16x16x32_bf16 v[124:127], v[36:39], v[28:31], v[124:127]\n\t" \
  "ds_read_b64_tr_b16 v[34:35], v59 offset:512\n\t" \
  "s_waitcnt lgkmcnt(6)\n\t" \
  "v_mfma_f32_16x16x32_bf16 v[132:135], v[0:3], v[40:43], v[132:135]\n\t" \
  "ds_read_b64_tr_b16 v[36:37], v59 offset:12288\n\t" \
  "v_mfma_f32_16x16x32_bf16 v[132:135], v[16:19], v[44:47], v[132:135]\n\t" \
  "ds_read_b64_tr_b16 v[38:39], v59 offset:12800\n\t" \
  "v_mfma_f32_16x16x32_bf16 v[140:143], v[4:7], v[40:43], v[140:143]\n\t" \
  "v_mfma_f32_16x16x32_bf16 v[140:143], v[20:23], v[44:47], v[140:143]\n\t" \
  "v_mfma_f32_16x16x32_bf16 v[148:151], v[8:11], v[40:43], v[148:151]\n\t" \
  "v_mfma_f32_16x16x32_bf16 v[148:151], v[24:27], v[44:47], v[148:151]\n\t" \
  "v_mfma_f32_16x16x32_bf16 v[156:159], v[12:15], v[40:43], v[156:159]\n\t" \
  "v_mfma_f32_16x16x32_bf16 v[156:159], v[28:31], v[44:47], v[156:159]\n\t" \
  "s_waitcnt lgkmcnt(0)\n\t" \
  "s_barrier\n\t" \
  "v_mfma_f32_16x16x32_bf16 v[64:67], v[48:51], v[0:3], v[64:67]\n\t" \
  "v_mfma_f32_16x16x32_bf16 v[64:67], v[52:55], v[16:19], v[64:67]\n\t" \
  "ds_read_b64_tr_b16 v[40:41], v58 offset:26624\n\t" \
  "v_mfma_f32_16x16x32_bf16 v[68:71], v[32:35], v[0:3], v[68:71]\n\t" \
  "ds_read_b128 v[0:3], v56 offset:20480\n\t" \
  "v_mfma_f32_16x16x32_bf16 v[68:71], v[36:39], v[16:19], v[68:71]\n\t" \
  "ds_read_b128 v[16:19], v57 offset:20480\n\t" \
  "v_mfma_f32_16x16x32_bf16 v[72:75], v[48:51], v[4:7], v[72:75]\n\t" \
  "ds_read_b64_tr_b16 v[42:43], v58 offset:27136\n\t" \
  "v_mfma_f32_16x16x32_bf16 v[72:75], v[52:55], v[20:23], v[72:75]\n\t" \
  "ds_read_b64_tr_b16 v[44:45], v60 offset:2048\n\t" \
  "v_mfma_f32_16x16x32_bf16 v[76:79], v[32:35], v[4:7], v[76:79]\n\t" \
  "ds_read_b128 v[4:7], v56 offset:22528\n\t" \
  "v_mfma_f32_16x16x32_bf16 v[76:79], v[36:39], v[20:23], v[76:79]\n\t" \
  "ds_read_b128 v[20:23], v57 offset:22528\n\t" \
  "v_mfma_f32_16x16x32_bf16 v[80:83], v[48:51], v[8:11], v[80:83]\n\t" \
  "ds_read_b64_tr_b16 v[46:47], v60 offset:2560\n\t" \
  "v_mfma_f32_16x16x32_bf16 v[80:83], v[52:55], v[24:27], v[80:83]\n\t" \
  "v_mfma_f32_16x16x32_bf16 v[84:87], v[32:35], v[8:11], v[84:87]\n\t" \
  "ds_read_b128 v[8:11], v56 offset:24576\n\t" \
  "v_mfma_f32_16x16x32_bf16 v[84:87], v[36:39], v[24:27], v[84:87]\n\t" \
  "ds_read_b128 v[24:27], v57 offset:24576\n\t" \
  "v_mfma_f32_16x16x32_bf16 v[88:91], v[48:51], v[12:15], v[88:91]\n\t" \
  "ds_read_b64_tr_b16 v[48:49], v58 offset:25600\n\t" \
  "v_mfma_f32_16x16x32_bf16 v[88:91], v[52:55], v[28:31], v[88:91]\n\t" \
  "ds_read_b64_tr_b16 v[50:51], v58 offset:26112\n\t" \
  "v_mfma_f32_16x16x32_bf16 v[92:95], v[32:35], v[12:15], v[92:95]\n\t" \
  "ds_read_b128 v[12:15], v56 offset:26624\n\t" \
  "v_mfma_f32_16x16x32_bf16 v[92:95], v[36:39], v[28:31], v[92:95]\n\t" \
  "ds_read_b128 v[28:31], v57 offset:26624\n\t" \
  "N1a_%=:\n\t" \
  "s_waitcnt lgkmcnt(10)\n\t" \
  "v_mfma_f32_16x16x32_bf16 v[128:131], v[0:3], v[40:43], v[128:131]\n\t" \
  "ds_read_b64_tr_b16 v[52:53], v60 offset:1024\n\t" \
  "s_waitcnt lgkmcnt(7)\n\t" \
  "v_mfma_f32_16x16x32_bf16 v[128:131], v[16:19], v[44:47], v[128:131]\n\t" \
  "ds_read_b64_tr_b16 v[54:55], v60 offset:1536\n\t" \
  "v_mfma_f32_16x16x32_bf16 v[136:139], v[4:7], v[40:43], v[136:139]\n\t" \
  "ds_read_b128 v[32:35], v56 offset:18432\n\t" \
  "v_mfma_f32_16x16x32_bf16 v[136:139], v[20:23], v[44:47], v[136:139]\n\t" \
  "ds_read_b128 v[36:39], v57 offset:18432\n\t" \
  "s_waitcnt lgkmcnt(8)\n\t" \
  "v_mfma_f32_16x16x32_bf16 v[144:147], v[8:11], v[40:43], v[144:147]\n\t" \
  "v_mfma_f32_16x16x32_bf16 v[144:147], v[24:27], v[44:47], v[144:147]\n\t" \
  "s_waitcnt lgkmcnt(4)\n\t" \
  "v_mfma_f32_16x16x32_bf16 v[152:155], v[12:15], v[40:43], v[152:155]\n\t" \
  "v_mfma_f32_16x16x32_bf16 v[152:155], v[28:31], v[44:47], v[152:155]\n\t" \
  "ds_read_b64_tr_b16 v[40:41], v59 offset:25600\n\t" \
  "v_mfma_f32_16x16x32_bf16 v[96:99], v[48:51], v[0:3], v[96:99]\n\t" \
  "ds_read_b64_tr_b16 v[42:43], v59 offset:26112\n\t" \
  "s_waitcnt lgkmcnt(4)\n\t" \
  "v_mfma_f32_16x16x32_bf16 v[96:99], v[52:55], v[16:19], v[96:99]\n\t" \
  "ds_read_b64_tr_b16 v[44:45], v61 offset:1024\n\t" \
  "v_mfma_f32_16x16x32_bf16 v[104:107], v[48:51], v[4:7], v[104:107]\n\t" \
  "v_mfma_f32_16x16x32_bf16 v[104:107], v[52:55], v[20:23], v[104:107]\n\t" \
  "ds_read_b64_tr_b16 v[46:47], v61 offset:1536\n\t" \
  "v_mfma_f32_16x16x32_bf16 v[112:115], v[48:51], v[8:11], v[112:115]\n\t" \
  "v_mfma_f32_16x16x32_bf16 v[112:115], v[52:55], v[24:27], v[112:115]\n\t" \
  "v_mfma_f32_16x16x32_bf16 v[120:123], v[48:51], v[12:15], v[120:123]\n\t" \
  "v_mfma_f32_16x16x32_bf16 v[120:123], v[52:55], v[28:31], v[120:123]\n\t" \
  "s_waitcnt lgkmcnt(4)\n\t" \
  "v_mfma_f32_16x16x32_bf16 v[160:163], v[48:51], v[32:35], v[160:163]\n\t" \
  "v_mfma_f32_16x16x32_bf16 v[160:163], v[52:55], v[36:39], v[160:163]\n\t" \
  "ds_read_b64_tr_b16 v[48:49], v59 offset:26624\n\t" \
  "s_waitcnt lgkmcnt(1)\n\t" \
  "v_mfma_f32_16x16x32_bf16 v[164:167], v[40:43], v[32:35], v[164:167]\n\t" \
  "ds_read_b64_tr_b16 v[50:51], v59 offset:27136\n\t" \
  "v_mfma_f32_16x16x32_bf16 v[164:167], v[44:47], v[36:39], v[164:167]\n\t" \
  "ds_read_b64_tr_b16 v[52:53], v61 offset:2048\n\t" \
  "v_mfma_f32_16x16x32_bf16 v[100:103], v[40:43], v[0:3], v[100:103]\n\t" \
  "ds_read_b64_tr_b16 v[54:55], v61 offset:2560\n\t" \
  "v_mfma_f32_16x16x32_bf16 v[100:103], v[44:47], v[16:19], v[100:103]\n\t" \
  "ds_read_b64_tr_b16 v[32:33], v58 offset:24576\n\t" \
  "v_mfma_f32_16x16x32_bf16 v[108:111], v[40:43], v[4:7], v[108:111]\n\t" \
  "ds_read_b64_tr_b16 v[34:35], v58 offset:25088\n\t" \
  "v_mfma_f32_16x16x32_bf16 v[108:111], v[44:47], v[20:23], v[108:111]\n\t" \
  "ds_read_b64_tr_b16 v[36:37], v60 offset:0\n\t" \
  "v_mfma_f32_16x16x32_bf16 v[116:119], v[40:43], v[8:11], v[116:119]\n\t" \
  "ds_read_b64_tr_b16 v[38:39], v60 offset:512\n\t" \
  "v_mfma_f32_16x16x32_bf16 v[116:119], v[44:47], v[24:27], v[116:119]\n\t" \
  "v_mfma_f32_16x16x32_bf16 v[124:127], v[40:43], v[12:15], v[124:127]\n\t" \
  "ds_read_b64_tr_b16 v[40:41], v59 offset:24576\n\t" \
  "v_mfma_f32_16x16x32_bf16 v[124:127], v[44:47], v[28:31], v[124:127]\n\t" \
  "ds_read_b64_tr_b16 v[42:43], v59 offset:25088\n\t" \
  "s_waitcnt lgkmcnt(6)\n\t" \
  "v_mfma_f32_16x16x32_bf16 v[132:135], v[0:3], v[48:51], v[132:135]\n\t" \
  "ds_read_b64_tr_b16 v[44:45], v61 offset:0\n\t" \
  "v_mfma_f32_16x16x32_bf16 v[132:135], v[16:19], v[52:55], v[132:135]\n\t" \
  "ds_read_b64_tr_b16 v[46:47], v61 offset:512\n\t" \
  "v_mfma_f32_16x16x32_bf16 v[140:143], v[4:7], v[48:51], v[140:143]\n\t" \
  "v_mfma_f32_16x16x32_bf16 v[140:143], v[20:23], v[52:55], v[140:143]\n\t" \
  "v_mfma_f32_16x16x32_bf16 v[148:151], v[8:11], v[48:51], v[148:151]\n\t" \
  "v_mfma_f32_16x16x32_bf16 v[148:151], v[24:27], v[52:55], v[148:151]\n\t" \
  "v_mfma_f32_16x16x32_bf16 v[156:159], v[12:15], v[48:51], v[156:159]\n\t" \
  "v_mfma_f32_16x16x32_bf16 v[156:159], v[28:31], v[52:55], v[156:159]\n\t" \
  "s_waitcnt lgkmcnt(0)\n\t" \
  "s_barrier\n\t" \
  "v_mfma_f32_16x16x32_bf16 v[64:67], v[32:35], v[0:3], v[64:67]\n\t" \
  "v_mfma_f32_16x16x32_bf16 v[64:67], v[36:39], v[16:19], v[64:67]\n\t" \
  "ds_read_b64_tr_b16 v[48:49], v60 offset:14336\n\t" \
  "v_mfma_f32_16x16x32_bf16 v[68:71], v[40:43], v[0:3], v[68:71]\n\t" \
  "ds_read_b128 v[0:3], v56 offset:38912\n\t" \
  "v_mfma_f32_16x16x32_bf16 v[68:71], v[44:47], v[16:19], v[68:71]\n\t" \
  "ds_read_b128 v[16:19], v57 offset:38912\n\t" \
  "v_mfma_f32_16x16x32_bf16 v[72:75], v[32:35], v[4:7], v[72:75]\n\t" \
  "ds_read_b64_tr_b16 v[50:51], v60 offset:14848\n\t" \
  "v_mfma_f32_16x16x32_bf16 v[72:75], v[36:39], v[20:23], v[72:75]\n\t" \
  "ds_read_b64_tr_b16 v[52:53], v60 offset:26624\n\t" \
  "v_mfma_f32_16x16x32_bf16 v[76:79], v[40:43], v[4:7], v[76:79]\n\t" \
  "ds_read_b128 v[4:7], v56 offset:40960\n\t" \
  "v_mfma_f32_16x16x32_bf16 v[76:79], v[44:47], v[20:23], v[76:79]\n\t" \
  "ds_read_b128 v[20:23], v57 offset:40960\n\t" \
  "v_mfma_f32_16x16x32_bf16 v[80:83], v[32:35], v[8:11], v[80:83]\n\t" \
  "ds_read_b64_tr_b16 v[54:55], v60 offset:27136\n\t" \
  "v_mfma_f32_16x16x32_bf16 v[80:83], v[36:39], v[24:27], v[80:83]\n\t" \
  "v_mfma_f32_16x16x32_bf16 v[84:87], v[40:43], v[8:11], v[84:87]\n\t" \
  "ds_read_b128 v[8:11], v56 offset:43008\n\t" \
  "v_mfma_f32_16x16x32_bf16 v[84:87], v[44:47], v[24:27], v[84:87]\n\t" \
  "ds_read_b128 v[24:27], v57 offset:43008\n\t" \
  "v_mfma_f32_16x16x32_bf16 v[88:91], v[32:35], v[12:15], v[88:91]\n\t" \
  "ds_read_b64_tr_b16 v[32:33], v60 offset:13312\n\t" \
  "v_mfma_f32_16x16x32_bf16 v[88:91], v[36:39], v[28:31], v[88:91]\n\t" \
  "ds_read_b64_tr_b16 v[34:35], v60 offset:13824\n\t" \
  "v_mfma_f32_16x16x32_bf16 v[92:95], v[40:43], v[12:15], v[92:95]\n\t" \
  "ds_read_b128 v[12:15], v56 offset:45056\n\t" \
  "v_mfma_f32_16x16x32_bf16 v[92:95], v[44:47], v[28:31], v[92:95]\n\t" \
  "ds_read_b128 v[28:31], v57 offset:45056\n\t" \
  "N2a_%=:\n\t" \
  "s_waitcnt lgkmcnt(10)\n\t" \
  "v_mfma_f32_16x16x32_bf16 v[128:131], v[0:3], v[48:51], v[128:131]\n\t" \
  "ds_read_b64_tr_b16 v[36:37], v60 offset:25600\n\t" \
  "s_waitcnt lgkmcnt(7)\n\t" \
  "v_mfma_f32_16x16x32_bf16 v[128:131], v[16:19], v[52:55], v[128:131]\n\t" \
  "ds_read_b64_tr_b16 v[38:39], v60 offset:26112\n\t" \
  "v_mfma_f32_16x16x32_bf16 v[136:139], v[4:7], v[48:51], v[136:139]\n\t" \
  "ds_read_b128 v[40:43], v56 offset:36864\n\t" \
  "v_mfma_f32_16x16x32_bf16 v[136:139], v[20:23], v[52:55], v[136:139]\n\t" \
  "ds_read_b128 v[44:47], v57 offset:36864\n\t" \
  "s_waitcnt lgkmcnt(8)\n\t" \
  "v_mfma_f32_16x16x32_bf16 v[144:147], v[8:11], v[48:51], v[144:147]\n\t" \
  "v_mfma_f32_16x16x32_bf16 v[144:147], v[24:27], v[52:55], v[144:147]\n\t" \
  "s_waitcnt lgkmcnt(4)\n\t" \
  "v_mfma_f32_16x16x32_bf16 v[152:155], v[12:15], v[48:51], v[152:155]\n\t" \
  "v_mfma_f32_16x16x32_bf16 v[152:155], v[28:31], v[52:55], v[152:155]\n\t" \
  "ds_read_b64_tr_b16 v[48:49], v61 offset:13312\n\t" \
  "v_mfma_f32_16x16x32_bf16 v[96:99], v[32:35], v[0:3], v[96:99]\n\t" \
  "ds_read_b64_tr_b16 v[50:51], v61 offset:13824\n\t" \
  "s_waitcnt lgkmcnt(4)\n\t" \
  "v_mfma_f32_16x16x32_bf16 v[96:99], v[36:39], v[16:19], v[96:99]\n\t" \
  "ds_read_b64_tr_b16 v[52:53], v61 offset:25600\n\t" \
  "v_mfma_f32_16x16x32_bf16 v[104:107], v[32:35], v[4:7], v[104:107]\n\t" \
  "v_mfma_f32_16x16x32_bf16 v[104:107], v[36:39], v[20:23], v[104:107]\n\t" \
  "ds_read_b64_tr_b16 v[54:55], v61 offset:26112\n\t" \
  "v_mfma_f32_16x16x32_bf16 v[112:115], v[32:35], v[8:11], v[112:115]\n\t" \
  "v_mfma_f32_16x16x32_bf16 v[112:115], v[36:39], v[24:27], v[112:115]\n\t" \
  "v_mfma_f32_16x16x32_bf16 v[120:123], v[32:35], v[12:15], v[120:123]\n\t" \
  "v_mfma_f32_16x16x32_bf16 v[120:123], v[36:39], v[28:31], v[120:123]\n\t" \
  "s_waitcnt lgkmcnt(4)\n\t" \
  "v_mfma_f32_16x16x32_bf16 v[160:163], v[32:35], v[40:43], v[160:163]\n\t" \
  "v_mfma_f32_16x16x32_bf16 v[160:163], v[36:39], v[44:47], v[160:163]\n\t" \
  "ds_read_b64_tr_b16 v[32:33], v61 offset:14336\n\t" \
  "s_waitcnt lgkmcnt(1)\n\t" \
  "v_mfma_f32_16x16x32_bf16 v[164:167], v[48:51], v[40:43], v[164:167]\n\t" \
  "ds_read_b64_tr_b16 v[34:35], v61 offset:14848\n\t" \
  "v_mfma_f32_16x16x32_bf16 v[164:167], v[52:55], v[44:47], v[164:167]\n\t" \
  "ds_read_b64_tr_b16 v[36:37], v61 offset:26624\n\t" \
  "v_mfma_f32_16x16x32_bf16 v[100:103], v[48:51], v[0:3], v[100:103]\n\t" \
  "ds_read_b64_tr_b16 v[38:39], v61 offset:27136\n\t" \
  "v_mfma_f32_16x16x32_bf16 v[100:103], v[52:55], v[16:19], v[100:103]\n\t" \
  "ds_read_b64_tr_b16 v[40:41], v60 offset:12288\n\t" \
  "v_mfma_f32_16x16x32_bf16 v[108:111], v[48:51], v[4:7], v[108:111]\n\t" \
  "ds_read_b64_tr_b16 v[42:43], v60 offset:12800\n\t" \
  "v_mfma_f32_16x16x32_bf16 v[108:111], v[52:55], v[20:23], v[108:111]\n\t" \
  "ds_read_b64_tr_b16 v[44:45], v60 offset:24576\n\t" \
  "v_mfma_f32_16x16x32_bf16 v[116:119], v[48:51], v[8:11], v[116:119]\n\t" \
  "ds_read_b64_tr_b16 v[46:47], v60 offset:25088\n\t" \
  "v_mfma_f32_16x16x32_bf16 v[116:119], v[52:55], v[24:27], v[116:119]\n\t" \
  "v_mfma_f32_16x16x32_bf16 v[124:127], v[48:51], v[12:15], v[124:127]\n\t" \
  "ds_read_b64_tr_b16 v[48:49], v61 offset:12288\n\t" \
  "v_mfma_f32_16x16x32_bf16 v[124:127], v[52:55], v[28:31], v[124:127]\n\t" \
  "ds_read_b64_tr_b16 v[50:51], v61 offset:12800\n\t" \
  "s_waitcnt lgkmcnt(6)\n\t" \
  "v_mfma_f32_16x16x32_bf16 v[132:135], v[0:3], v[32:35], v[132:135]\n\t" \
  "ds_read_b64_tr_b16 v[52:53], v61 offset:24576\n\t" \
  "v_mfma_f32_16x16x32_bf16 v[132:135], v[16:19], v[36:39], v[132:135]\n\t" \
  "ds_read_b64_tr_b16 v[54:55], v61 offset:25088\n\t" \
  "v_mfma_f32_16x16x32_bf16 v[140:143], v[4:7], v[32:35], v[140:143]\n\t" \
  "v_mfma_f32_16x16x32_bf16 v[140:143], v[20:23], v[36:39], v[140:143]\n\t" \
  "s_addk_i32 %[it], 1\n\t" \
  "v_mfma_f32_16x16x32_bf16 v[148:151], v[8:11], v[32:35], v[148:151]\n\t" \
  "s_cmp_lt_u32 %[it], 6\n\t" \
  "v_mfma_f32_16x16x32_bf16 v[148:151], v[24:27], v[36:39], v[148:151]\n\t" \
  "v_mfma_f32_16x16x32_bf16 v[156:159], v[12:15], v[32:35], v[156:159]\n\t" \
  "v_mfma_f32_16x16x32_bf16 v[156:159], v[28:31], v[36:39], v[156:159]\n\t" \
  "s_waitcnt lgkmcnt(0)\n\t" \
  "s_barrier\n\t" \
  "s_cbranch_scc0 NTa_%=\n\t" \
  "v_mfma_f32_16x16x32_bf16 v[64:67], v[40:43], v[0:3], v[64:67]\n\t" \
  "v_mfma_f32_16x16x32_bf16 v[64:67], v[44:47], v[16:19], v[64:67]\n\t" \
  "ds_read_b64_tr_b16 v[32:33], v58 offset:2048\n\t" \
  "v_mfma_f32_16x16x32_bf16 v[68:71], v[48:51], v[0:3], v[68:71]\n\t" \
  "ds_read_b128 v[0:3], v56 offset:2048\n\t" \
  "v_mfma_f32_16x16x32_bf16 v[68:71], v[52:55], v[16:19], v[68:71]\n\t" \
  "ds_read_b128 v[16:19], v57 offset:2048\n\t" \
  "v_mfma_f32_16x16x32_bf16 v[72:75], v[40:43], v[4:7], v[72:75]\n\t" \
  "ds_read_b64_tr_b16 v[34:35], v58 offset:2560\n\t" \
  "v_mfma_f32_16x16x32_bf16 v[72:75], v[44:47], v[20:23], v[72:75]\n\t" \
  "ds_read_b64_tr_b16 v[36:37], v58 offset:14336\n\t" \
  "v_mfma_f32_16x16x32_bf16 v[76:79], v[48:51], v[4:7], v[76:79]\n\t" \
  "ds_read_b128 v[4:7], v56 offset:4096\n\t" \
  "v_mfma_f32_16x16x32_bf16 v[76:79], v[52:55], v[20:23], v[76:79]\n\t" \
  "ds_read_b128 v[20:23], v57 offset:4096\n\t" \
  "v_mfma_f32_16x16x32_bf16 v[80:83], v[40:43], v[8:11], v[80:83]\n\t" \
  "ds_read_b64_tr_b16 v[38:39], v58 offset:14848\n\t" \
  "v_mfma_f32_16x16x32_bf16 v[80:83], v[44:47], v[24:27], v[80:83]\n\t" \
  "v_mfma_f32_16x16x32_bf16 v[84:87], v[48:51], v[8:11], v[84:87]\n\t" \
  "ds_read_b128 v[8:11], v56 offset:6144\n\t" \
  "v_mfma_f32_16x16x32_bf16 v[84:87], v[52:55], v[24:27], v[84:87]\n\t" \
  "ds_read_b128 v[24:27], v57 offset:6144\n\t" \
  "v_mfma_f32_16x16x32_bf16 v[88:91], v[40:43], v[12:15], v[88:91]\n\t" \
  "ds_read_b64_tr_b16 v[40:41], v58 offset:1024\n\t" \
  "v_mfma_f32_16x16x32_bf16 v[88:91], v[44:47], v[28:31], v[88:91]\n\t" \
  "ds_read_b64_tr_b16 v[42:43], v58 offset:1536\n\t" \
  "v_mfma_f32_16x16x32_bf16 v[92:95], v[48:51], v[12:15], v[92:95]\n\t" \
  "ds_read_b128 v[12:15], v56 offset:8192\n\t" \
  "v_mfma_f32_16x16x32_bf16 v[92:95], v[52:55], v[28:31], v[92:95]\n\t" \
  "ds_read_b128 v[28:31], v57 offset:8192\n\t" \
  "s_branch N0a_%=\n\t" \
  "NTa_%=:\n\t" \
  "s_setprio 3\n\t" \
  "v_mfma_f32_16x16x32_bf16 v[88:91], v[40:43], v[12:15], v[88:91]\n\t" \
  "v_cvt_pk_bf16_f32 v120, v120, v121\n\t" \
  "v_cvt_pk_bf16_f32 v121, v122, v123\n\t" \
  "v_mfma_f32_16x16x32_bf16 v[88:91], v[44:47], v[28:31], v[88:91]\n\t" \
  "v_cvt_pk_bf16_f32 v122, v124, v125\n\t" \
  "v_cvt_pk_bf16_f32 v123, v126, v127\n\t" \
  "v_mfma_f32_16x16x32_bf16 v[92:95], v[48:51], v[12:15], v[92:95]\n\t" \
  "v_mov_b32 v57, %[fa]\n\t" \
  "v_mov_b32 v58, 1\n\t" \
  "v_mfma_f32_16x16x32_bf16 v[92:95], v[52:55], v[28:31], v[92:95]\n\t" \
  "ds_write_b128 v63, v[120:123]\n\t" \
  "v_cvt_pk_bf16_f32 v38, v156, v157\n\t" \
  "v_mfma_f32_16x16x32_bf16 v[80:83], v[40:43], v[8:11], v[80:83]\n\t" \
  "v_cvt_pk_bf16_f32 v39, v158, v159\n\t" \
  "v_cvt_pk_bf16_f32 v28, v152, v153\n\t" \
  "v_mfma_f32_16x16x32_bf16 v[80:83], v[44:47], v[24:27], v[80:83]\n\t" \
  "v_cvt_pk_bf16_f32 v29, v154, v155\n\t" \
  "v_mov_b32 v60, 0xff61b1e6\n\t" \
  "v_mfma_f32_16x16x32_bf16 v[84:87], v[48:51], v[8:11], v[84:87]\n\t" \
  "ds_write2_b64 v63, v[28:29], v[38:39] offset0:128 offset1:129\n\t" \
  "s_mov_b64 exec, 1\n\t" \
  "ds_write_b32 v57, v58\n\t" \
  "s_mov_b64 exec, -1\n\t" \
  "v_mfma_f32_16x16x32_bf16 v[84:87], v[52:55], v[24:27], v[84:87]\n\t" \
  "v_cvt_pk_bf16_f32 v160, v160, v161\n\t" \
  "v_cvt_pk_bf16_f32 v161, v162, v163\n\t" \
  "v_mfma_f32_16x16x32_bf16 v[72:75], v[40:43], v[4:7], v[72:75]\n\t" \
  "v_cvt_pk_bf16_f32 v162, v164, v165\n\t" \
  "v_cvt_pk_bf16_f32 v163, v166, v167\n\t" \
  "v_mfma_f32_16x16x32_bf16 v[72:75], v[44:47], v[20:23], v[72:75]\n\t" \
  "v_cvt_pk_bf16_f32 v112, v112, v113\n\t" \
  "v_cvt_pk_bf16_f32 v113, v114, v115\n\t" \
  "v_mfma_f32_16x16x32_bf16 v[76:79], v[48:51], v[4:7], v[76:79]\n\t" \
  "v_cvt_pk_bf16_f32 v114, v116, v117\n\t" \
  "v_cvt_pk_bf16_f32 v115, v118, v119\n\t" \
  "v_mfma_f32_16x16x32_bf16 v[76:79], v[52:55], v[20:23], v[76:79]\n\t" \
  "v_cndmask_b32_e64 v12, v60, 0, s[76:77]\n\t" \
  "v_cndmask_b32_e64 v13, v60, 0, s[78:79]\n\t" \
  "v_mfma_f32_16x16x32_bf16 v[64:67], v[40:43], v[0:3], v[64:67]\n\t" \
  "v_cndmask_b32_e64 v14, v60, 0, s[80:81]\n\t" \
  "v_cndmask_b32_e64 v15, v60, 0, s[82:83]\n\t" \
  "v_mfma_f32_16x16x32_bf16 v[64:67], v[44:47], v[16:19], v[64:67]\n\t" \
  "v_cndmask_b32_e64 v56, v60, 0, s[68:69]\n\t" \
  "v_cndmask_b32_e64 v57, v60, 0, s[70:71]\n\t" \
  "v_mfma_f32_16x16x32_bf16 v[68:71], v[48:51], v[0:3], v[68:71]\n\t" \
  "v_cndmask_b32_e64 v58, v60, 0, s[72:73]\n\t" \
  "v_cndmask_b32_e64 v59, v60, 0, s[74:75]\n\t" \
  "v_mfma_f32_16x16x32_bf16 v[68:71], v[52:55], v[16:19], v[68:71]\n\t" \
  "s_branch NX_%=\n\t" \
  "NB_%=:\n\t" \
  "ds_read_b64_tr_b16 v[48:49], v60 offset:13312\n\t" \
  "ds_read_b64_tr_b16 v[50:51], v60 offset:13824\n\t" \
  "ds_read_b128 v[0:3], v56 offset:47104\n\t" \
  "ds_read_b64_tr_b16 v[52:53], v60 offset:25600\n\t" \
  "ds_read_b64_tr_b16 v[54:55], v60 offset:26112\n\t" \
  "ds_read_b128 v[16:19], v57 offset:47104\n\t" \
  "ds_read_b128 v[4:7], v56 offset:49152\n\t" \
  "ds_read_b128 v[20:23], v57 offset:49152\n\t" \
  "ds_read_b128 v[8:11], v56 offset:51200\n\t" \
  "ds_read_b128 v[24:27], v57 offset:51200\n\t" \
  "ds_read_b128 v[12:15], v56 offset:53248\n\t" \
  "ds_read_b128 v[28:31], v57 offset:53248\n\t" \
  "ds_read_b64_tr_b16 v[32:33], v60 offset:14336\n\t" \
  "ds_read_b64_tr_b16 v[34:35], v60 offset:14848\n\t" \
  "s_mov_b32 s68, 0x50000\n\t" \
  "s_mov_b32 s69, 0x5550055\n\t" \
  "s_mov_b32 s70, 0xa0000\n\t" \
  "s_mov_b32 s71, 0xaaa00aa\n\t" \
  "s_mov_b32 s72, 0x150001\n\t" \
  "s_mov_b32 s73, 0x15550155\n\t" \
  "s_mov_b32 s74, 0x2a0002\n\t" \
  "s_mov_b32 s75, 0x2aaa02aa\n\t" \
  "s_mov_b32 s76, 0x55505555\n\t" \
  "s_mov_b32 s77, 0x50005500\n\t" \
  "s_mov_b32 s78, 0xaaa0aaaa\n\t" \
  "s_mov_b32 s79, 0xa000aa00\n\t" \
  "s_mov_b32 s80, 0x55405554\n\t" \
  "s_mov_b32 s81, 0x40005400\n\t" \
  "s_mov_b32 s82, 0xaa80aaa8\n\t" \
  "s_mov_b32 s83, 0x8000a800\n\t" \
  "s_mov_b32 s84, 0xaaaaaaaa\n\t" \
  "s_mov_b32 s85, 0xaaaaaaaa\n\t" \
  "s_waitcnt lgkmcnt(0)\n\t" \
  "s_branch N2b_%=\n\t" \
  "N0b_%=:\n\t" \
  "s_waitcnt lgkmcnt(10)\n\t" \
  "v_mfma_f32_16x16x32_bf16 v[96:99], v[32:35], v[0:3], v[96:99]\n\t" \
  "ds_read_b64_tr_b16 v[44:45], v58 offset:14336\n\t" \
  "s_waitcnt lgkmcnt(7)\n\t" \
  "v_mfma_f32_16x16x32_bf16 v[96:99], v[36:39], v[16:19], v[96:99]\n\t" \
  "ds_read_b64_tr_b16 v[46:47], v58 offset:14848\n\t" \
  "v_mfma_f32_16x16x32_bf16 v[104:107], v[32:35], v[4:7], v[104:107]\n\t" \
  "ds_read_b128 v[48:51], v56 offset:0\n\t" \
  "v_mfma_f32_16x16x32_bf16 v[104:107], v[36:39], v[20:23], v[104:107]\n\t" \
  "ds_read_b128 v[52:55], v57 offset:0\n\t" \
  "s_waitcnt lgkmcnt(8)\n\t" \
  "v_mfma_f32_16x16x32_bf16 v[112:115], v[32:35], v[8:11], v[112:115]\n\t" \
  "v_mfma_f32_16x16x32_bf16 v[112:115], v[36:39], v[24:27], v[112:115]\n\t" \
  "s_waitcnt lgkmcnt(4)\n\t" \
  "v_mfma_f32_16x16x32_bf16 v[120:123], v[32:35], v[12:15], v[120:123]\n\t" \
  "v_mfma_f32_16x16x32_bf16 v[120:123], v[36:39], v[28:31], v[120:123]\n\t" \
  "ds_read_b64_tr_b16 v[32:33], v59 offset:2048\n\t" \
  "v_mfma_f32_16x16x32_bf16 v[128:131], v[0:3], v[40:43], v[128:131]\n\t" \
  "ds_read_b64_tr_b16 v[34:35], v59 offset:2560\n\t" \
  "s_waitcnt lgkmcnt(4)\n\t" \
  "v_mfma_f32_16x16x32_bf16 v[128:131], v[16:19], v[44:47], v[128:131]\n\t" \
  "ds_read_b64_tr_b16 v[36:37], v59 offset:14336\n\t" \
  "v_mfma_f32_16x16x32_bf16 v[136:139], v[4:7], v[40:43], v[136:139]\n\t" \
  "v_mfma_f32_16x16x32_bf16 v[136:139], v[20:23], v[44:47], v[136:139]\n\t" \
  "ds_read_b64_tr_b16 v[38:39], v59 offset:14848\n\t" \
  "v_mfma_f32_16x16x32_bf16 v[144:147], v[8:11], v[40:43], v[144:147]\n\t" \
  "v_mfma_f32_16x16x32_bf16 v[144:147], v[24:27], v[44:47], v[144:147]\n\t" \
  "v_mfma_f32_16x16x32_bf16 v[152:155], v[12:15], v[40:43], v[152:155]\n\t" \
  "v_mfma_f32_16x16x32_bf16 v[152:155], v[28:31], v[44:47], v[152:155]\n\t" \
  "s_waitcnt lgkmcnt(4)\n\t" \
  "v_mfma_f32_16x16x32_bf16 v[160:163], v[48:51], v[40:43], v[160:163]\n\t" \
  "v_mfma_f32_16x16x32_bf16 v[160:163], v[52:55], v[44:47], v[160:163]\n\t" \
  "ds_read_b64_tr_b16 v[40:41], v59 offset:1024\n\t" \
  "s_waitcnt lgkmcnt(1)\n\t" \
  "v_mfma_f32_16x16x32_bf16 v[164:167], v[48:51], v[32:35], v[164:167]\n\t" \
  "ds_read_b64_tr_b16 v[42:43], v59 offset:1536\n\t" \
  "v_mfma_f32_16x16x32_bf16 v[164:167], v[52:55], v[36:39], v[164:167]\n\t" \
  "ds_read_b64_tr_b16 v[44:45], v59 offset:13312\n\t" \
  "v_mfma_f32_16x16x32_bf16 v[132:135], v[0:3], v[32:35], v[132:135]\n\t" \
  "ds_read_b64_tr_b16 v[46:47], v59 offset:13824\n\t" \
  "v_mfma_f32_16x16x32_bf16 v[132:135], v[16:19], v[36:39], v[132:135]\n\t" \
  "ds_read_b64_tr_b16 v[48:49], v58 offset:0\n\t" \
  "v_mfma_f32_16x16x32_bf16 v[140:143], v[4:7], v[32:35], v[140:143]\n\t" \
  "ds_read_b64_tr_b16 v[50:51], v58 offset:512\n\t" \
  "v_mfma_f32_16x16x32_bf16 v[140:143], v[20:23], v[36:39], v[140:143]\n\t" \
  "ds_read_b64_tr_b16 v[52:53], v58 offset:12288\n\t" \
  "v_mfma_f32_16x16x32_bf16 v[148:151], v[8:11], v[32:35], v[148:151]\n\t" \
  "ds_read_b64_tr_b16 v[54:55], v58 offset:12800\n\t" \
  "v_mfma_f32_16x16x32_bf16 v[148:151], v[24:27], v[36:39], v[148:151]\n\t" \
  "v_mfma_f32_16x16x32_bf16 v[156:159], v[12:15], v[32:35], v[156:159]\n\t" \
  "ds_read_b64_tr_b16 v[32:33], v59 offset:0\n\t" \
  "v_mfma_f32_16x16x32_bf16 v[156:159], v[28:31], v[36:39], v[156:159]\n\t" \
  "ds_read_b64_tr_b16 v[34:35], v59 offset:512\n\t" \
  "s_waitcnt lgkmcnt(6)\n\t" \
  "v_mfma_f32_16x16x32_bf16 v[100:103], v[40:43], v[0:3], v[100:103]\n\t" \
  "ds_read_b64_tr_b16 v[36:37], v59 offset:12288\n\t" \
  "v_mfma_f32_16x16x32_bf16 v[100:103], v[44:47], v[16:19], v[100:103]\n\t" \
  "ds_read_b64_tr_b16 v[38:39], v59 offset:12800\n\t" \
  "v_mfma_f32_16x16x32_bf16 v[108:111], v[40:43], v[4:7], v[108:111]\n\t" \
  "v_mfma_f32_16x16x32_bf16 v[108:111], v[44:47], v[20:23], v[108:111]\n\t" \
  "v_mfma_f32_16x16x32_bf16 v[116:119], v[40:43], v[8:11], v[116:119]\n\t" \
  "v_mfma_f32_16x16x32_bf16 v[116:119], v[44:47], v[24:27], v[116:119]\n\t" \
  "v_mfma_f32_16x16x32_bf16 v[124:127], v[40:43], v[12:15], v[124:127]\n\t" \
  "v_mfma_f32_16x16x32_bf16 v[124:127], v[44:47], v[28:31], v[124:127]\n\t" \
  "s_waitcnt lgkmcnt(0)\n\t" \
  "s_barrier\n\t" \
  "v_mfma_f32_16x16x32_bf16 v[64:67], v[48:51], v[0:3], v[64:67]\n\t" \
  "v_mfma_f32_16x16x32_bf16 v[64:67], v[52:55], v[16:19], v[64:67]\n\t" \
  "ds_read_b64_tr_b16 v[40:41], v58 offset:25600\n\t" \
  "v_mfma_f32_16x16x32_bf16 v[68:71], v[32:35], v[0:3], v[68:71]\n\t" \
  "ds_read_b128 v[0:3], v56 offset:28672\n\t" \
  "v_mfma_f32_16x16x32_bf16 v[68:71], v[36:39], v[16:19], v[68:71]\n\t" \
  "ds_read_b128 v[16:19], v57 offset:28672\n\t" \
  "v_mfma_f32_16x16x32_bf16 v[72:75], v[48:51], v[4:7], v[72:75]\n\t" \
  "ds_read_b64_tr_b16 v[42:43], v58 offset:26112\n\t" \
  "v_mfma_f32_16x16x32_bf16 v[72:75], v[52:55], v[20:23], v[72:75]\n\t" \
  "ds_read_b64_tr_b16 v[44:45], v60 offset:1024\n\t" \
  "v_mfma_f32_16x16x32_bf16 v[76:79], v[32:35], v[4:7], v[76:79]\n\t" \
  "ds_read_b128 v[4:7], v56 offset:30720\n\t" \
  "v_mfma_f32_16x16x32_bf16 v[76:79], v[36:39], v[20:23], v[76:79]\n\t" \
  "ds_read_b128 v[20:23], v57 offset:30720\n\t" \
  "v_mfma_f32_16x16x32_bf16 v[80:83], v[48:51], v[8:11], v[80:83]\n\t" \
  "ds_read_b64_tr_b16 v[46:47], v60 offset:1536\n\t" \
  "v_mfma_f32_16x16x32_bf16 v[80:83], v[52:55], v[24:27], v[80:83]\n\t" \
  "v_mfma_f32_16x16x32_bf16 v[84:87], v[32:35], v[8:11], v[84:87]\n\t" \
  "ds_read_b128 v[8:11], v56 offset:32768\n\t" \
  "v_mfma_f32_16x16x32_bf16 v[84:87], v[36:39], v[24:27], v[84:87]\n\t" \
  "ds_read_b128 v[24:27], v57 offset:32768\n\t" \
  "v_mfma_f32_16x16x32_bf16 v[88:91], v[48:51], v[12:15], v[88:91]\n\t" \
  "ds_read_b64_tr_b16 v[48:49], v58 offset:26624\n\t" \
  "v_mfma_f32_16x16x32_bf16 v[88:91], v[52:55], v[28:31], v[88:91]\n\t" \
  "ds_read_b64_tr_b16 v[50:51], v58 offset:27136\n\t" \
  "v_mfma_f32_16x16x32_bf16 v[92:95], v[32:35], v[12:15], v[92:95]\n\t" \
  "ds_read_b128 v[12:15], v56 offset:34816\n\t" \
  "v_mfma_f32_16x16x32_bf16 v[92:95], v[36:39], v[28:31], v[92:95]\n\t" \
  "ds_read_b128 v[28:31], v57 offset:34816\n\t" \
  "N1b_%=:\n\t" \
  "s_waitcnt lgkmcnt(10)\n\t" \
  "v_mfma_f32_16x16x32_bf16 v[96:99], v[40:43], v[0:3], v[96:99]\n\t" \
  "ds_read_b64_tr_b16 v[52:53], v60 offset:2048\n\t" \
  "s_waitcnt lgkmcnt(7)\n\t" \
  "v_mfma_f32_16x16x32_bf16 v[96:99], v[44:47], v[16:19], v[96:99]\n\t" \
  "ds_read_b64_tr_b16 v[54:55], v60 offset:2560\n\t" \
  "v_mfma_f32_16x16x32_bf16 v[104:107], v[40:43], v[4:7], v[104:107]\n\t" \
  "ds_read_b128 v[32:35], v56 offset:18432\n\t" \
  "v_mfma_f32_16x16x32_bf16 v[104:107], v[44:47], v[20:23], v[104:107]\n\t" \
  "ds_read_b128 v[36:39], v57 offset:18432\n\t" \
  "s_waitcnt lgkmcnt(8)\n\t" \
  "v_mfma_f32_16x16x32_bf16 v[112:115], v[40:43], v[8:11], v[112:115]\n\t" \
  "v_mfma_f32_16x16x32_bf16 v[112:115], v[44:47], v[24:27], v[112:115]\n\t" \
  "s_waitcnt lgkmcnt(4)\n\t" \
  "v_mfma_f32_16x16x32_bf16 v[120:123], v[40:43], v[12:15], v[120:123]\n\t" \
  "v_mfma_f32_16x16x32_bf16 v[120:123], v[44:47], v[28:31], v[120:123]\n\t" \
  "ds_read_b64_tr_b16 v[40:41], v59 offset:26624\n\t" \
  "v_mfma_f32_16x16x32_bf16 v[128:131], v[0:3], v[48:51], v[128:131]\n\t" \
  "ds_read_b64_tr_b16 v[42:43], v59 offset:27136\n\t" \
  "s_waitcnt lgkmcnt(4)\n\t" \
  "v_mfma_f32_16x16x32_bf16 v[128:131], v[16:19], v[52:55], v[128:131]\n\t" \
  "ds_read_b64_tr_b16 v[44:45], v61 offset:2048\n\t" \
  "v_mfma_f32_16x16x32_bf16 v[136:139], v[4:7], v[48:51], v[136:139]\n\t" \
  "v_mfma_f32_16x16x32_bf16 v[136:139], v[20:23], v[52:55], v[136:139]\n\t" \
  "ds_read_b64_tr_b16 v[46:47], v61 offset:2560\n\t" \
  "v_mfma_f32_16x16x32_bf16 v[144:147], v[8:11], v[48:51], v[144:147]\n\t" \
  "v_mfma_f32_16x16x32_bf16 v[144:147], v[24:27], v[52:55], v[144:147]\n\t" \
  "v_mfma_f32_16x16x32_bf16 v[152:155], v[12:15], v[48:51], v[152:155]\n\t" \
  "v_mfma_f32_16x16x32_bf16 v[152:155], v[28:31], v[52:55], v[152:155]\n\t" \
  "s_waitcnt lgkmcnt(4)\n\t" \
  "v_mfma_f32_16x16x32_bf16 v[160:163], v[32:35], v[48:51], v[160:163]\n\t" \
  "v_mfma_f32_16x16x32_bf16 v[160:163], v[36:39], v[52:55], v[160:163]\n\t" \
  "ds_read_b64_tr_b16 v[48:49], v59 offset:25600\n\t" \
  "s_waitcnt lgkmcnt(1)\n\t" \
  "v_mfma_f32_16x16x32_bf16 v[164:167], v[32:35], v[40:43], v[164:167]\n\t" \
  "ds_read_b64_tr_b16 v[50:51], v59 offset:26112\n\t" \
  "v_mfma_f32_16x16x32_bf16 v[164:167], v[36:39], v[44:47], v[164:167]\n\t" \
  "ds_read_b64_tr_b16 v[52:53], v61 offset:1024\n\t" \
  "v_mfma_f32_16x16x32_bf16 v[132:135], v[0:3], v[40:43], v[132:135]\n\t" \
  "ds_read_b64_tr_b16 v[54:55], v61 offset:1536\n\t" \
  "v_mfma_f32_16x16x32_bf16 v[132:135], v[16:19], v[44:47], v[132:135]\n\t" \
  "ds_read_b64_tr_b16 v[32:33], v58 offset:24576\n\t" \
  "v_mfma_f32_16x16x32_bf16 v[140:143], v[4:7], v[40:43], v[140:143]\n\t" \
  "ds_read_b64_tr_b16 v[34:35], v58 offset:25088\n\t" \
  "v_mfma_f32_16x16x32_bf16 v[140:143], v[20:23], v[44:47], v[140:143]\n\t" \
  "ds_read_b64_tr_b16 v[36:37], v60 offset:0\n\t" \
  "v_mfma_f32_16x16x32_bf16 v[148:151], v[8:11], v[40:43], v[148:151]\n\t" \
  "ds_read_b64_tr_b16 v[38:39], v60 offset:512\n\t" \
  "v_mfma_f32_16x16x32_bf16 v[148:151], v[24:27], v[44:47], v[148:151]\n\t" \
  "v_mfma_f32_16x16x32_bf16 v[156:159], v[12:15], v[40:43], v[156:159]\n\t" \
  "ds_read_b64_tr_b16 v[40:41], v59 offset:24576\n\t" \
  "v_mfma_f32_16x16x32_bf16 v[156:159], v[28:31], v[44:47], v[156:159]\n\t" \
  "ds_read_b64_tr_b16 v[42:43], v59 offset:25088\n\t" \
  "s_waitcnt lgkmcnt(6)\n\t" \
  "v_mfma_f32_16x16x32_bf16 v[100:103], v[48:51], v[0:3], v[100:103]\n\t" \
  "ds_read_b64_tr_b16 v[44:45], v61 offset:0\n\t" \
  "v_mfma_f32_16x16x32_bf16 v[100:103], v[52:55], v[16:19], v[100:103]\n\t" \
  "ds_read_b64_tr_b16 v[46:47], v61 offset:512\n\t" \
  "v_mfma_f32_16x16x32_bf16 v[108:111], v[48:51], v[4:7], v[108:111]\n\t" \
  "v_mfma_f32_16x16x32_bf16 v[108:111], v[52:55], v[20:23], v[108:111]\n\t" \
  "v_mfma_f32_16x16x32_bf16 v[116:119], v[48:51], v[8:11], v[116:119]\n\t" \
  "v_mfma_f32_16x16x32_bf16 v[116:119], v[52:55], v[24:27], v[116:119]\n\t" \
  "v_mfma_f32_16x16x32_bf16 v[124:127], v[48:51], v[12:15], v[124:127]\n\t" \
  "v_mfma_f32_16x16x32_bf16 v[124:127], v[52:55], v[28:31], v[124:127]\n\t" \
  "s_waitcnt lgkmcnt(0)\n\t" \
  "s_barrier\n\t" \
  "v_mfma_f32_16x16x32_bf16 v[64:67], v[32:35], v[0:3], v[64:67]\n\t" \
  "v_mfma_f32_16x16x32_bf16 v[64:67], v[36:39], v[16:19], v[64:67]\n\t" \
  "ds_read_b64_tr_b16 v[48:49], v60 offset:13312\n\t" \
  "v_mfma_f32_16x16x32_bf16 v[68:71], v[40:43], v[0:3], v[68:71]\n\t" \
  "ds_read_b128 v[0:3], v56 offset:47104\n\t" \
  "v_mfma_f32_16x16x32_bf16 v[68:71], v[44:47], v[16:19], v[68:71]\n\t" \
  "ds_read_b128 v[16:19], v57 offset:47104\n\t" \
  "v_mfma_f32_16x16x32_bf16 v[72:75], v[32:35], v[4:7], v[72:75]\n\t" \
  "ds_read_b64_tr_b16 v[50:51], v60 offset:13824\n\t" \
  "v_mfma_f32_16x16x32_bf16 v[72:75], v[36:39], v[20:23], v[72:75]\n\t" \
  "ds_read_b64_tr_b16 v[52:53], v60 offset:25600\n\t" \
  "v_mfma_f32_16x16x32_bf16 v[76:79], v[40:43], v[4:7], v[76:79]\n\t" \
  "ds_read_b128 v[4:7], v56 offset:49152\n\t" \
  "v_mfma_f32_16x16x32_bf16 v[76:79], v[44:47], v[20:23], v[76:79]\n\t" \
  "ds_read_b128 v[20:23], v57 offset:49152\n\t" \
  "v_mfma_f32_16x16x32_bf16 v[80:83], v[32:35], v[8:11], v[80:83]\n\t" \
  "ds_read_b64_tr_b16 v[54:55], v60 offset:26112\n\t" \
  "v_mfma_f32_16x16x32_bf16 v[80:83], v[36:39], v[24:27], v[80:83]\n\t" \
  "v_mfma_f32_16x16x32_bf16 v[84:87], v[40:43], v[8:11], v[84:87]\n\t" \
  "ds_read_b128 v[8:11], v56 offset:51200\n\t" \
  "v_mfma_f32_16x16x32_bf16 v[84:87], v[44:47], v[24:27], v[84:87]\n\t" \
  "ds_read_b128 v[24:27], v57 offset:51200\n\t" \
  "v_mfma_f32_16x16x32_bf16 v[88:91], v[32:35], v[12:15], v[88:91]\n\t" \
  "ds_read_b64_tr_b16 v[32:33], v60 offset:14336\n\t" \
  "v_mfma_f32_16x16x32_bf16 v[88:91], v[36:39], v[28:31], v[88:91]\n\t" \
  "ds_read_b64_tr_b16 v[34:35], v60 offset:14848\n\t" \
  "v_mfma_f32_16x16x32_bf16 v[92:95], v[40:43], v[12:15], v[92:95]\n\t" \
  "ds_read_b128 v[12:15], v56 offset:53248\n\t" \
  "v_mfma_f32_16x16x32_bf16 v[92:95], v[44:47], v[28:31], v[92:95]\n\t" \
  "ds_read_b128 v[28:31], v57 offset:53248\n\t" \
  "N2b_%=:\n\t" \
  "s_waitcnt lgkmcnt(10)\n\t" \
  "v_mfma_f32_16x16x32_bf16 v[96:99], v[48:51], v[0:3], v[96:99]\n\t" \
  "ds_read_b64_tr_b16 v[36:37], v60 offset:26624\n\t" \
  "s_waitcnt lgkmcnt(7)\n\t" \
  "v_mfma_f32_16x16x32_bf16 v[96:99], v[52:55], v[16:19], v[96:99]\n\t" \
  "ds_read_b64_tr_b16 v[38:39], v60 offset:27136\n\t" \
  "v_mfma_f32_16x16x32_bf16 v[104:107], v[48:51], v[4:7], v[104:107]\n\t" \
  "ds_read_b128 v[40:43], v56 offset:36864\n\t" \
  "v_mfma_f32_16x16x32_bf16 v[104:107], v[52:55], v[20:23], v[104:107]\n\t" \
  "ds_read_b128 v[44:47], v57 offset:36864\n\t" \
  "s_waitcnt lgkmcnt(8)\n\t" \
  "v_mfma_f32_16x16x32_bf16 v[112:115], v[48:51], v[8:11], v[112:115]\n\t" \
  "v_mfma_f32_16x16x32_bf16 v[112:115], v[52:55], v[24:27], v[112:115]\n\t" \
  "s_waitcnt lgkmcnt(4)\n\t" \
  "v_mfma_f32_16x16x32_bf16 v[120:123], v[48:51], v[12:15], v[120:123]\n\t" \
  "v_mfma_f32_16x16x32_bf16 v[120:123], v[52:55], v[28:31], v[120:123]\n\t" \
  "ds_read_b64_tr_b16 v[48:49], v61 offset:14336\n\t" \
  "v_mfma_f32_16x16x32_bf16 v[128:131], v[0:3], v[32:35], v[128:131]\n\t" \
  "ds_read_b64_tr_b16 v[50:51], v61 offset:14848\n\t" \
  "s_waitcnt lgkmcnt(4)\n\t" \
  "v_mfma_f32_16x16x32_bf16 v[128:131], v[16:19], v[36:39], v[128:131]\n\t" \
  "ds_read_b64_tr_b16 v[52:53], v61 offset:26624\n\t" \
  "v_mfma_f32_16x16x32_bf16 v[136:139], v[4:7], v[32:35], v[136:139]\n\t" \
  "v_mfma_f32_16x16x32_bf16 v[136:139], v[20:23], v[36:39], v[136:139]\n\t" \
  "ds_read_b64_tr_b16 v[54:55], v61 offset:27136\n\t" \
  "v_mfma_f32_16x16x32_bf16 v[144:147], v[8:11], v[32:35], v[144:147]\n\t" \
  "v_mfma_f32_16x16x32_bf16 v[144:147], v[24:27], v[36:39], v[144:147]\n\t" \
  "v_mfma_f32_16x16x32_bf16 v[152:155], v[12:15], v[32:35], v[152:155]\n\t" \
  "v_mfma_f32_16x16x32_bf16 v[152:155], v[28:31], v[36:39], v[152:155]\n\t" \
  "s_waitcnt lgkmcnt(4)\n\t" \
  "v_mfma_f32_16x16x32_bf16 v[160:163], v[40:43], v[32:35], v[160:163]\n\t" \
  "v_mfma_f32_16x16x32_bf16 v[160:163], v[44:47], v[36:39], v[160:163]\n\t" \
  "ds_read_b64_tr_b16 v[32:33], v61 offset:13312\n\t" \
  "s_waitcnt lgkmcnt(1)\n\t" \
  "v_mfma_f32_16x16x32_bf16 v[164:167], v[40:43], v[48:51], v[164:167]\n\t" \
  "ds_read_b64_tr_b16 v[34:35], v61 offset:13824\n\t" \
  "v_mfma_f32_16x16x32_bf16 v[164:167], v[44:47], v[52:55], v[164:167]\n\t" \
  "ds_read_b64_tr_b16 v[36:37], v61 offset:25600\n\t" \
  "v_mfma_f32_16x16x32_bf16 v[132:135], v[0:3], v[48:51], v[132:135]\n\t" \
  "ds_read_b64_tr_b16 v[38:39], v61 offset:26112\n\t" \
  "v_mfma_f32_16x16x32_bf16 v[132:135], v[16:19], v[52:55], v[132:135]\n\t" \
  "ds_read_b64_tr_b16 v[40:41], v60 offset:12288\n\t" \
  "v_mfma_f32_16x16x32_bf16 v[140:143], v[4:7], v[48:51], v[140:143]\n\t" \
  "ds_read_b64_tr_b16 v[42:43], v60 offset:12800\n\t" \
  "v_mfma_f32_16x16x32_bf16 v[140:143], v[20:23], v[52:55], v[140:143]\n\t" \
  "ds_read_b64_tr_b16 v[44:45], v60 offset:24576\n\t" \
  "v_mfma_f32_16x16x32_bf16 v[148:151], v[8:11], v[48:51], v[148:151]\n\t" \
  "ds_read_b64_tr_b16 v[46:47], v60 offset:25088\n\t" \
  "v_mfma_f32_16x16x32_bf16 v[148:151], v[24:27], v[52:55], v[148:151]\n\t" \
  "v_mfma_f32_16x16x32_bf16 v[156:159], v[12:15], v[48:51], v[156:159]\n\t" \
  "ds_read_b64_tr_b16 v[48:49], v61 offset:12288\n\t" \
  "v_mfma_f32_16x16x32_bf16 v[156:159], v[28:31], v[52:55], v[156:159]\n\t" \
  "ds_read_b64_tr_b16 v[50:51], v61 offset:12800\n\t" \
  "s_waitcnt lgkmcnt(6)\n\t" \
  "v_mfma_f32_16x16x32_bf16 v[100:103], v[32:35], v[0:3], v[100:103]\n\t" \
  "ds_read_b64_tr_b16 v[52:53], v61 offset:24576\n\t" \
  "v_mfma_f32_16x16x32_bf16 v[100:103], v[36:39], v[16:19], v[100:103]\n\t" \
  "ds_read_b64_tr_b16 v[54:55], v61 offset:25088\n\t" \
  "v_mfma_f32_16x16x32_bf16 v[108:111], v[32:35], v[4:7], v[108:111]\n\t" \
  "v_mfma_f32_16x16x32_bf16 v[108:111], v[36:39], v[20:23], v[108:111]\n\t" \
  "s_addk_i32 %[it], 1\n\t" \
  "v_mfma_f32_16x16x32_bf16 v[116:119], v[32:35], v[8:11], v[116:119]\n\t" \
  "s_cmp_lt_u32 %[it], 6\n\t" \
  "v_mfma_f32_16x16x32_bf16 v[116:119], v[36:39], v[24:27], v[116:119]\n\t" \
  "v_mfma_f32_16x16x32_bf16 v[124:127], v[32:35], v[12:15], v[124:127]\n\t" \
  "v_mfma_f32_16x16x32_bf16 v[124:127], v[36:39], v[28:31], v[124:127]\n\t" \
  "s_waitcnt lgkmcnt(0)\n\t" \
  "s_barrier\n\t" \
  "s_cbranch_scc0 NTb_%=\n\t" \
  "v_mfma_f32_16x16x32_bf16 v[64:67], v[40:43], v[0:3], v[64:67]\n\t" \
  "v_mfma_f32_16x16x32_bf16 v[64:67], v[44:47], v[16:19], v[64:67]\n\t" \
  "ds_read_b64_tr_b16 v[32:33], v58 offset:1024\n\t" \
  "v_mfma_f32_16x16x32_bf16 v[68:71], v[48:51], v[0:3], v[68:71]\n\t" \
  "ds_read_b128 v[0:3], v56 offset:10240\n\t" \
  "v_mfma_f32_16x16x32_bf16 v[68:71], v[52:55], v[16:19], v[68:71]\n\t" \
  "ds_read_b128 v[16:19], v57 offset:10240\n\t" \
  "v_mfma_f32_16x16x32_bf16 v[72:75], v[40:43], v[4:7], v[72:75]\n\t" \
  "ds_read_b64_tr_b16 v[34:35], v58 offset:1536\n\t" \
  "v_mfma_f32_16x16x32_bf16 v[72:75], v[44:47], v[20:23], v[72:75]\n\t" \
  "ds_read_b64_tr_b16 v[36:37], v58 offset:13312\n\t" \
  "v_mfma_f32_16x16x32_bf16 v[76:79], v[48:51], v[4:7], v[76:79]\n\t" \
  "ds_read_b128 v[4:7], v56 offset:12288\n\t" \
  "v_mfma_f32_16x16x32_bf16 v[76:79], v[52:55], v[20:23], v[76:79]\n\t" \
  "ds_read_b128 v[20:23], v57 offset:12288\n\t" \
  "v_mfma_f32_16x16x32_bf16 v[80:83], v[40:43], v[8:11], v[80:83]\n\t" \
  "ds_read_b64_tr_b16 v[38:39], v58 offset:13824\n\t" \
  "v_mfma_f32_16x16x32_bf16 v[80:83], v[44:47], v[24:27], v[80:83]\n\t" \
  "v_mfma_f32_16x16x32_bf16 v[84:87], v[48:51], v[8:11], v[84:87]\n\t" \
  "ds_read_b128 v[8:11], v56 offset:14336\n\t" \
  "v_mfma_f32_16x16x32_bf16 v[84:87], v[52:55], v[24:27], v[84:87]\n\t" \
  "ds_read_b128 v[24:27], v57 offset:14336\n\t" \
  "v_mfma_f32_16x16x32_bf16 v[88:91], v[40:43], v[12:15], v[88:91]\n\t" \
  "ds_read_b64_tr_b16 v[40:41], v58 offset:2048\n\t" \
  "v_mfma_f32_16x16x32_bf16 v[88:91], v[44:47], v[28:31], v[88:91]\n\t" \
  "ds_read_b64_tr_b16 v[42:43], v58 offset:2560\n\t" \
  "v_mfma_f32_16x16x32_bf16 v[92:95], v[48:51], v[12:15], v[92:95]\n\t" \
  "ds_read_b128 v[12:15], v56 offset:16384\n\t" \
  "v_mfma_f32_16x16x32_bf16 v[92:95], v[52:55], v[28:31], v[92:95]\n\t" \
  "ds_read_b128 v[28:31], v57 offset:16384\n\t" \
  "s_branch N0b_%=\n\t" \
  "NTb_%=:\n\t" \
  "s_setprio 3\n\t" \
  "v_mfma_f32_16x16x32_bf16 v[88:91], v[40:43], v[12:15], v[88:91]\n\t" \
  "v_cvt_pk_bf16_f32 v160, v160, v161\n\t" \
  "v_cvt_pk_bf16_f32 v161, v162, v163\n\t" \
  "v_mfma_f32_16x16x32_bf16 v[88:91], v[44:47], v[28:31], v[88:91]\n\t" \
  "v_cvt_pk_bf16_f32 v162, v164, v165\n\t" \
  "v_cvt_pk_bf16_f32 v163, v166, v167\n\t" \
  "v_mfma_f32_16x16x32_bf16 v[92:95], v[48:51], v[12:15], v[92:95]\n\t" \
  "v_mov_b32 v57, %[fa]\n\t" \
  "v_mov_b32 v58, 1\n\t" \
  "v_mfma_f32_16x16x32_bf16 v[92:95], v[52:55], v[28:31], v[92:95]\n\t" \
  "ds_write_b128 v63, v[160:163] offset:2048\n\t" \
  "s_mov_b64 exec, 1\n\t" \
  "ds_write_b32 v57, v58\n\t" \
  "s_mov_b64 exec, -1\n\t" \
  "v_mfma_f32_16x16x32_bf16 v[80:83], v[40:43], v[8:11], v[80:83]\n\t" \
  "v_mov_b32 v60, 0xff61b1e6\n\t" \
  "v_cvt_pk_bf16_f32 v120, v120, v121\n\t" \
  "v_mfma_f32_16x16x32_bf16 v[80:83], v[44:47], v[24:27], v[80:83]\n\t" \
  "v_cvt_pk_bf16_f32 v121, v122, v123\n\t" \
  "v_cvt_pk_bf16_f32 v122, v124, v125\n\t" \
  "v_mfma_f32_16x16x32_bf16 v[84:87], v[48:51], v[8:11], v[84:87]\n\t" \
  "v_cvt_pk_bf16_f32 v123, v126, v127\n\t" \
  "v_cvt_pk_bf16_f32 v28, v152, v153\n\t" \
  "v_mfma_f32_16x16x32_bf16 v[84:87], v[52:55], v[24:27], v[84:87]\n\t" \
  "v_cvt_pk_bf16_f32 v29, v154, v155\n\t" \
  "v_cvt_pk_bf16_f32 v38, v156, v157\n\t" \
  "v_mfma_f32_16x16x32_bf16 v[72:75], v[40:43], v[4:7], v[72:75]\n\t" \
  "v_cvt_pk_bf16_f32 v39, v158, v159\n\t" \
  "v_cvt_pk_bf16_f32 v112, v112, v113\n\t" \
  "v_mfma_f32_16x16x32_bf16 v[72:75], v[44:47], v[20:23], v[72:75]\n\t" \
  "v_cvt_pk_bf16_f32 v113, v114, v115\n\t" \
  "v_cvt_pk_bf16_f32 v114, v116, v117\n\t" \
  "v_mfma_f32_16x16x32_bf16 v[76:79], v[48:51], v[4:7], v[76:79]\n\t" \
  "v_cvt_pk_bf16_f32 v115, v118, v119\n\t" \
  "v_cndmask_b32_e64 v12, v60, 0, s[76:77]\n\t" \
  "v_mfma_f32_16x16x32_bf16 v[76:79], v[52:55], v[20:23], v[76:79]\n\t" \
  "v_cndmask_b32_e64 v13, v60, 0, s[78:79]\n\t" \
  "v_cndmask_b32_e64 v14, v60, 0, s[80:81]\n\t" \
  "v_mfma_f32_16x16x32_bf16 v[64:67], v[40:43], v[0:3], v[64:67]\n\t" \
  "v_cndmask_b32_e64 v15, v60, 0, s[82:83]\n\t" \
  "v_cndmask_b32_e64 v56, v60, 0, s[68:69]\n\t" \
  "v_mfma_f32_16x16x32_bf16 v[64:67], v[44:47], v[16:19], v[64:67]\n\t" \
  "v_cndmask_b32_e64 v57, v60, 0, s[70:71]\n\t" \
  "v_cndmask_b32_e64 v58, v60, 0, s[72:73]\n\t" \
  "v_mfma_f32_16x16x32_bf16 v[68:71], v[48:51], v[0:3], v[68:71]\n\t" \
  "v_cndmask_b32_e64 v59, v60, 0, s[74:75]\n\t" \
  "v_mfma_f32_16x16x32_bf16 v[68:71], v[52:55], v[16:19], v[68:71]\n\t" \
  "NX_%=:\n\t" \
  "v_cvt_pk_bf16_f32 v88, v88, v89\n\t" \
  "v_cvt_pk_bf16_f32 v89, v90, v91\n\t" \
  "v_cvt_pk_bf16_f32 v90, v92, v93\n\t" \
  "v_cvt_pk_bf16_f32 v91, v94, v95\n\t" \
  "v_mov_b32 v61, %[fp]\n\t" \
  "v_add_u32 v60, %[vo], v63\n\t" \
  "v_mfma_f32_16x16x32_bf16 v[152:155], v[112:115], v[88:91], v[56:59]\n\t" \
  "v_cvt_pk_bf16_f32 v26, v144, v145\n\t" \
  "v_cvt_pk_bf16_f32 v27, v146, v147\n\t" \
  "v_mfma_f32_16x16x32_bf16 v[156:159], v[120:123], v[88:91], v[12:15]\n\t" \
  "v_cvt_pk_bf16_f32 v36, v148, v149\n\t" \
  "v_cvt_pk_bf16_f32 v37, v150, v151\n\t" \
  "v_cvt_pk_bf16_f32 v80, v80, v81\n\t" \
  "v_cvt_pk_bf16_f32 v81, v82, v83\n\t" \
  "v_cvt_pk_bf16_f32 v82, v84, v85\n\t" \
  "v_cvt_pk_bf16_f32 v83, v86, v87\n\t" \
  "v_cvt_pk_bf16_f32 v104, v104, v105\n\t" \
  "v_cvt_pk_bf16_f32 v105, v106, v107\n\t" \
  "v_mfma_f32_16x16x32_bf16 v[148:151], v[112:115], v[80:83], v[12:15]\n\t" \
  "v_cvt_pk_bf16_f32 v106, v108, v109\n\t" \
  "v_cvt_pk_bf16_f32 v107, v110, v111\n\t" \
  "ds_read_b32 v0, v61\n\t" \
  "ds_read_b64 v[20:21], v60\n\t" \
  "ds_read_b64 v[30:31], v60 offset:8\n\t" \
  "s_mov_b64 exec, %[rm]\n\t" \
  "ds_read_b128 v[160:163], v63\n\t" \
  "s_mov_b64 exec, -1\n\t" \
  "v_mfma_f32_16x16x32_bf16 v[144:147], v[104:107], v[80:83], v[56:59]\n\t" \
  "v_cvt_pk_bf16_f32 v24, v136, v137\n\t" \
  "v_cvt_pk_bf16_f32 v25, v138, v139\n\t" \
  "v_cvt_pk_bf16_f32 v34, v140, v141\n\t" \
  "v_cvt_pk_bf16_f32 v35, v142, v143\n\t" \
  "v_cvt_pk_bf16_f32 v72, v72, v73\n\t" \
  "v_cvt_pk_bf16_f32 v73, v74, v75\n\t" \
  "v_cvt_pk_bf16_f32 v74, v76, v77\n\t" \
  "v_cvt_pk_bf16_f32 v75, v78, v79\n\t" \
  "v_cvt_pk_bf16_f32 v96, v96, v97\n\t" \
  "v_cvt_pk_bf16_f32 v97, v98, v99\n\t" \
  "v_mfma_f32_16x16x32_bf16 v[140:143], v[104:107], v[72:75], v[12:15]\n\t" \
  "v_cvt_pk_bf16_f32 v98, v100, v101\n\t" \
  "v_cvt_pk_bf16_f32 v99, v102, v103\n\t" \
  "v_cvt_pk_bf16_f32 v22, v128, v129\n\t" \
  "v_cvt_pk_bf16_f32 v23, v130, v131\n\t" \
  "v_mfma_f32_16x16x32_bf16 v[136:139], v[96:99], v[72:75], v[56:59]\n\t" \
  "v_cvt_pk_bf16_f32 v32, v132, v133\n\t" \
  "v_cvt_pk_bf16_f32 v33, v134, v135\n\t" \
  "v_cvt_pk_bf16_f32 v64, v64, v65\n\t" \
  "v_cvt_pk_bf16_f32 v65, v66, v67\n\t" \
  "v_cvt_pk_bf16_f32 v66, v68, v69\n\t" \
  "v_cvt_pk_bf16_f32 v67, v70, v71\n\t" \
  "v_max_f32 v152, v152, v153\n\t" \
  "v_max_f32 v154, v154, v155\n\t" \
  "v_mfma_f32_16x16x32_bf16 v[132:135], v[96:99], v[64:67], v[12:15]\n\t" \
  "AW_%=:\n\t" \
  "s_waitcnt lgkmcnt(0)\n\t" \
  "v_cmp_eq_u32 vcc, 0, v0\n\t" \
  "v_mov_b32 v8, 0xc0c0100\n\t" \
  "v_mov_b32 v9, 0x1000c0c\n\t" \
  "s_cbranch_vccnz AR_%=\n\t" \
  "s_cmp_eq_u32 %[z], 0\n\t" \
  "s_cbranch_scc1 AZ_%=\n\t" \
  "AY_%=:\n\t" \
  "v_mfma_f32_16x16x32_bf16 v[128:131], v[160:163], v[64:67], v[56:59]\n\t" \
  "v_max_f32 v156, v156, v157\n\t" \
  "v_max_f32 v158, v158, v159\n\t" \
  "v_max3_f32 v7, v152, v154, v156\n\t" \
  "v_max_f32 v7, v7, v158\n\t" \
  "v_max_f32 v144, v144, v145\n\t" \
  "v_max_f32 v146, v146, v147\n\t" \
  "v_max_f32 v148, v148, v149\n\t" \
  "v_max_f32 v150, v150, v151\n\t" \
  "v_max3_f32 v6, v144, v146, v148\n\t" \
  "v_max_f32 v6, v6, v150\n\t" \
  "v_max_f32 v136, v136, v137\n\t" \
  "v_max_f32 v138, v138, v139\n\t" \
  "v_max_f32 v140, v140, v141\n\t" \
  "v_max_f32 v142, v142, v143\n\t" \
  "v_max3_f32 v5, v136, v138, v140\n\t" \
  "v_max_f32 v5, v5, v142\n\t" \
  "v_max_f32 v128, v128, v129\n\t" \
  "v_max_f32 v130, v130, v131\n\t" \
  "v_max_f32 v132, v132, v133\n\t" \
  "v_max_f32 v134, v134, v135\n\t" \
  "v_max3_f32 v4, v128, v130, v132\n\t" \
  "v_max_f32 v4, v4, v134\n\t" \
  "v_cndmask_b32_e64 v60, v8, v9, s[84:85]\n\t" \
  "v_mov_b32 v8, 0xc0c0302\n\t" \
  "v_permlane16_swap_b32 v6, v7\n\t" \
  "v_permlane16_swap_b32 v4, v5\n\t" \
  "v_max_f32 v6, v6, v7\n\t" \
  "v_max_f32 v4, v4, v5\n\t" \
  "v_mov_b32 v9, 0x3020c0c\n\t" \
  "v_cndmask_b32_e64 v61, v8, v9, s[84:85]\n\t" \
  "v_permlane32_swap_b32 v4, v6\n\t" \
  "v_max_f32 v4, v4, v6\n\t" \
  "v_mul_f32 v6, 0xbe8293ee, v4\n\t" \
  "v_mul_f32 v4, 0xbe8293ee, v4\n\t" \
  "s_nop 1\n\t" \
  "v_permlane32_swap_b32 v4, v6\n\t" \
  "v_mov_b32 v5, v4\n\t" \
  "v_mov_b32 v7, v6\n\t" \
  "s_nop 0\n\t" \
  "v_permlane16_swap_b32 v4, v5\n\t" \
  "v_permlane16_swap_b32 v6, v7\n\t" \
  "v_fmamk_f32 v128, v128, 0x3e8293ee, v4\n\t" \
  "v_fmamk_f32 v130, v130, 0x3e8293ee, v4\n\t" \
  "v_fmamk_f32 v132, v132, 0x3e8293ee, v4\n\t" \
  "v_fmamk_f32 v134, v134, 0x3e8293ee, v4\n\t" \
  "v_fmamk_f32 v136, v136, 0x3e8293ee, v5\n\t" \
  "v_fmamk_f32 v138, v138, 0x3e8293ee, v5\n\t" \
  "v_fmamk_f32 v140, v140, 0x3e8293ee, v5\n\t" \
  "v_fmamk_f32 v142, v142, 0x3e8293ee, v5\n\t" \
  "v_fmamk_f32 v144, v144, 0x3e8293ee, v6\n\t" \
  "v_fmamk_f32 v146, v146, 0x3e8293ee, v6\n\t" \
  "v_fmamk_f32 v148, v148, 0x3e8293ee, v6\n\t" \
  "v_fmamk_f32 v150, v150, 0x3e8293ee, v6\n\t" \
  "v_fmamk_f32 v152, v152, 0x3e8293ee, v7\n\t" \
  "v_fmamk_f32 v154, v154, 0x3e8293ee, v7\n\t" \
  "v_fmamk_f32 v156, v156, 0x3e8293ee, v7\n\t" \
  "v_fmamk_f32 v158, v158, 0x3e8293ee, v7\n\t" \
  "v_exp_f32 v128, v128\n\t" \
  "v_exp_f32 v130, v130\n\t" \
  "v_exp_f32 v132, v132\n\t" \
  "v_exp_f32 v134, v134\n\t" \
  "v_exp_f32 v136, v136\n\t" \
  "v_exp_f32 v138, v138\n\t" \
  "v_exp_f32 v140, v140\n\t" \
  "v_exp_f32 v142, v142\n\t" \
  "v_exp_f32 v144, v144\n\t" \
  "v_exp_f32 v146, v146\n\t" \
  "v_exp_f32 v148, v148\n\t" \
  "v_exp_f32 v150, v150\n\t" \
  "v_exp_f32 v152, v152\n\t" \
  "v_exp_f32 v154, v154\n\t" \
  "v_exp_f32 v156, v156\n\t" \
  "v_exp_f32 v158, v158\n\t" \
  "v_add_f32 v4, v128, v130\n\t" \
  "v_add_f32 v8, v132, v134\n\t" \
  "v_add_f32 v5, v136, v138\n\t" \
  "v_add_f32 v9, v140, v142\n\t" \
  "v_add_f32 v6, v144, v146\n\t" \
  "v_add_f32 v10, v148, v150\n\t" \
  "v_add_f32 v7, v152, v154\n\t" \
  "v_add_f32 v11, v156, v158\n\t" \
  "v_add_f32 v4, v4, v8\n\t" \
  "v_add_f32 v5, v5, v9\n\t" \
  "v_add_f32 v6, v6, v10\n\t" \
  "v_add_f32 v7, v7, v11\n\t" \
  "v_permlane16_swap_b32 v4, v5\n\t" \
  "s_nop 0\n\t" \
  "v_permlane16_swap_b32 v6, v7\n\t" \
  "v_add_f32 v4, v4, v5\n\t" \
  "v_add_f32 v6, v6, v7\n\t" \
  "s_nop 1\n\t" \
  "v_permlane32_swap_b32 v4, v6\n\t" \
  "v_add_f32 v4, v4, v6\n\t" \
  "v_rcp_f32 v4, v4\n\t" \
  "s_nop 0\n\t" \
  "v_mov_b32 v6, v4\n\t" \
  "s_nop 1\n\t" \
  "v_permlane32_swap_b32 v4, v6\n\t" \
  "v_mov_b32 v5, v4\n\t" \
  "v_mov_b32 v7, v6\n\t" \
  "s_nop 0\n\t" \
  "v_permlane16_swap_b32 v4, v5\n\t" \
  "v_permlane16_swap_b32 v6, v7\n\t" \
  "v_mul_f32 v128, v128, v4\n\t" \
  "v_mul_f32 v130, v130, v4\n\t" \
  "v_mul_f32 v132, v132, v4\n\t" \
  "v_mul_f32 v134, v134, v4\n\t" \
  "v_cvt_pk_bf16_f32 v128, v128, v130\n\t" \
  "v_cvt_pk_bf16_f32 v132, v132, v134\n\t" \
  "v_perm_b32 v68, 0, v128, v60\n\t" \
  "v_perm_b32 v69, 0, v128, v61\n\t" \
  "v_perm_b32 v70, 0, v132, v60\n\t" \
  "v_perm_b32 v71, 0, v132, v61\n\t" \
  "v_mul_f32 v136, v136, v5\n\t" \
  "v_mul_f32 v138, v138, v5\n\t" \
  "v_mfma_f32_16x16x32_bf16 v[128:131], v[20:23], v[68:71], 0\n\t" \
  "v_mul_f32 v140, v140, v5\n\t" \
  "v_mul_f32 v142, v142, v5\n\t" \
  "v_cvt_pk_bf16_f32 v136, v136, v138\n\t" \
  "v_mfma_f32_16x16x32_bf16 v[132:135], v[30:33], v[68:71], 0\n\t" \
  "v_cvt_pk_bf16_f32 v140, v140, v142\n\t" \
  "v_perm_b32 v76, 0, v136, v60\n\t" \
  "v_perm_b32 v77, 0, v136, v61\n\t" \
  "v_perm_b32 v78, 0, v140, v60\n\t" \
  "v_perm_b32 v79, 0, v140, v61\n\t" \
  "v_mul_f32 v144, v144, v6\n\t" \
  "v_mul_f32 v146, v146, v6\n\t" \
  "v_mfma_f32_16x16x32_bf16 v[136:139], v[22:25], v[76:79], 0\n\t" \
  "v_mul_f32 v148, v148, v6\n\t" \
  "v_mul_f32 v150, v150, v6\n\t" \
  "v_cvt_pk_bf16_f32 v144, v144, v146\n\t" \
  "v_mfma_f32_16x16x32_bf16 v[140:143], v[32:35], v[76:79], 0\n\t" \
  "v_cvt_pk_bf16_f32 v148, v148, v150\n\t" \
  "v_perm_b32 v84, 0, v144, v60\n\t" \
  "v_perm_b32 v85, 0, v144, v61\n\t" \
  "v_perm_b32 v86, 0, v148, v60\n\t" \
  "v_perm_b32 v87, 0, v148, v61\n\t" \
  "v_cvt_pk_bf16_f32 v100, v128, v129\n\t" \
  "v_cvt_pk_bf16_f32 v101, v130, v131\n\t" \
  "v_mfma_f32_16x16x32_bf16 v[144:147], v[24:27], v[84:87], 0\n\t" \
  "v_cvt_pk_bf16_f32 v102, v132, v133\n\t" \
  "v_cvt_pk_bf16_f32 v103, v134, v135\n\t" \
  "global_store_dwordx4 v62, v[100:103], %[ot] offset:0 sc1\n\t" \
  "v_mfma_f32_16x16x32_bf16 v[148:151], v[34:37], v[84:87], 0\n\t" \
  "v_mul_f32 v152, v152, v7\n\t" \
  "v_mul_f32 v154, v154, v7\n\t" \
  "v_mul_f32 v156, v156, v7\n\t" \
  "v_mul_f32 v158, v158, v7\n\t" \
  "v_cvt_pk_bf16_f32 v152, v152, v154\n\t" \
  "v_cvt_pk_bf16_f32 v156, v156, v158\n\t" \
  "v_perm_b32 v92, 0, v152, v60\n\t" \
  "v_perm_b32 v93, 0, v152, v61\n\t" \
  "v_perm_b32 v94, 0, v156, v60\n\t" \
  "v_perm_b32 v95, 0, v156, v61\n\t" \
  "v_cvt_pk_bf16_f32 v108, v136, v137\n\t" \
  "v_cvt_pk_bf16_f32 v109, v138, v139\n\t" \
  "v_mfma_f32_16x16x32_bf16 v[152:155], v[26:29], v[92:95], 0\n\t" \
  "v_cvt_pk_bf16_f32 v110, v140, v141\n\t" \
  "v_cvt_pk_bf16_f32 v111, v142, v143\n\t" \
  "global_store_dwordx4 v62, v[108:111], %[ot] offset:1024 sc1\n\t" \
  "v_mfma_f32_16x16x32_bf16 v[156:159], v[36:39], v[92:95], 0\n\t" \
  "v_cvt_pk_bf16_f32 v116, v144, v145\n\t" \
  "v_cvt_pk_bf16_f32 v117, v146, v147\n\t" \
  "v_cvt_pk_bf16_f32 v118, v148, v149\n\t" \
  "v_cvt_pk_bf16_f32 v119, v150, v151\n\t" \
  "global_store_dwordx4 v62, v[116:119], %[ou] offset:0 sc1\n\t" \
  "v_cvt_pk_bf16_f32 v124, v152, v153\n\t" \
  "v_cvt_pk_bf16_f32 v125, v154, v155\n\t" \
  "s_nop 0\n\t" \
  "v_cvt_pk_bf16_f32 v126, v156, v157\n\t" \
  "v_cvt_pk_bf16_f32 v127, v158, v159\n\t" \
  "global_store_dwordx4 v62, v[124:127], %[ou] offset:1024 sc1\n\t" \
  "s_branch AE_%=\n\t" \
  "AR_%=:\n\t" \
  "s_addk_i32 %[it], 1\n\t" \
  "s_cmp_gt_u32 %[it], 0x400000\n\t" \
  "s_cbranch_scc1 AY_%=\n\t" \
  "ds_read_b32 v0, v61\n\t" \
  "ds_read_b64 v[20:21], v60\n\t" \
  "ds_read_b64 v[30:31], v60 offset:8\n\t" \
  "s_mov_b64 exec, %[rm]\n\t" \
  "ds_read_b128 v[160:163], v63\n\t" \
  "s_mov_b64 exec, -1\n\t" \
  "s_branch AW_%=\n\t" \
  "AZ_%=:\n\t" \
  "v_mov_b32 v0, 0xff61b1e6\n\t" \
  "v_mov_b32 v1, 0xcfa8965f\n\t" \
  "s_nop 3\n\t" \
  "v_cndmask_b32_e64 v56, v0, v1, s[68:69]\n\t" \
  "v_cndmask_b32_e64 v57, v0, v1, s[70:71]\n\t" \
  "v_cndmask_b32_e64 v58, v0, v1, s[72:73]\n\t" \
  "v_cndmask_b32_e64 v59, v0, v1, s[74:75]\n\t" \
  "s_nop 1\n\t" \
  "s_branch AY_%=\n\t" \
  "AE_%=:\n\t" \
  "s_nop 1\n\t"
#define N2_CLOB "v0", "v1", "v2", "v3", "v4", "v5", "v6", "v7", "v8", "v9", "v10", "v11", "v12", "v13", "v14", "v15", "v16", "v17", "v18", "v19", "v20", "v21", "v22", "v23", "v24", "v25", "v26", "v27", "v28", "v29", "v30", "v31", "v32", "v33", "v34", "v35", "v36", "v37", "v38", "v39", "v40", "v41", "v42", "v43", "v44", "v45", "v46", "v47", "v48", "v49", "v50", "v51", "v52", "v53", "v54", "v55", "v60", "v61", "v64", "v65", "v66", "v67", "v68", "v69", "v70", "v71", "v72", "v73", "v74", "v75", "v76", "v77", "v78", "v79", "v80", "v81", "v82", "v83", "v84", "v85", "v86", "v87", "v88", "v89", "v90", "v91", "v92", "v93", "v94", "v95", "v96", "v97", "v98", "v99", "v100", "v101", "v102", "v103", "v104", "v105", "v106", "v107", "v108", "v109", "v110", "v111", "v112", "v113", "v114", "v115", "v116", "v117", "v118", "v119", "v120", "v121", "v122", "v123", "v124", "v125", "v126", "v127", "v128", "v129", "v130", "v131", "v132", "v133", "v134", "v135", "v136", "v137", "v138", "v139", "v140", "v141", "v142", "v143", "v144", "v145", "v146", "v147", "v148", "v149", "v150", "v151", "v152", "v153", "v154", "v155", "v156", "v157", "v158", "v159", "v160", "v161", "v162", "v163", "v164", "v165", "v166", "v167", "s68", "s69", "s70", "s71", "s72", "s73", "s74", "s75", "s76", "s77", "s78", "s79", "s80", "s81", "s82", "s83", "s84", "s85"

#define CV_PRE 1
#define CV_Y 1
#define CV_ASM \
  "CS_%=:\n\t" \
  "ds_read_b32 v34, v61\n\t" \
  "s_waitcnt lgkmcnt(0)\n\t" \
  "v_cmp_ne_u32 vcc, %[tg], v34\n\t" \
  "s_cbranch_vccz CR_%=\n\t" \
  "s_cmp_eq_u32 %[rem], 0\n\t" \
  "s_cbranch_scc1 CT_%=\n\t" \
  "s_barrier\n\t" \
  "s_sub_u32 %[rem], %[rem], 1\n\t" \
  "s_branch CS_%=\n\t" \
  "CT_%=:\n\t" \
  "s_addk_i32 %[cn], 1\n\t" \
  "s_cmp_gt_u32 %[cn], 0x400000\n\t" \
  "s_cbranch_scc1 CR_%=\n\t" \
  "s_sleep 1\n\t" \
  "s_branch CS_%=\n\t" \
  "CR_%=:\n\t" \
  "ds_read_b64 v[0:1], v28 offset:0\n\t" \
  "ds_read_b64 v[2:3], v29 offset:0\n\t" \
  "ds_read_b64 v[4:5], v30 offset:1024\n\t" \
  "ds_read_b64 v[6:7], v31 offset:1024\n\t" \
  "ds_read_b64 v[8:9], v24 offset:2048\n\t" \
  "ds_read_b64 v[10:11], v25 offset:2048\n\t" \
  "ds_read_b64 v[12:13], v26 offset:3072\n\t" \
  "ds_read_b64 v[14:15], v27 offset:3072\n\t" \
  "ds_read_b64 v[16:17], v28 offset:4096\n\t" \
  "ds_read_b64 v[18:19], v29 offset:4096\n\t" \
  "ds_read_b64 v[20:21], v30 offset:5120\n\t" \
  "ds_read_b64 v[22:23], v31 offset:5120\n\t" \
  "s_mov_b64 vcc, 1\n\t" \
  "s_setprio 0\n\t" \
  "s_cmp_eq_u32 %[rem], 0\n\t" \
  "s_cbranch_scc1 CM_%=\n\t" \
  "CB_%=:\n\t" \
  "s_barrier\n\t" \
  "s_sub_u32 %[rem], %[rem], 1\n\t" \
  "s_cmp_lg_u32 %[rem], 0\n\t" \
  "s_cbranch_scc1 CB_%=\n\t" \
  "CM_%=:\n\t" \
  "s_waitcnt lgkmcnt(0)\n\t" \
  "v_mfma_f32_16x16x32_bf16 v[128:131], v[68:71], v[0:3], 0\n\t" \
  "v_mfma_f32_16x16x32_bf16 v[128:131], v[64:67], v[4:7], v[128:131]\n\t" \
  "v_mfma_f32_16x16x32_bf16 v[132:135], v[76:79], v[0:3], 0\n\t" \
  "v_mfma_f32_16x16x32_bf16 v[132:135], v[72:75], v[4:7], v[132:135]\n\t" \
  "v_mfma_f32_16x16x32_bf16 v[136:139], v[84:87], v[0:3], 0\n\t" \
  "v_mfma_f32_16x16x32_bf16 v[136:139], v[80:83], v[4:7], v[136:139]\n\t" \
  "v_mfma_f32_16x16x32_bf16 v[140:143], v[92:95], v[0:3], 0\n\t" \
  "s_setprio 1\n\t" \
  "v_mfma_f32_16x16x32_bf16 v[140:143], v[88:91], v[4:7], v[140:143]\n\t" \
  "v_mfma_f32_16x16x32_bf16 v[144:147], v[100:103], v[0:3], 0\n\t" \
  "ds_bpermute_b32 v34, v32, v131\n\t" \
  "v_mfma_f32_16x16x32_bf16 v[144:147], v[96:99], v[4:7], v[144:147]\n\t" \
  "v_mov_b32 v131, v130\n\t" \
  "v_mov_b32 v130, v129\n\t" \
  "v_mfma_f32_16x16x32_bf16 v[148:151], v[108:111], v[0:3], 0\n\t" \
  "v_mov_b32 v129, v128\n\t" \
  "v_mfma_f32_16x16x32_bf16 v[148:151], v[104:107], v[4:7], v[148:151]\n\t" \
  "v_mfma_f32_16x16x32_bf16 v[152:155], v[116:119], v[0:3], 0\n\t" \
  "v_mfma_f32_16x16x32_bf16 v[152:155], v[112:115], v[4:7], v[152:155]\n\t" \
  "v_mfma_f32_16x16x32_bf16 v[156:159], v[124:127], v[0:3], 0\n\t" \
  "ds_read_b64 v[0:1], v24 offset:6144\n\t" \
  "v_mfma_f32_16x16x32_bf16 v[156:159], v[120:123], v[4:7], v[156:159]\n\t" \
  "ds_read_b64 v[2:3], v25 offset:6144\n\t" \
  "v_mfma_f32_16x16x32_bf16 v[132:135], v[68:71], v[8:11], v[132:135]\n\t" \
  "ds_read_b64 v[4:5], v26 offset:7168\n\t" \
  "v_mfma_f32_16x16x32_bf16 v[132:135], v[64:67], v[12:15], v[132:135]\n\t" \
  "ds_read_b64 v[6:7], v27 offset:7168\n\t" \
  "v_mfma_f32_16x16x32_bf16 v[136:139], v[76:79], v[8:11], v[136:139]\n\t" \
  "v_mfma_f32_16x16x32_bf16 v[136:139], v[72:75], v[12:15], v[136:139]\n\t" \
  "s_waitcnt lgkmcnt(4)\n\t" \
  "v_cndmask_b32_e64 v128, v34, 0, vcc\n\t" \
  "v_mfma_f32_16x16x32_bf16 v[140:143], v[84:87], v[8:11], v[140:143]\n\t" \
  "v_mfma_f32_16x16x32_bf16 v[140:143], v[80:83], v[12:15], v[140:143]\n\t" \
  "v_mfma_f32_16x16x32_bf16 v[144:147], v[92:95], v[8:11], v[144:147]\n\t" \
  "ds_bpermute_b32 v35, v32, v135\n\t" \
  "v_mfma_f32_16x16x32_bf16 v[144:147], v[88:91], v[12:15], v[144:147]\n\t" \
  "v_mov_b32 v135, v134\n\t" \
  "v_mov_b32 v134, v133\n\t" \
  "v_mfma_f32_16x16x32_bf16 v[148:151], v[100:103], v[8:11], v[148:151]\n\t" \
  "v_mov_b32 v133, v132\n\t" \
  "v_mfma_f32_16x16x32_bf16 v[148:151], v[96:99], v[12:15], v[148:151]\n\t" \
  "v_mfma_f32_16x16x32_bf16 v[152:155], v[108:111], v[8:11], v[152:155]\n\t" \
  "v_mfma_f32_16x16x32_bf16 v[152:155], v[104:107], v[12:15], v[152:155]\n\t" \
  "v_mfma_f32_16x16x32_bf16 v[156:159], v[116:119], v[8:11], v[156:159]\n\t" \
  "ds_read_b64 v[8:9], v28 offset:8192\n\t" \
  "v_mfma_f32_16x16x32_bf16 v[156:159], v[112:115], v[12:15], v[156:159]\n\t" \
  "ds_read_b64 v[10:11], v29 offset:8192\n\t" \
  "v_mfma_f32_16x16x32_bf16 v[136:139], v[68:71], v[16:19], v[136:139]\n\t" \
  "ds_read_b64 v[12:13], v30 offset:9216\n\t" \
  "v_mfma_f32_16x16x32_bf16 v[136:139], v[64:67], v[20:23], v[136:139]\n\t" \
  "ds_read_b64 v[14:15], v31 offset:9216\n\t" \
  "v_mfma_f32_16x16x32_bf16 v[140:143], v[76:79], v[16:19], v[140:143]\n\t" \
  "v_mfma_f32_16x16x32_bf16 v[140:143], v[72:75], v[20:23], v[140:143]\n\t" \
  "s_waitcnt lgkmcnt(4)\n\t" \
  "v_cndmask_b32_e64 v132, v35, v34, vcc\n\t" \
  "v_mfma_f32_16x16x32_bf16 v[144:147], v[84:87], v[16:19], v[144:147]\n\t" \
  "v_mfma_f32_16x16x32_bf16 v[144:147], v[80:83], v[20:23], v[144:147]\n\t" \
  "v_mfma_f32_16x16x32_bf16 v[148:151], v[92:95], v[16:19], v[148:151]\n\t" \
  "ds_bpermute_b32 v36, v32, v139\n\t" \
  "v_mfma_f32_16x16x32_bf16 v[148:151], v[88:91], v[20:23], v[148:151]\n\t" \
  "v_mov_b32 v139, v138\n\t" \
  "v_mov_b32 v138, v137\n\t" \
  "v_mfma_f32_16x16x32_bf16 v[152:155], v[100:103], v[16:19], v[152:155]\n\t" \
  "v_mov_b32 v137, v136\n\t" \
  "v_mfma_f32_16x16x32_bf16 v[152:155], v[96:99], v[20:23], v[152:155]\n\t" \
  "v_mfma_f32_16x16x32_bf16 v[156:159], v[108:111], v[16:19], v[156:159]\n\t" \
  "ds_read_b64 v[16:17], v24 offset:10240\n\t" \
  "v_mfma_f32_16x16x32_bf16 v[156:159], v[104:107], v[20:23], v[156:159]\n\t" \
  "ds_read_b64 v[18:19], v25 offset:10240\n\t" \
  "v_mfma_f32_16x16x32_bf16 v[140:143], v[68:71], v[0:3], v[140:143]\n\t" \
  "ds_read_b64 v[20:21], v26 offset:11264\n\t" \
  "v_mfma_f32_16x16x32_bf16 v[140:143], v[64:67], v[4:7], v[140:143]\n\t" \
  "ds_read_b64 v[22:23], v27 offset:11264\n\t" \
  "v_mfma_f32_16x16x32_bf16 v[144:147], v[76:79], v[0:3], v[144:147]\n\t" \
  "v_mfma_f32_16x16x32_bf16 v[144:147], v[72:75], v[4:7], v[144:147]\n\t" \
  "v_mfma_f32_16x16x32_bf16 v[148:151], v[84:87], v[0:3], v[148:151]\n\t" \
  "v_mfma_f32_16x16x32_bf16 v[148:151], v[80:83], v[4:7], v[148:151]\n\t" \
  "s_waitcnt lgkmcnt(4)\n\t" \
  "v_cndmask_b32_e64 v136, v36, v35, vcc\n\t" \
  "v_mfma_f32_16x16x32_bf16 v[152:155], v[92:95], v[0:3], v[152:155]\n\t" \
  "ds_bpermute_b32 v37, v32, v143\n\t" \
  "v_mfma_f32_16x16x32_bf16 v[152:155], v[88:91], v[4:7], v[152:155]\n\t" \
  "v_mov_b32 v143, v142\n\t" \
  "v_mov_b32 v142, v141\n\t" \
  "v_mfma_f32_16x16x32_bf16 v[156:159], v[100:103], v[0:3], v[156:159]\n\t" \
  "ds_read_b64 v[0:1], v28 offset:12288\n\t" \
  "v_mov_b32 v141, v140\n\t" \
  "v_mfma_f32_16x16x32_bf16 v[156:159], v[96:99], v[4:7], v[156:159]\n\t" \
  "ds_read_b64 v[2:3], v29 offset:12288\n\t" \
  "v_mfma_f32_16x16x32_bf16 v[144:147], v[68:71], v[8:11], v[144:147]\n\t" \
  "ds_read_b64 v[4:5], v30 offset:13312\n\t" \
  "v_mfma_f32_16x16x32_bf16 v[144:147], v[64:67], v[12:15], v[144:147]\n\t" \
  "ds_read_b64 v[6:7], v31 offset:13312\n\t" \
  "v_mfma_f32_16x16x32_bf16 v[148:151], v[76:79], v[8:11], v[148:151]\n\t" \
  "v_mfma_f32_16x16x32_bf16 v[148:151], v[72:75], v[12:15], v[148:151]\n\t" \
  "v_mfma_f32_16x16x32_bf16 v[152:155], v[84:87], v[8:11], v[152:155]\n\t" \
  "v_mfma_f32_16x16x32_bf16 v[152:155], v[80:83], v[12:15], v[152:155]\n\t" \
  "v_mfma_f32_16x16x32_bf16 v[156:159], v[92:95], v[8:11], v[156:159]\n\t" \
  "ds_read_b64 v[8:9], v24 offset:14336\n\t" \
  "v_mfma_f32_16x16x32_bf16 v[156:159], v[88:91], v[12:15], v[156:159]\n\t" \
  "ds_read_b64 v[10:11], v25 offset:14336\n\t" \
  "s_waitcnt lgkmcnt(6)\n\t" \
  "v_mfma_f32_16x16x32_bf16 v[148:151], v[68:71], v[16:19], v[148:151]\n\t" \
  "ds_read_b64 v[12:13], v26 offset:15360\n\t" \
  "v_cndmask_b32_e64 v140, v37, v36, vcc\n\t" \
  "v_mfma_f32_16x16x32_bf16 v[148:151], v[64:67], v[20:23], v[148:151]\n\t" \
  "ds_read_b64 v[14:15], v27 offset:15360\n\t" \
  "v_mfma_f32_16x16x32_bf16 v[152:155], v[76:79], v[16:19], v[152:155]\n\t" \
  "ds_bpermute_b32 v38, v32, v147\n\t" \
  "v_mfma_f32_16x16x32_bf16 v[152:155], v[72:75], v[20:23], v[152:155]\n\t" \
  "v_mov_b32 v147, v146\n\t" \
  "v_mov_b32 v146, v145\n\t" \
  "v_mfma_f32_16x16x32_bf16 v[156:159], v[84:87], v[16:19], v[156:159]\n\t" \
  "ds_read_b64 v[16:17], v26 offset:0\n\t" \
  "ds_read_b64 v[18:19], v27 offset:0\n\t" \
  "v_mfma_f32_16x16x32_bf16 v[156:159], v[80:83], v[20:23], v[156:159]\n\t" \
  "ds_read_b64 v[20:21], v28 offset:1024\n\t" \
  "ds_read_b64 v[22:23], v29 offset:1024\n\t" \
  "s_waitcnt lgkmcnt(9)\n\t" \
  "v_mfma_f32_16x16x32_bf16 v[152:155], v[68:71], v[0:3], v[152:155]\n\t" \
  "ds_bpermute_b32 v39, v32, v151\n\t" \
  "v_mov_b32 v145, v144\n\t" \
  "v_mfma_f32_16x16x32_bf16 v[152:155], v[64:67], v[4:7], v[152:155]\n\t" \
  "v_mov_b32 v151, v150\n\t" \
  "v_mov_b32 v150, v149\n\t" \
  "v_mfma_f32_16x16x32_bf16 v[156:159], v[76:79], v[0:3], v[156:159]\n\t" \
  "ds_read_b64 v[0:1], v30 offset:2048\n\t" \
  "v_mov_b32 v149, v148\n\t" \
  "v_mfma_f32_16x16x32_bf16 v[156:159], v[72:75], v[4:7], v[156:159]\n\t" \
  "ds_read_b64 v[2:3], v31 offset:2048\n\t" \
  "s_waitcnt lgkmcnt(10)\n\t" \
  "v_mfma_f32_16x16x32_bf16 v[156:159], v[68:71], v[8:11], v[156:159]\n\t" \
  "ds_bpermute_b32 v40, v32, v155\n\t" \
  "s_waitcnt lgkmcnt(9)\n\t" \
  "v_mfma_f32_16x16x32_bf16 v[156:159], v[64:67], v[12:15], v[156:159]\n\t" \
  "ds_read_b64 v[4:5], v24 offset:3072\n\t" \
  "s_waitcnt lgkmcnt(7)\n\t" \
  "v_mfma_f32_16x16x32_bf16 v[128:131], v[68:71], v[16:19], v[128:131]\n\t" \
  "ds_read_b64 v[6:7], v25 offset:3072\n\t" \
  "s_waitcnt lgkmcnt(6)\n\t" \
  "v_mfma_f32_16x16x32_bf16 v[128:131], v[64:67], v[20:23], v[128:131]\n\t" \
  "v_cndmask_b32_e64 v144, v38, v37, vcc\n\t" \
  "v_mov_b32 v155, v154\n\t" \
  "v_mfma_f32_16x16x32_bf16 v[132:135], v[76:79], v[16:19], v[132:135]\n\t" \
  "ds_bpermute_b32 v41, v32, v159\n\t" \
  "v_mov_b32 v154, v153\n\t" \
  "v_mfma_f32_16x16x32_bf16 v[132:135], v[72:75], v[20:23], v[132:135]\n\t" \
  "v_mov_b32 v153, v152\n\t" \
  "v_mov_b32 v159, v158\n\t" \
  "v_mfma_f32_16x16x32_bf16 v[136:139], v[84:87], v[16:19], v[136:139]\n\t" \
  "v_mov_b32 v158, v157\n\t" \
  "v_mov_b32 v157, v156\n\t" \
  "v_mfma_f32_16x16x32_bf16 v[136:139], v[80:83], v[20:23], v[136:139]\n\t" \
  "s_waitcnt lgkmcnt(6)\n\t" \
  "v_cndmask_b32_e64 v148, v39, v38, vcc\n\t" \
  "v_mfma_f32_16x16x32_bf16 v[140:143], v[92:95], v[16:19], v[140:143]\n\t" \
  "ds_read_b64 v[8:9], v26 offset:4096\n\t" \
  "v_mfma_f32_16x16x32_bf16 v[140:143], v[88:91], v[20:23], v[140:143]\n\t" \
  "ds_read_b64 v[10:11], v27 offset:4096\n\t" \
  "v_mfma_f32_16x16x32_bf16 v[144:147], v[100:103], v[16:19], v[144:147]\n\t" \
  "s_waitcnt lgkmcnt(5)\n\t" \
  "v_cndmask_b32_e64 v152, v40, v39, vcc\n\t" \
  "v_mfma_f32_16x16x32_bf16 v[144:147], v[96:99], v[20:23], v[144:147]\n\t" \
  "global_load_dwordx4 v[44:47], v43, %[q0] offset:0\n\t" \
  "v_mfma_f32_16x16x32_bf16 v[148:151], v[108:111], v[16:19], v[148:151]\n\t" \
  "s_waitcnt lgkmcnt(2)\n\t" \
  "v_cndmask_b32_e64 v156, v41, v40, vcc\n\t" \
  "v_mfma_f32_16x16x32_bf16 v[148:151], v[104:107], v[20:23], v[148:151]\n\t" \
  "ds_read_b64 v[12:13], v28 offset:5120\n\t" \
  "v_mfma_f32_16x16x32_bf16 v[152:155], v[116:119], v[16:19], v[152:155]\n\t" \
  "ds_read_b64 v[14:15], v29 offset:5120\n\t" \
  "v_mfma_f32_16x16x32_bf16 v[152:155], v[112:115], v[20:23], v[152:155]\n\t" \
  "global_load_dwordx4 v[48:51], v43, %[q0] offset:2048\n\t" \
  "v_mfma_f32_16x16x32_bf16 v[156:159], v[124:127], v[16:19], v[156:159]\n\t" \
  "ds_read_b64 v[16:17], v30 offset:6144\n\t" \
  "v_mfma_f32_16x16x32_bf16 v[156:159], v[120:123], v[20:23], v[156:159]\n\t" \
  "ds_read_b64 v[18:19], v31 offset:6144\n\t" \
  "v_mfma_f32_16x16x32_bf16 v[132:135], v[68:71], v[0:3], v[132:135]\n\t" \
  "global_load_dwordx4 v[52:55], v43, %[q2] offset:0\n\t" \
  "v_mfma_f32_16x16x32_bf16 v[132:135], v[64:67], v[4:7], v[132:135]\n\t" \
  "ds_read_b64 v[20:21], v24 offset:7168\n\t" \
  "v_mfma_f32_16x16x32_bf16 v[136:139], v[76:79], v[0:3], v[136:139]\n\t" \
  "ds_read_b64 v[22:23], v25 offset:7168\n\t" \
  "v_mfma_f32_16x16x32_bf16 v[136:139], v[72:75], v[4:7], v[136:139]\n\t" \
  "ds_bpermute_b32 v34, v32, v131\n\t" \
  "v_mfma_f32_16x16x32_bf16 v[140:143], v[84:87], v[0:3], v[140:143]\n\t" \
  "global_load_dwordx4 v[56:59], v43, %[q2] offset:2048\n\t" \
  "v_mfma_f32_16x16x32_bf16 v[140:143], v[80:83], v[4:7], v[140:143]\n\t" \
  "ds_bpermute_b32 v35, v32, v135\n\t" \
  "v_mov_b32 v131, v130\n\t" \
  "v_mfma_f32_16x16x32_bf16 v[144:147], v[92:95], v[0:3], v[144:147]\n\t" \
  "v_mov_b32 v130, v129\n\t" \
  "v_mov_b32 v129, v128\n\t" \
  "v_mfma_f32_16x16x32_bf16 v[144:147], v[88:91], v[4:7], v[144:147]\n\t" \
  "v_mov_b32 v135, v134\n\t" \
  "v_mov_b32 v134, v133\n\t" \
  "v_mfma_f32_16x16x32_bf16 v[148:151], v[100:103], v[0:3], v[148:151]\n\t" \
  "v_mov_b32 v133, v132\n\t" \
  "v_mfma_f32_16x16x32_bf16 v[148:151], v[96:99], v[4:7], v[148:151]\n\t" \
  "v_mfma_f32_16x16x32_bf16 v[152:155], v[108:111], v[0:3], v[152:155]\n\t" \
  "v_mfma_f32_16x16x32_bf16 v[152:155], v[104:107], v[4:7], v[152:155]\n\t" \
  "v_mfma_f32_16x16x32_bf16 v[156:159], v[116:119], v[0:3], v[156:159]\n\t" \
  "ds_read_b64 v[0:1], v26 offset:8192\n\t" \
  "v_mfma_f32_16x16x32_bf16 v[156:159], v[112:115], v[4:7], v[156:159]\n\t" \
  "ds_read_b64 v[2:3], v27 offset:8192\n\t" \
  "s_waitcnt lgkmcnt(4)\n\t" \
  "v_mfma_f32_16x16x32_bf16 v[136:139], v[68:71], v[8:11], v[136:139]\n\t" \
  "ds_read_b64 v[4:5], v28 offset:9216\n\t" \
  "v_mfma_f32_16x16x32_bf16 v[136:139], v[64:67], v[12:15], v[136:139]\n\t" \
  "ds_read_b64 v[6:7], v29 offset:9216\n\t" \
  "v_mfma_f32_16x16x32_bf16 v[140:143], v[76:79], v[8:11], v[140:143]\n\t" \
  "s_waitcnt lgkmcnt(5)\n\t" \
  "v_cndmask_b32_e64 v128, v34, 0, vcc\n\t" \
  "v_mfma_f32_16x16x32_bf16 v[140:143], v[72:75], v[12:15], v[140:143]\n\t" \
  "s_waitcnt lgkmcnt(4)\n\t" \
  "v_cndmask_b32_e64 v132, v35, v34, vcc\n\t" \
  "v_mfma_f32_16x16x32_bf16 v[144:147], v[84:87], v[8:11], v[144:147]\n\t" \
  "ds_bpermute_b32 v36, v32, v139\n\t" \
  "v_mfma_f32_16x16x32_bf16 v[144:147], v[80:83], v[12:15], v[144:147]\n\t" \
  "v_mov_b32 v139, v138\n\t" \
  "v_mov_b32 v138, v137\n\t" \
  "v_mfma_f32_16x16x32_bf16 v[148:151], v[92:95], v[8:11], v[148:151]\n\t" \
  "v_mov_b32 v137, v136\n\t" \
  "v_mfma_f32_16x16x32_bf16 v[148:151], v[88:91], v[12:15], v[148:151]\n\t" \
  "v_mfma_f32_16x16x32_bf16 v[152:155], v[100:103], v[8:11], v[152:155]\n\t" \
  "v_mfma_f32_16x16x32_bf16 v[152:155], v[96:99], v[12:15], v[152:155]\n\t" \
  "v_mfma_f32_16x16x32_bf16 v[156:159], v[108:111], v[8:11], v[156:159]\n\t" \
  "ds_read_b64 v[8:9], v30 offset:10240\n\t" \
  "v_mfma_f32_16x16x32_bf16 v[156:159], v[104:107], v[12:15], v[156:159]\n\t" \
  "ds_read_b64 v[10:11], v31 offset:10240\n\t" \
  "v_mfma_f32_16x16x32_bf16 v[140:143], v[68:71], v[16:19], v[140:143]\n\t" \
  "ds_read_b64 v[12:13], v24 offset:11264\n\t" \
  "v_mfma_f32_16x16x32_bf16 v[140:143], v[64:67], v[20:23], v[140:143]\n\t" \
  "ds_read_b64 v[14:15], v25 offset:11264\n\t" \
  "v_mfma_f32_16x16x32_bf16 v[144:147], v[76:79], v[16:19], v[144:147]\n\t" \
  "v_mfma_f32_16x16x32_bf16 v[144:147], v[72:75], v[20:23], v[144:147]\n\t" \
  "s_waitcnt lgkmcnt(4)\n\t" \
  "v_cndmask_b32_e64 v136, v36, v35, vcc\n\t" \
  "v_mfma_f32_16x16x32_bf16 v[148:151], v[84:87], v[16:19], v[148:151]\n\t" \
  "v_mfma_f32_16x16x32_bf16 v[148:151], v[80:83], v[20:23], v[148:151]\n\t" \
  "v_mfma_f32_16x16x32_bf16 v[152:155], v[92:95], v[16:19], v[152:155]\n\t" \
  "ds_bpermute_b32 v37, v32, v143\n\t" \
  "v_mfma_f32_16x16x32_bf16 v[152:155], v[88:91], v[20:23], v[152:155]\n\t" \
  "v_mov_b32 v143, v142\n\t" \
  "v_mov_b32 v142, v141\n\t" \
  "v_mfma_f32_16x16x32_bf16 v[156:159], v[100:103], v[16:19], v[156:159]\n\t" \
  "ds_read_b64 v[16:17], v26 offset:12288\n\t" \
  "v_mov_b32 v141, v140\n\t" \
  "v_mfma_f32_16x16x32_bf16 v[156:159], v[96:99], v[20:23], v[156:159]\n\t" \
  "ds_read_b64 v[18:19], v27 offset:12288\n\t" \
  "v_mfma_f32_16x16x32_bf16 v[144:147], v[68:71], v[0:3], v[144:147]\n\t" \
  "ds_read_b64 v[20:21], v28 offset:13312\n\t" \
  "v_mfma_f32_16x16x32_bf16 v[144:147], v[64:67], v[4:7], v[144:147]\n\t" \
  "ds_read_b64 v[22:23], v29 offset:13312\n\t" \
  "v_mfma_f32_16x16x32_bf16 v[148:151], v[76:79], v[0:3], v[148:151]\n\t" \
  "v_mfma_f32_16x16x32_bf16 v[148:151], v[72:75], v[4:7], v[148:151]\n\t" \
  "v_mfma_f32_16x16x32_bf16 v[152:155], v[84:87], v[0:3], v[152:155]\n\t" \
  "v_mfma_f32_16x16x32_bf16 v[152:155], v[80:83], v[4:7], v[152:155]\n\t" \
  "v_mfma_f32_16x16x32_bf16 v[156:159], v[92:95], v[0:3], v[156:159]\n\t" \
  "ds_read_b64 v[0:1], v30 offset:14336\n\t" \
  "v_mfma_f32_16x16x32_bf16 v[156:159], v[88:91], v[4:7], v[156:159]\n\t" \
  "ds_read_b64 v[2:3], v31 offset:14336\n\t" \
  "s_waitcnt lgkmcnt(6)\n\t" \
  "v_mfma_f32_16x16x32_bf16 v[148:151], v[68:71], v[8:11], v[148:151]\n\t" \
  "ds_read_b64 v[4:5], v24 offset:15360\n\t" \
  "v_cndmask_b32_e64 v140, v37, v36, vcc\n\t" \
  "v_mfma_f32_16x16x32_bf16 v[148:151], v[64:67], v[12:15], v[148:151]\n\t" \
  "ds_read_b64 v[6:7], v25 offset:15360\n\t" \
  "v_mfma_f32_16x16x32_bf16 v[152:155], v[76:79], v[8:11], v[152:155]\n\t" \
  "ds_bpermute_b32 v38, v32, v147\n\t" \
  "v_mfma_f32_16x16x32_bf16 v[152:155], v[72:75], v[12:15], v[152:155]\n\t" \
  "v_mov_b32 v147, v146\n\t" \
  "v_mov_b32 v146, v145\n\t" \
  "v_mfma_f32_16x16x32_bf16 v[156:159], v[84:87], v[8:11], v[156:159]\n\t" \
  "ds_read_b64 v[8:9], v24 offset:0\n\t" \
  "ds_read_b64 v[10:11], v25 offset:0\n\t" \
  "v_mfma_f32_16x16x32_bf16 v[156:159], v[80:83], v[12:15], v[156:159]\n\t" \
  "ds_read_b64 v[12:13], v26 offset:1024\n\t" \
  "ds_read_b64 v[14:15], v27 offset:1024\n\t" \
  "s_waitcnt lgkmcnt(9)\n\t" \
  "v_mfma_f32_16x16x32_bf16 v[152:155], v[68:71], v[16:19], v[152:155]\n\t" \
  "ds_bpermute_b32 v39, v32, v151\n\t" \
  "v_mov_b32 v145, v144\n\t" \
  "v_mfma_f32_16x16x32_bf16 v[152:155], v[64:67], v[20:23], v[152:155]\n\t" \
  "v_mov_b32 v151, v150\n\t" \
  "v_mov_b32 v150, v149\n\t" \
  "v_mfma_f32_16x16x32_bf16 v[156:159], v[76:79], v[16:19], v[156:159]\n\t" \
  "ds_read_b64 v[16:17], v28 offset:2048\n\t" \
  "v_mov_b32 v149, v148\n\t" \
  "v_mfma_f32_16x16x32_bf16 v[156:159], v[72:75], v[20:23], v[156:159]\n\t" \
  "ds_read_b64 v[18:19], v29 offset:2048\n\t" \
  "s_waitcnt lgkmcnt(10)\n\t" \
  "v_mfma_f32_16x16x32_bf16 v[156:159], v[68:71], v[0:3], v[156:159]\n\t" \
  "ds_bpermute_b32 v40, v32, v155\n\t" \
  "s_waitcnt lgkmcnt(9)\n\t" \
  "v_mfma_f32_16x16x32_bf16 v[156:159], v[64:67], v[4:7], v[156:159]\n\t" \
  "ds_read_b64 v[20:21], v30 offset:3072\n\t" \
  "s_waitcnt lgkmcnt(7)\n\t" \
  "v_mfma_f32_16x16x32_bf16 v[128:131], v[68:71], v[8:11], v[128:131]\n\t" \
  "ds_read_b64 v[22:23], v31 offset:3072\n\t" \
  "s_waitcnt lgkmcnt(6)\n\t" \
  "v_mfma_f32_16x16x32_bf16 v[128:131], v[64:67], v[12:15], v[128:131]\n\t" \
  "v_cndmask_b32_e64 v144, v38, v37, vcc\n\t" \
  "v_mov_b32 v155, v154\n\t" \
  "v_mfma_f32_16x16x32_bf16 v[132:135], v[76:79], v[8:11], v[132:135]\n\t" \
  "ds_bpermute_b32 v41, v32, v159\n\t" \
  "v_mov_b32 v154, v153\n\t" \
  "v_mfma_f32_16x16x32_bf16 v[132:135], v[72:75], v[12:15], v[132:135]\n\t" \
  "v_mov_b32 v153, v152\n\t" \
  "v_mov_b32 v159, v158\n\t" \
  "v_mfma_f32_16x16x32_bf16 v[136:139], v[84:87], v[8:11], v[136:139]\n\t" \
  "v_mov_b32 v158, v157\n\t" \
  "v_mov_b32 v157, v156\n\t" \
  "v_mfma_f32_16x16x32_bf16 v[136:139], v[80:83], v[12:15], v[136:139]\n\t" \
  "s_waitcnt lgkmcnt(6)\n\t" \
  "v_cndmask_b32_e64 v148, v39, v38, vcc\n\t" \
  "v_mfma_f32_16x16x32_bf16 v[140:143], v[92:95], v[8:11], v[140:143]\n\t" \
  "ds_read_b64 v[0:1], v24 offset:4096\n\t" \
  "v_mfma_f32_16x16x32_bf16 v[140:143], v[88:91], v[12:15], v[140:143]\n\t" \
  "ds_read_b64 v[2:3], v25 offset:4096\n\t" \
  "v_mfma_f32_16x16x32_bf16 v[144:147], v[100:103], v[8:11], v[144:147]\n\t" \
  "s_waitcnt lgkmcnt(5)\n\t" \
  "v_cndmask_b32_e64 v152, v40, v39, vcc\n\t" \
  "v_mfma_f32_16x16x32_bf16 v[144:147], v[96:99], v[12:15], v[144:147]\n\t" \
  "ds_read_b64 v[4:5], v26 offset:5120\n\t" \
  "v_mfma_f32_16x16x32_bf16 v[148:151], v[108:111], v[8:11], v[148:151]\n\t" \
  "s_waitcnt lgkmcnt(3)\n\t" \
  "v_cndmask_b32_e64 v156, v41, v40, vcc\n\t" \
  "v_mfma_f32_16x16x32_bf16 v[148:151], v[104:107], v[12:15], v[148:151]\n\t" \
  "ds_read_b64 v[6:7], v27 offset:5120\n\t" \
  "v_mfma_f32_16x16x32_bf16 v[152:155], v[116:119], v[8:11], v[152:155]\n\t" \
  "ds_bpermute_b32 v34, v32, v131\n\t" \
  "v_mfma_f32_16x16x32_bf16 v[152:155], v[112:115], v[12:15], v[152:155]\n\t" \
  "v_mov_b32 v131, v130\n\t" \
  "v_mov_b32 v130, v129\n\t" \
  "v_mfma_f32_16x16x32_bf16 v[156:159], v[124:127], v[8:11], v[156:159]\n\t" \
  "ds_read_b64 v[8:9], v28 offset:6144\n\t" \
  "v_mov_b32 v129, v128\n\t" \
  "v_mfma_f32_16x16x32_bf16 v[156:159], v[120:123], v[12:15], v[156:159]\n\t" \
  "ds_read_b64 v[10:11], v29 offset:6144\n\t" \
  "v_mfma_f32_16x16x32_bf16 v[132:135], v[68:71], v[16:19], v[132:135]\n\t" \
  "ds_read_b64 v[12:13], v30 offset:7168\n\t" \
  "v_mfma_f32_16x16x32_bf16 v[132:135], v[64:67], v[20:23], v[132:135]\n\t" \
  "ds_read_b64 v[14:15], v31 offset:7168\n\t" \
  "v_mfma_f32_16x16x32_bf16 v[136:139], v[76:79], v[16:19], v[136:139]\n\t" \
  "v_mfma_f32_16x16x32_bf16 v[136:139], v[72:75], v[20:23], v[136:139]\n\t" \
  "v_mfma_f32_16x16x32_bf16 v[140:143], v[84:87], v[16:19], v[140:143]\n\t" \
  "v_mfma_f32_16x16x32_bf16 v[140:143], v[80:83], v[20:23], v[140:143]\n\t" \
  "v_mfma_f32_16x16x32_bf16 v[144:147], v[92:95], v[16:19], v[144:147]\n\t" \
  "v_mfma_f32_16x16x32_bf16 v[144:147], v[88:91], v[20:23], v[144:147]\n\t" \
  "s_waitcnt lgkmcnt(4)\n\t" \
  "v_cndmask_b32_e64 v128, v34, 0, vcc\n\t" \
  "v_mfma_f32_16x16x32_bf16 v[148:151], v[100:103], v[16:19], v[148:151]\n\t" \
  "ds_bpermute_b32 v35, v32, v135\n\t" \
  "v_mfma_f32_16x16x32_bf16 v[148:151], v[96:99], v[20:23], v[148:151]\n\t" \
  "v_mov_b32 v135, v134\n\t" \
  "v_mov_b32 v134, v133\n\t" \
  "v_mfma_f32_16x16x32_bf16 v[152:155], v[108:111], v[16:19], v[152:155]\n\t" \
  "v_mov_b32 v133, v132\n\t" \
  "v_mfma_f32_16x16x32_bf16 v[152:155], v[104:107], v[20:23], v[152:155]\n\t" \
  "v_mfma_f32_16x16x32_bf16 v[156:159], v[116:119], v[16:19], v[156:159]\n\t" \
  "ds_read_b64 v[16:17], v24 offset:8192\n\t" \
  "v_mfma_f32_16x16x32_bf16 v[156:159], v[112:115], v[20:23], v[156:159]\n\t" \
  "ds_read_b64 v[18:19], v25 offset:8192\n\t" \
  "v_mfma_f32_16x16x32_bf16 v[136:139], v[68:71], v[0:3], v[136:139]\n\t" \
  "ds_read_b64 v[20:21], v26 offset:9216\n\t" \
  "v_mfma_f32_16x16x32_bf16 v[136:139], v[64:67], v[4:7], v[136:139]\n\t" \
  "ds_read_b64 v[22:23], v27 offset:9216\n\t" \
  "v_mfma_f32_16x16x32_bf16 v[140:143], v[76:79], v[0:3], v[140:143]\n\t" \
  "v_mfma_f32_16x16x32_bf16 v[140:143], v[72:75], v[4:7], v[140:143]\n\t" \
  "v_mfma_f32_16x16x32_bf16 v[144:147], v[84:87], v[0:3], v[144:147]\n\t" \
  "v_mfma_f32_16x16x32_bf16 v[144:147], v[80:83], v[4:7], v[144:147]\n\t" \
  "s_waitcnt lgkmcnt(4)\n\t" \
  "v_cndmask_b32_e64 v132, v35, v34, vcc\n\t" \
  "v_mfma_f32_16x16x32_bf16 v[148:151], v[92:95], v[0:3], v[148:151]\n\t" \
  "ds_bpermute_b32 v36, v32, v139\n\t" \
  "v_mfma_f32_16x16x32_bf16 v[148:151], v[88:91], v[4:7], v[148:151]\n\t" \
  "v_mov_b32 v139, v138\n\t" \
  "v_mov_b32 v138, v137\n\t" \
  "v_mfma_f32_16x16x32_bf16 v[152:155], v[100:103], v[0:3], v[152:155]\n\t" \
  "v_mov_b32 v137, v136\n\t" \
  "v_mfma_f32_16x16x32_bf16 v[152:155], v[96:99], v[4:7], v[152:155]\n\t" \
  "v_mfma_f32_16x16x32_bf16 v[156:159], v[108:111], v[0:3], v[156:159]\n\t" \
  "ds_read_b64 v[0:1], v28 offset:10240\n\t" \
  "v_mfma_f32_16x16x32_bf16 v[156:159], v[104:107], v[4:7], v[156:159]\n\t" \
  "ds_read_b64 v[2:3], v29 offset:10240\n\t" \
  "v_mfma_f32_16x16x32_bf16 v[140:143], v[68:71], v[8:11], v[140:143]\n\t" \
  "ds_read_b64 v[4:5], v30 offset:11264\n\t" \
  "v_mfma_f32_16x16x32_bf16 v[140:143], v[64:67], v[12:15], v[140:143]\n\t" \
  "ds_read_b64 v[6:7], v31 offset:11264\n\t" \
  "v_mfma_f32_16x16x32_bf16 v[144:147], v[76:79], v[8:11], v[144:147]\n\t" \
  "v_mfma_f32_16x16x32_bf16 v[144:147], v[72:75], v[12:15], v[144:147]\n\t" \
  "v_mfma_f32_16x16x32_bf16 v[148:151], v[84:87], v[8:11], v[148:151]\n\t" \
  "v_mfma_f32_16x16x32_bf16 v[148:151], v[80:83], v[12:15], v[148:151]\n\t" \
  "s_waitcnt lgkmcnt(4)\n\t" \
  "v_cndmask_b32_e64 v136, v36, v35, vcc\n\t" \
  "v_mfma_f32_16x16x32_bf16 v[152:155], v[92:95], v[8:11], v[152:155]\n\t" \
  "ds_bpermute_b32 v37, v32, v143\n\t" \
  "v_mfma_f32_16x16x32_bf16 v[152:155], v[88:91], v[12:15], v[152:155]\n\t" \
  "v_mov_b32 v143, v142\n\t" \
  "v_mov_b32 v142, v141\n\t" \
  "v_mfma_f32_16x16x32_bf16 v[156:159], v[100:103], v[8:11], v[156:159]\n\t" \
  "ds_read_b64 v[8:9], v24 offset:12288\n\t" \
  "v_mov_b32 v141, v140\n\t" \
  "v_mfma_f32_16x16x32_bf16 v[156:159], v[96:99], v[12:15], v[156:159]\n\t" \
  "ds_read_b64 v[10:11], v25 offset:12288\n\t" \
  "v_mfma_f32_16x16x32_bf16 v[144:147], v[68:71], v[16:19], v[144:147]\n\t" \
  "ds_read_b64 v[12:13], v26 offset:13312\n\t" \
  "v_mfma_f32_16x16x32_bf16 v[144:147], v[64:67], v[20:23], v[144:147]\n\t" \
  "ds_read_b64 v[14:15], v27 offset:13312\n\t" \
  "v_mfma_f32_16x16x32_bf16 v[148:151], v[76:79], v[16:19], v[148:151]\n\t" \
  "v_mfma_f32_16x16x32_bf16 v[148:151], v[72:75], v[20:23], v[148:151]\n\t" \
  "v_mfma_f32_16x16x32_bf16 v[152:155], v[84:87], v[16:19], v[152:155]\n\t" \
  "v_mfma_f32_16x16x32_bf16 v[152:155], v[80:83], v[20:23], v[152:155]\n\t" \
  "v_mfma_f32_16x16x32_bf16 v[156:159], v[92:95], v[16:19], v[156:159]\n\t" \
  "ds_read_b64 v[16:17], v28 offset:14336\n\t" \
  "v_mfma_f32_16x16x32_bf16 v[156:159], v[88:91], v[20:23], v[156:159]\n\t" \
  "ds_read_b64 v[18:19], v29 offset:14336\n\t" \
  "s_waitcnt lgkmcnt(6)\n\t" \
  "v_mfma_f32_16x16x32_bf16 v[148:151], v[68:71], v[0:3], v[148:151]\n\t" \
  "ds_read_b64 v[20:21], v30 offset:15360\n\t" \
  "v_cndmask_b32_e64 v140, v37, v36, vcc\n\t" \
  "v_mfma_f32_16x16x32_bf16 v[148:151], v[64:67], v[4:7], v[148:151]\n\t" \
  "ds_read_b64 v[22:23], v31 offset:15360\n\t" \
  "v_mfma_f32_16x16x32_bf16 v[152:155], v[76:79], v[0:3], v[152:155]\n\t" \
  "ds_bpermute_b32 v38, v32, v147\n\t" \
  "v_mfma_f32_16x16x32_bf16 v[152:155], v[72:75], v[4:7], v[152:155]\n\t" \
  "v_mov_b32 v147, v146\n\t" \
  "v_mov_b32 v146, v145\n\t" \
  "v_mfma_f32_16x16x32_bf16 v[156:159], v[84:87], v[0:3], v[156:159]\n\t" \
  "ds_read_b64 v[0:1], v30 offset:0\n\t" \
  "ds_read_b64 v[2:3], v31 offset:0\n\t" \
  "v_mfma_f32_16x16x32_bf16 v[156:159], v[80:83], v[4:7], v[156:159]\n\t" \
  "ds_read_b64 v[4:5], v24 offset:1024\n\t" \
  "ds_read_b64 v[6:7], v25 offset:1024\n\t" \
  "s_waitcnt lgkmcnt(9)\n\t" \
  "v_mfma_f32_16x16x32_bf16 v[152:155], v[68:71], v[8:11], v[152:155]\n\t" \
  "ds_bpermute_b32 v39, v32, v151\n\t" \
  "v_mov_b32 v145, v144\n\t" \
  "v_mfma_f32_16x16x32_bf16 v[152:155], v[64:67], v[12:15], v[152:155]\n\t" \
  "v_mov_b32 v151, v150\n\t" \
  "v_mov_b32 v150, v149\n\t" \
  "v_mfma_f32_16x16x32_bf16 v[156:159], v[76:79], v[8:11], v[156:159]\n\t" \
  "ds_read_b64 v[8:9], v26 offset:2048\n\t" \
  "v_mov_b32 v149, v148\n\t" \
  "v_mfma_f32_16x16x32_bf16 v[156:159], v[72:75], v[12:15], v[156:159]\n\t" \
  "ds_read_b64 v[10:11], v27 offset:2048\n\t" \
  "s_waitcnt lgkmcnt(10)\n\t" \
  "v_mfma_f32_16x16x32_bf16 v[156:159], v[68:71], v[16:19], v[156:159]\n\t" \
  "ds_bpermute_b32 v40, v32, v155\n\t" \
  "s_waitcnt lgkmcnt(9)\n\t" \
  "v_mfma_f32_16x16x32_bf16 v[156:159], v[64:67], v[20:23], v[156:159]\n\t" \
  "ds_read_b64 v[12:13], v28 offset:3072\n\t" \
  "s_waitcnt lgkmcnt(7)\n\t" \
  "v_mfma_f32_16x16x32_bf16 v[128:131], v[68:71], v[0:3], v[128:131]\n\t" \
  "ds_read_b64 v[14:15], v29 offset:3072\n\t" \
  "s_waitcnt lgkmcnt(6)\n\t" \
  "v_mfma_f32_16x16x32_bf16 v[128:131], v[64:67], v[4:7], v[128:131]\n\t" \
  "v_cndmask_b32_e64 v144, v38, v37, vcc\n\t" \
  "v_mov_b32 v155, v154\n\t" \
  "v_mfma_f32_16x16x32_bf16 v[132:135], v[76:79], v[0:3], v[132:135]\n\t" \
  "ds_bpermute_b32 v41, v32, v159\n\t" \
  "v_mov_b32 v154, v153\n\t" \
  "v_mfma_f32_16x16x32_bf16 v[132:135], v[72:75], v[4:7], v[132:135]\n\t" \
  "v_mov_b32 v153, v152\n\t" \
  "v_mov_b32 v159, v158\n\t" \
  "v_mfma_f32_16x16x32_bf16 v[136:139], v[84:87], v[0:3], v[136:139]\n\t" \
  "v_mov_b32 v158, v157\n\t" \
  "v_mov_b32 v157, v156\n\t" \
  "v_mfma_f32_16x16x32_bf16 v[136:139], v[80:83], v[4:7], v[136:139]\n\t" \
  "s_waitcnt lgkmcnt(6)\n\t" \
  "v_cndmask_b32_e64 v148, v39, v38, vcc\n\t" \
  "v_mfma_f32_16x16x32_bf16 v[140:143], v[92:95], v[0:3], v[140:143]\n\t" \
  "ds_read_b64 v[16:17], v30 offset:4096\n\t" \
  "v_mfma_f32_16x16x32_bf16 v[140:143], v[88:91], v[4:7], v[140:143]\n\t" \
  "ds_read_b64 v[18:19], v31 offset:4096\n\t" \
  "v_mfma_f32_16x16x32_bf16 v[144:147], v[100:103], v[0:3], v[144:147]\n\t" \
  "s_waitcnt lgkmcnt(5)\n\t" \
  "v_cndmask_b32_e64 v152, v40, v39, vcc\n\t" \
  "v_mfma_f32_16x16x32_bf16 v[144:147], v[96:99], v[4:7], v[144:147]\n\t" \
  "ds_read_b64 v[20:21], v24 offset:5120\n\t" \
  "v_mfma_f32_16x16x32_bf16 v[148:151], v[108:111], v[0:3], v[148:151]\n\t" \
  "s_waitcnt lgkmcnt(3)\n\t" \
  "v_cndmask_b32_e64 v156, v41, v40, vcc\n\t" \
  "v_mfma_f32_16x16x32_bf16 v[148:151], v[104:107], v[4:7], v[148:151]\n\t" \
  "v_cvt_pk_bf16_f32 v34, v128, v129\n\t" \
  "v_cvt_pk_bf16_f32 v35, v130, v131\n\t" \
  "v_mfma_f32_16x16x32_bf16 v[152:155], v[116:119], v[0:3], v[152:155]\n\t" \
  "ds_read_b64 v[22:23], v25 offset:5120\n\t" \
  "v_mfma_f32_16x16x32_bf16 v[152:155], v[112:115], v[4:7], v[152:155]\n\t" \
  "v_mfma_f32_16x16x32_bf16 v[156:159], v[124:127], v[0:3], v[156:159]\n\t" \
  "ds_read_b64 v[0:1], v26 offset:6144\n\t" \
  "v_mfma_f32_16x16x32_bf16 v[156:159], v[120:123], v[4:7], v[156:159]\n\t" \
  "ds_read_b64 v[2:3], v27 offset:6144\n\t" \
  "v_mfma_f32_16x16x32_bf16 v[132:135], v[68:71], v[8:11], v[132:135]\n\t" \
  "ds_read_b64 v[4:5], v28 offset:7168\n\t" \
  "v_mfma_f32_16x16x32_bf16 v[132:135], v[64:67], v[12:15], v[132:135]\n\t" \
  "ds_read_b64 v[6:7], v29 offset:7168\n\t" \
  "v_mfma_f32_16x16x32_bf16 v[136:139], v[76:79], v[8:11], v[136:139]\n\t" \
  "v_mfma_f32_16x16x32_bf16 v[136:139], v[72:75], v[12:15], v[136:139]\n\t" \
  "v_mfma_f32_16x16x32_bf16 v[140:143], v[84:87], v[8:11], v[140:143]\n\t" \
  "v_mfma_f32_16x16x32_bf16 v[140:143], v[80:83], v[12:15], v[140:143]\n\t" \
  "v_mfma_f32_16x16x32_bf16 v[144:147], v[92:95], v[8:11], v[144:147]\n\t" \
  "v_mfma_f32_16x16x32_bf16 v[144:147], v[88:91], v[12:15], v[144:147]\n\t" \
  "v_mfma_f32_16x16x32_bf16 v[148:151], v[100:103], v[8:11], v[148:151]\n\t" \
  "v_cvt_pk_bf16_f32 v36, v132, v133\n\t" \
  "v_cvt_pk_bf16_f32 v37, v134, v135\n\t" \
  "v_mfma_f32_16x16x32_bf16 v[148:151], v[96:99], v[12:15], v[148:151]\n\t" \
  "v_mfma_f32_16x16x32_bf16 v[152:155], v[108:111], v[8:11], v[152:155]\n\t" \
  "v_permlane16_swap_b32 v34, v36\n\t" \
  "v_permlane16_swap_b32 v35, v37\n\t" \
  "v_mfma_f32_16x16x32_bf16 v[152:155], v[104:107], v[12:15], v[152:155]\n\t" \
  "global_store_dwordx4 v42, v[34:37], %[yb] offset:0 sc1\n\t" \
  "v_mfma_f32_16x16x32_bf16 v[156:159], v[116:119], v[8:11], v[156:159]\n\t" \
  "ds_read_b64 v[8:9], v30 offset:8192\n\t" \
  "v_mfma_f32_16x16x32_bf16 v[156:159], v[112:115], v[12:15], v[156:159]\n\t" \
  "ds_read_b64 v[10:11], v31 offset:8192\n\t" \
  "s_waitcnt lgkmcnt(2)\n\t" \
  "v_mfma_f32_16x16x32_bf16 v[136:139], v[68:71], v[16:19], v[136:139]\n\t" \
  "ds_read_b64 v[12:13], v24 offset:9216\n\t" \
  "v_mfma_f32_16x16x32_bf16 v[136:139], v[64:67], v[20:23], v[136:139]\n\t" \
  "ds_read_b64 v[14:15], v25 offset:9216\n\t" \
  "v_mfma_f32_16x16x32_bf16 v[140:143], v[76:79], v[16:19], v[140:143]\n\t" \
  "v_mfma_f32_16x16x32_bf16 v[140:143], v[72:75], v[20:23], v[140:143]\n\t" \
  "v_mfma_f32_16x16x32_bf16 v[144:147], v[84:87], v[16:19], v[144:147]\n\t" \
  "s_mov_b64 exec, 1\n\t" \
  "ds_write_b32 v33, v33\n\t" \
  "s_mov_b64 exec, -1\n\t" \
  "v_mfma_f32_16x16x32_bf16 v[144:147], v[80:83], v[20:23], v[144:147]\n\t" \
  "v_cvt_pk_bf16_f32 v38, v136, v137\n\t" \
  "v_cvt_pk_bf16_f32 v39, v138, v139\n\t" \
  "v_mfma_f32_16x16x32_bf16 v[148:151], v[92:95], v[16:19], v[148:151]\n\t" \
  "v_mfma_f32_16x16x32_bf16 v[148:151], v[88:91], v[20:23], v[148:151]\n\t" \
  "v_mfma_f32_16x16x32_bf16 v[152:155], v[100:103], v[16:19], v[152:155]\n\t" \
  "v_mfma_f32_16x16x32_bf16 v[152:155], v[96:99], v[20:23], v[152:155]\n\t" \
  "v_mfma_f32_16x16x32_bf16 v[156:159], v[108:111], v[16:19], v[156:159]\n\t" \
  "ds_read_b64 v[16:17], v26 offset:10240\n\t" \
  "v_mfma_f32_16x16x32_bf16 v[156:159], v[104:107], v[20:23], v[156:159]\n\t" \
  "ds_read_b64 v[18:19], v27 offset:10240\n\t" \
  "v_mfma_f32_16x16x32_bf16 v[140:143], v[68:71], v[0:3], v[140:143]\n\t" \
  "ds_read_b64 v[20:21], v28 offset:11264\n\t" \
  "v_mfma_f32_16x16x32_bf16 v[140:143], v[64:67], v[4:7], v[140:143]\n\t" \
  "ds_read_b64 v[22:23], v29 offset:11264\n\t" \
  "v_mfma_f32_16x16x32_bf16 v[144:147], v[76:79], v[0:3], v[144:147]\n\t" \
  "v_mfma_f32_16x16x32_bf16 v[144:147], v[72:75], v[4:7], v[144:147]\n\t" \
  "v_mfma_f32_16x16x32_bf16 v[148:151], v[84:87], v[0:3], v[148:151]\n\t" \
  "v_mfma_f32_16x16x32_bf16 v[148:151], v[80:83], v[4:7], v[148:151]\n\t" \
  "v_mfma_f32_16x16x32_bf16 v[152:155], v[92:95], v[0:3], v[152:155]\n\t" \
  "v_mfma_f32_16x16x32_bf16 v[152:155], v[88:91], v[4:7], v[152:155]\n\t" \
  "v_mfma_f32_16x16x32_bf16 v[156:159], v[100:103], v[0:3], v[156:159]\n\t" \
  "v_cvt_pk_bf16_f32 v40, v140, v141\n\t" \
  "v_cvt_pk_bf16_f32 v41, v142, v143\n\t" \
  "v_mfma_f32_16x16x32_bf16 v[156:159], v[96:99], v[4:7], v[156:159]\n\t" \
  "ds_read_b64 v[0:1], v30 offset:12288\n\t" \
  "s_waitcnt lgkmcnt(6)\n\t" \
  "v_mfma_f32_16x16x32_bf16 v[144:147], v[68:71], v[8:11], v[144:147]\n\t" \
  "v_permlane16_swap_b32 v38, v40\n\t" \
  "ds_read_b64 v[2:3], v31 offset:12288\n\t" \
  "v_mfma_f32_16x16x32_bf16 v[144:147], v[64:67], v[12:15], v[144:147]\n\t" \
  "v_permlane16_swap_b32 v39, v41\n\t" \
  "ds_read_b64 v[4:5], v24 offset:13312\n\t" \
  "v_mfma_f32_16x16x32_bf16 v[148:151], v[76:79], v[8:11], v[148:151]\n\t" \
  "ds_read_b64 v[6:7], v25 offset:13312\n\t" \
  "v_mfma_f32_16x16x32_bf16 v[148:151], v[72:75], v[12:15], v[148:151]\n\t" \
  "global_store_dwordx4 v42, v[38:41], %[yb] offset:1024 sc1\n\t" \
  "v_mfma_f32_16x16x32_bf16 v[152:155], v[84:87], v[8:11], v[152:155]\n\t" \
  "v_mfma_f32_16x16x32_bf16 v[152:155], v[80:83], v[12:15], v[152:155]\n\t" \
  "v_cvt_pk_bf16_f32 v34, v144, v145\n\t" \
  "v_cvt_pk_bf16_f32 v35, v146, v147\n\t" \
  "v_mfma_f32_16x16x32_bf16 v[156:159], v[92:95], v[8:11], v[156:159]\n\t" \
  "ds_read_b64 v[8:9], v26 offset:14336\n\t" \
  "v_mfma_f32_16x16x32_bf16 v[156:159], v[88:91], v[12:15], v[156:159]\n\t" \
  "ds_read_b64 v[10:11], v27 offset:14336\n\t" \
  "s_waitcnt lgkmcnt(6)\n\t" \
  "v_mfma_f32_16x16x32_bf16 v[148:151], v[68:71], v[16:19], v[148:151]\n\t" \
  "ds_read_b64 v[12:13], v28 offset:15360\n\t" \
  "v_mfma_f32_16x16x32_bf16 v[148:151], v[64:67], v[20:23], v[148:151]\n\t" \
  "ds_read_b64 v[14:15], v29 offset:15360\n\t" \
  "v_mfma_f32_16x16x32_bf16 v[152:155], v[76:79], v[16:19], v[152:155]\n\t" \
  "v_mfma_f32_16x16x32_bf16 v[152:155], v[72:75], v[20:23], v[152:155]\n\t" \
  "v_mfma_f32_16x16x32_bf16 v[156:159], v[84:87], v[16:19], v[156:159]\n\t" \
  "v_mfma_f32_16x16x32_bf16 v[156:159], v[80:83], v[20:23], v[156:159]\n\t" \
  "s_waitcnt lgkmcnt(4)\n\t" \
  "v_mfma_f32_16x16x32_bf16 v[152:155], v[68:71], v[0:3], v[152:155]\n\t" \
  "v_mfma_f32_16x16x32_bf16 v[152:155], v[64:67], v[4:7], v[152:155]\n\t" \
  "v_cvt_pk_bf16_f32 v36, v148, v149\n\t" \
  "v_cvt_pk_bf16_f32 v37, v150, v151\n\t" \
  "v_mfma_f32_16x16x32_bf16 v[156:159], v[76:79], v[0:3], v[156:159]\n\t" \
  "v_mfma_f32_16x16x32_bf16 v[156:159], v[72:75], v[4:7], v[156:159]\n\t" \
  "v_permlane16_swap_b32 v34, v36\n\t" \
  "s_waitcnt lgkmcnt(2)\n\t" \
  "v_mfma_f32_16x16x32_bf16 v[156:159], v[68:71], v[8:11], v[156:159]\n\t" \
  "v_permlane16_swap_b32 v35, v37\n\t" \
  "s_waitcnt lgkmcnt(0)\n\t" \
  "v_mfma_f32_16x16x32_bf16 v[156:159], v[64:67], v[12:15], v[156:159]\n\t" \
  "global_store_dwordx4 v42, v[34:37], %[yb] offset:2048 sc1\n\t" \
  "s_waitcnt lgkmcnt(0)\n\t" \
  "s_nop 4\n\t" \
  "v_cvt_pk_bf16_f32 v38, v152, v153\n\t" \
  "v_cvt_pk_bf16_f32 v39, v154, v155\n\t" \
  "v_cvt_pk_bf16_f32 v40, v156, v157\n\t" \
  "v_cvt_pk_bf16_f32 v41, v158, v159\n\t" \
  "s_nop 1\n\t" \
  "v_permlane16_swap_b32 v38, v40\n\t" \
  "v_permlane16_swap_b32 v39, v41\n\t" \
  "global_store_dwordx4 v42, v[38:41], %[yb] offset:3072 sc1\n\t" \
  "s_nop 1\n\t"
#define CV_Q 1
#define CV_CLOB "vcc", "v0", "v1", "v2", "v3", "v4", "v5", "v6", "v7", "v8", "v9", "v10", "v11", "v12", "v13", "v14", "v15", "v16", "v17", "v18", "v19", "v20", "v21", "v22", "v23", "v34", "v35", "v36", "v37", "v38", "v39"

#define G3_ASM \
  "v_readlane_b32 s64, v96, 0\n\t" \
  "v_readlane_b32 s65, v96, 1\n\t" \
  "v_readlane_b32 s66, v96, 2\n\t" \
  "v_readlane_b32 s67, v96, 3\n\t" \
  "v_readlane_b32 s68, v96, 4\n\t" \
  "v_readlane_b32 s69, v96, 5\n\t" \
  "v_readlane_b32 s70, v96, 6\n\t" \
  "v_readlane_b32 s71, v96, 7\n\t" \
  "v_readlane_b32 s72, v96, 8\n\t" \
  "v_readlane_b32 s73, v96, 9\n\t" \
  "v_readlane_b32 s74, v96, 10\n\t" \
  "v_readlane_b32 s75, v96, 11\n\t" \
  "v_readlane_b32 s76, v96, 12\n\t" \
  "v_readlane_b32 s77, v96, 13\n\t" \
  "v_readlane_b32 s78, v96, 14\n\t" \
  "v_readlane_b32 s79, v96, 15\n\t" \
  "v_add_u32 v97, %[go], v165\n\t" \
  "s_branch QG_%=\n\t" \
  "QA14_%=:\n\t" \
  "v_mov_b32 v165, %[sy]\n\t" \
  "s_sleep 1\n\t" \
  "ds_read_b32 v165, v165\n\t" \
  "s_addk_i32 %[cn], 1\n\t" \
  "s_cmp_gt_u32 %[cn], 0x400000\n\t" \
  "s_cbranch_scc1 QC14_%=\n\t" \
  "s_waitcnt lgkmcnt(0)\n\t" \
  "v_cmp_lt_u32 vcc, 15, v165\n\t" \
  "s_cbranch_vccz QA14_%=\n\t" \
  "QC14_%=:\n\t" \
  "s_waitcnt lgkmcnt(0)\n\t" \
  "s_branch QB14_%=\n\t" \
  "QA7_%=:\n\t" \
  "v_mov_b32 v165, %[sy]\n\t" \
  "s_sleep 1\n\t" \
  "ds_read_b32 v165, v165\n\t" \
  "s_addk_i32 %[cn], 1\n\t" \
  "s_cmp_gt_u32 %[cn], 0x400000\n\t" \
  "s_cbranch_scc1 QC7_%=\n\t" \
  "s_waitcnt lgkmcnt(0)\n\t" \
  "v_cmp_lt_u32 vcc, 13, v165\n\t" \
  "s_cbranch_vccz QA7_%=\n\t" \
  "QC7_%=:\n\t" \
  "s_waitcnt lgkmcnt(0)\n\t" \
  "s_branch QB7_%=\n\t" \
  "QS_%=:\n\t" \
  "s_sleep 1\n\t" \
  "QG_%=:\n\t" \
  "ds_read_b32 v98, v165\n\t" \
  "ds_read_b32 v99, v97\n\t" \
  "s_addk_i32 %[cn], 1\n\t" \
  "s_cmp_gt_u32 %[cn], 0x400000\n\t" \
  "s_cselect_b32 %[t2], 16, 0\n\t" \
  "s_waitcnt lgkmcnt(0)\n\t" \
  "v_readfirstlane_b32 %[t0], v98\n\t" \
  "v_readfirstlane_b32 %[t1], v99\n\t" \
  "s_max_u32 %[t0], %[t0], %[t2]\n\t" \
  "s_or_b32 %[t1], %[t1], %[t2]\n\t" \
  "s_min_u32 %[t2], %[t0], 5\n\t" \
  "s_cmp_ge_u32 %[iss], %[t2]\n\t" \
  "s_cbranch_scc1 QD_%=\n\t" \
  "s_cmp_eq_u32 %[iss], 1\n\t" \
  "s_cbranch_scc1 QL1_%=\n\t" \
  "s_cmp_eq_u32 %[iss], 2\n\t" \
  "s_cbranch_scc1 QL2_%=\n\t" \
  "s_cmp_eq_u32 %[iss], 3\n\t" \
  "s_cbranch_scc1 QL3_%=\n\t" \
  "s_cmp_eq_u32 %[iss], 4\n\t" \
  "s_cbranch_scc1 QL4_%=\n\t" \
  "QL0_%=:\n\t" \
  "buffer_load_dwordx4 v[0:3], v164, %[ob], s64 offen offset:0 sc1\n\t" \
  "buffer_load_dwordx4 v[4:7], v164, %[ob], s64 offen offset:1024 sc1\n\t" \
  "buffer_load_dwordx4 v[8:11], v164, %[ob], s64 offen offset:2048 sc1\n\t" \
  "buffer_load_dwordx4 v[12:15], v164, %[ob], s64 offen offset:3072 sc1\n\t" \
  "s_mov_b32 %[iss], 1\n\t" \
  "s_cmp_le_u32 %[t2], 1\n\t" \
  "s_cbranch_scc1 QD_%=\n\t" \
  "QL1_%=:\n\t" \
  "buffer_load_dwordx4 v[16:19], v164, %[ob], s65 offen offset:0 sc1\n\t" \
  "buffer_load_dwordx4 v[20:23], v164, %[ob], s65 offen offset:1024 sc1\n\t" \
  "buffer_load_dwordx4 v[24:27], v164, %[ob], s65 offen offset:2048 sc1\n\t" \
  "buffer_load_dwordx4 v[28:31], v164, %[ob], s65 offen offset:3072 sc1\n\t" \
  "s_mov_b32 %[iss], 2\n\t" \
  "s_cmp_le_u32 %[t2], 2\n\t" \
  "s_cbranch_scc1 QD_%=\n\t" \
  "QL2_%=:\n\t" \
  "buffer_load_dwordx4 v[32:35], v164, %[ob], s66 offen offset:0 sc1\n\t" \
  "buffer_load_dwordx4 v[36:39], v164, %[ob], s66 offen offset:1024 sc1\n\t" \
  "buffer_load_dwordx4 v[40:43], v164, %[ob], s66 offen offset:2048 sc1\n\t" \
  "buffer_load_dwordx4 v[44:47], v164, %[ob], s66 offen offset:3072 sc1\n\t" \
  "s_mov_b32 %[iss], 3\n\t" \
  "s_cmp_le_u32 %[t2], 3\n\t" \
  "s_cbranch_scc1 QD_%=\n\t" \
  "QL3_%=:\n\t" \
  "buffer_load_dwordx4 v[48:51], v164, %[ob], s67 offen offset:0 sc1\n\t" \
  "buffer_load_dwordx4 v[52:55], v164, %[ob], s67 offen offset:1024 sc1\n\t" \
  "buffer_load_dwordx4 v[56:59], v164, %[ob], s67 offen offset:2048 sc1\n\t" \
  "buffer_load_dwordx4 v[60:63], v164, %[ob], s67 offen offset:3072 sc1\n\t" \
  "s_mov_b32 %[iss], 4\n\t" \
  "s_cmp_le_u32 %[t2], 4\n\t" \
  "s_cbranch_scc1 QD_%=\n\t" \
  "QL4_%=:\n\t" \
  "buffer_load_dwordx4 v[64:67], v164, %[ob], s68 offen offset:0 sc1\n\t" \
  "buffer_load_dwordx4 v[68:71], v164, %[ob], s68 offen offset:1024 sc1\n\t" \
  "buffer_load_dwordx4 v[72:75], v164, %[ob], s68 offen offset:2048 sc1\n\t" \
  "buffer_load_dwordx4 v[76:79], v164, %[ob], s68 offen offset:3072 sc1\n\t" \
  "s_mov_b32 %[iss], 5\n\t" \
  "QD_%=:\n\t" \
  "s_cmp_lt_u32 %[t0], 7\n\t" \
  "s_cbranch_scc1 QS_%=\n\t" \
  "s_cmp_eq_u32 %[t1], 0\n\t" \
  "s_cbranch_scc1 QS_%=\n\t" \
  "s_setprio 0\n\t" \
  "ds_read_b64_tr_b16 v[96:97], v160 offset:0\n\t" \
  "ds_read_b64_tr_b16 v[98:99], v160 offset:2048\n\t" \
  "ds_read_b64_tr_b16 v[100:101], v161 offset:0\n\t" \
  "ds_read_b64_tr_b16 v[102:103], v161 offset:2048\n\t" \
  "ds_read_b64_tr_b16 v[104:105], v162 offset:0\n\t" \
  "ds_read_b64_tr_b16 v[106:107], v162 offset:2048\n\t" \
  "ds_read_b64_tr_b16 v[108:109], v163 offset:0\n\t" \
  "ds_read_b64_tr_b16 v[110:111], v163 offset:2048\n\t" \
  "ds_read_b64_tr_b16 v[112:113], v160 offset:4096\n\t" \
  "ds_read_b64_tr_b16 v[114:115], v160 offset:6144\n\t" \
  "ds_read_b64_tr_b16 v[116:117], v161 offset:4096\n\t" \
  "ds_read_b64_tr_b16 v[118:119], v161 offset:6144\n\t" \
  "ds_read_b64_tr_b16 v[120:121], v162 offset:4096\n\t" \
  "ds_read_b64_tr_b16 v[122:123], v162 offset:6144\n\t" \
  "s_waitcnt vmcnt(18) lgkmcnt(10)\n\t" \
  "v_mfma_f32_16x16x32_bf16 v[128:131], v[96:99], v[0:3], 0\n\t" \
  "ds_read_b64_tr_b16 v[124:125], v163 offset:4096\n\t" \
  "v_mfma_f32_16x16x32_bf16 v[144:147], v[96:99], v[4:7], 0\n\t" \
  "ds_read_b64_tr_b16 v[126:127], v163 offset:6144\n\t" \
  "v_mfma_f32_16x16x32_bf16 v[132:135], v[100:103], v[0:3], 0\n\t" \
  "ds_read_b64_tr_b16 v[96:97], v160 offset:8192\n\t" \
  "v_mfma_f32_16x16x32_bf16 v[148:151], v[100:103], v[4:7], 0\n\t" \
  "ds_read_b64_tr_b16 v[98:99], v160 offset:10240\n\t" \
  "s_waitcnt lgkmcnt(10)\n\t" \
  "v_mfma_f32_16x16x32_bf16 v[136:139], v[104:107], v[0:3], 0\n\t" \
  "ds_read_b64_tr_b16 v[100:101], v161 offset:8192\n\t" \
  "v_mfma_f32_16x16x32_bf16 v[152:155], v[104:107], v[4:7], 0\n\t" \
  "ds_read_b64_tr_b16 v[102:103], v161 offset:10240\n\t" \
  "v_mfma_f32_16x16x32_bf16 v[140:143], v[108:111], v[0:3], 0\n\t" \
  "ds_read_b64_tr_b16 v[104:105], v162 offset:8192\n\t" \
  "v_mfma_f32_16x16x32_bf16 v[156:159], v[108:111], v[4:7], 0\n\t" \
  "ds_read_b64_tr_b16 v[106:107], v162 offset:10240\n\t" \
  "s_waitcnt vmcnt(16) lgkmcnt(10)\n\t" \
  "v_mfma_f32_16x16x32_bf16 v[128:131], v[112:115], v[8:11], v[128:131]\n\t" \
  "ds_read_b64_tr_b16 v[108:109], v163 offset:8192\n\t" \
  "buffer_load_dwordx4 v[80:83], v164, %[ob], s69 offen offset:0 sc1\n\t" \
  "v_mfma_f32_16x16x32_bf16 v[144:147], v[112:115], v[12:15], v[144:147]\n\t" \
  "ds_read_b64_tr_b16 v[110:111], v163 offset:10240\n\t" \
  "v_mfma_f32_16x16x32_bf16 v[132:135], v[116:119], v[8:11], v[132:135]\n\t" \
  "ds_read_b64_tr_b16 v[112:113], v160 offset:12288\n\t" \
  "v_mfma_f32_16x16x32_bf16 v[148:151], v[116:119], v[12:15], v[148:151]\n\t" \
  "ds_read_b64_tr_b16 v[114:115], v160 offset:14336\n\t" \
  "s_waitcnt lgkmcnt(10)\n\t" \
  "v_mfma_f32_16x16x32_bf16 v[136:139], v[120:123], v[8:11], v[136:139]\n\t" \
  "ds_read_b64_tr_b16 v[116:117], v161 offset:12288\n\t" \
  "buffer_load_dwordx4 v[84:87], v164, %[ob], s69 offen offset:1024 sc1\n\t" \
  "v_mfma_f32_16x16x32_bf16 v[152:155], v[120:123], v[12:15], v[152:155]\n\t" \
  "ds_read_b64_tr_b16 v[118:119], v161 offset:14336\n\t" \
  "v_mfma_f32_16x16x32_bf16 v[140:143], v[124:127], v[8:11], v[140:143]\n\t" \
  "ds_read_b64_tr_b16 v[120:121], v162 offset:12288\n\t" \
  "v_mfma_f32_16x16x32_bf16 v[156:159], v[124:127], v[12:15], v[156:159]\n\t" \
  "ds_read_b64_tr_b16 v[122:123], v162 offset:14336\n\t" \
  "s_waitcnt vmcnt(16) lgkmcnt(10)\n\t" \
  "v_mfma_f32_16x16x32_bf16 v[128:131], v[96:99], v[16:19], v[128:131]\n\t" \
  "ds_read_b64_tr_b16 v[124:125], v163 offset:12288\n\t" \
  "buffer_load_dwordx4 v[88:91], v164, %[ob], s69 offen offset:2048 sc1\n\t" \
  "v_mfma_f32_16x16x32_bf16 v[144:147], v[96:99], v[20:23], v[144:147]\n\t" \
  "ds_read_b64_tr_b16 v[126:127], v163 offset:14336\n\t" \
  "v_mfma_f32_16x16x32_bf16 v[132:135], v[100:103], v[16:19], v[132:135]\n\t" \
  "ds_read_b64_tr_b16 v[96:97], v160 offset:16384\n\t" \
  "v_mfma_f32_16x16x32_bf16 v[148:151], v[100:103], v[20:23], v[148:151]\n\t" \
  "ds_read_b64_tr_b16 v[98:99], v160 offset:18432\n\t" \
  "s_waitcnt lgkmcnt(10)\n\t" \
  "v_mfma_f32_16x16x32_bf16 v[136:139], v[104:107], v[16:19], v[136:139]\n\t" \
  "ds_read_b64_tr_b16 v[100:101], v161 offset:16384\n\t" \
  "v_mfma_f32_16x16x32_bf16 v[152:155], v[104:107], v[20:23], v[152:155]\n\t" \
  "ds_read_b64_tr_b16 v[102:103], v161 offset:18432\n\t" \
  "buffer_load_dwordx4 v[92:95], v164, %[ob], s69 offen offset:3072 sc1\n\t" \
  "v_mfma_f32_16x16x32_bf16 v[140:143], v[108:111], v[16:19], v[140:143]\n\t" \
  "ds_read_b64_tr_b16 v[104:105], v162 offset:16384\n\t" \
  "v_mfma_f32_16x16x32_bf16 v[156:159], v[108:111], v[20:23], v[156:159]\n\t" \
  "ds_read_b64_tr_b16 v[106:107], v162 offset:18432\n\t" \
  "s_waitcnt vmcnt(16) lgkmcnt(10)\n\t" \
  "v_mfma_f32_16x16x32_bf16 v[128:131], v[112:115], v[24:27], v[128:131]\n\t" \
  "ds_read_b64_tr_b16 v[108:109], v163 offset:16384\n\t" \
  "v_mfma_f32_16x16x32_bf16 v[144:147], v[112:115], v[28:31], v[144:147]\n\t" \
  "ds_read_b64_tr_b16 v[110:111], v163 offset:18432\n\t" \
  "buffer_load_dwordx4 v[0:3], v164, %[ob], s70 offen offset:0 sc1\n\t" \
  "v_mfma_f32_16x16x32_bf16 v[132:135], v[116:119], v[24:27], v[132:135]\n\t" \
  "ds_read_b64_tr_b16 v[112:113], v160 offset:20480\n\t" \
  "v_mfma_f32_16x16x32_bf16 v[148:151], v[116:119], v[28:31], v[148:151]\n\t" \
  "ds_read_b64_tr_b16 v[114:115], v160 offset:22528\n\t" \
  "s_waitcnt lgkmcnt(10)\n\t" \
  "v_mfma_f32_16x16x32_bf16 v[136:139], v[120:123], v[24:27], v[136:139]\n\t" \
  "ds_read_b64_tr_b16 v[116:117], v161 offset:20480\n\t" \
  "v_mfma_f32_16x16x32_bf16 v[152:155], v[120:123], v[28:31], v[152:155]\n\t" \
  "ds_read_b64_tr_b16 v[118:119], v161 offset:22528\n\t" \
  "v_mfma_f32_16x16x32_bf16 v[140:143], v[124:127], v[24:27], v[140:143]\n\t" \
  "ds_read_b64_tr_b16 v[120:121], v162 offset:20480\n\t" \
  "buffer_load_dwordx4 v[4:7], v164, %[ob], s70 offen offset:1024 sc1\n\t" \
  "v_mfma_f32_16x16x32_bf16 v[156:159], v[124:127], v[28:31], v[156:159]\n\t" \
  "ds_read_b64_tr_b16 v[122:123], v162 offset:22528\n\t" \
  "s_waitcnt vmcnt(16) lgkmcnt(10)\n\t" \
  "v_mfma_f32_16x16x32_bf16 v[128:131], v[96:99], v[32:35], v[128:131]\n\t" \
  "ds_read_b64_tr_b16 v[124:125], v163 offset:20480\n\t" \
  "ds_read_b32 v165, v165\n\t" \
  "v_mfma_f32_16x16x32_bf16 v[144:147], v[96:99], v[36:39], v[144:147]\n\t" \
  "ds_read_b64_tr_b16 v[126:127], v163 offset:22528\n\t" \
  "v_mfma_f32_16x16x32_bf16 v[132:135], v[100:103], v[32:35], v[132:135]\n\t" \
  "ds_read_b64_tr_b16 v[96:97], v160 offset:24576\n\t" \
  "buffer_load_dwordx4 v[8:11], v164, %[ob], s70 offen offset:2048 sc1\n\t" \
  "v_mfma_f32_16x16x32_bf16 v[148:151], v[100:103], v[36:39], v[148:151]\n\t" \
  "ds_read_b64_tr_b16 v[98:99], v160 offset:26624\n\t" \
  "s_waitcnt lgkmcnt(11)\n\t" \
  "v_mfma_f32_16x16x32_bf16 v[136:139], v[104:107], v[32:35], v[136:139]\n\t" \
  "ds_read_b64_tr_b16 v[100:101], v161 offset:24576\n\t" \
  "v_mfma_f32_16x16x32_bf16 v[152:155], v[104:107], v[36:39], v[152:155]\n\t" \
  "ds_read_b64_tr_b16 v[102:103], v161 offset:26624\n\t" \
  "v_mfma_f32_16x16x32_bf16 v[140:143], v[108:111], v[32:35], v[140:143]\n\t" \
  "ds_read_b64_tr_b16 v[104:105], v162 offset:24576\n\t" \
  "v_mfma_f32_16x16x32_bf16 v[156:159], v[108:111], v[36:39], v[156:159]\n\t" \
  "ds_read_b64_tr_b16 v[106:107], v162 offset:26624\n\t" \
  "s_waitcnt vmcnt(15) lgkmcnt(11)\n\t" \
  "v_mfma_f32_16x16x32_bf16 v[128:131], v[112:115], v[40:43], v[128:131]\n\t" \
  "ds_read_b64_tr_b16 v[108:109], v163 offset:24576\n\t" \
  "buffer_load_dwordx4 v[12:15], v164, %[ob], s70 offen offset:3072 sc1\n\t" \
  "v_mfma_f32_16x16x32_bf16 v[144:147], v[112:115], v[44:47], v[144:147]\n\t" \
  "ds_read_b64_tr_b16 v[110:111], v163 offset:26624\n\t" \
  "s_waitcnt lgkmcnt(9)\n\t" \
  "v_mfma_f32_16x16x32_bf16 v[132:135], v[116:119], v[40:43], v[132:135]\n\t" \
  "ds_read_b64_tr_b16 v[112:113], v160 offset:28672\n\t" \
  "v_cmp_lt_u32 vcc, 13, v165\n\t" \
  "v_mfma_f32_16x16x32_bf16 v[148:151], v[116:119], v[44:47], v[148:151]\n\t" \
  "ds_read_b64_tr_b16 v[114:115], v160 offset:30720\n\t" \
  "s_cbranch_vccz QA7_%=\n\t" \
  "QB7_%=:\n\t" \
  "s_waitcnt lgkmcnt(10)\n\t" \
  "v_mfma_f32_16x16x32_bf16 v[136:139], v[120:123], v[40:43], v[136:139]\n\t" \
  "ds_read_b64_tr_b16 v[116:117], v161 offset:28672\n\t" \
  "buffer_load_dwordx4 v[16:19], v164, %[ob], s71 offen offset:0 sc1\n\t" \
  "v_mfma_f32_16x16x32_bf16 v[152:155], v[120:123], v[44:47], v[152:155]\n\t" \
  "ds_read_b64_tr_b16 v[118:119], v161 offset:30720\n\t" \
  "v_mov_b32 v165, %[sy]\n\t" \
  "v_mfma_f32_16x16x32_bf16 v[140:143], v[124:127], v[40:43], v[140:143]\n\t" \
  "ds_read_b64_tr_b16 v[120:121], v162 offset:28672\n\t" \
  "v_mfma_f32_16x16x32_bf16 v[156:159], v[124:127], v[44:47], v[156:159]\n\t" \
  "ds_read_b64_tr_b16 v[122:123], v162 offset:30720\n\t" \
  "s_waitcnt vmcnt(15) lgkmcnt(10)\n\t" \
  "v_mfma_f32_16x16x32_bf16 v[128:131], v[96:99], v[48:51], v[128:131]\n\t" \
  "ds_read_b64_tr_b16 v[124:125], v163 offset:28672\n\t" \
  "buffer_load_dwordx4 v[20:23], v164, %[ob], s71 offen offset:1024 sc1\n\t" \
  "v_mfma_f32_16x16x32_bf16 v[144:147], v[96:99], v[52:55], v[144:147]\n\t" \
  "ds_read_b64_tr_b16 v[126:127], v163 offset:30720\n\t" \
  "v_mfma_f32_16x16x32_bf16 v[132:135], v[100:103], v[48:51], v[132:135]\n\t" \
  "ds_read_b64_tr_b16 v[96:97], v160 offset:32768\n\t" \
  "v_mfma_f32_16x16x32_bf16 v[148:151], v[100:103], v[52:55], v[148:151]\n\t" \
  "ds_read_b64_tr_b16 v[98:99], v160 offset:34816\n\t" \
  "s_waitcnt lgkmcnt(10)\n\t" \
  "v_mfma_f32_16x16x32_bf16 v[136:139], v[104:107], v[48:51], v[136:139]\n\t" \
  "ds_read_b64_tr_b16 v[100:101], v161 offset:32768\n\t" \
  "buffer_load_dwordx4 v[24:27], v164, %[ob], s71 offen offset:2048 sc1\n\t" \
  "v_mfma_f32_16x16x32_bf16 v[152:155], v[104:107], v[52:55], v[152:155]\n\t" \
  "ds_read_b64_tr_b16 v[102:103], v161 offset:34816\n\t" \
  "v_mfma_f32_16x16x32_bf16 v[140:143], v[108:111], v[48:51], v[140:143]\n\t" \
  "ds_read_b64_tr_b16 v[104:105], v162 offset:32768\n\t" \
  "v_mfma_f32_16x16x32_bf16 v[156:159], v[108:111], v[52:55], v[156:159]\n\t" \
  "ds_read_b64_tr_b16 v[106:107], v162 offset:34816\n\t" \
  "s_waitcnt vmcnt(15) lgkmcnt(10)\n\t" \
  "v_mfma_f32_16x16x32_bf16 v[128:131], v[112:115], v[56:59], v[128:131]\n\t" \
  "ds_read_b64_tr_b16 v[108:109], v163 offset:32768\n\t" \
  "v_mfma_f32_16x16x32_bf16 v[144:147], v[112:115], v[60:63], v[144:147]\n\t" \
  "ds_read_b64_tr_b16 v[110:111], v163 offset:34816\n\t" \
  "buffer_load_dwordx4 v[28:31], v164, %[ob], s71 offen offset:3072 sc1\n\t" \
  "v_mfma_f32_16x16x32_bf16 v[132:135], v[116:119], v[56:59], v[132:135]\n\t" \
  "ds_read_b64_tr_b16 v[112:113], v160 offset:36864\n\t" \
  "v_mfma_f32_16x16x32_bf16 v[148:151], v[116:119], v[60:63], v[148:151]\n\t" \
  "ds_read_b64_tr_b16 v[114:115], v160 offset:38912\n\t" \
  "s_waitcnt lgkmcnt(10)\n\t" \
  "v_mfma_f32_16x16x32_bf16 v[136:139], v[120:123], v[56:59], v[136:139]\n\t" \
  "ds_read_b64_tr_b16 v[116:117], v161 offset:36864\n\t" \
  "v_mfma_f32_16x16x32_bf16 v[152:155], v[120:123], v[60:63], v[152:155]\n\t" \
  "ds_read_b64_tr_b16 v[118:119], v161 offset:38912\n\t" \
  "buffer_load_dwordx4 v[32:35], v164, %[ob], s72 offen offset:0 sc1\n\t" \
  "v_mfma_f32_16x16x32_bf16 v[140:143], v[124:127], v[56:59], v[140:143]\n\t" \
  "ds_read_b64_tr_b16 v[120:121], v162 offset:36864\n\t" \
  "v_mfma_f32_16x16x32_bf16 v[156:159], v[124:127], v[60:63], v[156:159]\n\t" \
  "ds_read_b64_tr_b16 v[122:123], v162 offset:38912\n\t" \
  "s_waitcnt vmcnt(15) lgkmcnt(10)\n\t" \
  "v_mfma_f32_16x16x32_bf16 v[128:131], v[96:99], v[64:67], v[128:131]\n\t" \
  "ds_read_b64_tr_b16 v[124:125], v163 offset:36864\n\t" \
  "v_mfma_f32_16x16x32_bf16 v[144:147], v[96:99], v[68:71], v[144:147]\n\t" \
  "ds_read_b64_tr_b16 v[126:127], v163 offset:38912\n\t" \
  "v_mfma_f32_16x16x32_bf16 v[132:135], v[100:103], v[64:67], v[132:135]\n\t" \
  "ds_read_b64_tr_b16 v[96:97], v160 offset:40960\n\t" \
  "buffer_load_dwordx4 v[36:39], v164, %[ob], s72 offen offset:1024 sc1\n\t" \
  "v_mfma_f32_16x16x32_bf16 v[148:151], v[100:103], v[68:71], v[148:151]\n\t" \
  "ds_read_b64_tr_b16 v[98:99], v160 offset:43008\n\t" \
  "s_waitcnt lgkmcnt(10)\n\t" \
  "v_mfma_f32_16x16x32_bf16 v[136:139], v[104:107], v[64:67], v[136:139]\n\t" \
  "ds_read_b64_tr_b16 v[100:101], v161 offset:40960\n\t" \
  "v_mfma_f32_16x16x32_bf16 v[152:155], v[104:107], v[68:71], v[152:155]\n\t" \
  "ds_read_b64_tr_b16 v[102:103], v161 offset:43008\n\t" \
  "v_mfma_f32_16x16x32_bf16 v[140:143], v[108:111], v[64:67], v[140:143]\n\t" \
  "ds_read_b64_tr_b16 v[104:105], v162 offset:40960\n\t" \
  "buffer_load_dwordx4 v[40:43], v164, %[ob], s72 offen offset:2048 sc1\n\t" \
  "v_mfma_f32_16x16x32_bf16 v[156:159], v[108:111], v[68:71], v[156:159]\n\t" \
  "ds_read_b64_tr_b16 v[106:107], v162 offset:43008\n\t" \
  "s_waitcnt vmcnt(15) lgkmcnt(10)\n\t" \
  "v_mfma_f32_16x16x32_bf16 v[128:131], v[112:115], v[72:75], v[128:131]\n\t" \
  "ds_read_b64_tr_b16 v[108:109], v163 offset:40960\n\t" \
  "v_mfma_f32_16x16x32_bf16 v[144:147], v[112:115], v[76:79], v[144:147]\n\t" \
  "ds_read_b64_tr_b16 v[110:111], v163 offset:43008\n\t" \
  "v_mfma_f32_16x16x32_bf16 v[132:135], v[116:119], v[72:75], v[132:135]\n\t" \
  "ds_read_b64_tr_b16 v[112:113], v160 offset:45056\n\t" \
  "buffer_load_dwordx4 v[44:47], v164, %[ob], s72 offen offset:3072 sc1\n\t" \
  "v_mfma_f32_16x16x32_bf16 v[148:151], v[116:119], v[76:79], v[148:151]\n\t" \
  "ds_read_b64_tr_b16 v[114:115], v160 offset:47104\n\t" \
  "s_waitcnt lgkmcnt(10)\n\t" \
  "v_mfma_f32_16x16x32_bf16 v[136:139], v[120:123], v[72:75], v[136:139]\n\t" \
  "ds_read_b64_tr_b16 v[116:117], v161 offset:45056\n\t" \
  "v_mfma_f32_16x16x32_bf16 v[152:155], v[120:123], v[76:79], v[152:155]\n\t" \
  "ds_read_b64_tr_b16 v[118:119], v161 offset:47104\n\t" \
  "v_mfma_f32_16x16x32_bf16 v[140:143], v[124:127], v[72:75], v[140:143]\n\t" \
  "ds_read_b64_tr_b16 v[120:121], v162 offset:45056\n\t" \
  "v_mfma_f32_16x16x32_bf16 v[156:159], v[124:127], v[76:79], v[156:159]\n\t" \
  "ds_read_b64_tr_b16 v[122:123], v162 offset:47104\n\t" \
  "s_waitcnt vmcnt(14) lgkmcnt(10)\n\t" \
  "v_mfma_f32_16x16x32_bf16 v[128:131], v[96:99], v[80:83], v[128:131]\n\t" \
  "ds_read_b64_tr_b16 v[124:125], v163 offset:45056\n\t" \
  "buffer_load_dwordx4 v[48:51], v164, %[ob], s73 offen offset:0 sc1\n\t" \
  "v_mfma_f32_16x16x32_bf16 v[144:147], v[96:99], v[84:87], v[144:147]\n\t" \
  "ds_read_b64_tr_b16 v[126:127], v163 offset:47104\n\t" \
  "v_mfma_f32_16x16x32_bf16 v[132:135], v[100:103], v[80:83], v[132:135]\n\t" \
  "ds_read_b64_tr_b16 v[96:97], v160 offset:49152\n\t" \
  "v_mfma_f32_16x16x32_bf16 v[148:151], v[100:103], v[84:87], v[148:151]\n\t" \
  "ds_read_b64_tr_b16 v[98:99], v160 offset:51200\n\t" \
  "s_waitcnt lgkmcnt(10)\n\t" \
  "v_mfma_f32_16x16x32_bf16 v[136:139], v[104:107], v[80:83], v[136:139]\n\t" \
  "ds_read_b64_tr_b16 v[100:101], v161 offset:49152\n\t" \
  "buffer_load_dwordx4 v[52:55], v164, %[ob], s73 offen offset:1024 sc1\n\t" \
  "v_mfma_f32_16x16x32_bf16 v[152:155], v[104:107], v[84:87], v[152:155]\n\t" \
  "ds_read_b64_tr_b16 v[102:103], v161 offset:51200\n\t" \
  "v_mfma_f32_16x16x32_bf16 v[140:143], v[108:111], v[80:83], v[140:143]\n\t" \
  "ds_read_b64_tr_b16 v[104:105], v162 offset:49152\n\t" \
  "v_mfma_f32_16x16x32_bf16 v[156:159], v[108:111], v[84:87], v[156:159]\n\t" \
  "ds_read_b64_tr_b16 v[106:107], v162 offset:51200\n\t" \
  "s_waitcnt vmcnt(14) lgkmcnt(10)\n\t" \
  "v_mfma_f32_16x16x32_bf16 v[128:131], v[112:115], v[88:91], v[128:131]\n\t" \
  "ds_read_b64_tr_b16 v[108:109], v163 offset:49152\n\t" \
  "buffer_load_dwordx4 v[56:59], v164, %[ob], s73 offen offset:2048 sc1\n\t" \
  "v_mfma_f32_16x16x32_bf16 v[144:147], v[112:115], v[92:95], v[144:147]\n\t" \
  "ds_read_b64_tr_b16 v[110:111], v163 offset:51200\n\t" \
  "v_mfma_f32_16x16x32_bf16 v[132:135], v[116:119], v[88:91], v[132:135]\n\t" \
  "ds_read_b64_tr_b16 v[112:113], v160 offset:53248\n\t" \
  "v_mfma_f32_16x16x32_bf16 v[148:151], v[116:119], v[92:95], v[148:151]\n\t" \
  "ds_read_b64_tr_b16 v[114:115], v160 offset:55296\n\t" \
  "s_waitcnt lgkmcnt(10)\n\t" \
  "v_mfma_f32_16x16x32_bf16 v[136:139], v[120:123], v[88:91], v[136:139]\n\t" \
  "ds_read_b64_tr_b16 v[116:117], v161 offset:53248\n\t" \
  "buffer_load_dwordx4 v[60:63], v164, %[ob], s73 offen offset:3072 sc1\n\t" \
  "v_mfma_f32_16x16x32_bf16 v[152:155], v[120:123], v[92:95], v[152:155]\n\t" \
  "ds_read_b64_tr_b16 v[118:119], v161 offset:55296\n\t" \
  "v_mfma_f32_16x16x32_bf16 v[140:143], v[124:127], v[88:91], v[140:143]\n\t" \
  "ds_read_b64_tr_b16 v[120:121], v162 offset:53248\n\t" \
  "v_mfma_f32_16x16x32_bf16 v[156:159], v[124:127], v[92:95], v[156:159]\n\t" \
  "ds_read_b64_tr_b16 v[122:123], v162 offset:55296\n\t" \
  "s_waitcnt vmcnt(14) lgkmcnt(10)\n\t" \
  "v_mfma_f32_16x16x32_bf16 v[128:131], v[96:99], v[0:3], v[128:131]\n\t" \
  "ds_read_b64_tr_b16 v[124:125], v163 offset:53248\n\t" \
  "v_mfma_f32_16x16x32_bf16 v[144:147], v[96:99], v[4:7], v[144:147]\n\t" \
  "ds_read_b64_tr_b16 v[126:127], v163 offset:55296\n\t" \
  "buffer_load_dwordx4 v[64:67], v164, %[ob], s74 offen offset:0 sc1\n\t" \
  "v_mfma_f32_16x16x32_bf16 v[132:135], v[100:103], v[0:3], v[132:135]\n\t" \
  "ds_read_b64_tr_b16 v[96:97], v160 offset:57344\n\t" \
  "v_mfma_f32_16x16x32_bf16 v[148:151], v[100:103], v[4:7], v[148:151]\n\t" \
  "ds_read_b64_tr_b16 v[98:99], v160 offset:59392\n\t" \
  "s_waitcnt lgkmcnt(10)\n\t" \
  "v_mfma_f32_16x16x32_bf16 v[136:139], v[104:107], v[0:3], v[136:139]\n\t" \
  "ds_read_b64_tr_b16 v[100:101], v161 offset:57344\n\t" \
  "v_mfma_f32_16x16x32_bf16 v[152:155], v[104:107], v[4:7], v[152:155]\n\t" \
  "ds_read_b64_tr_b16 v[102:103], v161 offset:59392\n\t" \
  "buffer_load_dwordx4 v[68:71], v164, %[ob], s74 offen offset:1024 sc1\n\t" \
  "v_mfma_f32_16x16x32_bf16 v[140:143], v[108:111], v[0:3], v[140:143]\n\t" \
  "ds_read_b64_tr_b16 v[104:105], v162 offset:57344\n\t" \
  "v_mfma_f32_16x16x32_bf16 v[156:159], v[108:111], v[4:7], v[156:159]\n\t" \
  "ds_read_b64_tr_b16 v[106:107], v162 offset:59392\n\t" \
  "s_waitcnt vmcnt(14) lgkmcnt(10)\n\t" \
  "v_mfma_f32_16x16x32_bf16 v[128:131], v[112:115], v[8:11], v[128:131]\n\t" \
  "ds_read_b64_tr_b16 v[108:109], v163 offset:57344\n\t" \
  "v_mfma_f32_16x16x32_bf16 v[144:147], v[112:115], v[12:15], v[144:147]\n\t" \
  "ds_read_b64_tr_b16 v[110:111], v163 offset:59392\n\t" \
  "v_mfma_f32_16x16x32_bf16 v[132:135], v[116:119], v[8:11], v[132:135]\n\t" \
  "ds_read_b64_tr_b16 v[112:113], v160 offset:61440\n\t" \
  "buffer_load_dwordx4 v[72:75], v164, %[ob], s74 offen offset:2048 sc1\n\t" \
  "v_mfma_f32_16x16x32_bf16 v[148:151], v[116:119], v[12:15], v[148:151]\n\t" \
  "ds_read_b64_tr_b16 v[114:115], v160 offset:63488\n\t" \
  "s_waitcnt lgkmcnt(10)\n\t" \
  "v_mfma_f32_16x16x32_bf16 v[136:139], v[120:123], v[8:11], v[136:139]\n\t" \
  "ds_read_b64_tr_b16 v[116:117], v161 offset:61440\n\t" \
  "v_mfma_f32_16x16x32_bf16 v[152:155], v[120:123], v[12:15], v[152:155]\n\t" \
  "ds_read_b64_tr_b16 v[118:119], v161 offset:63488\n\t" \
  "v_mfma_f32_16x16x32_bf16 v[140:143], v[124:127], v[8:11], v[140:143]\n\t" \
  "ds_read_b64_tr_b16 v[120:121], v162 offset:61440\n\t" \
  "buffer_load_dwordx4 v[76:79], v164, %[ob], s74 offen offset:3072 sc1\n\t" \
  "v_mfma_f32_16x16x32_bf16 v[156:159], v[124:127], v[12:15], v[156:159]\n\t" \
  "ds_read_b64_tr_b16 v[122:123], v162 offset:63488\n\t" \
  "s_waitcnt vmcnt(14) lgkmcnt(10)\n\t" \
  "v_mfma_f32_16x16x32_bf16 v[128:131], v[96:99], v[16:19], v[128:131]\n\t" \
  "ds_read_b64_tr_b16 v[124:125], v163 offset:61440\n\t" \
  "v_mfma_f32_16x16x32_bf16 v[144:147], v[96:99], v[20:23], v[144:147]\n\t" \
  "ds_read_b64_tr_b16 v[126:127], v163 offset:63488\n\t" \
  "v_mfma_f32_16x16x32_bf16 v[132:135], v[100:103], v[16:19], v[132:135]\n\t" \
  "v_add_u32 v160, 0x10000, v160\n\t" \
  "ds_read_b64_tr_b16 v[96:97], v160 offset:0\n\t" \
  "v_mfma_f32_16x16x32_bf16 v[148:151], v[100:103], v[20:23], v[148:151]\n\t" \
  "ds_read_b64_tr_b16 v[98:99], v160 offset:2048\n\t" \
  "s_waitcnt lgkmcnt(10)\n\t" \
  "v_mfma_f32_16x16x32_bf16 v[136:139], v[104:107], v[16:19], v[136:139]\n\t" \
  "v_add_u32 v161, 0x10000, v161\n\t" \
  "ds_read_b64_tr_b16 v[100:101], v161 offset:0\n\t" \
  "buffer_load_dwordx4 v[80:83], v164, %[ob], s75 offen offset:0 sc1\n\t" \
  "v_mfma_f32_16x16x32_bf16 v[152:155], v[104:107], v[20:23], v[152:155]\n\t" \
  "ds_read_b64_tr_b16 v[102:103], v161 offset:2048\n\t" \
  "v_mfma_f32_16x16x32_bf16 v[140:143], v[108:111], v[16:19], v[140:143]\n\t" \
  "v_add_u32 v162, 0x10000, v162\n\t" \
  "ds_read_b64_tr_b16 v[104:105], v162 offset:0\n\t" \
  "v_mfma_f32_16x16x32_bf16 v[156:159], v[108:111], v[20:23], v[156:159]\n\t" \
  "ds_read_b64_tr_b16 v[106:107], v162 offset:2048\n\t" \
  "s_waitcnt vmcnt(13) lgkmcnt(10)\n\t" \
  "v_mfma_f32_16x16x32_bf16 v[128:131], v[112:115], v[24:27], v[128:131]\n\t" \
  "v_add_u32 v163, 0x10000, v163\n\t" \
  "ds_read_b64_tr_b16 v[108:109], v163 offset:0\n\t" \
  "buffer_load_dwordx4 v[84:87], v164, %[ob], s75 offen offset:1024 sc1\n\t" \
  "v_mfma_f32_16x16x32_bf16 v[144:147], v[112:115], v[28:31], v[144:147]\n\t" \
  "ds_read_b64_tr_b16 v[110:111], v163 offset:2048\n\t" \
  "v_mfma_f32_16x16x32_bf16 v[132:135], v[116:119], v[24:27], v[132:135]\n\t" \
  "ds_read_b64_tr_b16 v[112:113], v160 offset:4096\n\t" \
  "v_mfma_f32_16x16x32_bf16 v[148:151], v[116:119], v[28:31], v[148:151]\n\t" \
  "ds_read_b64_tr_b16 v[114:115], v160 offset:6144\n\t" \
  "s_waitcnt lgkmcnt(10)\n\t" \
  "v_mfma_f32_16x16x32_bf16 v[136:139], v[120:123], v[24:27], v[136:139]\n\t" \
  "ds_read_b64_tr_b16 v[116:117], v161 offset:4096\n\t" \
  "buffer_load_dwordx4 v[88:91], v164, %[ob], s75 offen offset:2048 sc1\n\t" \
  "v_mfma_f32_16x16x32_bf16 v[152:155], v[120:123], v[28:31], v[152:155]\n\t" \
  "ds_read_b64_tr_b16 v[118:119], v161 offset:6144\n\t" \
  "v_mfma_f32_16x16x32_bf16 v[140:143], v[124:127], v[24:27], v[140:143]\n\t" \
  "ds_read_b64_tr_b16 v[120:121], v162 offset:4096\n\t" \
  "v_mfma_f32_16x16x32_bf16 v[156:159], v[124:127], v[28:31], v[156:159]\n\t" \
  "ds_read_b64_tr_b16 v[122:123], v162 offset:6144\n\t" \
  "s_waitcnt vmcnt(13) lgkmcnt(10)\n\t" \
  "v_mfma_f32_16x16x32_bf16 v[128:131], v[96:99], v[32:35], v[128:131]\n\t" \
  "ds_read_b64_tr_b16 v[124:125], v163 offset:4096\n\t" \
  "buffer_load_dwordx4 v[92:95], v164, %[ob], s75 offen offset:3072 sc1\n\t" \
  "v_mfma_f32_16x16x32_bf16 v[144:147], v[96:99], v[36:39], v[144:147]\n\t" \
  "ds_read_b64_tr_b16 v[126:127], v163 offset:6144\n\t" \
  "v_mfma_f32_16x16x32_bf16 v[132:135], v[100:103], v[32:35], v[132:135]\n\t" \
  "ds_read_b64_tr_b16 v[96:97], v160 offset:8192\n\t" \
  "v_mfma_f32_16x16x32_bf16 v[148:151], v[100:103], v[36:39], v[148:151]\n\t" \
  "ds_read_b64_tr_b16 v[98:99], v160 offset:10240\n\t" \
  "s_waitcnt lgkmcnt(10)\n\t" \
  "v_mfma_f32_16x16x32_bf16 v[136:139], v[104:107], v[32:35], v[136:139]\n\t" \
  "ds_read_b64_tr_b16 v[100:101], v161 offset:8192\n\t" \
  "v_mfma_f32_16x16x32_bf16 v[152:155], v[104:107], v[36:39], v[152:155]\n\t" \
  "ds_read_b64_tr_b16 v[102:103], v161 offset:10240\n\t" \
  "buffer_load_dwordx4 v[0:3], v164, %[ob], s76 offen offset:0 sc1\n\t" \
  "v_mfma_f32_16x16x32_bf16 v[140:143], v[108:111], v[32:35], v[140:143]\n\t" \
  "ds_read_b64_tr_b16 v[104:105], v162 offset:8192\n\t" \
  "v_mfma_f32_16x16x32_bf16 v[156:159], v[108:111], v[36:39], v[156:159]\n\t" \
  "ds_read_b64_tr_b16 v[106:107], v162 offset:10240\n\t" \
  "s_waitcnt vmcnt(13) lgkmcnt(10)\n\t" \
  "v_mfma_f32_16x16x32_bf16 v[128:131], v[112:115], v[40:43], v[128:131]\n\t" \
  "ds_read_b64_tr_b16 v[108:109], v163 offset:8192\n\t" \
  "v_mfma_f32_16x16x32_bf16 v[144:147], v[112:115], v[44:47], v[144:147]\n\t" \
  "ds_read_b64_tr_b16 v[110:111], v163 offset:10240\n\t" \
  "buffer_load_dwordx4 v[4:7], v164, %[ob], s76 offen offset:1024 sc1\n\t" \
  "v_mfma_f32_16x16x32_bf16 v[132:135], v[116:119], v[40:43], v[132:135]\n\t" \
  "ds_read_b64_tr_b16 v[112:113], v160 offset:12288\n\t" \
  "v_mfma_f32_16x16x32_bf16 v[148:151], v[116:119], v[44:47], v[148:151]\n\t" \
  "ds_read_b64_tr_b16 v[114:115], v160 offset:14336\n\t" \
  "s_waitcnt lgkmcnt(10)\n\t" \
  "v_mfma_f32_16x16x32_bf16 v[136:139], v[120:123], v[40:43], v[136:139]\n\t" \
  "ds_read_b64_tr_b16 v[116:117], v161 offset:12288\n\t" \
  "v_mfma_f32_16x16x32_bf16 v[152:155], v[120:123], v[44:47], v[152:155]\n\t" \
  "ds_read_b64_tr_b16 v[118:119], v161 offset:14336\n\t" \
  "buffer_load_dwordx4 v[8:11], v164, %[ob], s76 offen offset:2048 sc1\n\t" \
  "v_mfma_f32_16x16x32_bf16 v[140:143], v[124:127], v[40:43], v[140:143]\n\t" \
  "ds_read_b64_tr_b16 v[120:121], v162 offset:12288\n\t" \
  "v_mfma_f32_16x16x32_bf16 v[156:159], v[124:127], v[44:47], v[156:159]\n\t" \
  "ds_read_b64_tr_b16 v[122:123], v162 offset:14336\n\t" \
  "s_waitcnt vmcnt(13) lgkmcnt(10)\n\t" \
  "v_mfma_f32_16x16x32_bf16 v[128:131], v[96:99], v[48:51], v[128:131]\n\t" \
  "ds_read_b64_tr_b16 v[124:125], v163 offset:12288\n\t" \
  "v_mfma_f32_16x16x32_bf16 v[144:147], v[96:99], v[52:55], v[144:147]\n\t" \
  "ds_read_b64_tr_b16 v[126:127], v163 offset:14336\n\t" \
  "v_mfma_f32_16x16x32_bf16 v[132:135], v[100:103], v[48:51], v[132:135]\n\t" \
  "ds_read_b64_tr_b16 v[96:97], v160 offset:16384\n\t" \
  "buffer_load_dwordx4 v[12:15], v164, %[ob], s76 offen offset:3072 sc1\n\t" \
  "v_mfma_f32_16x16x32_bf16 v[148:151], v[100:103], v[52:55], v[148:151]\n\t" \
  "ds_read_b64_tr_b16 v[98:99], v160 offset:18432\n\t" \
  "s_waitcnt lgkmcnt(10)\n\t" \
  "v_mfma_f32_16x16x32_bf16 v[136:139], v[104:107], v[48:51], v[136:139]\n\t" \
  "ds_read_b64_tr_b16 v[100:101], v161 offset:16384\n\t" \
  "v_mfma_f32_16x16x32_bf16 v[152:155], v[104:107], v[52:55], v[152:155]\n\t" \
  "ds_read_b64_tr_b16 v[102:103], v161 offset:18432\n\t" \
  "v_mfma_f32_16x16x32_bf16 v[140:143], v[108:111], v[48:51], v[140:143]\n\t" \
  "ds_read_b64_tr_b16 v[104:105], v162 offset:16384\n\t" \
  "buffer_load_dwordx4 v[16:19], v164, %[ob], s77 offen offset:0 sc1\n\t" \
  "v_mfma_f32_16x16x32_bf16 v[156:159], v[108:111], v[52:55], v[156:159]\n\t" \
  "ds_read_b64_tr_b16 v[106:107], v162 offset:18432\n\t" \
  "s_waitcnt vmcnt(13) lgkmcnt(10)\n\t" \
  "v_mfma_f32_16x16x32_bf16 v[128:131], v[112:115], v[56:59], v[128:131]\n\t" \
  "ds_read_b64_tr_b16 v[108:109], v163 offset:16384\n\t" \
  "v_mfma_f32_16x16x32_bf16 v[144:147], v[112:115], v[60:63], v[144:147]\n\t" \
  "ds_read_b64_tr_b16 v[110:111], v163 offset:18432\n\t" \
  "v_mfma_f32_16x16x32_bf16 v[132:135], v[116:119], v[56:59], v[132:135]\n\t" \
  "ds_read_b64_tr_b16 v[112:113], v160 offset:20480\n\t" \
  "v_mfma_f32_16x16x32_bf16 v[148:151], v[116:119], v[60:63], v[148:151]\n\t" \
  "ds_read_b64_tr_b16 v[114:115], v160 offset:22528\n\t" \
  "s_waitcnt lgkmcnt(10)\n\t" \
  "v_mfma_f32_16x16x32_bf16 v[136:139], v[120:123], v[56:59], v[136:139]\n\t" \
  "ds_read_b64_tr_b16 v[116:117], v161 offset:20480\n\t" \
  "buffer_load_dwordx4 v[20:23], v164, %[ob], s77 offen offset:1024 sc1\n\t" \
  "ds_read_b32 v165, v165\n\t" \
  "v_mfma_f32_16x16x32_bf16 v[152:155], v[120:123], v[60:63], v[152:155]\n\t" \
  "ds_read_b64_tr_b16 v[118:119], v161 offset:22528\n\t" \
  "v_mfma_f32_16x16x32_bf16 v[140:143], v[124:127], v[56:59], v[140:143]\n\t" \
  "ds_read_b64_tr_b16 v[120:121], v162 offset:20480\n\t" \
  "v_mfma_f32_16x16x32_bf16 v[156:159], v[124:127], v[60:63], v[156:159]\n\t" \
  "ds_read_b64_tr_b16 v[122:123], v162 offset:22528\n\t" \
  "s_waitcnt vmcnt(12) lgkmcnt(11)\n\t" \
  "v_mfma_f32_16x16x32_bf16 v[128:131], v[96:99], v[64:67], v[128:131]\n\t" \
  "ds_read_b64_tr_b16 v[124:125], v163 offset:20480\n\t" \
  "buffer_load_dwordx4 v[24:27], v164, %[ob], s77 offen offset:2048 sc1\n\t" \
  "v_mfma_f32_16x16x32_bf16 v[144:147], v[96:99], v[68:71], v[144:147]\n\t" \
  "ds_read_b64_tr_b16 v[126:127], v163 offset:22528\n\t" \
  "v_mfma_f32_16x16x32_bf16 v[132:135], v[100:103], v[64:67], v[132:135]\n\t" \
  "ds_read_b64_tr_b16 v[96:97], v160 offset:24576\n\t" \
  "v_mfma_f32_16x16x32_bf16 v[148:151], v[100:103], v[68:71], v[148:151]\n\t" \
  "ds_read_b64_tr_b16 v[98:99], v160 offset:26624\n\t" \
  "s_waitcnt lgkmcnt(11)\n\t" \
  "v_mfma_f32_16x16x32_bf16 v[136:139], v[104:107], v[64:67], v[136:139]\n\t" \
  "ds_read_b64_tr_b16 v[100:101], v161 offset:24576\n\t" \
  "buffer_load_dwordx4 v[28:31], v164, %[ob], s77 offen offset:3072 sc1\n\t" \
  "v_mfma_f32_16x16x32_bf16 v[152:155], v[104:107], v[68:71], v[152:155]\n\t" \
  "ds_read_b64_tr_b16 v[102:103], v161 offset:26624\n\t" \
  "s_waitcnt lgkmcnt(9)\n\t" \
  "v_mfma_f32_16x16x32_bf16 v[140:143], v[108:111], v[64:67], v[140:143]\n\t" \
  "ds_read_b64_tr_b16 v[104:105], v162 offset:24576\n\t" \
  "v_cmp_lt_u32 vcc, 15, v165\n\t" \
  "v_mfma_f32_16x16x32_bf16 v[156:159], v[108:111], v[68:71], v[156:159]\n\t" \
  "ds_read_b64_tr_b16 v[106:107], v162 offset:26624\n\t" \
  "s_cbranch_vccz QA14_%=\n\t" \
  "QB14_%=:\n\t" \
  "s_waitcnt vmcnt(12) lgkmcnt(10)\n\t" \
  "v_mfma_f32_16x16x32_bf16 v[128:131], v[112:115], v[72:75], v[128:131]\n\t" \
  "ds_read_b64_tr_b16 v[108:109], v163 offset:24576\n\t" \
  "buffer_load_dwordx4 v[32:35], v164, %[ob], s78 offen offset:0 sc1\n\t" \
  "v_mfma_f32_16x16x32_bf16 v[144:147], v[112:115], v[76:79], v[144:147]\n\t" \
  "ds_read_b64_tr_b16 v[110:111], v163 offset:26624\n\t" \
  "v_mfma_f32_16x16x32_bf16 v[132:135], v[116:119], v[72:75], v[132:135]\n\t" \
  "ds_read_b64_tr_b16 v[112:113], v160 offset:28672\n\t" \
  "v_mfma_f32_16x16x32_bf16 v[148:151], v[116:119], v[76:79], v[148:151]\n\t" \
  "ds_read_b64_tr_b16 v[114:115], v160 offset:30720\n\t" \
  "s_waitcnt lgkmcnt(10)\n\t" \
  "v_mfma_f32_16x16x32_bf16 v[136:139], v[120:123], v[72:75], v[136:139]\n\t" \
  "ds_read_b64_tr_b16 v[116:117], v161 offset:28672\n\t" \
  "v_mfma_f32_16x16x32_bf16 v[152:155], v[120:123], v[76:79], v[152:155]\n\t" \
  "ds_read_b64_tr_b16 v[118:119], v161 offset:30720\n\t" \
  "buffer_load_dwordx4 v[36:39], v164, %[ob], s78 offen offset:1024 sc1\n\t" \
  "v_mfma_f32_16x16x32_bf16 v[140:143], v[124:127], v[72:75], v[140:143]\n\t" \
  "ds_read_b64_tr_b16 v[120:121], v162 offset:28672\n\t" \
  "v_mfma_f32_16x16x32_bf16 v[156:159], v[124:127], v[76:79], v[156:159]\n\t" \
  "ds_read_b64_tr_b16 v[122:123], v162 offset:30720\n\t" \
  "s_waitcnt vmcnt(12) lgkmcnt(10)\n\t" \
  "v_mfma_f32_16x16x32_bf16 v[128:131], v[96:99], v[80:83], v[128:131]\n\t" \
  "ds_read_b64_tr_b16 v[124:125], v163 offset:28672\n\t" \
  "v_mfma_f32_16x16x32_bf16 v[144:147], v[96:99], v[84:87], v[144:147]\n\t" \
  "ds_read_b64_tr_b16 v[126:127], v163 offset:30720\n\t" \
  "buffer_load_dwordx4 v[40:43], v164, %[ob], s78 offen offset:2048 sc1\n\t" \
  "v_mfma_f32_16x16x32_bf16 v[132:135], v[100:103], v[80:83], v[132:135]\n\t" \
  "ds_read_b64_tr_b16 v[96:97], v160 offset:32768\n\t" \
  "v_mfma_f32_16x16x32_bf16 v[148:151], v[100:103], v[84:87], v[148:151]\n\t" \
  "ds_read_b64_tr_b16 v[98:99], v160 offset:34816\n\t" \
  "s_waitcnt lgkmcnt(10)\n\t" \
  "v_mfma_f32_16x16x32_bf16 v[136:139], v[104:107], v[80:83], v[136:139]\n\t" \
  "ds_read_b64_tr_b16 v[100:101], v161 offset:32768\n\t" \
  "v_mfma_f32_16x16x32_bf16 v[152:155], v[104:107], v[84:87], v[152:155]\n\t" \
  "ds_read_b64_tr_b16 v[102:103], v161 offset:34816\n\t" \
  "v_mfma_f32_16x16x32_bf16 v[140:143], v[108:111], v[80:83], v[140:143]\n\t" \
  "ds_read_b64_tr_b16 v[104:105], v162 offset:32768\n\t" \
  "buffer_load_dwordx4 v[44:47], v164, %[ob], s78 offen offset:3072 sc1\n\t" \
  "v_mfma_f32_16x16x32_bf16 v[156:159], v[108:111], v[84:87], v[156:159]\n\t" \
  "ds_read_b64_tr_b16 v[106:107], v162 offset:34816\n\t" \
  "s_waitcnt vmcnt(12) lgkmcnt(10)\n\t" \
  "v_mfma_f32_16x16x32_bf16 v[128:131], v[112:115], v[88:91], v[128:131]\n\t" \
  "ds_read_b64_tr_b16 v[108:109], v163 offset:32768\n\t" \
  "v_mfma_f32_16x16x32_bf16 v[144:147], v[112:115], v[92:95], v[144:147]\n\t" \
  "ds_read_b64_tr_b16 v[110:111], v163 offset:34816\n\t" \
  "v_mfma_f32_16x16x32_bf16 v[132:135], v[116:119], v[88:91], v[132:135]\n\t" \
  "ds_read_b64_tr_b16 v[112:113], v160 offset:36864\n\t" \
  "buffer_load_dwordx4 v[48:51], v164, %[ob], s79 offen offset:0 sc1\n\t" \
  "v_mfma_f32_16x16x32_bf16 v[148:151], v[116:119], v[92:95], v[148:151]\n\t" \
  "ds_read_b64_tr_b16 v[114:115], v160 offset:38912\n\t" \
  "s_waitcnt lgkmcnt(10)\n\t" \
  "v_mfma_f32_16x16x32_bf16 v[136:139], v[120:123], v[88:91], v[136:139]\n\t" \
  "ds_read_b64_tr_b16 v[116:117], v161 offset:36864\n\t" \
  "v_mfma_f32_16x16x32_bf16 v[152:155], v[120:123], v[92:95], v[152:155]\n\t" \
  "ds_read_b64_tr_b16 v[118:119], v161 offset:38912\n\t" \
  "v_mfma_f32_16x16x32_bf16 v[140:143], v[124:127], v[88:91], v[140:143]\n\t" \
  "ds_read_b64_tr_b16 v[120:121], v162 offset:36864\n\t" \
  "v_mfma_f32_16x16x32_bf16 v[156:159], v[124:127], v[92:95], v[156:159]\n\t" \
  "ds_read_b64_tr_b16 v[122:123], v162 offset:38912\n\t" \
  "s_waitcnt vmcnt(11) lgkmcnt(10)\n\t" \
  "v_mfma_f32_16x16x32_bf16 v[128:131], v[96:99], v[0:3], v[128:131]\n\t" \
  "ds_read_b64_tr_b16 v[124:125], v163 offset:36864\n\t" \
  "buffer_load_dwordx4 v[52:55], v164, %[ob], s79 offen offset:1024 sc1\n\t" \
  "v_mfma_f32_16x16x32_bf16 v[144:147], v[96:99], v[4:7], v[144:147]\n\t" \
  "ds_read_b64_tr_b16 v[126:127], v163 offset:38912\n\t" \
  "v_mfma_f32_16x16x32_bf16 v[132:135], v[100:103], v[0:3], v[132:135]\n\t" \
  "ds_read_b64_tr_b16 v[96:97], v160 offset:40960\n\t" \
  "v_mfma_f32_16x16x32_bf16 v[148:151], v[100:103], v[4:7], v[148:151]\n\t" \
  "ds_read_b64_tr_b16 v[98:99], v160 offset:43008\n\t" \
  "s_waitcnt lgkmcnt(10)\n\t" \
  "v_mfma_f32_16x16x32_bf16 v[136:139], v[104:107], v[0:3], v[136:139]\n\t" \
  "ds_read_b64_tr_b16 v[100:101], v161 offset:40960\n\t" \
  "buffer_load_dwordx4 v[56:59], v164, %[ob], s79 offen offset:2048 sc1\n\t" \
  "v_mfma_f32_16x16x32_bf16 v[152:155], v[104:107], v[4:7], v[152:155]\n\t" \
  "ds_read_b64_tr_b16 v[102:103], v161 offset:43008\n\t" \
  "v_mfma_f32_16x16x32_bf16 v[140:143], v[108:111], v[0:3], v[140:143]\n\t" \
  "ds_read_b64_tr_b16 v[104:105], v162 offset:40960\n\t" \
  "v_mfma_f32_16x16x32_bf16 v[156:159], v[108:111], v[4:7], v[156:159]\n\t" \
  "ds_read_b64_tr_b16 v[106:107], v162 offset:43008\n\t" \
  "s_waitcnt vmcnt(11) lgkmcnt(10)\n\t" \
  "v_mfma_f32_16x16x32_bf16 v[128:131], v[112:115], v[8:11], v[128:131]\n\t" \
  "ds_read_b64_tr_b16 v[108:109], v163 offset:40960\n\t" \
  "buffer_load_dwordx4 v[60:63], v164, %[ob], s79 offen offset:3072 sc1\n\t" \
  "v_mfma_f32_16x16x32_bf16 v[144:147], v[112:115], v[12:15], v[144:147]\n\t" \
  "ds_read_b64_tr_b16 v[110:111], v163 offset:43008\n\t" \
  "v_mfma_f32_16x16x32_bf16 v[132:135], v[116:119], v[8:11], v[132:135]\n\t" \
  "ds_read_b64_tr_b16 v[112:113], v160 offset:45056\n\t" \
  "v_mfma_f32_16x16x32_bf16 v[148:151], v[116:119], v[12:15], v[148:151]\n\t" \
  "ds_read_b64_tr_b16 v[114:115], v160 offset:47104\n\t" \
  "s_waitcnt lgkmcnt(10)\n\t" \
  "v_mfma_f32_16x16x32_bf16 v[136:139], v[120:123], v[8:11], v[136:139]\n\t" \
  "ds_read_b64_tr_b16 v[116:117], v161 offset:45056\n\t" \
  "v_mfma_f32_16x16x32_bf16 v[152:155], v[120:123], v[12:15], v[152:155]\n\t" \
  "ds_read_b64_tr_b16 v[118:119], v161 offset:47104\n\t" \
  "v_mfma_f32_16x16x32_bf16 v[140:143], v[124:127], v[8:11], v[140:143]\n\t" \
  "ds_read_b64_tr_b16 v[120:121], v162 offset:45056\n\t" \
  "v_mfma_f32_16x16x32_bf16 v[156:159], v[124:127], v[12:15], v[156:159]\n\t" \
  "ds_read_b64_tr_b16 v[122:123], v162 offset:47104\n\t" \
  "s_waitcnt vmcnt(10) lgkmcnt(10)\n\t" \
  "v_mfma_f32_16x16x32_bf16 v[128:131], v[96:99], v[16:19], v[128:131]\n\t" \
  "ds_read_b64_tr_b16 v[124:125], v163 offset:45056\n\t" \
  "v_mfma_f32_16x16x32_bf16 v[144:147], v[96:99], v[20:23], v[144:147]\n\t" \
  "ds_read_b64_tr_b16 v[126:127], v163 offset:47104\n\t" \
  "v_mfma_f32_16x16x32_bf16 v[132:135], v[100:103], v[16:19], v[132:135]\n\t" \
  "ds_read_b64_tr_b16 v[96:97], v160 offset:49152\n\t" \
  "v_mfma_f32_16x16x32_bf16 v[148:151], v[100:103], v[20:23], v[148:151]\n\t" \
  "ds_read_b64_tr_b16 v[98:99], v160 offset:51200\n\t" \
  "s_waitcnt lgkmcnt(10)\n\t" \
  "v_mfma_f32_16x16x32_bf16 v[136:139], v[104:107], v[16:19], v[136:139]\n\t" \
  "ds_read_b64_tr_b16 v[100:101], v161 offset:49152\n\t" \
  "v_xor_b32 v0, 32, v167\n\t" \
  "v_mfma_f32_16x16x32_bf16 v[152:155], v[104:107], v[20:23], v[152:155]\n\t" \
  "ds_read_b64_tr_b16 v[102:103], v161 offset:51200\n\t" \
  "v_xor_b32 v1, 64, v167\n\t" \
  "v_mfma_f32_16x16x32_bf16 v[140:143], v[108:111], v[16:19], v[140:143]\n\t" \
  "ds_read_b64_tr_b16 v[104:105], v162 offset:49152\n\t" \
  "v_xor_b32 v2, 0x60, v167\n\t" \
  "v_mfma_f32_16x16x32_bf16 v[156:159], v[108:111], v[20:23], v[156:159]\n\t" \
  "ds_read_b64_tr_b16 v[106:107], v162 offset:51200\n\t" \
  "s_waitcnt vmcnt(8) lgkmcnt(10)\n\t" \
  "v_mfma_f32_16x16x32_bf16 v[128:131], v[112:115], v[24:27], v[128:131]\n\t" \
  "ds_read_b64_tr_b16 v[108:109], v163 offset:49152\n\t" \
  "v_bfe_u32 v5, v164, 7, 3\n\t" \
  "v_mfma_f32_16x16x32_bf16 v[144:147], v[112:115], v[28:31], v[144:147]\n\t" \
  "ds_read_b64_tr_b16 v[110:111], v163 offset:51200\n\t" \
  "v_and_b32 v4, 0x70, v164\n\t" \
  "v_mfma_f32_16x16x32_bf16 v[132:135], v[116:119], v[24:27], v[132:135]\n\t" \
  "ds_read_b64_tr_b16 v[112:113], v160 offset:53248\n\t" \
  "v_lshl_or_b32 v4, v5, 12, v4\n\t" \
  "v_mfma_f32_16x16x32_bf16 v[148:151], v[116:119], v[28:31], v[148:151]\n\t" \
  "ds_read_b64_tr_b16 v[114:115], v160 offset:55296\n\t" \
  "s_waitcnt lgkmcnt(10)\n\t" \
  "v_mfma_f32_16x16x32_bf16 v[136:139], v[120:123], v[24:27], v[136:139]\n\t" \
  "ds_read_b64_tr_b16 v[116:117], v161 offset:53248\n\t" \
  "v_add_u32 v5, 0x8000, v4\n\t" \
  "v_mfma_f32_16x16x32_bf16 v[152:155], v[120:123], v[28:31], v[152:155]\n\t" \
  "ds_read_b64_tr_b16 v[118:119], v161 offset:55296\n\t" \
  "v_add_u32 v6, 0x10000, v4\n\t" \
  "v_mfma_f32_16x16x32_bf16 v[140:143], v[124:127], v[24:27], v[140:143]\n\t" \
  "ds_read_b64_tr_b16 v[120:121], v162 offset:53248\n\t" \
  "v_add_u32 v7, 0x18000, v4\n\t" \
  "v_mfma_f32_16x16x32_bf16 v[156:159], v[124:127], v[28:31], v[156:159]\n\t" \
  "ds_read_b64_tr_b16 v[122:123], v162 offset:55296\n\t" \
  "s_waitcnt vmcnt(6) lgkmcnt(10)\n\t" \
  "v_mfma_f32_16x16x32_bf16 v[128:131], v[96:99], v[32:35], v[128:131]\n\t" \
  "ds_read_b64_tr_b16 v[124:125], v163 offset:53248\n\t" \
  "v_mfma_f32_16x16x32_bf16 v[144:147], v[96:99], v[36:39], v[144:147]\n\t" \
  "ds_read_b64_tr_b16 v[126:127], v163 offset:55296\n\t" \
  "v_mfma_f32_16x16x32_bf16 v[132:135], v[100:103], v[32:35], v[132:135]\n\t" \
  "ds_read_b64_tr_b16 v[96:97], v160 offset:57344\n\t" \
  "v_mfma_f32_16x16x32_bf16 v[148:151], v[100:103], v[36:39], v[148:151]\n\t" \
  "ds_read_b64_tr_b16 v[98:99], v160 offset:59392\n\t" \
  "s_waitcnt lgkmcnt(10)\n\t" \
  "v_mfma_f32_16x16x32_bf16 v[136:139], v[104:107], v[32:35], v[136:139]\n\t" \
  "ds_read_b64_tr_b16 v[100:101], v161 offset:57344\n\t" \
  "v_mfma_f32_16x16x32_bf16 v[152:155], v[104:107], v[36:39], v[152:155]\n\t" \
  "ds_read_b64_tr_b16 v[102:103], v161 offset:59392\n\t" \
  "v_mfma_f32_16x16x32_bf16 v[140:143], v[108:111], v[32:35], v[140:143]\n\t" \
  "ds_read_b64_tr_b16 v[104:105], v162 offset:57344\n\t" \
  "v_mfma_f32_16x16x32_bf16 v[156:159], v[108:111], v[36:39], v[156:159]\n\t" \
  "ds_read_b64_tr_b16 v[106:107], v162 offset:59392\n\t" \
  "s_waitcnt vmcnt(4) lgkmcnt(10)\n\t" \
  "v_mfma_f32_16x16x32_bf16 v[128:131], v[112:115], v[40:43], v[128:131]\n\t" \
  "ds_read_b64_tr_b16 v[108:109], v163 offset:57344\n\t" \
  "v_mfma_f32_16x16x32_bf16 v[144:147], v[112:115], v[44:47], v[144:147]\n\t" \
  "ds_read_b64_tr_b16 v[110:111], v163 offset:59392\n\t" \
  "v_mfma_f32_16x16x32_bf16 v[132:135], v[116:119], v[40:43], v[132:135]\n\t" \
  "ds_read_b64_tr_b16 v[112:113], v160 offset:61440\n\t" \
  "v_mfma_f32_16x16x32_bf16 v[148:151], v[116:119], v[44:47], v[148:151]\n\t" \
  "ds_read_b64_tr_b16 v[114:115], v160 offset:63488\n\t" \
  "s_waitcnt lgkmcnt(10)\n\t" \
  "v_mfma_f32_16x16x32_bf16 v[136:139], v[120:123], v[40:43], v[136:139]\n\t" \
  "ds_read_b64_tr_b16 v[116:117], v161 offset:61440\n\t" \
  "v_mfma_f32_16x16x32_bf16 v[152:155], v[120:123], v[44:47], v[152:155]\n\t" \
  "ds_read_b64_tr_b16 v[118:119], v161 offset:63488\n\t" \
  "v_mfma_f32_16x16x32_bf16 v[140:143], v[124:127], v[40:43], v[140:143]\n\t" \
  "ds_read_b64_tr_b16 v[120:121], v162 offset:61440\n\t" \
  "v_mfma_f32_16x16x32_bf16 v[156:159], v[124:127], v[44:47], v[156:159]\n\t" \
  "ds_read_b64_tr_b16 v[122:123], v162 offset:63488\n\t" \
  "s_waitcnt vmcnt(0) lgkmcnt(10)\n\t" \
  "v_mfma_f32_16x16x32_bf16 v[128:131], v[96:99], v[48:51], v[128:131]\n\t" \
  "ds_read_b64_tr_b16 v[124:125], v163 offset:61440\n\t" \
  "v_mfma_f32_16x16x32_bf16 v[132:135], v[100:103], v[48:51], v[132:135]\n\t" \
  "ds_read_b64_tr_b16 v[126:127], v163 offset:63488\n\t" \
  "s_waitcnt lgkmcnt(6)\n\t" \
  "v_mfma_f32_16x16x32_bf16 v[136:139], v[104:107], v[48:51], v[136:139]\n\t" \
  "v_mfma_f32_16x16x32_bf16 v[140:143], v[108:111], v[48:51], v[140:143]\n\t" \
  "v_mfma_f32_16x16x32_bf16 v[128:131], v[112:115], v[56:59], v[128:131]\n\t" \
  "s_waitcnt lgkmcnt(2)\n\t" \
  "v_mfma_f32_16x16x32_bf16 v[132:135], v[116:119], v[56:59], v[132:135]\n\t" \
  "v_mfma_f32_16x16x32_bf16 v[136:139], v[120:123], v[56:59], v[136:139]\n\t" \
  "v_mfma_f32_16x16x32_bf16 v[144:147], v[96:99], v[52:55], v[144:147]\n\t" \
  "s_waitcnt lgkmcnt(0)\n\t" \
  "v_mfma_f32_16x16x32_bf16 v[140:143], v[124:127], v[56:59], v[140:143]\n\t" \
  "v_mfma_f32_16x16x32_bf16 v[144:147], v[112:115], v[60:63], v[144:147]\n\t" \
  "s_nop 0\n\t" \
  "v_cvt_pk_bf16_f32 v16, v128, v129\n\t" \
  "v_mfma_f32_16x16x32_bf16 v[148:151], v[100:103], v[52:55], v[148:151]\n\t" \
  "v_cvt_pk_bf16_f32 v17, v130, v131\n\t" \
  "v_cvt_pk_bf16_f32 v18, v132, v133\n\t" \
  "v_mfma_f32_16x16x32_bf16 v[148:151], v[116:119], v[60:63], v[148:151]\n\t" \
  "v_cvt_pk_bf16_f32 v19, v134, v135\n\t" \
  "ds_write_b64 v167, v[16:17]\n\t" \
  "v_mfma_f32_16x16x32_bf16 v[152:155], v[104:107], v[52:55], v[152:155]\n\t" \
  "v_cvt_pk_bf16_f32 v20, v136, v137\n\t" \
  "ds_write_b64 v0, v[18:19]\n\t" \
  "v_mfma_f32_16x16x32_bf16 v[152:155], v[120:123], v[60:63], v[152:155]\n\t" \
  "v_cvt_pk_bf16_f32 v21, v138, v139\n\t" \
  "v_cvt_pk_bf16_f32 v22, v140, v141\n\t" \
  "v_mfma_f32_16x16x32_bf16 v[156:159], v[108:111], v[52:55], v[156:159]\n\t" \
  "v_cvt_pk_bf16_f32 v23, v142, v143\n\t" \
  "ds_write_b64 v1, v[20:21]\n\t" \
  "v_mfma_f32_16x16x32_bf16 v[156:159], v[124:127], v[60:63], v[156:159]\n\t" \
  "v_cvt_pk_bf16_f32 v24, v144, v145\n\t" \
  "ds_write_b64 v2, v[22:23]\n\t" \
  "v_cvt_pk_bf16_f32 v25, v146, v147\n\t" \
  "ds_read_b128 v[64:67], v166 offset:0\n\t" \
  "v_cvt_pk_bf16_f32 v26, v148, v149\n\t" \
  "ds_read_b128 v[68:71], v166 offset:1024\n\t" \
  "v_cvt_pk_bf16_f32 v27, v150, v151\n\t" \
  "ds_write_b64 v167, v[24:25]\n\t" \
  "v_cvt_pk_bf16_f32 v28, v152, v153\n\t" \
  "v_cvt_pk_bf16_f32 v29, v154, v155\n\t" \
  "ds_write_b64 v0, v[26:27]\n\t" \
  "v_cvt_pk_bf16_f32 v30, v156, v157\n\t" \
  "v_cvt_pk_bf16_f32 v31, v158, v159\n\t" \
  "ds_write_b64 v1, v[28:29]\n\t" \
  "ds_write_b64 v2, v[30:31]\n\t" \
  "ds_read_b128 v[72:75], v166 offset:0\n\t" \
  "ds_read_b128 v[76:79], v166 offset:1024\n\t" \
  "s_waitcnt lgkmcnt(7)\n\t" \
  "global_store_dwordx4 v4, v[64:67], %[op] sc1\n\t" \
  "s_waitcnt lgkmcnt(6)\n\t" \
  "global_store_dwordx4 v5, v[68:71], %[op] sc1\n\t" \
  "s_waitcnt lgkmcnt(1)\n\t" \
  "global_store_dwordx4 v6, v[72:75], %[op] sc1\n\t" \
  "s_waitcnt lgkmcnt(0)\n\t" \
  "global_store_dwordx4 v7, v[76:79], %[op] sc1\n\t" \
  "s_nop 1\n\t"
#define G3_EP 2
#define G3_CLOB "v0", "v1", "v2", "v3", "v4", "v5", "v6", "v7", "v8", "v9", "v10", "v11", "v12", "v13", "v14", "v15", "v16", "v17", "v18", "v19", "v20", "v21", "v22", "v23", "v24", "v25", "v26", "v27", "v28", "v29", "v30", "v31", "v32", "v33", "v34", "v35", "v36", "v37", "v38", "v39", "v40", "v41", "v42", "v43", "v44", "v45", "v46", "v47", "v48", "v49", "v50", "v51", "v52", "v53", "v54", "v55", "v56", "v57", "v58", "v59", "v60", "v61", "v62", "v63", "v64", "v65", "v66", "v67", "v68", "v69", "v70", "v71", "v72", "v73", "v74", "v75", "v76", "v77", "v78", "v79", "v80", "v81", "v82", "v83", "v84", "v85", "v86", "v87", "v88", "v89", "v90", "v91", "v92", "v93", "v94", "v95", "v97", "v98", "v99", "v100", "v101", "v102", "v103", "v104", "v105", "v106", "v107", "v108", "v109", "v110", "v111", "v112", "v113", "v114", "v115", "v116", "v117", "v118", "v119", "v120", "v121", "v122", "v123", "v124", "v125", "v126", "v127", "v128", "v129", "v130", "v131", "v132", "v133", "v134", "v135", "v136", "v137", "v138", "v139", "v140", "v141", "v142", "v143", "v144", "v145", "v146", "v147", "v148", "v149", "v150", "v151", "v152", "v153", "v154", "v155", "v156", "v157", "v158", "v159", "s64", "s65", "s66", "s67", "s68", "s69", "s70", "s71", "s72", "s73", "s74", "s75", "s76", "s77", "s78", "s79"

#define PP_ASM \
  "v_mov_b32 v60, 0\n\t" \
  "s_mov_b32 %[t0], %[n0]\n\t" \
  "s_cmp_eq_u32 %[t0], 0\n\t" \
  "s_cbranch_scc1 PAx_%=\n\t" \
  "PA_%=:\n\t" \
  "s_barrier\n\t" \
  "s_sub_u32 %[t0], %[t0], 1\n\t" \
  "s_cmp_lg_u32 %[t0], 0\n\t" \
  "s_cbranch_scc1 PA_%=\n\t" \
  "PAx_%=:\n\t" \
  "s_mov_b32 m0, %[shd]\n\t" \
  "global_load_dword v160, v60, %[fl] sc1\n\t" \
  "global_load_lds_dwordx4 v163, %[shp]\n\t" \
  "s_mov_b32 m0, %[tgd]\n\t" \
  "s_mov_b64 exec, %[em]\n\t" \
  "global_load_lds_dword v60, %[fl] sc1\n\t" \
  "s_mov_b64 exec, -1\n\t" \
  "global_load_ushort v161, v60, %[hp]\n\t" \
  "global_load_ushort v162, v60, %[dp]\n\t" \
  "global_load_dwordx4 v[0:3], v152, %[kb0]\n\t" \
  "global_load_dwordx4 v[4:7], v153, %[kb0]\n\t" \
  "global_load_dwordx4 v[8:11], v154, %[kb0]\n\t" \
  "global_load_dwordx4 v[12:15], v155, %[kb3] offset:0\n\t" \
  "global_load_dwordx4 v[28:31], v155, %[vb0] offset:0\n\t" \
  "s_setprio 2\n\t" \
  "s_setprio 3\n\t" \
  "s_barrier\n\t" \
  "global_load_dwordx4 v[16:19], v155, %[kb3] offset:2048\n\t" \
  "global_load_dwordx4 v[32:35], v155, %[vb0] offset:2048\n\t" \
  "global_load_dwordx4 v[20:23], v155, %[kb5] offset:0\n\t" \
  "s_setprio 2\n\t" \
  "s_mov_b32 s85, 0x3c0000\n\t" \
  "s_load_dword s84, %[ph], s85\n\t" \
  "s_mov_b32 s85, 0x3c0800\n\t" \
  "s_load_dword s84, %[ph], s85\n\t" \
  "s_mov_b32 s85, 0x3c1000\n\t" \
  "s_load_dword s84, %[ph], s85\n\t" \
  "s_setprio 3\n\t" \
  "s_barrier\n\t" \
  "global_load_dwordx4 v[36:39], v155, %[vb2] offset:0\n\t" \
  "global_load_dwordx4 v[24:27], v155, %[kb5] offset:2048\n\t" \
  "global_load_dwordx4 v[40:43], v155, %[vb2] offset:2048\n\t" \
  "s_setprio 2\n\t" \
  "s_waitcnt vmcnt(11)\n\t" \
  "ds_read_b128 v[128:131], v156 offset:0\n\t" \
  "ds_read_b128 v[132:135], v156 offset:16\n\t" \
  "ds_read_b128 v[136:139], v156 offset:32\n\t" \
  "ds_read_b128 v[140:143], v156 offset:48\n\t" \
  "s_waitcnt lgkmcnt(0)\n\t" \
  "v_lshlrev_b32 v64, 16, v128\n\t" \
  "v_and_b32 v65, 0xffff0000, v128\n\t" \
  "v_lshlrev_b32 v66, 16, v129\n\t" \
  "v_and_b32 v67, 0xffff0000, v129\n\t" \
  "v_lshlrev_b32 v68, 16, v130\n\t" \
  "v_and_b32 v69, 0xffff0000, v130\n\t" \
  "v_lshlrev_b32 v70, 16, v131\n\t" \
  "v_and_b32 v71, 0xffff0000, v131\n\t" \
  "v_lshlrev_b32 v72, 16, v132\n\t" \
  "v_and_b32 v73, 0xffff0000, v132\n\t" \
  "v_lshlrev_b32 v74, 16, v133\n\t" \
  "v_and_b32 v75, 0xffff0000, v133\n\t" \
  "v_lshlrev_b32 v76, 16, v134\n\t" \
  "v_and_b32 v77, 0xffff0000, v134\n\t" \
  "v_lshlrev_b32 v78, 16, v135\n\t" \
  "v_and_b32 v79, 0xffff0000, v135\n\t" \
  "v_lshlrev_b32 v80, 16, v136\n\t" \
  "v_and_b32 v81, 0xffff0000, v136\n\t" \
  "v_lshlrev_b32 v82, 16, v137\n\t" \
  "v_and_b32 v83, 0xffff0000, v137\n\t" \
  "v_lshlrev_b32 v84, 16, v138\n\t" \
  "v_and_b32 v85, 0xffff0000, v138\n\t" \
  "v_lshlrev_b32 v86, 16, v139\n\t" \
  "v_and_b32 v87, 0xffff0000, v139\n\t" \
  "v_lshlrev_b32 v88, 16, v140\n\t" \
  "v_and_b32 v89, 0xffff0000, v140\n\t" \
  "v_lshlrev_b32 v90, 16, v141\n\t" \
  "v_and_b32 v91, 0xffff0000, v141\n\t" \
  "v_lshlrev_b32 v92, 16, v142\n\t" \
  "v_and_b32 v93, 0xffff0000, v142\n\t" \
  "v_lshlrev_b32 v94, 16, v143\n\t" \
  "v_and_b32 v95, 0xffff0000, v143\n\t" \
  "s_waitcnt vmcnt(10)\n\t" \
  "s_mov_b64 exec, %[zm]\n\t" \
  "v_mov_b32 v0, 0\n\t" \
  "v_mov_b32 v1, 0\n\t" \
  "v_mov_b32 v2, 0\n\t" \
  "v_mov_b32 v3, 0\n\t" \
  "s_mov_b64 exec, -1\n\t" \
  "v_lshlrev_b32 v128, 16, v0\n\t" \
  "v_and_b32 v129, 0xffff0000, v0\n\t" \
  "v_lshlrev_b32 v130, 16, v1\n\t" \
  "v_and_b32 v131, 0xffff0000, v1\n\t" \
  "v_lshlrev_b32 v132, 16, v2\n\t" \
  "v_and_b32 v133, 0xffff0000, v2\n\t" \
  "v_lshlrev_b32 v134, 16, v3\n\t" \
  "v_and_b32 v135, 0xffff0000, v3\n\t" \
  "v_mul_f32 v96, v128, v67\n\t" \
  "v_mul_f32 v97, v129, v71\n\t" \
  "v_mul_f32 v98, v130, v75\n\t" \
  "v_mul_f32 v99, v131, v79\n\t" \
  "v_mul_f32 v100, v132, v83\n\t" \
  "v_mul_f32 v101, v133, v87\n\t" \
  "v_mul_f32 v102, v134, v91\n\t" \
  "v_mul_f32 v103, v135, v95\n\t" \
  "s_waitcnt vmcnt(9)\n\t" \
  "s_mov_b64 exec, %[zm]\n\t" \
  "v_mov_b32 v4, 0\n\t" \
  "v_mov_b32 v5, 0\n\t" \
  "v_mov_b32 v6, 0\n\t" \
  "v_mov_b32 v7, 0\n\t" \
  "s_mov_b64 exec, -1\n\t" \
  "v_lshlrev_b32 v128, 16, v4\n\t" \
  "v_and_b32 v129, 0xffff0000, v4\n\t" \
  "v_lshlrev_b32 v130, 16, v5\n\t" \
  "v_and_b32 v131, 0xffff0000, v5\n\t" \
  "v_lshlrev_b32 v132, 16, v6\n\t" \
  "v_and_b32 v133, 0xffff0000, v6\n\t" \
  "v_lshlrev_b32 v134, 16, v7\n\t" \
  "v_and_b32 v135, 0xffff0000, v7\n\t" \
  "v_fmac_f32 v96, v128, v66\n\t" \
  "v_fmac_f32 v97, v129, v70\n\t" \
  "v_fmac_f32 v98, v130, v74\n\t" \
  "v_fmac_f32 v99, v131, v78\n\t" \
  "v_fmac_f32 v100, v132, v82\n\t" \
  "v_fmac_f32 v101, v133, v86\n\t" \
  "v_fmac_f32 v102, v134, v90\n\t" \
  "v_fmac_f32 v103, v135, v94\n\t" \
  "v_mul_f32 v104, v128, v67\n\t" \
  "v_mul_f32 v105, v129, v71\n\t" \
  "v_mul_f32 v106, v130, v75\n\t" \
  "v_mul_f32 v107, v131, v79\n\t" \
  "v_mul_f32 v108, v132, v83\n\t" \
  "v_mul_f32 v109, v133, v87\n\t" \
  "v_mul_f32 v110, v134, v91\n\t" \
  "v_mul_f32 v111, v135, v95\n\t" \
  "s_waitcnt vmcnt(8)\n\t" \
  "s_mov_b64 exec, %[zm]\n\t" \
  "v_mov_b32 v8, 0\n\t" \
  "v_mov_b32 v9, 0\n\t" \
  "v_mov_b32 v10, 0\n\t" \
  "v_mov_b32 v11, 0\n\t" \
  "s_mov_b64 exec, -1\n\t" \
  "v_lshlrev_b32 v128, 16, v8\n\t" \
  "v_and_b32 v129, 0xffff0000, v8\n\t" \
  "v_lshlrev_b32 v130, 16, v9\n\t" \
  "v_and_b32 v131, 0xffff0000, v9\n\t" \
  "v_lshlrev_b32 v132, 16, v10\n\t" \
  "v_and_b32 v133, 0xffff0000, v10\n\t" \
  "v_lshlrev_b32 v134, 16, v11\n\t" \
  "v_and_b32 v135, 0xffff0000, v11\n\t" \
  "v_fmac_f32 v96, v128, v65\n\t" \
  "v_fmac_f32 v97, v129, v69\n\t" \
  "v_fmac_f32 v98, v130, v73\n\t" \
  "v_fmac_f32 v99, v131, v77\n\t" \
  "v_fmac_f32 v100, v132, v81\n\t" \
  "v_fmac_f32 v101, v133, v85\n\t" \
  "v_fmac_f32 v102, v134, v89\n\t" \
  "v_fmac_f32 v103, v135, v93\n\t" \
  "v_fmac_f32 v104, v128, v66\n\t" \
  "v_fmac_f32 v105, v129, v70\n\t" \
  "v_fmac_f32 v106, v130, v74\n\t" \
  "v_fmac_f32 v107, v131, v78\n\t" \
  "s_mov_b32 s85, 0x420000\n\t" \
  "s_load_dword s84, %[ph], s85\n\t" \
  "s_mov_b32 s85, 0x420800\n\t" \
  "s_load_dword s84, %[ph], s85\n\t" \
  "s_mov_b32 s85, 0x421000\n\t" \
  "s_load_dword s84, %[ph], s85\n\t" \
  "s_barrier\n\t" \
  "v_fmac_f32 v108, v132, v82\n\t" \
  "v_fmac_f32 v109, v133, v86\n\t" \
  "v_fmac_f32 v110, v134, v90\n\t" \
  "v_fmac_f32 v111, v135, v94\n\t" \
  "v_mul_f32 v112, v128, v67\n\t" \
  "v_mul_f32 v113, v129, v71\n\t" \
  "v_mul_f32 v114, v130, v75\n\t" \
  "v_mul_f32 v115, v131, v79\n\t" \
  "v_mul_f32 v116, v132, v83\n\t" \
  "v_mul_f32 v117, v133, v87\n\t" \
  "v_mul_f32 v118, v134, v91\n\t" \
  "v_mul_f32 v119, v135, v95\n\t" \
  "s_waitcnt vmcnt(7)\n\t" \
  "v_lshlrev_b32 v128, 16, v12\n\t" \
  "v_and_b32 v129, 0xffff0000, v12\n\t" \
  "v_lshlrev_b32 v130, 16, v13\n\t" \
  "v_and_b32 v131, 0xffff0000, v13\n\t" \
  "v_lshlrev_b32 v132, 16, v14\n\t" \
  "v_and_b32 v133, 0xffff0000, v14\n\t" \
  "v_lshlrev_b32 v134, 16, v15\n\t" \
  "v_and_b32 v135, 0xffff0000, v15\n\t" \
  "v_fmac_f32 v96, v128, v64\n\t" \
  "v_fmac_f32 v97, v129, v68\n\t" \
  "v_fmac_f32 v98, v130, v72\n\t" \
  "v_fmac_f32 v99, v131, v76\n\t" \
  "v_fmac_f32 v100, v132, v80\n\t" \
  "v_fmac_f32 v101, v133, v84\n\t" \
  "v_fmac_f32 v102, v134, v88\n\t" \
  "v_fmac_f32 v103, v135, v92\n\t" \
  "v_fmac_f32 v104, v128, v65\n\t" \
  "v_fmac_f32 v105, v129, v69\n\t" \
  "v_fmac_f32 v106, v130, v73\n\t" \
  "v_fmac_f32 v107, v131, v77\n\t" \
  "v_fmac_f32 v108, v132, v81\n\t" \
  "v_fmac_f32 v109, v133, v85\n\t" \
  "v_fmac_f32 v110, v134, v89\n\t" \
  "v_fmac_f32 v111, v135, v93\n\t" \
  "v_fmac_f32 v112, v128, v66\n\t" \
  "v_fmac_f32 v113, v129, v70\n\t" \
  "v_fmac_f32 v114, v130, v74\n\t" \
  "v_fmac_f32 v115, v131, v78\n\t" \
  "v_fmac_f32 v116, v132, v82\n\t" \
  "v_fmac_f32 v117, v133, v86\n\t" \
  "v_fmac_f32 v118, v134, v90\n\t" \
  "v_fmac_f32 v119, v135, v94\n\t" \
  "v_mul_f32 v120, v128, v67\n\t" \
  "v_mul_f32 v121, v129, v71\n\t" \
  "v_mul_f32 v122, v130, v75\n\t" \
  "v_mul_f32 v123, v131, v79\n\t" \
  "v_mul_f32 v124, v132, v83\n\t" \
  "v_mul_f32 v125, v133, v87\n\t" \
  "v_mul_f32 v126, v134, v91\n\t" \
  "v_mul_f32 v127, v135, v95\n\t" \
  "s_waitcnt vmcnt(6)\n\t" \
  "v_lshlrev_b32 v128, 16, v28\n\t" \
  "v_and_b32 v129, 0xffff0000, v28\n\t" \
  "v_lshlrev_b32 v130, 16, v29\n\t" \
  "v_and_b32 v131, 0xffff0000, v29\n\t" \
  "v_lshlrev_b32 v132, 16, v30\n\t" \
  "v_and_b32 v133, 0xffff0000, v30\n\t" \
  "v_lshlrev_b32 v134, 16, v31\n\t" \
  "v_and_b32 v135, 0xffff0000, v31\n\t" \
  "v_mul_f32 v96, v96, v128\n\t" \
  "v_mul_f32 v97, v97, v129\n\t" \
  "v_mul_f32 v98, v98, v130\n\t" \
  "v_mul_f32 v99, v99, v131\n\t" \
  "v_mul_f32 v100, v100, v132\n\t" \
  "v_mul_f32 v101, v101, v133\n\t" \
  "v_mul_f32 v102, v102, v134\n\t" \
  "v_mul_f32 v103, v103, v135\n\t" \
  "v_cvt_pk_bf16_f32 v136, v96, v97\n\t" \
  "v_cvt_pk_bf16_f32 v137, v98, v99\n\t" \
  "v_cvt_pk_bf16_f32 v138, v100, v101\n\t" \
  "v_cvt_pk_bf16_f32 v139, v102, v103\n\t" \
  "v_mov_b32_dpp v128, v136 row_ror:8 row_mask:0xf bank_mask:0xf\n\t" \
  "v_mov_b32_dpp v129, v137 row_ror:8 row_mask:0xf bank_mask:0xf\n\t" \
  "v_mov_b32_dpp v130, v138 row_ror:8 row_mask:0xf bank_mask:0xf\n\t" \
  "v_mov_b32_dpp v131, v139 row_ror:8 row_mask:0xf bank_mask:0xf\n\t" \
  "v_perm_b32 v136, v128, v136, v157\n\t" \
  "v_perm_b32 v137, v129, v137, v157\n\t" \
  "v_perm_b32 v138, v130, v138, v157\n\t" \
  "v_perm_b32 v139, v131, v139, v157\n\t" \
  "v_permlane16_swap_b32 v136, v137\n\t" \
  "s_nop 0\n\t" \
  "v_permlane16_swap_b32 v138, v139\n\t" \
  "s_nop 1\n\t" \
  "v_permlane32_swap_b32 v136, v138\n\t" \
  "v_permlane32_swap_b32 v137, v139\n\t" \
  "s_waitcnt vmcnt(5)\n\t" \
  "v_lshlrev_b32 v128, 16, v16\n\t" \
  "v_and_b32 v129, 0xffff0000, v16\n\t" \
  "v_lshlrev_b32 v130, 16, v17\n\t" \
  "v_and_b32 v131, 0xffff0000, v17\n\t" \
  "v_lshlrev_b32 v132, 16, v18\n\t" \
  "v_and_b32 v133, 0xffff0000, v18\n\t" \
  "v_lshlrev_b32 v134, 16, v19\n\t" \
  "v_and_b32 v135, 0xffff0000, v19\n\t" \
  "v_fmac_f32 v104, v128, v64\n\t" \
  "v_fmac_f32 v105, v129, v68\n\t" \
  "v_fmac_f32 v106, v130, v72\n\t" \
  "v_fmac_f32 v107, v131, v76\n\t" \
  "v_fmac_f32 v108, v132, v80\n\t" \
  "v_fmac_f32 v109, v133, v84\n\t" \
  "s_mov_b32 s85, 0x480000\n\t" \
  "s_load_dword s84, %[ph], s85\n\t" \
  "s_mov_b32 s85, 0x480800\n\t" \
  "s_load_dword s84, %[ph], s85\n\t" \
  "s_mov_b32 s85, 0x481000\n\t" \
  "s_load_dword s84, %[ph], s85\n\t" \
  "s_setprio 3\n\t" \
  "s_barrier\n\t" \
  "global_store_dwordx4 v159, v[136:139], %[sb] sc1\n\t" \
  "v_add_u32 v158, 0x80, v159\n\t" \
  "v_bfi_b32 v159, %[mk], v158, v159\n\t" \
  "s_setprio 2\n\t" \
  "v_fmac_f32 v110, v134, v88\n\t" \
  "v_fmac_f32 v111, v135, v92\n\t" \
  "v_fmac_f32 v112, v128, v65\n\t" \
  "v_fmac_f32 v113, v129, v69\n\t" \
  "v_fmac_f32 v114, v130, v73\n\t" \
  "v_fmac_f32 v115, v131, v77\n\t" \
  "v_fmac_f32 v116, v132, v81\n\t" \
  "v_fmac_f32 v117, v133, v85\n\t" \
  "v_fmac_f32 v118, v134, v89\n\t" \
  "v_fmac_f32 v119, v135, v93\n\t" \
  "v_fmac_f32 v120, v128, v66\n\t" \
  "v_fmac_f32 v121, v129, v70\n\t" \
  "v_fmac_f32 v122, v130, v74\n\t" \
  "v_fmac_f32 v123, v131, v78\n\t" \
  "v_fmac_f32 v124, v132, v82\n\t" \
  "v_fmac_f32 v125, v133, v86\n\t" \
  "v_fmac_f32 v126, v134, v90\n\t" \
  "v_fmac_f32 v127, v135, v94\n\t" \
  "s_waitcnt vmcnt(5)\n\t" \
  "v_lshlrev_b32 v128, 16, v32\n\t" \
  "v_and_b32 v129, 0xffff0000, v32\n\t" \
  "v_lshlrev_b32 v130, 16, v33\n\t" \
  "v_and_b32 v131, 0xffff0000, v33\n\t" \
  "v_lshlrev_b32 v132, 16, v34\n\t" \
  "v_and_b32 v133, 0xffff0000, v34\n\t" \
  "v_lshlrev_b32 v134, 16, v35\n\t" \
  "v_and_b32 v135, 0xffff0000, v35\n\t" \
  "v_mul_f32 v104, v104, v128\n\t" \
  "v_mul_f32 v105, v105, v129\n\t" \
  "v_mul_f32 v106, v106, v130\n\t" \
  "v_mul_f32 v107, v107, v131\n\t" \
  "v_mul_f32 v108, v108, v132\n\t" \
  "v_mul_f32 v109, v109, v133\n\t" \
  "v_mul_f32 v110, v110, v134\n\t" \
  "v_mul_f32 v111, v111, v135\n\t" \
  "v_cvt_pk_bf16_f32 v140, v104, v105\n\t" \
  "v_cvt_pk_bf16_f32 v141, v106, v107\n\t" \
  "v_cvt_pk_bf16_f32 v142, v108, v109\n\t" \
  "v_cvt_pk_bf16_f32 v143, v110, v111\n\t" \
  "v_mov_b32_dpp v128, v140 row_ror:8 row_mask:0xf bank_mask:0xf\n\t" \
  "v_mov_b32_dpp v129, v141 row_ror:8 row_mask:0xf bank_mask:0xf\n\t" \
  "v_mov_b32_dpp v130, v142 row_ror:8 row_mask:0xf bank_mask:0xf\n\t" \
  "v_mov_b32_dpp v131, v143 row_ror:8 row_mask:0xf bank_mask:0xf\n\t" \
  "v_perm_b32 v140, v128, v140, v157\n\t" \
  "v_perm_b32 v141, v129, v141, v157\n\t" \
  "v_perm_b32 v142, v130, v142, v157\n\t" \
  "v_perm_b32 v143, v131, v143, v157\n\t" \
  "v_permlane16_swap_b32 v140, v141\n\t" \
  "s_nop 0\n\t" \
  "v_permlane16_swap_b32 v142, v143\n\t" \
  "s_nop 1\n\t" \
  "v_permlane32_swap_b32 v140, v142\n\t" \
  "v_permlane32_swap_b32 v141, v143\n\t" \
  "s_waitcnt vmcnt(4)\n\t" \
  "v_lshlrev_b32 v128, 16, v20\n\t" \
  "v_and_b32 v129, 0xffff0000, v20\n\t" \
  "v_lshlrev_b32 v130, 16, v21\n\t" \
  "v_and_b32 v131, 0xffff0000, v21\n\t" \
  "v_lshlrev_b32 v132, 16, v22\n\t" \
  "v_and_b32 v133, 0xffff0000, v22\n\t" \
  "v_lshlrev_b32 v134, 16, v23\n\t" \
  "v_and_b32 v135, 0xffff0000, v23\n\t" \
  "v_fmac_f32 v112, v128, v64\n\t" \
  "v_fmac_f32 v113, v129, v68\n\t" \
  "v_fmac_f32 v114, v130, v72\n\t" \
  "v_fmac_f32 v115, v131, v76\n\t" \
  "v_fmac_f32 v116, v132, v80\n\t" \
  "v_fmac_f32 v117, v133, v84\n\t" \
  "v_fmac_f32 v118, v134, v88\n\t" \
  "v_fmac_f32 v119, v135, v92\n\t" \
  "v_fmac_f32 v120, v128, v65\n\t" \
  "v_fmac_f32 v121, v129, v69\n\t" \
  "v_fmac_f32 v122, v130, v73\n\t" \
  "v_fmac_f32 v123, v131, v77\n\t" \
  "v_fmac_f32 v124, v132, v81\n\t" \
  "v_fmac_f32 v125, v133, v85\n\t" \
  "v_fmac_f32 v126, v134, v89\n\t" \
  "v_fmac_f32 v127, v135, v93\n\t" \
  "s_waitcnt vmcnt(3)\n\t" \
  "v_lshlrev_b32 v128, 16, v36\n\t" \
  "v_and_b32 v129, 0xffff0000, v36\n\t" \
  "v_lshlrev_b32 v130, 16, v37\n\t" \
  "v_and_b32 v131, 0xffff0000, v37\n\t" \
  "v_lshlrev_b32 v132, 16, v38\n\t" \
  "v_and_b32 v133, 0xffff0000, v38\n\t" \
  "v_lshlrev_b32 v134, 16, v39\n\t" \
  "v_and_b32 v135, 0xffff0000, v39\n\t" \
  "v_mul_f32 v112, v112, v128\n\t" \
  "v_mul_f32 v113, v113, v129\n\t" \
  "v_mul_f32 v114, v114, v130\n\t" \
  "v_mul_f32 v115, v115, v131\n\t" \
  "v_mul_f32 v116, v116, v132\n\t" \
  "v_mul_f32 v117, v117, v133\n\t" \
  "v_mul_f32 v118, v118, v134\n\t" \
  "v_mul_f32 v119, v119, v135\n\t" \
  "v_cvt_pk_bf16_f32 v144, v112, v113\n\t" \
  "v_cvt_pk_bf16_f32 v145, v114, v115\n\t" \
  "v_cvt_pk_bf16_f32 v146, v116, v117\n\t" \
  "v_cvt_pk_bf16_f32 v147, v118, v119\n\t" \
  "v_mov_b32_dpp v128, v144 row_ror:8 row_mask:0xf bank_mask:0xf\n\t" \
  "v_mov_b32_dpp v129, v145 row_ror:8 row_mask:0xf bank_mask:0xf\n\t" \
  "v_mov_b32_dpp v130, v146 row_ror:8 row_mask:0xf bank_mask:0xf\n\t" \
  "v_mov_b32_dpp v131, v147 row_ror:8 row_mask:0xf bank_mask:0xf\n\t" \
  "s_mov_b32 s85, 0x4e0000\n\t" \
  "s_load_dword s84, %[ph], s85\n\t" \
  "s_mov_b32 s85, 0x4e0800\n\t" \
  "s_load_dword s84, %[ph], s85\n\t" \
  "s_mov_b32 s85, 0x4e1000\n\t" \
  "s_load_dword s84, %[ph], s85\n\t" \
  "s_mov_b32 m0, %[hd]\n\t" \
  "s_nop 0\n\t" \
  "global_load_lds_dwordx4 v163, %[hp] offset:0\n\t" \
  "global_load_lds_dwordx4 v163, %[hp] offset:1024\n\t" \
  "global_load_lds_dwordx4 v163, %[hp] offset:2048\n\t" \
  "global_load_lds_dwordx4 v163, %[hp] offset:3072\n\t" \
  "s_setprio 3\n\t" \
  "s_barrier\n\t" \
  "global_store_dwordx4 v159, v[140:143], %[sb] sc1\n\t" \
  "v_add_u32 v158, 0x80, v159\n\t" \
  "v_bfi_b32 v159, %[mk], v158, v159\n\t" \
  "s_setprio 2\n\t" \
  "v_perm_b32 v144, v128, v144, v157\n\t" \
  "v_perm_b32 v145, v129, v145, v157\n\t" \
  "v_perm_b32 v146, v130, v146, v157\n\t" \
  "v_perm_b32 v147, v131, v147, v157\n\t" \
  "v_permlane16_swap_b32 v144, v145\n\t" \
  "s_nop 0\n\t" \
  "v_permlane16_swap_b32 v146, v147\n\t" \
  "s_nop 1\n\t" \
  "v_permlane32_swap_b32 v144, v146\n\t" \
  "v_permlane32_swap_b32 v145, v147\n\t" \
  "s_waitcnt vmcnt(7)\n\t" \
  "v_lshlrev_b32 v128, 16, v24\n\t" \
  "v_and_b32 v129, 0xffff0000, v24\n\t" \
  "v_lshlrev_b32 v130, 16, v25\n\t" \
  "v_and_b32 v131, 0xffff0000, v25\n\t" \
  "v_lshlrev_b32 v132, 16, v26\n\t" \
  "v_and_b32 v133, 0xffff0000, v26\n\t" \
  "v_lshlrev_b32 v134, 16, v27\n\t" \
  "v_and_b32 v135, 0xffff0000, v27\n\t" \
  "v_fmac_f32 v120, v128, v64\n\t" \
  "v_fmac_f32 v121, v129, v68\n\t" \
  "v_fmac_f32 v122, v130, v72\n\t" \
  "v_fmac_f32 v123, v131, v76\n\t" \
  "v_fmac_f32 v124, v132, v80\n\t" \
  "v_fmac_f32 v125, v133, v84\n\t" \
  "v_fmac_f32 v126, v134, v88\n\t" \
  "v_fmac_f32 v127, v135, v92\n\t" \
  "s_waitcnt vmcnt(6)\n\t" \
  "v_lshlrev_b32 v128, 16, v40\n\t" \
  "v_and_b32 v129, 0xffff0000, v40\n\t" \
  "v_lshlrev_b32 v130, 16, v41\n\t" \
  "v_and_b32 v131, 0xffff0000, v41\n\t" \
  "v_lshlrev_b32 v132, 16, v42\n\t" \
  "v_and_b32 v133, 0xffff0000, v42\n\t" \
  "v_lshlrev_b32 v134, 16, v43\n\t" \
  "v_and_b32 v135, 0xffff0000, v43\n\t" \
  "v_mul_f32 v120, v120, v128\n\t" \
  "v_mul_f32 v121, v121, v129\n\t" \
  "v_mul_f32 v122, v122, v130\n\t" \
  "v_mul_f32 v123, v123, v131\n\t" \
  "v_mul_f32 v124, v124, v132\n\t" \
  "v_mul_f32 v125, v125, v133\n\t" \
  "v_mul_f32 v126, v126, v134\n\t" \
  "v_mul_f32 v127, v127, v135\n\t" \
  "v_cvt_pk_bf16_f32 v148, v120, v121\n\t" \
  "v_cvt_pk_bf16_f32 v149, v122, v123\n\t" \
  "v_cvt_pk_bf16_f32 v150, v124, v125\n\t" \
  "v_cvt_pk_bf16_f32 v151, v126, v127\n\t" \
  "v_mov_b32_dpp v128, v148 row_ror:8 row_mask:0xf bank_mask:0xf\n\t" \
  "v_mov_b32_dpp v129, v149 row_ror:8 row_mask:0xf bank_mask:0xf\n\t" \
  "v_mov_b32_dpp v130, v150 row_ror:8 row_mask:0xf bank_mask:0xf\n\t" \
  "v_mov_b32_dpp v131, v151 row_ror:8 row_mask:0xf bank_mask:0xf\n\t" \
  "v_perm_b32 v148, v128, v148, v157\n\t" \
  "v_perm_b32 v149, v129, v149, v157\n\t" \
  "v_perm_b32 v150, v130, v150, v157\n\t" \
  "v_perm_b32 v151, v131, v151, v157\n\t" \
  "v_permlane16_swap_b32 v148, v149\n\t" \
  "s_nop 0\n\t" \
  "v_permlane16_swap_b32 v150, v151\n\t" \
  "s_nop 1\n\t" \
  "v_permlane32_swap_b32 v148, v150\n\t" \
  "v_permlane32_swap_b32 v149, v151\n\t" \
  "s_mov_b32 s85, 0x540000\n\t" \
  "s_load_dword s84, %[ph], s85\n\t" \
  "s_mov_b32 s85, 0x540800\n\t" \
  "s_load_dword s84, %[ph], s85\n\t" \
  "s_mov_b32 s85, 0x541000\n\t" \
  "s_load_dword s84, %[ph], s85\n\t" \
  "s_setprio 3\n\t" \
  "s_barrier\n\t" \
  "global_store_dwordx4 v159, v[144:147], %[sb] sc1\n\t" \
  "v_add_u32 v158, 0x80, v159\n\t" \
  "v_bfi_b32 v159, %[mk], v158, v159\n\t" \
  "global_store_dwordx4 v159, v[148:151], %[sb] sc1\n\t" \
  "s_setprio 2\n\t" \
  "s_waitcnt vmcnt(3)\n\t" \
  "ds_read_b64_tr_b16 v[64:65], v165 offset:32\n\t" \
  "ds_read_b64_tr_b16 v[66:67], v165 offset:0\n\t" \
  "ds_read_b64_tr_b16 v[68:69], v165 offset:288\n\t" \
  "ds_read_b64_tr_b16 v[70:71], v165 offset:256\n\t" \
  "ds_read_b64_tr_b16 v[72:73], v165 offset:544\n\t" \
  "ds_read_b64_tr_b16 v[74:75], v165 offset:512\n\t" \
  "ds_read_b64_tr_b16 v[76:77], v165 offset:800\n\t" \
  "ds_read_b64_tr_b16 v[78:79], v165 offset:768\n\t" \
  "ds_read_b64_tr_b16 v[80:81], v165 offset:1056\n\t" \
  "ds_read_b64_tr_b16 v[82:83], v165 offset:1024\n\t" \
  "ds_read_b64_tr_b16 v[84:85], v165 offset:1312\n\t" \
  "ds_read_b64_tr_b16 v[86:87], v165 offset:1280\n\t" \
  "ds_read_b64_tr_b16 v[88:89], v165 offset:1568\n\t" \
  "ds_read_b64_tr_b16 v[90:91], v165 offset:1536\n\t" \
  "ds_read_b64_tr_b16 v[92:93], v165 offset:1824\n\t" \
  "ds_read_b64_tr_b16 v[94:95], v165 offset:1792\n\t" \
  "v_mov_b32 v24, v166\n\t" \
  "v_mov_b32 v25, v167\n\t" \
  "v_mov_b32 v63, 0xc0\n\t" \
  "v_add_u32 v158, 64, v24\n\t" \
  "v_bfi_b32 v26, v63, v158, v24\n\t" \
  "v_add_u32 v158, 64, v25\n\t" \
  "v_bfi_b32 v27, v63, v158, v25\n\t" \
  "v_add_u32 v158, 64, v26\n\t" \
  "v_bfi_b32 v28, v63, v158, v26\n\t" \
  "v_add_u32 v158, 64, v27\n\t" \
  "v_bfi_b32 v29, v63, v158, v27\n\t" \
  "v_add_u32 v158, 64, v28\n\t" \
  "v_bfi_b32 v30, v63, v158, v28\n\t" \
  "v_add_u32 v158, 64, v29\n\t" \
  "v_bfi_b32 v31, v63, v158, v29\n\t" \
  "s_mov_b32 s85, 0x5a0000\n\t" \
  "s_load_dword s84, %[ph], s85\n\t" \
  "s_mov_b32 s85, 0x5a0800\n\t" \
  "s_load_dword s84, %[ph], s85\n\t" \
  "s_mov_b32 s85, 0x5a1000\n\t" \
  "s_load_dword s84, %[ph], s85\n\t" \
  "s_setprio 3\n\t" \
  "s_barrier\n\t" \
  "s_waitcnt vmcnt(0)\n\t" \
  "v_add_u32 v61, 1, v160\n\t" \
  "s_mov_b64 exec, 1\n\t" \
  "global_store_dword v60, v61, %[ff] sc1\n\t" \
  "s_mov_b64 exec, -1\n\t" \
  "s_setprio 2\n\t" \
  "ds_read_b64_tr_b16 v[96:97], v165 offset:2080\n\t" \
  "ds_read_b64_tr_b16 v[98:99], v165 offset:2048\n\t" \
  "ds_read_b64_tr_b16 v[100:101], v165 offset:2336\n\t" \
  "ds_read_b64_tr_b16 v[102:103], v165 offset:2304\n\t" \
  "ds_read_b64_tr_b16 v[104:105], v165 offset:2592\n\t" \
  "ds_read_b64_tr_b16 v[106:107], v165 offset:2560\n\t" \
  "ds_read_b64_tr_b16 v[108:109], v165 offset:2848\n\t" \
  "ds_read_b64_tr_b16 v[110:111], v165 offset:2816\n\t" \
  "ds_read_b64_tr_b16 v[112:113], v165 offset:3104\n\t" \
  "ds_read_b64_tr_b16 v[114:115], v165 offset:3072\n\t" \
  "ds_read_b64_tr_b16 v[116:117], v165 offset:3360\n\t" \
  "ds_read_b64_tr_b16 v[118:119], v165 offset:3328\n\t" \
  "ds_read_b64_tr_b16 v[120:121], v165 offset:3616\n\t" \
  "ds_read_b64_tr_b16 v[122:123], v165 offset:3584\n\t" \
  "ds_read_b64_tr_b16 v[124:125], v165 offset:3872\n\t" \
  "ds_read_b64_tr_b16 v[126:127], v165 offset:3840\n\t" \
  "s_barrier\n\t" \
  "s_waitcnt lgkmcnt(0)\n\t" \
  "v_lshlrev_b32 v61, 16, v161\n\t" \
  "v_lshlrev_b32 v62, 16, v162\n\t" \
  "v_add_f32 v61, v61, v62\n\t" \
  "v_mov_b32 v62, 0xffff\n\t" \
  "v_cvt_pk_bf16_f32 v61, v61, v61\n\t" \
  "s_mov_b64 exec, 1\n\t" \
  "v_bfi_b32 v66, v62, v61, v66\n\t" \
  "s_mov_b64 exec, 15\n\t" \
  "v_and_b32 v66, v62, v66\n\t" \
  "s_mov_b64 exec, 16\n\t" \
  "v_bfi_b32 v66, v62, v66, v61\n\t" \
  "s_mov_b64 exec, 0xff\n\t" \
  "v_mov_b32 v67, 0\n\t" \
  "s_mov_b64 exec, 0x100\n\t" \
  "v_mov_b32 v67, v61\n\t" \
  "s_mov_b64 exec, 0xf00\n\t" \
  "v_and_b32 v67, v62, v67\n\t" \
  "s_mov_b64 exec, 0x1000\n\t" \
  "v_bfi_b32 v67, v62, v67, v61\n\t" \
  "s_mov_b64 exec, -1\n\t" \
  "s_waitcnt lgkmcnt(0)\n\t"
#define PP_SM 0
#define PP_NB 8
#define PP_Q 0
#define PP_CLOB "v0", "v1", "v2", "v3", "v4", "v5", "v6", "v7", "v8", "v9", "v10", "v11", "v12", "v13", "v14", "v15", "v16", "v17", "v18", "v19", "v20", "v21", "v22", "v23", "v32", "v33", "v34", "v35", "v36", "v37", "v38", "v39", "v40", "v41", "v42", "v43", "v60", "v61", "v62", "v63", "v128", "v129", "v130", "v131", "v132", "v133", "v134", "v135", "v136", "v137", "v138", "v139", "v140", "v141", "v142", "v143", "v144", "v145", "v146", "v147", "v148", "v149", "v150", "v151", "v158", "v161", "v162", "s82", "s83", "s84", "s85"

#define LD_ASM1 \
  "s_add_u32 m0, s39, 49152\n\t" \
  "s_mov_b32 s68, 0x0\n\t" \
  "buffer_load_dwordx4 v5, s[44:47], s68 offen offset:0 lds\n\t" \
  "buffer_load_dwordx4 v6, s[44:47], s68 offen offset:1024 lds\n\t" \
  "buffer_load_dwordx4 v7, s[44:47], s68 offen offset:2048 lds\n\t" \
  "s_add_u32 m0, s38, 36864\n\t" \
  "s_mov_b32 s68, 0x0\n\t" \
  "buffer_load_dwordx4 v0, s[40:43], s68 offen offset:0 lds\n\t" \
  "buffer_load_dwordx4 v1, s[40:43], s68 offen offset:1024 lds\n\t" \
  "buffer_load_dwordx4 v2, s[40:43], s68 offen offset:2048 lds\n\t" \
  "buffer_load_dwordx4 v3, s[40:43], s68 offen offset:3072 lds\n\t" \
  "s_add_u32 m0, s37, 36864\n\t" \
  "s_mov_b32 s68, 0x0\n\t" \
  "s_mov_b32 exec_hi, 0\n\t" \
  "buffer_load_dwordx4 v4, s[40:43], s68 offen offset:0 lds\n\t" \
  "s_mov_b32 exec_hi, -1\n\t" \
  "s_add_u32 m0, s39, 61440\n\t" \
  "s_mov_b32 s68, 0x30000\n\t" \
  "buffer_load_dwordx4 v5, s[44:47], s68 offen offset:0 lds\n\t" \
  "buffer_load_dwordx4 v6, s[44:47], s68 offen offset:1024 lds\n\t" \
  "buffer_load_dwordx4 v7, s[44:47], s68 offen offset:2048 lds\n\t" \
  "s_mov_b32 m0, %[sk]\n\t" \
  "s_mov_b32 exec_hi, 0\n\t" \
  "buffer_load_dword v9, s[40:43], 0 offen lds\n\t" \
  "s_mov_b32 exec_lo, %[wm]\n\t" \
  "buffer_load_dword v10, s[44:47], 0 offen lds\n\t" \
  "s_mov_b64 exec, -1\n\t"
#define LD_ASM2 \
  "s_add_u32 m0, s38, 0\n\t" \
  "s_mov_b32 s68, 0x80\n\t" \
  "buffer_load_dwordx4 v0, s[40:43], s68 offen offset:0 lds\n\t" \
  "s_mov_b32 s85, 0x180000\n\t" \
  "s_load_dword s84, %[pw], s85\n\t" \
  "buffer_load_dwordx4 v1, s[40:43], s68 offen offset:1024 lds\n\t" \
  "s_mov_b32 s85, 0x180800\n\t" \
  "s_load_dword s84, %[pw], s85\n\t" \
  "buffer_load_dwordx4 v2, s[40:43], s68 offen offset:2048 lds\n\t" \
  "s_mov_b32 s85, 0x181000\n\t" \
  "s_load_dword s84, %[pw], s85\n\t" \
  "buffer_load_dwordx4 v3, s[40:43], s68 offen offset:3072 lds\n\t" \
  "s_mov_b32 s85, 0x1b0000\n\t" \
  "s_load_dword s84, %[pw], s85\n\t" \
  "s_add_u32 m0, s37, 0\n\t" \
  "s_mov_b32 s68, 0x80\n\t" \
  "s_mov_b32 exec_hi, 0\n\t" \
  "buffer_load_dwordx4 v4, s[40:43], s68 offen offset:0 lds\n\t" \
  "s_mov_b32 exec_hi, -1\n\t" \
  "s_mov_b32 m0, %[tsk]\n\t" \
  "s_mov_b32 s85, 0x0\n\t" \
  "s_mov_b64 exec, %[hm]\n\t" \
  "buffer_load_dword v16, %[sh], s85 offen lds\n\t" \
  "s_mov_b64 exec, -1\n\t" \
  "s_mov_b32 s68, 0x60000\n\t" \
  "s_mov_b32 s69, 0x90000\n\t" \
  "s_mov_b32 s70, 0x100\n\t" \
  "s_mov_b32 s71, 0x100\n\t" \
  "s_mov_b32 s72, 0xc0000\n\t" \
  "s_mov_b32 s73, 0xf0000\n\t" \
  "s_add_u32 m0, s39, 0\n\t" \
  "s_waitcnt vmcnt(8)\n\t" \
  "s_barrier\n\t" \
  "buffer_load_dwordx4 v5, s[44:47], s68 offen offset:0 lds\n\t" \
  "s_mov_b32 s85, 0x1b0800\n\t" \
  "s_load_dword s84, %[pw], s85\n\t" \
  "buffer_load_dwordx4 v6, s[44:47], s68 offen offset:1024 lds\n\t" \
  "s_mov_b32 s85, 0x1b1000\n\t" \
  "s_load_dword s84, %[pw], s85\n\t" \
  "buffer_load_dwordx4 v7, s[44:47], s68 offen offset:2048 lds\n\t" \
  "s_mov_b32 s85, 0x1e0000\n\t" \
  "s_load_dword s84, %[pw], s85\n\t" \
  "s_add_u32 m0, s39, 12288\n\t" \
  "s_nop 0\n\t" \
  "buffer_load_dwordx4 v5, s[44:47], s69 offen offset:0 lds\n\t" \
  "s_mov_b32 s85, 0x1e0800\n\t" \
  "s_load_dword s84, %[pw], s85\n\t" \
  "buffer_load_dwordx4 v6, s[44:47], s69 offen offset:1024 lds\n\t" \
  "s_mov_b32 s85, 0x1e1000\n\t" \
  "s_load_dword s84, %[pw], s85\n\t" \
  "buffer_load_dwordx4 v7, s[44:47], s69 offen offset:2048 lds\n\t" \
  "s_mov_b32 s85, 0x210000\n\t" \
  "s_load_dword s84, %[pw], s85\n\t" \
  "s_add_u32 m0, s38, 18432\n\t" \
  "s_nop 0\n\t" \
  "buffer_load_dwordx4 v0, s[40:43], s70 offen offset:0 lds\n\t" \
  "buffer_load_dwordx4 v1, s[40:43], s70 offen offset:1024 lds\n\t" \
  "buffer_load_dwordx4 v2, s[40:43], s70 offen offset:2048 lds\n\t" \
  "buffer_load_dwordx4 v3, s[40:43], s70 offen offset:3072 lds\n\t" \
  "s_add_u32 m0, s37, 18432\n\t" \
  "s_nop 0\n\t" \
  "s_mov_b32 exec_hi, 0\n\t" \
  "buffer_load_dwordx4 v4, s[40:43], s71 offen offset:0 lds\n\t" \
  "s_add_u32 m0, s39, 24576\n\t" \
  "s_mov_b32 exec_hi, -1\n\t" \
  "buffer_load_dwordx4 v5, s[44:47], s72 offen offset:0 lds\n\t" \
  "buffer_load_dwordx4 v6, s[44:47], s72 offen offset:1024 lds\n\t" \
  "buffer_load_dwordx4 v7, s[44:47], s72 offen offset:2048 lds\n\t" \
  "s_add_u32 m0, s39, 36864\n\t" \
  "s_nop 0\n\t" \
  "buffer_load_dwordx4 v5, s[44:47], s73 offen offset:0 lds\n\t" \
  "buffer_load_dwordx4 v6, s[44:47], s73 offen offset:1024 lds\n\t" \
  "buffer_load_dwordx4 v7, s[44:47], s73 offen offset:2048 lds\n\t" \
  "s_mov_b32 m0, %[tsk]\n\t" \
  "s_mov_b32 s85, 0x180000\n\t" \
  "s_mov_b64 exec, %[hm]\n\t" \
  "buffer_load_dword v16, %[sh], s85 offen lds\n\t" \
  "s_mov_b64 exec, -1\n\t"
#define LD_ASM3 \
  "s_mov_b32 s68, 0x180\n\t" \
  "s_mov_b32 s69, 0x180\n\t" \
  "s_mov_b32 s70, 0x120000\n\t" \
  "s_mov_b32 s71, 0x150000\n\t" \
  "s_add_u32 m0, s38, 36864\n\t" \
  "s_waitcnt vmcnt(12)\n\t" \
  "s_barrier\n\t" \
  "buffer_load_dwordx4 v0, s[40:43], s68 offen offset:0 lds\n\t" \
  "s_mov_b32 s85, 0x210800\n\t" \
  "s_load_dword s84, %[pw], s85\n\t" \
  "buffer_load_dwordx4 v1, s[40:43], s68 offen offset:1024 lds\n\t" \
  "s_mov_b32 s85, 0x211000\n\t" \
  "s_load_dword s84, %[pw], s85\n\t" \
  "buffer_load_dwordx4 v2, s[40:43], s68 offen offset:2048 lds\n\t" \
  "s_mov_b32 s85, 0x240000\n\t" \
  "s_load_dword s84, %[pw], s85\n\t" \
  "buffer_load_dwordx4 v3, s[40:43], s68 offen offset:3072 lds\n\t" \
  "s_mov_b32 s85, 0x240800\n\t" \
  "s_load_dword s84, %[pw], s85\n\t" \
  "s_add_u32 m0, s37, 36864\n\t" \
  "s_nop 0\n\t" \
  "s_mov_b32 exec_hi, 0\n\t" \
  "buffer_load_dwordx4 v4, s[40:43], s69 offen offset:0 lds\n\t" \
  "s_add_u32 m0, s39, 49152\n\t" \
  "s_mov_b32 exec_hi, -1\n\t" \
  "buffer_load_dwordx4 v5, s[44:47], s70 offen offset:0 lds\n\t" \
  "s_mov_b32 s85, 0x241000\n\t" \
  "s_load_dword s84, %[pw], s85\n\t" \
  "buffer_load_dwordx4 v6, s[44:47], s70 offen offset:1024 lds\n\t" \
  "s_mov_b32 s85, 0x270000\n\t" \
  "s_load_dword s84, %[pw], s85\n\t" \
  "buffer_load_dwordx4 v7, s[44:47], s70 offen offset:2048 lds\n\t" \
  "s_add_u32 m0, s39, 61440\n\t" \
  "s_nop 0\n\t" \
  "buffer_load_dwordx4 v5, s[44:47], s71 offen offset:0 lds\n\t" \
  "buffer_load_dwordx4 v6, s[44:47], s71 offen offset:1024 lds\n\t" \
  "buffer_load_dwordx4 v7, s[44:47], s71 offen offset:2048 lds\n\t" \
  "s_mov_b32 s68, 0x200\n\t" \
  "s_mov_b32 s69, 0x200\n\t" \
  "s_mov_b32 s70, 0x180000\n\t" \
  "s_mov_b32 s71, 0x1b0000\n\t" \
  "s_add_u32 m0, s38, 0\n\t" \
  "s_waitcnt vmcnt(12)\n\t" \
  "s_barrier\n\t" \
  "buffer_load_dwordx4 v0, s[40:43], s68 offen offset:0 lds\n\t" \
  "s_mov_b32 s85, 0x270800\n\t" \
  "s_load_dword s84, %[pw], s85\n\t" \
  "buffer_load_dwordx4 v1, s[40:43], s68 offen offset:1024 lds\n\t" \
  "s_mov_b32 s85, 0x271000\n\t" \
  "s_load_dword s84, %[pw], s85\n\t" \
  "buffer_load_dwordx4 v2, s[40:43], s68 offen offset:2048 lds\n\t" \
  "s_mov_b32 s85, 0x2a0000\n\t" \
  "s_load_dword s84, %[pw], s85\n\t" \
  "buffer_load_dwordx4 v3, s[40:43], s68 offen offset:3072 lds\n\t" \
  "s_mov_b32 s85, 0x2a0800\n\t" \
  "s_load_dword s84, %[pw], s85\n\t" \
  "s_add_u32 m0, s37, 0\n\t" \
  "s_nop 0\n\t" \
  "s_mov_b32 exec_hi, 0\n\t" \
  "buffer_load_dwordx4 v4, s[40:43], s69 offen offset:0 lds\n\t" \
  "s_add_u32 m0, s39, 0\n\t" \
  "s_mov_b32 exec_hi, -1\n\t" \
  "buffer_load_dwordx4 v5, s[44:47], s70 offen offset:0 lds\n\t" \
  "s_mov_b32 s85, 0x2a1000\n\t" \
  "s_load_dword s84, %[pw], s85\n\t" \
  "buffer_load_dwordx4 v6, s[44:47], s70 offen offset:1024 lds\n\t" \
  "s_mov_b32 s85, 0x2d0000\n\t" \
  "s_load_dword s84, %[pw], s85\n\t" \
  "buffer_load_dwordx4 v7, s[44:47], s70 offen offset:2048 lds\n\t" \
  "s_add_u32 m0, s39, 12288\n\t" \
  "s_nop 0\n\t" \
  "buffer_load_dwordx4 v5, s[44:47], s71 offen offset:0 lds\n\t" \
  "buffer_load_dwordx4 v6, s[44:47], s71 offen offset:1024 lds\n\t" \
  "buffer_load_dwordx4 v7, s[44:47], s71 offen offset:2048 lds\n\t" \
  "s_mov_b32 s68, 0x280\n\t" \
  "s_mov_b32 s69, 0x280\n\t" \
  "s_mov_b32 s70, 0x1e0000\n\t" \
  "s_mov_b32 s71, 0x210000\n\t" \
  "s_add_u32 m0, s38, 18432\n\t" \
  "s_waitcnt vmcnt(11)\n\t" \
  "s_barrier\n\t" \
  "buffer_load_dwordx4 v0, s[40:43], s68 offen offset:0 lds\n\t" \
  "s_mov_b32 s85, 0x2d0800\n\t" \
  "s_load_dword s84, %[pw], s85\n\t" \
  "buffer_load_dwordx4 v1, s[40:43], s68 offen offset:1024 lds\n\t" \
  "s_mov_b32 s85, 0x2d1000\n\t" \
  "s_load_dword s84, %[pw], s85\n\t" \
  "buffer_load_dwordx4 v2, s[40:43], s68 offen offset:2048 lds\n\t" \
  "s_mov_b32 s85, 0x300000\n\t" \
  "s_load_dword s84, %[pw], s85\n\t" \
  "buffer_load_dwordx4 v3, s[40:43], s68 offen offset:3072 lds\n\t" \
  "s_mov_b32 s85, 0x300800\n\t" \
  "s_load_dword s84, %[pw], s85\n\t" \
  "s_add_u32 m0, s37, 18432\n\t" \
  "s_nop 0\n\t" \
  "s_mov_b32 exec_hi, 0\n\t" \
  "buffer_load_dwordx4 v4, s[40:43], s69 offen offset:0 lds\n\t" \
  "s_add_u32 m0, s39, 24576\n\t" \
  "s_mov_b32 exec_hi, -1\n\t" \
  "buffer_load_dwordx4 v5, s[44:47], s70 offen offset:0 lds\n\t" \
  "s_mov_b32 s85, 0x301000\n\t" \
  "s_load_dword s84, %[pw], s85\n\t" \
  "buffer_load_dwordx4 v6, s[44:47], s70 offen offset:1024 lds\n\t" \
  "s_mov_b32 s85, 0x330000\n\t" \
  "s_load_dword s84, %[pw], s85\n\t" \
  "buffer_load_dwordx4 v7, s[44:47], s70 offen offset:2048 lds\n\t" \
  "s_add_u32 m0, s39, 36864\n\t" \
  "s_nop 0\n\t" \
  "buffer_load_dwordx4 v5, s[44:47], s71 offen offset:0 lds\n\t" \
  "buffer_load_dwordx4 v6, s[44:47], s71 offen offset:1024 lds\n\t" \
  "buffer_load_dwordx4 v7, s[44:47], s71 offen offset:2048 lds\n\t" \
  "s_mov_b32 s68, 0x300\n\t" \
  "s_mov_b32 s69, 0x300\n\t" \
  "s_mov_b32 s70, 0x240000\n\t" \
  "s_mov_b32 s71, 0x270000\n\t" \
  "s_add_u32 m0, s38, 36864\n\t" \
  "s_waitcnt vmcnt(11)\n\t" \
  "s_barrier\n\t" \
  "buffer_load_dwordx4 v0, s[40:43], s68 offen offset:0 lds\n\t" \
  "s_mov_b32 s85, 0x330800\n\t" \
  "s_load_dword s84, %[pw], s85\n\t" \
  "buffer_load_dwordx4 v1, s[40:43], s68 offen offset:1024 lds\n\t" \
  "s_mov_b32 s85, 0x331000\n\t" \
  "s_load_dword s84, %[pw], s85\n\t" \
  "buffer_load_dwordx4 v2, s[40:43], s68 offen offset:2048 lds\n\t" \
  "s_mov_b32 s85, 0x360000\n\t" \
  "s_load_dword s84, %[pw], s85\n\t" \
  "buffer_load_dwordx4 v3, s[40:43], s68 offen offset:3072 lds\n\t" \
  "s_mov_b32 s85, 0x360800\n\t" \
  "s_load_dword s84, %[pw], s85\n\t" \
  "s_add_u32 m0, s37, 36864\n\t" \
  "s_nop 0\n\t" \
  "s_mov_b32 exec_hi, 0\n\t" \
  "buffer_load_dwordx4 v4, s[40:43], s69 offen offset:0 lds\n\t" \
  "s_add_u32 m0, s39, 49152\n\t" \
  "s_mov_b32 exec_hi, -1\n\t" \
  "buffer_load_dwordx4 v5, s[44:47], s70 offen offset:0 lds\n\t" \
  "s_mov_b32 s85, 0x361000\n\t" \
  "s_load_dword s84, %[pw], s85\n\t" \
  "buffer_load_dwordx4 v6, s[44:47], s70 offen offset:1024 lds\n\t" \
  "s_mov_b32 s85, 0x390000\n\t" \
  "s_load_dword s84, %[pw], s85\n\t" \
  "buffer_load_dwordx4 v7, s[44:47], s70 offen offset:2048 lds\n\t" \
  "s_add_u32 m0, s39, 61440\n\t" \
  "s_nop 0\n\t" \
  "buffer_load_dwordx4 v5, s[44:47], s71 offen offset:0 lds\n\t" \
  "buffer_load_dwordx4 v6, s[44:47], s71 offen offset:1024 lds\n\t" \
  "buffer_load_dwordx4 v7, s[44:47], s71 offen offset:2048 lds\n\t" \
  "s_mov_b32 s68, 0x380\n\t" \
  "s_mov_b32 s69, 0x380\n\t" \
  "s_mov_b32 s70, 0x2a0000\n\t" \
  "s_mov_b32 s71, 0x2d0000\n\t" \
  "s_add_u32 m0, s38, 0\n\t" \
  "s_waitcnt vmcnt(11)\n\t" \
  "s_barrier\n\t" \
  "buffer_load_dwordx4 v0, s[40:43], s68 offen offset:0 lds\n\t" \
  "s_mov_b32 s85, 0x390800\n\t" \
  "s_load_dword s84, %[pw], s85\n\t" \
  "buffer_load_dwordx4 v1, s[40:43], s68 offen offset:1024 lds\n\t" \
  "s_mov_b32 s85, 0x391000\n\t" \
  "s_load_dword s84, %[pw], s85\n\t" \
  "buffer_load_dwordx4 v2, s[40:43], s68 offen offset:2048 lds\n\t" \
  "s_mov_b32 s85, 0x3c0000\n\t" \
  "s_load_dword s84, %[pw], s85\n\t" \
  "buffer_load_dwordx4 v3, s[40:43], s68 offen offset:3072 lds\n\t" \
  "s_mov_b32 s85, 0x3c0800\n\t" \
  "s_load_dword s84, %[pw], s85\n\t" \
  "s_add_u32 m0, s37, 0\n\t" \
  "s_nop 0\n\t" \
  "s_mov_b32 exec_hi, 0\n\t" \
  "buffer_load_dwordx4 v4, s[40:43], s69 offen offset:0 lds\n\t" \
  "s_add_u32 m0, s39, 0\n\t" \
  "s_mov_b32 exec_hi, -1\n\t" \
  "buffer_load_dwordx4 v5, s[44:47], s70 offen offset:0 lds\n\t" \
  "s_mov_b32 s85, 0x3c1000\n\t" \
  "s_load_dword s84, %[pw], s85\n\t" \
  "buffer_load_dwordx4 v6, s[44:47], s70 offen offset:1024 lds\n\t" \
  "s_mov_b32 s85, 0x3f0000\n\t" \
  "s_load_dword s84, %[pw], s85\n\t" \
  "buffer_load_dwordx4 v7, s[44:47], s70 offen offset:2048 lds\n\t" \
  "s_add_u32 m0, s39, 12288\n\t" \
  "s_nop 0\n\t" \
  "buffer_load_dwordx4 v5, s[44:47], s71 offen offset:0 lds\n\t" \
  "buffer_load_dwordx4 v6, s[44:47], s71 offen offset:1024 lds\n\t" \
  "buffer_load_dwordx4 v7, s[44:47], s71 offen offset:2048 lds\n\t" \
  "s_mov_b32 s68, 0x400\n\t" \
  "s_mov_b32 s69, 0x400\n\t" \
  "s_mov_b32 s70, 0x300000\n\t" \
  "s_mov_b32 s71, 0x330000\n\t" \
  "s_add_u32 m0, s38, 18432\n\t" \
  "s_waitcnt vmcnt(11)\n\t" \
  "s_barrier\n\t" \
  "buffer_load_dwordx4 v0, s[40:43], s68 offen offset:0 lds\n\t" \
  "s_mov_b32 s85, 0x3f0800\n\t" \
  "s_load_dword s84, %[pw], s85\n\t" \
  "buffer_load_dwordx4 v1, s[40:43], s68 offen offset:1024 lds\n\t" \
  "s_mov_b32 s85, 0x3f1000\n\t" \
  "s_load_dword s84, %[pw], s85\n\t" \
  "buffer_load_dwordx4 v2, s[40:43], s68 offen offset:2048 lds\n\t" \
  "s_mov_b32 s85, 0x420000\n\t" \
  "s_load_dword s84, %[pw], s85\n\t" \
  "buffer_load_dwordx4 v3, s[40:43], s68 offen offset:3072 lds\n\t" \
  "s_mov_b32 s85, 0x420800\n\t" \
  "s_load_dword s84, %[pw], s85\n\t" \
  "s_add_u32 m0, s37, 18432\n\t" \
  "s_nop 0\n\t" \
  "s_mov_b32 exec_hi, 0\n\t" \
  "buffer_load_dwordx4 v4, s[40:43], s69 offen offset:0 lds\n\t" \
  "s_add_u32 m0, s39, 24576\n\t" \
  "s_mov_b32 exec_hi, -1\n\t" \
  "buffer_load_dwordx4 v5, s[44:47], s70 offen offset:0 lds\n\t" \
  "s_mov_b32 s85, 0x421000\n\t" \
  "s_load_dword s84, %[pw], s85\n\t" \
  "buffer_load_dwordx4 v6, s[44:47], s70 offen offset:1024 lds\n\t" \
  "s_mov_b32 s85, 0x450000\n\t" \
  "s_load_dword s84, %[pw], s85\n\t" \
  "buffer_load_dwordx4 v7, s[44:47], s70 offen offset:2048 lds\n\t" \
  "s_add_u32 m0, s39, 36864\n\t" \
  "s_nop 0\n\t" \
  "buffer_load_dwordx4 v5, s[44:47], s71 offen offset:0 lds\n\t" \
  "buffer_load_dwordx4 v6, s[44:47], s71 offen offset:1024 lds\n\t" \
  "buffer_load_dwordx4 v7, s[44:47], s71 offen offset:2048 lds\n\t" \
  "s_mov_b32 s68, 0x480\n\t" \
  "s_mov_b32 s69, 0x480\n\t" \
  "s_mov_b32 s70, 0x360000\n\t" \
  "s_mov_b32 s71, 0x390000\n\t" \
  "s_add_u32 m0, s38, 36864\n\t" \
  "s_waitcnt vmcnt(11)\n\t" \
  "s_barrier\n\t" \
  "buffer_load_dwordx4 v0, s[40:43], s68 offen offset:0 lds\n\t" \
  "s_mov_b32 s85, 0x450800\n\t" \
  "s_load_dword s84, %[pw], s85\n\t" \
  "buffer_load_dwordx4 v1, s[40:43], s68 offen offset:1024 lds\n\t" \
  "s_mov_b32 s85, 0x451000\n\t" \
  "s_load_dword s84, %[pw], s85\n\t" \
  "buffer_load_dwordx4 v2, s[40:43], s68 offen offset:2048 lds\n\t" \
  "s_mov_b32 s85, 0x480000\n\t" \
  "s_load_dword s84, %[pw], s85\n\t" \
  "buffer_load_dwordx4 v3, s[40:43], s68 offen offset:3072 lds\n\t" \
  "s_mov_b32 s85, 0x480800\n\t" \
  "s_load_dword s84, %[pw], s85\n\t" \
  "s_add_u32 m0, s37, 36864\n\t" \
  "s_nop 0\n\t" \
  "s_mov_b32 exec_hi, 0\n\t" \
  "buffer_load_dwordx4 v4, s[40:43], s69 offen offset:0 lds\n\t" \
  "s_add_u32 m0, s39, 49152\n\t" \
  "s_mov_b32 exec_hi, -1\n\t" \
  "buffer_load_dwordx4 v5, s[44:47], s70 offen offset:0 lds\n\t" \
  "s_mov_b32 s85, 0x481000\n\t" \
  "s_load_dword s84, %[pw], s85\n\t" \
  "buffer_load_dwordx4 v6, s[44:47], s70 offen offset:1024 lds\n\t" \
  "s_mov_b32 s85, 0x4b0000\n\t" \
  "s_load_dword s84, %[pw], s85\n\t" \
  "buffer_load_dwordx4 v7, s[44:47], s70 offen offset:2048 lds\n\t" \
  "s_add_u32 m0, s39, 61440\n\t" \
  "s_nop 0\n\t" \
  "buffer_load_dwordx4 v5, s[44:47], s71 offen offset:0 lds\n\t" \
  "buffer_load_dwordx4 v6, s[44:47], s71 offen offset:1024 lds\n\t" \
  "buffer_load_dwordx4 v7, s[44:47], s71 offen offset:2048 lds\n\t" \
  "s_mov_b32 s68, 0x500\n\t" \
  "s_mov_b32 s69, 0x500\n\t" \
  "s_mov_b32 s70, 0x3c0000\n\t" \
  "s_mov_b32 s71, 0x3f0000\n\t" \
  "s_add_u32 m0, s38, 0\n\t" \
  "s_waitcnt vmcnt(11)\n\t" \
  "s_barrier\n\t" \
  "buffer_load_dwordx4 v0, s[40:43], s68 offen offset:0 lds\n\t" \
  "s_mov_b32 s85, 0x4b0800\n\t" \
  "s_load_dword s84, %[pw], s85\n\t" \
  "buffer_load_dwordx4 v1, s[40:43], s68 offen offset:1024 lds\n\t" \
  "s_mov_b32 s85, 0x4b1000\n\t" \
  "s_load_dword s84, %[pw], s85\n\t" \
  "buffer_load_dwordx4 v2, s[40:43], s68 offen offset:2048 lds\n\t" \
  "s_mov_b32 s85, 0x4e0000\n\t" \
  "s_load_dword s84, %[pw], s85\n\t" \
  "buffer_load_dwordx4 v3, s[40:43], s68 offen offset:3072 lds\n\t" \
  "s_mov_b32 s85, 0x4e0800\n\t" \
  "s_load_dword s84, %[pw], s85\n\t" \
  "s_add_u32 m0, s37, 0\n\t" \
  "s_nop 0\n\t" \
  "s_mov_b32 exec_hi, 0\n\t" \
  "buffer_load_dwordx4 v4, s[40:43], s69 offen offset:0 lds\n\t" \
  "s_add_u32 m0, s39, 0\n\t" \
  "s_mov_b32 exec_hi, -1\n\t" \
  "buffer_load_dwordx4 v5, s[44:47], s70 offen offset:0 lds\n\t" \
  "s_mov_b32 s85, 0x4e1000\n\t" \
  "s_load_dword s84, %[pw], s85\n\t" \
  "buffer_load_dwordx4 v6, s[44:47], s70 offen offset:1024 lds\n\t" \
  "s_mov_b32 s85, 0x510000\n\t" \
  "s_load_dword s84, %[pw], s85\n\t" \
  "buffer_load_dwordx4 v7, s[44:47], s70 offen offset:2048 lds\n\t" \
  "s_add_u32 m0, s39, 12288\n\t" \
  "s_nop 0\n\t" \
  "buffer_load_dwordx4 v5, s[44:47], s71 offen offset:0 lds\n\t" \
  "buffer_load_dwordx4 v6, s[44:47], s71 offen offset:1024 lds\n\t" \
  "buffer_load_dwordx4 v7, s[44:47], s71 offen offset:2048 lds\n\t" \
  "s_mov_b32 s68, 0x580\n\t" \
  "s_mov_b32 s69, 0x580\n\t" \
  "s_mov_b32 s70, 0x420000\n\t" \
  "s_mov_b32 s71, 0x450000\n\t" \
  "s_add_u32 m0, s38, 18432\n\t" \
  "s_waitcnt vmcnt(11)\n\t" \
  "s_barrier\n\t" \
  "buffer_load_dwordx4 v0, s[40:43], s68 offen offset:0 lds\n\t" \
  "s_mov_b32 s85, 0x510800\n\t" \
  "s_load_dword s84, %[pw], s85\n\t" \
  "buffer_load_dwordx4 v1, s[40:43], s68 offen offset:1024 lds\n\t" \
  "s_mov_b32 s85, 0x511000\n\t" \
  "s_load_dword s84, %[pw], s85\n\t" \
  "buffer_load_dwordx4 v2, s[40:43], s68 offen offset:2048 lds\n\t" \
  "s_mov_b32 s85, 0x540000\n\t" \
  "s_load_dword s84, %[pw], s85\n\t" \
  "buffer_load_dwordx4 v3, s[40:43], s68 offen offset:3072 lds\n\t" \
  "s_mov_b32 s85, 0x540800\n\t" \
  "s_load_dword s84, %[pw], s85\n\t" \
  "s_add_u32 m0, s37, 18432\n\t" \
  "s_nop 0\n\t" \
  "s_mov_b32 exec_hi, 0\n\t" \
  "buffer_load_dwordx4 v4, s[40:43], s69 offen offset:0 lds\n\t" \
  "s_add_u32 m0, s39, 24576\n\t" \
  "s_mov_b32 exec_hi, -1\n\t" \
  "buffer_load_dwordx4 v5, s[44:47], s70 offen offset:0 lds\n\t" \
  "s_mov_b32 s85, 0x541000\n\t" \
  "s_load_dword s84, %[pw], s85\n\t" \
  "buffer_load_dwordx4 v6, s[44:47], s70 offen offset:1024 lds\n\t" \
  "s_mov_b32 s85, 0x570000\n\t" \
  "s_load_dword s84, %[pw], s85\n\t" \
  "buffer_load_dwordx4 v7, s[44:47], s70 offen offset:2048 lds\n\t" \
  "s_add_u32 m0, s39, 36864\n\t" \
  "s_nop 0\n\t" \
  "buffer_load_dwordx4 v5, s[44:47], s71 offen offset:0 lds\n\t" \
  "buffer_load_dwordx4 v6, s[44:47], s71 offen offset:1024 lds\n\t" \
  "buffer_load_dwordx4 v7, s[44:47], s71 offen offset:2048 lds\n\t" \
  "s_mov_b32 s68, 0x600\n\t" \
  "s_mov_b32 s69, 0x600\n\t" \
  "s_mov_b32 s70, 0x480000\n\t" \
  "s_mov_b32 s71, 0x4b0000\n\t" \
  "s_add_u32 m0, s38, 36864\n\t" \
  "s_waitcnt vmcnt(11)\n\t" \
  "s_barrier\n\t" \
  "buffer_load_dwordx4 v0, s[40:43], s68 offen offset:0 lds\n\t" \
  "s_mov_b32 s85, 0x570800\n\t" \
  "s_load_dword s84, %[pw], s85\n\t" \
  "buffer_load_dwordx4 v1, s[40:43], s68 offen offset:1024 lds\n\t" \
  "s_mov_b32 s85, 0x571000\n\t" \
  "s_load_dword s84, %[pw], s85\n\t" \
  "buffer_load_dwordx4 v2, s[40:43], s68 offen offset:2048 lds\n\t" \
  "s_mov_b32 s85, 0x5a0000\n\t" \
  "s_load_dword s84, %[pw], s85\n\t" \
  "buffer_load_dwordx4 v3, s[40:43], s68 offen offset:3072 lds\n\t" \
  "s_mov_b32 s85, 0x5a0800\n\t" \
  "s_load_dword s84, %[pw], s85\n\t" \
  "s_add_u32 m0, s37, 36864\n\t" \
  "s_nop 0\n\t" \
  "s_mov_b32 exec_hi, 0\n\t" \
  "buffer_load_dwordx4 v4, s[40:43], s69 offen offset:0 lds\n\t" \
  "s_add_u32 m0, s39, 49152\n\t" \
  "s_mov_b32 exec_hi, -1\n\t" \
  "buffer_load_dwordx4 v5, s[44:47], s70 offen offset:0 lds\n\t" \
  "s_mov_b32 s85, 0x5a1000\n\t" \
  "s_load_dword s84, %[pw], s85\n\t" \
  "buffer_load_dwordx4 v6, s[44:47], s70 offen offset:1024 lds\n\t" \
  "s_mov_b32 s85, 0x5d0000\n\t" \
  "s_load_dword s84, %[pw], s85\n\t" \
  "buffer_load_dwordx4 v7, s[44:47], s70 offen offset:2048 lds\n\t" \
  "s_add_u32 m0, s39, 61440\n\t" \
  "s_nop 0\n\t" \
  "buffer_load_dwordx4 v5, s[44:47], s71 offen offset:0 lds\n\t" \
  "buffer_load_dwordx4 v6, s[44:47], s71 offen offset:1024 lds\n\t" \
  "buffer_load_dwordx4 v7, s[44:47], s71 offen offset:2048 lds\n\t" \
  "s_mov_b32 s68, 0x680\n\t" \
  "s_mov_b32 s69, 0x680\n\t" \
  "s_mov_b32 s70, 0x4e0000\n\t" \
  "s_mov_b32 s71, 0x510000\n\t" \
  "s_add_u32 m0, s38, 0\n\t" \
  "s_waitcnt vmcnt(11)\n\t" \
  "s_barrier\n\t" \
  "buffer_load_dwordx4 v0, s[40:43], s68 offen offset:0 lds\n\t" \
  "s_load_dword s84, %[pq], 0x0\n\t" \
  "buffer_load_dwordx4 v1, s[40:43], s68 offen offset:1024 lds\n\t" \
  "s_load_dword s84, %[pq], 0x40\n\t" \
  "buffer_load_dwordx4 v2, s[40:43], s68 offen offset:2048 lds\n\t" \
  "s_load_dword s84, %[pq], 0x800\n\t" \
  "buffer_load_dwordx4 v3, s[40:43], s68 offen offset:3072 lds\n\t" \
  "s_load_dword s84, %[pq], 0x840\n\t" \
  "s_add_u32 m0, s37, 0\n\t" \
  "s_nop 0\n\t" \
  "s_mov_b32 exec_hi, 0\n\t" \
  "buffer_load_dwordx4 v4, s[40:43], s69 offen offset:0 lds\n\t" \
  "s_add_u32 m0, s39, 0\n\t" \
  "s_mov_b32 exec_hi, -1\n\t" \
  "buffer_load_dwordx4 v5, s[44:47], s70 offen offset:0 lds\n\t" \
  "s_load_dword s84, %[pq], 0x1000\n\t" \
  "buffer_load_dwordx4 v6, s[44:47], s70 offen offset:1024 lds\n\t" \
  "s_load_dword s84, %[pq], 0x1040\n\t" \
  "buffer_load_dwordx4 v7, s[44:47], s70 offen offset:2048 lds\n\t" \
  "s_mov_b32 s85, 0x5d0800\n\t" \
  "s_load_dword s84, %[pw], s85\n\t" \
  "s_add_u32 m0, s39, 12288\n\t" \
  "s_nop 0\n\t" \
  "buffer_load_dwordx4 v5, s[44:47], s71 offen offset:0 lds\n\t" \
  "s_mov_b32 s85, 0x5d1000\n\t" \
  "s_load_dword s84, %[pw], s85\n\t" \
  "buffer_load_dwordx4 v6, s[44:47], s71 offen offset:1024 lds\n\t" \
  "buffer_load_dwordx4 v7, s[44:47], s71 offen offset:2048 lds\n\t" \
  "s_mov_b32 s68, 0x700\n\t" \
  "s_mov_b32 s69, 0x700\n\t" \
  "s_mov_b32 s70, 0x540000\n\t" \
  "s_mov_b32 s71, 0x570000\n\t" \
  "s_add_u32 m0, s38, 18432\n\t" \
  "s_waitcnt vmcnt(11)\n\t" \
  "s_barrier\n\t" \
  "buffer_load_dwordx4 v0, s[40:43], s68 offen offset:0 lds\n\t" \
  "s_load_dword s84, %[pq], 0x1800\n\t" \
  "buffer_load_dwordx4 v1, s[40:43], s68 offen offset:1024 lds\n\t" \
  "s_load_dword s84, %[pq], 0x1840\n\t" \
  "buffer_load_dwordx4 v2, s[40:43], s68 offen offset:2048 lds\n\t" \
  "s_load_dword s84, %[pq], 0x2000\n\t" \
  "buffer_load_dwordx4 v3, s[40:43], s68 offen offset:3072 lds\n\t" \
  "s_load_dword s84, %[pq], 0x2040\n\t" \
  "s_add_u32 m0, s37, 18432\n\t" \
  "s_nop 0\n\t" \
  "s_mov_b32 exec_hi, 0\n\t" \
  "buffer_load_dwordx4 v4, s[40:43], s69 offen offset:0 lds\n\t" \
  "s_add_u32 m0, s39, 24576\n\t" \
  "s_mov_b32 exec_hi, -1\n\t" \
  "buffer_load_dwordx4 v5, s[44:47], s70 offen offset:0 lds\n\t" \
  "s_load_dword s84, %[pq], 0x2800\n\t" \
  "buffer_load_dwordx4 v6, s[44:47], s70 offen offset:1024 lds\n\t" \
  "s_load_dword s84, %[pq], 0x2840\n\t" \
  "buffer_load_dwordx4 v7, s[44:47], s70 offen offset:2048 lds\n\t" \
  "s_add_u32 m0, s39, 36864\n\t" \
  "s_nop 0\n\t" \
  "buffer_load_dwordx4 v5, s[44:47], s71 offen offset:0 lds\n\t" \
  "buffer_load_dwordx4 v6, s[44:47], s71 offen offset:1024 lds\n\t" \
  "buffer_load_dwordx4 v7, s[44:47], s71 offen offset:2048 lds\n\t" \
  "s_mov_b32 s68, 0x780\n\t" \
  "s_mov_b32 s69, 0x780\n\t" \
  "s_mov_b32 s70, 0x5a0000\n\t" \
  "s_mov_b32 s71, 0x5d0000\n\t" \
  "s_add_u32 m0, s38, 36864\n\t" \
  "s_waitcnt vmcnt(11)\n\t" \
  "s_barrier\n\t" \
  "buffer_load_dwordx4 v0, s[40:43], s68 offen offset:0 lds\n\t" \
  "s_load_dword s84, %[pq], 0x3000\n\t" \
  "buffer_load_dwordx4 v1, s[40:43], s68 offen offset:1024 lds\n\t" \
  "s_load_dword s84, %[pq], 0x3040\n\t" \
  "buffer_load_dwordx4 v2, s[40:43], s68 offen offset:2048 lds\n\t" \
  "s_load_dword s84, %[pq], 0x3800\n\t" \
  "buffer_load_dwordx4 v3, s[40:43], s68 offen offset:3072 lds\n\t" \
  "s_load_dword s84, %[pq], 0x3840\n\t" \
  "s_add_u32 m0, s37, 36864\n\t" \
  "s_nop 0\n\t" \
  "s_mov_b32 exec_hi, 0\n\t" \
  "buffer_load_dwordx4 v4, s[40:43], s69 offen offset:0 lds\n\t" \
  "s_add_u32 m0, s39, 49152\n\t" \
  "s_mov_b32 exec_hi, -1\n\t" \
  "buffer_load_dwordx4 v5, s[44:47], s70 offen offset:0 lds\n\t" \
  "buffer_load_dwordx4 v6, s[44:47], s70 offen offset:1024 lds\n\t" \
  "buffer_load_dwordx4 v7, s[44:47], s70 offen offset:2048 lds\n\t" \
  "s_add_u32 m0, s39, 61440\n\t" \
  "s_nop 0\n\t" \
  "buffer_load_dwordx4 v5, s[44:47], s71 offen offset:0 lds\n\t" \
  "buffer_load_dwordx4 v6, s[44:47], s71 offen offset:1024 lds\n\t" \
  "buffer_load_dwordx4 v7, s[44:47], s71 offen offset:2048 lds\n\t" \
  "v_readlane_b32 s68, v9, 18\n\t" \
  "v_readlane_b32 s69, v9, 19\n\t" \
  "v_readlane_b32 s70, v9, 20\n\t" \
  "v_readlane_b32 s71, v9, 21\n\t" \
  "v_readlane_b32 s72, v9, 0\n\t" \
  "v_readlane_b32 s73, v9, 1\n\t" \
  "v_readlane_b32 s74, v9, 2\n\t" \
  "v_readlane_b32 s75, v9, 3\n\t" \
  "v_readlane_b32 s76, v9, 4\n\t" \
  "v_readlane_b32 s77, v9, 5\n\t" \
  "s_add_u32 m0, %[ob], 73728\n\t" \
  "s_waitcnt vmcnt(11)\n\t" \
  "s_barrier\n\t" \
  "s_nop 2\n\t" \
  "buffer_load_dwordx4 v8, %[so], s68 offen lds\n\t" \
  "s_add_u32 m0, %[ob], 77824\n\t" \
  "s_nop 0\n\t" \
  "buffer_load_dwordx4 v8, %[so], s69 offen lds\n\t" \
  "s_add_u32 m0, %[ob], 81920\n\t" \
  "s_nop 0\n\t" \
  "buffer_load_dwordx4 v8, %[so], s70 offen lds\n\t" \
  "s_add_u32 m0, %[ob], 86016\n\t" \
  "s_nop 0\n\t" \
  "buffer_load_dwordx4 v8, %[so], s71 offen lds\n\t" \
  "s_add_u32 m0, %[ob], 0\n\t" \
  "s_nop 0\n\t" \
  "buffer_load_dwordx4 v8, %[so], s72 offen lds\n\t" \
  "s_add_u32 m0, %[ob], 4096\n\t" \
  "s_nop 0\n\t" \
  "buffer_load_dwordx4 v8, %[so], s73 offen lds\n\t" \
  "s_add_u32 m0, %[ob], 8192\n\t" \
  "s_nop 0\n\t" \
  "buffer_load_dwordx4 v8, %[so], s74 offen lds\n\t" \
  "s_add_u32 m0, %[ob], 12288\n\t" \
  "s_nop 0\n\t" \
  "buffer_load_dwordx4 v8, %[so], s75 offen lds\n\t" \
  "s_add_u32 m0, %[ob], 16384\n\t" \
  "s_nop 0\n\t" \
  "buffer_load_dwordx4 v8, %[so], s76 offen lds\n\t" \
  "s_add_u32 m0, %[ob], 20480\n\t" \
  "s_nop 0\n\t" \
  "buffer_load_dwordx4 v8, %[so], s77 offen lds\n\t" \
  "s_waitcnt vmcnt(10)\n\t" \
  "s_barrier\n\t" \
  "s_nop 2\n\t" \
  "s_add_u32 m0, %[ob], 90112\n\t" \
  "v_readlane_b32 s68, v9, 22\n\t" \
  "s_nop 4\n\t" \
  "buffer_load_dwordx4 v8, %[so], s68 offen lds\n\t" \
  "s_add_u32 m0, %[ob], 94208\n\t" \
  "v_readlane_b32 s68, v9, 23\n\t" \
  "s_nop 4\n\t" \
  "buffer_load_dwordx4 v8, %[so], s68 offen lds\n\t" \
  "s_add_u32 m0, %[ob], 98304\n\t" \
  "v_readlane_b32 s68, v9, 24\n\t" \
  "s_nop 4\n\t" \
  "buffer_load_dwordx4 v8, %[so], s68 offen lds\n\t" \
  "s_add_u32 m0, %[ob], 102400\n\t" \
  "v_readlane_b32 s68, v9, 25\n\t" \
  "s_nop 4\n\t" \
  "buffer_load_dwordx4 v8, %[so], s68 offen lds\n\t" \
  "s_add_u32 m0, %[ob], 106496\n\t" \
  "v_readlane_b32 s68, v9, 26\n\t" \
  "s_nop 4\n\t" \
  "buffer_load_dwordx4 v8, %[so], s68 offen lds\n\t" \
  "s_add_u32 m0, %[ob], 24576\n\t" \
  "v_readlane_b32 s68, v9, 6\n\t" \
  "s_nop 4\n\t" \
  "buffer_load_dwordx4 v8, %[so], s68 offen lds\n\t" \
  "s_add_u32 m0, %[ob], 28672\n\t" \
  "v_readlane_b32 s68, v9, 7\n\t" \
  "s_nop 4\n\t" \
  "buffer_load_dwordx4 v8, %[so], s68 offen lds\n\t" \
  "s_add_u32 m0, %[ob], 32768\n\t" \
  "v_readlane_b32 s68, v9, 8\n\t" \
  "s_nop 4\n\t" \
  "buffer_load_dwordx4 v8, %[so], s68 offen lds\n\t" \
  "s_add_u32 m0, %[ob], 36864\n\t" \
  "v_readlane_b32 s68, v9, 9\n\t" \
  "s_nop 4\n\t" \
  "buffer_load_dwordx4 v8, %[so], s68 offen lds\n\t" \
  "s_add_u32 m0, %[ob], 40960\n\t" \
  "v_readlane_b32 s68, v9, 10\n\t" \
  "s_nop 4\n\t" \
  "buffer_load_dwordx4 v8, %[so], s68 offen lds\n\t" \
  "s_add_u32 m0, %[ob], 45056\n\t" \
  "v_readlane_b32 s68, v9, 11\n\t" \
  "s_nop 4\n\t" \
  "buffer_load_dwordx4 v8, %[so], s68 offen lds\n\t" \
  "s_nop 3\n\t" \
  "s_barrier\n\t" \
  "s_nop 2\n\t" \
  "s_add_u32 m0, %[ob], 110592\n\t" \
  "v_readlane_b32 s68, v9, 27\n\t" \
  "s_nop 4\n\t" \
  "buffer_load_dwordx4 v8, %[so], s68 offen lds\n\t" \
  "s_add_u32 m0, %[ob], 114688\n\t" \
  "v_readlane_b32 s68, v9, 28\n\t" \
  "s_nop 4\n\t" \
  "buffer_load_dwordx4 v8, %[so], s68 offen lds\n\t" \
  "s_add_u32 m0, %[ob], 118784\n\t" \
  "v_readlane_b32 s68, v9, 29\n\t" \
  "s_nop 4\n\t" \
  "buffer_load_dwordx4 v8, %[so], s68 offen lds\n\t" \
  "s_add_u32 m0, %[ob], 122880\n\t" \
  "v_readlane_b32 s68, v9, 30\n\t" \
  "s_nop 4\n\t" \
  "buffer_load_dwordx4 v8, %[so], s68 offen lds\n\t" \
  "s_add_u32 m0, %[ob], 126976\n\t" \
  "v_readlane_b32 s68, v9, 31\n\t" \
  "s_nop 4\n\t" \
  "buffer_load_dwordx4 v8, %[so], s68 offen lds\n\t" \
  "s_add_u32 m0, %[ob], 49152\n\t" \
  "v_readlane_b32 s68, v9, 12\n\t" \
  "s_nop 4\n\t" \
  "buffer_load_dwordx4 v8, %[so], s68 offen lds\n\t" \
  "s_add_u32 m0, %[ob], 53248\n\t" \
  "v_readlane_b32 s68, v9, 13\n\t" \
  "s_nop 4\n\t" \
  "buffer_load_dwordx4 v8, %[so], s68 offen lds\n\t" \
  "s_add_u32 m0, %[ob], 57344\n\t" \
  "v_readlane_b32 s68, v9, 14\n\t" \
  "s_nop 4\n\t" \
  "buffer_load_dwordx4 v8, %[so], s68 offen lds\n\t" \
  "s_add_u32 m0, %[ob], 61440\n\t" \
  "v_readlane_b32 s68, v9, 15\n\t" \
  "s_nop 4\n\t" \
  "buffer_load_dwordx4 v8, %[so], s68 offen lds\n\t" \
  "s_add_u32 m0, %[ob], 65536\n\t" \
  "v_readlane_b32 s68, v9, 16\n\t" \
  "s_nop 4\n\t" \
  "buffer_load_dwordx4 v8, %[so], s68 offen lds\n\t" \
  "s_add_u32 m0, %[ob], 69632\n\t" \
  "v_readlane_b32 s68, v9, 17\n\t" \
  "s_nop 4\n\t" \
  "buffer_load_dwordx4 v8, %[so], s68 offen lds\n\t" \
  "s_waitcnt vmcnt(0) lgkmcnt(0)\n\t"
#define LD_NBAR 17
#define LD_CO 0
#define LD_X5 1
#define LD_HT 8
#define LD_TP 3
#define LD_CLOB "v11", "s68", "s69", "s70", "s71", "s72", "s73", "s74", "s75", "s76", "s77", "s78", "s79", "s80", "s81", "s82", "s83", "s85"

#define ENDW
#define TS(i)
#define TSS(i)
#define TOUT(n)

#define SPIN(cond,slp) do{ int sp_=0; while(cond){ __builtin_amdgcn_s_sleep(slp); if(++sp_>(1<<22)) break; } }while(0)
__device__ __forceinline__ unsigned rd1(const char* p){ return __builtin_amdgcn_readfirstlane(*(volatile LDS_AS unsigned*)(LDS_AS char*)p); }
__device__ __forceinline__ unsigned lds_inc(const char* p, int l){ unsigned r=0; if(l==0) r=__hip_atomic_fetch_add((LDS_AS unsigned*)(LDS_AS char*)p, 1u, __ATOMIC_RELAXED, __HIP_MEMORY_SCOPE_WORKGROUP); return __builtin_amdgcn_readfirstlane(r); }
__device__ __forceinline__ void lds_add(unsigned adr){ asm volatile("s_mov_b64 exec, 1\n\tds_add_u32 %0, %1\n\ts_mov_b64 exec, -1" :: "v"(adr), "v"(1u) : "memory"); }
__device__ __forceinline__ void publish(unsigned cnt, const unsigned* base, unsigned voff, unsigned tag){
  unsigned t;
  asm volatile("s_waitcnt vmcnt(0)\n\ts_mov_b64 exec, 1\n\tds_add_rtn_u32 %0, %1, %2\n\ts_waitcnt lgkmcnt(0)\n\tv_cmp_eq_u32 vcc, 3, %0\n\ts_cbranch_vccz PB_%=\n\t"
    "s_mov_b64 exec, 0xffff\n\tglobal_store_dword %3, %4, %5 sc1\n\tPB_%=:\n\ts_mov_b64 exec, -1"
    : "=&v"(t) : "v"(cnt), "v"(1u), "v"(voff), "v"(tag), "s"(base) : "memory", "vcc");
}
__device__ __forceinline__ void bars(int n){
  asm volatile("BL_%=:\n\ts_barrier\n\ts_sub_u32 %0, %0, 1\n\ts_cmp_lg_u32 %0, 0\n\ts_cbranch_scc1 BL_%=" : "+s"(n) :: "memory", "scc");
}
#define barc(rem) barc_(rem)
#define CTRA
#define CTRP
__device__ __forceinline__ void barc_(unsigned& rem){
  asm volatile("s_cmp_eq_u32 %0, 0\n\ts_cbranch_scc1 BC_%=\n\ts_barrier\n\ts_sub_u32 %0, %0, 1\n\tBC_%=:" : "+s"(rem) :: "memory", "scc");
}
template<int SLP> __device__ __forceinline__ void wait_flag_b(const unsigned* p, unsigned voff, unsigned tag, unsigned& rem CTRA){
  int spins=0;
  while(true){
    unsigned v; asm volatile("global_load_dword %0, %1, %2 sc1" : "=v"(v) : "v"(voff), "s"(p) : "memory");
    barc(rem); if(PB2) barc(rem);
    asm volatile("s_waitcnt vmcnt(0)" : "+v"(v) :: "memory");
    if(__all(v==tag) || ++spins>(1<<21)) break;
    if(rem==0) __builtin_amdgcn_s_sleep(SLP);
  }
}
__device__ __forceinline__ void stout(void* sbase, unsigned voff, u32x4 v){
  st16cs(sbase,voff,v);
}

typedef unsigned u32x8 __attribute__((ext_vector_type(8)));
typedef unsigned u32x16 __attribute__((ext_vector_type(16)));
__device__ __forceinline__ char* ptr64(unsigned lo, unsigned hi){ return (char*)(((unsigned long long)hi<<32)|lo); }
__device__ __forceinline__ u32x4 srd(const char* p, unsigned bytes){ return u32x4{(unsigned)(size_t)p,(unsigned)((size_t)p>>32)&0xffffu,bytes,0x20000u}; }
constexpr size_t WS_O=0, WS_KV=(size_t)S*DM*2, WS_Y=(size_t)S*DM*4, WS_FL=(size_t)S*DM*12;

__global__ __launch_bounds__(768,3) void fused_kernel(const u16* x_, const u16* wqkv_, const u16* wo_, char* ws_, u16* out_,
    const u16* kin_, const u16* vin_, const u16* shk_, const u16* hk_, const u16* qin_, const u16* sD_)
{
  __shared__ __attribute__((aligned(128))) char smem[ALL_LDS];
  TS(0);
  int wv;
  asm volatile(
      "s_load_dwordx16 s[48:63], %[ka], 0x0\n\ts_load_dwordx4 s[64:67], %[ka], 0x40\n\ts_load_dwordx2 s[78:79], %[ka], 0x50\n\ts_getpc_b64 s[72:73]\n\tv_readfirstlane_b32 s75, v0\n\ts_lshr_b32 s74, %[b], 3\n\ts_mul_i32 s74, s74, 12\n\ts_lshr_b32 s75, s75, 6\n\t"
      "s_add_u32 s74, s74, s75\n\ts_min_u32 s74, s74, %[cl]\n\ts_lshl_b32 s74, s74, 7\n\ts_mov_b32 m0, %[sk]\n\tv_mov_b32 v1, s74\n\ts_mov_b64 exec, 1\n\tglobal_load_lds_dword v1, s[72:73]\n\ts_mov_b64 exec, -1\n\t"
      "s_mov_b32 s76, %[b]\n\ts_cmp_lt_u32 s75, 4\n\ts_cbranch_scc1 FK_G\n\ts_cmp_lt_u32 s75, 8\n\ts_cbranch_scc0 FK_C\n\t"
      "v_and_b32 v9, 7, v0\n\tv_and_b32 v10, 63, v0\n\ts_lshl_b32 s74, %[b], 19\n\ts_lshl_b32 s73, %[b], 11\n\ts_or_b32 s74, s74, s73\n\ts_and_b32 s74, s74, 0x3fc000\n\ts_lshl_b32 s73, s75, 9\n\t"
      "s_add_u32 s74, s74, s73\n\ts_add_u32 s74, s74, 0x7800\n\tv_lshl_add_u32 v9, v9, 11, s74\n\tv_and_b32 v1, 24, v0\n\ts_lshl2_add_u32 s74, %[b], s75\n\ts_mul_i32 s74, s74, %[nw]\n\tv_lshl_add_u32 v9, v1, 4, v9\n\t"
      "s_add_u32 s74, s74, %[w0]\n\tv_add_lshl_u32 v10, v10, s74, 7\n\t"
      "s_setprio %[lp]\n\ts_and_b32 s74, s75, 3\n\tv_lshrrev_b32 v1, 3, v0\n\tv_lshrrev_b32 v3, 4, v0\n\ts_and_b32 s72, %[b], 1\n\ts_and_b32 s73, %[b], 0xe0\n\ts_lshl_b32 s72, s72, 10\n\ts_lshl2_add_u32 s72, s73, s72\n\t"
      "v_and_b32 v4, 2, v1\n\tv_and_or_b32 v3, v3, 4, v4\n\tv_and_b32 v4, 31, v1\n\tv_xor_b32 v3, v3, v0\n\tv_mul_u32_u24 v4, 0x1800, v4\n\tv_and_b32 v3, 7, v3\n\tv_lshl_add_u32 v3, v3, 4, s72\n\t"
      "s_mul_i32 s39, s74, 0xc00\n\ts_lshl_b32 s38, s74, 12\n\ts_lshl_b32 s37, s74, 9\n\tv_add_u32 v5, v3, v4\n\tv_and_b32 v1, 7, v1\n\tv_add_u32 v6, 0x400, v5\n\tv_add_u32 v7, 0x800, v5\n\t"
      "s_and_b32 s72, %[b], 6\n\ts_and_b32 s73, %[b], 24\n\ts_lshl4_add_u32 s72, s72, s73\n\tv_xor_b32 v3, v1, v0\n\ts_lshl_b32 s72, s72, 15\n\tv_and_b32 v3, 7, v3\n\tv_lshl_add_u32 v1, v1, 11, s72\n\t"
      "s_mov_b32 s47, 0x20000\n\ts_mov_b32 s46, 0x600000\n\tv_lshl_add_u32 v1, v3, 4, v1\n\ts_mov_b32 s43, s47\n\ts_mov_b32 s42, 0x408000\n\ts_lshl_b32 s72, s74, 6\n\ts_and_b32 s72, s72, 64\n\t"
      "v_xor_b32 v4, s72, v1\n\ts_lshl_b32 s72, s74, 13\n\ts_add_u32 s72, s72, 0x40000\n\tv_add_u32 v4, s72, v4\n\ts_lshl_b32 s72, s74, 16\n\tv_add_u32 v0, s72, v1\n\ts_movk_i32 s73, 0x3c00\n\t"
      "v_add_u32 v1, s73, v0\n\ts_add_u32 s38, s38, %[xf]\n\tv_add_u32 v2, s73, v1\n\ts_add_u32 s37, s37, %[xf]+16384\n\tv_add_u32 v3, s73, v2\n\tv_max_u32 v0, 0x8000, v0\n\tv_max_u32 v1, 0x8000, v1\n\t"
      "s_waitcnt lgkmcnt(0)\n\ts_mov_b64 s[44:45], s[50:51]\n\ts_sub_u32 s40, s48, 0x8000\n\ts_subb_u32 s41, s49, 0\n\t"
      LD_ASM1
      "s_branch FK_L"
      : "=s"(wv)
      : "{v0}"(threadIdx.x), [ka]"s"(__builtin_amdgcn_kernarg_segment_ptr()), [b]"s"(blockIdx.x), [cl]"n"(CODEL-1+CODEX), [sk]"n"(SINK+256), [lp]"n"(LPR), [xf]"n"(X_OFF), [nw]"n"(3*TPW), [w0]"n"(TW0*3072-12*TPW), [wm]"n"((1<<(3*TPW))-1)
      : "memory", "scc", "vcc", "s48", "s49", "s50", "s51", "s52", "s53", "s54", "s55", "s72", "s73", "s74", "s75", "s76", "s78", "s79", "v1", "v2", "v3", "v4", "v5", "v6", "v7", "v9", "v10",
        "s40", "s41", "s42", "s43", "s44", "s45", "s46", "s47", "s37", "s38", "s39", "s56", "s57", "s58", "s59", "s60", "s61", "s62", "s63", "s64", "s65", "s66", "s67", "s68");
  #define ENTER(lbl) int w8, bid; u32x8 a0, a1; u32x4 a2; u32x2 a3; asm volatile(lbl ":" : "={s75}"(w8), "={s76}"(bid), "={s[48:55]}"(a0), "={s[56:63]}"(a1), "={s[64:67]}"(a2), "={s[78:79]}"(a3) :: "memory");
  #define ARGW asm volatile("s_waitcnt lgkmcnt(0)" : "+{s[48:55]}"(a0), "+{s[56:63]}"(a1), "+{s[64:67]}"(a2), "+{s[78:79]}"(a3));
  #define ROLE const int w=w8&3, xcd=bid&7, idx=bid>>3, rb=(xcd>>1)*4+(idx&3), hp=(xcd&1)*8+(idx>>2), s0=rb*128; (void)s0; (void)hp;
  #define LANEVARS int l; asm volatile("v_mbcnt_lo_u32_b32 %0, -1, 0\n\tv_mbcnt_hi_u32_b32 %0, -1, %0" : "=v"(l)); const int g=l>>4, fr=l&15, tq=(l&15)>>2, tp=l&3; (void)g; (void)fr; (void)tq; (void)tp;
  #define ARGS0 const char* x=ptr64(a0[0],a0[1]); const char* wqkv=ptr64(a0[2],a0[3]); const char* wo=ptr64(a0[4],a0[5]); char* ws=ptr64(a0[6],a0[7]); \
    unsigned* flags=(unsigned*)(ws+WS_FL); const int hot0=(3-(int)(a0[2]>>7))&7; (void)x; (void)wqkv; (void)wo; (void)flags; (void)hot0;
  #define OCOL ((hp&8)|((hp+(int)(a0[2]>>7)-(int)(a0[4]>>7)+OSH)&7))
  #define ORA ((unsigned)(hp-hot0-1)&7u)
  #define ORE(c) (ORA==7u? (c)-1u : (c))
  #define ORO(c) (ORE(c)>=NF && ORE(c)<NF+7u)
  #define ORD(c) (((((ORE(c)<14u? ((ORA==7u? 0u:ORA)+(ORO(c)? ORE(c)-NF : (ORE(c)<NF? ORE(c) : ORE(c)-7u)))%7u : 7u)+hot0+1)&7u))+8u*(((hp>>3)^(ORO(c) || (c)==15u))&1u))
  const unsigned sb0=__builtin_amdgcn_readfirstlane(lds_addr(smem));
  const char* syn=smem+SYN;

  if(__builtin_expect(wv==1,1)){
    ENTER("FK_L")
    u32x4 sx, sw; unsigned xt, xl, wl, xoA, xoB, xo, xo1, xo2, wof[3];
    asm volatile("; descriptors and LDS bases" : "={s[40:43]}"(sx), "={s[44:47]}"(sw), "={s38}"(xl), "={s39}"(wl), "={s37}"(xt));
    asm volatile("; lane offsets" : "={v0}"(xoA), "={v1}"(xoB), "={v2}"(xo), "={v3}"(xo1), "={v4}"(xo2), "={v5}"(wof[0]), "={v6}"(wof[1]), "={v7}"(wof[2]));
    unsigned ltr=0, ltr2=0, ltr3=0, sd; (void)ltr; (void)ltr2; (void)ltr3;
    const char* pw;
    {
      ROLE
      LANEVARS
      ARGS0
      const int hg=((idx>>2)-hot0)&7, hi=hot0*4+(idx&3);
      const unsigned long long hm= hg==0? 1ull : hg==HGR? 0xfffull : 0x3full;
      const unsigned hto=(unsigned)((hg? w*393216 : 0x40000000)+(l<6? l*2048+idx*12288 : (l-6)*2048+hi*12288));
      pw=wqkv+(idx*6144+(xcd&1)*1024+(xcd>>1)*256+w*64);
      asm volatile(LD_ASM2
        : "=&{s84}"(sd), "+s"(w8), "+s"(bid), "+s"(a0)
        : "{v0}"(xoA), "{v1}"(xoB), "{v2}"(xo), "{v3}"(xo1), "{v4}"(xo2), "{v5}"(wof[0]), "{v6}"(wof[1]), "{v7}"(wof[2]),
          "{v16}"(hto), [sh]"s"(srd(wqkv+((xcd&1)*8+hot0)*128,6291456u-1920u)), [hm]"s"(hm),
          "{s[40:43]}"(sx), "{s[44:47]}"(sw), "{s37}"(xt), "{s38}"(xl), "{s39}"(wl), [pw]"s"(pw), [tsk]"n"(SINK+256)
        : "memory", "scc", "vcc", LD_CLOB);
    }
    ROLE
    LANEVARS
    ARGS0
    const unsigned wd=OCOL*128;
    {
      const unsigned wof2=(unsigned)(w*16384+(l>>3)*2048+((((l&7)>>1)^((l>>4)&3))*32)+(l&1)*16);
      const unsigned to=ORD((unsigned)(l>>1)&15u);
      const unsigned tab=to*131072u+(l&1)*65536;
      asm volatile(LD_ASM3
        : "+{s84}"(sd)
        : "{v0}"(xoA), "{v1}"(xoB), "{v2}"(xo), "{v3}"(xo1), "{v4}"(xo2), "{v5}"(wof[0]), "{v6}"(wof[1]), "{v7}"(wof[2]), "{v8}"(wof2), "{v9}"(tab),
          "{s[40:43]}"(sx), "{s[44:47]}"(sw), [so]"s"(srd(wo+wd,2097152u-wd)), "{s37}"(xt), "{s38}"(xl), "{s39}"(wl), [ob]"s"(sb0+w*1024), [pw]"s"(pw),
          [pq]"s"(wo+((xcd&1)*1024+((3-(int)(a0[4]>>7))&7)*128+(idx*32+w*8)*2048))
        : "memory", "scc", "vcc", LD_CLOB);
    }
    const unsigned tag=rd1(syn+64)+1u;
    lds_add(sb0+SYN+12);
    TS(2);
    if(w>=4-NPOL){
      const unsigned* fp=flags+F_O+(rb*16+hp)*16; const unsigned fo=ORD((unsigned)l&15u)*4u;
      const unsigned madr=sb0+(l==0? SYN+20 : SINK+l*4);
      SPIN(rd1(syn+12)<4u || rd1(syn+16)<4u,1);
      if(w==3) PUBM(madr,1u);
      if(PD0) __builtin_amdgcn_s_sleep(PD0);
      if(w==2) __builtin_amdgcn_s_sleep(PD1); if(w==1) __builtin_amdgcn_s_sleep(2*PD1); if(w==0) __builtin_amdgcn_s_sleep(3*PD1);
      unsigned rdy=1; int spins=0;
      while(rdy!=0xFFFFu){
        unsigned v;
        asm volatile("s_mov_b64 exec, 0xffff\n\tglobal_atomic_or %0, %1, %3, %2 sc0 sc1\n\ts_mov_b64 exec, -1\n\ts_waitcnt vmcnt(0)" : "=&v"(v) : "v"(fo), "s"(fp), "v"(0u) : "memory");
        unsigned r=((unsigned)__ballot(v==tag)&0xFFFFu)|1u;
        if(++spins>(1<<21)) r=0xFFFFu;
        if(r!=rdy){ PUBM(madr,(unsigned)__builtin_ctz(~r)); rdy=r; if(w<3 && (r&0x3FFFu)==0x3FFFu) break; }
        else __builtin_amdgcn_s_sleep(OSLP);
      }
      TS(3);
      if(bid==0 && w==3){
        if(GWD){ SPIN(rd1(syn+28)==0u,8); __builtin_amdgcn_s_sleep(GWD); }
        int sp=0;
        while(true){
          u32x4 v; asm volatile("global_load_dwordx4 %0, %1, %2 sc1\n\ts_waitcnt vmcnt(0)" : "=&v"(v) : "v"(l*16), "s"(flags+F_Y) : "memory");
          if(__all(v[0]==tag && v[1]==tag && v[2]==tag && v[3]==tag) || ++sp>(1<<20)) break;
          __builtin_amdgcn_s_sleep(12);
        }
        if(l==0) stflag(flags, tag);
      }
    } else { TS(3); }
    TOUT(4);
    __builtin_amdgcn_endpgm();
  }

  if(wv==0){
    ENTER("FK_G")
    ROLE
    __builtin_amdgcn_s_setprio(GPR);
    const int h=w&1, r=w>>1;
    LANEVARS
    TSS(3);
    ARGW
    ARGS0
    if(PGT&1){ if(w==1 && l<7) touch(ws, l<6? l*2097152+PGO : (unsigned)WS_FL+2048, sb0+SINK+512); }
    if(PGT&2){ if(w==2 && l<4) touch(ptr64(a1[0],a1[1]), l*2097152+PGO, sb0+SINK+512); }
    if(PGT&4){ if(w==3 && l<1) touch(wo, PGO, sb0+SINK+512); if(w==0 && l<2) touch(wqkv, l*2097152+2097152, sb0+SINK+512); }
    TSS(4);
    {
      unsigned lx0=sb0+X_OFF+fr*128+((g^(fr&7))*16), lx1=sb0+X_OFF+fr*128+(((4+g)^(fr&7))*16);
      const int sw=wswz(8*g+tq);
      unsigned lw0=sb0+g*3072+tq*128+(unsigned)((((2*h)^sw)*32)+8*tp), lw1=sb0+g*3072+tq*128+(unsigned)((((2*h+1)^sw)*32)+8*tp);
      char* ot=ws+WS_O+(rb*16+hp)*16384+r*8192+h*2048;
      unsigned it; unsigned long long rm; asm("s_cmp_lg_u32 %1, 0\n\ts_cselect_b64 %0, -1, 0" : "=s"(rm) : "s"(r) : "scc");
      asm volatile(N2_ASM
        : [it]"=&s"(it), "+{v56}"(lx0), "+{v57}"(lx1), "+{v58}"(lw0), "+{v59}"(lw1)
        : "{v62}"(l*16), "{v63}"(sb0+EX_OFF+h*3072+l*16), [r]"s"(r), [ot]"s"(ot), [ou]"s"(ot+4096), [fa]"s"(sb0+SYN+32+w*4), [fp]"s"(sb0+SYN+32+(w^2)*4),
          [vo]"s"(2048-1024*r), [rm]"s"(rm), [z]"s"(rb|r)
        : "memory", "scc", "vcc", N2_CLOB);
    }
    TS(3);
    unsigned tb;
    {
      LANEVARS
      const unsigned tag=rd1(syn+64)+1u;
      const unsigned ra=sb0+(4*g+tq)*128+8*tp; const int sw=(2*g+(tq>>1))&3;
      unsigned b0=ra+((0^sw)*32), b1=ra+((1^sw)*32), b2=ra+((2^sw)*32), b3=ra+((3^sw)*32), vo=(unsigned)(w*4096+l*16), sy=sb0+SYN+20;
      unsigned sr=sb0+EX_OFF+w*2048+(l>>3)*128+(((l&7)^(l>>3))*16), sg=sb0+EX_OFF+w*2048+fr*128+(((g>>1)^(fr&7))*16)+(g&1)*8;
      tb=ORD((unsigned)l&15u)*16384u;
      asm volatile("" : "+{v160}"(b0), "+{v161}"(b1), "+{v162}"(b2), "+{v163}"(b3), "+{v164}"(vo), "+{v165}"(sy), "+{v166}"(sr), "+{v167}"(sg), "+{v96}"(tb));
      publish(sb0+SYN+16, flags+F_O+rb*256+hp, l*64, tag);
      TS(4);
      __builtin_amdgcn_s_setprio(G2P);
      unsigned iss=0, q0, q1, q2, cn=0;
      asm volatile(G3_ASM
        : [iss]"+&s"(iss), [t0]"=&s"(q0), [t1]"=&s"(q1), [t2]"=&s"(q2), [cn]"+&s"(cn),
          "+{v160}"(b0), "+{v161}"(b1), "+{v162}"(b2), "+{v163}"(b3), "+{v165}"(sy), "+{v96}"(tb)
        : "{v164}"(vo), "{v166}"(sr), "{v167}"(sg), [go]"s"(60+w*4), [sy]"n"(SYN+20), [ob]"s"(srd(ws+WS_O+rb*262144,262144u)), [op]"s"(ptr64(a1[0],a1[1])+((s0+w*32)*4096+2048+OCOL*128))
        : "memory", "scc", "vcc", G3_CLOB);
    }
    {
      ENDW TS(6); TOUT(7);
    }
    __builtin_amdgcn_endpgm();
  }

  {
    ENTER("FK_C")
    ROLE
    char* cr=smem+C_OFF+2048+w*4096;
    const unsigned crs=sb0+C_OFF+2048+w*4096;
    const int sblk=bid>>3, td0=(bid&7)*128+(w&1)*64;
    const int vb=(LOCA&1)? xcd*32+(idx&16)+(idx&1)*8+((idx>>1)&7) : bid, fb=(LOCA&2)? (idx&15)*16+xcd*2+(idx>>4) : bid;
    const int d=(vb>>4)*64+(((vb>>3)&1)*4+w)*8+(vb&7);
    int kv0=KV0+((sblk>>KSH)&KVS);
    static_assert(KV0+KVS+PP_NB<=LD_NBAR && KV0>=EB, "barrier budget");
    unsigned tagv, ctr; f32x8 RB;
    const int fd0=(fb&15)*64, fs0=(fb>>4)*128+w*32;
    u32x4 qv[4];
    f32x16 A0,A1,A2,A3;
    const int rw=sblk*64+(w>>1)*32, j=rw>>5;
    unsigned k0,k1,k2,ko,ta,se,so,ha,l16,rb0[2];
    {
      LANEVARS
      if(w==0 && l<25 && l!=16) ((unsigned*)(smem+SYN))[l]=0u;
      if(l<32) *(u32x4*)(smem+C_OFF+w*512+l*16)=u32x4{0,0,0,0};
      asm volatile("" ::: "memory");
      __builtin_amdgcn_s_setprio(CPR);
      const int dg=l&7, rg=l>>3;
      ko=(unsigned)(rg*8192+dg*16);
      const bool top=(rw==0 && rg==0);
      k0=top? ko+6144:ko; k1=top? ko+6144:ko+2048; k2=top? ko+6144:ko+4096;
      ta=sb0+EX_OFF+(PP_SM? 0:w*1024)+(w&1)*512+dg*64; se=(l&8)? 0x03020706u : 0x05040100u; ha=crs+(32*g-4*tq+4*tp)*2; l16=l*16;
      so=(unsigned)(rg*32768+((j>>2)&3)*128+((dg&4)|((dg+j)&3))*16);
      #pragma unroll
      for(int k=0;k<2;k++){ const int q=fr-1-2*g+k; rb0[k]=sb0+C_OFF+(4+(q>>1))*256+((q>>3)&3)*64+((w+(q>>1))&3)*16+(q&1)*8; }
      asm volatile("" : "+{v152}"(k0), "+{v153}"(k1), "+{v154}"(k2), "+{v155}"(ko), "+{v156}"(ta), "+{v157}"(se), "+{v159}"(so), "+{v163}"(l16), "+{v165}"(ha), "+{v166}"(rb0[0]), "+{v167}"(rb0[1]));
    }
    TSS(3);
    ARGW
    if(EB==1) asm volatile("s_barrier" : "+s"(a1), "+s"(a2), "+s"(a3), "+s"(a0) :: "memory");
    if(EB==2) asm volatile("s_barrier\n\ts_barrier" : "+s"(a1), "+s"(a2), "+s"(a3), "+s"(a0) :: "memory");
    ARGS0
    char* out=ptr64(a1[0],a1[1]); const char* qp=ptr64(a2[2],a2[3])+(fs0*DM+fd0)*2;
    {
      const char* kb=ptr64(a1[2],a1[3])+(rw*DM+td0)*2; const char* vb=ptr64(a1[4],a1[5])+(rw*DM+td0)*2;
      unsigned long long zm; asm("s_cmp_eq_u32 %1, 0\n\ts_cselect_b64 %0, 0xff, 0" : "=s"(zm) : "s"(rw) : "scc");
      unsigned t0;
      if(PGT&8){ const char* tp= w==0? kb : w==1? vb : w==2? qp : ptr64(a2[0],a2[1])+d*4096;
        asm volatile("s_mov_b32 m0, %1\n\ts_mov_b64 exec, 1\n\tglobal_load_lds_dword v163, %0\n\ts_mov_b64 exec, -1" :: "s"(tp), "s"(sb0+SINK+512), "{v163}"(l16) : "memory"); }
      TSS(5);
      asm volatile(PP_ASM
        : [t0]"=&s"(t0), "=&{v164}"(ctr), "+{v159}"(so), "=&{v160}"(tagv), "=&{v[24:31]}"(RB), "=&{v[44:47]}"(qv[0]), "=&{v[48:51]}"(qv[1]), "=&{v[52:55]}"(qv[2]), "=&{v[56:59]}"(qv[3]),
          "=&{v[64:79]}"(A0), "=&{v[80:95]}"(A1), "=&{v[96:111]}"(A2), "=&{v[112:127]}"(A3)
        : "{v152}"(k0), "{v153}"(k1), "{v154}"(k2), "{v155}"(ko), "{v156}"(ta), "{v157}"(se), "{v166}"(rb0[0]), "{v167}"(rb0[1]), "{v165}"(ha), "{v163}"(l16),
          [qb0]"s"(qp), [qb2]"s"(qp+4096), [mk]"s"(0x180u), [em]"s"(1ull|(512ull<<w)), [hd]"s"(crs),
          [kb0]"s"(kb-6144), [kb3]"s"(kb), [kb5]"s"(kb+4096), [vb0]"s"(vb), [vb2]"s"(vb+4096), [sb]"s"(ws+WS_KV+td0*4096+j*512),
          [n0]"s"(kv0-EB-(HH==2)), [ff]"s"(flags+F_PP+(td0>>6)*64+j), [zm]"s"(zm), [shd]"s"(sb0+EX_OFF+w*1024), [tgd]"s"(sb0+SYN+64), [shp]"s"(ptr64(a1[6],a1[7])+(bid&7)*1024), [fl]"s"(flags),
          [hp]"s"(ptr64(a2[0],a2[1])+d*4096), [dp]"s"(ptr64(a3[0],a3[1])+(PP_SM? d&~1:d)*2), [dsh]"s"((d&1)*16), [h0]"n"(C_OFF+2048),
          [ph]"s"(wqkv+(((xcd&1)*8+hot0)*128+idx*6144+(w>>1)*196608+(w&1)*64))
        : "memory", "scc", PP_CLOB);
    }
    LANEVARS
    const unsigned tag=__builtin_amdgcn_readfirstlane(tagv)+1u;
    unsigned rem=LD_NBAR-PP_NB-kv0;
    barc(rem);
    const unsigned* pf=flags+F_PP+(d>>6)*64+(KPOL? w*16:0);
    const char* kvp=ws+WS_KV+((vb>>4)*8+(vb&7))*32768+((vb>>3)&1)*64+w*8192;
    const unsigned kvo=(unsigned)((l>>2)*128+(l&3)*16);
    int bidx=((l>=16)? l-16 : (l==0? 63 : l+47))*4;
    asm volatile("" : "+{v32}"(bidx));
    __builtin_amdgcn_s_setprio(3);
    while(rem>PREM) barc(rem);
    wait_flag_b<8>(pf, (KPOL? fr:l)*4, tag, rem CTRP);
    #pragma unroll
    for(int i=0;i<4;i++){ if(i%KTH==0 && i) barc(rem); gldsc(kvp+i*2048, kvo, crs+i*1024); }
    barc(rem); if(PB2) barc(rem);
    WAITV(0);
    asm volatile("s_mov_b32 m0, %2\n\ts_mov_b64 exec, 1\n\tglobal_load_lds_dword %0, %1 sc1\n\ts_mov_b64 exec, -1" :: "v"(0u), "s"(pf), "s"(sb0+SYN+100+w*4) : "memory");
    f32x16 ac0, ac1; unsigned vtr, cn=0; (void)vtr;
    unsigned ca=sb0+CAR+w*1280+(fr*4+g)*4, cz= l? ca : sb0+C_OFF;
    asm volatile("s_nop 1\n\t" CV_ASM
      : "=&{v[128:143]}"(ac0), "=&{v[144:159]}"(ac1), [rem]"+&s"(rem), [cn]"+&s"(cn), "+{v40}"(ca), "+{v41}"(cz)
        , "+{v[44:47]}"(qv[0]), "+{v[48:51]}"(qv[1]), "+{v[52:55]}"(qv[2]), "+{v[56:59]}"(qv[3])
      : "{v[64:79]}"(A0), "{v[80:95]}"(A1), "{v[96:111]}"(A2), "{v[112:127]}"(A3),
        "{v[24:31]}"(RB), "{v32}"(bidx), "{v33}"(sb0+SYN+80+w*4), "{v61}"(sb0+SYN+100+(l&3)*4), [tg]"s"(tag),
        "{v42}"((256*(g&1)+16*fr+4*(g&2))*2), [yb]"s"(ws+WS_Y+d*4096), "{v43}"((l>>3)*8192+(l&7)*16), [q0]"s"(qp), [q2]"s"(qp+4096)
      : "memory", "scc", CV_CLOB);
    TS(4);
    __builtin_amdgcn_s_setprio(3);
    publish(sb0+SYN+24, flags+F_Y+vb, l*1024, tag);
    asm volatile("" : "+v"(qv[0]), "+v"(qv[1]), "+v"(qv[2]), "+v"(qv[3]));
    TS(5);
    float qf[32];
    #pragma unroll
    for(int i=0;i<16;i++){ qf[2*i]=bflo(qv[i>>2][i&3]); qf[2*i+1]=bfhi(qv[i>>2][i&3]); }
    unsigned ya=crs+(g*8+tq)*64+tp*8, wa0=crs+fr*128+((g^(fr&7))*16), wa1=crs+fr*128+(((4+g)^(fr&7))*16), ra[4], oo=(unsigned)((l>>3)*16384+(l&7)*16), yo2=(l>>2)*4096+(l&3)*16;
    #pragma unroll
    for(int i=0;i<4;i++){ const int row=4*(l>>3)+i; ra[i]=crs+row*128+(((l&7)^(row&7))*16); }
    char* op=out+fs0*4096+fd0*2; char* op2=op+8192; const char* yg=ws+WS_Y+fd0*4096+fs0*2;
    #pragma unroll
    for(int i=0;i<32;i++) asm volatile("" : "+v"(qf[i]));
    asm volatile("" : "+v"(ya), "+v"(wa0), "+v"(wa1), "+v"(ra[0]), "+v"(ra[1]), "+v"(ra[2]), "+v"(ra[3]), "+v"(oo), "+v"(yo2));
    asm volatile("" : "+s"(op), "+s"(op2), "+s"(yg));
    if((YPM>>w)&1){
      SPIN(rd1(syn+24)<4u,1);
      if(YPM>1){ if(w==1) __builtin_amdgcn_s_sleep(YPD); if(w==2) __builtin_amdgcn_s_sleep(2*YPD); if(w==3) __builtin_amdgcn_s_sleep(3*YPD); }
      { int sp=0;
        while(true){
          unsigned v=tag; asm volatile("s_mov_b64 exec, 0xffff\n\tglobal_atomic_or %0, %1, %3, %2 sc0 sc1\n\ts_mov_b64 exec, -1\n\ts_waitcnt vmcnt(0)" : "+&v"(v) : "v"(l*4), "s"(flags+F_Y+fb*16), "v"(0u) : "memory");
          if(__all(v==tag) || ++sp>(1<<21)) break;
          if(YPM>1){ if(rd1(syn+28)) break; } else __builtin_amdgcn_s_sleep(YSLP);
        } }
      PUB(sb0+(l==0? SYN+28 : SINK+l*4), 1u);
    } else SPIN(rd1(syn+28)==0u,YS2);
    #pragma unroll
    for(int i=0;i<4;i++) gldsc(yg+i*65536, yo2, crs+i*1024);
    {
      u32x4 y4[4];
      asm volatile("s_waitcnt vmcnt(0)\n\t"
        "ds_read_b64_tr_b16 v[0:1], %4\n\tds_read_b64_tr_b16 v[2:3], %4 offset:256\n\tds_read_b64_tr_b16 v[4:5], %4 offset:32\n\tds_read_b64_tr_b16 v[6:7], %4 offset:288\n\t"
        "ds_read_b64_tr_b16 v[8:9], %4 offset:2048\n\tds_read_b64_tr_b16 v[10:11], %4 offset:2304\n\tds_read_b64_tr_b16 v[12:13], %4 offset:2080\n\tds_read_b64_tr_b16 v[14:15], %4 offset:2336\n\t"
        "s_waitcnt lgkmcnt(6)\n\tds_write_b128 %5, v[0:3]\n\ts_waitcnt lgkmcnt(5)\n\tds_write_b128 %5, v[4:7] offset:2048\n\t"
        "s_waitcnt lgkmcnt(4)\n\tds_write_b128 %6, v[8:11]\n\ts_waitcnt lgkmcnt(3)\n\tds_write_b128 %6, v[12:15] offset:2048\n\t"
        "ds_read_b128 %0, %7\n\tds_read_b128 %1, %8\n\tds_read_b128 %2, %9\n\tds_read_b128 %3, %10\n\ts_waitcnt lgkmcnt(0)"
        : "=&v"(y4[0]), "=&v"(y4[1]), "=&v"(y4[2]), "=&v"(y4[3])
        : "v"(ya), "v"(wa0), "v"(wa1), "v"(ra[0]), "v"(ra[1]), "v"(ra[2]), "v"(ra[3])
        : "memory", "v0", "v1", "v2", "v3", "v4", "v5", "v6", "v7", "v8", "v9", "v10", "v11", "v12", "v13", "v14", "v15");
      #pragma unroll
      for(int i=0;i<4;i++){
        u32x4 ov;
        #pragma unroll
        for(int e=0;e<4;e++) ov[e]=pack2(mul1(qf[8*i+2*e],bflo(y4[i][e])), mul1(qf[8*i+2*e+1],bfhi(y4[i][e])));
        stout((i&2? op2:op)+(i&1)*4096, oo, ov);
      }
    }
    ENDW TS(6); TOUT(7);
    __builtin_amdgcn_endpgm();
  }
}

extern "C" void kernel_launch(void* const* d_in, const int* in_sizes, int n_in, void* d_out, int out_size, void* d_ws, size_t ws_size, hipStream_t stream){
  const u16* q=(const u16*)d_in[0]; const u16* k=(const u16*)d_in[1]; const u16* v=(const u16*)d_in[2];
  const u16* shk=(const u16*)d_in[3]; const u16* hk=(const u16*)d_in[4]; const u16* sD=(const u16*)d_in[5];
  const u16* x=(const u16*)d_in[6]; const u16* wqkv=(const u16*)d_in[7]; const u16* wo=(const u16*)d_in[8];
  fused_kernel<<<256,768,0,stream>>>(x,wqkv,wo,(char*)d_ws,(u16*)d_out,k,v,shk,hk,q,sD);
}
```

```cpp
#define STAMP
#include <hip/hip_runtime.h>
#include <hip/hip_bf16.h>
#include <stdint.h>

typedef unsigned short u16;
typedef short bf16x8 __attribute__((ext_vector_type(8)));
typedef short s16x4 __attribute__((ext_vector_type(4)));
typedef float f32x2 __attribute__((ext_vector_type(2)));
typedef float f32x4 __attribute__((ext_vector_type(4)));
typedef float f32x8 __attribute__((ext_vector_type(8)));
typedef float f32x16 __attribute__((ext_vector_type(16)));
typedef unsigned u32x4 __attribute__((ext_vector_type(4)));
typedef unsigned u32x2 __attribute__((ext_vector_type(2)));
typedef __bf16 bf16x2_t __attribute__((ext_vector_type(2)));
#define LDS_AS __attribute__((address_space(3)))

__device__ __forceinline__ float bflo(unsigned x){ return __uint_as_float(x<<16); }
__device__ __forceinline__ float bfhi(unsigned x){ return __uint_as_float(x&0xffff0000u); }
__device__ __forceinline__ unsigned pack2(float lo, float hi){ return __builtin_bit_cast(unsigned, __builtin_convertvector(f32x2{lo,hi}, bf16x2_t)); }
__device__ __forceinline__ float mul1(float a, float b){ float r; asm("v_mul_f32 %0, %1, %2" : "=v"(r) : "v"(a), "v"(b)); return r; }
__device__ __forceinline__ float fma1(float a, float b, float c){ float r; asm("v_fma_f32 %0, %1, %2, %3" : "=v"(r) : "v"(a), "v"(b), "v"(c)); return r; }

__device__ __forceinline__ void glds(const void* sbase, unsigned voff, unsigned ldsaddr){
  asm volatile("s_mov_b32 m0, %2\n\ts_nop 0\n\tglobal_load_lds_dwordx4 %0, %1" :: "v"(voff), "s"(sbase), "s"(ldsaddr) : "memory");
}
__device__ __forceinline__ void gldsc(const void* sbase, unsigned voff, unsigned ldsaddr){
  asm volatile("s_mov_b32 m0, %2\n\ts_nop 0\n\tglobal_load_lds_dwordx4 %0, %1 sc1" :: "v"(voff), "s"(sbase), "s"(ldsaddr) : "memory");
}
__device__ __forceinline__ void touch(const void* sbase, unsigned voff, unsigned ldsaddr){
  asm volatile("s_mov_b32 m0, %2\n\ts_nop 0\n\tglobal_load_lds_dword %0, %1" :: "v"(voff), "s"(sbase), "s"(ldsaddr) : "memory");
}
__device__ __forceinline__ void st16c(void* p, u32x4 v){ asm volatile("global_store_dwordx4 %0, %1, off sc1\n\ts_nop 1" :: "v"(p), "v"(v) : "memory"); }
__device__ __forceinline__ void st16cs(void* sbase, unsigned voff, u32x4 v){ asm volatile("global_store_dwordx4 %0, %1, %2 sc1\n\ts_nop 1" :: "v"(voff), "v"(v), "s"(sbase) : "memory"); }
#define GLB_AS __attribute__((address_space(1)))
__device__ __forceinline__ unsigned ldflag(const unsigned* p){ return __hip_atomic_load((const GLB_AS unsigned*)p, __ATOMIC_RELAXED, __HIP_MEMORY_SCOPE_AGENT); }
__device__ __forceinline__ void stflag(unsigned* p, unsigned v){ __hip_atomic_store((GLB_AS unsigned*)p, v, __ATOMIC_RELAXED, __HIP_MEMORY_SCOPE_AGENT); }
template<int SLP> __device__ __forceinline__ void wait_flag(const unsigned* p, unsigned tag){
  int spins=0;
  while(true){ unsigned v=ldflag(p); if(__all(v==tag)) break; __builtin_amdgcn_s_sleep(SLP); if(++spins>(1<<21)) break; }
}
__device__ __forceinline__ unsigned lds_addr(const void* p){ return (unsigned)(size_t)(const LDS_AS void*)p; }
#define WAITV(n) asm volatile("s_waitcnt vmcnt(" #n ")":::"memory")
#define WAITL() asm volatile("s_waitcnt lgkmcnt(0)":::"memory")
#define SBAR() asm volatile("s_barrier":::"memory")
#define PUBM(addr,val) asm volatile("ds_max_u32 %0, %1" :: "v"(addr), "v"(val) : "memory")
#define PUB(addr,val) asm volatile("ds_write_b32 %0, %1" :: "v"(addr), "v"(val) : "memory")
__device__ __forceinline__ s16x4 trread(const char* p){ return __builtin_amdgcn_ds_read_tr16_b64_v4i16((LDS_AS s16x4*)p); }
__device__ __forceinline__ bf16x8 cat44(s16x4 a, s16x4 b){ return __builtin_shufflevector(a,b,0,1,2,3,4,5,6,7); }
__device__ __forceinline__ int wswz(int row){ return ((row>>1)&1) | (((row>>3)&1)<<1); }
__device__ __forceinline__ u32x4 widen(u32x2 a, u32x2 b){
  auto r0=__builtin_amdgcn_permlane16_swap(a[0],b[0],false,false);
  auto r1=__builtin_amdgcn_permlane16_swap(a[1],b[1],false,false);
  return u32x4{r0[0],r1[0],r0[1],r1[1]};
}

constexpr int S=2048, DM=1024;
constexpr int WS=12288, XS=18432, X_OFF=6*WS;
constexpr int CREG=4608, C_OFF=131072;
constexpr int EX_OFF=C_OFF+4*CREG;
constexpr int SYN=EX_OFF+8192;
constexpr int SINK=SYN+256;
constexpr int CAR=SINK+768;
constexpr int ALL_LDS=CAR+4*1280;
constexpr int F_PP=1024, F_O=4096, F_Y=8192;
#define KV0 1
#define KVS 1
#define LPR 2
#define CPR 2
#define GPR 1
#define CGATE 2
#define OSLP 2
#define YSLP 2
#define TPX 16
#define TPW 2
#define TWO 0
#define CVP 1
#define G2P 3
#define OUTC 1
#define ROT 0
#define WTL 2
#define PB2 0
#define PREM 5
#define KTH 2
#define YDEF 0
#define OPOL 1
#define CODEL 260
#define MIX 0
#define LOCA 3
#define XLOC 1
#define PGT 0
#define PGO 0
#define QREM 0
#define EB 0
#define TW0 1
#define ORDM 1
#define NF 7u
#define NPOL 1
#define PD0 0
#define PD1 4
#define HH 0
#define HW 3
#define HKV 4
#define YPOL 1
#define YS2 2
#define OSH 4
#define HSK 3
#define HGR 2
#define KSH 0
#define EPI 0
#define YFL 0
#define CODEX 7
#define YPM 1
#define YPD 4
#define KPOL 0
#define GWD 16
#define HLAST 1

#define N2_ASM \
  "v_mov_b64 v[64:65], 0\n\t" \
  "v_mov_b64 v[66:67], 0\n\t" \
  "v_mov_b64 v[68:69], 0\n\t" \
  "v_mov_b64 v[70:71], 0\n\t" \
  "v_mov_b64 v[72:73], 0\n\t" \
  "v_mov_b64 v[74:75], 0\n\t" \
  "v_mov_b64 v[76:77], 0\n\t" \
  "v_mov_b64 v[78:79], 0\n\t" \
  "v_mov_b64 v[80:81], 0\n\t" \
  "v_mov_b64 v[82:83], 0\n\t" \
  "v_mov_b64 v[84:85], 0\n\t" \
  "v_mov_b64 v[86:87], 0\n\t" \
  "v_mov_b64 v[88:89], 0\n\t" \
  "v_mov_b64 v[90:91], 0\n\t" \
  "v_mov_b64 v[92:93], 0\n\t" \
  "v_mov_b64 v[94:95], 0\n\t" \
  "v_mov_b64 v[96:97], 0\n\t" \
  "v_mov_b64 v[98:99], 0\n\t" \
  "v_mov_b64 v[100:101], 0\n\t" \
  "v_mov_b64 v[102:103], 0\n\t" \
  "v_mov_b64 v[104:105], 0\n\t" \
  "v_mov_b64 v[106:107], 0\n\t" \
  "v_mov_b64 v[108:109], 0\n\t" \
  "v_mov_b64 v[110:111], 0\n\t" \
  "v_mov_b64 v[112:113], 0\n\t" \
  "v_mov_b64 v[114:115], 0\n\t" \
  "v_mov_b64 v[116:117], 0\n\t" \
  "v_mov_b64 v[118:119], 0\n\t" \
  "v_mov_b64 v[120:121], 0\n\t" \
  "v_mov_b64 v[122:123], 0\n\t" \
  "v_mov_b64 v[124:125], 0\n\t" \
  "v_mov_b64 v[126:127], 0\n\t" \
  "v_mov_b64 v[128:129], 0\n\t" \
  "v_mov_b64 v[130:131], 0\n\t" \
  "v_mov_b64 v[132:133], 0\n\t" \
  "v_mov_b64 v[134:135], 0\n\t" \
  "v_mov_b64 v[136:137], 0\n\t" \
  "v_mov_b64 v[138:139], 0\n\t" \
  "v_mov_b64 v[140:141], 0\n\t" \
  "v_mov_b64 v[142:143], 0\n\t" \
  "v_mov_b64 v[144:145], 0\n\t" \
  "v_mov_b64 v[146:147], 0\n\t" \
  "v_mov_b64 v[148:149], 0\n\t" \
  "v_mov_b64 v[150:151], 0\n\t" \
  "v_mov_b64 v[152:153], 0\n\t" \
  "v_mov_b64 v[154:155], 0\n\t" \
  "v_mov_b64 v[156:157], 0\n\t" \
  "v_mov_b64 v[158:159], 0\n\t" \
  "v_mov_b64 v[160:161], 0\n\t" \
  "v_mov_b64 v[162:163], 0\n\t" \
  "v_mov_b64 v[164:165], 0\n\t" \
  "v_mov_b64 v[166:167], 0\n\t" \
  "v_add_u32 v60, 36864, v58\n\t" \
  "v_add_u32 v61, 36864, v59\n\t" \
  "s_mov_b32 %[it], 0\n\t" \
  "s_cmp_lg_u32 %[r], 0\n\t" \
  "s_barrier\n\t" \
  "s_memrealtime %[st1]\n\t" \
  "s_waitcnt lgkmcnt(0)\n\t" \
  "s_cbranch_scc1 NB_%=\n\t" \
  "ds_read_b64_tr_b16 v[48:49], v60 offset:14336\n\t" \
  "ds_read_b64_tr_b16 v[50:51], v60 offset:14848\n\t" \
  "ds_read_b128 v[0:3], v56 offset:38912\n\t" \
  "ds_read_b64_tr_b16 v[52:53], v60 offset:26624\n\t" \
  "ds_read_b64_tr_b16 v[54:55], v60 offset:27136\n\t" \
  "ds_read_b128 v[16:19], v57 offset:38912\n\t" \
  "ds_read_b128 v[4:7], v56 offset:40960\n\t" \
  "ds_read_b128 v[20:23], v57 offset:40960\n\t" \
  "ds_read_b128 v[8:11], v56 offset:43008\n\t" \
  "ds_read_b128 v[24:27], v57 offset:43008\n\t" \
  "ds_read_b128 v[12:15], v56 offset:45056\n\t" \
  "ds_read_b128 v[28:31], v57 offset:45056\n\t" \
  "ds_read_b64_tr_b16 v[32:33], v60 offset:13312\n\t" \
  "ds_read_b64_tr_b16 v[34:35], v60 offset:13824\n\t" \
  "s_mov_b32 s68, 0x50000\n\t" \
  "s_mov_b32 s69, 0x5550055\n\t" \
  "s_mov_b32 s70, 0xa0000\n\t" \
  "s_mov_b32 s71, 0xaaa00aa\n\t" \
  "s_mov_b32 s72, 0x150001\n\t" \
  "s_mov_b32 s73, 0x15550155\n\t" \
  "s_mov_b32 s74, 0x2a0002\n\t" \
  "s_mov_b32 s75, 0x2aaa02aa\n\t" \
  "s_mov_b32 s76, 0x55505555\n\t" \
  "s_mov_b32 s77, 0x50005500\n\t" \
  "s_mov_b32 s78, 0xaaa0aaaa\n\t" \
  "s_mov_b32 s79, 0xa000aa00\n\t" \
  "s_mov_b32 s80, 0x55405554\n\t" \
  "s_mov_b32 s81, 0x40005400\n\t" \
  "s_mov_b32 s82, 0xaa80aaa8\n\t" \
  "s_mov_b32 s83, 0x8000a800\n\t" \
  "s_mov_b32 s84, 0xaaaaaaaa\n\t" \
  "s_mov_b32 s85, 0xaaaaaaaa\n\t" \
  "s_waitcnt lgkmcnt(0)\n\t" \
  "s_branch N2a_%=\n\t" \
  "N0a_%=:\n\t" \
  "s_waitcnt lgkmcnt(10)\n\t" \
  "v_mfma_f32_16x16x32_bf16 v[128:131], v[0:3], v[32:35], v[128:131]\n\t" \
  "ds_read_b64_tr_b16 v[44:45], v58 offset:13312\n\t" \
  "s_waitcnt lgkmcnt(7)\n\t" \
  "v_mfma_f32_16x16x32_bf16 v[128:131], v[16:19], v[36:39], v[128:131]\n\t" \
  "ds_read_b64_tr_b16 v[46:47], v58 offset:13824\n\t" \
  "v_mfma_f32_16x16x32_bf16 v[136:139], v[4:7], v[32:35], v[136:139]\n\t" \
  "ds_read_b128 v[48:51], v56 offset:0\n\t" \
  "v_mfma_f32_16x16x32_bf16 v[136:139], v[20:23], v[36:39], v[136:139]\n\t" \
  "ds_read_b128 v[52:55], v57 offset:0\n\t" \
  "s_waitcnt lgkmcnt(8)\n\t" \
  "v_mfma_f32_16x16x32_bf16 v[144:147], v[8:11], v[32:35], v[144:147]\n\t" \
  "v_mfma_f32_16x16x32_bf16 v[144:147], v[24:27], v[36:39], v[144:147]\n\t" \
  "s_waitcnt lgkmcnt(4)\n\t" \
  "v_mfma_f32_16x16x32_bf16 v[152:155], v[12:15], v[32:35], v[152:155]\n\t" \
  "v_mfma_f32_16x16x32_bf16 v[152:155], v[28:31], v[36:39], v[152:155]\n\t" \
  "ds_read_b64_tr_b16 v[32:33], v59 offset:1024\n\t" \
  "v_mfma_f32_16x16x32_bf16 v[96:99], v[40:43], v[0:3], v[96:99]\n\t" \
  "ds_read_b64_tr_b16 v[34:35], v59 offset:1536\n\t" \
  "s_waitcnt lgkmcnt(4)\n\t" \
  "v_mfma_f32_16x16x32_bf16 v[96:99], v[44:47], v[16:19], v[96:99]\n\t" \
  "ds_read_b64_tr_b16 v[36:37], v59 offset:13312\n\t" \
  "v_mfma_f32_16x16x32_bf16 v[104:107], v[40:43], v[4:7], v[104:107]\n\t" \
  "v_mfma_f32_16x16x32_bf16 v[104:107], v[44:47], v[20:23], v[104:107]\n\t" \
  "ds_read_b64_tr_b16 v[38:39], v59 offset:13824\n\t" \
  "v_mfma_f32_16x16x32_bf16 v[112:115], v[40:43], v[8:11], v[112:115]\n\t" \
  "v_mfma_f32_16x16x32_bf16 v[112:115], v[44:47], v[24:27], v[112:115]\n\t" \
  "v_mfma_f32_16x16x32_bf16 v[120:123], v[40:43], v[12:15], v[120:123]\n\t" \
  "v_mfma_f32_16x16x32_bf16 v[120:123], v[44:47], v[28:31], v[120:123]\n\t" \
  "s_waitcnt lgkmcnt(4)\n\t" \
  "v_mfma_f32_16x16x32_bf16 v[160:163], v[40:43], v[48:51], v[160:163]\n\t" \
  "v_mfma_f32_16x16x32_bf16 v[160:163], v[44:47], v[52:55], v[160:163]\n\t" \
  "ds_read_b64_tr_b16 v[40:41], v59 offset:2048\n\t" \
  "s_waitcnt lgkmcnt(1)\n\t" \
  "v_mfma_f32_16x16x32_bf16 v[164:167], v[32:35], v[48:51], v[164:167]\n\t" \
  "ds_read_b64_tr_b16 v[42:43], v59 offset:2560\n\t" \
  "v_mfma_f32_16x16x32_bf16 v[164:167], v[36:39], v[52:55], v[164:167]\n\t" \
  "ds_read_b64_tr_b16 v[44:45], v59 offset:14336\n\t" \
  "v_mfma_f32_16x16x32_bf16 v[100:103], v[32:35], v[0:3], v[100:103]\n\t" \
  "ds_read_b64_tr_b16 v[46:47], v59 offset:14848\n\t" \
  "v_mfma_f32_16x16x32_bf16 v[100:103], v[36:39], v[16:19], v[100:103]\n\t" \
  "ds_read_b64_tr_b16 v[48:49], v58 offset:0\n\t" \
  "v_mfma_f32_16x16x32_bf16 v[108:111], v[32:35], v[4:7], v[108:111]\n\t" \
  "ds_read_b64_tr_b16 v[50:51], v58 offset:512\n\t" \
  "v_mfma_f32_16x16x32_bf16 v[108:111], v[36:39], v[20:23], v[108:111]\n\t" \
  "ds_read_b64_tr_b16 v[52:53], v58 offset:12288\n\t" \
  "v_mfma_f32_16x16x32_bf16 v[116:119], v[32:35], v[8:11], v[116:119]\n\t" \
  "ds_read_b64_tr_b16 v[54:55], v58 offset:12800\n\t" \
  "v_mfma_f32_16x16x32_bf16 v[116:119], v[36:39], v[24:27], v[116:119]\n\t" \
  "v_mfma_f32_16x16x32_bf16 v[124:127], v[32:35], v[12:15], v[124:127]\n\t" \
  "ds_read_b64_tr_b16 v[32:33], v59 offset:0\n\t" \
  "v_mfma_f32_16x16x32_bf16 v[124:127], v[36:39], v[28:31], v[124:127]\n\t" \
  "ds_read_b64_tr_b16 v[34:35], v59 offset:512\n\t" \
  "s_waitcnt lgkmcnt(6)\n\t" \
  "v_mfma_f32_16x16x32_bf16 v[132:135], v[0:3], v[40:43], v[132:135]\n\t" \
  "ds_read_b64_tr_b16 v[36:37], v59 offset:12288\n\t" \
  "v_mfma_f32_16x16x32_bf16 v[132:135], v[16:19], v[44:47], v[132:135]\n\t" \
  "ds_read_b64_tr_b16 v[38:39], v59 offset:12800\n\t" \
  "v_mfma_f32_16x16x32_bf16 v[140:143], v[4:7], v[40:43], v[140:143]\n\t" \
  "v_mfma_f32_16x16x32_bf16 v[140:143], v[20:23], v[44:47], v[140:143]\n\t" \
  "v_mfma_f32_16x16x32_bf16 v[148:151], v[8:11], v[40:43], v[148:151]\n\t" \
  "v_mfma_f32_16x16x32_bf16 v[148:151], v[24:27], v[44:47], v[148:151]\n\t" \
  "v_mfma_f32_16x16x32_bf16 v[156:159], v[12:15], v[40:43], v[156:159]\n\t" \
  "v_mfma_f32_16x16x32_bf16 v[156:159], v[28:31], v[44:47], v[156:159]\n\t" \
  "s_waitcnt lgkmcnt(0)\n\t" \
  "s_barrier\n\t" \
  "v_mfma_f32_16x16x32_bf16 v[64:67], v[48:51], v[0:3], v[64:67]\n\t" \
  "v_mfma_f32_16x16x32_bf16 v[64:67], v[52:55], v[16:19], v[64:67]\n\t" \
  "ds_read_b64_tr_b16 v[40:41], v58 offset:26624\n\t" \
  "v_mfma_f32_16x16x32_bf16 v[68:71], v[32:35], v[0:3], v[68:71]\n\t" \
  "ds_read_b128 v[0:3], v56 offset:20480\n\t" \
  "v_mfma_f32_16x16x32_bf16 v[68:71], v[36:39], v[16:19], v[68:71]\n\t" \
  "ds_read_b128 v[16:19], v57 offset:20480\n\t" \
  "v_mfma_f32_16x16x32_bf16 v[72:75], v[48:51], v[4:7], v[72:75]\n\t" \
  "ds_read_b64_tr_b16 v[42:43], v58 offset:27136\n\t" \
  "v_mfma_f32_16x16x32_bf16 v[72:75], v[52:55], v[20:23], v[72:75]\n\t" \
  "ds_read_b64_tr_b16 v[44:45], v60 offset:2048\n\t" \
  "v_mfma_f32_16x16x32_bf16 v[76:79], v[32:35], v[4:7], v[76:79]\n\t" \
  "ds_read_b128 v[4:7], v56 offset:22528\n\t" \
  "v_mfma_f32_16x16x32_bf16 v[76:79], v[36:39], v[20:23], v[76:79]\n\t" \
  "ds_read_b128 v[20:23], v57 offset:22528\n\t" \
  "v_mfma_f32_16x16x32_bf16 v[80:83], v[48:51], v[8:11], v[80:83]\n\t" \
  "ds_read_b64_tr_b16 v[46:47], v60 offset:2560\n\t" \
  "v_mfma_f32_16x16x32_bf16 v[80:83], v[52:55], v[24:27], v[80:83]\n\t" \
  "v_mfma_f32_16x16x32_bf16 v[84:87], v[32:35], v[8:11], v[84:87]\n\t" \
  "ds_read_b128 v[8:11], v56 offset:24576\n\t" \
  "v_mfma_f32_16x16x32_bf16 v[84:87], v[36:39], v[24:27], v[84:87]\n\t" \
  "ds_read_b128 v[24:27], v57 offset:24576\n\t" \
  "v_mfma_f32_16x16x32_bf16 v[88:91], v[48:51], v[12:15], v[88:91]\n\t" \
  "ds_read_b64_tr_b16 v[48:49], v58 offset:25600\n\t" \
  "v_mfma_f32_16x16x32_bf16 v[88:91], v[52:55], v[28:31], v[88:91]\n\t" \
  "ds_read_b64_tr_b16 v[50:51], v58 offset:26112\n\t" \
  "v_mfma_f32_16x16x32_bf16 v[92:95], v[32:35], v[12:15], v[92:95]\n\t" \
  "ds_read_b128 v[12:15], v56 offset:26624\n\t" \
  "v_mfma_f32_16x16x32_bf16 v[92:95], v[36:39], v[28:31], v[92:95]\n\t" \
  "ds_read_b128 v[28:31], v57 offset:26624\n\t" \
  "N1a_%=:\n\t" \
  "s_waitcnt lgkmcnt(10)\n\t" \
  "v_mfma_f32_16x16x32_bf16 v[128:131], v[0:3], v[40:43], v[128:131]\n\t" \
  "ds_read_b64_tr_b16 v[52:53], v60 offset:1024\n\t" \
  "s_waitcnt lgkmcnt(7)\n\t" \
  "v_mfma_f32_16x16x32_bf16 v[128:131], v[16:19], v[44:47], v[128:131]\n\t" \
  "ds_read_b64_tr_b16 v[54:55], v60 offset:1536\n\t" \
  "v_mfma_f32_16x16x32_bf16 v[136:139], v[4:7], v[40:43], v[136:139]\n\t" \
  "ds_read_b128 v[32:35], v56 offset:18432\n\t" \
  "v_mfma_f32_16x16x32_bf16 v[136:139], v[20:23], v[44:47], v[136:139]\n\t" \
  "ds_read_b128 v[36:39], v57 offset:18432\n\t" \
  "s_waitcnt lgkmcnt(8)\n\t" \
  "v_mfma_f32_16x16x32_bf16 v[144:147], v[8:11], v[40:43], v[144:147]\n\t" \
  "v_mfma_f32_16x16x32_bf16 v[144:147], v[24:27], v[44:47], v[144:147]\n\t" \
  "s_waitcnt lgkmcnt(4)\n\t" \
  "v_mfma_f32_16x16x32_bf16 v[152:155], v[12:15], v[40:43], v[152:155]\n\t" \
  "v_mfma_f32_16x16x32_bf16 v[152:155], v[28:31], v[44:47], v[152:155]\n\t" \
  "ds_read_b64_tr_b16 v[40:41], v59 offset:25600\n\t" \
  "v_mfma_f32_16x16x32_bf16 v[96:99], v[48:51], v[0:3], v[96:99]\n\t" \
  "ds_read_b64_tr_b16 v[42:43], v59 offset:26112\n\t" \
  "s_waitcnt lgkmcnt(4)\n\t" \
  "v_mfma_f32_16x16x32_bf16 v[96:99], v[52:55], v[16:19], v[96:99]\n\t" \
  "ds_read_b64_tr_b16 v[44:45], v61 offset:1024\n\t" \
  "v_mfma_f32_16x16x32_bf16 v[104:107], v[48:51], v[4:7], v[104:107]\n\t" \
  "v_mfma_f32_16x16x32_bf16 v[104:107], v[52:55], v[20:23], v[104:107]\n\t" \
  "ds_read_b64_tr_b16 v[46:47], v61 offset:1536\n\t" \
  "v_mfma_f32_16x16x32_bf16 v[112:115], v[48:51], v[8:11], v[112:115]\n\t" \
  "v_mfma_f32_16x16x32_bf16 v[112:115], v[52:55], v[24:27], v[112:115]\n\t" \
  "v_mfma_f32_16x16x32_bf16 v[120:123], v[48:51], v[12:15], v[120:123]\n\t" \
  "v_mfma_f32_16x16x32_bf16 v[120:123], v[52:55], v[28:31], v[120:123]\n\t" \
  "s_waitcnt lgkmcnt(4)\n\t" \
  "v_mfma_f32_16x16x32_bf16 v[160:163], v[48:51], v[32:35], v[160:163]\n\t" \
  "v_mfma_f32_16x16x32_bf16 v[160:163], v[52:55], v[36:39], v[160:163]\n\t" \
  "ds_read_b64_tr_b16 v[48:49], v59 offset:26624\n\t" \
  "s_waitcnt lgkmcnt(1)\n\t" \
  "v_mfma_f32_16x16x32_bf16 v[164:167], v[40:43], v[32:35], v[164:167]\n\t" \
  "ds_read_b64_tr_b16 v[50:51], v59 offset:27136\n\t" \
  "v_mfma_f32_16x16x32_bf16 v[164:167], v[44:47], v[36:39], v[164:167]\n\t" \
  "ds_read_b64_tr_b16 v[52:53], v61 offset:2048\n\t" \
  "v_mfma_f32_16x16x32_bf16 v[100:103], v[40:43], v[0:3], v[100:103]\n\t" \
  "ds_read_b64_tr_b16 v[54:55], v61 offset:2560\n\t" \
  "v_mfma_f32_16x16x32_bf16 v[100:103], v[44:47], v[16:19], v[100:103]\n\t" \
  "ds_read_b64_tr_b16 v[32:33], v58 offset:24576\n\t" \
  "v_mfma_f32_16x16x32_bf16 v[108:111], v[40:43], v[4:7], v[108:111]\n\t" \
  "ds_read_b64_tr_b16 v[34:35], v58 offset:25088\n\t" \
  "v_mfma_f32_16x16x32_bf16 v[108:111], v[44:47], v[20:23], v[108:111]\n\t" \
  "ds_read_b64_tr_b16 v[36:37], v60 offset:0\n\t" \
  "v_mfma_f32_16x16x32_bf16 v[116:119], v[40:43], v[8:11], v[116:119]\n\t" \
  "ds_read_b64_tr_b16 v[38:39], v60 offset:512\n\t" \
  "v_mfma_f32_16x16x32_bf16 v[116:119], v[44:47], v[24:27], v[116:119]\n\t" \
  "v_mfma_f32_16x16x32_bf16 v[124:127], v[40:43], v[12:15], v[124:127]\n\t" \
  "ds_read_b64_tr_b16 v[40:41], v59 offset:24576\n\t" \
  "v_mfma_f32_16x16x32_bf16 v[124:127], v[44:47], v[28:31], v[124:127]\n\t" \
  "ds_read_b64_tr_b16 v[42:43], v59 offset:25088\n\t" \
  "s_waitcnt lgkmcnt(6)\n\t" \
  "v_mfma_f32_16x16x32_bf16 v[132:135], v[0:3], v[48:51], v[132:135]\n\t" \
  "ds_read_b64_tr_b16 v[44:45], v61 offset:0\n\t" \
  "v_mfma_f32_16x16x32_bf16 v[132:135], v[16:19], v[52:55], v[132:135]\n\t" \
  "ds_read_b64_tr_b16 v[46:47], v61 offset:512\n\t" \
  "v_mfma_f32_16x16x32_bf16 v[140:143], v[4:7], v[48:51], v[140:143]\n\t" \
  "v_mfma_f32_16x16x32_bf16 v[140:143], v[20:23], v[52:55], v[140:143]\n\t" \
  "v_mfma_f32_16x16x32_bf16 v[148:151], v[8:11], v[48:51], v[148:151]\n\t" \
  "v_mfma_f32_16x16x32_bf16 v[148:151], v[24:27], v[52:55], v[148:151]\n\t" \
  "v_mfma_f32_16x16x32_bf16 v[156:159], v[12:15], v[48:51], v[156:159]\n\t" \
  "v_mfma_f32_16x16x32_bf16 v[156:159], v[28:31], v[52:55], v[156:159]\n\t" \
  "s_waitcnt lgkmcnt(0)\n\t" \
  "s_barrier\n\t" \
  "v_mfma_f32_16x16x32_bf16 v[64:67], v[32:35], v[0:3], v[64:67]\n\t" \
  "v_mfma_f32_16x16x32_bf16 v[64:67], v[36:39], v[16:19], v[64:67]\n\t" \
  "ds_read_b64_tr_b16 v[48:49], v60 offset:14336\n\t" \
  "v_mfma_f32_16x16x32_bf16 v[68:71], v[40:43], v[0:3], v[68:71]\n\t" \
  "ds_read_b128 v[0:3], v56 offset:38912\n\t" \
  "v_mfma_f32_16x16x32_bf16 v[68:71], v[44:47], v[16:19], v[68:71]\n\t" \
  "ds_read_b128 v[16:19], v57 offset:38912\n\t" \
  "v_mfma_f32_16x16x32_bf16 v[72:75], v[32:35], v[4:7], v[72:75]\n\t" \
  "ds_read_b64_tr_b16 v[50:51], v60 offset:14848\n\t" \
  "v_mfma_f32_16x16x32_bf16 v[72:75], v[36:39], v[20:23], v[72:75]\n\t" \
  "ds_read_b64_tr_b16 v[52:53], v60 offset:26624\n\t" \
  "v_mfma_f32_16x16x32_bf16 v[76:79], v[40:43], v[4:7], v[76:79]\n\t" \
  "ds_read_b128 v[4:7], v56 offset:40960\n\t" \
  "v_mfma_f32_16x16x32_bf16 v[76:79], v[44:47], v[20:23], v[76:79]\n\t" \
  "ds_read_b128 v[20:23], v57 offset:40960\n\t" \
  "v_mfma_f32_16x16x32_bf16 v[80:83], v[32:35], v[8:11], v[80:83]\n\t" \
  "ds_read_b64_tr_b16 v[54:55], v60 offset:27136\n\t" \
  "v_mfma_f32_16x16x32_bf16 v[80:83], v[36:39], v[24:27], v[80:83]\n\t" \
  "v_mfma_f32_16x16x32_bf16 v[84:87], v[40:43], v[8:11], v[84:87]\n\t" \
  "ds_read_b128 v[8:11], v56 offset:43008\n\t" \
  "v_mfma_f32_16x16x32_bf16 v[84:87], v[44:47], v[24:27], v[84:87]\n\t" \
  "ds_read_b128 v[24:27], v57 offset:43008\n\t" \
  "v_mfma_f32_16x16x32_bf16 v[88:91], v[32:35], v[12:15], v[88:91]\n\t" \
  "ds_read_b64_tr_b16 v[32:33], v60 offset:13312\n\t" \
  "v_mfma_f32_16x16x32_bf16 v[88:91], v[36:39], v[28:31], v[88:91]\n\t" \
  "ds_read_b64_tr_b16 v[34:35], v60 offset:13824\n\t" \
  "v_mfma_f32_16x16x32_bf16 v[92:95], v[40:43], v[12:15], v[92:95]\n\t" \
  "ds_read_b128 v[12:15], v56 offset:45056\n\t" \
  "v_mfma_f32_16x16x32_bf16 v[92:95], v[44:47], v[28:31], v[92:95]\n\t" \
  "ds_read_b128 v[28:31], v57 offset:45056\n\t" \
  "N2a_%=:\n\t" \
  "s_waitcnt lgkmcnt(10)\n\t" \
  "v_mfma_f32_16x16x32_bf16 v[128:131], v[0:3], v[48:51], v[128:131]\n\t" \
  "ds_read_b64_tr_b16 v[36:37], v60 offset:25600\n\t" \
  "s_waitcnt lgkmcnt(7)\n\t" \
  "v_mfma_f32_16x16x32_bf16 v[128:131], v[16:19], v[52:55], v[128:131]\n\t" \
  "ds_read_b64_tr_b16 v[38:39], v60 offset:26112\n\t" \
  "v_mfma_f32_16x16x32_bf16 v[136:139], v[4:7], v[48:51], v[136:139]\n\t" \
  "ds_read_b128 v[40:43], v56 offset:36864\n\t" \
  "v_mfma_f32_16x16x32_bf16 v[136:139], v[20:23], v[52:55], v[136:139]\n\t" \
  "ds_read_b128 v[44:47], v57 offset:36864\n\t" \
  "s_waitcnt lgkmcnt(8)\n\t" \
  "v_mfma_f32_16x16x32_bf16 v[144:147], v[8:11], v[48:51], v[144:147]\n\t" \
  "v_mfma_f32_16x16x32_bf16 v[144:147], v[24:27], v[52:55], v[144:147]\n\t" \
  "s_waitcnt lgkmcnt(4)\n\t" \
  "v_mfma_f32_16x16x32_bf16 v[152:155], v[12:15], v[48:51], v[152:155]\n\t" \
  "v_mfma_f32_16x16x32_bf16 v[152:155], v[28:31], v[52:55], v[152:155]\n\t" \
  "ds_read_b64_tr_b16 v[48:49], v61 offset:13312\n\t" \
  "v_mfma_f32_16x16x32_bf16 v[96:99], v[32:35], v[0:3], v[96:99]\n\t" \
  "ds_read_b64_tr_b16 v[50:51], v61 offset:13824\n\t" \
  "s_waitcnt lgkmcnt(4)\n\t" \
  "v_mfma_f32_16x16x32_bf16 v[96:99], v[36:39], v[16:19], v[96:99]\n\t" \
  "ds_read_b64_tr_b16 v[52:53], v61 offset:25600\n\t" \
  "v_mfma_f32_16x16x32_bf16 v[104:107], v[32:35], v[4:7], v[104:107]\n\t" \
  "v_mfma_f32_16x16x32_bf16 v[104:107], v[36:39], v[20:23], v[104:107]\n\t" \
  "ds_read_b64_tr_b16 v[54:55], v61 offset:26112\n\t" \
  "v_mfma_f32_16x16x32_bf16 v[112:115], v[32:35], v[8:11], v[112:115]\n\t" \
  "v_mfma_f32_16x16x32_bf16 v[112:115], v[36:39], v[24:27], v[112:115]\n\t" \
  "v_mfma_f32_16x16x32_bf16 v[120:123], v[32:35], v[12:15], v[120:123]\n\t" \
  "v_mfma_f32_16x16x32_bf16 v[120:123], v[36:39], v[28:31], v[120:123]\n\t" \
  "s_waitcnt lgkmcnt(4)\n\t" \
  "v_mfma_f32_16x16x32_bf16 v[160:163], v[32:35], v[40:43], v[160:163]\n\t" \
  "v_mfma_f32_16x16x32_bf16 v[160:163], v[36:39], v[44:47], v[160:163]\n\t" \
  "ds_read_b64_tr_b16 v[32:33], v61 offset:14336\n\t" \
  "s_waitcnt lgkmcnt(1)\n\t" \
  "v_mfma_f32_16x16x32_bf16 v[164:167], v[48:51], v[40:43], v[164:167]\n\t" \
  "ds_read_b64_tr_b16 v[34:35], v61 offset:14848\n\t" \
  "v_mfma_f32_16x16x32_bf16 v[164:167], v[52:55], v[44:47], v[164:167]\n\t" \
  "ds_read_b64_tr_b16 v[36:37], v61 offset:26624\n\t" \
  "v_mfma_f32_16x16x32_bf16 v[100:103], v[48:51], v[0:3], v[100:103]\n\t" \
  "ds_read_b64_tr_b16 v[38:39], v61 offset:27136\n\t" \
  "v_mfma_f32_16x16x32_bf16 v[100:103], v[52:55], v[16:19], v[100:103]\n\t" \
  "ds_read_b64_tr_b16 v[40:41], v60 offset:12288\n\t" \
  "v_mfma_f32_16x16x32_bf16 v[108:111], v[48:51], v[4:7], v[108:111]\n\t" \
  "ds_read_b64_tr_b16 v[42:43], v60 offset:12800\n\t" \
  "v_mfma_f32_16x16x32_bf16 v[108:111], v[52:55], v[20:23], v[108:111]\n\t" \
  "ds_read_b64_tr_b16 v[44:45], v60 offset:24576\n\t" \
  "v_mfma_f32_16x16x32_bf16 v[116:119], v[48:51], v[8:11], v[116:119]\n\t" \
  "ds_read_b64_tr_b16 v[46:47], v60 offset:25088\n\t" \
  "v_mfma_f32_16x16x32_bf16 v[116:119], v[52:55], v[24:27], v[116:119]\n\t" \
  "v_mfma_f32_16x16x32_bf16 v[124:127], v[48:51], v[12:15], v[124:127]\n\t" \
  "ds_read_b64_tr_b16 v[48:49], v61 offset:12288\n\t" \
  "v_mfma_f32_16x16x32_bf16 v[124:127], v[52:55], v[28:31], v[124:127]\n\t" \
  "ds_read_b64_tr_b16 v[50:51], v61 offset:12800\n\t" \
  "s_waitcnt lgkmcnt(6)\n\t" \
  "v_mfma_f32_16x16x32_bf16 v[132:135], v[0:3], v[32:35], v[132:135]\n\t" \
  "ds_read_b64_tr_b16 v[52:53], v61 offset:24576\n\t" \
  "v_mfma_f32_16x16x32_bf16 v[132:135], v[16:19], v[36:39], v[132:135]\n\t" \
  "ds_read_b64_tr_b16 v[54:55], v61 offset:25088\n\t" \
  "v_mfma_f32_16x16x32_bf16 v[140:143], v[4:7], v[32:35], v[140:143]\n\t" \
  "v_mfma_f32_16x16x32_bf16 v[140:143], v[20:23], v[36:39], v[140:143]\n\t" \
  "s_addk_i32 %[it], 1\n\t" \
  "v_mfma_f32_16x16x32_bf16 v[148:151], v[8:11], v[32:35], v[148:151]\n\t" \
  "s_cmp_lt_u32 %[it], 6\n\t" \
  "v_mfma_f32_16x16x32_bf16 v[148:151], v[24:27], v[36:39], v[148:151]\n\t" \
  "v_mfma_f32_16x16x32_bf16 v[156:159], v[12:15], v[32:35], v[156:159]\n\t" \
  "v_mfma_f32_16x16x32_bf16 v[156:159], v[28:31], v[36:39], v[156:159]\n\t" \
  "s_waitcnt lgkmcnt(0)\n\t" \
  "s_barrier\n\t" \
  "s_cbranch_scc0 NTa_%=\n\t" \
  "v_mfma_f32_16x16x32_bf16 v[64:67], v[40:43], v[0:3], v[64:67]\n\t" \
  "v_mfma_f32_16x16x32_bf16 v[64:67], v[44:47], v[16:19], v[64:67]\n\t" \
  "ds_read_b64_tr_b16 v[32:33], v58 offset:2048\n\t" \
  "v_mfma_f32_16x16x32_bf16 v[68:71], v[48:51], v[0:3], v[68:71]\n\t" \
  "ds_read_b128 v[0:3], v56 offset:2048\n\t" \
  "v_mfma_f32_16x16x32_bf16 v[68:71], v[52:55], v[16:19], v[68:71]\n\t" \
  "ds_read_b128 v[16:19], v57 offset:2048\n\t" \
  "v_mfma_f32_16x16x32_bf16 v[72:75], v[40:43], v[4:7], v[72:75]\n\t" \
  "ds_read_b64_tr_b16 v[34:35], v58 offset:2560\n\t" \
  "v_mfma_f32_16x16x32_bf16 v[72:75], v[44:47], v[20:23], v[72:75]\n\t" \
  "ds_read_b64_tr_b16 v[36:37], v58 offset:14336\n\t" \
  "v_mfma_f32_16x16x32_bf16 v[76:79], v[48:51], v[4:7], v[76:79]\n\t" \
  "ds_read_b128 v[4:7], v56 offset:4096\n\t" \
  "v_mfma_f32_16x16x32_bf16 v[76:79], v[52:55], v[20:23], v[76:79]\n\t" \
  "ds_read_b128 v[20:23], v57 offset:4096\n\t" \
  "v_mfma_f32_16x16x32_bf16 v[80:83], v[40:43], v[8:11], v[80:83]\n\t" \
  "ds_read_b64_tr_b16 v[38:39], v58 offset:14848\n\t" \
  "v_mfma_f32_16x16x32_bf16 v[80:83], v[44:47], v[24:27], v[80:83]\n\t" \
  "v_mfma_f32_16x16x32_bf16 v[84:87], v[48:51], v[8:11], v[84:87]\n\t" \
  "ds_read_b128 v[8:11], v56 offset:6144\n\t" \
  "v_mfma_f32_16x16x32_bf16 v[84:87], v[52:55], v[24:27], v[84:87]\n\t" \
  "ds_read_b128 v[24:27], v57 offset:6144\n\t" \
  "v_mfma_f32_16x16x32_bf16 v[88:91], v[40:43], v[12:15], v[88:91]\n\t" \
  "ds_read_b64_tr_b16 v[40:41], v58 offset:1024\n\t" \
  "v_mfma_f32_16x16x32_bf16 v[88:91], v[44:47], v[28:31], v[88:91]\n\t" \
  "ds_read_b64_tr_b16 v[42:43], v58 offset:1536\n\t" \
  "v_mfma_f32_16x16x32_bf16 v[92:95], v[48:51], v[12:15], v[92:95]\n\t" \
  "ds_read_b128 v[12:15], v56 offset:8192\n\t" \
  "v_mfma_f32_16x16x32_bf16 v[92:95], v[52:55], v[28:31], v[92:95]\n\t" \
  "ds_read_b128 v[28:31], v57 offset:8192\n\t" \
  "s_branch N0a_%=\n\t" \
  "NTa_%=:\n\t" \
  "s_setprio 3\n\t" \
  "v_mfma_f32_16x16x32_bf16 v[88:91], v[40:43], v[12:15], v[88:91]\n\t" \
  "v_cvt_pk_bf16_f32 v120, v120, v121\n\t" \
  "v_cvt_pk_bf16_f32 v121, v122, v123\n\t" \
  "v_mfma_f32_16x16x32_bf16 v[88:91], v[44:47], v[28:31], v[88:91]\n\t" \
  "v_cvt_pk_bf16_f32 v122, v124, v125\n\t" \
  "v_cvt_pk_bf16_f32 v123, v126, v127\n\t" \
  "v_mfma_f32_16x16x32_bf16 v[92:95], v[48:51], v[12:15], v[92:95]\n\t" \
  "v_mov_b32 v57, %[fa]\n\t" \
  "v_mov_b32 v58, 1\n\t" \
  "v_mfma_f32_16x16x32_bf16 v[92:95], v[52:55], v[28:31], v[92:95]\n\t" \
  "ds_write_b128 v63, v[120:123]\n\t" \
  "v_cvt_pk_bf16_f32 v38, v156, v157\n\t" \
  "v_mfma_f32_16x16x32_bf16 v[80:83], v[40:43], v[8:11], v[80:83]\n\t" \
  "v_cvt_pk_bf16_f32 v39, v158, v159\n\t" \
  "v_cvt_pk_bf16_f32 v28, v152, v153\n\t" \
  "v_mfma_f32_16x16x32_bf16 v[80:83], v[44:47], v[24:27], v[80:83]\n\t" \
  "v_cvt_pk_bf16_f32 v29, v154, v155\n\t" \
  "v_mov_b32 v60, 0xff61b1e6\n\t" \
  "v_mfma_f32_16x16x32_bf16 v[84:87], v[48:51], v[8:11], v[84:87]\n\t" \
  "ds_write2_b64 v63, v[28:29], v[38:39] offset0:128 offset1:129\n\t" \
  "s_mov_b64 exec, 1\n\t" \
  "ds_write_b32 v57, v58\n\t" \
  "s_mov_b64 exec, -1\n\t" \
  "v_mfma_f32_16x16x32_bf16 v[84:87], v[52:55], v[24:27], v[84:87]\n\t" \
  "v_cvt_pk_bf16_f32 v160, v160, v161\n\t" \
  "v_cvt_pk_bf16_f32 v161, v162, v163\n\t" \
  "v_mfma_f32_16x16x32_bf16 v[72:75], v[40:43], v[4:7], v[72:75]\n\t" \
  "v_cvt_pk_bf16_f32 v162, v164, v165\n\t" \
  "v_cvt_pk_bf16_f32 v163, v166, v167\n\t" \
  "v_mfma_f32_16x16x32_bf16 v[72:75], v[44:47], v[20:23], v[72:75]\n\t" \
  "v_cvt_pk_bf16_f32 v112, v112, v113\n\t" \
  "v_cvt_pk_bf16_f32 v113, v114, v115\n\t" \
  "v_mfma_f32_16x16x32_bf16 v[76:79], v[48:51], v[4:7], v[76:79]\n\t" \
  "v_cvt_pk_bf16_f32 v114, v116, v117\n\t" \
  "v_cvt_pk_bf16_f32 v115, v118, v119\n\t" \
  "v_mfma_f32_16x16x32_bf16 v[76:79], v[52:55], v[20:23], v[76:79]\n\t" \
  "v_cndmask_b32_e64 v12, v60, 0, s[76:77]\n\t" \
  "v_cndmask_b32_e64 v13, v60, 0, s[78:79]\n\t" \
  "v_mfma_f32_16x16x32_bf16 v[64:67], v[40:43], v[0:3], v[64:67]\n\t" \
  "v_cndmask_b32_e64 v14, v60, 0, s[80:81]\n\t" \
  "v_cndmask_b32_e64 v15, v60, 0, s[82:83]\n\t" \
  "v_mfma_f32_16x16x32_bf16 v[64:67], v[44:47], v[16:19], v[64:67]\n\t" \
  "v_cndmask_b32_e64 v56, v60, 0, s[68:69]\n\t" \
  "v_cndmask_b32_e64 v57, v60, 0, s[70:71]\n\t" \
  "v_mfma_f32_16x16x32_bf16 v[68:71], v[48:51], v[0:3], v[68:71]\n\t" \
  "v_cndmask_b32_e64 v58, v60, 0, s[72:73]\n\t" \
  "v_cndmask_b32_e64 v59, v60, 0, s[74:75]\n\t" \
  "v_mfma_f32_16x16x32_bf16 v[68:71], v[52:55], v[16:19], v[68:71]\n\t" \
  "s_memrealtime %[st2]\n\t" \
  "s_branch NX_%=\n\t" \
  "NB_%=:\n\t" \
  "ds_read_b64_tr_b16 v[48:49], v60 offset:13312\n\t" \
  "ds_read_b64_tr_b16 v[50:51], v60 offset:13824\n\t" \
  "ds_read_b128 v[0:3], v56 offset:47104\n\t" \
  "ds_read_b64_tr_b16 v[52:53], v60 offset:25600\n\t" \
  "ds_read_b64_tr_b16 v[54:55], v60 offset:26112\n\t" \
  "ds_read_b128 v[16:19], v57 offset:47104\n\t" \
  "ds_read_b128 v[4:7], v56 offset:49152\n\t" \
  "ds_read_b128 v[20:23], v57 offset:49152\n\t" \
  "ds_read_b128 v[8:11], v56 offset:51200\n\t" \
  "ds_read_b128 v[24:27], v57 offset:51200\n\t" \
  "ds_read_b128 v[12:15], v56 offset:53248\n\t" \
  "ds_read_b128 v[28:31], v57 offset:53248\n\t" \
  "ds_read_b64_tr_b16 v[32:33], v60 offset:14336\n\t" \
  "ds_read_b64_tr_b16 v[34:35], v60 offset:14848\n\t" \
  "s_mov_b32 s68, 0x50000\n\t" \
  "s_mov_b32 s69, 0x5550055\n\t" \
  "s_mov_b32 s70, 0xa0000\n\t" \
  "s_mov_b32 s71, 0xaaa00aa\n\t" \
  "s_mov_b32 s72, 0x150001\n\t" \
  "s_mov_b32 s73, 0x15550155\n\t" \
  "s_mov_b32 s74, 0x2a0002\n\t" \
  "s_mov_b32 s75, 0x2aaa02aa\n\t" \
  "s_mov_b32 s76, 0x55505555\n\t" \
  "s_mov_b32 s77, 0x50005500\n\t" \
  "s_mov_b32 s78, 0xaaa0aaaa\n\t" \
  "s_mov_b32 s79, 0xa000aa00\n\t" \
  "s_mov_b32 s80, 0x55405554\n\t" \
  "s_mov_b32 s81, 0x40005400\n\t" \
  "s_mov_b32 s82, 0xaa80aaa8\n\t" \
  "s_mov_b32 s83, 0x8000a800\n\t" \
  "s_mov_b32 s84, 0xaaaaaaaa\n\t" \
  "s_mov_b32 s85, 0xaaaaaaaa\n\t" \
  "s_waitcnt lgkmcnt(0)\n\t" \
  "s_branch N2b_%=\n\t" \
  "N0b_%=:\n\t" \
  "s_waitcnt lgkmcnt(10)\n\t" \
  "v_mfma_f32_16x16x32_bf16 v[96:99], v[32:35], v[0:3], v[96:99]\n\t" \
  "ds_read_b64_tr_b16 v[44:45], v58 offset:14336\n\t" \
  "s_waitcnt lgkmcnt(7)\n\t" \
  "v_mfma_f32_16x16x32_bf16 v[96:99], v[36:39], v[16:19], v[96:99]\n\t" \
  "ds_read_b64_tr_b16 v[46:47], v58 offset:14848\n\t" \
  "v_mfma_f32_16x16x32_bf16 v[104:107], v[32:35], v[4:7], v[104:107]\n\t" \
  "ds_read_b128 v[48:51], v56 offset:0\n\t" \
  "v_mfma_f32_16x16x32_bf16 v[104:107], v[36:39], v[20:23], v[104:107]\n\t" \
  "ds_read_b128 v[52:55], v57 offset:0\n\t" \
  "s_waitcnt lgkmcnt(8)\n\t" \
  "v_mfma_f32_16x16x32_bf16 v[112:115], v[32:35], v[8:11], v[112:115]\n\t" \
  "v_mfma_f32_16x16x32_bf16 v[112:115], v[36:39], v[24:27], v[112:115]\n\t" \
  "s_waitcnt lgkmcnt(4)\n\t" \
  "v_mfma_f32_16x16x32_bf16 v[120:123], v[32:35], v[12:15], v[120:123]\n\t" \
  "v_mfma_f32_16x16x32_bf16 v[120:123], v[36:39], v[28:31], v[120:123]\n\t" \
  "ds_read_b64_tr_b16 v[32:33], v59 offset:2048\n\t" \
  "v_mfma_f32_16x16x32_bf16 v[128:131], v[0:3], v[40:43], v[128:131]\n\t" \
  "ds_read_b64_tr_b16 v[34:35], v59 offset:2560\n\t" \
  "s_waitcnt lgkmcnt(4)\n\t" \
  "v_mfma_f32_16x16x32_bf16 v[128:131], v[16:19], v[44:47], v[128:131]\n\t" \
  "ds_read_b64_tr_b16 v[36:37], v59 offset:14336\n\t" \
  "v_mfma_f32_16x16x32_bf16 v[136:139], v[4:7], v[40:43], v[136:139]\n\t" \
  "v_mfma_f32_16x16x32_bf16 v[136:139], v[20:23], v[44:47], v[136:139]\n\t" \
  "ds_read_b64_tr_b16 v[38:39], v59 offset:14848\n\t" \
  "v_mfma_f32_16x16x32_bf16 v[144:147], v[8:11], v[40:43], v[144:147]\n\t" \
  "v_mfma_f32_16x16x32_bf16 v[144:147], v[24:27], v[44:47], v[144:147]\n\t" \
  "v_mfma_f32_16x16x32_bf16 v[152:155], v[12:15], v[40:43], v[152:155]\n\t" \
  "v_mfma_f32_16x16x32_bf16 v[152:155], v[28:31], v[44:47], v[152:155]\n\t" \
  "s_waitcnt lgkmcnt(4)\n\t" \
  "v_mfma_f32_16x16x32_bf16 v[160:163], v[48:51], v[40:43], v[160:163]\n\t" \
  "v_mfma_f32_16x16x32_bf16 v[160:163], v[52:55], v[44:47], v[160:163]\n\t" \
  "ds_read_b64_tr_b16 v[40:41], v59 offset:1024\n\t" \
  "s_waitcnt lgkmcnt(1)\n\t" \
  "v_mfma_f32_16x16x32_bf16 v[164:167], v[48:51], v[32:35], v[164:167]\n\t" \
  "ds_read_b64_tr_b16 v[42:43], v59 offset:1536\n\t" \
  "v_mfma_f32_16x16x32_bf16 v[164:167], v[52:55], v[36:39], v[164:167]\n\t" \
  "ds_read_b64_tr_b16 v[44:45], v59 offset:13312\n\t" \
  "v_mfma_f32_16x16x32_bf16 v[132:135], v[0:3], v[32:35], v[132:135]\n\t" \
  "ds_read_b64_tr_b16 v[46:47], v59 offset:13824\n\t" \
  "v_mfma_f32_16x16x32_bf16 v[132:135], v[16:19], v[36:39], v[132:135]\n\t" \
  "ds_read_b64_tr_b16 v[48:49], v58 offset:0\n\t" \
  "v_mfma_f32_16x16x32_bf16 v[140:143], v[4:7], v[32:35], v[140:143]\n\t" \
  "ds_read_b64_tr_b16 v[50:51], v58 offset:512\n\t" \
  "v_mfma_f32_16x16x32_bf16 v[140:143], v[20:23], v[36:39], v[140:143]\n\t" \
  "ds_read_b64_tr_b16 v[52:53], v58 offset:12288\n\t" \
  "v_mfma_f32_16x16x32_bf16 v[148:151], v[8:11], v[32:35], v[148:151]\n\t" \
  "ds_read_b64_tr_b16 v[54:55], v58 offset:12800\n\t" \
  "v_mfma_f32_16x16x32_bf16 v[148:151], v[24:27], v[36:39], v[148:151]\n\t" \
  "v_mfma_f32_16x16x32_bf16 v[156:159], v[12:15], v[32:35], v[156:159]\n\t" \
  "ds_read_b64_tr_b16 v[32:33], v59 offset:0\n\t" \
  "v_mfma_f32_16x16x32_bf16 v[156:159], v[28:31], v[36:39], v[156:159]\n\t" \
  "ds_read_b64_tr_b16 v[34:35], v59 offset:512\n\t" \
  "s_waitcnt lgkmcnt(6)\n\t" \
  "v_mfma_f32_16x16x32_bf16 v[100:103], v[40:43], v[0:3], v[100:103]\n\t" \
  "ds_read_b64_tr_b16 v[36:37], v59 offset:12288\n\t" \
  "v_mfma_f32_16x16x32_bf16 v[100:103], v[44:47], v[16:19], v[100:103]\n\t" \
  "ds_read_b64_tr_b16 v[38:39], v59 offset:12800\n\t" \
  "v_mfma_f32_16x16x32_bf16 v[108:111], v[40:43], v[4:7], v[108:111]\n\t" \
  "v_mfma_f32_16x16x32_bf16 v[108:111], v[44:47], v[20:23], v[108:111]\n\t" \
  "v_mfma_f32_16x16x32_bf16 v[116:119], v[40:43], v[8:11], v[116:119]\n\t" \
  "v_mfma_f32_16x16x32_bf16 v[116:119], v[44:47], v[24:27], v[116:119]\n\t" \
  "v_mfma_f32_16x16x32_bf16 v[124:127], v[40:43], v[12:15], v[124:127]\n\t" \
  "v_mfma_f32_16x16x32_bf16 v[124:127], v[44:47], v[28:31], v[124:127]\n\t" \
  "s_waitcnt lgkmcnt(0)\n\t" \
  "s_barrier\n\t" \
  "v_mfma_f32_16x16x32_bf16 v[64:67], v[48:51], v[0:3], v[64:67]\n\t" \
  "v_mfma_f32_16x16x32_bf16 v[64:67], v[52:55], v[16:19], v[64:67]\n\t" \
  "ds_read_b64_tr_b16 v[40:41], v58 offset:25600\n\t" \
  "v_mfma_f32_16x16x32_bf16 v[68:71], v[32:35], v[0:3], v[68:71]\n\t" \
  "ds_read_b128 v[0:3], v56 offset:28672\n\t" \
  "v_mfma_f32_16x16x32_bf16 v[68:71], v[36:39], v[16:19], v[68:71]\n\t" \
  "ds_read_b128 v[16:19], v57 offset:28672\n\t" \
  "v_mfma_f32_16x16x32_bf16 v[72:75], v[48:51], v[4:7], v[72:75]\n\t" \
  "ds_read_b64_tr_b16 v[42:43], v58 offset:26112\n\t" \
  "v_mfma_f32_16x16x32_bf16 v[72:75], v[52:55], v[20:23], v[72:75]\n\t" \
  "ds_read_b64_tr_b16 v[44:45], v60 offset:1024\n\t" \
  "v_mfma_f32_16x16x32_bf16 v[76:79], v[32:35], v[4:7], v[76:79]\n\t" \
  "ds_read_b128 v[4:7], v56 offset:30720\n\t" \
  "v_mfma_f32_16x16x32_bf16 v[76:79], v[36:39], v[20:23], v[76:79]\n\t" \
  "ds_read_b128 v[20:23], v57 offset:30720\n\t" \
  "v_mfma_f32_16x16x32_bf16 v[80:83], v[48:51], v[8:11], v[80:83]\n\t" \
  "ds_read_b64_tr_b16 v[46:47], v60 offset:1536\n\t" \
  "v_mfma_f32_16x16x32_bf16 v[80:83], v[52:55], v[24:27], v[80:83]\n\t" \
  "v_mfma_f32_16x16x32_bf16 v[84:87], v[32:35], v[8:11], v[84:87]\n\t" \
  "ds_read_b128 v[8:11], v56 offset:32768\n\t" \
  "v_mfma_f32_16x16x32_bf16 v[84:87], v[36:39], v[24:27], v[84:87]\n\t" \
  "ds_read_b128 v[24:27], v57 offset:32768\n\t" \
  "v_mfma_f32_16x16x32_bf16 v[88:91], v[48:51], v[12:15], v[88:91]\n\t" \
  "ds_read_b64_tr_b16 v[48:49], v58 offset:26624\n\t" \
  "v_mfma_f32_16x16x32_bf16 v[88:91], v[52:55], v[28:31], v[88:91]\n\t" \
  "ds_read_b64_tr_b16 v[50:51], v58 offset:27136\n\t" \
  "v_mfma_f32_16x16x32_bf16 v[92:95], v[32:35], v[12:15], v[92:95]\n\t" \
  "ds_read_b128 v[12:15], v56 offset:34816\n\t" \
  "v_mfma_f32_16x16x32_bf16 v[92:95], v[36:39], v[28:31], v[92:95]\n\t" \
  "ds_read_b128 v[28:31], v57 offset:34816\n\t" \
  "N1b_%=:\n\t" \
  "s_waitcnt lgkmcnt(10)\n\t" \
  "v_mfma_f32_16x16x32_bf16 v[96:99], v[40:43], v[0:3], v[96:99]\n\t" \
  "ds_read_b64_tr_b16 v[52:53], v60 offset:2048\n\t" \
  "s_waitcnt lgkmcnt(7)\n\t" \
  "v_mfma_f32_16x16x32_bf16 v[96:99], v[44:47], v[16:19], v[96:99]\n\t" \
  "ds_read_b64_tr_b16 v[54:55], v60 offset:2560\n\t" \
  "v_mfma_f32_16x16x32_bf16 v[104:107], v[40:43], v[4:7], v[104:107]\n\t" \
  "ds_read_b128 v[32:35], v56 offset:18432\n\t" \
  "v_mfma_f32_16x16x32_bf16 v[104:107], v[44:47], v[20:23], v[104:107]\n\t" \
  "ds_read_b128 v[36:39], v57 offset:18432\n\t" \
  "s_waitcnt lgkmcnt(8)\n\t" \
  "v_mfma_f32_16x16x32_bf16 v[112:115], v[40:43], v[8:11], v[112:115]\n\t" \
  "v_mfma_f32_16x16x32_bf16 v[112:115], v[44:47], v[24:27], v[112:115]\n\t" \
  "s_waitcnt lgkmcnt(4)\n\t" \
  "v_mfma_f32_16x16x32_bf16 v[120:123], v[40:43], v[12:15], v[120:123]\n\t" \
  "v_mfma_f32_16x16x32_bf16 v[120:123], v[44:47], v[28:31], v[120:123]\n\t" \
  "ds_read_b64_tr_b16 v[40:41], v59 offset:26624\n\t" \
  "v_mfma_f32_16x16x32_bf16 v[128:131], v[0:3], v[48:51], v[128:131]\n\t" \
  "ds_read_b64_tr_b16 v[42:43], v59 offset:27136\n\t" \
  "s_waitcnt lgkmcnt(4)\n\t" \
  "v_mfma_f32_16x16x32_bf16 v[128:131], v[16:19], v[52:55], v[128:131]\n\t" \
  "ds_read_b64_tr_b16 v[44:45], v61 offset:2048\n\t" \
  "v_mfma_f32_16x16x32_bf16 v[136:139], v[4:7], v[48:51], v[136:139]\n\t" \
  "v_mfma_f32_16x16x32_bf16 v[136:139], v[20:23], v[52:55], v[136:139]\n\t" \
  "ds_read_b64_tr_b16 v[46:47], v61 offset:2560\n\t" \
  "v_mfma_f32_16x16x32_bf16 v[144:147], v[8:11], v[48:51], v[144:147]\n\t" \
  "v_mfma_f32_16x16x32_bf16 v[144:147], v[24:27], v[52:55], v[144:147]\n\t" \
  "v_mfma_f32_16x16x32_bf16 v[152:155], v[12:15], v[48:51], v[152:155]\n\t" \
  "v_mfma_f32_16x16x32_bf16 v[152:155], v[28:31], v[52:55], v[152:155]\n\t" \
  "s_waitcnt lgkmcnt(4)\n\t" \
  "v_mfma_f32_16x16x32_bf16 v[160:163], v[32:35], v[48:51], v[160:163]\n\t" \
  "v_mfma_f32_16x16x32_bf16 v[160:163], v[36:39], v[52:55], v[160:163]\n\t" \
  "ds_read_b64_tr_b16 v[48:49], v59 offset:25600\n\t" \
  "s_waitcnt lgkmcnt(1)\n\t" \
  "v_mfma_f32_16x16x32_bf16 v[164:167], v[32:35], v[40:43], v[164:167]\n\t" \
  "ds_read_b64_tr_b16 v[50:51], v59 offset:26112\n\t" \
  "v_mfma_f32_16x16x32_bf16 v[164:167], v[36:39], v[44:47], v[164:167]\n\t" \
  "ds_read_b64_tr_b16 v[52:53], v61 offset:1024\n\t" \
  "v_mfma_f32_16x16x32_bf16 v[132:135], v[0:3], v[40:43], v[132:135]\n\t" \
  "ds_read_b64_tr_b16 v[54:55], v61 offset:1536\n\t" \
  "v_mfma_f32_16x16x32_bf16 v[132:135], v[16:19], v[44:47], v[132:135]\n\t" \
  "ds_read_b64_tr_b16 v[32:33], v58 offset:24576\n\t" \
  "v_mfma_f32_16x16x32_bf16 v[140:143], v[4:7], v[40:43], v[140:143]\n\t" \
  "ds_read_b64_tr_b16 v[34:35], v58 offset:25088\n\t" \
  "v_mfma_f32_16x16x32_bf16 v[140:143], v[20:23], v[44:47], v[140:143]\n\t" \
  "ds_read_b64_tr_b16 v[36:37], v60 offset:0\n\t" \
  "v_mfma_f32_16x16x32_bf16 v[148:151], v[8:11], v[40:43], v[148:151]\n\t" \
  "ds_read_b64_tr_b16 v[38:39], v60 offset:512\n\t" \
  "v_mfma_f32_16x16x32_bf16 v[148:151], v[24:27], v[44:47], v[148:151]\n\t" \
  "v_mfma_f32_16x16x32_bf16 v[156:159], v[12:15], v[40:43], v[156:159]\n\t" \
  "ds_read_b64_tr_b16 v[40:41], v59 offset:24576\n\t" \
  "v_mfma_f32_16x16x32_bf16 v[156:159], v[28:31], v[44:47], v[156:159]\n\t" \
  "ds_read_b64_tr_b16 v[42:43], v59 offset:25088\n\t" \
  "s_waitcnt lgkmcnt(6)\n\t" \
  "v_mfma_f32_16x16x32_bf16 v[100:103], v[48:51], v[0:3], v[100:103]\n\t" \
  "ds_read_b64_tr_b16 v[44:45], v61 offset:0\n\t" \
  "v_mfma_f32_16x16x32_bf16 v[100:103], v[52:55], v[16:19], v[100:103]\n\t" \
  "ds_read_b64_tr_b16 v[46:47], v61 offset:512\n\t" \
  "v_mfma_f32_16x16x32_bf16 v[108:111], v[48:51], v[4:7], v[108:111]\n\t" \
  "v_mfma_f32_16x16x32_bf16 v[108:111], v[52:55], v[20:23], v[108:111]\n\t" \
  "v_mfma_f32_16x16x32_bf16 v[116:119], v[48:51], v[8:11], v[116:119]\n\t" \
  "v_mfma_f32_16x16x32_bf16 v[116:119], v[52:55], v[24:27], v[116:119]\n\t" \
  "v_mfma_f32_16x16x32_bf16 v[124:127], v[48:51], v[12:15], v[124:127]\n\t" \
  "v_mfma_f32_16x16x32_bf16 v[124:127], v[52:55], v[28:31], v[124:127]\n\t" \
  "s_waitcnt lgkmcnt(0)\n\t" \
  "s_barrier\n\t" \
  "v_mfma_f32_16x16x32_bf16 v[64:67], v[32:35], v[0:3], v[64:67]\n\t" \
  "v_mfma_f32_16x16x32_bf16 v[64:67], v[36:39], v[16:19], v[64:67]\n\t" \
  "ds_read_b64_tr_b16 v[48:49], v60 offset:13312\n\t" \
  "v_mfma_f32_16x16x32_bf16 v[68:71], v[40:43], v[0:3], v[68:71]\n\t" \
  "ds_read_b128 v[0:3], v56 offset:47104\n\t" \
  "v_mfma_f32_16x16x32_bf16 v[68:71], v[44:47], v[16:19], v[68:71]\n\t" \
  "ds_read_b128 v[16:19], v57 offset:47104\n\t" \
  "v_mfma_f32_16x16x32_bf16 v[72:75], v[32:35], v[4:7], v[72:75]\n\t" \
  "ds_read_b64_tr_b16 v[50:51], v60 offset:13824\n\t" \
  "v_mfma_f32_16x16x32_bf16 v[72:75], v[36:39], v[20:23], v[72:75]\n\t" \
  "ds_read_b64_tr_b16 v[52:53], v60 offset:25600\n\t" \
  "v_mfma_f32_16x16x32_bf16 v[76:79], v[40:43], v[4:7], v[76:79]\n\t" \
  "ds_read_b128 v[4:7], v56 offset:49152\n\t" \
  "v_mfma_f32_16x16x32_bf16 v[76:79], v[44:47], v[20:23], v[76:79]\n\t" \
  "ds_read_b128 v[20:23], v57 offset:49152\n\t" \
  "v_mfma_f32_16x16x32_bf16 v[80:83], v[32:35], v[8:11], v[80:83]\n\t" \
  "ds_read_b64_tr_b16 v[54:55], v60 offset:26112\n\t" \
  "v_mfma_f32_16x16x32_bf16 v[80:83], v[36:39], v[24:27], v[80:83]\n\t" \
  "v_mfma_f32_16x16x32_bf16 v[84:87], v[40:43], v[8:11], v[84:87]\n\t" \
  "ds_read_b128 v[8:11], v56 offset:51200\n\t" \
  "v_mfma_f32_16x16x32_bf16 v[84:87], v[44:47], v[24:27], v[84:87]\n\t" \
  "ds_read_b128 v[24:27], v57 offset:51200\n\t" \
  "v_mfma_f32_16x16x32_bf16 v[88:91], v[32:35], v[12:15], v[88:91]\n\t" \
  "ds_read_b64_tr_b16 v[32:33], v60 offset:14336\n\t" \
  "v_mfma_f32_16x16x32_bf16 v[88:91], v[36:39], v[28:31], v[88:91]\n\t" \
  "ds_read_b64_tr_b16 v[34:35], v60 offset:14848\n\t" \
  "v_mfma_f32_16x16x32_bf16 v[92:95], v[40:43], v[12:15], v[92:95]\n\t" \
  "ds_read_b128 v[12:15], v56 offset:53248\n\t" \
  "v_mfma_f32_16x16x32_bf16 v[92:95], v[44:47], v[28:31], v[92:95]\n\t" \
  "ds_read_b128 v[28:31], v57 offset:53248\n\t" \
  "N2b_%=:\n\t" \
  "s_waitcnt lgkmcnt(10)\n\t" \
  "v_mfma_f32_16x16x32_bf16 v[96:99], v[48:51], v[0:3], v[96:99]\n\t" \
  "ds_read_b64_tr_b16 v[36:37], v60 offset:26624\n\t" \
  "s_waitcnt lgkmcnt(7)\n\t" \
  "v_mfma_f32_16x16x32_bf16 v[96:99], v[52:55], v[16:19], v[96:99]\n\t" \
  "ds_read_b64_tr_b16 v[38:39], v60 offset:27136\n\t" \
  "v_mfma_f32_16x16x32_bf16 v[104:107], v[48:51], v[4:7], v[104:107]\n\t" \
  "ds_read_b128 v[40:43], v56 offset:36864\n\t" \
  "v_mfma_f32_16x16x32_bf16 v[104:107], v[52:55], v[20:23], v[104:107]\n\t" \
  "ds_read_b128 v[44:47], v57 offset:36864\n\t" \
  "s_waitcnt lgkmcnt(8)\n\t" \
  "v_mfma_f32_16x16x32_bf16 v[112:115], v[48:51], v[8:11], v[112:115]\n\t" \
  "v_mfma_f32_16x16x32_bf16 v[112:115], v[52:55], v[24:27], v[112:115]\n\t" \
  "s_waitcnt lgkmcnt(4)\n\t" \
  "v_mfma_f32_16x16x32_bf16 v[120:123], v[48:51], v[12:15], v[120:123]\n\t" \
  "v_mfma_f32_16x16x32_bf16 v[120:123], v[52:55], v[28:31], v[120:123]\n\t" \
  "ds_read_b64_tr_b16 v[48:49], v61 offset:14336\n\t" \
  "v_mfma_f32_16x16x32_bf16 v[128:131], v[0:3], v[32:35], v[128:131]\n\t" \
  "ds_read_b64_tr_b16 v[50:51], v61 offset:14848\n\t" \
  "s_waitcnt lgkmcnt(4)\n\t" \
  "v_mfma_f32_16x16x32_bf16 v[128:131], v[16:19], v[36:39], v[128:131]\n\t" \
  "ds_read_b64_tr_b16 v[52:53], v61 offset:26624\n\t" \
  "v_mfma_f32_16x16x32_bf16 v[136:139], v[4:7], v[32:35], v[136:139]\n\t" \
  "v_mfma_f32_16x16x32_bf16 v[136:139], v[20:23], v[36:39], v[136:139]\n\t" \
  "ds_read_b64_tr_b16 v[54:55], v61 offset:27136\n\t" \
  "v_mfma_f32_16x16x32_bf16 v[144:147], v[8:11], v[32:35], v[144:147]\n\t" \
  "v_mfma_f32_16x16x32_bf16 v[144:147], v[24:27], v[36:39], v[144:147]\n\t" \
  "v_mfma_f32_16x16x32_bf16 v[152:155], v[12:15], v[32:35], v[152:155]\n\t" \
  "v_mfma_f32_16x16x32_bf16 v[152:155], v[28:31], v[36:39], v[152:155]\n\t" \
  "s_waitcnt lgkmcnt(4)\n\t" \
  "v_mfma_f32_16x16x32_bf16 v[160:163], v[40:43], v[32:35], v[160:163]\n\t" \
  "v_mfma_f32_16x16x32_bf16 v[160:163], v[44:47], v[36:39], v[160:163]\n\t" \
  "ds_read_b64_tr_b16 v[32:33], v61 offset:13312\n\t" \
  "s_waitcnt lgkmcnt(1)\n\t" \
  "v_mfma_f32_16x16x32_bf16 v[164:167], v[40:43], v[48:51], v[164:167]\n\t" \
  "ds_read_b64_tr_b16 v[34:35], v61 offset:13824\n\t" \
  "v_mfma_f32_16x16x32_bf16 v[164:167], v[44:47], v[52:55], v[164:167]\n\t" \
  "ds_read_b64_tr_b16 v[36:37], v61 offset:25600\n\t" \
  "v_mfma_f32_16x16x32_bf16 v[132:135], v[0:3], v[48:51], v[132:135]\n\t" \
  "ds_read_b64_tr_b16 v[38:39], v61 offset:26112\n\t" \
  "v_mfma_f32_16x16x32_bf16 v[132:135], v[16:19], v[52:55], v[132:135]\n\t" \
  "ds_read_b64_tr_b16 v[40:41], v60 offset:12288\n\t" \
  "v_mfma_f32_16x16x32_bf16 v[140:143], v[4:7], v[48:51], v[140:143]\n\t" \
  "ds_read_b64_tr_b16 v[42:43], v60 offset:12800\n\t" \
  "v_mfma_f32_16x16x32_bf16 v[140:143], v[20:23], v[52:55], v[140:143]\n\t" \
  "ds_read_b64_tr_b16 v[44:45], v60 offset:24576\n\t" \
  "v_mfma_f32_16x16x32_bf16 v[148:151], v[8:11], v[48:51], v[148:151]\n\t" \
  "ds_read_b64_tr_b16 v[46:47], v60 offset:25088\n\t" \
  "v_mfma_f32_16x16x32_bf16 v[148:151], v[24:27], v[52:55], v[148:151]\n\t" \
  "v_mfma_f32_16x16x32_bf16 v[156:159], v[12:15], v[48:51], v[156:159]\n\t" \
  "ds_read_b64_tr_b16 v[48:49], v61 offset:12288\n\t" \
  "v_mfma_f32_16x16x32_bf16 v[156:159], v[28:31], v[52:55], v[156:159]\n\t" \
  "ds_read_b64_tr_b16 v[50:51], v61 offset:12800\n\t" \
  "s_waitcnt lgkmcnt(6)\n\t" \
  "v_mfma_f32_16x16x32_bf16 v[100:103], v[32:35], v[0:3], v[100:103]\n\t" \
  "ds_read_b64_tr_b16 v[52:53], v61 offset:24576\n\t" \
  "v_mfma_f32_16x16x32_bf16 v[100:103], v[36:39], v[16:19], v[100:103]\n\t" \
  "ds_read_b64_tr_b16 v[54:55], v61 offset:25088\n\t" \
  "v_mfma_f32_16x16x32_bf16 v[108:111], v[32:35], v[4:7], v[108:111]\n\t" \
  "v_mfma_f32_16x16x32_bf16 v[108:111], v[36:39], v[20:23], v[108:111]\n\t" \
  "s_addk_i32 %[it], 1\n\t" \
  "v_mfma_f32_16x16x32_bf16 v[116:119], v[32:35], v[8:11], v[116:119]\n\t" \
  "s_cmp_lt_u32 %[it], 6\n\t" \
  "v_mfma_f32_16x16x32_bf16 v[116:119], v[36:39], v[24:27], v[116:119]\n\t" \
  "v_mfma_f32_16x16x32_bf16 v[124:127], v[32:35], v[12:15], v[124:127]\n\t" \
  "v_mfma_f32_16x16x32_bf16 v[124:127], v[36:39], v[28:31], v[124:127]\n\t" \
  "s_waitcnt lgkmcnt(0)\n\t" \
  "s_barrier\n\t" \
  "s_cbranch_scc0 NTb_%=\n\t" \
  "v_mfma_f32_16x16x32_bf16 v[64:67], v[40:43], v[0:3], v[64:67]\n\t" \
  "v_mfma_f32_16x16x32_bf16 v[64:67], v[44:47], v[16:19], v[64:67]\n\t" \
  "ds_read_b64_tr_b16 v[32:33], v58 offset:1024\n\t" \
  "v_mfma_f32_16x16x32_bf16 v[68:71], v[48:51], v[0:3], v[68:71]\n\t" \
  "ds_read_b128 v[0:3], v56 offset:10240\n\t" \
  "v_mfma_f32_16x16x32_bf16 v[68:71], v[52:55], v[16:19], v[68:71]\n\t" \
  "ds_read_b128 v[16:19], v57 offset:10240\n\t" \
  "v_mfma_f32_16x16x32_bf16 v[72:75], v[40:43], v[4:7], v[72:75]\n\t" \
  "ds_read_b64_tr_b16 v[34:35], v58 offset:1536\n\t" \
  "v_mfma_f32_16x16x32_bf16 v[72:75], v[44:47], v[20:23], v[72:75]\n\t" \
  "ds_read_b64_tr_b16 v[36:37], v58 offset:13312\n\t" \
  "v_mfma_f32_16x16x32_bf16 v[76:79], v[48:51], v[4:7], v[76:79]\n\t" \
  "ds_read_b128 v[4:7], v56 offset:12288\n\t" \
  "v_mfma_f32_16x16x32_bf16 v[76:79], v[52:55], v[20:23], v[76:79]\n\t" \
  "ds_read_b128 v[20:23], v57 offset:12288\n\t" \
  "v_mfma_f32_16x16x32_bf16 v[80:83], v[40:43], v[8:11], v[80:83]\n\t" \
  "ds_read_b64_tr_b16 v[38:39], v58 offset:13824\n\t" \
  "v_mfma_f32_16x16x32_bf16 v[80:83], v[44:47], v[24:27], v[80:83]\n\t" \
  "v_mfma_f32_16x16x32_bf16 v[84:87], v[48:51], v[8:11], v[84:87]\n\t" \
  "ds_read_b128 v[8:11], v56 offset:14336\n\t" \
  "v_mfma_f32_16x16x32_bf16 v[84:87], v[52:55], v[24:27], v[84:87]\n\t" \
  "ds_read_b128 v[24:27], v57 offset:14336\n\t" \
  "v_mfma_f32_16x16x32_bf16 v[88:91], v[40:43], v[12:15], v[88:91]\n\t" \
  "ds_read_b64_tr_b16 v[40:41], v58 offset:2048\n\t" \
  "v_mfma_f32_16x16x32_bf16 v[88:91], v[44:47], v[28:31], v[88:91]\n\t" \
  "ds_read_b64_tr_b16 v[42:43], v58 offset:2560\n\t" \
  "v_mfma_f32_16x16x32_bf16 v[92:95], v[48:51], v[12:15], v[92:95]\n\t" \
  "ds_read_b128 v[12:15], v56 offset:16384\n\t" \
  "v_mfma_f32_16x16x32_bf16 v[92:95], v[52:55], v[28:31], v[92:95]\n\t" \
  "ds_read_b128 v[28:31], v57 offset:16384\n\t" \
  "s_branch N0b_%=\n\t" \
  "NTb_%=:\n\t" \
  "s_setprio 3\n\t" \
  "v_mfma_f32_16x16x32_bf16 v[88:91], v[40:43], v[12:15], v[88:91]\n\t" \
  "v_cvt_pk_bf16_f32 v160, v160, v161\n\t" \
  "v_cvt_pk_bf16_f32 v161, v162, v163\n\t" \
  "v_mfma_f32_16x16x32_bf16 v[88:91], v[44:47], v[28:31], v[88:91]\n\t" \
  "v_cvt_pk_bf16_f32 v162, v164, v165\n\t" \
  "v_cvt_pk_bf16_f32 v163, v166, v167\n\t" \
  "v_mfma_f32_16x16x32_bf16 v[92:95], v[48:51], v[12:15], v[92:95]\n\t" \
  "v_mov_b32 v57, %[fa]\n\t" \
  "v_mov_b32 v58, 1\n\t" \
  "v_mfma_f32_16x16x32_bf16 v[92:95], v[52:55], v[28:31], v[92:95]\n\t" \
  "ds_write_b128 v63, v[160:163] offset:2048\n\t" \
  "s_mov_b64 exec, 1\n\t" \
  "ds_write_b32 v57, v58\n\t" \
  "s_mov_b64 exec, -1\n\t" \
  "v_mfma_f32_16x16x32_bf16 v[80:83], v[40:43], v[8:11], v[80:83]\n\t" \
  "v_mov_b32 v60, 0xff61b1e6\n\t" \
  "v_cvt_pk_bf16_f32 v120, v120, v121\n\t" \
  "v_mfma_f32_16x16x32_bf16 v[80:83], v[44:47], v[24:27], v[80:83]\n\t" \
  "v_cvt_pk_bf16_f32 v121, v122, v123\n\t" \
  "v_cvt_pk_bf16_f32 v122, v124, v125\n\t" \
  "v_mfma_f32_16x16x32_bf16 v[84:87], v[48:51], v[8:11], v[84:87]\n\t" \
  "v_cvt_pk_bf16_f32 v123, v126, v127\n\t" \
  "v_cvt_pk_bf16_f32 v28, v152, v153\n\t" \
  "v_mfma_f32_16x16x32_bf16 v[84:87], v[52:55], v[24:27], v[84:87]\n\t" \
  "v_cvt_pk_bf16_f32 v29, v154, v155\n\t" \
  "v_cvt_pk_bf16_f32 v38, v156, v157\n\t" \
  "v_mfma_f32_16x16x32_bf16 v[72:75], v[40:43], v[4:7], v[72:75]\n\t" \
  "v_cvt_pk_bf16_f32 v39, v158, v159\n\t" \
  "v_cvt_pk_bf16_f32 v112, v112, v113\n\t" \
  "v_mfma_f32_16x16x32_bf16 v[72:75], v[44:47], v[20:23], v[72:75]\n\t" \
  "v_cvt_pk_bf16_f32 v113, v114, v115\n\t" \
  "v_cvt_pk_bf16_f32 v114, v116, v117\n\t" \
  "v_mfma_f32_16x16x32_bf16 v[76:79], v[48:51], v[4:7], v[76:79]\n\t" \
  "v_cvt_pk_bf16_f32 v115, v118, v119\n\t" \
  "v_cndmask_b32_e64 v12, v60, 0, s[76:77]\n\t" \
  "v_mfma_f32_16x16x32_bf16 v[76:79], v[52:55], v[20:23], v[76:79]\n\t" \
  "v_cndmask_b32_e64 v13, v60, 0, s[78:79]\n\t" \
  "v_cndmask_b32_e64 v14, v60, 0, s[80:81]\n\t" \
  "v_mfma_f32_16x16x32_bf16 v[64:67], v[40:43], v[0:3], v[64:67]\n\t" \
  "v_cndmask_b32_e64 v15, v60, 0, s[82:83]\n\t" \
  "v_cndmask_b32_e64 v56, v60, 0, s[68:69]\n\t" \
  "v_mfma_f32_16x16x32_bf16 v[64:67], v[44:47], v[16:19], v[64:67]\n\t" \
  "v_cndmask_b32_e64 v57, v60, 0, s[70:71]\n\t" \
  "v_cndmask_b32_e64 v58, v60, 0, s[72:73]\n\t" \
  "v_mfma_f32_16x16x32_bf16 v[68:71], v[48:51], v[0:3], v[68:71]\n\t" \
  "v_cndmask_b32_e64 v59, v60, 0, s[74:75]\n\t" \
  "v_mfma_f32_16x16x32_bf16 v[68:71], v[52:55], v[16:19], v[68:71]\n\t" \
  "s_memrealtime %[st2]\n\t" \
  "NX_%=:\n\t" \
  "v_cvt_pk_bf16_f32 v88, v88, v89\n\t" \
  "v_cvt_pk_bf16_f32 v89, v90, v91\n\t" \
  "v_cvt_pk_bf16_f32 v90, v92, v93\n\t" \
  "v_cvt_pk_bf16_f32 v91, v94, v95\n\t" \
  "v_mov_b32 v61, %[fp]\n\t" \
  "v_add_u32 v60, %[vo], v63\n\t" \
  "v_mfma_f32_16x16x32_bf16 v[152:155], v[112:115], v[88:91], v[56:59]\n\t" \
  "v_cvt_pk_bf16_f32 v26, v144, v145\n\t" \
  "v_cvt_pk_bf16_f32 v27, v146, v147\n\t" \
  "v_mfma_f32_16x16x32_bf16 v[156:159], v[120:123], v[88:91], v[12:15]\n\t" \
  "v_cvt_pk_bf16_f32 v36, v148, v149\n\t" \
  "v_cvt_pk_bf16_f32 v37, v150, v151\n\t" \
  "v_cvt_pk_bf16_f32 v80, v80, v81\n\t" \
  "v_cvt_pk_bf16_f32 v81, v82, v83\n\t" \
  "v_cvt_pk_bf16_f32 v82, v84, v85\n\t" \
  "v_cvt_pk_bf16_f32 v83, v86, v87\n\t" \
  "v_cvt_pk_bf16_f32 v104, v104, v105\n\t" \
  "v_cvt_pk_bf16_f32 v105, v106, v107\n\t" \
  "v_mfma_f32_16x16x32_bf16 v[148:151], v[112:115], v[80:83], v[12:15]\n\t" \
  "v_cvt_pk_bf16_f32 v106, v108, v109\n\t" \
  "v_cvt_pk_bf16_f32 v107, v110, v111\n\t" \
  "ds_read_b32 v0, v61\n\t" \
  "ds_read_b64 v[20:21], v60\n\t" \
  "ds_read_b64 v[30:31], v60 offset:8\n\t" \
  "s_mov_b64 exec, %[rm]\n\t" \
  "ds_read_b128 v[160:163], v63\n\t" \
  "s_mov_b64 exec, -1\n\t" \
  "v_mfma_f32_16x16x32_bf16 v[144:147], v[104:107], v[80:83], v[56:59]\n\t" \
  "v_cvt_pk_bf16_f32 v24, v136, v137\n\t" \
  "v_cvt_pk_bf16_f32 v25, v138, v139\n\t" \
  "v_cvt_pk_bf16_f32 v34, v140, v141\n\t" \
  "v_cvt_pk_bf16_f32 v35, v142, v143\n\t" \
  "v_cvt_pk_bf16_f32 v72, v72, v73\n\t" \
  "v_cvt_pk_bf16_f32 v73, v74, v75\n\t" \
  "v_cvt_pk_bf16_f32 v74, v76, v77\n\t" \
  "v_cvt_pk_bf16_f32 v75, v78, v79\n\t" \
  "v_cvt_pk_bf16_f32 v96, v96, v97\n\t" \
  "v_cvt_pk_bf16_f32 v97, v98, v99\n\t" \
  "v_mfma_f32_16x16x32_bf16 v[140:143], v[104:107], v[72:75], v[12:15]\n\t" \
  "v_cvt_pk_bf16_f32 v98, v100, v101\n\t" \
  "v_cvt_pk_bf16_f32 v99, v102, v103\n\t" \
  "v_cvt_pk_bf16_f32 v22, v128, v129\n\t" \
  "v_cvt_pk_bf16_f32 v23, v130, v131\n\t" \
  "v_mfma_f32_16x16x32_bf16 v[136:139], v[96:99], v[72:75], v[56:59]\n\t" \
  "v_cvt_pk_bf16_f32 v32, v132, v133\n\t" \
  "v_cvt_pk_bf16_f32 v33, v134, v135\n\t" \
  "v_cvt_pk_bf16_f32 v64, v64, v65\n\t" \
  "v_cvt_pk_bf16_f32 v65, v66, v67\n\t" \
  "v_cvt_pk_bf16_f32 v66, v68, v69\n\t" \
  "v_cvt_pk_bf16_f32 v67, v70, v71\n\t" \
  "v_max_f32 v152, v152, v153\n\t" \
  "v_max_f32 v154, v154, v155\n\t" \
  "v_mfma_f32_16x16x32_bf16 v[132:135], v[96:99], v[64:67], v[12:15]\n\t" \
  "AW_%=:\n\t" \
  "s_waitcnt lgkmcnt(0)\n\t" \
  "v_cmp_eq_u32 vcc, 0, v0\n\t" \
  "v_mov_b32 v8, 0xc0c0100\n\t" \
  "v_mov_b32 v9, 0x1000c0c\n\t" \
  "s_cbranch_vccnz AR_%=\n\t" \
  "s_cmp_eq_u32 %[z], 0\n\t" \
  "s_cbranch_scc1 AZ_%=\n\t" \
  "AY_%=:\n\t" \
  "v_mfma_f32_16x16x32_bf16 v[128:131], v[160:163], v[64:67], v[56:59]\n\t" \
  "v_max_f32 v156, v156, v157\n\t" \
  "v_max_f32 v158, v158, v159\n\t" \
  "v_max3_f32 v7, v152, v154, v156\n\t" \
  "v_max_f32 v7, v7, v158\n\t" \
  "v_max_f32 v144, v144, v145\n\t" \
  "v_max_f32 v146, v146, v147\n\t" \
  "v_max_f32 v148, v148, v149\n\t" \
  "v_max_f32 v150, v150, v151\n\t" \
  "v_max3_f32 v6, v144, v146, v148\n\t" \
  "v_max_f32 v6, v6, v150\n\t" \
  "v_max_f32 v136, v136, v137\n\t" \
  "v_max_f32 v138, v138, v139\n\t" \
  "v_max_f32 v140, v140, v141\n\t" \
  "v_max_f32 v142, v142, v143\n\t" \
  "v_max3_f32 v5, v136, v138, v140\n\t" \
  "v_max_f32 v5, v5, v142\n\t" \
  "v_max_f32 v128, v128, v129\n\t" \
  "v_max_f32 v130, v130, v131\n\t" \
  "v_max_f32 v132, v132, v133\n\t" \
  "v_max_f32 v134, v134, v135\n\t" \
  "v_max3_f32 v4, v128, v130, v132\n\t" \
  "v_max_f32 v4, v4, v134\n\t" \
  "v_cndmask_b32_e64 v60, v8, v9, s[84:85]\n\t" \
  "v_mov_b32 v8, 0xc0c0302\n\t" \
  "v_permlane16_swap_b32 v6, v7\n\t" \
  "v_permlane16_swap_b32 v4, v5\n\t" \
  "v_max_f32 v6, v6, v7\n\t" \
  "v_max_f32 v4, v4, v5\n\t" \
  "v_mov_b32 v9, 0x3020c0c\n\t" \
  "v_cndmask_b32_e64 v61, v8, v9, s[84:85]\n\t" \
  "v_permlane32_swap_b32 v4, v6\n\t" \
  "v_max_f32 v4, v4, v6\n\t" \
  "v_mul_f32 v6, 0xbe8293ee, v4\n\t" \
  "v_mul_f32 v4, 0xbe8293ee, v4\n\t" \
  "s_nop 1\n\t" \
  "v_permlane32_swap_b32 v4, v6\n\t" \
  "v_mov_b32 v5, v4\n\t" \
  "v_mov_b32 v7, v6\n\t" \
  "s_nop 0\n\t" \
  "v_permlane16_swap_b32 v4, v5\n\t" \
  "v_permlane16_swap_b32 v6, v7\n\t" \
  "v_fmamk_f32 v128, v128, 0x3e8293ee, v4\n\t" \
  "v_fmamk_f32 v130, v130, 0x3e8293ee, v4\n\t" \
  "v_fmamk_f32 v132, v132, 0x3e8293ee, v4\n\t" \
  "v_fmamk_f32 v134, v134, 0x3e8293ee, v4\n\t" \
  "v_fmamk_f32 v136, v136, 0x3e8293ee, v5\n\t" \
  "v_fmamk_f32 v138, v138, 0x3e8293ee, v5\n\t" \
  "v_fmamk_f32 v140, v140, 0x3e8293ee, v5\n\t" \
  "v_fmamk_f32 v142, v142, 0x3e8293ee, v5\n\t" \
  "v_fmamk_f32 v144, v144, 0x3e8293ee, v6\n\t" \
  "v_fmamk_f32 v146, v146, 0x3e8293ee, v6\n\t" \
  "v_fmamk_f32 v148, v148, 0x3e8293ee, v6\n\t" \
  "v_fmamk_f32 v150, v150, 0x3e8293ee, v6\n\t" \
  "v_fmamk_f32 v152, v152, 0x3e8293ee, v7\n\t" \
  "v_fmamk_f32 v154, v154, 0x3e8293ee, v7\n\t" \
  "v_fmamk_f32 v156, v156, 0x3e8293ee, v7\n\t" \
  "v_fmamk_f32 v158, v158, 0x3e8293ee, v7\n\t" \
  "v_exp_f32 v128, v128\n\t" \
  "v_exp_f32 v130, v130\n\t" \
  "v_exp_f32 v132, v132\n\t" \
  "v_exp_f32 v134, v134\n\t" \
  "v_exp_f32 v136, v136\n\t" \
  "v_exp_f32 v138, v138\n\t" \
  "v_exp_f32 v140, v140\n\t" \
  "v_exp_f32 v142, v142\n\t" \
  "v_exp_f32 v144, v144\n\t" \
  "v_exp_f32 v146, v146\n\t" \
  "v_exp_f32 v148, v148\n\t" \
  "v_exp_f32 v150, v150\n\t" \
  "v_exp_f32 v152, v152\n\t" \
  "v_exp_f32 v154, v154\n\t" \
  "v_exp_f32 v156, v156\n\t" \
  "v_exp_f32 v158, v158\n\t" \
  "v_add_f32 v4, v128, v130\n\t" \
  "v_add_f32 v8, v132, v134\n\t" \
  "v_add_f32 v5, v136, v138\n\t" \
  "v_add_f32 v9, v140, v142\n\t" \
  "v_add_f32 v6, v144, v146\n\t" \
  "v_add_f32 v10, v148, v150\n\t" \
  "v_add_f32 v7, v152, v154\n\t" \
  "v_add_f32 v11, v156, v158\n\t" \
  "v_add_f32 v4, v4, v8\n\t" \
  "v_add_f32 v5, v5, v9\n\t" \
  "v_add_f32 v6, v6, v10\n\t" \
  "v_add_f32 v7, v7, v11\n\t" \
  "v_permlane16_swap_b32 v4, v5\n\t" \
  "s_nop 0\n\t" \
  "v_permlane16_swap_b32 v6, v7\n\t" \
  "v_add_f32 v4, v4, v5\n\t" \
  "v_add_f32 v6, v6, v7\n\t" \
  "s_nop 1\n\t" \
  "v_permlane32_swap_b32 v4, v6\n\t" \
  "v_add_f32 v4, v4, v6\n\t" \
  "v_rcp_f32 v4, v4\n\t" \
  "s_nop 0\n\t" \
  "v_mov_b32 v6, v4\n\t" \
  "s_nop 1\n\t" \
  "v_permlane32_swap_b32 v4, v6\n\t" \
  "v_mov_b32 v5, v4\n\t" \
  "v_mov_b32 v7, v6\n\t" \
  "s_nop 0\n\t" \
  "v_permlane16_swap_b32 v4, v5\n\t" \
  "v_permlane16_swap_b32 v6, v7\n\t" \
  "v_mul_f32 v128, v128, v4\n\t" \
  "v_mul_f32 v130, v130, v4\n\t" \
  "v_mul_f32 v132, v132, v4\n\t" \
  "v_mul_f32 v134, v134, v4\n\t" \
  "v_cvt_pk_bf16_f32 v128, v128, v130\n\t" \
  "v_cvt_pk_bf16_f32 v132, v132, v134\n\t" \
  "v_perm_b32 v68, 0, v128, v60\n\t" \
  "v_perm_b32 v69, 0, v128, v61\n\t" \
  "v_perm_b32 v70, 0, v132, v60\n\t" \
  "v_perm_b32 v71, 0, v132, v61\n\t" \
  "v_mul_f32 v136, v136, v5\n\t" \
  "v_mul_f32 v138, v138, v5\n\t" \
  "v_mfma_f32_16x16x32_bf16 v[128:131], v[20:23], v[68:71], 0\n\t" \
  "v_mul_f32 v140, v140, v5\n\t" \
  "v_mul_f32 v142, v142, v5\n\t" \
  "v_cvt_pk_bf16_f32 v136, v136, v138\n\t" \
  "v_mfma_f32_16x16x32_bf16 v[132:135], v[30:33], v[68:71], 0\n\t" \
  "v_cvt_pk_bf16_f32 v140, v140, v142\n\t" \
  "v_perm_b32 v76, 0, v136, v60\n\t" \
  "v_perm_b32 v77, 0, v136, v61\n\t" \
  "v_perm_b32 v78, 0, v140, v60\n\t" \
  "v_perm_b32 v79, 0, v140, v61\n\t" \
  "v_mul_f32 v144, v144, v6\n\t" \
  "v_mul_f32 v146, v146, v6\n\t" \
  "v_mfma_f32_16x16x32_bf16 v[136:139], v[22:25], v[76:79], 0\n\t" \
  "v_mul_f32 v148, v148, v6\n\t" \
  "v_mul_f32 v150, v150, v6\n\t" \
  "v_cvt_pk_bf16_f32 v144, v144, v146\n\t" \
  "v_mfma_f32_16x16x32_bf16 v[140:143], v[32:35], v[76:79], 0\n\t" \
  "v_cvt_pk_bf16_f32 v148, v148, v150\n\t" \
  "v_perm_b32 v84, 0, v144, v60\n\t" \
  "v_perm_b32 v85, 0, v144, v61\n\t" \
  "v_perm_b32 v86, 0, v148, v60\n\t" \
  "v_perm_b32 v87, 0, v148, v61\n\t" \
  "v_cvt_pk_bf16_f32 v100, v128, v129\n\t" \
  "v_cvt_pk_bf16_f32 v101, v130, v131\n\t" \
  "v_mfma_f32_16x16x32_bf16 v[144:147], v[24:27], v[84:87], 0\n\t" \
  "v_cvt_pk_bf16_f32 v102, v132, v133\n\t" \
  "v_cvt_pk_bf16_f32 v103, v134, v135\n\t" \
  "global_store_dwordx4 v62, v[100:103], %[ot] offset:0 sc1\n\t" \
  "v_mfma_f32_16x16x32_bf16 v[148:151], v[34:37], v[84:87], 0\n\t" \
  "v_mul_f32 v152, v152, v7\n\t" \
  "v_mul_f32 v154, v154, v7\n\t" \
  "v_mul_f32 v156, v156, v7\n\t" \
  "v_mul_f32 v158, v158, v7\n\t" \
  "v_cvt_pk_bf16_f32 v152, v152, v154\n\t" \
  "v_cvt_pk_bf16_f32 v156, v156, v158\n\t" \
  "v_perm_b32 v92, 0, v152, v60\n\t" \
  "v_perm_b32 v93, 0, v152, v61\n\t" \
  "v_perm_b32 v94, 0, v156, v60\n\t" \
  "v_perm_b32 v95, 0, v156, v61\n\t" \
  "v_cvt_pk_bf16_f32 v108, v136, v137\n\t" \
  "v_cvt_pk_bf16_f32 v109, v138, v139\n\t" \
  "v_mfma_f32_16x16x32_bf16 v[152:155], v[26:29], v[92:95], 0\n\t" \
  "v_cvt_pk_bf16_f32 v110, v140, v141\n\t" \
  "v_cvt_pk_bf16_f32 v111, v142, v143\n\t" \
  "global_store_dwordx4 v62, v[108:111], %[ot] offset:1024 sc1\n\t" \
  "v_mfma_f32_16x16x32_bf16 v[156:159], v[36:39], v[92:95], 0\n\t" \
  "v_cvt_pk_bf16_f32 v116, v144, v145\n\t" \
  "v_cvt_pk_bf16_f32 v117, v146, v147\n\t" \
  "v_cvt_pk_bf16_f32 v118, v148, v149\n\t" \
  "v_cvt_pk_bf16_f32 v119, v150, v151\n\t" \
  "global_store_dwordx4 v62, v[116:119], %[ou] offset:0 sc1\n\t" \
  "v_cvt_pk_bf16_f32 v124, v152, v153\n\t" \
  "v_cvt_pk_bf16_f32 v125, v154, v155\n\t" \
  "s_nop 0\n\t" \
  "v_cvt_pk_bf16_f32 v126, v156, v157\n\t" \
  "v_cvt_pk_bf16_f32 v127, v158, v159\n\t" \
  "global_store_dwordx4 v62, v[124:127], %[ou] offset:1024 sc1\n\t" \
  "s_branch AE_%=\n\t" \
  "AR_%=:\n\t" \
  "s_addk_i32 %[it], 1\n\t" \
  "s_cmp_gt_u32 %[it], 0x400000\n\t" \
  "s_cbranch_scc1 AY_%=\n\t" \
  "ds_read_b32 v0, v61\n\t" \
  "ds_read_b64 v[20:21], v60\n\t" \
  "ds_read_b64 v[30:31], v60 offset:8\n\t" \
  "s_mov_b64 exec, %[rm]\n\t" \
  "ds_read_b128 v[160:163], v63\n\t" \
  "s_mov_b64 exec, -1\n\t" \
  "s_branch AW_%=\n\t" \
  "AZ_%=:\n\t" \
  "v_mov_b32 v0, 0xff61b1e6\n\t" \
  "v_mov_b32 v1, 0xcfa8965f\n\t" \
  "s_nop 3\n\t" \
  "v_cndmask_b32_e64 v56, v0, v1, s[68:69]\n\t" \
  "v_cndmask_b32_e64 v57, v0, v1, s[70:71]\n\t" \
  "v_cndmask_b32_e64 v58, v0, v1, s[72:73]\n\t" \
  "v_cndmask_b32_e64 v59, v0, v1, s[74:75]\n\t" \
  "s_nop 1\n\t" \
  "s_branch AY_%=\n\t" \
  "AE_%=:\n\t" \
  "s_nop 1\n\t"
#define N2_CLOB "v0", "v1", "v2", "v3", "v4", "v5", "v6", "v7", "v8", "v9", "v10", "v11", "v12", "v13", "v14", "v15", "v16", "v17", "v18", "v19", "v20", "v21", "v22", "v23", "v24", "v25", "v26", "v27", "v28", "v29", "v30", "v31", "v32", "v33", "v34", "v35", "v36", "v37", "v38", "v39", "v40", "v41", "v42", "v43", "v44", "v45", "v46", "v47", "v48", "v49", "v50", "v51", "v52", "v53", "v54", "v55", "v60", "v61", "v64", "v65", "v66", "v67", "v68", "v69", "v70", "v71", "v72", "v73", "v74", "v75", "v76", "v77", "v78", "v79", "v80", "v81", "v82", "v83", "v84", "v85", "v86", "v87", "v88", "v89", "v90", "v91", "v92", "v93", "v94", "v95", "v96", "v97", "v98", "v99", "v100", "v101", "v102", "v103", "v104", "v105", "v106", "v107", "v108", "v109", "v110", "v111", "v112", "v113", "v114", "v115", "v116", "v117", "v118", "v119", "v120", "v121", "v122", "v123", "v124", "v125", "v126", "v127", "v128", "v129", "v130", "v131", "v132", "v133", "v134", "v135", "v136", "v137", "v138", "v139", "v140", "v141", "v142", "v143", "v144", "v145", "v146", "v147", "v148", "v149", "v150", "v151", "v152", "v153", "v154", "v155", "v156", "v157", "v158", "v159", "v160", "v161", "v162", "v163", "v164", "v165", "v166", "v167", "s68", "s69", "s70", "s71", "s72", "s73", "s74", "s75", "s76", "s77", "s78", "s79", "s80", "s81", "s82", "s83", "s84", "s85"

#define CV_PRE 1
#define CV_Y 1
#define CV_ASM \
  "CS_%=:\n\t" \
  "ds_read_b32 v34, v61\n\t" \
  "s_waitcnt lgkmcnt(0)\n\t" \
  "v_cmp_ne_u32 vcc, %[tg], v34\n\t" \
  "s_cbranch_vccz CR_%=\n\t" \
  "s_cmp_eq_u32 %[rem], 0\n\t" \
  "s_cbranch_scc1 CT_%=\n\t" \
  "s_barrier\n\t" \
  "s_sub_u32 %[rem], %[rem], 1\n\t" \
  "s_branch CS_%=\n\t" \
  "CT_%=:\n\t" \
  "s_addk_i32 %[cn], 1\n\t" \
  "s_cmp_gt_u32 %[cn], 0x400000\n\t" \
  "s_cbranch_scc1 CR_%=\n\t" \
  "s_sleep 1\n\t" \
  "s_branch CS_%=\n\t" \
  "CR_%=:\n\t" \
  "ds_read_b64 v[0:1], v28 offset:0\n\t" \
  "ds_read_b64 v[2:3], v29 offset:0\n\t" \
  "ds_read_b64 v[4:5], v30 offset:1024\n\t" \
  "ds_read_b64 v[6:7], v31 offset:1024\n\t" \
  "ds_read_b64 v[8:9], v24 offset:2048\n\t" \
  "ds_read_b64 v[10:11], v25 offset:2048\n\t" \
  "ds_read_b64 v[12:13], v26 offset:3072\n\t" \
  "ds_read_b64 v[14:15], v27 offset:3072\n\t" \
  "ds_read_b64 v[16:17], v28 offset:4096\n\t" \
  "ds_read_b64 v[18:19], v29 offset:4096\n\t" \
  "ds_read_b64 v[20:21], v30 offset:5120\n\t" \
  "ds_read_b64 v[22:23], v31 offset:5120\n\t" \
  "s_mov_b64 vcc, 1\n\t" \
  "s_setprio 0\n\t" \
  "s_cmp_eq_u32 %[rem], 0\n\t" \
  "s_cbranch_scc1 CM_%=\n\t" \
  "CB_%=:\n\t" \
  "s_barrier\n\t" \
  "s_sub_u32 %[rem], %[rem], 1\n\t" \
  "s_cmp_lg_u32 %[rem], 0\n\t" \
  "s_cbranch_scc1 CB_%=\n\t" \
  "CM_%=:\n\t" \
  "s_memrealtime %[st3]\n\t" \
  "s_waitcnt lgkmcnt(0)\n\t" \
  "v_mfma_f32_16x16x32_bf16 v[128:131], v[68:71], v[0:3], 0\n\t" \
  "v_mfma_f32_16x16x32_bf16 v[128:131], v[64:67], v[4:7], v[128:131]\n\t" \
  "v_mfma_f32_16x16x32_bf16 v[132:135], v[76:79], v[0:3], 0\n\t" \
  "v_mfma_f32_16x16x32_bf16 v[132:135], v[72:75], v[4:7], v[132:135]\n\t" \
  "v_mfma_f32_16x16x32_bf16 v[136:139], v[84:87], v[0:3], 0\n\t" \
  "v_mfma_f32_16x16x32_bf16 v[136:139], v[80:83], v[4:7], v[136:139]\n\t" \
  "v_mfma_f32_16x16x32_bf16 v[140:143], v[92:95], v[0:3], 0\n\t" \
  "s_setprio 1\n\t" \
  "v_mfma_f32_16x16x32_bf16 v[140:143], v[88:91], v[4:7], v[140:143]\n\t" \
  "v_mfma_f32_16x16x32_bf16 v[144:147], v[100:103], v[0:3], 0\n\t" \
  "ds_bpermute_b32 v34, v32, v131\n\t" \
  "v_mfma_f32_16x16x32_bf16 v[144:147], v[96:99], v[4:7], v[144:147]\n\t" \
  "v_mov_b32 v131, v130\n\t" \
  "v_mov_b32 v130, v129\n\t" \
  "v_mfma_f32_16x16x32_bf16 v[148:151], v[108:111], v[0:3], 0\n\t" \
  "v_mov_b32 v129, v128\n\t" \
  "v_mfma_f32_16x16x32_bf16 v[148:151], v[104:107], v[4:7], v[148:151]\n\t" \
  "v_mfma_f32_16x16x32_bf16 v[152:155], v[116:119], v[0:3], 0\n\t" \
  "v_mfma_f32_16x16x32_bf16 v[152:155], v[112:115], v[4:7], v[152:155]\n\t" \
  "v_mfma_f32_16x16x32_bf16 v[156:159], v[124:127], v[0:3], 0\n\t" \
  "ds_read_b64 v[0:1], v24 offset:6144\n\t" \
  "v_mfma_f32_16x16x32_bf16 v[156:159], v[120:123], v[4:7], v[156:159]\n\t" \
  "ds_read_b64 v[2:3], v25 offset:6144\n\t" \
  "v_mfma_f32_16x16x32_bf16 v[132:135], v[68:71], v[8:11], v[132:135]\n\t" \
  "ds_read_b64 v[4:5], v26 offset:7168\n\t" \
  "v_mfma_f32_16x16x32_bf16 v[132:135], v[64:67], v[12:15], v[132:135]\n\t" \
  "ds_read_b64 v[6:7], v27 offset:7168\n\t" \
  "v_mfma_f32_16x16x32_bf16 v[136:139], v[76:79], v[8:11], v[136:139]\n\t" \
  "v_mfma_f32_16x16x32_bf16 v[136:139], v[72:75], v[12:15], v[136:139]\n\t" \
  "s_waitcnt lgkmcnt(4)\n\t" \
  "v_cndmask_b32_e64 v128, v34, 0, vcc\n\t" \
  "v_mfma_f32_16x16x32_bf16 v[140:143], v[84:87], v[8:11], v[140:143]\n\t" \
  "v_mfma_f32_16x16x32_bf16 v[140:143], v[80:83], v[12:15], v[140:143]\n\t" \
  "v_mfma_f32_16x16x32_bf16 v[144:147], v[92:95], v[8:11], v[144:147]\n\t" \
  "ds_bpermute_b32 v35, v32, v135\n\t" \
  "v_mfma_f32_16x16x32_bf16 v[144:147], v[88:91], v[12:15], v[144:147]\n\t" \
  "v_mov_b32 v135, v134\n\t" \
  "v_mov_b32 v134, v133\n\t" \
  "v_mfma_f32_16x16x32_bf16 v[148:151], v[100:103], v[8:11], v[148:151]\n\t" \
  "v_mov_b32 v133, v132\n\t" \
  "v_mfma_f32_16x16x32_bf16 v[148:151], v[96:99], v[12:15], v[148:151]\n\t" \
  "v_mfma_f32_16x16x32_bf16 v[152:155], v[108:111], v[8:11], v[152:155]\n\t" \
  "v_mfma_f32_16x16x32_bf16 v[152:155], v[104:107], v[12:15], v[152:155]\n\t" \
  "v_mfma_f32_16x16x32_bf16 v[156:159], v[116:119], v[8:11], v[156:159]\n\t" \
  "ds_read_b64 v[8:9], v28 offset:8192\n\t" \
  "v_mfma_f32_16x16x32_bf16 v[156:159], v[112:115], v[12:15], v[156:159]\n\t" \
  "ds_read_b64 v[10:11], v29 offset:8192\n\t" \
  "v_mfma_f32_16x16x32_bf16 v[136:139], v[68:71], v[16:19], v[136:139]\n\t" \
  "ds_read_b64 v[12:13], v30 offset:9216\n\t" \
  "v_mfma_f32_16x16x32_bf16 v[136:139], v[64:67], v[20:23], v[136:139]\n\t" \
  "ds_read_b64 v[14:15], v31 offset:9216\n\t" \
  "v_mfma_f32_16x16x32_bf16 v[140:143], v[76:79], v[16:19], v[140:143]\n\t" \
  "v_mfma_f32_16x16x32_bf16 v[140:143], v[72:75], v[20:23], v[140:143]\n\t" \
  "s_waitcnt lgkmcnt(4)\n\t" \
  "v_cndmask_b32_e64 v132, v35, v34, vcc\n\t" \
  "v_mfma_f32_16x16x32_bf16 v[144:147], v[84:87], v[16:19], v[144:147]\n\t" \
  "v_mfma_f32_16x16x32_bf16 v[144:147], v[80:83], v[20:23], v[144:147]\n\t" \
  "v_mfma_f32_16x16x32_bf16 v[148:151], v[92:95], v[16:19], v[148:151]\n\t" \
  "ds_bpermute_b32 v36, v32, v139\n\t" \
  "v_mfma_f32_16x16x32_bf16 v[148:151], v[88:91], v[20:23], v[148:151]\n\t" \
  "v_mov_b32 v139, v138\n\t" \
  "v_mov_b32 v138, v137\n\t" \
  "v_mfma_f32_16x16x32_bf16 v[152:155], v[100:103], v[16:19], v[152:155]\n\t" \
  "v_mov_b32 v137, v136\n\t" \
  "v_mfma_f32_16x16x32_bf16 v[152:155], v[96:99], v[20:23], v[152:155]\n\t" \
  "v_mfma_f32_16x16x32_bf16 v[156:159], v[108:111], v[16:19], v[156:159]\n\t" \
  "ds_read_b64 v[16:17], v24 offset:10240\n\t" \
  "v_mfma_f32_16x16x32_bf16 v[156:159], v[104:107], v[20:23], v[156:159]\n\t" \
  "ds_read_b64 v[18:19], v25 offset:10240\n\t" \
  "v_mfma_f32_16x16x32_bf16 v[140:143], v[68:71], v[0:3], v[140:143]\n\t" \
  "ds_read_b64 v[20:21], v26 offset:11264\n\t" \
  "v_mfma_f32_16x16x32_bf16 v[140:143], v[64:67], v[4:7], v[140:143]\n\t" \
  "ds_read_b64 v[22:23], v27 offset:11264\n\t" \
  "v_mfma_f32_16x16x32_bf16 v[144:147], v[76:79], v[0:3], v[144:147]\n\t" \
  "v_mfma_f32_16x16x32_bf16 v[144:147], v[72:75], v[4:7], v[144:147]\n\t" \
  "v_mfma_f32_16x16x32_bf16 v[148:151], v[84:87], v[0:3], v[148:151]\n\t" \
  "v_mfma_f32_16x16x32_bf16 v[148:151], v[80:83], v[4:7], v[148:151]\n\t" \
  "s_waitcnt lgkmcnt(4)\n\t" \
  "v_cndmask_b32_e64 v136, v36, v35, vcc\n\t" \
  "v_mfma_f32_16x16x32_bf16 v[152:155], v[92:95], v[0:3], v[152:155]\n\t" \
  "ds_bpermute_b32 v37, v32, v143\n\t" \
  "v_mfma_f32_16x16x32_bf16 v[152:155], v[88:91], v[4:7], v[152:155]\n\t" \
  "v_mov_b32 v143, v142\n\t" \
  "v_mov_b32 v142, v141\n\t" \
  "v_mfma_f32_16x16x32_bf16 v[156:159], v[100:103], v[0:3], v[156:159]\n\t" \
  "ds_read_b64 v[0:1], v28 offset:12288\n\t" \
  "v_mov_b32 v141, v140\n\t" \
  "v_mfma_f32_16x16x32_bf16 v[156:159], v[96:99], v[4:7], v[156:159]\n\t" \
  "ds_read_b64 v[2:3], v29 offset:12288\n\t" \
  "v_mfma_f32_16x16x32_bf16 v[144:147], v[68:71], v[8:11], v[144:147]\n\t" \
  "ds_read_b64 v[4:5], v30 offset:13312\n\t" \
  "v_mfma_f32_16x16x32_bf16 v[144:147], v[64:67], v[12:15], v[144:147]\n\t" \
  "ds_read_b64 v[6:7], v31 offset:13312\n\t" \
  "v_mfma_f32_16x16x32_bf16 v[148:151], v[76:79], v[8:11], v[148:151]\n\t" \
  "v_mfma_f32_16x16x32_bf16 v[148:151], v[72:75], v[12:15], v[148:151]\n\t" \
  "v_mfma_f32_16x16x32_bf16 v[152:155], v[84:87], v[8:11], v[152:155]\n\t" \
  "v_mfma_f32_16x16x32_bf16 v[152:155], v[80:83], v[12:15], v[152:155]\n\t" \
  "v_mfma_f32_16x16x32_bf16 v[156:159], v[92:95], v[8:11], v[156:159]\n\t" \
  "ds_read_b64 v[8:9], v24 offset:14336\n\t" \
  "v_mfma_f32_16x16x32_bf16 v[156:159], v[88:91], v[12:15], v[156:159]\n\t" \
  "ds_read_b64 v[10:11], v25 offset:14336\n\t" \
  "s_waitcnt lgkmcnt(6)\n\t" \
  "v_mfma_f32_16x16x32_bf16 v[148:151], v[68:71], v[16:19], v[148:151]\n\t" \
  "ds_read_b64 v[12:13], v26 offset:15360\n\t" \
  "v_cndmask_b32_e64 v140, v37, v36, vcc\n\t" \
  "v_mfma_f32_16x16x32_bf16 v[148:151], v[64:67], v[20:23], v[148:151]\n\t" \
  "ds_read_b64 v[14:15], v27 offset:15360\n\t" \
  "v_mfma_f32_16x16x32_bf16 v[152:155], v[76:79], v[16:19], v[152:155]\n\t" \
  "ds_bpermute_b32 v38, v32, v147\n\t" \
  "v_mfma_f32_16x16x32_bf16 v[152:155], v[72:75], v[20:23], v[152:155]\n\t" \
  "v_mov_b32 v147, v146\n\t" \
  "v_mov_b32 v146, v145\n\t" \
  "v_mfma_f32_16x16x32_bf16 v[156:159], v[84:87], v[16:19], v[156:159]\n\t" \
  "ds_read_b64 v[16:17], v26 offset:0\n\t" \
  "ds_read_b64 v[18:19], v27 offset:0\n\t" \
  "v_mfma_f32_16x16x32_bf16 v[156:159], v[80:83], v[20:23], v[156:159]\n\t" \
  "ds_read_b64 v[20:21], v28 offset:1024\n\t" \
  "ds_read_b64 v[22:23], v29 offset:1024\n\t" \
  "s_waitcnt lgkmcnt(9)\n\t" \
  "v_mfma_f32_16x16x32_bf16 v[152:155], v[68:71], v[0:3], v[152:155]\n\t" \
  "ds_bpermute_b32 v39, v32, v151\n\t" \
  "v_mov_b32 v145, v144\n\t" \
  "v_mfma_f32_16x16x32_bf16 v[152:155], v[64:67], v[4:7], v[152:155]\n\t" \
  "v_mov_b32 v151, v150\n\t" \
  "v_mov_b32 v150, v149\n\t" \
  "v_mfma_f32_16x16x32_bf16 v[156:159], v[76:79], v[0:3], v[156:159]\n\t" \
  "ds_read_b64 v[0:1], v30 offset:2048\n\t" \
  "v_mov_b32 v149, v148\n\t" \
  "v_mfma_f32_16x16x32_bf16 v[156:159], v[72:75], v[4:7], v[156:159]\n\t" \
  "ds_read_b64 v[2:3], v31 offset:2048\n\t" \
  "s_waitcnt lgkmcnt(10)\n\t" \
  "v_mfma_f32_16x16x32_bf16 v[156:159], v[68:71], v[8:11], v[156:159]\n\t" \
  "ds_bpermute_b32 v40, v32, v155\n\t" \
  "s_waitcnt lgkmcnt(9)\n\t" \
  "v_mfma_f32_16x16x32_bf16 v[156:159], v[64:67], v[12:15], v[156:159]\n\t" \
  "ds_read_b64 v[4:5], v24 offset:3072\n\t" \
  "s_waitcnt lgkmcnt(7)\n\t" \
  "v_mfma_f32_16x16x32_bf16 v[128:131], v[68:71], v[16:19], v[128:131]\n\t" \
  "ds_read_b64 v[6:7], v25 offset:3072\n\t" \
  "s_waitcnt lgkmcnt(6)\n\t" \
  "v_mfma_f32_16x16x32_bf16 v[128:131], v[64:67], v[20:23], v[128:131]\n\t" \
  "v_cndmask_b32_e64 v144, v38, v37, vcc\n\t" \
  "v_mov_b32 v155, v154\n\t" \
  "v_mfma_f32_16x16x32_bf16 v[132:135], v[76:79], v[16:19], v[132:135]\n\t" \
  "ds_bpermute_b32 v41, v32, v159\n\t" \
  "v_mov_b32 v154, v153\n\t" \
  "v_mfma_f32_16x16x32_bf16 v[132:135], v[72:75], v[20:23], v[132:135]\n\t" \
  "v_mov_b32 v153, v152\n\t" \
  "v_mov_b32 v159, v158\n\t" \
  "v_mfma_f32_16x16x32_bf16 v[136:139], v[84:87], v[16:19], v[136:139]\n\t" \
  "v_mov_b32 v158, v157\n\t" \
  "v_mov_b32 v157, v156\n\t" \
  "v_mfma_f32_16x16x32_bf16 v[136:139], v[80:83], v[20:23], v[136:139]\n\t" \
  "s_waitcnt lgkmcnt(6)\n\t" \
  "v_cndmask_b32_e64 v148, v39, v38, vcc\n\t" \
  "v_mfma_f32_16x16x32_bf16 v[140:143], v[92:95], v[16:19], v[140:143]\n\t" \
  "ds_read_b64 v[8:9], v26 offset:4096\n\t" \
  "v_mfma_f32_16x16x32_bf16 v[140:143], v[88:91], v[20:23], v[140:143]\n\t" \
  "ds_read_b64 v[10:11], v27 offset:4096\n\t" \
  "v_mfma_f32_16x16x32_bf16 v[144:147], v[100:103], v[16:19], v[144:147]\n\t" \
  "s_waitcnt lgkmcnt(5)\n\t" \
  "v_cndmask_b32_e64 v152, v40, v39, vcc\n\t" \
  "v_mfma_f32_16x16x32_bf16 v[144:147], v[96:99], v[20:23], v[144:147]\n\t" \
  "global_load_dwordx4 v[44:47], v43, %[q0] offset:0\n\t" \
  "v_mfma_f32_16x16x32_bf16 v[148:151], v[108:111], v[16:19], v[148:151]\n\t" \
  "s_waitcnt lgkmcnt(2)\n\t" \
  "v_cndmask_b32_e64 v156, v41, v40, vcc\n\t" \
  "v_mfma_f32_16x16x32_bf16 v[148:151], v[104:107], v[20:23], v[148:151]\n\t" \
  "ds_read_b64 v[12:13], v28 offset:5120\n\t" \
  "v_mfma_f32_16x16x32_bf16 v[152:155], v[116:119], v[16:19], v[152:155]\n\t" \
  "ds_read_b64 v[14:15], v29 offset:5120\n\t" \
  "v_mfma_f32_16x16x32_bf16 v[152:155], v[112:115], v[20:23], v[152:155]\n\t" \
  "global_load_dwordx4 v[48:51], v43, %[q0] offset:2048\n\t" \
  "v_mfma_f32_16x16x32_bf16 v[156:159], v[124:127], v[16:19], v[156:159]\n\t" \
  "ds_read_b64 v[16:17], v30 offset:6144\n\t" \
  "v_mfma_f32_16x16x32_bf16 v[156:159], v[120:123], v[20:23], v[156:159]\n\t" \
  "ds_read_b64 v[18:19], v31 offset:6144\n\t" \
  "v_mfma_f32_16x16x32_bf16 v[132:135], v[68:71], v[0:3], v[132:135]\n\t" \
  "global_load_dwordx4 v[52:55], v43, %[q2] offset:0\n\t" \
  "v_mfma_f32_16x16x32_bf16 v[132:135], v[64:67], v[4:7], v[132:135]\n\t" \
  "ds_read_b64 v[20:21], v24 offset:7168\n\t" \
  "v_mfma_f32_16x16x32_bf16 v[136:139], v[76:79], v[0:3], v[136:139]\n\t" \
  "ds_read_b64 v[22:23], v25 offset:7168\n\t" \
  "v_mfma_f32_16x16x32_bf16 v[136:139], v[72:75], v[4:7], v[136:139]\n\t" \
  "ds_bpermute_b32 v34, v32, v131\n\t" \
  "v_mfma_f32_16x16x32_bf16 v[140:143], v[84:87], v[0:3], v[140:143]\n\t" \
  "global_load_dwordx4 v[56:59], v43, %[q2] offset:2048\n\t" \
  "v_mfma_f32_16x16x32_bf16 v[140:143], v[80:83], v[4:7], v[140:143]\n\t" \
  "ds_bpermute_b32 v35, v32, v135\n\t" \
  "v_mov_b32 v131, v130\n\t" \
  "v_mfma_f32_16x16x32_bf16 v[144:147], v[92:95], v[0:3], v[144:147]\n\t" \
  "v_mov_b32 v130, v129\n\t" \
  "v_mov_b32 v129, v128\n\t" \
  "v_mfma_f32_16x16x32_bf16 v[144:147], v[88:91], v[4:7], v[144:147]\n\t" \
  "v_mov_b32 v135, v134\n\t" \
  "v_mov_b32 v134, v133\n\t" \
  "v_mfma_f32_16x16x32_bf16 v[148:151], v[100:103], v[0:3], v[148:151]\n\t" \
  "v_mov_b32 v133, v132\n\t" \
  "v_mfma_f32_16x16x32_bf16 v[148:151], v[96:99], v[4:7], v[148:151]\n\t" \
  "v_mfma_f32_16x16x32_bf16 v[152:155], v[108:111], v[0:3], v[152:155]\n\t" \
  "v_mfma_f32_16x16x32_bf16 v[152:155], v[104:107], v[4:7], v[152:155]\n\t" \
  "v_mfma_f32_16x16x32_bf16 v[156:159], v[116:119], v[0:3], v[156:159]\n\t" \
  "ds_read_b64 v[0:1], v26 offset:8192\n\t" \
  "v_mfma_f32_16x16x32_bf16 v[156:159], v[112:115], v[4:7], v[156:159]\n\t" \
  "ds_read_b64 v[2:3], v27 offset:8192\n\t" \
  "s_waitcnt lgkmcnt(4)\n\t" \
  "v_mfma_f32_16x16x32_bf16 v[136:139], v[68:71], v[8:11], v[136:139]\n\t" \
  "ds_read_b64 v[4:5], v28 offset:9216\n\t" \
  "v_mfma_f32_16x16x32_bf16 v[136:139], v[64:67], v[12:15], v[136:139]\n\t" \
  "ds_read_b64 v[6:7], v29 offset:9216\n\t" \
  "v_mfma_f32_16x16x32_bf16 v[140:143], v[76:79], v[8:11], v[140:143]\n\t" \
  "s_waitcnt lgkmcnt(5)\n\t" \
  "v_cndmask_b32_e64 v128, v34, 0, vcc\n\t" \
  "v_mfma_f32_16x16x32_bf16 v[140:143], v[72:75], v[12:15], v[140:143]\n\t" \
  "s_waitcnt lgkmcnt(4)\n\t" \
  "v_cndmask_b32_e64 v132, v35, v34, vcc\n\t" \
  "v_mfma_f32_16x16x32_bf16 v[144:147], v[84:87], v[8:11], v[144:147]\n\t" \
  "ds_bpermute_b32 v36, v32, v139\n\t" \
  "v_mfma_f32_16x16x32_bf16 v[144:147], v[80:83], v[12:15], v[144:147]\n\t" \
  "v_mov_b32 v139, v138\n\t" \
  "v_mov_b32 v138, v137\n\t" \
  "v_mfma_f32_16x16x32_bf16 v[148:151], v[92:95], v[8:11], v[148:151]\n\t" \
  "v_mov_b32 v137, v136\n\t" \
  "v_mfma_f32_16x16x32_bf16 v[148:151], v[88:91], v[12:15], v[148:151]\n\t" \
  "v_mfma_f32_16x16x32_bf16 v[152:155], v[100:103], v[8:11], v[152:155]\n\t" \
  "v_mfma_f32_16x16x32_bf16 v[152:155], v[96:99], v[12:15], v[152:155]\n\t" \
  "v_mfma_f32_16x16x32_bf16 v[156:159], v[108:111], v[8:11], v[156:159]\n\t" \
  "ds_read_b64 v[8:9], v30 offset:10240\n\t" \
  "v_mfma_f32_16x16x32_bf16 v[156:159], v[104:107], v[12:15], v[156:159]\n\t" \
  "ds_read_b64 v[10:11], v31 offset:10240\n\t" \
  "v_mfma_f32_16x16x32_bf16 v[140:143], v[68:71], v[16:19], v[140:143]\n\t" \
  "ds_read_b64 v[12:13], v24 offset:11264\n\t" \
  "v_mfma_f32_16x16x32_bf16 v[140:143], v[64:67], v[20:23], v[140:143]\n\t" \
  "ds_read_b64 v[14:15], v25 offset:11264\n\t" \
  "v_mfma_f32_16x16x32_bf16 v[144:147], v[76:79], v[16:19], v[144:147]\n\t" \
  "v_mfma_f32_16x16x32_bf16 v[144:147], v[72:75], v[20:23], v[144:147]\n\t" \
  "s_waitcnt lgkmcnt(4)\n\t" \
  "v_cndmask_b32_e64 v136, v36, v35, vcc\n\t" \
  "v_mfma_f32_16x16x32_bf16 v[148:151], v[84:87], v[16:19], v[148:151]\n\t" \
  "v_mfma_f32_16x16x32_bf16 v[148:151], v[80:83], v[20:23], v[148:151]\n\t" \
  "v_mfma_f32_16x16x32_bf16 v[152:155], v[92:95], v[16:19], v[152:155]\n\t" \
  "ds_bpermute_b32 v37, v32, v143\n\t" \
  "v_mfma_f32_16x16x32_bf16 v[152:155], v[88:91], v[20:23], v[152:155]\n\t" \
  "v_mov_b32 v143, v142\n\t" \
  "v_mov_b32 v142, v141\n\t" \
  "v_mfma_f32_16x16x32_bf16 v[156:159], v[100:103], v[16:19], v[156:159]\n\t" \
  "ds_read_b64 v[16:17], v26 offset:12288\n\t" \
  "v_mov_b32 v141, v140\n\t" \
  "v_mfma_f32_16x16x32_bf16 v[156:159], v[96:99], v[20:23], v[156:159]\n\t" \
  "ds_read_b64 v[18:19], v27 offset:12288\n\t" \
  "v_mfma_f32_16x16x32_bf16 v[144:147], v[68:71], v[0:3], v[144:147]\n\t" \
  "ds_read_b64 v[20:21], v28 offset:13312\n\t" \
  "v_mfma_f32_16x16x32_bf16 v[144:147], v[64:67], v[4:7], v[144:147]\n\t" \
  "ds_read_b64 v[22:23], v29 offset:13312\n\t" \
  "v_mfma_f32_16x16x32_bf16 v[148:151], v[76:79], v[0:3], v[148:151]\n\t" \
  "v_mfma_f32_16x16x32_bf16 v[148:151], v[72:75], v[4:7], v[148:151]\n\t" \
  "v_mfma_f32_16x16x32_bf16 v[152:155], v[84:87], v[0:3], v[152:155]\n\t" \
  "v_mfma_f32_16x16x32_bf16 v[152:155], v[80:83], v[4:7], v[152:155]\n\t" \
  "v_mfma_f32_16x16x32_bf16 v[156:159], v[92:95], v[0:3], v[156:159]\n\t" \
  "ds_read_b64 v[0:1], v30 offset:14336\n\t" \
  "v_mfma_f32_16x16x32_bf16 v[156:159], v[88:91], v[4:7], v[156:159]\n\t" \
  "ds_read_b64 v[2:3], v31 offset:14336\n\t" \
  "s_waitcnt lgkmcnt(6)\n\t" \
  "v_mfma_f32_16x16x32_bf16 v[148:151], v[68:71], v[8:11], v[148:151]\n\t" \
  "ds_read_b64 v[4:5], v24 offset:15360\n\t" \
  "v_cndmask_b32_e64 v140, v37, v36, vcc\n\t" \
  "v_mfma_f32_16x16x32_bf16 v[148:151], v[64:67], v[12:15], v[148:151]\n\t" \
  "ds_read_b64 v[6:7], v25 offset:15360\n\t" \
  "v_mfma_f32_16x16x32_bf16 v[152:155], v[76:79], v[8:11], v[152:155]\n\t" \
  "ds_bpermute_b32 v38, v32, v147\n\t" \
  "v_mfma_f32_16x16x32_bf16 v[152:155], v[72:75], v[12:15], v[152:155]\n\t" \
  "v_mov_b32 v147, v146\n\t" \
  "v_mov_b32 v146, v145\n\t" \
  "v_mfma_f32_16x16x32_bf16 v[156:159], v[84:87], v[8:11], v[156:159]\n\t" \
  "ds_read_b64 v[8:9], v24 offset:0\n\t" \
  "ds_read_b64 v[10:11], v25 offset:0\n\t" \
  "v_mfma_f32_16x16x32_bf16 v[156:159], v[80:83], v[12:15], v[156:159]\n\t" \
  "ds_read_b64 v[12:13], v26 offset:1024\n\t" \
  "ds_read_b64 v[14:15], v27 offset:1024\n\t" \
  "s_waitcnt lgkmcnt(9)\n\t" \
  "v_mfma_f32_16x16x32_bf16 v[152:155], v[68:71], v[16:19], v[152:155]\n\t" \
  "ds_bpermute_b32 v39, v32, v151\n\t" \
  "v_mov_b32 v145, v144\n\t" \
  "v_mfma_f32_16x16x32_bf16 v[152:155], v[64:67], v[20:23], v[152:155]\n\t" \
  "v_mov_b32 v151, v150\n\t" \
  "v_mov_b32 v150, v149\n\t" \
  "v_mfma_f32_16x16x32_bf16 v[156:159], v[76:79], v[16:19], v[156:159]\n\t" \
  "ds_read_b64 v[16:17], v28 offset:2048\n\t" \
  "v_mov_b32 v149, v148\n\t" \
  "v_mfma_f32_16x16x32_bf16 v[156:159], v[72:75], v[20:23], v[156:159]\n\t" \
  "ds_read_b64 v[18:19], v29 offset:2048\n\t" \
  "s_waitcnt lgkmcnt(10)\n\t" \
  "v_mfma_f32_16x16x32_bf16 v[156:159], v[68:71], v[0:3], v[156:159]\n\t" \
  "ds_bpermute_b32 v40, v32, v155\n\t" \
  "s_waitcnt lgkmcnt(9)\n\t" \
  "v_mfma_f32_16x16x32_bf16 v[156:159], v[64:67], v[4:7], v[156:159]\n\t" \
  "ds_read_b64 v[20:21], v30 offset:3072\n\t" \
  "s_waitcnt lgkmcnt(7)\n\t" \
  "v_mfma_f32_16x16x32_bf16 v[128:131], v[68:71], v[8:11], v[128:131]\n\t" \
  "ds_read_b64 v[22:23], v31 offset:3072\n\t" \
  "s_waitcnt lgkmcnt(6)\n\t" \
  "v_mfma_f32_16x16x32_bf16 v[128:131], v[64:67], v[12:15], v[128:131]\n\t" \
  "v_cndmask_b32_e64 v144, v38, v37, vcc\n\t" \
  "v_mov_b32 v155, v154\n\t" \
  "v_mfma_f32_16x16x32_bf16 v[132:135], v[76:79], v[8:11], v[132:135]\n\t" \
  "ds_bpermute_b32 v41, v32, v159\n\t" \
  "v_mov_b32 v154, v153\n\t" \
  "v_mfma_f32_16x16x32_bf16 v[132:135], v[72:75], v[12:15], v[132:135]\n\t" \
  "v_mov_b32 v153, v152\n\t" \
  "v_mov_b32 v159, v158\n\t" \
  "v_mfma_f32_16x16x32_bf16 v[136:139], v[84:87], v[8:11], v[136:139]\n\t" \
  "v_mov_b32 v158, v157\n\t" \
  "v_mov_b32 v157, v156\n\t" \
  "v_mfma_f32_16x16x32_bf16 v[136:139], v[80:83], v[12:15], v[136:139]\n\t" \
  "s_waitcnt lgkmcnt(6)\n\t" \
  "v_cndmask_b32_e64 v148, v39, v38, vcc\n\t" \
  "v_mfma_f32_16x16x32_bf16 v[140:143], v[92:95], v[8:11], v[140:143]\n\t" \
  "ds_read_b64 v[0:1], v24 offset:4096\n\t" \
  "v_mfma_f32_16x16x32_bf16 v[140:143], v[88:91], v[12:15], v[140:143]\n\t" \
  "ds_read_b64 v[2:3], v25 offset:4096\n\t" \
  "v_mfma_f32_16x16x32_bf16 v[144:147], v[100:103], v[8:11], v[144:147]\n\t" \
  "s_waitcnt lgkmcnt(5)\n\t" \
  "v_cndmask_b32_e64 v152, v40, v39, vcc\n\t" \
  "v_mfma_f32_16x16x32_bf16 v[144:147], v[96:99], v[12:15], v[144:147]\n\t" \
  "ds_read_b64 v[4:5], v26 offset:5120\n\t" \
  "v_mfma_f32_16x16x32_bf16 v[148:151], v[108:111], v[8:11], v[148:151]\n\t" \
  "s_waitcnt lgkmcnt(3)\n\t" \
  "v_cndmask_b32_e64 v156, v41, v40, vcc\n\t" \
  "v_mfma_f32_16x16x32_bf16 v[148:151], v[104:107], v[12:15], v[148:151]\n\t" \
  "ds_read_b64 v[6:7], v27 offset:5120\n\t" \
  "v_mfma_f32_16x16x32_bf16 v[152:155], v[116:119], v[8:11], v[152:155]\n\t" \
  "ds_bpermute_b32 v34, v32, v131\n\t" \
  "v_mfma_f32_16x16x32_bf16 v[152:155], v[112:115], v[12:15], v[152:155]\n\t" \
  "v_mov_b32 v131, v130\n\t" \
  "v_mov_b32 v130, v129\n\t" \
  "v_mfma_f32_16x16x32_bf16 v[156:159], v[124:127], v[8:11], v[156:159]\n\t" \
  "ds_read_b64 v[8:9], v28 offset:6144\n\t" \
  "v_mov_b32 v129, v128\n\t" \
  "v_mfma_f32_16x16x32_bf16 v[156:159], v[120:123], v[12:15], v[156:159]\n\t" \
  "ds_read_b64 v[10:11], v29 offset:6144\n\t" \
  "v_mfma_f32_16x16x32_bf16 v[132:135], v[68:71], v[16:19], v[132:135]\n\t" \
  "ds_read_b64 v[12:13], v30 offset:7168\n\t" \
  "v_mfma_f32_16x16x32_bf16 v[132:135], v[64:67], v[20:23], v[132:135]\n\t" \
  "ds_read_b64 v[14:15], v31 offset:7168\n\t" \
  "v_mfma_f32_16x16x32_bf16 v[136:139], v[76:79], v[16:19], v[136:139]\n\t" \
  "v_mfma_f32_16x16x32_bf16 v[136:139], v[72:75], v[20:23], v[136:139]\n\t" \
  "v_mfma_f32_16x16x32_bf16 v[140:143], v[84:87], v[16:19], v[140:143]\n\t" \
  "v_mfma_f32_16x16x32_bf16 v[140:143], v[80:83], v[20:23], v[140:143]\n\t" \
  "v_mfma_f32_16x16x32_bf16 v[144:147], v[92:95], v[16:19], v[144:147]\n\t" \
  "v_mfma_f32_16x16x32_bf16 v[144:147], v[88:91], v[20:23], v[144:147]\n\t" \
  "s_waitcnt lgkmcnt(4)\n\t" \
  "v_cndmask_b32_e64 v128, v34, 0, vcc\n\t" \
  "v_mfma_f32_16x16x32_bf16 v[148:151], v[100:103], v[16:19], v[148:151]\n\t" \
  "ds_bpermute_b32 v35, v32, v135\n\t" \
  "v_mfma_f32_16x16x32_bf16 v[148:151], v[96:99], v[20:23], v[148:151]\n\t" \
  "v_mov_b32 v135, v134\n\t" \
  "v_mov_b32 v134, v133\n\t" \
  "v_mfma_f32_16x16x32_bf16 v[152:155], v[108:111], v[16:19], v[152:155]\n\t" \
  "v_mov_b32 v133, v132\n\t" \
  "v_mfma_f32_16x16x32_bf16 v[152:155], v[104:107], v[20:23], v[152:155]\n\t" \
  "v_mfma_f32_16x16x32_bf16 v[156:159], v[116:119], v[16:19], v[156:159]\n\t" \
  "ds_read_b64 v[16:17], v24 offset:8192\n\t" \
  "v_mfma_f32_16x16x32_bf16 v[156:159], v[112:115], v[20:23], v[156:159]\n\t" \
  "ds_read_b64 v[18:19], v25 offset:8192\n\t" \
  "v_mfma_f32_16x16x32_bf16 v[136:139], v[68:71], v[0:3], v[136:139]\n\t" \
  "ds_read_b64 v[20:21], v26 offset:9216\n\t" \
  "v_mfma_f32_16x16x32_bf16 v[136:139], v[64:67], v[4:7], v[136:139]\n\t" \
  "ds_read_b64 v[22:23], v27 offset:9216\n\t" \
  "v_mfma_f32_16x16x32_bf16 v[140:143], v[76:79], v[0:3], v[140:143]\n\t" \
  "v_mfma_f32_16x16x32_bf16 v[140:143], v[72:75], v[4:7], v[140:143]\n\t" \
  "v_mfma_f32_16x16x32_bf16 v[144:147], v[84:87], v[0:3], v[144:147]\n\t" \
  "v_mfma_f32_16x16x32_bf16 v[144:147], v[80:83], v[4:7], v[144:147]\n\t" \
  "s_waitcnt lgkmcnt(4)\n\t" \
  "v_cndmask_b32_e64 v132, v35, v34, vcc\n\t" \
  "v_mfma_f32_16x16x32_bf16 v[148:151], v[92:95], v[0:3], v[148:151]\n\t" \
  "ds_bpermute_b32 v36, v32, v139\n\t" \
  "v_mfma_f32_16x16x32_bf16 v[148:151], v[88:91], v[4:7], v[148:151]\n\t" \
  "v_mov_b32 v139, v138\n\t" \
  "v_mov_b32 v138, v137\n\t" \
  "v_mfma_f32_16x16x32_bf16 v[152:155], v[100:103], v[0:3], v[152:155]\n\t" \
  "v_mov_b32 v137, v136\n\t" \
  "v_mfma_f32_16x16x32_bf16 v[152:155], v[96:99], v[4:7], v[152:155]\n\t" \
  "v_mfma_f32_16x16x32_bf16 v[156:159], v[108:111], v[0:3], v[156:159]\n\t" \
  "ds_read_b64 v[0:1], v28 offset:10240\n\t" \
  "v_mfma_f32_16x16x32_bf16 v[156:159], v[104:107], v[4:7], v[156:159]\n\t" \
  "ds_read_b64 v[2:3], v29 offset:10240\n\t" \
  "v_mfma_f32_16x16x32_bf16 v[140:143], v[68:71], v[8:11], v[140:143]\n\t" \
  "ds_read_b64 v[4:5], v30 offset:11264\n\t" \
  "v_mfma_f32_16x16x32_bf16 v[140:143], v[64:67], v[12:15], v[140:143]\n\t" \
  "ds_read_b64 v[6:7], v31 offset:11264\n\t" \
  "v_mfma_f32_16x16x32_bf16 v[144:147], v[76:79], v[8:11], v[144:147]\n\t" \
  "v_mfma_f32_16x16x32_bf16 v[144:147], v[72:75], v[12:15], v[144:147]\n\t" \
  "v_mfma_f32_16x16x32_bf16 v[148:151], v[84:87], v[8:11], v[148:151]\n\t" \
  "v_mfma_f32_16x16x32_bf16 v[148:151], v[80:83], v[12:15], v[148:151]\n\t" \
  "s_waitcnt lgkmcnt(4)\n\t" \
  "v_cndmask_b32_e64 v136, v36, v35, vcc\n\t" \
  "v_mfma_f32_16x16x32_bf16 v[152:155], v[92:95], v[8:11], v[152:155]\n\t" \
  "ds_bpermute_b32 v37, v32, v143\n\t" \
  "v_mfma_f32_16x16x32_bf16 v[152:155], v[88:91], v[12:15], v[152:155]\n\t" \
  "v_mov_b32 v143, v142\n\t" \
  "v_mov_b32 v142, v141\n\t" \
  "v_mfma_f32_16x16x32_bf16 v[156:159], v[100:103], v[8:11], v[156:159]\n\t" \
  "ds_read_b64 v[8:9], v24 offset:12288\n\t" \
  "v_mov_b32 v141, v140\n\t" \
  "v_mfma_f32_16x16x32_bf16 v[156:159], v[96:99], v[12:15], v[156:159]\n\t" \
  "ds_read_b64 v[10:11], v25 offset:12288\n\t" \
  "v_mfma_f32_16x16x32_bf16 v[144:147], v[68:71], v[16:19], v[144:147]\n\t" \
  "ds_read_b64 v[12:13], v26 offset:13312\n\t" \
  "v_mfma_f32_16x16x32_bf16 v[144:147], v[64:67], v[20:23], v[144:147]\n\t" \
  "ds_read_b64 v[14:15], v27 offset:13312\n\t" \
  "v_mfma_f32_16x16x32_bf16 v[148:151], v[76:79], v[16:19], v[148:151]\n\t" \
  "v_mfma_f32_16x16x32_bf16 v[148:151], v[72:75], v[20:23], v[148:151]\n\t" \
  "v_mfma_f32_16x16x32_bf16 v[152:155], v[84:87], v[16:19], v[152:155]\n\t" \
  "v_mfma_f32_16x16x32_bf16 v[152:155], v[80:83], v[20:23], v[152:155]\n\t" \
  "v_mfma_f32_16x16x32_bf16 v[156:159], v[92:95], v[16:19], v[156:159]\n\t" \
  "ds_read_b64 v[16:17], v28 offset:14336\n\t" \
  "v_mfma_f32_16x16x32_bf16 v[156:159], v[88:91], v[20:23], v[156:159]\n\t" \
  "ds_read_b64 v[18:19], v29 offset:14336\n\t" \
  "s_waitcnt lgkmcnt(6)\n\t" \
  "v_mfma_f32_16x16x32_bf16 v[148:151], v[68:71], v[0:3], v[148:151]\n\t" \
  "ds_read_b64 v[20:21], v30 offset:15360\n\t" \
  "v_cndmask_b32_e64 v140, v37, v36, vcc\n\t" \
  "v_mfma_f32_16x16x32_bf16 v[148:151], v[64:67], v[4:7], v[148:151]\n\t" \
  "ds_read_b64 v[22:23], v31 offset:15360\n\t" \
  "v_mfma_f32_16x16x32_bf16 v[152:155], v[76:79], v[0:3], v[152:155]\n\t" \
  "ds_bpermute_b32 v38, v32, v147\n\t" \
  "v_mfma_f32_16x16x32_bf16 v[152:155], v[72:75], v[4:7], v[152:155]\n\t" \
  "v_mov_b32 v147, v146\n\t" \
  "v_mov_b32 v146, v145\n\t" \
  "v_mfma_f32_16x16x32_bf16 v[156:159], v[84:87], v[0:3], v[156:159]\n\t" \
  "ds_read_b64 v[0:1], v30 offset:0\n\t" \
  "ds_read_b64 v[2:3], v31 offset:0\n\t" \
  "v_mfma_f32_16x16x32_bf16 v[156:159], v[80:83], v[4:7], v[156:159]\n\t" \
  "ds_read_b64 v[4:5], v24 offset:1024\n\t" \
  "ds_read_b64 v[6:7], v25 offset:1024\n\t" \
  "s_waitcnt lgkmcnt(9)\n\t" \
  "v_mfma_f32_16x16x32_bf16 v[152:155], v[68:71], v[8:11], v[152:155]\n\t" \
  "ds_bpermute_b32 v39, v32, v151\n\t" \
  "v_mov_b32 v145, v144\n\t" \
  "v_mfma_f32_16x16x32_bf16 v[152:155], v[64:67], v[12:15], v[152:155]\n\t" \
  "v_mov_b32 v151, v150\n\t" \
  "v_mov_b32 v150, v149\n\t" \
  "v_mfma_f32_16x16x32_bf16 v[156:159], v[76:79], v[8:11], v[156:159]\n\t" \
  "ds_read_b64 v[8:9], v26 offset:2048\n\t" \
  "v_mov_b32 v149, v148\n\t" \
  "v_mfma_f32_16x16x32_bf16 v[156:159], v[72:75], v[12:15], v[156:159]\n\t" \
  "ds_read_b64 v[10:11], v27 offset:2048\n\t" \
  "s_waitcnt lgkmcnt(10)\n\t" \
  "v_mfma_f32_16x16x32_bf16 v[156:159], v[68:71], v[16:19], v[156:159]\n\t" \
  "ds_bpermute_b32 v40, v32, v155\n\t" \
  "s_waitcnt lgkmcnt(9)\n\t" \
  "v_mfma_f32_16x16x32_bf16 v[156:159], v[64:67], v[20:23], v[156:159]\n\t" \
  "ds_read_b64 v[12:13], v28 offset:3072\n\t" \
  "s_waitcnt lgkmcnt(7)\n\t" \
  "v_mfma_f32_16x16x32_bf16 v[128:131], v[68:71], v[0:3], v[128:131]\n\t" \
  "ds_read_b64 v[14:15], v29 offset:3072\n\t" \
  "s_waitcnt lgkmcnt(6)\n\t" \
  "v_mfma_f32_16x16x32_bf16 v[128:131], v[64:67], v[4:7], v[128:131]\n\t" \
  "v_cndmask_b32_e64 v144, v38, v37, vcc\n\t" \
  "v_mov_b32 v155, v154\n\t" \
  "v_mfma_f32_16x16x32_bf16 v[132:135], v[76:79], v[0:3], v[132:135]\n\t" \
  "ds_bpermute_b32 v41, v32, v159\n\t" \
  "v_mov_b32 v154, v153\n\t" \
  "v_mfma_f32_16x16x32_bf16 v[132:135], v[72:75], v[4:7], v[132:135]\n\t" \
  "v_mov_b32 v153, v152\n\t" \
  "v_mov_b32 v159, v158\n\t" \
  "v_mfma_f32_16x16x32_bf16 v[136:139], v[84:87], v[0:3], v[136:139]\n\t" \
  "v_mov_b32 v158, v157\n\t" \
  "v_mov_b32 v157, v156\n\t" \
  "v_mfma_f32_16x16x32_bf16 v[136:139], v[80:83], v[4:7], v[136:139]\n\t" \
  "s_waitcnt lgkmcnt(6)\n\t" \
  "v_cndmask_b32_e64 v148, v39, v38, vcc\n\t" \
  "v_mfma_f32_16x16x32_bf16 v[140:143], v[92:95], v[0:3], v[140:143]\n\t" \
  "ds_read_b64 v[16:17], v30 offset:4096\n\t" \
  "v_mfma_f32_16x16x32_bf16 v[140:143], v[88:91], v[4:7], v[140:143]\n\t" \
  "ds_read_b64 v[18:19], v31 offset:4096\n\t" \
  "v_mfma_f32_16x16x32_bf16 v[144:147], v[100:103], v[0:3], v[144:147]\n\t" \
  "s_waitcnt lgkmcnt(5)\n\t" \
  "v_cndmask_b32_e64 v152, v40, v39, vcc\n\t" \
  "v_mfma_f32_16x16x32_bf16 v[144:147], v[96:99], v[4:7], v[144:147]\n\t" \
  "ds_read_b64 v[20:21], v24 offset:5120\n\t" \
  "v_mfma_f32_16x16x32_bf16 v[148:151], v[108:111], v[0:3], v[148:151]\n\t" \
  "s_waitcnt lgkmcnt(3)\n\t" \
  "v_cndmask_b32_e64 v156, v41, v40, vcc\n\t" \
  "v_mfma_f32_16x16x32_bf16 v[148:151], v[104:107], v[4:7], v[148:151]\n\t" \
  "v_cvt_pk_bf16_f32 v34, v128, v129\n\t" \
  "v_cvt_pk_bf16_f32 v35, v130, v131\n\t" \
  "v_mfma_f32_16x16x32_bf16 v[152:155], v[116:119], v[0:3], v[152:155]\n\t" \
  "ds_read_b64 v[22:23], v25 offset:5120\n\t" \
  "v_mfma_f32_16x16x32_bf16 v[152:155], v[112:115], v[4:7], v[152:155]\n\t" \
  "v_mfma_f32_16x16x32_bf16 v[156:159], v[124:127], v[0:3], v[156:159]\n\t" \
  "ds_read_b64 v[0:1], v26 offset:6144\n\t" \
  "v_mfma_f32_16x16x32_bf16 v[156:159], v[120:123], v[4:7], v[156:159]\n\t" \
  "ds_read_b64 v[2:3], v27 offset:6144\n\t" \
  "v_mfma_f32_16x16x32_bf16 v[132:135], v[68:71], v[8:11], v[132:135]\n\t" \
  "ds_read_b64 v[4:5], v28 offset:7168\n\t" \
  "v_mfma_f32_16x16x32_bf16 v[132:135], v[64:67], v[12:15], v[132:135]\n\t" \
  "ds_read_b64 v[6:7], v29 offset:7168\n\t" \
  "v_mfma_f32_16x16x32_bf16 v[136:139], v[76:79], v[8:11], v[136:139]\n\t" \
  "v_mfma_f32_16x16x32_bf16 v[136:139], v[72:75], v[12:15], v[136:139]\n\t" \
  "v_mfma_f32_16x16x32_bf16 v[140:143], v[84:87], v[8:11], v[140:143]\n\t" \
  "v_mfma_f32_16x16x32_bf16 v[140:143], v[80:83], v[12:15], v[140:143]\n\t" \
  "v_mfma_f32_16x16x32_bf16 v[144:147], v[92:95], v[8:11], v[144:147]\n\t" \
  "v_mfma_f32_16x16x32_bf16 v[144:147], v[88:91], v[12:15], v[144:147]\n\t" \
  "v_mfma_f32_16x16x32_bf16 v[148:151], v[100:103], v[8:11], v[148:151]\n\t" \
  "v_cvt_pk_bf16_f32 v36, v132, v133\n\t" \
  "v_cvt_pk_bf16_f32 v37, v134, v135\n\t" \
  "v_mfma_f32_16x16x32_bf16 v[148:151], v[96:99], v[12:15], v[148:151]\n\t" \
  "v_mfma_f32_16x16x32_bf16 v[152:155], v[108:111], v[8:11], v[152:155]\n\t" \
  "v_permlane16_swap_b32 v34, v36\n\t" \
  "v_permlane16_swap_b32 v35, v37\n\t" \
  "v_mfma_f32_16x16x32_bf16 v[152:155], v[104:107], v[12:15], v[152:155]\n\t" \
  "global_store_dwordx4 v42, v[34:37], %[yb] offset:0 sc1\n\t" \
  "v_mfma_f32_16x16x32_bf16 v[156:159], v[116:119], v[8:11], v[156:159]\n\t" \
  "ds_read_b64 v[8:9], v30 offset:8192\n\t" \
  "v_mfma_f32_16x16x32_bf16 v[156:159], v[112:115], v[12:15], v[156:159]\n\t" \
  "ds_read_b64 v[10:11], v31 offset:8192\n\t" \
  "s_waitcnt lgkmcnt(2)\n\t" \
  "v_mfma_f32_16x16x32_bf16 v[136:139], v[68:71], v[16:19], v[136:139]\n\t" \
  "ds_read_b64 v[12:13], v24 offset:9216\n\t" \
  "v_mfma_f32_16x16x32_bf16 v[136:139], v[64:67], v[20:23], v[136:139]\n\t" \
  "ds_read_b64 v[14:15], v25 offset:9216\n\t" \
  "v_mfma_f32_16x16x32_bf16 v[140:143], v[76:79], v[16:19], v[140:143]\n\t" \
  "v_mfma_f32_16x16x32_bf16 v[140:143], v[72:75], v[20:23], v[140:143]\n\t" \
  "v_mfma_f32_16x16x32_bf16 v[144:147], v[84:87], v[16:19], v[144:147]\n\t" \
  "s_mov_b64 exec, 1\n\t" \
  "ds_write_b32 v33, v33\n\t" \
  "s_mov_b64 exec, -1\n\t" \
  "v_mfma_f32_16x16x32_bf16 v[144:147], v[80:83], v[20:23], v[144:147]\n\t" \
  "v_cvt_pk_bf16_f32 v38, v136, v137\n\t" \
  "v_cvt_pk_bf16_f32 v39, v138, v139\n\t" \
  "v_mfma_f32_16x16x32_bf16 v[148:151], v[92:95], v[16:19], v[148:151]\n\t" \
  "v_mfma_f32_16x16x32_bf16 v[148:151], v[88:91], v[20:23], v[148:151]\n\t" \
  "v_mfma_f32_16x16x32_bf16 v[152:155], v[100:103], v[16:19], v[152:155]\n\t" \
  "v_mfma_f32_16x16x32_bf16 v[152:155], v[96:99], v[20:23], v[152:155]\n\t" \
  "v_mfma_f32_16x16x32_bf16 v[156:159], v[108:111], v[16:19], v[156:159]\n\t" \
  "ds_read_b64 v[16:17], v26 offset:10240\n\t" \
  "v_mfma_f32_16x16x32_bf16 v[156:159], v[104:107], v[20:23], v[156:159]\n\t" \
  "ds_read_b64 v[18:19], v27 offset:10240\n\t" \
  "v_mfma_f32_16x16x32_bf16 v[140:143], v[68:71], v[0:3], v[140:143]\n\t" \
  "ds_read_b64 v[20:21], v28 offset:11264\n\t" \
  "v_mfma_f32_16x16x32_bf16 v[140:143], v[64:67], v[4:7], v[140:143]\n\t" \
  "ds_read_b64 v[22:23], v29 offset:11264\n\t" \
  "v_mfma_f32_16x16x32_bf16 v[144:147], v[76:79], v[0:3], v[144:147]\n\t" \
  "v_mfma_f32_16x16x32_bf16 v[144:147], v[72:75], v[4:7], v[144:147]\n\t" \
  "v_mfma_f32_16x16x32_bf16 v[148:151], v[84:87], v[0:3], v[148:151]\n\t" \
  "v_mfma_f32_16x16x32_bf16 v[148:151], v[80:83], v[4:7], v[148:151]\n\t" \
  "v_mfma_f32_16x16x32_bf16 v[152:155], v[92:95], v[0:3], v[152:155]\n\t" \
  "v_mfma_f32_16x16x32_bf16 v[152:155], v[88:91], v[4:7], v[152:155]\n\t" \
  "v_mfma_f32_16x16x32_bf16 v[156:159], v[100:103], v[0:3], v[156:159]\n\t" \
  "v_cvt_pk_bf16_f32 v40, v140, v141\n\t" \
  "v_cvt_pk_bf16_f32 v41, v142, v143\n\t" \
  "v_mfma_f32_16x16x32_bf16 v[156:159], v[96:99], v[4:7], v[156:159]\n\t" \
  "ds_read_b64 v[0:1], v30 offset:12288\n\t" \
  "s_waitcnt lgkmcnt(6)\n\t" \
  "v_mfma_f32_16x16x32_bf16 v[144:147], v[68:71], v[8:11], v[144:147]\n\t" \
  "v_permlane16_swap_b32 v38, v40\n\t" \
  "ds_read_b64 v[2:3], v31 offset:12288\n\t" \
  "v_mfma_f32_16x16x32_bf16 v[144:147], v[64:67], v[12:15], v[144:147]\n\t" \
  "v_permlane16_swap_b32 v39, v41\n\t" \
  "ds_read_b64 v[4:5], v24 offset:13312\n\t" \
  "v_mfma_f32_16x16x32_bf16 v[148:151], v[76:79], v[8:11], v[148:151]\n\t" \
  "ds_read_b64 v[6:7], v25 offset:13312\n\t" \
  "v_mfma_f32_16x16x32_bf16 v[148:151], v[72:75], v[12:15], v[148:151]\n\t" \
  "global_store_dwordx4 v42, v[38:41], %[yb] offset:1024 sc1\n\t" \
  "v_mfma_f32_16x16x32_bf16 v[152:155], v[84:87], v[8:11], v[152:155]\n\t" \
  "v_mfma_f32_16x16x32_bf16 v[152:155], v[80:83], v[12:15], v[152:155]\n\t" \
  "v_cvt_pk_bf16_f32 v34, v144, v145\n\t" \
  "v_cvt_pk_bf16_f32 v35, v146, v147\n\t" \
  "v_mfma_f32_16x16x32_bf16 v[156:159], v[92:95], v[8:11], v[156:159]\n\t" \
  "ds_read_b64 v[8:9], v26 offset:14336\n\t" \
  "v_mfma_f32_16x16x32_bf16 v[156:159], v[88:91], v[12:15], v[156:159]\n\t" \
  "ds_read_b64 v[10:11], v27 offset:14336\n\t" \
  "s_waitcnt lgkmcnt(6)\n\t" \
  "v_mfma_f32_16x16x32_bf16 v[148:151], v[68:71], v[16:19], v[148:151]\n\t" \
  "ds_read_b64 v[12:13], v28 offset:15360\n\t" \
  "v_mfma_f32_16x16x32_bf16 v[148:151], v[64:67], v[20:23], v[148:151]\n\t" \
  "ds_read_b64 v[14:15], v29 offset:15360\n\t" \
  "v_mfma_f32_16x16x32_bf16 v[152:155], v[76:79], v[16:19], v[152:155]\n\t" \
  "v_mfma_f32_16x16x32_bf16 v[152:155], v[72:75], v[20:23], v[152:155]\n\t" \
  "v_mfma_f32_16x16x32_bf16 v[156:159], v[84:87], v[16:19], v[156:159]\n\t" \
  "v_mfma_f32_16x16x32_bf16 v[156:159], v[80:83], v[20:23], v[156:159]\n\t" \
  "s_waitcnt lgkmcnt(4)\n\t" \
  "v_mfma_f32_16x16x32_bf16 v[152:155], v[68:71], v[0:3], v[152:155]\n\t" \
  "v_mfma_f32_16x16x32_bf16 v[152:155], v[64:67], v[4:7], v[152:155]\n\t" \
  "v_cvt_pk_bf16_f32 v36, v148, v149\n\t" \
  "v_cvt_pk_bf16_f32 v37, v150, v151\n\t" \
  "v_mfma_f32_16x16x32_bf16 v[156:159], v[76:79], v[0:3], v[156:159]\n\t" \
  "v_mfma_f32_16x16x32_bf16 v[156:159], v[72:75], v[4:7], v[156:159]\n\t" \
  "v_permlane16_swap_b32 v34, v36\n\t" \
  "s_waitcnt lgkmcnt(2)\n\t" \
  "v_mfma_f32_16x16x32_bf16 v[156:159], v[68:71], v[8:11], v[156:159]\n\t" \
  "v_permlane16_swap_b32 v35, v37\n\t" \
  "s_waitcnt lgkmcnt(0)\n\t" \
  "v_mfma_f32_16x16x32_bf16 v[156:159], v[64:67], v[12:15], v[156:159]\n\t" \
  "global_store_dwordx4 v42, v[34:37], %[yb] offset:2048 sc1\n\t" \
  "s_waitcnt lgkmcnt(0)\n\t" \
  "s_nop 4\n\t" \
  "v_cvt_pk_bf16_f32 v38, v152, v153\n\t" \
  "v_cvt_pk_bf16_f32 v39, v154, v155\n\t" \
  "v_cvt_pk_bf16_f32 v40, v156, v157\n\t" \
  "v_cvt_pk_bf16_f32 v41, v158, v159\n\t" \
  "s_nop 1\n\t" \
  "v_permlane16_swap_b32 v38, v40\n\t" \
  "v_permlane16_swap_b32 v39, v41\n\t" \
  "global_store_dwordx4 v42, v[38:41], %[yb] offset:3072 sc1\n\t" \
  "s_nop 1\n\t"
#define CV_Q 1
#define CV_CLOB "vcc", "v0", "v1", "v2", "v3", "v4", "v5", "v6", "v7", "v8", "v9", "v10", "v11", "v12", "v13", "v14", "v15", "v16", "v17", "v18", "v19", "v20", "v21", "v22", "v23", "v34", "v35", "v36", "v37", "v38", "v39"

#define G3_ASM \
  "v_readlane_b32 s64, v96, 0\n\t" \
  "v_readlane_b32 s65, v96, 1\n\t" \
  "v_readlane_b32 s66, v96, 2\n\t" \
  "v_readlane_b32 s67, v96, 3\n\t" \
  "v_readlane_b32 s68, v96, 4\n\t" \
  "v_readlane_b32 s69, v96, 5\n\t" \
  "v_readlane_b32 s70, v96, 6\n\t" \
  "v_readlane_b32 s71, v96, 7\n\t" \
  "v_readlane_b32 s72, v96, 8\n\t" \
  "v_readlane_b32 s73, v96, 9\n\t" \
  "v_readlane_b32 s74, v96, 10\n\t" \
  "v_readlane_b32 s75, v96, 11\n\t" \
  "v_readlane_b32 s76, v96, 12\n\t" \
  "v_readlane_b32 s77, v96, 13\n\t" \
  "v_readlane_b32 s78, v96, 14\n\t" \
  "v_readlane_b32 s79, v96, 15\n\t" \
  "v_add_u32 v97, %[go], v165\n\t" \
  "s_branch QG_%=\n\t" \
  "QA14_%=:\n\t" \
  "v_mov_b32 v165, %[sy]\n\t" \
  "s_sleep 1\n\t" \
  "ds_read_b32 v165, v165\n\t" \
  "s_addk_i32 %[cn], 1\n\t" \
  "s_cmp_gt_u32 %[cn], 0x400000\n\t" \
  "s_cbranch_scc1 QC14_%=\n\t" \
  "s_waitcnt lgkmcnt(0)\n\t" \
  "v_cmp_lt_u32 vcc, 15, v165\n\t" \
  "s_cbranch_vccz QA14_%=\n\t" \
  "QC14_%=:\n\t" \
  "s_waitcnt lgkmcnt(0)\n\t" \
  "s_branch QB14_%=\n\t" \
  "QA7_%=:\n\t" \
  "v_mov_b32 v165, %[sy]\n\t" \
  "s_sleep 1\n\t" \
  "ds_read_b32 v165, v165\n\t" \
  "s_addk_i32 %[cn], 1\n\t" \
  "s_cmp_gt_u32 %[cn], 0x400000\n\t" \
  "s_cbranch_scc1 QC7_%=\n\t" \
  "s_waitcnt lgkmcnt(0)\n\t" \
  "v_cmp_lt_u32 vcc, 13, v165\n\t" \
  "s_cbranch_vccz QA7_%=\n\t" \
  "QC7_%=:\n\t" \
  "s_waitcnt lgkmcnt(0)\n\t" \
  "s_branch QB7_%=\n\t" \
  "QS_%=:\n\t" \
  "s_sleep 1\n\t" \
  "QG_%=:\n\t" \
  "ds_read_b32 v98, v165\n\t" \
  "ds_read_b32 v99, v97\n\t" \
  "s_addk_i32 %[cn], 1\n\t" \
  "s_cmp_gt_u32 %[cn], 0x400000\n\t" \
  "s_cselect_b32 %[t2], 16, 0\n\t" \
  "s_waitcnt lgkmcnt(0)\n\t" \
  "v_readfirstlane_b32 %[t0], v98\n\t" \
  "v_readfirstlane_b32 %[t1], v99\n\t" \
  "s_max_u32 %[t0], %[t0], %[t2]\n\t" \
  "s_or_b32 %[t1], %[t1], %[t2]\n\t" \
  "s_min_u32 %[t2], %[t0], 5\n\t" \
  "s_cmp_ge_u32 %[iss], %[t2]\n\t" \
  "s_cbranch_scc1 QD_%=\n\t" \
  "s_cmp_eq_u32 %[iss], 1\n\t" \
  "s_cbranch_scc1 QL1_%=\n\t" \
  "s_cmp_eq_u32 %[iss], 2\n\t" \
  "s_cbranch_scc1 QL2_%=\n\t" \
  "s_cmp_eq_u32 %[iss], 3\n\t" \
  "s_cbranch_scc1 QL3_%=\n\t" \
  "s_cmp_eq_u32 %[iss], 4\n\t" \
  "s_cbranch_scc1 QL4_%=\n\t" \
  "QL0_%=:\n\t" \
  "buffer_load_dwordx4 v[0:3], v164, %[ob], s64 offen offset:0 sc1\n\t" \
  "buffer_load_dwordx4 v[4:7], v164, %[ob], s64 offen offset:1024 sc1\n\t" \
  "buffer_load_dwordx4 v[8:11], v164, %[ob], s64 offen offset:2048 sc1\n\t" \
  "buffer_load_dwordx4 v[12:15], v164, %[ob], s64 offen offset:3072 sc1\n\t" \
  "s_mov_b32 %[iss], 1\n\t" \
  "s_cmp_le_u32 %[t2], 1\n\t" \
  "s_cbranch_scc1 QD_%=\n\t" \
  "QL1_%=:\n\t" \
  "buffer_load_dwordx4 v[16:19], v164, %[ob], s65 offen offset:0 sc1\n\t" \
  "buffer_load_dwordx4 v[20:23], v164, %[ob], s65 offen offset:1024 sc1\n\t" \
  "buffer_load_dwordx4 v[24:27], v164, %[ob], s65 offen offset:2048 sc1\n\t" \
  "buffer_load_dwordx4 v[28:31], v164, %[ob], s65 offen offset:3072 sc1\n\t" \
  "s_mov_b32 %[iss], 2\n\t" \
  "s_cmp_le_u32 %[t2], 2\n\t" \
  "s_cbranch_scc1 QD_%=\n\t" \
  "QL2_%=:\n\t" \
  "buffer_load_dwordx4 v[32:35], v164, %[ob], s66 offen offset:0 sc1\n\t" \
  "buffer_load_dwordx4 v[36:39], v164, %[ob], s66 offen offset:1024 sc1\n\t" \
  "buffer_load_dwordx4 v[40:43], v164, %[ob], s66 offen offset:2048 sc1\n\t" \
  "buffer_load_dwordx4 v[44:47], v164, %[ob], s66 offen offset:3072 sc1\n\t" \
  "s_mov_b32 %[iss], 3\n\t" \
  "s_cmp_le_u32 %[t2], 3\n\t" \
  "s_cbranch_scc1 QD_%=\n\t" \
  "QL3_%=:\n\t" \
  "buffer_load_dwordx4 v[48:51], v164, %[ob], s67 offen offset:0 sc1\n\t" \
  "buffer_load_dwordx4 v[52:55], v164, %[ob], s67 offen offset:1024 sc1\n\t" \
  "buffer_load_dwordx4 v[56:59], v164, %[ob], s67 offen offset:2048 sc1\n\t" \
  "buffer_load_dwordx4 v[60:63], v164, %[ob], s67 offen offset:3072 sc1\n\t" \
  "s_mov_b32 %[iss], 4\n\t" \
  "s_cmp_le_u32 %[t2], 4\n\t" \
  "s_cbranch_scc1 QD_%=\n\t" \
  "QL4_%=:\n\t" \
  "buffer_load_dwordx4 v[64:67], v164, %[ob], s68 offen offset:0 sc1\n\t" \
  "buffer_load_dwordx4 v[68:71], v164, %[ob], s68 offen offset:1024 sc1\n\t" \
  "buffer_load_dwordx4 v[72:75], v164, %[ob], s68 offen offset:2048 sc1\n\t" \
  "buffer_load_dwordx4 v[76:79], v164, %[ob], s68 offen offset:3072 sc1\n\t" \
  "s_mov_b32 %[iss], 5\n\t" \
  "QD_%=:\n\t" \
  "s_cmp_lt_u32 %[t0], 7\n\t" \
  "s_cbranch_scc1 QS_%=\n\t" \
  "s_cmp_eq_u32 %[t1], 0\n\t" \
  "s_cbranch_scc1 QS_%=\n\t" \
  "s_memrealtime %[st5]\n\t" \
  "s_waitcnt lgkmcnt(0)\n\t" \
  "s_setprio 0\n\t" \
  "ds_read_b64_tr_b16 v[96:97], v160 offset:0\n\t" \
  "ds_read_b64_tr_b16 v[98:99], v160 offset:2048\n\t" \
  "ds_read_b64_tr_b16 v[100:101], v161 offset:0\n\t" \
  "ds_read_b64_tr_b16 v[102:103], v161 offset:2048\n\t" \
  "ds_read_b64_tr_b16 v[104:105], v162 offset:0\n\t" \
  "ds_read_b64_tr_b16 v[106:107], v162 offset:2048\n\t" \
  "ds_read_b64_tr_b16 v[108:109], v163 offset:0\n\t" \
  "ds_read_b64_tr_b16 v[110:111], v163 offset:2048\n\t" \
  "ds_read_b64_tr_b16 v[112:113], v160 offset:4096\n\t" \
  "ds_read_b64_tr_b16 v[114:115], v160 offset:6144\n\t" \
  "ds_read_b64_tr_b16 v[116:117], v161 offset:4096\n\t" \
  "ds_read_b64_tr_b16 v[118:119], v161 offset:6144\n\t" \
  "ds_read_b64_tr_b16 v[120:121], v162 offset:4096\n\t" \
  "ds_read_b64_tr_b16 v[122:123], v162 offset:6144\n\t" \
  "s_waitcnt vmcnt(18) lgkmcnt(10)\n\t" \
  "v_mfma_f32_16x16x32_bf16 v[128:131], v[96:99], v[0:3], 0\n\t" \
  "ds_read_b64_tr_b16 v[124:125], v163 offset:4096\n\t" \
  "v_mfma_f32_16x16x32_bf16 v[144:147], v[96:99], v[4:7], 0\n\t" \
  "ds_read_b64_tr_b16 v[126:127], v163 offset:6144\n\t" \
  "v_mfma_f32_16x16x32_bf16 v[132:135], v[100:103], v[0:3], 0\n\t" \
  "ds_read_b64_tr_b16 v[96:97], v160 offset:8192\n\t" \
  "v_mfma_f32_16x16x32_bf16 v[148:151], v[100:103], v[4:7], 0\n\t" \
  "ds_read_b64_tr_b16 v[98:99], v160 offset:10240\n\t" \
  "s_waitcnt lgkmcnt(10)\n\t" \
  "v_mfma_f32_16x16x32_bf16 v[136:139], v[104:107], v[0:3], 0\n\t" \
  "ds_read_b64_tr_b16 v[100:101], v161 offset:8192\n\t" \
  "v_mfma_f32_16x16x32_bf16 v[152:155], v[104:107], v[4:7], 0\n\t" \
  "ds_read_b64_tr_b16 v[102:103], v161 offset:10240\n\t" \
  "v_mfma_f32_16x16x32_bf16 v[140:143], v[108:111], v[0:3], 0\n\t" \
  "ds_read_b64_tr_b16 v[104:105], v162 offset:8192\n\t" \
  "v_mfma_f32_16x16x32_bf16 v[156:159], v[108:111], v[4:7], 0\n\t" \
  "ds_read_b64_tr_b16 v[106:107], v162 offset:10240\n\t" \
  "s_waitcnt vmcnt(16) lgkmcnt(10)\n\t" \
  "v_mfma_f32_16x16x32_bf16 v[128:131], v[112:115], v[8:11], v[128:131]\n\t" \
  "ds_read_b64_tr_b16 v[108:109], v163 offset:8192\n\t" \
  "buffer_load_dwordx4 v[80:83], v164, %[ob], s69 offen offset:0 sc1\n\t" \
  "v_mfma_f32_16x16x32_bf16 v[144:147], v[112:115], v[12:15], v[144:147]\n\t" \
  "ds_read_b64_tr_b16 v[110:111], v163 offset:10240\n\t" \
  "v_mfma_f32_16x16x32_bf16 v[132:135], v[116:119], v[8:11], v[132:135]\n\t" \
  "ds_read_b64_tr_b16 v[112:113], v160 offset:12288\n\t" \
  "v_mfma_f32_16x16x32_bf16 v[148:151], v[116:119], v[12:15], v[148:151]\n\t" \
  "ds_read_b64_tr_b16 v[114:115], v160 offset:14336\n\t" \
  "s_waitcnt lgkmcnt(10)\n\t" \
  "v_mfma_f32_16x16x32_bf16 v[136:139], v[120:123], v[8:11], v[136:139]\n\t" \
  "ds_read_b64_tr_b16 v[116:117], v161 offset:12288\n\t" \
  "buffer_load_dwordx4 v[84:87], v164, %[ob], s69 offen offset:1024 sc1\n\t" \
  "v_mfma_f32_16x16x32_bf16 v[152:155], v[120:123], v[12:15], v[152:155]\n\t" \
  "ds_read_b64_tr_b16 v[118:119], v161 offset:14336\n\t" \
  "v_mfma_f32_16x16x32_bf16 v[140:143], v[124:127], v[8:11], v[140:143]\n\t" \
  "ds_read_b64_tr_b16 v[120:121], v162 offset:12288\n\t" \
  "v_mfma_f32_16x16x32_bf16 v[156:159], v[124:127], v[12:15], v[156:159]\n\t" \
  "ds_read_b64_tr_b16 v[122:123], v162 offset:14336\n\t" \
  "s_waitcnt vmcnt(16) lgkmcnt(10)\n\t" \
  "v_mfma_f32_16x16x32_bf16 v[128:131], v[96:99], v[16:19], v[128:131]\n\t" \
  "ds_read_b64_tr_b16 v[124:125], v163 offset:12288\n\t" \
  "buffer_load_dwordx4 v[88:91], v164, %[ob], s69 offen offset:2048 sc1\n\t" \
  "v_mfma_f32_16x16x32_bf16 v[144:147], v[96:99], v[20:23], v[144:147]\n\t" \
  "ds_read_b64_tr_b16 v[126:127], v163 offset:14336\n\t" \
  "v_mfma_f32_16x16x32_bf16 v[132:135], v[100:103], v[16:19], v[132:135]\n\t" \
  "ds_read_b64_tr_b16 v[96:97], v160 offset:16384\n\t" \
  "v_mfma_f32_16x16x32_bf16 v[148:151], v[100:103], v[20:23], v[148:151]\n\t" \
  "ds_read_b64_tr_b16 v[98:99], v160 offset:18432\n\t" \
  "s_waitcnt lgkmcnt(10)\n\t" \
  "v_mfma_f32_16x16x32_bf16 v[136:139], v[104:107], v[16:19], v[136:139]\n\t" \
  "ds_read_b64_tr_b16 v[100:101], v161 offset:16384\n\t" \
  "v_mfma_f32_16x16x32_bf16 v[152:155], v[104:107], v[20:23], v[152:155]\n\t" \
  "ds_read_b64_tr_b16 v[102:103], v161 offset:18432\n\t" \
  "buffer_load_dwordx4 v[92:95], v164, %[ob], s69 offen offset:3072 sc1\n\t" \
  "v_mfma_f32_16x16x32_bf16 v[140:143], v[108:111], v[16:19], v[140:143]\n\t" \
  "ds_read_b64_tr_b16 v[104:105], v162 offset:16384\n\t" \
  "v_mfma_f32_16x16x32_bf16 v[156:159], v[108:111], v[20:23], v[156:159]\n\t" \
  "ds_read_b64_tr_b16 v[106:107], v162 offset:18432\n\t" \
  "s_waitcnt vmcnt(16) lgkmcnt(10)\n\t" \
  "v_mfma_f32_16x16x32_bf16 v[128:131], v[112:115], v[24:27], v[128:131]\n\t" \
  "ds_read_b64_tr_b16 v[108:109], v163 offset:16384\n\t" \
  "v_mfma_f32_16x16x32_bf16 v[144:147], v[112:115], v[28:31], v[144:147]\n\t" \
  "ds_read_b64_tr_b16 v[110:111], v163 offset:18432\n\t" \
  "buffer_load_dwordx4 v[0:3], v164, %[ob], s70 offen offset:0 sc1\n\t" \
  "v_mfma_f32_16x16x32_bf16 v[132:135], v[116:119], v[24:27], v[132:135]\n\t" \
  "ds_read_b64_tr_b16 v[112:113], v160 offset:20480\n\t" \
  "v_mfma_f32_16x16x32_bf16 v[148:151], v[116:119], v[28:31], v[148:151]\n\t" \
  "ds_read_b64_tr_b16 v[114:115], v160 offset:22528\n\t" \
  "s_waitcnt lgkmcnt(10)\n\t" \
  "v_mfma_f32_16x16x32_bf16 v[136:139], v[120:123], v[24:27], v[136:139]\n\t" \
  "ds_read_b64_tr_b16 v[116:117], v161 offset:20480\n\t" \
  "v_mfma_f32_16x16x32_bf16 v[152:155], v[120:123], v[28:31], v[152:155]\n\t" \
  "ds_read_b64_tr_b16 v[118:119], v161 offset:22528\n\t" \
  "v_mfma_f32_16x16x32_bf16 v[140:143], v[124:127], v[24:27], v[140:143]\n\t" \
  "ds_read_b64_tr_b16 v[120:121], v162 offset:20480\n\t" \
  "buffer_load_dwordx4 v[4:7], v164, %[ob], s70 offen offset:1024 sc1\n\t" \
  "v_mfma_f32_16x16x32_bf16 v[156:159], v[124:127], v[28:31], v[156:159]\n\t" \
  "ds_read_b64_tr_b16 v[122:123], v162 offset:22528\n\t" \
  "s_waitcnt vmcnt(16) lgkmcnt(10)\n\t" \
  "v_mfma_f32_16x16x32_bf16 v[128:131], v[96:99], v[32:35], v[128:131]\n\t" \
  "ds_read_b64_tr_b16 v[124:125], v163 offset:20480\n\t" \
  "ds_read_b32 v165, v165\n\t" \
  "v_mfma_f32_16x16x32_bf16 v[144:147], v[96:99], v[36:39], v[144:147]\n\t" \
  "ds_read_b64_tr_b16 v[126:127], v163 offset:22528\n\t" \
  "v_mfma_f32_16x16x32_bf16 v[132:135], v[100:103], v[32:35], v[132:135]\n\t" \
  "ds_read_b64_tr_b16 v[96:97], v160 offset:24576\n\t" \
  "buffer_load_dwordx4 v[8:11], v164, %[ob], s70 offen offset:2048 sc1\n\t" \
  "v_mfma_f32_16x16x32_bf16 v[148:151], v[100:103], v[36:39], v[148:151]\n\t" \
  "ds_read_b64_tr_b16 v[98:99], v160 offset:26624\n\t" \
  "s_waitcnt lgkmcnt(11)\n\t" \
  "v_mfma_f32_16x16x32_bf16 v[136:139], v[104:107], v[32:35], v[136:139]\n\t" \
  "ds_read_b64_tr_b16 v[100:101], v161 offset:24576\n\t" \
  "v_mfma_f32_16x16x32_bf16 v[152:155], v[104:107], v[36:39], v[152:155]\n\t" \
  "ds_read_b64_tr_b16 v[102:103], v161 offset:26624\n\t" \
  "v_mfma_f32_16x16x32_bf16 v[140:143], v[108:111], v[32:35], v[140:143]\n\t" \
  "ds_read_b64_tr_b16 v[104:105], v162 offset:24576\n\t" \
  "v_mfma_f32_16x16x32_bf16 v[156:159], v[108:111], v[36:39], v[156:159]\n\t" \
  "ds_read_b64_tr_b16 v[106:107], v162 offset:26624\n\t" \
  "s_waitcnt vmcnt(15) lgkmcnt(11)\n\t" \
  "v_mfma_f32_16x16x32_bf16 v[128:131], v[112:115], v[40:43], v[128:131]\n\t" \
  "ds_read_b64_tr_b16 v[108:109], v163 offset:24576\n\t" \
  "buffer_load_dwordx4 v[12:15], v164, %[ob], s70 offen offset:3072 sc1\n\t" \
  "v_mfma_f32_16x16x32_bf16 v[144:147], v[112:115], v[44:47], v[144:147]\n\t" \
  "ds_read_b64_tr_b16 v[110:111], v163 offset:26624\n\t" \
  "s_waitcnt lgkmcnt(9)\n\t" \
  "v_mfma_f32_16x16x32_bf16 v[132:135], v[116:119], v[40:43], v[132:135]\n\t" \
  "ds_read_b64_tr_b16 v[112:113], v160 offset:28672\n\t" \
  "v_cmp_lt_u32 vcc, 13, v165\n\t" \
  "v_mfma_f32_16x16x32_bf16 v[148:151], v[116:119], v[44:47], v[148:151]\n\t" \
  "ds_read_b64_tr_b16 v[114:115], v160 offset:30720\n\t" \
  "s_cbranch_vccz QA7_%=\n\t" \
  "QB7_%=:\n\t" \
  "s_waitcnt lgkmcnt(10)\n\t" \
  "v_mfma_f32_16x16x32_bf16 v[136:139], v[120:123], v[40:43], v[136:139]\n\t" \
  "ds_read_b64_tr_b16 v[116:117], v161 offset:28672\n\t" \
  "buffer_load_dwordx4 v[16:19], v164, %[ob], s71 offen offset:0 sc1\n\t" \
  "v_mfma_f32_16x16x32_bf16 v[152:155], v[120:123], v[44:47], v[152:155]\n\t" \
  "ds_read_b64_tr_b16 v[118:119], v161 offset:30720\n\t" \
  "v_mov_b32 v165, %[sy]\n\t" \
  "v_mfma_f32_16x16x32_bf16 v[140:143], v[124:127], v[40:43], v[140:143]\n\t" \
  "ds_read_b64_tr_b16 v[120:121], v162 offset:28672\n\t" \
  "v_mfma_f32_16x16x32_bf16 v[156:159], v[124:127], v[44:47], v[156:159]\n\t" \
  "ds_read_b64_tr_b16 v[122:123], v162 offset:30720\n\t" \
  "s_waitcnt vmcnt(15) lgkmcnt(10)\n\t" \
  "v_mfma_f32_16x16x32_bf16 v[128:131], v[96:99], v[48:51], v[128:131]\n\t" \
  "ds_read_b64_tr_b16 v[124:125], v163 offset:28672\n\t" \
  "buffer_load_dwordx4 v[20:23], v164, %[ob], s71 offen offset:1024 sc1\n\t" \
  "v_mfma_f32_16x16x32_bf16 v[144:147], v[96:99], v[52:55], v[144:147]\n\t" \
  "ds_read_b64_tr_b16 v[126:127], v163 offset:30720\n\t" \
  "v_mfma_f32_16x16x32_bf16 v[132:135], v[100:103], v[48:51], v[132:135]\n\t" \
  "ds_read_b64_tr_b16 v[96:97], v160 offset:32768\n\t" \
  "v_mfma_f32_16x16x32_bf16 v[148:151], v[100:103], v[52:55], v[148:151]\n\t" \
  "ds_read_b64_tr_b16 v[98:99], v160 offset:34816\n\t" \
  "s_waitcnt lgkmcnt(10)\n\t" \
  "v_mfma_f32_16x16x32_bf16 v[136:139], v[104:107], v[48:51], v[136:139]\n\t" \
  "ds_read_b64_tr_b16 v[100:101], v161 offset:32768\n\t" \
  "buffer_load_dwordx4 v[24:27], v164, %[ob], s71 offen offset:2048 sc1\n\t" \
  "v_mfma_f32_16x16x32_bf16 v[152:155], v[104:107], v[52:55], v[152:155]\n\t" \
  "ds_read_b64_tr_b16 v[102:103], v161 offset:34816\n\t" \
  "v_mfma_f32_16x16x32_bf16 v[140:143], v[108:111], v[48:51], v[140:143]\n\t" \
  "ds_read_b64_tr_b16 v[104:105], v162 offset:32768\n\t" \
  "v_mfma_f32_16x16x32_bf16 v[156:159], v[108:111], v[52:55], v[156:159]\n\t" \
  "ds_read_b64_tr_b16 v[106:107], v162 offset:34816\n\t" \
  "s_waitcnt vmcnt(15) lgkmcnt(10)\n\t" \
  "v_mfma_f32_16x16x32_bf16 v[128:131], v[112:115], v[56:59], v[128:131]\n\t" \
  "ds_read_b64_tr_b16 v[108:109], v163 offset:32768\n\t" \
  "v_mfma_f32_16x16x32_bf16 v[144:147], v[112:115], v[60:63], v[144:147]\n\t" \
  "ds_read_b64_tr_b16 v[110:111], v163 offset:34816\n\t" \
  "buffer_load_dwordx4 v[28:31], v164, %[ob], s71 offen offset:3072 sc1\n\t" \
  "v_mfma_f32_16x16x32_bf16 v[132:135], v[116:119], v[56:59], v[132:135]\n\t" \
  "ds_read_b64_tr_b16 v[112:113], v160 offset:36864\n\t" \
  "v_mfma_f32_16x16x32_bf16 v[148:151], v[116:119], v[60:63], v[148:151]\n\t" \
  "ds_read_b64_tr_b16 v[114:115], v160 offset:38912\n\t" \
  "s_waitcnt lgkmcnt(10)\n\t" \
  "v_mfma_f32_16x16x32_bf16 v[136:139], v[120:123], v[56:59], v[136:139]\n\t" \
  "ds_read_b64_tr_b16 v[116:117], v161 offset:36864\n\t" \
  "v_mfma_f32_16x16x32_bf16 v[152:155], v[120:123], v[60:63], v[152:155]\n\t" \
  "ds_read_b64_tr_b16 v[118:119], v161 offset:38912\n\t" \
  "buffer_load_dwordx4 v[32:35], v164, %[ob], s72 offen offset:0 sc1\n\t" \
  "v_mfma_f32_16x16x32_bf16 v[140:143], v[124:127], v[56:59], v[140:143]\n\t" \
  "ds_read_b64_tr_b16 v[120:121], v162 offset:36864\n\t" \
  "v_mfma_f32_16x16x32_bf16 v[156:159], v[124:127], v[60:63], v[156:159]\n\t" \
  "ds_read_b64_tr_b16 v[122:123], v162 offset:38912\n\t" \
  "s_waitcnt vmcnt(15) lgkmcnt(10)\n\t" \
  "v_mfma_f32_16x16x32_bf16 v[128:131], v[96:99], v[64:67], v[128:131]\n\t" \
  "ds_read_b64_tr_b16 v[124:125], v163 offset:36864\n\t" \
  "v_mfma_f32_16x16x32_bf16 v[144:147], v[96:99], v[68:71], v[144:147]\n\t" \
  "ds_read_b64_tr_b16 v[126:127], v163 offset:38912\n\t" \
  "v_mfma_f32_16x16x32_bf16 v[132:135], v[100:103], v[64:67], v[132:135]\n\t" \
  "ds_read_b64_tr_b16 v[96:97], v160 offset:40960\n\t" \
  "buffer_load_dwordx4 v[36:39], v164, %[ob], s72 offen offset:1024 sc1\n\t" \
  "v_mfma_f32_16x16x32_bf16 v[148:151], v[100:103], v[68:71], v[148:151]\n\t" \
  "ds_read_b64_tr_b16 v[98:99], v160 offset:43008\n\t" \
  "s_waitcnt lgkmcnt(10)\n\t" \
  "v_mfma_f32_16x16x32_bf16 v[136:139], v[104:107], v[64:67], v[136:139]\n\t" \
  "ds_read_b64_tr_b16 v[100:101], v161 offset:40960\n\t" \
  "v_mfma_f32_16x16x32_bf16 v[152:155], v[104:107], v[68:71], v[152:155]\n\t" \
  "ds_read_b64_tr_b16 v[102:103], v161 offset:43008\n\t" \
  "v_mfma_f32_16x16x32_bf16 v[140:143], v[108:111], v[64:67], v[140:143]\n\t" \
  "ds_read_b64_tr_b16 v[104:105], v162 offset:40960\n\t" \
  "buffer_load_dwordx4 v[40:43], v164, %[ob], s72 offen offset:2048 sc1\n\t" \
  "v_mfma_f32_16x16x32_bf16 v[156:159], v[108:111], v[68:71], v[156:159]\n\t" \
  "ds_read_b64_tr_b16 v[106:107], v162 offset:43008\n\t" \
  "s_waitcnt vmcnt(15) lgkmcnt(10)\n\t" \
  "v_mfma_f32_16x16x32_bf16 v[128:131], v[112:115], v[72:75], v[128:131]\n\t" \
  "ds_read_b64_tr_b16 v[108:109], v163 offset:40960\n\t" \
  "v_mfma_f32_16x16x32_bf16 v[144:147], v[112:115], v[76:79], v[144:147]\n\t" \
  "ds_read_b64_tr_b16 v[110:111], v163 offset:43008\n\t" \
  "v_mfma_f32_16x16x32_bf16 v[132:135], v[116:119], v[72:75], v[132:135]\n\t" \
  "ds_read_b64_tr_b16 v[112:113], v160 offset:45056\n\t" \
  "buffer_load_dwordx4 v[44:47], v164, %[ob], s72 offen offset:3072 sc1\n\t" \
  "v_mfma_f32_16x16x32_bf16 v[148:151], v[116:119], v[76:79], v[148:151]\n\t" \
  "ds_read_b64_tr_b16 v[114:115], v160 offset:47104\n\t" \
  "s_waitcnt lgkmcnt(10)\n\t" \
  "v_mfma_f32_16x16x32_bf16 v[136:139], v[120:123], v[72:75], v[136:139]\n\t" \
  "ds_read_b64_tr_b16 v[116:117], v161 offset:45056\n\t" \
  "v_mfma_f32_16x16x32_bf16 v[152:155], v[120:123], v[76:79], v[152:155]\n\t" \
  "ds_read_b64_tr_b16 v[118:119], v161 offset:47104\n\t" \
  "v_mfma_f32_16x16x32_bf16 v[140:143], v[124:127], v[72:75], v[140:143]\n\t" \
  "ds_read_b64_tr_b16 v[120:121], v162 offset:45056\n\t" \
  "v_mfma_f32_16x16x32_bf16 v[156:159], v[124:127], v[76:79], v[156:159]\n\t" \
  "ds_read_b64_tr_b16 v[122:123], v162 offset:47104\n\t" \
  "s_waitcnt vmcnt(14) lgkmcnt(10)\n\t" \
  "v_mfma_f32_16x16x32_bf16 v[128:131], v[96:99], v[80:83], v[128:131]\n\t" \
  "ds_read_b64_tr_b16 v[124:125], v163 offset:45056\n\t" \
  "buffer_load_dwordx4 v[48:51], v164, %[ob], s73 offen offset:0 sc1\n\t" \
  "v_mfma_f32_16x16x32_bf16 v[144:147], v[96:99], v[84:87], v[144:147]\n\t" \
  "ds_read_b64_tr_b16 v[126:127], v163 offset:47104\n\t" \
  "v_mfma_f32_16x16x32_bf16 v[132:135], v[100:103], v[80:83], v[132:135]\n\t" \
  "ds_read_b64_tr_b16 v[96:97], v160 offset:49152\n\t" \
  "v_mfma_f32_16x16x32_bf16 v[148:151], v[100:103], v[84:87], v[148:151]\n\t" \
  "ds_read_b64_tr_b16 v[98:99], v160 offset:51200\n\t" \
  "s_waitcnt lgkmcnt(10)\n\t" \
  "v_mfma_f32_16x16x32_bf16 v[136:139], v[104:107], v[80:83], v[136:139]\n\t" \
  "ds_read_b64_tr_b16 v[100:101], v161 offset:49152\n\t" \
  "buffer_load_dwordx4 v[52:55], v164, %[ob], s73 offen offset:1024 sc1\n\t" \
  "v_mfma_f32_16x16x32_bf16 v[152:155], v[104:107], v[84:87], v[152:155]\n\t" \
  "ds_read_b64_tr_b16 v[102:103], v161 offset:51200\n\t" \
  "v_mfma_f32_16x16x32_bf16 v[140:143], v[108:111], v[80:83], v[140:143]\n\t" \
  "ds_read_b64_tr_b16 v[104:105], v162 offset:49152\n\t" \
  "v_mfma_f32_16x16x32_bf16 v[156:159], v[108:111], v[84:87], v[156:159]\n\t" \
  "ds_read_b64_tr_b16 v[106:107], v162 offset:51200\n\t" \
  "s_waitcnt vmcnt(14) lgkmcnt(10)\n\t" \
  "v_mfma_f32_16x16x32_bf16 v[128:131], v[112:115], v[88:91], v[128:131]\n\t" \
  "ds_read_b64_tr_b16 v[108:109], v163 offset:49152\n\t" \
  "buffer_load_dwordx4 v[56:59], v164, %[ob], s73 offen offset:2048 sc1\n\t" \
  "v_mfma_f32_16x16x32_bf16 v[144:147], v[112:115], v[92:95], v[144:147]\n\t" \
  "ds_read_b64_tr_b16 v[110:111], v163 offset:51200\n\t" \
  "v_mfma_f32_16x16x32_bf16 v[132:135], v[116:119], v[88:91], v[132:135]\n\t" \
  "ds_read_b64_tr_b16 v[112:113], v160 offset:53248\n\t" \
  "v_mfma_f32_16x16x32_bf16 v[148:151], v[116:119], v[92:95], v[148:151]\n\t" \
  "ds_read_b64_tr_b16 v[114:115], v160 offset:55296\n\t" \
  "s_waitcnt lgkmcnt(10)\n\t" \
  "v_mfma_f32_16x16x32_bf16 v[136:139], v[120:123], v[88:91], v[136:139]\n\t" \
  "ds_read_b64_tr_b16 v[116:117], v161 offset:53248\n\t" \
  "buffer_load_dwordx4 v[60:63], v164, %[ob], s73 offen offset:3072 sc1\n\t" \
  "v_mfma_f32_16x16x32_bf16 v[152:155], v[120:123], v[92:95], v[152:155]\n\t" \
  "ds_read_b64_tr_b16 v[118:119], v161 offset:55296\n\t" \
  "v_mfma_f32_16x16x32_bf16 v[140:143], v[124:127], v[88:91], v[140:143]\n\t" \
  "ds_read_b64_tr_b16 v[120:121], v162 offset:53248\n\t" \
  "v_mfma_f32_16x16x32_bf16 v[156:159], v[124:127], v[92:95], v[156:159]\n\t" \
  "ds_read_b64_tr_b16 v[122:123], v162 offset:55296\n\t" \
  "s_waitcnt vmcnt(14) lgkmcnt(10)\n\t" \
  "v_mfma_f32_16x16x32_bf16 v[128:131], v[96:99], v[0:3], v[128:131]\n\t" \
  "ds_read_b64_tr_b16 v[124:125], v163 offset:53248\n\t" \
  "v_mfma_f32_16x16x32_bf16 v[144:147], v[96:99], v[4:7], v[144:147]\n\t" \
  "ds_read_b64_tr_b16 v[126:127], v163 offset:55296\n\t" \
  "buffer_load_dwordx4 v[64:67], v164, %[ob], s74 offen offset:0 sc1\n\t" \
  "v_mfma_f32_16x16x32_bf16 v[132:135], v[100:103], v[0:3], v[132:135]\n\t" \
  "ds_read_b64_tr_b16 v[96:97], v160 offset:57344\n\t" \
  "v_mfma_f32_16x16x32_bf16 v[148:151], v[100:103], v[4:7], v[148:151]\n\t" \
  "ds_read_b64_tr_b16 v[98:99], v160 offset:59392\n\t" \
  "s_waitcnt lgkmcnt(10)\n\t" \
  "v_mfma_f32_16x16x32_bf16 v[136:139], v[104:107], v[0:3], v[136:139]\n\t" \
  "ds_read_b64_tr_b16 v[100:101], v161 offset:57344\n\t" \
  "v_mfma_f32_16x16x32_bf16 v[152:155], v[104:107], v[4:7], v[152:155]\n\t" \
  "ds_read_b64_tr_b16 v[102:103], v161 offset:59392\n\t" \
  "buffer_load_dwordx4 v[68:71], v164, %[ob], s74 offen offset:1024 sc1\n\t" \
  "v_mfma_f32_16x16x32_bf16 v[140:143], v[108:111], v[0:3], v[140:143]\n\t" \
  "ds_read_b64_tr_b16 v[104:105], v162 offset:57344\n\t" \
  "v_mfma_f32_16x16x32_bf16 v[156:159], v[108:111], v[4:7], v[156:159]\n\t" \
  "ds_read_b64_tr_b16 v[106:107], v162 offset:59392\n\t" \
  "s_waitcnt vmcnt(14) lgkmcnt(10)\n\t" \
  "v_mfma_f32_16x16x32_bf16 v[128:131], v[112:115], v[8:11], v[128:131]\n\t" \
  "ds_read_b64_tr_b16 v[108:109], v163 offset:57344\n\t" \
  "v_mfma_f32_16x16x32_bf16 v[144:147], v[112:115], v[12:15], v[144:147]\n\t" \
  "ds_read_b64_tr_b16 v[110:111], v163 offset:59392\n\t" \
  "v_mfma_f32_16x16x32_bf16 v[132:135], v[116:119], v[8:11], v[132:135]\n\t" \
  "ds_read_b64_tr_b16 v[112:113], v160 offset:61440\n\t" \
  "buffer_load_dwordx4 v[72:75], v164, %[ob], s74 offen offset:2048 sc1\n\t" \
  "v_mfma_f32_16x16x32_bf16 v[148:151], v[116:119], v[12:15], v[148:151]\n\t" \
  "ds_read_b64_tr_b16 v[114:115], v160 offset:63488\n\t" \
  "s_waitcnt lgkmcnt(10)\n\t" \
  "v_mfma_f32_16x16x32_bf16 v[136:139], v[120:123], v[8:11], v[136:139]\n\t" \
  "ds_read_b64_tr_b16 v[116:117], v161 offset:61440\n\t" \
  "v_mfma_f32_16x16x32_bf16 v[152:155], v[120:123], v[12:15], v[152:155]\n\t" \
  "ds_read_b64_tr_b16 v[118:119], v161 offset:63488\n\t" \
  "v_mfma_f32_16x16x32_bf16 v[140:143], v[124:127], v[8:11], v[140:143]\n\t" \
  "ds_read_b64_tr_b16 v[120:121], v162 offset:61440\n\t" \
  "buffer_load_dwordx4 v[76:79], v164, %[ob], s74 offen offset:3072 sc1\n\t" \
  "v_mfma_f32_16x16x32_bf16 v[156:159], v[124:127], v[12:15], v[156:159]\n\t" \
  "ds_read_b64_tr_b16 v[122:123], v162 offset:63488\n\t" \
  "s_waitcnt vmcnt(14) lgkmcnt(10)\n\t" \
  "v_mfma_f32_16x16x32_bf16 v[128:131], v[96:99], v[16:19], v[128:131]\n\t" \
  "ds_read_b64_tr_b16 v[124:125], v163 offset:61440\n\t" \
  "v_mfma_f32_16x16x32_bf16 v[144:147], v[96:99], v[20:23], v[144:147]\n\t" \
  "ds_read_b64_tr_b16 v[126:127], v163 offset:63488\n\t" \
  "v_mfma_f32_16x16x32_bf16 v[132:135], v[100:103], v[16:19], v[132:135]\n\t" \
  "v_add_u32 v160, 0x10000, v160\n\t" \
  "ds_read_b64_tr_b16 v[96:97], v160 offset:0\n\t" \
  "v_mfma_f32_16x16x32_bf16 v[148:151], v[100:103], v[20:23], v[148:151]\n\t" \
  "ds_read_b64_tr_b16 v[98:99], v160 offset:2048\n\t" \
  "s_waitcnt lgkmcnt(10)\n\t" \
  "v_mfma_f32_16x16x32_bf16 v[136:139], v[104:107], v[16:19], v[136:139]\n\t" \
  "v_add_u32 v161, 0x10000, v161\n\t" \
  "ds_read_b64_tr_b16 v[100:101], v161 offset:0\n\t" \
  "buffer_load_dwordx4 v[80:83], v164, %[ob], s75 offen offset:0 sc1\n\t" \
  "v_mfma_f32_16x16x32_bf16 v[152:155], v[104:107], v[20:23], v[152:155]\n\t" \
  "ds_read_b64_tr_b16 v[102:103], v161 offset:2048\n\t" \
  "v_mfma_f32_16x16x32_bf16 v[140:143], v[108:111], v[16:19], v[140:143]\n\t" \
  "v_add_u32 v162, 0x10000, v162\n\t" \
  "ds_read_b64_tr_b16 v[104:105], v162 offset:0\n\t" \
  "v_mfma_f32_16x16x32_bf16 v[156:159], v[108:111], v[20:23], v[156:159]\n\t" \
  "ds_read_b64_tr_b16 v[106:107], v162 offset:2048\n\t" \
  "s_waitcnt vmcnt(13) lgkmcnt(10)\n\t" \
  "v_mfma_f32_16x16x32_bf16 v[128:131], v[112:115], v[24:27], v[128:131]\n\t" \
  "v_add_u32 v163, 0x10000, v163\n\t" \
  "ds_read_b64_tr_b16 v[108:109], v163 offset:0\n\t" \
  "buffer_load_dwordx4 v[84:87], v164, %[ob], s75 offen offset:1024 sc1\n\t" \
  "v_mfma_f32_16x16x32_bf16 v[144:147], v[112:115], v[28:31], v[144:147]\n\t" \
  "ds_read_b64_tr_b16 v[110:111], v163 offset:2048\n\t" \
  "v_mfma_f32_16x16x32_bf16 v[132:135], v[116:119], v[24:27], v[132:135]\n\t" \
  "ds_read_b64_tr_b16 v[112:113], v160 offset:4096\n\t" \
  "v_mfma_f32_16x16x32_bf16 v[148:151], v[116:119], v[28:31], v[148:151]\n\t" \
  "ds_read_b64_tr_b16 v[114:115], v160 offset:6144\n\t" \
  "s_waitcnt lgkmcnt(10)\n\t" \
  "v_mfma_f32_16x16x32_bf16 v[136:139], v[120:123], v[24:27], v[136:139]\n\t" \
  "ds_read_b64_tr_b16 v[116:117], v161 offset:4096\n\t" \
  "buffer_load_dwordx4 v[88:91], v164, %[ob], s75 offen offset:2048 sc1\n\t" \
  "v_mfma_f32_16x16x32_bf16 v[152:155], v[120:123], v[28:31], v[152:155]\n\t" \
  "ds_read_b64_tr_b16 v[118:119], v161 offset:6144\n\t" \
  "v_mfma_f32_16x16x32_bf16 v[140:143], v[124:127], v[24:27], v[140:143]\n\t" \
  "ds_read_b64_tr_b16 v[120:121], v162 offset:4096\n\t" \
  "v_mfma_f32_16x16x32_bf16 v[156:159], v[124:127], v[28:31], v[156:159]\n\t" \
  "ds_read_b64_tr_b16 v[122:123], v162 offset:6144\n\t" \
  "s_waitcnt vmcnt(13) lgkmcnt(10)\n\t" \
  "v_mfma_f32_16x16x32_bf16 v[128:131], v[96:99], v[32:35], v[128:131]\n\t" \
  "ds_read_b64_tr_b16 v[124:125], v163 offset:4096\n\t" \
  "buffer_load_dwordx4 v[92:95], v164, %[ob], s75 offen offset:3072 sc1\n\t" \
  "v_mfma_f32_16x16x32_bf16 v[144:147], v[96:99], v[36:39], v[144:147]\n\t" \
  "ds_read_b64_tr_b16 v[126:127], v163 offset:6144\n\t" \
  "v_mfma_f32_16x16x32_bf16 v[132:135], v[100:103], v[32:35], v[132:135]\n\t" \
  "ds_read_b64_tr_b16 v[96:97], v160 offset:8192\n\t" \
  "v_mfma_f32_16x16x32_bf16 v[148:151], v[100:103], v[36:39], v[148:151]\n\t" \
  "ds_read_b64_tr_b16 v[98:99], v160 offset:10240\n\t" \
  "s_waitcnt lgkmcnt(10)\n\t" \
  "v_mfma_f32_16x16x32_bf16 v[136:139], v[104:107], v[32:35], v[136:139]\n\t" \
  "ds_read_b64_tr_b16 v[100:101], v161 offset:8192\n\t" \
  "v_mfma_f32_16x16x32_bf16 v[152:155], v[104:107], v[36:39], v[152:155]\n\t" \
  "ds_read_b64_tr_b16 v[102:103], v161 offset:10240\n\t" \
  "buffer_load_dwordx4 v[0:3], v164, %[ob], s76 offen offset:0 sc1\n\t" \
  "v_mfma_f32_16x16x32_bf16 v[140:143], v[108:111], v[32:35], v[140:143]\n\t" \
  "ds_read_b64_tr_b16 v[104:105], v162 offset:8192\n\t" \
  "v_mfma_f32_16x16x32_bf16 v[156:159], v[108:111], v[36:39], v[156:159]\n\t" \
  "ds_read_b64_tr_b16 v[106:107], v162 offset:10240\n\t" \
  "s_waitcnt vmcnt(13) lgkmcnt(10)\n\t" \
  "v_mfma_f32_16x16x32_bf16 v[128:131], v[112:115], v[40:43], v[128:131]\n\t" \
  "ds_read_b64_tr_b16 v[108:109], v163 offset:8192\n\t" \
  "v_mfma_f32_16x16x32_bf16 v[144:147], v[112:115], v[44:47], v[144:147]\n\t" \
  "ds_read_b64_tr_b16 v[110:111], v163 offset:10240\n\t" \
  "buffer_load_dwordx4 v[4:7], v164, %[ob], s76 offen offset:1024 sc1\n\t" \
  "v_mfma_f32_16x16x32_bf16 v[132:135], v[116:119], v[40:43], v[132:135]\n\t" \
  "ds_read_b64_tr_b16 v[112:113], v160 offset:12288\n\t" \
  "v_mfma_f32_16x16x32_bf16 v[148:151], v[116:119], v[44:47], v[148:151]\n\t" \
  "ds_read_b64_tr_b16 v[114:115], v160 offset:14336\n\t" \
  "s_waitcnt lgkmcnt(10)\n\t" \
  "v_mfma_f32_16x16x32_bf16 v[136:139], v[120:123], v[40:43], v[136:139]\n\t" \
  "ds_read_b64_tr_b16 v[116:117], v161 offset:12288\n\t" \
  "v_mfma_f32_16x16x32_bf16 v[152:155], v[120:123], v[44:47], v[152:155]\n\t" \
  "ds_read_b64_tr_b16 v[118:119], v161 offset:14336\n\t" \
  "buffer_load_dwordx4 v[8:11], v164, %[ob], s76 offen offset:2048 sc1\n\t" \
  "v_mfma_f32_16x16x32_bf16 v[140:143], v[124:127], v[40:43], v[140:143]\n\t" \
  "ds_read_b64_tr_b16 v[120:121], v162 offset:12288\n\t" \
  "v_mfma_f32_16x16x32_bf16 v[156:159], v[124:127], v[44:47], v[156:159]\n\t" \
  "ds_read_b64_tr_b16 v[122:123], v162 offset:14336\n\t" \
  "s_waitcnt vmcnt(13) lgkmcnt(10)\n\t" \
  "v_mfma_f32_16x16x32_bf16 v[128:131], v[96:99], v[48:51], v[128:131]\n\t" \
  "ds_read_b64_tr_b16 v[124:125], v163 offset:12288\n\t" \
  "v_mfma_f32_16x16x32_bf16 v[144:147], v[96:99], v[52:55], v[144:147]\n\t" \
  "ds_read_b64_tr_b16 v[126:127], v163 offset:14336\n\t" \
  "v_mfma_f32_16x16x32_bf16 v[132:135], v[100:103], v[48:51], v[132:135]\n\t" \
  "ds_read_b64_tr_b16 v[96:97], v160 offset:16384\n\t" \
  "buffer_load_dwordx4 v[12:15], v164, %[ob], s76 offen offset:3072 sc1\n\t" \
  "v_mfma_f32_16x16x32_bf16 v[148:151], v[100:103], v[52:55], v[148:151]\n\t" \
  "ds_read_b64_tr_b16 v[98:99], v160 offset:18432\n\t" \
  "s_waitcnt lgkmcnt(10)\n\t" \
  "v_mfma_f32_16x16x32_bf16 v[136:139], v[104:107], v[48:51], v[136:139]\n\t" \
  "ds_read_b64_tr_b16 v[100:101], v161 offset:16384\n\t" \
  "v_mfma_f32_16x16x32_bf16 v[152:155], v[104:107], v[52:55], v[152:155]\n\t" \
  "ds_read_b64_tr_b16 v[102:103], v161 offset:18432\n\t" \
  "v_mfma_f32_16x16x32_bf16 v[140:143], v[108:111], v[48:51], v[140:143]\n\t" \
  "ds_read_b64_tr_b16 v[104:105], v162 offset:16384\n\t" \
  "buffer_load_dwordx4 v[16:19], v164, %[ob], s77 offen offset:0 sc1\n\t" \
  "v_mfma_f32_16x16x32_bf16 v[156:159], v[108:111], v[52:55], v[156:159]\n\t" \
  "ds_read_b64_tr_b16 v[106:107], v162 offset:18432\n\t" \
  "s_waitcnt vmcnt(13) lgkmcnt(10)\n\t" \
  "v_mfma_f32_16x16x32_bf16 v[128:131], v[112:115], v[56:59], v[128:131]\n\t" \
  "ds_read_b64_tr_b16 v[108:109], v163 offset:16384\n\t" \
  "v_mfma_f32_16x16x32_bf16 v[144:147], v[112:115], v[60:63], v[144:147]\n\t" \
  "ds_read_b64_tr_b16 v[110:111], v163 offset:18432\n\t" \
  "v_mfma_f32_16x16x32_bf16 v[132:135], v[116:119], v[56:59], v[132:135]\n\t" \
  "ds_read_b64_tr_b16 v[112:113], v160 offset:20480\n\t" \
  "v_mfma_f32_16x16x32_bf16 v[148:151], v[116:119], v[60:63], v[148:151]\n\t" \
  "ds_read_b64_tr_b16 v[114:115], v160 offset:22528\n\t" \
  "s_waitcnt lgkmcnt(10)\n\t" \
  "v_mfma_f32_16x16x32_bf16 v[136:139], v[120:123], v[56:59], v[136:139]\n\t" \
  "ds_read_b64_tr_b16 v[116:117], v161 offset:20480\n\t" \
  "buffer_load_dwordx4 v[20:23], v164, %[ob], s77 offen offset:1024 sc1\n\t" \
  "ds_read_b32 v165, v165\n\t" \
  "v_mfma_f32_16x16x32_bf16 v[152:155], v[120:123], v[60:63], v[152:155]\n\t" \
  "ds_read_b64_tr_b16 v[118:119], v161 offset:22528\n\t" \
  "v_mfma_f32_16x16x32_bf16 v[140:143], v[124:127], v[56:59], v[140:143]\n\t" \
  "ds_read_b64_tr_b16 v[120:121], v162 offset:20480\n\t" \
  "v_mfma_f32_16x16x32_bf16 v[156:159], v[124:127], v[60:63], v[156:159]\n\t" \
  "ds_read_b64_tr_b16 v[122:123], v162 offset:22528\n\t" \
  "s_waitcnt vmcnt(12) lgkmcnt(11)\n\t" \
  "v_mfma_f32_16x16x32_bf16 v[128:131], v[96:99], v[64:67], v[128:131]\n\t" \
  "ds_read_b64_tr_b16 v[124:125], v163 offset:20480\n\t" \
  "buffer_load_dwordx4 v[24:27], v164, %[ob], s77 offen offset:2048 sc1\n\t" \
  "v_mfma_f32_16x16x32_bf16 v[144:147], v[96:99], v[68:71], v[144:147]\n\t" \
  "ds_read_b64_tr_b16 v[126:127], v163 offset:22528\n\t" \
  "v_mfma_f32_16x16x32_bf16 v[132:135], v[100:103], v[64:67], v[132:135]\n\t" \
  "ds_read_b64_tr_b16 v[96:97], v160 offset:24576\n\t" \
  "v_mfma_f32_16x16x32_bf16 v[148:151], v[100:103], v[68:71], v[148:151]\n\t" \
  "ds_read_b64_tr_b16 v[98:99], v160 offset:26624\n\t" \
  "s_waitcnt lgkmcnt(11)\n\t" \
  "v_mfma_f32_16x16x32_bf16 v[136:139], v[104:107], v[64:67], v[136:139]\n\t" \
  "ds_read_b64_tr_b16 v[100:101], v161 offset:24576\n\t" \
  "buffer_load_dwordx4 v[28:31], v164, %[ob], s77 offen offset:3072 sc1\n\t" \
  "v_mfma_f32_16x16x32_bf16 v[152:155], v[104:107], v[68:71], v[152:155]\n\t" \
  "ds_read_b64_tr_b16 v[102:103], v161 offset:26624\n\t" \
  "s_waitcnt lgkmcnt(9)\n\t" \
  "v_mfma_f32_16x16x32_bf16 v[140:143], v[108:111], v[64:67], v[140:143]\n\t" \
  "ds_read_b64_tr_b16 v[104:105], v162 offset:24576\n\t" \
  "v_cmp_lt_u32 vcc, 15, v165\n\t" \
  "v_mfma_f32_16x16x32_bf16 v[156:159], v[108:111], v[68:71], v[156:159]\n\t" \
  "ds_read_b64_tr_b16 v[106:107], v162 offset:26624\n\t" \
  "s_cbranch_vccz QA14_%=\n\t" \
  "QB14_%=:\n\t" \
  "s_waitcnt vmcnt(12) lgkmcnt(10)\n\t" \
  "v_mfma_f32_16x16x32_bf16 v[128:131], v[112:115], v[72:75], v[128:131]\n\t" \
  "ds_read_b64_tr_b16 v[108:109], v163 offset:24576\n\t" \
  "buffer_load_dwordx4 v[32:35], v164, %[ob], s78 offen offset:0 sc1\n\t" \
  "v_mfma_f32_16x16x32_bf16 v[144:147], v[112:115], v[76:79], v[144:147]\n\t" \
  "ds_read_b64_tr_b16 v[110:111], v163 offset:26624\n\t" \
  "v_mfma_f32_16x16x32_bf16 v[132:135], v[116:119], v[72:75], v[132:135]\n\t" \
  "ds_read_b64_tr_b16 v[112:113], v160 offset:28672\n\t" \
  "v_mfma_f32_16x16x32_bf16 v[148:151], v[116:119], v[76:79], v[148:151]\n\t" \
  "ds_read_b64_tr_b16 v[114:115], v160 offset:30720\n\t" \
  "s_waitcnt lgkmcnt(10)\n\t" \
  "v_mfma_f32_16x16x32_bf16 v[136:139], v[120:123], v[72:75], v[136:139]\n\t" \
  "ds_read_b64_tr_b16 v[116:117], v161 offset:28672\n\t" \
  "v_mfma_f32_16x16x32_bf16 v[152:155], v[120:123], v[76:79], v[152:155]\n\t" \
  "ds_read_b64_tr_b16 v[118:119], v161 offset:30720\n\t" \
  "buffer_load_dwordx4 v[36:39], v164, %[ob], s78 offen offset:1024 sc1\n\t" \
  "v_mfma_f32_16x16x32_bf16 v[140:143], v[124:127], v[72:75], v[140:143]\n\t" \
  "ds_read_b64_tr_b16 v[120:121], v162 offset:28672\n\t" \
  "v_mfma_f32_16x16x32_bf16 v[156:159], v[124:127], v[76:79], v[156:159]\n\t" \
  "ds_read_b64_tr_b16 v[122:123], v162 offset:30720\n\t" \
  "s_waitcnt vmcnt(12) lgkmcnt(10)\n\t" \
  "v_mfma_f32_16x16x32_bf16 v[128:131], v[96:99], v[80:83], v[128:131]\n\t" \
  "ds_read_b64_tr_b16 v[124:125], v163 offset:28672\n\t" \
  "v_mfma_f32_16x16x32_bf16 v[144:147], v[96:99], v[84:87], v[144:147]\n\t" \
  "ds_read_b64_tr_b16 v[126:127], v163 offset:30720\n\t" \
  "buffer_load_dwordx4 v[40:43], v164, %[ob], s78 offen offset:2048 sc1\n\t" \
  "v_mfma_f32_16x16x32_bf16 v[132:135], v[100:103], v[80:83], v[132:135]\n\t" \
  "ds_read_b64_tr_b16 v[96:97], v160 offset:32768\n\t" \
  "v_mfma_f32_16x16x32_bf16 v[148:151], v[100:103], v[84:87], v[148:151]\n\t" \
  "ds_read_b64_tr_b16 v[98:99], v160 offset:34816\n\t" \
  "s_waitcnt lgkmcnt(10)\n\t" \
  "v_mfma_f32_16x16x32_bf16 v[136:139], v[104:107], v[80:83], v[136:139]\n\t" \
  "ds_read_b64_tr_b16 v[100:101], v161 offset:32768\n\t" \
  "v_mfma_f32_16x16x32_bf16 v[152:155], v[104:107], v[84:87], v[152:155]\n\t" \
  "ds_read_b64_tr_b16 v[102:103], v161 offset:34816\n\t" \
  "v_mfma_f32_16x16x32_bf16 v[140:143], v[108:111], v[80:83], v[140:143]\n\t" \
  "ds_read_b64_tr_b16 v[104:105], v162 offset:32768\n\t" \
  "buffer_load_dwordx4 v[44:47], v164, %[ob], s78 offen offset:3072 sc1\n\t" \
  "v_mfma_f32_16x16x32_bf16 v[156:159], v[108:111], v[84:87], v[156:159]\n\t" \
  "ds_read_b64_tr_b16 v[106:107], v162 offset:34816\n\t" \
  "s_waitcnt vmcnt(12) lgkmcnt(10)\n\t" \
  "v_mfma_f32_16x16x32_bf16 v[128:131], v[112:115], v[88:91], v[128:131]\n\t" \
  "ds_read_b64_tr_b16 v[108:109], v163 offset:32768\n\t" \
  "v_mfma_f32_16x16x32_bf16 v[144:147], v[112:115], v[92:95], v[144:147]\n\t" \
  "ds_read_b64_tr_b16 v[110:111], v163 offset:34816\n\t" \
  "v_mfma_f32_16x16x32_bf16 v[132:135], v[116:119], v[88:91], v[132:135]\n\t" \
  "ds_read_b64_tr_b16 v[112:113], v160 offset:36864\n\t" \
  "buffer_load_dwordx4 v[48:51], v164, %[ob], s79 offen offset:0 sc1\n\t" \
  "v_mfma_f32_16x16x32_bf16 v[148:151], v[116:119], v[92:95], v[148:151]\n\t" \
  "ds_read_b64_tr_b16 v[114:115], v160 offset:38912\n\t" \
  "s_waitcnt lgkmcnt(10)\n\t" \
  "v_mfma_f32_16x16x32_bf16 v[136:139], v[120:123], v[88:91], v[136:139]\n\t" \
  "ds_read_b64_tr_b16 v[116:117], v161 offset:36864\n\t" \
  "v_mfma_f32_16x16x32_bf16 v[152:155], v[120:123], v[92:95], v[152:155]\n\t" \
  "ds_read_b64_tr_b16 v[118:119], v161 offset:38912\n\t" \
  "v_mfma_f32_16x16x32_bf16 v[140:143], v[124:127], v[88:91], v[140:143]\n\t" \
  "ds_read_b64_tr_b16 v[120:121], v162 offset:36864\n\t" \
  "v_mfma_f32_16x16x32_bf16 v[156:159], v[124:127], v[92:95], v[156:159]\n\t" \
  "ds_read_b64_tr_b16 v[122:123], v162 offset:38912\n\t" \
  "s_waitcnt vmcnt(11) lgkmcnt(10)\n\t" \
  "v_mfma_f32_16x16x32_bf16 v[128:131], v[96:99], v[0:3], v[128:131]\n\t" \
  "ds_read_b64_tr_b16 v[124:125], v163 offset:36864\n\t" \
  "buffer_load_dwordx4 v[52:55], v164, %[ob], s79 offen offset:1024 sc1\n\t" \
  "v_mfma_f32_16x16x32_bf16 v[144:147], v[96:99], v[4:7], v[144:147]\n\t" \
  "ds_read_b64_tr_b16 v[126:127], v163 offset:38912\n\t" \
  "v_mfma_f32_16x16x32_bf16 v[132:135], v[100:103], v[0:3], v[132:135]\n\t" \
  "ds_read_b64_tr_b16 v[96:97], v160 offset:40960\n\t" \
  "v_mfma_f32_16x16x32_bf16 v[148:151], v[100:103], v[4:7], v[148:151]\n\t" \
  "ds_read_b64_tr_b16 v[98:99], v160 offset:43008\n\t" \
  "s_waitcnt lgkmcnt(10)\n\t" \
  "v_mfma_f32_16x16x32_bf16 v[136:139], v[104:107], v[0:3], v[136:139]\n\t" \
  "ds_read_b64_tr_b16 v[100:101], v161 offset:40960\n\t" \
  "buffer_load_dwordx4 v[56:59], v164, %[ob], s79 offen offset:2048 sc1\n\t" \
  "v_mfma_f32_16x16x32_bf16 v[152:155], v[104:107], v[4:7], v[152:155]\n\t" \
  "ds_read_b64_tr_b16 v[102:103], v161 offset:43008\n\t" \
  "v_mfma_f32_16x16x32_bf16 v[140:143], v[108:111], v[0:3], v[140:143]\n\t" \
  "ds_read_b64_tr_b16 v[104:105], v162 offset:40960\n\t" \
  "v_mfma_f32_16x16x32_bf16 v[156:159], v[108:111], v[4:7], v[156:159]\n\t" \
  "ds_read_b64_tr_b16 v[106:107], v162 offset:43008\n\t" \
  "s_waitcnt vmcnt(11) lgkmcnt(10)\n\t" \
  "v_mfma_f32_16x16x32_bf16 v[128:131], v[112:115], v[8:11], v[128:131]\n\t" \
  "ds_read_b64_tr_b16 v[108:109], v163 offset:40960\n\t" \
  "buffer_load_dwordx4 v[60:63], v164, %[ob], s79 offen offset:3072 sc1\n\t" \
  "v_mfma_f32_16x16x32_bf16 v[144:147], v[112:115], v[12:15], v[144:147]\n\t" \
  "ds_read_b64_tr_b16 v[110:111], v163 offset:43008\n\t" \
  "v_mfma_f32_16x16x32_bf16 v[132:135], v[116:119], v[8:11], v[132:135]\n\t" \
  "ds_read_b64_tr_b16 v[112:113], v160 offset:45056\n\t" \
  "v_mfma_f32_16x16x32_bf16 v[148:151], v[116:119], v[12:15], v[148:151]\n\t" \
  "ds_read_b64_tr_b16 v[114:115], v160 offset:47104\n\t" \
  "s_waitcnt lgkmcnt(10)\n\t" \
  "v_mfma_f32_16x16x32_bf16 v[136:139], v[120:123], v[8:11], v[136:139]\n\t" \
  "ds_read_b64_tr_b16 v[116:117], v161 offset:45056\n\t" \
  "v_mfma_f32_16x16x32_bf16 v[152:155], v[120:123], v[12:15], v[152:155]\n\t" \
  "ds_read_b64_tr_b16 v[118:119], v161 offset:47104\n\t" \
  "v_mfma_f32_16x16x32_bf16 v[140:143], v[124:127], v[8:11], v[140:143]\n\t" \
  "ds_read_b64_tr_b16 v[120:121], v162 offset:45056\n\t" \
  "v_mfma_f32_16x16x32_bf16 v[156:159], v[124:127], v[12:15], v[156:159]\n\t" \
  "ds_read_b64_tr_b16 v[122:123], v162 offset:47104\n\t" \
  "s_waitcnt vmcnt(10) lgkmcnt(10)\n\t" \
  "v_mfma_f32_16x16x32_bf16 v[128:131], v[96:99], v[16:19], v[128:131]\n\t" \
  "ds_read_b64_tr_b16 v[124:125], v163 offset:45056\n\t" \
  "v_mfma_f32_16x16x32_bf16 v[144:147], v[96:99], v[20:23], v[144:147]\n\t" \
  "ds_read_b64_tr_b16 v[126:127], v163 offset:47104\n\t" \
  "v_mfma_f32_16x16x32_bf16 v[132:135], v[100:103], v[16:19], v[132:135]\n\t" \
  "ds_read_b64_tr_b16 v[96:97], v160 offset:49152\n\t" \
  "v_mfma_f32_16x16x32_bf16 v[148:151], v[100:103], v[20:23], v[148:151]\n\t" \
  "ds_read_b64_tr_b16 v[98:99], v160 offset:51200\n\t" \
  "s_waitcnt lgkmcnt(10)\n\t" \
  "v_mfma_f32_16x16x32_bf16 v[136:139], v[104:107], v[16:19], v[136:139]\n\t" \
  "ds_read_b64_tr_b16 v[100:101], v161 offset:49152\n\t" \
  "v_xor_b32 v0, 32, v167\n\t" \
  "v_mfma_f32_16x16x32_bf16 v[152:155], v[104:107], v[20:23], v[152:155]\n\t" \
  "ds_read_b64_tr_b16 v[102:103], v161 offset:51200\n\t" \
  "v_xor_b32 v1, 64, v167\n\t" \
  "v_mfma_f32_16x16x32_bf16 v[140:143], v[108:111], v[16:19], v[140:143]\n\t" \
  "ds_read_b64_tr_b16 v[104:105], v162 offset:49152\n\t" \
  "v_xor_b32 v2, 0x60, v167\n\t" \
  "v_mfma_f32_16x16x32_bf16 v[156:159], v[108:111], v[20:23], v[156:159]\n\t" \
  "ds_read_b64_tr_b16 v[106:107], v162 offset:51200\n\t" \
  "s_waitcnt vmcnt(8) lgkmcnt(10)\n\t" \
  "v_mfma_f32_16x16x32_bf16 v[128:131], v[112:115], v[24:27], v[128:131]\n\t" \
  "ds_read_b64_tr_b16 v[108:109], v163 offset:49152\n\t" \
  "v_bfe_u32 v5, v164, 7, 3\n\t" \
  "v_mfma_f32_16x16x32_bf16 v[144:147], v[112:115], v[28:31], v[144:147]\n\t" \
  "ds_read_b64_tr_b16 v[110:111], v163 offset:51200\n\t" \
  "v_and_b32 v4, 0x70, v164\n\t" \
  "v_mfma_f32_16x16x32_bf16 v[132:135], v[116:119], v[24:27], v[132:135]\n\t" \
  "ds_read_b64_tr_b16 v[112:113], v160 offset:53248\n\t" \
  "v_lshl_or_b32 v4, v5, 12, v4\n\t" \
  "v_mfma_f32_16x16x32_bf16 v[148:151], v[116:119], v[28:31], v[148:151]\n\t" \
  "ds_read_b64_tr_b16 v[114:115], v160 offset:55296\n\t" \
  "s_waitcnt lgkmcnt(10)\n\t" \
  "v_mfma_f32_16x16x32_bf16 v[136:139], v[120:123], v[24:27], v[136:139]\n\t" \
  "ds_read_b64_tr_b16 v[116:117], v161 offset:53248\n\t" \
  "v_add_u32 v5, 0x8000, v4\n\t" \
  "v_mfma_f32_16x16x32_bf16 v[152:155], v[120:123], v[28:31], v[152:155]\n\t" \
  "ds_read_b64_tr_b16 v[118:119], v161 offset:55296\n\t" \
  "v_add_u32 v6, 0x10000, v4\n\t" \
  "v_mfma_f32_16x16x32_bf16 v[140:143], v[124:127], v[24:27], v[140:143]\n\t" \
  "ds_read_b64_tr_b16 v[120:121], v162 offset:53248\n\t" \
  "v_add_u32 v7, 0x18000, v4\n\t" \
  "v_mfma_f32_16x16x32_bf16 v[156:159], v[124:127], v[28:31], v[156:159]\n\t" \
  "ds_read_b64_tr_b16 v[122:123], v162 offset:55296\n\t" \
  "s_waitcnt vmcnt(6) lgkmcnt(10)\n\t" \
  "v_mfma_f32_16x16x32_bf16 v[128:131], v[96:99], v[32:35], v[128:131]\n\t" \
  "ds_read_b64_tr_b16 v[124:125], v163 offset:53248\n\t" \
  "v_mfma_f32_16x16x32_bf16 v[144:147], v[96:99], v[36:39], v[144:147]\n\t" \
  "ds_read_b64_tr_b16 v[126:127], v163 offset:55296\n\t" \
  "v_mfma_f32_16x16x32_bf16 v[132:135], v[100:103], v[32:35], v[132:135]\n\t" \
  "ds_read_b64_tr_b16 v[96:97], v160 offset:57344\n\t" \
  "v_mfma_f32_16x16x32_bf16 v[148:151], v[100:103], v[36:39], v[148:151]\n\t" \
  "ds_read_b64_tr_b16 v[98:99], v160 offset:59392\n\t" \
  "s_waitcnt lgkmcnt(10)\n\t" \
  "v_mfma_f32_16x16x32_bf16 v[136:139], v[104:107], v[32:35], v[136:139]\n\t" \
  "ds_read_b64_tr_b16 v[100:101], v161 offset:57344\n\t" \
  "v_mfma_f32_16x16x32_bf16 v[152:155], v[104:107], v[36:39], v[152:155]\n\t" \
  "ds_read_b64_tr_b16 v[102:103], v161 offset:59392\n\t" \
  "v_mfma_f32_16x16x32_bf16 v[140:143], v[108:111], v[32:35], v[140:143]\n\t" \
  "ds_read_b64_tr_b16 v[104:105], v162 offset:57344\n\t" \
  "v_mfma_f32_16x16x32_bf16 v[156:159], v[108:111], v[36:39], v[156:159]\n\t" \
  "ds_read_b64_tr_b16 v[106:107], v162 offset:59392\n\t" \
  "s_waitcnt vmcnt(4) lgkmcnt(10)\n\t" \
  "v_mfma_f32_16x16x32_bf16 v[128:131], v[112:115], v[40:43], v[128:131]\n\t" \
  "ds_read_b64_tr_b16 v[108:109], v163 offset:57344\n\t" \
  "v_mfma_f32_16x16x32_bf16 v[144:147], v[112:115], v[44:47], v[144:147]\n\t" \
  "ds_read_b64_tr_b16 v[110:111], v163 offset:59392\n\t" \
  "v_mfma_f32_16x16x32_bf16 v[132:135], v[116:119], v[40:43], v[132:135]\n\t" \
  "ds_read_b64_tr_b16 v[112:113], v160 offset:61440\n\t" \
  "v_mfma_f32_16x16x32_bf16 v[148:151], v[116:119], v[44:47], v[148:151]\n\t" \
  "ds_read_b64_tr_b16 v[114:115], v160 offset:63488\n\t" \
  "s_waitcnt lgkmcnt(10)\n\t" \
  "v_mfma_f32_16x16x32_bf16 v[136:139], v[120:123], v[40:43], v[136:139]\n\t" \
  "ds_read_b64_tr_b16 v[116:117], v161 offset:61440\n\t" \
  "v_mfma_f32_16x16x32_bf16 v[152:155], v[120:123], v[44:47], v[152:155]\n\t" \
  "ds_read_b64_tr_b16 v[118:119], v161 offset:63488\n\t" \
  "v_mfma_f32_16x16x32_bf16 v[140:143], v[124:127], v[40:43], v[140:143]\n\t" \
  "ds_read_b64_tr_b16 v[120:121], v162 offset:61440\n\t" \
  "v_mfma_f32_16x16x32_bf16 v[156:159], v[124:127], v[44:47], v[156:159]\n\t" \
  "ds_read_b64_tr_b16 v[122:123], v162 offset:63488\n\t" \
  "s_waitcnt vmcnt(0) lgkmcnt(10)\n\t" \
  "v_mfma_f32_16x16x32_bf16 v[128:131], v[96:99], v[48:51], v[128:131]\n\t" \
  "ds_read_b64_tr_b16 v[124:125], v163 offset:61440\n\t" \
  "v_mfma_f32_16x16x32_bf16 v[132:135], v[100:103], v[48:51], v[132:135]\n\t" \
  "ds_read_b64_tr_b16 v[126:127], v163 offset:63488\n\t" \
  "s_waitcnt lgkmcnt(6)\n\t" \
  "v_mfma_f32_16x16x32_bf16 v[136:139], v[104:107], v[48:51], v[136:139]\n\t" \
  "v_mfma_f32_16x16x32_bf16 v[140:143], v[108:111], v[48:51], v[140:143]\n\t" \
  "v_mfma_f32_16x16x32_bf16 v[128:131], v[112:115], v[56:59], v[128:131]\n\t" \
  "s_waitcnt lgkmcnt(2)\n\t" \
  "v_mfma_f32_16x16x32_bf16 v[132:135], v[116:119], v[56:59], v[132:135]\n\t" \
  "v_mfma_f32_16x16x32_bf16 v[136:139], v[120:123], v[56:59], v[136:139]\n\t" \
  "v_mfma_f32_16x16x32_bf16 v[144:147], v[96:99], v[52:55], v[144:147]\n\t" \
  "s_waitcnt lgkmcnt(0)\n\t" \
  "v_mfma_f32_16x16x32_bf16 v[140:143], v[124:127], v[56:59], v[140:143]\n\t" \
  "v_mfma_f32_16x16x32_bf16 v[144:147], v[112:115], v[60:63], v[144:147]\n\t" \
  "s_nop 0\n\t" \
  "v_cvt_pk_bf16_f32 v16, v128, v129\n\t" \
  "v_mfma_f32_16x16x32_bf16 v[148:151], v[100:103], v[52:55], v[148:151]\n\t" \
  "v_cvt_pk_bf16_f32 v17, v130, v131\n\t" \
  "v_cvt_pk_bf16_f32 v18, v132, v133\n\t" \
  "v_mfma_f32_16x16x32_bf16 v[148:151], v[116:119], v[60:63], v[148:151]\n\t" \
  "v_cvt_pk_bf16_f32 v19, v134, v135\n\t" \
  "ds_write_b64 v167, v[16:17]\n\t" \
  "v_mfma_f32_16x16x32_bf16 v[152:155], v[104:107], v[52:55], v[152:155]\n\t" \
  "v_cvt_pk_bf16_f32 v20, v136, v137\n\t" \
  "ds_write_b64 v0, v[18:19]\n\t" \
  "v_mfma_f32_16x16x32_bf16 v[152:155], v[120:123], v[60:63], v[152:155]\n\t" \
  "v_cvt_pk_bf16_f32 v21, v138, v139\n\t" \
  "v_cvt_pk_bf16_f32 v22, v140, v141\n\t" \
  "v_mfma_f32_16x16x32_bf16 v[156:159], v[108:111], v[52:55], v[156:159]\n\t" \
  "v_cvt_pk_bf16_f32 v23, v142, v143\n\t" \
  "ds_write_b64 v1, v[20:21]\n\t" \
  "v_mfma_f32_16x16x32_bf16 v[156:159], v[124:127], v[60:63], v[156:159]\n\t" \
  "v_cvt_pk_bf16_f32 v24, v144, v145\n\t" \
  "ds_write_b64 v2, v[22:23]\n\t" \
  "v_cvt_pk_bf16_f32 v25, v146, v147\n\t" \
  "ds_read_b128 v[64:67], v166 offset:0\n\t" \
  "v_cvt_pk_bf16_f32 v26, v148, v149\n\t" \
  "ds_read_b128 v[68:71], v166 offset:1024\n\t" \
  "v_cvt_pk_bf16_f32 v27, v150, v151\n\t" \
  "ds_write_b64 v167, v[24:25]\n\t" \
  "v_cvt_pk_bf16_f32 v28, v152, v153\n\t" \
  "v_cvt_pk_bf16_f32 v29, v154, v155\n\t" \
  "ds_write_b64 v0, v[26:27]\n\t" \
  "v_cvt_pk_bf16_f32 v30, v156, v157\n\t" \
  "v_cvt_pk_bf16_f32 v31, v158, v159\n\t" \
  "ds_write_b64 v1, v[28:29]\n\t" \
  "ds_write_b64 v2, v[30:31]\n\t" \
  "ds_read_b128 v[72:75], v166 offset:0\n\t" \
  "ds_read_b128 v[76:79], v166 offset:1024\n\t" \
  "s_waitcnt lgkmcnt(7)\n\t" \
  "global_store_dwordx4 v4, v[64:67], %[op] sc1\n\t" \
  "s_waitcnt lgkmcnt(6)\n\t" \
  "global_store_dwordx4 v5, v[68:71], %[op] sc1\n\t" \
  "s_waitcnt lgkmcnt(1)\n\t" \
  "global_store_dwordx4 v6, v[72:75], %[op] sc1\n\t" \
  "s_waitcnt lgkmcnt(0)\n\t" \
  "global_store_dwordx4 v7, v[76:79], %[op] sc1\n\t" \
  "s_nop 1\n\t"
#define G3_EP 2
#define G3_CLOB "v0", "v1", "v2", "v3", "v4", "v5", "v6", "v7", "v8", "v9", "v10", "v11", "v12", "v13", "v14", "v15", "v16", "v17", "v18", "v19", "v20", "v21", "v22", "v23", "v24", "v25", "v26", "v27", "v28", "v29", "v30", "v31", "v32", "v33", "v34", "v35", "v36", "v37", "v38", "v39", "v40", "v41", "v42", "v43", "v44", "v45", "v46", "v47", "v48", "v49", "v50", "v51", "v52", "v53", "v54", "v55", "v56", "v57", "v58", "v59", "v60", "v61", "v62", "v63", "v64", "v65", "v66", "v67", "v68", "v69", "v70", "v71", "v72", "v73", "v74", "v75", "v76", "v77", "v78", "v79", "v80", "v81", "v82", "v83", "v84", "v85", "v86", "v87", "v88", "v89", "v90", "v91", "v92", "v93", "v94", "v95", "v97", "v98", "v99", "v100", "v101", "v102", "v103", "v104", "v105", "v106", "v107", "v108", "v109", "v110", "v111", "v112", "v113", "v114", "v115", "v116", "v117", "v118", "v119", "v120", "v121", "v122", "v123", "v124", "v125", "v126", "v127", "v128", "v129", "v130", "v131", "v132", "v133", "v134", "v135", "v136", "v137", "v138", "v139", "v140", "v141", "v142", "v143", "v144", "v145", "v146", "v147", "v148", "v149", "v150", "v151", "v152", "v153", "v154", "v155", "v156", "v157", "v158", "v159", "s64", "s65", "s66", "s67", "s68", "s69", "s70", "s71", "s72", "s73", "s74", "s75", "s76", "s77", "s78", "s79"

#define PP_ASM \
  "v_mov_b32 v60, 0\n\t" \
  "s_mov_b32 %[t0], %[n0]\n\t" \
  "s_cmp_eq_u32 %[t0], 0\n\t" \
  "s_cbranch_scc1 PAx_%=\n\t" \
  "PA_%=:\n\t" \
  "s_barrier\n\t" \
  "s_sub_u32 %[t0], %[t0], 1\n\t" \
  "s_cmp_lg_u32 %[t0], 0\n\t" \
  "s_cbranch_scc1 PA_%=\n\t" \
  "PAx_%=:\n\t" \
  "s_mov_b32 m0, %[shd]\n\t" \
  "global_load_dword v160, v60, %[fl] sc1\n\t" \
  "global_load_lds_dwordx4 v163, %[shp]\n\t" \
  "s_mov_b32 m0, %[tgd]\n\t" \
  "s_mov_b64 exec, %[em]\n\t" \
  "global_load_lds_dword v60, %[fl] sc1\n\t" \
  "s_mov_b64 exec, -1\n\t" \
  "global_load_ushort v161, v60, %[hp]\n\t" \
  "global_load_ushort v162, v60, %[dp]\n\t" \
  "global_load_dwordx4 v[0:3], v152, %[kb0]\n\t" \
  "global_load_dwordx4 v[4:7], v153, %[kb0]\n\t" \
  "global_load_dwordx4 v[8:11], v154, %[kb0]\n\t" \
  "global_load_dwordx4 v[12:15], v155, %[kb3] offset:0\n\t" \
  "global_load_dwordx4 v[28:31], v155, %[vb0] offset:0\n\t" \
  "s_setprio 2\n\t" \
  "s_setprio 3\n\t" \
  "s_barrier\n\t" \
  "global_load_dwordx4 v[16:19], v155, %[kb3] offset:2048\n\t" \
  "global_load_dwordx4 v[32:35], v155, %[vb0] offset:2048\n\t" \
  "global_load_dwordx4 v[20:23], v155, %[kb5] offset:0\n\t" \
  "s_setprio 2\n\t" \
  "s_mov_b32 s85, 0x3c0000\n\t" \
  "s_load_dword s84, %[ph], s85\n\t" \
  "s_mov_b32 s85, 0x3c0800\n\t" \
  "s_load_dword s84, %[ph], s85\n\t" \
  "s_mov_b32 s85, 0x3c1000\n\t" \
  "s_load_dword s84, %[ph], s85\n\t" \
  "s_setprio 3\n\t" \
  "s_barrier\n\t" \
  "global_load_dwordx4 v[36:39], v155, %[vb2] offset:0\n\t" \
  "global_load_dwordx4 v[24:27], v155, %[kb5] offset:2048\n\t" \
  "global_load_dwordx4 v[40:43], v155, %[vb2] offset:2048\n\t" \
  "s_setprio 2\n\t" \
  "s_waitcnt vmcnt(11)\n\t" \
  "ds_read_b128 v[128:131], v156 offset:0\n\t" \
  "ds_read_b128 v[132:135], v156 offset:16\n\t" \
  "ds_read_b128 v[136:139], v156 offset:32\n\t" \
  "ds_read_b128 v[140:143], v156 offset:48\n\t" \
  "s_waitcnt lgkmcnt(0)\n\t" \
  "v_lshlrev_b32 v64, 16, v128\n\t" \
  "v_and_b32 v65, 0xffff0000, v128\n\t" \
  "v_lshlrev_b32 v66, 16, v129\n\t" \
  "v_and_b32 v67, 0xffff0000, v129\n\t" \
  "v_lshlrev_b32 v68, 16, v130\n\t" \
  "v_and_b32 v69, 0xffff0000, v130\n\t" \
  "v_lshlrev_b32 v70, 16, v131\n\t" \
  "v_and_b32 v71, 0xffff0000, v131\n\t" \
  "v_lshlrev_b32 v72, 16, v132\n\t" \
  "v_and_b32 v73, 0xffff0000, v132\n\t" \
  "v_lshlrev_b32 v74, 16, v133\n\t" \
  "v_and_b32 v75, 0xffff0000, v133\n\t" \
  "v_lshlrev_b32 v76, 16, v134\n\t" \
  "v_and_b32 v77, 0xffff0000, v134\n\t" \
  "v_lshlrev_b32 v78, 16, v135\n\t" \
  "v_and_b32 v79, 0xffff0000, v135\n\t" \
  "v_lshlrev_b32 v80, 16, v136\n\t" \
  "v_and_b32 v81, 0xffff0000, v136\n\t" \
  "v_lshlrev_b32 v82, 16, v137\n\t" \
  "v_and_b32 v83, 0xffff0000, v137\n\t" \
  "v_lshlrev_b32 v84, 16, v138\n\t" \
  "v_and_b32 v85, 0xffff0000, v138\n\t" \
  "v_lshlrev_b32 v86, 16, v139\n\t" \
  "v_and_b32 v87, 0xffff0000, v139\n\t" \
  "v_lshlrev_b32 v88, 16, v140\n\t" \
  "v_and_b32 v89, 0xffff0000, v140\n\t" \
  "v_lshlrev_b32 v90, 16, v141\n\t" \
  "v_and_b32 v91, 0xffff0000, v141\n\t" \
  "v_lshlrev_b32 v92, 16, v142\n\t" \
  "v_and_b32 v93, 0xffff0000, v142\n\t" \
  "v_lshlrev_b32 v94, 16, v143\n\t" \
  "v_and_b32 v95, 0xffff0000, v143\n\t" \
  "s_waitcnt vmcnt(10)\n\t" \
  "s_memrealtime %[st1]\n\t" \
  "s_mov_b64 exec, %[zm]\n\t" \
  "v_mov_b32 v0, 0\n\t" \
  "v_mov_b32 v1, 0\n\t" \
  "v_mov_b32 v2, 0\n\t" \
  "v_mov_b32 v3, 0\n\t" \
  "s_mov_b64 exec, -1\n\t" \
  "v_lshlrev_b32 v128, 16, v0\n\t" \
  "v_and_b32 v129, 0xffff0000, v0\n\t" \
  "v_lshlrev_b32 v130, 16, v1\n\t" \
  "v_and_b32 v131, 0xffff0000, v1\n\t" \
  "v_lshlrev_b32 v132, 16, v2\n\t" \
  "v_and_b32 v133, 0xffff0000, v2\n\t" \
  "v_lshlrev_b32 v134, 16, v3\n\t" \
  "v_and_b32 v135, 0xffff0000, v3\n\t" \
  "v_mul_f32 v96, v128, v67\n\t" \
  "v_mul_f32 v97, v129, v71\n\t" \
  "v_mul_f32 v98, v130, v75\n\t" \
  "v_mul_f32 v99, v131, v79\n\t" \
  "v_mul_f32 v100, v132, v83\n\t" \
  "v_mul_f32 v101, v133, v87\n\t" \
  "v_mul_f32 v102, v134, v91\n\t" \
  "v_mul_f32 v103, v135, v95\n\t" \
  "s_waitcnt vmcnt(9)\n\t" \
  "s_mov_b64 exec, %[zm]\n\t" \
  "v_mov_b32 v4, 0\n\t" \
  "v_mov_b32 v5, 0\n\t" \
  "v_mov_b32 v6, 0\n\t" \
  "v_mov_b32 v7, 0\n\t" \
  "s_mov_b64 exec, -1\n\t" \
  "v_lshlrev_b32 v128, 16, v4\n\t" \
  "v_and_b32 v129, 0xffff0000, v4\n\t" \
  "v_lshlrev_b32 v130, 16, v5\n\t" \
  "v_and_b32 v131, 0xffff0000, v5\n\t" \
  "v_lshlrev_b32 v132, 16, v6\n\t" \
  "v_and_b32 v133, 0xffff0000, v6\n\t" \
  "v_lshlrev_b32 v134, 16, v7\n\t" \
  "v_and_b32 v135, 0xffff0000, v7\n\t" \
  "v_fmac_f32 v96, v128, v66\n\t" \
  "v_fmac_f32 v97, v129, v70\n\t" \
  "v_fmac_f32 v98, v130, v74\n\t" \
  "v_fmac_f32 v99, v131, v78\n\t" \
  "v_fmac_f32 v100, v132, v82\n\t" \
  "v_fmac_f32 v101, v133, v86\n\t" \
  "v_fmac_f32 v102, v134, v90\n\t" \
  "v_fmac_f32 v103, v135, v94\n\t" \
  "v_mul_f32 v104, v128, v67\n\t" \
  "v_mul_f32 v105, v129, v71\n\t" \
  "v_mul_f32 v106, v130, v75\n\t" \
  "v_mul_f32 v107, v131, v79\n\t" \
  "v_mul_f32 v108, v132, v83\n\t" \
  "v_mul_f32 v109, v133, v87\n\t" \
  "v_mul_f32 v110, v134, v91\n\t" \
  "v_mul_f32 v111, v135, v95\n\t" \
  "s_waitcnt vmcnt(8)\n\t" \
  "s_mov_b64 exec, %[zm]\n\t" \
  "v_mov_b32 v8, 0\n\t" \
  "v_mov_b32 v9, 0\n\t" \
  "v_mov_b32 v10, 0\n\t" \
  "v_mov_b32 v11, 0\n\t" \
  "s_mov_b64 exec, -1\n\t" \
  "v_lshlrev_b32 v128, 16, v8\n\t" \
  "v_and_b32 v129, 0xffff0000, v8\n\t" \
  "v_lshlrev_b32 v130, 16, v9\n\t" \
  "v_and_b32 v131, 0xffff0000, v9\n\t" \
  "v_lshlrev_b32 v132, 16, v10\n\t" \
  "v_and_b32 v133, 0xffff0000, v10\n\t" \
  "v_lshlrev_b32 v134, 16, v11\n\t" \
  "v_and_b32 v135, 0xffff0000, v11\n\t" \
  "v_fmac_f32 v96, v128, v65\n\t" \
  "v_fmac_f32 v97, v129, v69\n\t" \
  "v_fmac_f32 v98, v130, v73\n\t" \
  "v_fmac_f32 v99, v131, v77\n\t" \
  "v_fmac_f32 v100, v132, v81\n\t" \
  "v_fmac_f32 v101, v133, v85\n\t" \
  "v_fmac_f32 v102, v134, v89\n\t" \
  "v_fmac_f32 v103, v135, v93\n\t" \
  "v_fmac_f32 v104, v128, v66\n\t" \
  "v_fmac_f32 v105, v129, v70\n\t" \
  "v_fmac_f32 v106, v130, v74\n\t" \
  "v_fmac_f32 v107, v131, v78\n\t" \
  "s_mov_b32 s85, 0x420000\n\t" \
  "s_load_dword s84, %[ph], s85\n\t" \
  "s_mov_b32 s85, 0x420800\n\t" \
  "s_load_dword s84, %[ph], s85\n\t" \
  "s_mov_b32 s85, 0x421000\n\t" \
  "s_load_dword s84, %[ph], s85\n\t" \
  "s_barrier\n\t" \
  "v_fmac_f32 v108, v132, v82\n\t" \
  "v_fmac_f32 v109, v133, v86\n\t" \
  "v_fmac_f32 v110, v134, v90\n\t" \
  "v_fmac_f32 v111, v135, v94\n\t" \
  "v_mul_f32 v112, v128, v67\n\t" \
  "v_mul_f32 v113, v129, v71\n\t" \
  "v_mul_f32 v114, v130, v75\n\t" \
  "v_mul_f32 v115, v131, v79\n\t" \
  "v_mul_f32 v116, v132, v83\n\t" \
  "v_mul_f32 v117, v133, v87\n\t" \
  "v_mul_f32 v118, v134, v91\n\t" \
  "v_mul_f32 v119, v135, v95\n\t" \
  "s_waitcnt vmcnt(7)\n\t" \
  "v_lshlrev_b32 v128, 16, v12\n\t" \
  "v_and_b32 v129, 0xffff0000, v12\n\t" \
  "v_lshlrev_b32 v130, 16, v13\n\t" \
  "v_and_b32 v131, 0xffff0000, v13\n\t" \
  "v_lshlrev_b32 v132, 16, v14\n\t" \
  "v_and_b32 v133, 0xffff0000, v14\n\t" \
  "v_lshlrev_b32 v134, 16, v15\n\t" \
  "v_and_b32 v135, 0xffff0000, v15\n\t" \
  "v_fmac_f32 v96, v128, v64\n\t" \
  "v_fmac_f32 v97, v129, v68\n\t" \
  "v_fmac_f32 v98, v130, v72\n\t" \
  "v_fmac_f32 v99, v131, v76\n\t" \
  "v_fmac_f32 v100, v132, v80\n\t" \
  "v_fmac_f32 v101, v133, v84\n\t" \
  "v_fmac_f32 v102, v134, v88\n\t" \
  "v_fmac_f32 v103, v135, v92\n\t" \
  "v_fmac_f32 v104, v128, v65\n\t" \
  "v_fmac_f32 v105, v129, v69\n\t" \
  "v_fmac_f32 v106, v130, v73\n\t" \
  "v_fmac_f32 v107, v131, v77\n\t" \
  "v_fmac_f32 v108, v132, v81\n\t" \
  "v_fmac_f32 v109, v133, v85\n\t" \
  "v_fmac_f32 v110, v134, v89\n\t" \
  "v_fmac_f32 v111, v135, v93\n\t" \
  "v_fmac_f32 v112, v128, v66\n\t" \
  "v_fmac_f32 v113, v129, v70\n\t" \
  "v_fmac_f32 v114, v130, v74\n\t" \
  "v_fmac_f32 v115, v131, v78\n\t" \
  "v_fmac_f32 v116, v132, v82\n\t" \
  "v_fmac_f32 v117, v133, v86\n\t" \
  "v_fmac_f32 v118, v134, v90\n\t" \
  "v_fmac_f32 v119, v135, v94\n\t" \
  "v_mul_f32 v120, v128, v67\n\t" \
  "v_mul_f32 v121, v129, v71\n\t" \
  "v_mul_f32 v122, v130, v75\n\t" \
  "v_mul_f32 v123, v131, v79\n\t" \
  "v_mul_f32 v124, v132, v83\n\t" \
  "v_mul_f32 v125, v133, v87\n\t" \
  "v_mul_f32 v126, v134, v91\n\t" \
  "v_mul_f32 v127, v135, v95\n\t" \
  "s_waitcnt vmcnt(6)\n\t" \
  "v_lshlrev_b32 v128, 16, v28\n\t" \
  "v_and_b32 v129, 0xffff0000, v28\n\t" \
  "v_lshlrev_b32 v130, 16, v29\n\t" \
  "v_and_b32 v131, 0xffff0000, v29\n\t" \
  "v_lshlrev_b32 v132, 16, v30\n\t" \
  "v_and_b32 v133, 0xffff0000, v30\n\t" \
  "v_lshlrev_b32 v134, 16, v31\n\t" \
  "v_and_b32 v135, 0xffff0000, v31\n\t" \
  "v_mul_f32 v96, v96, v128\n\t" \
  "v_mul_f32 v97, v97, v129\n\t" \
  "v_mul_f32 v98, v98, v130\n\t" \
  "v_mul_f32 v99, v99, v131\n\t" \
  "v_mul_f32 v100, v100, v132\n\t" \
  "v_mul_f32 v101, v101, v133\n\t" \
  "v_mul_f32 v102, v102, v134\n\t" \
  "v_mul_f32 v103, v103, v135\n\t" \
  "v_cvt_pk_bf16_f32 v136, v96, v97\n\t" \
  "v_cvt_pk_bf16_f32 v137, v98, v99\n\t" \
  "v_cvt_pk_bf16_f32 v138, v100, v101\n\t" \
  "v_cvt_pk_bf16_f32 v139, v102, v103\n\t" \
  "v_mov_b32_dpp v128, v136 row_ror:8 row_mask:0xf bank_mask:0xf\n\t" \
  "v_mov_b32_dpp v129, v137 row_ror:8 row_mask:0xf bank_mask:0xf\n\t" \
  "v_mov_b32_dpp v130, v138 row_ror:8 row_mask:0xf bank_mask:0xf\n\t" \
  "v_mov_b32_dpp v131, v139 row_ror:8 row_mask:0xf bank_mask:0xf\n\t" \
  "v_perm_b32 v136, v128, v136, v157\n\t" \
  "v_perm_b32 v137, v129, v137, v157\n\t" \
  "v_perm_b32 v138, v130, v138, v157\n\t" \
  "v_perm_b32 v139, v131, v139, v157\n\t" \
  "v_permlane16_swap_b32 v136, v137\n\t" \
  "s_nop 0\n\t" \
  "v_permlane16_swap_b32 v138, v139\n\t" \
  "s_nop 1\n\t" \
  "v_permlane32_swap_b32 v136, v138\n\t" \
  "v_permlane32_swap_b32 v137, v139\n\t" \
  "s_waitcnt vmcnt(5)\n\t" \
  "v_lshlrev_b32 v128, 16, v16\n\t" \
  "v_and_b32 v129, 0xffff0000, v16\n\t" \
  "v_lshlrev_b32 v130, 16, v17\n\t" \
  "v_and_b32 v131, 0xffff0000, v17\n\t" \
  "v_lshlrev_b32 v132, 16, v18\n\t" \
  "v_and_b32 v133, 0xffff0000, v18\n\t" \
  "v_lshlrev_b32 v134, 16, v19\n\t" \
  "v_and_b32 v135, 0xffff0000, v19\n\t" \
  "v_fmac_f32 v104, v128, v64\n\t" \
  "v_fmac_f32 v105, v129, v68\n\t" \
  "v_fmac_f32 v106, v130, v72\n\t" \
  "v_fmac_f32 v107, v131, v76\n\t" \
  "v_fmac_f32 v108, v132, v80\n\t" \
  "v_fmac_f32 v109, v133, v84\n\t" \
  "s_mov_b32 s85, 0x480000\n\t" \
  "s_load_dword s84, %[ph], s85\n\t" \
  "s_mov_b32 s85, 0x480800\n\t" \
  "s_load_dword s84, %[ph], s85\n\t" \
  "s_mov_b32 s85, 0x481000\n\t" \
  "s_load_dword s84, %[ph], s85\n\t" \
  "s_setprio 3\n\t" \
  "s_barrier\n\t" \
  "global_store_dwordx4 v159, v[136:139], %[sb] sc1\n\t" \
  "v_add_u32 v158, 0x80, v159\n\t" \
  "v_bfi_b32 v159, %[mk], v158, v159\n\t" \
  "s_setprio 2\n\t" \
  "v_fmac_f32 v110, v134, v88\n\t" \
  "v_fmac_f32 v111, v135, v92\n\t" \
  "v_fmac_f32 v112, v128, v65\n\t" \
  "v_fmac_f32 v113, v129, v69\n\t" \
  "v_fmac_f32 v114, v130, v73\n\t" \
  "v_fmac_f32 v115, v131, v77\n\t" \
  "v_fmac_f32 v116, v132, v81\n\t" \
  "v_fmac_f32 v117, v133, v85\n\t" \
  "v_fmac_f32 v118, v134, v89\n\t" \
  "v_fmac_f32 v119, v135, v93\n\t" \
  "v_fmac_f32 v120, v128, v66\n\t" \
  "v_fmac_f32 v121, v129, v70\n\t" \
  "v_fmac_f32 v122, v130, v74\n\t" \
  "v_fmac_f32 v123, v131, v78\n\t" \
  "v_fmac_f32 v124, v132, v82\n\t" \
  "v_fmac_f32 v125, v133, v86\n\t" \
  "v_fmac_f32 v126, v134, v90\n\t" \
  "v_fmac_f32 v127, v135, v94\n\t" \
  "s_waitcnt vmcnt(5)\n\t" \
  "v_lshlrev_b32 v128, 16, v32\n\t" \
  "v_and_b32 v129, 0xffff0000, v32\n\t" \
  "v_lshlrev_b32 v130, 16, v33\n\t" \
  "v_and_b32 v131, 0xffff0000, v33\n\t" \
  "v_lshlrev_b32 v132, 16, v34\n\t" \
  "v_and_b32 v133, 0xffff0000, v34\n\t" \
  "v_lshlrev_b32 v134, 16, v35\n\t" \
  "v_and_b32 v135, 0xffff0000, v35\n\t" \
  "v_mul_f32 v104, v104, v128\n\t" \
  "v_mul_f32 v105, v105, v129\n\t" \
  "v_mul_f32 v106, v106, v130\n\t" \
  "v_mul_f32 v107, v107, v131\n\t" \
  "v_mul_f32 v108, v108, v132\n\t" \
  "v_mul_f32 v109, v109, v133\n\t" \
  "v_mul_f32 v110, v110, v134\n\t" \
  "v_mul_f32 v111, v111, v135\n\t" \
  "v_cvt_pk_bf16_f32 v140, v104, v105\n\t" \
  "v_cvt_pk_bf16_f32 v141, v106, v107\n\t" \
  "v_cvt_pk_bf16_f32 v142, v108, v109\n\t" \
  "v_cvt_pk_bf16_f32 v143, v110, v111\n\t" \
  "v_mov_b32_dpp v128, v140 row_ror:8 row_mask:0xf bank_mask:0xf\n\t" \
  "v_mov_b32_dpp v129, v141 row_ror:8 row_mask:0xf bank_mask:0xf\n\t" \
  "v_mov_b32_dpp v130, v142 row_ror:8 row_mask:0xf bank_mask:0xf\n\t" \
  "v_mov_b32_dpp v131, v143 row_ror:8 row_mask:0xf bank_mask:0xf\n\t" \
  "v_perm_b32 v140, v128, v140, v157\n\t" \
  "v_perm_b32 v141, v129, v141, v157\n\t" \
  "v_perm_b32 v142, v130, v142, v157\n\t" \
  "v_perm_b32 v143, v131, v143, v157\n\t" \
  "v_permlane16_swap_b32 v140, v141\n\t" \
  "s_nop 0\n\t" \
  "v_permlane16_swap_b32 v142, v143\n\t" \
  "s_nop 1\n\t" \
  "v_permlane32_swap_b32 v140, v142\n\t" \
  "v_permlane32_swap_b32 v141, v143\n\t" \
  "s_waitcnt vmcnt(4)\n\t" \
  "v_lshlrev_b32 v128, 16, v20\n\t" \
  "v_and_b32 v129, 0xffff0000, v20\n\t" \
  "v_lshlrev_b32 v130, 16, v21\n\t" \
  "v_and_b32 v131, 0xffff0000, v21\n\t" \
  "v_lshlrev_b32 v132, 16, v22\n\t" \
  "v_and_b32 v133, 0xffff0000, v22\n\t" \
  "v_lshlrev_b32 v134, 16, v23\n\t" \
  "v_and_b32 v135, 0xffff0000, v23\n\t" \
  "v_fmac_f32 v112, v128, v64\n\t" \
  "v_fmac_f32 v113, v129, v68\n\t" \
  "v_fmac_f32 v114, v130, v72\n\t" \
  "v_fmac_f32 v115, v131, v76\n\t" \
  "v_fmac_f32 v116, v132, v80\n\t" \
  "v_fmac_f32 v117, v133, v84\n\t" \
  "v_fmac_f32 v118, v134, v88\n\t" \
  "v_fmac_f32 v119, v135, v92\n\t" \
  "v_fmac_f32 v120, v128, v65\n\t" \
  "v_fmac_f32 v121, v129, v69\n\t" \
  "v_fmac_f32 v122, v130, v73\n\t" \
  "v_fmac_f32 v123, v131, v77\n\t" \
  "v_fmac_f32 v124, v132, v81\n\t" \
  "v_fmac_f32 v125, v133, v85\n\t" \
  "v_fmac_f32 v126, v134, v89\n\t" \
  "v_fmac_f32 v127, v135, v93\n\t" \
  "s_waitcnt vmcnt(3)\n\t" \
  "v_lshlrev_b32 v128, 16, v36\n\t" \
  "v_and_b32 v129, 0xffff0000, v36\n\t" \
  "v_lshlrev_b32 v130, 16, v37\n\t" \
  "v_and_b32 v131, 0xffff0000, v37\n\t" \
  "v_lshlrev_b32 v132, 16, v38\n\t" \
  "v_and_b32 v133, 0xffff0000, v38\n\t" \
  "v_lshlrev_b32 v134, 16, v39\n\t" \
  "v_and_b32 v135, 0xffff0000, v39\n\t" \
  "v_mul_f32 v112, v112, v128\n\t" \
  "v_mul_f32 v113, v113, v129\n\t" \
  "v_mul_f32 v114, v114, v130\n\t" \
  "v_mul_f32 v115, v115, v131\n\t" \
  "v_mul_f32 v116, v116, v132\n\t" \
  "v_mul_f32 v117, v117, v133\n\t" \
  "v_mul_f32 v118, v118, v134\n\t" \
  "v_mul_f32 v119, v119, v135\n\t" \
  "v_cvt_pk_bf16_f32 v144, v112, v113\n\t" \
  "v_cvt_pk_bf16_f32 v145, v114, v115\n\t" \
  "v_cvt_pk_bf16_f32 v146, v116, v117\n\t" \
  "v_cvt_pk_bf16_f32 v147, v118, v119\n\t" \
  "v_mov_b32_dpp v128, v144 row_ror:8 row_mask:0xf bank_mask:0xf\n\t" \
  "v_mov_b32_dpp v129, v145 row_ror:8 row_mask:0xf bank_mask:0xf\n\t" \
  "v_mov_b32_dpp v130, v146 row_ror:8 row_mask:0xf bank_mask:0xf\n\t" \
  "v_mov_b32_dpp v131, v147 row_ror:8 row_mask:0xf bank_mask:0xf\n\t" \
  "s_mov_b32 s85, 0x4e0000\n\t" \
  "s_load_dword s84, %[ph], s85\n\t" \
  "s_mov_b32 s85, 0x4e0800\n\t" \
  "s_load_dword s84, %[ph], s85\n\t" \
  "s_mov_b32 s85, 0x4e1000\n\t" \
  "s_load_dword s84, %[ph], s85\n\t" \
  "s_mov_b32 m0, %[hd]\n\t" \
  "s_nop 0\n\t" \
  "global_load_lds_dwordx4 v163, %[hp] offset:0\n\t" \
  "global_load_lds_dwordx4 v163, %[hp] offset:1024\n\t" \
  "global_load_lds_dwordx4 v163, %[hp] offset:2048\n\t" \
  "global_load_lds_dwordx4 v163, %[hp] offset:3072\n\t" \
  "s_setprio 3\n\t" \
  "s_barrier\n\t" \
  "global_store_dwordx4 v159, v[140:143], %[sb] sc1\n\t" \
  "v_add_u32 v158, 0x80, v159\n\t" \
  "v_bfi_b32 v159, %[mk], v158, v159\n\t" \
  "s_setprio 2\n\t" \
  "v_perm_b32 v144, v128, v144, v157\n\t" \
  "v_perm_b32 v145, v129, v145, v157\n\t" \
  "v_perm_b32 v146, v130, v146, v157\n\t" \
  "v_perm_b32 v147, v131, v147, v157\n\t" \
  "v_permlane16_swap_b32 v144, v145\n\t" \
  "s_nop 0\n\t" \
  "v_permlane16_swap_b32 v146, v147\n\t" \
  "s_nop 1\n\t" \
  "v_permlane32_swap_b32 v144, v146\n\t" \
  "v_permlane32_swap_b32 v145, v147\n\t" \
  "s_waitcnt vmcnt(7)\n\t" \
  "v_lshlrev_b32 v128, 16, v24\n\t" \
  "v_and_b32 v129, 0xffff0000, v24\n\t" \
  "v_lshlrev_b32 v130, 16, v25\n\t" \
  "v_and_b32 v131, 0xffff0000, v25\n\t" \
  "v_lshlrev_b32 v132, 16, v26\n\t" \
  "v_and_b32 v133, 0xffff0000, v26\n\t" \
  "v_lshlrev_b32 v134, 16, v27\n\t" \
  "v_and_b32 v135, 0xffff0000, v27\n\t" \
  "v_fmac_f32 v120, v128, v64\n\t" \
  "v_fmac_f32 v121, v129, v68\n\t" \
  "v_fmac_f32 v122, v130, v72\n\t" \
  "v_fmac_f32 v123, v131, v76\n\t" \
  "v_fmac_f32 v124, v132, v80\n\t" \
  "v_fmac_f32 v125, v133, v84\n\t" \
  "v_fmac_f32 v126, v134, v88\n\t" \
  "v_fmac_f32 v127, v135, v92\n\t" \
  "s_waitcnt vmcnt(6)\n\t" \
  "v_lshlrev_b32 v128, 16, v40\n\t" \
  "v_and_b32 v129, 0xffff0000, v40\n\t" \
  "v_lshlrev_b32 v130, 16, v41\n\t" \
  "v_and_b32 v131, 0xffff0000, v41\n\t" \
  "v_lshlrev_b32 v132, 16, v42\n\t" \
  "v_and_b32 v133, 0xffff0000, v42\n\t" \
  "v_lshlrev_b32 v134, 16, v43\n\t" \
  "v_and_b32 v135, 0xffff0000, v43\n\t" \
  "v_mul_f32 v120, v120, v128\n\t" \
  "v_mul_f32 v121, v121, v129\n\t" \
  "v_mul_f32 v122, v122, v130\n\t" \
  "v_mul_f32 v123, v123, v131\n\t" \
  "v_mul_f32 v124, v124, v132\n\t" \
  "v_mul_f32 v125, v125, v133\n\t" \
  "v_mul_f32 v126, v126, v134\n\t" \
  "v_mul_f32 v127, v127, v135\n\t" \
  "v_cvt_pk_bf16_f32 v148, v120, v121\n\t" \
  "v_cvt_pk_bf16_f32 v149, v122, v123\n\t" \
  "v_cvt_pk_bf16_f32 v150, v124, v125\n\t" \
  "v_cvt_pk_bf16_f32 v151, v126, v127\n\t" \
  "v_mov_b32_dpp v128, v148 row_ror:8 row_mask:0xf bank_mask:0xf\n\t" \
  "v_mov_b32_dpp v129, v149 row_ror:8 row_mask:0xf bank_mask:0xf\n\t" \
  "v_mov_b32_dpp v130, v150 row_ror:8 row_mask:0xf bank_mask:0xf\n\t" \
  "v_mov_b32_dpp v131, v151 row_ror:8 row_mask:0xf bank_mask:0xf\n\t" \
  "v_perm_b32 v148, v128, v148, v157\n\t" \
  "v_perm_b32 v149, v129, v149, v157\n\t" \
  "v_perm_b32 v150, v130, v150, v157\n\t" \
  "v_perm_b32 v151, v131, v151, v157\n\t" \
  "v_permlane16_swap_b32 v148, v149\n\t" \
  "s_nop 0\n\t" \
  "v_permlane16_swap_b32 v150, v151\n\t" \
  "s_nop 1\n\t" \
  "v_permlane32_swap_b32 v148, v150\n\t" \
  "v_permlane32_swap_b32 v149, v151\n\t" \
  "s_mov_b32 s85, 0x540000\n\t" \
  "s_load_dword s84, %[ph], s85\n\t" \
  "s_mov_b32 s85, 0x540800\n\t" \
  "s_load_dword s84, %[ph], s85\n\t" \
  "s_mov_b32 s85, 0x541000\n\t" \
  "s_load_dword s84, %[ph], s85\n\t" \
  "s_setprio 3\n\t" \
  "s_barrier\n\t" \
  "global_store_dwordx4 v159, v[144:147], %[sb] sc1\n\t" \
  "v_add_u32 v158, 0x80, v159\n\t" \
  "v_bfi_b32 v159, %[mk], v158, v159\n\t" \
  "global_store_dwordx4 v159, v[148:151], %[sb] sc1\n\t" \
  "s_setprio 2\n\t" \
  "s_waitcnt vmcnt(3)\n\t" \
  "ds_read_b64_tr_b16 v[64:65], v165 offset:32\n\t" \
  "ds_read_b64_tr_b16 v[66:67], v165 offset:0\n\t" \
  "ds_read_b64_tr_b16 v[68:69], v165 offset:288\n\t" \
  "ds_read_b64_tr_b16 v[70:71], v165 offset:256\n\t" \
  "ds_read_b64_tr_b16 v[72:73], v165 offset:544\n\t" \
  "ds_read_b64_tr_b16 v[74:75], v165 offset:512\n\t" \
  "ds_read_b64_tr_b16 v[76:77], v165 offset:800\n\t" \
  "ds_read_b64_tr_b16 v[78:79], v165 offset:768\n\t" \
  "ds_read_b64_tr_b16 v[80:81], v165 offset:1056\n\t" \
  "ds_read_b64_tr_b16 v[82:83], v165 offset:1024\n\t" \
  "ds_read_b64_tr_b16 v[84:85], v165 offset:1312\n\t" \
  "ds_read_b64_tr_b16 v[86:87], v165 offset:1280\n\t" \
  "ds_read_b64_tr_b16 v[88:89], v165 offset:1568\n\t" \
  "ds_read_b64_tr_b16 v[90:91], v165 offset:1536\n\t" \
  "ds_read_b64_tr_b16 v[92:93], v165 offset:1824\n\t" \
  "ds_read_b64_tr_b16 v[94:95], v165 offset:1792\n\t" \
  "v_mov_b32 v24, v166\n\t" \
  "v_mov_b32 v25, v167\n\t" \
  "v_mov_b32 v63, 0xc0\n\t" \
  "v_add_u32 v158, 64, v24\n\t" \
  "v_bfi_b32 v26, v63, v158, v24\n\t" \
  "v_add_u32 v158, 64, v25\n\t" \
  "v_bfi_b32 v27, v63, v158, v25\n\t" \
  "v_add_u32 v158, 64, v26\n\t" \
  "v_bfi_b32 v28, v63, v158, v26\n\t" \
  "v_add_u32 v158, 64, v27\n\t" \
  "v_bfi_b32 v29, v63, v158, v27\n\t" \
  "v_add_u32 v158, 64, v28\n\t" \
  "v_bfi_b32 v30, v63, v158, v28\n\t" \
  "v_add_u32 v158, 64, v29\n\t" \
  "v_bfi_b32 v31, v63, v158, v29\n\t" \
  "s_mov_b32 s85, 0x5a0000\n\t" \
  "s_load_dword s84, %[ph], s85\n\t" \
  "s_mov_b32 s85, 0x5a0800\n\t" \
  "s_load_dword s84, %[ph], s85\n\t" \
  "s_mov_b32 s85, 0x5a1000\n\t" \
  "s_load_dword s84, %[ph], s85\n\t" \
  "s_setprio 3\n\t" \
  "s_barrier\n\t" \
  "s_waitcnt vmcnt(0)\n\t" \
  "v_add_u32 v61, 1, v160\n\t" \
  "s_mov_b64 exec, 1\n\t" \
  "global_store_dword v60, v61, %[ff] sc1\n\t" \
  "s_mov_b64 exec, -1\n\t" \
  "s_memrealtime %[st2]\n\t" \
  "s_setprio 2\n\t" \
  "ds_read_b64_tr_b16 v[96:97], v165 offset:2080\n\t" \
  "ds_read_b64_tr_b16 v[98:99], v165 offset:2048\n\t" \
  "ds_read_b64_tr_b16 v[100:101], v165 offset:2336\n\t" \
  "ds_read_b64_tr_b16 v[102:103], v165 offset:2304\n\t" \
  "ds_read_b64_tr_b16 v[104:105], v165 offset:2592\n\t" \
  "ds_read_b64_tr_b16 v[106:107], v165 offset:2560\n\t" \
  "ds_read_b64_tr_b16 v[108:109], v165 offset:2848\n\t" \
  "ds_read_b64_tr_b16 v[110:111], v165 offset:2816\n\t" \
  "ds_read_b64_tr_b16 v[112:113], v165 offset:3104\n\t" \
  "ds_read_b64_tr_b16 v[114:115], v165 offset:3072\n\t" \
  "ds_read_b64_tr_b16 v[116:117], v165 offset:3360\n\t" \
  "ds_read_b64_tr_b16 v[118:119], v165 offset:3328\n\t" \
  "ds_read_b64_tr_b16 v[120:121], v165 offset:3616\n\t" \
  "ds_read_b64_tr_b16 v[122:123], v165 offset:3584\n\t" \
  "ds_read_b64_tr_b16 v[124:125], v165 offset:3872\n\t" \
  "ds_read_b64_tr_b16 v[126:127], v165 offset:3840\n\t" \
  "s_barrier\n\t" \
  "s_waitcnt lgkmcnt(0)\n\t" \
  "v_lshlrev_b32 v61, 16, v161\n\t" \
  "v_lshlrev_b32 v62, 16, v162\n\t" \
  "v_add_f32 v61, v61, v62\n\t" \
  "v_mov_b32 v62, 0xffff\n\t" \
  "v_cvt_pk_bf16_f32 v61, v61, v61\n\t" \
  "s_mov_b64 exec, 1\n\t" \
  "v_bfi_b32 v66, v62, v61, v66\n\t" \
  "s_mov_b64 exec, 15\n\t" \
  "v_and_b32 v66, v62, v66\n\t" \
  "s_mov_b64 exec, 16\n\t" \
  "v_bfi_b32 v66, v62, v66, v61\n\t" \
  "s_mov_b64 exec, 0xff\n\t" \
  "v_mov_b32 v67, 0\n\t" \
  "s_mov_b64 exec, 0x100\n\t" \
  "v_mov_b32 v67, v61\n\t" \
  "s_mov_b64 exec, 0xf00\n\t" \
  "v_and_b32 v67, v62, v67\n\t" \
  "s_mov_b64 exec, 0x1000\n\t" \
  "v_bfi_b32 v67, v62, v67, v61\n\t" \
  "s_mov_b64 exec, -1\n\t" \
  "s_waitcnt lgkmcnt(0)\n\t"
#define PP_SM 0
#define PP_NB 8
#define PP_Q 0
#define PP_CLOB "v0", "v1", "v2", "v3", "v4", "v5", "v6", "v7", "v8", "v9", "v10", "v11", "v12", "v13", "v14", "v15", "v16", "v17", "v18", "v19", "v20", "v21", "v22", "v23", "v32", "v33", "v34", "v35", "v36", "v37", "v38", "v39", "v40", "v41", "v42", "v43", "v60", "v61", "v62", "v63", "v128", "v129", "v130", "v131", "v132", "v133", "v134", "v135", "v136", "v137", "v138", "v139", "v140", "v141", "v142", "v143", "v144", "v145", "v146", "v147", "v148", "v149", "v150", "v151", "v158", "v161", "v162", "s82", "s83", "s84", "s85"

#define LD_ASM1 \
  "s_add_u32 m0, s39, 49152\n\t" \
  "s_mov_b32 s68, 0x0\n\t" \
  "buffer_load_dwordx4 v5, s[44:47], s68 offen offset:0 lds\n\t" \
  "buffer_load_dwordx4 v6, s[44:47], s68 offen offset:1024 lds\n\t" \
  "buffer_load_dwordx4 v7, s[44:47], s68 offen offset:2048 lds\n\t" \
  "s_add_u32 m0, s38, 36864\n\t" \
  "s_mov_b32 s68, 0x0\n\t" \
  "buffer_load_dwordx4 v0, s[40:43], s68 offen offset:0 lds\n\t" \
  "buffer_load_dwordx4 v1, s[40:43], s68 offen offset:1024 lds\n\t" \
  "buffer_load_dwordx4 v2, s[40:43], s68 offen offset:2048 lds\n\t" \
  "buffer_load_dwordx4 v3, s[40:43], s68 offen offset:3072 lds\n\t" \
  "s_add_u32 m0, s37, 36864\n\t" \
  "s_mov_b32 s68, 0x0\n\t" \
  "s_mov_b32 exec_hi, 0\n\t" \
  "buffer_load_dwordx4 v4, s[40:43], s68 offen offset:0 lds\n\t" \
  "s_mov_b32 exec_hi, -1\n\t" \
  "s_add_u32 m0, s39, 61440\n\t" \
  "s_mov_b32 s68, 0x30000\n\t" \
  "buffer_load_dwordx4 v5, s[44:47], s68 offen offset:0 lds\n\t" \
  "buffer_load_dwordx4 v6, s[44:47], s68 offen offset:1024 lds\n\t" \
  "buffer_load_dwordx4 v7, s[44:47], s68 offen offset:2048 lds\n\t" \
  "s_mov_b32 m0, %[sk]\n\t" \
  "s_mov_b32 exec_hi, 0\n\t" \
  "buffer_load_dword v9, s[40:43], 0 offen lds\n\t" \
  "s_mov_b32 exec_lo, %[wm]\n\t" \
  "buffer_load_dword v10, s[44:47], 0 offen lds\n\t" \
  "s_mov_b64 exec, -1\n\t"
#define LD_ASM2 \
  "s_add_u32 m0, s38, 0\n\t" \
  "s_mov_b32 s68, 0x80\n\t" \
  "buffer_load_dwordx4 v0, s[40:43], s68 offen offset:0 lds\n\t" \
  "s_mov_b32 s85, 0x180000\n\t" \
  "s_load_dword s84, %[pw], s85\n\t" \
  "buffer_load_dwordx4 v1, s[40:43], s68 offen offset:1024 lds\n\t" \
  "s_mov_b32 s85, 0x180800\n\t" \
  "s_load_dword s84, %[pw], s85\n\t" \
  "buffer_load_dwordx4 v2, s[40:43], s68 offen offset:2048 lds\n\t" \
  "s_mov_b32 s85, 0x181000\n\t" \
  "s_load_dword s84, %[pw], s85\n\t" \
  "buffer_load_dwordx4 v3, s[40:43], s68 offen offset:3072 lds\n\t" \
  "s_mov_b32 s85, 0x1b0000\n\t" \
  "s_load_dword s84, %[pw], s85\n\t" \
  "s_add_u32 m0, s37, 0\n\t" \
  "s_mov_b32 s68, 0x80\n\t" \
  "s_mov_b32 exec_hi, 0\n\t" \
  "buffer_load_dwordx4 v4, s[40:43], s68 offen offset:0 lds\n\t" \
  "s_mov_b32 exec_hi, -1\n\t" \
  "s_mov_b32 m0, %[tsk]\n\t" \
  "s_mov_b32 s85, 0x0\n\t" \
  "s_mov_b64 exec, %[hm]\n\t" \
  "buffer_load_dword v16, %[sh], s85 offen lds\n\t" \
  "s_mov_b64 exec, -1\n\t" \
  "s_mov_b32 s68, 0x60000\n\t" \
  "s_mov_b32 s69, 0x90000\n\t" \
  "s_mov_b32 s70, 0x100\n\t" \
  "s_mov_b32 s71, 0x100\n\t" \
  "s_mov_b32 s72, 0xc0000\n\t" \
  "s_mov_b32 s73, 0xf0000\n\t" \
  "s_add_u32 m0, s39, 0\n\t" \
  "s_waitcnt vmcnt(8)\n\t" \
  "s_barrier\n\t" \
  "buffer_load_dwordx4 v5, s[44:47], s68 offen offset:0 lds\n\t" \
  "s_mov_b32 s85, 0x1b0800\n\t" \
  "s_load_dword s84, %[pw], s85\n\t" \
  "buffer_load_dwordx4 v6, s[44:47], s68 offen offset:1024 lds\n\t" \
  "s_mov_b32 s85, 0x1b1000\n\t" \
  "s_load_dword s84, %[pw], s85\n\t" \
  "buffer_load_dwordx4 v7, s[44:47], s68 offen offset:2048 lds\n\t" \
  "s_mov_b32 s85, 0x1e0000\n\t" \
  "s_load_dword s84, %[pw], s85\n\t" \
  "s_add_u32 m0, s39, 12288\n\t" \
  "s_nop 0\n\t" \
  "buffer_load_dwordx4 v5, s[44:47], s69 offen offset:0 lds\n\t" \
  "s_mov_b32 s85, 0x1e0800\n\t" \
  "s_load_dword s84, %[pw], s85\n\t" \
  "buffer_load_dwordx4 v6, s[44:47], s69 offen offset:1024 lds\n\t" \
  "s_mov_b32 s85, 0x1e1000\n\t" \
  "s_load_dword s84, %[pw], s85\n\t" \
  "buffer_load_dwordx4 v7, s[44:47], s69 offen offset:2048 lds\n\t" \
  "s_mov_b32 s85, 0x210000\n\t" \
  "s_load_dword s84, %[pw], s85\n\t" \
  "s_add_u32 m0, s38, 18432\n\t" \
  "s_nop 0\n\t" \
  "buffer_load_dwordx4 v0, s[40:43], s70 offen offset:0 lds\n\t" \
  "buffer_load_dwordx4 v1, s[40:43], s70 offen offset:1024 lds\n\t" \
  "buffer_load_dwordx4 v2, s[40:43], s70 offen offset:2048 lds\n\t" \
  "buffer_load_dwordx4 v3, s[40:43], s70 offen offset:3072 lds\n\t" \
  "s_add_u32 m0, s37, 18432\n\t" \
  "s_nop 0\n\t" \
  "s_mov_b32 exec_hi, 0\n\t" \
  "buffer_load_dwordx4 v4, s[40:43], s71 offen offset:0 lds\n\t" \
  "s_add_u32 m0, s39, 24576\n\t" \
  "s_mov_b32 exec_hi, -1\n\t" \
  "buffer_load_dwordx4 v5, s[44:47], s72 offen offset:0 lds\n\t" \
  "buffer_load_dwordx4 v6, s[44:47], s72 offen offset:1024 lds\n\t" \
  "buffer_load_dwordx4 v7, s[44:47], s72 offen offset:2048 lds\n\t" \
  "s_add_u32 m0, s39, 36864\n\t" \
  "s_nop 0\n\t" \
  "buffer_load_dwordx4 v5, s[44:47], s73 offen offset:0 lds\n\t" \
  "buffer_load_dwordx4 v6, s[44:47], s73 offen offset:1024 lds\n\t" \
  "buffer_load_dwordx4 v7, s[44:47], s73 offen offset:2048 lds\n\t" \
  "s_mov_b32 m0, %[tsk]\n\t" \
  "s_mov_b32 s85, 0x180000\n\t" \
  "s_mov_b64 exec, %[hm]\n\t" \
  "buffer_load_dword v16, %[sh], s85 offen lds\n\t" \
  "s_mov_b64 exec, -1\n\t"
#define LD_ASM3 \
  "s_mov_b32 s68, 0x180\n\t" \
  "s_mov_b32 s69, 0x180\n\t" \
  "s_mov_b32 s70, 0x120000\n\t" \
  "s_mov_b32 s71, 0x150000\n\t" \
  "s_add_u32 m0, s38, 36864\n\t" \
  "s_waitcnt vmcnt(12)\n\t" \
  "s_barrier\n\t" \
  "buffer_load_dwordx4 v0, s[40:43], s68 offen offset:0 lds\n\t" \
  "s_mov_b32 s85, 0x210800\n\t" \
  "s_load_dword s84, %[pw], s85\n\t" \
  "buffer_load_dwordx4 v1, s[40:43], s68 offen offset:1024 lds\n\t" \
  "s_mov_b32 s85, 0x211000\n\t" \
  "s_load_dword s84, %[pw], s85\n\t" \
  "buffer_load_dwordx4 v2, s[40:43], s68 offen offset:2048 lds\n\t" \
  "s_mov_b32 s85, 0x240000\n\t" \
  "s_load_dword s84, %[pw], s85\n\t" \
  "buffer_load_dwordx4 v3, s[40:43], s68 offen offset:3072 lds\n\t" \
  "s_mov_b32 s85, 0x240800\n\t" \
  "s_load_dword s84, %[pw], s85\n\t" \
  "s_add_u32 m0, s37, 36864\n\t" \
  "s_nop 0\n\t" \
  "s_mov_b32 exec_hi, 0\n\t" \
  "buffer_load_dwordx4 v4, s[40:43], s69 offen offset:0 lds\n\t" \
  "s_add_u32 m0, s39, 49152\n\t" \
  "s_mov_b32 exec_hi, -1\n\t" \
  "buffer_load_dwordx4 v5, s[44:47], s70 offen offset:0 lds\n\t" \
  "s_mov_b32 s85, 0x241000\n\t" \
  "s_load_dword s84, %[pw], s85\n\t" \
  "buffer_load_dwordx4 v6, s[44:47], s70 offen offset:1024 lds\n\t" \
  "s_mov_b32 s85, 0x270000\n\t" \
  "s_load_dword s84, %[pw], s85\n\t" \
  "buffer_load_dwordx4 v7, s[44:47], s70 offen offset:2048 lds\n\t" \
  "s_add_u32 m0, s39, 61440\n\t" \
  "s_nop 0\n\t" \
  "buffer_load_dwordx4 v5, s[44:47], s71 offen offset:0 lds\n\t" \
  "buffer_load_dwordx4 v6, s[44:47], s71 offen offset:1024 lds\n\t" \
  "buffer_load_dwordx4 v7, s[44:47], s71 offen offset:2048 lds\n\t" \
  "s_mov_b32 s68, 0x200\n\t" \
  "s_mov_b32 s69, 0x200\n\t" \
  "s_mov_b32 s70, 0x180000\n\t" \
  "s_mov_b32 s71, 0x1b0000\n\t" \
  "s_add_u32 m0, s38, 0\n\t" \
  "s_waitcnt vmcnt(12)\n\t" \
  "s_barrier\n\t" \
  "buffer_load_dwordx4 v0, s[40:43], s68 offen offset:0 lds\n\t" \
  "s_mov_b32 s85, 0x270800\n\t" \
  "s_load_dword s84, %[pw], s85\n\t" \
  "buffer_load_dwordx4 v1, s[40:43], s68 offen offset:1024 lds\n\t" \
  "s_mov_b32 s85, 0x271000\n\t" \
  "s_load_dword s84, %[pw], s85\n\t" \
  "buffer_load_dwordx4 v2, s[40:43], s68 offen offset:2048 lds\n\t" \
  "s_mov_b32 s85, 0x2a0000\n\t" \
  "s_load_dword s84, %[pw], s85\n\t" \
  "buffer_load_dwordx4 v3, s[40:43], s68 offen offset:3072 lds\n\t" \
  "s_mov_b32 s85, 0x2a0800\n\t" \
  "s_load_dword s84, %[pw], s85\n\t" \
  "s_add_u32 m0, s37, 0\n\t" \
  "s_nop 0\n\t" \
  "s_mov_b32 exec_hi, 0\n\t" \
  "buffer_load_dwordx4 v4, s[40:43], s69 offen offset:0 lds\n\t" \
  "s_add_u32 m0, s39, 0\n\t" \
  "s_mov_b32 exec_hi, -1\n\t" \
  "buffer_load_dwordx4 v5, s[44:47], s70 offen offset:0 lds\n\t" \
  "s_mov_b32 s85, 0x2a1000\n\t" \
  "s_load_dword s84, %[pw], s85\n\t" \
  "buffer_load_dwordx4 v6, s[44:47], s70 offen offset:1024 lds\n\t" \
  "s_mov_b32 s85, 0x2d0000\n\t" \
  "s_load_dword s84, %[pw], s85\n\t" \
  "buffer_load_dwordx4 v7, s[44:47], s70 offen offset:2048 lds\n\t" \
  "s_add_u32 m0, s39, 12288\n\t" \
  "s_nop 0\n\t" \
  "buffer_load_dwordx4 v5, s[44:47], s71 offen offset:0 lds\n\t" \
  "buffer_load_dwordx4 v6, s[44:47], s71 offen offset:1024 lds\n\t" \
  "buffer_load_dwordx4 v7, s[44:47], s71 offen offset:2048 lds\n\t" \
  "s_mov_b32 s68, 0x280\n\t" \
  "s_mov_b32 s69, 0x280\n\t" \
  "s_mov_b32 s70, 0x1e0000\n\t" \
  "s_mov_b32 s71, 0x210000\n\t" \
  "s_add_u32 m0, s38, 18432\n\t" \
  "s_waitcnt vmcnt(11)\n\t" \
  "s_barrier\n\t" \
  "buffer_load_dwordx4 v0, s[40:43], s68 offen offset:0 lds\n\t" \
  "s_mov_b32 s85, 0x2d0800\n\t" \
  "s_load_dword s84, %[pw], s85\n\t" \
  "buffer_load_dwordx4 v1, s[40:43], s68 offen offset:1024 lds\n\t" \
  "s_mov_b32 s85, 0x2d1000\n\t" \
  "s_load_dword s84, %[pw], s85\n\t" \
  "buffer_load_dwordx4 v2, s[40:43], s68 offen offset:2048 lds\n\t" \
  "s_mov_b32 s85, 0x300000\n\t" \
  "s_load_dword s84, %[pw], s85\n\t" \
  "buffer_load_dwordx4 v3, s[40:43], s68 offen offset:3072 lds\n\t" \
  "s_mov_b32 s85, 0x300800\n\t" \
  "s_load_dword s84, %[pw], s85\n\t" \
  "s_add_u32 m0, s37, 18432\n\t" \
  "s_nop 0\n\t" \
  "s_mov_b32 exec_hi, 0\n\t" \
  "buffer_load_dwordx4 v4, s[40:43], s69 offen offset:0 lds\n\t" \
  "s_add_u32 m0, s39, 24576\n\t" \
  "s_mov_b32 exec_hi, -1\n\t" \
  "buffer_load_dwordx4 v5, s[44:47], s70 offen offset:0 lds\n\t" \
  "s_mov_b32 s85, 0x301000\n\t" \
  "s_load_dword s84, %[pw], s85\n\t" \
  "buffer_load_dwordx4 v6, s[44:47], s70 offen offset:1024 lds\n\t" \
  "s_mov_b32 s85, 0x330000\n\t" \
  "s_load_dword s84, %[pw], s85\n\t" \
  "buffer_load_dwordx4 v7, s[44:47], s70 offen offset:2048 lds\n\t" \
  "s_add_u32 m0, s39, 36864\n\t" \
  "s_nop 0\n\t" \
  "buffer_load_dwordx4 v5, s[44:47], s71 offen offset:0 lds\n\t" \
  "buffer_load_dwordx4 v6, s[44:47], s71 offen offset:1024 lds\n\t" \
  "buffer_load_dwordx4 v7, s[44:47], s71 offen offset:2048 lds\n\t" \
  "s_mov_b32 s68, 0x300\n\t" \
  "s_mov_b32 s69, 0x300\n\t" \
  "s_mov_b32 s70, 0x240000\n\t" \
  "s_mov_b32 s71, 0x270000\n\t" \
  "s_add_u32 m0, s38, 36864\n\t" \
  "s_waitcnt vmcnt(11)\n\t" \
  "s_barrier\n\t" \
  "buffer_load_dwordx4 v0, s[40:43], s68 offen offset:0 lds\n\t" \
  "s_mov_b32 s85, 0x330800\n\t" \
  "s_load_dword s84, %[pw], s85\n\t" \
  "buffer_load_dwordx4 v1, s[40:43], s68 offen offset:1024 lds\n\t" \
  "s_mov_b32 s85, 0x331000\n\t" \
  "s_load_dword s84, %[pw], s85\n\t" \
  "buffer_load_dwordx4 v2, s[40:43], s68 offen offset:2048 lds\n\t" \
  "s_mov_b32 s85, 0x360000\n\t" \
  "s_load_dword s84, %[pw], s85\n\t" \
  "buffer_load_dwordx4 v3, s[40:43], s68 offen offset:3072 lds\n\t" \
  "s_mov_b32 s85, 0x360800\n\t" \
  "s_load_dword s84, %[pw], s85\n\t" \
  "s_add_u32 m0, s37, 36864\n\t" \
  "s_nop 0\n\t" \
  "s_mov_b32 exec_hi, 0\n\t" \
  "buffer_load_dwordx4 v4, s[40:43], s69 offen offset:0 lds\n\t" \
  "s_add_u32 m0, s39, 49152\n\t" \
  "s_mov_b32 exec_hi, -1\n\t" \
  "buffer_load_dwordx4 v5, s[44:47], s70 offen offset:0 lds\n\t" \
  "s_mov_b32 s85, 0x361000\n\t" \
  "s_load_dword s84, %[pw], s85\n\t" \
  "buffer_load_dwordx4 v6, s[44:47], s70 offen offset:1024 lds\n\t" \
  "s_mov_b32 s85, 0x390000\n\t" \
  "s_load_dword s84, %[pw], s85\n\t" \
  "buffer_load_dwordx4 v7, s[44:47], s70 offen offset:2048 lds\n\t" \
  "s_add_u32 m0, s39, 61440\n\t" \
  "s_nop 0\n\t" \
  "buffer_load_dwordx4 v5, s[44:47], s71 offen offset:0 lds\n\t" \
  "buffer_load_dwordx4 v6, s[44:47], s71 offen offset:1024 lds\n\t" \
  "buffer_load_dwordx4 v7, s[44:47], s71 offen offset:2048 lds\n\t" \
  "s_mov_b32 s68, 0x380\n\t" \
  "s_mov_b32 s69, 0x380\n\t" \
  "s_mov_b32 s70, 0x2a0000\n\t" \
  "s_mov_b32 s71, 0x2d0000\n\t" \
  "s_add_u32 m0, s38, 0\n\t" \
  "s_waitcnt vmcnt(11)\n\t" \
  "s_barrier\n\t" \
  "buffer_load_dwordx4 v0, s[40:43], s68 offen offset:0 lds\n\t" \
  "s_mov_b32 s85, 0x390800\n\t" \
  "s_load_dword s84, %[pw], s85\n\t" \
  "buffer_load_dwordx4 v1, s[40:43], s68 offen offset:1024 lds\n\t" \
  "s_mov_b32 s85, 0x391000\n\t" \
  "s_load_dword s84, %[pw], s85\n\t" \
  "buffer_load_dwordx4 v2, s[40:43], s68 offen offset:2048 lds\n\t" \
  "s_mov_b32 s85, 0x3c0000\n\t" \
  "s_load_dword s84, %[pw], s85\n\t" \
  "buffer_load_dwordx4 v3, s[40:43], s68 offen offset:3072 lds\n\t" \
  "s_mov_b32 s85, 0x3c0800\n\t" \
  "s_load_dword s84, %[pw], s85\n\t" \
  "s_add_u32 m0, s37, 0\n\t" \
  "s_nop 0\n\t" \
  "s_mov_b32 exec_hi, 0\n\t" \
  "buffer_load_dwordx4 v4, s[40:43], s69 offen offset:0 lds\n\t" \
  "s_add_u32 m0, s39, 0\n\t" \
  "s_mov_b32 exec_hi, -1\n\t" \
  "buffer_load_dwordx4 v5, s[44:47], s70 offen offset:0 lds\n\t" \
  "s_mov_b32 s85, 0x3c1000\n\t" \
  "s_load_dword s84, %[pw], s85\n\t" \
  "buffer_load_dwordx4 v6, s[44:47], s70 offen offset:1024 lds\n\t" \
  "s_mov_b32 s85, 0x3f0000\n\t" \
  "s_load_dword s84, %[pw], s85\n\t" \
  "buffer_load_dwordx4 v7, s[44:47], s70 offen offset:2048 lds\n\t" \
  "s_add_u32 m0, s39, 12288\n\t" \
  "s_nop 0\n\t" \
  "buffer_load_dwordx4 v5, s[44:47], s71 offen offset:0 lds\n\t" \
  "buffer_load_dwordx4 v6, s[44:47], s71 offen offset:1024 lds\n\t" \
  "buffer_load_dwordx4 v7, s[44:47], s71 offen offset:2048 lds\n\t" \
  "s_mov_b32 s68, 0x400\n\t" \
  "s_mov_b32 s69, 0x400\n\t" \
  "s_mov_b32 s70, 0x300000\n\t" \
  "s_mov_b32 s71, 0x330000\n\t" \
  "s_add_u32 m0, s38, 18432\n\t" \
  "s_waitcnt vmcnt(11)\n\t" \
  "s_barrier\n\t" \
  "buffer_load_dwordx4 v0, s[40:43], s68 offen offset:0 lds\n\t" \
  "s_mov_b32 s85, 0x3f0800\n\t" \
  "s_load_dword s84, %[pw], s85\n\t" \
  "buffer_load_dwordx4 v1, s[40:43], s68 offen offset:1024 lds\n\t" \
  "s_mov_b32 s85, 0x3f1000\n\t" \
  "s_load_dword s84, %[pw], s85\n\t" \
  "buffer_load_dwordx4 v2, s[40:43], s68 offen offset:2048 lds\n\t" \
  "s_mov_b32 s85, 0x420000\n\t" \
  "s_load_dword s84, %[pw], s85\n\t" \
  "buffer_load_dwordx4 v3, s[40:43], s68 offen offset:3072 lds\n\t" \
  "s_mov_b32 s85, 0x420800\n\t" \
  "s_load_dword s84, %[pw], s85\n\t" \
  "s_add_u32 m0, s37, 18432\n\t" \
  "s_nop 0\n\t" \
  "s_mov_b32 exec_hi, 0\n\t" \
  "buffer_load_dwordx4 v4, s[40:43], s69 offen offset:0 lds\n\t" \
  "s_add_u32 m0, s39, 24576\n\t" \
  "s_mov_b32 exec_hi, -1\n\t" \
  "buffer_load_dwordx4 v5, s[44:47], s70 offen offset:0 lds\n\t" \
  "s_mov_b32 s85, 0x421000\n\t" \
  "s_load_dword s84, %[pw], s85\n\t" \
  "buffer_load_dwordx4 v6, s[44:47], s70 offen offset:1024 lds\n\t" \
  "s_mov_b32 s85, 0x450000\n\t" \
  "s_load_dword s84, %[pw], s85\n\t" \
  "buffer_load_dwordx4 v7, s[44:47], s70 offen offset:2048 lds\n\t" \
  "s_add_u32 m0, s39, 36864\n\t" \
  "s_nop 0\n\t" \
  "buffer_load_dwordx4 v5, s[44:47], s71 offen offset:0 lds\n\t" \
  "buffer_load_dwordx4 v6, s[44:47], s71 offen offset:1024 lds\n\t" \
  "buffer_load_dwordx4 v7, s[44:47], s71 offen offset:2048 lds\n\t" \
  "s_mov_b32 s68, 0x480\n\t" \
  "s_mov_b32 s69, 0x480\n\t" \
  "s_mov_b32 s70, 0x360000\n\t" \
  "s_mov_b32 s71, 0x390000\n\t" \
  "s_add_u32 m0, s38, 36864\n\t" \
  "s_waitcnt vmcnt(11)\n\t" \
  "s_barrier\n\t" \
  "buffer_load_dwordx4 v0, s[40:43], s68 offen offset:0 lds\n\t" \
  "s_mov_b32 s85, 0x450800\n\t" \
  "s_load_dword s84, %[pw], s85\n\t" \
  "buffer_load_dwordx4 v1, s[40:43], s68 offen offset:1024 lds\n\t" \
  "s_mov_b32 s85, 0x451000\n\t" \
  "s_load_dword s84, %[pw], s85\n\t" \
  "buffer_load_dwordx4 v2, s[40:43], s68 offen offset:2048 lds\n\t" \
  "s_mov_b32 s85, 0x480000\n\t" \
  "s_load_dword s84, %[pw], s85\n\t" \
  "buffer_load_dwordx4 v3, s[40:43], s68 offen offset:3072 lds\n\t" \
  "s_mov_b32 s85, 0x480800\n\t" \
  "s_load_dword s84, %[pw], s85\n\t" \
  "s_add_u32 m0, s37, 36864\n\t" \
  "s_nop 0\n\t" \
  "s_mov_b32 exec_hi, 0\n\t" \
  "buffer_load_dwordx4 v4, s[40:43], s69 offen offset:0 lds\n\t" \
  "s_add_u32 m0, s39, 49152\n\t" \
  "s_mov_b32 exec_hi, -1\n\t" \
  "buffer_load_dwordx4 v5, s[44:47], s70 offen offset:0 lds\n\t" \
  "s_mov_b32 s85, 0x481000\n\t" \
  "s_load_dword s84, %[pw], s85\n\t" \
  "buffer_load_dwordx4 v6, s[44:47], s70 offen offset:1024 lds\n\t" \
  "s_mov_b32 s85, 0x4b0000\n\t" \
  "s_load_dword s84, %[pw], s85\n\t" \
  "buffer_load_dwordx4 v7, s[44:47], s70 offen offset:2048 lds\n\t" \
  "s_add_u32 m0, s39, 61440\n\t" \
  "s_nop 0\n\t" \
  "buffer_load_dwordx4 v5, s[44:47], s71 offen offset:0 lds\n\t" \
  "buffer_load_dwordx4 v6, s[44:47], s71 offen offset:1024 lds\n\t" \
  "buffer_load_dwordx4 v7, s[44:47], s71 offen offset:2048 lds\n\t" \
  "s_mov_b32 s68, 0x500\n\t" \
  "s_mov_b32 s69, 0x500\n\t" \
  "s_mov_b32 s70, 0x3c0000\n\t" \
  "s_mov_b32 s71, 0x3f0000\n\t" \
  "s_add_u32 m0, s38, 0\n\t" \
  "s_waitcnt vmcnt(11)\n\t" \
  "s_barrier\n\t" \
  "buffer_load_dwordx4 v0, s[40:43], s68 offen offset:0 lds\n\t" \
  "s_mov_b32 s85, 0x4b0800\n\t" \
  "s_load_dword s84, %[pw], s85\n\t" \
  "buffer_load_dwordx4 v1, s[40:43], s68 offen offset:1024 lds\n\t" \
  "s_mov_b32 s85, 0x4b1000\n\t" \
  "s_load_dword s84, %[pw], s85\n\t" \
  "buffer_load_dwordx4 v2, s[40:43], s68 offen offset:2048 lds\n\t" \
  "s_mov_b32 s85, 0x4e0000\n\t" \
  "s_load_dword s84, %[pw], s85\n\t" \
  "buffer_load_dwordx4 v3, s[40:43], s68 offen offset:3072 lds\n\t" \
  "s_mov_b32 s85, 0x4e0800\n\t" \
  "s_load_dword s84, %[pw], s85\n\t" \
  "s_add_u32 m0, s37, 0\n\t" \
  "s_nop 0\n\t" \
  "s_mov_b32 exec_hi, 0\n\t" \
  "buffer_load_dwordx4 v4, s[40:43], s69 offen offset:0 lds\n\t" \
  "s_add_u32 m0, s39, 0\n\t" \
  "s_mov_b32 exec_hi, -1\n\t" \
  "buffer_load_dwordx4 v5, s[44:47], s70 offen offset:0 lds\n\t" \
  "s_mov_b32 s85, 0x4e1000\n\t" \
  "s_load_dword s84, %[pw], s85\n\t" \
  "buffer_load_dwordx4 v6, s[44:47], s70 offen offset:1024 lds\n\t" \
  "s_mov_b32 s85, 0x510000\n\t" \
  "s_load_dword s84, %[pw], s85\n\t" \
  "buffer_load_dwordx4 v7, s[44:47], s70 offen offset:2048 lds\n\t" \
  "s_add_u32 m0, s39, 12288\n\t" \
  "s_nop 0\n\t" \
  "buffer_load_dwordx4 v5, s[44:47], s71 offen offset:0 lds\n\t" \
  "buffer_load_dwordx4 v6, s[44:47], s71 offen offset:1024 lds\n\t" \
  "buffer_load_dwordx4 v7, s[44:47], s71 offen offset:2048 lds\n\t" \
  "s_mov_b32 s68, 0x580\n\t" \
  "s_mov_b32 s69, 0x580\n\t" \
  "s_mov_b32 s70, 0x420000\n\t" \
  "s_mov_b32 s71, 0x450000\n\t" \
  "s_add_u32 m0, s38, 18432\n\t" \
  "s_waitcnt vmcnt(11)\n\t" \
  "s_barrier\n\t" \
  "buffer_load_dwordx4 v0, s[40:43], s68 offen offset:0 lds\n\t" \
  "s_mov_b32 s85, 0x510800\n\t" \
  "s_load_dword s84, %[pw], s85\n\t" \
  "buffer_load_dwordx4 v1, s[40:43], s68 offen offset:1024 lds\n\t" \
  "s_mov_b32 s85, 0x511000\n\t" \
  "s_load_dword s84, %[pw], s85\n\t" \
  "buffer_load_dwordx4 v2, s[40:43], s68 offen offset:2048 lds\n\t" \
  "s_mov_b32 s85, 0x540000\n\t" \
  "s_load_dword s84, %[pw], s85\n\t" \
  "buffer_load_dwordx4 v3, s[40:43], s68 offen offset:3072 lds\n\t" \
  "s_mov_b32 s85, 0x540800\n\t" \
  "s_load_dword s84, %[pw], s85\n\t" \
  "s_add_u32 m0, s37, 18432\n\t" \
  "s_nop 0\n\t" \
  "s_mov_b32 exec_hi, 0\n\t" \
  "buffer_load_dwordx4 v4, s[40:43], s69 offen offset:0 lds\n\t" \
  "s_add_u32 m0, s39, 24576\n\t" \
  "s_mov_b32 exec_hi, -1\n\t" \
  "buffer_load_dwordx4 v5, s[44:47], s70 offen offset:0 lds\n\t" \
  "s_mov_b32 s85, 0x541000\n\t" \
  "s_load_dword s84, %[pw], s85\n\t" \
  "buffer_load_dwordx4 v6, s[44:47], s70 offen offset:1024 lds\n\t" \
  "s_mov_b32 s85, 0x570000\n\t" \
  "s_load_dword s84, %[pw], s85\n\t" \
  "buffer_load_dwordx4 v7, s[44:47], s70 offen offset:2048 lds\n\t" \
  "s_add_u32 m0, s39, 36864\n\t" \
  "s_nop 0\n\t" \
  "buffer_load_dwordx4 v5, s[44:47], s71 offen offset:0 lds\n\t" \
  "buffer_load_dwordx4 v6, s[44:47], s71 offen offset:1024 lds\n\t" \
  "buffer_load_dwordx4 v7, s[44:47], s71 offen offset:2048 lds\n\t" \
  "s_mov_b32 s68, 0x600\n\t" \
  "s_mov_b32 s69, 0x600\n\t" \
  "s_mov_b32 s70, 0x480000\n\t" \
  "s_mov_b32 s71, 0x4b0000\n\t" \
  "s_add_u32 m0, s38, 36864\n\t" \
  "s_waitcnt vmcnt(11)\n\t" \
  "s_barrier\n\t" \
  "buffer_load_dwordx4 v0, s[40:43], s68 offen offset:0 lds\n\t" \
  "s_mov_b32 s85, 0x570800\n\t" \
  "s_load_dword s84, %[pw], s85\n\t" \
  "buffer_load_dwordx4 v1, s[40:43], s68 offen offset:1024 lds\n\t" \
  "s_mov_b32 s85, 0x571000\n\t" \
  "s_load_dword s84, %[pw], s85\n\t" \
  "buffer_load_dwordx4 v2, s[40:43], s68 offen offset:2048 lds\n\t" \
  "s_mov_b32 s85, 0x5a0000\n\t" \
  "s_load_dword s84, %[pw], s85\n\t" \
  "buffer_load_dwordx4 v3, s[40:43], s68 offen offset:3072 lds\n\t" \
  "s_mov_b32 s85, 0x5a0800\n\t" \
  "s_load_dword s84, %[pw], s85\n\t" \
  "s_add_u32 m0, s37, 36864\n\t" \
  "s_nop 0\n\t" \
  "s_mov_b32 exec_hi, 0\n\t" \
  "buffer_load_dwordx4 v4, s[40:43], s69 offen offset:0 lds\n\t" \
  "s_add_u32 m0, s39, 49152\n\t" \
  "s_mov_b32 exec_hi, -1\n\t" \
  "buffer_load_dwordx4 v5, s[44:47], s70 offen offset:0 lds\n\t" \
  "s_mov_b32 s85, 0x5a1000\n\t" \
  "s_load_dword s84, %[pw], s85\n\t" \
  "buffer_load_dwordx4 v6, s[44:47], s70 offen offset:1024 lds\n\t" \
  "s_mov_b32 s85, 0x5d0000\n\t" \
  "s_load_dword s84, %[pw], s85\n\t" \
  "buffer_load_dwordx4 v7, s[44:47], s70 offen offset:2048 lds\n\t" \
  "s_add_u32 m0, s39, 61440\n\t" \
  "s_nop 0\n\t" \
  "buffer_load_dwordx4 v5, s[44:47], s71 offen offset:0 lds\n\t" \
  "buffer_load_dwordx4 v6, s[44:47], s71 offen offset:1024 lds\n\t" \
  "buffer_load_dwordx4 v7, s[44:47], s71 offen offset:2048 lds\n\t" \
  "s_mov_b32 s68, 0x680\n\t" \
  "s_mov_b32 s69, 0x680\n\t" \
  "s_mov_b32 s70, 0x4e0000\n\t" \
  "s_mov_b32 s71, 0x510000\n\t" \
  "s_add_u32 m0, s38, 0\n\t" \
  "s_waitcnt vmcnt(11)\n\t" \
  "s_barrier\n\t" \
  "buffer_load_dwordx4 v0, s[40:43], s68 offen offset:0 lds\n\t" \
  "s_load_dword s84, %[pq], 0x0\n\t" \
  "buffer_load_dwordx4 v1, s[40:43], s68 offen offset:1024 lds\n\t" \
  "s_load_dword s84, %[pq], 0x40\n\t" \
  "buffer_load_dwordx4 v2, s[40:43], s68 offen offset:2048 lds\n\t" \
  "s_load_dword s84, %[pq], 0x800\n\t" \
  "buffer_load_dwordx4 v3, s[40:43], s68 offen offset:3072 lds\n\t" \
  "s_load_dword s84, %[pq], 0x840\n\t" \
  "s_add_u32 m0, s37, 0\n\t" \
  "s_nop 0\n\t" \
  "s_mov_b32 exec_hi, 0\n\t" \
  "buffer_load_dwordx4 v4, s[40:43], s69 offen offset:0 lds\n\t" \
  "s_add_u32 m0, s39, 0\n\t" \
  "s_mov_b32 exec_hi, -1\n\t" \
  "buffer_load_dwordx4 v5, s[44:47], s70 offen offset:0 lds\n\t" \
  "s_load_dword s84, %[pq], 0x1000\n\t" \
  "buffer_load_dwordx4 v6, s[44:47], s70 offen offset:1024 lds\n\t" \
  "s_load_dword s84, %[pq], 0x1040\n\t" \
  "buffer_load_dwordx4 v7, s[44:47], s70 offen offset:2048 lds\n\t" \
  "s_mov_b32 s85, 0x5d0800\n\t" \
  "s_load_dword s84, %[pw], s85\n\t" \
  "s_add_u32 m0, s39, 12288\n\t" \
  "s_nop 0\n\t" \
  "buffer_load_dwordx4 v5, s[44:47], s71 offen offset:0 lds\n\t" \
  "s_mov_b32 s85, 0x5d1000\n\t" \
  "s_load_dword s84, %[pw], s85\n\t" \
  "buffer_load_dwordx4 v6, s[44:47], s71 offen offset:1024 lds\n\t" \
  "buffer_load_dwordx4 v7, s[44:47], s71 offen offset:2048 lds\n\t" \
  "s_mov_b32 s68, 0x700\n\t" \
  "s_mov_b32 s69, 0x700\n\t" \
  "s_mov_b32 s70, 0x540000\n\t" \
  "s_mov_b32 s71, 0x570000\n\t" \
  "s_add_u32 m0, s38, 18432\n\t" \
  "s_waitcnt vmcnt(11)\n\t" \
  "s_barrier\n\t" \
  "buffer_load_dwordx4 v0, s[40:43], s68 offen offset:0 lds\n\t" \
  "s_load_dword s84, %[pq], 0x1800\n\t" \
  "buffer_load_dwordx4 v1, s[40:43], s68 offen offset:1024 lds\n\t" \
  "s_load_dword s84, %[pq], 0x1840\n\t" \
  "buffer_load_dwordx4 v2, s[40:43], s68 offen offset:2048 lds\n\t" \
  "s_load_dword s84, %[pq], 0x2000\n\t" \
  "buffer_load_dwordx4 v3, s[40:43], s68 offen offset:3072 lds\n\t" \
  "s_load_dword s84, %[pq], 0x2040\n\t" \
  "s_add_u32 m0, s37, 18432\n\t" \
  "s_nop 0\n\t" \
  "s_mov_b32 exec_hi, 0\n\t" \
  "buffer_load_dwordx4 v4, s[40:43], s69 offen offset:0 lds\n\t" \
  "s_add_u32 m0, s39, 24576\n\t" \
  "s_mov_b32 exec_hi, -1\n\t" \
  "buffer_load_dwordx4 v5, s[44:47], s70 offen offset:0 lds\n\t" \
  "s_load_dword s84, %[pq], 0x2800\n\t" \
  "buffer_load_dwordx4 v6, s[44:47], s70 offen offset:1024 lds\n\t" \
  "s_load_dword s84, %[pq], 0x2840\n\t" \
  "buffer_load_dwordx4 v7, s[44:47], s70 offen offset:2048 lds\n\t" \
  "s_add_u32 m0, s39, 36864\n\t" \
  "s_nop 0\n\t" \
  "buffer_load_dwordx4 v5, s[44:47], s71 offen offset:0 lds\n\t" \
  "buffer_load_dwordx4 v6, s[44:47], s71 offen offset:1024 lds\n\t" \
  "buffer_load_dwordx4 v7, s[44:47], s71 offen offset:2048 lds\n\t" \
  "s_mov_b32 s68, 0x780\n\t" \
  "s_mov_b32 s69, 0x780\n\t" \
  "s_mov_b32 s70, 0x5a0000\n\t" \
  "s_mov_b32 s71, 0x5d0000\n\t" \
  "s_add_u32 m0, s38, 36864\n\t" \
  "s_waitcnt vmcnt(11)\n\t" \
  "s_barrier\n\t" \
  "buffer_load_dwordx4 v0, s[40:43], s68 offen offset:0 lds\n\t" \
  "s_load_dword s84, %[pq], 0x3000\n\t" \
  "buffer_load_dwordx4 v1, s[40:43], s68 offen offset:1024 lds\n\t" \
  "s_load_dword s84, %[pq], 0x3040\n\t" \
  "buffer_load_dwordx4 v2, s[40:43], s68 offen offset:2048 lds\n\t" \
  "s_load_dword s84, %[pq], 0x3800\n\t" \
  "buffer_load_dwordx4 v3, s[40:43], s68 offen offset:3072 lds\n\t" \
  "s_load_dword s84, %[pq], 0x3840\n\t" \
  "s_add_u32 m0, s37, 36864\n\t" \
  "s_nop 0\n\t" \
  "s_mov_b32 exec_hi, 0\n\t" \
  "buffer_load_dwordx4 v4, s[40:43], s69 offen offset:0 lds\n\t" \
  "s_add_u32 m0, s39, 49152\n\t" \
  "s_mov_b32 exec_hi, -1\n\t" \
  "buffer_load_dwordx4 v5, s[44:47], s70 offen offset:0 lds\n\t" \
  "buffer_load_dwordx4 v6, s[44:47], s70 offen offset:1024 lds\n\t" \
  "buffer_load_dwordx4 v7, s[44:47], s70 offen offset:2048 lds\n\t" \
  "s_add_u32 m0, s39, 61440\n\t" \
  "s_nop 0\n\t" \
  "buffer_load_dwordx4 v5, s[44:47], s71 offen offset:0 lds\n\t" \
  "buffer_load_dwordx4 v6, s[44:47], s71 offen offset:1024 lds\n\t" \
  "buffer_load_dwordx4 v7, s[44:47], s71 offen offset:2048 lds\n\t" \
  "s_memrealtime %[st1]\n\t" \
  "v_readlane_b32 s68, v9, 18\n\t" \
  "v_readlane_b32 s69, v9, 19\n\t" \
  "v_readlane_b32 s70, v9, 20\n\t" \
  "v_readlane_b32 s71, v9, 21\n\t" \
  "v_readlane_b32 s72, v9, 0\n\t" \
  "v_readlane_b32 s73, v9, 1\n\t" \
  "v_readlane_b32 s74, v9, 2\n\t" \
  "v_readlane_b32 s75, v9, 3\n\t" \
  "v_readlane_b32 s76, v9, 4\n\t" \
  "v_readlane_b32 s77, v9, 5\n\t" \
  "s_add_u32 m0, %[ob], 73728\n\t" \
  "s_waitcnt vmcnt(11)\n\t" \
  "s_barrier\n\t" \
  "s_nop 2\n\t" \
  "buffer_load_dwordx4 v8, %[so], s68 offen lds\n\t" \
  "s_add_u32 m0, %[ob], 77824\n\t" \
  "s_nop 0\n\t" \
  "buffer_load_dwordx4 v8, %[so], s69 offen lds\n\t" \
  "s_add_u32 m0, %[ob], 81920\n\t" \
  "s_nop 0\n\t" \
  "buffer_load_dwordx4 v8, %[so], s70 offen lds\n\t" \
  "s_add_u32 m0, %[ob], 86016\n\t" \
  "s_nop 0\n\t" \
  "buffer_load_dwordx4 v8, %[so], s71 offen lds\n\t" \
  "s_add_u32 m0, %[ob], 0\n\t" \
  "s_nop 0\n\t" \
  "buffer_load_dwordx4 v8, %[so], s72 offen lds\n\t" \
  "s_add_u32 m0, %[ob], 4096\n\t" \
  "s_nop 0\n\t" \
  "buffer_load_dwordx4 v8, %[so], s73 offen lds\n\t" \
  "s_add_u32 m0, %[ob], 8192\n\t" \
  "s_nop 0\n\t" \
  "buffer_load_dwordx4 v8, %[so], s74 offen lds\n\t" \
  "s_add_u32 m0, %[ob], 12288\n\t" \
  "s_nop 0\n\t" \
  "buffer_load_dwordx4 v8, %[so], s75 offen lds\n\t" \
  "s_add_u32 m0, %[ob], 16384\n\t" \
  "s_nop 0\n\t" \
  "buffer_load_dwordx4 v8, %[so], s76 offen lds\n\t" \
  "s_add_u32 m0, %[ob], 20480\n\t" \
  "s_nop 0\n\t" \
  "buffer_load_dwordx4 v8, %[so], s77 offen lds\n\t" \
  "s_waitcnt vmcnt(10)\n\t" \
  "s_barrier\n\t" \
  "s_nop 2\n\t" \
  "s_add_u32 m0, %[ob], 90112\n\t" \
  "v_readlane_b32 s68, v9, 22\n\t" \
  "s_nop 4\n\t" \
  "buffer_load_dwordx4 v8, %[so], s68 offen lds\n\t" \
  "s_add_u32 m0, %[ob], 94208\n\t" \
  "v_readlane_b32 s68, v9, 23\n\t" \
  "s_nop 4\n\t" \
  "buffer_load_dwordx4 v8, %[so], s68 offen lds\n\t" \
  "s_add_u32 m0, %[ob], 98304\n\t" \
  "v_readlane_b32 s68, v9, 24\n\t" \
  "s_nop 4\n\t" \
  "buffer_load_dwordx4 v8, %[so], s68 offen lds\n\t" \
  "s_add_u32 m0, %[ob], 102400\n\t" \
  "v_readlane_b32 s68, v9, 25\n\t" \
  "s_nop 4\n\t" \
  "buffer_load_dwordx4 v8, %[so], s68 offen lds\n\t" \
  "s_add_u32 m0, %[ob], 106496\n\t" \
  "v_readlane_b32 s68, v9, 26\n\t" \
  "s_nop 4\n\t" \
  "buffer_load_dwordx4 v8, %[so], s68 offen lds\n\t" \
  "s_add_u32 m0, %[ob], 24576\n\t" \
  "v_readlane_b32 s68, v9, 6\n\t" \
  "s_nop 4\n\t" \
  "buffer_load_dwordx4 v8, %[so], s68 offen lds\n\t" \
  "s_add_u32 m0, %[ob], 28672\n\t" \
  "v_readlane_b32 s68, v9, 7\n\t" \
  "s_nop 4\n\t" \
  "buffer_load_dwordx4 v8, %[so], s68 offen lds\n\t" \
  "s_add_u32 m0, %[ob], 32768\n\t" \
  "v_readlane_b32 s68, v9, 8\n\t" \
  "s_nop 4\n\t" \
  "buffer_load_dwordx4 v8, %[so], s68 offen lds\n\t" \
  "s_add_u32 m0, %[ob], 36864\n\t" \
  "v_readlane_b32 s68, v9, 9\n\t" \
  "s_nop 4\n\t" \
  "buffer_load_dwordx4 v8, %[so], s68 offen lds\n\t" \
  "s_add_u32 m0, %[ob], 40960\n\t" \
  "v_readlane_b32 s68, v9, 10\n\t" \
  "s_nop 4\n\t" \
  "buffer_load_dwordx4 v8, %[so], s68 offen lds\n\t" \
  "s_add_u32 m0, %[ob], 45056\n\t" \
  "v_readlane_b32 s68, v9, 11\n\t" \
  "s_nop 4\n\t" \
  "buffer_load_dwordx4 v8, %[so], s68 offen lds\n\t" \
  "s_nop 3\n\t" \
  "s_barrier\n\t" \
  "s_nop 2\n\t" \
  "s_add_u32 m0, %[ob], 110592\n\t" \
  "v_readlane_b32 s68, v9, 27\n\t" \
  "s_nop 4\n\t" \
  "buffer_load_dwordx4 v8, %[so], s68 offen lds\n\t" \
  "s_add_u32 m0, %[ob], 114688\n\t" \
  "v_readlane_b32 s68, v9, 28\n\t" \
  "s_nop 4\n\t" \
  "buffer_load_dwordx4 v8, %[so], s68 offen lds\n\t" \
  "s_add_u32 m0, %[ob], 118784\n\t" \
  "v_readlane_b32 s68, v9, 29\n\t" \
  "s_nop 4\n\t" \
  "buffer_load_dwordx4 v8, %[so], s68 offen lds\n\t" \
  "s_add_u32 m0, %[ob], 122880\n\t" \
  "v_readlane_b32 s68, v9, 30\n\t" \
  "s_nop 4\n\t" \
  "buffer_load_dwordx4 v8, %[so], s68 offen lds\n\t" \
  "s_add_u32 m0, %[ob], 126976\n\t" \
  "v_readlane_b32 s68, v9, 31\n\t" \
  "s_nop 4\n\t" \
  "buffer_load_dwordx4 v8, %[so], s68 offen lds\n\t" \
  "s_add_u32 m0, %[ob], 49152\n\t" \
  "v_readlane_b32 s68, v9, 12\n\t" \
  "s_nop 4\n\t" \
  "buffer_load_dwordx4 v8, %[so], s68 offen lds\n\t" \
  "s_add_u32 m0, %[ob], 53248\n\t" \
  "v_readlane_b32 s68, v9, 13\n\t" \
  "s_nop 4\n\t" \
  "buffer_load_dwordx4 v8, %[so], s68 offen lds\n\t" \
  "s_add_u32 m0, %[ob], 57344\n\t" \
  "v_readlane_b32 s68, v9, 14\n\t" \
  "s_nop 4\n\t" \
  "buffer_load_dwordx4 v8, %[so], s68 offen lds\n\t" \
  "s_add_u32 m0, %[ob], 61440\n\t" \
  "v_readlane_b32 s68, v9, 15\n\t" \
  "s_nop 4\n\t" \
  "buffer_load_dwordx4 v8, %[so], s68 offen lds\n\t" \
  "s_add_u32 m0, %[ob], 65536\n\t" \
  "v_readlane_b32 s68, v9, 16\n\t" \
  "s_nop 4\n\t" \
  "buffer_load_dwordx4 v8, %[so], s68 offen lds\n\t" \
  "s_add_u32 m0, %[ob], 69632\n\t" \
  "v_readlane_b32 s68, v9, 17\n\t" \
  "s_nop 4\n\t" \
  "buffer_load_dwordx4 v8, %[so], s68 offen lds\n\t" \
  "s_waitcnt vmcnt(0) lgkmcnt(0)\n\t"
#define LD_NBAR 17
#define LD_CO 0
#define LD_X5 1
#define LD_HT 8
#define LD_TP 3
#define LD_CLOB "v11", "s68", "s69", "s70", "s71", "s72", "s73", "s74", "s75", "s76", "s77", "s78", "s79", "s80", "s81", "s82", "s83", "s85"

#define ENDW WAITV(0);
#define TS(i) do{ T[i]=__builtin_amdgcn_s_memrealtime(); }while(0)
#define TSS(i)
#define TOUT(n) do{ if(l==0){ unsigned long long* dbg=(unsigned long long*)(flags+65536)+(bid*12+w8)*8; for(int i_=1;i_<(n);i_++) dbg[i_-1]=T[i_]-T[0]; dbg[6]=R0; dbg[7]=__builtin_amdgcn_s_memrealtime(); } }while(0)

#define SPIN(cond,slp) do{ int sp_=0; while(cond){ __builtin_amdgcn_s_sleep(slp); if(++sp_>(1<<22)) break; } }while(0)
__device__ __forceinline__ unsigned rd1(const char* p){ return __builtin_amdgcn_readfirstlane(*(volatile LDS_AS unsigned*)(LDS_AS char*)p); }
__device__ __forceinline__ unsigned lds_inc(const char* p, int l){ unsigned r=0; if(l==0) r=__hip_atomic_fetch_add((LDS_AS unsigned*)(LDS_AS char*)p, 1u, __ATOMIC_RELAXED, __HIP_MEMORY_SCOPE_WORKGROUP); return __builtin_amdgcn_readfirstlane(r); }
__device__ __forceinline__ void lds_add(unsigned adr){ asm volatile("s_mov_b64 exec, 1\n\tds_add_u32 %0, %1\n\ts_mov_b64 exec, -1" :: "v"(adr), "v"(1u) : "memory"); }
__device__ __forceinline__ void publish(unsigned cnt, const unsigned* base, unsigned voff, unsigned tag){
  unsigned t;
  asm volatile("s_waitcnt vmcnt(0)\n\ts_mov_b64 exec, 1\n\tds_add_rtn_u32 %0, %1, %2\n\ts_waitcnt lgkmcnt(0)\n\tv_cmp_eq_u32 vcc, 3, %0\n\ts_cbranch_vccz PB_%=\n\t"
    "s_mov_b64 exec, 0xffff\n\tglobal_store_dword %3, %4, %5 sc1\n\tPB_%=:\n\ts_mov_b64 exec, -1"
    : "=&v"(t) : "v"(cnt), "v"(1u), "v"(voff), "v"(tag), "s"(base) : "memory", "vcc");
}
__device__ __forceinline__ void bars(int n){
  asm volatile("BL_%=:\n\ts_barrier\n\ts_sub_u32 %0, %0, 1\n\ts_cmp_lg_u32 %0, 0\n\ts_cbranch_scc1 BL_%=" : "+s"(n) :: "memory", "scc");
}
#define barc(rem) barc_(rem)
#define CTRA
#define CTRP
__device__ __forceinline__ void barc_(unsigned& rem){
  asm volatile("s_cmp_eq_u32 %0, 0\n\ts_cbranch_scc1 BC_%=\n\ts_barrier\n\ts_sub_u32 %0, %0, 1\n\tBC_%=:" : "+s"(rem) :: "memory", "scc");
}
template<int SLP> __device__ __forceinline__ void wait_flag_b(const unsigned* p, unsigned voff, unsigned tag, unsigned& rem CTRA){
  int spins=0;
  while(true){
    unsigned v; asm volatile("global_load_dword %0, %1, %2 sc1" : "=v"(v) : "v"(voff), "s"(p) : "memory");
    barc(rem); if(PB2) barc(rem);
    asm volatile("s_waitcnt vmcnt(0)" : "+v"(v) :: "memory");
    if(__all(v==tag) || ++spins>(1<<21)) break;
    if(rem==0) __builtin_amdgcn_s_sleep(SLP);
  }
}
__device__ __forceinline__ void stout(void* sbase, unsigned voff, u32x4 v){
  st16cs(sbase,voff,v);
}

typedef unsigned u32x8 __attribute__((ext_vector_type(8)));
typedef unsigned u32x16 __attribute__((ext_vector_type(16)));
__device__ __forceinline__ char* ptr64(unsigned lo, unsigned hi){ return (char*)(((unsigned long long)hi<<32)|lo); }
__device__ __forceinline__ u32x4 srd(const char* p, unsigned bytes){ return u32x4{(unsigned)(size_t)p,(unsigned)((size_t)p>>32)&0xffffu,bytes,0x20000u}; }
constexpr size_t WS_O=0, WS_KV=(size_t)S*DM*2, WS_Y=(size_t)S*DM*4, WS_FL=(size_t)S*DM*12;

__global__ __launch_bounds__(768,3) void fused_kernel(const u16* x_, const u16* wqkv_, const u16* wo_, char* ws_, u16* out_,
    const u16* kin_, const u16* vin_, const u16* shk_, const u16* hk_, const u16* qin_, const u16* sD_)
{
  __shared__ __attribute__((aligned(128))) char smem[ALL_LDS];
  unsigned long long T[8]; unsigned long long R0=__builtin_amdgcn_s_memrealtime();
  TS(0);
  int wv;
  asm volatile(
      "s_load_dwordx16 s[48:63], %[ka], 0x0\n\ts_load_dwordx4 s[64:67], %[ka], 0x40\n\ts_load_dwordx2 s[78:79], %[ka], 0x50\n\ts_getpc_b64 s[72:73]\n\tv_readfirstlane_b32 s75, v0\n\ts_lshr_b32 s74, %[b], 3\n\ts_mul_i32 s74, s74, 12\n\ts_lshr_b32 s75, s75, 6\n\t"
      "s_add_u32 s74, s74, s75\n\ts_min_u32 s74, s74, %[cl]\n\ts_lshl_b32 s74, s74, 7\n\ts_mov_b32 m0, %[sk]\n\tv_mov_b32 v1, s74\n\ts_mov_b64 exec, 1\n\tglobal_load_lds_dword v1, s[72:73]\n\ts_mov_b64 exec, -1\n\t"
      "s_mov_b32 s76, %[b]\n\ts_cmp_lt_u32 s75, 4\n\ts_cbranch_scc1 FK_G\n\ts_cmp_lt_u32 s75, 8\n\ts_cbranch_scc0 FK_C\n\t"
      "v_and_b32 v9, 7, v0\n\tv_and_b32 v10, 63, v0\n\ts_lshl_b32 s74, %[b], 19\n\ts_lshl_b32 s73, %[b], 11\n\ts_or_b32 s74, s74, s73\n\ts_and_b32 s74, s74, 0x3fc000\n\ts_lshl_b32 s73, s75, 9\n\t"
      "s_add_u32 s74, s74, s73\n\ts_add_u32 s74, s74, 0x7800\n\tv_lshl_add_u32 v9, v9, 11, s74\n\tv_and_b32 v1, 24, v0\n\ts_lshl2_add_u32 s74, %[b], s75\n\ts_mul_i32 s74, s74, %[nw]\n\tv_lshl_add_u32 v9, v1, 4, v9\n\t"
      "s_add_u32 s74, s74, %[w0]\n\tv_add_lshl_u32 v10, v10, s74, 7\n\t"
      "s_setprio %[lp]\n\ts_and_b32 s74, s75, 3\n\tv_lshrrev_b32 v1, 3, v0\n\tv_lshrrev_b32 v3, 4, v0\n\ts_and_b32 s72, %[b], 1\n\ts_and_b32 s73, %[b], 0xe0\n\ts_lshl_b32 s72, s72, 10\n\ts_lshl2_add_u32 s72, s73, s72\n\t"
      "v_and_b32 v4, 2, v1\n\tv_and_or_b32 v3, v3, 4, v4\n\tv_and_b32 v4, 31, v1\n\tv_xor_b32 v3, v3, v0\n\tv_mul_u32_u24 v4, 0x1800, v4\n\tv_and_b32 v3, 7, v3\n\tv_lshl_add_u32 v3, v3, 4, s72\n\t"
      "s_mul_i32 s39, s74, 0xc00\n\ts_lshl_b32 s38, s74, 12\n\ts_lshl_b32 s37, s74, 9\n\tv_add_u32 v5, v3, v4\n\tv_and_b32 v1, 7, v1\n\tv_add_u32 v6, 0x400, v5\n\tv_add_u32 v7, 0x800, v5\n\t"
      "s_and_b32 s72, %[b], 6\n\ts_and_b32 s73, %[b], 24\n\ts_lshl4_add_u32 s72, s72, s73\n\tv_xor_b32 v3, v1, v0\n\ts_lshl_b32 s72, s72, 15\n\tv_and_b32 v3, 7, v3\n\tv_lshl_add_u32 v1, v1, 11, s72\n\t"
      "s_mov_b32 s47, 0x20000\n\ts_mov_b32 s46, 0x600000\n\tv_lshl_add_u32 v1, v3, 4, v1\n\ts_mov_b32 s43, s47\n\ts_mov_b32 s42, 0x408000\n\ts_lshl_b32 s72, s74, 6\n\ts_and_b32 s72, s72, 64\n\t"
      "v_xor_b32 v4, s72, v1\n\ts_lshl_b32 s72, s74, 13\n\ts_add_u32 s72, s72, 0x40000\n\tv_add_u32 v4, s72, v4\n\ts_lshl_b32 s72, s74, 16\n\tv_add_u32 v0, s72, v1\n\ts_movk_i32 s73, 0x3c00\n\t"
      "v_add_u32 v1, s73, v0\n\ts_add_u32 s38, s38, %[xf]\n\tv_add_u32 v2, s73, v1\n\ts_add_u32 s37, s37, %[xf]+16384\n\tv_add_u32 v3, s73, v2\n\tv_max_u32 v0, 0x8000, v0\n\tv_max_u32 v1, 0x8000, v1\n\t"
      "s_waitcnt lgkmcnt(0)\n\ts_mov_b64 s[44:45], s[50:51]\n\ts_sub_u32 s40, s48, 0x8000\n\ts_subb_u32 s41, s49, 0\n\t"
      LD_ASM1
      "s_branch FK_L"
      : "=s"(wv)
      : "{v0}"(threadIdx.x), [ka]"s"(__builtin_amdgcn_kernarg_segment_ptr()), [b]"s"(blockIdx.x), [cl]"n"(CODEL-1+CODEX), [sk]"n"(SINK+256), [lp]"n"(LPR), [xf]"n"(X_OFF), [nw]"n"(3*TPW), [w0]"n"(TW0*3072-12*TPW), [wm]"n"((1<<(3*TPW))-1)
      : "memory", "scc", "vcc", "s48", "s49", "s50", "s51", "s52", "s53", "s54", "s55", "s72", "s73", "s74", "s75", "s76", "s78", "s79", "v1", "v2", "v3", "v4", "v5", "v6", "v7", "v9", "v10",
        "s40", "s41", "s42", "s43", "s44", "s45", "s46", "s47", "s37", "s38", "s39", "s56", "s57", "s58", "s59", "s60", "s61", "s62", "s63", "s64", "s65", "s66", "s67", "s68");
  #define ENTER(lbl) int w8, bid; u32x8 a0, a1; u32x4 a2; u32x2 a3; asm volatile(lbl ":" : "={s75}"(w8), "={s76}"(bid), "={s[48:55]}"(a0), "={s[56:63]}"(a1), "={s[64:67]}"(a2), "={s[78:79]}"(a3) :: "memory");
  #define ARGW asm volatile("s_waitcnt lgkmcnt(0)" : "+{s[48:55]}"(a0), "+{s[56:63]}"(a1), "+{s[64:67]}"(a2), "+{s[78:79]}"(a3));
  #define ROLE const int w=w8&3, xcd=bid&7, idx=bid>>3, rb=(xcd>>1)*4+(idx&3), hp=(xcd&1)*8+(idx>>2), s0=rb*128; (void)s0; (void)hp;
  #define LANEVARS int l; asm volatile("v_mbcnt_lo_u32_b32 %0, -1, 0\n\tv_mbcnt_hi_u32_b32 %0, -1, %0" : "=v"(l)); const int g=l>>4, fr=l&15, tq=(l&15)>>2, tp=l&3; (void)g; (void)fr; (void)tq; (void)tp;
  #define ARGS0 const char* x=ptr64(a0[0],a0[1]); const char* wqkv=ptr64(a0[2],a0[3]); const char* wo=ptr64(a0[4],a0[5]); char* ws=ptr64(a0[6],a0[7]); \
    unsigned* flags=(unsigned*)(ws+WS_FL); const int hot0=(3-(int)(a0[2]>>7))&7; (void)x; (void)wqkv; (void)wo; (void)flags; (void)hot0;
  #define OCOL ((hp&8)|((hp+(int)(a0[2]>>7)-(int)(a0[4]>>7)+OSH)&7))
  #define ORA ((unsigned)(hp-hot0-1)&7u)
  #define ORE(c) (ORA==7u? (c)-1u : (c))
  #define ORO(c) (ORE(c)>=NF && ORE(c)<NF+7u)
  #define ORD(c) (((((ORE(c)<14u? ((ORA==7u? 0u:ORA)+(ORO(c)? ORE(c)-NF : (ORE(c)<NF? ORE(c) : ORE(c)-7u)))%7u : 7u)+hot0+1)&7u))+8u*(((hp>>3)^(ORO(c) || (c)==15u))&1u))
  const unsigned sb0=__builtin_amdgcn_readfirstlane(lds_addr(smem));
  const char* syn=smem+SYN;

  if(__builtin_expect(wv==1,1)){
    ENTER("FK_L")
    u32x4 sx, sw; unsigned xt, xl, wl, xoA, xoB, xo, xo1, xo2, wof[3];
    asm volatile("; descriptors and LDS bases" : "={s[40:43]}"(sx), "={s[44:47]}"(sw), "={s38}"(xl), "={s39}"(wl), "={s37}"(xt));
    asm volatile("; lane offsets" : "={v0}"(xoA), "={v1}"(xoB), "={v2}"(xo), "={v3}"(xo1), "={v4}"(xo2), "={v5}"(wof[0]), "={v6}"(wof[1]), "={v7}"(wof[2]));
    unsigned ltr=0, ltr2=0, ltr3=0, sd; (void)ltr; (void)ltr2; (void)ltr3;
    const char* pw;
    {
      ROLE
      LANEVARS
      ARGS0
      const int hg=((idx>>2)-hot0)&7, hi=hot0*4+(idx&3);
      const unsigned long long hm= hg==0? 1ull : hg==HGR? 0xfffull : 0x3full;
      const unsigned hto=(unsigned)((hg? w*393216 : 0x40000000)+(l<6? l*2048+idx*12288 : (l-6)*2048+hi*12288));
      pw=wqkv+(idx*6144+(xcd&1)*1024+(xcd>>1)*256+w*64);
      asm volatile(LD_ASM2
        : "=&{s84}"(sd), "+s"(w8), "+s"(bid), "+s"(a0)
        : "{v0}"(xoA), "{v1}"(xoB), "{v2}"(xo), "{v3}"(xo1), "{v4}"(xo2), "{v5}"(wof[0]), "{v6}"(wof[1]), "{v7}"(wof[2]),
          "{v16}"(hto), [sh]"s"(srd(wqkv+((xcd&1)*8+hot0)*128,6291456u-1920u)), [hm]"s"(hm),
          "{s[40:43]}"(sx), "{s[44:47]}"(sw), "{s37}"(xt), "{s38}"(xl), "{s39}"(wl), [pw]"s"(pw), [tsk]"n"(SINK+256)
        : "memory", "scc", "vcc", LD_CLOB);
    }
    ROLE
    LANEVARS
    ARGS0
    const unsigned wd=OCOL*128;
    {
      const unsigned wof2=(unsigned)(w*16384+(l>>3)*2048+((((l&7)>>1)^((l>>4)&3))*32)+(l&1)*16);
      const unsigned to=ORD((unsigned)(l>>1)&15u);
      const unsigned tab=to*131072u+(l&1)*65536;
      asm volatile(LD_ASM3
        : "+{s84}"(sd)
          , [st1]"=&s"(T[1])
        : "{v0}"(xoA), "{v1}"(xoB), "{v2}"(xo), "{v3}"(xo1), "{v4}"(xo2), "{v5}"(wof[0]), "{v6}"(wof[1]), "{v7}"(wof[2]), "{v8}"(wof2), "{v9}"(tab),
          "{s[40:43]}"(sx), "{s[44:47]}"(sw), [so]"s"(srd(wo+wd,2097152u-wd)), "{s37}"(xt), "{s38}"(xl), "{s39}"(wl), [ob]"s"(sb0+w*1024), [pw]"s"(pw),
          [pq]"s"(wo+((xcd&1)*1024+((3-(int)(a0[4]>>7))&7)*128+(idx*32+w*8)*2048))
        : "memory", "scc", "vcc", LD_CLOB);
    }
    const unsigned tag=rd1(syn+64)+1u;
    lds_add(sb0+SYN+12);
    TS(2);
    if(w>=4-NPOL){
      const unsigned* fp=flags+F_O+(rb*16+hp)*16; const unsigned fo=ORD((unsigned)l&15u)*4u;
      const unsigned madr=sb0+(l==0? SYN+20 : SINK+l*4);
      SPIN(rd1(syn+12)<4u || rd1(syn+16)<4u,1);
      if(w==3) PUBM(madr,1u);
      if(PD0) __builtin_amdgcn_s_sleep(PD0);
      if(w==2) __builtin_amdgcn_s_sleep(PD1); if(w==1) __builtin_amdgcn_s_sleep(2*PD1); if(w==0) __builtin_amdgcn_s_sleep(3*PD1);
      unsigned rdy=1; int spins=0;
      while(rdy!=0xFFFFu){
        unsigned v;
        asm volatile("s_mov_b64 exec, 0xffff\n\tglobal_atomic_or %0, %1, %3, %2 sc0 sc1\n\ts_mov_b64 exec, -1\n\ts_waitcnt vmcnt(0)" : "=&v"(v) : "v"(fo), "s"(fp), "v"(0u) : "memory");
        unsigned r=((unsigned)__ballot(v==tag)&0xFFFFu)|1u;
        if(++spins>(1<<21)) r=0xFFFFu;
        if(r!=rdy){ PUBM(madr,(unsigned)__builtin_ctz(~r)); rdy=r; if(w<3 && (r&0x3FFFu)==0x3FFFu) break; }
        else __builtin_amdgcn_s_sleep(OSLP);
      }
      TS(3);
      if(bid==0 && w==3){
        if(GWD){ SPIN(rd1(syn+28)==0u,8); __builtin_amdgcn_s_sleep(GWD); }
        int sp=0;
        while(true){
          u32x4 v; asm volatile("global_load_dwordx4 %0, %1, %2 sc1\n\ts_waitcnt vmcnt(0)" : "=&v"(v) : "v"(l*16), "s"(flags+F_Y) : "memory");
          if(__all(v[0]==tag && v[1]==tag && v[2]==tag && v[3]==tag) || ++sp>(1<<20)) break;
          __builtin_amdgcn_s_sleep(12);
        }
        if(l==0) stflag(flags, tag);
      }
    } else { TS(3); }
    if(l==0){ unsigned long long pc; asm volatile("s_getpc_b64 %0" : "=s"(pc)); ((unsigned long long*)(flags+65536)+(bid*12+w8)*8)[4]=pc; }
    TOUT(4);
    __builtin_amdgcn_endpgm();
  }

  if(wv==0){
    ENTER("FK_G")
    ROLE
    __builtin_amdgcn_s_setprio(GPR);
    const int h=w&1, r=w>>1;
    LANEVARS
    TSS(3);
    ARGW
    ARGS0
    if(PGT&1){ if(w==1 && l<7) touch(ws, l<6? l*2097152+PGO : (unsigned)WS_FL+2048, sb0+SINK+512); }
    if(PGT&2){ if(w==2 && l<4) touch(ptr64(a1[0],a1[1]), l*2097152+PGO, sb0+SINK+512); }
    if(PGT&4){ if(w==3 && l<1) touch(wo, PGO, sb0+SINK+512); if(w==0 && l<2) touch(wqkv, l*2097152+2097152, sb0+SINK+512); }
    TSS(4);
    {
      unsigned lx0=sb0+X_OFF+fr*128+((g^(fr&7))*16), lx1=sb0+X_OFF+fr*128+(((4+g)^(fr&7))*16);
      const int sw=wswz(8*g+tq);
      unsigned lw0=sb0+g*3072+tq*128+(unsigned)((((2*h)^sw)*32)+8*tp), lw1=sb0+g*3072+tq*128+(unsigned)((((2*h+1)^sw)*32)+8*tp);
      char* ot=ws+WS_O+(rb*16+hp)*16384+r*8192+h*2048;
      unsigned it; unsigned long long rm; asm("s_cmp_lg_u32 %1, 0\n\ts_cselect_b64 %0, -1, 0" : "=s"(rm) : "s"(r) : "scc");
      asm volatile(N2_ASM
        : [it]"=&s"(it), "+{v56}"(lx0), "+{v57}"(lx1), "+{v58}"(lw0), "+{v59}"(lw1)
          , [st1]"=&s"(T[1]), [st2]"=&s"(T[2])
        : "{v62}"(l*16), "{v63}"(sb0+EX_OFF+h*3072+l*16), [r]"s"(r), [ot]"s"(ot), [ou]"s"(ot+4096), [fa]"s"(sb0+SYN+32+w*4), [fp]"s"(sb0+SYN+32+(w^2)*4),
          [vo]"s"(2048-1024*r), [rm]"s"(rm), [z]"s"(rb|r)
        : "memory", "scc", "vcc", N2_CLOB);
    }
    TS(3);
    unsigned tb;
    {
      LANEVARS
      const unsigned tag=rd1(syn+64)+1u;
      const unsigned ra=sb0+(4*g+tq)*128+8*tp; const int sw=(2*g+(tq>>1))&3;
      unsigned b0=ra+((0^sw)*32), b1=ra+((1^sw)*32), b2=ra+((2^sw)*32), b3=ra+((3^sw)*32), vo=(unsigned)(w*4096+l*16), sy=sb0+SYN+20;
      unsigned sr=sb0+EX_OFF+w*2048+(l>>3)*128+(((l&7)^(l>>3))*16), sg=sb0+EX_OFF+w*2048+fr*128+(((g>>1)^(fr&7))*16)+(g&1)*8;
      tb=ORD((unsigned)l&15u)*16384u;
      asm volatile("" : "+{v160}"(b0), "+{v161}"(b1), "+{v162}"(b2), "+{v163}"(b3), "+{v164}"(vo), "+{v165}"(sy), "+{v166}"(sr), "+{v167}"(sg), "+{v96}"(tb));
      publish(sb0+SYN+16, flags+F_O+rb*256+hp, l*64, tag);
      TS(4);
      __builtin_amdgcn_s_setprio(G2P);
      unsigned iss=0, q0, q1, q2, cn=0;
      asm volatile(G3_ASM
        : [iss]"+&s"(iss), [t0]"=&s"(q0), [t1]"=&s"(q1), [t2]"=&s"(q2), [cn]"+&s"(cn),
          "+{v160}"(b0), "+{v161}"(b1), "+{v162}"(b2), "+{v163}"(b3), "+{v165}"(sy), "+{v96}"(tb)
          , [st5]"=&s"(T[5])
        : "{v164}"(vo), "{v166}"(sr), "{v167}"(sg), [go]"s"(60+w*4), [sy]"n"(SYN+20), [ob]"s"(srd(ws+WS_O+rb*262144,262144u)), [op]"s"(ptr64(a1[0],a1[1])+((s0+w*32)*4096+2048+OCOL*128))
        : "memory", "scc", "vcc", G3_CLOB);
    }
    {
      LANEVARS
      ENDW TS(6); TOUT(7);
    }
    __builtin_amdgcn_endpgm();
  }

  {
    ENTER("FK_C")
    ROLE
    char* cr=smem+C_OFF+2048+w*4096;
    const unsigned crs=sb0+C_OFF+2048+w*4096;
    const int sblk=bid>>3, td0=(bid&7)*128+(w&1)*64;
    const int vb=(LOCA&1)? xcd*32+(idx&16)+(idx&1)*8+((idx>>1)&7) : bid, fb=(LOCA&2)? (idx&15)*16+xcd*2+(idx>>4) : bid;
    const int d=(vb>>4)*64+(((vb>>3)&1)*4+w)*8+(vb&7);
    int kv0=KV0+((sblk>>KSH)&KVS);
    static_assert(KV0+KVS+PP_NB<=LD_NBAR && KV0>=EB, "barrier budget");
    unsigned tagv, ctr; f32x8 RB;
    const int fd0=(fb&15)*64, fs0=(fb>>4)*128+w*32;
    u32x4 qv[4];
    f32x16 A0,A1,A2,A3;
    const int rw=sblk*64+(w>>1)*32, j=rw>>5;
    unsigned k0,k1,k2,ko,ta,se,so,ha,l16,rb0[2];
    {
      LANEVARS
      if(w==0 && l<25 && l!=16) ((unsigned*)(smem+SYN))[l]=0u;
      if(l<32) *(u32x4*)(smem+C_OFF+w*512+l*16)=u32x4{0,0,0,0};
      asm volatile("" ::: "memory");
      __builtin_amdgcn_s_setprio(CPR);
      const int dg=l&7, rg=l>>3;
      ko=(unsigned)(rg*8192+dg*16);
      const bool top=(rw==0 && rg==0);
      k0=top? ko+6144:ko; k1=top? ko+6144:ko+2048; k2=top? ko+6144:ko+4096;
      ta=sb0+EX_OFF+(PP_SM? 0:w*1024)+(w&1)*512+dg*64; se=(l&8)? 0x03020706u : 0x05040100u; ha=crs+(32*g-4*tq+4*tp)*2; l16=l*16;
      so=(unsigned)(rg*32768+((j>>2)&3)*128+((dg&4)|((dg+j)&3))*16);
      #pragma unroll
      for(int k=0;k<2;k++){ const int q=fr-1-2*g+k; rb0[k]=sb0+C_OFF+(4+(q>>1))*256+((q>>3)&3)*64+((w+(q>>1))&3)*16+(q&1)*8; }
      asm volatile("" : "+{v152}"(k0), "+{v153}"(k1), "+{v154}"(k2), "+{v155}"(ko), "+{v156}"(ta), "+{v157}"(se), "+{v159}"(so), "+{v163}"(l16), "+{v165}"(ha), "+{v166}"(rb0[0]), "+{v167}"(rb0[1]));
    }
    TSS(3);
    ARGW
    if(EB==1) asm volatile("s_barrier" : "+s"(a1), "+s"(a2), "+s"(a3), "+s"(a0) :: "memory");
    if(EB==2) asm volatile("s_barrier\n\ts_barrier" : "+s"(a1), "+s"(a2), "+s"(a3), "+s"(a0) :: "memory");
    ARGS0
    char* out=ptr64(a1[0],a1[1]); const char* qp=ptr64(a2[2],a2[3])+(fs0*DM+fd0)*2;
    {
      const char* kb=ptr64(a1[2],a1[3])+(rw*DM+td0)*2; const char* vb=ptr64(a1[4],a1[5])+(rw*DM+td0)*2;
      unsigned long long zm; asm("s_cmp_eq_u32 %1, 0\n\ts_cselect_b64 %0, 0xff, 0" : "=s"(zm) : "s"(rw) : "scc");
      unsigned t0;
      if(PGT&8){ const char* tp= w==0? kb : w==1? vb : w==2? qp : ptr64(a2[0],a2[1])+d*4096;
        asm volatile("s_mov_b32 m0, %1\n\ts_mov_b64 exec, 1\n\tglobal_load_lds_dword v163, %0\n\ts_mov_b64 exec, -1" :: "s"(tp), "s"(sb0+SINK+512), "{v163}"(l16) : "memory"); }
      TSS(5);
      asm volatile(PP_ASM
        : [t0]"=&s"(t0), "=&{v164}"(ctr), "+{v159}"(so), "=&{v160}"(tagv), "=&{v[24:31]}"(RB), "=&{v[44:47]}"(qv[0]), "=&{v[48:51]}"(qv[1]), "=&{v[52:55]}"(qv[2]), "=&{v[56:59]}"(qv[3]),
          "=&{v[64:79]}"(A0), "=&{v[80:95]}"(A1), "=&{v[96:111]}"(A2), "=&{v[112:127]}"(A3)
          , [st1]"=&s"(T[1]), [st2]"=&s"(T[2])
        : "{v152}"(k0), "{v153}"(k1), "{v154}"(k2), "{v155}"(ko), "{v156}"(ta), "{v157}"(se), "{v166}"(rb0[0]), "{v167}"(rb0[1]), "{v165}"(ha), "{v163}"(l16),
          [qb0]"s"(qp), [qb2]"s"(qp+4096), [mk]"s"(0x180u), [em]"s"(1ull|(512ull<<w)), [hd]"s"(crs),
          [kb0]"s"(kb-6144), [kb3]"s"(kb), [kb5]"s"(kb+4096), [vb0]"s"(vb), [vb2]"s"(vb+4096), [sb]"s"(ws+WS_KV+td0*4096+j*512),
          [n0]"s"(kv0-EB-(HH==2)), [ff]"s"(flags+F_PP+(td0>>6)*64+j), [zm]"s"(zm), [shd]"s"(sb0+EX_OFF+w*1024), [tgd]"s"(sb0+SYN+64), [shp]"s"(ptr64(a1[6],a1[7])+(bid&7)*1024), [fl]"s"(flags),
          [hp]"s"(ptr64(a2[0],a2[1])+d*4096), [dp]"s"(ptr64(a3[0],a3[1])+(PP_SM? d&~1:d)*2), [dsh]"s"((d&1)*16), [h0]"n"(C_OFF+2048),
          [ph]"s"(wqkv+(((xcd&1)*8+hot0)*128+idx*6144+(w>>1)*196608+(w&1)*64))
        : "memory", "scc", PP_CLOB);
    }
    LANEVARS
    const unsigned tag=__builtin_amdgcn_readfirstlane(tagv)+1u;
    unsigned rem=LD_NBAR-PP_NB-kv0;
    barc(rem);
    const unsigned* pf=flags+F_PP+(d>>6)*64+(KPOL? w*16:0);
    const char* kvp=ws+WS_KV+((vb>>4)*8+(vb&7))*32768+((vb>>3)&1)*64+w*8192;
    const unsigned kvo=(unsigned)((l>>2)*128+(l&3)*16);
    int bidx=((l>=16)? l-16 : (l==0? 63 : l+47))*4;
    asm volatile("" : "+{v32}"(bidx));
    __builtin_amdgcn_s_setprio(3);
    while(rem>PREM) barc(rem);
    wait_flag_b<8>(pf, (KPOL? fr:l)*4, tag, rem CTRP);
    #pragma unroll
    for(int i=0;i<4;i++){ if(i%KTH==0 && i) barc(rem); gldsc(kvp+i*2048, kvo, crs+i*1024); }
    barc(rem); if(PB2) barc(rem);
    WAITV(0);
    asm volatile("s_mov_b32 m0, %2\n\ts_mov_b64 exec, 1\n\tglobal_load_lds_dword %0, %1 sc1\n\ts_mov_b64 exec, -1" :: "v"(0u), "s"(pf), "s"(sb0+SYN+100+w*4) : "memory");
    f32x16 ac0, ac1; unsigned vtr, cn=0; (void)vtr;
    unsigned ca=sb0+CAR+w*1280+(fr*4+g)*4, cz= l? ca : sb0+C_OFF;
    asm volatile("s_nop 1\n\t" CV_ASM
      : "=&{v[128:143]}"(ac0), "=&{v[144:159]}"(ac1), [rem]"+&s"(rem), [cn]"+&s"(cn), "+{v40}"(ca), "+{v41}"(cz)
        , "+{v[44:47]}"(qv[0]), "+{v[48:51]}"(qv[1]), "+{v[52:55]}"(qv[2]), "+{v[56:59]}"(qv[3])
        , [st3]"=&s"(T[3])
      : "{v[64:79]}"(A0), "{v[80:95]}"(A1), "{v[96:111]}"(A2), "{v[112:127]}"(A3),
        "{v[24:31]}"(RB), "{v32}"(bidx), "{v33}"(sb0+SYN+80+w*4), "{v61}"(sb0+SYN+100+(l&3)*4), [tg]"s"(tag),
        "{v42}"((256*(g&1)+16*fr+4*(g&2))*2), [yb]"s"(ws+WS_Y+d*4096), "{v43}"((l>>3)*8192+(l&7)*16), [q0]"s"(qp), [q2]"s"(qp+4096)
      : "memory", "scc", CV_CLOB);
    TS(4);
    __builtin_amdgcn_s_setprio(3);
    publish(sb0+SYN+24, flags+F_Y+vb, l*1024, tag);
    asm volatile("" : "+v"(qv[0]), "+v"(qv[1]), "+v"(qv[2]), "+v"(qv[3]));
    TS(5);
    float qf[32];
    #pragma unroll
    for(int i=0;i<16;i++){ qf[2*i]=bflo(qv[i>>2][i&3]); qf[2*i+1]=bfhi(qv[i>>2][i&3]); }
    unsigned ya=crs+(g*8+tq)*64+tp*8, wa0=crs+fr*128+((g^(fr&7))*16), wa1=crs+fr*128+(((4+g)^(fr&7))*16), ra[4], oo=(unsigned)((l>>3)*16384+(l&7)*16), yo2=(l>>2)*4096+(l&3)*16;
    #pragma unroll
    for(int i=0;i<4;i++){ const int row=4*(l>>3)+i; ra[i]=crs+row*128+(((l&7)^(row&7))*16); }
    char* op=out+fs0*4096+fd0*2; char* op2=op+8192; const char* yg=ws+WS_Y+fd0*4096+fs0*2;
    #pragma unroll
    for(int i=0;i<32;i++) asm volatile("" : "+v"(qf[i]));
    asm volatile("" : "+v"(ya), "+v"(wa0), "+v"(wa1), "+v"(ra[0]), "+v"(ra[1]), "+v"(ra[2]), "+v"(ra[3]), "+v"(oo), "+v"(yo2));
    asm volatile("" : "+s"(op), "+s"(op2), "+s"(yg));
    if((YPM>>w)&1){
      SPIN(rd1(syn+24)<4u,1);
      if(YPM>1){ if(w==1) __builtin_amdgcn_s_sleep(YPD); if(w==2) __builtin_amdgcn_s_sleep(2*YPD); if(w==3) __builtin_amdgcn_s_sleep(3*YPD); }
      { int sp=0;
        while(true){
          unsigned v=tag; asm volatile("s_mov_b64 exec, 0xffff\n\tglobal_atomic_or %0, %1, %3, %2 sc0 sc1\n\ts_mov_b64 exec, -1\n\ts_waitcnt vmcnt(0)" : "+&v"(v) : "v"(l*4), "s"(flags+F_Y+fb*16), "v"(0u) : "memory");
          if(__all(v==tag) || ++sp>(1<<21)) break;
          if(YPM>1){ if(rd1(syn+28)) break; } else __builtin_amdgcn_s_sleep(YSLP);
        } }
      PUB(sb0+(l==0? SYN+28 : SINK+l*4), 1u);
    } else SPIN(rd1(syn+28)==0u,YS2);
    #pragma unroll
    for(int i=0;i<4;i++) gldsc(yg+i*65536, yo2, crs+i*1024);
    {
      u32x4 y4[4];
      asm volatile("s_waitcnt vmcnt(0)\n\t"
        "ds_read_b64_tr_b16 v[0:1], %4\n\tds_read_b64_tr_b16 v[2:3], %4 offset:256\n\tds_read_b64_tr_b16 v[4:5], %4 offset:32\n\tds_read_b64_tr_b16 v[6:7], %4 offset:288\n\t"
        "ds_read_b64_tr_b16 v[8:9], %4 offset:2048\n\tds_read_b64_tr_b16 v[10:11], %4 offset:2304\n\tds_read_b64_tr_b16 v[12:13], %4 offset:2080\n\tds_read_b64_tr_b16 v[14:15], %4 offset:2336\n\t"
        "s_waitcnt lgkmcnt(6)\n\tds_write_b128 %5, v[0:3]\n\ts_waitcnt lgkmcnt(5)\n\tds_write_b128 %5, v[4:7] offset:2048\n\t"
        "s_waitcnt lgkmcnt(4)\n\tds_write_b128 %6, v[8:11]\n\ts_waitcnt lgkmcnt(3)\n\tds_write_b128 %6, v[12:15] offset:2048\n\t"
        "ds_read_b128 %0, %7\n\tds_read_b128 %1, %8\n\tds_read_b128 %2, %9\n\tds_read_b128 %3, %10\n\ts_waitcnt lgkmcnt(0)"
        : "=&v"(y4[0]), "=&v"(y4[1]), "=&v"(y4[2]), "=&v"(y4[3])
        : "v"(ya), "v"(wa0), "v"(wa1), "v"(ra[0]), "v"(ra[1]), "v"(ra[2]), "v"(ra[3])
        : "memory", "v0", "v1", "v2", "v3", "v4", "v5", "v6", "v7", "v8", "v9", "v10", "v11", "v12", "v13", "v14", "v15");
      #pragma unroll
      for(int i=0;i<4;i++){
        u32x4 ov;
        #pragma unroll
        for(int e=0;e<4;e++) ov[e]=pack2(mul1(qf[8*i+2*e],bflo(y4[i][e])), mul1(qf[8*i+2*e+1],bfhi(y4[i][e])));
        stout((i&2? op2:op)+(i&1)*4096, oo, ov);
      }
    }
    ENDW TS(6); TOUT(7);
    __builtin_amdgcn_endpgm();
  }
}

#define K0G 1
#define K0B 64
__global__ void k0_kernel(unsigned* flags){ unsigned long long t=__builtin_amdgcn_s_memrealtime(); if(threadIdx.x==0 && blockIdx.x==0) ((unsigned long long*)(flags+65536))[40000]=t; }
__global__ void amp_kernel(unsigned* flags){
  const int l=threadIdx.x;
  const unsigned long long* tb=(const unsigned long long*)(flags+65536);
  const unsigned long long ref=tb[6]-5000;
  unsigned sum=0, mx=0, mns=0xffffffffu, mxe=0, mxs=0;
  for(int q=0;q<4;q++){ const int b=q*64+l;
    unsigned v=(unsigned)tb[(b*12+0)*8+0]-0u; sum+=v; mx=max(mx,v);
    for(int r=0;r<12;r++){ unsigned st=(unsigned)(tb[(b*12+r)*8+6]-ref), en=(unsigned)(tb[(b*12+r)*8+7]-ref); mns=min(mns,st); mxs=max(mxs,st); mxe=max(mxe,en); } }
  for(int o=32;o;o>>=1){ sum+=__shfl_xor(sum,o); mx=max(mx,(unsigned)__shfl_xor(mx,o)); mns=min(mns,(unsigned)__shfl_xor(mns,o)); mxs=max(mxs,(unsigned)__shfl_xor(mxs,o)); mxe=max(mxe,(unsigned)__shfl_xor(mxe,o)); }
  unsigned X4= 0u;
  if(X4>20000u) X4=20000u;
  const unsigned long long end=ref+mns+5000u+X4;
  int sp=0; while(__builtin_amdgcn_s_memrealtime()<end && ++sp<(1<<20)) __builtin_amdgcn_s_sleep(1);
}
extern "C" void kernel_launch(void* const* d_in, const int* in_sizes, int n_in, void* d_out, int out_size, void* d_ws, size_t ws_size, hipStream_t stream){
  const u16* q=(const u16*)d_in[0]; const u16* k=(const u16*)d_in[1]; const u16* v=(const u16*)d_in[2];
  const u16* shk=(const u16*)d_in[3]; const u16* hk=(const u16*)d_in[4]; const u16* sD=(const u16*)d_in[5];
  const u16* x=(const u16*)d_in[6]; const u16* wqkv=(const u16*)d_in[7]; const u16* wo=(const u16*)d_in[8];
  fused_kernel<<<256,768,0,stream>>>(x,wqkv,wo,(char*)d_ws,(u16*)d_out,k,v,shk,hk,q,sD);
  amp_kernel<<<1,64,0,stream>>>((unsigned*)((char*)d_ws+WS_FL));
}
```

```cpp
#define PGT 15
#include <hip/hip_runtime.h>
#include <hip/hip_bf16.h>
#include <stdint.h>

typedef unsigned short u16;
typedef short bf16x8 __attribute__((ext_vector_type(8)));
typedef short s16x4 __attribute__((ext_vector_type(4)));
typedef float f32x2 __attribute__((ext_vector_type(2)));
typedef float f32x4 __attribute__((ext_vector_type(4)));
typedef float f32x8 __attribute__((ext_vector_type(8)));
typedef float f32x16 __attribute__((ext_vector_type(16)));
typedef unsigned u32x4 __attribute__((ext_vector_type(4)));
typedef unsigned u32x2 __attribute__((ext_vector_type(2)));
typedef __bf16 bf16x2_t __attribute__((ext_vector_type(2)));
#define LDS_AS __attribute__((address_space(3)))

__device__ __forceinline__ float bflo(unsigned x){ return __uint_as_float(x<<16); }
__device__ __forceinline__ float bfhi(unsigned x){ return __uint_as_float(x&0xffff0000u); }
__device__ __forceinline__ unsigned pack2(float lo, float hi){ return __builtin_bit_cast(unsigned, __builtin_convertvector(f32x2{lo,hi}, bf16x2_t)); }
__device__ __forceinline__ float mul1(float a, float b){ float r; asm("v_mul_f32 %0, %1, %2" : "=v"(r) : "v"(a), "v"(b)); return r; }
__device__ __forceinline__ float fma1(float a, float b, float c){ float r; asm("v_fma_f32 %0, %1, %2, %3" : "=v"(r) : "v"(a), "v"(b), "v"(c)); return r; }

__device__ __forceinline__ void glds(const void* sbase, unsigned voff, unsigned ldsaddr){
  asm volatile("s_mov_b32 m0, %2\n\ts_nop 0\n\tglobal_load_lds_dwordx4 %0, %1" :: "v"(voff), "s"(sbase), "s"(ldsaddr) : "memory");
}
__device__ __forceinline__ void gldsc(const void* sbase, unsigned voff, unsigned ldsaddr){
  asm volatile("s_mov_b32 m0, %2\n\ts_nop 0\n\tglobal_load_lds_dwordx4 %0, %1 sc1" :: "v"(voff), "s"(sbase), "s"(ldsaddr) : "memory");
}
__device__ __forceinline__ void touch(const void* sbase, unsigned voff, unsigned ldsaddr){
  asm volatile("s_mov_b32 m0, %2\n\ts_nop 0\n\tglobal_load_lds_dword %0, %1" :: "v"(voff), "s"(sbase), "s"(ldsaddr) : "memory");
}
__device__ __forceinline__ void st16c(void* p, u32x4 v){ asm volatile("global_store_dwordx4 %0, %1, off sc1\n\ts_nop 1" :: "v"(p), "v"(v) : "memory"); }
__device__ __forceinline__ void st16cs(void* sbase, unsigned voff, u32x4 v){ asm volatile("global_store_dwordx4 %0, %1, %2 sc1\n\ts_nop 1" :: "v"(voff), "v"(v), "s"(sbase) : "memory"); }
#define GLB_AS __attribute__((address_space(1)))
__device__ __forceinline__ unsigned ldflag(const unsigned* p){ return __hip_atomic_load((const GLB_AS unsigned*)p, __ATOMIC_RELAXED, __HIP_MEMORY_SCOPE_AGENT); }
__device__ __forceinline__ void stflag(unsigned* p, unsigned v){ __hip_atomic_store((GLB_AS unsigned*)p, v, __ATOMIC_RELAXED, __HIP_MEMORY_SCOPE_AGENT); }
template<int SLP> __device__ __forceinline__ void wait_flag(const unsigned* p, unsigned tag){
  int spins=0;
  while(true){ unsigned v=ldflag(p); if(__all(v==tag)) break; __builtin_amdgcn_s_sleep(SLP); if(++spins>(1<<21)) break; }
}
__device__ __forceinline__ unsigned lds_addr(const void* p){ return (unsigned)(size_t)(const LDS_AS void*)p; }
#define WAITV(n) asm volatile("s_waitcnt vmcnt(" #n ")":::"memory")
#define WAITL() asm volatile("s_waitcnt lgkmcnt(0)":::"memory")
#define SBAR() asm volatile("s_barrier":::"memory")
#define PUB(addr,val) asm volatile("ds_write_b32 %0, %1" :: "v"(addr), "v"(val) : "memory")
__device__ __forceinline__ s16x4 trread(const char* p){ return __builtin_amdgcn_ds_read_tr16_b64_v4i16((LDS_AS s16x4*)p); }
__device__ __forceinline__ bf16x8 cat44(s16x4 a, s16x4 b){ return __builtin_shufflevector(a,b,0,1,2,3,4,5,6,7); }
__device__ __forceinline__ int wswz(int row){ return ((row>>1)&1) | (((row>>3)&1)<<1); }
__device__ __forceinline__ u32x4 widen(u32x2 a, u32x2 b){
  auto r0=__builtin_amdgcn_permlane16_swap(a[0],b[0],false,false);
  auto r1=__builtin_amdgcn_permlane16_swap(a[1],b[1],false,false);
  return u32x4{r0[0],r1[0],r0[1],r1[1]};
}

constexpr int S=2048, DM=1024;
constexpr int WS=12288, XS=18432, X_OFF=6*WS;
constexpr int CREG=4608, C_OFF=131072;
constexpr int EX_OFF=C_OFF+4*CREG;
constexpr int SYN=EX_OFF+8192;
constexpr int SINK=SYN+256;
constexpr int ALL_LDS=SINK+768;
constexpr int F_PP=1024, F_O=4096, F_Y=8192;
#define KV0 1
#define KVS 3
#define LPR 2
#define CPR 2
#define GPR 1
#define CGATE 0
#define OSLP 2
#define YSLP 8
#define TPX 16
#define TPW 2
#define TWO 0
#define CVP 1
#define G2P 3
#define OUTC 1
#define ROT 1
#define WTL 2
#define PB2 1
#define PREM 9
#define KTH 1
#define YDEF 3
#define OPOL 1
#define CODEL 280
#define MIX 0
#define LOCA 3
#define XLOC 1
#define PGO 0
#define HLAST 1

#define N2_ASM \
  "v_mov_b32 v64, 0\n\t" \
  "v_mov_b32 v65, 0\n\t" \
  "v_mov_b32 v66, 0\n\t" \
  "v_mov_b32 v67, 0\n\t" \
  "v_mov_b32 v68, 0\n\t" \
  "v_mov_b32 v69, 0\n\t" \
  "v_mov_b32 v70, 0\n\t" \
  "v_mov_b32 v71, 0\n\t" \
  "v_mov_b32 v72, 0\n\t" \
  "v_mov_b32 v73, 0\n\t" \
  "v_mov_b32 v74, 0\n\t" \
  "v_mov_b32 v75, 0\n\t" \
  "v_mov_b32 v76, 0\n\t" \
  "v_mov_b32 v77, 0\n\t" \
  "v_mov_b32 v78, 0\n\t" \
  "v_mov_b32 v79, 0\n\t" \
  "v_mov_b32 v80, 0\n\t" \
  "v_mov_b32 v81, 0\n\t" \
  "v_mov_b32 v82, 0\n\t" \
  "v_mov_b32 v83, 0\n\t" \
  "v_mov_b32 v84, 0\n\t" \
  "v_mov_b32 v85, 0\n\t" \
  "v_mov_b32 v86, 0\n\t" \
  "v_mov_b32 v87, 0\n\t" \
  "v_mov_b32 v88, 0\n\t" \
  "v_mov_b32 v89, 0\n\t" \
  "v_mov_b32 v90, 0\n\t" \
  "v_mov_b32 v91, 0\n\t" \
  "v_mov_b32 v92, 0\n\t" \
  "v_mov_b32 v93, 0\n\t" \
  "v_mov_b32 v94, 0\n\t" \
  "v_mov_b32 v95, 0\n\t" \
  "v_mov_b32 v96, 0\n\t" \
  "v_mov_b32 v97, 0\n\t" \
  "v_mov_b32 v98, 0\n\t" \
  "v_mov_b32 v99, 0\n\t" \
  "v_mov_b32 v100, 0\n\t" \
  "v_mov_b32 v101, 0\n\t" \
  "v_mov_b32 v102, 0\n\t" \
  "v_mov_b32 v103, 0\n\t" \
  "v_mov_b32 v104, 0\n\t" \
  "v_mov_b32 v105, 0\n\t" \
  "v_mov_b32 v106, 0\n\t" \
  "v_mov_b32 v107, 0\n\t" \
  "v_mov_b32 v108, 0\n\t" \
  "v_mov_b32 v109, 0\n\t" \
  "v_mov_b32 v110, 0\n\t" \
  "v_mov_b32 v111, 0\n\t" \
  "v_mov_b32 v112, 0\n\t" \
  "v_mov_b32 v113, 0\n\t" \
  "v_mov_b32 v114, 0\n\t" \
  "v_mov_b32 v115, 0\n\t" \
  "v_mov_b32 v116, 0\n\t" \
  "v_mov_b32 v117, 0\n\t" \
  "v_mov_b32 v118, 0\n\t" \
  "v_mov_b32 v119, 0\n\t" \
  "v_mov_b32 v120, 0\n\t" \
  "v_mov_b32 v121, 0\n\t" \
  "v_mov_b32 v122, 0\n\t" \
  "v_mov_b32 v123, 0\n\t" \
  "v_mov_b32 v124, 0\n\t" \
  "v_mov_b32 v125, 0\n\t" \
  "v_mov_b32 v126, 0\n\t" \
  "v_mov_b32 v127, 0\n\t" \
  "v_mov_b32 v128, 0\n\t" \
  "v_mov_b32 v129, 0\n\t" \
  "v_mov_b32 v130, 0\n\t" \
  "v_mov_b32 v131, 0\n\t" \
  "v_mov_b32 v132, 0\n\t" \
  "v_mov_b32 v133, 0\n\t" \
  "v_mov_b32 v134, 0\n\t" \
  "v_mov_b32 v135, 0\n\t" \
  "v_mov_b32 v136, 0\n\t" \
  "v_mov_b32 v137, 0\n\t" \
  "v_mov_b32 v138, 0\n\t" \
  "v_mov_b32 v139, 0\n\t" \
  "v_mov_b32 v140, 0\n\t" \
  "v_mov_b32 v141, 0\n\t" \
  "v_mov_b32 v142, 0\n\t" \
  "v_mov_b32 v143, 0\n\t" \
  "v_mov_b32 v144, 0\n\t" \
  "v_mov_b32 v145, 0\n\t" \
  "v_mov_b32 v146, 0\n\t" \
  "v_mov_b32 v147, 0\n\t" \
  "v_mov_b32 v148, 0\n\t" \
  "v_mov_b32 v149, 0\n\t" \
  "v_mov_b32 v150, 0\n\t" \
  "v_mov_b32 v151, 0\n\t" \
  "v_mov_b32 v152, 0\n\t" \
  "v_mov_b32 v153, 0\n\t" \
  "v_mov_b32 v154, 0\n\t" \
  "v_mov_b32 v155, 0\n\t" \
  "v_mov_b32 v156, 0\n\t" \
  "v_mov_b32 v157, 0\n\t" \
  "v_mov_b32 v158, 0\n\t" \
  "v_mov_b32 v159, 0\n\t" \
  "v_mov_b32 v160, 0\n\t" \
  "v_mov_b32 v161, 0\n\t" \
  "v_mov_b32 v162, 0\n\t" \
  "v_mov_b32 v163, 0\n\t" \
  "v_mov_b32 v164, 0\n\t" \
  "v_mov_b32 v165, 0\n\t" \
  "v_mov_b32 v166, 0\n\t" \
  "v_mov_b32 v167, 0\n\t" \
  "s_mov_b32 s68, 0x50000\n\t" \
  "s_mov_b32 s69, 0x5550055\n\t" \
  "s_mov_b32 s70, 0xa0000\n\t" \
  "s_mov_b32 s71, 0xaaa00aa\n\t" \
  "s_mov_b32 s72, 0x150001\n\t" \
  "s_mov_b32 s73, 0x15550155\n\t" \
  "s_mov_b32 s74, 0x2a0002\n\t" \
  "s_mov_b32 s75, 0x2aaa02aa\n\t" \
  "s_mov_b32 s76, 0x55505555\n\t" \
  "s_mov_b32 s77, 0x50005500\n\t" \
  "s_mov_b32 s78, 0xaaa0aaaa\n\t" \
  "s_mov_b32 s79, 0xa000aa00\n\t" \
  "s_mov_b32 s80, 0x55405554\n\t" \
  "s_mov_b32 s81, 0x40005400\n\t" \
  "s_mov_b32 s82, 0xaa80aaa8\n\t" \
  "s_mov_b32 s83, 0x8000a800\n\t" \
  "s_mov_b32 s84, 0xaaaaaaaa\n\t" \
  "s_mov_b32 s85, 0xaaaaaaaa\n\t" \
  "v_add_u32 v60, 36864, v58\n\t" \
  "v_add_u32 v61, 36864, v59\n\t" \
  "s_mov_b32 %[it], 0\n\t" \
  "s_cmp_lg_u32 %[r], 0\n\t" \
  "s_barrier\n\t" \
  "s_cbranch_scc1 NB_%=\n\t" \
  "ds_read_b64_tr_b16 v[40:41], v60 offset:16384\n\t" \
  "ds_read_b64_tr_b16 v[42:43], v60 offset:16896\n\t" \
  "ds_read_b64_tr_b16 v[44:45], v60 offset:20480\n\t" \
  "ds_read_b64_tr_b16 v[46:47], v60 offset:20992\n\t" \
  "ds_read_b64_tr_b16 v[48:49], v61 offset:16384\n\t" \
  "ds_read_b64_tr_b16 v[50:51], v61 offset:16896\n\t" \
  "ds_read_b128 v[0:3], v56 offset:36864\n\t" \
  "ds_read_b128 v[4:7], v56 offset:38912\n\t" \
  "ds_read_b128 v[8:11], v56 offset:40960\n\t" \
  "ds_read_b128 v[12:15], v56 offset:43008\n\t" \
  "ds_read_b128 v[16:19], v56 offset:45056\n\t" \
  "s_waitcnt lgkmcnt(0)\n\t" \
  "s_branch N4a_%=\n\t" \
  "N0a_%=:\n\t" \
  "s_waitcnt lgkmcnt(4)\n\t" \
  "v_mfma_f32_16x16x32_bf16 v[160:163], v[40:43], v[0:3], v[160:163]\n\t" \
  "ds_read_b64_tr_b16 v[52:53], v59 offset:8192\n\t" \
  "v_mfma_f32_16x16x32_bf16 v[96:99], v[40:43], v[4:7], v[96:99]\n\t" \
  "ds_read_b64_tr_b16 v[54:55], v59 offset:8704\n\t" \
  "v_mfma_f32_16x16x32_bf16 v[104:107], v[40:43], v[8:11], v[104:107]\n\t" \
  "s_barrier\n\t" \
  "v_mfma_f32_16x16x32_bf16 v[112:115], v[40:43], v[12:15], v[112:115]\n\t" \
  "v_mfma_f32_16x16x32_bf16 v[120:123], v[40:43], v[16:19], v[120:123]\n\t" \
  "s_waitcnt lgkmcnt(4)\n\t" \
  "v_mfma_f32_16x16x32_bf16 v[128:131], v[4:7], v[44:47], v[128:131]\n\t" \
  "ds_read_b64_tr_b16 v[40:41], v58 offset:0\n\t" \
  "v_mfma_f32_16x16x32_bf16 v[136:139], v[8:11], v[44:47], v[136:139]\n\t" \
  "ds_read_b64_tr_b16 v[42:43], v58 offset:512\n\t" \
  "v_mfma_f32_16x16x32_bf16 v[144:147], v[12:15], v[44:47], v[144:147]\n\t" \
  "ds_read_b128 v[20:23], v57 offset:0\n\t" \
  "v_mfma_f32_16x16x32_bf16 v[152:155], v[16:19], v[44:47], v[152:155]\n\t" \
  "ds_read_b128 v[24:27], v57 offset:2048\n\t" \
  "s_waitcnt lgkmcnt(6)\n\t" \
  "v_mfma_f32_16x16x32_bf16 v[164:167], v[48:51], v[0:3], v[164:167]\n\t" \
  "ds_read_b64_tr_b16 v[44:45], v59 offset:0\n\t" \
  "v_mfma_f32_16x16x32_bf16 v[100:103], v[48:51], v[4:7], v[100:103]\n\t" \
  "ds_read_b64_tr_b16 v[46:47], v59 offset:512\n\t" \
  "v_mfma_f32_16x16x32_bf16 v[108:111], v[48:51], v[8:11], v[108:111]\n\t" \
  "ds_read_b128 v[28:31], v57 offset:4096\n\t" \
  "v_mfma_f32_16x16x32_bf16 v[116:119], v[48:51], v[12:15], v[116:119]\n\t" \
  "ds_read_b128 v[32:35], v57 offset:6144\n\t" \
  "v_mfma_f32_16x16x32_bf16 v[124:127], v[48:51], v[16:19], v[124:127]\n\t" \
  "s_waitcnt lgkmcnt(8)\n\t" \
  "v_mfma_f32_16x16x32_bf16 v[132:135], v[4:7], v[52:55], v[132:135]\n\t" \
  "ds_read_b64_tr_b16 v[48:49], v58 offset:16384\n\t" \
  "v_mfma_f32_16x16x32_bf16 v[140:143], v[8:11], v[52:55], v[140:143]\n\t" \
  "ds_read_b64_tr_b16 v[50:51], v58 offset:16896\n\t" \
  "v_mfma_f32_16x16x32_bf16 v[148:151], v[12:15], v[52:55], v[148:151]\n\t" \
  "ds_read_b128 v[36:39], v57 offset:8192\n\t" \
  "v_mfma_f32_16x16x32_bf16 v[156:159], v[16:19], v[52:55], v[156:159]\n\t" \
  "s_waitcnt lgkmcnt(9)\n\t" \
  "v_mfma_f32_16x16x32_bf16 v[64:67], v[40:43], v[4:7], v[64:67]\n\t" \
  "ds_read_b64_tr_b16 v[52:53], v58 offset:20480\n\t" \
  "v_mfma_f32_16x16x32_bf16 v[72:75], v[40:43], v[8:11], v[72:75]\n\t" \
  "ds_read_b64_tr_b16 v[54:55], v58 offset:20992\n\t" \
  "v_mfma_f32_16x16x32_bf16 v[80:83], v[40:43], v[12:15], v[80:83]\n\t" \
  "v_mfma_f32_16x16x32_bf16 v[88:91], v[40:43], v[16:19], v[88:91]\n\t" \
  "s_waitcnt lgkmcnt(7)\n\t" \
  "v_mfma_f32_16x16x32_bf16 v[68:71], v[44:47], v[4:7], v[68:71]\n\t" \
  "ds_read_b64_tr_b16 v[40:41], v59 offset:16384\n\t" \
  "v_mfma_f32_16x16x32_bf16 v[76:79], v[44:47], v[8:11], v[76:79]\n\t" \
  "ds_read_b64_tr_b16 v[42:43], v59 offset:16896\n\t" \
  "v_mfma_f32_16x16x32_bf16 v[84:87], v[44:47], v[12:15], v[84:87]\n\t" \
  "v_mfma_f32_16x16x32_bf16 v[92:95], v[44:47], v[16:19], v[92:95]\n\t" \
  "N1a_%=:\n\t" \
  "s_waitcnt lgkmcnt(4)\n\t" \
  "v_mfma_f32_16x16x32_bf16 v[160:163], v[48:51], v[20:23], v[160:163]\n\t" \
  "ds_read_b64_tr_b16 v[44:45], v59 offset:20480\n\t" \
  "v_mfma_f32_16x16x32_bf16 v[96:99], v[48:51], v[24:27], v[96:99]\n\t" \
  "ds_read_b64_tr_b16 v[46:47], v59 offset:20992\n\t" \
  "v_mfma_f32_16x16x32_bf16 v[104:107], v[48:51], v[28:31], v[104:107]\n\t" \
  "s_barrier\n\t" \
  "v_mfma_f32_16x16x32_bf16 v[112:115], v[48:51], v[32:35], v[112:115]\n\t" \
  "v_mfma_f32_16x16x32_bf16 v[120:123], v[48:51], v[36:39], v[120:123]\n\t" \
  "s_waitcnt lgkmcnt(4)\n\t" \
  "v_mfma_f32_16x16x32_bf16 v[128:131], v[24:27], v[52:55], v[128:131]\n\t" \
  "ds_read_b64_tr_b16 v[48:49], v58 offset:12288\n\t" \
  "v_mfma_f32_16x16x32_bf16 v[136:139], v[28:31], v[52:55], v[136:139]\n\t" \
  "ds_read_b64_tr_b16 v[50:51], v58 offset:12800\n\t" \
  "v_mfma_f32_16x16x32_bf16 v[144:147], v[32:35], v[52:55], v[144:147]\n\t" \
  "ds_read_b128 v[0:3], v56 offset:18432\n\t" \
  "v_mfma_f32_16x16x32_bf16 v[152:155], v[36:39], v[52:55], v[152:155]\n\t" \
  "ds_read_b128 v[4:7], v56 offset:20480\n\t" \
  "s_waitcnt lgkmcnt(6)\n\t" \
  "v_mfma_f32_16x16x32_bf16 v[164:167], v[40:43], v[20:23], v[164:167]\n\t" \
  "ds_read_b64_tr_b16 v[52:53], v59 offset:12288\n\t" \
  "v_mfma_f32_16x16x32_bf16 v[100:103], v[40:43], v[24:27], v[100:103]\n\t" \
  "ds_read_b64_tr_b16 v[54:55], v59 offset:12800\n\t" \
  "v_mfma_f32_16x16x32_bf16 v[108:111], v[40:43], v[28:31], v[108:111]\n\t" \
  "ds_read_b128 v[8:11], v56 offset:22528\n\t" \
  "v_mfma_f32_16x16x32_bf16 v[116:119], v[40:43], v[32:35], v[116:119]\n\t" \
  "ds_read_b128 v[12:15], v56 offset:24576\n\t" \
  "v_mfma_f32_16x16x32_bf16 v[124:127], v[40:43], v[36:39], v[124:127]\n\t" \
  "s_waitcnt lgkmcnt(8)\n\t" \
  "v_mfma_f32_16x16x32_bf16 v[132:135], v[24:27], v[44:47], v[132:135]\n\t" \
  "ds_read_b64_tr_b16 v[40:41], v58 offset:28672\n\t" \
  "v_mfma_f32_16x16x32_bf16 v[140:143], v[28:31], v[44:47], v[140:143]\n\t" \
  "ds_read_b64_tr_b16 v[42:43], v58 offset:29184\n\t" \
  "v_mfma_f32_16x16x32_bf16 v[148:151], v[32:35], v[44:47], v[148:151]\n\t" \
  "ds_read_b128 v[16:19], v56 offset:26624\n\t" \
  "v_mfma_f32_16x16x32_bf16 v[156:159], v[36:39], v[44:47], v[156:159]\n\t" \
  "s_waitcnt lgkmcnt(9)\n\t" \
  "v_mfma_f32_16x16x32_bf16 v[64:67], v[48:51], v[24:27], v[64:67]\n\t" \
  "ds_read_b64_tr_b16 v[44:45], v58 offset:32768\n\t" \
  "v_mfma_f32_16x16x32_bf16 v[72:75], v[48:51], v[28:31], v[72:75]\n\t" \
  "ds_read_b64_tr_b16 v[46:47], v58 offset:33280\n\t" \
  "v_mfma_f32_16x16x32_bf16 v[80:83], v[48:51], v[32:35], v[80:83]\n\t" \
  "v_mfma_f32_16x16x32_bf16 v[88:91], v[48:51], v[36:39], v[88:91]\n\t" \
  "s_waitcnt lgkmcnt(7)\n\t" \
  "v_mfma_f32_16x16x32_bf16 v[68:71], v[52:55], v[24:27], v[68:71]\n\t" \
  "ds_read_b64_tr_b16 v[48:49], v59 offset:28672\n\t" \
  "v_mfma_f32_16x16x32_bf16 v[76:79], v[52:55], v[28:31], v[76:79]\n\t" \
  "ds_read_b64_tr_b16 v[50:51], v59 offset:29184\n\t" \
  "v_mfma_f32_16x16x32_bf16 v[84:87], v[52:55], v[32:35], v[84:87]\n\t" \
  "v_mfma_f32_16x16x32_bf16 v[92:95], v[52:55], v[36:39], v[92:95]\n\t" \
  "N2a_%=:\n\t" \
  "s_waitcnt lgkmcnt(4)\n\t" \
  "v_mfma_f32_16x16x32_bf16 v[160:163], v[40:43], v[0:3], v[160:163]\n\t" \
  "ds_read_b64_tr_b16 v[52:53], v59 offset:32768\n\t" \
  "v_mfma_f32_16x16x32_bf16 v[96:99], v[40:43], v[4:7], v[96:99]\n\t" \
  "ds_read_b64_tr_b16 v[54:55], v59 offset:33280\n\t" \
  "v_mfma_f32_16x16x32_bf16 v[104:107], v[40:43], v[8:11], v[104:107]\n\t" \
  "s_barrier\n\t" \
  "v_mfma_f32_16x16x32_bf16 v[112:115], v[40:43], v[12:15], v[112:115]\n\t" \
  "v_mfma_f32_16x16x32_bf16 v[120:123], v[40:43], v[16:19], v[120:123]\n\t" \
  "s_waitcnt lgkmcnt(4)\n\t" \
  "v_mfma_f32_16x16x32_bf16 v[128:131], v[4:7], v[44:47], v[128:131]\n\t" \
  "ds_read_b64_tr_b16 v[40:41], v58 offset:24576\n\t" \
  "v_mfma_f32_16x16x32_bf16 v[136:139], v[8:11], v[44:47], v[136:139]\n\t" \
  "ds_read_b64_tr_b16 v[42:43], v58 offset:25088\n\t" \
  "v_mfma_f32_16x16x32_bf16 v[144:147], v[12:15], v[44:47], v[144:147]\n\t" \
  "ds_read_b128 v[20:23], v57 offset:18432\n\t" \
  "v_mfma_f32_16x16x32_bf16 v[152:155], v[16:19], v[44:47], v[152:155]\n\t" \
  "ds_read_b128 v[24:27], v57 offset:20480\n\t" \
  "s_waitcnt lgkmcnt(6)\n\t" \
  "v_mfma_f32_16x16x32_bf16 v[164:167], v[48:51], v[0:3], v[164:167]\n\t" \
  "ds_read_b64_tr_b16 v[44:45], v59 offset:24576\n\t" \
  "v_mfma_f32_16x16x32_bf16 v[100:103], v[48:51], v[4:7], v[100:103]\n\t" \
  "ds_read_b64_tr_b16 v[46:47], v59 offset:25088\n\t" \
  "v_mfma_f32_16x16x32_bf16 v[108:111], v[48:51], v[8:11], v[108:111]\n\t" \
  "ds_read_b128 v[28:31], v57 offset:22528\n\t" \
  "v_mfma_f32_16x16x32_bf16 v[116:119], v[48:51], v[12:15], v[116:119]\n\t" \
  "ds_read_b128 v[32:35], v57 offset:24576\n\t" \
  "v_mfma_f32_16x16x32_bf16 v[124:127], v[48:51], v[16:19], v[124:127]\n\t" \
  "s_waitcnt lgkmcnt(8)\n\t" \
  "v_mfma_f32_16x16x32_bf16 v[132:135], v[4:7], v[52:55], v[132:135]\n\t" \
  "ds_read_b64_tr_b16 v[48:49], v60 offset:4096\n\t" \
  "v_mfma_f32_16x16x32_bf16 v[140:143], v[8:11], v[52:55], v[140:143]\n\t" \
  "ds_read_b64_tr_b16 v[50:51], v60 offset:4608\n\t" \
  "v_mfma_f32_16x16x32_bf16 v[148:151], v[12:15], v[52:55], v[148:151]\n\t" \
  "ds_read_b128 v[36:39], v57 offset:26624\n\t" \
  "v_mfma_f32_16x16x32_bf16 v[156:159], v[16:19], v[52:55], v[156:159]\n\t" \
  "s_waitcnt lgkmcnt(9)\n\t" \
  "v_mfma_f32_16x16x32_bf16 v[64:67], v[40:43], v[4:7], v[64:67]\n\t" \
  "ds_read_b64_tr_b16 v[52:53], v60 offset:8192\n\t" \
  "v_mfma_f32_16x16x32_bf16 v[72:75], v[40:43], v[8:11], v[72:75]\n\t" \
  "ds_read_b64_tr_b16 v[54:55], v60 offset:8704\n\t" \
  "v_mfma_f32_16x16x32_bf16 v[80:83], v[40:43], v[12:15], v[80:83]\n\t" \
  "v_mfma_f32_16x16x32_bf16 v[88:91], v[40:43], v[16:19], v[88:91]\n\t" \
  "s_waitcnt lgkmcnt(7)\n\t" \
  "v_mfma_f32_16x16x32_bf16 v[68:71], v[44:47], v[4:7], v[68:71]\n\t" \
  "ds_read_b64_tr_b16 v[40:41], v61 offset:4096\n\t" \
  "v_mfma_f32_16x16x32_bf16 v[76:79], v[44:47], v[8:11], v[76:79]\n\t" \
  "ds_read_b64_tr_b16 v[42:43], v61 offset:4608\n\t" \
  "v_mfma_f32_16x16x32_bf16 v[84:87], v[44:47], v[12:15], v[84:87]\n\t" \
  "v_mfma_f32_16x16x32_bf16 v[92:95], v[44:47], v[16:19], v[92:95]\n\t" \
  "N3a_%=:\n\t" \
  "s_waitcnt lgkmcnt(4)\n\t" \
  "v_mfma_f32_16x16x32_bf16 v[160:163], v[48:51], v[20:23], v[160:163]\n\t" \
  "ds_read_b64_tr_b16 v[44:45], v61 offset:8192\n\t" \
  "v_mfma_f32_16x16x32_bf16 v[96:99], v[48:51], v[24:27], v[96:99]\n\t" \
  "ds_read_b64_tr_b16 v[46:47], v61 offset:8704\n\t" \
  "v_mfma_f32_16x16x32_bf16 v[104:107], v[48:51], v[28:31], v[104:107]\n\t" \
  "s_barrier\n\t" \
  "v_mfma_f32_16x16x32_bf16 v[112:115], v[48:51], v[32:35], v[112:115]\n\t" \
  "v_mfma_f32_16x16x32_bf16 v[120:123], v[48:51], v[36:39], v[120:123]\n\t" \
  "s_waitcnt lgkmcnt(4)\n\t" \
  "v_mfma_f32_16x16x32_bf16 v[128:131], v[24:27], v[52:55], v[128:131]\n\t" \
  "ds_read_b64_tr_b16 v[48:49], v60 offset:0\n\t" \
  "v_mfma_f32_16x16x32_bf16 v[136:139], v[28:31], v[52:55], v[136:139]\n\t" \
  "ds_read_b64_tr_b16 v[50:51], v60 offset:512\n\t" \
  "v_mfma_f32_16x16x32_bf16 v[144:147], v[32:35], v[52:55], v[144:147]\n\t" \
  "ds_read_b128 v[0:3], v56 offset:36864\n\t" \
  "v_mfma_f32_16x16x32_bf16 v[152:155], v[36:39], v[52:55], v[152:155]\n\t" \
  "ds_read_b128 v[4:7], v56 offset:38912\n\t" \
  "s_waitcnt lgkmcnt(6)\n\t" \
  "v_mfma_f32_16x16x32_bf16 v[164:167], v[40:43], v[20:23], v[164:167]\n\t" \
  "ds_read_b64_tr_b16 v[52:53], v61 offset:0\n\t" \
  "v_mfma_f32_16x16x32_bf16 v[100:103], v[40:43], v[24:27], v[100:103]\n\t" \
  "ds_read_b64_tr_b16 v[54:55], v61 offset:512\n\t" \
  "v_mfma_f32_16x16x32_bf16 v[108:111], v[40:43], v[28:31], v[108:111]\n\t" \
  "ds_read_b128 v[8:11], v56 offset:40960\n\t" \
  "v_mfma_f32_16x16x32_bf16 v[116:119], v[40:43], v[32:35], v[116:119]\n\t" \
  "ds_read_b128 v[12:15], v56 offset:43008\n\t" \
  "v_mfma_f32_16x16x32_bf16 v[124:127], v[40:43], v[36:39], v[124:127]\n\t" \
  "s_waitcnt lgkmcnt(8)\n\t" \
  "v_mfma_f32_16x16x32_bf16 v[132:135], v[24:27], v[44:47], v[132:135]\n\t" \
  "ds_read_b64_tr_b16 v[40:41], v60 offset:16384\n\t" \
  "v_mfma_f32_16x16x32_bf16 v[140:143], v[28:31], v[44:47], v[140:143]\n\t" \
  "ds_read_b64_tr_b16 v[42:43], v60 offset:16896\n\t" \
  "v_mfma_f32_16x16x32_bf16 v[148:151], v[32:35], v[44:47], v[148:151]\n\t" \
  "ds_read_b128 v[16:19], v56 offset:45056\n\t" \
  "v_mfma_f32_16x16x32_bf16 v[156:159], v[36:39], v[44:47], v[156:159]\n\t" \
  "s_waitcnt lgkmcnt(9)\n\t" \
  "v_mfma_f32_16x16x32_bf16 v[64:67], v[48:51], v[24:27], v[64:67]\n\t" \
  "ds_read_b64_tr_b16 v[44:45], v60 offset:20480\n\t" \
  "v_mfma_f32_16x16x32_bf16 v[72:75], v[48:51], v[28:31], v[72:75]\n\t" \
  "ds_read_b64_tr_b16 v[46:47], v60 offset:20992\n\t" \
  "v_mfma_f32_16x16x32_bf16 v[80:83], v[48:51], v[32:35], v[80:83]\n\t" \
  "v_mfma_f32_16x16x32_bf16 v[88:91], v[48:51], v[36:39], v[88:91]\n\t" \
  "s_waitcnt lgkmcnt(7)\n\t" \
  "v_mfma_f32_16x16x32_bf16 v[68:71], v[52:55], v[24:27], v[68:71]\n\t" \
  "ds_read_b64_tr_b16 v[48:49], v61 offset:16384\n\t" \
  "v_mfma_f32_16x16x32_bf16 v[76:79], v[52:55], v[28:31], v[76:79]\n\t" \
  "ds_read_b64_tr_b16 v[50:51], v61 offset:16896\n\t" \
  "v_mfma_f32_16x16x32_bf16 v[84:87], v[52:55], v[32:35], v[84:87]\n\t" \
  "v_mfma_f32_16x16x32_bf16 v[92:95], v[52:55], v[36:39], v[92:95]\n\t" \
  "N4a_%=:\n\t" \
  "s_waitcnt lgkmcnt(4)\n\t" \
  "v_mfma_f32_16x16x32_bf16 v[160:163], v[40:43], v[0:3], v[160:163]\n\t" \
  "ds_read_b64_tr_b16 v[52:53], v61 offset:20480\n\t" \
  "v_mfma_f32_16x16x32_bf16 v[96:99], v[40:43], v[4:7], v[96:99]\n\t" \
  "ds_read_b64_tr_b16 v[54:55], v61 offset:20992\n\t" \
  "v_mfma_f32_16x16x32_bf16 v[104:107], v[40:43], v[8:11], v[104:107]\n\t" \
  "s_barrier\n\t" \
  "v_mfma_f32_16x16x32_bf16 v[112:115], v[40:43], v[12:15], v[112:115]\n\t" \
  "v_mfma_f32_16x16x32_bf16 v[120:123], v[40:43], v[16:19], v[120:123]\n\t" \
  "s_waitcnt lgkmcnt(4)\n\t" \
  "v_mfma_f32_16x16x32_bf16 v[128:131], v[4:7], v[44:47], v[128:131]\n\t" \
  "ds_read_b64_tr_b16 v[40:41], v60 offset:12288\n\t" \
  "v_mfma_f32_16x16x32_bf16 v[136:139], v[8:11], v[44:47], v[136:139]\n\t" \
  "ds_read_b64_tr_b16 v[42:43], v60 offset:12800\n\t" \
  "v_mfma_f32_16x16x32_bf16 v[144:147], v[12:15], v[44:47], v[144:147]\n\t" \
  "ds_read_b128 v[20:23], v57 offset:36864\n\t" \
  "v_mfma_f32_16x16x32_bf16 v[152:155], v[16:19], v[44:47], v[152:155]\n\t" \
  "ds_read_b128 v[24:27], v57 offset:38912\n\t" \
  "s_waitcnt lgkmcnt(6)\n\t" \
  "v_mfma_f32_16x16x32_bf16 v[164:167], v[48:51], v[0:3], v[164:167]\n\t" \
  "ds_read_b64_tr_b16 v[44:45], v61 offset:12288\n\t" \
  "v_mfma_f32_16x16x32_bf16 v[100:103], v[48:51], v[4:7], v[100:103]\n\t" \
  "ds_read_b64_tr_b16 v[46:47], v61 offset:12800\n\t" \
  "v_mfma_f32_16x16x32_bf16 v[108:111], v[48:51], v[8:11], v[108:111]\n\t" \
  "ds_read_b128 v[28:31], v57 offset:40960\n\t" \
  "v_mfma_f32_16x16x32_bf16 v[116:119], v[48:51], v[12:15], v[116:119]\n\t" \
  "ds_read_b128 v[32:35], v57 offset:43008\n\t" \
  "v_mfma_f32_16x16x32_bf16 v[124:127], v[48:51], v[16:19], v[124:127]\n\t" \
  "s_waitcnt lgkmcnt(8)\n\t" \
  "v_mfma_f32_16x16x32_bf16 v[132:135], v[4:7], v[52:55], v[132:135]\n\t" \
  "ds_read_b64_tr_b16 v[48:49], v60 offset:28672\n\t" \
  "v_mfma_f32_16x16x32_bf16 v[140:143], v[8:11], v[52:55], v[140:143]\n\t" \
  "ds_read_b64_tr_b16 v[50:51], v60 offset:29184\n\t" \
  "v_mfma_f32_16x16x32_bf16 v[148:151], v[12:15], v[52:55], v[148:151]\n\t" \
  "ds_read_b128 v[36:39], v57 offset:45056\n\t" \
  "v_mfma_f32_16x16x32_bf16 v[156:159], v[16:19], v[52:55], v[156:159]\n\t" \
  "s_waitcnt lgkmcnt(9)\n\t" \
  "v_mfma_f32_16x16x32_bf16 v[64:67], v[40:43], v[4:7], v[64:67]\n\t" \
  "ds_read_b64_tr_b16 v[52:53], v60 offset:32768\n\t" \
  "v_mfma_f32_16x16x32_bf16 v[72:75], v[40:43], v[8:11], v[72:75]\n\t" \
  "ds_read_b64_tr_b16 v[54:55], v60 offset:33280\n\t" \
  "v_mfma_f32_16x16x32_bf16 v[80:83], v[40:43], v[12:15], v[80:83]\n\t" \
  "v_mfma_f32_16x16x32_bf16 v[88:91], v[40:43], v[16:19], v[88:91]\n\t" \
  "s_waitcnt lgkmcnt(7)\n\t" \
  "v_mfma_f32_16x16x32_bf16 v[68:71], v[44:47], v[4:7], v[68:71]\n\t" \
  "ds_read_b64_tr_b16 v[40:41], v61 offset:28672\n\t" \
  "v_mfma_f32_16x16x32_bf16 v[76:79], v[44:47], v[8:11], v[76:79]\n\t" \
  "ds_read_b64_tr_b16 v[42:43], v61 offset:29184\n\t" \
  "v_mfma_f32_16x16x32_bf16 v[84:87], v[44:47], v[12:15], v[84:87]\n\t" \
  "v_mfma_f32_16x16x32_bf16 v[92:95], v[44:47], v[16:19], v[92:95]\n\t" \
  "N5a_%=:\n\t" \
  "s_waitcnt lgkmcnt(4)\n\t" \
  "v_mfma_f32_16x16x32_bf16 v[160:163], v[48:51], v[20:23], v[160:163]\n\t" \
  "ds_read_b64_tr_b16 v[44:45], v61 offset:32768\n\t" \
  "v_mfma_f32_16x16x32_bf16 v[96:99], v[48:51], v[24:27], v[96:99]\n\t" \
  "ds_read_b64_tr_b16 v[46:47], v61 offset:33280\n\t" \
  "v_mfma_f32_16x16x32_bf16 v[104:107], v[48:51], v[28:31], v[104:107]\n\t" \
  "s_barrier\n\t" \
  "v_mfma_f32_16x16x32_bf16 v[112:115], v[48:51], v[32:35], v[112:115]\n\t" \
  "v_mfma_f32_16x16x32_bf16 v[120:123], v[48:51], v[36:39], v[120:123]\n\t" \
  "s_waitcnt lgkmcnt(4)\n\t" \
  "v_mfma_f32_16x16x32_bf16 v[128:131], v[24:27], v[52:55], v[128:131]\n\t" \
  "ds_read_b64_tr_b16 v[48:49], v60 offset:24576\n\t" \
  "v_mfma_f32_16x16x32_bf16 v[136:139], v[28:31], v[52:55], v[136:139]\n\t" \
  "ds_read_b64_tr_b16 v[50:51], v60 offset:25088\n\t" \
  "v_mfma_f32_16x16x32_bf16 v[144:147], v[32:35], v[52:55], v[144:147]\n\t" \
  "ds_read_b128 v[0:3], v56 offset:0\n\t" \
  "v_mfma_f32_16x16x32_bf16 v[152:155], v[36:39], v[52:55], v[152:155]\n\t" \
  "ds_read_b128 v[4:7], v56 offset:2048\n\t" \
  "s_waitcnt lgkmcnt(6)\n\t" \
  "v_mfma_f32_16x16x32_bf16 v[164:167], v[40:43], v[20:23], v[164:167]\n\t" \
  "ds_read_b64_tr_b16 v[52:53], v61 offset:24576\n\t" \
  "v_mfma_f32_16x16x32_bf16 v[100:103], v[40:43], v[24:27], v[100:103]\n\t" \
  "ds_read_b64_tr_b16 v[54:55], v61 offset:25088\n\t" \
  "v_mfma_f32_16x16x32_bf16 v[108:111], v[40:43], v[28:31], v[108:111]\n\t" \
  "ds_read_b128 v[8:11], v56 offset:4096\n\t" \
  "v_mfma_f32_16x16x32_bf16 v[116:119], v[40:43], v[32:35], v[116:119]\n\t" \
  "ds_read_b128 v[12:15], v56 offset:6144\n\t" \
  "v_mfma_f32_16x16x32_bf16 v[124:127], v[40:43], v[36:39], v[124:127]\n\t" \
  "s_waitcnt lgkmcnt(8)\n\t" \
  "v_mfma_f32_16x16x32_bf16 v[132:135], v[24:27], v[44:47], v[132:135]\n\t" \
  "ds_read_b64_tr_b16 v[40:41], v58 offset:4096\n\t" \
  "v_mfma_f32_16x16x32_bf16 v[140:143], v[28:31], v[44:47], v[140:143]\n\t" \
  "ds_read_b64_tr_b16 v[42:43], v58 offset:4608\n\t" \
  "v_mfma_f32_16x16x32_bf16 v[148:151], v[32:35], v[44:47], v[148:151]\n\t" \
  "ds_read_b128 v[16:19], v56 offset:8192\n\t" \
  "v_mfma_f32_16x16x32_bf16 v[156:159], v[36:39], v[44:47], v[156:159]\n\t" \
  "s_waitcnt lgkmcnt(9)\n\t" \
  "v_mfma_f32_16x16x32_bf16 v[64:67], v[48:51], v[24:27], v[64:67]\n\t" \
  "ds_read_b64_tr_b16 v[44:45], v58 offset:8192\n\t" \
  "v_mfma_f32_16x16x32_bf16 v[72:75], v[48:51], v[28:31], v[72:75]\n\t" \
  "ds_read_b64_tr_b16 v[46:47], v58 offset:8704\n\t" \
  "v_mfma_f32_16x16x32_bf16 v[80:83], v[48:51], v[32:35], v[80:83]\n\t" \
  "v_mfma_f32_16x16x32_bf16 v[88:91], v[48:51], v[36:39], v[88:91]\n\t" \
  "s_waitcnt lgkmcnt(7)\n\t" \
  "v_mfma_f32_16x16x32_bf16 v[68:71], v[52:55], v[24:27], v[68:71]\n\t" \
  "ds_read_b64_tr_b16 v[48:49], v59 offset:4096\n\t" \
  "v_mfma_f32_16x16x32_bf16 v[76:79], v[52:55], v[28:31], v[76:79]\n\t" \
  "ds_read_b64_tr_b16 v[50:51], v59 offset:4608\n\t" \
  "s_addk_i32 %[it], 1\n\t" \
  "v_mfma_f32_16x16x32_bf16 v[84:87], v[52:55], v[32:35], v[84:87]\n\t" \
  "s_cmp_lt_u32 %[it], 6\n\t" \
  "v_mfma_f32_16x16x32_bf16 v[92:95], v[52:55], v[36:39], v[92:95]\n\t" \
  "s_cbranch_scc1 N0a_%=\n\t" \
  "s_setprio 3\n\t" \
  "v_cvt_pk_bf16_f32 v120, v120, v121\n\t" \
  "v_cvt_pk_bf16_f32 v121, v122, v123\n\t" \
  "v_cvt_pk_bf16_f32 v122, v124, v125\n\t" \
  "v_cvt_pk_bf16_f32 v123, v126, v127\n\t" \
  "v_cvt_pk_bf16_f32 v28, v152, v153\n\t" \
  "v_cvt_pk_bf16_f32 v29, v154, v155\n\t" \
  "v_cvt_pk_bf16_f32 v38, v156, v157\n\t" \
  "v_cvt_pk_bf16_f32 v39, v158, v159\n\t" \
  "ds_write_b128 v63, v[120:123]\n\t" \
  "ds_write2_b64 v63, v[28:29], v[38:39] offset0:128 offset1:129\n\t" \
  "v_mov_b32 v57, %[fa]\n\t" \
  "v_mov_b32 v58, 1\n\t" \
  "s_mov_b64 exec, 1\n\t" \
  "ds_write_b32 v57, v58\n\t" \
  "s_mov_b64 exec, -1\n\t" \
  "v_cvt_pk_bf16_f32 v160, v160, v161\n\t" \
  "v_cvt_pk_bf16_f32 v161, v162, v163\n\t" \
  "v_cvt_pk_bf16_f32 v162, v164, v165\n\t" \
  "v_cvt_pk_bf16_f32 v163, v166, v167\n\t" \
  "s_branch NX_%=\n\t" \
  "NB_%=:\n\t" \
  "ds_read_b64_tr_b16 v[40:41], v60 offset:20480\n\t" \
  "ds_read_b64_tr_b16 v[42:43], v60 offset:20992\n\t" \
  "ds_read_b64_tr_b16 v[44:45], v60 offset:16384\n\t" \
  "ds_read_b64_tr_b16 v[46:47], v60 offset:16896\n\t" \
  "ds_read_b64_tr_b16 v[48:49], v61 offset:20480\n\t" \
  "ds_read_b64_tr_b16 v[50:51], v61 offset:20992\n\t" \
  "ds_read_b128 v[0:3], v56 offset:36864\n\t" \
  "ds_read_b128 v[4:7], v56 offset:47104\n\t" \
  "ds_read_b128 v[8:11], v56 offset:49152\n\t" \
  "ds_read_b128 v[12:15], v56 offset:51200\n\t" \
  "ds_read_b128 v[16:19], v56 offset:53248\n\t" \
  "s_waitcnt lgkmcnt(0)\n\t" \
  "s_branch N4b_%=\n\t" \
  "N0b_%=:\n\t" \
  "s_waitcnt lgkmcnt(4)\n\t" \
  "v_mfma_f32_16x16x32_bf16 v[160:163], v[0:3], v[40:43], v[160:163]\n\t" \
  "ds_read_b64_tr_b16 v[52:53], v59 offset:4096\n\t" \
  "v_mfma_f32_16x16x32_bf16 v[128:131], v[4:7], v[40:43], v[128:131]\n\t" \
  "ds_read_b64_tr_b16 v[54:55], v59 offset:4608\n\t" \
  "v_mfma_f32_16x16x32_bf16 v[136:139], v[8:11], v[40:43], v[136:139]\n\t" \
  "s_barrier\n\t" \
  "v_mfma_f32_16x16x32_bf16 v[144:147], v[12:15], v[40:43], v[144:147]\n\t" \
  "v_mfma_f32_16x16x32_bf16 v[152:155], v[16:19], v[40:43], v[152:155]\n\t" \
  "s_waitcnt lgkmcnt(4)\n\t" \
  "v_mfma_f32_16x16x32_bf16 v[96:99], v[44:47], v[4:7], v[96:99]\n\t" \
  "ds_read_b64_tr_b16 v[40:41], v58 offset:0\n\t" \
  "v_mfma_f32_16x16x32_bf16 v[104:107], v[44:47], v[8:11], v[104:107]\n\t" \
  "ds_read_b64_tr_b16 v[42:43], v58 offset:512\n\t" \
  "v_mfma_f32_16x16x32_bf16 v[112:115], v[44:47], v[12:15], v[112:115]\n\t" \
  "ds_read_b128 v[20:23], v57 offset:0\n\t" \
  "v_mfma_f32_16x16x32_bf16 v[120:123], v[44:47], v[16:19], v[120:123]\n\t" \
  "ds_read_b128 v[24:27], v57 offset:10240\n\t" \
  "s_waitcnt lgkmcnt(6)\n\t" \
  "v_mfma_f32_16x16x32_bf16 v[164:167], v[0:3], v[48:51], v[164:167]\n\t" \
  "ds_read_b64_tr_b16 v[44:45], v59 offset:0\n\t" \
  "v_mfma_f32_16x16x32_bf16 v[132:135], v[4:7], v[48:51], v[132:135]\n\t" \
  "ds_read_b64_tr_b16 v[46:47], v59 offset:512\n\t" \
  "v_mfma_f32_16x16x32_bf16 v[140:143], v[8:11], v[48:51], v[140:143]\n\t" \
  "ds_read_b128 v[28:31], v57 offset:12288\n\t" \
  "v_mfma_f32_16x16x32_bf16 v[148:151], v[12:15], v[48:51], v[148:151]\n\t" \
  "ds_read_b128 v[32:35], v57 offset:14336\n\t" \
  "v_mfma_f32_16x16x32_bf16 v[156:159], v[16:19], v[48:51], v[156:159]\n\t" \
  "s_waitcnt lgkmcnt(8)\n\t" \
  "v_mfma_f32_16x16x32_bf16 v[100:103], v[52:55], v[4:7], v[100:103]\n\t" \
  "ds_read_b64_tr_b16 v[48:49], v58 offset:20480\n\t" \
  "v_mfma_f32_16x16x32_bf16 v[108:111], v[52:55], v[8:11], v[108:111]\n\t" \
  "ds_read_b64_tr_b16 v[50:51], v58 offset:20992\n\t" \
  "v_mfma_f32_16x16x32_bf16 v[116:119], v[52:55], v[12:15], v[116:119]\n\t" \
  "ds_read_b128 v[36:39], v57 offset:16384\n\t" \
  "v_mfma_f32_16x16x32_bf16 v[124:127], v[52:55], v[16:19], v[124:127]\n\t" \
  "s_waitcnt lgkmcnt(9)\n\t" \
  "v_mfma_f32_16x16x32_bf16 v[64:67], v[40:43], v[4:7], v[64:67]\n\t" \
  "ds_read_b64_tr_b16 v[52:53], v58 offset:16384\n\t" \
  "v_mfma_f32_16x16x32_bf16 v[72:75], v[40:43], v[8:11], v[72:75]\n\t" \
  "ds_read_b64_tr_b16 v[54:55], v58 offset:16896\n\t" \
  "v_mfma_f32_16x16x32_bf16 v[80:83], v[40:43], v[12:15], v[80:83]\n\t" \
  "v_mfma_f32_16x16x32_bf16 v[88:91], v[40:43], v[16:19], v[88:91]\n\t" \
  "s_waitcnt lgkmcnt(7)\n\t" \
  "v_mfma_f32_16x16x32_bf16 v[68:71], v[44:47], v[4:7], v[68:71]\n\t" \
  "ds_read_b64_tr_b16 v[40:41], v59 offset:20480\n\t" \
  "v_mfma_f32_16x16x32_bf16 v[76:79], v[44:47], v[8:11], v[76:79]\n\t" \
  "ds_read_b64_tr_b16 v[42:43], v59 offset:20992\n\t" \
  "v_mfma_f32_16x16x32_bf16 v[84:87], v[44:47], v[12:15], v[84:87]\n\t" \
  "v_mfma_f32_16x16x32_bf16 v[92:95], v[44:47], v[16:19], v[92:95]\n\t" \
  "N1b_%=:\n\t" \
  "s_waitcnt lgkmcnt(4)\n\t" \
  "v_mfma_f32_16x16x32_bf16 v[160:163], v[20:23], v[48:51], v[160:163]\n\t" \
  "ds_read_b64_tr_b16 v[44:45], v59 offset:16384\n\t" \
  "v_mfma_f32_16x16x32_bf16 v[128:131], v[24:27], v[48:51], v[128:131]\n\t" \
  "ds_read_b64_tr_b16 v[46:47], v59 offset:16896\n\t" \
  "v_mfma_f32_16x16x32_bf16 v[136:139], v[28:31], v[48:51], v[136:139]\n\t" \
  "s_barrier\n\t" \
  "v_mfma_f32_16x16x32_bf16 v[144:147], v[32:35], v[48:51], v[144:147]\n\t" \
  "v_mfma_f32_16x16x32_bf16 v[152:155], v[36:39], v[48:51], v[152:155]\n\t" \
  "s_waitcnt lgkmcnt(4)\n\t" \
  "v_mfma_f32_16x16x32_bf16 v[96:99], v[52:55], v[24:27], v[96:99]\n\t" \
  "ds_read_b64_tr_b16 v[48:49], v58 offset:12288\n\t" \
  "v_mfma_f32_16x16x32_bf16 v[104:107], v[52:55], v[28:31], v[104:107]\n\t" \
  "ds_read_b64_tr_b16 v[50:51], v58 offset:12800\n\t" \
  "v_mfma_f32_16x16x32_bf16 v[112:115], v[52:55], v[32:35], v[112:115]\n\t" \
  "ds_read_b128 v[0:3], v56 offset:18432\n\t" \
  "v_mfma_f32_16x16x32_bf16 v[120:123], v[52:55], v[36:39], v[120:123]\n\t" \
  "ds_read_b128 v[4:7], v56 offset:28672\n\t" \
  "s_waitcnt lgkmcnt(6)\n\t" \
  "v_mfma_f32_16x16x32_bf16 v[164:167], v[20:23], v[40:43], v[164:167]\n\t" \
  "ds_read_b64_tr_b16 v[52:53], v59 offset:12288\n\t" \
  "v_mfma_f32_16x16x32_bf16 v[132:135], v[24:27], v[40:43], v[132:135]\n\t" \
  "ds_read_b64_tr_b16 v[54:55], v59 offset:12800\n\t" \
  "v_mfma_f32_16x16x32_bf16 v[140:143], v[28:31], v[40:43], v[140:143]\n\t" \
  "ds_read_b128 v[8:11], v56 offset:30720\n\t" \
  "v_mfma_f32_16x16x32_bf16 v[148:151], v[32:35], v[40:43], v[148:151]\n\t" \
  "ds_read_b128 v[12:15], v56 offset:32768\n\t" \
  "v_mfma_f32_16x16x32_bf16 v[156:159], v[36:39], v[40:43], v[156:159]\n\t" \
  "s_waitcnt lgkmcnt(8)\n\t" \
  "v_mfma_f32_16x16x32_bf16 v[100:103], v[44:47], v[24:27], v[100:103]\n\t" \
  "ds_read_b64_tr_b16 v[40:41], v58 offset:32768\n\t" \
  "v_mfma_f32_16x16x32_bf16 v[108:111], v[44:47], v[28:31], v[108:111]\n\t" \
  "ds_read_b64_tr_b16 v[42:43], v58 offset:33280\n\t" \
  "v_mfma_f32_16x16x32_bf16 v[116:119], v[44:47], v[32:35], v[116:119]\n\t" \
  "ds_read_b128 v[16:19], v56 offset:34816\n\t" \
  "v_mfma_f32_16x16x32_bf16 v[124:127], v[44:47], v[36:39], v[124:127]\n\t" \
  "s_waitcnt lgkmcnt(9)\n\t" \
  "v_mfma_f32_16x16x32_bf16 v[64:67], v[48:51], v[24:27], v[64:67]\n\t" \
  "ds_read_b64_tr_b16 v[44:45], v58 offset:28672\n\t" \
  "v_mfma_f32_16x16x32_bf16 v[72:75], v[48:51], v[28:31], v[72:75]\n\t" \
  "ds_read_b64_tr_b16 v[46:47], v58 offset:29184\n\t" \
  "v_mfma_f32_16x16x32_bf16 v[80:83], v[48:51], v[32:35], v[80:83]\n\t" \
  "v_mfma_f32_16x16x32_bf16 v[88:91], v[48:51], v[36:39], v[88:91]\n\t" \
  "s_waitcnt lgkmcnt(7)\n\t" \
  "v_mfma_f32_16x16x32_bf16 v[68:71], v[52:55], v[24:27], v[68:71]\n\t" \
  "ds_read_b64_tr_b16 v[48:49], v59 offset:32768\n\t" \
  "v_mfma_f32_16x16x32_bf16 v[76:79], v[52:55], v[28:31], v[76:79]\n\t" \
  "ds_read_b64_tr_b16 v[50:51], v59 offset:33280\n\t" \
  "v_mfma_f32_16x16x32_bf16 v[84:87], v[52:55], v[32:35], v[84:87]\n\t" \
  "v_mfma_f32_16x16x32_bf16 v[92:95], v[52:55], v[36:39], v[92:95]\n\t" \
  "N2b_%=:\n\t" \
  "s_waitcnt lgkmcnt(4)\n\t" \
  "v_mfma_f32_16x16x32_bf16 v[160:163], v[0:3], v[40:43], v[160:163]\n\t" \
  "ds_read_b64_tr_b16 v[52:53], v59 offset:28672\n\t" \
  "v_mfma_f32_16x16x32_bf16 v[128:131], v[4:7], v[40:43], v[128:131]\n\t" \
  "ds_read_b64_tr_b16 v[54:55], v59 offset:29184\n\t" \
  "v_mfma_f32_16x16x32_bf16 v[136:139], v[8:11], v[40:43], v[136:139]\n\t" \
  "s_barrier\n\t" \
  "v_mfma_f32_16x16x32_bf16 v[144:147], v[12:15], v[40:43], v[144:147]\n\t" \
  "v_mfma_f32_16x16x32_bf16 v[152:155], v[16:19], v[40:43], v[152:155]\n\t" \
  "s_waitcnt lgkmcnt(4)\n\t" \
  "v_mfma_f32_16x16x32_bf16 v[96:99], v[44:47], v[4:7], v[96:99]\n\t" \
  "ds_read_b64_tr_b16 v[40:41], v58 offset:24576\n\t" \
  "v_mfma_f32_16x16x32_bf16 v[104:107], v[44:47], v[8:11], v[104:107]\n\t" \
  "ds_read_b64_tr_b16 v[42:43], v58 offset:25088\n\t" \
  "v_mfma_f32_16x16x32_bf16 v[112:115], v[44:47], v[12:15], v[112:115]\n\t" \
  "ds_read_b128 v[20:23], v57 offset:18432\n\t" \
  "v_mfma_f32_16x16x32_bf16 v[120:123], v[44:47], v[16:19], v[120:123]\n\t" \
  "ds_read_b128 v[24:27], v57 offset:28672\n\t" \
  "s_waitcnt lgkmcnt(6)\n\t" \
  "v_mfma_f32_16x16x32_bf16 v[164:167], v[0:3], v[48:51], v[164:167]\n\t" \
  "ds_read_b64_tr_b16 v[44:45], v59 offset:24576\n\t" \
  "v_mfma_f32_16x16x32_bf16 v[132:135], v[4:7], v[48:51], v[132:135]\n\t" \
  "ds_read_b64_tr_b16 v[46:47], v59 offset:25088\n\t" \
  "v_mfma_f32_16x16x32_bf16 v[140:143], v[8:11], v[48:51], v[140:143]\n\t" \
  "ds_read_b128 v[28:31], v57 offset:30720\n\t" \
  "v_mfma_f32_16x16x32_bf16 v[148:151], v[12:15], v[48:51], v[148:151]\n\t" \
  "ds_read_b128 v[32:35], v57 offset:32768\n\t" \
  "v_mfma_f32_16x16x32_bf16 v[156:159], v[16:19], v[48:51], v[156:159]\n\t" \
  "s_waitcnt lgkmcnt(8)\n\t" \
  "v_mfma_f32_16x16x32_bf16 v[100:103], v[52:55], v[4:7], v[100:103]\n\t" \
  "ds_read_b64_tr_b16 v[48:49], v60 offset:8192\n\t" \
  "v_mfma_f32_16x16x32_bf16 v[108:111], v[52:55], v[8:11], v[108:111]\n\t" \
  "ds_read_b64_tr_b16 v[50:51], v60 offset:8704\n\t" \
  "v_mfma_f32_16x16x32_bf16 v[116:119], v[52:55], v[12:15], v[116:119]\n\t" \
  "ds_read_b128 v[36:39], v57 offset:34816\n\t" \
  "v_mfma_f32_16x16x32_bf16 v[124:127], v[52:55], v[16:19], v[124:127]\n\t" \
  "s_waitcnt lgkmcnt(9)\n\t" \
  "v_mfma_f32_16x16x32_bf16 v[64:67], v[40:43], v[4:7], v[64:67]\n\t" \
  "ds_read_b64_tr_b16 v[52:53], v60 offset:4096\n\t" \
  "v_mfma_f32_16x16x32_bf16 v[72:75], v[40:43], v[8:11], v[72:75]\n\t" \
  "ds_read_b64_tr_b16 v[54:55], v60 offset:4608\n\t" \
  "v_mfma_f32_16x16x32_bf16 v[80:83], v[40:43], v[12:15], v[80:83]\n\t" \
  "v_mfma_f32_16x16x32_bf16 v[88:91], v[40:43], v[16:19], v[88:91]\n\t" \
  "s_waitcnt lgkmcnt(7)\n\t" \
  "v_mfma_f32_16x16x32_bf16 v[68:71], v[44:47], v[4:7], v[68:71]\n\t" \
  "ds_read_b64_tr_b16 v[40:41], v61 offset:8192\n\t" \
  "v_mfma_f32_16x16x32_bf16 v[76:79], v[44:47], v[8:11], v[76:79]\n\t" \
  "ds_read_b64_tr_b16 v[42:43], v61 offset:8704\n\t" \
  "v_mfma_f32_16x16x32_bf16 v[84:87], v[44:47], v[12:15], v[84:87]\n\t" \
  "v_mfma_f32_16x16x32_bf16 v[92:95], v[44:47], v[16:19], v[92:95]\n\t" \
  "N3b_%=:\n\t" \
  "s_waitcnt lgkmcnt(4)\n\t" \
  "v_mfma_f32_16x16x32_bf16 v[160:163], v[20:23], v[48:51], v[160:163]\n\t" \
  "ds_read_b64_tr_b16 v[44:45], v61 offset:4096\n\t" \
  "v_mfma_f32_16x16x32_bf16 v[128:131], v[24:27], v[48:51], v[128:131]\n\t" \
  "ds_read_b64_tr_b16 v[46:47], v61 offset:4608\n\t" \
  "v_mfma_f32_16x16x32_bf16 v[136:139], v[28:31], v[48:51], v[136:139]\n\t" \
  "s_barrier\n\t" \
  "v_mfma_f32_16x16x32_bf16 v[144:147], v[32:35], v[48:51], v[144:147]\n\t" \
  "v_mfma_f32_16x16x32_bf16 v[152:155], v[36:39], v[48:51], v[152:155]\n\t" \
  "s_waitcnt lgkmcnt(4)\n\t" \
  "v_mfma_f32_16x16x32_bf16 v[96:99], v[52:55], v[24:27], v[96:99]\n\t" \
  "ds_read_b64_tr_b16 v[48:49], v60 offset:0\n\t" \
  "v_mfma_f32_16x16x32_bf16 v[104:107], v[52:55], v[28:31], v[104:107]\n\t" \
  "ds_read_b64_tr_b16 v[50:51], v60 offset:512\n\t" \
  "v_mfma_f32_16x16x32_bf16 v[112:115], v[52:55], v[32:35], v[112:115]\n\t" \
  "ds_read_b128 v[0:3], v56 offset:36864\n\t" \
  "v_mfma_f32_16x16x32_bf16 v[120:123], v[52:55], v[36:39], v[120:123]\n\t" \
  "ds_read_b128 v[4:7], v56 offset:47104\n\t" \
  "s_waitcnt lgkmcnt(6)\n\t" \
  "v_mfma_f32_16x16x32_bf16 v[164:167], v[20:23], v[40:43], v[164:167]\n\t" \
  "ds_read_b64_tr_b16 v[52:53], v61 offset:0\n\t" \
  "v_mfma_f32_16x16x32_bf16 v[132:135], v[24:27], v[40:43], v[132:135]\n\t" \
  "ds_read_b64_tr_b16 v[54:55], v61 offset:512\n\t" \
  "v_mfma_f32_16x16x32_bf16 v[140:143], v[28:31], v[40:43], v[140:143]\n\t" \
  "ds_read_b128 v[8:11], v56 offset:49152\n\t" \
  "v_mfma_f32_16x16x32_bf16 v[148:151], v[32:35], v[40:43], v[148:151]\n\t" \
  "ds_read_b128 v[12:15], v56 offset:51200\n\t" \
  "v_mfma_f32_16x16x32_bf16 v[156:159], v[36:39], v[40:43], v[156:159]\n\t" \
  "s_waitcnt lgkmcnt(8)\n\t" \
  "v_mfma_f32_16x16x32_bf16 v[100:103], v[44:47], v[24:27], v[100:103]\n\t" \
  "ds_read_b64_tr_b16 v[40:41], v60 offset:20480\n\t" \
  "v_mfma_f32_16x16x32_bf16 v[108:111], v[44:47], v[28:31], v[108:111]\n\t" \
  "ds_read_b64_tr_b16 v[42:43], v60 offset:20992\n\t" \
  "v_mfma_f32_16x16x32_bf16 v[116:119], v[44:47], v[32:35], v[116:119]\n\t" \
  "ds_read_b128 v[16:19], v56 offset:53248\n\t" \
  "v_mfma_f32_16x16x32_bf16 v[124:127], v[44:47], v[36:39], v[124:127]\n\t" \
  "s_waitcnt lgkmcnt(9)\n\t" \
  "v_mfma_f32_16x16x32_bf16 v[64:67], v[48:51], v[24:27], v[64:67]\n\t" \
  "ds_read_b64_tr_b16 v[44:45], v60 offset:16384\n\t" \
  "v_mfma_f32_16x16x32_bf16 v[72:75], v[48:51], v[28:31], v[72:75]\n\t" \
  "ds_read_b64_tr_b16 v[46:47], v60 offset:16896\n\t" \
  "v_mfma_f32_16x16x32_bf16 v[80:83], v[48:51], v[32:35], v[80:83]\n\t" \
  "v_mfma_f32_16x16x32_bf16 v[88:91], v[48:51], v[36:39], v[88:91]\n\t" \
  "s_waitcnt lgkmcnt(7)\n\t" \
  "v_mfma_f32_16x16x32_bf16 v[68:71], v[52:55], v[24:27], v[68:71]\n\t" \
  "ds_read_b64_tr_b16 v[48:49], v61 offset:20480\n\t" \
  "v_mfma_f32_16x16x32_bf16 v[76:79], v[52:55], v[28:31], v[76:79]\n\t" \
  "ds_read_b64_tr_b16 v[50:51], v61 offset:20992\n\t" \
  "v_mfma_f32_16x16x32_bf16 v[84:87], v[52:55], v[32:35], v[84:87]\n\t" \
  "v_mfma_f32_16x16x32_bf16 v[92:95], v[52:55], v[36:39], v[92:95]\n\t" \
  "N4b_%=:\n\t" \
  "s_waitcnt lgkmcnt(4)\n\t" \
  "v_mfma_f32_16x16x32_bf16 v[160:163], v[0:3], v[40:43], v[160:163]\n\t" \
  "ds_read_b64_tr_b16 v[52:53], v61 offset:16384\n\t" \
  "v_mfma_f32_16x16x32_bf16 v[128:131], v[4:7], v[40:43], v[128:131]\n\t" \
  "ds_read_b64_tr_b16 v[54:55], v61 offset:16896\n\t" \
  "v_mfma_f32_16x16x32_bf16 v[136:139], v[8:11], v[40:43], v[136:139]\n\t" \
  "s_barrier\n\t" \
  "v_mfma_f32_16x16x32_bf16 v[144:147], v[12:15], v[40:43], v[144:147]\n\t" \
  "v_mfma_f32_16x16x32_bf16 v[152:155], v[16:19], v[40:43], v[152:155]\n\t" \
  "s_waitcnt lgkmcnt(4)\n\t" \
  "v_mfma_f32_16x16x32_bf16 v[96:99], v[44:47], v[4:7], v[96:99]\n\t" \
  "ds_read_b64_tr_b16 v[40:41], v60 offset:12288\n\t" \
  "v_mfma_f32_16x16x32_bf16 v[104:107], v[44:47], v[8:11], v[104:107]\n\t" \
  "ds_read_b64_tr_b16 v[42:43], v60 offset:12800\n\t" \
  "v_mfma_f32_16x16x32_bf16 v[112:115], v[44:47], v[12:15], v[112:115]\n\t" \
  "ds_read_b128 v[20:23], v57 offset:36864\n\t" \
  "v_mfma_f32_16x16x32_bf16 v[120:123], v[44:47], v[16:19], v[120:123]\n\t" \
  "ds_read_b128 v[24:27], v57 offset:47104\n\t" \
  "s_waitcnt lgkmcnt(6)\n\t" \
  "v_mfma_f32_16x16x32_bf16 v[164:167], v[0:3], v[48:51], v[164:167]\n\t" \
  "ds_read_b64_tr_b16 v[44:45], v61 offset:12288\n\t" \
  "v_mfma_f32_16x16x32_bf16 v[132:135], v[4:7], v[48:51], v[132:135]\n\t" \
  "ds_read_b64_tr_b16 v[46:47], v61 offset:12800\n\t" \
  "v_mfma_f32_16x16x32_bf16 v[140:143], v[8:11], v[48:51], v[140:143]\n\t" \
  "ds_read_b128 v[28:31], v57 offset:49152\n\t" \
  "v_mfma_f32_16x16x32_bf16 v[148:151], v[12:15], v[48:51], v[148:151]\n\t" \
  "ds_read_b128 v[32:35], v57 offset:51200\n\t" \
  "v_mfma_f32_16x16x32_bf16 v[156:159], v[16:19], v[48:51], v[156:159]\n\t" \
  "s_waitcnt lgkmcnt(8)\n\t" \
  "v_mfma_f32_16x16x32_bf16 v[100:103], v[52:55], v[4:7], v[100:103]\n\t" \
  "ds_read_b64_tr_b16 v[48:49], v60 offset:32768\n\t" \
  "v_mfma_f32_16x16x32_bf16 v[108:111], v[52:55], v[8:11], v[108:111]\n\t" \
  "ds_read_b64_tr_b16 v[50:51], v60 offset:33280\n\t" \
  "v_mfma_f32_16x16x32_bf16 v[116:119], v[52:55], v[12:15], v[116:119]\n\t" \
  "ds_read_b128 v[36:39], v57 offset:53248\n\t" \
  "v_mfma_f32_16x16x32_bf16 v[124:127], v[52:55], v[16:19], v[124:127]\n\t" \
  "s_waitcnt lgkmcnt(9)\n\t" \
  "v_mfma_f32_16x16x32_bf16 v[64:67], v[40:43], v[4:7], v[64:67]\n\t" \
  "ds_read_b64_tr_b16 v[52:53], v60 offset:28672\n\t" \
  "v_mfma_f32_16x16x32_bf16 v[72:75], v[40:43], v[8:11], v[72:75]\n\t" \
  "ds_read_b64_tr_b16 v[54:55], v60 offset:29184\n\t" \
  "v_mfma_f32_16x16x32_bf16 v[80:83], v[40:43], v[12:15], v[80:83]\n\t" \
  "v_mfma_f32_16x16x32_bf16 v[88:91], v[40:43], v[16:19], v[88:91]\n\t" \
  "s_waitcnt lgkmcnt(7)\n\t" \
  "v_mfma_f32_16x16x32_bf16 v[68:71], v[44:47], v[4:7], v[68:71]\n\t" \
  "ds_read_b64_tr_b16 v[40:41], v61 offset:32768\n\t" \
  "v_mfma_f32_16x16x32_bf16 v[76:79], v[44:47], v[8:11], v[76:79]\n\t" \
  "ds_read_b64_tr_b16 v[42:43], v61 offset:33280\n\t" \
  "v_mfma_f32_16x16x32_bf16 v[84:87], v[44:47], v[12:15], v[84:87]\n\t" \
  "v_mfma_f32_16x16x32_bf16 v[92:95], v[44:47], v[16:19], v[92:95]\n\t" \
  "N5b_%=:\n\t" \
  "s_waitcnt lgkmcnt(4)\n\t" \
  "v_mfma_f32_16x16x32_bf16 v[160:163], v[20:23], v[48:51], v[160:163]\n\t" \
  "ds_read_b64_tr_b16 v[44:45], v61 offset:28672\n\t" \
  "v_mfma_f32_16x16x32_bf16 v[128:131], v[24:27], v[48:51], v[128:131]\n\t" \
  "ds_read_b64_tr_b16 v[46:47], v61 offset:29184\n\t" \
  "v_mfma_f32_16x16x32_bf16 v[136:139], v[28:31], v[48:51], v[136:139]\n\t" \
  "s_barrier\n\t" \
  "v_mfma_f32_16x16x32_bf16 v[144:147], v[32:35], v[48:51], v[144:147]\n\t" \
  "v_mfma_f32_16x16x32_bf16 v[152:155], v[36:39], v[48:51], v[152:155]\n\t" \
  "s_waitcnt lgkmcnt(4)\n\t" \
  "v_mfma_f32_16x16x32_bf16 v[96:99], v[52:55], v[24:27], v[96:99]\n\t" \
  "ds_read_b64_tr_b16 v[48:49], v60 offset:24576\n\t" \
  "v_mfma_f32_16x16x32_bf16 v[104:107], v[52:55], v[28:31], v[104:107]\n\t" \
  "ds_read_b64_tr_b16 v[50:51], v60 offset:25088\n\t" \
  "v_mfma_f32_16x16x32_bf16 v[112:115], v[52:55], v[32:35], v[112:115]\n\t" \
  "ds_read_b128 v[0:3], v56 offset:0\n\t" \
  "v_mfma_f32_16x16x32_bf16 v[120:123], v[52:55], v[36:39], v[120:123]\n\t" \
  "ds_read_b128 v[4:7], v56 offset:10240\n\t" \
  "s_waitcnt lgkmcnt(6)\n\t" \
  "v_mfma_f32_16x16x32_bf16 v[164:167], v[20:23], v[40:43], v[164:167]\n\t" \
  "ds_read_b64_tr_b16 v[52:53], v61 offset:24576\n\t" \
  "v_mfma_f32_16x16x32_bf16 v[132:135], v[24:27], v[40:43], v[132:135]\n\t" \
  "ds_read_b64_tr_b16 v[54:55], v61 offset:25088\n\t" \
  "v_mfma_f32_16x16x32_bf16 v[140:143], v[28:31], v[40:43], v[140:143]\n\t" \
  "ds_read_b128 v[8:11], v56 offset:12288\n\t" \
  "v_mfma_f32_16x16x32_bf16 v[148:151], v[32:35], v[40:43], v[148:151]\n\t" \
  "ds_read_b128 v[12:15], v56 offset:14336\n\t" \
  "v_mfma_f32_16x16x32_bf16 v[156:159], v[36:39], v[40:43], v[156:159]\n\t" \
  "s_waitcnt lgkmcnt(8)\n\t" \
  "v_mfma_f32_16x16x32_bf16 v[100:103], v[44:47], v[24:27], v[100:103]\n\t" \
  "ds_read_b64_tr_b16 v[40:41], v58 offset:8192\n\t" \
  "v_mfma_f32_16x16x32_bf16 v[108:111], v[44:47], v[28:31], v[108:111]\n\t" \
  "ds_read_b64_tr_b16 v[42:43], v58 offset:8704\n\t" \
  "v_mfma_f32_16x16x32_bf16 v[116:119], v[44:47], v[32:35], v[116:119]\n\t" \
  "ds_read_b128 v[16:19], v56 offset:16384\n\t" \
  "v_mfma_f32_16x16x32_bf16 v[124:127], v[44:47], v[36:39], v[124:127]\n\t" \
  "s_waitcnt lgkmcnt(9)\n\t" \
  "v_mfma_f32_16x16x32_bf16 v[64:67], v[48:51], v[24:27], v[64:67]\n\t" \
  "ds_read_b64_tr_b16 v[44:45], v58 offset:4096\n\t" \
  "v_mfma_f32_16x16x32_bf16 v[72:75], v[48:51], v[28:31], v[72:75]\n\t" \
  "ds_read_b64_tr_b16 v[46:47], v58 offset:4608\n\t" \
  "v_mfma_f32_16x16x32_bf16 v[80:83], v[48:51], v[32:35], v[80:83]\n\t" \
  "v_mfma_f32_16x16x32_bf16 v[88:91], v[48:51], v[36:39], v[88:91]\n\t" \
  "s_waitcnt lgkmcnt(7)\n\t" \
  "v_mfma_f32_16x16x32_bf16 v[68:71], v[52:55], v[24:27], v[68:71]\n\t" \
  "ds_read_b64_tr_b16 v[48:49], v59 offset:8192\n\t" \
  "v_mfma_f32_16x16x32_bf16 v[76:79], v[52:55], v[28:31], v[76:79]\n\t" \
  "ds_read_b64_tr_b16 v[50:51], v59 offset:8704\n\t" \
  "s_addk_i32 %[it], 1\n\t" \
  "v_mfma_f32_16x16x32_bf16 v[84:87], v[52:55], v[32:35], v[84:87]\n\t" \
  "s_cmp_lt_u32 %[it], 6\n\t" \
  "v_mfma_f32_16x16x32_bf16 v[92:95], v[52:55], v[36:39], v[92:95]\n\t" \
  "s_cbranch_scc1 N0b_%=\n\t" \
  "s_setprio 3\n\t" \
  "v_cvt_pk_bf16_f32 v160, v160, v161\n\t" \
  "v_cvt_pk_bf16_f32 v161, v162, v163\n\t" \
  "v_cvt_pk_bf16_f32 v162, v164, v165\n\t" \
  "v_cvt_pk_bf16_f32 v163, v166, v167\n\t" \
  "ds_write_b128 v63, v[160:163] offset:2048\n\t" \
  "v_mov_b32 v57, %[fa]\n\t" \
  "v_mov_b32 v58, 1\n\t" \
  "s_mov_b64 exec, 1\n\t" \
  "ds_write_b32 v57, v58\n\t" \
  "s_mov_b64 exec, -1\n\t" \
  "v_cvt_pk_bf16_f32 v120, v120, v121\n\t" \
  "v_cvt_pk_bf16_f32 v121, v122, v123\n\t" \
  "v_cvt_pk_bf16_f32 v122, v124, v125\n\t" \
  "v_cvt_pk_bf16_f32 v123, v126, v127\n\t" \
  "v_cvt_pk_bf16_f32 v28, v152, v153\n\t" \
  "v_cvt_pk_bf16_f32 v29, v154, v155\n\t" \
  "v_cvt_pk_bf16_f32 v38, v156, v157\n\t" \
  "v_cvt_pk_bf16_f32 v39, v158, v159\n\t" \
  "NX_%=:\n\t" \
  "v_cvt_pk_bf16_f32 v96, v96, v97\n\t" \
  "v_cvt_pk_bf16_f32 v97, v98, v99\n\t" \
  "v_cvt_pk_bf16_f32 v98, v100, v101\n\t" \
  "v_cvt_pk_bf16_f32 v99, v102, v103\n\t" \
  "v_cvt_pk_bf16_f32 v104, v104, v105\n\t" \
  "v_cvt_pk_bf16_f32 v105, v106, v107\n\t" \
  "v_cvt_pk_bf16_f32 v106, v108, v109\n\t" \
  "v_cvt_pk_bf16_f32 v107, v110, v111\n\t" \
  "v_mov_b32 v60, 0xff61b1e6\n\t" \
  "v_cndmask_b32_e64 v56, v60, 0, s[68:69]\n\t" \
  "v_cndmask_b32_e64 v57, v60, 0, s[70:71]\n\t" \
  "v_cndmask_b32_e64 v58, v60, 0, s[72:73]\n\t" \
  "v_cndmask_b32_e64 v59, v60, 0, s[74:75]\n\t" \
  "v_cvt_pk_bf16_f32 v72, v72, v73\n\t" \
  "v_cvt_pk_bf16_f32 v73, v74, v75\n\t" \
  "v_cvt_pk_bf16_f32 v74, v76, v77\n\t" \
  "v_cvt_pk_bf16_f32 v75, v78, v79\n\t" \
  "v_cndmask_b32_e64 v52, v60, 0, s[76:77]\n\t" \
  "v_cndmask_b32_e64 v53, v60, 0, s[78:79]\n\t" \
  "v_cndmask_b32_e64 v54, v60, 0, s[80:81]\n\t" \
  "v_cndmask_b32_e64 v55, v60, 0, s[82:83]\n\t" \
  "v_cvt_pk_bf16_f32 v24, v136, v137\n\t" \
  "v_cvt_pk_bf16_f32 v25, v138, v139\n\t" \
  "v_cvt_pk_bf16_f32 v34, v140, v141\n\t" \
  "v_cvt_pk_bf16_f32 v35, v142, v143\n\t" \
  "v_cvt_pk_bf16_f32 v26, v144, v145\n\t" \
  "v_cvt_pk_bf16_f32 v27, v146, v147\n\t" \
  "v_cvt_pk_bf16_f32 v36, v148, v149\n\t" \
  "v_cvt_pk_bf16_f32 v37, v150, v151\n\t" \
  "v_mfma_f32_16x16x32_bf16 v[136:139], v[96:99], v[72:75], v[56:59]\n\t" \
  "v_cvt_pk_bf16_f32 v112, v112, v113\n\t" \
  "v_cvt_pk_bf16_f32 v113, v114, v115\n\t" \
  "v_mfma_f32_16x16x32_bf16 v[140:143], v[104:107], v[72:75], v[52:55]\n\t" \
  "v_cvt_pk_bf16_f32 v114, v116, v117\n\t" \
  "v_cvt_pk_bf16_f32 v115, v118, v119\n\t" \
  "v_cvt_pk_bf16_f32 v80, v80, v81\n\t" \
  "v_cvt_pk_bf16_f32 v81, v82, v83\n\t" \
  "v_cvt_pk_bf16_f32 v82, v84, v85\n\t" \
  "v_cvt_pk_bf16_f32 v83, v86, v87\n\t" \
  "v_cvt_pk_bf16_f32 v22, v128, v129\n\t" \
  "v_cvt_pk_bf16_f32 v23, v130, v131\n\t" \
  "v_mfma_f32_16x16x32_bf16 v[144:147], v[104:107], v[80:83], v[56:59]\n\t" \
  "v_cvt_pk_bf16_f32 v32, v132, v133\n\t" \
  "v_cvt_pk_bf16_f32 v33, v134, v135\n\t" \
  "v_mfma_f32_16x16x32_bf16 v[148:151], v[112:115], v[80:83], v[52:55]\n\t" \
  "v_cvt_pk_bf16_f32 v88, v88, v89\n\t" \
  "v_cvt_pk_bf16_f32 v89, v90, v91\n\t" \
  "v_cvt_pk_bf16_f32 v90, v92, v93\n\t" \
  "v_cvt_pk_bf16_f32 v91, v94, v95\n\t" \
  "v_mov_b32 v61, %[fp]\n\t" \
  "v_add_u32 v60, %[vo], v63\n\t" \
  "v_mfma_f32_16x16x32_bf16 v[152:155], v[112:115], v[88:91], v[56:59]\n\t" \
  "ds_read_b32 v0, v61\n\t" \
  "ds_read_b64 v[20:21], v60\n\t" \
  "ds_read_b64 v[30:31], v60 offset:8\n\t" \
  "s_mov_b64 exec, %[rm]\n\t" \
  "ds_read_b128 v[160:163], v63\n\t" \
  "s_mov_b64 exec, -1\n\t" \
  "v_mfma_f32_16x16x32_bf16 v[156:159], v[120:123], v[88:91], v[52:55]\n\t" \
  "v_cvt_pk_bf16_f32 v64, v64, v65\n\t" \
  "v_cvt_pk_bf16_f32 v65, v66, v67\n\t" \
  "v_cvt_pk_bf16_f32 v66, v68, v69\n\t" \
  "v_cvt_pk_bf16_f32 v67, v70, v71\n\t" \
  "v_max_f32 v136, v136, v137\n\t" \
  "v_max_f32 v138, v138, v139\n\t" \
  "v_mfma_f32_16x16x32_bf16 v[132:135], v[96:99], v[64:67], v[52:55]\n\t" \
  "v_max_f32 v140, v140, v141\n\t" \
  "v_max_f32 v142, v142, v143\n\t" \
  "AW_%=:\n\t" \
  "s_waitcnt lgkmcnt(0)\n\t" \
  "v_cmp_eq_u32 vcc, 0, v0\n\t" \
  "v_max3_f32 v5, v136, v138, v140\n\t" \
  "v_max_f32 v5, v5, v142\n\t" \
  "s_cbranch_vccnz AR_%=\n\t" \
  "s_cmp_eq_u32 %[z], 0\n\t" \
  "s_cbranch_scc1 AZ_%=\n\t" \
  "AY_%=:\n\t" \
  "v_mfma_f32_16x16x32_bf16 v[128:131], v[160:163], v[64:67], v[56:59]\n\t" \
  "v_max_f32 v144, v144, v145\n\t" \
  "v_max_f32 v146, v146, v147\n\t" \
  "v_max_f32 v148, v148, v149\n\t" \
  "v_max_f32 v150, v150, v151\n\t" \
  "v_max3_f32 v6, v144, v146, v148\n\t" \
  "v_max_f32 v6, v6, v150\n\t" \
  "v_max_f32 v152, v152, v153\n\t" \
  "v_max_f32 v154, v154, v155\n\t" \
  "v_max_f32 v156, v156, v157\n\t" \
  "v_max_f32 v158, v158, v159\n\t" \
  "v_max3_f32 v7, v152, v154, v156\n\t" \
  "v_max_f32 v7, v7, v158\n\t" \
  "v_mov_b32 v8, 0xc0c0100\n\t" \
  "v_mov_b32 v9, 0x1000c0c\n\t" \
  "v_max_f32 v128, v128, v129\n\t" \
  "v_max_f32 v130, v130, v131\n\t" \
  "v_max_f32 v132, v132, v133\n\t" \
  "v_max_f32 v134, v134, v135\n\t" \
  "v_max3_f32 v4, v128, v130, v132\n\t" \
  "v_max_f32 v4, v4, v134\n\t" \
  "v_cndmask_b32_e64 v60, v8, v9, s[84:85]\n\t" \
  "v_mov_b32 v8, 0xc0c0302\n\t" \
  "v_permlane16_swap_b32 v6, v7\n\t" \
  "v_permlane16_swap_b32 v4, v5\n\t" \
  "v_max_f32 v6, v6, v7\n\t" \
  "v_max_f32 v4, v4, v5\n\t" \
  "v_mov_b32 v9, 0x3020c0c\n\t" \
  "v_cndmask_b32_e64 v61, v8, v9, s[84:85]\n\t" \
  "v_permlane32_swap_b32 v4, v6\n\t" \
  "v_max_f32 v4, v4, v6\n\t" \
  "v_mul_f32 v6, 0xbe8293ee, v4\n\t" \
  "v_mul_f32 v4, 0xbe8293ee, v4\n\t" \
  "s_nop 1\n\t" \
  "v_permlane32_swap_b32 v4, v6\n\t" \
  "v_mov_b32 v5, v4\n\t" \
  "v_mov_b32 v7, v6\n\t" \
  "s_nop 0\n\t" \
  "v_permlane16_swap_b32 v4, v5\n\t" \
  "v_permlane16_swap_b32 v6, v7\n\t" \
  "v_fmamk_f32 v128, v128, 0x3e8293ee, v4\n\t" \
  "v_fmamk_f32 v130, v130, 0x3e8293ee, v4\n\t" \
  "v_fmamk_f32 v132, v132, 0x3e8293ee, v4\n\t" \
  "v_fmamk_f32 v134, v134, 0x3e8293ee, v4\n\t" \
  "v_fmamk_f32 v136, v136, 0x3e8293ee, v5\n\t" \
  "v_fmamk_f32 v138, v138, 0x3e8293ee, v5\n\t" \
  "v_fmamk_f32 v140, v140, 0x3e8293ee, v5\n\t" \
  "v_fmamk_f32 v142, v142, 0x3e8293ee, v5\n\t" \
  "v_fmamk_f32 v144, v144, 0x3e8293ee, v6\n\t" \
  "v_fmamk_f32 v146, v146, 0x3e8293ee, v6\n\t" \
  "v_fmamk_f32 v148, v148, 0x3e8293ee, v6\n\t" \
  "v_fmamk_f32 v150, v150, 0x3e8293ee, v6\n\t" \
  "v_fmamk_f32 v152, v152, 0x3e8293ee, v7\n\t" \
  "v_fmamk_f32 v154, v154, 0x3e8293ee, v7\n\t" \
  "v_fmamk_f32 v156, v156, 0x3e8293ee, v7\n\t" \
  "v_fmamk_f32 v158, v158, 0x3e8293ee, v7\n\t" \
  "v_exp_f32 v128, v128\n\t" \
  "v_exp_f32 v130, v130\n\t" \
  "v_exp_f32 v132, v132\n\t" \
  "v_exp_f32 v134, v134\n\t" \
  "v_exp_f32 v136, v136\n\t" \
  "v_exp_f32 v138, v138\n\t" \
  "v_exp_f32 v140, v140\n\t" \
  "v_exp_f32 v142, v142\n\t" \
  "v_exp_f32 v144, v144\n\t" \
  "v_exp_f32 v146, v146\n\t" \
  "v_exp_f32 v148, v148\n\t" \
  "v_exp_f32 v150, v150\n\t" \
  "v_exp_f32 v152, v152\n\t" \
  "v_exp_f32 v154, v154\n\t" \
  "v_exp_f32 v156, v156\n\t" \
  "v_exp_f32 v158, v158\n\t" \
  "v_add_f32 v4, v128, v130\n\t" \
  "v_add_f32 v8, v132, v134\n\t" \
  "v_add_f32 v5, v136, v138\n\t" \
  "v_add_f32 v9, v140, v142\n\t" \
  "v_add_f32 v6, v144, v146\n\t" \
  "v_add_f32 v10, v148, v150\n\t" \
  "v_add_f32 v7, v152, v154\n\t" \
  "v_add_f32 v11, v156, v158\n\t" \
  "v_add_f32 v4, v4, v8\n\t" \
  "v_add_f32 v5, v5, v9\n\t" \
  "v_add_f32 v6, v6, v10\n\t" \
  "v_add_f32 v7, v7, v11\n\t" \
  "v_permlane16_swap_b32 v4, v5\n\t" \
  "s_nop 0\n\t" \
  "v_permlane16_swap_b32 v6, v7\n\t" \
  "v_add_f32 v4, v4, v5\n\t" \
  "v_add_f32 v6, v6, v7\n\t" \
  "s_nop 1\n\t" \
  "v_permlane32_swap_b32 v4, v6\n\t" \
  "v_add_f32 v4, v4, v6\n\t" \
  "v_rcp_f32 v4, v4\n\t" \
  "s_nop 0\n\t" \
  "v_mov_b32 v6, v4\n\t" \
  "s_nop 1\n\t" \
  "v_permlane32_swap_b32 v4, v6\n\t" \
  "v_mov_b32 v5, v4\n\t" \
  "v_mov_b32 v7, v6\n\t" \
  "s_nop 0\n\t" \
  "v_permlane16_swap_b32 v4, v5\n\t" \
  "v_permlane16_swap_b32 v6, v7\n\t" \
  "v_mul_f32 v128, v128, v4\n\t" \
  "v_mul_f32 v130, v130, v4\n\t" \
  "v_mul_f32 v132, v132, v4\n\t" \
  "v_mul_f32 v134, v134, v4\n\t" \
  "v_cvt_pk_bf16_f32 v128, v128, v130\n\t" \
  "v_cvt_pk_bf16_f32 v132, v132, v134\n\t" \
  "v_perm_b32 v68, 0, v128, v60\n\t" \
  "v_perm_b32 v69, 0, v128, v61\n\t" \
  "v_perm_b32 v70, 0, v132, v60\n\t" \
  "v_perm_b32 v71, 0, v132, v61\n\t" \
  "v_mul_f32 v136, v136, v5\n\t" \
  "v_mul_f32 v138, v138, v5\n\t" \
  "v_mfma_f32_16x16x32_bf16 v[128:131], v[20:23], v[68:71], 0\n\t" \
  "v_mul_f32 v140, v140, v5\n\t" \
  "v_mul_f32 v142, v142, v5\n\t" \
  "v_cvt_pk_bf16_f32 v136, v136, v138\n\t" \
  "v_mfma_f32_16x16x32_bf16 v[132:135], v[30:33], v[68:71], 0\n\t" \
  "v_cvt_pk_bf16_f32 v140, v140, v142\n\t" \
  "v_perm_b32 v76, 0, v136, v60\n\t" \
  "v_perm_b32 v77, 0, v136, v61\n\t" \
  "v_perm_b32 v78, 0, v140, v60\n\t" \
  "v_perm_b32 v79, 0, v140, v61\n\t" \
  "v_mul_f32 v144, v144, v6\n\t" \
  "v_mul_f32 v146, v146, v6\n\t" \
  "v_mfma_f32_16x16x32_bf16 v[136:139], v[22:25], v[76:79], 0\n\t" \
  "v_mul_f32 v148, v148, v6\n\t" \
  "v_mul_f32 v150, v150, v6\n\t" \
  "v_cvt_pk_bf16_f32 v144, v144, v146\n\t" \
  "v_mfma_f32_16x16x32_bf16 v[140:143], v[32:35], v[76:79], 0\n\t" \
  "v_cvt_pk_bf16_f32 v148, v148, v150\n\t" \
  "v_perm_b32 v84, 0, v144, v60\n\t" \
  "v_perm_b32 v85, 0, v144, v61\n\t" \
  "v_perm_b32 v86, 0, v148, v60\n\t" \
  "v_perm_b32 v87, 0, v148, v61\n\t" \
  "v_cvt_pk_bf16_f32 v100, v128, v129\n\t" \
  "v_cvt_pk_bf16_f32 v101, v130, v131\n\t" \
  "v_mfma_f32_16x16x32_bf16 v[144:147], v[24:27], v[84:87], 0\n\t" \
  "v_cvt_pk_bf16_f32 v102, v132, v133\n\t" \
  "v_cvt_pk_bf16_f32 v103, v134, v135\n\t" \
  "global_store_dwordx4 v62, v[100:103], %[ot] offset:0 sc1\n\t" \
  "v_mfma_f32_16x16x32_bf16 v[148:151], v[34:37], v[84:87], 0\n\t" \
  "v_mul_f32 v152, v152, v7\n\t" \
  "v_mul_f32 v154, v154, v7\n\t" \
  "v_mul_f32 v156, v156, v7\n\t" \
  "v_mul_f32 v158, v158, v7\n\t" \
  "v_cvt_pk_bf16_f32 v152, v152, v154\n\t" \
  "v_cvt_pk_bf16_f32 v156, v156, v158\n\t" \
  "v_perm_b32 v92, 0, v152, v60\n\t" \
  "v_perm_b32 v93, 0, v152, v61\n\t" \
  "v_perm_b32 v94, 0, v156, v60\n\t" \
  "v_perm_b32 v95, 0, v156, v61\n\t" \
  "v_cvt_pk_bf16_f32 v108, v136, v137\n\t" \
  "v_cvt_pk_bf16_f32 v109, v138, v139\n\t" \
  "v_mfma_f32_16x16x32_bf16 v[152:155], v[26:29], v[92:95], 0\n\t" \
  "v_cvt_pk_bf16_f32 v110, v140, v141\n\t" \
  "v_cvt_pk_bf16_f32 v111, v142, v143\n\t" \
  "global_store_dwordx4 v62, v[108:111], %[ot] offset:1024 sc1\n\t" \
  "v_mfma_f32_16x16x32_bf16 v[156:159], v[36:39], v[92:95], 0\n\t" \
  "v_cvt_pk_bf16_f32 v116, v144, v145\n\t" \
  "v_cvt_pk_bf16_f32 v117, v146, v147\n\t" \
  "v_cvt_pk_bf16_f32 v118, v148, v149\n\t" \
  "v_cvt_pk_bf16_f32 v119, v150, v151\n\t" \
  "global_store_dwordx4 v62, v[116:119], %[ou] offset:0 sc1\n\t" \
  "v_cvt_pk_bf16_f32 v124, v152, v153\n\t" \
  "v_cvt_pk_bf16_f32 v125, v154, v155\n\t" \
  "s_nop 0\n\t" \
  "v_cvt_pk_bf16_f32 v126, v156, v157\n\t" \
  "v_cvt_pk_bf16_f32 v127, v158, v159\n\t" \
  "global_store_dwordx4 v62, v[124:127], %[ou] offset:1024 sc1\n\t" \
  "s_branch AE_%=\n\t" \
  "AR_%=:\n\t" \
  "s_addk_i32 %[it], 1\n\t" \
  "s_cmp_gt_u32 %[it], 0x400000\n\t" \
  "s_cbranch_scc1 AY_%=\n\t" \
  "ds_read_b32 v0, v61\n\t" \
  "ds_read_b64 v[20:21], v60\n\t" \
  "ds_read_b64 v[30:31], v60 offset:8\n\t" \
  "s_mov_b64 exec, %[rm]\n\t" \
  "ds_read_b128 v[160:163], v63\n\t" \
  "s_mov_b64 exec, -1\n\t" \
  "s_branch AW_%=\n\t" \
  "AZ_%=:\n\t" \
  "v_mov_b32 v0, 0xff61b1e6\n\t" \
  "v_mov_b32 v1, 0xcfa8965f\n\t" \
  "s_nop 3\n\t" \
  "v_cndmask_b32_e64 v56, v0, v1, s[68:69]\n\t" \
  "v_cndmask_b32_e64 v57, v0, v1, s[70:71]\n\t" \
  "v_cndmask_b32_e64 v58, v0, v1, s[72:73]\n\t" \
  "v_cndmask_b32_e64 v59, v0, v1, s[74:75]\n\t" \
  "s_nop 1\n\t" \
  "s_branch AY_%=\n\t" \
  "AE_%=:\n\t" \
  "s_nop 1\n\t"
#define N2_CLOB "v0", "v1", "v2", "v3", "v4", "v5", "v6", "v7", "v8", "v9", "v10", "v11", "v12", "v13", "v14", "v15", "v16", "v17", "v18", "v19", "v20", "v21", "v22", "v23", "v24", "v25", "v26", "v27", "v28", "v29", "v30", "v31", "v32", "v33", "v34", "v35", "v36", "v37", "v38", "v39", "v40", "v41", "v42", "v43", "v44", "v45", "v46", "v47", "v48", "v49", "v50", "v51", "v52", "v53", "v54", "v55", "v60", "v61", "v64", "v65", "v66", "v67", "v68", "v69", "v70", "v71", "v72", "v73", "v74", "v75", "v76", "v77", "v78", "v79", "v80", "v81", "v82", "v83", "v84", "v85", "v86", "v87", "v88", "v89", "v90", "v91", "v92", "v93", "v94", "v95", "v96", "v97", "v98", "v99", "v100", "v101", "v102", "v103", "v104", "v105", "v106", "v107", "v108", "v109", "v110", "v111", "v112", "v113", "v114", "v115", "v116", "v117", "v118", "v119", "v120", "v121", "v122", "v123", "v124", "v125", "v126", "v127", "v128", "v129", "v130", "v131", "v132", "v133", "v134", "v135", "v136", "v137", "v138", "v139", "v140", "v141", "v142", "v143", "v144", "v145", "v146", "v147", "v148", "v149", "v150", "v151", "v152", "v153", "v154", "v155", "v156", "v157", "v158", "v159", "v160", "v161", "v162", "v163", "v164", "v165", "v166", "v167", "s68", "s69", "s70", "s71", "s72", "s73", "s74", "s75", "s76", "s77", "s78", "s79", "s80", "s81", "s82", "s83", "s84", "s85"

#define CV_ASM \
  "ds_read_b64 v[0:1], v28 offset:0\n\t" \
  "ds_read_b64 v[2:3], v29 offset:0\n\t" \
  "ds_read_b64 v[4:5], v30 offset:1024\n\t" \
  "ds_read_b64 v[6:7], v31 offset:1024\n\t" \
  "ds_read_b64 v[8:9], v24 offset:2048\n\t" \
  "ds_read_b64 v[10:11], v25 offset:2048\n\t" \
  "ds_read_b64 v[12:13], v26 offset:3072\n\t" \
  "ds_read_b64 v[14:15], v27 offset:3072\n\t" \
  "ds_read_b64 v[16:17], v28 offset:4096\n\t" \
  "ds_read_b64 v[18:19], v29 offset:4096\n\t" \
  "ds_read_b64 v[20:21], v30 offset:5120\n\t" \
  "ds_read_b64 v[22:23], v31 offset:5120\n\t" \
  "s_waitcnt lgkmcnt(10)\n\t" \
  "v_mfma_f32_16x16x32_bf16 v[128:131], v[68:71], v[0:3], 0\n\t" \
  "v_mfma_f32_16x16x32_bf16 v[132:135], v[76:79], v[0:3], 0\n\t" \
  "v_mfma_f32_16x16x32_bf16 v[136:139], v[84:87], v[0:3], 0\n\t" \
  "v_mfma_f32_16x16x32_bf16 v[140:143], v[92:95], v[0:3], 0\n\t" \
  "v_mfma_f32_16x16x32_bf16 v[144:147], v[100:103], v[0:3], 0\n\t" \
  "v_mfma_f32_16x16x32_bf16 v[148:151], v[108:111], v[0:3], 0\n\t" \
  "v_mfma_f32_16x16x32_bf16 v[152:155], v[116:119], v[0:3], 0\n\t" \
  "v_mfma_f32_16x16x32_bf16 v[156:159], v[124:127], v[0:3], 0\n\t" \
  "ds_read_b64 v[0:1], v24 offset:6144\n\t" \
  "s_waitcnt lgkmcnt(9)\n\t" \
  "v_mfma_f32_16x16x32_bf16 v[128:131], v[64:67], v[4:7], v[128:131]\n\t" \
  "ds_read_b64 v[2:3], v25 offset:6144\n\t" \
  "v_mfma_f32_16x16x32_bf16 v[132:135], v[72:75], v[4:7], v[132:135]\n\t" \
  "v_mfma_f32_16x16x32_bf16 v[136:139], v[80:83], v[4:7], v[136:139]\n\t" \
  "v_mfma_f32_16x16x32_bf16 v[140:143], v[88:91], v[4:7], v[140:143]\n\t" \
  "v_mfma_f32_16x16x32_bf16 v[144:147], v[96:99], v[4:7], v[144:147]\n\t" \
  "v_mfma_f32_16x16x32_bf16 v[148:151], v[104:107], v[4:7], v[148:151]\n\t" \
  "v_mfma_f32_16x16x32_bf16 v[152:155], v[112:115], v[4:7], v[152:155]\n\t" \
  "v_mfma_f32_16x16x32_bf16 v[156:159], v[120:123], v[4:7], v[156:159]\n\t" \
  "ds_read_b64 v[4:5], v26 offset:7168\n\t" \
  "ds_bpermute_b32 v34, v32, v131\n\t" \
  "v_readlane_b32 %[p0], v131, 63\n\t" \
  "s_waitcnt lgkmcnt(10)\n\t" \
  "v_mfma_f32_16x16x32_bf16 v[132:135], v[68:71], v[8:11], v[132:135]\n\t" \
  "ds_read_b64 v[6:7], v27 offset:7168\n\t" \
  "v_mov_b32 v131, v130\n\t" \
  "v_mfma_f32_16x16x32_bf16 v[136:139], v[76:79], v[8:11], v[136:139]\n\t" \
  "v_mov_b32 v130, v129\n\t" \
  "v_mfma_f32_16x16x32_bf16 v[140:143], v[84:87], v[8:11], v[140:143]\n\t" \
  "v_mov_b32 v129, v128\n\t" \
  "v_mfma_f32_16x16x32_bf16 v[144:147], v[92:95], v[8:11], v[144:147]\n\t" \
  "v_mfma_f32_16x16x32_bf16 v[148:151], v[100:103], v[8:11], v[148:151]\n\t" \
  "v_mfma_f32_16x16x32_bf16 v[152:155], v[108:111], v[8:11], v[152:155]\n\t" \
  "v_mfma_f32_16x16x32_bf16 v[156:159], v[116:119], v[8:11], v[156:159]\n\t" \
  "ds_read_b64 v[8:9], v28 offset:8192\n\t" \
  "s_waitcnt lgkmcnt(10)\n\t" \
  "v_mfma_f32_16x16x32_bf16 v[132:135], v[64:67], v[12:15], v[132:135]\n\t" \
  "ds_read_b64 v[10:11], v29 offset:8192\n\t" \
  "v_mfma_f32_16x16x32_bf16 v[136:139], v[72:75], v[12:15], v[136:139]\n\t" \
  "v_mfma_f32_16x16x32_bf16 v[140:143], v[80:83], v[12:15], v[140:143]\n\t" \
  "v_mfma_f32_16x16x32_bf16 v[144:147], v[88:91], v[12:15], v[144:147]\n\t" \
  "v_mfma_f32_16x16x32_bf16 v[148:151], v[96:99], v[12:15], v[148:151]\n\t" \
  "s_waitcnt lgkmcnt(3)\n\t" \
  "v_mov_b32 v128, v34\n\t" \
  "v_writelane_b32 v128, 0, 0\n\t" \
  "v_mfma_f32_16x16x32_bf16 v[152:155], v[104:107], v[12:15], v[152:155]\n\t" \
  "v_mfma_f32_16x16x32_bf16 v[156:159], v[112:115], v[12:15], v[156:159]\n\t" \
  "ds_read_b64 v[12:13], v30 offset:9216\n\t" \
  "s_waitcnt lgkmcnt(10)\n\t" \
  "v_mfma_f32_16x16x32_bf16 v[136:139], v[68:71], v[16:19], v[136:139]\n\t" \
  "ds_read_b64 v[14:15], v31 offset:9216\n\t" \
  "ds_bpermute_b32 v35, v32, v135\n\t" \
  "v_readlane_b32 %[p1], v135, 63\n\t" \
  "v_mfma_f32_16x16x32_bf16 v[140:143], v[76:79], v[16:19], v[140:143]\n\t" \
  "v_mov_b32 v135, v134\n\t" \
  "v_mfma_f32_16x16x32_bf16 v[144:147], v[84:87], v[16:19], v[144:147]\n\t" \
  "v_mov_b32 v134, v133\n\t" \
  "v_mfma_f32_16x16x32_bf16 v[148:151], v[92:95], v[16:19], v[148:151]\n\t" \
  "v_mov_b32 v133, v132\n\t" \
  "v_mfma_f32_16x16x32_bf16 v[152:155], v[100:103], v[16:19], v[152:155]\n\t" \
  "v_mfma_f32_16x16x32_bf16 v[156:159], v[108:111], v[16:19], v[156:159]\n\t" \
  "ds_read_b64 v[16:17], v24 offset:10240\n\t" \
  "s_waitcnt lgkmcnt(11)\n\t" \
  "v_mfma_f32_16x16x32_bf16 v[136:139], v[64:67], v[20:23], v[136:139]\n\t" \
  "ds_read_b64 v[18:19], v25 offset:10240\n\t" \
  "v_mfma_f32_16x16x32_bf16 v[140:143], v[72:75], v[20:23], v[140:143]\n\t" \
  "v_mfma_f32_16x16x32_bf16 v[144:147], v[80:83], v[20:23], v[144:147]\n\t" \
  "v_mfma_f32_16x16x32_bf16 v[148:151], v[88:91], v[20:23], v[148:151]\n\t" \
  "v_mfma_f32_16x16x32_bf16 v[152:155], v[96:99], v[20:23], v[152:155]\n\t" \
  "v_mfma_f32_16x16x32_bf16 v[156:159], v[104:107], v[20:23], v[156:159]\n\t" \
  "ds_read_b64 v[20:21], v26 offset:11264\n\t" \
  "s_waitcnt lgkmcnt(11)\n\t" \
  "v_mfma_f32_16x16x32_bf16 v[140:143], v[68:71], v[0:3], v[140:143]\n\t" \
  "ds_read_b64 v[22:23], v27 offset:11264\n\t" \
  "s_waitcnt lgkmcnt(4)\n\t" \
  "v_mov_b32 v132, v35\n\t" \
  "v_writelane_b32 v132, %[p0], 0\n\t" \
  "v_mfma_f32_16x16x32_bf16 v[144:147], v[76:79], v[0:3], v[144:147]\n\t" \
  "ds_bpermute_b32 v36, v32, v139\n\t" \
  "v_readlane_b32 %[p2], v139, 63\n\t" \
  "v_mfma_f32_16x16x32_bf16 v[148:151], v[84:87], v[0:3], v[148:151]\n\t" \
  "v_mov_b32 v139, v138\n\t" \
  "v_mfma_f32_16x16x32_bf16 v[152:155], v[92:95], v[0:3], v[152:155]\n\t" \
  "v_mov_b32 v138, v137\n\t" \
  "v_mfma_f32_16x16x32_bf16 v[156:159], v[100:103], v[0:3], v[156:159]\n\t" \
  "ds_read_b64 v[0:1], v28 offset:12288\n\t" \
  "v_mov_b32 v137, v136\n\t" \
  "s_waitcnt lgkmcnt(11)\n\t" \
  "v_mfma_f32_16x16x32_bf16 v[140:143], v[64:67], v[4:7], v[140:143]\n\t" \
  "ds_read_b64 v[2:3], v29 offset:12288\n\t" \
  "v_mfma_f32_16x16x32_bf16 v[144:147], v[72:75], v[4:7], v[144:147]\n\t" \
  "v_mfma_f32_16x16x32_bf16 v[148:151], v[80:83], v[4:7], v[148:151]\n\t" \
  "v_mfma_f32_16x16x32_bf16 v[152:155], v[88:91], v[4:7], v[152:155]\n\t" \
  "v_mfma_f32_16x16x32_bf16 v[156:159], v[96:99], v[4:7], v[156:159]\n\t" \
  "ds_read_b64 v[4:5], v30 offset:13312\n\t" \
  "s_waitcnt lgkmcnt(11)\n\t" \
  "v_mfma_f32_16x16x32_bf16 v[144:147], v[68:71], v[8:11], v[144:147]\n\t" \
  "ds_read_b64 v[6:7], v31 offset:13312\n\t" \
  "v_mfma_f32_16x16x32_bf16 v[148:151], v[76:79], v[8:11], v[148:151]\n\t" \
  "v_mfma_f32_16x16x32_bf16 v[152:155], v[84:87], v[8:11], v[152:155]\n\t" \
  "ds_bpermute_b32 v37, v32, v143\n\t" \
  "v_readlane_b32 %[p3], v143, 63\n\t" \
  "v_mfma_f32_16x16x32_bf16 v[156:159], v[92:95], v[8:11], v[156:159]\n\t" \
  "ds_read_b64 v[8:9], v24 offset:14336\n\t" \
  "s_waitcnt lgkmcnt(6)\n\t" \
  "v_mov_b32 v136, v36\n\t" \
  "v_writelane_b32 v136, %[p1], 0\n\t" \
  "v_mov_b32 v143, v142\n\t" \
  "s_waitcnt lgkmcnt(12)\n\t" \
  "v_mfma_f32_16x16x32_bf16 v[144:147], v[64:67], v[12:15], v[144:147]\n\t" \
  "ds_read_b64 v[10:11], v25 offset:14336\n\t" \
  "v_mov_b32 v142, v141\n\t" \
  "v_mfma_f32_16x16x32_bf16 v[148:151], v[72:75], v[12:15], v[148:151]\n\t" \
  "v_mov_b32 v141, v140\n\t" \
  "v_mfma_f32_16x16x32_bf16 v[152:155], v[80:83], v[12:15], v[152:155]\n\t" \
  "v_mfma_f32_16x16x32_bf16 v[156:159], v[88:91], v[12:15], v[156:159]\n\t" \
  "ds_read_b64 v[12:13], v26 offset:15360\n\t" \
  "s_waitcnt lgkmcnt(11)\n\t" \
  "v_mfma_f32_16x16x32_bf16 v[148:151], v[68:71], v[16:19], v[148:151]\n\t" \
  "ds_read_b64 v[14:15], v27 offset:15360\n\t" \
  "v_mfma_f32_16x16x32_bf16 v[152:155], v[76:79], v[16:19], v[152:155]\n\t" \
  "v_mfma_f32_16x16x32_bf16 v[156:159], v[84:87], v[16:19], v[156:159]\n\t" \
  "ds_read_b64 v[16:17], v26 offset:0\n\t" \
  "s_waitcnt lgkmcnt(11)\n\t" \
  "v_mfma_f32_16x16x32_bf16 v[148:151], v[64:67], v[20:23], v[148:151]\n\t" \
  "ds_read_b64 v[18:19], v27 offset:0\n\t" \
  "ds_bpermute_b32 v38, v32, v147\n\t" \
  "v_readlane_b32 %[p4], v147, 63\n\t" \
  "v_mfma_f32_16x16x32_bf16 v[152:155], v[72:75], v[20:23], v[152:155]\n\t" \
  "v_mov_b32 v147, v146\n\t" \
  "v_mfma_f32_16x16x32_bf16 v[156:159], v[80:83], v[20:23], v[156:159]\n\t" \
  "ds_read_b64 v[20:21], v28 offset:1024\n\t" \
  "v_mov_b32 v146, v145\n\t" \
  "s_waitcnt lgkmcnt(11)\n\t" \
  "v_mfma_f32_16x16x32_bf16 v[152:155], v[68:71], v[0:3], v[152:155]\n\t" \
  "ds_read_b64 v[22:23], v29 offset:1024\n\t" \
  "s_waitcnt lgkmcnt(9)\n\t" \
  "v_mov_b32 v140, v37\n\t" \
  "v_writelane_b32 v140, %[p2], 0\n\t" \
  "v_mov_b32 v145, v144\n\t" \
  "v_mfma_f32_16x16x32_bf16 v[156:159], v[76:79], v[0:3], v[156:159]\n\t" \
  "ds_read_b64 v[0:1], v30 offset:2048\n\t" \
  "ds_bpermute_b32 v39, v32, v151\n\t" \
  "v_readlane_b32 %[p5], v151, 63\n\t" \
  "s_waitcnt lgkmcnt(12)\n\t" \
  "v_mfma_f32_16x16x32_bf16 v[152:155], v[64:67], v[4:7], v[152:155]\n\t" \
  "ds_read_b64 v[2:3], v31 offset:2048\n\t" \
  "v_mov_b32 v151, v150\n\t" \
  "v_mfma_f32_16x16x32_bf16 v[156:159], v[72:75], v[4:7], v[156:159]\n\t" \
  "ds_read_b64 v[4:5], v24 offset:3072\n\t" \
  "v_mov_b32 v150, v149\n\t" \
  "s_waitcnt lgkmcnt(11)\n\t" \
  "v_mfma_f32_16x16x32_bf16 v[156:159], v[68:71], v[8:11], v[156:159]\n\t" \
  "ds_read_b64 v[6:7], v25 offset:3072\n\t" \
  "ds_read_b64 v[8:9], v26 offset:4096\n\t" \
  "v_mov_b32 v149, v148\n\t" \
  "ds_bpermute_b32 v40, v32, v155\n\t" \
  "v_readlane_b32 %[p6], v155, 63\n\t" \
  "s_waitcnt lgkmcnt(12)\n\t" \
  "v_mfma_f32_16x16x32_bf16 v[156:159], v[64:67], v[12:15], v[156:159]\n\t" \
  "ds_read_b64 v[10:11], v27 offset:4096\n\t" \
  "ds_read_b64 v[12:13], v28 offset:5120\n\t" \
  "v_mov_b32 v155, v154\n\t" \
  "s_waitcnt lgkmcnt(12)\n\t" \
  "v_mfma_f32_16x16x32_bf16 v[128:131], v[68:71], v[16:19], v[128:131]\n\t" \
  "ds_read_b64 v[14:15], v29 offset:5120\n\t" \
  "v_mov_b32 v154, v153\n\t" \
  "v_mfma_f32_16x16x32_bf16 v[132:135], v[76:79], v[16:19], v[132:135]\n\t" \
  "s_waitcnt lgkmcnt(12)\n\t" \
  "v_mov_b32 v144, v38\n\t" \
  "v_writelane_b32 v144, %[p3], 0\n\t" \
  "v_mov_b32 v153, v152\n\t" \
  "v_mfma_f32_16x16x32_bf16 v[136:139], v[84:87], v[16:19], v[136:139]\n\t" \
  "s_waitcnt lgkmcnt(8)\n\t" \
  "v_mov_b32 v148, v39\n\t" \
  "v_writelane_b32 v148, %[p4], 0\n\t" \
  "v_mfma_f32_16x16x32_bf16 v[140:143], v[92:95], v[16:19], v[140:143]\n\t" \
  "s_waitcnt lgkmcnt(3)\n\t" \
  "v_mov_b32 v152, v40\n\t" \
  "v_writelane_b32 v152, %[p5], 0\n\t" \
  "v_mfma_f32_16x16x32_bf16 v[144:147], v[100:103], v[16:19], v[144:147]\n\t" \
  "ds_bpermute_b32 v41, v32, v159\n\t" \
  "v_mfma_f32_16x16x32_bf16 v[148:151], v[108:111], v[16:19], v[148:151]\n\t" \
  "v_mov_b32 v159, v158\n\t" \
  "v_mfma_f32_16x16x32_bf16 v[152:155], v[116:119], v[16:19], v[152:155]\n\t" \
  "v_mov_b32 v158, v157\n\t" \
  "s_waitcnt lgkmcnt(11)\n\t" \
  "v_mfma_f32_16x16x32_bf16 v[128:131], v[64:67], v[20:23], v[128:131]\n\t" \
  "v_mov_b32 v157, v156\n\t" \
  "v_mfma_f32_16x16x32_bf16 v[132:135], v[72:75], v[20:23], v[132:135]\n\t" \
  "v_mfma_f32_16x16x32_bf16 v[136:139], v[80:83], v[20:23], v[136:139]\n\t" \
  "v_mfma_f32_16x16x32_bf16 v[140:143], v[88:91], v[20:23], v[140:143]\n\t" \
  "v_mfma_f32_16x16x32_bf16 v[144:147], v[96:99], v[20:23], v[144:147]\n\t" \
  "s_waitcnt lgkmcnt(0)\n\t" \
  "v_mov_b32 v156, v41\n\t" \
  "v_writelane_b32 v156, %[p6], 0\n\t" \
  "v_mfma_f32_16x16x32_bf16 v[148:151], v[104:107], v[20:23], v[148:151]\n\t" \
  "v_mfma_f32_16x16x32_bf16 v[152:155], v[112:115], v[20:23], v[152:155]\n\t" \
  "v_mfma_f32_16x16x32_bf16 v[156:159], v[124:127], v[16:19], v[156:159]\n\t" \
  "ds_read_b64 v[16:17], v30 offset:6144\n\t" \
  "ds_bpermute_b32 v34, v32, v131\n\t" \
  "v_readlane_b32 %[p0], v131, 63\n\t" \
  "v_mfma_f32_16x16x32_bf16 v[156:159], v[120:123], v[20:23], v[156:159]\n\t" \
  "ds_read_b64 v[18:19], v31 offset:6144\n\t" \
  "ds_read_b64 v[20:21], v24 offset:7168\n\t" \
  "v_mov_b32 v131, v130\n\t" \
  "s_waitcnt lgkmcnt(12)\n\t" \
  "v_mfma_f32_16x16x32_bf16 v[132:135], v[68:71], v[0:3], v[132:135]\n\t" \
  "ds_read_b64 v[22:23], v25 offset:7168\n\t" \
  "v_mov_b32 v130, v129\n\t" \
  "v_mfma_f32_16x16x32_bf16 v[136:139], v[76:79], v[0:3], v[136:139]\n\t" \
  "v_mov_b32 v129, v128\n\t" \
  "v_mfma_f32_16x16x32_bf16 v[140:143], v[84:87], v[0:3], v[140:143]\n\t" \
  "v_mfma_f32_16x16x32_bf16 v[144:147], v[92:95], v[0:3], v[144:147]\n\t" \
  "v_mfma_f32_16x16x32_bf16 v[148:151], v[100:103], v[0:3], v[148:151]\n\t" \
  "v_mfma_f32_16x16x32_bf16 v[152:155], v[108:111], v[0:3], v[152:155]\n\t" \
  "v_mfma_f32_16x16x32_bf16 v[156:159], v[116:119], v[0:3], v[156:159]\n\t" \
  "ds_read_b64 v[0:1], v26 offset:8192\n\t" \
  "s_waitcnt lgkmcnt(12)\n\t" \
  "v_mfma_f32_16x16x32_bf16 v[132:135], v[64:67], v[4:7], v[132:135]\n\t" \
  "ds_read_b64 v[2:3], v27 offset:8192\n\t" \
  "v_mfma_f32_16x16x32_bf16 v[136:139], v[72:75], v[4:7], v[136:139]\n\t" \
  "v_mfma_f32_16x16x32_bf16 v[140:143], v[80:83], v[4:7], v[140:143]\n\t" \
  "v_mfma_f32_16x16x32_bf16 v[144:147], v[88:91], v[4:7], v[144:147]\n\t" \
  "s_waitcnt lgkmcnt(5)\n\t" \
  "v_mov_b32 v128, v34\n\t" \
  "v_writelane_b32 v128, 0, 0\n\t" \
  "v_mfma_f32_16x16x32_bf16 v[148:151], v[96:99], v[4:7], v[148:151]\n\t" \
  "v_mfma_f32_16x16x32_bf16 v[152:155], v[104:107], v[4:7], v[152:155]\n\t" \
  "v_mfma_f32_16x16x32_bf16 v[156:159], v[112:115], v[4:7], v[156:159]\n\t" \
  "ds_read_b64 v[4:5], v28 offset:9216\n\t" \
  "s_waitcnt lgkmcnt(11)\n\t" \
  "v_mfma_f32_16x16x32_bf16 v[136:139], v[68:71], v[8:11], v[136:139]\n\t" \
  "ds_read_b64 v[6:7], v29 offset:9216\n\t" \
  "ds_bpermute_b32 v35, v32, v135\n\t" \
  "v_readlane_b32 %[p1], v135, 63\n\t" \
  "v_mfma_f32_16x16x32_bf16 v[140:143], v[76:79], v[8:11], v[140:143]\n\t" \
  "v_mov_b32 v135, v134\n\t" \
  "v_mfma_f32_16x16x32_bf16 v[144:147], v[84:87], v[8:11], v[144:147]\n\t" \
  "v_mov_b32 v134, v133\n\t" \
  "v_mfma_f32_16x16x32_bf16 v[148:151], v[92:95], v[8:11], v[148:151]\n\t" \
  "v_mov_b32 v133, v132\n\t" \
  "v_mfma_f32_16x16x32_bf16 v[152:155], v[100:103], v[8:11], v[152:155]\n\t" \
  "v_mfma_f32_16x16x32_bf16 v[156:159], v[108:111], v[8:11], v[156:159]\n\t" \
  "ds_read_b64 v[8:9], v30 offset:10240\n\t" \
  "s_waitcnt lgkmcnt(12)\n\t" \
  "v_mfma_f32_16x16x32_bf16 v[136:139], v[64:67], v[12:15], v[136:139]\n\t" \
  "ds_read_b64 v[10:11], v31 offset:10240\n\t" \
  "v_mfma_f32_16x16x32_bf16 v[140:143], v[72:75], v[12:15], v[140:143]\n\t" \
  "v_mfma_f32_16x16x32_bf16 v[144:147], v[80:83], v[12:15], v[144:147]\n\t" \
  "v_mfma_f32_16x16x32_bf16 v[148:151], v[88:91], v[12:15], v[148:151]\n\t" \
  "v_mfma_f32_16x16x32_bf16 v[152:155], v[96:99], v[12:15], v[152:155]\n\t" \
  "v_mfma_f32_16x16x32_bf16 v[156:159], v[104:107], v[12:15], v[156:159]\n\t" \
  "ds_read_b64 v[12:13], v24 offset:11264\n\t" \
  "s_waitcnt lgkmcnt(10)\n\t" \
  "v_mfma_f32_16x16x32_bf16 v[140:143], v[68:71], v[16:19], v[140:143]\n\t" \
  "ds_read_b64 v[14:15], v25 offset:11264\n\t" \
  "s_waitcnt lgkmcnt(4)\n\t" \
  "v_mov_b32 v132, v35\n\t" \
  "v_writelane_b32 v132, %[p0], 0\n\t" \
  "v_mfma_f32_16x16x32_bf16 v[144:147], v[76:79], v[16:19], v[144:147]\n\t" \
  "ds_bpermute_b32 v36, v32, v139\n\t" \
  "v_readlane_b32 %[p2], v139, 63\n\t" \
  "v_mfma_f32_16x16x32_bf16 v[148:151], v[84:87], v[16:19], v[148:151]\n\t" \
  "v_mov_b32 v139, v138\n\t" \
  "v_mfma_f32_16x16x32_bf16 v[152:155], v[92:95], v[16:19], v[152:155]\n\t" \
  "v_mov_b32 v138, v137\n\t" \
  "v_mfma_f32_16x16x32_bf16 v[156:159], v[100:103], v[16:19], v[156:159]\n\t" \
  "ds_read_b64 v[16:17], v26 offset:12288\n\t" \
  "v_mov_b32 v137, v136\n\t" \
  "s_waitcnt lgkmcnt(11)\n\t" \
  "v_mfma_f32_16x16x32_bf16 v[140:143], v[64:67], v[20:23], v[140:143]\n\t" \
  "ds_read_b64 v[18:19], v27 offset:12288\n\t" \
  "v_mfma_f32_16x16x32_bf16 v[144:147], v[72:75], v[20:23], v[144:147]\n\t" \
  "v_mfma_f32_16x16x32_bf16 v[148:151], v[80:83], v[20:23], v[148:151]\n\t" \
  "v_mfma_f32_16x16x32_bf16 v[152:155], v[88:91], v[20:23], v[152:155]\n\t" \
  "v_mfma_f32_16x16x32_bf16 v[156:159], v[96:99], v[20:23], v[156:159]\n\t" \
  "ds_read_b64 v[20:21], v28 offset:13312\n\t" \
  "s_waitcnt lgkmcnt(11)\n\t" \
  "v_mfma_f32_16x16x32_bf16 v[144:147], v[68:71], v[0:3], v[144:147]\n\t" \
  "ds_read_b64 v[22:23], v29 offset:13312\n\t" \
  "v_mfma_f32_16x16x32_bf16 v[148:151], v[76:79], v[0:3], v[148:151]\n\t" \
  "v_mfma_f32_16x16x32_bf16 v[152:155], v[84:87], v[0:3], v[152:155]\n\t" \
  "ds_bpermute_b32 v37, v32, v143\n\t" \
  "v_readlane_b32 %[p3], v143, 63\n\t" \
  "v_mfma_f32_16x16x32_bf16 v[156:159], v[92:95], v[0:3], v[156:159]\n\t" \
  "ds_read_b64 v[0:1], v30 offset:14336\n\t" \
  "s_waitcnt lgkmcnt(6)\n\t" \
  "v_mov_b32 v136, v36\n\t" \
  "v_writelane_b32 v136, %[p1], 0\n\t" \
  "v_mov_b32 v143, v142\n\t" \
  "s_waitcnt lgkmcnt(12)\n\t" \
  "v_mfma_f32_16x16x32_bf16 v[144:147], v[64:67], v[4:7], v[144:147]\n\t" \
  "ds_read_b64 v[2:3], v31 offset:14336\n\t" \
  "v_mov_b32 v142, v141\n\t" \
  "v_mfma_f32_16x16x32_bf16 v[148:151], v[72:75], v[4:7], v[148:151]\n\t" \
  "v_mov_b32 v141, v140\n\t" \
  "v_mfma_f32_16x16x32_bf16 v[152:155], v[80:83], v[4:7], v[152:155]\n\t" \
  "v_mfma_f32_16x16x32_bf16 v[156:159], v[88:91], v[4:7], v[156:159]\n\t" \
  "ds_read_b64 v[4:5], v24 offset:15360\n\t" \
  "s_waitcnt lgkmcnt(11)\n\t" \
  "v_mfma_f32_16x16x32_bf16 v[148:151], v[68:71], v[8:11], v[148:151]\n\t" \
  "ds_read_b64 v[6:7], v25 offset:15360\n\t" \
  "v_mfma_f32_16x16x32_bf16 v[152:155], v[76:79], v[8:11], v[152:155]\n\t" \
  "v_mfma_f32_16x16x32_bf16 v[156:159], v[84:87], v[8:11], v[156:159]\n\t" \
  "ds_read_b64 v[8:9], v24 offset:0\n\t" \
  "s_waitcnt lgkmcnt(11)\n\t" \
  "v_mfma_f32_16x16x32_bf16 v[148:151], v[64:67], v[12:15], v[148:151]\n\t" \
  "ds_read_b64 v[10:11], v25 offset:0\n\t" \
  "ds_bpermute_b32 v38, v32, v147\n\t" \
  "v_readlane_b32 %[p4], v147, 63\n\t" \
  "v_mfma_f32_16x16x32_bf16 v[152:155], v[72:75], v[12:15], v[152:155]\n\t" \
  "v_mov_b32 v147, v146\n\t" \
  "v_mfma_f32_16x16x32_bf16 v[156:159], v[80:83], v[12:15], v[156:159]\n\t" \
  "ds_read_b64 v[12:13], v26 offset:1024\n\t" \
  "v_mov_b32 v146, v145\n\t" \
  "s_waitcnt lgkmcnt(11)\n\t" \
  "v_mfma_f32_16x16x32_bf16 v[152:155], v[68:71], v[16:19], v[152:155]\n\t" \
  "ds_read_b64 v[14:15], v27 offset:1024\n\t" \
  "s_waitcnt lgkmcnt(9)\n\t" \
  "v_mov_b32 v140, v37\n\t" \
  "v_writelane_b32 v140, %[p2], 0\n\t" \
  "v_mov_b32 v145, v144\n\t" \
  "v_mfma_f32_16x16x32_bf16 v[156:159], v[76:79], v[16:19], v[156:159]\n\t" \
  "ds_read_b64 v[16:17], v28 offset:2048\n\t" \
  "ds_bpermute_b32 v39, v32, v151\n\t" \
  "v_readlane_b32 %[p5], v151, 63\n\t" \
  "s_waitcnt lgkmcnt(12)\n\t" \
  "v_mfma_f32_16x16x32_bf16 v[152:155], v[64:67], v[20:23], v[152:155]\n\t" \
  "ds_read_b64 v[18:19], v29 offset:2048\n\t" \
  "v_mov_b32 v151, v150\n\t" \
  "v_mfma_f32_16x16x32_bf16 v[156:159], v[72:75], v[20:23], v[156:159]\n\t" \
  "ds_read_b64 v[20:21], v30 offset:3072\n\t" \
  "v_mov_b32 v150, v149\n\t" \
  "s_waitcnt lgkmcnt(11)\n\t" \
  "v_mfma_f32_16x16x32_bf16 v[156:159], v[68:71], v[0:3], v[156:159]\n\t" \
  "ds_read_b64 v[22:23], v31 offset:3072\n\t" \
  "ds_read_b64 v[0:1], v24 offset:4096\n\t" \
  "v_mov_b32 v149, v148\n\t" \
  "ds_bpermute_b32 v40, v32, v155\n\t" \
  "v_readlane_b32 %[p6], v155, 63\n\t" \
  "s_waitcnt lgkmcnt(12)\n\t" \
  "v_mfma_f32_16x16x32_bf16 v[156:159], v[64:67], v[4:7], v[156:159]\n\t" \
  "ds_read_b64 v[2:3], v25 offset:4096\n\t" \
  "ds_read_b64 v[4:5], v26 offset:5120\n\t" \
  "v_mov_b32 v155, v154\n\t" \
  "s_waitcnt lgkmcnt(12)\n\t" \
  "v_mfma_f32_16x16x32_bf16 v[128:131], v[68:71], v[8:11], v[128:131]\n\t" \
  "ds_read_b64 v[6:7], v27 offset:5120\n\t" \
  "v_mov_b32 v154, v153\n\t" \
  "v_mfma_f32_16x16x32_bf16 v[132:135], v[76:79], v[8:11], v[132:135]\n\t" \
  "s_waitcnt lgkmcnt(12)\n\t" \
  "v_mov_b32 v144, v38\n\t" \
  "v_writelane_b32 v144, %[p3], 0\n\t" \
  "v_mov_b32 v153, v152\n\t" \
  "v_mfma_f32_16x16x32_bf16 v[136:139], v[84:87], v[8:11], v[136:139]\n\t" \
  "s_waitcnt lgkmcnt(8)\n\t" \
  "v_mov_b32 v148, v39\n\t" \
  "v_writelane_b32 v148, %[p4], 0\n\t" \
  "v_mfma_f32_16x16x32_bf16 v[140:143], v[92:95], v[8:11], v[140:143]\n\t" \
  "s_waitcnt lgkmcnt(3)\n\t" \
  "v_mov_b32 v152, v40\n\t" \
  "v_writelane_b32 v152, %[p5], 0\n\t" \
  "v_mfma_f32_16x16x32_bf16 v[144:147], v[100:103], v[8:11], v[144:147]\n\t" \
  "ds_bpermute_b32 v41, v32, v159\n\t" \
  "v_mfma_f32_16x16x32_bf16 v[148:151], v[108:111], v[8:11], v[148:151]\n\t" \
  "v_mov_b32 v159, v158\n\t" \
  "v_mfma_f32_16x16x32_bf16 v[152:155], v[116:119], v[8:11], v[152:155]\n\t" \
  "v_mov_b32 v158, v157\n\t" \
  "s_waitcnt lgkmcnt(11)\n\t" \
  "v_mfma_f32_16x16x32_bf16 v[128:131], v[64:67], v[12:15], v[128:131]\n\t" \
  "v_mov_b32 v157, v156\n\t" \
  "v_mfma_f32_16x16x32_bf16 v[132:135], v[72:75], v[12:15], v[132:135]\n\t" \
  "v_mfma_f32_16x16x32_bf16 v[136:139], v[80:83], v[12:15], v[136:139]\n\t" \
  "v_mfma_f32_16x16x32_bf16 v[140:143], v[88:91], v[12:15], v[140:143]\n\t" \
  "v_mfma_f32_16x16x32_bf16 v[144:147], v[96:99], v[12:15], v[144:147]\n\t" \
  "s_waitcnt lgkmcnt(0)\n\t" \
  "v_mov_b32 v156, v41\n\t" \
  "v_writelane_b32 v156, %[p6], 0\n\t" \
  "v_mfma_f32_16x16x32_bf16 v[148:151], v[104:107], v[12:15], v[148:151]\n\t" \
  "v_mfma_f32_16x16x32_bf16 v[152:155], v[112:115], v[12:15], v[152:155]\n\t" \
  "v_mfma_f32_16x16x32_bf16 v[156:159], v[124:127], v[8:11], v[156:159]\n\t" \
  "ds_read_b64 v[8:9], v28 offset:6144\n\t" \
  "ds_bpermute_b32 v34, v32, v131\n\t" \
  "v_readlane_b32 %[p0], v131, 63\n\t" \
  "v_mfma_f32_16x16x32_bf16 v[156:159], v[120:123], v[12:15], v[156:159]\n\t" \
  "ds_read_b64 v[10:11], v29 offset:6144\n\t" \
  "ds_read_b64 v[12:13], v30 offset:7168\n\t" \
  "v_mov_b32 v131, v130\n\t" \
  "s_waitcnt lgkmcnt(12)\n\t" \
  "v_mfma_f32_16x16x32_bf16 v[132:135], v[68:71], v[16:19], v[132:135]\n\t" \
  "ds_read_b64 v[14:15], v31 offset:7168\n\t" \
  "v_mov_b32 v130, v129\n\t" \
  "v_mfma_f32_16x16x32_bf16 v[136:139], v[76:79], v[16:19], v[136:139]\n\t" \
  "v_mov_b32 v129, v128\n\t" \
  "v_mfma_f32_16x16x32_bf16 v[140:143], v[84:87], v[16:19], v[140:143]\n\t" \
  "v_mfma_f32_16x16x32_bf16 v[144:147], v[92:95], v[16:19], v[144:147]\n\t" \
  "v_mfma_f32_16x16x32_bf16 v[148:151], v[100:103], v[16:19], v[148:151]\n\t" \
  "v_mfma_f32_16x16x32_bf16 v[152:155], v[108:111], v[16:19], v[152:155]\n\t" \
  "v_mfma_f32_16x16x32_bf16 v[156:159], v[116:119], v[16:19], v[156:159]\n\t" \
  "ds_read_b64 v[16:17], v24 offset:8192\n\t" \
  "s_waitcnt lgkmcnt(12)\n\t" \
  "v_mfma_f32_16x16x32_bf16 v[132:135], v[64:67], v[20:23], v[132:135]\n\t" \
  "ds_read_b64 v[18:19], v25 offset:8192\n\t" \
  "v_mfma_f32_16x16x32_bf16 v[136:139], v[72:75], v[20:23], v[136:139]\n\t" \
  "v_mfma_f32_16x16x32_bf16 v[140:143], v[80:83], v[20:23], v[140:143]\n\t" \
  "v_mfma_f32_16x16x32_bf16 v[144:147], v[88:91], v[20:23], v[144:147]\n\t" \
  "s_waitcnt lgkmcnt(5)\n\t" \
  "v_mov_b32 v128, v34\n\t" \
  "v_writelane_b32 v128, 0, 0\n\t" \
  "v_mfma_f32_16x16x32_bf16 v[148:151], v[96:99], v[20:23], v[148:151]\n\t" \
  "v_mfma_f32_16x16x32_bf16 v[152:155], v[104:107], v[20:23], v[152:155]\n\t" \
  "v_mfma_f32_16x16x32_bf16 v[156:159], v[112:115], v[20:23], v[156:159]\n\t" \
  "ds_read_b64 v[20:21], v26 offset:9216\n\t" \
  "s_waitcnt lgkmcnt(11)\n\t" \
  "v_mfma_f32_16x16x32_bf16 v[136:139], v[68:71], v[0:3], v[136:139]\n\t" \
  "ds_read_b64 v[22:23], v27 offset:9216\n\t" \
  "ds_bpermute_b32 v35, v32, v135\n\t" \
  "v_readlane_b32 %[p1], v135, 63\n\t" \
  "v_mfma_f32_16x16x32_bf16 v[140:143], v[76:79], v[0:3], v[140:143]\n\t" \
  "v_mov_b32 v135, v134\n\t" \
  "v_mfma_f32_16x16x32_bf16 v[144:147], v[84:87], v[0:3], v[144:147]\n\t" \
  "v_mov_b32 v134, v133\n\t" \
  "v_mfma_f32_16x16x32_bf16 v[148:151], v[92:95], v[0:3], v[148:151]\n\t" \
  "v_mov_b32 v133, v132\n\t" \
  "v_mfma_f32_16x16x32_bf16 v[152:155], v[100:103], v[0:3], v[152:155]\n\t" \
  "v_mfma_f32_16x16x32_bf16 v[156:159], v[108:111], v[0:3], v[156:159]\n\t" \
  "ds_read_b64 v[0:1], v28 offset:10240\n\t" \
  "s_waitcnt lgkmcnt(12)\n\t" \
  "v_mfma_f32_16x16x32_bf16 v[136:139], v[64:67], v[4:7], v[136:139]\n\t" \
  "ds_read_b64 v[2:3], v29 offset:10240\n\t" \
  "v_mfma_f32_16x16x32_bf16 v[140:143], v[72:75], v[4:7], v[140:143]\n\t" \
  "v_mfma_f32_16x16x32_bf16 v[144:147], v[80:83], v[4:7], v[144:147]\n\t" \
  "v_mfma_f32_16x16x32_bf16 v[148:151], v[88:91], v[4:7], v[148:151]\n\t" \
  "v_mfma_f32_16x16x32_bf16 v[152:155], v[96:99], v[4:7], v[152:155]\n\t" \
  "v_mfma_f32_16x16x32_bf16 v[156:159], v[104:107], v[4:7], v[156:159]\n\t" \
  "ds_read_b64 v[4:5], v30 offset:11264\n\t" \
  "s_waitcnt lgkmcnt(10)\n\t" \
  "v_mfma_f32_16x16x32_bf16 v[140:143], v[68:71], v[8:11], v[140:143]\n\t" \
  "ds_read_b64 v[6:7], v31 offset:11264\n\t" \
  "s_waitcnt lgkmcnt(4)\n\t" \
  "v_mov_b32 v132, v35\n\t" \
  "v_writelane_b32 v132, %[p0], 0\n\t" \
  "v_mfma_f32_16x16x32_bf16 v[144:147], v[76:79], v[8:11], v[144:147]\n\t" \
  "ds_bpermute_b32 v36, v32, v139\n\t" \
  "v_readlane_b32 %[p2], v139, 63\n\t" \
  "v_mfma_f32_16x16x32_bf16 v[148:151], v[84:87], v[8:11], v[148:151]\n\t" \
  "v_mov_b32 v139, v138\n\t" \
  "v_mfma_f32_16x16x32_bf16 v[152:155], v[92:95], v[8:11], v[152:155]\n\t" \
  "v_mov_b32 v138, v137\n\t" \
  "v_mfma_f32_16x16x32_bf16 v[156:159], v[100:103], v[8:11], v[156:159]\n\t" \
  "ds_read_b64 v[8:9], v24 offset:12288\n\t" \
  "v_mov_b32 v137, v136\n\t" \
  "s_waitcnt lgkmcnt(11)\n\t" \
  "v_mfma_f32_16x16x32_bf16 v[140:143], v[64:67], v[12:15], v[140:143]\n\t" \
  "ds_read_b64 v[10:11], v25 offset:12288\n\t" \
  "v_mfma_f32_16x16x32_bf16 v[144:147], v[72:75], v[12:15], v[144:147]\n\t" \
  "v_mfma_f32_16x16x32_bf16 v[148:151], v[80:83], v[12:15], v[148:151]\n\t" \
  "v_mfma_f32_16x16x32_bf16 v[152:155], v[88:91], v[12:15], v[152:155]\n\t" \
  "v_mfma_f32_16x16x32_bf16 v[156:159], v[96:99], v[12:15], v[156:159]\n\t" \
  "ds_read_b64 v[12:13], v26 offset:13312\n\t" \
  "s_waitcnt lgkmcnt(11)\n\t" \
  "v_mfma_f32_16x16x32_bf16 v[144:147], v[68:71], v[16:19], v[144:147]\n\t" \
  "ds_read_b64 v[14:15], v27 offset:13312\n\t" \
  "v_mfma_f32_16x16x32_bf16 v[148:151], v[76:79], v[16:19], v[148:151]\n\t" \
  "v_mfma_f32_16x16x32_bf16 v[152:155], v[84:87], v[16:19], v[152:155]\n\t" \
  "ds_bpermute_b32 v37, v32, v143\n\t" \
  "v_readlane_b32 %[p3], v143, 63\n\t" \
  "v_mfma_f32_16x16x32_bf16 v[156:159], v[92:95], v[16:19], v[156:159]\n\t" \
  "ds_read_b64 v[16:17], v28 offset:14336\n\t" \
  "s_waitcnt lgkmcnt(6)\n\t" \
  "v_mov_b32 v136, v36\n\t" \
  "v_writelane_b32 v136, %[p1], 0\n\t" \
  "v_mov_b32 v143, v142\n\t" \
  "s_waitcnt lgkmcnt(12)\n\t" \
  "v_mfma_f32_16x16x32_bf16 v[144:147], v[64:67], v[20:23], v[144:147]\n\t" \
  "ds_read_b64 v[18:19], v29 offset:14336\n\t" \
  "v_mov_b32 v142, v141\n\t" \
  "v_mfma_f32_16x16x32_bf16 v[148:151], v[72:75], v[20:23], v[148:151]\n\t" \
  "v_mov_b32 v141, v140\n\t" \
  "v_mfma_f32_16x16x32_bf16 v[152:155], v[80:83], v[20:23], v[152:155]\n\t" \
  "v_mfma_f32_16x16x32_bf16 v[156:159], v[88:91], v[20:23], v[156:159]\n\t" \
  "ds_read_b64 v[20:21], v30 offset:15360\n\t" \
  "s_waitcnt lgkmcnt(11)\n\t" \
  "v_mfma_f32_16x16x32_bf16 v[148:151], v[68:71], v[0:3], v[148:151]\n\t" \
  "ds_read_b64 v[22:23], v31 offset:15360\n\t" \
  "v_mfma_f32_16x16x32_bf16 v[152:155], v[76:79], v[0:3], v[152:155]\n\t" \
  "v_mfma_f32_16x16x32_bf16 v[156:159], v[84:87], v[0:3], v[156:159]\n\t" \
  "ds_read_b64 v[0:1], v30 offset:0\n\t" \
  "s_waitcnt lgkmcnt(11)\n\t" \
  "v_mfma_f32_16x16x32_bf16 v[148:151], v[64:67], v[4:7], v[148:151]\n\t" \
  "ds_read_b64 v[2:3], v31 offset:0\n\t" \
  "ds_bpermute_b32 v38, v32, v147\n\t" \
  "v_readlane_b32 %[p4], v147, 63\n\t" \
  "v_mfma_f32_16x16x32_bf16 v[152:155], v[72:75], v[4:7], v[152:155]\n\t" \
  "v_mov_b32 v147, v146\n\t" \
  "v_mfma_f32_16x16x32_bf16 v[156:159], v[80:83], v[4:7], v[156:159]\n\t" \
  "ds_read_b64 v[4:5], v24 offset:1024\n\t" \
  "v_mov_b32 v146, v145\n\t" \
  "s_waitcnt lgkmcnt(11)\n\t" \
  "v_mfma_f32_16x16x32_bf16 v[152:155], v[68:71], v[8:11], v[152:155]\n\t" \
  "ds_read_b64 v[6:7], v25 offset:1024\n\t" \
  "s_waitcnt lgkmcnt(9)\n\t" \
  "v_mov_b32 v140, v37\n\t" \
  "v_writelane_b32 v140, %[p2], 0\n\t" \
  "v_mov_b32 v145, v144\n\t" \
  "v_mfma_f32_16x16x32_bf16 v[156:159], v[76:79], v[8:11], v[156:159]\n\t" \
  "ds_read_b64 v[8:9], v26 offset:2048\n\t" \
  "ds_bpermute_b32 v39, v32, v151\n\t" \
  "v_readlane_b32 %[p5], v151, 63\n\t" \
  "s_waitcnt lgkmcnt(12)\n\t" \
  "v_mfma_f32_16x16x32_bf16 v[152:155], v[64:67], v[12:15], v[152:155]\n\t" \
  "ds_read_b64 v[10:11], v27 offset:2048\n\t" \
  "v_mov_b32 v151, v150\n\t" \
  "v_mfma_f32_16x16x32_bf16 v[156:159], v[72:75], v[12:15], v[156:159]\n\t" \
  "ds_read_b64 v[12:13], v28 offset:3072\n\t" \
  "v_mov_b32 v150, v149\n\t" \
  "s_waitcnt lgkmcnt(11)\n\t" \
  "v_mfma_f32_16x16x32_bf16 v[156:159], v[68:71], v[16:19], v[156:159]\n\t" \
  "ds_read_b64 v[14:15], v29 offset:3072\n\t" \
  "ds_read_b64 v[16:17], v30 offset:4096\n\t" \
  "v_mov_b32 v149, v148\n\t" \
  "ds_bpermute_b32 v40, v32, v155\n\t" \
  "v_readlane_b32 %[p6], v155, 63\n\t" \
  "s_waitcnt lgkmcnt(12)\n\t" \
  "v_mfma_f32_16x16x32_bf16 v[156:159], v[64:67], v[20:23], v[156:159]\n\t" \
  "ds_read_b64 v[18:19], v31 offset:4096\n\t" \
  "ds_read_b64 v[20:21], v24 offset:5120\n\t" \
  "v_mov_b32 v155, v154\n\t" \
  "s_waitcnt lgkmcnt(12)\n\t" \
  "v_mfma_f32_16x16x32_bf16 v[128:131], v[68:71], v[0:3], v[128:131]\n\t" \
  "ds_read_b64 v[22:23], v25 offset:5120\n\t" \
  "v_mov_b32 v154, v153\n\t" \
  "v_mfma_f32_16x16x32_bf16 v[132:135], v[76:79], v[0:3], v[132:135]\n\t" \
  "s_waitcnt lgkmcnt(12)\n\t" \
  "v_mov_b32 v144, v38\n\t" \
  "v_writelane_b32 v144, %[p3], 0\n\t" \
  "v_mov_b32 v153, v152\n\t" \
  "v_mfma_f32_16x16x32_bf16 v[136:139], v[84:87], v[0:3], v[136:139]\n\t" \
  "s_waitcnt lgkmcnt(8)\n\t" \
  "v_mov_b32 v148, v39\n\t" \
  "v_writelane_b32 v148, %[p4], 0\n\t" \
  "v_mfma_f32_16x16x32_bf16 v[140:143], v[92:95], v[0:3], v[140:143]\n\t" \
  "s_waitcnt lgkmcnt(3)\n\t" \
  "v_mov_b32 v152, v40\n\t" \
  "v_writelane_b32 v152, %[p5], 0\n\t" \
  "v_mfma_f32_16x16x32_bf16 v[144:147], v[100:103], v[0:3], v[144:147]\n\t" \
  "ds_bpermute_b32 v41, v32, v159\n\t" \
  "v_mfma_f32_16x16x32_bf16 v[148:151], v[108:111], v[0:3], v[148:151]\n\t" \
  "v_mov_b32 v159, v158\n\t" \
  "v_mfma_f32_16x16x32_bf16 v[152:155], v[116:119], v[0:3], v[152:155]\n\t" \
  "v_mov_b32 v158, v157\n\t" \
  "s_waitcnt lgkmcnt(11)\n\t" \
  "v_mfma_f32_16x16x32_bf16 v[128:131], v[64:67], v[4:7], v[128:131]\n\t" \
  "v_mov_b32 v157, v156\n\t" \
  "v_mfma_f32_16x16x32_bf16 v[132:135], v[72:75], v[4:7], v[132:135]\n\t" \
  "v_mfma_f32_16x16x32_bf16 v[136:139], v[80:83], v[4:7], v[136:139]\n\t" \
  "v_mfma_f32_16x16x32_bf16 v[140:143], v[88:91], v[4:7], v[140:143]\n\t" \
  "v_mfma_f32_16x16x32_bf16 v[144:147], v[96:99], v[4:7], v[144:147]\n\t" \
  "s_waitcnt lgkmcnt(0)\n\t" \
  "v_mov_b32 v156, v41\n\t" \
  "v_writelane_b32 v156, %[p6], 0\n\t" \
  "v_mfma_f32_16x16x32_bf16 v[148:151], v[104:107], v[4:7], v[148:151]\n\t" \
  "v_mfma_f32_16x16x32_bf16 v[152:155], v[112:115], v[4:7], v[152:155]\n\t" \
  "s_mov_b64 exec, 1\n\t" \
  "ds_write_b32 v33, v33\n\t" \
  "s_mov_b64 exec, -1\n\t" \
  "v_mfma_f32_16x16x32_bf16 v[156:159], v[124:127], v[0:3], v[156:159]\n\t" \
  "ds_read_b64 v[0:1], v26 offset:6144\n\t" \
  "v_mfma_f32_16x16x32_bf16 v[156:159], v[120:123], v[4:7], v[156:159]\n\t" \
  "ds_read_b64 v[2:3], v27 offset:6144\n\t" \
  "ds_read_b64 v[4:5], v28 offset:7168\n\t" \
  "s_waitcnt lgkmcnt(12)\n\t" \
  "v_mfma_f32_16x16x32_bf16 v[132:135], v[68:71], v[8:11], v[132:135]\n\t" \
  "ds_read_b64 v[6:7], v29 offset:7168\n\t" \
  "v_mfma_f32_16x16x32_bf16 v[136:139], v[76:79], v[8:11], v[136:139]\n\t" \
  "v_mfma_f32_16x16x32_bf16 v[140:143], v[84:87], v[8:11], v[140:143]\n\t" \
  "v_mfma_f32_16x16x32_bf16 v[144:147], v[92:95], v[8:11], v[144:147]\n\t" \
  "v_mfma_f32_16x16x32_bf16 v[148:151], v[100:103], v[8:11], v[148:151]\n\t" \
  "v_mfma_f32_16x16x32_bf16 v[152:155], v[108:111], v[8:11], v[152:155]\n\t" \
  "v_mfma_f32_16x16x32_bf16 v[156:159], v[116:119], v[8:11], v[156:159]\n\t" \
  "ds_read_b64 v[8:9], v30 offset:8192\n\t" \
  "s_waitcnt lgkmcnt(12)\n\t" \
  "v_mfma_f32_16x16x32_bf16 v[132:135], v[64:67], v[12:15], v[132:135]\n\t" \
  "ds_read_b64 v[10:11], v31 offset:8192\n\t" \
  "v_mfma_f32_16x16x32_bf16 v[136:139], v[72:75], v[12:15], v[136:139]\n\t" \
  "v_mfma_f32_16x16x32_bf16 v[140:143], v[80:83], v[12:15], v[140:143]\n\t" \
  "v_mfma_f32_16x16x32_bf16 v[144:147], v[88:91], v[12:15], v[144:147]\n\t" \
  "v_mfma_f32_16x16x32_bf16 v[148:151], v[96:99], v[12:15], v[148:151]\n\t" \
  "v_mfma_f32_16x16x32_bf16 v[152:155], v[104:107], v[12:15], v[152:155]\n\t" \
  "v_mfma_f32_16x16x32_bf16 v[156:159], v[112:115], v[12:15], v[156:159]\n\t" \
  "ds_read_b64 v[12:13], v24 offset:9216\n\t" \
  "s_waitcnt lgkmcnt(11)\n\t" \
  "v_mfma_f32_16x16x32_bf16 v[136:139], v[68:71], v[16:19], v[136:139]\n\t" \
  "ds_read_b64 v[14:15], v25 offset:9216\n\t" \
  "v_mfma_f32_16x16x32_bf16 v[140:143], v[76:79], v[16:19], v[140:143]\n\t" \
  "v_mfma_f32_16x16x32_bf16 v[144:147], v[84:87], v[16:19], v[144:147]\n\t" \
  "v_mfma_f32_16x16x32_bf16 v[148:151], v[92:95], v[16:19], v[148:151]\n\t" \
  "v_mfma_f32_16x16x32_bf16 v[152:155], v[100:103], v[16:19], v[152:155]\n\t" \
  "v_mfma_f32_16x16x32_bf16 v[156:159], v[108:111], v[16:19], v[156:159]\n\t" \
  "ds_read_b64 v[16:17], v26 offset:10240\n\t" \
  "s_waitcnt lgkmcnt(11)\n\t" \
  "v_mfma_f32_16x16x32_bf16 v[136:139], v[64:67], v[20:23], v[136:139]\n\t" \
  "ds_read_b64 v[18:19], v27 offset:10240\n\t" \
  "v_mfma_f32_16x16x32_bf16 v[140:143], v[72:75], v[20:23], v[140:143]\n\t" \
  "v_mfma_f32_16x16x32_bf16 v[144:147], v[80:83], v[20:23], v[144:147]\n\t" \
  "v_mfma_f32_16x16x32_bf16 v[148:151], v[88:91], v[20:23], v[148:151]\n\t" \
  "v_mfma_f32_16x16x32_bf16 v[152:155], v[96:99], v[20:23], v[152:155]\n\t" \
  "v_mfma_f32_16x16x32_bf16 v[156:159], v[104:107], v[20:23], v[156:159]\n\t" \
  "ds_read_b64 v[20:21], v28 offset:11264\n\t" \
  "s_waitcnt lgkmcnt(9)\n\t" \
  "v_mfma_f32_16x16x32_bf16 v[140:143], v[68:71], v[0:3], v[140:143]\n\t" \
  "ds_read_b64 v[22:23], v29 offset:11264\n\t" \
  "v_mfma_f32_16x16x32_bf16 v[144:147], v[76:79], v[0:3], v[144:147]\n\t" \
  "v_mfma_f32_16x16x32_bf16 v[148:151], v[84:87], v[0:3], v[148:151]\n\t" \
  "v_mfma_f32_16x16x32_bf16 v[152:155], v[92:95], v[0:3], v[152:155]\n\t" \
  "v_mfma_f32_16x16x32_bf16 v[156:159], v[100:103], v[0:3], v[156:159]\n\t" \
  "ds_read_b64 v[0:1], v30 offset:12288\n\t" \
  "s_waitcnt lgkmcnt(9)\n\t" \
  "v_mfma_f32_16x16x32_bf16 v[140:143], v[64:67], v[4:7], v[140:143]\n\t" \
  "ds_read_b64 v[2:3], v31 offset:12288\n\t" \
  "v_mfma_f32_16x16x32_bf16 v[144:147], v[72:75], v[4:7], v[144:147]\n\t" \
  "v_mfma_f32_16x16x32_bf16 v[148:151], v[80:83], v[4:7], v[148:151]\n\t" \
  "v_mfma_f32_16x16x32_bf16 v[152:155], v[88:91], v[4:7], v[152:155]\n\t" \
  "v_mfma_f32_16x16x32_bf16 v[156:159], v[96:99], v[4:7], v[156:159]\n\t" \
  "ds_read_b64 v[4:5], v24 offset:13312\n\t" \
  "s_waitcnt lgkmcnt(9)\n\t" \
  "v_mfma_f32_16x16x32_bf16 v[144:147], v[68:71], v[8:11], v[144:147]\n\t" \
  "ds_read_b64 v[6:7], v25 offset:13312\n\t" \
  "v_mfma_f32_16x16x32_bf16 v[148:151], v[76:79], v[8:11], v[148:151]\n\t" \
  "v_mfma_f32_16x16x32_bf16 v[152:155], v[84:87], v[8:11], v[152:155]\n\t" \
  "v_mfma_f32_16x16x32_bf16 v[156:159], v[92:95], v[8:11], v[156:159]\n\t" \
  "ds_read_b64 v[8:9], v26 offset:14336\n\t" \
  "s_waitcnt lgkmcnt(9)\n\t" \
  "v_mfma_f32_16x16x32_bf16 v[144:147], v[64:67], v[12:15], v[144:147]\n\t" \
  "ds_read_b64 v[10:11], v27 offset:14336\n\t" \
  "v_mfma_f32_16x16x32_bf16 v[148:151], v[72:75], v[12:15], v[148:151]\n\t" \
  "v_mfma_f32_16x16x32_bf16 v[152:155], v[80:83], v[12:15], v[152:155]\n\t" \
  "v_mfma_f32_16x16x32_bf16 v[156:159], v[88:91], v[12:15], v[156:159]\n\t" \
  "ds_read_b64 v[12:13], v28 offset:15360\n\t" \
  "s_waitcnt lgkmcnt(9)\n\t" \
  "v_mfma_f32_16x16x32_bf16 v[148:151], v[68:71], v[16:19], v[148:151]\n\t" \
  "ds_read_b64 v[14:15], v29 offset:15360\n\t" \
  "v_mfma_f32_16x16x32_bf16 v[152:155], v[76:79], v[16:19], v[152:155]\n\t" \
  "v_mfma_f32_16x16x32_bf16 v[156:159], v[84:87], v[16:19], v[156:159]\n\t" \
  "s_waitcnt lgkmcnt(8)\n\t" \
  "v_mfma_f32_16x16x32_bf16 v[148:151], v[64:67], v[20:23], v[148:151]\n\t" \
  "v_mfma_f32_16x16x32_bf16 v[152:155], v[72:75], v[20:23], v[152:155]\n\t" \
  "v_mfma_f32_16x16x32_bf16 v[156:159], v[80:83], v[20:23], v[156:159]\n\t" \
  "s_waitcnt lgkmcnt(6)\n\t" \
  "v_mfma_f32_16x16x32_bf16 v[152:155], v[68:71], v[0:3], v[152:155]\n\t" \
  "v_mfma_f32_16x16x32_bf16 v[156:159], v[76:79], v[0:3], v[156:159]\n\t" \
  "s_waitcnt lgkmcnt(4)\n\t" \
  "v_mfma_f32_16x16x32_bf16 v[152:155], v[64:67], v[4:7], v[152:155]\n\t" \
  "v_mfma_f32_16x16x32_bf16 v[156:159], v[72:75], v[4:7], v[156:159]\n\t" \
  "s_waitcnt lgkmcnt(2)\n\t" \
  "v_mfma_f32_16x16x32_bf16 v[156:159], v[68:71], v[8:11], v[156:159]\n\t" \
  "s_waitcnt lgkmcnt(0)\n\t" \
  "v_mfma_f32_16x16x32_bf16 v[156:159], v[64:67], v[12:15], v[156:159]\n\t" \
  "s_waitcnt lgkmcnt(0)\n\t" \
  "s_nop 7\n\t"
#define CV_CLOB "v0", "v1", "v2", "v3", "v4", "v5", "v6", "v7", "v8", "v9", "v10", "v11", "v12", "v13", "v14", "v15", "v16", "v17", "v18", "v19", "v20", "v21", "v22", "v23", "v34", "v35", "v36", "v37", "v38", "v39", "v40", "v41"

#define G3_ASM \
  "s_getpc_b64 s[92:93]\n\t" \
  "s_branch QC_%=\n\t" \
  "ds_read_b64_tr_b16 v[96:97], v160 offset:0\n\t" \
  "ds_read_b64_tr_b16 v[98:99], v160 offset:2048\n\t" \
  "ds_read_b64_tr_b16 v[100:101], v161 offset:0\n\t" \
  "ds_read_b64_tr_b16 v[102:103], v161 offset:2048\n\t" \
  "ds_read_b64_tr_b16 v[104:105], v162 offset:0\n\t" \
  "ds_read_b64_tr_b16 v[106:107], v162 offset:2048\n\t" \
  "ds_read_b64_tr_b16 v[108:109], v163 offset:0\n\t" \
  "ds_read_b64_tr_b16 v[110:111], v163 offset:2048\n\t" \
  "ds_read_b64_tr_b16 v[112:113], v160 offset:4096\n\t" \
  "ds_read_b64_tr_b16 v[114:115], v160 offset:6144\n\t" \
  "ds_read_b64_tr_b16 v[116:117], v161 offset:4096\n\t" \
  "ds_read_b64_tr_b16 v[118:119], v161 offset:6144\n\t" \
  "ds_read_b64_tr_b16 v[120:121], v162 offset:4096\n\t" \
  "ds_read_b64_tr_b16 v[122:123], v162 offset:6144\n\t" \
  "QH_%=:\n\t" \
  "v_add_u32 v166, %[go], v165\n\t" \
  "ds_read_b32 v166, v166\n\t" \
  "s_addk_i32 %[cn], 1\n\t" \
  "s_cmp_gt_u32 %[cn], 0x400000\n\t" \
  "s_cbranch_scc1 QI_%=\n\t" \
  "s_waitcnt lgkmcnt(0)\n\t" \
  "v_cmp_eq_u32 vcc, 0, v166\n\t" \
  "s_cbranch_vccz QI_%=\n\t" \
  "s_sleep 2\n\t" \
  "s_getpc_b64 s[92:93]\n\t" \
  "s_branch QC_%=\n\t" \
  "s_branch QH_%=\n\t" \
  "QI_%=:\n\t" \
  "s_setprio 0\n\t" \
  "ds_read_b32 v166, v165\n\t" \
  "s_waitcnt lgkmcnt(11)\n\t" \
  "v_mfma_f32_16x16x32_bf16 v[128:131], v[96:99], v[0:3], 0\n\t" \
  "ds_read_b64_tr_b16 v[124:125], v163 offset:4096\n\t" \
  "v_readlane_b32 s94, v167, 5\n\t" \
  "v_mfma_f32_16x16x32_bf16 v[144:147], v[96:99], v[4:7], 0\n\t" \
  "ds_read_b64_tr_b16 v[126:127], v163 offset:6144\n\t" \
  "s_waitcnt lgkmcnt(2)\n\t" \
  "v_cmp_lt_u32 vcc, %[th], v166\n\t" \
  "v_mfma_f32_16x16x32_bf16 v[132:135], v[100:103], v[0:3], 0\n\t" \
  "s_mov_b64 exec, vcc\n\t" \
  "buffer_load_dwordx4 v[80:83], v164, %[ob], s94 offen offset:0 sc1\n\t" \
  "s_mov_b64 exec, -1\n\t" \
  "v_mfma_f32_16x16x32_bf16 v[148:151], v[100:103], v[4:7], 0\n\t" \
  "ds_read_b64_tr_b16 v[96:97], v160 offset:8192\n\t" \
  "ds_read_b64_tr_b16 v[98:99], v160 offset:10240\n\t" \
  "v_mfma_f32_16x16x32_bf16 v[136:139], v[104:107], v[0:3], 0\n\t" \
  "s_mov_b64 exec, vcc\n\t" \
  "buffer_load_dwordx4 v[84:87], v164, %[ob], s94 offen offset:1024 sc1\n\t" \
  "s_mov_b64 exec, -1\n\t" \
  "v_mfma_f32_16x16x32_bf16 v[152:155], v[104:107], v[4:7], 0\n\t" \
  "ds_read_b64_tr_b16 v[100:101], v161 offset:8192\n\t" \
  "ds_read_b64_tr_b16 v[102:103], v161 offset:10240\n\t" \
  "v_mfma_f32_16x16x32_bf16 v[140:143], v[108:111], v[0:3], 0\n\t" \
  "s_mov_b64 exec, vcc\n\t" \
  "buffer_load_dwordx4 v[88:91], v164, %[ob], s94 offen offset:2048 sc1\n\t" \
  "s_mov_b64 exec, -1\n\t" \
  "v_mfma_f32_16x16x32_bf16 v[156:159], v[108:111], v[4:7], 0\n\t" \
  "ds_read_b64_tr_b16 v[104:105], v162 offset:8192\n\t" \
  "ds_read_b64_tr_b16 v[106:107], v162 offset:10240\n\t" \
  "v_mfma_f32_16x16x32_bf16 v[128:131], v[112:115], v[8:11], v[128:131]\n\t" \
  "s_mov_b64 exec, vcc\n\t" \
  "buffer_load_dwordx4 v[92:95], v164, %[ob], s94 offen offset:3072 sc1\n\t" \
  "s_mov_b64 exec, -1\n\t" \
  "v_mfma_f32_16x16x32_bf16 v[144:147], v[112:115], v[12:15], v[144:147]\n\t" \
  "ds_read_b64_tr_b16 v[108:109], v163 offset:8192\n\t" \
  "ds_read_b64_tr_b16 v[110:111], v163 offset:10240\n\t" \
  "v_mfma_f32_16x16x32_bf16 v[132:135], v[116:119], v[8:11], v[132:135]\n\t" \
  "ds_read_b64_tr_b16 v[112:113], v160 offset:12288\n\t" \
  "ds_read_b32 v166, v165\n\t" \
  "v_mfma_f32_16x16x32_bf16 v[148:151], v[116:119], v[12:15], v[148:151]\n\t" \
  "ds_read_b64_tr_b16 v[114:115], v160 offset:14336\n\t" \
  "s_waitcnt lgkmcnt(11)\n\t" \
  "v_mfma_f32_16x16x32_bf16 v[136:139], v[120:123], v[8:11], v[136:139]\n\t" \
  "ds_read_b64_tr_b16 v[116:117], v161 offset:12288\n\t" \
  "v_mfma_f32_16x16x32_bf16 v[152:155], v[120:123], v[12:15], v[152:155]\n\t" \
  "ds_read_b64_tr_b16 v[118:119], v161 offset:14336\n\t" \
  "s_sub_u32 %[iss], %[iss], vcc_lo\n\t" \
  "v_mfma_f32_16x16x32_bf16 v[140:143], v[124:127], v[8:11], v[140:143]\n\t" \
  "ds_read_b64_tr_b16 v[120:121], v162 offset:12288\n\t" \
  "v_mfma_f32_16x16x32_bf16 v[156:159], v[124:127], v[12:15], v[156:159]\n\t" \
  "ds_read_b64_tr_b16 v[122:123], v162 offset:14336\n\t" \
  "s_add_u32 %[th], %[th], 1\n\t" \
  "s_cbranch_vccz QF1_%=\n\t" \
  "s_waitcnt vmcnt(16)\n\t" \
  "QG1_%=:\n\t" \
  "s_waitcnt lgkmcnt(11)\n\t" \
  "v_mfma_f32_16x16x32_bf16 v[128:131], v[96:99], v[16:19], v[128:131]\n\t" \
  "ds_read_b64_tr_b16 v[124:125], v163 offset:12288\n\t" \
  "v_readlane_b32 s94, v167, 6\n\t" \
  "v_mfma_f32_16x16x32_bf16 v[144:147], v[96:99], v[20:23], v[144:147]\n\t" \
  "ds_read_b64_tr_b16 v[126:127], v163 offset:14336\n\t" \
  "s_waitcnt lgkmcnt(7)\n\t" \
  "v_cmp_lt_u32 vcc, %[th], v166\n\t" \
  "v_mfma_f32_16x16x32_bf16 v[132:135], v[100:103], v[16:19], v[132:135]\n\t" \
  "s_mov_b64 exec, vcc\n\t" \
  "buffer_load_dwordx4 v[0:3], v164, %[ob], s94 offen offset:0 sc1\n\t" \
  "s_mov_b64 exec, -1\n\t" \
  "v_mfma_f32_16x16x32_bf16 v[148:151], v[100:103], v[20:23], v[148:151]\n\t" \
  "ds_read_b64_tr_b16 v[96:97], v160 offset:16384\n\t" \
  "ds_read_b64_tr_b16 v[98:99], v160 offset:18432\n\t" \
  "v_mfma_f32_16x16x32_bf16 v[136:139], v[104:107], v[16:19], v[136:139]\n\t" \
  "s_mov_b64 exec, vcc\n\t" \
  "buffer_load_dwordx4 v[4:7], v164, %[ob], s94 offen offset:1024 sc1\n\t" \
  "s_mov_b64 exec, -1\n\t" \
  "v_mfma_f32_16x16x32_bf16 v[152:155], v[104:107], v[20:23], v[152:155]\n\t" \
  "ds_read_b64_tr_b16 v[100:101], v161 offset:16384\n\t" \
  "ds_read_b64_tr_b16 v[102:103], v161 offset:18432\n\t" \
  "v_mfma_f32_16x16x32_bf16 v[140:143], v[108:111], v[16:19], v[140:143]\n\t" \
  "s_mov_b64 exec, vcc\n\t" \
  "buffer_load_dwordx4 v[8:11], v164, %[ob], s94 offen offset:2048 sc1\n\t" \
  "s_mov_b64 exec, -1\n\t" \
  "v_mfma_f32_16x16x32_bf16 v[156:159], v[108:111], v[20:23], v[156:159]\n\t" \
  "ds_read_b64_tr_b16 v[104:105], v162 offset:16384\n\t" \
  "ds_read_b64_tr_b16 v[106:107], v162 offset:18432\n\t" \
  "s_waitcnt lgkmcnt(10)\n\t" \
  "v_mfma_f32_16x16x32_bf16 v[128:131], v[112:115], v[24:27], v[128:131]\n\t" \
  "s_mov_b64 exec, vcc\n\t" \
  "buffer_load_dwordx4 v[12:15], v164, %[ob], s94 offen offset:3072 sc1\n\t" \
  "s_mov_b64 exec, -1\n\t" \
  "v_mfma_f32_16x16x32_bf16 v[144:147], v[112:115], v[28:31], v[144:147]\n\t" \
  "ds_read_b64_tr_b16 v[108:109], v163 offset:16384\n\t" \
  "ds_read_b64_tr_b16 v[110:111], v163 offset:18432\n\t" \
  "v_mfma_f32_16x16x32_bf16 v[132:135], v[116:119], v[24:27], v[132:135]\n\t" \
  "ds_read_b64_tr_b16 v[112:113], v160 offset:20480\n\t" \
  "ds_read_b32 v166, v165\n\t" \
  "v_mfma_f32_16x16x32_bf16 v[148:151], v[116:119], v[28:31], v[148:151]\n\t" \
  "ds_read_b64_tr_b16 v[114:115], v160 offset:22528\n\t" \
  "s_waitcnt lgkmcnt(11)\n\t" \
  "v_mfma_f32_16x16x32_bf16 v[136:139], v[120:123], v[24:27], v[136:139]\n\t" \
  "ds_read_b64_tr_b16 v[116:117], v161 offset:20480\n\t" \
  "v_mfma_f32_16x16x32_bf16 v[152:155], v[120:123], v[28:31], v[152:155]\n\t" \
  "ds_read_b64_tr_b16 v[118:119], v161 offset:22528\n\t" \
  "s_sub_u32 %[iss], %[iss], vcc_lo\n\t" \
  "v_mfma_f32_16x16x32_bf16 v[140:143], v[124:127], v[24:27], v[140:143]\n\t" \
  "ds_read_b64_tr_b16 v[120:121], v162 offset:20480\n\t" \
  "v_mfma_f32_16x16x32_bf16 v[156:159], v[124:127], v[28:31], v[156:159]\n\t" \
  "ds_read_b64_tr_b16 v[122:123], v162 offset:22528\n\t" \
  "s_add_u32 %[th], %[th], 1\n\t" \
  "s_cbranch_vccz QF2_%=\n\t" \
  "s_waitcnt vmcnt(16)\n\t" \
  "QG2_%=:\n\t" \
  "s_waitcnt lgkmcnt(11)\n\t" \
  "v_mfma_f32_16x16x32_bf16 v[128:131], v[96:99], v[32:35], v[128:131]\n\t" \
  "ds_read_b64_tr_b16 v[124:125], v163 offset:20480\n\t" \
  "v_readlane_b32 s94, v167, 7\n\t" \
  "v_mfma_f32_16x16x32_bf16 v[144:147], v[96:99], v[36:39], v[144:147]\n\t" \
  "ds_read_b64_tr_b16 v[126:127], v163 offset:22528\n\t" \
  "s_waitcnt lgkmcnt(7)\n\t" \
  "v_cmp_lt_u32 vcc, %[th], v166\n\t" \
  "v_mfma_f32_16x16x32_bf16 v[132:135], v[100:103], v[32:35], v[132:135]\n\t" \
  "s_mov_b64 exec, vcc\n\t" \
  "buffer_load_dwordx4 v[16:19], v164, %[ob], s94 offen offset:0 sc1\n\t" \
  "s_mov_b64 exec, -1\n\t" \
  "v_mfma_f32_16x16x32_bf16 v[148:151], v[100:103], v[36:39], v[148:151]\n\t" \
  "ds_read_b64_tr_b16 v[96:97], v160 offset:24576\n\t" \
  "ds_read_b64_tr_b16 v[98:99], v160 offset:26624\n\t" \
  "v_mfma_f32_16x16x32_bf16 v[136:139], v[104:107], v[32:35], v[136:139]\n\t" \
  "s_mov_b64 exec, vcc\n\t" \
  "buffer_load_dwordx4 v[20:23], v164, %[ob], s94 offen offset:1024 sc1\n\t" \
  "s_mov_b64 exec, -1\n\t" \
  "v_mfma_f32_16x16x32_bf16 v[152:155], v[104:107], v[36:39], v[152:155]\n\t" \
  "ds_read_b64_tr_b16 v[100:101], v161 offset:24576\n\t" \
  "ds_read_b64_tr_b16 v[102:103], v161 offset:26624\n\t" \
  "v_mfma_f32_16x16x32_bf16 v[140:143], v[108:111], v[32:35], v[140:143]\n\t" \
  "s_mov_b64 exec, vcc\n\t" \
  "buffer_load_dwordx4 v[24:27], v164, %[ob], s94 offen offset:2048 sc1\n\t" \
  "s_mov_b64 exec, -1\n\t" \
  "v_mfma_f32_16x16x32_bf16 v[156:159], v[108:111], v[36:39], v[156:159]\n\t" \
  "ds_read_b64_tr_b16 v[104:105], v162 offset:24576\n\t" \
  "ds_read_b64_tr_b16 v[106:107], v162 offset:26624\n\t" \
  "s_waitcnt lgkmcnt(10)\n\t" \
  "v_mfma_f32_16x16x32_bf16 v[128:131], v[112:115], v[40:43], v[128:131]\n\t" \
  "s_mov_b64 exec, vcc\n\t" \
  "buffer_load_dwordx4 v[28:31], v164, %[ob], s94 offen offset:3072 sc1\n\t" \
  "s_mov_b64 exec, -1\n\t" \
  "v_mfma_f32_16x16x32_bf16 v[144:147], v[112:115], v[44:47], v[144:147]\n\t" \
  "ds_read_b64_tr_b16 v[108:109], v163 offset:24576\n\t" \
  "ds_read_b64_tr_b16 v[110:111], v163 offset:26624\n\t" \
  "v_mfma_f32_16x16x32_bf16 v[132:135], v[116:119], v[40:43], v[132:135]\n\t" \
  "ds_read_b64_tr_b16 v[112:113], v160 offset:28672\n\t" \
  "ds_read_b32 v166, v165\n\t" \
  "v_mfma_f32_16x16x32_bf16 v[148:151], v[116:119], v[44:47], v[148:151]\n\t" \
  "ds_read_b64_tr_b16 v[114:115], v160 offset:30720\n\t" \
  "s_waitcnt lgkmcnt(11)\n\t" \
  "v_mfma_f32_16x16x32_bf16 v[136:139], v[120:123], v[40:43], v[136:139]\n\t" \
  "ds_read_b64_tr_b16 v[116:117], v161 offset:28672\n\t" \
  "v_mfma_f32_16x16x32_bf16 v[152:155], v[120:123], v[44:47], v[152:155]\n\t" \
  "ds_read_b64_tr_b16 v[118:119], v161 offset:30720\n\t" \
  "s_sub_u32 %[iss], %[iss], vcc_lo\n\t" \
  "v_mfma_f32_16x16x32_bf16 v[140:143], v[124:127], v[40:43], v[140:143]\n\t" \
  "ds_read_b64_tr_b16 v[120:121], v162 offset:28672\n\t" \
  "v_mfma_f32_16x16x32_bf16 v[156:159], v[124:127], v[44:47], v[156:159]\n\t" \
  "ds_read_b64_tr_b16 v[122:123], v162 offset:30720\n\t" \
  "s_add_u32 %[th], %[th], 1\n\t" \
  "s_cbranch_vccz QF3_%=\n\t" \
  "s_waitcnt vmcnt(16)\n\t" \
  "QG3_%=:\n\t" \
  "s_waitcnt lgkmcnt(11)\n\t" \
  "v_mfma_f32_16x16x32_bf16 v[128:131], v[96:99], v[48:51], v[128:131]\n\t" \
  "ds_read_b64_tr_b16 v[124:125], v163 offset:28672\n\t" \
  "v_readlane_b32 s94, v167, 8\n\t" \
  "v_mfma_f32_16x16x32_bf16 v[144:147], v[96:99], v[52:55], v[144:147]\n\t" \
  "ds_read_b64_tr_b16 v[126:127], v163 offset:30720\n\t" \
  "s_waitcnt lgkmcnt(7)\n\t" \
  "v_cmp_lt_u32 vcc, %[th], v166\n\t" \
  "v_mfma_f32_16x16x32_bf16 v[132:135], v[100:103], v[48:51], v[132:135]\n\t" \
  "s_mov_b64 exec, vcc\n\t" \
  "buffer_load_dwordx4 v[32:35], v164, %[ob], s94 offen offset:0 sc1\n\t" \
  "s_mov_b64 exec, -1\n\t" \
  "v_mfma_f32_16x16x32_bf16 v[148:151], v[100:103], v[52:55], v[148:151]\n\t" \
  "ds_read_b64_tr_b16 v[96:97], v160 offset:32768\n\t" \
  "ds_read_b64_tr_b16 v[98:99], v160 offset:34816\n\t" \
  "v_mfma_f32_16x16x32_bf16 v[136:139], v[104:107], v[48:51], v[136:139]\n\t" \
  "s_mov_b64 exec, vcc\n\t" \
  "buffer_load_dwordx4 v[36:39], v164, %[ob], s94 offen offset:1024 sc1\n\t" \
  "s_mov_b64 exec, -1\n\t" \
  "v_mfma_f32_16x16x32_bf16 v[152:155], v[104:107], v[52:55], v[152:155]\n\t" \
  "ds_read_b64_tr_b16 v[100:101], v161 offset:32768\n\t" \
  "ds_read_b64_tr_b16 v[102:103], v161 offset:34816\n\t" \
  "v_mfma_f32_16x16x32_bf16 v[140:143], v[108:111], v[48:51], v[140:143]\n\t" \
  "s_mov_b64 exec, vcc\n\t" \
  "buffer_load_dwordx4 v[40:43], v164, %[ob], s94 offen offset:2048 sc1\n\t" \
  "s_mov_b64 exec, -1\n\t" \
  "v_mfma_f32_16x16x32_bf16 v[156:159], v[108:111], v[52:55], v[156:159]\n\t" \
  "ds_read_b64_tr_b16 v[104:105], v162 offset:32768\n\t" \
  "ds_read_b64_tr_b16 v[106:107], v162 offset:34816\n\t" \
  "s_waitcnt lgkmcnt(10)\n\t" \
  "v_mfma_f32_16x16x32_bf16 v[128:131], v[112:115], v[56:59], v[128:131]\n\t" \
  "s_mov_b64 exec, vcc\n\t" \
  "buffer_load_dwordx4 v[44:47], v164, %[ob], s94 offen offset:3072 sc1\n\t" \
  "s_mov_b64 exec, -1\n\t" \
  "v_mfma_f32_16x16x32_bf16 v[144:147], v[112:115], v[60:63], v[144:147]\n\t" \
  "ds_read_b64_tr_b16 v[108:109], v163 offset:32768\n\t" \
  "ds_read_b64_tr_b16 v[110:111], v163 offset:34816\n\t" \
  "v_mfma_f32_16x16x32_bf16 v[132:135], v[116:119], v[56:59], v[132:135]\n\t" \
  "ds_read_b64_tr_b16 v[112:113], v160 offset:36864\n\t" \
  "ds_read_b32 v166, v165\n\t" \
  "v_mfma_f32_16x16x32_bf16 v[148:151], v[116:119], v[60:63], v[148:151]\n\t" \
  "ds_read_b64_tr_b16 v[114:115], v160 offset:38912\n\t" \
  "s_waitcnt lgkmcnt(11)\n\t" \
  "v_mfma_f32_16x16x32_bf16 v[136:139], v[120:123], v[56:59], v[136:139]\n\t" \
  "ds_read_b64_tr_b16 v[116:117], v161 offset:36864\n\t" \
  "v_mfma_f32_16x16x32_bf16 v[152:155], v[120:123], v[60:63], v[152:155]\n\t" \
  "ds_read_b64_tr_b16 v[118:119], v161 offset:38912\n\t" \
  "s_sub_u32 %[iss], %[iss], vcc_lo\n\t" \
  "v_mfma_f32_16x16x32_bf16 v[140:143], v[124:127], v[56:59], v[140:143]\n\t" \
  "ds_read_b64_tr_b16 v[120:121], v162 offset:36864\n\t" \
  "v_mfma_f32_16x16x32_bf16 v[156:159], v[124:127], v[60:63], v[156:159]\n\t" \
  "ds_read_b64_tr_b16 v[122:123], v162 offset:38912\n\t" \
  "s_add_u32 %[th], %[th], 1\n\t" \
  "s_cbranch_vccz QF4_%=\n\t" \
  "s_waitcnt vmcnt(16)\n\t" \
  "QG4_%=:\n\t" \
  "s_waitcnt lgkmcnt(11)\n\t" \
  "v_mfma_f32_16x16x32_bf16 v[128:131], v[96:99], v[64:67], v[128:131]\n\t" \
  "ds_read_b64_tr_b16 v[124:125], v163 offset:36864\n\t" \
  "v_readlane_b32 s94, v167, 9\n\t" \
  "v_mfma_f32_16x16x32_bf16 v[144:147], v[96:99], v[68:71], v[144:147]\n\t" \
  "ds_read_b64_tr_b16 v[126:127], v163 offset:38912\n\t" \
  "s_waitcnt lgkmcnt(7)\n\t" \
  "v_cmp_lt_u32 vcc, %[th], v166\n\t" \
  "v_mfma_f32_16x16x32_bf16 v[132:135], v[100:103], v[64:67], v[132:135]\n\t" \
  "s_mov_b64 exec, vcc\n\t" \
  "buffer_load_dwordx4 v[48:51], v164, %[ob], s94 offen offset:0 sc1\n\t" \
  "s_mov_b64 exec, -1\n\t" \
  "v_mfma_f32_16x16x32_bf16 v[148:151], v[100:103], v[68:71], v[148:151]\n\t" \
  "ds_read_b64_tr_b16 v[96:97], v160 offset:40960\n\t" \
  "ds_read_b64_tr_b16 v[98:99], v160 offset:43008\n\t" \
  "v_mfma_f32_16x16x32_bf16 v[136:139], v[104:107], v[64:67], v[136:139]\n\t" \
  "s_mov_b64 exec, vcc\n\t" \
  "buffer_load_dwordx4 v[52:55], v164, %[ob], s94 offen offset:1024 sc1\n\t" \
  "s_mov_b64 exec, -1\n\t" \
  "v_mfma_f32_16x16x32_bf16 v[152:155], v[104:107], v[68:71], v[152:155]\n\t" \
  "ds_read_b64_tr_b16 v[100:101], v161 offset:40960\n\t" \
  "ds_read_b64_tr_b16 v[102:103], v161 offset:43008\n\t" \
  "v_mfma_f32_16x16x32_bf16 v[140:143], v[108:111], v[64:67], v[140:143]\n\t" \
  "s_mov_b64 exec, vcc\n\t" \
  "buffer_load_dwordx4 v[56:59], v164, %[ob], s94 offen offset:2048 sc1\n\t" \
  "s_mov_b64 exec, -1\n\t" \
  "v_mfma_f32_16x16x32_bf16 v[156:159], v[108:111], v[68:71], v[156:159]\n\t" \
  "ds_read_b64_tr_b16 v[104:105], v162 offset:40960\n\t" \
  "ds_read_b64_tr_b16 v[106:107], v162 offset:43008\n\t" \
  "s_waitcnt lgkmcnt(10)\n\t" \
  "v_mfma_f32_16x16x32_bf16 v[128:131], v[112:115], v[72:75], v[128:131]\n\t" \
  "s_mov_b64 exec, vcc\n\t" \
  "buffer_load_dwordx4 v[60:63], v164, %[ob], s94 offen offset:3072 sc1\n\t" \
  "s_mov_b64 exec, -1\n\t" \
  "v_mfma_f32_16x16x32_bf16 v[144:147], v[112:115], v[76:79], v[144:147]\n\t" \
  "ds_read_b64_tr_b16 v[108:109], v163 offset:40960\n\t" \
  "ds_read_b64_tr_b16 v[110:111], v163 offset:43008\n\t" \
  "v_mfma_f32_16x16x32_bf16 v[132:135], v[116:119], v[72:75], v[132:135]\n\t" \
  "ds_read_b64_tr_b16 v[112:113], v160 offset:45056\n\t" \
  "ds_read_b32 v166, v165\n\t" \
  "v_mfma_f32_16x16x32_bf16 v[148:151], v[116:119], v[76:79], v[148:151]\n\t" \
  "ds_read_b64_tr_b16 v[114:115], v160 offset:47104\n\t" \
  "s_waitcnt lgkmcnt(11)\n\t" \
  "v_mfma_f32_16x16x32_bf16 v[136:139], v[120:123], v[72:75], v[136:139]\n\t" \
  "ds_read_b64_tr_b16 v[116:117], v161 offset:45056\n\t" \
  "v_mfma_f32_16x16x32_bf16 v[152:155], v[120:123], v[76:79], v[152:155]\n\t" \
  "ds_read_b64_tr_b16 v[118:119], v161 offset:47104\n\t" \
  "s_sub_u32 %[iss], %[iss], vcc_lo\n\t" \
  "v_mfma_f32_16x16x32_bf16 v[140:143], v[124:127], v[72:75], v[140:143]\n\t" \
  "ds_read_b64_tr_b16 v[120:121], v162 offset:45056\n\t" \
  "v_mfma_f32_16x16x32_bf16 v[156:159], v[124:127], v[76:79], v[156:159]\n\t" \
  "ds_read_b64_tr_b16 v[122:123], v162 offset:47104\n\t" \
  "s_add_u32 %[th], %[th], 1\n\t" \
  "s_cbranch_vccz QF5_%=\n\t" \
  "s_waitcnt vmcnt(16)\n\t" \
  "QG5_%=:\n\t" \
  "s_waitcnt lgkmcnt(11)\n\t" \
  "v_mfma_f32_16x16x32_bf16 v[128:131], v[96:99], v[80:83], v[128:131]\n\t" \
  "ds_read_b64_tr_b16 v[124:125], v163 offset:45056\n\t" \
  "v_readlane_b32 s94, v167, 10\n\t" \
  "v_mfma_f32_16x16x32_bf16 v[144:147], v[96:99], v[84:87], v[144:147]\n\t" \
  "ds_read_b64_tr_b16 v[126:127], v163 offset:47104\n\t" \
  "s_waitcnt lgkmcnt(7)\n\t" \
  "v_cmp_lt_u32 vcc, %[th], v166\n\t" \
  "v_mfma_f32_16x16x32_bf16 v[132:135], v[100:103], v[80:83], v[132:135]\n\t" \
  "s_mov_b64 exec, vcc\n\t" \
  "buffer_load_dwordx4 v[64:67], v164, %[ob], s94 offen offset:0 sc1\n\t" \
  "s_mov_b64 exec, -1\n\t" \
  "v_mfma_f32_16x16x32_bf16 v[148:151], v[100:103], v[84:87], v[148:151]\n\t" \
  "ds_read_b64_tr_b16 v[96:97], v160 offset:49152\n\t" \
  "ds_read_b64_tr_b16 v[98:99], v160 offset:51200\n\t" \
  "v_mfma_f32_16x16x32_bf16 v[136:139], v[104:107], v[80:83], v[136:139]\n\t" \
  "s_mov_b64 exec, vcc\n\t" \
  "buffer_load_dwordx4 v[68:71], v164, %[ob], s94 offen offset:1024 sc1\n\t" \
  "s_mov_b64 exec, -1\n\t" \
  "v_mfma_f32_16x16x32_bf16 v[152:155], v[104:107], v[84:87], v[152:155]\n\t" \
  "ds_read_b64_tr_b16 v[100:101], v161 offset:49152\n\t" \
  "ds_read_b64_tr_b16 v[102:103], v161 offset:51200\n\t" \
  "v_mfma_f32_16x16x32_bf16 v[140:143], v[108:111], v[80:83], v[140:143]\n\t" \
  "s_mov_b64 exec, vcc\n\t" \
  "buffer_load_dwordx4 v[72:75], v164, %[ob], s94 offen offset:2048 sc1\n\t" \
  "s_mov_b64 exec, -1\n\t" \
  "v_mfma_f32_16x16x32_bf16 v[156:159], v[108:111], v[84:87], v[156:159]\n\t" \
  "ds_read_b64_tr_b16 v[104:105], v162 offset:49152\n\t" \
  "ds_read_b64_tr_b16 v[106:107], v162 offset:51200\n\t" \
  "s_waitcnt lgkmcnt(10)\n\t" \
  "v_mfma_f32_16x16x32_bf16 v[128:131], v[112:115], v[88:91], v[128:131]\n\t" \
  "s_mov_b64 exec, vcc\n\t" \
  "buffer_load_dwordx4 v[76:79], v164, %[ob], s94 offen offset:3072 sc1\n\t" \
  "s_mov_b64 exec, -1\n\t" \
  "v_mfma_f32_16x16x32_bf16 v[144:147], v[112:115], v[92:95], v[144:147]\n\t" \
  "ds_read_b64_tr_b16 v[108:109], v163 offset:49152\n\t" \
  "ds_read_b64_tr_b16 v[110:111], v163 offset:51200\n\t" \
  "v_mfma_f32_16x16x32_bf16 v[132:135], v[116:119], v[88:91], v[132:135]\n\t" \
  "ds_read_b64_tr_b16 v[112:113], v160 offset:53248\n\t" \
  "ds_read_b32 v166, v165\n\t" \
  "v_mfma_f32_16x16x32_bf16 v[148:151], v[116:119], v[92:95], v[148:151]\n\t" \
  "ds_read_b64_tr_b16 v[114:115], v160 offset:55296\n\t" \
  "s_waitcnt lgkmcnt(11)\n\t" \
  "v_mfma_f32_16x16x32_bf16 v[136:139], v[120:123], v[88:91], v[136:139]\n\t" \
  "ds_read_b64_tr_b16 v[116:117], v161 offset:53248\n\t" \
  "v_mfma_f32_16x16x32_bf16 v[152:155], v[120:123], v[92:95], v[152:155]\n\t" \
  "ds_read_b64_tr_b16 v[118:119], v161 offset:55296\n\t" \
  "s_sub_u32 %[iss], %[iss], vcc_lo\n\t" \
  "v_mfma_f32_16x16x32_bf16 v[140:143], v[124:127], v[88:91], v[140:143]\n\t" \
  "ds_read_b64_tr_b16 v[120:121], v162 offset:53248\n\t" \
  "v_mfma_f32_16x16x32_bf16 v[156:159], v[124:127], v[92:95], v[156:159]\n\t" \
  "ds_read_b64_tr_b16 v[122:123], v162 offset:55296\n\t" \
  "s_add_u32 %[th], %[th], 1\n\t" \
  "s_cbranch_vccz QF6_%=\n\t" \
  "s_waitcnt vmcnt(16)\n\t" \
  "QG6_%=:\n\t" \
  "s_waitcnt lgkmcnt(11)\n\t" \
  "v_mfma_f32_16x16x32_bf16 v[128:131], v[96:99], v[0:3], v[128:131]\n\t" \
  "ds_read_b64_tr_b16 v[124:125], v163 offset:53248\n\t" \
  "v_readlane_b32 s94, v167, 11\n\t" \
  "v_mfma_f32_16x16x32_bf16 v[144:147], v[96:99], v[4:7], v[144:147]\n\t" \
  "ds_read_b64_tr_b16 v[126:127], v163 offset:55296\n\t" \
  "s_waitcnt lgkmcnt(7)\n\t" \
  "v_cmp_lt_u32 vcc, %[th], v166\n\t" \
  "v_mfma_f32_16x16x32_bf16 v[132:135], v[100:103], v[0:3], v[132:135]\n\t" \
  "s_mov_b64 exec, vcc\n\t" \
  "buffer_load_dwordx4 v[80:83], v164, %[ob], s94 offen offset:0 sc1\n\t" \
  "s_mov_b64 exec, -1\n\t" \
  "v_mfma_f32_16x16x32_bf16 v[148:151], v[100:103], v[4:7], v[148:151]\n\t" \
  "ds_read_b64_tr_b16 v[96:97], v160 offset:57344\n\t" \
  "ds_read_b64_tr_b16 v[98:99], v160 offset:59392\n\t" \
  "v_mfma_f32_16x16x32_bf16 v[136:139], v[104:107], v[0:3], v[136:139]\n\t" \
  "s_mov_b64 exec, vcc\n\t" \
  "buffer_load_dwordx4 v[84:87], v164, %[ob], s94 offen offset:1024 sc1\n\t" \
  "s_mov_b64 exec, -1\n\t" \
  "v_mfma_f32_16x16x32_bf16 v[152:155], v[104:107], v[4:7], v[152:155]\n\t" \
  "ds_read_b64_tr_b16 v[100:101], v161 offset:57344\n\t" \
  "ds_read_b64_tr_b16 v[102:103], v161 offset:59392\n\t" \
  "v_mfma_f32_16x16x32_bf16 v[140:143], v[108:111], v[0:3], v[140:143]\n\t" \
  "s_mov_b64 exec, vcc\n\t" \
  "buffer_load_dwordx4 v[88:91], v164, %[ob], s94 offen offset:2048 sc1\n\t" \
  "s_mov_b64 exec, -1\n\t" \
  "v_mfma_f32_16x16x32_bf16 v[156:159], v[108:111], v[4:7], v[156:159]\n\t" \
  "ds_read_b64_tr_b16 v[104:105], v162 offset:57344\n\t" \
  "ds_read_b64_tr_b16 v[106:107], v162 offset:59392\n\t" \
  "s_waitcnt lgkmcnt(10)\n\t" \
  "v_mfma_f32_16x16x32_bf16 v[128:131], v[112:115], v[8:11], v[128:131]\n\t" \
  "s_mov_b64 exec, vcc\n\t" \
  "buffer_load_dwordx4 v[92:95], v164, %[ob], s94 offen offset:3072 sc1\n\t" \
  "s_mov_b64 exec, -1\n\t" \
  "v_mfma_f32_16x16x32_bf16 v[144:147], v[112:115], v[12:15], v[144:147]\n\t" \
  "ds_read_b64_tr_b16 v[108:109], v163 offset:57344\n\t" \
  "ds_read_b64_tr_b16 v[110:111], v163 offset:59392\n\t" \
  "v_mfma_f32_16x16x32_bf16 v[132:135], v[116:119], v[8:11], v[132:135]\n\t" \
  "ds_read_b64_tr_b16 v[112:113], v160 offset:61440\n\t" \
  "ds_read_b32 v166, v165\n\t" \
  "v_mfma_f32_16x16x32_bf16 v[148:151], v[116:119], v[12:15], v[148:151]\n\t" \
  "ds_read_b64_tr_b16 v[114:115], v160 offset:63488\n\t" \
  "s_waitcnt lgkmcnt(11)\n\t" \
  "v_mfma_f32_16x16x32_bf16 v[136:139], v[120:123], v[8:11], v[136:139]\n\t" \
  "ds_read_b64_tr_b16 v[116:117], v161 offset:61440\n\t" \
  "v_mfma_f32_16x16x32_bf16 v[152:155], v[120:123], v[12:15], v[152:155]\n\t" \
  "ds_read_b64_tr_b16 v[118:119], v161 offset:63488\n\t" \
  "s_sub_u32 %[iss], %[iss], vcc_lo\n\t" \
  "v_mfma_f32_16x16x32_bf16 v[140:143], v[124:127], v[8:11], v[140:143]\n\t" \
  "ds_read_b64_tr_b16 v[120:121], v162 offset:61440\n\t" \
  "s_mov_b64 exec, 1\n\t" \
  "ds_write_b32 v165, v165 offset:76\n\t" \
  "s_mov_b64 exec, -1\n\t" \
  "v_mfma_f32_16x16x32_bf16 v[156:159], v[124:127], v[12:15], v[156:159]\n\t" \
  "ds_read_b64_tr_b16 v[122:123], v162 offset:63488\n\t" \
  "s_add_u32 %[th], %[th], 1\n\t" \
  "s_cbranch_vccz QF7_%=\n\t" \
  "s_waitcnt vmcnt(16)\n\t" \
  "QG7_%=:\n\t" \
  "s_waitcnt lgkmcnt(12)\n\t" \
  "v_mfma_f32_16x16x32_bf16 v[128:131], v[96:99], v[16:19], v[128:131]\n\t" \
  "ds_read_b64_tr_b16 v[124:125], v163 offset:61440\n\t" \
  "v_readlane_b32 s94, v167, 12\n\t" \
  "v_mfma_f32_16x16x32_bf16 v[144:147], v[96:99], v[20:23], v[144:147]\n\t" \
  "ds_read_b64_tr_b16 v[126:127], v163 offset:63488\n\t" \
  "s_waitcnt lgkmcnt(8)\n\t" \
  "v_cmp_lt_u32 vcc, %[th], v166\n\t" \
  "v_mfma_f32_16x16x32_bf16 v[132:135], v[100:103], v[16:19], v[132:135]\n\t" \
  "v_add_u32 v160, 0x10000, v160\n\t" \
  "s_mov_b64 exec, vcc\n\t" \
  "buffer_load_dwordx4 v[0:3], v164, %[ob], s94 offen offset:0 sc1\n\t" \
  "s_mov_b64 exec, -1\n\t" \
  "v_mfma_f32_16x16x32_bf16 v[148:151], v[100:103], v[20:23], v[148:151]\n\t" \
  "ds_read_b64_tr_b16 v[96:97], v160 offset:0\n\t" \
  "ds_read_b64_tr_b16 v[98:99], v160 offset:2048\n\t" \
  "v_mfma_f32_16x16x32_bf16 v[136:139], v[104:107], v[16:19], v[136:139]\n\t" \
  "v_add_u32 v161, 0x10000, v161\n\t" \
  "s_mov_b64 exec, vcc\n\t" \
  "buffer_load_dwordx4 v[4:7], v164, %[ob], s94 offen offset:1024 sc1\n\t" \
  "s_mov_b64 exec, -1\n\t" \
  "v_mfma_f32_16x16x32_bf16 v[152:155], v[104:107], v[20:23], v[152:155]\n\t" \
  "ds_read_b64_tr_b16 v[100:101], v161 offset:0\n\t" \
  "ds_read_b64_tr_b16 v[102:103], v161 offset:2048\n\t" \
  "v_mfma_f32_16x16x32_bf16 v[140:143], v[108:111], v[16:19], v[140:143]\n\t" \
  "v_add_u32 v162, 0x10000, v162\n\t" \
  "s_mov_b64 exec, vcc\n\t" \
  "buffer_load_dwordx4 v[8:11], v164, %[ob], s94 offen offset:2048 sc1\n\t" \
  "s_mov_b64 exec, -1\n\t" \
  "v_mfma_f32_16x16x32_bf16 v[156:159], v[108:111], v[20:23], v[156:159]\n\t" \
  "ds_read_b64_tr_b16 v[104:105], v162 offset:0\n\t" \
  "ds_read_b64_tr_b16 v[106:107], v162 offset:2048\n\t" \
  "s_waitcnt lgkmcnt(11)\n\t" \
  "v_mfma_f32_16x16x32_bf16 v[128:131], v[112:115], v[24:27], v[128:131]\n\t" \
  "v_add_u32 v163, 0x10000, v163\n\t" \
  "s_mov_b64 exec, vcc\n\t" \
  "buffer_load_dwordx4 v[12:15], v164, %[ob], s94 offen offset:3072 sc1\n\t" \
  "s_mov_b64 exec, -1\n\t" \
  "v_mfma_f32_16x16x32_bf16 v[144:147], v[112:115], v[28:31], v[144:147]\n\t" \
  "ds_read_b64_tr_b16 v[108:109], v163 offset:0\n\t" \
  "ds_read_b64_tr_b16 v[110:111], v163 offset:2048\n\t" \
  "v_mfma_f32_16x16x32_bf16 v[132:135], v[116:119], v[24:27], v[132:135]\n\t" \
  "ds_read_b64_tr_b16 v[112:113], v160 offset:4096\n\t" \
  "ds_read_b32 v166, v165\n\t" \
  "v_mfma_f32_16x16x32_bf16 v[148:151], v[116:119], v[28:31], v[148:151]\n\t" \
  "ds_read_b64_tr_b16 v[114:115], v160 offset:6144\n\t" \
  "s_waitcnt lgkmcnt(11)\n\t" \
  "v_mfma_f32_16x16x32_bf16 v[136:139], v[120:123], v[24:27], v[136:139]\n\t" \
  "ds_read_b64_tr_b16 v[116:117], v161 offset:4096\n\t" \
  "v_mfma_f32_16x16x32_bf16 v[152:155], v[120:123], v[28:31], v[152:155]\n\t" \
  "ds_read_b64_tr_b16 v[118:119], v161 offset:6144\n\t" \
  "s_sub_u32 %[iss], %[iss], vcc_lo\n\t" \
  "v_mfma_f32_16x16x32_bf16 v[140:143], v[124:127], v[24:27], v[140:143]\n\t" \
  "ds_read_b64_tr_b16 v[120:121], v162 offset:4096\n\t" \
  "v_mfma_f32_16x16x32_bf16 v[156:159], v[124:127], v[28:31], v[156:159]\n\t" \
  "ds_read_b64_tr_b16 v[122:123], v162 offset:6144\n\t" \
  "s_add_u32 %[th], %[th], 1\n\t" \
  "s_cbranch_vccz QF8_%=\n\t" \
  "s_waitcnt vmcnt(16)\n\t" \
  "QG8_%=:\n\t" \
  "s_waitcnt lgkmcnt(11)\n\t" \
  "v_mfma_f32_16x16x32_bf16 v[128:131], v[96:99], v[32:35], v[128:131]\n\t" \
  "ds_read_b64_tr_b16 v[124:125], v163 offset:4096\n\t" \
  "v_readlane_b32 s94, v167, 13\n\t" \
  "v_mfma_f32_16x16x32_bf16 v[144:147], v[96:99], v[36:39], v[144:147]\n\t" \
  "ds_read_b64_tr_b16 v[126:127], v163 offset:6144\n\t" \
  "s_waitcnt lgkmcnt(7)\n\t" \
  "v_cmp_lt_u32 vcc, %[th], v166\n\t" \
  "v_mfma_f32_16x16x32_bf16 v[132:135], v[100:103], v[32:35], v[132:135]\n\t" \
  "s_mov_b64 exec, vcc\n\t" \
  "buffer_load_dwordx4 v[16:19], v164, %[ob], s94 offen offset:0 sc1\n\t" \
  "s_mov_b64 exec, -1\n\t" \
  "v_mfma_f32_16x16x32_bf16 v[148:151], v[100:103], v[36:39], v[148:151]\n\t" \
  "ds_read_b64_tr_b16 v[96:97], v160 offset:8192\n\t" \
  "ds_read_b64_tr_b16 v[98:99], v160 offset:10240\n\t" \
  "v_mfma_f32_16x16x32_bf16 v[136:139], v[104:107], v[32:35], v[136:139]\n\t" \
  "s_mov_b64 exec, vcc\n\t" \
  "buffer_load_dwordx4 v[20:23], v164, %[ob], s94 offen offset:1024 sc1\n\t" \
  "s_mov_b64 exec, -1\n\t" \
  "v_mfma_f32_16x16x32_bf16 v[152:155], v[104:107], v[36:39], v[152:155]\n\t" \
  "ds_read_b64_tr_b16 v[100:101], v161 offset:8192\n\t" \
  "ds_read_b64_tr_b16 v[102:103], v161 offset:10240\n\t" \
  "v_mfma_f32_16x16x32_bf16 v[140:143], v[108:111], v[32:35], v[140:143]\n\t" \
  "s_mov_b64 exec, vcc\n\t" \
  "buffer_load_dwordx4 v[24:27], v164, %[ob], s94 offen offset:2048 sc1\n\t" \
  "s_mov_b64 exec, -1\n\t" \
  "v_mfma_f32_16x16x32_bf16 v[156:159], v[108:111], v[36:39], v[156:159]\n\t" \
  "ds_read_b64_tr_b16 v[104:105], v162 offset:8192\n\t" \
  "ds_read_b64_tr_b16 v[106:107], v162 offset:10240\n\t" \
  "s_waitcnt lgkmcnt(10)\n\t" \
  "v_mfma_f32_16x16x32_bf16 v[128:131], v[112:115], v[40:43], v[128:131]\n\t" \
  "s_mov_b64 exec, vcc\n\t" \
  "buffer_load_dwordx4 v[28:31], v164, %[ob], s94 offen offset:3072 sc1\n\t" \
  "s_mov_b64 exec, -1\n\t" \
  "v_mfma_f32_16x16x32_bf16 v[144:147], v[112:115], v[44:47], v[144:147]\n\t" \
  "ds_read_b64_tr_b16 v[108:109], v163 offset:8192\n\t" \
  "ds_read_b64_tr_b16 v[110:111], v163 offset:10240\n\t" \
  "v_mfma_f32_16x16x32_bf16 v[132:135], v[116:119], v[40:43], v[132:135]\n\t" \
  "ds_read_b64_tr_b16 v[112:113], v160 offset:12288\n\t" \
  "ds_read_b32 v166, v165\n\t" \
  "v_mfma_f32_16x16x32_bf16 v[148:151], v[116:119], v[44:47], v[148:151]\n\t" \
  "ds_read_b64_tr_b16 v[114:115], v160 offset:14336\n\t" \
  "s_waitcnt lgkmcnt(11)\n\t" \
  "v_mfma_f32_16x16x32_bf16 v[136:139], v[120:123], v[40:43], v[136:139]\n\t" \
  "ds_read_b64_tr_b16 v[116:117], v161 offset:12288\n\t" \
  "v_mfma_f32_16x16x32_bf16 v[152:155], v[120:123], v[44:47], v[152:155]\n\t" \
  "ds_read_b64_tr_b16 v[118:119], v161 offset:14336\n\t" \
  "s_sub_u32 %[iss], %[iss], vcc_lo\n\t" \
  "v_mfma_f32_16x16x32_bf16 v[140:143], v[124:127], v[40:43], v[140:143]\n\t" \
  "ds_read_b64_tr_b16 v[120:121], v162 offset:12288\n\t" \
  "v_mfma_f32_16x16x32_bf16 v[156:159], v[124:127], v[44:47], v[156:159]\n\t" \
  "ds_read_b64_tr_b16 v[122:123], v162 offset:14336\n\t" \
  "s_add_u32 %[th], %[th], 1\n\t" \
  "s_cbranch_vccz QF9_%=\n\t" \
  "s_waitcnt vmcnt(16)\n\t" \
  "QG9_%=:\n\t" \
  "s_waitcnt lgkmcnt(11)\n\t" \
  "v_mfma_f32_16x16x32_bf16 v[128:131], v[96:99], v[48:51], v[128:131]\n\t" \
  "ds_read_b64_tr_b16 v[124:125], v163 offset:12288\n\t" \
  "v_readlane_b32 s94, v167, 14\n\t" \
  "v_mfma_f32_16x16x32_bf16 v[144:147], v[96:99], v[52:55], v[144:147]\n\t" \
  "ds_read_b64_tr_b16 v[126:127], v163 offset:14336\n\t" \
  "s_waitcnt lgkmcnt(7)\n\t" \
  "v_cmp_lt_u32 vcc, %[th], v166\n\t" \
  "v_mfma_f32_16x16x32_bf16 v[132:135], v[100:103], v[48:51], v[132:135]\n\t" \
  "s_mov_b64 exec, vcc\n\t" \
  "buffer_load_dwordx4 v[32:35], v164, %[ob], s94 offen offset:0 sc1\n\t" \
  "s_mov_b64 exec, -1\n\t" \
  "v_mfma_f32_16x16x32_bf16 v[148:151], v[100:103], v[52:55], v[148:151]\n\t" \
  "ds_read_b64_tr_b16 v[96:97], v160 offset:16384\n\t" \
  "ds_read_b64_tr_b16 v[98:99], v160 offset:18432\n\t" \
  "v_mfma_f32_16x16x32_bf16 v[136:139], v[104:107], v[48:51], v[136:139]\n\t" \
  "s_mov_b64 exec, vcc\n\t" \
  "buffer_load_dwordx4 v[36:39], v164, %[ob], s94 offen offset:1024 sc1\n\t" \
  "s_mov_b64 exec, -1\n\t" \
  "v_mfma_f32_16x16x32_bf16 v[152:155], v[104:107], v[52:55], v[152:155]\n\t" \
  "ds_read_b64_tr_b16 v[100:101], v161 offset:16384\n\t" \
  "ds_read_b64_tr_b16 v[102:103], v161 offset:18432\n\t" \
  "v_mfma_f32_16x16x32_bf16 v[140:143], v[108:111], v[48:51], v[140:143]\n\t" \
  "s_mov_b64 exec, vcc\n\t" \
  "buffer_load_dwordx4 v[40:43], v164, %[ob], s94 offen offset:2048 sc1\n\t" \
  "s_mov_b64 exec, -1\n\t" \
  "v_mfma_f32_16x16x32_bf16 v[156:159], v[108:111], v[52:55], v[156:159]\n\t" \
  "ds_read_b64_tr_b16 v[104:105], v162 offset:16384\n\t" \
  "ds_read_b64_tr_b16 v[106:107], v162 offset:18432\n\t" \
  "s_waitcnt lgkmcnt(10)\n\t" \
  "v_mfma_f32_16x16x32_bf16 v[128:131], v[112:115], v[56:59], v[128:131]\n\t" \
  "s_mov_b64 exec, vcc\n\t" \
  "buffer_load_dwordx4 v[44:47], v164, %[ob], s94 offen offset:3072 sc1\n\t" \
  "s_mov_b64 exec, -1\n\t" \
  "v_mfma_f32_16x16x32_bf16 v[144:147], v[112:115], v[60:63], v[144:147]\n\t" \
  "ds_read_b64_tr_b16 v[108:109], v163 offset:16384\n\t" \
  "ds_read_b64_tr_b16 v[110:111], v163 offset:18432\n\t" \
  "v_mfma_f32_16x16x32_bf16 v[132:135], v[116:119], v[56:59], v[132:135]\n\t" \
  "ds_read_b64_tr_b16 v[112:113], v160 offset:20480\n\t" \
  "ds_read_b32 v166, v165\n\t" \
  "v_mfma_f32_16x16x32_bf16 v[148:151], v[116:119], v[60:63], v[148:151]\n\t" \
  "ds_read_b64_tr_b16 v[114:115], v160 offset:22528\n\t" \
  "s_waitcnt lgkmcnt(11)\n\t" \
  "v_mfma_f32_16x16x32_bf16 v[136:139], v[120:123], v[56:59], v[136:139]\n\t" \
  "ds_read_b64_tr_b16 v[116:117], v161 offset:20480\n\t" \
  "v_mfma_f32_16x16x32_bf16 v[152:155], v[120:123], v[60:63], v[152:155]\n\t" \
  "ds_read_b64_tr_b16 v[118:119], v161 offset:22528\n\t" \
  "s_sub_u32 %[iss], %[iss], vcc_lo\n\t" \
  "v_mfma_f32_16x16x32_bf16 v[140:143], v[124:127], v[56:59], v[140:143]\n\t" \
  "ds_read_b64_tr_b16 v[120:121], v162 offset:20480\n\t" \
  "v_mfma_f32_16x16x32_bf16 v[156:159], v[124:127], v[60:63], v[156:159]\n\t" \
  "ds_read_b64_tr_b16 v[122:123], v162 offset:22528\n\t" \
  "s_add_u32 %[th], %[th], 1\n\t" \
  "s_cbranch_vccz QF10_%=\n\t" \
  "s_waitcnt vmcnt(16)\n\t" \
  "QG10_%=:\n\t" \
  "s_waitcnt lgkmcnt(11)\n\t" \
  "v_mfma_f32_16x16x32_bf16 v[128:131], v[96:99], v[64:67], v[128:131]\n\t" \
  "ds_read_b64_tr_b16 v[124:125], v163 offset:20480\n\t" \
  "v_readlane_b32 s94, v167, 15\n\t" \
  "v_mfma_f32_16x16x32_bf16 v[144:147], v[96:99], v[68:71], v[144:147]\n\t" \
  "ds_read_b64_tr_b16 v[126:127], v163 offset:22528\n\t" \
  "s_waitcnt lgkmcnt(7)\n\t" \
  "v_cmp_lt_u32 vcc, %[th], v166\n\t" \
  "v_mfma_f32_16x16x32_bf16 v[132:135], v[100:103], v[64:67], v[132:135]\n\t" \
  "s_mov_b64 exec, vcc\n\t" \
  "buffer_load_dwordx4 v[48:51], v164, %[ob], s94 offen offset:0 sc1\n\t" \
  "s_mov_b64 exec, -1\n\t" \
  "v_mfma_f32_16x16x32_bf16 v[148:151], v[100:103], v[68:71], v[148:151]\n\t" \
  "ds_read_b64_tr_b16 v[96:97], v160 offset:24576\n\t" \
  "ds_read_b64_tr_b16 v[98:99], v160 offset:26624\n\t" \
  "v_mfma_f32_16x16x32_bf16 v[136:139], v[104:107], v[64:67], v[136:139]\n\t" \
  "s_mov_b64 exec, vcc\n\t" \
  "buffer_load_dwordx4 v[52:55], v164, %[ob], s94 offen offset:1024 sc1\n\t" \
  "s_mov_b64 exec, -1\n\t" \
  "v_mfma_f32_16x16x32_bf16 v[152:155], v[104:107], v[68:71], v[152:155]\n\t" \
  "ds_read_b64_tr_b16 v[100:101], v161 offset:24576\n\t" \
  "ds_read_b64_tr_b16 v[102:103], v161 offset:26624\n\t" \
  "v_mfma_f32_16x16x32_bf16 v[140:143], v[108:111], v[64:67], v[140:143]\n\t" \
  "s_mov_b64 exec, vcc\n\t" \
  "buffer_load_dwordx4 v[56:59], v164, %[ob], s94 offen offset:2048 sc1\n\t" \
  "s_mov_b64 exec, -1\n\t" \
  "v_mfma_f32_16x16x32_bf16 v[156:159], v[108:111], v[68:71], v[156:159]\n\t" \
  "ds_read_b64_tr_b16 v[104:105], v162 offset:24576\n\t" \
  "ds_read_b64_tr_b16 v[106:107], v162 offset:26624\n\t" \
  "s_waitcnt lgkmcnt(10)\n\t" \
  "v_mfma_f32_16x16x32_bf16 v[128:131], v[112:115], v[72:75], v[128:131]\n\t" \
  "s_mov_b64 exec, vcc\n\t" \
  "buffer_load_dwordx4 v[60:63], v164, %[ob], s94 offen offset:3072 sc1\n\t" \
  "s_mov_b64 exec, -1\n\t" \
  "v_mfma_f32_16x16x32_bf16 v[144:147], v[112:115], v[76:79], v[144:147]\n\t" \
  "ds_read_b64_tr_b16 v[108:109], v163 offset:24576\n\t" \
  "ds_read_b64_tr_b16 v[110:111], v163 offset:26624\n\t" \
  "v_mfma_f32_16x16x32_bf16 v[132:135], v[116:119], v[72:75], v[132:135]\n\t" \
  "ds_read_b64_tr_b16 v[112:113], v160 offset:28672\n\t" \
  "v_mfma_f32_16x16x32_bf16 v[148:151], v[116:119], v[76:79], v[148:151]\n\t" \
  "ds_read_b64_tr_b16 v[114:115], v160 offset:30720\n\t" \
  "s_waitcnt lgkmcnt(10)\n\t" \
  "v_mfma_f32_16x16x32_bf16 v[136:139], v[120:123], v[72:75], v[136:139]\n\t" \
  "ds_read_b64_tr_b16 v[116:117], v161 offset:28672\n\t" \
  "v_mfma_f32_16x16x32_bf16 v[152:155], v[120:123], v[76:79], v[152:155]\n\t" \
  "ds_read_b64_tr_b16 v[118:119], v161 offset:30720\n\t" \
  "s_sub_u32 %[iss], %[iss], vcc_lo\n\t" \
  "v_mfma_f32_16x16x32_bf16 v[140:143], v[124:127], v[72:75], v[140:143]\n\t" \
  "ds_read_b64_tr_b16 v[120:121], v162 offset:28672\n\t" \
  "v_mfma_f32_16x16x32_bf16 v[156:159], v[124:127], v[76:79], v[156:159]\n\t" \
  "ds_read_b64_tr_b16 v[122:123], v162 offset:30720\n\t" \
  "s_add_u32 %[th], %[th], 1\n\t" \
  "s_cbranch_vccz QF11_%=\n\t" \
  "s_waitcnt vmcnt(16)\n\t" \
  "QG11_%=:\n\t" \
  "s_waitcnt lgkmcnt(10)\n\t" \
  "v_mfma_f32_16x16x32_bf16 v[128:131], v[96:99], v[80:83], v[128:131]\n\t" \
  "ds_read_b64_tr_b16 v[124:125], v163 offset:28672\n\t" \
  "v_mfma_f32_16x16x32_bf16 v[144:147], v[96:99], v[84:87], v[144:147]\n\t" \
  "ds_read_b64_tr_b16 v[126:127], v163 offset:30720\n\t" \
  "v_mfma_f32_16x16x32_bf16 v[132:135], v[100:103], v[80:83], v[132:135]\n\t" \
  "ds_read_b64_tr_b16 v[96:97], v160 offset:32768\n\t" \
  "v_mfma_f32_16x16x32_bf16 v[148:151], v[100:103], v[84:87], v[148:151]\n\t" \
  "ds_read_b64_tr_b16 v[98:99], v160 offset:34816\n\t" \
  "s_waitcnt lgkmcnt(10)\n\t" \
  "v_mfma_f32_16x16x32_bf16 v[136:139], v[104:107], v[80:83], v[136:139]\n\t" \
  "ds_read_b64_tr_b16 v[100:101], v161 offset:32768\n\t" \
  "v_mfma_f32_16x16x32_bf16 v[152:155], v[104:107], v[84:87], v[152:155]\n\t" \
  "ds_read_b64_tr_b16 v[102:103], v161 offset:34816\n\t" \
  "v_mfma_f32_16x16x32_bf16 v[140:143], v[108:111], v[80:83], v[140:143]\n\t" \
  "ds_read_b64_tr_b16 v[104:105], v162 offset:32768\n\t" \
  "v_mfma_f32_16x16x32_bf16 v[156:159], v[108:111], v[84:87], v[156:159]\n\t" \
  "ds_read_b64_tr_b16 v[106:107], v162 offset:34816\n\t" \
  "s_waitcnt lgkmcnt(10)\n\t" \
  "v_mfma_f32_16x16x32_bf16 v[128:131], v[112:115], v[88:91], v[128:131]\n\t" \
  "ds_read_b64_tr_b16 v[108:109], v163 offset:32768\n\t" \
  "v_mfma_f32_16x16x32_bf16 v[144:147], v[112:115], v[92:95], v[144:147]\n\t" \
  "ds_read_b64_tr_b16 v[110:111], v163 offset:34816\n\t" \
  "v_mfma_f32_16x16x32_bf16 v[132:135], v[116:119], v[88:91], v[132:135]\n\t" \
  "ds_read_b64_tr_b16 v[112:113], v160 offset:36864\n\t" \
  "v_mfma_f32_16x16x32_bf16 v[148:151], v[116:119], v[92:95], v[148:151]\n\t" \
  "ds_read_b64_tr_b16 v[114:115], v160 offset:38912\n\t" \
  "s_waitcnt lgkmcnt(10)\n\t" \
  "v_mfma_f32_16x16x32_bf16 v[136:139], v[120:123], v[88:91], v[136:139]\n\t" \
  "ds_read_b64_tr_b16 v[116:117], v161 offset:36864\n\t" \
  "v_mfma_f32_16x16x32_bf16 v[152:155], v[120:123], v[92:95], v[152:155]\n\t" \
  "ds_read_b64_tr_b16 v[118:119], v161 offset:38912\n\t" \
  "v_mfma_f32_16x16x32_bf16 v[140:143], v[124:127], v[88:91], v[140:143]\n\t" \
  "ds_read_b64_tr_b16 v[120:121], v162 offset:36864\n\t" \
  "v_mfma_f32_16x16x32_bf16 v[156:159], v[124:127], v[92:95], v[156:159]\n\t" \
  "ds_read_b64_tr_b16 v[122:123], v162 offset:38912\n\t" \
  "s_cbranch_vccz QF12_%=\n\t" \
  "s_waitcnt vmcnt(12)\n\t" \
  "QG12_%=:\n\t" \
  "s_waitcnt lgkmcnt(10)\n\t" \
  "v_mfma_f32_16x16x32_bf16 v[128:131], v[96:99], v[0:3], v[128:131]\n\t" \
  "ds_read_b64_tr_b16 v[124:125], v163 offset:36864\n\t" \
  "v_mfma_f32_16x16x32_bf16 v[144:147], v[96:99], v[4:7], v[144:147]\n\t" \
  "ds_read_b64_tr_b16 v[126:127], v163 offset:38912\n\t" \
  "v_mfma_f32_16x16x32_bf16 v[132:135], v[100:103], v[0:3], v[132:135]\n\t" \
  "ds_read_b64_tr_b16 v[96:97], v160 offset:40960\n\t" \
  "v_mfma_f32_16x16x32_bf16 v[148:151], v[100:103], v[4:7], v[148:151]\n\t" \
  "ds_read_b64_tr_b16 v[98:99], v160 offset:43008\n\t" \
  "s_waitcnt lgkmcnt(10)\n\t" \
  "v_mfma_f32_16x16x32_bf16 v[136:139], v[104:107], v[0:3], v[136:139]\n\t" \
  "ds_read_b64_tr_b16 v[100:101], v161 offset:40960\n\t" \
  "v_mfma_f32_16x16x32_bf16 v[152:155], v[104:107], v[4:7], v[152:155]\n\t" \
  "ds_read_b64_tr_b16 v[102:103], v161 offset:43008\n\t" \
  "v_mfma_f32_16x16x32_bf16 v[140:143], v[108:111], v[0:3], v[140:143]\n\t" \
  "ds_read_b64_tr_b16 v[104:105], v162 offset:40960\n\t" \
  "v_mfma_f32_16x16x32_bf16 v[156:159], v[108:111], v[4:7], v[156:159]\n\t" \
  "ds_read_b64_tr_b16 v[106:107], v162 offset:43008\n\t" \
  "s_waitcnt lgkmcnt(10)\n\t" \
  "v_mfma_f32_16x16x32_bf16 v[128:131], v[112:115], v[8:11], v[128:131]\n\t" \
  "ds_read_b64_tr_b16 v[108:109], v163 offset:40960\n\t" \
  "v_mfma_f32_16x16x32_bf16 v[144:147], v[112:115], v[12:15], v[144:147]\n\t" \
  "ds_read_b64_tr_b16 v[110:111], v163 offset:43008\n\t" \
  "v_mfma_f32_16x16x32_bf16 v[132:135], v[116:119], v[8:11], v[132:135]\n\t" \
  "ds_read_b64_tr_b16 v[112:113], v160 offset:45056\n\t" \
  "v_mfma_f32_16x16x32_bf16 v[148:151], v[116:119], v[12:15], v[148:151]\n\t" \
  "ds_read_b64_tr_b16 v[114:115], v160 offset:47104\n\t" \
  "s_waitcnt lgkmcnt(10)\n\t" \
  "v_mfma_f32_16x16x32_bf16 v[136:139], v[120:123], v[8:11], v[136:139]\n\t" \
  "ds_read_b64_tr_b16 v[116:117], v161 offset:45056\n\t" \
  "v_mfma_f32_16x16x32_bf16 v[152:155], v[120:123], v[12:15], v[152:155]\n\t" \
  "ds_read_b64_tr_b16 v[118:119], v161 offset:47104\n\t" \
  "v_mfma_f32_16x16x32_bf16 v[140:143], v[124:127], v[8:11], v[140:143]\n\t" \
  "ds_read_b64_tr_b16 v[120:121], v162 offset:45056\n\t" \
  "v_mfma_f32_16x16x32_bf16 v[156:159], v[124:127], v[12:15], v[156:159]\n\t" \
  "ds_read_b64_tr_b16 v[122:123], v162 offset:47104\n\t" \
  "s_cbranch_vccz QF13_%=\n\t" \
  "s_waitcnt vmcnt(8)\n\t" \
  "QG13_%=:\n\t" \
  "s_waitcnt lgkmcnt(10)\n\t" \
  "v_mfma_f32_16x16x32_bf16 v[128:131], v[96:99], v[16:19], v[128:131]\n\t" \
  "ds_read_b64_tr_b16 v[124:125], v163 offset:45056\n\t" \
  "v_mfma_f32_16x16x32_bf16 v[144:147], v[96:99], v[20:23], v[144:147]\n\t" \
  "ds_read_b64_tr_b16 v[126:127], v163 offset:47104\n\t" \
  "v_mfma_f32_16x16x32_bf16 v[132:135], v[100:103], v[16:19], v[132:135]\n\t" \
  "ds_read_b64_tr_b16 v[96:97], v160 offset:49152\n\t" \
  "v_mfma_f32_16x16x32_bf16 v[148:151], v[100:103], v[20:23], v[148:151]\n\t" \
  "ds_read_b64_tr_b16 v[98:99], v160 offset:51200\n\t" \
  "s_waitcnt lgkmcnt(10)\n\t" \
  "v_mfma_f32_16x16x32_bf16 v[136:139], v[104:107], v[16:19], v[136:139]\n\t" \
  "ds_read_b64_tr_b16 v[100:101], v161 offset:49152\n\t" \
  "v_mfma_f32_16x16x32_bf16 v[152:155], v[104:107], v[20:23], v[152:155]\n\t" \
  "ds_read_b64_tr_b16 v[102:103], v161 offset:51200\n\t" \
  "v_mfma_f32_16x16x32_bf16 v[140:143], v[108:111], v[16:19], v[140:143]\n\t" \
  "ds_read_b64_tr_b16 v[104:105], v162 offset:49152\n\t" \
  "v_mfma_f32_16x16x32_bf16 v[156:159], v[108:111], v[20:23], v[156:159]\n\t" \
  "ds_read_b64_tr_b16 v[106:107], v162 offset:51200\n\t" \
  "s_waitcnt lgkmcnt(10)\n\t" \
  "v_mfma_f32_16x16x32_bf16 v[128:131], v[112:115], v[24:27], v[128:131]\n\t" \
  "ds_read_b64_tr_b16 v[108:109], v163 offset:49152\n\t" \
  "v_mfma_f32_16x16x32_bf16 v[144:147], v[112:115], v[28:31], v[144:147]\n\t" \
  "ds_read_b64_tr_b16 v[110:111], v163 offset:51200\n\t" \
  "v_mfma_f32_16x16x32_bf16 v[132:135], v[116:119], v[24:27], v[132:135]\n\t" \
  "ds_read_b64_tr_b16 v[112:113], v160 offset:53248\n\t" \
  "v_mfma_f32_16x16x32_bf16 v[148:151], v[116:119], v[28:31], v[148:151]\n\t" \
  "ds_read_b64_tr_b16 v[114:115], v160 offset:55296\n\t" \
  "s_waitcnt lgkmcnt(10)\n\t" \
  "v_mfma_f32_16x16x32_bf16 v[136:139], v[120:123], v[24:27], v[136:139]\n\t" \
  "ds_read_b64_tr_b16 v[116:117], v161 offset:53248\n\t" \
  "v_mfma_f32_16x16x32_bf16 v[152:155], v[120:123], v[28:31], v[152:155]\n\t" \
  "ds_read_b64_tr_b16 v[118:119], v161 offset:55296\n\t" \
  "v_mfma_f32_16x16x32_bf16 v[140:143], v[124:127], v[24:27], v[140:143]\n\t" \
  "ds_read_b64_tr_b16 v[120:121], v162 offset:53248\n\t" \
  "v_mfma_f32_16x16x32_bf16 v[156:159], v[124:127], v[28:31], v[156:159]\n\t" \
  "ds_read_b64_tr_b16 v[122:123], v162 offset:55296\n\t" \
  "s_cbranch_vccz QF14_%=\n\t" \
  "s_waitcnt vmcnt(4)\n\t" \
  "QG14_%=:\n\t" \
  "s_waitcnt lgkmcnt(10)\n\t" \
  "v_mfma_f32_16x16x32_bf16 v[128:131], v[96:99], v[32:35], v[128:131]\n\t" \
  "ds_read_b64_tr_b16 v[124:125], v163 offset:53248\n\t" \
  "v_mfma_f32_16x16x32_bf16 v[144:147], v[96:99], v[36:39], v[144:147]\n\t" \
  "ds_read_b64_tr_b16 v[126:127], v163 offset:55296\n\t" \
  "v_mfma_f32_16x16x32_bf16 v[132:135], v[100:103], v[32:35], v[132:135]\n\t" \
  "ds_read_b64_tr_b16 v[96:97], v160 offset:57344\n\t" \
  "v_mfma_f32_16x16x32_bf16 v[148:151], v[100:103], v[36:39], v[148:151]\n\t" \
  "ds_read_b64_tr_b16 v[98:99], v160 offset:59392\n\t" \
  "s_waitcnt lgkmcnt(10)\n\t" \
  "v_mfma_f32_16x16x32_bf16 v[136:139], v[104:107], v[32:35], v[136:139]\n\t" \
  "ds_read_b64_tr_b16 v[100:101], v161 offset:57344\n\t" \
  "v_mfma_f32_16x16x32_bf16 v[152:155], v[104:107], v[36:39], v[152:155]\n\t" \
  "ds_read_b64_tr_b16 v[102:103], v161 offset:59392\n\t" \
  "v_mfma_f32_16x16x32_bf16 v[140:143], v[108:111], v[32:35], v[140:143]\n\t" \
  "ds_read_b64_tr_b16 v[104:105], v162 offset:57344\n\t" \
  "v_mfma_f32_16x16x32_bf16 v[156:159], v[108:111], v[36:39], v[156:159]\n\t" \
  "ds_read_b64_tr_b16 v[106:107], v162 offset:59392\n\t" \
  "s_waitcnt lgkmcnt(10)\n\t" \
  "v_mfma_f32_16x16x32_bf16 v[128:131], v[112:115], v[40:43], v[128:131]\n\t" \
  "ds_read_b64_tr_b16 v[108:109], v163 offset:57344\n\t" \
  "v_mfma_f32_16x16x32_bf16 v[144:147], v[112:115], v[44:47], v[144:147]\n\t" \
  "ds_read_b64_tr_b16 v[110:111], v163 offset:59392\n\t" \
  "v_mfma_f32_16x16x32_bf16 v[132:135], v[116:119], v[40:43], v[132:135]\n\t" \
  "ds_read_b64_tr_b16 v[112:113], v160 offset:61440\n\t" \
  "v_mfma_f32_16x16x32_bf16 v[148:151], v[116:119], v[44:47], v[148:151]\n\t" \
  "ds_read_b64_tr_b16 v[114:115], v160 offset:63488\n\t" \
  "s_waitcnt lgkmcnt(10)\n\t" \
  "v_mfma_f32_16x16x32_bf16 v[136:139], v[120:123], v[40:43], v[136:139]\n\t" \
  "ds_read_b64_tr_b16 v[116:117], v161 offset:61440\n\t" \
  "v_mfma_f32_16x16x32_bf16 v[152:155], v[120:123], v[44:47], v[152:155]\n\t" \
  "ds_read_b64_tr_b16 v[118:119], v161 offset:63488\n\t" \
  "v_mfma_f32_16x16x32_bf16 v[140:143], v[124:127], v[40:43], v[140:143]\n\t" \
  "ds_read_b64_tr_b16 v[120:121], v162 offset:61440\n\t" \
  "v_mfma_f32_16x16x32_bf16 v[156:159], v[124:127], v[44:47], v[156:159]\n\t" \
  "ds_read_b64_tr_b16 v[122:123], v162 offset:63488\n\t" \
  "s_cbranch_vccz QF15_%=\n\t" \
  "s_waitcnt vmcnt(0)\n\t" \
  "QG15_%=:\n\t" \
  "s_waitcnt lgkmcnt(10)\n\t" \
  "v_mfma_f32_16x16x32_bf16 v[128:131], v[96:99], v[48:51], v[128:131]\n\t" \
  "ds_read_b64_tr_b16 v[124:125], v163 offset:61440\n\t" \
  "v_mfma_f32_16x16x32_bf16 v[144:147], v[96:99], v[52:55], v[144:147]\n\t" \
  "ds_read_b64_tr_b16 v[126:127], v163 offset:63488\n\t" \
  "v_mfma_f32_16x16x32_bf16 v[132:135], v[100:103], v[48:51], v[132:135]\n\t" \
  "v_mfma_f32_16x16x32_bf16 v[148:151], v[100:103], v[52:55], v[148:151]\n\t" \
  "s_waitcnt lgkmcnt(8)\n\t" \
  "v_mfma_f32_16x16x32_bf16 v[136:139], v[104:107], v[48:51], v[136:139]\n\t" \
  "v_mfma_f32_16x16x32_bf16 v[152:155], v[104:107], v[52:55], v[152:155]\n\t" \
  "v_mfma_f32_16x16x32_bf16 v[140:143], v[108:111], v[48:51], v[140:143]\n\t" \
  "v_mfma_f32_16x16x32_bf16 v[156:159], v[108:111], v[52:55], v[156:159]\n\t" \
  "s_waitcnt lgkmcnt(4)\n\t" \
  "v_mfma_f32_16x16x32_bf16 v[128:131], v[112:115], v[56:59], v[128:131]\n\t" \
  "v_mfma_f32_16x16x32_bf16 v[144:147], v[112:115], v[60:63], v[144:147]\n\t" \
  "v_mfma_f32_16x16x32_bf16 v[132:135], v[116:119], v[56:59], v[132:135]\n\t" \
  "v_mfma_f32_16x16x32_bf16 v[148:151], v[116:119], v[60:63], v[148:151]\n\t" \
  "s_waitcnt lgkmcnt(0)\n\t" \
  "v_mfma_f32_16x16x32_bf16 v[136:139], v[120:123], v[56:59], v[136:139]\n\t" \
  "v_mfma_f32_16x16x32_bf16 v[152:155], v[120:123], v[60:63], v[152:155]\n\t" \
  "v_mfma_f32_16x16x32_bf16 v[140:143], v[124:127], v[56:59], v[140:143]\n\t" \
  "v_mfma_f32_16x16x32_bf16 v[156:159], v[124:127], v[60:63], v[156:159]\n\t" \
  "s_branch QX_%=\n\t" \
  "QF1_%=:\n\t" \
  "s_mov_b32 %[c], 1\n\t" \
  "s_getpc_b64 s[92:93]\n\t" \
  "s_branch QC_%=\n\t" \
  "s_branch QG1_%=\n\t" \
  "QF2_%=:\n\t" \
  "s_mov_b32 %[c], 2\n\t" \
  "s_getpc_b64 s[92:93]\n\t" \
  "s_branch QC_%=\n\t" \
  "s_branch QG2_%=\n\t" \
  "QF3_%=:\n\t" \
  "s_mov_b32 %[c], 3\n\t" \
  "s_getpc_b64 s[92:93]\n\t" \
  "s_branch QC_%=\n\t" \
  "s_branch QG3_%=\n\t" \
  "QF4_%=:\n\t" \
  "s_mov_b32 %[c], 4\n\t" \
  "s_getpc_b64 s[92:93]\n\t" \
  "s_branch QC_%=\n\t" \
  "s_branch QG4_%=\n\t" \
  "QF5_%=:\n\t" \
  "s_mov_b32 %[c], 5\n\t" \
  "s_getpc_b64 s[92:93]\n\t" \
  "s_branch QC_%=\n\t" \
  "s_branch QG5_%=\n\t" \
  "QF6_%=:\n\t" \
  "s_mov_b32 %[c], 6\n\t" \
  "s_getpc_b64 s[92:93]\n\t" \
  "s_branch QC_%=\n\t" \
  "s_branch QG6_%=\n\t" \
  "QF7_%=:\n\t" \
  "s_mov_b32 %[c], 7\n\t" \
  "s_getpc_b64 s[92:93]\n\t" \
  "s_branch QC_%=\n\t" \
  "s_branch QG7_%=\n\t" \
  "QF8_%=:\n\t" \
  "s_mov_b32 %[c], 8\n\t" \
  "s_getpc_b64 s[92:93]\n\t" \
  "s_branch QC_%=\n\t" \
  "s_branch QG8_%=\n\t" \
  "QF9_%=:\n\t" \
  "s_mov_b32 %[c], 9\n\t" \
  "s_getpc_b64 s[92:93]\n\t" \
  "s_branch QC_%=\n\t" \
  "s_branch QG9_%=\n\t" \
  "QF10_%=:\n\t" \
  "s_mov_b32 %[c], 10\n\t" \
  "s_getpc_b64 s[92:93]\n\t" \
  "s_branch QC_%=\n\t" \
  "s_branch QG10_%=\n\t" \
  "QF11_%=:\n\t" \
  "s_mov_b32 %[c], 11\n\t" \
  "s_getpc_b64 s[92:93]\n\t" \
  "s_branch QC_%=\n\t" \
  "s_branch QG11_%=\n\t" \
  "QF12_%=:\n\t" \
  "s_mov_b32 %[c], 12\n\t" \
  "s_getpc_b64 s[92:93]\n\t" \
  "s_branch QC_%=\n\t" \
  "s_branch QG12_%=\n\t" \
  "QF13_%=:\n\t" \
  "s_mov_b32 %[c], 13\n\t" \
  "s_getpc_b64 s[92:93]\n\t" \
  "s_branch QC_%=\n\t" \
  "s_branch QG13_%=\n\t" \
  "QF14_%=:\n\t" \
  "s_mov_b32 %[c], 14\n\t" \
  "s_getpc_b64 s[92:93]\n\t" \
  "s_branch QC_%=\n\t" \
  "s_branch QG14_%=\n\t" \
  "QF15_%=:\n\t" \
  "s_mov_b32 %[c], 15\n\t" \
  "s_getpc_b64 s[92:93]\n\t" \
  "s_branch QC_%=\n\t" \
  "s_branch QG15_%=\n\t" \
  "QC_%=:\n\t" \
  "s_add_u32 s92, s92, 4\n\t" \
  "s_addc_u32 s93, s93, 0\n\t" \
  "QU_%=:\n\t" \
  "ds_read_b32 v166, v165\n\t" \
  "s_add_u32 %[t1], %[c], 5\n\t" \
  "s_waitcnt lgkmcnt(0)\n\t" \
  "v_readfirstlane_b32 %[t0], v166\n\t" \
  "s_nop 0\n\t" \
  "s_min_u32 %[t1], %[t1], %[t0]\n\t" \
  "QL_%=:\n\t" \
  "s_cmp_ge_u32 %[iss], %[t1]\n\t" \
  "s_cbranch_scc1 QD_%=\n\t" \
  "s_mov_b32 %[t0], %[iss]\n\t" \
  "s_sub_u32 %[th], %[t0], 6\n\t" \
  "s_cselect_b32 %[t0], %[t0], %[th]\n\t" \
  "s_sub_u32 %[th], %[t0], 6\n\t" \
  "s_cselect_b32 %[t0], %[t0], %[th]\n\t" \
  "s_cmp_eq_u32 %[t0], 1\n\t" \
  "s_cbranch_scc1 QS1_%=\n\t" \
  "s_cmp_eq_u32 %[t0], 2\n\t" \
  "s_cbranch_scc1 QS2_%=\n\t" \
  "s_cmp_eq_u32 %[t0], 3\n\t" \
  "s_cbranch_scc1 QS3_%=\n\t" \
  "s_cmp_eq_u32 %[t0], 4\n\t" \
  "s_cbranch_scc1 QS4_%=\n\t" \
  "s_cmp_eq_u32 %[t0], 5\n\t" \
  "s_cbranch_scc1 QS5_%=\n\t" \
  "QS0_%=:\n\t" \
  "v_readlane_b32 s94, v167, %[iss]\n\t" \
  "s_nop 4\n\t" \
  "buffer_load_dwordx4 v[0:3], v164, %[ob], s94 offen offset:0 sc1\n\t" \
  "buffer_load_dwordx4 v[4:7], v164, %[ob], s94 offen offset:1024 sc1\n\t" \
  "buffer_load_dwordx4 v[8:11], v164, %[ob], s94 offen offset:2048 sc1\n\t" \
  "buffer_load_dwordx4 v[12:15], v164, %[ob], s94 offen offset:3072 sc1\n\t" \
  "s_add_u32 %[iss], %[iss], 1\n\t" \
  "s_cmp_ge_u32 %[iss], %[t1]\n\t" \
  "s_cbranch_scc1 QD_%=\n\t" \
  "QS1_%=:\n\t" \
  "v_readlane_b32 s94, v167, %[iss]\n\t" \
  "s_nop 4\n\t" \
  "buffer_load_dwordx4 v[16:19], v164, %[ob], s94 offen offset:0 sc1\n\t" \
  "buffer_load_dwordx4 v[20:23], v164, %[ob], s94 offen offset:1024 sc1\n\t" \
  "buffer_load_dwordx4 v[24:27], v164, %[ob], s94 offen offset:2048 sc1\n\t" \
  "buffer_load_dwordx4 v[28:31], v164, %[ob], s94 offen offset:3072 sc1\n\t" \
  "s_add_u32 %[iss], %[iss], 1\n\t" \
  "s_cmp_ge_u32 %[iss], %[t1]\n\t" \
  "s_cbranch_scc1 QD_%=\n\t" \
  "QS2_%=:\n\t" \
  "v_readlane_b32 s94, v167, %[iss]\n\t" \
  "s_nop 4\n\t" \
  "buffer_load_dwordx4 v[32:35], v164, %[ob], s94 offen offset:0 sc1\n\t" \
  "buffer_load_dwordx4 v[36:39], v164, %[ob], s94 offen offset:1024 sc1\n\t" \
  "buffer_load_dwordx4 v[40:43], v164, %[ob], s94 offen offset:2048 sc1\n\t" \
  "buffer_load_dwordx4 v[44:47], v164, %[ob], s94 offen offset:3072 sc1\n\t" \
  "s_add_u32 %[iss], %[iss], 1\n\t" \
  "s_cmp_ge_u32 %[iss], %[t1]\n\t" \
  "s_cbranch_scc1 QD_%=\n\t" \
  "QS3_%=:\n\t" \
  "v_readlane_b32 s94, v167, %[iss]\n\t" \
  "s_nop 4\n\t" \
  "buffer_load_dwordx4 v[48:51], v164, %[ob], s94 offen offset:0 sc1\n\t" \
  "buffer_load_dwordx4 v[52:55], v164, %[ob], s94 offen offset:1024 sc1\n\t" \
  "buffer_load_dwordx4 v[56:59], v164, %[ob], s94 offen offset:2048 sc1\n\t" \
  "buffer_load_dwordx4 v[60:63], v164, %[ob], s94 offen offset:3072 sc1\n\t" \
  "s_add_u32 %[iss], %[iss], 1\n\t" \
  "s_cmp_ge_u32 %[iss], %[t1]\n\t" \
  "s_cbranch_scc1 QD_%=\n\t" \
  "QS4_%=:\n\t" \
  "v_readlane_b32 s94, v167, %[iss]\n\t" \
  "s_nop 4\n\t" \
  "buffer_load_dwordx4 v[64:67], v164, %[ob], s94 offen offset:0 sc1\n\t" \
  "buffer_load_dwordx4 v[68:71], v164, %[ob], s94 offen offset:1024 sc1\n\t" \
  "buffer_load_dwordx4 v[72:75], v164, %[ob], s94 offen offset:2048 sc1\n\t" \
  "buffer_load_dwordx4 v[76:79], v164, %[ob], s94 offen offset:3072 sc1\n\t" \
  "s_add_u32 %[iss], %[iss], 1\n\t" \
  "s_cmp_ge_u32 %[iss], %[t1]\n\t" \
  "s_cbranch_scc1 QD_%=\n\t" \
  "QS5_%=:\n\t" \
  "v_readlane_b32 s94, v167, %[iss]\n\t" \
  "s_nop 4\n\t" \
  "buffer_load_dwordx4 v[80:83], v164, %[ob], s94 offen offset:0 sc1\n\t" \
  "buffer_load_dwordx4 v[84:87], v164, %[ob], s94 offen offset:1024 sc1\n\t" \
  "buffer_load_dwordx4 v[88:91], v164, %[ob], s94 offen offset:2048 sc1\n\t" \
  "buffer_load_dwordx4 v[92:95], v164, %[ob], s94 offen offset:3072 sc1\n\t" \
  "s_add_u32 %[iss], %[iss], 1\n\t" \
  "s_cmp_ge_u32 %[iss], %[t1]\n\t" \
  "s_cbranch_scc0 QS0_%=\n\t" \
  "QD_%=:\n\t" \
  "s_cmp_gt_u32 %[iss], %[c]\n\t" \
  "s_cbranch_scc1 QW_%=\n\t" \
  "s_addk_i32 %[cn], 1\n\t" \
  "s_cmp_gt_u32 %[cn], 0x400000\n\t" \
  "s_cbranch_scc1 QT_%=\n\t" \
  "s_sleep 1\n\t" \
  "s_branch QU_%=\n\t" \
  "QT_%=:\n\t" \
  "s_add_u32 %[t1], %[c], 1\n\t" \
  "s_branch QL_%=\n\t" \
  "QW_%=:\n\t" \
  "s_cmp_eq_u32 %[iss], 16\n\t" \
  "s_cselect_b64 vcc, -1, 0\n\t" \
  "s_sub_u32 %[t0], %[iss], %[c]\n\t" \
  "s_cmp_eq_u32 %[t0], 5\n\t" \
  "s_cselect_b32 %[th], %[iss], 99\n\t" \
  "s_waitcnt vmcnt(16)\n\t" \
  "s_cmp_ge_u32 %[t0], 5\n\t" \
  "s_cbranch_scc1 QR_%=\n\t" \
  "s_waitcnt vmcnt(12)\n\t" \
  "s_cmp_ge_u32 %[t0], 4\n\t" \
  "s_cbranch_scc1 QR_%=\n\t" \
  "s_waitcnt vmcnt(8)\n\t" \
  "s_cmp_ge_u32 %[t0], 3\n\t" \
  "s_cbranch_scc1 QR_%=\n\t" \
  "s_waitcnt vmcnt(4)\n\t" \
  "s_cmp_ge_u32 %[t0], 2\n\t" \
  "s_cbranch_scc1 QR_%=\n\t" \
  "s_waitcnt vmcnt(0)\n\t" \
  "QR_%=:\n\t" \
  "s_setpc_b64 s[92:93]\n\t" \
  "QX_%=:\n\t" \
  "s_nop 7\n\t"
#define G3_CLOB "v0", "v1", "v2", "v3", "v4", "v5", "v6", "v7", "v8", "v9", "v10", "v11", "v12", "v13", "v14", "v15", "v16", "v17", "v18", "v19", "v20", "v21", "v22", "v23", "v24", "v25", "v26", "v27", "v28", "v29", "v30", "v31", "v32", "v33", "v34", "v35", "v36", "v37", "v38", "v39", "v40", "v41", "v42", "v43", "v44", "v45", "v46", "v47", "v48", "v49", "v50", "v51", "v52", "v53", "v54", "v55", "v56", "v57", "v58", "v59", "v60", "v61", "v62", "v63", "v64", "v65", "v66", "v67", "v68", "v69", "v70", "v71", "v72", "v73", "v74", "v75", "v76", "v77", "v78", "v79", "v80", "v81", "v82", "v83", "v84", "v85", "v86", "v87", "v88", "v89", "v90", "v91", "v92", "v93", "v94", "v95", "v96", "v97", "v98", "v99", "v100", "v101", "v102", "v103", "v104", "v105", "v106", "v107", "v108", "v109", "v110", "v111", "v112", "v113", "v114", "v115", "v116", "v117", "v118", "v119", "v120", "v121", "v122", "v123", "v124", "v125", "v126", "v127", "v166", "s88", "s89", "s92", "s93", "s94"

#define PP_ASM \
  "v_mov_b32 v60, 0\n\t" \
  "s_mov_b32 %[t0], %[n0]\n\t" \
  "s_cmp_eq_u32 %[t0], 0\n\t" \
  "s_cbranch_scc1 PAx_%=\n\t" \
  "PA_%=:\n\t" \
  "s_barrier\n\t" \
  "s_sub_u32 %[t0], %[t0], 1\n\t" \
  "s_cmp_lg_u32 %[t0], 0\n\t" \
  "s_cbranch_scc1 PA_%=\n\t" \
  "PAx_%=:\n\t" \
  "s_mov_b32 m0, %[shd]\n\t" \
  "global_load_dword v160, v60, %[fl] sc1\n\t" \
  "global_load_lds_dwordx4 v163, %[shp]\n\t" \
  "s_mov_b32 m0, %[tgd]\n\t" \
  "s_mov_b64 exec, %[em]\n\t" \
  "global_load_lds_dword v60, %[fl] sc1\n\t" \
  "s_mov_b64 exec, -1\n\t" \
  "global_load_ushort v161, v60, %[hp]\n\t" \
  "global_load_ushort v162, v60, %[dp]\n\t" \
  "global_load_dwordx4 v[0:3], v152, %[kb0]\n\t" \
  "global_load_dwordx4 v[4:7], v153, %[kb0]\n\t" \
  "global_load_dwordx4 v[8:11], v154, %[kb0]\n\t" \
  "global_load_dwordx4 v[12:15], v155, %[kb3] offset:0\n\t" \
  "global_load_dwordx4 v[28:31], v155, %[vb0] offset:0\n\t" \
  "global_load_dwordx4 v[16:19], v155, %[kb3] offset:2048\n\t" \
  "global_load_dwordx4 v[32:35], v155, %[vb0] offset:2048\n\t" \
  "global_load_dwordx4 v[20:23], v155, %[kb5] offset:0\n\t" \
  "global_load_dwordx4 v[36:39], v155, %[vb2] offset:0\n\t" \
  "global_load_dwordx4 v[24:27], v155, %[kb5] offset:2048\n\t" \
  "global_load_dwordx4 v[40:43], v155, %[vb2] offset:2048\n\t" \
  "s_setprio 2\n\t" \
  "s_barrier\n\t" \
  "s_barrier\n\t" \
  "s_waitcnt vmcnt(11)\n\t" \
  "ds_read_b128 v[128:131], v156 offset:0\n\t" \
  "ds_read_b128 v[132:135], v156 offset:16\n\t" \
  "ds_read_b128 v[136:139], v156 offset:32\n\t" \
  "ds_read_b128 v[140:143], v156 offset:48\n\t" \
  "s_waitcnt lgkmcnt(0)\n\t" \
  "v_lshlrev_b32 v64, 16, v128\n\t" \
  "v_and_b32 v65, 0xffff0000, v128\n\t" \
  "v_lshlrev_b32 v66, 16, v129\n\t" \
  "v_and_b32 v67, 0xffff0000, v129\n\t" \
  "v_lshlrev_b32 v68, 16, v130\n\t" \
  "v_and_b32 v69, 0xffff0000, v130\n\t" \
  "v_lshlrev_b32 v70, 16, v131\n\t" \
  "v_and_b32 v71, 0xffff0000, v131\n\t" \
  "v_lshlrev_b32 v72, 16, v132\n\t" \
  "v_and_b32 v73, 0xffff0000, v132\n\t" \
  "v_lshlrev_b32 v74, 16, v133\n\t" \
  "v_and_b32 v75, 0xffff0000, v133\n\t" \
  "v_lshlrev_b32 v76, 16, v134\n\t" \
  "v_and_b32 v77, 0xffff0000, v134\n\t" \
  "v_lshlrev_b32 v78, 16, v135\n\t" \
  "v_and_b32 v79, 0xffff0000, v135\n\t" \
  "v_lshlrev_b32 v80, 16, v136\n\t" \
  "v_and_b32 v81, 0xffff0000, v136\n\t" \
  "v_lshlrev_b32 v82, 16, v137\n\t" \
  "v_and_b32 v83, 0xffff0000, v137\n\t" \
  "v_lshlrev_b32 v84, 16, v138\n\t" \
  "v_and_b32 v85, 0xffff0000, v138\n\t" \
  "v_lshlrev_b32 v86, 16, v139\n\t" \
  "v_and_b32 v87, 0xffff0000, v139\n\t" \
  "v_lshlrev_b32 v88, 16, v140\n\t" \
  "v_and_b32 v89, 0xffff0000, v140\n\t" \
  "v_lshlrev_b32 v90, 16, v141\n\t" \
  "v_and_b32 v91, 0xffff0000, v141\n\t" \
  "v_lshlrev_b32 v92, 16, v142\n\t" \
  "v_and_b32 v93, 0xffff0000, v142\n\t" \
  "v_lshlrev_b32 v94, 16, v143\n\t" \
  "v_and_b32 v95, 0xffff0000, v143\n\t" \
  "s_waitcnt vmcnt(10)\n\t" \
  "s_mov_b64 exec, %[zm]\n\t" \
  "v_mov_b32 v0, 0\n\t" \
  "v_mov_b32 v1, 0\n\t" \
  "v_mov_b32 v2, 0\n\t" \
  "v_mov_b32 v3, 0\n\t" \
  "s_mov_b64 exec, -1\n\t" \
  "v_lshlrev_b32 v128, 16, v0\n\t" \
  "v_and_b32 v129, 0xffff0000, v0\n\t" \
  "s_barrier\n\t" \
  "v_lshlrev_b32 v130, 16, v1\n\t" \
  "v_and_b32 v131, 0xffff0000, v1\n\t" \
  "v_lshlrev_b32 v132, 16, v2\n\t" \
  "v_and_b32 v133, 0xffff0000, v2\n\t" \
  "v_lshlrev_b32 v134, 16, v3\n\t" \
  "v_and_b32 v135, 0xffff0000, v3\n\t" \
  "v_mul_f32 v96, v128, v67\n\t" \
  "v_mul_f32 v97, v129, v71\n\t" \
  "v_mul_f32 v98, v130, v75\n\t" \
  "v_mul_f32 v99, v131, v79\n\t" \
  "v_mul_f32 v100, v132, v83\n\t" \
  "v_mul_f32 v101, v133, v87\n\t" \
  "v_mul_f32 v102, v134, v91\n\t" \
  "v_mul_f32 v103, v135, v95\n\t" \
  "s_waitcnt vmcnt(9)\n\t" \
  "s_mov_b64 exec, %[zm]\n\t" \
  "v_mov_b32 v4, 0\n\t" \
  "v_mov_b32 v5, 0\n\t" \
  "v_mov_b32 v6, 0\n\t" \
  "v_mov_b32 v7, 0\n\t" \
  "s_mov_b64 exec, -1\n\t" \
  "v_lshlrev_b32 v128, 16, v4\n\t" \
  "v_and_b32 v129, 0xffff0000, v4\n\t" \
  "v_lshlrev_b32 v130, 16, v5\n\t" \
  "v_and_b32 v131, 0xffff0000, v5\n\t" \
  "v_lshlrev_b32 v132, 16, v6\n\t" \
  "v_and_b32 v133, 0xffff0000, v6\n\t" \
  "v_lshlrev_b32 v134, 16, v7\n\t" \
  "v_and_b32 v135, 0xffff0000, v7\n\t" \
  "v_fmac_f32 v96, v128, v66\n\t" \
  "v_fmac_f32 v97, v129, v70\n\t" \
  "v_fmac_f32 v98, v130, v74\n\t" \
  "v_fmac_f32 v99, v131, v78\n\t" \
  "v_fmac_f32 v100, v132, v82\n\t" \
  "v_fmac_f32 v101, v133, v86\n\t" \
  "v_fmac_f32 v102, v134, v90\n\t" \
  "v_fmac_f32 v103, v135, v94\n\t" \
  "v_mul_f32 v104, v128, v67\n\t" \
  "v_mul_f32 v105, v129, v71\n\t" \
  "v_mul_f32 v106, v130, v75\n\t" \
  "v_mul_f32 v107, v131, v79\n\t" \
  "v_mul_f32 v108, v132, v83\n\t" \
  "v_mul_f32 v109, v133, v87\n\t" \
  "v_mul_f32 v110, v134, v91\n\t" \
  "v_mul_f32 v111, v135, v95\n\t" \
  "s_waitcnt vmcnt(8)\n\t" \
  "s_mov_b64 exec, %[zm]\n\t" \
  "v_mov_b32 v8, 0\n\t" \
  "v_mov_b32 v9, 0\n\t" \
  "v_mov_b32 v10, 0\n\t" \
  "v_mov_b32 v11, 0\n\t" \
  "s_mov_b64 exec, -1\n\t" \
  "s_setprio 3\n\t" \
  "s_barrier\n\t" \
  "s_mov_b32 m0, %[hd]\n\t" \
  "s_nop 0\n\t" \
  "global_load_lds_dwordx4 v163, %[hp] offset:0\n\t" \
  "global_load_lds_dwordx4 v163, %[hp] offset:1024\n\t" \
  "global_load_lds_dwordx4 v163, %[hp] offset:2048\n\t" \
  "global_load_lds_dwordx4 v163, %[hp] offset:3072\n\t" \
  "global_load_dwordx4 v[44:47], v155, %[qb0] offset:0\n\t" \
  "global_load_dwordx4 v[48:51], v155, %[qb0] offset:2048\n\t" \
  "global_load_dwordx4 v[52:55], v155, %[qb2] offset:0\n\t" \
  "global_load_dwordx4 v[56:59], v155, %[qb2] offset:2048\n\t" \
  "s_setprio 2\n\t" \
  "v_lshlrev_b32 v128, 16, v8\n\t" \
  "v_and_b32 v129, 0xffff0000, v8\n\t" \
  "v_lshlrev_b32 v130, 16, v9\n\t" \
  "v_and_b32 v131, 0xffff0000, v9\n\t" \
  "v_lshlrev_b32 v132, 16, v10\n\t" \
  "v_and_b32 v133, 0xffff0000, v10\n\t" \
  "v_lshlrev_b32 v134, 16, v11\n\t" \
  "v_and_b32 v135, 0xffff0000, v11\n\t" \
  "v_fmac_f32 v96, v128, v65\n\t" \
  "v_fmac_f32 v97, v129, v69\n\t" \
  "v_fmac_f32 v98, v130, v73\n\t" \
  "v_fmac_f32 v99, v131, v77\n\t" \
  "v_fmac_f32 v100, v132, v81\n\t" \
  "v_fmac_f32 v101, v133, v85\n\t" \
  "v_fmac_f32 v102, v134, v89\n\t" \
  "v_fmac_f32 v103, v135, v93\n\t" \
  "v_fmac_f32 v104, v128, v66\n\t" \
  "v_fmac_f32 v105, v129, v70\n\t" \
  "v_fmac_f32 v106, v130, v74\n\t" \
  "v_fmac_f32 v107, v131, v78\n\t" \
  "v_fmac_f32 v108, v132, v82\n\t" \
  "v_fmac_f32 v109, v133, v86\n\t" \
  "v_fmac_f32 v110, v134, v90\n\t" \
  "v_fmac_f32 v111, v135, v94\n\t" \
  "v_mul_f32 v112, v128, v67\n\t" \
  "v_mul_f32 v113, v129, v71\n\t" \
  "v_mul_f32 v114, v130, v75\n\t" \
  "v_mul_f32 v115, v131, v79\n\t" \
  "v_mul_f32 v116, v132, v83\n\t" \
  "v_mul_f32 v117, v133, v87\n\t" \
  "v_mul_f32 v118, v134, v91\n\t" \
  "v_mul_f32 v119, v135, v95\n\t" \
  "s_waitcnt vmcnt(15)\n\t" \
  "v_lshlrev_b32 v128, 16, v12\n\t" \
  "v_and_b32 v129, 0xffff0000, v12\n\t" \
  "v_lshlrev_b32 v130, 16, v13\n\t" \
  "v_and_b32 v131, 0xffff0000, v13\n\t" \
  "v_lshlrev_b32 v132, 16, v14\n\t" \
  "v_and_b32 v133, 0xffff0000, v14\n\t" \
  "v_lshlrev_b32 v134, 16, v15\n\t" \
  "v_and_b32 v135, 0xffff0000, v15\n\t" \
  "s_barrier\n\t" \
  "v_fmac_f32 v96, v128, v64\n\t" \
  "v_fmac_f32 v97, v129, v68\n\t" \
  "v_fmac_f32 v98, v130, v72\n\t" \
  "v_fmac_f32 v99, v131, v76\n\t" \
  "v_fmac_f32 v100, v132, v80\n\t" \
  "v_fmac_f32 v101, v133, v84\n\t" \
  "v_fmac_f32 v102, v134, v88\n\t" \
  "v_fmac_f32 v103, v135, v92\n\t" \
  "v_fmac_f32 v104, v128, v65\n\t" \
  "v_fmac_f32 v105, v129, v69\n\t" \
  "v_fmac_f32 v106, v130, v73\n\t" \
  "v_fmac_f32 v107, v131, v77\n\t" \
  "v_fmac_f32 v108, v132, v81\n\t" \
  "v_fmac_f32 v109, v133, v85\n\t" \
  "v_fmac_f32 v110, v134, v89\n\t" \
  "v_fmac_f32 v111, v135, v93\n\t" \
  "v_fmac_f32 v112, v128, v66\n\t" \
  "v_fmac_f32 v113, v129, v70\n\t" \
  "v_fmac_f32 v114, v130, v74\n\t" \
  "v_fmac_f32 v115, v131, v78\n\t" \
  "v_fmac_f32 v116, v132, v82\n\t" \
  "v_fmac_f32 v117, v133, v86\n\t" \
  "v_fmac_f32 v118, v134, v90\n\t" \
  "v_fmac_f32 v119, v135, v94\n\t" \
  "v_mul_f32 v120, v128, v67\n\t" \
  "v_mul_f32 v121, v129, v71\n\t" \
  "v_mul_f32 v122, v130, v75\n\t" \
  "v_mul_f32 v123, v131, v79\n\t" \
  "v_mul_f32 v124, v132, v83\n\t" \
  "v_mul_f32 v125, v133, v87\n\t" \
  "v_mul_f32 v126, v134, v91\n\t" \
  "v_mul_f32 v127, v135, v95\n\t" \
  "s_waitcnt vmcnt(14)\n\t" \
  "v_lshlrev_b32 v128, 16, v28\n\t" \
  "v_and_b32 v129, 0xffff0000, v28\n\t" \
  "v_lshlrev_b32 v130, 16, v29\n\t" \
  "v_and_b32 v131, 0xffff0000, v29\n\t" \
  "v_lshlrev_b32 v132, 16, v30\n\t" \
  "v_and_b32 v133, 0xffff0000, v30\n\t" \
  "v_lshlrev_b32 v134, 16, v31\n\t" \
  "v_and_b32 v135, 0xffff0000, v31\n\t" \
  "s_barrier\n\t" \
  "v_mul_f32 v96, v96, v128\n\t" \
  "v_mul_f32 v97, v97, v129\n\t" \
  "v_mul_f32 v98, v98, v130\n\t" \
  "v_mul_f32 v99, v99, v131\n\t" \
  "v_mul_f32 v100, v100, v132\n\t" \
  "v_mul_f32 v101, v101, v133\n\t" \
  "v_mul_f32 v102, v102, v134\n\t" \
  "v_mul_f32 v103, v103, v135\n\t" \
  "v_cvt_pk_bf16_f32 v136, v96, v97\n\t" \
  "v_cvt_pk_bf16_f32 v137, v98, v99\n\t" \
  "v_cvt_pk_bf16_f32 v138, v100, v101\n\t" \
  "v_cvt_pk_bf16_f32 v139, v102, v103\n\t" \
  "v_mov_b32_dpp v128, v136 row_ror:8 row_mask:0xf bank_mask:0xf\n\t" \
  "v_mov_b32_dpp v129, v137 row_ror:8 row_mask:0xf bank_mask:0xf\n\t" \
  "v_mov_b32_dpp v130, v138 row_ror:8 row_mask:0xf bank_mask:0xf\n\t" \
  "v_mov_b32_dpp v131, v139 row_ror:8 row_mask:0xf bank_mask:0xf\n\t" \
  "v_perm_b32 v136, v128, v136, v157\n\t" \
  "v_perm_b32 v137, v129, v137, v157\n\t" \
  "v_perm_b32 v138, v130, v138, v157\n\t" \
  "v_perm_b32 v139, v131, v139, v157\n\t" \
  "v_permlane16_swap_b32 v136, v137\n\t" \
  "s_nop 0\n\t" \
  "v_permlane16_swap_b32 v138, v139\n\t" \
  "s_nop 1\n\t" \
  "v_permlane32_swap_b32 v136, v138\n\t" \
  "v_permlane32_swap_b32 v137, v139\n\t" \
  "s_waitcnt vmcnt(13)\n\t" \
  "v_lshlrev_b32 v128, 16, v16\n\t" \
  "v_and_b32 v129, 0xffff0000, v16\n\t" \
  "v_lshlrev_b32 v130, 16, v17\n\t" \
  "v_and_b32 v131, 0xffff0000, v17\n\t" \
  "v_lshlrev_b32 v132, 16, v18\n\t" \
  "v_and_b32 v133, 0xffff0000, v18\n\t" \
  "v_lshlrev_b32 v134, 16, v19\n\t" \
  "v_and_b32 v135, 0xffff0000, v19\n\t" \
  "v_fmac_f32 v104, v128, v64\n\t" \
  "v_fmac_f32 v105, v129, v68\n\t" \
  "v_fmac_f32 v106, v130, v72\n\t" \
  "v_fmac_f32 v107, v131, v76\n\t" \
  "v_fmac_f32 v108, v132, v80\n\t" \
  "v_fmac_f32 v109, v133, v84\n\t" \
  "s_setprio 3\n\t" \
  "s_barrier\n\t" \
  "global_store_dwordx4 v159, v[136:139], %[sb] sc1\n\t" \
  "v_add_u32 v158, 0x80, v159\n\t" \
  "v_bfi_b32 v159, %[mk], v158, v159\n\t" \
  "s_setprio 2\n\t" \
  "v_fmac_f32 v110, v134, v88\n\t" \
  "v_fmac_f32 v111, v135, v92\n\t" \
  "v_fmac_f32 v112, v128, v65\n\t" \
  "v_fmac_f32 v113, v129, v69\n\t" \
  "v_fmac_f32 v114, v130, v73\n\t" \
  "v_fmac_f32 v115, v131, v77\n\t" \
  "v_fmac_f32 v116, v132, v81\n\t" \
  "v_fmac_f32 v117, v133, v85\n\t" \
  "v_fmac_f32 v118, v134, v89\n\t" \
  "v_fmac_f32 v119, v135, v93\n\t" \
  "v_fmac_f32 v120, v128, v66\n\t" \
  "v_fmac_f32 v121, v129, v70\n\t" \
  "v_fmac_f32 v122, v130, v74\n\t" \
  "v_fmac_f32 v123, v131, v78\n\t" \
  "v_fmac_f32 v124, v132, v82\n\t" \
  "v_fmac_f32 v125, v133, v86\n\t" \
  "v_fmac_f32 v126, v134, v90\n\t" \
  "v_fmac_f32 v127, v135, v94\n\t" \
  "s_waitcnt vmcnt(13)\n\t" \
  "v_lshlrev_b32 v128, 16, v32\n\t" \
  "v_and_b32 v129, 0xffff0000, v32\n\t" \
  "v_lshlrev_b32 v130, 16, v33\n\t" \
  "v_and_b32 v131, 0xffff0000, v33\n\t" \
  "v_lshlrev_b32 v132, 16, v34\n\t" \
  "v_and_b32 v133, 0xffff0000, v34\n\t" \
  "v_lshlrev_b32 v134, 16, v35\n\t" \
  "v_and_b32 v135, 0xffff0000, v35\n\t" \
  "v_mul_f32 v104, v104, v128\n\t" \
  "v_mul_f32 v105, v105, v129\n\t" \
  "v_mul_f32 v106, v106, v130\n\t" \
  "v_mul_f32 v107, v107, v131\n\t" \
  "v_mul_f32 v108, v108, v132\n\t" \
  "v_mul_f32 v109, v109, v133\n\t" \
  "v_mul_f32 v110, v110, v134\n\t" \
  "v_mul_f32 v111, v111, v135\n\t" \
  "v_cvt_pk_bf16_f32 v140, v104, v105\n\t" \
  "v_cvt_pk_bf16_f32 v141, v106, v107\n\t" \
  "v_cvt_pk_bf16_f32 v142, v108, v109\n\t" \
  "v_cvt_pk_bf16_f32 v143, v110, v111\n\t" \
  "v_mov_b32_dpp v128, v140 row_ror:8 row_mask:0xf bank_mask:0xf\n\t" \
  "v_mov_b32_dpp v129, v141 row_ror:8 row_mask:0xf bank_mask:0xf\n\t" \
  "s_barrier\n\t" \
  "v_mov_b32_dpp v130, v142 row_ror:8 row_mask:0xf bank_mask:0xf\n\t" \
  "v_mov_b32_dpp v131, v143 row_ror:8 row_mask:0xf bank_mask:0xf\n\t" \
  "v_perm_b32 v140, v128, v140, v157\n\t" \
  "v_perm_b32 v141, v129, v141, v157\n\t" \
  "v_perm_b32 v142, v130, v142, v157\n\t" \
  "v_perm_b32 v143, v131, v143, v157\n\t" \
  "v_permlane16_swap_b32 v140, v141\n\t" \
  "s_nop 0\n\t" \
  "v_permlane16_swap_b32 v142, v143\n\t" \
  "s_nop 1\n\t" \
  "v_permlane32_swap_b32 v140, v142\n\t" \
  "v_permlane32_swap_b32 v141, v143\n\t" \
  "s_waitcnt vmcnt(12)\n\t" \
  "v_lshlrev_b32 v128, 16, v20\n\t" \
  "v_and_b32 v129, 0xffff0000, v20\n\t" \
  "v_lshlrev_b32 v130, 16, v21\n\t" \
  "v_and_b32 v131, 0xffff0000, v21\n\t" \
  "v_lshlrev_b32 v132, 16, v22\n\t" \
  "v_and_b32 v133, 0xffff0000, v22\n\t" \
  "v_lshlrev_b32 v134, 16, v23\n\t" \
  "v_and_b32 v135, 0xffff0000, v23\n\t" \
  "v_fmac_f32 v112, v128, v64\n\t" \
  "v_fmac_f32 v113, v129, v68\n\t" \
  "v_fmac_f32 v114, v130, v72\n\t" \
  "v_fmac_f32 v115, v131, v76\n\t" \
  "v_fmac_f32 v116, v132, v80\n\t" \
  "v_fmac_f32 v117, v133, v84\n\t" \
  "v_fmac_f32 v118, v134, v88\n\t" \
  "v_fmac_f32 v119, v135, v92\n\t" \
  "v_fmac_f32 v120, v128, v65\n\t" \
  "v_fmac_f32 v121, v129, v69\n\t" \
  "v_fmac_f32 v122, v130, v73\n\t" \
  "v_fmac_f32 v123, v131, v77\n\t" \
  "v_fmac_f32 v124, v132, v81\n\t" \
  "v_fmac_f32 v125, v133, v85\n\t" \
  "v_fmac_f32 v126, v134, v89\n\t" \
  "v_fmac_f32 v127, v135, v93\n\t" \
  "s_waitcnt vmcnt(11)\n\t" \
  "v_lshlrev_b32 v128, 16, v36\n\t" \
  "v_and_b32 v129, 0xffff0000, v36\n\t" \
  "v_lshlrev_b32 v130, 16, v37\n\t" \
  "v_and_b32 v131, 0xffff0000, v37\n\t" \
  "s_setprio 3\n\t" \
  "s_barrier\n\t" \
  "global_store_dwordx4 v159, v[140:143], %[sb] sc1\n\t" \
  "v_add_u32 v158, 0x80, v159\n\t" \
  "v_bfi_b32 v159, %[mk], v158, v159\n\t" \
  "s_setprio 2\n\t" \
  "v_lshlrev_b32 v132, 16, v38\n\t" \
  "v_and_b32 v133, 0xffff0000, v38\n\t" \
  "v_lshlrev_b32 v134, 16, v39\n\t" \
  "v_and_b32 v135, 0xffff0000, v39\n\t" \
  "v_mul_f32 v112, v112, v128\n\t" \
  "v_mul_f32 v113, v113, v129\n\t" \
  "v_mul_f32 v114, v114, v130\n\t" \
  "v_mul_f32 v115, v115, v131\n\t" \
  "v_mul_f32 v116, v116, v132\n\t" \
  "v_mul_f32 v117, v117, v133\n\t" \
  "v_mul_f32 v118, v118, v134\n\t" \
  "v_mul_f32 v119, v119, v135\n\t" \
  "v_cvt_pk_bf16_f32 v144, v112, v113\n\t" \
  "v_cvt_pk_bf16_f32 v145, v114, v115\n\t" \
  "v_cvt_pk_bf16_f32 v146, v116, v117\n\t" \
  "v_cvt_pk_bf16_f32 v147, v118, v119\n\t" \
  "v_mov_b32_dpp v128, v144 row_ror:8 row_mask:0xf bank_mask:0xf\n\t" \
  "v_mov_b32_dpp v129, v145 row_ror:8 row_mask:0xf bank_mask:0xf\n\t" \
  "v_mov_b32_dpp v130, v146 row_ror:8 row_mask:0xf bank_mask:0xf\n\t" \
  "v_mov_b32_dpp v131, v147 row_ror:8 row_mask:0xf bank_mask:0xf\n\t" \
  "v_perm_b32 v144, v128, v144, v157\n\t" \
  "v_perm_b32 v145, v129, v145, v157\n\t" \
  "v_perm_b32 v146, v130, v146, v157\n\t" \
  "v_perm_b32 v147, v131, v147, v157\n\t" \
  "v_permlane16_swap_b32 v144, v145\n\t" \
  "s_nop 0\n\t" \
  "v_permlane16_swap_b32 v146, v147\n\t" \
  "s_nop 1\n\t" \
  "v_permlane32_swap_b32 v144, v146\n\t" \
  "v_permlane32_swap_b32 v145, v147\n\t" \
  "s_waitcnt vmcnt(11)\n\t" \
  "v_lshlrev_b32 v128, 16, v24\n\t" \
  "v_and_b32 v129, 0xffff0000, v24\n\t" \
  "v_lshlrev_b32 v130, 16, v25\n\t" \
  "v_and_b32 v131, 0xffff0000, v25\n\t" \
  "v_lshlrev_b32 v132, 16, v26\n\t" \
  "v_and_b32 v133, 0xffff0000, v26\n\t" \
  "v_lshlrev_b32 v134, 16, v27\n\t" \
  "v_and_b32 v135, 0xffff0000, v27\n\t" \
  "v_fmac_f32 v120, v128, v64\n\t" \
  "v_fmac_f32 v121, v129, v68\n\t" \
  "s_setprio 3\n\t" \
  "s_barrier\n\t" \
  "global_store_dwordx4 v159, v[144:147], %[sb] sc1\n\t" \
  "v_add_u32 v158, 0x80, v159\n\t" \
  "v_bfi_b32 v159, %[mk], v158, v159\n\t" \
  "s_setprio 2\n\t" \
  "v_fmac_f32 v122, v130, v72\n\t" \
  "v_fmac_f32 v123, v131, v76\n\t" \
  "v_fmac_f32 v124, v132, v80\n\t" \
  "v_fmac_f32 v125, v133, v84\n\t" \
  "v_fmac_f32 v126, v134, v88\n\t" \
  "v_fmac_f32 v127, v135, v92\n\t" \
  "s_waitcnt vmcnt(11)\n\t" \
  "v_lshlrev_b32 v128, 16, v40\n\t" \
  "v_and_b32 v129, 0xffff0000, v40\n\t" \
  "v_lshlrev_b32 v130, 16, v41\n\t" \
  "v_and_b32 v131, 0xffff0000, v41\n\t" \
  "v_lshlrev_b32 v132, 16, v42\n\t" \
  "v_and_b32 v133, 0xffff0000, v42\n\t" \
  "v_lshlrev_b32 v134, 16, v43\n\t" \
  "v_and_b32 v135, 0xffff0000, v43\n\t" \
  "v_mul_f32 v120, v120, v128\n\t" \
  "v_mul_f32 v121, v121, v129\n\t" \
  "v_mul_f32 v122, v122, v130\n\t" \
  "v_mul_f32 v123, v123, v131\n\t" \
  "v_mul_f32 v124, v124, v132\n\t" \
  "v_mul_f32 v125, v125, v133\n\t" \
  "v_mul_f32 v126, v126, v134\n\t" \
  "v_mul_f32 v127, v127, v135\n\t" \
  "v_cvt_pk_bf16_f32 v148, v120, v121\n\t" \
  "v_cvt_pk_bf16_f32 v149, v122, v123\n\t" \
  "v_cvt_pk_bf16_f32 v150, v124, v125\n\t" \
  "v_cvt_pk_bf16_f32 v151, v126, v127\n\t" \
  "v_mov_b32_dpp v128, v148 row_ror:8 row_mask:0xf bank_mask:0xf\n\t" \
  "v_mov_b32_dpp v129, v149 row_ror:8 row_mask:0xf bank_mask:0xf\n\t" \
  "v_mov_b32_dpp v130, v150 row_ror:8 row_mask:0xf bank_mask:0xf\n\t" \
  "v_mov_b32_dpp v131, v151 row_ror:8 row_mask:0xf bank_mask:0xf\n\t" \
  "v_perm_b32 v148, v128, v148, v157\n\t" \
  "v_perm_b32 v149, v129, v149, v157\n\t" \
  "v_perm_b32 v150, v130, v150, v157\n\t" \
  "v_perm_b32 v151, v131, v151, v157\n\t" \
  "v_permlane16_swap_b32 v148, v149\n\t" \
  "s_nop 0\n\t" \
  "v_permlane16_swap_b32 v150, v151\n\t" \
  "s_nop 1\n\t" \
  "v_permlane32_swap_b32 v148, v150\n\t" \
  "v_permlane32_swap_b32 v149, v151\n\t" \
  "s_setprio 3\n\t" \
  "s_barrier\n\t" \
  "global_store_dwordx4 v159, v[148:151], %[sb] sc1\n\t" \
  "s_setprio 2\n\t" \
  "s_waitcnt vmcnt(8)\n\t" \
  "ds_read_b64_tr_b16 v[64:65], v165 offset:32\n\t" \
  "ds_read_b64_tr_b16 v[66:67], v165 offset:0\n\t" \
  "ds_read_b64_tr_b16 v[68:69], v165 offset:288\n\t" \
  "ds_read_b64_tr_b16 v[70:71], v165 offset:256\n\t" \
  "ds_read_b64_tr_b16 v[72:73], v165 offset:544\n\t" \
  "ds_read_b64_tr_b16 v[74:75], v165 offset:512\n\t" \
  "ds_read_b64_tr_b16 v[76:77], v165 offset:800\n\t" \
  "ds_read_b64_tr_b16 v[78:79], v165 offset:768\n\t" \
  "v_mov_b32 v24, v166\n\t" \
  "v_mov_b32 v25, v167\n\t" \
  "v_mov_b32 v63, 0xc0\n\t" \
  "v_add_u32 v158, 64, v24\n\t" \
  "v_bfi_b32 v26, v63, v158, v24\n\t" \
  "v_add_u32 v158, 64, v25\n\t" \
  "v_bfi_b32 v27, v63, v158, v25\n\t" \
  "v_add_u32 v158, 64, v26\n\t" \
  "v_bfi_b32 v28, v63, v158, v26\n\t" \
  "v_add_u32 v158, 64, v27\n\t" \
  "v_bfi_b32 v29, v63, v158, v27\n\t" \
  "v_add_u32 v158, 64, v28\n\t" \
  "v_bfi_b32 v30, v63, v158, v28\n\t" \
  "v_add_u32 v158, 64, v29\n\t" \
  "v_bfi_b32 v31, v63, v158, v29\n\t" \
  "s_barrier\n\t" \
  "ds_read_b64_tr_b16 v[80:81], v165 offset:1056\n\t" \
  "ds_read_b64_tr_b16 v[82:83], v165 offset:1024\n\t" \
  "ds_read_b64_tr_b16 v[84:85], v165 offset:1312\n\t" \
  "ds_read_b64_tr_b16 v[86:87], v165 offset:1280\n\t" \
  "ds_read_b64_tr_b16 v[88:89], v165 offset:1568\n\t" \
  "ds_read_b64_tr_b16 v[90:91], v165 offset:1536\n\t" \
  "ds_read_b64_tr_b16 v[92:93], v165 offset:1824\n\t" \
  "ds_read_b64_tr_b16 v[94:95], v165 offset:1792\n\t" \
  "s_setprio 3\n\t" \
  "s_barrier\n\t" \
  "s_waitcnt vmcnt(0)\n\t" \
  "v_add_u32 v61, 1, v160\n\t" \
  "s_mov_b64 exec, 1\n\t" \
  "global_store_dword v60, v61, %[ff] sc1\n\t" \
  "s_mov_b64 exec, -1\n\t" \
  "s_setprio 2\n\t" \
  "ds_read_b64_tr_b16 v[96:97], v165 offset:2080\n\t" \
  "ds_read_b64_tr_b16 v[98:99], v165 offset:2048\n\t" \
  "ds_read_b64_tr_b16 v[100:101], v165 offset:2336\n\t" \
  "ds_read_b64_tr_b16 v[102:103], v165 offset:2304\n\t" \
  "ds_read_b64_tr_b16 v[104:105], v165 offset:2592\n\t" \
  "ds_read_b64_tr_b16 v[106:107], v165 offset:2560\n\t" \
  "ds_read_b64_tr_b16 v[108:109], v165 offset:2848\n\t" \
  "ds_read_b64_tr_b16 v[110:111], v165 offset:2816\n\t" \
  "s_barrier\n\t" \
  "ds_read_b64_tr_b16 v[112:113], v165 offset:3104\n\t" \
  "ds_read_b64_tr_b16 v[114:115], v165 offset:3072\n\t" \
  "ds_read_b64_tr_b16 v[116:117], v165 offset:3360\n\t" \
  "ds_read_b64_tr_b16 v[118:119], v165 offset:3328\n\t" \
  "ds_read_b64_tr_b16 v[120:121], v165 offset:3616\n\t" \
  "ds_read_b64_tr_b16 v[122:123], v165 offset:3584\n\t" \
  "ds_read_b64_tr_b16 v[124:125], v165 offset:3872\n\t" \
  "ds_read_b64_tr_b16 v[126:127], v165 offset:3840\n\t" \
  "s_barrier\n\t" \
  "s_waitcnt lgkmcnt(0)\n\t" \
  "v_lshlrev_b32 v61, 16, v161\n\t" \
  "v_lshlrev_b32 v62, 16, v162\n\t" \
  "v_add_f32 v61, v61, v62\n\t" \
  "v_mov_b32 v62, 0xffff\n\t" \
  "v_cvt_pk_bf16_f32 v61, v61, v61\n\t" \
  "s_mov_b64 exec, 1\n\t" \
  "v_bfi_b32 v66, v62, v61, v66\n\t" \
  "s_mov_b64 exec, 15\n\t" \
  "v_and_b32 v66, v62, v66\n\t" \
  "s_mov_b64 exec, 16\n\t" \
  "v_bfi_b32 v66, v62, v66, v61\n\t" \
  "s_mov_b64 exec, 0xff\n\t" \
  "v_mov_b32 v67, 0\n\t" \
  "s_mov_b64 exec, 0x100\n\t" \
  "v_mov_b32 v67, v61\n\t" \
  "s_mov_b64 exec, 0xf00\n\t" \
  "v_and_b32 v67, v62, v67\n\t" \
  "s_mov_b64 exec, 0x1000\n\t" \
  "v_bfi_b32 v67, v62, v67, v61\n\t" \
  "s_mov_b64 exec, -1\n\t" \
  "s_waitcnt lgkmcnt(0)\n\t"
#define PP_NB 15
#define PP_Q 1
#define PP_CLOB "v0", "v1", "v2", "v3", "v4", "v5", "v6", "v7", "v8", "v9", "v10", "v11", "v12", "v13", "v14", "v15", "v16", "v17", "v18", "v19", "v20", "v21", "v22", "v23", "v32", "v33", "v34", "v35", "v36", "v37", "v38", "v39", "v40", "v41", "v42", "v43", "v60", "v61", "v62", "v63", "v128", "v129", "v130", "v131", "v132", "v133", "v134", "v135", "v136", "v137", "v138", "v139", "v140", "v141", "v142", "v143", "v144", "v145", "v146", "v147", "v148", "v149", "v150", "v151", "v158", "v161", "v162"

#define LD_ASM1 \
  "s_add_u32 m0, %[wb], 49152\n\t" \
  "s_add_u32 s68, %[rw0], 0\n\t" \
  "buffer_load_dwordx4 v5, %[sw], s68 offen offset:0 lds\n\t" \
  "buffer_load_dwordx4 v6, %[sw], s68 offen offset:1024 lds\n\t" \
  "buffer_load_dwordx4 v7, %[sw], s68 offen offset:2048 lds\n\t" \
  "s_add_u32 m0, %[xb], 36864\n\t" \
  "s_add_u32 s68, %[rx0], 0\n\t" \
  "buffer_load_dwordx4 v0, %[sx], s68 offen offset:0 lds\n\t" \
  "buffer_load_dwordx4 v1, %[sx], s68 offen offset:1024 lds\n\t" \
  "s_mov_b64 exec, 0xffff\n\t" \
  "buffer_load_dwordx4 v4, %[sx], s68 offen offset:2048 lds\n\t" \
  "s_mov_b64 exec, -1\n\t" \
  "s_add_u32 m0, %[xb], 46080\n\t" \
  "s_add_u32 s68, %[rx0], 147456\n\t" \
  "buffer_load_dwordx4 v2, %[sx], s68 offen offset:0 lds\n\t" \
  "buffer_load_dwordx4 v3, %[sx], s68 offen offset:1024 lds\n\t" \
  "s_mov_b64 exec, 0xffff\n\t" \
  "buffer_load_dwordx4 v4, %[sx], s68 offen offset:2048 lds\n\t" \
  "s_mov_b64 exec, -1\n\t" \
  "s_add_u32 m0, %[wb], 61440\n\t" \
  "s_add_u32 s68, %[rw0], 196608\n\t" \
  "buffer_load_dwordx4 v5, %[sw], s68 offen offset:0 lds\n\t" \
  "buffer_load_dwordx4 v6, %[sw], s68 offen offset:1024 lds\n\t" \
  "buffer_load_dwordx4 v7, %[sw], s68 offen offset:2048 lds\n\t"
#define LD_ASM2 \
  "s_mov_b32 s85, 0x180000\n\t" \
  "s_load_dword s84, %[pw], s85\n\t" \
  "s_mov_b32 s85, 0x180800\n\t" \
  "s_load_dword s84, %[pw], s85\n\t" \
  "s_mov_b32 s85, 0x181000\n\t" \
  "s_load_dword s84, %[pw], s85\n\t" \
  "s_mov_b32 s85, 0x1b0000\n\t" \
  "s_load_dword s84, %[pw], s85\n\t" \
  "s_mov_b32 s85, 0x1b0800\n\t" \
  "s_load_dword s84, %[pw], s85\n\t" \
  "s_mov_b32 s85, 0x1b1000\n\t" \
  "s_load_dword s84, %[pw], s85\n\t" \
  "s_mov_b32 s85, 0x1e0000\n\t" \
  "s_load_dword s84, %[pw], s85\n\t" \
  "s_mov_b32 s85, 0x1e0800\n\t" \
  "s_load_dword s84, %[pw], s85\n\t" \
  "s_add_u32 s68, %[rx1], 0\n\t" \
  "s_add_u32 s69, %[rx1], 147456\n\t" \
  "s_add_u32 s70, %[rw1], 0\n\t" \
  "s_add_u32 m0, %[xb], 0\n\t" \
  "s_waitcnt vmcnt(3)\n\t" \
  "s_barrier\n\t" \
  "buffer_load_dwordx4 v0, %[sx], s68 offen offset:0 lds\n\t" \
  "s_mov_b32 s85, 0x1e1000\n\t" \
  "s_load_dword s84, %[pw], s85\n\t" \
  "buffer_load_dwordx4 v1, %[sx], s68 offen offset:1024 lds\n\t" \
  "s_mov_b32 s85, 0x210000\n\t" \
  "s_load_dword s84, %[pw], s85\n\t" \
  "s_mov_b64 exec, 0xffff\n\t" \
  "buffer_load_dwordx4 v4, %[sx], s68 offen offset:2048 lds\n\t" \
  "s_add_u32 m0, %[xb], 9216\n\t" \
  "s_mov_b64 exec, -1\n\t" \
  "buffer_load_dwordx4 v2, %[sx], s69 offen offset:0 lds\n\t" \
  "s_mov_b32 s85, 0x210800\n\t" \
  "s_load_dword s84, %[pw], s85\n\t" \
  "buffer_load_dwordx4 v3, %[sx], s69 offen offset:1024 lds\n\t" \
  "s_mov_b64 exec, 0xffff\n\t" \
  "buffer_load_dwordx4 v4, %[sx], s69 offen offset:2048 lds\n\t" \
  "s_add_u32 m0, %[wb], 0\n\t" \
  "s_mov_b64 exec, -1\n\t" \
  "buffer_load_dwordx4 v5, %[sw], s70 offen offset:0 lds\n\t" \
  "buffer_load_dwordx4 v6, %[sw], s70 offen offset:1024 lds\n\t" \
  "buffer_load_dwordx4 v7, %[sw], s70 offen offset:2048 lds\n\t" \
  "s_add_u32 s68, %[rw1], 196608\n\t" \
  "s_add_u32 s69, %[rx2], 0\n\t" \
  "s_add_u32 s70, %[rx2], 147456\n\t" \
  "s_add_u32 m0, %[wb], 12288\n\t" \
  "s_waitcnt vmcnt(9)\n\t" \
  "s_barrier\n\t" \
  "buffer_load_dwordx4 v5, %[sw], s68 offen offset:0 lds\n\t" \
  "s_mov_b32 s85, 0x211000\n\t" \
  "s_load_dword s84, %[pw], s85\n\t" \
  "buffer_load_dwordx4 v6, %[sw], s68 offen offset:1024 lds\n\t" \
  "s_mov_b32 s85, 0x240000\n\t" \
  "s_load_dword s84, %[pw], s85\n\t" \
  "buffer_load_dwordx4 v7, %[sw], s68 offen offset:2048 lds\n\t" \
  "s_mov_b32 s85, 0x240800\n\t" \
  "s_load_dword s84, %[pw], s85\n\t" \
  "s_add_u32 m0, %[xb], 18432\n\t" \
  "s_nop 0\n\t" \
  "buffer_load_dwordx4 v0, %[sx], s69 offen offset:0 lds\n\t" \
  "buffer_load_dwordx4 v1, %[sx], s69 offen offset:1024 lds\n\t" \
  "s_mov_b64 exec, 0xffff\n\t" \
  "buffer_load_dwordx4 v4, %[sx], s69 offen offset:2048 lds\n\t" \
  "s_add_u32 m0, %[xb], 27648\n\t" \
  "s_mov_b64 exec, -1\n\t" \
  "buffer_load_dwordx4 v2, %[sx], s70 offen offset:0 lds\n\t" \
  "buffer_load_dwordx4 v3, %[sx], s70 offen offset:1024 lds\n\t" \
  "s_mov_b64 exec, 0xffff\n\t" \
  "buffer_load_dwordx4 v4, %[sx], s70 offen offset:2048 lds\n\t" \
  "s_mov_b64 exec, -1\n\t" \
  "s_add_u32 s68, %[rw2], 0\n\t" \
  "s_add_u32 s69, %[rw2], 196608\n\t" \
  "s_add_u32 s70, %[rx3], 0\n\t" \
  "s_add_u32 m0, %[wb], 24576\n\t" \
  "s_waitcnt vmcnt(9)\n\t" \
  "s_barrier\n\t" \
  "buffer_load_dwordx4 v5, %[sw], s68 offen offset:0 lds\n\t" \
  "s_mov_b32 s85, 0x241000\n\t" \
  "s_load_dword s84, %[pw], s85\n\t" \
  "buffer_load_dwordx4 v6, %[sw], s68 offen offset:1024 lds\n\t" \
  "s_mov_b32 s85, 0x270000\n\t" \
  "s_load_dword s84, %[pw], s85\n\t" \
  "buffer_load_dwordx4 v7, %[sw], s68 offen offset:2048 lds\n\t" \
  "s_mov_b32 s85, 0x270800\n\t" \
  "s_load_dword s84, %[pw], s85\n\t" \
  "s_add_u32 m0, %[wb], 36864\n\t" \
  "s_nop 0\n\t" \
  "buffer_load_dwordx4 v5, %[sw], s69 offen offset:0 lds\n\t" \
  "buffer_load_dwordx4 v6, %[sw], s69 offen offset:1024 lds\n\t" \
  "buffer_load_dwordx4 v7, %[sw], s69 offen offset:2048 lds\n\t" \
  "s_add_u32 m0, %[xb], 36864\n\t" \
  "s_nop 0\n\t" \
  "buffer_load_dwordx4 v0, %[sx], s70 offen offset:0 lds\n\t" \
  "buffer_load_dwordx4 v1, %[sx], s70 offen offset:1024 lds\n\t" \
  "s_mov_b64 exec, 0xffff\n\t" \
  "buffer_load_dwordx4 v4, %[sx], s70 offen offset:2048 lds\n\t" \
  "s_mov_b64 exec, -1\n\t" \
  "s_add_u32 s68, %[rw3], 0\n\t" \
  "s_add_u32 s69, %[rx3], 147456\n\t" \
  "s_add_u32 s70, %[rw3], 196608\n\t" \
  "s_add_u32 m0, %[wb], 49152\n\t" \
  "s_waitcnt vmcnt(15)\n\t" \
  "s_barrier\n\t" \
  "buffer_load_dwordx4 v5, %[sw], s68 offen offset:0 lds\n\t" \
  "s_mov_b32 s85, 0x271000\n\t" \
  "s_load_dword s84, %[pw], s85\n\t" \
  "buffer_load_dwordx4 v6, %[sw], s68 offen offset:1024 lds\n\t" \
  "s_mov_b32 s85, 0x2a0000\n\t" \
  "s_load_dword s84, %[pw], s85\n\t" \
  "buffer_load_dwordx4 v7, %[sw], s68 offen offset:2048 lds\n\t" \
  "s_mov_b32 s85, 0x2a0800\n\t" \
  "s_load_dword s84, %[pw], s85\n\t" \
  "s_add_u32 m0, %[xb], 46080\n\t" \
  "s_nop 0\n\t" \
  "buffer_load_dwordx4 v2, %[sx], s69 offen offset:0 lds\n\t" \
  "buffer_load_dwordx4 v3, %[sx], s69 offen offset:1024 lds\n\t" \
  "s_mov_b64 exec, 0xffff\n\t" \
  "buffer_load_dwordx4 v4, %[sx], s69 offen offset:2048 lds\n\t" \
  "s_add_u32 m0, %[wb], 61440\n\t" \
  "s_mov_b64 exec, -1\n\t" \
  "buffer_load_dwordx4 v5, %[sw], s70 offen offset:0 lds\n\t" \
  "buffer_load_dwordx4 v6, %[sw], s70 offen offset:1024 lds\n\t" \
  "buffer_load_dwordx4 v7, %[sw], s70 offen offset:2048 lds\n\t" \
  "s_add_u32 s68, %[rx0], 512\n\t" \
  "s_add_u32 s69, %[rw0], 1572864\n\t" \
  "s_add_u32 s70, %[rx0], 147968\n\t" \
  "s_add_u32 m0, %[xb], 0\n\t" \
  "s_waitcnt vmcnt(15)\n\t" \
  "s_barrier\n\t" \
  "buffer_load_dwordx4 v0, %[sx], s68 offen offset:0 lds\n\t" \
  "s_mov_b32 s85, 0x2a1000\n\t" \
  "s_load_dword s84, %[pw], s85\n\t" \
  "buffer_load_dwordx4 v1, %[sx], s68 offen offset:1024 lds\n\t" \
  "s_mov_b32 s85, 0x2d0000\n\t" \
  "s_load_dword s84, %[pw], s85\n\t" \
  "s_mov_b64 exec, 0xffff\n\t" \
  "buffer_load_dwordx4 v4, %[sx], s68 offen offset:2048 lds\n\t" \
  "s_add_u32 m0, %[wb], 0\n\t" \
  "s_mov_b64 exec, -1\n\t" \
  "buffer_load_dwordx4 v5, %[sw], s69 offen offset:0 lds\n\t" \
  "s_mov_b32 s85, 0x2d0800\n\t" \
  "s_load_dword s84, %[pw], s85\n\t" \
  "buffer_load_dwordx4 v6, %[sw], s69 offen offset:1024 lds\n\t" \
  "buffer_load_dwordx4 v7, %[sw], s69 offen offset:2048 lds\n\t" \
  "s_add_u32 m0, %[xb], 9216\n\t" \
  "s_nop 0\n\t" \
  "buffer_load_dwordx4 v2, %[sx], s70 offen offset:0 lds\n\t" \
  "buffer_load_dwordx4 v3, %[sx], s70 offen offset:1024 lds\n\t" \
  "s_mov_b64 exec, 0xffff\n\t" \
  "buffer_load_dwordx4 v4, %[sx], s70 offen offset:2048 lds\n\t" \
  "s_mov_b64 exec, -1\n\t" \
  "s_add_u32 s68, %[rw0], 1769472\n\t" \
  "s_add_u32 m0, %[wb], 12288\n\t" \
  "s_waitcnt vmcnt(21)\n\t" \
  "s_barrier\n\t" \
  "buffer_load_dwordx4 v5, %[sw], s68 offen offset:0 lds\n\t" \
  "s_mov_b32 s85, 0x2d1000\n\t" \
  "s_load_dword s84, %[pw], s85\n\t" \
  "buffer_load_dwordx4 v6, %[sw], s68 offen offset:1024 lds\n\t" \
  "s_mov_b32 s85, 0x300000\n\t" \
  "s_load_dword s84, %[pw], s85\n\t" \
  "buffer_load_dwordx4 v7, %[sw], s68 offen offset:2048 lds\n\t" \
  "s_mov_b32 s85, 0x300800\n\t" \
  "s_load_dword s84, %[pw], s85\n\t" \
  "s_add_u32 s68, %[rx1], 512\n\t" \
  "s_add_u32 s69, %[rw1], 1572864\n\t" \
  "s_add_u32 s70, %[rx1], 147968\n\t" \
  "s_add_u32 m0, %[xb], 18432\n\t" \
  "s_waitcnt vmcnt(15)\n\t" \
  "s_barrier\n\t" \
  "buffer_load_dwordx4 v0, %[sx], s68 offen offset:0 lds\n\t" \
  "s_mov_b32 s85, 0x301000\n\t" \
  "s_load_dword s84, %[pw], s85\n\t" \
  "buffer_load_dwordx4 v1, %[sx], s68 offen offset:1024 lds\n\t" \
  "s_mov_b32 s85, 0x330000\n\t" \
  "s_load_dword s84, %[pw], s85\n\t" \
  "s_mov_b64 exec, 0xffff\n\t" \
  "buffer_load_dwordx4 v4, %[sx], s68 offen offset:2048 lds\n\t" \
  "s_add_u32 m0, %[wb], 24576\n\t" \
  "s_mov_b64 exec, -1\n\t" \
  "buffer_load_dwordx4 v5, %[sw], s69 offen offset:0 lds\n\t" \
  "s_mov_b32 s85, 0x330800\n\t" \
  "s_load_dword s84, %[pw], s85\n\t" \
  "buffer_load_dwordx4 v6, %[sw], s69 offen offset:1024 lds\n\t" \
  "buffer_load_dwordx4 v7, %[sw], s69 offen offset:2048 lds\n\t" \
  "s_add_u32 m0, %[xb], 27648\n\t" \
  "s_nop 0\n\t" \
  "buffer_load_dwordx4 v2, %[sx], s70 offen offset:0 lds\n\t" \
  "buffer_load_dwordx4 v3, %[sx], s70 offen offset:1024 lds\n\t" \
  "s_mov_b64 exec, 0xffff\n\t" \
  "buffer_load_dwordx4 v4, %[sx], s70 offen offset:2048 lds\n\t" \
  "s_mov_b64 exec, -1\n\t" \
  "s_add_u32 s68, %[rw1], 1769472\n\t" \
  "s_add_u32 m0, %[wb], 36864\n\t" \
  "s_waitcnt vmcnt(21)\n\t" \
  "s_barrier\n\t" \
  "buffer_load_dwordx4 v5, %[sw], s68 offen offset:0 lds\n\t" \
  "s_mov_b32 s85, 0x331000\n\t" \
  "s_load_dword s84, %[pw], s85\n\t" \
  "buffer_load_dwordx4 v6, %[sw], s68 offen offset:1024 lds\n\t" \
  "s_mov_b32 s85, 0x360000\n\t" \
  "s_load_dword s84, %[pw], s85\n\t" \
  "buffer_load_dwordx4 v7, %[sw], s68 offen offset:2048 lds\n\t" \
  "s_mov_b32 s85, 0x360800\n\t" \
  "s_load_dword s84, %[pw], s85\n\t" \
  "s_add_u32 s68, %[rx2], 512\n\t" \
  "s_add_u32 s69, %[rw2], 1572864\n\t" \
  "s_add_u32 m0, %[xb], 36864\n\t" \
  "s_waitcnt vmcnt(15)\n\t" \
  "s_barrier\n\t" \
  "buffer_load_dwordx4 v0, %[sx], s68 offen offset:0 lds\n\t" \
  "s_mov_b32 s85, 0x361000\n\t" \
  "s_load_dword s84, %[pw], s85\n\t" \
  "buffer_load_dwordx4 v1, %[sx], s68 offen offset:1024 lds\n\t" \
  "s_mov_b32 s85, 0x390000\n\t" \
  "s_load_dword s84, %[pw], s85\n\t" \
  "s_mov_b64 exec, 0xffff\n\t" \
  "buffer_load_dwordx4 v4, %[sx], s68 offen offset:2048 lds\n\t" \
  "s_add_u32 m0, %[wb], 49152\n\t" \
  "s_mov_b64 exec, -1\n\t" \
  "buffer_load_dwordx4 v5, %[sw], s69 offen offset:0 lds\n\t" \
  "s_mov_b32 s85, 0x390800\n\t" \
  "s_load_dword s84, %[pw], s85\n\t" \
  "buffer_load_dwordx4 v6, %[sw], s69 offen offset:1024 lds\n\t" \
  "buffer_load_dwordx4 v7, %[sw], s69 offen offset:2048 lds\n\t" \
  "s_add_u32 s68, %[rx2], 147968\n\t" \
  "s_add_u32 s69, %[rw2], 1769472\n\t" \
  "s_add_u32 m0, %[xb], 46080\n\t" \
  "s_waitcnt vmcnt(18)\n\t" \
  "s_barrier\n\t" \
  "buffer_load_dwordx4 v2, %[sx], s68 offen offset:0 lds\n\t" \
  "s_mov_b32 s85, 0x391000\n\t" \
  "s_load_dword s84, %[pw], s85\n\t" \
  "buffer_load_dwordx4 v3, %[sx], s68 offen offset:1024 lds\n\t" \
  "s_mov_b32 s85, 0x3c0000\n\t" \
  "s_load_dword s84, %[pw], s85\n\t" \
  "s_mov_b64 exec, 0xffff\n\t" \
  "buffer_load_dwordx4 v4, %[sx], s68 offen offset:2048 lds\n\t" \
  "s_add_u32 m0, %[wb], 61440\n\t" \
  "s_mov_b64 exec, -1\n\t" \
  "buffer_load_dwordx4 v5, %[sw], s69 offen offset:0 lds\n\t" \
  "s_mov_b32 s85, 0x3c0800\n\t" \
  "s_load_dword s84, %[pw], s85\n\t" \
  "buffer_load_dwordx4 v6, %[sw], s69 offen offset:1024 lds\n\t" \
  "buffer_load_dwordx4 v7, %[sw], s69 offen offset:2048 lds\n\t" \
  "s_add_u32 s68, %[rx3], 512\n\t" \
  "s_add_u32 s69, %[rw3], 1572864\n\t" \
  "s_add_u32 m0, %[xb], 0\n\t" \
  "s_waitcnt vmcnt(15)\n\t" \
  "s_barrier\n\t" \
  "buffer_load_dwordx4 v0, %[sx], s68 offen offset:0 lds\n\t" \
  "s_mov_b32 s85, 0x3c1000\n\t" \
  "s_load_dword s84, %[pw], s85\n\t" \
  "buffer_load_dwordx4 v1, %[sx], s68 offen offset:1024 lds\n\t" \
  "s_mov_b32 s85, 0x3f0000\n\t" \
  "s_load_dword s84, %[pw], s85\n\t" \
  "s_mov_b64 exec, 0xffff\n\t" \
  "buffer_load_dwordx4 v4, %[sx], s68 offen offset:2048 lds\n\t" \
  "s_add_u32 m0, %[wb], 0\n\t" \
  "s_mov_b64 exec, -1\n\t" \
  "buffer_load_dwordx4 v5, %[sw], s69 offen offset:0 lds\n\t" \
  "s_mov_b32 s85, 0x3f0800\n\t" \
  "s_load_dword s84, %[pw], s85\n\t" \
  "buffer_load_dwordx4 v6, %[sw], s69 offen offset:1024 lds\n\t" \
  "buffer_load_dwordx4 v7, %[sw], s69 offen offset:2048 lds\n\t" \
  "s_add_u32 s68, %[rx3], 147968\n\t" \
  "s_add_u32 s69, %[rw3], 1769472\n\t" \
  "s_add_u32 m0, %[xb], 9216\n\t" \
  "s_waitcnt vmcnt(18)\n\t" \
  "s_barrier\n\t" \
  "buffer_load_dwordx4 v2, %[sx], s68 offen offset:0 lds\n\t" \
  "s_mov_b32 s85, 0x3f1000\n\t" \
  "s_load_dword s84, %[pw], s85\n\t" \
  "buffer_load_dwordx4 v3, %[sx], s68 offen offset:1024 lds\n\t" \
  "s_mov_b32 s85, 0x420000\n\t" \
  "s_load_dword s84, %[pw], s85\n\t" \
  "s_mov_b64 exec, 0xffff\n\t" \
  "buffer_load_dwordx4 v4, %[sx], s68 offen offset:2048 lds\n\t" \
  "s_add_u32 m0, %[wb], 12288\n\t" \
  "s_mov_b64 exec, -1\n\t" \
  "buffer_load_dwordx4 v5, %[sw], s69 offen offset:0 lds\n\t" \
  "s_mov_b32 s85, 0x420800\n\t" \
  "s_load_dword s84, %[pw], s85\n\t" \
  "buffer_load_dwordx4 v6, %[sw], s69 offen offset:1024 lds\n\t" \
  "buffer_load_dwordx4 v7, %[sw], s69 offen offset:2048 lds\n\t" \
  "s_add_u32 s68, %[rx0], 1024\n\t" \
  "s_add_u32 s69, %[rw0], 3145728\n\t" \
  "s_add_u32 m0, %[xb], 18432\n\t" \
  "s_waitcnt vmcnt(15)\n\t" \
  "s_barrier\n\t" \
  "buffer_load_dwordx4 v0, %[sx], s68 offen offset:0 lds\n\t" \
  "s_mov_b32 s85, 0x421000\n\t" \
  "s_load_dword s84, %[pw], s85\n\t" \
  "buffer_load_dwordx4 v1, %[sx], s68 offen offset:1024 lds\n\t" \
  "s_mov_b32 s85, 0x450000\n\t" \
  "s_load_dword s84, %[pw], s85\n\t" \
  "s_mov_b64 exec, 0xffff\n\t" \
  "buffer_load_dwordx4 v4, %[sx], s68 offen offset:2048 lds\n\t" \
  "s_add_u32 m0, %[wb], 24576\n\t" \
  "s_mov_b64 exec, -1\n\t" \
  "buffer_load_dwordx4 v5, %[sw], s69 offen offset:0 lds\n\t" \
  "s_mov_b32 s85, 0x450800\n\t" \
  "s_load_dword s84, %[pw], s85\n\t" \
  "buffer_load_dwordx4 v6, %[sw], s69 offen offset:1024 lds\n\t" \
  "buffer_load_dwordx4 v7, %[sw], s69 offen offset:2048 lds\n\t" \
  "s_add_u32 s68, %[rx0], 148480\n\t" \
  "s_add_u32 s69, %[rw0], 3342336\n\t" \
  "s_add_u32 m0, %[xb], 27648\n\t" \
  "s_waitcnt vmcnt(18)\n\t" \
  "s_barrier\n\t" \
  "buffer_load_dwordx4 v2, %[sx], s68 offen offset:0 lds\n\t" \
  "s_mov_b32 s85, 0x451000\n\t" \
  "s_load_dword s84, %[pw], s85\n\t" \
  "buffer_load_dwordx4 v3, %[sx], s68 offen offset:1024 lds\n\t" \
  "s_mov_b32 s85, 0x480000\n\t" \
  "s_load_dword s84, %[pw], s85\n\t" \
  "s_mov_b64 exec, 0xffff\n\t" \
  "buffer_load_dwordx4 v4, %[sx], s68 offen offset:2048 lds\n\t" \
  "s_add_u32 m0, %[wb], 36864\n\t" \
  "s_mov_b64 exec, -1\n\t" \
  "buffer_load_dwordx4 v5, %[sw], s69 offen offset:0 lds\n\t" \
  "s_mov_b32 s85, 0x480800\n\t" \
  "s_load_dword s84, %[pw], s85\n\t" \
  "buffer_load_dwordx4 v6, %[sw], s69 offen offset:1024 lds\n\t" \
  "buffer_load_dwordx4 v7, %[sw], s69 offen offset:2048 lds\n\t" \
  "s_add_u32 s68, %[rx1], 1024\n\t" \
  "s_add_u32 s69, %[rw1], 3145728\n\t" \
  "s_add_u32 m0, %[xb], 36864\n\t" \
  "s_waitcnt vmcnt(15)\n\t" \
  "s_barrier\n\t" \
  "buffer_load_dwordx4 v0, %[sx], s68 offen offset:0 lds\n\t" \
  "s_mov_b32 s85, 0x481000\n\t" \
  "s_load_dword s84, %[pw], s85\n\t" \
  "buffer_load_dwordx4 v1, %[sx], s68 offen offset:1024 lds\n\t" \
  "s_mov_b32 s85, 0x4b0000\n\t" \
  "s_load_dword s84, %[pw], s85\n\t" \
  "s_mov_b64 exec, 0xffff\n\t" \
  "buffer_load_dwordx4 v4, %[sx], s68 offen offset:2048 lds\n\t" \
  "s_add_u32 m0, %[wb], 49152\n\t" \
  "s_mov_b64 exec, -1\n\t" \
  "buffer_load_dwordx4 v5, %[sw], s69 offen offset:0 lds\n\t" \
  "s_mov_b32 s85, 0x4b0800\n\t" \
  "s_load_dword s84, %[pw], s85\n\t" \
  "buffer_load_dwordx4 v6, %[sw], s69 offen offset:1024 lds\n\t" \
  "buffer_load_dwordx4 v7, %[sw], s69 offen offset:2048 lds\n\t" \
  "s_add_u32 s68, %[rx1], 148480\n\t" \
  "s_add_u32 s69, %[rw1], 3342336\n\t" \
  "s_add_u32 m0, %[xb], 46080\n\t" \
  "s_waitcnt vmcnt(18)\n\t" \
  "s_barrier\n\t" \
  "buffer_load_dwordx4 v2, %[sx], s68 offen offset:0 lds\n\t" \
  "s_mov_b32 s85, 0x4b1000\n\t" \
  "s_load_dword s84, %[pw], s85\n\t" \
  "buffer_load_dwordx4 v3, %[sx], s68 offen offset:1024 lds\n\t" \
  "s_mov_b32 s85, 0x4e0000\n\t" \
  "s_load_dword s84, %[pw], s85\n\t" \
  "s_mov_b64 exec, 0xffff\n\t" \
  "buffer_load_dwordx4 v4, %[sx], s68 offen offset:2048 lds\n\t" \
  "s_add_u32 m0, %[wb], 61440\n\t" \
  "s_mov_b64 exec, -1\n\t" \
  "buffer_load_dwordx4 v5, %[sw], s69 offen offset:0 lds\n\t" \
  "s_mov_b32 s85, 0x4e0800\n\t" \
  "s_load_dword s84, %[pw], s85\n\t" \
  "buffer_load_dwordx4 v6, %[sw], s69 offen offset:1024 lds\n\t" \
  "buffer_load_dwordx4 v7, %[sw], s69 offen offset:2048 lds\n\t" \
  "s_add_u32 s68, %[rx2], 1024\n\t" \
  "s_add_u32 s69, %[rw2], 3145728\n\t" \
  "s_add_u32 m0, %[xb], 0\n\t" \
  "s_waitcnt vmcnt(15)\n\t" \
  "s_barrier\n\t" \
  "buffer_load_dwordx4 v0, %[sx], s68 offen offset:0 lds\n\t" \
  "s_mov_b32 s85, 0x4e1000\n\t" \
  "s_load_dword s84, %[pw], s85\n\t" \
  "buffer_load_dwordx4 v1, %[sx], s68 offen offset:1024 lds\n\t" \
  "s_mov_b32 s85, 0x510000\n\t" \
  "s_load_dword s84, %[pw], s85\n\t" \
  "s_mov_b64 exec, 0xffff\n\t" \
  "buffer_load_dwordx4 v4, %[sx], s68 offen offset:2048 lds\n\t" \
  "s_add_u32 m0, %[wb], 0\n\t" \
  "s_mov_b64 exec, -1\n\t" \
  "buffer_load_dwordx4 v5, %[sw], s69 offen offset:0 lds\n\t" \
  "s_mov_b32 s85, 0x510800\n\t" \
  "s_load_dword s84, %[pw], s85\n\t" \
  "buffer_load_dwordx4 v6, %[sw], s69 offen offset:1024 lds\n\t" \
  "buffer_load_dwordx4 v7, %[sw], s69 offen offset:2048 lds\n\t" \
  "s_add_u32 s68, %[rx2], 148480\n\t" \
  "s_add_u32 s69, %[rw2], 3342336\n\t" \
  "s_add_u32 m0, %[xb], 9216\n\t" \
  "s_waitcnt vmcnt(18)\n\t" \
  "s_barrier\n\t" \
  "buffer_load_dwordx4 v2, %[sx], s68 offen offset:0 lds\n\t" \
  "s_mov_b32 s85, 0x511000\n\t" \
  "s_load_dword s84, %[pw], s85\n\t" \
  "buffer_load_dwordx4 v3, %[sx], s68 offen offset:1024 lds\n\t" \
  "s_mov_b32 s85, 0x540000\n\t" \
  "s_load_dword s84, %[pw], s85\n\t" \
  "s_mov_b64 exec, 0xffff\n\t" \
  "buffer_load_dwordx4 v4, %[sx], s68 offen offset:2048 lds\n\t" \
  "s_add_u32 m0, %[wb], 12288\n\t" \
  "s_mov_b64 exec, -1\n\t" \
  "buffer_load_dwordx4 v5, %[sw], s69 offen offset:0 lds\n\t" \
  "s_mov_b32 s85, 0x540800\n\t" \
  "s_load_dword s84, %[pw], s85\n\t" \
  "buffer_load_dwordx4 v6, %[sw], s69 offen offset:1024 lds\n\t" \
  "buffer_load_dwordx4 v7, %[sw], s69 offen offset:2048 lds\n\t" \
  "s_mov_b32 m0, %[tsk]\n\t" \
  "s_mov_b64 exec, %[tm]\n\t" \
  "global_load_lds_dword v12, %[tp]\n\t" \
  "s_mov_b64 exec, -1\n\t" \
  "s_add_u32 s68, %[rx3], 1024\n\t" \
  "s_add_u32 s69, %[rw3], 3145728\n\t" \
  "s_add_u32 m0, %[xb], 18432\n\t" \
  "s_waitcnt vmcnt(15)\n\t" \
  "s_barrier\n\t" \
  "buffer_load_dwordx4 v0, %[sx], s68 offen offset:0 lds\n\t" \
  "s_mov_b32 s85, 0x541000\n\t" \
  "s_load_dword s84, %[pw], s85\n\t" \
  "buffer_load_dwordx4 v1, %[sx], s68 offen offset:1024 lds\n\t" \
  "s_mov_b32 s85, 0x570000\n\t" \
  "s_load_dword s84, %[pw], s85\n\t" \
  "s_mov_b64 exec, 0xffff\n\t" \
  "buffer_load_dwordx4 v4, %[sx], s68 offen offset:2048 lds\n\t" \
  "s_add_u32 m0, %[wb], 24576\n\t" \
  "s_mov_b64 exec, -1\n\t" \
  "buffer_load_dwordx4 v5, %[sw], s69 offen offset:0 lds\n\t" \
  "s_mov_b32 s85, 0x570800\n\t" \
  "s_load_dword s84, %[pw], s85\n\t" \
  "buffer_load_dwordx4 v6, %[sw], s69 offen offset:1024 lds\n\t" \
  "buffer_load_dwordx4 v7, %[sw], s69 offen offset:2048 lds\n\t" \
  "s_add_u32 s68, %[rx3], 148480\n\t" \
  "s_add_u32 s69, %[rw3], 3342336\n\t" \
  "s_add_u32 m0, %[xb], 27648\n\t" \
  "s_waitcnt vmcnt(18)\n\t" \
  "s_barrier\n\t" \
  "buffer_load_dwordx4 v2, %[sx], s68 offen offset:0 lds\n\t" \
  "s_mov_b32 s85, 0x571000\n\t" \
  "s_load_dword s84, %[pw], s85\n\t" \
  "buffer_load_dwordx4 v3, %[sx], s68 offen offset:1024 lds\n\t" \
  "s_mov_b32 s85, 0x5a0000\n\t" \
  "s_load_dword s84, %[pw], s85\n\t" \
  "s_mov_b64 exec, 0xffff\n\t" \
  "buffer_load_dwordx4 v4, %[sx], s68 offen offset:2048 lds\n\t" \
  "s_add_u32 m0, %[wb], 36864\n\t" \
  "s_mov_b64 exec, -1\n\t" \
  "buffer_load_dwordx4 v5, %[sw], s69 offen offset:0 lds\n\t" \
  "s_mov_b32 s85, 0x5a0800\n\t" \
  "s_load_dword s84, %[pw], s85\n\t" \
  "buffer_load_dwordx4 v6, %[sw], s69 offen offset:1024 lds\n\t" \
  "buffer_load_dwordx4 v7, %[sw], s69 offen offset:2048 lds\n\t" \
  "s_add_u32 s68, %[rx0], 1536\n\t" \
  "s_add_u32 s69, %[rw0], 4718592\n\t" \
  "s_add_u32 m0, %[xb], 36864\n\t" \
  "s_waitcnt vmcnt(15)\n\t" \
  "s_barrier\n\t" \
  "buffer_load_dwordx4 v0, %[sx], s68 offen offset:0 lds\n\t" \
  "s_mov_b32 s85, 0x5a1000\n\t" \
  "s_load_dword s84, %[pw], s85\n\t" \
  "buffer_load_dwordx4 v1, %[sx], s68 offen offset:1024 lds\n\t" \
  "s_mov_b32 s85, 0x5d0000\n\t" \
  "s_load_dword s84, %[pw], s85\n\t" \
  "s_mov_b64 exec, 0xffff\n\t" \
  "buffer_load_dwordx4 v4, %[sx], s68 offen offset:2048 lds\n\t" \
  "s_add_u32 m0, %[wb], 49152\n\t" \
  "s_mov_b64 exec, -1\n\t" \
  "buffer_load_dwordx4 v5, %[sw], s69 offen offset:0 lds\n\t" \
  "s_mov_b32 s85, 0x5d0800\n\t" \
  "s_load_dword s84, %[pw], s85\n\t" \
  "buffer_load_dwordx4 v6, %[sw], s69 offen offset:1024 lds\n\t" \
  "buffer_load_dwordx4 v7, %[sw], s69 offen offset:2048 lds\n\t" \
  "s_add_u32 s68, %[rx0], 148992\n\t" \
  "s_add_u32 s69, %[rw0], 4915200\n\t" \
  "s_add_u32 m0, %[xb], 46080\n\t" \
  "s_waitcnt vmcnt(18)\n\t" \
  "s_barrier\n\t" \
  "buffer_load_dwordx4 v2, %[sx], s68 offen offset:0 lds\n\t" \
  "s_mov_b32 s85, 0x5d1000\n\t" \
  "s_load_dword s84, %[pw], s85\n\t" \
  "buffer_load_dwordx4 v3, %[sx], s68 offen offset:1024 lds\n\t" \
  "s_load_dword s84, %[po], 0x0\n\t" \
  "s_mov_b64 exec, 0xffff\n\t" \
  "buffer_load_dwordx4 v4, %[sx], s68 offen offset:2048 lds\n\t" \
  "s_add_u32 m0, %[wb], 61440\n\t" \
  "s_mov_b64 exec, -1\n\t" \
  "buffer_load_dwordx4 v5, %[sw], s69 offen offset:0 lds\n\t" \
  "s_load_dword s84, %[po], 0x40\n\t" \
  "buffer_load_dwordx4 v6, %[sw], s69 offen offset:1024 lds\n\t" \
  "buffer_load_dwordx4 v7, %[sw], s69 offen offset:2048 lds\n\t" \
  "s_add_u32 s68, %[rx1], 1536\n\t" \
  "s_add_u32 s69, %[rw1], 4718592\n\t" \
  "s_add_u32 m0, %[xb], 0\n\t" \
  "s_waitcnt vmcnt(15)\n\t" \
  "s_barrier\n\t" \
  "buffer_load_dwordx4 v0, %[sx], s68 offen offset:0 lds\n\t" \
  "s_load_dword s84, %[po], 0x80\n\t" \
  "buffer_load_dwordx4 v1, %[sx], s68 offen offset:1024 lds\n\t" \
  "s_load_dword s84, %[po], 0xc0\n\t" \
  "s_mov_b64 exec, 0xffff\n\t" \
  "buffer_load_dwordx4 v4, %[sx], s68 offen offset:2048 lds\n\t" \
  "s_add_u32 m0, %[wb], 0\n\t" \
  "s_mov_b64 exec, -1\n\t" \
  "buffer_load_dwordx4 v5, %[sw], s69 offen offset:0 lds\n\t" \
  "s_load_dword s84, %[po], 0x100\n\t" \
  "buffer_load_dwordx4 v6, %[sw], s69 offen offset:1024 lds\n\t" \
  "buffer_load_dwordx4 v7, %[sw], s69 offen offset:2048 lds\n\t" \
  "s_add_u32 s68, %[rx1], 148992\n\t" \
  "s_add_u32 s69, %[rw1], 4915200\n\t" \
  "s_add_u32 m0, %[xb], 9216\n\t" \
  "s_waitcnt vmcnt(18)\n\t" \
  "s_barrier\n\t" \
  "buffer_load_dwordx4 v2, %[sx], s68 offen offset:0 lds\n\t" \
  "s_load_dword s84, %[po], 0x140\n\t" \
  "buffer_load_dwordx4 v3, %[sx], s68 offen offset:1024 lds\n\t" \
  "s_load_dword s84, %[po], 0x180\n\t" \
  "s_mov_b64 exec, 0xffff\n\t" \
  "buffer_load_dwordx4 v4, %[sx], s68 offen offset:2048 lds\n\t" \
  "s_add_u32 m0, %[wb], 12288\n\t" \
  "s_mov_b64 exec, -1\n\t" \
  "buffer_load_dwordx4 v5, %[sw], s69 offen offset:0 lds\n\t" \
  "s_load_dword s84, %[po], 0x1c0\n\t" \
  "buffer_load_dwordx4 v6, %[sw], s69 offen offset:1024 lds\n\t" \
  "buffer_load_dwordx4 v7, %[sw], s69 offen offset:2048 lds\n\t" \
  "s_add_u32 s68, %[rx2], 1536\n\t" \
  "s_add_u32 s69, %[rw2], 4718592\n\t" \
  "s_add_u32 m0, %[xb], 18432\n\t" \
  "s_waitcnt vmcnt(15)\n\t" \
  "s_barrier\n\t" \
  "buffer_load_dwordx4 v0, %[sx], s68 offen offset:0 lds\n\t" \
  "s_load_dword s84, %[po], 0x200\n\t" \
  "buffer_load_dwordx4 v1, %[sx], s68 offen offset:1024 lds\n\t" \
  "s_load_dword s84, %[po], 0x240\n\t" \
  "s_mov_b64 exec, 0xffff\n\t" \
  "buffer_load_dwordx4 v4, %[sx], s68 offen offset:2048 lds\n\t" \
  "s_add_u32 m0, %[wb], 24576\n\t" \
  "s_mov_b64 exec, -1\n\t" \
  "buffer_load_dwordx4 v5, %[sw], s69 offen offset:0 lds\n\t" \
  "s_load_dword s84, %[po], 0x280\n\t" \
  "buffer_load_dwordx4 v6, %[sw], s69 offen offset:1024 lds\n\t" \
  "buffer_load_dwordx4 v7, %[sw], s69 offen offset:2048 lds\n\t" \
  "s_add_u32 s68, %[rx2], 148992\n\t" \
  "s_add_u32 s69, %[rw2], 4915200\n\t" \
  "s_add_u32 m0, %[xb], 27648\n\t" \
  "s_waitcnt vmcnt(18)\n\t" \
  "s_barrier\n\t" \
  "buffer_load_dwordx4 v2, %[sx], s68 offen offset:0 lds\n\t" \
  "s_load_dword s84, %[po], 0x2c0\n\t" \
  "buffer_load_dwordx4 v3, %[sx], s68 offen offset:1024 lds\n\t" \
  "s_load_dword s84, %[po], 0x300\n\t" \
  "s_mov_b64 exec, 0xffff\n\t" \
  "buffer_load_dwordx4 v4, %[sx], s68 offen offset:2048 lds\n\t" \
  "s_add_u32 m0, %[wb], 36864\n\t" \
  "s_mov_b64 exec, -1\n\t" \
  "buffer_load_dwordx4 v5, %[sw], s69 offen offset:0 lds\n\t" \
  "s_load_dword s84, %[po], 0x340\n\t" \
  "buffer_load_dwordx4 v6, %[sw], s69 offen offset:1024 lds\n\t" \
  "buffer_load_dwordx4 v7, %[sw], s69 offen offset:2048 lds\n\t" \
  "s_add_u32 s68, %[rx3], 1536\n\t" \
  "s_add_u32 s69, %[rw3], 4718592\n\t" \
  "s_add_u32 m0, %[xb], 36864\n\t" \
  "s_waitcnt vmcnt(15)\n\t" \
  "s_barrier\n\t" \
  "buffer_load_dwordx4 v0, %[sx], s68 offen offset:0 lds\n\t" \
  "s_load_dword s84, %[po], 0x380\n\t" \
  "buffer_load_dwordx4 v1, %[sx], s68 offen offset:1024 lds\n\t" \
  "s_load_dword s84, %[po], 0x3c0\n\t" \
  "s_mov_b64 exec, 0xffff\n\t" \
  "buffer_load_dwordx4 v4, %[sx], s68 offen offset:2048 lds\n\t" \
  "s_add_u32 m0, %[wb], 49152\n\t" \
  "s_mov_b64 exec, -1\n\t" \
  "buffer_load_dwordx4 v5, %[sw], s69 offen offset:0 lds\n\t" \
  "s_load_dword s84, %[po], 0x400\n\t" \
  "buffer_load_dwordx4 v6, %[sw], s69 offen offset:1024 lds\n\t" \
  "buffer_load_dwordx4 v7, %[sw], s69 offen offset:2048 lds\n\t" \
  "s_add_u32 s68, %[rx3], 148992\n\t" \
  "s_add_u32 s69, %[rw3], 4915200\n\t" \
  "s_add_u32 m0, %[xb], 46080\n\t" \
  "s_waitcnt vmcnt(18)\n\t" \
  "s_barrier\n\t" \
  "buffer_load_dwordx4 v2, %[sx], s68 offen offset:0 lds\n\t" \
  "s_load_dword s84, %[po], 0x440\n\t" \
  "buffer_load_dwordx4 v3, %[sx], s68 offen offset:1024 lds\n\t" \
  "s_load_dword s84, %[po], 0x480\n\t" \
  "s_mov_b64 exec, 0xffff\n\t" \
  "buffer_load_dwordx4 v4, %[sx], s68 offen offset:2048 lds\n\t" \
  "s_add_u32 m0, %[wb], 61440\n\t" \
  "s_mov_b64 exec, -1\n\t" \
  "buffer_load_dwordx4 v5, %[sw], s69 offen offset:0 lds\n\t" \
  "s_load_dword s84, %[po], 0x4c0\n\t" \
  "buffer_load_dwordx4 v6, %[sw], s69 offen offset:1024 lds\n\t" \
  "buffer_load_dwordx4 v7, %[sw], s69 offen offset:2048 lds\n\t" \
  "v_readlane_b32 s68, v9, 0\n\t" \
  "v_readlane_b32 s69, v9, 1\n\t" \
  "v_readlane_b32 s70, v9, 2\n\t" \
  "v_readlane_b32 s71, v9, 18\n\t" \
  "v_readlane_b32 s72, v9, 19\n\t" \
  "s_add_u32 m0, %[ob], 0\n\t" \
  "s_waitcnt vmcnt(15)\n\t" \
  "s_barrier\n\t" \
  "s_nop 2\n\t" \
  "buffer_load_dwordx4 v8, %[so], s68 offen lds\n\t" \
  "s_load_dword s84, %[po], 0x500\n\t" \
  "s_add_u32 m0, %[ob], 4096\n\t" \
  "s_nop 0\n\t" \
  "buffer_load_dwordx4 v8, %[so], s69 offen lds\n\t" \
  "s_load_dword s84, %[po], 0x540\n\t" \
  "s_add_u32 m0, %[ob], 8192\n\t" \
  "s_nop 0\n\t" \
  "buffer_load_dwordx4 v8, %[so], s70 offen lds\n\t" \
  "s_load_dword s84, %[po], 0x580\n\t" \
  "s_add_u32 m0, %[ob], 73728\n\t" \
  "s_nop 0\n\t" \
  "buffer_load_dwordx4 v8, %[so], s71 offen lds\n\t" \
  "s_add_u32 m0, %[ob], 77824\n\t" \
  "s_nop 0\n\t" \
  "buffer_load_dwordx4 v8, %[so], s72 offen lds\n\t" \
  "v_readlane_b32 s68, v9, 3\n\t" \
  "v_readlane_b32 s69, v9, 4\n\t" \
  "v_readlane_b32 s70, v9, 5\n\t" \
  "v_readlane_b32 s71, v9, 20\n\t" \
  "v_readlane_b32 s72, v9, 21\n\t" \
  "s_add_u32 m0, %[ob], 12288\n\t" \
  "s_waitcnt vmcnt(17)\n\t" \
  "s_barrier\n\t" \
  "s_nop 2\n\t" \
  "buffer_load_dwordx4 v8, %[so], s68 offen lds\n\t" \
  "s_load_dword s84, %[po], 0x5c0\n\t" \
  "s_add_u32 m0, %[ob], 16384\n\t" \
  "s_nop 0\n\t" \
  "buffer_load_dwordx4 v8, %[so], s69 offen lds\n\t" \
  "s_load_dword s84, %[po], 0x600\n\t" \
  "s_add_u32 m0, %[ob], 20480\n\t" \
  "s_nop 0\n\t" \
  "buffer_load_dwordx4 v8, %[so], s70 offen lds\n\t" \
  "s_load_dword s84, %[po], 0x640\n\t" \
  "s_add_u32 m0, %[ob], 81920\n\t" \
  "s_nop 0\n\t" \
  "buffer_load_dwordx4 v8, %[so], s71 offen lds\n\t" \
  "s_add_u32 m0, %[ob], 86016\n\t" \
  "s_nop 0\n\t" \
  "buffer_load_dwordx4 v8, %[so], s72 offen lds\n\t" \
  "v_readlane_b32 s68, v9, 6\n\t" \
  "v_readlane_b32 s69, v9, 7\n\t" \
  "v_readlane_b32 s70, v9, 8\n\t" \
  "v_readlane_b32 s71, v9, 22\n\t" \
  "v_readlane_b32 s72, v9, 23\n\t" \
  "v_readlane_b32 s73, v9, 24\n\t" \
  "s_add_u32 m0, %[ob], 24576\n\t" \
  "s_waitcnt vmcnt(13)\n\t" \
  "s_barrier\n\t" \
  "s_nop 2\n\t" \
  "buffer_load_dwordx4 v8, %[so], s68 offen lds\n\t" \
  "s_load_dword s84, %[po], 0x680\n\t" \
  "s_add_u32 m0, %[ob], 28672\n\t" \
  "s_nop 0\n\t" \
  "buffer_load_dwordx4 v8, %[so], s69 offen lds\n\t" \
  "s_load_dword s84, %[po], 0x6c0\n\t" \
  "s_add_u32 m0, %[ob], 32768\n\t" \
  "s_nop 0\n\t" \
  "buffer_load_dwordx4 v8, %[so], s70 offen lds\n\t" \
  "s_load_dword s84, %[po], 0x700\n\t" \
  "s_add_u32 m0, %[ob], 90112\n\t" \
  "s_nop 0\n\t" \
  "buffer_load_dwordx4 v8, %[so], s71 offen lds\n\t" \
  "s_add_u32 m0, %[ob], 94208\n\t" \
  "s_nop 0\n\t" \
  "buffer_load_dwordx4 v8, %[so], s72 offen lds\n\t" \
  "s_add_u32 m0, %[ob], 98304\n\t" \
  "s_nop 0\n\t" \
  "buffer_load_dwordx4 v8, %[so], s73 offen lds\n\t" \
  "v_readlane_b32 s68, v9, 9\n\t" \
  "v_readlane_b32 s69, v9, 10\n\t" \
  "v_readlane_b32 s70, v9, 11\n\t" \
  "v_readlane_b32 s71, v9, 25\n\t" \
  "v_readlane_b32 s72, v9, 26\n\t" \
  "s_add_u32 m0, %[ob], 36864\n\t" \
  "s_waitcnt vmcnt(16)\n\t" \
  "s_barrier\n\t" \
  "s_nop 2\n\t" \
  "buffer_load_dwordx4 v8, %[so], s68 offen lds\n\t" \
  "s_load_dword s84, %[po], 0x740\n\t" \
  "s_add_u32 m0, %[ob], 40960\n\t" \
  "s_nop 0\n\t" \
  "buffer_load_dwordx4 v8, %[so], s69 offen lds\n\t" \
  "s_load_dword s84, %[po], 0x780\n\t" \
  "s_add_u32 m0, %[ob], 45056\n\t" \
  "s_nop 0\n\t" \
  "buffer_load_dwordx4 v8, %[so], s70 offen lds\n\t" \
  "s_load_dword s84, %[po], 0x7c0\n\t" \
  "s_add_u32 m0, %[ob], 102400\n\t" \
  "s_nop 0\n\t" \
  "buffer_load_dwordx4 v8, %[so], s71 offen lds\n\t" \
  "s_add_u32 m0, %[ob], 106496\n\t" \
  "s_nop 0\n\t" \
  "buffer_load_dwordx4 v8, %[so], s72 offen lds\n\t" \
  "v_readlane_b32 s68, v9, 12\n\t" \
  "v_readlane_b32 s69, v9, 13\n\t" \
  "v_readlane_b32 s70, v9, 14\n\t" \
  "v_readlane_b32 s71, v9, 27\n\t" \
  "v_readlane_b32 s72, v9, 28\n\t" \
  "v_readlane_b32 s73, v9, 29\n\t" \
  "s_add_u32 m0, %[ob], 49152\n\t" \
  "s_nop 3\n\t" \
  "s_barrier\n\t" \
  "s_nop 2\n\t" \
  "buffer_load_dwordx4 v8, %[so], s68 offen lds\n\t" \
  "s_add_u32 m0, %[ob], 53248\n\t" \
  "s_nop 0\n\t" \
  "buffer_load_dwordx4 v8, %[so], s69 offen lds\n\t" \
  "s_add_u32 m0, %[ob], 57344\n\t" \
  "s_nop 0\n\t" \
  "buffer_load_dwordx4 v8, %[so], s70 offen lds\n\t" \
  "s_add_u32 m0, %[ob], 110592\n\t" \
  "s_nop 0\n\t" \
  "buffer_load_dwordx4 v8, %[so], s71 offen lds\n\t" \
  "s_add_u32 m0, %[ob], 114688\n\t" \
  "s_nop 0\n\t" \
  "buffer_load_dwordx4 v8, %[so], s72 offen lds\n\t" \
  "s_add_u32 m0, %[ob], 118784\n\t" \
  "s_nop 0\n\t" \
  "buffer_load_dwordx4 v8, %[so], s73 offen lds\n\t" \
  "v_readlane_b32 s68, v9, 15\n\t" \
  "v_readlane_b32 s69, v9, 16\n\t" \
  "v_readlane_b32 s70, v9, 17\n\t" \
  "v_readlane_b32 s71, v9, 30\n\t" \
  "v_readlane_b32 s72, v9, 31\n\t" \
  "s_add_u32 m0, %[ob], 61440\n\t" \
  "LS_%=:\n\t" \
  "ds_read_b32 v11, v10\n\t" \
  "s_addk_i32 %[cn], 1\n\t" \
  "s_cmp_gt_u32 %[cn], 0x400000\n\t" \
  "s_cbranch_scc1 LX_%=\n\t" \
  "s_waitcnt lgkmcnt(0)\n\t" \
  "v_cmp_eq_u32 vcc, 0, v11\n\t" \
  "s_cbranch_vccz LX_%=\n\t" \
  "s_sleep 1\n\t" \
  "s_branch LS_%=\n\t" \
  "LX_%=:\n\t" \
  "s_nop 2\n\t" \
  "buffer_load_dwordx4 v8, %[so], s68 offen lds\n\t" \
  "s_add_u32 m0, %[ob], 65536\n\t" \
  "s_nop 0\n\t" \
  "buffer_load_dwordx4 v8, %[so], s69 offen lds\n\t" \
  "s_add_u32 m0, %[ob], 69632\n\t" \
  "s_nop 0\n\t" \
  "buffer_load_dwordx4 v8, %[so], s70 offen lds\n\t" \
  "s_add_u32 m0, %[ob], 122880\n\t" \
  "s_nop 0\n\t" \
  "buffer_load_dwordx4 v8, %[so], s71 offen lds\n\t" \
  "s_add_u32 m0, %[ob], 126976\n\t" \
  "s_nop 0\n\t" \
  "buffer_load_dwordx4 v8, %[so], s72 offen lds\n\t" \
  "s_waitcnt vmcnt(0) lgkmcnt(0)\n\t"
#define LD_HT 0
#define LD_CLOB "v11", "s68", "s69", "s70", "s71", "s72", "s73", "s74", "s75", "s76", "s77", "s78", "s79", "s80", "s81", "s82", "s83", "s84", "s85"

#define ENDW
#define TS(i)
#define TSS(i)
#define TOUT(n)

#define SPIN(cond,slp) do{ int sp_=0; while(cond){ __builtin_amdgcn_s_sleep(slp); if(++sp_>(1<<22)) break; } }while(0)
__device__ __forceinline__ unsigned rd1(const char* p){ return __builtin_amdgcn_readfirstlane(*(volatile LDS_AS unsigned*)(LDS_AS char*)p); }
__device__ __forceinline__ unsigned lds_inc(const char* p, int l){ unsigned r=0; if(l==0) r=__hip_atomic_fetch_add((LDS_AS unsigned*)(LDS_AS char*)p, 1u, __ATOMIC_RELAXED, __HIP_MEMORY_SCOPE_WORKGROUP); return __builtin_amdgcn_readfirstlane(r); }
__device__ __forceinline__ void lds_add(unsigned adr){ asm volatile("s_mov_b64 exec, 1\n\tds_add_u32 %0, %1\n\ts_mov_b64 exec, -1" :: "v"(adr), "v"(1u) : "memory"); }
__device__ __forceinline__ void bars(int n){
  asm volatile("BL_%=:\n\ts_barrier\n\ts_sub_u32 %0, %0, 1\n\ts_cmp_lg_u32 %0, 0\n\ts_cbranch_scc1 BL_%=" : "+s"(n) :: "memory", "scc");
}
#define barc(rem) barc_(rem)
#define CTRA
#define CTRP
__device__ __forceinline__ void barc_(unsigned& rem){
  asm volatile("s_cmp_eq_u32 %0, 0\n\ts_cbranch_scc1 BC_%=\n\ts_barrier\n\ts_sub_u32 %0, %0, 1\n\tBC_%=:" : "+s"(rem) :: "memory", "scc");
}
template<int SLP> __device__ __forceinline__ void wait_flag_b(const unsigned* p, unsigned voff, unsigned tag, unsigned& rem CTRA){
  int spins=0;
  while(true){
    unsigned v; asm volatile("global_load_dword %0, %1, %2 sc1" : "=v"(v) : "v"(voff), "s"(p) : "memory");
    barc(rem); if(PB2) barc(rem);
    asm volatile("s_waitcnt vmcnt(0)" : "+v"(v) :: "memory");
    if(__all(v==tag) || ++spins>(1<<21)) break;
    if(rem==0) __builtin_amdgcn_s_sleep(SLP);
  }
}
__device__ __forceinline__ void stout(void* sbase, unsigned voff, u32x4 v){
  st16cs(sbase,voff,v);
}

typedef unsigned u32x8 __attribute__((ext_vector_type(8)));
typedef unsigned u32x16 __attribute__((ext_vector_type(16)));
__device__ __forceinline__ char* ptr64(unsigned lo, unsigned hi){ return (char*)(((unsigned long long)hi<<32)|lo); }
__device__ __forceinline__ u32x4 srd(const char* p, unsigned bytes){ return u32x4{(unsigned)(size_t)p,(unsigned)((size_t)p>>32)&0xffffu,bytes,0x20000u}; }
constexpr size_t WS_O=0, WS_KV=(size_t)S*DM*2, WS_Y=(size_t)S*DM*4, WS_FL=(size_t)S*DM*12;

__global__ __launch_bounds__(768,3) void fused_kernel(const u16* x_, const u16* wqkv_, const u16* wo_, char* ws_, u16* out_,
    const u16* kin_, const u16* vin_, const u16* shk_, const u16* hk_, const u16* qin_, const u16* sD_)
{
  __shared__ __attribute__((aligned(128))) char smem[ALL_LDS];
  TS(0);
  const auto ka=__builtin_amdgcn_kernarg_segment_ptr();
  int w8;
  {
    unsigned t=threadIdx.x;
    asm volatile(
      "s_getpc_b64 s[72:73]\n\tv_readfirstlane_b32 %[w8], %[t]\n\ts_lshr_b32 s74, %[b], 3\n\ts_mul_i32 s74, s74, 12\n\ts_lshr_b32 %[w8], %[w8], 6\n\t"
      "s_add_u32 s74, s74, %[w8]\n\ts_min_u32 s74, s74, %[cl]\n\ts_lshl_b32 s74, s74, 7\n\ts_mov_b32 m0, %[sk]\n\tv_mov_b32 v1, s74\n\ts_mov_b64 exec, 1\n\tglobal_load_lds_dword v1, s[72:73]\n\ts_mov_b64 exec, -1\n\t"
      "s_bitcmp1_b32 %[w8], 2\n\ts_cbranch_scc1 EX_%=\n\t"
      "s_load_dwordx4 s[68:71], %[ka], 0x0\n\tv_and_b32 v1, 0xff, %[t]\n\ts_bitcmp1_b32 %[w8], 3\n\ts_cbranch_scc1 EC_%=\n\t"
      "s_mul_i32 s74, %[b], %[nw]\n\tv_cmp_gt_u32 vcc, %[nw], v1\n\tv_add_lshl_u32 v1, v1, s74, 7\n\ts_and_b64 exec, vcc, exec\n\ts_cbranch_execz EE_%=\n\ts_waitcnt lgkmcnt(0)\n\tglobal_load_lds_dword v1, s[70:71]\n\ts_branch EE_%=\n\t"
      "EC_%=:\n\tv_cmp_gt_u32 vcc, %[nx], v1\n\tv_and_b32 v2, 7, v1\n\tv_lshlrev_b32 v1, 4, v1\n\t"
      "s_lshl_b32 s74, %[b], 19\n\ts_lshl_b32 s73, %[b], 11\n\ts_or_b32 s74, s74, s73\n\ts_and_b32 s74, s74, 0x3fc000\n\t"
      "v_and_b32 v1, 0xf80, v1\n\tv_lshl_add_u32 v1, v2, 11, v1\n\tv_add_u32 v1, s74, v1\n\t"
      "s_and_b64 exec, vcc, exec\n\ts_cbranch_execz EE_%=\n\ts_waitcnt lgkmcnt(0)\n\tglobal_load_lds_dword v1, s[68:69]\n\t"
      "EE_%=:\n\ts_mov_b64 exec, -1\n\ts_waitcnt lgkmcnt(0)\n\tEX_%=:"
      : [w8]"=&s"(w8) : [ka]"s"(ka), [t]"v"(t), [b]"s"(blockIdx.x), [cl]"n"(CODEL-1), [sk]"s"(lds_addr(smem)+SINK+256), [nw]"s"(12*TPW), [nx]"s"(8*TPX)
      : "memory", "scc", "vcc", "v1", "v2", "s68", "s69", "s70", "s71", "s72", "s73", "s74");
  }
  #define ROLE u32x8 a0; int bid=blockIdx.x; asm volatile("s_load_dwordx8 %0, %2, 0x0" : "=&s"(a0), "+s"(bid) : "s"(ka)); \
    const int w=w8&3, xcd=bid&7, idx=bid>>3, rb=(xcd>>1)*4+(idx&3), hp=(xcd&1)*8+(idx>>2), s0=rb*128; (void)s0; (void)hp;
  #define LANEVARS int l; asm volatile("v_mbcnt_lo_u32_b32 %0, -1, 0\n\tv_mbcnt_hi_u32_b32 %0, -1, %0" : "=v"(l)); const int g=l>>4, fr=l&15, tq=(l&15)>>2, tp=l&3; (void)g; (void)fr; (void)tq; (void)tp;
  #define ARGS0 asm volatile("s_waitcnt lgkmcnt(0)" : "+s"(a0)); const char* x=ptr64(a0[0],a0[1]); const char* wqkv=ptr64(a0[2],a0[3]); const char* wo=ptr64(a0[4],a0[5]); char* ws=ptr64(a0[6],a0[7]); \
    unsigned* flags=(unsigned*)(ws+WS_FL); const int hot0=(3-(int)(a0[2]>>7))&7; (void)x; (void)wqkv; (void)wo; (void)flags; (void)hot0;
  #define ORA ((unsigned)(hp-hot0-1)&7u)
  #define ORD(c) ((((ORA==7u? ((c)==0u||(c)==15u? 7u : ((c)-1u)>>1) : ((c)<14u? (ORA+((c)>>1))%7u : 7u))+hot0+1)&7u)+8u*(((hp>>3)^(c))&1u))
  const unsigned sb0=__builtin_amdgcn_readfirstlane(lds_addr(smem));
  const char* syn=smem+SYN;

  if(__builtin_expect((w8>>2)==1,1)){
    ROLE
    LANEVARS
    __builtin_amdgcn_s_setprio(LPR);
    unsigned ltr=0, ltr2=0, ltr3=0; (void)ltr; (void)ltr2; (void)ltr3;
    const int xr=18*w+(l>>3);
    const unsigned ch=(unsigned)(((l&7)^(xr&7))*16);
    unsigned xo=(unsigned)(xr*2048)+ch;
    unsigned xoA=(s0==0 && xr<16)? 16*2048+ch : xo, xoB=((s0==0 && xr+8<16)? 8*2048+ch : xo)+15360;
    unsigned xo1=xo+15360, xo2=xo+30720;
    unsigned wof[3];
    #pragma unroll
    for(int i=0;i<3;i++){ const int P=3*w+i, wr=8*(P&3)+(l>>3); const int ci=((l&7)>>1)^wswz(wr); wof[i]=(unsigned)(wr*6144+(P>>2)*2048-1024*i+(MIX? (ci>>1)*128+(ci&1)*32 : ci*32)+(l&1)*16); }
    unsigned xl=sb0+X_OFF+w*2304, wl=sb0+w*3072, xd=(unsigned)(s0-16)*2048u, wd=hp*128, hq=hp&7, rq=rb&3;
    asm volatile("" : "+{v0}"(xoA), "+{v1}"(xoB), "+{v2}"(xo), "+{v3}"(xo1), "+{v4}"(xo2), "+{v5}"(wof[0]), "+{v6}"(wof[1]), "+{v7}"(wof[2]), "+s"(xl), "+s"(wl), "+s"(xd), "+s"(wd), "+s"(hq), "+s"(rq));
    TSS(2);
    ARGS0
    const unsigned wm=MIX? (wd&~255u)+((wd>>1)&64u) : wd;
    const u32x4 sx=srd(x+(int)xd,144u*2048u), sw=srd(wqkv+wm,6291456u-wm);
    const int jj=(ROT && (MIX? ((int)hq|1)==(hot0|1) : (int)hq==hot0))? rq : 0;
    TSS(3);
    asm volatile(LD_ASM1
      :: "{v0}"(xoA), "{v1}"(xoB), "{v2}"(xo), "{v3}"(xo1), "{v4}"(xo2), "{v5}"(wof[0]), "{v6}"(wof[1]), "{v7}"(wof[2]),
        [sx]"s"(sx), [sw]"s"(sw), [xb]"s"(xl), [wb]"s"(wl), [rx0]"s"(jj*128), [rw0]"s"(jj*393216)
      : "memory", "scc", "s68");
    {
      const unsigned wof2=(unsigned)(w*16384+(l>>3)*2048+((((l&7)>>1)^((l>>4)&3))*32)+(l&1)*16);
      const unsigned to=ORD((unsigned)(l>>1)&15u);
      const unsigned tab=MIX? (to&~1u)*131072u+(l&1)*131072u+(to&1u)*65536u : to*131072u+(l&1)*65536;
      unsigned long long tm; asm("s_cmp_eq_u32 %1, 0\n\ts_cselect_b64 %0, %2, 0" : "=s"(tm) : "s"(w), "s"(WTL==2? 0xffffffffull : 0ull) : "scc");
      unsigned cn=0;
      const unsigned hn=(unsigned)(w*(3*LD_HT/4)+l), hto=(hn/6u)*393216u+(hn%6u)*2048u;
      asm volatile(LD_ASM2
        : [cn]"+&s"(cn)
        : "{v0}"(xoA), "{v1}"(xoB), "{v2}"(xo), "{v3}"(xo1), "{v4}"(xo2), "{v5}"(wof[0]), "{v6}"(wof[1]), "{v7}"(wof[2]), "{v8}"(wof2), "{v9}"(tab),
          "{v10}"(sb0+SYN+32+(l&3)*4), "{v12}"((l&31)*2048),
          [sx]"s"(sx), [sw]"s"(sw), [so]"s"(srd(wo+wd,2097152u-wd)), [xb]"s"(xl), [wb]"s"(wl), [ob]"s"(sb0+w*1024),
          [rx0]"s"(jj*128), [rx1]"s"(((jj+1)&3)*128), [rx2]"s"(((jj+2)&3)*128), [rx3]"s"(((jj+3)&3)*128),
          [rw0]"s"(jj*393216), [rw1]"s"(((jj+1)&3)*393216), [rw2]"s"(((jj+2)&3)*393216), [rw3]"s"(((jj+3)&3)*393216),
          [pw]"s"(wqkv+idx*6144+(xcd&1)*1024+(xcd>>1)*256+w*64), [px]"s"(x+(xcd*256+idx*8+w*2)*2048), [po]"s"(wo+(bid*4+w)*2048),
          [tsk]"s"(sb0+SINK+256), [tm]"s"(tm), [tp]"s"(wo+idx*65536+(xcd&1)*1024+((3-(int)(a0[4]>>7))&7)*128)
        : "memory", "scc", "vcc", LD_CLOB);
    }
    const unsigned tag=rd1(syn+64)+1u;
    lds_add(sb0+SYN+12);
    TS(2);
    if(w==3){
      const unsigned* fp=flags+F_O+(rb*16+hp)*16; const unsigned fo=ORD((unsigned)l&15u)*4u;
      const unsigned madr=sb0+(l==0? SYN+20 : SINK+l*4);
      SPIN(rd1(syn+12)<4u || rd1(syn+16)<4u,1);
      PUB(madr,1u);
      unsigned rdy=1; int spins=0;
      while(rdy!=0xFFFFu){
        unsigned v;
        asm volatile("s_mov_b64 exec, 0xffff\n\tglobal_atomic_or %0, %1, %3, %2 sc0 sc1\n\ts_mov_b64 exec, -1\n\ts_waitcnt vmcnt(0)" : "=&v"(v) : "v"(fo), "s"(fp), "v"(0u) : "memory");
        unsigned r=((unsigned)__ballot(v==tag)&0xFFFFu)|1u;
        if(++spins>(1<<21)) r=0xFFFFu;
        if(r!=rdy){ PUB(madr,(unsigned)__builtin_ctz(~r)); rdy=r; }
        else __builtin_amdgcn_s_sleep(OSLP);
      }
      TS(3);
      if(bid==0){
        int sp=0;
        while(true){
          u32x4 v; asm volatile("global_load_dwordx4 %0, %1, %2 sc1\n\ts_waitcnt vmcnt(0)" : "=&v"(v) : "v"(l*16), "s"(flags+F_Y) : "memory");
          if(__all(v[0]==tag && v[1]==tag && v[2]==tag && v[3]==tag) || ++sp>(1<<20)) break;
          __builtin_amdgcn_s_sleep(12);
        }
        if(l==0) stflag(flags, tag);
      }
    } else { TS(3); }
    TOUT(4);
    return;
  }

  if(w8<4){
    ROLE
    u32x2 a1; asm volatile("s_load_dwordx2 %0, %1, 0x20" : "=&s"(a1) : "s"(ka));
    __builtin_amdgcn_s_setprio(GPR);
    const int h=w&1, r=w>>1;
    f32x16 e0, e1;
    LANEVARS
    TSS(3);
    ARGS0
    asm volatile("" : "+s"(a1));
    if(PGT&1){ if(w==1 && l<7) touch(ws, l<6? l*2097152+PGO : (unsigned)WS_FL+2048, sb0+SINK+512); }
    if(PGT&2){ if(w==2 && l<4) touch(ptr64(a1[0],a1[1]), l*2097152+PGO, sb0+SINK+512); }
    if(PGT&4){ if(w==3 && l<1) touch(wo, PGO, sb0+SINK+512); if(w==0 && l<2) touch(wqkv, l*2097152+2097152, sb0+SINK+512); }
    TSS(4);
    {
      unsigned lx0=sb0+X_OFF+fr*128+((g^(fr&7))*16), lx1=sb0+X_OFF+fr*128+(((4+g)^(fr&7))*16);
      const int sw=wswz(8*g+tq);
      unsigned lw0=sb0+(8*g+tq)*128+(unsigned)((((2*h)^sw)*32)+8*tp), lw1=sb0+(8*g+tq)*128+(unsigned)((((2*h+1)^sw)*32)+8*tp);
      char* ot=ws+WS_O+(rb*16+hp)*16384+r*8192+h*2048;
      unsigned it; unsigned long long rm; asm("s_cmp_lg_u32 %1, 0\n\ts_cselect_b64 %0, -1, 0" : "=s"(rm) : "s"(r) : "scc");
      asm volatile(N2_ASM
        : [it]"=&s"(it), "+{v56}"(lx0), "+{v57}"(lx1), "+{v58}"(lw0), "+{v59}"(lw1)
        : "{v62}"(l*16), "{v63}"(sb0+EX_OFF+h*3072+l*16), [r]"s"(r), [ot]"s"(ot), [ou]"s"(ot+4096), [fa]"s"(sb0+SYN+32+w*4), [fp]"s"(sb0+SYN+32+(w^2)*4),
          [vo]"s"(2048-1024*r), [rm]"s"(rm), [z]"s"(rb|r)
        : "memory", "scc", "vcc", N2_CLOB);
    }
    TS(3);
    unsigned tb;
    {
      LANEVARS
      const unsigned tag=rd1(syn+64)+1u;
      const unsigned ra=sb0+(4*g+tq)*128+8*tp; const int sw=(2*g+(tq>>1))&3;
      unsigned b0=ra+((0^sw)*32), b1=ra+((1^sw)*32), b2=ra+((2^sw)*32), b3=ra+((3^sw)*32), vo=(unsigned)(w*4096+l*16);
      tb=ORD((unsigned)l&15u)*16384u;
      asm volatile("" : "+{v160}"(b0), "+{v161}"(b1), "+{v162}"(b2), "+{v163}"(b3), "+{v164}"(vo), "+{v167}"(tb));
      WAITV(0);
      if(lds_inc(syn+16,l)==3u){ if(l<16) stflag(flags+F_O+(rb*16+l)*16+hp, tag); }
      TS(4);
      __builtin_amdgcn_s_setprio(G2P);
      unsigned c=0, iss=0, th, q0, q1, cn=0;
      asm volatile(G3_ASM
        : "={v[128:143]}"(e0), "={v[144:159]}"(e1), [c]"+s"(c), [iss]"+s"(iss), [th]"=&s"(th), [t0]"=&s"(q0), [t1]"=&s"(q1), [cn]"+&s"(cn),
          "+{v160}"(b0), "+{v161}"(b1), "+{v162}"(b2), "+{v163}"(b3), "+{v167}"(tb)
        : "{v164}"(vo), "{v165}"(sb0+SYN+20), [go]"s"(60+w*4), [ob]"s"(srd(ws+WS_O+rb*262144,262144u))
        : "memory", "scc", "vcc", G3_CLOB);
    }
    {
      LANEVARS
      char* sg=smem+EX_OFF+w*2048;
      char* op=ptr64(a1[0],a1[1])+(s0+w*32)*4096+2048+hp*128; const unsigned oo=(unsigned)((l>>3)*4096+(l&7)*16);
      u32x4 ov[4];
      #pragma unroll
      for(int t=0;t<2;t++){
        #pragma unroll
        for(int j=0;j<4;j++){
          #define E2(i) (t? e1[(i)&15] : e0[(i)&15])
          *(volatile LDS_AS u32x2*)(LDS_AS char*)(sg+fr*128+(((2*j+(g>>1))^(fr&7))*16)+(g&1)*8)=u32x2{pack2(E2(4*j),E2(4*j+1)),pack2(E2(4*j+2),E2(4*j+3))};
        }
        #pragma unroll
        for(int i=0;i<2;i++) ov[2*t+i]=*(volatile LDS_AS u32x4*)(LDS_AS char*)(sg+(i*8+(l>>3))*128+(((l&7)^(l>>3))*16));
      }
      #pragma unroll
      for(int i=0;i<4;i++) stout(op+i*32768, oo, ov[i]);
      ENDW TS(6); TOUT(7);
    }
    return;
  }

  {
    ROLE
    u32x8 a1; u32x4 a2; u32x2 a3;
    asm volatile("s_load_dwordx8 %0, %3, 0x20\n\ts_load_dwordx4 %1, %3, 0x40\n\ts_load_dwordx2 %2, %3, 0x50" : "=&s"(a1), "=&s"(a2), "=&s"(a3) : "s"(ka));
    char* cr=smem+C_OFF+2048+w*4096;
    const unsigned crs=sb0+C_OFF+2048+w*4096;
    const int sblk=bid>>3, td0=(bid&7)*128+(w&1)*64;
    const int vb=(LOCA&1)? xcd*32+(idx&16)+(idx&1)*8+((idx>>1)&7) : bid, fb=(LOCA&2)? (idx&15)*16+xcd*2+(idx>>4) : bid;
    const int d=(vb>>4)*64+(((vb>>3)&1)*4+w)*8+(vb&7);
    const int kv0=KV0+(sblk&KVS);
    static_assert(KV0+KVS+PP_NB<=33, "barrier budget");
    unsigned tagv, ctr; f32x8 RB;
    const int fd0=(fb&15)*64, fs0=(fb>>4)*128+w*32;
    u32x4 qv[4];
    f32x16 A0,A1,A2,A3;
    const int rw=sblk*64+(w>>1)*32, j=rw>>5;
    unsigned k0,k1,k2,ko,ta,se,so,ha,l16,rb0[2];
    {
      LANEVARS
      if(w==0 && l<25 && l!=16) ((unsigned*)(smem+SYN))[l]=0u;
      if(l<32) *(u32x4*)(smem+C_OFF+w*512+l*16)=u32x4{0,0,0,0};
      asm volatile("" ::: "memory");
      __builtin_amdgcn_s_setprio(CPR);
      const int dg=l&7, rg=l>>3;
      ko=(unsigned)(rg*8192+dg*16);
      const bool top=(rw==0 && rg==0);
      k0=top? ko+6144:ko; k1=top? ko+6144:ko+2048; k2=top? ko+6144:ko+4096;
      ta=sb0+EX_OFF+w*1024+(w&1)*512+dg*64; se=(l&8)? 0x03020706u : 0x05040100u; ha=crs+(32*g-4*tq+4*tp)*2; l16=l*16;
      so=(unsigned)(rg*32768+((j>>2)&3)*128+((dg&4)|((dg+j)&3))*16);
      #pragma unroll
      for(int k=0;k<2;k++){ const int q=fr-1-2*g+k; rb0[k]=sb0+C_OFF+(4+(q>>1))*256+((q>>3)&3)*64+((w+(q>>1))&3)*16+(q&1)*8; }
      asm volatile("" : "+{v152}"(k0), "+{v153}"(k1), "+{v154}"(k2), "+{v155}"(ko), "+{v156}"(ta), "+{v157}"(se), "+{v159}"(so), "+{v163}"(l16), "+{v165}"(ha), "+{v166}"(rb0[0]), "+{v167}"(rb0[1]));
    }
    TSS(3);
    ARGS0
    asm volatile("" : "+s"(a1), "+s"(a2), "+s"(a3));
    char* out=ptr64(a1[0],a1[1]); const char* qp=ptr64(a2[2],a2[3])+(fs0*DM+fd0)*2;
    {
      const char* kb=ptr64(a1[2],a1[3])+(rw*DM+td0)*2; const char* vb=ptr64(a1[4],a1[5])+(rw*DM+td0)*2;
      unsigned long long zm; asm("s_cmp_eq_u32 %1, 0\n\ts_cselect_b64 %0, 0xff, 0" : "=s"(zm) : "s"(rw) : "scc");
      unsigned t0;
      if(PGT&8){ const char* tp= w==0? kb : w==1? vb : w==2? qp : ptr64(a2[0],a2[1])+d*4096;
        asm volatile("s_mov_b32 m0, %1\n\ts_mov_b64 exec, 1\n\tglobal_load_lds_dword v163, %0\n\ts_mov_b64 exec, -1" :: "s"(tp), "s"(sb0+SINK+512), "{v163}"(l16) : "memory"); }
      TSS(5);
      asm volatile(PP_ASM
        : [t0]"=&s"(t0), "=&{v164}"(ctr), "+{v159}"(so), "=&{v160}"(tagv), "=&{v[24:31]}"(RB), "=&{v[44:47]}"(qv[0]), "=&{v[48:51]}"(qv[1]), "=&{v[52:55]}"(qv[2]), "=&{v[56:59]}"(qv[3]),
          "=&{v[64:79]}"(A0), "=&{v[80:95]}"(A1), "=&{v[96:111]}"(A2), "=&{v[112:127]}"(A3)
        : "{v152}"(k0), "{v153}"(k1), "{v154}"(k2), "{v155}"(ko), "{v156}"(ta), "{v157}"(se), "{v166}"(rb0[0]), "{v167}"(rb0[1]), "{v165}"(ha), "{v163}"(l16),
          [qb0]"s"(qp), [qb2]"s"(qp+4096), [mk]"s"(0x180u), [em]"s"(1ull|(512ull<<w)), [hd]"s"(crs),
          [kb0]"s"(kb-6144), [kb3]"s"(kb), [kb5]"s"(kb+4096), [vb0]"s"(vb), [vb2]"s"(vb+4096), [sb]"s"(ws+WS_KV+td0*4096+j*512),
          [n0]"s"(kv0), [ff]"s"(flags+F_PP+(td0>>6)*64+j), [zm]"s"(zm), [shd]"s"(sb0+EX_OFF+w*1024), [tgd]"s"(sb0+SYN+64), [shp]"s"(ptr64(a1[6],a1[7])+(bid&7)*1024), [fl]"s"(flags),
          [hp]"s"(ptr64(a2[0],a2[1])+d*4096), [dp]"s"(ptr64(a3[0],a3[1])+d*2)
        : "memory", "scc", PP_CLOB);
    }
    LANEVARS
    const unsigned tag=__builtin_amdgcn_readfirstlane(tagv)+1u;
    unsigned rem=33-PP_NB-kv0;
    barc(rem);
    const unsigned* pf=flags+F_PP+(d>>6)*64;
    const char* kvp=ws+WS_KV+((vb>>4)*8+(vb&7))*32768+((vb>>3)&1)*64+w*8192;
    const unsigned kvo=(unsigned)((l>>2)*128+(l&3)*16);
    int bidx=((l>=16)? l-16 : (l==0? 63 : l+47))*4;
    asm volatile("" : "+{v32}"(bidx));
    __builtin_amdgcn_s_setprio(3);
    while(rem>PREM) barc(rem);
    wait_flag_b<8>(pf, l*4, tag, rem CTRP);
    #pragma unroll
    for(int i=0;i<4;i++){ if(i%KTH==0 && i) barc(rem); gldsc(kvp+i*2048, kvo, crs+i*1024); }
    barc(rem); barc(rem);
    WAITV(0);
    asm volatile("s_mov_b32 m0, %2\n\ts_mov_b64 exec, 1\n\tglobal_load_lds_dword %0, %1 sc1\n\ts_mov_b64 exec, -1" :: "v"(0u), "s"(pf), "s"(sb0+SYN+100+w*4) : "memory");
    while(rem) barc(rem);
    { int sp=0; while(!__all(*(volatile LDS_AS unsigned*)(LDS_AS char*)(syn+100+(l&3)*4)==tag) && ++sp<(1<<22)) __builtin_amdgcn_s_sleep(1); }
    __builtin_amdgcn_s_setprio(CVP);
    TS(3);
    f32x16 ac0, ac1;
    {
      unsigned p0,p1,p2,p3,p4,p5,p6;
      asm volatile("s_nop 1\n\t" CV_ASM
        : "=&{v[128:143]}"(ac0), "=&{v[144:159]}"(ac1), [p0]"=&s"(p0), [p1]"=&s"(p1), [p2]"=&s"(p2), [p3]"=&s"(p3), [p4]"=&s"(p4), [p5]"=&s"(p5), [p6]"=&s"(p6)
          , "+{v[44:47]}"(qv[0]), "+{v[48:51]}"(qv[1]), "+{v[52:55]}"(qv[2]), "+{v[56:59]}"(qv[3])
        : "{v[64:79]}"(A0), "{v[80:95]}"(A1), "{v[96:111]}"(A2), "{v[112:127]}"(A3),
          "{v[24:31]}"(RB), "{v32}"(bidx), "{v33}"(sb0+SYN+80+w*4)
        : "memory", "scc", CV_CLOB);
    }
    TS(4);
    __builtin_amdgcn_s_setprio(3);
    {
      const unsigned yo=(unsigned)((256*(g&1)+16*fr+4*(g&2))*2);
      #pragma unroll
      for(int J=0;J<8;J+=2){
        #define YA(i) ((i)<16? ac0[(i)&15] : ac1[(i)&15])
        const u32x2 a={pack2(YA(4*J),YA(4*J+1)),pack2(YA(4*J+2),YA(4*J+3))}, b={pack2(YA(4*J+4),YA(4*J+5)),pack2(YA(4*J+6),YA(4*J+7))};
        st16cs(ws+WS_Y+d*4096+512*J, yo, widen(a,b));
      }
    }
    WAITV(0);
    if(lds_inc(syn+24,l)==3u){ if(l<16) stflag(flags+F_Y+(l*16+(vb>>4))*16+(vb&15), tag); }
    TS(5);
    if(w==0){
      SPIN(rd1(syn+96)==0u,2);
      wait_flag<YSLP>(flags+F_Y+fb*16+(l&15), tag);
      SPIN(rd1(syn+24)<4u,1);
      PUB(sb0+(l==0? SYN+28 : SINK+l*4), 1u);
    } else SPIN(rd1(syn+28)==0u,4);
    #pragma unroll
    for(int i=0;i<4;i++) gldsc(ws+WS_Y+(fd0+i*16)*4096+fs0*2, (l>>2)*4096+(l&3)*16, crs+i*1024);
    WAITV(0);
    {
      u32x4 y2[4];
      #pragma unroll
      for(int i=0;i<4;i++){ const char* yp=cr+((i>>1)*32+g*8+tq)*64+(i&1)*32+tp*8; y2[i]=__builtin_bit_cast(u32x4,cat44(trread(yp),trread(yp+256))); }
      asm volatile("" : "+v"(y2[0]), "+v"(y2[1]), "+v"(y2[2]), "+v"(y2[3]) :: "memory");
      #pragma unroll
      for(int i=0;i<4;i++) *(u32x4*)(cr+((i&1)*16+fr)*128+((((i>>1)*4+g)^(fr&7))*16))=y2[i];
      asm volatile("" ::: "memory");
      char* op=out+fs0*4096+fd0*2; const unsigned oo=(unsigned)((l>>3)*16384+(l&7)*16);
      #pragma unroll
      for(int i=0;i<4;i++){
        const int row=4*(l>>3)+i;
        const u32x4 y4=*(const u32x4*)(cr+row*128+(((l&7)^(row&7))*16)), q4=qv[i]; u32x4 ov;
        #pragma unroll
        for(int e=0;e<4;e++) ov[e]=pack2(bflo(q4[e])*bflo(y4[e]), bfhi(q4[e])*bfhi(y4[e]));
        stout(op+i*4096, oo, ov);
      }
    }
    ENDW TS(6); TOUT(7);
  }
}

extern "C" void kernel_launch(void* const* d_in, const int* in_sizes, int n_in, void* d_out, int out_size, void* d_ws, size_t ws_size, hipStream_t stream){
  const u16* q=(const u16*)d_in[0]; const u16* k=(const u16*)d_in[1]; const u16* v=(const u16*)d_in[2];
  const u16* shk=(const u16*)d_in[3]; const u16* hk=(const u16*)d_in[4]; const u16* sD=(const u16*)d_in[5];
  const u16* x=(const u16*)d_in[6]; const u16* wqkv=(const u16*)d_in[7]; const u16* wo=(const u16*)d_in[8];
  fused_kernel<<<256,768,0,stream>>>(x,wqkv,wo,(char*)d_ws,(u16*)d_out,k,v,shk,hk,q,sD);
}
```

```cpp
#include <hip/hip_runtime.h>
#include <hip/hip_bf16.h>
#include <stdint.h>

typedef unsigned short u16;
typedef short bf16x8 __attribute__((ext_vector_type(8)));
typedef short s16x4 __attribute__((ext_vector_type(4)));
typedef float f32x2 __attribute__((ext_vector_type(2)));
typedef float f32x4 __attribute__((ext_vector_type(4)));
typedef float f32x8 __attribute__((ext_vector_type(8)));
typedef float f32x16 __attribute__((ext_vector_type(16)));
typedef unsigned u32x4 __attribute__((ext_vector_type(4)));
typedef unsigned u32x2 __attribute__((ext_vector_type(2)));
typedef __bf16 bf16x2_t __attribute__((ext_vector_type(2)));
#define LDS_AS __attribute__((address_space(3)))

__device__ __forceinline__ float bflo(unsigned x){ return __uint_as_float(x<<16); }
__device__ __forceinline__ float bfhi(unsigned x){ return __uint_as_float(x&0xffff0000u); }
__device__ __forceinline__ unsigned pack2(float lo, float hi){ return __builtin_bit_cast(unsigned, __builtin_convertvector(f32x2{lo,hi}, bf16x2_t)); }
__device__ __forceinline__ float mul1(float a, float b){ float r; asm("v_mul_f32 %0, %1, %2" : "=v"(r) : "v"(a), "v"(b)); return r; }
__device__ __forceinline__ float fma1(float a, float b, float c){ float r; asm("v_fma_f32 %0, %1, %2, %3" : "=v"(r) : "v"(a), "v"(b), "v"(c)); return r; }

__device__ __forceinline__ void glds(const void* sbase, unsigned voff, unsigned ldsaddr){
  asm volatile("s_mov_b32 m0, %2\n\ts_nop 0\n\tglobal_load_lds_dwordx4 %0, %1" :: "v"(voff), "s"(sbase), "s"(ldsaddr) : "memory");
}
__device__ __forceinline__ void gldsc(const void* sbase, unsigned voff, unsigned ldsaddr){
  asm volatile("s_mov_b32 m0, %2\n\ts_nop 0\n\tglobal_load_lds_dwordx4 %0, %1 sc1" :: "v"(voff), "s"(sbase), "s"(ldsaddr) : "memory");
}
__device__ __forceinline__ void touch(const void* sbase, unsigned voff, unsigned ldsaddr){
  asm volatile("s_mov_b32 m0, %2\n\ts_nop 0\n\tglobal_load_lds_dword %0, %1" :: "v"(voff), "s"(sbase), "s"(ldsaddr) : "memory");
}
__device__ __forceinline__ void st16c(void* p, u32x4 v){ asm volatile("global_store_dwordx4 %0, %1, off sc1\n\ts_nop 1" :: "v"(p), "v"(v) : "memory"); }
__device__ __forceinline__ void st16cs(void* sbase, unsigned voff, u32x4 v){ asm volatile("global_store_dwordx4 %0, %1, %2 sc1\n\ts_nop 1" :: "v"(voff), "v"(v), "s"(sbase) : "memory"); }
#define GLB_AS __attribute__((address_space(1)))
__device__ __forceinline__ unsigned ldflag(const unsigned* p){ return __hip_atomic_load((const GLB_AS unsigned*)p, __ATOMIC_RELAXED, __HIP_MEMORY_SCOPE_AGENT); }
__device__ __forceinline__ void stflag(unsigned* p, unsigned v){ __hip_atomic_store((GLB_AS unsigned*)p, v, __ATOMIC_RELAXED, __HIP_MEMORY_SCOPE_AGENT); }
template<int SLP> __device__ __forceinline__ void wait_flag(const unsigned* p, unsigned tag){
  int spins=0;
  while(true){ unsigned v=ldflag(p); if(__all(v==tag)) break; __builtin_amdgcn_s_sleep(SLP); if(++spins>(1<<21)) break; }
}
__device__ __forceinline__ unsigned lds_addr(const void* p){ return (unsigned)(size_t)(const LDS_AS void*)p; }
#define WAITV(n) asm volatile("s_waitcnt vmcnt(" #n ")":::"memory")
#define WAITL() asm volatile("s_waitcnt lgkmcnt(0)":::"memory")
#define SBAR() asm volatile("s_barrier":::"memory")
#define PUBM(addr,val) asm volatile("ds_max_u32 %0, %1" :: "v"(addr), "v"(val) : "memory")
#define PUB(addr,val) asm volatile("ds_write_b32 %0, %1" :: "v"(addr), "v"(val) : "memory")
__device__ __forceinline__ s16x4 trread(const char* p){ return __builtin_amdgcn_ds_read_tr16_b64_v4i16((LDS_AS s16x4*)p); }
__device__ __forceinline__ bf16x8 cat44(s16x4 a, s16x4 b){ return __builtin_shufflevector(a,b,0,1,2,3,4,5,6,7); }
__device__ __forceinline__ int wswz(int row){ return ((row>>1)&1) | (((row>>3)&1)<<1); }
__device__ __forceinline__ u32x4 widen(u32x2 a, u32x2 b){
  auto r0=__builtin_amdgcn_permlane16_swap(a[0],b[0],false,false);
  auto r1=__builtin_amdgcn_permlane16_swap(a[1],b[1],false,false);
  return u32x4{r0[0],r1[0],r0[1],r1[1]};
}

constexpr int S=2048, DM=1024;
constexpr int WS=12288, XS=18432, X_OFF=6*WS;
constexpr int CREG=4608, C_OFF=131072;
constexpr int EX_OFF=C_OFF+4*CREG;
constexpr int SYN=EX_OFF+8192;
constexpr int SINK=SYN+256;
constexpr int CAR=SINK+768;
constexpr int ALL_LDS=CAR+4*1280;
constexpr int F_PP=1024, F_O=4096, F_Y=8192;
#define KV0 1
#define KVS 1
#define LPR 2
#define CPR 2
#define GPR 1
#define CGATE 2
#define OSLP 2
#define YSLP 2
#define TPX 16
#define TPW 2
#define TWO 0
#define CVP 1
#define G2P 3
#define OUTC 1
#define ROT 0
#define WTL 2
#define PB2 0
#define PREM 4
#define KTH 4
#define YDEF 0
#define OPOL 1
#define CODEL 262
#define MIX 0
#define LOCA 3
#define XLOC 1
#define PGT 0
#define PGO 0
#define QREM 0
#define EB 0
#define TW0 1
#define ORDM 1
#define NF 7u
#define NPOL 1
#define PD0 0
#define PD1 4
#define HH 2
#define HW 3
#define HTW 3
#define KVT 0x3210
#define HKV 4
#define YPOL 1
#define YS2 2
#define OSH 4
#define HSK 3
#define HGR 2
#define KSH 0
#define EPI 0
#define YFL 0
#define CODEX 0
#define YPM 1
#define YPD 4
#define KPOL 0
#define GWD 16
#define HLAST 1

#define N2_ASM \
  "v_mov_b64 v[64:65], 0\n\t" \
  "v_mov_b64 v[66:67], 0\n\t" \
  "v_mov_b64 v[68:69], 0\n\t" \
  "v_mov_b64 v[70:71], 0\n\t" \
  "v_mov_b64 v[72:73], 0\n\t" \
  "v_mov_b64 v[74:75], 0\n\t" \
  "v_mov_b64 v[76:77], 0\n\t" \
  "v_mov_b64 v[78:79], 0\n\t" \
  "v_mov_b64 v[80:81], 0\n\t" \
  "v_mov_b64 v[82:83], 0\n\t" \
  "v_mov_b64 v[84:85], 0\n\t" \
  "v_mov_b64 v[86:87], 0\n\t" \
  "v_mov_b64 v[88:89], 0\n\t" \
  "v_mov_b64 v[90:91], 0\n\t" \
  "v_mov_b64 v[92:93], 0\n\t" \
  "v_mov_b64 v[94:95], 0\n\t" \
  "v_mov_b64 v[96:97], 0\n\t" \
  "v_mov_b64 v[98:99], 0\n\t" \
  "v_mov_b64 v[100:101], 0\n\t" \
  "v_mov_b64 v[102:103], 0\n\t" \
  "v_mov_b64 v[104:105], 0\n\t" \
  "v_mov_b64 v[106:107], 0\n\t" \
  "v_mov_b64 v[108:109], 0\n\t" \
  "v_mov_b64 v[110:111], 0\n\t" \
  "v_mov_b64 v[112:113], 0\n\t" \
  "v_mov_b64 v[114:115], 0\n\t" \
  "v_mov_b64 v[116:117], 0\n\t" \
  "v_mov_b64 v[118:119], 0\n\t" \
  "v_mov_b64 v[120:121], 0\n\t" \
  "v_mov_b64 v[122:123], 0\n\t" \
  "v_mov_b64 v[124:125], 0\n\t" \
  "v_mov_b64 v[126:127], 0\n\t" \
  "v_mov_b64 v[128:129], 0\n\t" \
  "v_mov_b64 v[130:131], 0\n\t" \
  "v_mov_b64 v[132:133], 0\n\t" \
  "v_mov_b64 v[134:135], 0\n\t" \
  "v_mov_b64 v[136:137], 0\n\t" \
  "v_mov_b64 v[138:139], 0\n\t" \
  "v_mov_b64 v[140:141], 0\n\t" \
  "v_mov_b64 v[142:143], 0\n\t" \
  "v_mov_b64 v[144:145], 0\n\t" \
  "v_mov_b64 v[146:147], 0\n\t" \
  "v_mov_b64 v[148:149], 0\n\t" \
  "v_mov_b64 v[150:151], 0\n\t" \
  "v_mov_b64 v[152:153], 0\n\t" \
  "v_mov_b64 v[154:155], 0\n\t" \
  "v_mov_b64 v[156:157], 0\n\t" \
  "v_mov_b64 v[158:159], 0\n\t" \
  "v_mov_b64 v[160:161], 0\n\t" \
  "v_mov_b64 v[162:163], 0\n\t" \
  "v_mov_b64 v[164:165], 0\n\t" \
  "v_mov_b64 v[166:167], 0\n\t" \
  "v_add_u32 v60, 36864, v58\n\t" \
  "v_add_u32 v61, 36864, v59\n\t" \
  "s_mov_b32 %[it], 0\n\t" \
  "s_cmp_lg_u32 %[r], 0\n\t" \
  "s_barrier\n\t" \
  "s_cbranch_scc1 NB_%=\n\t" \
  "ds_read_b64_tr_b16 v[48:49], v60 offset:14336\n\t" \
  "ds_read_b64_tr_b16 v[50:51], v60 offset:14848\n\t" \
  "ds_read_b128 v[0:3], v56 offset:38912\n\t" \
  "ds_read_b64_tr_b16 v[52:53], v60 offset:26624\n\t" \
  "ds_read_b64_tr_b16 v[54:55], v60 offset:27136\n\t" \
  "ds_read_b128 v[16:19], v57 offset:38912\n\t" \
  "ds_read_b128 v[4:7], v56 offset:40960\n\t" \
  "ds_read_b128 v[20:23], v57 offset:40960\n\t" \
  "ds_read_b128 v[8:11], v56 offset:43008\n\t" \
  "ds_read_b128 v[24:27], v57 offset:43008\n\t" \
  "ds_read_b128 v[12:15], v56 offset:45056\n\t" \
  "ds_read_b128 v[28:31], v57 offset:45056\n\t" \
  "ds_read_b64_tr_b16 v[32:33], v60 offset:13312\n\t" \
  "ds_read_b64_tr_b16 v[34:35], v60 offset:13824\n\t" \
  "s_mov_b32 s68, 0x50000\n\t" \
  "s_mov_b32 s69, 0x5550055\n\t" \
  "s_mov_b32 s70, 0xa0000\n\t" \
  "s_mov_b32 s71, 0xaaa00aa\n\t" \
  "s_mov_b32 s72, 0x150001\n\t" \
  "s_mov_b32 s73, 0x15550155\n\t" \
  "s_mov_b32 s74, 0x2a0002\n\t" \
  "s_mov_b32 s75, 0x2aaa02aa\n\t" \
  "s_mov_b32 s76, 0x55505555\n\t" \
  "s_mov_b32 s77, 0x50005500\n\t" \
  "s_mov_b32 s78, 0xaaa0aaaa\n\t" \
  "s_mov_b32 s79, 0xa000aa00\n\t" \
  "s_mov_b32 s80, 0x55405554\n\t" \
  "s_mov_b32 s81, 0x40005400\n\t" \
  "s_mov_b32 s82, 0xaa80aaa8\n\t" \
  "s_mov_b32 s83, 0x8000a800\n\t" \
  "s_mov_b32 s84, 0xaaaaaaaa\n\t" \
  "s_mov_b32 s85, 0xaaaaaaaa\n\t" \
  "s_waitcnt lgkmcnt(0)\n\t" \
  "s_branch N2a_%=\n\t" \
  "N0a_%=:\n\t" \
  "s_waitcnt lgkmcnt(10)\n\t" \
  "v_mfma_f32_16x16x32_bf16 v[128:131], v[0:3], v[32:35], v[128:131]\n\t" \
  "ds_read_b64_tr_b16 v[44:45], v58 offset:13312\n\t" \
  "s_waitcnt lgkmcnt(7)\n\t" \
  "v_mfma_f32_16x16x32_bf16 v[128:131], v[16:19], v[36:39], v[128:131]\n\t" \
  "ds_read_b64_tr_b16 v[46:47], v58 offset:13824\n\t" \
  "v_mfma_f32_16x16x32_bf16 v[136:139], v[4:7], v[32:35], v[136:139]\n\t" \
  "ds_read_b128 v[48:51], v56 offset:0\n\t" \
  "v_mfma_f32_16x16x32_bf16 v[136:139], v[20:23], v[36:39], v[136:139]\n\t" \
  "ds_read_b128 v[52:55], v57 offset:0\n\t" \
  "s_waitcnt lgkmcnt(8)\n\t" \
  "v_mfma_f32_16x16x32_bf16 v[144:147], v[8:11], v[32:35], v[144:147]\n\t" \
  "v_mfma_f32_16x16x32_bf16 v[144:147], v[24:27], v[36:39], v[144:147]\n\t" \
  "s_waitcnt lgkmcnt(4)\n\t" \
  "v_mfma_f32_16x16x32_bf16 v[152:155], v[12:15], v[32:35], v[152:155]\n\t" \
  "v_mfma_f32_16x16x32_bf16 v[152:155], v[28:31], v[36:39], v[152:155]\n\t" \
  "ds_read_b64_tr_b16 v[32:33], v59 offset:1024\n\t" \
  "v_mfma_f32_16x16x32_bf16 v[96:99], v[40:43], v[0:3], v[96:99]\n\t" \
  "ds_read_b64_tr_b16 v[34:35], v59 offset:1536\n\t" \
  "s_waitcnt lgkmcnt(4)\n\t" \
  "v_mfma_f32_16x16x32_bf16 v[96:99], v[44:47], v[16:19], v[96:99]\n\t" \
  "ds_read_b64_tr_b16 v[36:37], v59 offset:13312\n\t" \
  "v_mfma_f32_16x16x32_bf16 v[104:107], v[40:43], v[4:7], v[104:107]\n\t" \
  "v_mfma_f32_16x16x32_bf16 v[104:107], v[44:47], v[20:23], v[104:107]\n\t" \
  "ds_read_b64_tr_b16 v[38:39], v59 offset:13824\n\t" \
  "v_mfma_f32_16x16x32_bf16 v[112:115], v[40:43], v[8:11], v[112:115]\n\t" \
  "v_mfma_f32_16x16x32_bf16 v[112:115], v[44:47], v[24:27], v[112:115]\n\t" \
  "v_mfma_f32_16x16x32_bf16 v[120:123], v[40:43], v[12:15], v[120:123]\n\t" \
  "v_mfma_f32_16x16x32_bf16 v[120:123], v[44:47], v[28:31], v[120:123]\n\t" \
  "s_waitcnt lgkmcnt(4)\n\t" \
  "v_mfma_f32_16x16x32_bf16 v[160:163], v[40:43], v[48:51], v[160:163]\n\t" \
  "v_mfma_f32_16x16x32_bf16 v[160:163], v[44:47], v[52:55], v[160:163]\n\t" \
  "ds_read_b64_tr_b16 v[40:41], v59 offset:2048\n\t" \
  "s_waitcnt lgkmcnt(1)\n\t" \
  "v_mfma_f32_16x16x32_bf16 v[164:167], v[32:35], v[48:51], v[164:167]\n\t" \
  "ds_read_b64_tr_b16 v[42:43], v59 offset:2560\n\t" \
  "v_mfma_f32_16x16x32_bf16 v[164:167], v[36:39], v[52:55], v[164:167]\n\t" \
  "ds_read_b64_tr_b16 v[44:45], v59 offset:14336\n\t" \
  "v_mfma_f32_16x16x32_bf16 v[100:103], v[32:35], v[0:3], v[100:103]\n\t" \
  "ds_read_b64_tr_b16 v[46:47], v59 offset:14848\n\t" \
  "v_mfma_f32_16x16x32_bf16 v[100:103], v[36:39], v[16:19], v[100:103]\n\t" \
  "ds_read_b64_tr_b16 v[48:49], v58 offset:0\n\t" \
  "v_mfma_f32_16x16x32_bf16 v[108:111], v[32:35], v[4:7], v[108:111]\n\t" \
  "ds_read_b64_tr_b16 v[50:51], v58 offset:512\n\t" \
  "v_mfma_f32_16x16x32_bf16 v[108:111], v[36:39], v[20:23], v[108:111]\n\t" \
  "ds_read_b64_tr_b16 v[52:53], v58 offset:12288\n\t" \
  "v_mfma_f32_16x16x32_bf16 v[116:119], v[32:35], v[8:11], v[116:119]\n\t" \
  "ds_read_b64_tr_b16 v[54:55], v58 offset:12800\n\t" \
  "v_mfma_f32_16x16x32_bf16 v[116:119], v[36:39], v[24:27], v[116:119]\n\t" \
  "v_mfma_f32_16x16x32_bf16 v[124:127], v[32:35], v[12:15], v[124:127]\n\t" \
  "ds_read_b64_tr_b16 v[32:33], v59 offset:0\n\t" \
  "v_mfma_f32_16x16x32_bf16 v[124:127], v[36:39], v[28:31], v[124:127]\n\t" \
  "ds_read_b64_tr_b16 v[34:35], v59 offset:512\n\t" \
  "s_waitcnt lgkmcnt(6)\n\t" \
  "v_mfma_f32_16x16x32_bf16 v[132:135], v[0:3], v[40:43], v[132:135]\n\t" \
  "ds_read_b64_tr_b16 v[36:37], v59 offset:12288\n\t" \
  "v_mfma_f32_16x16x32_bf16 v[132:135], v[16:19], v[44:47], v[132:135]\n\t" \
  "ds_read_b64_tr_b16 v[38:39], v59 offset:12800\n\t" \
  "v_mfma_f32_16x16x32_bf16 v[140:143], v[4:7], v[40:43], v[140:143]\n\t" \
  "v_mfma_f32_16x16x32_bf16 v[140:143], v[20:23], v[44:47], v[140:143]\n\t" \
  "v_mfma_f32_16x16x32_bf16 v[148:151], v[8:11], v[40:43], v[148:151]\n\t" \
  "v_mfma_f32_16x16x32_bf16 v[148:151], v[24:27], v[44:47], v[148:151]\n\t" \
  "v_mfma_f32_16x16x32_bf16 v[156:159], v[12:15], v[40:43], v[156:159]\n\t" \
  "v_mfma_f32_16x16x32_bf16 v[156:159], v[28:31], v[44:47], v[156:159]\n\t" \
  "s_waitcnt lgkmcnt(0)\n\t" \
  "s_barrier\n\t" \
  "v_mfma_f32_16x16x32_bf16 v[64:67], v[48:51], v[0:3], v[64:67]\n\t" \
  "v_mfma_f32_16x16x32_bf16 v[64:67], v[52:55], v[16:19], v[64:67]\n\t" \
  "ds_read_b64_tr_b16 v[40:41], v58 offset:26624\n\t" \
  "v_mfma_f32_16x16x32_bf16 v[68:71], v[32:35], v[0:3], v[68:71]\n\t" \
  "ds_read_b128 v[0:3], v56 offset:20480\n\t" \
  "v_mfma_f32_16x16x32_bf16 v[68:71], v[36:39], v[16:19], v[68:71]\n\t" \
  "ds_read_b128 v[16:19], v57 offset:20480\n\t" \
  "v_mfma_f32_16x16x32_bf16 v[72:75], v[48:51], v[4:7], v[72:75]\n\t" \
  "ds_read_b64_tr_b16 v[42:43], v58 offset:27136\n\t" \
  "v_mfma_f32_16x16x32_bf16 v[72:75], v[52:55], v[20:23], v[72:75]\n\t" \
  "ds_read_b64_tr_b16 v[44:45], v60 offset:2048\n\t" \
  "v_mfma_f32_16x16x32_bf16 v[76:79], v[32:35], v[4:7], v[76:79]\n\t" \
  "ds_read_b128 v[4:7], v56 offset:22528\n\t" \
  "v_mfma_f32_16x16x32_bf16 v[76:79], v[36:39], v[20:23], v[76:79]\n\t" \
  "ds_read_b128 v[20:23], v57 offset:22528\n\t" \
  "v_mfma_f32_16x16x32_bf16 v[80:83], v[48:51], v[8:11], v[80:83]\n\t" \
  "ds_read_b64_tr_b16 v[46:47], v60 offset:2560\n\t" \
  "v_mfma_f32_16x16x32_bf16 v[80:83], v[52:55], v[24:27], v[80:83]\n\t" \
  "v_mfma_f32_16x16x32_bf16 v[84:87], v[32:35], v[8:11], v[84:87]\n\t" \
  "ds_read_b128 v[8:11], v56 offset:24576\n\t" \
  "v_mfma_f32_16x16x32_bf16 v[84:87], v[36:39], v[24:27], v[84:87]\n\t" \
  "ds_read_b128 v[24:27], v57 offset:24576\n\t" \
  "v_mfma_f32_16x16x32_bf16 v[88:91], v[48:51], v[12:15], v[88:91]\n\t" \
  "ds_read_b64_tr_b16 v[48:49], v58 offset:25600\n\t" \
  "v_mfma_f32_16x16x32_bf16 v[88:91], v[52:55], v[28:31], v[88:91]\n\t" \
  "ds_read_b64_tr_b16 v[50:51], v58 offset:26112\n\t" \
  "v_mfma_f32_16x16x32_bf16 v[92:95], v[32:35], v[12:15], v[92:95]\n\t" \
  "ds_read_b128 v[12:15], v56 offset:26624\n\t" \
  "v_mfma_f32_16x16x32_bf16 v[92:95], v[36:39], v[28:31], v[92:95]\n\t" \
  "ds_read_b128 v[28:31], v57 offset:26624\n\t" \
  "N1a_%=:\n\t" \
  "s_waitcnt lgkmcnt(10)\n\t" \
  "v_mfma_f32_16x16x32_bf16 v[128:131], v[0:3], v[40:43], v[128:131]\n\t" \
  "ds_read_b64_tr_b16 v[52:53], v60 offset:1024\n\t" \
  "s_waitcnt lgkmcnt(7)\n\t" \
  "v_mfma_f32_16x16x32_bf16 v[128:131], v[16:19], v[44:47], v[128:131]\n\t" \
  "ds_read_b64_tr_b16 v[54:55], v60 offset:1536\n\t" \
  "v_mfma_f32_16x16x32_bf16 v[136:139], v[4:7], v[40:43], v[136:139]\n\t" \
  "ds_read_b128 v[32:35], v56 offset:18432\n\t" \
  "v_mfma_f32_16x16x32_bf16 v[136:139], v[20:23], v[44:47], v[136:139]\n\t" \
  "ds_read_b128 v[36:39], v57 offset:18432\n\t" \
  "s_waitcnt lgkmcnt(8)\n\t" \
  "v_mfma_f32_16x16x32_bf16 v[144:147], v[8:11], v[40:43], v[144:147]\n\t" \
  "v_mfma_f32_16x16x32_bf16 v[144:147], v[24:27], v[44:47], v[144:147]\n\t" \
  "s_waitcnt lgkmcnt(4)\n\t" \
  "v_mfma_f32_16x16x32_bf16 v[152:155], v[12:15], v[40:43], v[152:155]\n\t" \
  "v_mfma_f32_16x16x32_bf16 v[152:155], v[28:31], v[44:47], v[152:155]\n\t" \
  "ds_read_b64_tr_b16 v[40:41], v59 offset:25600\n\t" \
  "v_mfma_f32_16x16x32_bf16 v[96:99], v[48:51], v[0:3], v[96:99]\n\t" \
  "ds_read_b64_tr_b16 v[42:43], v59 offset:26112\n\t" \
  "s_waitcnt lgkmcnt(4)\n\t" \
  "v_mfma_f32_16x16x32_bf16 v[96:99], v[52:55], v[16:19], v[96:99]\n\t" \
  "ds_read_b64_tr_b16 v[44:45], v61 offset:1024\n\t" \
  "v_mfma_f32_16x16x32_bf16 v[104:107], v[48:51], v[4:7], v[104:107]\n\t" \
  "v_mfma_f32_16x16x32_bf16 v[104:107], v[52:55], v[20:23], v[104:107]\n\t" \
  "ds_read_b64_tr_b16 v[46:47], v61 offset:1536\n\t" \
  "v_mfma_f32_16x16x32_bf16 v[112:115], v[48:51], v[8:11], v[112:115]\n\t" \
  "v_mfma_f32_16x16x32_bf16 v[112:115], v[52:55], v[24:27], v[112:115]\n\t" \
  "v_mfma_f32_16x16x32_bf16 v[120:123], v[48:51], v[12:15], v[120:123]\n\t" \
  "v_mfma_f32_16x16x32_bf16 v[120:123], v[52:55], v[28:31], v[120:123]\n\t" \
  "s_waitcnt lgkmcnt(4)\n\t" \
  "v_mfma_f32_16x16x32_bf16 v[160:163], v[48:51], v[32:35], v[160:163]\n\t" \
  "v_mfma_f32_16x16x32_bf16 v[160:163], v[52:55], v[36:39], v[160:163]\n\t" \
  "ds_read_b64_tr_b16 v[48:49], v59 offset:26624\n\t" \
  "s_waitcnt lgkmcnt(1)\n\t" \
  "v_mfma_f32_16x16x32_bf16 v[164:167], v[40:43], v[32:35], v[164:167]\n\t" \
  "ds_read_b64_tr_b16 v[50:51], v59 offset:27136\n\t" \
  "v_mfma_f32_16x16x32_bf16 v[164:167], v[44:47], v[36:39], v[164:167]\n\t" \
  "ds_read_b64_tr_b16 v[52:53], v61 offset:2048\n\t" \
  "v_mfma_f32_16x16x32_bf16 v[100:103], v[40:43], v[0:3], v[100:103]\n\t" \
  "ds_read_b64_tr_b16 v[54:55], v61 offset:2560\n\t" \
  "v_mfma_f32_16x16x32_bf16 v[100:103], v[44:47], v[16:19], v[100:103]\n\t" \
  "ds_read_b64_tr_b16 v[32:33], v58 offset:24576\n\t" \
  "v_mfma_f32_16x16x32_bf16 v[108:111], v[40:43], v[4:7], v[108:111]\n\t" \
  "ds_read_b64_tr_b16 v[34:35], v58 offset:25088\n\t" \
  "v_mfma_f32_16x16x32_bf16 v[108:111], v[44:47], v[20:23], v[108:111]\n\t" \
  "ds_read_b64_tr_b16 v[36:37], v60 offset:0\n\t" \
  "v_mfma_f32_16x16x32_bf16 v[116:119], v[40:43], v[8:11], v[116:119]\n\t" \
  "ds_read_b64_tr_b16 v[38:39], v60 offset:512\n\t" \
  "v_mfma_f32_16x16x32_bf16 v[116:119], v[44:47], v[24:27], v[116:119]\n\t" \
  "v_mfma_f32_16x16x32_bf16 v[124:127], v[40:43], v[12:15], v[124:127]\n\t" \
  "ds_read_b64_tr_b16 v[40:41], v59 offset:24576\n\t" \
  "v_mfma_f32_16x16x32_bf16 v[124:127], v[44:47], v[28:31], v[124:127]\n\t" \
  "ds_read_b64_tr_b16 v[42:43], v59 offset:25088\n\t" \
  "s_waitcnt lgkmcnt(6)\n\t" \
  "v_mfma_f32_16x16x32_bf16 v[132:135], v[0:3], v[48:51], v[132:135]\n\t" \
  "ds_read_b64_tr_b16 v[44:45], v61 offset:0\n\t" \
  "v_mfma_f32_16x16x32_bf16 v[132:135], v[16:19], v[52:55], v[132:135]\n\t" \
  "ds_read_b64_tr_b16 v[46:47], v61 offset:512\n\t" \
  "v_mfma_f32_16x16x32_bf16 v[140:143], v[4:7], v[48:51], v[140:143]\n\t" \
  "v_mfma_f32_16x16x32_bf16 v[140:143], v[20:23], v[52:55], v[140:143]\n\t" \
  "v_mfma_f32_16x16x32_bf16 v[148:151], v[8:11], v[48:51], v[148:151]\n\t" \
  "v_mfma_f32_16x16x32_bf16 v[148:151], v[24:27], v[52:55], v[148:151]\n\t" \
  "v_mfma_f32_16x16x32_bf16 v[156:159], v[12:15], v[48:51], v[156:159]\n\t" \
  "v_mfma_f32_16x16x32_bf16 v[156:159], v[28:31], v[52:55], v[156:159]\n\t" \
  "s_waitcnt lgkmcnt(0)\n\t" \
  "s_barrier\n\t" \
  "v_mfma_f32_16x16x32_bf16 v[64:67], v[32:35], v[0:3], v[64:67]\n\t" \
  "v_mfma_f32_16x16x32_bf16 v[64:67], v[36:39], v[16:19], v[64:67]\n\t" \
  "ds_read_b64_tr_b16 v[48:49], v60 offset:14336\n\t" \
  "v_mfma_f32_16x16x32_bf16 v[68:71], v[40:43], v[0:3], v[68:71]\n\t" \
  "ds_read_b128 v[0:3], v56 offset:38912\n\t" \
  "v_mfma_f32_16x16x32_bf16 v[68:71], v[44:47], v[16:19], v[68:71]\n\t" \
  "ds_read_b128 v[16:19], v57 offset:38912\n\t" \
  "v_mfma_f32_16x16x32_bf16 v[72:75], v[32:35], v[4:7], v[72:75]\n\t" \
  "ds_read_b64_tr_b16 v[50:51], v60 offset:14848\n\t" \
  "v_mfma_f32_16x16x32_bf16 v[72:75], v[36:39], v[20:23], v[72:75]\n\t" \
  "ds_read_b64_tr_b16 v[52:53], v60 offset:26624\n\t" \
  "v_mfma_f32_16x16x32_bf16 v[76:79], v[40:43], v[4:7], v[76:79]\n\t" \
  "ds_read_b128 v[4:7], v56 offset:40960\n\t" \
  "v_mfma_f32_16x16x32_bf16 v[76:79], v[44:47], v[20:23], v[76:79]\n\t" \
  "ds_read_b128 v[20:23], v57 offset:40960\n\t" \
  "v_mfma_f32_16x16x32_bf16 v[80:83], v[32:35], v[8:11], v[80:83]\n\t" \
  "ds_read_b64_tr_b16 v[54:55], v60 offset:27136\n\t" \
  "v_mfma_f32_16x16x32_bf16 v[80:83], v[36:39], v[24:27], v[80:83]\n\t" \
  "v_mfma_f32_16x16x32_bf16 v[84:87], v[40:43], v[8:11], v[84:87]\n\t" \
  "ds_read_b128 v[8:11], v56 offset:43008\n\t" \
  "v_mfma_f32_16x16x32_bf16 v[84:87], v[44:47], v[24:27], v[84:87]\n\t" \
  "ds_read_b128 v[24:27], v57 offset:43008\n\t" \
  "v_mfma_f32_16x16x32_bf16 v[88:91], v[32:35], v[12:15], v[88:91]\n\t" \
  "ds_read_b64_tr_b16 v[32:33], v60 offset:13312\n\t" \
  "v_mfma_f32_16x16x32_bf16 v[88:91], v[36:39], v[28:31], v[88:91]\n\t" \
  "ds_read_b64_tr_b16 v[34:35], v60 offset:13824\n\t" \
  "v_mfma_f32_16x16x32_bf16 v[92:95], v[40:43], v[12:15], v[92:95]\n\t" \
  "ds_read_b128 v[12:15], v56 offset:45056\n\t" \
  "v_mfma_f32_16x16x32_bf16 v[92:95], v[44:47], v[28:31], v[92:95]\n\t" \
  "ds_read_b128 v[28:31], v57 offset:45056\n\t" \
  "N2a_%=:\n\t" \
  "s_waitcnt lgkmcnt(10)\n\t" \
  "v_mfma_f32_16x16x32_bf16 v[128:131], v[0:3], v[48:51], v[128:131]\n\t" \
  "ds_read_b64_tr_b16 v[36:37], v60 offset:25600\n\t" \
  "s_waitcnt lgkmcnt(7)\n\t" \
  "v_mfma_f32_16x16x32_bf16 v[128:131], v[16:19], v[52:55], v[128:131]\n\t" \
  "ds_read_b64_tr_b16 v[38:39], v60 offset:26112\n\t" \
  "v_mfma_f32_16x16x32_bf16 v[136:139], v[4:7], v[48:51], v[136:139]\n\t" \
  "ds_read_b128 v[40:43], v56 offset:36864\n\t" \
  "v_mfma_f32_16x16x32_bf16 v[136:139], v[20:23], v[52:55], v[136:139]\n\t" \
  "ds_read_b128 v[44:47], v57 offset:36864\n\t" \
  "s_waitcnt lgkmcnt(8)\n\t" \
  "v_mfma_f32_16x16x32_bf16 v[144:147], v[8:11], v[48:51], v[144:147]\n\t" \
  "v_mfma_f32_16x16x32_bf16 v[144:147], v[24:27], v[52:55], v[144:147]\n\t" \
  "s_waitcnt lgkmcnt(4)\n\t" \
  "v_mfma_f32_16x16x32_bf16 v[152:155], v[12:15], v[48:51], v[152:155]\n\t" \
  "v_mfma_f32_16x16x32_bf16 v[152:155], v[28:31], v[52:55], v[152:155]\n\t" \
  "ds_read_b64_tr_b16 v[48:49], v61 offset:13312\n\t" \
  "v_mfma_f32_16x16x32_bf16 v[96:99], v[32:35], v[0:3], v[96:99]\n\t" \
  "ds_read_b64_tr_b16 v[50:51], v61 offset:13824\n\t" \
  "s_waitcnt lgkmcnt(4)\n\t" \
  "v_mfma_f32_16x16x32_bf16 v[96:99], v[36:39], v[16:19], v[96:99]\n\t" \
  "ds_read_b64_tr_b16 v[52:53], v61 offset:25600\n\t" \
  "v_mfma_f32_16x16x32_bf16 v[104:107], v[32:35], v[4:7], v[104:107]\n\t" \
  "v_mfma_f32_16x16x32_bf16 v[104:107], v[36:39], v[20:23], v[104:107]\n\t" \
  "ds_read_b64_tr_b16 v[54:55], v61 offset:26112\n\t" \
  "v_mfma_f32_16x16x32_bf16 v[112:115], v[32:35], v[8:11], v[112:115]\n\t" \
  "v_mfma_f32_16x16x32_bf16 v[112:115], v[36:39], v[24:27], v[112:115]\n\t" \
  "v_mfma_f32_16x16x32_bf16 v[120:123], v[32:35], v[12:15], v[120:123]\n\t" \
  "v_mfma_f32_16x16x32_bf16 v[120:123], v[36:39], v[28:31], v[120:123]\n\t" \
  "s_waitcnt lgkmcnt(4)\n\t" \
  "v_mfma_f32_16x16x32_bf16 v[160:163], v[32:35], v[40:43], v[160:163]\n\t" \
  "v_mfma_f32_16x16x32_bf16 v[160:163], v[36:39], v[44:47], v[160:163]\n\t" \
  "ds_read_b64_tr_b16 v[32:33], v61 offset:14336\n\t" \
  "s_waitcnt lgkmcnt(1)\n\t" \
  "v_mfma_f32_16x16x32_bf16 v[164:167], v[48:51], v[40:43], v[164:167]\n\t" \
  "ds_read_b64_tr_b16 v[34:35], v61 offset:14848\n\t" \
  "v_mfma_f32_16x16x32_bf16 v[164:167], v[52:55], v[44:47], v[164:167]\n\t" \
  "ds_read_b64_tr_b16 v[36:37], v61 offset:26624\n\t" \
  "v_mfma_f32_16x16x32_bf16 v[100:103], v[48:51], v[0:3], v[100:103]\n\t" \
  "ds_read_b64_tr_b16 v[38:39], v61 offset:27136\n\t" \
  "v_mfma_f32_16x16x32_bf16 v[100:103], v[52:55], v[16:19], v[100:103]\n\t" \
  "ds_read_b64_tr_b16 v[40:41], v60 offset:12288\n\t" \
  "v_mfma_f32_16x16x32_bf16 v[108:111], v[48:51], v[4:7], v[108:111]\n\t" \
  "ds_read_b64_tr_b16 v[42:43], v60 offset:12800\n\t" \
  "v_mfma_f32_16x16x32_bf16 v[108:111], v[52:55], v[20:23], v[108:111]\n\t" \
  "ds_read_b64_tr_b16 v[44:45], v60 offset:24576\n\t" \
  "v_mfma_f32_16x16x32_bf16 v[116:119], v[48:51], v[8:11], v[116:119]\n\t" \
  "ds_read_b64_tr_b16 v[46:47], v60 offset:25088\n\t" \
  "v_mfma_f32_16x16x32_bf16 v[116:119], v[52:55], v[24:27], v[116:119]\n\t" \
  "v_mfma_f32_16x16x32_bf16 v[124:127], v[48:51], v[12:15], v[124:127]\n\t" \
  "ds_read_b64_tr_b16 v[48:49], v61 offset:12288\n\t" \
  "v_mfma_f32_16x16x32_bf16 v[124:127], v[52:55], v[28:31], v[124:127]\n\t" \
  "ds_read_b64_tr_b16 v[50:51], v61 offset:12800\n\t" \
  "s_waitcnt lgkmcnt(6)\n\t" \
  "v_mfma_f32_16x16x32_bf16 v[132:135], v[0:3], v[32:35], v[132:135]\n\t" \
  "ds_read_b64_tr_b16 v[52:53], v61 offset:24576\n\t" \
  "v_mfma_f32_16x16x32_bf16 v[132:135], v[16:19], v[36:39], v[132:135]\n\t" \
  "ds_read_b64_tr_b16 v[54:55], v61 offset:25088\n\t" \
  "v_mfma_f32_16x16x32_bf16 v[140:143], v[4:7], v[32:35], v[140:143]\n\t" \
  "v_mfma_f32_16x16x32_bf16 v[140:143], v[20:23], v[36:39], v[140:143]\n\t" \
  "s_addk_i32 %[it], 1\n\t" \
  "v_mfma_f32_16x16x32_bf16 v[148:151], v[8:11], v[32:35], v[148:151]\n\t" \
  "s_cmp_lt_u32 %[it], 6\n\t" \
  "v_mfma_f32_16x16x32_bf16 v[148:151], v[24:27], v[36:39], v[148:151]\n\t" \
  "v_mfma_f32_16x16x32_bf16 v[156:159], v[12:15], v[32:35], v[156:159]\n\t" \
  "v_mfma_f32_16x16x32_bf16 v[156:159], v[28:31], v[36:39], v[156:159]\n\t" \
  "s_waitcnt lgkmcnt(0)\n\t" \
  "s_barrier\n\t" \
  "s_cbranch_scc0 NTa_%=\n\t" \
  "v_mfma_f32_16x16x32_bf16 v[64:67], v[40:43], v[0:3], v[64:67]\n\t" \
  "v_mfma_f32_16x16x32_bf16 v[64:67], v[44:47], v[16:19], v[64:67]\n\t" \
  "ds_read_b64_tr_b16 v[32:33], v58 offset:2048\n\t" \
  "v_mfma_f32_16x16x32_bf16 v[68:71], v[48:51], v[0:3], v[68:71]\n\t" \
  "ds_read_b128 v[0:3], v56 offset:2048\n\t" \
  "v_mfma_f32_16x16x32_bf16 v[68:71], v[52:55], v[16:19], v[68:71]\n\t" \
  "ds_read_b128 v[16:19], v57 offset:2048\n\t" \
  "v_mfma_f32_16x16x32_bf16 v[72:75], v[40:43], v[4:7], v[72:75]\n\t" \
  "ds_read_b64_tr_b16 v[34:35], v58 offset:2560\n\t" \
  "v_mfma_f32_16x16x32_bf16 v[72:75], v[44:47], v[20:23], v[72:75]\n\t" \
  "ds_read_b64_tr_b16 v[36:37], v58 offset:14336\n\t" \
  "v_mfma_f32_16x16x32_bf16 v[76:79], v[48:51], v[4:7], v[76:79]\n\t" \
  "ds_read_b128 v[4:7], v56 offset:4096\n\t" \
  "v_mfma_f32_16x16x32_bf16 v[76:79], v[52:55], v[20:23], v[76:79]\n\t" \
  "ds_read_b128 v[20:23], v57 offset:4096\n\t" \
  "v_mfma_f32_16x16x32_bf16 v[80:83], v[40:43], v[8:11], v[80:83]\n\t" \
  "ds_read_b64_tr_b16 v[38:39], v58 offset:14848\n\t" \
  "v_mfma_f32_16x16x32_bf16 v[80:83], v[44:47], v[24:27], v[80:83]\n\t" \
  "v_mfma_f32_16x16x32_bf16 v[84:87], v[48:51], v[8:11], v[84:87]\n\t" \
  "ds_read_b128 v[8:11], v56 offset:6144\n\t" \
  "v_mfma_f32_16x16x32_bf16 v[84:87], v[52:55], v[24:27], v[84:87]\n\t" \
  "ds_read_b128 v[24:27], v57 offset:6144\n\t" \
  "v_mfma_f32_16x16x32_bf16 v[88:91], v[40:43], v[12:15], v[88:91]\n\t" \
  "ds_read_b64_tr_b16 v[40:41], v58 offset:1024\n\t" \
  "v_mfma_f32_16x16x32_bf16 v[88:91], v[44:47], v[28:31], v[88:91]\n\t" \
  "ds_read_b64_tr_b16 v[42:43], v58 offset:1536\n\t" \
  "v_mfma_f32_16x16x32_bf16 v[92:95], v[48:51], v[12:15], v[92:95]\n\t" \
  "ds_read_b128 v[12:15], v56 offset:8192\n\t" \
  "v_mfma_f32_16x16x32_bf16 v[92:95], v[52:55], v[28:31], v[92:95]\n\t" \
  "ds_read_b128 v[28:31], v57 offset:8192\n\t" \
  "s_branch N0a_%=\n\t" \
  "NTa_%=:\n\t" \
  "s_setprio 3\n\t" \
  "v_mfma_f32_16x16x32_bf16 v[88:91], v[40:43], v[12:15], v[88:91]\n\t" \
  "v_cvt_pk_bf16_f32 v120, v120, v121\n\t" \
  "v_cvt_pk_bf16_f32 v121, v122, v123\n\t" \
  "v_mfma_f32_16x16x32_bf16 v[88:91], v[44:47], v[28:31], v[88:91]\n\t" \
  "v_cvt_pk_bf16_f32 v122, v124, v125\n\t" \
  "v_cvt_pk_bf16_f32 v123, v126, v127\n\t" \
  "v_mfma_f32_16x16x32_bf16 v[92:95], v[48:51], v[12:15], v[92:95]\n\t" \
  "v_mov_b32 v57, %[fa]\n\t" \
  "v_mov_b32 v58, 1\n\t" \
  "v_mfma_f32_16x16x32_bf16 v[92:95], v[52:55], v[28:31], v[92:95]\n\t" \
  "ds_write_b128 v63, v[120:123]\n\t" \
  "v_cvt_pk_bf16_f32 v38, v156, v157\n\t" \
  "v_mfma_f32_16x16x32_bf16 v[80:83], v[40:43], v[8:11], v[80:83]\n\t" \
  "v_cvt_pk_bf16_f32 v39, v158, v159\n\t" \
  "v_cvt_pk_bf16_f32 v28, v152, v153\n\t" \
  "v_mfma_f32_16x16x32_bf16 v[80:83], v[44:47], v[24:27], v[80:83]\n\t" \
  "v_cvt_pk_bf16_f32 v29, v154, v155\n\t" \
  "v_mov_b32 v60, 0xff61b1e6\n\t" \
  "v_mfma_f32_16x16x32_bf16 v[84:87], v[48:51], v[8:11], v[84:87]\n\t" \
  "ds_write2_b64 v63, v[28:29], v[38:39] offset0:128 offset1:129\n\t" \
  "s_mov_b64 exec, 1\n\t" \
  "ds_write_b32 v57, v58\n\t" \
  "s_mov_b64 exec, -1\n\t" \
  "v_mfma_f32_16x16x32_bf16 v[84:87], v[52:55], v[24:27], v[84:87]\n\t" \
  "v_cvt_pk_bf16_f32 v160, v160, v161\n\t" \
  "v_cvt_pk_bf16_f32 v161, v162, v163\n\t" \
  "v_mfma_f32_16x16x32_bf16 v[72:75], v[40:43], v[4:7], v[72:75]\n\t" \
  "v_cvt_pk_bf16_f32 v162, v164, v165\n\t" \
  "v_cvt_pk_bf16_f32 v163, v166, v167\n\t" \
  "v_mfma_f32_16x16x32_bf16 v[72:75], v[44:47], v[20:23], v[72:75]\n\t" \
  "v_cvt_pk_bf16_f32 v112, v112, v113\n\t" \
  "v_cvt_pk_bf16_f32 v113, v114, v115\n\t" \
  "v_mfma_f32_16x16x32_bf16 v[76:79], v[48:51], v[4:7], v[76:79]\n\t" \
  "v_cvt_pk_bf16_f32 v114, v116, v117\n\t" \
  "v_cvt_pk_bf16_f32 v115, v118, v119\n\t" \
  "v_mfma_f32_16x16x32_bf16 v[76:79], v[52:55], v[20:23], v[76:79]\n\t" \
  "v_cndmask_b32_e64 v12, v60, 0, s[76:77]\n\t" \
  "v_cndmask_b32_e64 v13, v60, 0, s[78:79]\n\t" \
  "v_mfma_f32_16x16x32_bf16 v[64:67], v[40:43], v[0:3], v[64:67]\n\t" \
  "v_cndmask_b32_e64 v14, v60, 0, s[80:81]\n\t" \
  "v_cndmask_b32_e64 v15, v60, 0, s[82:83]\n\t" \
  "v_mfma_f32_16x16x32_bf16 v[64:67], v[44:47], v[16:19], v[64:67]\n\t" \
  "v_cndmask_b32_e64 v56, v60, 0, s[68:69]\n\t" \
  "v_cndmask_b32_e64 v57, v60, 0, s[70:71]\n\t" \
  "v_mfma_f32_16x16x32_bf16 v[68:71], v[48:51], v[0:3], v[68:71]\n\t" \
  "v_cndmask_b32_e64 v58, v60, 0, s[72:73]\n\t" \
  "v_cndmask_b32_e64 v59, v60, 0, s[74:75]\n\t" \
  "v_mfma_f32_16x16x32_bf16 v[68:71], v[52:55], v[16:19], v[68:71]\n\t" \
  "s_branch NX_%=\n\t" \
  "NB_%=:\n\t" \
  "ds_read_b64_tr_b16 v[48:49], v60 offset:13312\n\t" \
  "ds_read_b64_tr_b16 v[50:51], v60 offset:13824\n\t" \
  "ds_read_b128 v[0:3], v56 offset:47104\n\t" \
  "ds_read_b64_tr_b16 v[52:53], v60 offset:25600\n\t" \
  "ds_read_b64_tr_b16 v[54:55], v60 offset:26112\n\t" \
  "ds_read_b128 v[16:19], v57 offset:47104\n\t" \
  "ds_read_b128 v[4:7], v56 offset:49152\n\t" \
  "ds_read_b128 v[20:23], v57 offset:49152\n\t" \
  "ds_read_b128 v[8:11], v56 offset:51200\n\t" \
  "ds_read_b128 v[24:27], v57 offset:51200\n\t" \
  "ds_read_b128 v[12:15], v56 offset:53248\n\t" \
  "ds_read_b128 v[28:31], v57 offset:53248\n\t" \
  "ds_read_b64_tr_b16 v[32:33], v60 offset:14336\n\t" \
  "ds_read_b64_tr_b16 v[34:35], v60 offset:14848\n\t" \
  "s_mov_b32 s68, 0x50000\n\t" \
  "s_mov_b32 s69, 0x5550055\n\t" \
  "s_mov_b32 s70, 0xa0000\n\t" \
  "s_mov_b32 s71, 0xaaa00aa\n\t" \
  "s_mov_b32 s72, 0x150001\n\t" \
  "s_mov_b32 s73, 0x15550155\n\t" \
  "s_mov_b32 s74, 0x2a0002\n\t" \
  "s_mov_b32 s75, 0x2aaa02aa\n\t" \
  "s_mov_b32 s76, 0x55505555\n\t" \
  "s_mov_b32 s77, 0x50005500\n\t" \
  "s_mov_b32 s78, 0xaaa0aaaa\n\t" \
  "s_mov_b32 s79, 0xa000aa00\n\t" \
  "s_mov_b32 s80, 0x55405554\n\t" \
  "s_mov_b32 s81, 0x40005400\n\t" \
  "s_mov_b32 s82, 0xaa80aaa8\n\t" \
  "s_mov_b32 s83, 0x8000a800\n\t" \
  "s_mov_b32 s84, 0xaaaaaaaa\n\t" \
  "s_mov_b32 s85, 0xaaaaaaaa\n\t" \
  "s_waitcnt lgkmcnt(0)\n\t" \
  "s_branch N2b_%=\n\t" \
  "N0b_%=:\n\t" \
  "s_waitcnt lgkmcnt(10)\n\t" \
  "v_mfma_f32_16x16x32_bf16 v[96:99], v[32:35], v[0:3], v[96:99]\n\t" \
  "ds_read_b64_tr_b16 v[44:45], v58 offset:14336\n\t" \
  "s_waitcnt lgkmcnt(7)\n\t" \
  "v_mfma_f32_16x16x32_bf16 v[96:99], v[36:39], v[16:19], v[96:99]\n\t" \
  "ds_read_b64_tr_b16 v[46:47], v58 offset:14848\n\t" \
  "v_mfma_f32_16x16x32_bf16 v[104:107], v[32:35], v[4:7], v[104:107]\n\t" \
  "ds_read_b128 v[48:51], v56 offset:0\n\t" \
  "v_mfma_f32_16x16x32_bf16 v[104:107], v[36:39], v[20:23], v[104:107]\n\t" \
  "ds_read_b128 v[52:55], v57 offset:0\n\t" \
  "s_waitcnt lgkmcnt(8)\n\t" \
  "v_mfma_f32_16x16x32_bf16 v[112:115], v[32:35], v[8:11], v[112:115]\n\t" \
  "v_mfma_f32_16x16x32_bf16 v[112:115], v[36:39], v[24:27], v[112:115]\n\t" \
  "s_waitcnt lgkmcnt(4)\n\t" \
  "v_mfma_f32_16x16x32_bf16 v[120:123], v[32:35], v[12:15], v[120:123]\n\t" \
  "v_mfma_f32_16x16x32_bf16 v[120:123], v[36:39], v[28:31], v[120:123]\n\t" \
  "ds_read_b64_tr_b16 v[32:33], v59 offset:2048\n\t" \
  "v_mfma_f32_16x16x32_bf16 v[128:131], v[0:3], v[40:43], v[128:131]\n\t" \
  "ds_read_b64_tr_b16 v[34:35], v59 offset:2560\n\t" \
  "s_waitcnt lgkmcnt(4)\n\t" \
  "v_mfma_f32_16x16x32_bf16 v[128:131], v[16:19], v[44:47], v[128:131]\n\t" \
  "ds_read_b64_tr_b16 v[36:37], v59 offset:14336\n\t" \
  "v_mfma_f32_16x16x32_bf16 v[136:139], v[4:7], v[40:43], v[136:139]\n\t" \
  "v_mfma_f32_16x16x32_bf16 v[136:139], v[20:23], v[44:47], v[136:139]\n\t" \
  "ds_read_b64_tr_b16 v[38:39], v59 offset:14848\n\t" \
  "v_mfma_f32_16x16x32_bf16 v[144:147], v[8:11], v[40:43], v[144:147]\n\t" \
  "v_mfma_f32_16x16x32_bf16 v[144:147], v[24:27], v[44:47], v[144:147]\n\t" \
  "v_mfma_f32_16x16x32_bf16 v[152:155], v[12:15], v[40:43], v[152:155]\n\t" \
  "v_mfma_f32_16x16x32_bf16 v[152:155], v[28:31], v[44:47], v[152:155]\n\t" \
  "s_waitcnt lgkmcnt(4)\n\t" \
  "v_mfma_f32_16x16x32_bf16 v[160:163], v[48:51], v[40:43], v[160:163]\n\t" \
  "v_mfma_f32_16x16x32_bf16 v[160:163], v[52:55], v[44:47], v[160:163]\n\t" \
  "ds_read_b64_tr_b16 v[40:41], v59 offset:1024\n\t" \
  "s_waitcnt lgkmcnt(1)\n\t" \
  "v_mfma_f32_16x16x32_bf16 v[164:167], v[48:51], v[32:35], v[164:167]\n\t" \
  "ds_read_b64_tr_b16 v[42:43], v59 offset:1536\n\t" \
  "v_mfma_f32_16x16x32_bf16 v[164:167], v[52:55], v[36:39], v[164:167]\n\t" \
  "ds_read_b64_tr_b16 v[44:45], v59 offset:13312\n\t" \
  "v_mfma_f32_16x16x32_bf16 v[132:135], v[0:3], v[32:35], v[132:135]\n\t" \
  "ds_read_b64_tr_b16 v[46:47], v59 offset:13824\n\t" \
  "v_mfma_f32_16x16x32_bf16 v[132:135], v[16:19], v[36:39], v[132:135]\n\t" \
  "ds_read_b64_tr_b16 v[48:49], v58 offset:0\n\t" \
  "v_mfma_f32_16x16x32_bf16 v[140:143], v[4:7], v[32:35], v[140:143]\n\t" \
  "ds_read_b64_tr_b16 v[50:51], v58 offset:512\n\t" \
  "v_mfma_f32_16x16x32_bf16 v[140:143], v[20:23], v[36:39], v[140:143]\n\t" \
  "ds_read_b64_tr_b16 v[52:53], v58 offset:12288\n\t" \
  "v_mfma_f32_16x16x32_bf16 v[148:151], v[8:11], v[32:35], v[148:151]\n\t" \
  "ds_read_b64_tr_b16 v[54:55], v58 offset:12800\n\t" \
  "v_mfma_f32_16x16x32_bf16 v[148:151], v[24:27], v[36:39], v[148:151]\n\t" \
  "v_mfma_f32_16x16x32_bf16 v[156:159], v[12:15], v[32:35], v[156:159]\n\t" \
  "ds_read_b64_tr_b16 v[32:33], v59 offset:0\n\t" \
  "v_mfma_f32_16x16x32_bf16 v[156:159], v[28:31], v[36:39], v[156:159]\n\t" \
  "ds_read_b64_tr_b16 v[34:35], v59 offset:512\n\t" \
  "s_waitcnt lgkmcnt(6)\n\t" \
  "v_mfma_f32_16x16x32_bf16 v[100:103], v[40:43], v[0:3], v[100:103]\n\t" \
  "ds_read_b64_tr_b16 v[36:37], v59 offset:12288\n\t" \
  "v_mfma_f32_16x16x32_bf16 v[100:103], v[44:47], v[16:19], v[100:103]\n\t" \
  "ds_read_b64_tr_b16 v[38:39], v59 offset:12800\n\t" \
  "v_mfma_f32_16x16x32_bf16 v[108:111], v[40:43], v[4:7], v[108:111]\n\t" \
  "v_mfma_f32_16x16x32_bf16 v[108:111], v[44:47], v[20:23], v[108:111]\n\t" \
  "v_mfma_f32_16x16x32_bf16 v[116:119], v[40:43], v[8:11], v[116:119]\n\t" \
  "v_mfma_f32_16x16x32_bf16 v[116:119], v[44:47], v[24:27], v[116:119]\n\t" \
  "v_mfma_f32_16x16x32_bf16 v[124:127], v[40:43], v[12:15], v[124:127]\n\t" \
  "v_mfma_f32_16x16x32_bf16 v[124:127], v[44:47], v[28:31], v[124:127]\n\t" \
  "s_waitcnt lgkmcnt(0)\n\t" \
  "s_barrier\n\t" \
  "v_mfma_f32_16x16x32_bf16 v[64:67], v[48:51], v[0:3], v[64:67]\n\t" \
  "v_mfma_f32_16x16x32_bf16 v[64:67], v[52:55], v[16:19], v[64:67]\n\t" \
  "ds_read_b64_tr_b16 v[40:41], v58 offset:25600\n\t" \
  "v_mfma_f32_16x16x32_bf16 v[68:71], v[32:35], v[0:3], v[68:71]\n\t" \
  "ds_read_b128 v[0:3], v56 offset:28672\n\t" \
  "v_mfma_f32_16x16x32_bf16 v[68:71], v[36:39], v[16:19], v[68:71]\n\t" \
  "ds_read_b128 v[16:19], v57 offset:28672\n\t" \
  "v_mfma_f32_16x16x32_bf16 v[72:75], v[48:51], v[4:7], v[72:75]\n\t" \
  "ds_read_b64_tr_b16 v[42:43], v58 offset:26112\n\t" \
  "v_mfma_f32_16x16x32_bf16 v[72:75], v[52:55], v[20:23], v[72:75]\n\t" \
  "ds_read_b64_tr_b16 v[44:45], v60 offset:1024\n\t" \
  "v_mfma_f32_16x16x32_bf16 v[76:79], v[32:35], v[4:7], v[76:79]\n\t" \
  "ds_read_b128 v[4:7], v56 offset:30720\n\t" \
  "v_mfma_f32_16x16x32_bf16 v[76:79], v[36:39], v[20:23], v[76:79]\n\t" \
  "ds_read_b128 v[20:23], v57 offset:30720\n\t" \
  "v_mfma_f32_16x16x32_bf16 v[80:83], v[48:51], v[8:11], v[80:83]\n\t" \
  "ds_read_b64_tr_b16 v[46:47], v60 offset:1536\n\t" \
  "v_mfma_f32_16x16x32_bf16 v[80:83], v[52:55], v[24:27], v[80:83]\n\t" \
  "v_mfma_f32_16x16x32_bf16 v[84:87], v[32:35], v[8:11], v[84:87]\n\t" \
  "ds_read_b128 v[8:11], v56 offset:32768\n\t" \
  "v_mfma_f32_16x16x32_bf16 v[84:87], v[36:39], v[24:27], v[84:87]\n\t" \
  "ds_read_b128 v[24:27], v57 offset:32768\n\t" \
  "v_mfma_f32_16x16x32_bf16 v[88:91], v[48:51], v[12:15], v[88:91]\n\t" \
  "ds_read_b64_tr_b16 v[48:49], v58 offset:26624\n\t" \
  "v_mfma_f32_16x16x32_bf16 v[88:91], v[52:55], v[28:31], v[88:91]\n\t" \
  "ds_read_b64_tr_b16 v[50:51], v58 offset:27136\n\t" \
  "v_mfma_f32_16x16x32_bf16 v[92:95], v[32:35], v[12:15], v[92:95]\n\t" \
  "ds_read_b128 v[12:15], v56 offset:34816\n\t" \
  "v_mfma_f32_16x16x32_bf16 v[92:95], v[36:39], v[28:31], v[92:95]\n\t" \
  "ds_read_b128 v[28:31], v57 offset:34816\n\t" \
  "N1b_%=:\n\t" \
  "s_waitcnt lgkmcnt(10)\n\t" \
  "v_mfma_f32_16x16x32_bf16 v[96:99], v[40:43], v[0:3], v[96:99]\n\t" \
  "ds_read_b64_tr_b16 v[52:53], v60 offset:2048\n\t" \
  "s_waitcnt lgkmcnt(7)\n\t" \
  "v_mfma_f32_16x16x32_bf16 v[96:99], v[44:47], v[16:19], v[96:99]\n\t" \
  "ds_read_b64_tr_b16 v[54:55], v60 offset:2560\n\t" \
  "v_mfma_f32_16x16x32_bf16 v[104:107], v[40:43], v[4:7], v[104:107]\n\t" \
  "ds_read_b128 v[32:35], v56 offset:18432\n\t" \
  "v_mfma_f32_16x16x32_bf16 v[104:107], v[44:47], v[20:23], v[104:107]\n\t" \
  "ds_read_b128 v[36:39], v57 offset:18432\n\t" \
  "s_waitcnt lgkmcnt(8)\n\t" \
  "v_mfma_f32_16x16x32_bf16 v[112:115], v[40:43], v[8:11], v[112:115]\n\t" \
  "v_mfma_f32_16x16x32_bf16 v[112:115], v[44:47], v[24:27], v[112:115]\n\t" \
  "s_waitcnt lgkmcnt(4)\n\t" \
  "v_mfma_f32_16x16x32_bf16 v[120:123], v[40:43], v[12:15], v[120:123]\n\t" \
  "v_mfma_f32_16x16x32_bf16 v[120:123], v[44:47], v[28:31], v[120:123]\n\t" \
  "ds_read_b64_tr_b16 v[40:41], v59 offset:26624\n\t" \
  "v_mfma_f32_16x16x32_bf16 v[128:131], v[0:3], v[48:51], v[128:131]\n\t" \
  "ds_read_b64_tr_b16 v[42:43], v59 offset:27136\n\t" \
  "s_waitcnt lgkmcnt(4)\n\t" \
  "v_mfma_f32_16x16x32_bf16 v[128:131], v[16:19], v[52:55], v[128:131]\n\t" \
  "ds_read_b64_tr_b16 v[44:45], v61 offset:2048\n\t" \
  "v_mfma_f32_16x16x32_bf16 v[136:139], v[4:7], v[48:51], v[136:139]\n\t" \
  "v_mfma_f32_16x16x32_bf16 v[136:139], v[20:23], v[52:55], v[136:139]\n\t" \
  "ds_read_b64_tr_b16 v[46:47], v61 offset:2560\n\t" \
  "v_mfma_f32_16x16x32_bf16 v[144:147], v[8:11], v[48:51], v[144:147]\n\t" \
  "v_mfma_f32_16x16x32_bf16 v[144:147], v[24:27], v[52:55], v[144:147]\n\t" \
  "v_mfma_f32_16x16x32_bf16 v[152:155], v[12:15], v[48:51], v[152:155]\n\t" \
  "v_mfma_f32_16x16x32_bf16 v[152:155], v[28:31], v[52:55], v[152:155]\n\t" \
  "s_waitcnt lgkmcnt(4)\n\t" \
  "v_mfma_f32_16x16x32_bf16 v[160:163], v[32:35], v[48:51], v[160:163]\n\t" \
  "v_mfma_f32_16x16x32_bf16 v[160:163], v[36:39], v[52:55], v[160:163]\n\t" \
  "ds_read_b64_tr_b16 v[48:49], v59 offset:25600\n\t" \
  "s_waitcnt lgkmcnt(1)\n\t" \
  "v_mfma_f32_16x16x32_bf16 v[164:167], v[32:35], v[40:43], v[164:167]\n\t" \
  "ds_read_b64_tr_b16 v[50:51], v59 offset:26112\n\t" \
  "v_mfma_f32_16x16x32_bf16 v[164:167], v[36:39], v[44:47], v[164:167]\n\t" \
  "ds_read_b64_tr_b16 v[52:53], v61 offset:1024\n\t" \
  "v_mfma_f32_16x16x32_bf16 v[132:135], v[0:3], v[40:43], v[132:135]\n\t" \
  "ds_read_b64_tr_b16 v[54:55], v61 offset:1536\n\t" \
  "v_mfma_f32_16x16x32_bf16 v[132:135], v[16:19], v[44:47], v[132:135]\n\t" \
  "ds_read_b64_tr_b16 v[32:33], v58 offset:24576\n\t" \
  "v_mfma_f32_16x16x32_bf16 v[140:143], v[4:7], v[40:43], v[140:143]\n\t" \
  "ds_read_b64_tr_b16 v[34:35], v58 offset:25088\n\t" \
  "v_mfma_f32_16x16x32_bf16 v[140:143], v[20:23], v[44:47], v[140:143]\n\t" \
  "ds_read_b64_tr_b16 v[36:37], v60 offset:0\n\t" \
  "v_mfma_f32_16x16x32_bf16 v[148:151], v[8:11], v[40:43], v[148:151]\n\t" \
  "ds_read_b64_tr_b16 v[38:39], v60 offset:512\n\t" \
  "v_mfma_f32_16x16x32_bf16 v[148:151], v[24:27], v[44:47], v[148:151]\n\t" \
  "v_mfma_f32_16x16x32_bf16 v[156:159], v[12:15], v[40:43], v[156:159]\n\t" \
  "ds_read_b64_tr_b16 v[40:41], v59 offset:24576\n\t" \
  "v_mfma_f32_16x16x32_bf16 v[156:159], v[28:31], v[44:47], v[156:159]\n\t" \
  "ds_read_b64_tr_b16 v[42:43], v59 offset:25088\n\t" \
  "s_waitcnt lgkmcnt(6)\n\t" \
  "v_mfma_f32_16x16x32_bf16 v[100:103], v[48:51], v[0:3], v[100:103]\n\t" \
  "ds_read_b64_tr_b16 v[44:45], v61 offset:0\n\t" \
  "v_mfma_f32_16x16x32_bf16 v[100:103], v[52:55], v[16:19], v[100:103]\n\t" \
  "ds_read_b64_tr_b16 v[46:47], v61 offset:512\n\t" \
  "v_mfma_f32_16x16x32_bf16 v[108:111], v[48:51], v[4:7], v[108:111]\n\t" \
  "v_mfma_f32_16x16x32_bf16 v[108:111], v[52:55], v[20:23], v[108:111]\n\t" \
  "v_mfma_f32_16x16x32_bf16 v[116:119], v[48:51], v[8:11], v[116:119]\n\t" \
  "v_mfma_f32_16x16x32_bf16 v[116:119], v[52:55], v[24:27], v[116:119]\n\t" \
  "v_mfma_f32_16x16x32_bf16 v[124:127], v[48:51], v[12:15], v[124:127]\n\t" \
  "v_mfma_f32_16x16x32_bf16 v[124:127], v[52:55], v[28:31], v[124:127]\n\t" \
  "s_waitcnt lgkmcnt(0)\n\t" \
  "s_barrier\n\t" \
  "v_mfma_f32_16x16x32_bf16 v[64:67], v[32:35], v[0:3], v[64:67]\n\t" \
  "v_mfma_f32_16x16x32_bf16 v[64:67], v[36:39], v[16:19], v[64:67]\n\t" \
  "ds_read_b64_tr_b16 v[48:49], v60 offset:13312\n\t" \
  "v_mfma_f32_16x16x32_bf16 v[68:71], v[40:43], v[0:3], v[68:71]\n\t" \
  "ds_read_b128 v[0:3], v56 offset:47104\n\t" \
  "v_mfma_f32_16x16x32_bf16 v[68:71], v[44:47], v[16:19], v[68:71]\n\t" \
  "ds_read_b128 v[16:19], v57 offset:47104\n\t" \
  "v_mfma_f32_16x16x32_bf16 v[72:75], v[32:35], v[4:7], v[72:75]\n\t" \
  "ds_read_b64_tr_b16 v[50:51], v60 offset:13824\n\t" \
  "v_mfma_f32_16x16x32_bf16 v[72:75], v[36:39], v[20:23], v[72:75]\n\t" \
  "ds_read_b64_tr_b16 v[52:53], v60 offset:25600\n\t" \
  "v_mfma_f32_16x16x32_bf16 v[76:79], v[40:43], v[4:7], v[76:79]\n\t" \
  "ds_read_b128 v[4:7], v56 offset:49152\n\t" \
  "v_mfma_f32_16x16x32_bf16 v[76:79], v[44:47], v[20:23], v[76:79]\n\t" \
  "ds_read_b128 v[20:23], v57 offset:49152\n\t" \
  "v_mfma_f32_16x16x32_bf16 v[80:83], v[32:35], v[8:11], v[80:83]\n\t" \
  "ds_read_b64_tr_b16 v[54:55], v60 offset:26112\n\t" \
  "v_mfma_f32_16x16x32_bf16 v[80:83], v[36:39], v[24:27], v[80:83]\n\t" \
  "v_mfma_f32_16x16x32_bf16 v[84:87], v[40:43], v[8:11], v[84:87]\n\t" \
  "ds_read_b128 v[8:11], v56 offset:51200\n\t" \
  "v_mfma_f32_16x16x32_bf16 v[84:87], v[44:47], v[24:27], v[84:87]\n\t" \
  "ds_read_b128 v[24:27], v57 offset:51200\n\t" \
  "v_mfma_f32_16x16x32_bf16 v[88:91], v[32:35], v[12:15], v[88:91]\n\t" \
  "ds_read_b64_tr_b16 v[32:33], v60 offset:14336\n\t" \
  "v_mfma_f32_16x16x32_bf16 v[88:91], v[36:39], v[28:31], v[88:91]\n\t" \
  "ds_read_b64_tr_b16 v[34:35], v60 offset:14848\n\t" \
  "v_mfma_f32_16x16x32_bf16 v[92:95], v[40:43], v[12:15], v[92:95]\n\t" \
  "ds_read_b128 v[12:15], v56 offset:53248\n\t" \
  "v_mfma_f32_16x16x32_bf16 v[92:95], v[44:47], v[28:31], v[92:95]\n\t" \
  "ds_read_b128 v[28:31], v57 offset:53248\n\t" \
  "N2b_%=:\n\t" \
  "s_waitcnt lgkmcnt(10)\n\t" \
  "v_mfma_f32_16x16x32_bf16 v[96:99], v[48:51], v[0:3], v[96:99]\n\t" \
  "ds_read_b64_tr_b16 v[36:37], v60 offset:26624\n\t" \
  "s_waitcnt lgkmcnt(7)\n\t" \
  "v_mfma_f32_16x16x32_bf16 v[96:99], v[52:55], v[16:19], v[96:99]\n\t" \
  "ds_read_b64_tr_b16 v[38:39], v60 offset:27136\n\t" \
  "v_mfma_f32_16x16x32_bf16 v[104:107], v[48:51], v[4:7], v[104:107]\n\t" \
  "ds_read_b128 v[40:43], v56 offset:36864\n\t" \
  "v_mfma_f32_16x16x32_bf16 v[104:107], v[52:55], v[20:23], v[104:107]\n\t" \
  "ds_read_b128 v[44:47], v57 offset:36864\n\t" \
  "s_waitcnt lgkmcnt(8)\n\t" \
  "v_mfma_f32_16x16x32_bf16 v[112:115], v[48:51], v[8:11], v[112:115]\n\t" \
  "v_mfma_f32_16x16x32_bf16 v[112:115], v[52:55], v[24:27], v[112:115]\n\t" \
  "s_waitcnt lgkmcnt(4)\n\t" \
  "v_mfma_f32_16x16x32_bf16 v[120:123], v[48:51], v[12:15], v[120:123]\n\t" \
  "v_mfma_f32_16x16x32_bf16 v[120:123], v[52:55], v[28:31], v[120:123]\n\t" \
  "ds_read_b64_tr_b16 v[48:49], v61 offset:14336\n\t" \
  "v_mfma_f32_16x16x32_bf16 v[128:131], v[0:3], v[32:35], v[128:131]\n\t" \
  "ds_read_b64_tr_b16 v[50:51], v61 offset:14848\n\t" \
  "s_waitcnt lgkmcnt(4)\n\t" \
  "v_mfma_f32_16x16x32_bf16 v[128:131], v[16:19], v[36:39], v[128:131]\n\t" \
  "ds_read_b64_tr_b16 v[52:53], v61 offset:26624\n\t" \
  "v_mfma_f32_16x16x32_bf16 v[136:139], v[4:7], v[32:35], v[136:139]\n\t" \
  "v_mfma_f32_16x16x32_bf16 v[136:139], v[20:23], v[36:39], v[136:139]\n\t" \
  "ds_read_b64_tr_b16 v[54:55], v61 offset:27136\n\t" \
  "v_mfma_f32_16x16x32_bf16 v[144:147], v[8:11], v[32:35], v[144:147]\n\t" \
  "v_mfma_f32_16x16x32_bf16 v[144:147], v[24:27], v[36:39], v[144:147]\n\t" \
  "v_mfma_f32_16x16x32_bf16 v[152:155], v[12:15], v[32:35], v[152:155]\n\t" \
  "v_mfma_f32_16x16x32_bf16 v[152:155], v[28:31], v[36:39], v[152:155]\n\t" \
  "s_waitcnt lgkmcnt(4)\n\t" \
  "v_mfma_f32_16x16x32_bf16 v[160:163], v[40:43], v[32:35], v[160:163]\n\t" \
  "v_mfma_f32_16x16x32_bf16 v[160:163], v[44:47], v[36:39], v[160:163]\n\t" \
  "ds_read_b64_tr_b16 v[32:33], v61 offset:13312\n\t" \
  "s_waitcnt lgkmcnt(1)\n\t" \
  "v_mfma_f32_16x16x32_bf16 v[164:167], v[40:43], v[48:51], v[164:167]\n\t" \
  "ds_read_b64_tr_b16 v[34:35], v61 offset:13824\n\t" \
  "v_mfma_f32_16x16x32_bf16 v[164:167], v[44:47], v[52:55], v[164:167]\n\t" \
  "ds_read_b64_tr_b16 v[36:37], v61 offset:25600\n\t" \
  "v_mfma_f32_16x16x32_bf16 v[132:135], v[0:3], v[48:51], v[132:135]\n\t" \
  "ds_read_b64_tr_b16 v[38:39], v61 offset:26112\n\t" \
  "v_mfma_f32_16x16x32_bf16 v[132:135], v[16:19], v[52:55], v[132:135]\n\t" \
  "ds_read_b64_tr_b16 v[40:41], v60 offset:12288\n\t" \
  "v_mfma_f32_16x16x32_bf16 v[140:143], v[4:7], v[48:51], v[140:143]\n\t" \
  "ds_read_b64_tr_b16 v[42:43], v60 offset:12800\n\t" \
  "v_mfma_f32_16x16x32_bf16 v[140:143], v[20:23], v[52:55], v[140:143]\n\t" \
  "ds_read_b64_tr_b16 v[44:45], v60 offset:24576\n\t" \
  "v_mfma_f32_16x16x32_bf16 v[148:151], v[8:11], v[48:51], v[148:151]\n\t" \
  "ds_read_b64_tr_b16 v[46:47], v60 offset:25088\n\t" \
  "v_mfma_f32_16x16x32_bf16 v[148:151], v[24:27], v[52:55], v[148:151]\n\t" \
  "v_mfma_f32_16x16x32_bf16 v[156:159], v[12:15], v[48:51], v[156:159]\n\t" \
  "ds_read_b64_tr_b16 v[48:49], v61 offset:12288\n\t" \
  "v_mfma_f32_16x16x32_bf16 v[156:159], v[28:31], v[52:55], v[156:159]\n\t" \
  "ds_read_b64_tr_b16 v[50:51], v61 offset:12800\n\t" \
  "s_waitcnt lgkmcnt(6)\n\t" \
  "v_mfma_f32_16x16x32_bf16 v[100:103], v[32:35], v[0:3], v[100:103]\n\t" \
  "ds_read_b64_tr_b16 v[52:53], v61 offset:24576\n\t" \
  "v_mfma_f32_16x16x32_bf16 v[100:103], v[36:39], v[16:19], v[100:103]\n\t" \
  "ds_read_b64_tr_b16 v[54:55], v61 offset:25088\n\t" \
  "v_mfma_f32_16x16x32_bf16 v[108:111], v[32:35], v[4:7], v[108:111]\n\t" \
  "v_mfma_f32_16x16x32_bf16 v[108:111], v[36:39], v[20:23], v[108:111]\n\t" \
  "s_addk_i32 %[it], 1\n\t" \
  "v_mfma_f32_16x16x32_bf16 v[116:119], v[32:35], v[8:11], v[116:119]\n\t" \
  "s_cmp_lt_u32 %[it], 6\n\t" \
  "v_mfma_f32_16x16x32_bf16 v[116:119], v[36:39], v[24:27], v[116:119]\n\t" \
  "v_mfma_f32_16x16x32_bf16 v[124:127], v[32:35], v[12:15], v[124:127]\n\t" \
  "v_mfma_f32_16x16x32_bf16 v[124:127], v[36:39], v[28:31], v[124:127]\n\t" \
  "s_waitcnt lgkmcnt(0)\n\t" \
  "s_barrier\n\t" \
  "s_cbranch_scc0 NTb_%=\n\t" \
  "v_mfma_f32_16x16x32_bf16 v[64:67], v[40:43], v[0:3], v[64:67]\n\t" \
  "v_mfma_f32_16x16x32_bf16 v[64:67], v[44:47], v[16:19], v[64:67]\n\t" \
  "ds_read_b64_tr_b16 v[32:33], v58 offset:1024\n\t" \
  "v_mfma_f32_16x16x32_bf16 v[68:71], v[48:51], v[0:3], v[68:71]\n\t" \
  "ds_read_b128 v[0:3], v56 offset:10240\n\t" \
  "v_mfma_f32_16x16x32_bf16 v[68:71], v[52:55], v[16:19], v[68:71]\n\t" \
  "ds_read_b128 v[16:19], v57 offset:10240\n\t" \
  "v_mfma_f32_16x16x32_bf16 v[72:75], v[40:43], v[4:7], v[72:75]\n\t" \
  "ds_read_b64_tr_b16 v[34:35], v58 offset:1536\n\t" \
  "v_mfma_f32_16x16x32_bf16 v[72:75], v[44:47], v[20:23], v[72:75]\n\t" \
  "ds_read_b64_tr_b16 v[36:37], v58 offset:13312\n\t" \
  "v_mfma_f32_16x16x32_bf16 v[76:79], v[48:51], v[4:7], v[76:79]\n\t" \
  "ds_read_b128 v[4:7], v56 offset:12288\n\t" \
  "v_mfma_f32_16x16x32_bf16 v[76:79], v[52:55], v[20:23], v[76:79]\n\t" \
  "ds_read_b128 v[20:23], v57 offset:12288\n\t" \
  "v_mfma_f32_16x16x32_bf16 v[80:83], v[40:43], v[8:11], v[80:83]\n\t" \
  "ds_read_b64_tr_b16 v[38:39], v58 offset:13824\n\t" \
  "v_mfma_f32_16x16x32_bf16 v[80:83], v[44:47], v[24:27], v[80:83]\n\t" \
  "v_mfma_f32_16x16x32_bf16 v[84:87], v[48:51], v[8:11], v[84:87]\n\t" \
  "ds_read_b128 v[8:11], v56 offset:14336\n\t" \
  "v_mfma_f32_16x16x32_bf16 v[84:87], v[52:55], v[24:27], v[84:87]\n\t" \
  "ds_read_b128 v[24:27], v57 offset:14336\n\t" \
  "v_mfma_f32_16x16x32_bf16 v[88:91], v[40:43], v[12:15], v[88:91]\n\t" \
  "ds_read_b64_tr_b16 v[40:41], v58 offset:2048\n\t" \
  "v_mfma_f32_16x16x32_bf16 v[88:91], v[44:47], v[28:31], v[88:91]\n\t" \
  "ds_read_b64_tr_b16 v[42:43], v58 offset:2560\n\t" \
  "v_mfma_f32_16x16x32_bf16 v[92:95], v[48:51], v[12:15], v[92:95]\n\t" \
  "ds_read_b128 v[12:15], v56 offset:16384\n\t" \
  "v_mfma_f32_16x16x32_bf16 v[92:95], v[52:55], v[28:31], v[92:95]\n\t" \
  "ds_read_b128 v[28:31], v57 offset:16384\n\t" \
  "s_branch N0b_%=\n\t" \
  "NTb_%=:\n\t" \
  "s_setprio 3\n\t" \
  "v_mfma_f32_16x16x32_bf16 v[88:91], v[40:43], v[12:15], v[88:91]\n\t" \
  "v_cvt_pk_bf16_f32 v160, v160, v161\n\t" \
  "v_cvt_pk_bf16_f32 v161, v162, v163\n\t" \
  "v_mfma_f32_16x16x32_bf16 v[88:91], v[44:47], v[28:31], v[88:91]\n\t" \
  "v_cvt_pk_bf16_f32 v162, v164, v165\n\t" \
  "v_cvt_pk_bf16_f32 v163, v166, v167\n\t" \
  "v_mfma_f32_16x16x32_bf16 v[92:95], v[48:51], v[12:15], v[92:95]\n\t" \
  "v_mov_b32 v57, %[fa]\n\t" \
  "v_mov_b32 v58, 1\n\t" \
  "v_mfma_f32_16x16x32_bf16 v[92:95], v[52:55], v[28:31], v[92:95]\n\t" \
  "ds_write_b128 v63, v[160:163] offset:2048\n\t" \
  "s_mov_b64 exec, 1\n\t" \
  "ds_write_b32 v57, v58\n\t" \
  "s_mov_b64 exec, -1\n\t" \
  "v_mfma_f32_16x16x32_bf16 v[80:83], v[40:43], v[8:11], v[80:83]\n\t" \
  "v_mov_b32 v60, 0xff61b1e6\n\t" \
  "v_cvt_pk_bf16_f32 v120, v120, v121\n\t" \
  "v_mfma_f32_16x16x32_bf16 v[80:83], v[44:47], v[24:27], v[80:83]\n\t" \
  "v_cvt_pk_bf16_f32 v121, v122, v123\n\t" \
  "v_cvt_pk_bf16_f32 v122, v124, v125\n\t" \
  "v_mfma_f32_16x16x32_bf16 v[84:87], v[48:51], v[8:11], v[84:87]\n\t" \
  "v_cvt_pk_bf16_f32 v123, v126, v127\n\t" \
  "v_cvt_pk_bf16_f32 v28, v152, v153\n\t" \
  "v_mfma_f32_16x16x32_bf16 v[84:87], v[52:55], v[24:27], v[84:87]\n\t" \
  "v_cvt_pk_bf16_f32 v29, v154, v155\n\t" \
  "v_cvt_pk_bf16_f32 v38, v156, v157\n\t" \
  "v_mfma_f32_16x16x32_bf16 v[72:75], v[40:43], v[4:7], v[72:75]\n\t" \
  "v_cvt_pk_bf16_f32 v39, v158, v159\n\t" \
  "v_cvt_pk_bf16_f32 v112, v112, v113\n\t" \
  "v_mfma_f32_16x16x32_bf16 v[72:75], v[44:47], v[20:23], v[72:75]\n\t" \
  "v_cvt_pk_bf16_f32 v113, v114, v115\n\t" \
  "v_cvt_pk_bf16_f32 v114, v116, v117\n\t" \
  "v_mfma_f32_16x16x32_bf16 v[76:79], v[48:51], v[4:7], v[76:79]\n\t" \
  "v_cvt_pk_bf16_f32 v115, v118, v119\n\t" \
  "v_cndmask_b32_e64 v12, v60, 0, s[76:77]\n\t" \
  "v_mfma_f32_16x16x32_bf16 v[76:79], v[52:55], v[20:23], v[76:79]\n\t" \
  "v_cndmask_b32_e64 v13, v60, 0, s[78:79]\n\t" \
  "v_cndmask_b32_e64 v14, v60, 0, s[80:81]\n\t" \
  "v_mfma_f32_16x16x32_bf16 v[64:67], v[40:43], v[0:3], v[64:67]\n\t" \
  "v_cndmask_b32_e64 v15, v60, 0, s[82:83]\n\t" \
  "v_cndmask_b32_e64 v56, v60, 0, s[68:69]\n\t" \
  "v_mfma_f32_16x16x32_bf16 v[64:67], v[44:47], v[16:19], v[64:67]\n\t" \
  "v_cndmask_b32_e64 v57, v60, 0, s[70:71]\n\t" \
  "v_cndmask_b32_e64 v58, v60, 0, s[72:73]\n\t" \
  "v_mfma_f32_16x16x32_bf16 v[68:71], v[48:51], v[0:3], v[68:71]\n\t" \
  "v_cndmask_b32_e64 v59, v60, 0, s[74:75]\n\t" \
  "v_mfma_f32_16x16x32_bf16 v[68:71], v[52:55], v[16:19], v[68:71]\n\t" \
  "NX_%=:\n\t" \
  "v_cvt_pk_bf16_f32 v88, v88, v89\n\t" \
  "v_cvt_pk_bf16_f32 v89, v90, v91\n\t" \
  "v_cvt_pk_bf16_f32 v90, v92, v93\n\t" \
  "v_cvt_pk_bf16_f32 v91, v94, v95\n\t" \
  "v_mov_b32 v61, %[fp]\n\t" \
  "v_add_u32 v60, %[vo], v63\n\t" \
  "v_mfma_f32_16x16x32_bf16 v[152:155], v[112:115], v[88:91], v[56:59]\n\t" \
  "v_cvt_pk_bf16_f32 v26, v144, v145\n\t" \
  "v_cvt_pk_bf16_f32 v27, v146, v147\n\t" \
  "v_mfma_f32_16x16x32_bf16 v[156:159], v[120:123], v[88:91], v[12:15]\n\t" \
  "v_cvt_pk_bf16_f32 v36, v148, v149\n\t" \
  "v_cvt_pk_bf16_f32 v37, v150, v151\n\t" \
  "v_cvt_pk_bf16_f32 v80, v80, v81\n\t" \
  "v_cvt_pk_bf16_f32 v81, v82, v83\n\t" \
  "v_cvt_pk_bf16_f32 v82, v84, v85\n\t" \
  "v_cvt_pk_bf16_f32 v83, v86, v87\n\t" \
  "v_cvt_pk_bf16_f32 v104, v104, v105\n\t" \
  "v_cvt_pk_bf16_f32 v105, v106, v107\n\t" \
  "v_mfma_f32_16x16x32_bf16 v[148:151], v[112:115], v[80:83], v[12:15]\n\t" \
  "v_cvt_pk_bf16_f32 v106, v108, v109\n\t" \
  "v_cvt_pk_bf16_f32 v107, v110, v111\n\t" \
  "ds_read_b32 v0, v61\n\t" \
  "ds_read_b64 v[20:21], v60\n\t" \
  "ds_read_b64 v[30:31], v60 offset:8\n\t" \
  "s_mov_b64 exec, %[rm]\n\t" \
  "ds_read_b128 v[160:163], v63\n\t" \
  "s_mov_b64 exec, -1\n\t" \
  "v_mfma_f32_16x16x32_bf16 v[144:147], v[104:107], v[80:83], v[56:59]\n\t" \
  "v_cvt_pk_bf16_f32 v24, v136, v137\n\t" \
  "v_cvt_pk_bf16_f32 v25, v138, v139\n\t" \
  "v_cvt_pk_bf16_f32 v34, v140, v141\n\t" \
  "v_cvt_pk_bf16_f32 v35, v142, v143\n\t" \
  "v_cvt_pk_bf16_f32 v72, v72, v73\n\t" \
  "v_cvt_pk_bf16_f32 v73, v74, v75\n\t" \
  "v_cvt_pk_bf16_f32 v74, v76, v77\n\t" \
  "v_cvt_pk_bf16_f32 v75, v78, v79\n\t" \
  "v_cvt_pk_bf16_f32 v96, v96, v97\n\t" \
  "v_cvt_pk_bf16_f32 v97, v98, v99\n\t" \
  "v_mfma_f32_16x16x32_bf16 v[140:143], v[104:107], v[72:75], v[12:15]\n\t" \
  "v_cvt_pk_bf16_f32 v98, v100, v101\n\t" \
  "v_cvt_pk_bf16_f32 v99, v102, v103\n\t" \
  "v_cvt_pk_bf16_f32 v22, v128, v129\n\t" \
  "v_cvt_pk_bf16_f32 v23, v130, v131\n\t" \
  "v_mfma_f32_16x16x32_bf16 v[136:139], v[96:99], v[72:75], v[56:59]\n\t" \
  "v_cvt_pk_bf16_f32 v32, v132, v133\n\t" \
  "v_cvt_pk_bf16_f32 v33, v134, v135\n\t" \
  "v_cvt_pk_bf16_f32 v64, v64, v65\n\t" \
  "v_cvt_pk_bf16_f32 v65, v66, v67\n\t" \
  "v_cvt_pk_bf16_f32 v66, v68, v69\n\t" \
  "v_cvt_pk_bf16_f32 v67, v70, v71\n\t" \
  "v_max_f32 v152, v152, v153\n\t" \
  "v_max_f32 v154, v154, v155\n\t" \
  "v_mfma_f32_16x16x32_bf16 v[132:135], v[96:99], v[64:67], v[12:15]\n\t" \
  "AW_%=:\n\t" \
  "s_waitcnt lgkmcnt(0)\n\t" \
  "v_cmp_eq_u32 vcc, 0, v0\n\t" \
  "v_mov_b32 v8, 0xc0c0100\n\t" \
  "v_mov_b32 v9, 0x1000c0c\n\t" \
  "s_cbranch_vccnz AR_%=\n\t" \
  "s_cmp_eq_u32 %[z], 0\n\t" \
  "s_cbranch_scc1 AZ_%=\n\t" \
  "AY_%=:\n\t" \
  "v_mfma_f32_16x16x32_bf16 v[128:131], v[160:163], v[64:67], v[56:59]\n\t" \
  "v_max_f32 v156, v156, v157\n\t" \
  "v_max_f32 v158, v158, v159\n\t" \
  "v_max3_f32 v7, v152, v154, v156\n\t" \
  "v_max_f32 v7, v7, v158\n\t" \
  "v_max_f32 v144, v144, v145\n\t" \
  "v_max_f32 v146, v146, v147\n\t" \
  "v_max_f32 v148, v148, v149\n\t" \
  "v_max_f32 v150, v150, v151\n\t" \
  "v_max3_f32 v6, v144, v146, v148\n\t" \
  "v_max_f32 v6, v6, v150\n\t" \
  "v_max_f32 v136, v136, v137\n\t" \
  "v_max_f32 v138, v138, v139\n\t" \
  "v_max_f32 v140, v140, v141\n\t" \
  "v_max_f32 v142, v142, v143\n\t" \
  "v_max3_f32 v5, v136, v138, v140\n\t" \
  "v_max_f32 v5, v5, v142\n\t" \
  "v_max_f32 v128, v128, v129\n\t" \
  "v_max_f32 v130, v130, v131\n\t" \
  "v_max_f32 v132, v132, v133\n\t" \
  "v_max_f32 v134, v134, v135\n\t" \
  "v_max3_f32 v4, v128, v130, v132\n\t" \
  "v_max_f32 v4, v4, v134\n\t" \
  "v_cndmask_b32_e64 v60, v8, v9, s[84:85]\n\t" \
  "v_mov_b32 v8, 0xc0c0302\n\t" \
  "v_permlane16_swap_b32 v6, v7\n\t" \
  "v_permlane16_swap_b32 v4, v5\n\t" \
  "v_max_f32 v6, v6, v7\n\t" \
  "v_max_f32 v4, v4, v5\n\t" \
  "v_mov_b32 v9, 0x3020c0c\n\t" \
  "v_cndmask_b32_e64 v61, v8, v9, s[84:85]\n\t" \
  "v_permlane32_swap_b32 v4, v6\n\t" \
  "v_max_f32 v4, v4, v6\n\t" \
  "v_mul_f32 v6, 0xbe8293ee, v4\n\t" \
  "v_mul_f32 v4, 0xbe8293ee, v4\n\t" \
  "s_nop 1\n\t" \
  "v_permlane32_swap_b32 v4, v6\n\t" \
  "v_mov_b32 v5, v4\n\t" \
  "v_mov_b32 v7, v6\n\t" \
  "s_nop 0\n\t" \
  "v_permlane16_swap_b32 v4, v5\n\t" \
  "v_permlane16_swap_b32 v6, v7\n\t" \
  "v_fmamk_f32 v128, v128, 0x3e8293ee, v4\n\t" \
  "v_fmamk_f32 v130, v130, 0x3e8293ee, v4\n\t" \
  "v_fmamk_f32 v132, v132, 0x3e8293ee, v4\n\t" \
  "v_fmamk_f32 v134, v134, 0x3e8293ee, v4\n\t" \
  "v_fmamk_f32 v136, v136, 0x3e8293ee, v5\n\t" \
  "v_fmamk_f32 v138, v138, 0x3e8293ee, v5\n\t" \
  "v_fmamk_f32 v140, v140, 0x3e8293ee, v5\n\t" \
  "v_fmamk_f32 v142, v142, 0x3e8293ee, v5\n\t" \
  "v_fmamk_f32 v144, v144, 0x3e8293ee, v6\n\t" \
  "v_fmamk_f32 v146, v146, 0x3e8293ee, v6\n\t" \
  "v_fmamk_f32 v148, v148, 0x3e8293ee, v6\n\t" \
  "v_fmamk_f32 v150, v150, 0x3e8293ee, v6\n\t" \
  "v_fmamk_f32 v152, v152, 0x3e8293ee, v7\n\t" \
  "v_fmamk_f32 v154, v154, 0x3e8293ee, v7\n\t" \
  "v_fmamk_f32 v156, v156, 0x3e8293ee, v7\n\t" \
  "v_fmamk_f32 v158, v158, 0x3e8293ee, v7\n\t" \
  "v_exp_f32 v128, v128\n\t" \
  "v_exp_f32 v130, v130\n\t" \
  "v_exp_f32 v132, v132\n\t" \
  "v_exp_f32 v134, v134\n\t" \
  "v_exp_f32 v136, v136\n\t" \
  "v_exp_f32 v138, v138\n\t" \
  "v_exp_f32 v140, v140\n\t" \
  "v_exp_f32 v142, v142\n\t" \
  "v_exp_f32 v144, v144\n\t" \
  "v_exp_f32 v146, v146\n\t" \
  "v_exp_f32 v148, v148\n\t" \
  "v_exp_f32 v150, v150\n\t" \
  "v_exp_f32 v152, v152\n\t" \
  "v_exp_f32 v154, v154\n\t" \
  "v_exp_f32 v156, v156\n\t" \
  "v_exp_f32 v158, v158\n\t" \
  "v_add_f32 v4, v128, v130\n\t" \
  "v_add_f32 v8, v132, v134\n\t" \
  "v_add_f32 v5, v136, v138\n\t" \
  "v_add_f32 v9, v140, v142\n\t" \
  "v_add_f32 v6, v144, v146\n\t" \
  "v_add_f32 v10, v148, v150\n\t" \
  "v_add_f32 v7, v152, v154\n\t" \
  "v_add_f32 v11, v156, v158\n\t" \
  "v_add_f32 v4, v4, v8\n\t" \
  "v_add_f32 v5, v5, v9\n\t" \
  "v_add_f32 v6, v6, v10\n\t" \
  "v_add_f32 v7, v7, v11\n\t" \
  "v_permlane16_swap_b32 v4, v5\n\t" \
  "s_nop 0\n\t" \
  "v_permlane16_swap_b32 v6, v7\n\t" \
  "v_add_f32 v4, v4, v5\n\t" \
  "v_add_f32 v6, v6, v7\n\t" \
  "s_nop 1\n\t" \
  "v_permlane32_swap_b32 v4, v6\n\t" \
  "v_add_f32 v4, v4, v6\n\t" \
  "v_rcp_f32 v4, v4\n\t" \
  "s_nop 0\n\t" \
  "v_mov_b32 v6, v4\n\t" \
  "s_nop 1\n\t" \
  "v_permlane32_swap_b32 v4, v6\n\t" \
  "v_mov_b32 v5, v4\n\t" \
  "v_mov_b32 v7, v6\n\t" \
  "s_nop 0\n\t" \
  "v_permlane16_swap_b32 v4, v5\n\t" \
  "v_permlane16_swap_b32 v6, v7\n\t" \
  "v_mul_f32 v128, v128, v4\n\t" \
  "v_mul_f32 v130, v130, v4\n\t" \
  "v_mul_f32 v132, v132, v4\n\t" \
  "v_mul_f32 v134, v134, v4\n\t" \
  "v_cvt_pk_bf16_f32 v128, v128, v130\n\t" \
  "v_cvt_pk_bf16_f32 v132, v132, v134\n\t" \
  "v_perm_b32 v68, 0, v128, v60\n\t" \
  "v_perm_b32 v69, 0, v128, v61\n\t" \
  "v_perm_b32 v70, 0, v132, v60\n\t" \
  "v_perm_b32 v71, 0, v132, v61\n\t" \
  "v_mul_f32 v136, v136, v5\n\t" \
  "v_mul_f32 v138, v138, v5\n\t" \
  "v_mfma_f32_16x16x32_bf16 v[128:131], v[20:23], v[68:71], 0\n\t" \
  "v_mul_f32 v140, v140, v5\n\t" \
  "v_mul_f32 v142, v142, v5\n\t" \
  "v_cvt_pk_bf16_f32 v136, v136, v138\n\t" \
  "v_mfma_f32_16x16x32_bf16 v[132:135], v[30:33], v[68:71], 0\n\t" \
  "v_cvt_pk_bf16_f32 v140, v140, v142\n\t" \
  "v_perm_b32 v76, 0, v136, v60\n\t" \
  "v_perm_b32 v77, 0, v136, v61\n\t" \
  "v_perm_b32 v78, 0, v140, v60\n\t" \
  "v_perm_b32 v79, 0, v140, v61\n\t" \
  "v_mul_f32 v144, v144, v6\n\t" \
  "v_mul_f32 v146, v146, v6\n\t" \
  "v_mfma_f32_16x16x32_bf16 v[136:139], v[22:25], v[76:79], 0\n\t" \
  "v_mul_f32 v148, v148, v6\n\t" \
  "v_mul_f32 v150, v150, v6\n\t" \
  "v_cvt_pk_bf16_f32 v144, v144, v146\n\t" \
  "v_mfma_f32_16x16x32_bf16 v[140:143], v[32:35], v[76:79], 0\n\t" \
  "v_cvt_pk_bf16_f32 v148, v148, v150\n\t" \
  "v_perm_b32 v84, 0, v144, v60\n\t" \
  "v_perm_b32 v85, 0, v144, v61\n\t" \
  "v_perm_b32 v86, 0, v148, v60\n\t" \
  "v_perm_b32 v87, 0, v148, v61\n\t" \
  "v_cvt_pk_bf16_f32 v100, v128, v129\n\t" \
  "v_cvt_pk_bf16_f32 v101, v130, v131\n\t" \
  "v_mfma_f32_16x16x32_bf16 v[144:147], v[24:27], v[84:87], 0\n\t" \
  "v_cvt_pk_bf16_f32 v102, v132, v133\n\t" \
  "v_cvt_pk_bf16_f32 v103, v134, v135\n\t" \
  "global_store_dwordx4 v62, v[100:103], %[ot] offset:0 sc1\n\t" \
  "v_mfma_f32_16x16x32_bf16 v[148:151], v[34:37], v[84:87], 0\n\t" \
  "v_mul_f32 v152, v152, v7\n\t" \
  "v_mul_f32 v154, v154, v7\n\t" \
  "v_mul_f32 v156, v156, v7\n\t" \
  "v_mul_f32 v158, v158, v7\n\t" \
  "v_cvt_pk_bf16_f32 v152, v152, v154\n\t" \
  "v_cvt_pk_bf16_f32 v156, v156, v158\n\t" \
  "v_perm_b32 v92, 0, v152, v60\n\t" \
  "v_perm_b32 v93, 0, v152, v61\n\t" \
  "v_perm_b32 v94, 0, v156, v60\n\t" \
  "v_perm_b32 v95, 0, v156, v61\n\t" \
  "v_cvt_pk_bf16_f32 v108, v136, v137\n\t" \
  "v_cvt_pk_bf16_f32 v109, v138, v139\n\t" \
  "v_mfma_f32_16x16x32_bf16 v[152:155], v[26:29], v[92:95], 0\n\t" \
  "v_cvt_pk_bf16_f32 v110, v140, v141\n\t" \
  "v_cvt_pk_bf16_f32 v111, v142, v143\n\t" \
  "global_store_dwordx4 v62, v[108:111], %[ot] offset:1024 sc1\n\t" \
  "v_mfma_f32_16x16x32_bf16 v[156:159], v[36:39], v[92:95], 0\n\t" \
  "v_cvt_pk_bf16_f32 v116, v144, v145\n\t" \
  "v_cvt_pk_bf16_f32 v117, v146, v147\n\t" \
  "v_cvt_pk_bf16_f32 v118, v148, v149\n\t" \
  "v_cvt_pk_bf16_f32 v119, v150, v151\n\t" \
  "global_store_dwordx4 v62, v[116:119], %[ou] offset:0 sc1\n\t" \
  "v_cvt_pk_bf16_f32 v124, v152, v153\n\t" \
  "v_cvt_pk_bf16_f32 v125, v154, v155\n\t" \
  "s_nop 0\n\t" \
  "v_cvt_pk_bf16_f32 v126, v156, v157\n\t" \
  "v_cvt_pk_bf16_f32 v127, v158, v159\n\t" \
  "global_store_dwordx4 v62, v[124:127], %[ou] offset:1024 sc1\n\t" \
  "s_branch AE_%=\n\t" \
  "AR_%=:\n\t" \
  "s_addk_i32 %[it], 1\n\t" \
  "s_cmp_gt_u32 %[it], 0x400000\n\t" \
  "s_cbranch_scc1 AY_%=\n\t" \
  "ds_read_b32 v0, v61\n\t" \
  "ds_read_b64 v[20:21], v60\n\t" \
  "ds_read_b64 v[30:31], v60 offset:8\n\t" \
  "s_mov_b64 exec, %[rm]\n\t" \
  "ds_read_b128 v[160:163], v63\n\t" \
  "s_mov_b64 exec, -1\n\t" \
  "s_branch AW_%=\n\t" \
  "AZ_%=:\n\t" \
  "v_mov_b32 v0, 0xff61b1e6\n\t" \
  "v_mov_b32 v1, 0xcfa8965f\n\t" \
  "s_nop 3\n\t" \
  "v_cndmask_b32_e64 v56, v0, v1, s[68:69]\n\t" \
  "v_cndmask_b32_e64 v57, v0, v1, s[70:71]\n\t" \
  "v_cndmask_b32_e64 v58, v0, v1, s[72:73]\n\t" \
  "v_cndmask_b32_e64 v59, v0, v1, s[74:75]\n\t" \
  "s_nop 1\n\t" \
  "s_branch AY_%=\n\t" \
  "AE_%=:\n\t" \
  "s_nop 1\n\t"
#define N2_CLOB "v0", "v1", "v2", "v3", "v4", "v5", "v6", "v7", "v8", "v9", "v10", "v11", "v12", "v13", "v14", "v15", "v16", "v17", "v18", "v19", "v20", "v21", "v22", "v23", "v24", "v25", "v26", "v27", "v28", "v29", "v30", "v31", "v32", "v33", "v34", "v35", "v36", "v37", "v38", "v39", "v40", "v41", "v42", "v43", "v44", "v45", "v46", "v47", "v48", "v49", "v50", "v51", "v52", "v53", "v54", "v55", "v60", "v61", "v64", "v65", "v66", "v67", "v68", "v69", "v70", "v71", "v72", "v73", "v74", "v75", "v76", "v77", "v78", "v79", "v80", "v81", "v82", "v83", "v84", "v85", "v86", "v87", "v88", "v89", "v90", "v91", "v92", "v93", "v94", "v95", "v96", "v97", "v98", "v99", "v100", "v101", "v102", "v103", "v104", "v105", "v106", "v107", "v108", "v109", "v110", "v111", "v112", "v113", "v114", "v115", "v116", "v117", "v118", "v119", "v120", "v121", "v122", "v123", "v124", "v125", "v126", "v127", "v128", "v129", "v130", "v131", "v132", "v133", "v134", "v135", "v136", "v137", "v138", "v139", "v140", "v141", "v142", "v143", "v144", "v145", "v146", "v147", "v148", "v149", "v150", "v151", "v152", "v153", "v154", "v155", "v156", "v157", "v158", "v159", "v160", "v161", "v162", "v163", "v164", "v165", "v166", "v167", "s68", "s69", "s70", "s71", "s72", "s73", "s74", "s75", "s76", "s77", "s78", "s79", "s80", "s81", "s82", "s83", "s84", "s85"

#define CV_PRE 1
#define CV_Y 1
#define CV_ASM \
  "CS_%=:\n\t" \
  "ds_read_b32 v34, v61\n\t" \
  "s_waitcnt lgkmcnt(0)\n\t" \
  "v_cmp_ne_u32 vcc, %[tg], v34\n\t" \
  "s_cbranch_vccz CR_%=\n\t" \
  "s_cmp_eq_u32 %[rem], 0\n\t" \
  "s_cbranch_scc1 CT_%=\n\t" \
  "s_barrier\n\t" \
  "s_sub_u32 %[rem], %[rem], 1\n\t" \
  "s_branch CS_%=\n\t" \
  "CT_%=:\n\t" \
  "s_addk_i32 %[cn], 1\n\t" \
  "s_cmp_gt_u32 %[cn], 0x400000\n\t" \
  "s_cbranch_scc1 CR_%=\n\t" \
  "s_sleep 1\n\t" \
  "s_branch CS_%=\n\t" \
  "CR_%=:\n\t" \
  "ds_read_b64 v[0:1], v28 offset:0\n\t" \
  "ds_read_b64 v[2:3], v29 offset:0\n\t" \
  "ds_read_b64 v[4:5], v30 offset:1024\n\t" \
  "ds_read_b64 v[6:7], v31 offset:1024\n\t" \
  "ds_read_b64 v[8:9], v24 offset:2048\n\t" \
  "ds_read_b64 v[10:11], v25 offset:2048\n\t" \
  "ds_read_b64 v[12:13], v26 offset:3072\n\t" \
  "ds_read_b64 v[14:15], v27 offset:3072\n\t" \
  "ds_read_b64 v[16:17], v28 offset:4096\n\t" \
  "ds_read_b64 v[18:19], v29 offset:4096\n\t" \
  "ds_read_b64 v[20:21], v30 offset:5120\n\t" \
  "ds_read_b64 v[22:23], v31 offset:5120\n\t" \
  "s_mov_b64 vcc, 1\n\t" \
  "s_setprio 0\n\t" \
  "s_cmp_eq_u32 %[rem], 0\n\t" \
  "s_cbranch_scc1 CM_%=\n\t" \
  "CB_%=:\n\t" \
  "s_barrier\n\t" \
  "s_sub_u32 %[rem], %[rem], 1\n\t" \
  "s_cmp_lg_u32 %[rem], 0\n\t" \
  "s_cbranch_scc1 CB_%=\n\t" \
  "CM_%=:\n\t" \
  "s_waitcnt lgkmcnt(0)\n\t" \
  "v_mfma_f32_16x16x32_bf16 v[128:131], v[68:71], v[0:3], 0\n\t" \
  "v_mfma_f32_16x16x32_bf16 v[128:131], v[64:67], v[4:7], v[128:131]\n\t" \
  "v_mfma_f32_16x16x32_bf16 v[132:135], v[76:79], v[0:3], 0\n\t" \
  "v_mfma_f32_16x16x32_bf16 v[132:135], v[72:75], v[4:7], v[132:135]\n\t" \
  "v_mfma_f32_16x16x32_bf16 v[136:139], v[84:87], v[0:3], 0\n\t" \
  "v_mfma_f32_16x16x32_bf16 v[136:139], v[80:83], v[4:7], v[136:139]\n\t" \
  "v_mfma_f32_16x16x32_bf16 v[140:143], v[92:95], v[0:3], 0\n\t" \
  "s_setprio 1\n\t" \
  "v_mfma_f32_16x16x32_bf16 v[140:143], v[88:91], v[4:7], v[140:143]\n\t" \
  "v_mfma_f32_16x16x32_bf16 v[144:147], v[100:103], v[0:3], 0\n\t" \
  "ds_bpermute_b32 v34, v32, v131\n\t" \
  "v_mfma_f32_16x16x32_bf16 v[144:147], v[96:99], v[4:7], v[144:147]\n\t" \
  "v_mov_b32 v131, v130\n\t" \
  "v_mov_b32 v130, v129\n\t" \
  "v_mfma_f32_16x16x32_bf16 v[148:151], v[108:111], v[0:3], 0\n\t" \
  "v_mov_b32 v129, v128\n\t" \
  "v_mfma_f32_16x16x32_bf16 v[148:151], v[104:107], v[4:7], v[148:151]\n\t" \
  "v_mfma_f32_16x16x32_bf16 v[152:155], v[116:119], v[0:3], 0\n\t" \
  "v_mfma_f32_16x16x32_bf16 v[152:155], v[112:115], v[4:7], v[152:155]\n\t" \
  "v_mfma_f32_16x16x32_bf16 v[156:159], v[124:127], v[0:3], 0\n\t" \
  "ds_read_b64 v[0:1], v24 offset:6144\n\t" \
  "v_mfma_f32_16x16x32_bf16 v[156:159], v[120:123], v[4:7], v[156:159]\n\t" \
  "ds_read_b64 v[2:3], v25 offset:6144\n\t" \
  "v_mfma_f32_16x16x32_bf16 v[132:135], v[68:71], v[8:11], v[132:135]\n\t" \
  "ds_read_b64 v[4:5], v26 offset:7168\n\t" \
  "v_mfma_f32_16x16x32_bf16 v[132:135], v[64:67], v[12:15], v[132:135]\n\t" \
  "ds_read_b64 v[6:7], v27 offset:7168\n\t" \
  "v_mfma_f32_16x16x32_bf16 v[136:139], v[76:79], v[8:11], v[136:139]\n\t" \
  "v_mfma_f32_16x16x32_bf16 v[136:139], v[72:75], v[12:15], v[136:139]\n\t" \
  "s_waitcnt lgkmcnt(4)\n\t" \
  "v_cndmask_b32_e64 v128, v34, 0, vcc\n\t" \
  "v_mfma_f32_16x16x32_bf16 v[140:143], v[84:87], v[8:11], v[140:143]\n\t" \
  "v_mfma_f32_16x16x32_bf16 v[140:143], v[80:83], v[12:15], v[140:143]\n\t" \
  "v_mfma_f32_16x16x32_bf16 v[144:147], v[92:95], v[8:11], v[144:147]\n\t" \
  "ds_bpermute_b32 v35, v32, v135\n\t" \
  "v_mfma_f32_16x16x32_bf16 v[144:147], v[88:91], v[12:15], v[144:147]\n\t" \
  "v_mov_b32 v135, v134\n\t" \
  "v_mov_b32 v134, v133\n\t" \
  "v_mfma_f32_16x16x32_bf16 v[148:151], v[100:103], v[8:11], v[148:151]\n\t" \
  "v_mov_b32 v133, v132\n\t" \
  "v_mfma_f32_16x16x32_bf16 v[148:151], v[96:99], v[12:15], v[148:151]\n\t" \
  "v_mfma_f32_16x16x32_bf16 v[152:155], v[108:111], v[8:11], v[152:155]\n\t" \
  "v_mfma_f32_16x16x32_bf16 v[152:155], v[104:107], v[12:15], v[152:155]\n\t" \
  "v_mfma_f32_16x16x32_bf16 v[156:159], v[116:119], v[8:11], v[156:159]\n\t" \
  "ds_read_b64 v[8:9], v28 offset:8192\n\t" \
  "v_mfma_f32_16x16x32_bf16 v[156:159], v[112:115], v[12:15], v[156:159]\n\t" \
  "ds_read_b64 v[10:11], v29 offset:8192\n\t" \
  "v_mfma_f32_16x16x32_bf16 v[136:139], v[68:71], v[16:19], v[136:139]\n\t" \
  "ds_read_b64 v[12:13], v30 offset:9216\n\t" \
  "v_mfma_f32_16x16x32_bf16 v[136:139], v[64:67], v[20:23], v[136:139]\n\t" \
  "ds_read_b64 v[14:15], v31 offset:9216\n\t" \
  "v_mfma_f32_16x16x32_bf16 v[140:143], v[76:79], v[16:19], v[140:143]\n\t" \
  "v_mfma_f32_16x16x32_bf16 v[140:143], v[72:75], v[20:23], v[140:143]\n\t" \
  "s_waitcnt lgkmcnt(4)\n\t" \
  "v_cndmask_b32_e64 v132, v35, v34, vcc\n\t" \
  "v_mfma_f32_16x16x32_bf16 v[144:147], v[84:87], v[16:19], v[144:147]\n\t" \
  "v_mfma_f32_16x16x32_bf16 v[144:147], v[80:83], v[20:23], v[144:147]\n\t" \
  "v_mfma_f32_16x16x32_bf16 v[148:151], v[92:95], v[16:19], v[148:151]\n\t" \
  "ds_bpermute_b32 v36, v32, v139\n\t" \
  "v_mfma_f32_16x16x32_bf16 v[148:151], v[88:91], v[20:23], v[148:151]\n\t" \
  "v_mov_b32 v139, v138\n\t" \
  "v_mov_b32 v138, v137\n\t" \
  "v_mfma_f32_16x16x32_bf16 v[152:155], v[100:103], v[16:19], v[152:155]\n\t" \
  "v_mov_b32 v137, v136\n\t" \
  "v_mfma_f32_16x16x32_bf16 v[152:155], v[96:99], v[20:23], v[152:155]\n\t" \
  "v_mfma_f32_16x16x32_bf16 v[156:159], v[108:111], v[16:19], v[156:159]\n\t" \
  "ds_read_b64 v[16:17], v24 offset:10240\n\t" \
  "v_mfma_f32_16x16x32_bf16 v[156:159], v[104:107], v[20:23], v[156:159]\n\t" \
  "ds_read_b64 v[18:19], v25 offset:10240\n\t" \
  "v_mfma_f32_16x16x32_bf16 v[140:143], v[68:71], v[0:3], v[140:143]\n\t" \
  "ds_read_b64 v[20:21], v26 offset:11264\n\t" \
  "v_mfma_f32_16x16x32_bf16 v[140:143], v[64:67], v[4:7], v[140:143]\n\t" \
  "ds_read_b64 v[22:23], v27 offset:11264\n\t" \
  "v_mfma_f32_16x16x32_bf16 v[144:147], v[76:79], v[0:3], v[144:147]\n\t" \
  "v_mfma_f32_16x16x32_bf16 v[144:147], v[72:75], v[4:7], v[144:147]\n\t" \
  "v_mfma_f32_16x16x32_bf16 v[148:151], v[84:87], v[0:3], v[148:151]\n\t" \
  "v_mfma_f32_16x16x32_bf16 v[148:151], v[80:83], v[4:7], v[148:151]\n\t" \
  "s_waitcnt lgkmcnt(4)\n\t" \
  "v_cndmask_b32_e64 v136, v36, v35, vcc\n\t" \
  "v_mfma_f32_16x16x32_bf16 v[152:155], v[92:95], v[0:3], v[152:155]\n\t" \
  "ds_bpermute_b32 v37, v32, v143\n\t" \
  "v_mfma_f32_16x16x32_bf16 v[152:155], v[88:91], v[4:7], v[152:155]\n\t" \
  "v_mov_b32 v143, v142\n\t" \
  "v_mov_b32 v142, v141\n\t" \
  "v_mfma_f32_16x16x32_bf16 v[156:159], v[100:103], v[0:3], v[156:159]\n\t" \
  "ds_read_b64 v[0:1], v28 offset:12288\n\t" \
  "v_mov_b32 v141, v140\n\t" \
  "v_mfma_f32_16x16x32_bf16 v[156:159], v[96:99], v[4:7], v[156:159]\n\t" \
  "ds_read_b64 v[2:3], v29 offset:12288\n\t" \
  "v_mfma_f32_16x16x32_bf16 v[144:147], v[68:71], v[8:11], v[144:147]\n\t" \
  "ds_read_b64 v[4:5], v30 offset:13312\n\t" \
  "v_mfma_f32_16x16x32_bf16 v[144:147], v[64:67], v[12:15], v[144:147]\n\t" \
  "ds_read_b64 v[6:7], v31 offset:13312\n\t" \
  "v_mfma_f32_16x16x32_bf16 v[148:151], v[76:79], v[8:11], v[148:151]\n\t" \
  "v_mfma_f32_16x16x32_bf16 v[148:151], v[72:75], v[12:15], v[148:151]\n\t" \
  "v_mfma_f32_16x16x32_bf16 v[152:155], v[84:87], v[8:11], v[152:155]\n\t" \
  "v_mfma_f32_16x16x32_bf16 v[152:155], v[80:83], v[12:15], v[152:155]\n\t" \
  "v_mfma_f32_16x16x32_bf16 v[156:159], v[92:95], v[8:11], v[156:159]\n\t" \
  "ds_read_b64 v[8:9], v24 offset:14336\n\t" \
  "v_mfma_f32_16x16x32_bf16 v[156:159], v[88:91], v[12:15], v[156:159]\n\t" \
  "ds_read_b64 v[10:11], v25 offset:14336\n\t" \
  "s_waitcnt lgkmcnt(6)\n\t" \
  "v_mfma_f32_16x16x32_bf16 v[148:151], v[68:71], v[16:19], v[148:151]\n\t" \
  "ds_read_b64 v[12:13], v26 offset:15360\n\t" \
  "v_cndmask_b32_e64 v140, v37, v36, vcc\n\t" \
  "v_mfma_f32_16x16x32_bf16 v[148:151], v[64:67], v[20:23], v[148:151]\n\t" \
  "ds_read_b64 v[14:15], v27 offset:15360\n\t" \
  "v_mfma_f32_16x16x32_bf16 v[152:155], v[76:79], v[16:19], v[152:155]\n\t" \
  "ds_bpermute_b32 v38, v32, v147\n\t" \
  "v_mfma_f32_16x16x32_bf16 v[152:155], v[72:75], v[20:23], v[152:155]\n\t" \
  "v_mov_b32 v147, v146\n\t" \
  "v_mov_b32 v146, v145\n\t" \
  "v_mfma_f32_16x16x32_bf16 v[156:159], v[84:87], v[16:19], v[156:159]\n\t" \
  "ds_read_b64 v[16:17], v26 offset:0\n\t" \
  "ds_read_b64 v[18:19], v27 offset:0\n\t" \
  "v_mfma_f32_16x16x32_bf16 v[156:159], v[80:83], v[20:23], v[156:159]\n\t" \
  "ds_read_b64 v[20:21], v28 offset:1024\n\t" \
  "ds_read_b64 v[22:23], v29 offset:1024\n\t" \
  "s_waitcnt lgkmcnt(9)\n\t" \
  "v_mfma_f32_16x16x32_bf16 v[152:155], v[68:71], v[0:3], v[152:155]\n\t" \
  "ds_bpermute_b32 v39, v32, v151\n\t" \
  "v_mov_b32 v145, v144\n\t" \
  "v_mfma_f32_16x16x32_bf16 v[152:155], v[64:67], v[4:7], v[152:155]\n\t" \
  "v_mov_b32 v151, v150\n\t" \
  "v_mov_b32 v150, v149\n\t" \
  "v_mfma_f32_16x16x32_bf16 v[156:159], v[76:79], v[0:3], v[156:159]\n\t" \
  "ds_read_b64 v[0:1], v30 offset:2048\n\t" \
  "v_mov_b32 v149, v148\n\t" \
  "v_mfma_f32_16x16x32_bf16 v[156:159], v[72:75], v[4:7], v[156:159]\n\t" \
  "ds_read_b64 v[2:3], v31 offset:2048\n\t" \
  "s_waitcnt lgkmcnt(10)\n\t" \
  "v_mfma_f32_16x16x32_bf16 v[156:159], v[68:71], v[8:11], v[156:159]\n\t" \
  "ds_bpermute_b32 v40, v32, v155\n\t" \
  "s_waitcnt lgkmcnt(9)\n\t" \
  "v_mfma_f32_16x16x32_bf16 v[156:159], v[64:67], v[12:15], v[156:159]\n\t" \
  "ds_read_b64 v[4:5], v24 offset:3072\n\t" \
  "s_waitcnt lgkmcnt(7)\n\t" \
  "v_mfma_f32_16x16x32_bf16 v[128:131], v[68:71], v[16:19], v[128:131]\n\t" \
  "ds_read_b64 v[6:7], v25 offset:3072\n\t" \
  "s_waitcnt lgkmcnt(6)\n\t" \
  "v_mfma_f32_16x16x32_bf16 v[128:131], v[64:67], v[20:23], v[128:131]\n\t" \
  "v_cndmask_b32_e64 v144, v38, v37, vcc\n\t" \
  "v_mov_b32 v155, v154\n\t" \
  "v_mfma_f32_16x16x32_bf16 v[132:135], v[76:79], v[16:19], v[132:135]\n\t" \
  "ds_bpermute_b32 v41, v32, v159\n\t" \
  "v_mov_b32 v154, v153\n\t" \
  "v_mfma_f32_16x16x32_bf16 v[132:135], v[72:75], v[20:23], v[132:135]\n\t" \
  "v_mov_b32 v153, v152\n\t" \
  "v_mov_b32 v159, v158\n\t" \
  "v_mfma_f32_16x16x32_bf16 v[136:139], v[84:87], v[16:19], v[136:139]\n\t" \
  "v_mov_b32 v158, v157\n\t" \
  "v_mov_b32 v157, v156\n\t" \
  "v_mfma_f32_16x16x32_bf16 v[136:139], v[80:83], v[20:23], v[136:139]\n\t" \
  "s_waitcnt lgkmcnt(6)\n\t" \
  "v_cndmask_b32_e64 v148, v39, v38, vcc\n\t" \
  "v_mfma_f32_16x16x32_bf16 v[140:143], v[92:95], v[16:19], v[140:143]\n\t" \
  "ds_read_b64 v[8:9], v26 offset:4096\n\t" \
  "v_mfma_f32_16x16x32_bf16 v[140:143], v[88:91], v[20:23], v[140:143]\n\t" \
  "ds_read_b64 v[10:11], v27 offset:4096\n\t" \
  "v_mfma_f32_16x16x32_bf16 v[144:147], v[100:103], v[16:19], v[144:147]\n\t" \
  "s_waitcnt lgkmcnt(5)\n\t" \
  "v_cndmask_b32_e64 v152, v40, v39, vcc\n\t" \
  "v_mfma_f32_16x16x32_bf16 v[144:147], v[96:99], v[20:23], v[144:147]\n\t" \
  "global_load_dwordx4 v[44:47], v43, %[q0] offset:0\n\t" \
  "v_mfma_f32_16x16x32_bf16 v[148:151], v[108:111], v[16:19], v[148:151]\n\t" \
  "s_waitcnt lgkmcnt(2)\n\t" \
  "v_cndmask_b32_e64 v156, v41, v40, vcc\n\t" \
  "v_mfma_f32_16x16x32_bf16 v[148:151], v[104:107], v[20:23], v[148:151]\n\t" \
  "ds_read_b64 v[12:13], v28 offset:5120\n\t" \
  "v_mfma_f32_16x16x32_bf16 v[152:155], v[116:119], v[16:19], v[152:155]\n\t" \
  "ds_read_b64 v[14:15], v29 offset:5120\n\t" \
  "v_mfma_f32_16x16x32_bf16 v[152:155], v[112:115], v[20:23], v[152:155]\n\t" \
  "global_load_dwordx4 v[48:51], v43, %[q0] offset:2048\n\t" \
  "v_mfma_f32_16x16x32_bf16 v[156:159], v[124:127], v[16:19], v[156:159]\n\t" \
  "ds_read_b64 v[16:17], v30 offset:6144\n\t" \
  "v_mfma_f32_16x16x32_bf16 v[156:159], v[120:123], v[20:23], v[156:159]\n\t" \
  "ds_read_b64 v[18:19], v31 offset:6144\n\t" \
  "v_mfma_f32_16x16x32_bf16 v[132:135], v[68:71], v[0:3], v[132:135]\n\t" \
  "global_load_dwordx4 v[52:55], v43, %[q2] offset:0\n\t" \
  "v_mfma_f32_16x16x32_bf16 v[132:135], v[64:67], v[4:7], v[132:135]\n\t" \
  "ds_read_b64 v[20:21], v24 offset:7168\n\t" \
  "v_mfma_f32_16x16x32_bf16 v[136:139], v[76:79], v[0:3], v[136:139]\n\t" \
  "ds_read_b64 v[22:23], v25 offset:7168\n\t" \
  "v_mfma_f32_16x16x32_bf16 v[136:139], v[72:75], v[4:7], v[136:139]\n\t" \
  "ds_bpermute_b32 v34, v32, v131\n\t" \
  "v_mfma_f32_16x16x32_bf16 v[140:143], v[84:87], v[0:3], v[140:143]\n\t" \
  "global_load_dwordx4 v[56:59], v43, %[q2] offset:2048\n\t" \
  "v_mfma_f32_16x16x32_bf16 v[140:143], v[80:83], v[4:7], v[140:143]\n\t" \
  "ds_bpermute_b32 v35, v32, v135\n\t" \
  "v_mov_b32 v131, v130\n\t" \
  "v_mfma_f32_16x16x32_bf16 v[144:147], v[92:95], v[0:3], v[144:147]\n\t" \
  "v_mov_b32 v130, v129\n\t" \
  "v_mov_b32 v129, v128\n\t" \
  "v_mfma_f32_16x16x32_bf16 v[144:147], v[88:91], v[4:7], v[144:147]\n\t" \
  "v_mov_b32 v135, v134\n\t" \
  "v_mov_b32 v134, v133\n\t" \
  "v_mfma_f32_16x16x32_bf16 v[148:151], v[100:103], v[0:3], v[148:151]\n\t" \
  "v_mov_b32 v133, v132\n\t" \
  "v_mfma_f32_16x16x32_bf16 v[148:151], v[96:99], v[4:7], v[148:151]\n\t" \
  "v_mfma_f32_16x16x32_bf16 v[152:155], v[108:111], v[0:3], v[152:155]\n\t" \
  "v_mfma_f32_16x16x32_bf16 v[152:155], v[104:107], v[4:7], v[152:155]\n\t" \
  "v_mfma_f32_16x16x32_bf16 v[156:159], v[116:119], v[0:3], v[156:159]\n\t" \
  "ds_read_b64 v[0:1], v26 offset:8192\n\t" \
  "v_mfma_f32_16x16x32_bf16 v[156:159], v[112:115], v[4:7], v[156:159]\n\t" \
  "ds_read_b64 v[2:3], v27 offset:8192\n\t" \
  "s_waitcnt lgkmcnt(4)\n\t" \
  "v_mfma_f32_16x16x32_bf16 v[136:139], v[68:71], v[8:11], v[136:139]\n\t" \
  "ds_read_b64 v[4:5], v28 offset:9216\n\t" \
  "v_mfma_f32_16x16x32_bf16 v[136:139], v[64:67], v[12:15], v[136:139]\n\t" \
  "ds_read_b64 v[6:7], v29 offset:9216\n\t" \
  "v_mfma_f32_16x16x32_bf16 v[140:143], v[76:79], v[8:11], v[140:143]\n\t" \
  "s_waitcnt lgkmcnt(5)\n\t" \
  "v_cndmask_b32_e64 v128, v34, 0, vcc\n\t" \
  "v_mfma_f32_16x16x32_bf16 v[140:143], v[72:75], v[12:15], v[140:143]\n\t" \
  "s_waitcnt lgkmcnt(4)\n\t" \
  "v_cndmask_b32_e64 v132, v35, v34, vcc\n\t" \
  "v_mfma_f32_16x16x32_bf16 v[144:147], v[84:87], v[8:11], v[144:147]\n\t" \
  "ds_bpermute_b32 v36, v32, v139\n\t" \
  "v_mfma_f32_16x16x32_bf16 v[144:147], v[80:83], v[12:15], v[144:147]\n\t" \
  "v_mov_b32 v139, v138\n\t" \
  "v_mov_b32 v138, v137\n\t" \
  "v_mfma_f32_16x16x32_bf16 v[148:151], v[92:95], v[8:11], v[148:151]\n\t" \
  "v_mov_b32 v137, v136\n\t" \
  "v_mfma_f32_16x16x32_bf16 v[148:151], v[88:91], v[12:15], v[148:151]\n\t" \
  "v_mfma_f32_16x16x32_bf16 v[152:155], v[100:103], v[8:11], v[152:155]\n\t" \
  "v_mfma_f32_16x16x32_bf16 v[152:155], v[96:99], v[12:15], v[152:155]\n\t" \
  "v_mfma_f32_16x16x32_bf16 v[156:159], v[108:111], v[8:11], v[156:159]\n\t" \
  "ds_read_b64 v[8:9], v30 offset:10240\n\t" \
  "v_mfma_f32_16x16x32_bf16 v[156:159], v[104:107], v[12:15], v[156:159]\n\t" \
  "ds_read_b64 v[10:11], v31 offset:10240\n\t" \
  "v_mfma_f32_16x16x32_bf16 v[140:143], v[68:71], v[16:19], v[140:143]\n\t" \
  "ds_read_b64 v[12:13], v24 offset:11264\n\t" \
  "v_mfma_f32_16x16x32_bf16 v[140:143], v[64:67], v[20:23], v[140:143]\n\t" \
  "ds_read_b64 v[14:15], v25 offset:11264\n\t" \
  "v_mfma_f32_16x16x32_bf16 v[144:147], v[76:79], v[16:19], v[144:147]\n\t" \
  "v_mfma_f32_16x16x32_bf16 v[144:147], v[72:75], v[20:23], v[144:147]\n\t" \
  "s_waitcnt lgkmcnt(4)\n\t" \
  "v_cndmask_b32_e64 v136, v36, v35, vcc\n\t" \
  "v_mfma_f32_16x16x32_bf16 v[148:151], v[84:87], v[16:19], v[148:151]\n\t" \
  "v_mfma_f32_16x16x32_bf16 v[148:151], v[80:83], v[20:23], v[148:151]\n\t" \
  "v_mfma_f32_16x16x32_bf16 v[152:155], v[92:95], v[16:19], v[152:155]\n\t" \
  "ds_bpermute_b32 v37, v32, v143\n\t" \
  "v_mfma_f32_16x16x32_bf16 v[152:155], v[88:91], v[20:23], v[152:155]\n\t" \
  "v_mov_b32 v143, v142\n\t" \
  "v_mov_b32 v142, v141\n\t" \
  "v_mfma_f32_16x16x32_bf16 v[156:159], v[100:103], v[16:19], v[156:159]\n\t" \
  "ds_read_b64 v[16:17], v26 offset:12288\n\t" \
  "v_mov_b32 v141, v140\n\t" \
  "v_mfma_f32_16x16x32_bf16 v[156:159], v[96:99], v[20:23], v[156:159]\n\t" \
  "ds_read_b64 v[18:19], v27 offset:12288\n\t" \
  "v_mfma_f32_16x16x32_bf16 v[144:147], v[68:71], v[0:3], v[144:147]\n\t" \
  "ds_read_b64 v[20:21], v28 offset:13312\n\t" \
  "v_mfma_f32_16x16x32_bf16 v[144:147], v[64:67], v[4:7], v[144:147]\n\t" \
  "ds_read_b64 v[22:23], v29 offset:13312\n\t" \
  "v_mfma_f32_16x16x32_bf16 v[148:151], v[76:79], v[0:3], v[148:151]\n\t" \
  "v_mfma_f32_16x16x32_bf16 v[148:151], v[72:75], v[4:7], v[148:151]\n\t" \
  "v_mfma_f32_16x16x32_bf16 v[152:155], v[84:87], v[0:3], v[152:155]\n\t" \
  "v_mfma_f32_16x16x32_bf16 v[152:155], v[80:83], v[4:7], v[152:155]\n\t" \
  "v_mfma_f32_16x16x32_bf16 v[156:159], v[92:95], v[0:3], v[156:159]\n\t" \
  "ds_read_b64 v[0:1], v30 offset:14336\n\t" \
  "v_mfma_f32_16x16x32_bf16 v[156:159], v[88:91], v[4:7], v[156:159]\n\t" \
  "ds_read_b64 v[2:3], v31 offset:14336\n\t" \
  "s_waitcnt lgkmcnt(6)\n\t" \
  "v_mfma_f32_16x16x32_bf16 v[148:151], v[68:71], v[8:11], v[148:151]\n\t" \
  "ds_read_b64 v[4:5], v24 offset:15360\n\t" \
  "v_cndmask_b32_e64 v140, v37, v36, vcc\n\t" \
  "v_mfma_f32_16x16x32_bf16 v[148:151], v[64:67], v[12:15], v[148:151]\n\t" \
  "ds_read_b64 v[6:7], v25 offset:15360\n\t" \
  "v_mfma_f32_16x16x32_bf16 v[152:155], v[76:79], v[8:11], v[152:155]\n\t" \
  "ds_bpermute_b32 v38, v32, v147\n\t" \
  "v_mfma_f32_16x16x32_bf16 v[152:155], v[72:75], v[12:15], v[152:155]\n\t" \
  "v_mov_b32 v147, v146\n\t" \
  "v_mov_b32 v146, v145\n\t" \
  "v_mfma_f32_16x16x32_bf16 v[156:159], v[84:87], v[8:11], v[156:159]\n\t" \
  "ds_read_b64 v[8:9], v24 offset:0\n\t" \
  "ds_read_b64 v[10:11], v25 offset:0\n\t" \
  "v_mfma_f32_16x16x32_bf16 v[156:159], v[80:83], v[12:15], v[156:159]\n\t" \
  "ds_read_b64 v[12:13], v26 offset:1024\n\t" \
  "ds_read_b64 v[14:15], v27 offset:1024\n\t" \
  "s_waitcnt lgkmcnt(9)\n\t" \
  "v_mfma_f32_16x16x32_bf16 v[152:155], v[68:71], v[16:19], v[152:155]\n\t" \
  "ds_bpermute_b32 v39, v32, v151\n\t" \
  "v_mov_b32 v145, v144\n\t" \
  "v_mfma_f32_16x16x32_bf16 v[152:155], v[64:67], v[20:23], v[152:155]\n\t" \
  "v_mov_b32 v151, v150\n\t" \
  "v_mov_b32 v150, v149\n\t" \
  "v_mfma_f32_16x16x32_bf16 v[156:159], v[76:79], v[16:19], v[156:159]\n\t" \
  "ds_read_b64 v[16:17], v28 offset:2048\n\t" \
  "v_mov_b32 v149, v148\n\t" \
  "v_mfma_f32_16x16x32_bf16 v[156:159], v[72:75], v[20:23], v[156:159]\n\t" \
  "ds_read_b64 v[18:19], v29 offset:2048\n\t" \
  "s_waitcnt lgkmcnt(10)\n\t" \
  "v_mfma_f32_16x16x32_bf16 v[156:159], v[68:71], v[0:3], v[156:159]\n\t" \
  "ds_bpermute_b32 v40, v32, v155\n\t" \
  "s_waitcnt lgkmcnt(9)\n\t" \
  "v_mfma_f32_16x16x32_bf16 v[156:159], v[64:67], v[4:7], v[156:159]\n\t" \
  "ds_read_b64 v[20:21], v30 offset:3072\n\t" \
  "s_waitcnt lgkmcnt(7)\n\t" \
  "v_mfma_f32_16x16x32_bf16 v[128:131], v[68:71], v[8:11], v[128:131]\n\t" \
  "ds_read_b64 v[22:23], v31 offset:3072\n\t" \
  "s_waitcnt lgkmcnt(6)\n\t" \
  "v_mfma_f32_16x16x32_bf16 v[128:131], v[64:67], v[12:15], v[128:131]\n\t" \
  "v_cndmask_b32_e64 v144, v38, v37, vcc\n\t" \
  "v_mov_b32 v155, v154\n\t" \
  "v_mfma_f32_16x16x32_bf16 v[132:135], v[76:79], v[8:11], v[132:135]\n\t" \
  "ds_bpermute_b32 v41, v32, v159\n\t" \
  "v_mov_b32 v154, v153\n\t" \
  "v_mfma_f32_16x16x32_bf16 v[132:135], v[72:75], v[12:15], v[132:135]\n\t" \
  "v_mov_b32 v153, v152\n\t" \
  "v_mov_b32 v159, v158\n\t" \
  "v_mfma_f32_16x16x32_bf16 v[136:139], v[84:87], v[8:11], v[136:139]\n\t" \
  "v_mov_b32 v158, v157\n\t" \
  "v_mov_b32 v157, v156\n\t" \
  "v_mfma_f32_16x16x32_bf16 v[136:139], v[80:83], v[12:15], v[136:139]\n\t" \
  "s_waitcnt lgkmcnt(6)\n\t" \
  "v_cndmask_b32_e64 v148, v39, v38, vcc\n\t" \
  "v_mfma_f32_16x16x32_bf16 v[140:143], v[92:95], v[8:11], v[140:143]\n\t" \
  "ds_read_b64 v[0:1], v24 offset:4096\n\t" \
  "v_mfma_f32_16x16x32_bf16 v[140:143], v[88:91], v[12:15], v[140:143]\n\t" \
  "ds_read_b64 v[2:3], v25 offset:4096\n\t" \
  "v_mfma_f32_16x16x32_bf16 v[144:147], v[100:103], v[8:11], v[144:147]\n\t" \
  "s_waitcnt lgkmcnt(5)\n\t" \
  "v_cndmask_b32_e64 v152, v40, v39, vcc\n\t" \
  "v_mfma_f32_16x16x32_bf16 v[144:147], v[96:99], v[12:15], v[144:147]\n\t" \
  "ds_read_b64 v[4:5], v26 offset:5120\n\t" \
  "v_mfma_f32_16x16x32_bf16 v[148:151], v[108:111], v[8:11], v[148:151]\n\t" \
  "s_waitcnt lgkmcnt(3)\n\t" \
  "v_cndmask_b32_e64 v156, v41, v40, vcc\n\t" \
  "v_mfma_f32_16x16x32_bf16 v[148:151], v[104:107], v[12:15], v[148:151]\n\t" \
  "ds_read_b64 v[6:7], v27 offset:5120\n\t" \
  "v_mfma_f32_16x16x32_bf16 v[152:155], v[116:119], v[8:11], v[152:155]\n\t" \
  "ds_bpermute_b32 v34, v32, v131\n\t" \
  "v_mfma_f32_16x16x32_bf16 v[152:155], v[112:115], v[12:15], v[152:155]\n\t" \
  "v_mov_b32 v131, v130\n\t" \
  "v_mov_b32 v130, v129\n\t" \
  "v_mfma_f32_16x16x32_bf16 v[156:159], v[124:127], v[8:11], v[156:159]\n\t" \
  "ds_read_b64 v[8:9], v28 offset:6144\n\t" \
  "v_mov_b32 v129, v128\n\t" \
  "v_mfma_f32_16x16x32_bf16 v[156:159], v[120:123], v[12:15], v[156:159]\n\t" \
  "ds_read_b64 v[10:11], v29 offset:6144\n\t" \
  "v_mfma_f32_16x16x32_bf16 v[132:135], v[68:71], v[16:19], v[132:135]\n\t" \
  "ds_read_b64 v[12:13], v30 offset:7168\n\t" \
  "v_mfma_f32_16x16x32_bf16 v[132:135], v[64:67], v[20:23], v[132:135]\n\t" \
  "ds_read_b64 v[14:15], v31 offset:7168\n\t" \
  "v_mfma_f32_16x16x32_bf16 v[136:139], v[76:79], v[16:19], v[136:139]\n\t" \
  "v_mfma_f32_16x16x32_bf16 v[136:139], v[72:75], v[20:23], v[136:139]\n\t" \
  "v_mfma_f32_16x16x32_bf16 v[140:143], v[84:87], v[16:19], v[140:143]\n\t" \
  "v_mfma_f32_16x16x32_bf16 v[140:143], v[80:83], v[20:23], v[140:143]\n\t" \
  "v_mfma_f32_16x16x32_bf16 v[144:147], v[92:95], v[16:19], v[144:147]\n\t" \
  "v_mfma_f32_16x16x32_bf16 v[144:147], v[88:91], v[20:23], v[144:147]\n\t" \
  "s_waitcnt lgkmcnt(4)\n\t" \
  "v_cndmask_b32_e64 v128, v34, 0, vcc\n\t" \
  "v_mfma_f32_16x16x32_bf16 v[148:151], v[100:103], v[16:19], v[148:151]\n\t" \
  "ds_bpermute_b32 v35, v32, v135\n\t" \
  "v_mfma_f32_16x16x32_bf16 v[148:151], v[96:99], v[20:23], v[148:151]\n\t" \
  "v_mov_b32 v135, v134\n\t" \
  "v_mov_b32 v134, v133\n\t" \
  "v_mfma_f32_16x16x32_bf16 v[152:155], v[108:111], v[16:19], v[152:155]\n\t" \
  "v_mov_b32 v133, v132\n\t" \
  "v_mfma_f32_16x16x32_bf16 v[152:155], v[104:107], v[20:23], v[152:155]\n\t" \
  "v_mfma_f32_16x16x32_bf16 v[156:159], v[116:119], v[16:19], v[156:159]\n\t" \
  "ds_read_b64 v[16:17], v24 offset:8192\n\t" \
  "v_mfma_f32_16x16x32_bf16 v[156:159], v[112:115], v[20:23], v[156:159]\n\t" \
  "ds_read_b64 v[18:19], v25 offset:8192\n\t" \
  "v_mfma_f32_16x16x32_bf16 v[136:139], v[68:71], v[0:3], v[136:139]\n\t" \
  "ds_read_b64 v[20:21], v26 offset:9216\n\t" \
  "v_mfma_f32_16x16x32_bf16 v[136:139], v[64:67], v[4:7], v[136:139]\n\t" \
  "ds_read_b64 v[22:23], v27 offset:9216\n\t" \
  "v_mfma_f32_16x16x32_bf16 v[140:143], v[76:79], v[0:3], v[140:143]\n\t" \
  "v_mfma_f32_16x16x32_bf16 v[140:143], v[72:75], v[4:7], v[140:143]\n\t" \
  "v_mfma_f32_16x16x32_bf16 v[144:147], v[84:87], v[0:3], v[144:147]\n\t" \
  "v_mfma_f32_16x16x32_bf16 v[144:147], v[80:83], v[4:7], v[144:147]\n\t" \
  "s_waitcnt lgkmcnt(4)\n\t" \
  "v_cndmask_b32_e64 v132, v35, v34, vcc\n\t" \
  "v_mfma_f32_16x16x32_bf16 v[148:151], v[92:95], v[0:3], v[148:151]\n\t" \
  "ds_bpermute_b32 v36, v32, v139\n\t" \
  "v_mfma_f32_16x16x32_bf16 v[148:151], v[88:91], v[4:7], v[148:151]\n\t" \
  "v_mov_b32 v139, v138\n\t" \
  "v_mov_b32 v138, v137\n\t" \
  "v_mfma_f32_16x16x32_bf16 v[152:155], v[100:103], v[0:3], v[152:155]\n\t" \
  "v_mov_b32 v137, v136\n\t" \
  "v_mfma_f32_16x16x32_bf16 v[152:155], v[96:99], v[4:7], v[152:155]\n\t" \
  "v_mfma_f32_16x16x32_bf16 v[156:159], v[108:111], v[0:3], v[156:159]\n\t" \
  "ds_read_b64 v[0:1], v28 offset:10240\n\t" \
  "v_mfma_f32_16x16x32_bf16 v[156:159], v[104:107], v[4:7], v[156:159]\n\t" \
  "ds_read_b64 v[2:3], v29 offset:10240\n\t" \
  "v_mfma_f32_16x16x32_bf16 v[140:143], v[68:71], v[8:11], v[140:143]\n\t" \
  "ds_read_b64 v[4:5], v30 offset:11264\n\t" \
  "v_mfma_f32_16x16x32_bf16 v[140:143], v[64:67], v[12:15], v[140:143]\n\t" \
  "ds_read_b64 v[6:7], v31 offset:11264\n\t" \
  "v_mfma_f32_16x16x32_bf16 v[144:147], v[76:79], v[8:11], v[144:147]\n\t" \
  "v_mfma_f32_16x16x32_bf16 v[144:147], v[72:75], v[12:15], v[144:147]\n\t" \
  "v_mfma_f32_16x16x32_bf16 v[148:151], v[84:87], v[8:11], v[148:151]\n\t" \
  "v_mfma_f32_16x16x32_bf16 v[148:151], v[80:83], v[12:15], v[148:151]\n\t" \
  "s_waitcnt lgkmcnt(4)\n\t" \
  "v_cndmask_b32_e64 v136, v36, v35, vcc\n\t" \
  "v_mfma_f32_16x16x32_bf16 v[152:155], v[92:95], v[8:11], v[152:155]\n\t" \
  "ds_bpermute_b32 v37, v32, v143\n\t" \
  "v_mfma_f32_16x16x32_bf16 v[152:155], v[88:91], v[12:15], v[152:155]\n\t" \
  "v_mov_b32 v143, v142\n\t" \
  "v_mov_b32 v142, v141\n\t" \
  "v_mfma_f32_16x16x32_bf16 v[156:159], v[100:103], v[8:11], v[156:159]\n\t" \
  "ds_read_b64 v[8:9], v24 offset:12288\n\t" \
  "v_mov_b32 v141, v140\n\t" \
  "v_mfma_f32_16x16x32_bf16 v[156:159], v[96:99], v[12:15], v[156:159]\n\t" \
  "ds_read_b64 v[10:11], v25 offset:12288\n\t" \
  "v_mfma_f32_16x16x32_bf16 v[144:147], v[68:71], v[16:19], v[144:147]\n\t" \
  "ds_read_b64 v[12:13], v26 offset:13312\n\t" \
  "v_mfma_f32_16x16x32_bf16 v[144:147], v[64:67], v[20:23], v[144:147]\n\t" \
  "ds_read_b64 v[14:15], v27 offset:13312\n\t" \
  "v_mfma_f32_16x16x32_bf16 v[148:151], v[76:79], v[16:19], v[148:151]\n\t" \
  "v_mfma_f32_16x16x32_bf16 v[148:151], v[72:75], v[20:23], v[148:151]\n\t" \
  "v_mfma_f32_16x16x32_bf16 v[152:155], v[84:87], v[16:19], v[152:155]\n\t" \
  "v_mfma_f32_16x16x32_bf16 v[152:155], v[80:83], v[20:23], v[152:155]\n\t" \
  "v_mfma_f32_16x16x32_bf16 v[156:159], v[92:95], v[16:19], v[156:159]\n\t" \
  "ds_read_b64 v[16:17], v28 offset:14336\n\t" \
  "v_mfma_f32_16x16x32_bf16 v[156:159], v[88:91], v[20:23], v[156:159]\n\t" \
  "ds_read_b64 v[18:19], v29 offset:14336\n\t" \
  "s_waitcnt lgkmcnt(6)\n\t" \
  "v_mfma_f32_16x16x32_bf16 v[148:151], v[68:71], v[0:3], v[148:151]\n\t" \
  "ds_read_b64 v[20:21], v30 offset:15360\n\t" \
  "v_cndmask_b32_e64 v140, v37, v36, vcc\n\t" \
  "v_mfma_f32_16x16x32_bf16 v[148:151], v[64:67], v[4:7], v[148:151]\n\t" \
  "ds_read_b64 v[22:23], v31 offset:15360\n\t" \
  "v_mfma_f32_16x16x32_bf16 v[152:155], v[76:79], v[0:3], v[152:155]\n\t" \
  "ds_bpermute_b32 v38, v32, v147\n\t" \
  "v_mfma_f32_16x16x32_bf16 v[152:155], v[72:75], v[4:7], v[152:155]\n\t" \
  "v_mov_b32 v147, v146\n\t" \
  "v_mov_b32 v146, v145\n\t" \
  "v_mfma_f32_16x16x32_bf16 v[156:159], v[84:87], v[0:3], v[156:159]\n\t" \
  "ds_read_b64 v[0:1], v30 offset:0\n\t" \
  "ds_read_b64 v[2:3], v31 offset:0\n\t" \
  "v_mfma_f32_16x16x32_bf16 v[156:159], v[80:83], v[4:7], v[156:159]\n\t" \
  "ds_read_b64 v[4:5], v24 offset:1024\n\t" \
  "ds_read_b64 v[6:7], v25 offset:1024\n\t" \
  "s_waitcnt lgkmcnt(9)\n\t" \
  "v_mfma_f32_16x16x32_bf16 v[152:155], v[68:71], v[8:11], v[152:155]\n\t" \
  "ds_bpermute_b32 v39, v32, v151\n\t" \
  "v_mov_b32 v145, v144\n\t" \
  "v_mfma_f32_16x16x32_bf16 v[152:155], v[64:67], v[12:15], v[152:155]\n\t" \
  "v_mov_b32 v151, v150\n\t" \
  "v_mov_b32 v150, v149\n\t" \
  "v_mfma_f32_16x16x32_bf16 v[156:159], v[76:79], v[8:11], v[156:159]\n\t" \
  "ds_read_b64 v[8:9], v26 offset:2048\n\t" \
  "v_mov_b32 v149, v148\n\t" \
  "v_mfma_f32_16x16x32_bf16 v[156:159], v[72:75], v[12:15], v[156:159]\n\t" \
  "ds_read_b64 v[10:11], v27 offset:2048\n\t" \
  "s_waitcnt lgkmcnt(10)\n\t" \
  "v_mfma_f32_16x16x32_bf16 v[156:159], v[68:71], v[16:19], v[156:159]\n\t" \
  "ds_bpermute_b32 v40, v32, v155\n\t" \
  "s_waitcnt lgkmcnt(9)\n\t" \
  "v_mfma_f32_16x16x32_bf16 v[156:159], v[64:67], v[20:23], v[156:159]\n\t" \
  "ds_read_b64 v[12:13], v28 offset:3072\n\t" \
  "s_waitcnt lgkmcnt(7)\n\t" \
  "v_mfma_f32_16x16x32_bf16 v[128:131], v[68:71], v[0:3], v[128:131]\n\t" \
  "ds_read_b64 v[14:15], v29 offset:3072\n\t" \
  "s_waitcnt lgkmcnt(6)\n\t" \
  "v_mfma_f32_16x16x32_bf16 v[128:131], v[64:67], v[4:7], v[128:131]\n\t" \
  "v_cndmask_b32_e64 v144, v38, v37, vcc\n\t" \
  "v_mov_b32 v155, v154\n\t" \
  "v_mfma_f32_16x16x32_bf16 v[132:135], v[76:79], v[0:3], v[132:135]\n\t" \
  "ds_bpermute_b32 v41, v32, v159\n\t" \
  "v_mov_b32 v154, v153\n\t" \
  "v_mfma_f32_16x16x32_bf16 v[132:135], v[72:75], v[4:7], v[132:135]\n\t" \
  "v_mov_b32 v153, v152\n\t" \
  "v_mov_b32 v159, v158\n\t" \
  "v_mfma_f32_16x16x32_bf16 v[136:139], v[84:87], v[0:3], v[136:139]\n\t" \
  "v_mov_b32 v158, v157\n\t" \
  "v_mov_b32 v157, v156\n\t" \
  "v_mfma_f32_16x16x32_bf16 v[136:139], v[80:83], v[4:7], v[136:139]\n\t" \
  "s_waitcnt lgkmcnt(6)\n\t" \
  "v_cndmask_b32_e64 v148, v39, v38, vcc\n\t" \
  "v_mfma_f32_16x16x32_bf16 v[140:143], v[92:95], v[0:3], v[140:143]\n\t" \
  "ds_read_b64 v[16:17], v30 offset:4096\n\t" \
  "v_mfma_f32_16x16x32_bf16 v[140:143], v[88:91], v[4:7], v[140:143]\n\t" \
  "ds_read_b64 v[18:19], v31 offset:4096\n\t" \
  "v_mfma_f32_16x16x32_bf16 v[144:147], v[100:103], v[0:3], v[144:147]\n\t" \
  "s_waitcnt lgkmcnt(5)\n\t" \
  "v_cndmask_b32_e64 v152, v40, v39, vcc\n\t" \
  "v_mfma_f32_16x16x32_bf16 v[144:147], v[96:99], v[4:7], v[144:147]\n\t" \
  "ds_read_b64 v[20:21], v24 offset:5120\n\t" \
  "v_mfma_f32_16x16x32_bf16 v[148:151], v[108:111], v[0:3], v[148:151]\n\t" \
  "s_waitcnt lgkmcnt(3)\n\t" \
  "v_cndmask_b32_e64 v156, v41, v40, vcc\n\t" \
  "v_mfma_f32_16x16x32_bf16 v[148:151], v[104:107], v[4:7], v[148:151]\n\t" \
  "v_cvt_pk_bf16_f32 v34, v128, v129\n\t" \
  "v_cvt_pk_bf16_f32 v35, v130, v131\n\t" \
  "v_mfma_f32_16x16x32_bf16 v[152:155], v[116:119], v[0:3], v[152:155]\n\t" \
  "ds_read_b64 v[22:23], v25 offset:5120\n\t" \
  "v_mfma_f32_16x16x32_bf16 v[152:155], v[112:115], v[4:7], v[152:155]\n\t" \
  "v_mfma_f32_16x16x32_bf16 v[156:159], v[124:127], v[0:3], v[156:159]\n\t" \
  "ds_read_b64 v[0:1], v26 offset:6144\n\t" \
  "v_mfma_f32_16x16x32_bf16 v[156:159], v[120:123], v[4:7], v[156:159]\n\t" \
  "ds_read_b64 v[2:3], v27 offset:6144\n\t" \
  "v_mfma_f32_16x16x32_bf16 v[132:135], v[68:71], v[8:11], v[132:135]\n\t" \
  "ds_read_b64 v[4:5], v28 offset:7168\n\t" \
  "v_mfma_f32_16x16x32_bf16 v[132:135], v[64:67], v[12:15], v[132:135]\n\t" \
  "ds_read_b64 v[6:7], v29 offset:7168\n\t" \
  "v_mfma_f32_16x16x32_bf16 v[136:139], v[76:79], v[8:11], v[136:139]\n\t" \
  "v_mfma_f32_16x16x32_bf16 v[136:139], v[72:75], v[12:15], v[136:139]\n\t" \
  "v_mfma_f32_16x16x32_bf16 v[140:143], v[84:87], v[8:11], v[140:143]\n\t" \
  "v_mfma_f32_16x16x32_bf16 v[140:143], v[80:83], v[12:15], v[140:143]\n\t" \
  "v_mfma_f32_16x16x32_bf16 v[144:147], v[92:95], v[8:11], v[144:147]\n\t" \
  "v_mfma_f32_16x16x32_bf16 v[144:147], v[88:91], v[12:15], v[144:147]\n\t" \
  "v_mfma_f32_16x16x32_bf16 v[148:151], v[100:103], v[8:11], v[148:151]\n\t" \
  "v_cvt_pk_bf16_f32 v36, v132, v133\n\t" \
  "v_cvt_pk_bf16_f32 v37, v134, v135\n\t" \
  "v_mfma_f32_16x16x32_bf16 v[148:151], v[96:99], v[12:15], v[148:151]\n\t" \
  "v_mfma_f32_16x16x32_bf16 v[152:155], v[108:111], v[8:11], v[152:155]\n\t" \
  "v_permlane16_swap_b32 v34, v36\n\t" \
  "v_permlane16_swap_b32 v35, v37\n\t" \
  "v_mfma_f32_16x16x32_bf16 v[152:155], v[104:107], v[12:15], v[152:155]\n\t" \
  "global_store_dwordx4 v42, v[34:37], %[yb] offset:0 sc1\n\t" \
  "v_mfma_f32_16x16x32_bf16 v[156:159], v[116:119], v[8:11], v[156:159]\n\t" \
  "ds_read_b64 v[8:9], v30 offset:8192\n\t" \
  "v_mfma_f32_16x16x32_bf16 v[156:159], v[112:115], v[12:15], v[156:159]\n\t" \
  "ds_read_b64 v[10:11], v31 offset:8192\n\t" \
  "s_waitcnt lgkmcnt(2)\n\t" \
  "v_mfma_f32_16x16x32_bf16 v[136:139], v[68:71], v[16:19], v[136:139]\n\t" \
  "ds_read_b64 v[12:13], v24 offset:9216\n\t" \
  "v_mfma_f32_16x16x32_bf16 v[136:139], v[64:67], v[20:23], v[136:139]\n\t" \
  "ds_read_b64 v[14:15], v25 offset:9216\n\t" \
  "v_mfma_f32_16x16x32_bf16 v[140:143], v[76:79], v[16:19], v[140:143]\n\t" \
  "v_mfma_f32_16x16x32_bf16 v[140:143], v[72:75], v[20:23], v[140:143]\n\t" \
  "v_mfma_f32_16x16x32_bf16 v[144:147], v[84:87], v[16:19], v[144:147]\n\t" \
  "s_mov_b64 exec, 1\n\t" \
  "ds_write_b32 v33, v33\n\t" \
  "s_mov_b64 exec, -1\n\t" \
  "v_mfma_f32_16x16x32_bf16 v[144:147], v[80:83], v[20:23], v[144:147]\n\t" \
  "v_cvt_pk_bf16_f32 v38, v136, v137\n\t" \
  "v_cvt_pk_bf16_f32 v39, v138, v139\n\t" \
  "v_mfma_f32_16x16x32_bf16 v[148:151], v[92:95], v[16:19], v[148:151]\n\t" \
  "v_mfma_f32_16x16x32_bf16 v[148:151], v[88:91], v[20:23], v[148:151]\n\t" \
  "v_mfma_f32_16x16x32_bf16 v[152:155], v[100:103], v[16:19], v[152:155]\n\t" \
  "v_mfma_f32_16x16x32_bf16 v[152:155], v[96:99], v[20:23], v[152:155]\n\t" \
  "v_mfma_f32_16x16x32_bf16 v[156:159], v[108:111], v[16:19], v[156:159]\n\t" \
  "ds_read_b64 v[16:17], v26 offset:10240\n\t" \
  "v_mfma_f32_16x16x32_bf16 v[156:159], v[104:107], v[20:23], v[156:159]\n\t" \
  "ds_read_b64 v[18:19], v27 offset:10240\n\t" \
  "v_mfma_f32_16x16x32_bf16 v[140:143], v[68:71], v[0:3], v[140:143]\n\t" \
  "ds_read_b64 v[20:21], v28 offset:11264\n\t" \
  "v_mfma_f32_16x16x32_bf16 v[140:143], v[64:67], v[4:7], v[140:143]\n\t" \
  "ds_read_b64 v[22:23], v29 offset:11264\n\t" \
  "v_mfma_f32_16x16x32_bf16 v[144:147], v[76:79], v[0:3], v[144:147]\n\t" \
  "v_mfma_f32_16x16x32_bf16 v[144:147], v[72:75], v[4:7], v[144:147]\n\t" \
  "v_mfma_f32_16x16x32_bf16 v[148:151], v[84:87], v[0:3], v[148:151]\n\t" \
  "v_mfma_f32_16x16x32_bf16 v[148:151], v[80:83], v[4:7], v[148:151]\n\t" \
  "v_mfma_f32_16x16x32_bf16 v[152:155], v[92:95], v[0:3], v[152:155]\n\t" \
  "v_mfma_f32_16x16x32_bf16 v[152:155], v[88:91], v[4:7], v[152:155]\n\t" \
  "v_mfma_f32_16x16x32_bf16 v[156:159], v[100:103], v[0:3], v[156:159]\n\t" \
  "v_cvt_pk_bf16_f32 v40, v140, v141\n\t" \
  "v_cvt_pk_bf16_f32 v41, v142, v143\n\t" \
  "v_mfma_f32_16x16x32_bf16 v[156:159], v[96:99], v[4:7], v[156:159]\n\t" \
  "ds_read_b64 v[0:1], v30 offset:12288\n\t" \
  "s_waitcnt lgkmcnt(6)\n\t" \
  "v_mfma_f32_16x16x32_bf16 v[144:147], v[68:71], v[8:11], v[144:147]\n\t" \
  "v_permlane16_swap_b32 v38, v40\n\t" \
  "ds_read_b64 v[2:3], v31 offset:12288\n\t" \
  "v_mfma_f32_16x16x32_bf16 v[144:147], v[64:67], v[12:15], v[144:147]\n\t" \
  "v_permlane16_swap_b32 v39, v41\n\t" \
  "ds_read_b64 v[4:5], v24 offset:13312\n\t" \
  "v_mfma_f32_16x16x32_bf16 v[148:151], v[76:79], v[8:11], v[148:151]\n\t" \
  "ds_read_b64 v[6:7], v25 offset:13312\n\t" \
  "v_mfma_f32_16x16x32_bf16 v[148:151], v[72:75], v[12:15], v[148:151]\n\t" \
  "global_store_dwordx4 v42, v[38:41], %[yb] offset:1024 sc1\n\t" \
  "v_mfma_f32_16x16x32_bf16 v[152:155], v[84:87], v[8:11], v[152:155]\n\t" \
  "v_mfma_f32_16x16x32_bf16 v[152:155], v[80:83], v[12:15], v[152:155]\n\t" \
  "v_cvt_pk_bf16_f32 v34, v144, v145\n\t" \
  "v_cvt_pk_bf16_f32 v35, v146, v147\n\t" \
  "v_mfma_f32_16x16x32_bf16 v[156:159], v[92:95], v[8:11], v[156:159]\n\t" \
  "ds_read_b64 v[8:9], v26 offset:14336\n\t" \
  "v_mfma_f32_16x16x32_bf16 v[156:159], v[88:91], v[12:15], v[156:159]\n\t" \
  "ds_read_b64 v[10:11], v27 offset:14336\n\t" \
  "s_waitcnt lgkmcnt(6)\n\t" \
  "v_mfma_f32_16x16x32_bf16 v[148:151], v[68:71], v[16:19], v[148:151]\n\t" \
  "ds_read_b64 v[12:13], v28 offset:15360\n\t" \
  "v_mfma_f32_16x16x32_bf16 v[148:151], v[64:67], v[20:23], v[148:151]\n\t" \
  "ds_read_b64 v[14:15], v29 offset:15360\n\t" \
  "v_mfma_f32_16x16x32_bf16 v[152:155], v[76:79], v[16:19], v[152:155]\n\t" \
  "v_mfma_f32_16x16x32_bf16 v[152:155], v[72:75], v[20:23], v[152:155]\n\t" \
  "v_mfma_f32_16x16x32_bf16 v[156:159], v[84:87], v[16:19], v[156:159]\n\t" \
  "v_mfma_f32_16x16x32_bf16 v[156:159], v[80:83], v[20:23], v[156:159]\n\t" \
  "s_waitcnt lgkmcnt(4)\n\t" \
  "v_mfma_f32_16x16x32_bf16 v[152:155], v[68:71], v[0:3], v[152:155]\n\t" \
  "v_mfma_f32_16x16x32_bf16 v[152:155], v[64:67], v[4:7], v[152:155]\n\t" \
  "v_cvt_pk_bf16_f32 v36, v148, v149\n\t" \
  "v_cvt_pk_bf16_f32 v37, v150, v151\n\t" \
  "v_mfma_f32_16x16x32_bf16 v[156:159], v[76:79], v[0:3], v[156:159]\n\t" \
  "v_mfma_f32_16x16x32_bf16 v[156:159], v[72:75], v[4:7], v[156:159]\n\t" \
  "v_permlane16_swap_b32 v34, v36\n\t" \
  "s_waitcnt lgkmcnt(2)\n\t" \
  "v_mfma_f32_16x16x32_bf16 v[156:159], v[68:71], v[8:11], v[156:159]\n\t" \
  "v_permlane16_swap_b32 v35, v37\n\t" \
  "s_waitcnt lgkmcnt(0)\n\t" \
  "v_mfma_f32_16x16x32_bf16 v[156:159], v[64:67], v[12:15], v[156:159]\n\t" \
  "global_store_dwordx4 v42, v[34:37], %[yb] offset:2048 sc1\n\t" \
  "s_waitcnt lgkmcnt(0)\n\t" \
  "s_nop 4\n\t" \
  "v_cvt_pk_bf16_f32 v38, v152, v153\n\t" \
  "v_cvt_pk_bf16_f32 v39, v154, v155\n\t" \
  "v_cvt_pk_bf16_f32 v40, v156, v157\n\t" \
  "v_cvt_pk_bf16_f32 v41, v158, v159\n\t" \
  "s_nop 1\n\t" \
  "v_permlane16_swap_b32 v38, v40\n\t" \
  "v_permlane16_swap_b32 v39, v41\n\t" \
  "global_store_dwordx4 v42, v[38:41], %[yb] offset:3072 sc1\n\t" \
  "s_nop 1\n\t"
#define CV_Q 1
#define CV_CLOB "vcc", "v0", "v1", "v2", "v3", "v4", "v5", "v6", "v7", "v8", "v9", "v10", "v11", "v12", "v13", "v14", "v15", "v16", "v17", "v18", "v19", "v20", "v21", "v22", "v23", "v34", "v35", "v36", "v37", "v38", "v39"

#define G3_ASM \
  "v_readlane_b32 s64, v96, 0\n\t" \
  "v_readlane_b32 s65, v96, 1\n\t" \
  "v_readlane_b32 s66, v96, 2\n\t" \
  "v_readlane_b32 s67, v96, 3\n\t" \
  "v_readlane_b32 s68, v96, 4\n\t" \
  "v_readlane_b32 s69, v96, 5\n\t" \
  "v_readlane_b32 s70, v96, 6\n\t" \
  "v_readlane_b32 s71, v96, 7\n\t" \
  "v_readlane_b32 s72, v96, 8\n\t" \
  "v_readlane_b32 s73, v96, 9\n\t" \
  "v_readlane_b32 s74, v96, 10\n\t" \
  "v_readlane_b32 s75, v96, 11\n\t" \
  "v_readlane_b32 s76, v96, 12\n\t" \
  "v_readlane_b32 s77, v96, 13\n\t" \
  "v_readlane_b32 s78, v96, 14\n\t" \
  "v_readlane_b32 s79, v96, 15\n\t" \
  "v_add_u32 v97, %[go], v165\n\t" \
  "s_branch QG_%=\n\t" \
  "QA14_%=:\n\t" \
  "v_mov_b32 v165, %[sy]\n\t" \
  "s_sleep 1\n\t" \
  "ds_read_b32 v165, v165\n\t" \
  "s_addk_i32 %[cn], 1\n\t" \
  "s_cmp_gt_u32 %[cn], 0x400000\n\t" \
  "s_cbranch_scc1 QC14_%=\n\t" \
  "s_waitcnt lgkmcnt(0)\n\t" \
  "v_cmp_lt_u32 vcc, 15, v165\n\t" \
  "s_cbranch_vccz QA14_%=\n\t" \
  "QC14_%=:\n\t" \
  "s_waitcnt lgkmcnt(0)\n\t" \
  "s_branch QB14_%=\n\t" \
  "QA7_%=:\n\t" \
  "v_mov_b32 v165, %[sy]\n\t" \
  "s_sleep 1\n\t" \
  "ds_read_b32 v165, v165\n\t" \
  "s_addk_i32 %[cn], 1\n\t" \
  "s_cmp_gt_u32 %[cn], 0x400000\n\t" \
  "s_cbranch_scc1 QC7_%=\n\t" \
  "s_waitcnt lgkmcnt(0)\n\t" \
  "v_cmp_lt_u32 vcc, 13, v165\n\t" \
  "s_cbranch_vccz QA7_%=\n\t" \
  "QC7_%=:\n\t" \
  "s_waitcnt lgkmcnt(0)\n\t" \
  "s_branch QB7_%=\n\t" \
  "QS_%=:\n\t" \
  "s_sleep 1\n\t" \
  "QG_%=:\n\t" \
  "ds_read_b32 v98, v165\n\t" \
  "ds_read_b32 v99, v97\n\t" \
  "s_addk_i32 %[cn], 1\n\t" \
  "s_cmp_gt_u32 %[cn], 0x400000\n\t" \
  "s_cselect_b32 %[t2], 16, 0\n\t" \
  "s_waitcnt lgkmcnt(0)\n\t" \
  "v_readfirstlane_b32 %[t0], v98\n\t" \
  "v_readfirstlane_b32 %[t1], v99\n\t" \
  "s_max_u32 %[t0], %[t0], %[t2]\n\t" \
  "s_or_b32 %[t1], %[t1], %[t2]\n\t" \
  "s_min_u32 %[t2], %[t0], 5\n\t" \
  "s_cmp_ge_u32 %[iss], %[t2]\n\t" \
  "s_cbranch_scc1 QD_%=\n\t" \
  "s_cmp_eq_u32 %[iss], 1\n\t" \
  "s_cbranch_scc1 QL1_%=\n\t" \
  "s_cmp_eq_u32 %[iss], 2\n\t" \
  "s_cbranch_scc1 QL2_%=\n\t" \
  "s_cmp_eq_u32 %[iss], 3\n\t" \
  "s_cbranch_scc1 QL3_%=\n\t" \
  "s_cmp_eq_u32 %[iss], 4\n\t" \
  "s_cbranch_scc1 QL4_%=\n\t" \
  "QL0_%=:\n\t" \
  "buffer_load_dwordx4 v[0:3], v164, %[ob], s64 offen offset:0 sc1\n\t" \
  "buffer_load_dwordx4 v[4:7], v164, %[ob], s64 offen offset:1024 sc1\n\t" \
  "buffer_load_dwordx4 v[8:11], v164, %[ob], s64 offen offset:2048 sc1\n\t" \
  "buffer_load_dwordx4 v[12:15], v164, %[ob], s64 offen offset:3072 sc1\n\t" \
  "s_mov_b32 %[iss], 1\n\t" \
  "s_cmp_le_u32 %[t2], 1\n\t" \
  "s_cbranch_scc1 QD_%=\n\t" \
  "QL1_%=:\n\t" \
  "buffer_load_dwordx4 v[16:19], v164, %[ob], s65 offen offset:0 sc1\n\t" \
  "buffer_load_dwordx4 v[20:23], v164, %[ob], s65 offen offset:1024 sc1\n\t" \
  "buffer_load_dwordx4 v[24:27], v164, %[ob], s65 offen offset:2048 sc1\n\t" \
  "buffer_load_dwordx4 v[28:31], v164, %[ob], s65 offen offset:3072 sc1\n\t" \
  "s_mov_b32 %[iss], 2\n\t" \
  "s_cmp_le_u32 %[t2], 2\n\t" \
  "s_cbranch_scc1 QD_%=\n\t" \
  "QL2_%=:\n\t" \
  "buffer_load_dwordx4 v[32:35], v164, %[ob], s66 offen offset:0 sc1\n\t" \
  "buffer_load_dwordx4 v[36:39], v164, %[ob], s66 offen offset:1024 sc1\n\t" \
  "buffer_load_dwordx4 v[40:43], v164, %[ob], s66 offen offset:2048 sc1\n\t" \
  "buffer_load_dwordx4 v[44:47], v164, %[ob], s66 offen offset:3072 sc1\n\t" \
  "s_mov_b32 %[iss], 3\n\t" \
  "s_cmp_le_u32 %[t2], 3\n\t" \
  "s_cbranch_scc1 QD_%=\n\t" \
  "QL3_%=:\n\t" \
  "buffer_load_dwordx4 v[48:51], v164, %[ob], s67 offen offset:0 sc1\n\t" \
  "buffer_load_dwordx4 v[52:55], v164, %[ob], s67 offen offset:1024 sc1\n\t" \
  "buffer_load_dwordx4 v[56:59], v164, %[ob], s67 offen offset:2048 sc1\n\t" \
  "buffer_load_dwordx4 v[60:63], v164, %[ob], s67 offen offset:3072 sc1\n\t" \
  "s_mov_b32 %[iss], 4\n\t" \
  "s_cmp_le_u32 %[t2], 4\n\t" \
  "s_cbranch_scc1 QD_%=\n\t" \
  "QL4_%=:\n\t" \
  "buffer_load_dwordx4 v[64:67], v164, %[ob], s68 offen offset:0 sc1\n\t" \
  "buffer_load_dwordx4 v[68:71], v164, %[ob], s68 offen offset:1024 sc1\n\t" \
  "buffer_load_dwordx4 v[72:75], v164, %[ob], s68 offen offset:2048 sc1\n\t" \
  "buffer_load_dwordx4 v[76:79], v164, %[ob], s68 offen offset:3072 sc1\n\t" \
  "s_mov_b32 %[iss], 5\n\t" \
  "QD_%=:\n\t" \
  "s_cmp_lt_u32 %[t0], 7\n\t" \
  "s_cbranch_scc1 QS_%=\n\t" \
  "s_cmp_eq_u32 %[t1], 0\n\t" \
  "s_cbranch_scc1 QS_%=\n\t" \
  "s_setprio 0\n\t" \
  "ds_read_b64_tr_b16 v[96:97], v160 offset:0\n\t" \
  "ds_read_b64_tr_b16 v[98:99], v160 offset:2048\n\t" \
  "ds_read_b64_tr_b16 v[100:101], v161 offset:0\n\t" \
  "ds_read_b64_tr_b16 v[102:103], v161 offset:2048\n\t" \
  "ds_read_b64_tr_b16 v[104:105], v162 offset:0\n\t" \
  "ds_read_b64_tr_b16 v[106:107], v162 offset:2048\n\t" \
  "ds_read_b64_tr_b16 v[108:109], v163 offset:0\n\t" \
  "ds_read_b64_tr_b16 v[110:111], v163 offset:2048\n\t" \
  "ds_read_b64_tr_b16 v[112:113], v160 offset:4096\n\t" \
  "ds_read_b64_tr_b16 v[114:115], v160 offset:6144\n\t" \
  "ds_read_b64_tr_b16 v[116:117], v161 offset:4096\n\t" \
  "ds_read_b64_tr_b16 v[118:119], v161 offset:6144\n\t" \
  "ds_read_b64_tr_b16 v[120:121], v162 offset:4096\n\t" \
  "ds_read_b64_tr_b16 v[122:123], v162 offset:6144\n\t" \
  "s_waitcnt vmcnt(18) lgkmcnt(10)\n\t" \
  "v_mfma_f32_16x16x32_bf16 v[128:131], v[96:99], v[0:3], 0\n\t" \
  "ds_read_b64_tr_b16 v[124:125], v163 offset:4096\n\t" \
  "v_mfma_f32_16x16x32_bf16 v[144:147], v[96:99], v[4:7], 0\n\t" \
  "ds_read_b64_tr_b16 v[126:127], v163 offset:6144\n\t" \
  "v_mfma_f32_16x16x32_bf16 v[132:135], v[100:103], v[0:3], 0\n\t" \
  "ds_read_b64_tr_b16 v[96:97], v160 offset:8192\n\t" \
  "v_mfma_f32_16x16x32_bf16 v[148:151], v[100:103], v[4:7], 0\n\t" \
  "ds_read_b64_tr_b16 v[98:99], v160 offset:10240\n\t" \
  "s_waitcnt lgkmcnt(10)\n\t" \
  "v_mfma_f32_16x16x32_bf16 v[136:139], v[104:107], v[0:3], 0\n\t" \
  "ds_read_b64_tr_b16 v[100:101], v161 offset:8192\n\t" \
  "v_mfma_f32_16x16x32_bf16 v[152:155], v[104:107], v[4:7], 0\n\t" \
  "ds_read_b64_tr_b16 v[102:103], v161 offset:10240\n\t" \
  "v_mfma_f32_16x16x32_bf16 v[140:143], v[108:111], v[0:3], 0\n\t" \
  "ds_read_b64_tr_b16 v[104:105], v162 offset:8192\n\t" \
  "v_mfma_f32_16x16x32_bf16 v[156:159], v[108:111], v[4:7], 0\n\t" \
  "ds_read_b64_tr_b16 v[106:107], v162 offset:10240\n\t" \
  "s_waitcnt vmcnt(16) lgkmcnt(10)\n\t" \
  "v_mfma_f32_16x16x32_bf16 v[128:131], v[112:115], v[8:11], v[128:131]\n\t" \
  "ds_read_b64_tr_b16 v[108:109], v163 offset:8192\n\t" \
  "buffer_load_dwordx4 v[80:83], v164, %[ob], s69 offen offset:0 sc1\n\t" \
  "v_mfma_f32_16x16x32_bf16 v[144:147], v[112:115], v[12:15], v[144:147]\n\t" \
  "ds_read_b64_tr_b16 v[110:111], v163 offset:10240\n\t" \
  "v_mfma_f32_16x16x32_bf16 v[132:135], v[116:119], v[8:11], v[132:135]\n\t" \
  "ds_read_b64_tr_b16 v[112:113], v160 offset:12288\n\t" \
  "v_mfma_f32_16x16x32_bf16 v[148:151], v[116:119], v[12:15], v[148:151]\n\t" \
  "ds_read_b64_tr_b16 v[114:115], v160 offset:14336\n\t" \
  "s_waitcnt lgkmcnt(10)\n\t" \
  "v_mfma_f32_16x16x32_bf16 v[136:139], v[120:123], v[8:11], v[136:139]\n\t" \
  "ds_read_b64_tr_b16 v[116:117], v161 offset:12288\n\t" \
  "buffer_load_dwordx4 v[84:87], v164, %[ob], s69 offen offset:1024 sc1\n\t" \
  "v_mfma_f32_16x16x32_bf16 v[152:155], v[120:123], v[12:15], v[152:155]\n\t" \
  "ds_read_b64_tr_b16 v[118:119], v161 offset:14336\n\t" \
  "v_mfma_f32_16x16x32_bf16 v[140:143], v[124:127], v[8:11], v[140:143]\n\t" \
  "ds_read_b64_tr_b16 v[120:121], v162 offset:12288\n\t" \
  "v_mfma_f32_16x16x32_bf16 v[156:159], v[124:127], v[12:15], v[156:159]\n\t" \
  "ds_read_b64_tr_b16 v[122:123], v162 offset:14336\n\t" \
  "s_waitcnt vmcnt(16) lgkmcnt(10)\n\t" \
  "v_mfma_f32_16x16x32_bf16 v[128:131], v[96:99], v[16:19], v[128:131]\n\t" \
  "ds_read_b64_tr_b16 v[124:125], v163 offset:12288\n\t" \
  "buffer_load_dwordx4 v[88:91], v164, %[ob], s69 offen offset:2048 sc1\n\t" \
  "v_mfma_f32_16x16x32_bf16 v[144:147], v[96:99], v[20:23], v[144:147]\n\t" \
  "ds_read_b64_tr_b16 v[126:127], v163 offset:14336\n\t" \
  "v_mfma_f32_16x16x32_bf16 v[132:135], v[100:103], v[16:19], v[132:135]\n\t" \
  "ds_read_b64_tr_b16 v[96:97], v160 offset:16384\n\t" \
  "v_mfma_f32_16x16x32_bf16 v[148:151], v[100:103], v[20:23], v[148:151]\n\t" \
  "ds_read_b64_tr_b16 v[98:99], v160 offset:18432\n\t" \
  "s_waitcnt lgkmcnt(10)\n\t" \
  "v_mfma_f32_16x16x32_bf16 v[136:139], v[104:107], v[16:19], v[136:139]\n\t" \
  "ds_read_b64_tr_b16 v[100:101], v161 offset:16384\n\t" \
  "v_mfma_f32_16x16x32_bf16 v[152:155], v[104:107], v[20:23], v[152:155]\n\t" \
  "ds_read_b64_tr_b16 v[102:103], v161 offset:18432\n\t" \
  "buffer_load_dwordx4 v[92:95], v164, %[ob], s69 offen offset:3072 sc1\n\t" \
  "v_mfma_f32_16x16x32_bf16 v[140:143], v[108:111], v[16:19], v[140:143]\n\t" \
  "ds_read_b64_tr_b16 v[104:105], v162 offset:16384\n\t" \
  "v_mfma_f32_16x16x32_bf16 v[156:159], v[108:111], v[20:23], v[156:159]\n\t" \
  "ds_read_b64_tr_b16 v[106:107], v162 offset:18432\n\t" \
  "s_waitcnt vmcnt(16) lgkmcnt(10)\n\t" \
  "v_mfma_f32_16x16x32_bf16 v[128:131], v[112:115], v[24:27], v[128:131]\n\t" \
  "ds_read_b64_tr_b16 v[108:109], v163 offset:16384\n\t" \
  "v_mfma_f32_16x16x32_bf16 v[144:147], v[112:115], v[28:31], v[144:147]\n\t" \
  "ds_read_b64_tr_b16 v[110:111], v163 offset:18432\n\t" \
  "buffer_load_dwordx4 v[0:3], v164, %[ob], s70 offen offset:0 sc1\n\t" \
  "v_mfma_f32_16x16x32_bf16 v[132:135], v[116:119], v[24:27], v[132:135]\n\t" \
  "ds_read_b64_tr_b16 v[112:113], v160 offset:20480\n\t" \
  "v_mfma_f32_16x16x32_bf16 v[148:151], v[116:119], v[28:31], v[148:151]\n\t" \
  "ds_read_b64_tr_b16 v[114:115], v160 offset:22528\n\t" \
  "s_waitcnt lgkmcnt(10)\n\t" \
  "v_mfma_f32_16x16x32_bf16 v[136:139], v[120:123], v[24:27], v[136:139]\n\t" \
  "ds_read_b64_tr_b16 v[116:117], v161 offset:20480\n\t" \
  "v_mfma_f32_16x16x32_bf16 v[152:155], v[120:123], v[28:31], v[152:155]\n\t" \
  "ds_read_b64_tr_b16 v[118:119], v161 offset:22528\n\t" \
  "v_mfma_f32_16x16x32_bf16 v[140:143], v[124:127], v[24:27], v[140:143]\n\t" \
  "ds_read_b64_tr_b16 v[120:121], v162 offset:20480\n\t" \
  "buffer_load_dwordx4 v[4:7], v164, %[ob], s70 offen offset:1024 sc1\n\t" \
  "v_mfma_f32_16x16x32_bf16 v[156:159], v[124:127], v[28:31], v[156:159]\n\t" \
  "ds_read_b64_tr_b16 v[122:123], v162 offset:22528\n\t" \
  "s_waitcnt vmcnt(16) lgkmcnt(10)\n\t" \
  "v_mfma_f32_16x16x32_bf16 v[128:131], v[96:99], v[32:35], v[128:131]\n\t" \
  "ds_read_b64_tr_b16 v[124:125], v163 offset:20480\n\t" \
  "ds_read_b32 v165, v165\n\t" \
  "v_mfma_f32_16x16x32_bf16 v[144:147], v[96:99], v[36:39], v[144:147]\n\t" \
  "ds_read_b64_tr_b16 v[126:127], v163 offset:22528\n\t" \
  "v_mfma_f32_16x16x32_bf16 v[132:135], v[100:103], v[32:35], v[132:135]\n\t" \
  "ds_read_b64_tr_b16 v[96:97], v160 offset:24576\n\t" \
  "buffer_load_dwordx4 v[8:11], v164, %[ob], s70 offen offset:2048 sc1\n\t" \
  "v_mfma_f32_16x16x32_bf16 v[148:151], v[100:103], v[36:39], v[148:151]\n\t" \
  "ds_read_b64_tr_b16 v[98:99], v160 offset:26624\n\t" \
  "s_waitcnt lgkmcnt(11)\n\t" \
  "v_mfma_f32_16x16x32_bf16 v[136:139], v[104:107], v[32:35], v[136:139]\n\t" \
  "ds_read_b64_tr_b16 v[100:101], v161 offset:24576\n\t" \
  "v_mfma_f32_16x16x32_bf16 v[152:155], v[104:107], v[36:39], v[152:155]\n\t" \
  "ds_read_b64_tr_b16 v[102:103], v161 offset:26624\n\t" \
  "v_mfma_f32_16x16x32_bf16 v[140:143], v[108:111], v[32:35], v[140:143]\n\t" \
  "ds_read_b64_tr_b16 v[104:105], v162 offset:24576\n\t" \
  "v_mfma_f32_16x16x32_bf16 v[156:159], v[108:111], v[36:39], v[156:159]\n\t" \
  "ds_read_b64_tr_b16 v[106:107], v162 offset:26624\n\t" \
  "s_waitcnt vmcnt(15) lgkmcnt(11)\n\t" \
  "v_mfma_f32_16x16x32_bf16 v[128:131], v[112:115], v[40:43], v[128:131]\n\t" \
  "ds_read_b64_tr_b16 v[108:109], v163 offset:24576\n\t" \
  "buffer_load_dwordx4 v[12:15], v164, %[ob], s70 offen offset:3072 sc1\n\t" \
  "v_mfma_f32_16x16x32_bf16 v[144:147], v[112:115], v[44:47], v[144:147]\n\t" \
  "ds_read_b64_tr_b16 v[110:111], v163 offset:26624\n\t" \
  "s_waitcnt lgkmcnt(9)\n\t" \
  "v_mfma_f32_16x16x32_bf16 v[132:135], v[116:119], v[40:43], v[132:135]\n\t" \
  "ds_read_b64_tr_b16 v[112:113], v160 offset:28672\n\t" \
  "v_cmp_lt_u32 vcc, 13, v165\n\t" \
  "v_mfma_f32_16x16x32_bf16 v[148:151], v[116:119], v[44:47], v[148:151]\n\t" \
  "ds_read_b64_tr_b16 v[114:115], v160 offset:30720\n\t" \
  "s_cbranch_vccz QA7_%=\n\t" \
  "QB7_%=:\n\t" \
  "s_waitcnt lgkmcnt(10)\n\t" \
  "v_mfma_f32_16x16x32_bf16 v[136:139], v[120:123], v[40:43], v[136:139]\n\t" \
  "ds_read_b64_tr_b16 v[116:117], v161 offset:28672\n\t" \
  "buffer_load_dwordx4 v[16:19], v164, %[ob], s71 offen offset:0 sc1\n\t" \
  "v_mfma_f32_16x16x32_bf16 v[152:155], v[120:123], v[44:47], v[152:155]\n\t" \
  "ds_read_b64_tr_b16 v[118:119], v161 offset:30720\n\t" \
  "v_mov_b32 v165, %[sy]\n\t" \
  "v_mfma_f32_16x16x32_bf16 v[140:143], v[124:127], v[40:43], v[140:143]\n\t" \
  "ds_read_b64_tr_b16 v[120:121], v162 offset:28672\n\t" \
  "v_mfma_f32_16x16x32_bf16 v[156:159], v[124:127], v[44:47], v[156:159]\n\t" \
  "ds_read_b64_tr_b16 v[122:123], v162 offset:30720\n\t" \
  "s_waitcnt vmcnt(15) lgkmcnt(10)\n\t" \
  "v_mfma_f32_16x16x32_bf16 v[128:131], v[96:99], v[48:51], v[128:131]\n\t" \
  "ds_read_b64_tr_b16 v[124:125], v163 offset:28672\n\t" \
  "buffer_load_dwordx4 v[20:23], v164, %[ob], s71 offen offset:1024 sc1\n\t" \
  "v_mfma_f32_16x16x32_bf16 v[144:147], v[96:99], v[52:55], v[144:147]\n\t" \
  "ds_read_b64_tr_b16 v[126:127], v163 offset:30720\n\t" \
  "v_mfma_f32_16x16x32_bf16 v[132:135], v[100:103], v[48:51], v[132:135]\n\t" \
  "ds_read_b64_tr_b16 v[96:97], v160 offset:32768\n\t" \
  "v_mfma_f32_16x16x32_bf16 v[148:151], v[100:103], v[52:55], v[148:151]\n\t" \
  "ds_read_b64_tr_b16 v[98:99], v160 offset:34816\n\t" \
  "s_waitcnt lgkmcnt(10)\n\t" \
  "v_mfma_f32_16x16x32_bf16 v[136:139], v[104:107], v[48:51], v[136:139]\n\t" \
  "ds_read_b64_tr_b16 v[100:101], v161 offset:32768\n\t" \
  "buffer_load_dwordx4 v[24:27], v164, %[ob], s71 offen offset:2048 sc1\n\t" \
  "v_mfma_f32_16x16x32_bf16 v[152:155], v[104:107], v[52:55], v[152:155]\n\t" \
  "ds_read_b64_tr_b16 v[102:103], v161 offset:34816\n\t" \
  "v_mfma_f32_16x16x32_bf16 v[140:143], v[108:111], v[48:51], v[140:143]\n\t" \
  "ds_read_b64_tr_b16 v[104:105], v162 offset:32768\n\t" \
  "v_mfma_f32_16x16x32_bf16 v[156:159], v[108:111], v[52:55], v[156:159]\n\t" \
  "ds_read_b64_tr_b16 v[106:107], v162 offset:34816\n\t" \
  "s_waitcnt vmcnt(15) lgkmcnt(10)\n\t" \
  "v_mfma_f32_16x16x32_bf16 v[128:131], v[112:115], v[56:59], v[128:131]\n\t" \
  "ds_read_b64_tr_b16 v[108:109], v163 offset:32768\n\t" \
  "v_mfma_f32_16x16x32_bf16 v[144:147], v[112:115], v[60:63], v[144:147]\n\t" \
  "ds_read_b64_tr_b16 v[110:111], v163 offset:34816\n\t" \
  "buffer_load_dwordx4 v[28:31], v164, %[ob], s71 offen offset:3072 sc1\n\t" \
  "v_mfma_f32_16x16x32_bf16 v[132:135], v[116:119], v[56:59], v[132:135]\n\t" \
  "ds_read_b64_tr_b16 v[112:113], v160 offset:36864\n\t" \
  "v_mfma_f32_16x16x32_bf16 v[148:151], v[116:119], v[60:63], v[148:151]\n\t" \
  "ds_read_b64_tr_b16 v[114:115], v160 offset:38912\n\t" \
  "s_waitcnt lgkmcnt(10)\n\t" \
  "v_mfma_f32_16x16x32_bf16 v[136:139], v[120:123], v[56:59], v[136:139]\n\t" \
  "ds_read_b64_tr_b16 v[116:117], v161 offset:36864\n\t" \
  "v_mfma_f32_16x16x32_bf16 v[152:155], v[120:123], v[60:63], v[152:155]\n\t" \
  "ds_read_b64_tr_b16 v[118:119], v161 offset:38912\n\t" \
  "buffer_load_dwordx4 v[32:35], v164, %[ob], s72 offen offset:0 sc1\n\t" \
  "v_mfma_f32_16x16x32_bf16 v[140:143], v[124:127], v[56:59], v[140:143]\n\t" \
  "ds_read_b64_tr_b16 v[120:121], v162 offset:36864\n\t" \
  "v_mfma_f32_16x16x32_bf16 v[156:159], v[124:127], v[60:63], v[156:159]\n\t" \
  "ds_read_b64_tr_b16 v[122:123], v162 offset:38912\n\t" \
  "s_waitcnt vmcnt(15) lgkmcnt(10)\n\t" \
  "v_mfma_f32_16x16x32_bf16 v[128:131], v[96:99], v[64:67], v[128:131]\n\t" \
  "ds_read_b64_tr_b16 v[124:125], v163 offset:36864\n\t" \
  "v_mfma_f32_16x16x32_bf16 v[144:147], v[96:99], v[68:71], v[144:147]\n\t" \
  "ds_read_b64_tr_b16 v[126:127], v163 offset:38912\n\t" \
  "v_mfma_f32_16x16x32_bf16 v[132:135], v[100:103], v[64:67], v[132:135]\n\t" \
  "ds_read_b64_tr_b16 v[96:97], v160 offset:40960\n\t" \
  "buffer_load_dwordx4 v[36:39], v164, %[ob], s72 offen offset:1024 sc1\n\t" \
  "v_mfma_f32_16x16x32_bf16 v[148:151], v[100:103], v[68:71], v[148:151]\n\t" \
  "ds_read_b64_tr_b16 v[98:99], v160 offset:43008\n\t" \
  "s_waitcnt lgkmcnt(10)\n\t" \
  "v_mfma_f32_16x16x32_bf16 v[136:139], v[104:107], v[64:67], v[136:139]\n\t" \
  "ds_read_b64_tr_b16 v[100:101], v161 offset:40960\n\t" \
  "v_mfma_f32_16x16x32_bf16 v[152:155], v[104:107], v[68:71], v[152:155]\n\t" \
  "ds_read_b64_tr_b16 v[102:103], v161 offset:43008\n\t" \
  "v_mfma_f32_16x16x32_bf16 v[140:143], v[108:111], v[64:67], v[140:143]\n\t" \
  "ds_read_b64_tr_b16 v[104:105], v162 offset:40960\n\t" \
  "buffer_load_dwordx4 v[40:43], v164, %[ob], s72 offen offset:2048 sc1\n\t" \
  "v_mfma_f32_16x16x32_bf16 v[156:159], v[108:111], v[68:71], v[156:159]\n\t" \
  "ds_read_b64_tr_b16 v[106:107], v162 offset:43008\n\t" \
  "s_waitcnt vmcnt(15) lgkmcnt(10)\n\t" \
  "v_mfma_f32_16x16x32_bf16 v[128:131], v[112:115], v[72:75], v[128:131]\n\t" \
  "ds_read_b64_tr_b16 v[108:109], v163 offset:40960\n\t" \
  "v_mfma_f32_16x16x32_bf16 v[144:147], v[112:115], v[76:79], v[144:147]\n\t" \
  "ds_read_b64_tr_b16 v[110:111], v163 offset:43008\n\t" \
  "v_mfma_f32_16x16x32_bf16 v[132:135], v[116:119], v[72:75], v[132:135]\n\t" \
  "ds_read_b64_tr_b16 v[112:113], v160 offset:45056\n\t" \
  "buffer_load_dwordx4 v[44:47], v164, %[ob], s72 offen offset:3072 sc1\n\t" \
  "v_mfma_f32_16x16x32_bf16 v[148:151], v[116:119], v[76:79], v[148:151]\n\t" \
  "ds_read_b64_tr_b16 v[114:115], v160 offset:47104\n\t" \
  "s_waitcnt lgkmcnt(10)\n\t" \
  "v_mfma_f32_16x16x32_bf16 v[136:139], v[120:123], v[72:75], v[136:139]\n\t" \
  "ds_read_b64_tr_b16 v[116:117], v161 offset:45056\n\t" \
  "v_mfma_f32_16x16x32_bf16 v[152:155], v[120:123], v[76:79], v[152:155]\n\t" \
  "ds_read_b64_tr_b16 v[118:119], v161 offset:47104\n\t" \
  "v_mfma_f32_16x16x32_bf16 v[140:143], v[124:127], v[72:75], v[140:143]\n\t" \
  "ds_read_b64_tr_b16 v[120:121], v162 offset:45056\n\t" \
  "v_mfma_f32_16x16x32_bf16 v[156:159], v[124:127], v[76:79], v[156:159]\n\t" \
  "ds_read_b64_tr_b16 v[122:123], v162 offset:47104\n\t" \
  "s_waitcnt vmcnt(14) lgkmcnt(10)\n\t" \
  "v_mfma_f32_16x16x32_bf16 v[128:131], v[96:99], v[80:83], v[128:131]\n\t" \
  "ds_read_b64_tr_b16 v[124:125], v163 offset:45056\n\t" \
  "buffer_load_dwordx4 v[48:51], v164, %[ob], s73 offen offset:0 sc1\n\t" \
  "v_mfma_f32_16x16x32_bf16 v[144:147], v[96:99], v[84:87], v[144:147]\n\t" \
  "ds_read_b64_tr_b16 v[126:127], v163 offset:47104\n\t" \
  "v_mfma_f32_16x16x32_bf16 v[132:135], v[100:103], v[80:83], v[132:135]\n\t" \
  "ds_read_b64_tr_b16 v[96:97], v160 offset:49152\n\t" \
  "v_mfma_f32_16x16x32_bf16 v[148:151], v[100:103], v[84:87], v[148:151]\n\t" \
  "ds_read_b64_tr_b16 v[98:99], v160 offset:51200\n\t" \
  "s_waitcnt lgkmcnt(10)\n\t" \
  "v_mfma_f32_16x16x32_bf16 v[136:139], v[104:107], v[80:83], v[136:139]\n\t" \
  "ds_read_b64_tr_b16 v[100:101], v161 offset:49152\n\t" \
  "buffer_load_dwordx4 v[52:55], v164, %[ob], s73 offen offset:1024 sc1\n\t" \
  "v_mfma_f32_16x16x32_bf16 v[152:155], v[104:107], v[84:87], v[152:155]\n\t" \
  "ds_read_b64_tr_b16 v[102:103], v161 offset:51200\n\t" \
  "v_mfma_f32_16x16x32_bf16 v[140:143], v[108:111], v[80:83], v[140:143]\n\t" \
  "ds_read_b64_tr_b16 v[104:105], v162 offset:49152\n\t" \
  "v_mfma_f32_16x16x32_bf16 v[156:159], v[108:111], v[84:87], v[156:159]\n\t" \
  "ds_read_b64_tr_b16 v[106:107], v162 offset:51200\n\t" \
  "s_waitcnt vmcnt(14) lgkmcnt(10)\n\t" \
  "v_mfma_f32_16x16x32_bf16 v[128:131], v[112:115], v[88:91], v[128:131]\n\t" \
  "ds_read_b64_tr_b16 v[108:109], v163 offset:49152\n\t" \
  "buffer_load_dwordx4 v[56:59], v164, %[ob], s73 offen offset:2048 sc1\n\t" \
  "v_mfma_f32_16x16x32_bf16 v[144:147], v[112:115], v[92:95], v[144:147]\n\t" \
  "ds_read_b64_tr_b16 v[110:111], v163 offset:51200\n\t" \
  "v_mfma_f32_16x16x32_bf16 v[132:135], v[116:119], v[88:91], v[132:135]\n\t" \
  "ds_read_b64_tr_b16 v[112:113], v160 offset:53248\n\t" \
  "v_mfma_f32_16x16x32_bf16 v[148:151], v[116:119], v[92:95], v[148:151]\n\t" \
  "ds_read_b64_tr_b16 v[114:115], v160 offset:55296\n\t" \
  "s_waitcnt lgkmcnt(10)\n\t" \
  "v_mfma_f32_16x16x32_bf16 v[136:139], v[120:123], v[88:91], v[136:139]\n\t" \
  "ds_read_b64_tr_b16 v[116:117], v161 offset:53248\n\t" \
  "buffer_load_dwordx4 v[60:63], v164, %[ob], s73 offen offset:3072 sc1\n\t" \
  "v_mfma_f32_16x16x32_bf16 v[152:155], v[120:123], v[92:95], v[152:155]\n\t" \
  "ds_read_b64_tr_b16 v[118:119], v161 offset:55296\n\t" \
  "v_mfma_f32_16x16x32_bf16 v[140:143], v[124:127], v[88:91], v[140:143]\n\t" \
  "ds_read_b64_tr_b16 v[120:121], v162 offset:53248\n\t" \
  "v_mfma_f32_16x16x32_bf16 v[156:159], v[124:127], v[92:95], v[156:159]\n\t" \
  "ds_read_b64_tr_b16 v[122:123], v162 offset:55296\n\t" \
  "s_waitcnt vmcnt(14) lgkmcnt(10)\n\t" \
  "v_mfma_f32_16x16x32_bf16 v[128:131], v[96:99], v[0:3], v[128:131]\n\t" \
  "ds_read_b64_tr_b16 v[124:125], v163 offset:53248\n\t" \
  "v_mfma_f32_16x16x32_bf16 v[144:147], v[96:99], v[4:7], v[144:147]\n\t" \
  "ds_read_b64_tr_b16 v[126:127], v163 offset:55296\n\t" \
  "buffer_load_dwordx4 v[64:67], v164, %[ob], s74 offen offset:0 sc1\n\t" \
  "v_mfma_f32_16x16x32_bf16 v[132:135], v[100:103], v[0:3], v[132:135]\n\t" \
  "ds_read_b64_tr_b16 v[96:97], v160 offset:57344\n\t" \
  "v_mfma_f32_16x16x32_bf16 v[148:151], v[100:103], v[4:7], v[148:151]\n\t" \
  "ds_read_b64_tr_b16 v[98:99], v160 offset:59392\n\t" \
  "s_waitcnt lgkmcnt(10)\n\t" \
  "v_mfma_f32_16x16x32_bf16 v[136:139], v[104:107], v[0:3], v[136:139]\n\t" \
  "ds_read_b64_tr_b16 v[100:101], v161 offset:57344\n\t" \
  "v_mfma_f32_16x16x32_bf16 v[152:155], v[104:107], v[4:7], v[152:155]\n\t" \
  "ds_read_b64_tr_b16 v[102:103], v161 offset:59392\n\t" \
  "buffer_load_dwordx4 v[68:71], v164, %[ob], s74 offen offset:1024 sc1\n\t" \
  "v_mfma_f32_16x16x32_bf16 v[140:143], v[108:111], v[0:3], v[140:143]\n\t" \
  "ds_read_b64_tr_b16 v[104:105], v162 offset:57344\n\t" \
  "v_mfma_f32_16x16x32_bf16 v[156:159], v[108:111], v[4:7], v[156:159]\n\t" \
  "ds_read_b64_tr_b16 v[106:107], v162 offset:59392\n\t" \
  "s_waitcnt vmcnt(14) lgkmcnt(10)\n\t" \
  "v_mfma_f32_16x16x32_bf16 v[128:131], v[112:115], v[8:11], v[128:131]\n\t" \
  "ds_read_b64_tr_b16 v[108:109], v163 offset:57344\n\t" \
  "v_mfma_f32_16x16x32_bf16 v[144:147], v[112:115], v[12:15], v[144:147]\n\t" \
  "ds_read_b64_tr_b16 v[110:111], v163 offset:59392\n\t" \
  "v_mfma_f32_16x16x32_bf16 v[132:135], v[116:119], v[8:11], v[132:135]\n\t" \
  "ds_read_b64_tr_b16 v[112:113], v160 offset:61440\n\t" \
  "buffer_load_dwordx4 v[72:75], v164, %[ob], s74 offen offset:2048 sc1\n\t" \
  "v_mfma_f32_16x16x32_bf16 v[148:151], v[116:119], v[12:15], v[148:151]\n\t" \
  "ds_read_b64_tr_b16 v[114:115], v160 offset:63488\n\t" \
  "s_waitcnt lgkmcnt(10)\n\t" \
  "v_mfma_f32_16x16x32_bf16 v[136:139], v[120:123], v[8:11], v[136:139]\n\t" \
  "ds_read_b64_tr_b16 v[116:117], v161 offset:61440\n\t" \
  "v_mfma_f32_16x16x32_bf16 v[152:155], v[120:123], v[12:15], v[152:155]\n\t" \
  "ds_read_b64_tr_b16 v[118:119], v161 offset:63488\n\t" \
  "v_mfma_f32_16x16x32_bf16 v[140:143], v[124:127], v[8:11], v[140:143]\n\t" \
  "ds_read_b64_tr_b16 v[120:121], v162 offset:61440\n\t" \
  "buffer_load_dwordx4 v[76:79], v164, %[ob], s74 offen offset:3072 sc1\n\t" \
  "v_mfma_f32_16x16x32_bf16 v[156:159], v[124:127], v[12:15], v[156:159]\n\t" \
  "ds_read_b64_tr_b16 v[122:123], v162 offset:63488\n\t" \
  "s_waitcnt vmcnt(14) lgkmcnt(10)\n\t" \
  "v_mfma_f32_16x16x32_bf16 v[128:131], v[96:99], v[16:19], v[128:131]\n\t" \
  "ds_read_b64_tr_b16 v[124:125], v163 offset:61440\n\t" \
  "v_mfma_f32_16x16x32_bf16 v[144:147], v[96:99], v[20:23], v[144:147]\n\t" \
  "ds_read_b64_tr_b16 v[126:127], v163 offset:63488\n\t" \
  "v_mfma_f32_16x16x32_bf16 v[132:135], v[100:103], v[16:19], v[132:135]\n\t" \
  "v_add_u32 v160, 0x10000, v160\n\t" \
  "ds_read_b64_tr_b16 v[96:97], v160 offset:0\n\t" \
  "v_mfma_f32_16x16x32_bf16 v[148:151], v[100:103], v[20:23], v[148:151]\n\t" \
  "ds_read_b64_tr_b16 v[98:99], v160 offset:2048\n\t" \
  "s_waitcnt lgkmcnt(10)\n\t" \
  "v_mfma_f32_16x16x32_bf16 v[136:139], v[104:107], v[16:19], v[136:139]\n\t" \
  "v_add_u32 v161, 0x10000, v161\n\t" \
  "ds_read_b64_tr_b16 v[100:101], v161 offset:0\n\t" \
  "buffer_load_dwordx4 v[80:83], v164, %[ob], s75 offen offset:0 sc1\n\t" \
  "v_mfma_f32_16x16x32_bf16 v[152:155], v[104:107], v[20:23], v[152:155]\n\t" \
  "ds_read_b64_tr_b16 v[102:103], v161 offset:2048\n\t" \
  "v_mfma_f32_16x16x32_bf16 v[140:143], v[108:111], v[16:19], v[140:143]\n\t" \
  "v_add_u32 v162, 0x10000, v162\n\t" \
  "ds_read_b64_tr_b16 v[104:105], v162 offset:0\n\t" \
  "v_mfma_f32_16x16x32_bf16 v[156:159], v[108:111], v[20:23], v[156:159]\n\t" \
  "ds_read_b64_tr_b16 v[106:107], v162 offset:2048\n\t" \
  "s_waitcnt vmcnt(13) lgkmcnt(10)\n\t" \
  "v_mfma_f32_16x16x32_bf16 v[128:131], v[112:115], v[24:27], v[128:131]\n\t" \
  "v_add_u32 v163, 0x10000, v163\n\t" \
  "ds_read_b64_tr_b16 v[108:109], v163 offset:0\n\t" \
  "buffer_load_dwordx4 v[84:87], v164, %[ob], s75 offen offset:1024 sc1\n\t" \
  "v_mfma_f32_16x16x32_bf16 v[144:147], v[112:115], v[28:31], v[144:147]\n\t" \
  "ds_read_b64_tr_b16 v[110:111], v163 offset:2048\n\t" \
  "v_mfma_f32_16x16x32_bf16 v[132:135], v[116:119], v[24:27], v[132:135]\n\t" \
  "ds_read_b64_tr_b16 v[112:113], v160 offset:4096\n\t" \
  "v_mfma_f32_16x16x32_bf16 v[148:151], v[116:119], v[28:31], v[148:151]\n\t" \
  "ds_read_b64_tr_b16 v[114:115], v160 offset:6144\n\t" \
  "s_waitcnt lgkmcnt(10)\n\t" \
  "v_mfma_f32_16x16x32_bf16 v[136:139], v[120:123], v[24:27], v[136:139]\n\t" \
  "ds_read_b64_tr_b16 v[116:117], v161 offset:4096\n\t" \
  "buffer_load_dwordx4 v[88:91], v164, %[ob], s75 offen offset:2048 sc1\n\t" \
  "v_mfma_f32_16x16x32_bf16 v[152:155], v[120:123], v[28:31], v[152:155]\n\t" \
  "ds_read_b64_tr_b16 v[118:119], v161 offset:6144\n\t" \
  "v_mfma_f32_16x16x32_bf16 v[140:143], v[124:127], v[24:27], v[140:143]\n\t" \
  "ds_read_b64_tr_b16 v[120:121], v162 offset:4096\n\t" \
  "v_mfma_f32_16x16x32_bf16 v[156:159], v[124:127], v[28:31], v[156:159]\n\t" \
  "ds_read_b64_tr_b16 v[122:123], v162 offset:6144\n\t" \
  "s_waitcnt vmcnt(13) lgkmcnt(10)\n\t" \
  "v_mfma_f32_16x16x32_bf16 v[128:131], v[96:99], v[32:35], v[128:131]\n\t" \
  "ds_read_b64_tr_b16 v[124:125], v163 offset:4096\n\t" \
  "buffer_load_dwordx4 v[92:95], v164, %[ob], s75 offen offset:3072 sc1\n\t" \
  "v_mfma_f32_16x16x32_bf16 v[144:147], v[96:99], v[36:39], v[144:147]\n\t" \
  "ds_read_b64_tr_b16 v[126:127], v163 offset:6144\n\t" \
  "v_mfma_f32_16x16x32_bf16 v[132:135], v[100:103], v[32:35], v[132:135]\n\t" \
  "ds_read_b64_tr_b16 v[96:97], v160 offset:8192\n\t" \
  "v_mfma_f32_16x16x32_bf16 v[148:151], v[100:103], v[36:39], v[148:151]\n\t" \
  "ds_read_b64_tr_b16 v[98:99], v160 offset:10240\n\t" \
  "s_waitcnt lgkmcnt(10)\n\t" \
  "v_mfma_f32_16x16x32_bf16 v[136:139], v[104:107], v[32:35], v[136:139]\n\t" \
  "ds_read_b64_tr_b16 v[100:101], v161 offset:8192\n\t" \
  "v_mfma_f32_16x16x32_bf16 v[152:155], v[104:107], v[36:39], v[152:155]\n\t" \
  "ds_read_b64_tr_b16 v[102:103], v161 offset:10240\n\t" \
  "buffer_load_dwordx4 v[0:3], v164, %[ob], s76 offen offset:0 sc1\n\t" \
  "v_mfma_f32_16x16x32_bf16 v[140:143], v[108:111], v[32:35], v[140:143]\n\t" \
  "ds_read_b64_tr_b16 v[104:105], v162 offset:8192\n\t" \
  "v_mfma_f32_16x16x32_bf16 v[156:159], v[108:111], v[36:39], v[156:159]\n\t" \
  "ds_read_b64_tr_b16 v[106:107], v162 offset:10240\n\t" \
  "s_waitcnt vmcnt(13) lgkmcnt(10)\n\t" \
  "v_mfma_f32_16x16x32_bf16 v[128:131], v[112:115], v[40:43], v[128:131]\n\t" \
  "ds_read_b64_tr_b16 v[108:109], v163 offset:8192\n\t" \
  "v_mfma_f32_16x16x32_bf16 v[144:147], v[112:115], v[44:47], v[144:147]\n\t" \
  "ds_read_b64_tr_b16 v[110:111], v163 offset:10240\n\t" \
  "buffer_load_dwordx4 v[4:7], v164, %[ob], s76 offen offset:1024 sc1\n\t" \
  "v_mfma_f32_16x16x32_bf16 v[132:135], v[116:119], v[40:43], v[132:135]\n\t" \
  "ds_read_b64_tr_b16 v[112:113], v160 offset:12288\n\t" \
  "v_mfma_f32_16x16x32_bf16 v[148:151], v[116:119], v[44:47], v[148:151]\n\t" \
  "ds_read_b64_tr_b16 v[114:115], v160 offset:14336\n\t" \
  "s_waitcnt lgkmcnt(10)\n\t" \
  "v_mfma_f32_16x16x32_bf16 v[136:139], v[120:123], v[40:43], v[136:139]\n\t" \
  "ds_read_b64_tr_b16 v[116:117], v161 offset:12288\n\t" \
  "v_mfma_f32_16x16x32_bf16 v[152:155], v[120:123], v[44:47], v[152:155]\n\t" \
  "ds_read_b64_tr_b16 v[118:119], v161 offset:14336\n\t" \
  "buffer_load_dwordx4 v[8:11], v164, %[ob], s76 offen offset:2048 sc1\n\t" \
  "v_mfma_f32_16x16x32_bf16 v[140:143], v[124:127], v[40:43], v[140:143]\n\t" \
  "ds_read_b64_tr_b16 v[120:121], v162 offset:12288\n\t" \
  "v_mfma_f32_16x16x32_bf16 v[156:159], v[124:127], v[44:47], v[156:159]\n\t" \
  "ds_read_b64_tr_b16 v[122:123], v162 offset:14336\n\t" \
  "s_waitcnt vmcnt(13) lgkmcnt(10)\n\t" \
  "v_mfma_f32_16x16x32_bf16 v[128:131], v[96:99], v[48:51], v[128:131]\n\t" \
  "ds_read_b64_tr_b16 v[124:125], v163 offset:12288\n\t" \
  "v_mfma_f32_16x16x32_bf16 v[144:147], v[96:99], v[52:55], v[144:147]\n\t" \
  "ds_read_b64_tr_b16 v[126:127], v163 offset:14336\n\t" \
  "v_mfma_f32_16x16x32_bf16 v[132:135], v[100:103], v[48:51], v[132:135]\n\t" \
  "ds_read_b64_tr_b16 v[96:97], v160 offset:16384\n\t" \
  "buffer_load_dwordx4 v[12:15], v164, %[ob], s76 offen offset:3072 sc1\n\t" \
  "v_mfma_f32_16x16x32_bf16 v[148:151], v[100:103], v[52:55], v[148:151]\n\t" \
  "ds_read_b64_tr_b16 v[98:99], v160 offset:18432\n\t" \
  "s_waitcnt lgkmcnt(10)\n\t" \
  "v_mfma_f32_16x16x32_bf16 v[136:139], v[104:107], v[48:51], v[136:139]\n\t" \
  "ds_read_b64_tr_b16 v[100:101], v161 offset:16384\n\t" \
  "v_mfma_f32_16x16x32_bf16 v[152:155], v[104:107], v[52:55], v[152:155]\n\t" \
  "ds_read_b64_tr_b16 v[102:103], v161 offset:18432\n\t" \
  "v_mfma_f32_16x16x32_bf16 v[140:143], v[108:111], v[48:51], v[140:143]\n\t" \
  "ds_read_b64_tr_b16 v[104:105], v162 offset:16384\n\t" \
  "buffer_load_dwordx4 v[16:19], v164, %[ob], s77 offen offset:0 sc1\n\t" \
  "v_mfma_f32_16x16x32_bf16 v[156:159], v[108:111], v[52:55], v[156:159]\n\t" \
  "ds_read_b64_tr_b16 v[106:107], v162 offset:18432\n\t" \
  "s_waitcnt vmcnt(13) lgkmcnt(10)\n\t" \
  "v_mfma_f32_16x16x32_bf16 v[128:131], v[112:115], v[56:59], v[128:131]\n\t" \
  "ds_read_b64_tr_b16 v[108:109], v163 offset:16384\n\t" \
  "v_mfma_f32_16x16x32_bf16 v[144:147], v[112:115], v[60:63], v[144:147]\n\t" \
  "ds_read_b64_tr_b16 v[110:111], v163 offset:18432\n\t" \
  "v_mfma_f32_16x16x32_bf16 v[132:135], v[116:119], v[56:59], v[132:135]\n\t" \
  "ds_read_b64_tr_b16 v[112:113], v160 offset:20480\n\t" \
  "v_mfma_f32_16x16x32_bf16 v[148:151], v[116:119], v[60:63], v[148:151]\n\t" \
  "ds_read_b64_tr_b16 v[114:115], v160 offset:22528\n\t" \
  "s_waitcnt lgkmcnt(10)\n\t" \
  "v_mfma_f32_16x16x32_bf16 v[136:139], v[120:123], v[56:59], v[136:139]\n\t" \
  "ds_read_b64_tr_b16 v[116:117], v161 offset:20480\n\t" \
  "buffer_load_dwordx4 v[20:23], v164, %[ob], s77 offen offset:1024 sc1\n\t" \
  "ds_read_b32 v165, v165\n\t" \
  "v_mfma_f32_16x16x32_bf16 v[152:155], v[120:123], v[60:63], v[152:155]\n\t" \
  "ds_read_b64_tr_b16 v[118:119], v161 offset:22528\n\t" \
  "v_mfma_f32_16x16x32_bf16 v[140:143], v[124:127], v[56:59], v[140:143]\n\t" \
  "ds_read_b64_tr_b16 v[120:121], v162 offset:20480\n\t" \
  "v_mfma_f32_16x16x32_bf16 v[156:159], v[124:127], v[60:63], v[156:159]\n\t" \
  "ds_read_b64_tr_b16 v[122:123], v162 offset:22528\n\t" \
  "s_waitcnt vmcnt(12) lgkmcnt(11)\n\t" \
  "v_mfma_f32_16x16x32_bf16 v[128:131], v[96:99], v[64:67], v[128:131]\n\t" \
  "ds_read_b64_tr_b16 v[124:125], v163 offset:20480\n\t" \
  "buffer_load_dwordx4 v[24:27], v164, %[ob], s77 offen offset:2048 sc1\n\t" \
  "v_mfma_f32_16x16x32_bf16 v[144:147], v[96:99], v[68:71], v[144:147]\n\t" \
  "ds_read_b64_tr_b16 v[126:127], v163 offset:22528\n\t" \
  "v_mfma_f32_16x16x32_bf16 v[132:135], v[100:103], v[64:67], v[132:135]\n\t" \
  "ds_read_b64_tr_b16 v[96:97], v160 offset:24576\n\t" \
  "v_mfma_f32_16x16x32_bf16 v[148:151], v[100:103], v[68:71], v[148:151]\n\t" \
  "ds_read_b64_tr_b16 v[98:99], v160 offset:26624\n\t" \
  "s_waitcnt lgkmcnt(11)\n\t" \
  "v_mfma_f32_16x16x32_bf16 v[136:139], v[104:107], v[64:67], v[136:139]\n\t" \
  "ds_read_b64_tr_b16 v[100:101], v161 offset:24576\n\t" \
  "buffer_load_dwordx4 v[28:31], v164, %[ob], s77 offen offset:3072 sc1\n\t" \
  "v_mfma_f32_16x16x32_bf16 v[152:155], v[104:107], v[68:71], v[152:155]\n\t" \
  "ds_read_b64_tr_b16 v[102:103], v161 offset:26624\n\t" \
  "s_waitcnt lgkmcnt(9)\n\t" \
  "v_mfma_f32_16x16x32_bf16 v[140:143], v[108:111], v[64:67], v[140:143]\n\t" \
  "ds_read_b64_tr_b16 v[104:105], v162 offset:24576\n\t" \
  "v_cmp_lt_u32 vcc, 15, v165\n\t" \
  "v_mfma_f32_16x16x32_bf16 v[156:159], v[108:111], v[68:71], v[156:159]\n\t" \
  "ds_read_b64_tr_b16 v[106:107], v162 offset:26624\n\t" \
  "s_cbranch_vccz QA14_%=\n\t" \
  "QB14_%=:\n\t" \
  "s_waitcnt vmcnt(12) lgkmcnt(10)\n\t" \
  "v_mfma_f32_16x16x32_bf16 v[128:131], v[112:115], v[72:75], v[128:131]\n\t" \
  "ds_read_b64_tr_b16 v[108:109], v163 offset:24576\n\t" \
  "buffer_load_dwordx4 v[32:35], v164, %[ob], s78 offen offset:0 sc1\n\t" \
  "v_mfma_f32_16x16x32_bf16 v[144:147], v[112:115], v[76:79], v[144:147]\n\t" \
  "ds_read_b64_tr_b16 v[110:111], v163 offset:26624\n\t" \
  "v_mfma_f32_16x16x32_bf16 v[132:135], v[116:119], v[72:75], v[132:135]\n\t" \
  "ds_read_b64_tr_b16 v[112:113], v160 offset:28672\n\t" \
  "v_mfma_f32_16x16x32_bf16 v[148:151], v[116:119], v[76:79], v[148:151]\n\t" \
  "ds_read_b64_tr_b16 v[114:115], v160 offset:30720\n\t" \
  "s_waitcnt lgkmcnt(10)\n\t" \
  "v_mfma_f32_16x16x32_bf16 v[136:139], v[120:123], v[72:75], v[136:139]\n\t" \
  "ds_read_b64_tr_b16 v[116:117], v161 offset:28672\n\t" \
  "v_mfma_f32_16x16x32_bf16 v[152:155], v[120:123], v[76:79], v[152:155]\n\t" \
  "ds_read_b64_tr_b16 v[118:119], v161 offset:30720\n\t" \
  "buffer_load_dwordx4 v[36:39], v164, %[ob], s78 offen offset:1024 sc1\n\t" \
  "v_mfma_f32_16x16x32_bf16 v[140:143], v[124:127], v[72:75], v[140:143]\n\t" \
  "ds_read_b64_tr_b16 v[120:121], v162 offset:28672\n\t" \
  "v_mfma_f32_16x16x32_bf16 v[156:159], v[124:127], v[76:79], v[156:159]\n\t" \
  "ds_read_b64_tr_b16 v[122:123], v162 offset:30720\n\t" \
  "s_waitcnt vmcnt(12) lgkmcnt(10)\n\t" \
  "v_mfma_f32_16x16x32_bf16 v[128:131], v[96:99], v[80:83], v[128:131]\n\t" \
  "ds_read_b64_tr_b16 v[124:125], v163 offset:28672\n\t" \
  "v_mfma_f32_16x16x32_bf16 v[144:147], v[96:99], v[84:87], v[144:147]\n\t" \
  "ds_read_b64_tr_b16 v[126:127], v163 offset:30720\n\t" \
  "buffer_load_dwordx4 v[40:43], v164, %[ob], s78 offen offset:2048 sc1\n\t" \
  "v_mfma_f32_16x16x32_bf16 v[132:135], v[100:103], v[80:83], v[132:135]\n\t" \
  "ds_read_b64_tr_b16 v[96:97], v160 offset:32768\n\t" \
  "v_mfma_f32_16x16x32_bf16 v[148:151], v[100:103], v[84:87], v[148:151]\n\t" \
  "ds_read_b64_tr_b16 v[98:99], v160 offset:34816\n\t" \
  "s_waitcnt lgkmcnt(10)\n\t" \
  "v_mfma_f32_16x16x32_bf16 v[136:139], v[104:107], v[80:83], v[136:139]\n\t" \
  "ds_read_b64_tr_b16 v[100:101], v161 offset:32768\n\t" \
  "v_mfma_f32_16x16x32_bf16 v[152:155], v[104:107], v[84:87], v[152:155]\n\t" \
  "ds_read_b64_tr_b16 v[102:103], v161 offset:34816\n\t" \
  "v_mfma_f32_16x16x32_bf16 v[140:143], v[108:111], v[80:83], v[140:143]\n\t" \
  "ds_read_b64_tr_b16 v[104:105], v162 offset:32768\n\t" \
  "buffer_load_dwordx4 v[44:47], v164, %[ob], s78 offen offset:3072 sc1\n\t" \
  "v_mfma_f32_16x16x32_bf16 v[156:159], v[108:111], v[84:87], v[156:159]\n\t" \
  "ds_read_b64_tr_b16 v[106:107], v162 offset:34816\n\t" \
  "s_waitcnt vmcnt(12) lgkmcnt(10)\n\t" \
  "v_mfma_f32_16x16x32_bf16 v[128:131], v[112:115], v[88:91], v[128:131]\n\t" \
  "ds_read_b64_tr_b16 v[108:109], v163 offset:32768\n\t" \
  "v_mfma_f32_16x16x32_bf16 v[144:147], v[112:115], v[92:95], v[144:147]\n\t" \
  "ds_read_b64_tr_b16 v[110:111], v163 offset:34816\n\t" \
  "v_mfma_f32_16x16x32_bf16 v[132:135], v[116:119], v[88:91], v[132:135]\n\t" \
  "ds_read_b64_tr_b16 v[112:113], v160 offset:36864\n\t" \
  "buffer_load_dwordx4 v[48:51], v164, %[ob], s79 offen offset:0 sc1\n\t" \
  "v_mfma_f32_16x16x32_bf16 v[148:151], v[116:119], v[92:95], v[148:151]\n\t" \
  "ds_read_b64_tr_b16 v[114:115], v160 offset:38912\n\t" \
  "s_waitcnt lgkmcnt(10)\n\t" \
  "v_mfma_f32_16x16x32_bf16 v[136:139], v[120:123], v[88:91], v[136:139]\n\t" \
  "ds_read_b64_tr_b16 v[116:117], v161 offset:36864\n\t" \
  "v_mfma_f32_16x16x32_bf16 v[152:155], v[120:123], v[92:95], v[152:155]\n\t" \
  "ds_read_b64_tr_b16 v[118:119], v161 offset:38912\n\t" \
  "v_mfma_f32_16x16x32_bf16 v[140:143], v[124:127], v[88:91], v[140:143]\n\t" \
  "ds_read_b64_tr_b16 v[120:121], v162 offset:36864\n\t" \
  "v_mfma_f32_16x16x32_bf16 v[156:159], v[124:127], v[92:95], v[156:159]\n\t" \
  "ds_read_b64_tr_b16 v[122:123], v162 offset:38912\n\t" \
  "s_waitcnt vmcnt(11) lgkmcnt(10)\n\t" \
  "v_mfma_f32_16x16x32_bf16 v[128:131], v[96:99], v[0:3], v[128:131]\n\t" \
  "ds_read_b64_tr_b16 v[124:125], v163 offset:36864\n\t" \
  "buffer_load_dwordx4 v[52:55], v164, %[ob], s79 offen offset:1024 sc1\n\t" \
  "v_mfma_f32_16x16x32_bf16 v[144:147], v[96:99], v[4:7], v[144:147]\n\t" \
  "ds_read_b64_tr_b16 v[126:127], v163 offset:38912\n\t" \
  "v_mfma_f32_16x16x32_bf16 v[132:135], v[100:103], v[0:3], v[132:135]\n\t" \
  "ds_read_b64_tr_b16 v[96:97], v160 offset:40960\n\t" \
  "v_mfma_f32_16x16x32_bf16 v[148:151], v[100:103], v[4:7], v[148:151]\n\t" \
  "ds_read_b64_tr_b16 v[98:99], v160 offset:43008\n\t" \
  "s_waitcnt lgkmcnt(10)\n\t" \
  "v_mfma_f32_16x16x32_bf16 v[136:139], v[104:107], v[0:3], v[136:139]\n\t" \
  "ds_read_b64_tr_b16 v[100:101], v161 offset:40960\n\t" \
  "buffer_load_dwordx4 v[56:59], v164, %[ob], s79 offen offset:2048 sc1\n\t" \
  "v_mfma_f32_16x16x32_bf16 v[152:155], v[104:107], v[4:7], v[152:155]\n\t" \
  "ds_read_b64_tr_b16 v[102:103], v161 offset:43008\n\t" \
  "v_mfma_f32_16x16x32_bf16 v[140:143], v[108:111], v[0:3], v[140:143]\n\t" \
  "ds_read_b64_tr_b16 v[104:105], v162 offset:40960\n\t" \
  "v_mfma_f32_16x16x32_bf16 v[156:159], v[108:111], v[4:7], v[156:159]\n\t" \
  "ds_read_b64_tr_b16 v[106:107], v162 offset:43008\n\t" \
  "s_waitcnt vmcnt(11) lgkmcnt(10)\n\t" \
  "v_mfma_f32_16x16x32_bf16 v[128:131], v[112:115], v[8:11], v[128:131]\n\t" \
  "ds_read_b64_tr_b16 v[108:109], v163 offset:40960\n\t" \
  "buffer_load_dwordx4 v[60:63], v164, %[ob], s79 offen offset:3072 sc1\n\t" \
  "v_mfma_f32_16x16x32_bf16 v[144:147], v[112:115], v[12:15], v[144:147]\n\t" \
  "ds_read_b64_tr_b16 v[110:111], v163 offset:43008\n\t" \
  "v_mfma_f32_16x16x32_bf16 v[132:135], v[116:119], v[8:11], v[132:135]\n\t" \
  "ds_read_b64_tr_b16 v[112:113], v160 offset:45056\n\t" \
  "v_mfma_f32_16x16x32_bf16 v[148:151], v[116:119], v[12:15], v[148:151]\n\t" \
  "ds_read_b64_tr_b16 v[114:115], v160 offset:47104\n\t" \
  "s_waitcnt lgkmcnt(10)\n\t" \
  "v_mfma_f32_16x16x32_bf16 v[136:139], v[120:123], v[8:11], v[136:139]\n\t" \
  "ds_read_b64_tr_b16 v[116:117], v161 offset:45056\n\t" \
  "v_mfma_f32_16x16x32_bf16 v[152:155], v[120:123], v[12:15], v[152:155]\n\t" \
  "ds_read_b64_tr_b16 v[118:119], v161 offset:47104\n\t" \
  "v_mfma_f32_16x16x32_bf16 v[140:143], v[124:127], v[8:11], v[140:143]\n\t" \
  "ds_read_b64_tr_b16 v[120:121], v162 offset:45056\n\t" \
  "v_mfma_f32_16x16x32_bf16 v[156:159], v[124:127], v[12:15], v[156:159]\n\t" \
  "ds_read_b64_tr_b16 v[122:123], v162 offset:47104\n\t" \
  "s_waitcnt vmcnt(10) lgkmcnt(10)\n\t" \
  "v_mfma_f32_16x16x32_bf16 v[128:131], v[96:99], v[16:19], v[128:131]\n\t" \
  "ds_read_b64_tr_b16 v[124:125], v163 offset:45056\n\t" \
  "v_mfma_f32_16x16x32_bf16 v[144:147], v[96:99], v[20:23], v[144:147]\n\t" \
  "ds_read_b64_tr_b16 v[126:127], v163 offset:47104\n\t" \
  "v_mfma_f32_16x16x32_bf16 v[132:135], v[100:103], v[16:19], v[132:135]\n\t" \
  "ds_read_b64_tr_b16 v[96:97], v160 offset:49152\n\t" \
  "v_mfma_f32_16x16x32_bf16 v[148:151], v[100:103], v[20:23], v[148:151]\n\t" \
  "ds_read_b64_tr_b16 v[98:99], v160 offset:51200\n\t" \
  "s_waitcnt lgkmcnt(10)\n\t" \
  "v_mfma_f32_16x16x32_bf16 v[136:139], v[104:107], v[16:19], v[136:139]\n\t" \
  "ds_read_b64_tr_b16 v[100:101], v161 offset:49152\n\t" \
  "v_xor_b32 v0, 32, v167\n\t" \
  "v_mfma_f32_16x16x32_bf16 v[152:155], v[104:107], v[20:23], v[152:155]\n\t" \
  "ds_read_b64_tr_b16 v[102:103], v161 offset:51200\n\t" \
  "v_xor_b32 v1, 64, v167\n\t" \
  "v_mfma_f32_16x16x32_bf16 v[140:143], v[108:111], v[16:19], v[140:143]\n\t" \
  "ds_read_b64_tr_b16 v[104:105], v162 offset:49152\n\t" \
  "v_xor_b32 v2, 0x60, v167\n\t" \
  "v_mfma_f32_16x16x32_bf16 v[156:159], v[108:111], v[20:23], v[156:159]\n\t" \
  "ds_read_b64_tr_b16 v[106:107], v162 offset:51200\n\t" \
  "s_waitcnt vmcnt(8) lgkmcnt(10)\n\t" \
  "v_mfma_f32_16x16x32_bf16 v[128:131], v[112:115], v[24:27], v[128:131]\n\t" \
  "ds_read_b64_tr_b16 v[108:109], v163 offset:49152\n\t" \
  "v_bfe_u32 v5, v164, 7, 3\n\t" \
  "v_mfma_f32_16x16x32_bf16 v[144:147], v[112:115], v[28:31], v[144:147]\n\t" \
  "ds_read_b64_tr_b16 v[110:111], v163 offset:51200\n\t" \
  "v_and_b32 v4, 0x70, v164\n\t" \
  "v_mfma_f32_16x16x32_bf16 v[132:135], v[116:119], v[24:27], v[132:135]\n\t" \
  "ds_read_b64_tr_b16 v[112:113], v160 offset:53248\n\t" \
  "v_lshl_or_b32 v4, v5, 12, v4\n\t" \
  "v_mfma_f32_16x16x32_bf16 v[148:151], v[116:119], v[28:31], v[148:151]\n\t" \
  "ds_read_b64_tr_b16 v[114:115], v160 offset:55296\n\t" \
  "s_waitcnt lgkmcnt(10)\n\t" \
  "v_mfma_f32_16x16x32_bf16 v[136:139], v[120:123], v[24:27], v[136:139]\n\t" \
  "ds_read_b64_tr_b16 v[116:117], v161 offset:53248\n\t" \
  "v_add_u32 v5, 0x8000, v4\n\t" \
  "v_mfma_f32_16x16x32_bf16 v[152:155], v[120:123], v[28:31], v[152:155]\n\t" \
  "ds_read_b64_tr_b16 v[118:119], v161 offset:55296\n\t" \
  "v_add_u32 v6, 0x10000, v4\n\t" \
  "v_mfma_f32_16x16x32_bf16 v[140:143], v[124:127], v[24:27], v[140:143]\n\t" \
  "ds_read_b64_tr_b16 v[120:121], v162 offset:53248\n\t" \
  "v_add_u32 v7, 0x18000, v4\n\t" \
  "v_mfma_f32_16x16x32_bf16 v[156:159], v[124:127], v[28:31], v[156:159]\n\t" \
  "ds_read_b64_tr_b16 v[122:123], v162 offset:55296\n\t" \
  "s_waitcnt vmcnt(6) lgkmcnt(10)\n\t" \
  "v_mfma_f32_16x16x32_bf16 v[128:131], v[96:99], v[32:35], v[128:131]\n\t" \
  "ds_read_b64_tr_b16 v[124:125], v163 offset:53248\n\t" \
  "v_mfma_f32_16x16x32_bf16 v[144:147], v[96:99], v[36:39], v[144:147]\n\t" \
  "ds_read_b64_tr_b16 v[126:127], v163 offset:55296\n\t" \
  "v_mfma_f32_16x16x32_bf16 v[132:135], v[100:103], v[32:35], v[132:135]\n\t" \
  "ds_read_b64_tr_b16 v[96:97], v160 offset:57344\n\t" \
  "v_mfma_f32_16x16x32_bf16 v[148:151], v[100:103], v[36:39], v[148:151]\n\t" \
  "ds_read_b64_tr_b16 v[98:99], v160 offset:59392\n\t" \
  "s_waitcnt lgkmcnt(10)\n\t" \
  "v_mfma_f32_16x16x32_bf16 v[136:139], v[104:107], v[32:35], v[136:139]\n\t" \
  "ds_read_b64_tr_b16 v[100:101], v161 offset:57344\n\t" \
  "v_mfma_f32_16x16x32_bf16 v[152:155], v[104:107], v[36:39], v[152:155]\n\t" \
  "ds_read_b64_tr_b16 v[102:103], v161 offset:59392\n\t" \
  "v_mfma_f32_16x16x32_bf16 v[140:143], v[108:111], v[32:35], v[140:143]\n\t" \
  "ds_read_b64_tr_b16 v[104:105], v162 offset:57344\n\t" \
  "v_mfma_f32_16x16x32_bf16 v[156:159], v[108:111], v[36:39], v[156:159]\n\t" \
  "ds_read_b64_tr_b16 v[106:107], v162 offset:59392\n\t" \
  "s_waitcnt vmcnt(4) lgkmcnt(10)\n\t" \
  "v_mfma_f32_16x16x32_bf16 v[128:131], v[112:115], v[40:43], v[128:131]\n\t" \
  "ds_read_b64_tr_b16 v[108:109], v163 offset:57344\n\t" \
  "v_mfma_f32_16x16x32_bf16 v[144:147], v[112:115], v[44:47], v[144:147]\n\t" \
  "ds_read_b64_tr_b16 v[110:111], v163 offset:59392\n\t" \
  "v_mfma_f32_16x16x32_bf16 v[132:135], v[116:119], v[40:43], v[132:135]\n\t" \
  "ds_read_b64_tr_b16 v[112:113], v160 offset:61440\n\t" \
  "v_mfma_f32_16x16x32_bf16 v[148:151], v[116:119], v[44:47], v[148:151]\n\t" \
  "ds_read_b64_tr_b16 v[114:115], v160 offset:63488\n\t" \
  "s_waitcnt lgkmcnt(10)\n\t" \
  "v_mfma_f32_16x16x32_bf16 v[136:139], v[120:123], v[40:43], v[136:139]\n\t" \
  "ds_read_b64_tr_b16 v[116:117], v161 offset:61440\n\t" \
  "v_mfma_f32_16x16x32_bf16 v[152:155], v[120:123], v[44:47], v[152:155]\n\t" \
  "ds_read_b64_tr_b16 v[118:119], v161 offset:63488\n\t" \
  "v_mfma_f32_16x16x32_bf16 v[140:143], v[124:127], v[40:43], v[140:143]\n\t" \
  "ds_read_b64_tr_b16 v[120:121], v162 offset:61440\n\t" \
  "v_mfma_f32_16x16x32_bf16 v[156:159], v[124:127], v[44:47], v[156:159]\n\t" \
  "ds_read_b64_tr_b16 v[122:123], v162 offset:63488\n\t" \
  "s_waitcnt vmcnt(0) lgkmcnt(10)\n\t" \
  "v_mfma_f32_16x16x32_bf16 v[128:131], v[96:99], v[48:51], v[128:131]\n\t" \
  "ds_read_b64_tr_b16 v[124:125], v163 offset:61440\n\t" \
  "v_mfma_f32_16x16x32_bf16 v[132:135], v[100:103], v[48:51], v[132:135]\n\t" \
  "ds_read_b64_tr_b16 v[126:127], v163 offset:63488\n\t" \
  "s_waitcnt lgkmcnt(6)\n\t" \
  "v_mfma_f32_16x16x32_bf16 v[136:139], v[104:107], v[48:51], v[136:139]\n\t" \
  "v_mfma_f32_16x16x32_bf16 v[140:143], v[108:111], v[48:51], v[140:143]\n\t" \
  "v_mfma_f32_16x16x32_bf16 v[128:131], v[112:115], v[56:59], v[128:131]\n\t" \
  "s_waitcnt lgkmcnt(2)\n\t" \
  "v_mfma_f32_16x16x32_bf16 v[132:135], v[116:119], v[56:59], v[132:135]\n\t" \
  "v_mfma_f32_16x16x32_bf16 v[136:139], v[120:123], v[56:59], v[136:139]\n\t" \
  "v_mfma_f32_16x16x32_bf16 v[144:147], v[96:99], v[52:55], v[144:147]\n\t" \
  "s_waitcnt lgkmcnt(0)\n\t" \
  "v_mfma_f32_16x16x32_bf16 v[140:143], v[124:127], v[56:59], v[140:143]\n\t" \
  "v_mfma_f32_16x16x32_bf16 v[144:147], v[112:115], v[60:63], v[144:147]\n\t" \
  "s_nop 0\n\t" \
  "v_cvt_pk_bf16_f32 v16, v128, v129\n\t" \
  "v_mfma_f32_16x16x32_bf16 v[148:151], v[100:103], v[52:55], v[148:151]\n\t" \
  "v_cvt_pk_bf16_f32 v17, v130, v131\n\t" \
  "v_cvt_pk_bf16_f32 v18, v132, v133\n\t" \
  "v_mfma_f32_16x16x32_bf16 v[148:151], v[116:119], v[60:63], v[148:151]\n\t" \
  "v_cvt_pk_bf16_f32 v19, v134, v135\n\t" \
  "ds_write_b64 v167, v[16:17]\n\t" \
  "v_mfma_f32_16x16x32_bf16 v[152:155], v[104:107], v[52:55], v[152:155]\n\t" \
  "v_cvt_pk_bf16_f32 v20, v136, v137\n\t" \
  "ds_write_b64 v0, v[18:19]\n\t" \
  "v_mfma_f32_16x16x32_bf16 v[152:155], v[120:123], v[60:63], v[152:155]\n\t" \
  "v_cvt_pk_bf16_f32 v21, v138, v139\n\t" \
  "v_cvt_pk_bf16_f32 v22, v140, v141\n\t" \
  "v_mfma_f32_16x16x32_bf16 v[156:159], v[108:111], v[52:55], v[156:159]\n\t" \
  "v_cvt_pk_bf16_f32 v23, v142, v143\n\t" \
  "ds_write_b64 v1, v[20:21]\n\t" \
  "v_mfma_f32_16x16x32_bf16 v[156:159], v[124:127], v[60:63], v[156:159]\n\t" \
  "v_cvt_pk_bf16_f32 v24, v144, v145\n\t" \
  "ds_write_b64 v2, v[22:23]\n\t" \
  "v_cvt_pk_bf16_f32 v25, v146, v147\n\t" \
  "ds_read_b128 v[64:67], v166 offset:0\n\t" \
  "v_cvt_pk_bf16_f32 v26, v148, v149\n\t" \
  "ds_read_b128 v[68:71], v166 offset:1024\n\t" \
  "v_cvt_pk_bf16_f32 v27, v150, v151\n\t" \
  "ds_write_b64 v167, v[24:25]\n\t" \
  "v_cvt_pk_bf16_f32 v28, v152, v153\n\t" \
  "v_cvt_pk_bf16_f32 v29, v154, v155\n\t" \
  "ds_write_b64 v0, v[26:27]\n\t" \
  "v_cvt_pk_bf16_f32 v30, v156, v157\n\t" \
  "v_cvt_pk_bf16_f32 v31, v158, v159\n\t" \
  "ds_write_b64 v1, v[28:29]\n\t" \
  "ds_write_b64 v2, v[30:31]\n\t" \
  "ds_read_b128 v[72:75], v166 offset:0\n\t" \
  "ds_read_b128 v[76:79], v166 offset:1024\n\t" \
  "s_waitcnt lgkmcnt(7)\n\t" \
  "global_store_dwordx4 v4, v[64:67], %[op] sc1\n\t" \
  "s_waitcnt lgkmcnt(6)\n\t" \
  "global_store_dwordx4 v5, v[68:71], %[op] sc1\n\t" \
  "s_waitcnt lgkmcnt(1)\n\t" \
  "global_store_dwordx4 v6, v[72:75], %[op] sc1\n\t" \
  "s_waitcnt lgkmcnt(0)\n\t" \
  "global_store_dwordx4 v7, v[76:79], %[op] sc1\n\t" \
  "s_nop 1\n\t"
#define G3_EP 2
#define G3_CLOB "v0", "v1", "v2", "v3", "v4", "v5", "v6", "v7", "v8", "v9", "v10", "v11", "v12", "v13", "v14", "v15", "v16", "v17", "v18", "v19", "v20", "v21", "v22", "v23", "v24", "v25", "v26", "v27", "v28", "v29", "v30", "v31", "v32", "v33", "v34", "v35", "v36", "v37", "v38", "v39", "v40", "v41", "v42", "v43", "v44", "v45", "v46", "v47", "v48", "v49", "v50", "v51", "v52", "v53", "v54", "v55", "v56", "v57", "v58", "v59", "v60", "v61", "v62", "v63", "v64", "v65", "v66", "v67", "v68", "v69", "v70", "v71", "v72", "v73", "v74", "v75", "v76", "v77", "v78", "v79", "v80", "v81", "v82", "v83", "v84", "v85", "v86", "v87", "v88", "v89", "v90", "v91", "v92", "v93", "v94", "v95", "v97", "v98", "v99", "v100", "v101", "v102", "v103", "v104", "v105", "v106", "v107", "v108", "v109", "v110", "v111", "v112", "v113", "v114", "v115", "v116", "v117", "v118", "v119", "v120", "v121", "v122", "v123", "v124", "v125", "v126", "v127", "v128", "v129", "v130", "v131", "v132", "v133", "v134", "v135", "v136", "v137", "v138", "v139", "v140", "v141", "v142", "v143", "v144", "v145", "v146", "v147", "v148", "v149", "v150", "v151", "v152", "v153", "v154", "v155", "v156", "v157", "v158", "v159", "s64", "s65", "s66", "s67", "s68", "s69", "s70", "s71", "s72", "s73", "s74", "s75", "s76", "s77", "s78", "s79"

#define PP_ASM \
  "v_mov_b32 v60, 0\n\t" \
  "s_mov_b32 %[t0], %[n0]\n\t" \
  "s_cmp_eq_u32 %[t0], 0\n\t" \
  "s_cbranch_scc1 PAx_%=\n\t" \
  "PA_%=:\n\t" \
  "s_barrier\n\t" \
  "s_sub_u32 %[t0], %[t0], 1\n\t" \
  "s_cmp_lg_u32 %[t0], 0\n\t" \
  "s_cbranch_scc1 PA_%=\n\t" \
  "PAx_%=:\n\t" \
  "s_mov_b32 m0, %[shd]\n\t" \
  "global_load_dword v160, v60, %[fl] sc1\n\t" \
  "global_load_lds_dwordx4 v163, %[shp]\n\t" \
  "s_mov_b32 m0, %[tgd]\n\t" \
  "s_mov_b64 exec, %[em]\n\t" \
  "global_load_lds_dword v60, %[fl] sc1\n\t" \
  "s_mov_b64 exec, -1\n\t" \
  "global_load_ushort v161, v60, %[hp]\n\t" \
  "global_load_ushort v162, v60, %[dp]\n\t" \
  "global_load_dwordx4 v[0:3], v152, %[kb0]\n\t" \
  "global_load_dwordx4 v[4:7], v153, %[kb0]\n\t" \
  "global_load_dwordx4 v[8:11], v154, %[kb0]\n\t" \
  "global_load_dwordx4 v[12:15], v155, %[kb3] offset:0\n\t" \
  "global_load_dwordx4 v[28:31], v155, %[vb0] offset:0\n\t" \
  "s_setprio 2\n\t" \
  "s_setprio 3\n\t" \
  "s_barrier\n\t" \
  "global_load_dwordx4 v[16:19], v155, %[kb3] offset:2048\n\t" \
  "global_load_dwordx4 v[32:35], v155, %[vb0] offset:2048\n\t" \
  "global_load_dwordx4 v[20:23], v155, %[kb5] offset:0\n\t" \
  "s_setprio 2\n\t" \
  "s_waitcnt vmcnt(8)\n\t" \
  "ds_read_b128 v[128:131], v156 offset:0\n\t" \
  "ds_read_b128 v[132:135], v156 offset:16\n\t" \
  "ds_read_b128 v[136:139], v156 offset:32\n\t" \
  "ds_read_b128 v[140:143], v156 offset:48\n\t" \
  "s_waitcnt lgkmcnt(0)\n\t" \
  "v_lshlrev_b32 v64, 16, v128\n\t" \
  "v_and_b32 v65, 0xffff0000, v128\n\t" \
  "v_lshlrev_b32 v66, 16, v129\n\t" \
  "v_and_b32 v67, 0xffff0000, v129\n\t" \
  "v_lshlrev_b32 v68, 16, v130\n\t" \
  "v_and_b32 v69, 0xffff0000, v130\n\t" \
  "v_lshlrev_b32 v70, 16, v131\n\t" \
  "v_and_b32 v71, 0xffff0000, v131\n\t" \
  "v_lshlrev_b32 v72, 16, v132\n\t" \
  "v_and_b32 v73, 0xffff0000, v132\n\t" \
  "v_lshlrev_b32 v74, 16, v133\n\t" \
  "v_and_b32 v75, 0xffff0000, v133\n\t" \
  "v_lshlrev_b32 v76, 16, v134\n\t" \
  "v_and_b32 v77, 0xffff0000, v134\n\t" \
  "v_lshlrev_b32 v78, 16, v135\n\t" \
  "v_and_b32 v79, 0xffff0000, v135\n\t" \
  "v_lshlrev_b32 v80, 16, v136\n\t" \
  "v_and_b32 v81, 0xffff0000, v136\n\t" \
  "v_lshlrev_b32 v82, 16, v137\n\t" \
  "v_and_b32 v83, 0xffff0000, v137\n\t" \
  "v_lshlrev_b32 v84, 16, v138\n\t" \
  "v_and_b32 v85, 0xffff0000, v138\n\t" \
  "v_lshlrev_b32 v86, 16, v139\n\t" \
  "v_and_b32 v87, 0xffff0000, v139\n\t" \
  "v_lshlrev_b32 v88, 16, v140\n\t" \
  "v_and_b32 v89, 0xffff0000, v140\n\t" \
  "v_lshlrev_b32 v90, 16, v141\n\t" \
  "v_and_b32 v91, 0xffff0000, v141\n\t" \
  "v_lshlrev_b32 v92, 16, v142\n\t" \
  "v_and_b32 v93, 0xffff0000, v142\n\t" \
  "v_lshlrev_b32 v94, 16, v143\n\t" \
  "v_and_b32 v95, 0xffff0000, v143\n\t" \
  "s_waitcnt vmcnt(7)\n\t" \
  "s_mov_b64 exec, %[zm]\n\t" \
  "v_mov_b32 v0, 0\n\t" \
  "v_mov_b32 v1, 0\n\t" \
  "v_mov_b32 v2, 0\n\t" \
  "v_mov_b32 v3, 0\n\t" \
  "s_mov_b64 exec, -1\n\t" \
  "v_lshlrev_b32 v128, 16, v0\n\t" \
  "v_and_b32 v129, 0xffff0000, v0\n\t" \
  "v_lshlrev_b32 v130, 16, v1\n\t" \
  "v_and_b32 v131, 0xffff0000, v1\n\t" \
  "v_lshlrev_b32 v132, 16, v2\n\t" \
  "v_and_b32 v133, 0xffff0000, v2\n\t" \
  "v_lshlrev_b32 v134, 16, v3\n\t" \
  "v_and_b32 v135, 0xffff0000, v3\n\t" \
  "v_mul_f32 v96, v128, v67\n\t" \
  "v_mul_f32 v97, v129, v71\n\t" \
  "v_mul_f32 v98, v130, v75\n\t" \
  "v_mul_f32 v99, v131, v79\n\t" \
  "v_mul_f32 v100, v132, v83\n\t" \
  "v_mul_f32 v101, v133, v87\n\t" \
  "v_mul_f32 v102, v134, v91\n\t" \
  "v_mul_f32 v103, v135, v95\n\t" \
  "s_waitcnt vmcnt(6)\n\t" \
  "s_mov_b64 exec, %[zm]\n\t" \
  "v_mov_b32 v4, 0\n\t" \
  "v_mov_b32 v5, 0\n\t" \
  "v_mov_b32 v6, 0\n\t" \
  "v_mov_b32 v7, 0\n\t" \
  "s_mov_b64 exec, -1\n\t" \
  "v_lshlrev_b32 v128, 16, v4\n\t" \
  "v_and_b32 v129, 0xffff0000, v4\n\t" \
  "v_lshlrev_b32 v130, 16, v5\n\t" \
  "v_and_b32 v131, 0xffff0000, v5\n\t" \
  "v_lshlrev_b32 v132, 16, v6\n\t" \
  "v_and_b32 v133, 0xffff0000, v6\n\t" \
  "v_lshlrev_b32 v134, 16, v7\n\t" \
  "v_and_b32 v135, 0xffff0000, v7\n\t" \
  "v_fmac_f32 v96, v128, v66\n\t" \
  "v_fmac_f32 v97, v129, v70\n\t" \
  "v_fmac_f32 v98, v130, v74\n\t" \
  "v_fmac_f32 v99, v131, v78\n\t" \
  "v_fmac_f32 v100, v132, v82\n\t" \
  "v_fmac_f32 v101, v133, v86\n\t" \
  "v_fmac_f32 v102, v134, v90\n\t" \
  "v_fmac_f32 v103, v135, v94\n\t" \
  "v_mul_f32 v104, v128, v67\n\t" \
  "v_mul_f32 v105, v129, v71\n\t" \
  "v_mul_f32 v106, v130, v75\n\t" \
  "v_mul_f32 v107, v131, v79\n\t" \
  "v_mul_f32 v108, v132, v83\n\t" \
  "v_mul_f32 v109, v133, v87\n\t" \
  "v_mul_f32 v110, v134, v91\n\t" \
  "v_mul_f32 v111, v135, v95\n\t" \
  "s_waitcnt vmcnt(5)\n\t" \
  "s_mov_b64 exec, %[zm]\n\t" \
  "v_mov_b32 v8, 0\n\t" \
  "v_mov_b32 v9, 0\n\t" \
  "v_mov_b32 v10, 0\n\t" \
  "v_mov_b32 v11, 0\n\t" \
  "s_mov_b64 exec, -1\n\t" \
  "v_lshlrev_b32 v128, 16, v8\n\t" \
  "v_and_b32 v129, 0xffff0000, v8\n\t" \
  "v_lshlrev_b32 v130, 16, v9\n\t" \
  "v_and_b32 v131, 0xffff0000, v9\n\t" \
  "v_lshlrev_b32 v132, 16, v10\n\t" \
  "v_and_b32 v133, 0xffff0000, v10\n\t" \
  "v_lshlrev_b32 v134, 16, v11\n\t" \
  "v_and_b32 v135, 0xffff0000, v11\n\t" \
  "v_fmac_f32 v96, v128, v65\n\t" \
  "v_fmac_f32 v97, v129, v69\n\t" \
  "v_fmac_f32 v98, v130, v73\n\t" \
  "v_fmac_f32 v99, v131, v77\n\t" \
  "v_fmac_f32 v100, v132, v81\n\t" \
  "v_fmac_f32 v101, v133, v85\n\t" \
  "v_fmac_f32 v102, v134, v89\n\t" \
  "v_fmac_f32 v103, v135, v93\n\t" \
  "v_fmac_f32 v104, v128, v66\n\t" \
  "v_fmac_f32 v105, v129, v70\n\t" \
  "v_fmac_f32 v106, v130, v74\n\t" \
  "v_fmac_f32 v107, v131, v78\n\t" \
  "s_mov_b32 s85, 0x3c0000\n\t" \
  "s_load_dword s84, %[ph], s85\n\t" \
  "s_mov_b32 s85, 0x3c0800\n\t" \
  "s_load_dword s84, %[ph], s85\n\t" \
  "s_mov_b32 s85, 0x3c1000\n\t" \
  "s_load_dword s84, %[ph], s85\n\t" \
  "s_setprio 3\n\t" \
  "s_barrier\n\t" \
  "global_load_dwordx4 v[36:39], v155, %[vb2] offset:0\n\t" \
  "global_load_dwordx4 v[24:27], v155, %[kb5] offset:2048\n\t" \
  "global_load_dwordx4 v[40:43], v155, %[vb2] offset:2048\n\t" \
  "s_setprio 2\n\t" \
  "v_fmac_f32 v108, v132, v82\n\t" \
  "v_fmac_f32 v109, v133, v86\n\t" \
  "v_fmac_f32 v110, v134, v90\n\t" \
  "v_fmac_f32 v111, v135, v94\n\t" \
  "v_mul_f32 v112, v128, v67\n\t" \
  "v_mul_f32 v113, v129, v71\n\t" \
  "v_mul_f32 v114, v130, v75\n\t" \
  "v_mul_f32 v115, v131, v79\n\t" \
  "v_mul_f32 v116, v132, v83\n\t" \
  "v_mul_f32 v117, v133, v87\n\t" \
  "v_mul_f32 v118, v134, v91\n\t" \
  "v_mul_f32 v119, v135, v95\n\t" \
  "s_waitcnt vmcnt(7)\n\t" \
  "v_lshlrev_b32 v128, 16, v12\n\t" \
  "v_and_b32 v129, 0xffff0000, v12\n\t" \
  "v_lshlrev_b32 v130, 16, v13\n\t" \
  "v_and_b32 v131, 0xffff0000, v13\n\t" \
  "v_lshlrev_b32 v132, 16, v14\n\t" \
  "v_and_b32 v133, 0xffff0000, v14\n\t" \
  "v_lshlrev_b32 v134, 16, v15\n\t" \
  "v_and_b32 v135, 0xffff0000, v15\n\t" \
  "v_fmac_f32 v96, v128, v64\n\t" \
  "v_fmac_f32 v97, v129, v68\n\t" \
  "v_fmac_f32 v98, v130, v72\n\t" \
  "v_fmac_f32 v99, v131, v76\n\t" \
  "v_fmac_f32 v100, v132, v80\n\t" \
  "v_fmac_f32 v101, v133, v84\n\t" \
  "v_fmac_f32 v102, v134, v88\n\t" \
  "v_fmac_f32 v103, v135, v92\n\t" \
  "v_fmac_f32 v104, v128, v65\n\t" \
  "v_fmac_f32 v105, v129, v69\n\t" \
  "v_fmac_f32 v106, v130, v73\n\t" \
  "v_fmac_f32 v107, v131, v77\n\t" \
  "v_fmac_f32 v108, v132, v81\n\t" \
  "v_fmac_f32 v109, v133, v85\n\t" \
  "v_fmac_f32 v110, v134, v89\n\t" \
  "v_fmac_f32 v111, v135, v93\n\t" \
  "v_fmac_f32 v112, v128, v66\n\t" \
  "v_fmac_f32 v113, v129, v70\n\t" \
  "v_fmac_f32 v114, v130, v74\n\t" \
  "v_fmac_f32 v115, v131, v78\n\t" \
  "v_fmac_f32 v116, v132, v82\n\t" \
  "v_fmac_f32 v117, v133, v86\n\t" \
  "v_fmac_f32 v118, v134, v90\n\t" \
  "v_fmac_f32 v119, v135, v94\n\t" \
  "v_mul_f32 v120, v128, v67\n\t" \
  "v_mul_f32 v121, v129, v71\n\t" \
  "v_mul_f32 v122, v130, v75\n\t" \
  "v_mul_f32 v123, v131, v79\n\t" \
  "v_mul_f32 v124, v132, v83\n\t" \
  "v_mul_f32 v125, v133, v87\n\t" \
  "v_mul_f32 v126, v134, v91\n\t" \
  "v_mul_f32 v127, v135, v95\n\t" \
  "s_waitcnt vmcnt(6)\n\t" \
  "v_lshlrev_b32 v128, 16, v28\n\t" \
  "v_and_b32 v129, 0xffff0000, v28\n\t" \
  "v_lshlrev_b32 v130, 16, v29\n\t" \
  "v_and_b32 v131, 0xffff0000, v29\n\t" \
  "v_lshlrev_b32 v132, 16, v30\n\t" \
  "v_and_b32 v133, 0xffff0000, v30\n\t" \
  "v_lshlrev_b32 v134, 16, v31\n\t" \
  "v_and_b32 v135, 0xffff0000, v31\n\t" \
  "v_mul_f32 v96, v96, v128\n\t" \
  "v_mul_f32 v97, v97, v129\n\t" \
  "v_mul_f32 v98, v98, v130\n\t" \
  "v_mul_f32 v99, v99, v131\n\t" \
  "v_mul_f32 v100, v100, v132\n\t" \
  "v_mul_f32 v101, v101, v133\n\t" \
  "v_mul_f32 v102, v102, v134\n\t" \
  "v_mul_f32 v103, v103, v135\n\t" \
  "v_cvt_pk_bf16_f32 v136, v96, v97\n\t" \
  "v_cvt_pk_bf16_f32 v137, v98, v99\n\t" \
  "v_cvt_pk_bf16_f32 v138, v100, v101\n\t" \
  "v_cvt_pk_bf16_f32 v139, v102, v103\n\t" \
  "v_mov_b32_dpp v128, v136 row_ror:8 row_mask:0xf bank_mask:0xf\n\t" \
  "v_mov_b32_dpp v129, v137 row_ror:8 row_mask:0xf bank_mask:0xf\n\t" \
  "v_mov_b32_dpp v130, v138 row_ror:8 row_mask:0xf bank_mask:0xf\n\t" \
  "v_mov_b32_dpp v131, v139 row_ror:8 row_mask:0xf bank_mask:0xf\n\t" \
  "v_perm_b32 v136, v128, v136, v157\n\t" \
  "v_perm_b32 v137, v129, v137, v157\n\t" \
  "v_perm_b32 v138, v130, v138, v157\n\t" \
  "v_perm_b32 v139, v131, v139, v157\n\t" \
  "v_permlane16_swap_b32 v136, v137\n\t" \
  "s_nop 0\n\t" \
  "v_permlane16_swap_b32 v138, v139\n\t" \
  "s_nop 1\n\t" \
  "v_permlane32_swap_b32 v136, v138\n\t" \
  "v_permlane32_swap_b32 v137, v139\n\t" \
  "s_waitcnt vmcnt(5)\n\t" \
  "v_lshlrev_b32 v128, 16, v16\n\t" \
  "v_and_b32 v129, 0xffff0000, v16\n\t" \
  "v_lshlrev_b32 v130, 16, v17\n\t" \
  "v_and_b32 v131, 0xffff0000, v17\n\t" \
  "v_lshlrev_b32 v132, 16, v18\n\t" \
  "v_and_b32 v133, 0xffff0000, v18\n\t" \
  "v_lshlrev_b32 v134, 16, v19\n\t" \
  "v_and_b32 v135, 0xffff0000, v19\n\t" \
  "v_fmac_f32 v104, v128, v64\n\t" \
  "v_fmac_f32 v105, v129, v68\n\t" \
  "v_fmac_f32 v106, v130, v72\n\t" \
  "v_fmac_f32 v107, v131, v76\n\t" \
  "v_fmac_f32 v108, v132, v80\n\t" \
  "v_fmac_f32 v109, v133, v84\n\t" \
  "s_mov_b32 s85, 0x420000\n\t" \
  "s_load_dword s84, %[ph], s85\n\t" \
  "s_mov_b32 s85, 0x420800\n\t" \
  "s_load_dword s84, %[ph], s85\n\t" \
  "s_mov_b32 s85, 0x421000\n\t" \
  "s_load_dword s84, %[ph], s85\n\t" \
  "s_setprio 3\n\t" \
  "s_barrier\n\t" \
  "global_store_dwordx4 v159, v[136:139], %[sb] sc1\n\t" \
  "v_add_u32 v158, 0x80, v159\n\t" \
  "v_bfi_b32 v159, %[mk], v158, v159\n\t" \
  "s_setprio 2\n\t" \
  "v_fmac_f32 v110, v134, v88\n\t" \
  "v_fmac_f32 v111, v135, v92\n\t" \
  "v_fmac_f32 v112, v128, v65\n\t" \
  "v_fmac_f32 v113, v129, v69\n\t" \
  "v_fmac_f32 v114, v130, v73\n\t" \
  "v_fmac_f32 v115, v131, v77\n\t" \
  "v_fmac_f32 v116, v132, v81\n\t" \
  "v_fmac_f32 v117, v133, v85\n\t" \
  "v_fmac_f32 v118, v134, v89\n\t" \
  "v_fmac_f32 v119, v135, v93\n\t" \
  "v_fmac_f32 v120, v128, v66\n\t" \
  "v_fmac_f32 v121, v129, v70\n\t" \
  "v_fmac_f32 v122, v130, v74\n\t" \
  "v_fmac_f32 v123, v131, v78\n\t" \
  "v_fmac_f32 v124, v132, v82\n\t" \
  "v_fmac_f32 v125, v133, v86\n\t" \
  "v_fmac_f32 v126, v134, v90\n\t" \
  "v_fmac_f32 v127, v135, v94\n\t" \
  "s_waitcnt vmcnt(5)\n\t" \
  "v_lshlrev_b32 v128, 16, v32\n\t" \
  "v_and_b32 v129, 0xffff0000, v32\n\t" \
  "v_lshlrev_b32 v130, 16, v33\n\t" \
  "v_and_b32 v131, 0xffff0000, v33\n\t" \
  "v_lshlrev_b32 v132, 16, v34\n\t" \
  "v_and_b32 v133, 0xffff0000, v34\n\t" \
  "v_lshlrev_b32 v134, 16, v35\n\t" \
  "v_and_b32 v135, 0xffff0000, v35\n\t" \
  "v_mul_f32 v104, v104, v128\n\t" \
  "v_mul_f32 v105, v105, v129\n\t" \
  "v_mul_f32 v106, v106, v130\n\t" \
  "v_mul_f32 v107, v107, v131\n\t" \
  "v_mul_f32 v108, v108, v132\n\t" \
  "v_mul_f32 v109, v109, v133\n\t" \
  "v_mul_f32 v110, v110, v134\n\t" \
  "v_mul_f32 v111, v111, v135\n\t" \
  "v_cvt_pk_bf16_f32 v140, v104, v105\n\t" \
  "v_cvt_pk_bf16_f32 v141, v106, v107\n\t" \
  "v_cvt_pk_bf16_f32 v142, v108, v109\n\t" \
  "v_cvt_pk_bf16_f32 v143, v110, v111\n\t" \
  "v_mov_b32_dpp v128, v140 row_ror:8 row_mask:0xf bank_mask:0xf\n\t" \
  "v_mov_b32_dpp v129, v141 row_ror:8 row_mask:0xf bank_mask:0xf\n\t" \
  "v_mov_b32_dpp v130, v142 row_ror:8 row_mask:0xf bank_mask:0xf\n\t" \
  "v_mov_b32_dpp v131, v143 row_ror:8 row_mask:0xf bank_mask:0xf\n\t" \
  "v_perm_b32 v140, v128, v140, v157\n\t" \
  "v_perm_b32 v141, v129, v141, v157\n\t" \
  "v_perm_b32 v142, v130, v142, v157\n\t" \
  "v_perm_b32 v143, v131, v143, v157\n\t" \
  "v_permlane16_swap_b32 v140, v141\n\t" \
  "s_nop 0\n\t" \
  "v_permlane16_swap_b32 v142, v143\n\t" \
  "s_nop 1\n\t" \
  "v_permlane32_swap_b32 v140, v142\n\t" \
  "v_permlane32_swap_b32 v141, v143\n\t" \
  "s_waitcnt vmcnt(4)\n\t" \
  "v_lshlrev_b32 v128, 16, v20\n\t" \
  "v_and_b32 v129, 0xffff0000, v20\n\t" \
  "v_lshlrev_b32 v130, 16, v21\n\t" \
  "v_and_b32 v131, 0xffff0000, v21\n\t" \
  "v_lshlrev_b32 v132, 16, v22\n\t" \
  "v_and_b32 v133, 0xffff0000, v22\n\t" \
  "v_lshlrev_b32 v134, 16, v23\n\t" \
  "v_and_b32 v135, 0xffff0000, v23\n\t" \
  "v_fmac_f32 v112, v128, v64\n\t" \
  "v_fmac_f32 v113, v129, v68\n\t" \
  "v_fmac_f32 v114, v130, v72\n\t" \
  "v_fmac_f32 v115, v131, v76\n\t" \
  "v_fmac_f32 v116, v132, v80\n\t" \
  "v_fmac_f32 v117, v133, v84\n\t" \
  "v_fmac_f32 v118, v134, v88\n\t" \
  "v_fmac_f32 v119, v135, v92\n\t" \
  "v_fmac_f32 v120, v128, v65\n\t" \
  "v_fmac_f32 v121, v129, v69\n\t" \
  "v_fmac_f32 v122, v130, v73\n\t" \
  "v_fmac_f32 v123, v131, v77\n\t" \
  "v_fmac_f32 v124, v132, v81\n\t" \
  "v_fmac_f32 v125, v133, v85\n\t" \
  "v_fmac_f32 v126, v134, v89\n\t" \
  "v_fmac_f32 v127, v135, v93\n\t" \
  "s_waitcnt vmcnt(3)\n\t" \
  "v_lshlrev_b32 v128, 16, v36\n\t" \
  "v_and_b32 v129, 0xffff0000, v36\n\t" \
  "v_lshlrev_b32 v130, 16, v37\n\t" \
  "v_and_b32 v131, 0xffff0000, v37\n\t" \
  "v_lshlrev_b32 v132, 16, v38\n\t" \
  "v_and_b32 v133, 0xffff0000, v38\n\t" \
  "v_lshlrev_b32 v134, 16, v39\n\t" \
  "v_and_b32 v135, 0xffff0000, v39\n\t" \
  "v_mul_f32 v112, v112, v128\n\t" \
  "v_mul_f32 v113, v113, v129\n\t" \
  "v_mul_f32 v114, v114, v130\n\t" \
  "v_mul_f32 v115, v115, v131\n\t" \
  "v_mul_f32 v116, v116, v132\n\t" \
  "v_mul_f32 v117, v117, v133\n\t" \
  "v_mul_f32 v118, v118, v134\n\t" \
  "v_mul_f32 v119, v119, v135\n\t" \
  "v_cvt_pk_bf16_f32 v144, v112, v113\n\t" \
  "v_cvt_pk_bf16_f32 v145, v114, v115\n\t" \
  "v_cvt_pk_bf16_f32 v146, v116, v117\n\t" \
  "v_cvt_pk_bf16_f32 v147, v118, v119\n\t" \
  "v_mov_b32_dpp v128, v144 row_ror:8 row_mask:0xf bank_mask:0xf\n\t" \
  "v_mov_b32_dpp v129, v145 row_ror:8 row_mask:0xf bank_mask:0xf\n\t" \
  "v_mov_b32_dpp v130, v146 row_ror:8 row_mask:0xf bank_mask:0xf\n\t" \
  "v_mov_b32_dpp v131, v147 row_ror:8 row_mask:0xf bank_mask:0xf\n\t" \
  "s_mov_b32 s85, 0x480000\n\t" \
  "s_load_dword s84, %[ph], s85\n\t" \
  "s_mov_b32 s85, 0x480800\n\t" \
  "s_load_dword s84, %[ph], s85\n\t" \
  "s_mov_b32 s85, 0x481000\n\t" \
  "s_load_dword s84, %[ph], s85\n\t" \
  "s_setprio 3\n\t" \
  "s_barrier\n\t" \
  "global_store_dwordx4 v159, v[140:143], %[sb] sc1\n\t" \
  "v_add_u32 v158, 0x80, v159\n\t" \
  "v_bfi_b32 v159, %[mk], v158, v159\n\t" \
  "s_setprio 2\n\t" \
  "v_perm_b32 v144, v128, v144, v157\n\t" \
  "v_perm_b32 v145, v129, v145, v157\n\t" \
  "v_perm_b32 v146, v130, v146, v157\n\t" \
  "v_perm_b32 v147, v131, v147, v157\n\t" \
  "v_permlane16_swap_b32 v144, v145\n\t" \
  "s_nop 0\n\t" \
  "v_permlane16_swap_b32 v146, v147\n\t" \
  "s_nop 1\n\t" \
  "v_permlane32_swap_b32 v144, v146\n\t" \
  "v_permlane32_swap_b32 v145, v147\n\t" \
  "s_waitcnt vmcnt(3)\n\t" \
  "v_lshlrev_b32 v128, 16, v24\n\t" \
  "v_and_b32 v129, 0xffff0000, v24\n\t" \
  "v_lshlrev_b32 v130, 16, v25\n\t" \
  "v_and_b32 v131, 0xffff0000, v25\n\t" \
  "v_lshlrev_b32 v132, 16, v26\n\t" \
  "v_and_b32 v133, 0xffff0000, v26\n\t" \
  "v_lshlrev_b32 v134, 16, v27\n\t" \
  "v_and_b32 v135, 0xffff0000, v27\n\t" \
  "v_fmac_f32 v120, v128, v64\n\t" \
  "v_fmac_f32 v121, v129, v68\n\t" \
  "v_fmac_f32 v122, v130, v72\n\t" \
  "v_fmac_f32 v123, v131, v76\n\t" \
  "v_fmac_f32 v124, v132, v80\n\t" \
  "v_fmac_f32 v125, v133, v84\n\t" \
  "v_fmac_f32 v126, v134, v88\n\t" \
  "v_fmac_f32 v127, v135, v92\n\t" \
  "s_waitcnt vmcnt(2)\n\t" \
  "v_lshlrev_b32 v128, 16, v40\n\t" \
  "v_and_b32 v129, 0xffff0000, v40\n\t" \
  "v_lshlrev_b32 v130, 16, v41\n\t" \
  "v_and_b32 v131, 0xffff0000, v41\n\t" \
  "v_lshlrev_b32 v132, 16, v42\n\t" \
  "v_and_b32 v133, 0xffff0000, v42\n\t" \
  "v_lshlrev_b32 v134, 16, v43\n\t" \
  "v_and_b32 v135, 0xffff0000, v43\n\t" \
  "v_mul_f32 v120, v120, v128\n\t" \
  "v_mul_f32 v121, v121, v129\n\t" \
  "v_mul_f32 v122, v122, v130\n\t" \
  "v_mul_f32 v123, v123, v131\n\t" \
  "v_mul_f32 v124, v124, v132\n\t" \
  "v_mul_f32 v125, v125, v133\n\t" \
  "v_mul_f32 v126, v126, v134\n\t" \
  "v_mul_f32 v127, v127, v135\n\t" \
  "v_cvt_pk_bf16_f32 v148, v120, v121\n\t" \
  "v_cvt_pk_bf16_f32 v149, v122, v123\n\t" \
  "v_cvt_pk_bf16_f32 v150, v124, v125\n\t" \
  "v_cvt_pk_bf16_f32 v151, v126, v127\n\t" \
  "v_mov_b32_dpp v128, v148 row_ror:8 row_mask:0xf bank_mask:0xf\n\t" \
  "v_mov_b32_dpp v129, v149 row_ror:8 row_mask:0xf bank_mask:0xf\n\t" \
  "v_mov_b32_dpp v130, v150 row_ror:8 row_mask:0xf bank_mask:0xf\n\t" \
  "v_mov_b32_dpp v131, v151 row_ror:8 row_mask:0xf bank_mask:0xf\n\t" \
  "v_perm_b32 v148, v128, v148, v157\n\t" \
  "v_perm_b32 v149, v129, v149, v157\n\t" \
  "v_perm_b32 v150, v130, v150, v157\n\t" \
  "v_perm_b32 v151, v131, v151, v157\n\t" \
  "v_permlane16_swap_b32 v148, v149\n\t" \
  "s_nop 0\n\t" \
  "v_permlane16_swap_b32 v150, v151\n\t" \
  "s_nop 1\n\t" \
  "v_permlane32_swap_b32 v148, v150\n\t" \
  "v_permlane32_swap_b32 v149, v151\n\t" \
  "s_mov_b32 s85, 0x4e0000\n\t" \
  "s_load_dword s84, %[ph], s85\n\t" \
  "s_mov_b32 s85, 0x4e0800\n\t" \
  "s_load_dword s84, %[ph], s85\n\t" \
  "s_mov_b32 s85, 0x4e1000\n\t" \
  "s_load_dword s84, %[ph], s85\n\t" \
  "s_mov_b32 m0, %[hd]\n\t" \
  "s_nop 0\n\t" \
  "global_load_lds_dwordx4 v163, %[hp] offset:0\n\t" \
  "global_load_lds_dwordx4 v163, %[hp] offset:1024\n\t" \
  "global_load_lds_dwordx4 v163, %[hp] offset:2048\n\t" \
  "global_load_lds_dwordx4 v163, %[hp] offset:3072\n\t" \
  "s_setprio 3\n\t" \
  "s_barrier\n\t" \
  "global_store_dwordx4 v159, v[144:147], %[sb] sc1\n\t" \
  "v_add_u32 v158, 0x80, v159\n\t" \
  "v_bfi_b32 v159, %[mk], v158, v159\n\t" \
  "global_store_dwordx4 v159, v[148:151], %[sb] sc1\n\t" \
  "s_setprio 2\n\t" \
  "s_mov_b32 s85, 0x540000\n\t" \
  "s_load_dword s84, %[ph], s85\n\t" \
  "s_mov_b32 s85, 0x540800\n\t" \
  "s_load_dword s84, %[ph], s85\n\t" \
  "s_mov_b32 s85, 0x541000\n\t" \
  "s_load_dword s84, %[ph], s85\n\t" \
  "s_setprio 3\n\t" \
  "s_barrier\n\t" \
  "s_waitcnt vmcnt(0)\n\t" \
  "v_add_u32 v61, 1, v160\n\t" \
  "s_mov_b64 exec, 1\n\t" \
  "global_store_dword v60, v61, %[ff] sc1\n\t" \
  "s_mov_b64 exec, -1\n\t" \
  "s_setprio 2\n\t" \
  "s_waitcnt vmcnt(3)\n\t" \
  "ds_read_b64_tr_b16 v[64:65], v165 offset:32\n\t" \
  "ds_read_b64_tr_b16 v[66:67], v165 offset:0\n\t" \
  "ds_read_b64_tr_b16 v[68:69], v165 offset:288\n\t" \
  "ds_read_b64_tr_b16 v[70:71], v165 offset:256\n\t" \
  "ds_read_b64_tr_b16 v[72:73], v165 offset:544\n\t" \
  "ds_read_b64_tr_b16 v[74:75], v165 offset:512\n\t" \
  "ds_read_b64_tr_b16 v[76:77], v165 offset:800\n\t" \
  "ds_read_b64_tr_b16 v[78:79], v165 offset:768\n\t" \
  "ds_read_b64_tr_b16 v[80:81], v165 offset:1056\n\t" \
  "ds_read_b64_tr_b16 v[82:83], v165 offset:1024\n\t" \
  "ds_read_b64_tr_b16 v[84:85], v165 offset:1312\n\t" \
  "ds_read_b64_tr_b16 v[86:87], v165 offset:1280\n\t" \
  "ds_read_b64_tr_b16 v[88:89], v165 offset:1568\n\t" \
  "ds_read_b64_tr_b16 v[90:91], v165 offset:1536\n\t" \
  "ds_read_b64_tr_b16 v[92:93], v165 offset:1824\n\t" \
  "ds_read_b64_tr_b16 v[94:95], v165 offset:1792\n\t" \
  "v_mov_b32 v24, v166\n\t" \
  "v_mov_b32 v25, v167\n\t" \
  "v_mov_b32 v63, 0xc0\n\t" \
  "v_add_u32 v158, 64, v24\n\t" \
  "v_bfi_b32 v26, v63, v158, v24\n\t" \
  "v_add_u32 v158, 64, v25\n\t" \
  "v_bfi_b32 v27, v63, v158, v25\n\t" \
  "v_add_u32 v158, 64, v26\n\t" \
  "v_bfi_b32 v28, v63, v158, v26\n\t" \
  "v_add_u32 v158, 64, v27\n\t" \
  "v_bfi_b32 v29, v63, v158, v27\n\t" \
  "v_add_u32 v158, 64, v28\n\t" \
  "v_bfi_b32 v30, v63, v158, v28\n\t" \
  "v_add_u32 v158, 64, v29\n\t" \
  "v_bfi_b32 v31, v63, v158, v29\n\t" \
  "s_mov_b32 s85, 0x5a0000\n\t" \
  "s_load_dword s84, %[ph], s85\n\t" \
  "s_mov_b32 s85, 0x5a0800\n\t" \
  "s_load_dword s84, %[ph], s85\n\t" \
  "s_mov_b32 s85, 0x5a1000\n\t" \
  "s_load_dword s84, %[ph], s85\n\t" \
  "s_barrier\n\t" \
  "ds_read_b64_tr_b16 v[96:97], v165 offset:2080\n\t" \
  "ds_read_b64_tr_b16 v[98:99], v165 offset:2048\n\t" \
  "ds_read_b64_tr_b16 v[100:101], v165 offset:2336\n\t" \
  "ds_read_b64_tr_b16 v[102:103], v165 offset:2304\n\t" \
  "ds_read_b64_tr_b16 v[104:105], v165 offset:2592\n\t" \
  "ds_read_b64_tr_b16 v[106:107], v165 offset:2560\n\t" \
  "ds_read_b64_tr_b16 v[108:109], v165 offset:2848\n\t" \
  "ds_read_b64_tr_b16 v[110:111], v165 offset:2816\n\t" \
  "ds_read_b64_tr_b16 v[112:113], v165 offset:3104\n\t" \
  "ds_read_b64_tr_b16 v[114:115], v165 offset:3072\n\t" \
  "ds_read_b64_tr_b16 v[116:117], v165 offset:3360\n\t" \
  "ds_read_b64_tr_b16 v[118:119], v165 offset:3328\n\t" \
  "ds_read_b64_tr_b16 v[120:121], v165 offset:3616\n\t" \
  "ds_read_b64_tr_b16 v[122:123], v165 offset:3584\n\t" \
  "ds_read_b64_tr_b16 v[124:125], v165 offset:3872\n\t" \
  "ds_read_b64_tr_b16 v[126:127], v165 offset:3840\n\t" \
  "s_barrier\n\t" \
  "s_waitcnt lgkmcnt(0)\n\t" \
  "v_lshlrev_b32 v61, 16, v161\n\t" \
  "v_lshlrev_b32 v62, 16, v162\n\t" \
  "v_add_f32 v61, v61, v62\n\t" \
  "v_mov_b32 v62, 0xffff\n\t" \
  "v_cvt_pk_bf16_f32 v61, v61, v61\n\t" \
  "s_mov_b64 exec, 1\n\t" \
  "v_bfi_b32 v66, v62, v61, v66\n\t" \
  "s_mov_b64 exec, 15\n\t" \
  "v_and_b32 v66, v62, v66\n\t" \
  "s_mov_b64 exec, 16\n\t" \
  "v_bfi_b32 v66, v62, v66, v61\n\t" \
  "s_mov_b64 exec, 0xff\n\t" \
  "v_mov_b32 v67, 0\n\t" \
  "s_mov_b64 exec, 0x100\n\t" \
  "v_mov_b32 v67, v61\n\t" \
  "s_mov_b64 exec, 0xf00\n\t" \
  "v_and_b32 v67, v62, v67\n\t" \
  "s_mov_b64 exec, 0x1000\n\t" \
  "v_bfi_b32 v67, v62, v67, v61\n\t" \
  "s_mov_b64 exec, -1\n\t" \
  "s_waitcnt lgkmcnt(0)\n\t"
#define PP_SM 0
#define PP_NB 8
#define PP_Q 0
#define PP_CLOB "v0", "v1", "v2", "v3", "v4", "v5", "v6", "v7", "v8", "v9", "v10", "v11", "v12", "v13", "v14", "v15", "v16", "v17", "v18", "v19", "v20", "v21", "v22", "v23", "v32", "v33", "v34", "v35", "v36", "v37", "v38", "v39", "v40", "v41", "v42", "v43", "v60", "v61", "v62", "v63", "v128", "v129", "v130", "v131", "v132", "v133", "v134", "v135", "v136", "v137", "v138", "v139", "v140", "v141", "v142", "v143", "v144", "v145", "v146", "v147", "v148", "v149", "v150", "v151", "v158", "v161", "v162", "s82", "s83", "s84", "s85"

#define LD_ASM1 \
  "s_add_u32 m0, s39, 49152\n\t" \
  "s_mov_b32 s68, 0x0\n\t" \
  "buffer_load_dwordx4 v5, s[44:47], s68 offen offset:0 lds\n\t" \
  "buffer_load_dwordx4 v6, s[44:47], s68 offen offset:1024 lds\n\t" \
  "buffer_load_dwordx4 v7, s[44:47], s68 offen offset:2048 lds\n\t" \
  "s_add_u32 m0, s38, 36864\n\t" \
  "s_mov_b32 s68, 0x0\n\t" \
  "buffer_load_dwordx4 v0, s[40:43], s68 offen offset:0 lds\n\t" \
  "buffer_load_dwordx4 v1, s[40:43], s68 offen offset:1024 lds\n\t" \
  "buffer_load_dwordx4 v2, s[40:43], s68 offen offset:2048 lds\n\t" \
  "buffer_load_dwordx4 v3, s[40:43], s68 offen offset:3072 lds\n\t" \
  "s_add_u32 m0, s37, 36864\n\t" \
  "s_mov_b32 s68, 0x0\n\t" \
  "s_mov_b32 exec_hi, 0\n\t" \
  "buffer_load_dwordx4 v4, s[40:43], s68 offen offset:0 lds\n\t" \
  "s_mov_b32 exec_hi, -1\n\t" \
  "s_add_u32 m0, s39, 61440\n\t" \
  "s_mov_b32 s68, 0x30000\n\t" \
  "buffer_load_dwordx4 v5, s[44:47], s68 offen offset:0 lds\n\t" \
  "buffer_load_dwordx4 v6, s[44:47], s68 offen offset:1024 lds\n\t" \
  "buffer_load_dwordx4 v7, s[44:47], s68 offen offset:2048 lds\n\t" \
  "s_mov_b32 m0, %[sk]\n\t" \
  "s_mov_b32 exec_hi, 0\n\t" \
  "buffer_load_dword v9, s[40:43], 0 offen lds\n\t" \
  "s_mov_b32 exec_lo, %[wm]\n\t" \
  "buffer_load_dword v10, s[44:47], 0 offen lds\n\t" \
  "s_mov_b64 exec, -1\n\t"
#define LD_ASM2 \
  "s_add_u32 m0, s38, 0\n\t" \
  "s_mov_b32 s68, 0x80\n\t" \
  "buffer_load_dwordx4 v0, s[40:43], s68 offen offset:0 lds\n\t" \
  "s_mov_b32 s85, 0x180000\n\t" \
  "s_load_dword s84, %[pw], s85\n\t" \
  "buffer_load_dwordx4 v1, s[40:43], s68 offen offset:1024 lds\n\t" \
  "s_mov_b32 s85, 0x180800\n\t" \
  "s_load_dword s84, %[pw], s85\n\t" \
  "buffer_load_dwordx4 v2, s[40:43], s68 offen offset:2048 lds\n\t" \
  "s_mov_b32 s85, 0x181000\n\t" \
  "s_load_dword s84, %[pw], s85\n\t" \
  "buffer_load_dwordx4 v3, s[40:43], s68 offen offset:3072 lds\n\t" \
  "s_mov_b32 s85, 0x1b0000\n\t" \
  "s_load_dword s84, %[pw], s85\n\t" \
  "s_add_u32 m0, s37, 0\n\t" \
  "s_mov_b32 s68, 0x80\n\t" \
  "s_mov_b32 exec_hi, 0\n\t" \
  "buffer_load_dwordx4 v4, s[40:43], s68 offen offset:0 lds\n\t" \
  "s_mov_b32 exec_hi, -1\n\t" \
  "s_mov_b32 m0, %[tsk]\n\t" \
  "s_mov_b32 s85, 0x0\n\t" \
  "s_mov_b64 exec, %[hm]\n\t" \
  "buffer_load_dword v16, %[sh], s85 offen lds\n\t" \
  "s_mov_b64 exec, -1\n\t" \
  "s_mov_b32 s68, 0x60000\n\t" \
  "s_mov_b32 s69, 0x90000\n\t" \
  "s_mov_b32 s70, 0x100\n\t" \
  "s_mov_b32 s71, 0x100\n\t" \
  "s_mov_b32 s72, 0xc0000\n\t" \
  "s_mov_b32 s73, 0xf0000\n\t" \
  "s_add_u32 m0, s39, 0\n\t" \
  "s_waitcnt vmcnt(8)\n\t" \
  "s_barrier\n\t" \
  "buffer_load_dwordx4 v5, s[44:47], s68 offen offset:0 lds\n\t" \
  "s_mov_b32 s85, 0x1b0800\n\t" \
  "s_load_dword s84, %[pw], s85\n\t" \
  "buffer_load_dwordx4 v6, s[44:47], s68 offen offset:1024 lds\n\t" \
  "s_mov_b32 s85, 0x1b1000\n\t" \
  "s_load_dword s84, %[pw], s85\n\t" \
  "buffer_load_dwordx4 v7, s[44:47], s68 offen offset:2048 lds\n\t" \
  "s_mov_b32 s85, 0x1e0000\n\t" \
  "s_load_dword s84, %[pw], s85\n\t" \
  "s_add_u32 m0, s39, 12288\n\t" \
  "s_nop 0\n\t" \
  "buffer_load_dwordx4 v5, s[44:47], s69 offen offset:0 lds\n\t" \
  "s_mov_b32 s85, 0x1e0800\n\t" \
  "s_load_dword s84, %[pw], s85\n\t" \
  "buffer_load_dwordx4 v6, s[44:47], s69 offen offset:1024 lds\n\t" \
  "s_mov_b32 s85, 0x1e1000\n\t" \
  "s_load_dword s84, %[pw], s85\n\t" \
  "buffer_load_dwordx4 v7, s[44:47], s69 offen offset:2048 lds\n\t" \
  "s_mov_b32 s85, 0x210000\n\t" \
  "s_load_dword s84, %[pw], s85\n\t" \
  "s_add_u32 m0, s38, 18432\n\t" \
  "s_nop 0\n\t" \
  "buffer_load_dwordx4 v0, s[40:43], s70 offen offset:0 lds\n\t" \
  "buffer_load_dwordx4 v1, s[40:43], s70 offen offset:1024 lds\n\t" \
  "buffer_load_dwordx4 v2, s[40:43], s70 offen offset:2048 lds\n\t" \
  "buffer_load_dwordx4 v3, s[40:43], s70 offen offset:3072 lds\n\t" \
  "s_add_u32 m0, s37, 18432\n\t" \
  "s_nop 0\n\t" \
  "s_mov_b32 exec_hi, 0\n\t" \
  "buffer_load_dwordx4 v4, s[40:43], s71 offen offset:0 lds\n\t" \
  "s_add_u32 m0, s39, 24576\n\t" \
  "s_mov_b32 exec_hi, -1\n\t" \
  "buffer_load_dwordx4 v5, s[44:47], s72 offen offset:0 lds\n\t" \
  "buffer_load_dwordx4 v6, s[44:47], s72 offen offset:1024 lds\n\t" \
  "buffer_load_dwordx4 v7, s[44:47], s72 offen offset:2048 lds\n\t" \
  "s_add_u32 m0, s39, 36864\n\t" \
  "s_nop 0\n\t" \
  "buffer_load_dwordx4 v5, s[44:47], s73 offen offset:0 lds\n\t" \
  "buffer_load_dwordx4 v6, s[44:47], s73 offen offset:1024 lds\n\t" \
  "buffer_load_dwordx4 v7, s[44:47], s73 offen offset:2048 lds\n\t" \
  "s_mov_b32 m0, %[tsk]\n\t" \
  "s_mov_b32 s85, 0x180000\n\t" \
  "s_mov_b64 exec, %[hm]\n\t" \
  "buffer_load_dword v16, %[sh], s85 offen lds\n\t" \
  "s_mov_b64 exec, -1\n\t"
#define LD_ASM3 \
  "s_mov_b32 s68, 0x180\n\t" \
  "s_mov_b32 s69, 0x180\n\t" \
  "s_mov_b32 s70, 0x120000\n\t" \
  "s_mov_b32 s71, 0x150000\n\t" \
  "s_add_u32 m0, s38, 36864\n\t" \
  "s_waitcnt vmcnt(12)\n\t" \
  "s_barrier\n\t" \
  "buffer_load_dwordx4 v0, s[40:43], s68 offen offset:0 lds\n\t" \
  "s_mov_b32 s85, 0x210800\n\t" \
  "s_load_dword s84, %[pw], s85\n\t" \
  "buffer_load_dwordx4 v1, s[40:43], s68 offen offset:1024 lds\n\t" \
  "s_mov_b32 s85, 0x211000\n\t" \
  "s_load_dword s84, %[pw], s85\n\t" \
  "buffer_load_dwordx4 v2, s[40:43], s68 offen offset:2048 lds\n\t" \
  "s_mov_b32 s85, 0x240000\n\t" \
  "s_load_dword s84, %[pw], s85\n\t" \
  "buffer_load_dwordx4 v3, s[40:43], s68 offen offset:3072 lds\n\t" \
  "s_mov_b32 s85, 0x240800\n\t" \
  "s_load_dword s84, %[pw], s85\n\t" \
  "s_add_u32 m0, s37, 36864\n\t" \
  "s_nop 0\n\t" \
  "s_mov_b32 exec_hi, 0\n\t" \
  "buffer_load_dwordx4 v4, s[40:43], s69 offen offset:0 lds\n\t" \
  "s_add_u32 m0, s39, 49152\n\t" \
  "s_mov_b32 exec_hi, -1\n\t" \
  "buffer_load_dwordx4 v5, s[44:47], s70 offen offset:0 lds\n\t" \
  "s_mov_b32 s85, 0x241000\n\t" \
  "s_load_dword s84, %[pw], s85\n\t" \
  "buffer_load_dwordx4 v6, s[44:47], s70 offen offset:1024 lds\n\t" \
  "s_mov_b32 s85, 0x270000\n\t" \
  "s_load_dword s84, %[pw], s85\n\t" \
  "buffer_load_dwordx4 v7, s[44:47], s70 offen offset:2048 lds\n\t" \
  "s_add_u32 m0, s39, 61440\n\t" \
  "s_nop 0\n\t" \
  "buffer_load_dwordx4 v5, s[44:47], s71 offen offset:0 lds\n\t" \
  "buffer_load_dwordx4 v6, s[44:47], s71 offen offset:1024 lds\n\t" \
  "buffer_load_dwordx4 v7, s[44:47], s71 offen offset:2048 lds\n\t" \
  "s_mov_b32 s68, 0x200\n\t" \
  "s_mov_b32 s69, 0x200\n\t" \
  "s_mov_b32 s70, 0x180000\n\t" \
  "s_mov_b32 s71, 0x1b0000\n\t" \
  "s_add_u32 m0, s38, 0\n\t" \
  "s_waitcnt vmcnt(12)\n\t" \
  "s_barrier\n\t" \
  "buffer_load_dwordx4 v0, s[40:43], s68 offen offset:0 lds\n\t" \
  "s_mov_b32 s85, 0x270800\n\t" \
  "s_load_dword s84, %[pw], s85\n\t" \
  "buffer_load_dwordx4 v1, s[40:43], s68 offen offset:1024 lds\n\t" \
  "s_mov_b32 s85, 0x271000\n\t" \
  "s_load_dword s84, %[pw], s85\n\t" \
  "buffer_load_dwordx4 v2, s[40:43], s68 offen offset:2048 lds\n\t" \
  "s_mov_b32 s85, 0x2a0000\n\t" \
  "s_load_dword s84, %[pw], s85\n\t" \
  "buffer_load_dwordx4 v3, s[40:43], s68 offen offset:3072 lds\n\t" \
  "s_mov_b32 s85, 0x2a0800\n\t" \
  "s_load_dword s84, %[pw], s85\n\t" \
  "s_add_u32 m0, s37, 0\n\t" \
  "s_nop 0\n\t" \
  "s_mov_b32 exec_hi, 0\n\t" \
  "buffer_load_dwordx4 v4, s[40:43], s69 offen offset:0 lds\n\t" \
  "s_add_u32 m0, s39, 0\n\t" \
  "s_mov_b32 exec_hi, -1\n\t" \
  "buffer_load_dwordx4 v5, s[44:47], s70 offen offset:0 lds\n\t" \
  "s_mov_b32 s85, 0x2a1000\n\t" \
  "s_load_dword s84, %[pw], s85\n\t" \
  "buffer_load_dwordx4 v6, s[44:47], s70 offen offset:1024 lds\n\t" \
  "s_mov_b32 s85, 0x2d0000\n\t" \
  "s_load_dword s84, %[pw], s85\n\t" \
  "buffer_load_dwordx4 v7, s[44:47], s70 offen offset:2048 lds\n\t" \
  "s_add_u32 m0, s39, 12288\n\t" \
  "s_nop 0\n\t" \
  "buffer_load_dwordx4 v5, s[44:47], s71 offen offset:0 lds\n\t" \
  "buffer_load_dwordx4 v6, s[44:47], s71 offen offset:1024 lds\n\t" \
  "buffer_load_dwordx4 v7, s[44:47], s71 offen offset:2048 lds\n\t" \
  "s_mov_b32 s68, 0x280\n\t" \
  "s_mov_b32 s69, 0x280\n\t" \
  "s_mov_b32 s70, 0x1e0000\n\t" \
  "s_mov_b32 s71, 0x210000\n\t" \
  "s_add_u32 m0, s38, 18432\n\t" \
  "s_waitcnt vmcnt(11)\n\t" \
  "s_barrier\n\t" \
  "buffer_load_dwordx4 v0, s[40:43], s68 offen offset:0 lds\n\t" \
  "s_mov_b32 s85, 0x2d0800\n\t" \
  "s_load_dword s84, %[pw], s85\n\t" \
  "buffer_load_dwordx4 v1, s[40:43], s68 offen offset:1024 lds\n\t" \
  "s_mov_b32 s85, 0x2d1000\n\t" \
  "s_load_dword s84, %[pw], s85\n\t" \
  "buffer_load_dwordx4 v2, s[40:43], s68 offen offset:2048 lds\n\t" \
  "s_mov_b32 s85, 0x300000\n\t" \
  "s_load_dword s84, %[pw], s85\n\t" \
  "buffer_load_dwordx4 v3, s[40:43], s68 offen offset:3072 lds\n\t" \
  "s_mov_b32 s85, 0x300800\n\t" \
  "s_load_dword s84, %[pw], s85\n\t" \
  "s_add_u32 m0, s37, 18432\n\t" \
  "s_nop 0\n\t" \
  "s_mov_b32 exec_hi, 0\n\t" \
  "buffer_load_dwordx4 v4, s[40:43], s69 offen offset:0 lds\n\t" \
  "s_add_u32 m0, s39, 24576\n\t" \
  "s_mov_b32 exec_hi, -1\n\t" \
  "buffer_load_dwordx4 v5, s[44:47], s70 offen offset:0 lds\n\t" \
  "s_mov_b32 s85, 0x301000\n\t" \
  "s_load_dword s84, %[pw], s85\n\t" \
  "buffer_load_dwordx4 v6, s[44:47], s70 offen offset:1024 lds\n\t" \
  "s_mov_b32 s85, 0x330000\n\t" \
  "s_load_dword s84, %[pw], s85\n\t" \
  "buffer_load_dwordx4 v7, s[44:47], s70 offen offset:2048 lds\n\t" \
  "s_add_u32 m0, s39, 36864\n\t" \
  "s_nop 0\n\t" \
  "buffer_load_dwordx4 v5, s[44:47], s71 offen offset:0 lds\n\t" \
  "buffer_load_dwordx4 v6, s[44:47], s71 offen offset:1024 lds\n\t" \
  "buffer_load_dwordx4 v7, s[44:47], s71 offen offset:2048 lds\n\t" \
  "s_mov_b32 s68, 0x300\n\t" \
  "s_mov_b32 s69, 0x300\n\t" \
  "s_mov_b32 s70, 0x240000\n\t" \
  "s_mov_b32 s71, 0x270000\n\t" \
  "s_add_u32 m0, s38, 36864\n\t" \
  "s_waitcnt vmcnt(11)\n\t" \
  "s_barrier\n\t" \
  "buffer_load_dwordx4 v0, s[40:43], s68 offen offset:0 lds\n\t" \
  "s_mov_b32 s85, 0x330800\n\t" \
  "s_load_dword s84, %[pw], s85\n\t" \
  "buffer_load_dwordx4 v1, s[40:43], s68 offen offset:1024 lds\n\t" \
  "s_mov_b32 s85, 0x331000\n\t" \
  "s_load_dword s84, %[pw], s85\n\t" \
  "buffer_load_dwordx4 v2, s[40:43], s68 offen offset:2048 lds\n\t" \
  "s_mov_b32 s85, 0x360000\n\t" \
  "s_load_dword s84, %[pw], s85\n\t" \
  "buffer_load_dwordx4 v3, s[40:43], s68 offen offset:3072 lds\n\t" \
  "s_mov_b32 s85, 0x360800\n\t" \
  "s_load_dword s84, %[pw], s85\n\t" \
  "s_add_u32 m0, s37, 36864\n\t" \
  "s_nop 0\n\t" \
  "s_mov_b32 exec_hi, 0\n\t" \
  "buffer_load_dwordx4 v4, s[40:43], s69 offen offset:0 lds\n\t" \
  "s_add_u32 m0, s39, 49152\n\t" \
  "s_mov_b32 exec_hi, -1\n\t" \
  "buffer_load_dwordx4 v5, s[44:47], s70 offen offset:0 lds\n\t" \
  "s_mov_b32 s85, 0x361000\n\t" \
  "s_load_dword s84, %[pw], s85\n\t" \
  "buffer_load_dwordx4 v6, s[44:47], s70 offen offset:1024 lds\n\t" \
  "s_mov_b32 s85, 0x390000\n\t" \
  "s_load_dword s84, %[pw], s85\n\t" \
  "buffer_load_dwordx4 v7, s[44:47], s70 offen offset:2048 lds\n\t" \
  "s_add_u32 m0, s39, 61440\n\t" \
  "s_nop 0\n\t" \
  "buffer_load_dwordx4 v5, s[44:47], s71 offen offset:0 lds\n\t" \
  "buffer_load_dwordx4 v6, s[44:47], s71 offen offset:1024 lds\n\t" \
  "buffer_load_dwordx4 v7, s[44:47], s71 offen offset:2048 lds\n\t" \
  "s_mov_b32 s68, 0x380\n\t" \
  "s_mov_b32 s69, 0x380\n\t" \
  "s_mov_b32 s70, 0x2a0000\n\t" \
  "s_mov_b32 s71, 0x2d0000\n\t" \
  "s_add_u32 m0, s38, 0\n\t" \
  "s_waitcnt vmcnt(11)\n\t" \
  "s_barrier\n\t" \
  "buffer_load_dwordx4 v0, s[40:43], s68 offen offset:0 lds\n\t" \
  "s_mov_b32 s85, 0x390800\n\t" \
  "s_load_dword s84, %[pw], s85\n\t" \
  "buffer_load_dwordx4 v1, s[40:43], s68 offen offset:1024 lds\n\t" \
  "s_mov_b32 s85, 0x391000\n\t" \
  "s_load_dword s84, %[pw], s85\n\t" \
  "buffer_load_dwordx4 v2, s[40:43], s68 offen offset:2048 lds\n\t" \
  "s_mov_b32 s85, 0x3c0000\n\t" \
  "s_load_dword s84, %[pw], s85\n\t" \
  "buffer_load_dwordx4 v3, s[40:43], s68 offen offset:3072 lds\n\t" \
  "s_mov_b32 s85, 0x3c0800\n\t" \
  "s_load_dword s84, %[pw], s85\n\t" \
  "s_add_u32 m0, s37, 0\n\t" \
  "s_nop 0\n\t" \
  "s_mov_b32 exec_hi, 0\n\t" \
  "buffer_load_dwordx4 v4, s[40:43], s69 offen offset:0 lds\n\t" \
  "s_add_u32 m0, s39, 0\n\t" \
  "s_mov_b32 exec_hi, -1\n\t" \
  "buffer_load_dwordx4 v5, s[44:47], s70 offen offset:0 lds\n\t" \
  "s_mov_b32 s85, 0x3c1000\n\t" \
  "s_load_dword s84, %[pw], s85\n\t" \
  "buffer_load_dwordx4 v6, s[44:47], s70 offen offset:1024 lds\n\t" \
  "s_mov_b32 s85, 0x3f0000\n\t" \
  "s_load_dword s84, %[pw], s85\n\t" \
  "buffer_load_dwordx4 v7, s[44:47], s70 offen offset:2048 lds\n\t" \
  "s_add_u32 m0, s39, 12288\n\t" \
  "s_nop 0\n\t" \
  "buffer_load_dwordx4 v5, s[44:47], s71 offen offset:0 lds\n\t" \
  "buffer_load_dwordx4 v6, s[44:47], s71 offen offset:1024 lds\n\t" \
  "buffer_load_dwordx4 v7, s[44:47], s71 offen offset:2048 lds\n\t" \
  "s_mov_b32 s68, 0x400\n\t" \
  "s_mov_b32 s69, 0x400\n\t" \
  "s_mov_b32 s70, 0x300000\n\t" \
  "s_mov_b32 s71, 0x330000\n\t" \
  "s_add_u32 m0, s38, 18432\n\t" \
  "s_waitcnt vmcnt(11)\n\t" \
  "s_barrier\n\t" \
  "buffer_load_dwordx4 v0, s[40:43], s68 offen offset:0 lds\n\t" \
  "s_mov_b32 s85, 0x3f0800\n\t" \
  "s_load_dword s84, %[pw], s85\n\t" \
  "buffer_load_dwordx4 v1, s[40:43], s68 offen offset:1024 lds\n\t" \
  "s_mov_b32 s85, 0x3f1000\n\t" \
  "s_load_dword s84, %[pw], s85\n\t" \
  "buffer_load_dwordx4 v2, s[40:43], s68 offen offset:2048 lds\n\t" \
  "s_mov_b32 s85, 0x420000\n\t" \
  "s_load_dword s84, %[pw], s85\n\t" \
  "buffer_load_dwordx4 v3, s[40:43], s68 offen offset:3072 lds\n\t" \
  "s_mov_b32 s85, 0x420800\n\t" \
  "s_load_dword s84, %[pw], s85\n\t" \
  "s_add_u32 m0, s37, 18432\n\t" \
  "s_nop 0\n\t" \
  "s_mov_b32 exec_hi, 0\n\t" \
  "buffer_load_dwordx4 v4, s[40:43], s69 offen offset:0 lds\n\t" \
  "s_add_u32 m0, s39, 24576\n\t" \
  "s_mov_b32 exec_hi, -1\n\t" \
  "buffer_load_dwordx4 v5, s[44:47], s70 offen offset:0 lds\n\t" \
  "s_mov_b32 s85, 0x421000\n\t" \
  "s_load_dword s84, %[pw], s85\n\t" \
  "buffer_load_dwordx4 v6, s[44:47], s70 offen offset:1024 lds\n\t" \
  "s_mov_b32 s85, 0x450000\n\t" \
  "s_load_dword s84, %[pw], s85\n\t" \
  "buffer_load_dwordx4 v7, s[44:47], s70 offen offset:2048 lds\n\t" \
  "s_add_u32 m0, s39, 36864\n\t" \
  "s_nop 0\n\t" \
  "buffer_load_dwordx4 v5, s[44:47], s71 offen offset:0 lds\n\t" \
  "buffer_load_dwordx4 v6, s[44:47], s71 offen offset:1024 lds\n\t" \
  "buffer_load_dwordx4 v7, s[44:47], s71 offen offset:2048 lds\n\t" \
  "s_mov_b32 s68, 0x480\n\t" \
  "s_mov_b32 s69, 0x480\n\t" \
  "s_mov_b32 s70, 0x360000\n\t" \
  "s_mov_b32 s71, 0x390000\n\t" \
  "s_add_u32 m0, s38, 36864\n\t" \
  "s_waitcnt vmcnt(11)\n\t" \
  "s_barrier\n\t" \
  "buffer_load_dwordx4 v0, s[40:43], s68 offen offset:0 lds\n\t" \
  "s_mov_b32 s85, 0x450800\n\t" \
  "s_load_dword s84, %[pw], s85\n\t" \
  "buffer_load_dwordx4 v1, s[40:43], s68 offen offset:1024 lds\n\t" \
  "s_mov_b32 s85, 0x451000\n\t" \
  "s_load_dword s84, %[pw], s85\n\t" \
  "buffer_load_dwordx4 v2, s[40:43], s68 offen offset:2048 lds\n\t" \
  "s_mov_b32 s85, 0x480000\n\t" \
  "s_load_dword s84, %[pw], s85\n\t" \
  "buffer_load_dwordx4 v3, s[40:43], s68 offen offset:3072 lds\n\t" \
  "s_mov_b32 s85, 0x480800\n\t" \
  "s_load_dword s84, %[pw], s85\n\t" \
  "s_add_u32 m0, s37, 36864\n\t" \
  "s_nop 0\n\t" \
  "s_mov_b32 exec_hi, 0\n\t" \
  "buffer_load_dwordx4 v4, s[40:43], s69 offen offset:0 lds\n\t" \
  "s_add_u32 m0, s39, 49152\n\t" \
  "s_mov_b32 exec_hi, -1\n\t" \
  "buffer_load_dwordx4 v5, s[44:47], s70 offen offset:0 lds\n\t" \
  "s_mov_b32 s85, 0x481000\n\t" \
  "s_load_dword s84, %[pw], s85\n\t" \
  "buffer_load_dwordx4 v6, s[44:47], s70 offen offset:1024 lds\n\t" \
  "s_mov_b32 s85, 0x4b0000\n\t" \
  "s_load_dword s84, %[pw], s85\n\t" \
  "buffer_load_dwordx4 v7, s[44:47], s70 offen offset:2048 lds\n\t" \
  "s_add_u32 m0, s39, 61440\n\t" \
  "s_nop 0\n\t" \
  "buffer_load_dwordx4 v5, s[44:47], s71 offen offset:0 lds\n\t" \
  "buffer_load_dwordx4 v6, s[44:47], s71 offen offset:1024 lds\n\t" \
  "buffer_load_dwordx4 v7, s[44:47], s71 offen offset:2048 lds\n\t" \
  "s_mov_b32 s68, 0x500\n\t" \
  "s_mov_b32 s69, 0x500\n\t" \
  "s_mov_b32 s70, 0x3c0000\n\t" \
  "s_mov_b32 s71, 0x3f0000\n\t" \
  "s_add_u32 m0, s38, 0\n\t" \
  "s_waitcnt vmcnt(11)\n\t" \
  "s_barrier\n\t" \
  "buffer_load_dwordx4 v0, s[40:43], s68 offen offset:0 lds\n\t" \
  "s_mov_b32 s85, 0x4b0800\n\t" \
  "s_load_dword s84, %[pw], s85\n\t" \
  "buffer_load_dwordx4 v1, s[40:43], s68 offen offset:1024 lds\n\t" \
  "s_mov_b32 s85, 0x4b1000\n\t" \
  "s_load_dword s84, %[pw], s85\n\t" \
  "buffer_load_dwordx4 v2, s[40:43], s68 offen offset:2048 lds\n\t" \
  "s_mov_b32 s85, 0x4e0000\n\t" \
  "s_load_dword s84, %[pw], s85\n\t" \
  "buffer_load_dwordx4 v3, s[40:43], s68 offen offset:3072 lds\n\t" \
  "s_mov_b32 s85, 0x4e0800\n\t" \
  "s_load_dword s84, %[pw], s85\n\t" \
  "s_add_u32 m0, s37, 0\n\t" \
  "s_nop 0\n\t" \
  "s_mov_b32 exec_hi, 0\n\t" \
  "buffer_load_dwordx4 v4, s[40:43], s69 offen offset:0 lds\n\t" \
  "s_add_u32 m0, s39, 0\n\t" \
  "s_mov_b32 exec_hi, -1\n\t" \
  "buffer_load_dwordx4 v5, s[44:47], s70 offen offset:0 lds\n\t" \
  "s_mov_b32 s85, 0x4e1000\n\t" \
  "s_load_dword s84, %[pw], s85\n\t" \
  "buffer_load_dwordx4 v6, s[44:47], s70 offen offset:1024 lds\n\t" \
  "s_mov_b32 s85, 0x510000\n\t" \
  "s_load_dword s84, %[pw], s85\n\t" \
  "buffer_load_dwordx4 v7, s[44:47], s70 offen offset:2048 lds\n\t" \
  "s_add_u32 m0, s39, 12288\n\t" \
  "s_nop 0\n\t" \
  "buffer_load_dwordx4 v5, s[44:47], s71 offen offset:0 lds\n\t" \
  "buffer_load_dwordx4 v6, s[44:47], s71 offen offset:1024 lds\n\t" \
  "buffer_load_dwordx4 v7, s[44:47], s71 offen offset:2048 lds\n\t" \
  "s_mov_b32 s68, 0x580\n\t" \
  "s_mov_b32 s69, 0x580\n\t" \
  "s_mov_b32 s70, 0x420000\n\t" \
  "s_mov_b32 s71, 0x450000\n\t" \
  "s_add_u32 m0, s38, 18432\n\t" \
  "s_waitcnt vmcnt(11)\n\t" \
  "s_barrier\n\t" \
  "buffer_load_dwordx4 v0, s[40:43], s68 offen offset:0 lds\n\t" \
  "s_mov_b32 s85, 0x510800\n\t" \
  "s_load_dword s84, %[pw], s85\n\t" \
  "buffer_load_dwordx4 v1, s[40:43], s68 offen offset:1024 lds\n\t" \
  "s_mov_b32 s85, 0x511000\n\t" \
  "s_load_dword s84, %[pw], s85\n\t" \
  "buffer_load_dwordx4 v2, s[40:43], s68 offen offset:2048 lds\n\t" \
  "s_mov_b32 s85, 0x540000\n\t" \
  "s_load_dword s84, %[pw], s85\n\t" \
  "buffer_load_dwordx4 v3, s[40:43], s68 offen offset:3072 lds\n\t" \
  "s_mov_b32 s85, 0x540800\n\t" \
  "s_load_dword s84, %[pw], s85\n\t" \
  "s_add_u32 m0, s37, 18432\n\t" \
  "s_nop 0\n\t" \
  "s_mov_b32 exec_hi, 0\n\t" \
  "buffer_load_dwordx4 v4, s[40:43], s69 offen offset:0 lds\n\t" \
  "s_add_u32 m0, s39, 24576\n\t" \
  "s_mov_b32 exec_hi, -1\n\t" \
  "buffer_load_dwordx4 v5, s[44:47], s70 offen offset:0 lds\n\t" \
  "s_mov_b32 s85, 0x541000\n\t" \
  "s_load_dword s84, %[pw], s85\n\t" \
  "buffer_load_dwordx4 v6, s[44:47], s70 offen offset:1024 lds\n\t" \
  "s_mov_b32 s85, 0x570000\n\t" \
  "s_load_dword s84, %[pw], s85\n\t" \
  "buffer_load_dwordx4 v7, s[44:47], s70 offen offset:2048 lds\n\t" \
  "s_add_u32 m0, s39, 36864\n\t" \
  "s_nop 0\n\t" \
  "buffer_load_dwordx4 v5, s[44:47], s71 offen offset:0 lds\n\t" \
  "buffer_load_dwordx4 v6, s[44:47], s71 offen offset:1024 lds\n\t" \
  "buffer_load_dwordx4 v7, s[44:47], s71 offen offset:2048 lds\n\t" \
  "s_mov_b32 s68, 0x600\n\t" \
  "s_mov_b32 s69, 0x600\n\t" \
  "s_mov_b32 s70, 0x480000\n\t" \
  "s_mov_b32 s71, 0x4b0000\n\t" \
  "s_add_u32 m0, s38, 36864\n\t" \
  "s_waitcnt vmcnt(11)\n\t" \
  "s_barrier\n\t" \
  "buffer_load_dwordx4 v0, s[40:43], s68 offen offset:0 lds\n\t" \
  "s_mov_b32 s85, 0x570800\n\t" \
  "s_load_dword s84, %[pw], s85\n\t" \
  "buffer_load_dwordx4 v1, s[40:43], s68 offen offset:1024 lds\n\t" \
  "s_mov_b32 s85, 0x571000\n\t" \
  "s_load_dword s84, %[pw], s85\n\t" \
  "buffer_load_dwordx4 v2, s[40:43], s68 offen offset:2048 lds\n\t" \
  "s_mov_b32 s85, 0x5a0000\n\t" \
  "s_load_dword s84, %[pw], s85\n\t" \
  "buffer_load_dwordx4 v3, s[40:43], s68 offen offset:3072 lds\n\t" \
  "s_mov_b32 s85, 0x5a0800\n\t" \
  "s_load_dword s84, %[pw], s85\n\t" \
  "s_add_u32 m0, s37, 36864\n\t" \
  "s_nop 0\n\t" \
  "s_mov_b32 exec_hi, 0\n\t" \
  "buffer_load_dwordx4 v4, s[40:43], s69 offen offset:0 lds\n\t" \
  "s_add_u32 m0, s39, 49152\n\t" \
  "s_mov_b32 exec_hi, -1\n\t" \
  "buffer_load_dwordx4 v5, s[44:47], s70 offen offset:0 lds\n\t" \
  "s_mov_b32 s85, 0x5a1000\n\t" \
  "s_load_dword s84, %[pw], s85\n\t" \
  "buffer_load_dwordx4 v6, s[44:47], s70 offen offset:1024 lds\n\t" \
  "s_mov_b32 s85, 0x5d0000\n\t" \
  "s_load_dword s84, %[pw], s85\n\t" \
  "buffer_load_dwordx4 v7, s[44:47], s70 offen offset:2048 lds\n\t" \
  "s_add_u32 m0, s39, 61440\n\t" \
  "s_nop 0\n\t" \
  "buffer_load_dwordx4 v5, s[44:47], s71 offen offset:0 lds\n\t" \
  "buffer_load_dwordx4 v6, s[44:47], s71 offen offset:1024 lds\n\t" \
  "buffer_load_dwordx4 v7, s[44:47], s71 offen offset:2048 lds\n\t" \
  "s_mov_b32 s68, 0x680\n\t" \
  "s_mov_b32 s69, 0x680\n\t" \
  "s_mov_b32 s70, 0x4e0000\n\t" \
  "s_mov_b32 s71, 0x510000\n\t" \
  "s_add_u32 m0, s38, 0\n\t" \
  "s_waitcnt vmcnt(11)\n\t" \
  "s_barrier\n\t" \
  "buffer_load_dwordx4 v0, s[40:43], s68 offen offset:0 lds\n\t" \
  "s_load_dword s84, %[pq], 0x0\n\t" \
  "buffer_load_dwordx4 v1, s[40:43], s68 offen offset:1024 lds\n\t" \
  "s_load_dword s84, %[pq], 0x40\n\t" \
  "buffer_load_dwordx4 v2, s[40:43], s68 offen offset:2048 lds\n\t" \
  "s_load_dword s84, %[pq], 0x800\n\t" \
  "buffer_load_dwordx4 v3, s[40:43], s68 offen offset:3072 lds\n\t" \
  "s_load_dword s84, %[pq], 0x840\n\t" \
  "s_add_u32 m0, s37, 0\n\t" \
  "s_nop 0\n\t" \
  "s_mov_b32 exec_hi, 0\n\t" \
  "buffer_load_dwordx4 v4, s[40:43], s69 offen offset:0 lds\n\t" \
  "s_add_u32 m0, s39, 0\n\t" \
  "s_mov_b32 exec_hi, -1\n\t" \
  "buffer_load_dwordx4 v5, s[44:47], s70 offen offset:0 lds\n\t" \
  "s_load_dword s84, %[pq], 0x1000\n\t" \
  "buffer_load_dwordx4 v6, s[44:47], s70 offen offset:1024 lds\n\t" \
  "s_load_dword s84, %[pq], 0x1040\n\t" \
  "buffer_load_dwordx4 v7, s[44:47], s70 offen offset:2048 lds\n\t" \
  "s_mov_b32 s85, 0x5d0800\n\t" \
  "s_load_dword s84, %[pw], s85\n\t" \
  "s_add_u32 m0, s39, 12288\n\t" \
  "s_nop 0\n\t" \
  "buffer_load_dwordx4 v5, s[44:47], s71 offen offset:0 lds\n\t" \
  "s_mov_b32 s85, 0x5d1000\n\t" \
  "s_load_dword s84, %[pw], s85\n\t" \
  "buffer_load_dwordx4 v6, s[44:47], s71 offen offset:1024 lds\n\t" \
  "buffer_load_dwordx4 v7, s[44:47], s71 offen offset:2048 lds\n\t" \
  "s_mov_b32 s68, 0x700\n\t" \
  "s_mov_b32 s69, 0x700\n\t" \
  "s_mov_b32 s70, 0x540000\n\t" \
  "s_mov_b32 s71, 0x570000\n\t" \
  "s_add_u32 m0, s38, 18432\n\t" \
  "s_waitcnt vmcnt(11)\n\t" \
  "s_barrier\n\t" \
  "buffer_load_dwordx4 v0, s[40:43], s68 offen offset:0 lds\n\t" \
  "s_load_dword s84, %[pq], 0x1800\n\t" \
  "buffer_load_dwordx4 v1, s[40:43], s68 offen offset:1024 lds\n\t" \
  "s_load_dword s84, %[pq], 0x1840\n\t" \
  "buffer_load_dwordx4 v2, s[40:43], s68 offen offset:2048 lds\n\t" \
  "s_load_dword s84, %[pq], 0x2000\n\t" \
  "buffer_load_dwordx4 v3, s[40:43], s68 offen offset:3072 lds\n\t" \
  "s_load_dword s84, %[pq], 0x2040\n\t" \
  "s_add_u32 m0, s37, 18432\n\t" \
  "s_nop 0\n\t" \
  "s_mov_b32 exec_hi, 0\n\t" \
  "buffer_load_dwordx4 v4, s[40:43], s69 offen offset:0 lds\n\t" \
  "s_add_u32 m0, s39, 24576\n\t" \
  "s_mov_b32 exec_hi, -1\n\t" \
  "buffer_load_dwordx4 v5, s[44:47], s70 offen offset:0 lds\n\t" \
  "s_load_dword s84, %[pq], 0x2800\n\t" \
  "buffer_load_dwordx4 v6, s[44:47], s70 offen offset:1024 lds\n\t" \
  "s_load_dword s84, %[pq], 0x2840\n\t" \
  "buffer_load_dwordx4 v7, s[44:47], s70 offen offset:2048 lds\n\t" \
  "s_add_u32 m0, s39, 36864\n\t" \
  "s_nop 0\n\t" \
  "buffer_load_dwordx4 v5, s[44:47], s71 offen offset:0 lds\n\t" \
  "buffer_load_dwordx4 v6, s[44:47], s71 offen offset:1024 lds\n\t" \
  "buffer_load_dwordx4 v7, s[44:47], s71 offen offset:2048 lds\n\t" \
  "s_mov_b32 s68, 0x780\n\t" \
  "s_mov_b32 s69, 0x780\n\t" \
  "s_mov_b32 s70, 0x5a0000\n\t" \
  "s_mov_b32 s71, 0x5d0000\n\t" \
  "s_add_u32 m0, s38, 36864\n\t" \
  "s_waitcnt vmcnt(11)\n\t" \
  "s_barrier\n\t" \
  "buffer_load_dwordx4 v0, s[40:43], s68 offen offset:0 lds\n\t" \
  "s_load_dword s84, %[pq], 0x3000\n\t" \
  "buffer_load_dwordx4 v1, s[40:43], s68 offen offset:1024 lds\n\t" \
  "s_load_dword s84, %[pq], 0x3040\n\t" \
  "buffer_load_dwordx4 v2, s[40:43], s68 offen offset:2048 lds\n\t" \
  "s_load_dword s84, %[pq], 0x3800\n\t" \
  "buffer_load_dwordx4 v3, s[40:43], s68 offen offset:3072 lds\n\t" \
  "s_load_dword s84, %[pq], 0x3840\n\t" \
  "s_add_u32 m0, s37, 36864\n\t" \
  "s_nop 0\n\t" \
  "s_mov_b32 exec_hi, 0\n\t" \
  "buffer_load_dwordx4 v4, s[40:43], s69 offen offset:0 lds\n\t" \
  "s_add_u32 m0, s39, 49152\n\t" \
  "s_mov_b32 exec_hi, -1\n\t" \
  "buffer_load_dwordx4 v5, s[44:47], s70 offen offset:0 lds\n\t" \
  "buffer_load_dwordx4 v6, s[44:47], s70 offen offset:1024 lds\n\t" \
  "buffer_load_dwordx4 v7, s[44:47], s70 offen offset:2048 lds\n\t" \
  "s_add_u32 m0, s39, 61440\n\t" \
  "s_nop 0\n\t" \
  "buffer_load_dwordx4 v5, s[44:47], s71 offen offset:0 lds\n\t" \
  "buffer_load_dwordx4 v6, s[44:47], s71 offen offset:1024 lds\n\t" \
  "buffer_load_dwordx4 v7, s[44:47], s71 offen offset:2048 lds\n\t" \
  "v_readlane_b32 s68, v9, 18\n\t" \
  "v_readlane_b32 s69, v9, 19\n\t" \
  "v_readlane_b32 s70, v9, 20\n\t" \
  "v_readlane_b32 s71, v9, 21\n\t" \
  "v_readlane_b32 s72, v9, 0\n\t" \
  "v_readlane_b32 s73, v9, 1\n\t" \
  "v_readlane_b32 s74, v9, 2\n\t" \
  "v_readlane_b32 s75, v9, 3\n\t" \
  "v_readlane_b32 s76, v9, 4\n\t" \
  "v_readlane_b32 s77, v9, 5\n\t" \
  "s_add_u32 m0, %[ob], 73728\n\t" \
  "s_waitcnt vmcnt(11)\n\t" \
  "s_barrier\n\t" \
  "s_nop 2\n\t" \
  "buffer_load_dwordx4 v8, %[so], s68 offen lds\n\t" \
  "s_add_u32 m0, %[ob], 77824\n\t" \
  "s_nop 0\n\t" \
  "buffer_load_dwordx4 v8, %[so], s69 offen lds\n\t" \
  "s_add_u32 m0, %[ob], 81920\n\t" \
  "s_nop 0\n\t" \
  "buffer_load_dwordx4 v8, %[so], s70 offen lds\n\t" \
  "s_add_u32 m0, %[ob], 86016\n\t" \
  "s_nop 0\n\t" \
  "buffer_load_dwordx4 v8, %[so], s71 offen lds\n\t" \
  "s_add_u32 m0, %[ob], 0\n\t" \
  "s_nop 0\n\t" \
  "buffer_load_dwordx4 v8, %[so], s72 offen lds\n\t" \
  "s_add_u32 m0, %[ob], 4096\n\t" \
  "s_nop 0\n\t" \
  "buffer_load_dwordx4 v8, %[so], s73 offen lds\n\t" \
  "s_add_u32 m0, %[ob], 8192\n\t" \
  "s_nop 0\n\t" \
  "buffer_load_dwordx4 v8, %[so], s74 offen lds\n\t" \
  "s_add_u32 m0, %[ob], 12288\n\t" \
  "s_nop 0\n\t" \
  "buffer_load_dwordx4 v8, %[so], s75 offen lds\n\t" \
  "s_add_u32 m0, %[ob], 16384\n\t" \
  "s_nop 0\n\t" \
  "buffer_load_dwordx4 v8, %[so], s76 offen lds\n\t" \
  "s_add_u32 m0, %[ob], 20480\n\t" \
  "s_nop 0\n\t" \
  "buffer_load_dwordx4 v8, %[so], s77 offen lds\n\t" \
  "s_waitcnt vmcnt(10)\n\t" \
  "s_barrier\n\t" \
  "s_nop 2\n\t" \
  "s_add_u32 m0, %[ob], 90112\n\t" \
  "v_readlane_b32 s68, v9, 22\n\t" \
  "s_nop 4\n\t" \
  "buffer_load_dwordx4 v8, %[so], s68 offen lds\n\t" \
  "s_add_u32 m0, %[ob], 94208\n\t" \
  "v_readlane_b32 s68, v9, 23\n\t" \
  "s_nop 4\n\t" \
  "buffer_load_dwordx4 v8, %[so], s68 offen lds\n\t" \
  "s_add_u32 m0, %[ob], 98304\n\t" \
  "v_readlane_b32 s68, v9, 24\n\t" \
  "s_nop 4\n\t" \
  "buffer_load_dwordx4 v8, %[so], s68 offen lds\n\t" \
  "s_add_u32 m0, %[ob], 102400\n\t" \
  "v_readlane_b32 s68, v9, 25\n\t" \
  "s_nop 4\n\t" \
  "buffer_load_dwordx4 v8, %[so], s68 offen lds\n\t" \
  "s_add_u32 m0, %[ob], 106496\n\t" \
  "v_readlane_b32 s68, v9, 26\n\t" \
  "s_nop 4\n\t" \
  "buffer_load_dwordx4 v8, %[so], s68 offen lds\n\t" \
  "s_add_u32 m0, %[ob], 24576\n\t" \
  "v_readlane_b32 s68, v9, 6\n\t" \
  "s_nop 4\n\t" \
  "buffer_load_dwordx4 v8, %[so], s68 offen lds\n\t" \
  "s_add_u32 m0, %[ob], 28672\n\t" \
  "v_readlane_b32 s68, v9, 7\n\t" \
  "s_nop 4\n\t" \
  "buffer_load_dwordx4 v8, %[so], s68 offen lds\n\t" \
  "s_add_u32 m0, %[ob], 32768\n\t" \
  "v_readlane_b32 s68, v9, 8\n\t" \
  "s_nop 4\n\t" \
  "buffer_load_dwordx4 v8, %[so], s68 offen lds\n\t" \
  "s_add_u32 m0, %[ob], 36864\n\t" \
  "v_readlane_b32 s68, v9, 9\n\t" \
  "s_nop 4\n\t" \
  "buffer_load_dwordx4 v8, %[so], s68 offen lds\n\t" \
  "s_add_u32 m0, %[ob], 40960\n\t" \
  "v_readlane_b32 s68, v9, 10\n\t" \
  "s_nop 4\n\t" \
  "buffer_load_dwordx4 v8, %[so], s68 offen lds\n\t" \
  "s_add_u32 m0, %[ob], 45056\n\t" \
  "v_readlane_b32 s68, v9, 11\n\t" \
  "s_nop 4\n\t" \
  "buffer_load_dwordx4 v8, %[so], s68 offen lds\n\t" \
  "s_nop 3\n\t" \
  "s_barrier\n\t" \
  "s_nop 2\n\t" \
  "s_add_u32 m0, %[ob], 110592\n\t" \
  "v_readlane_b32 s68, v9, 27\n\t" \
  "s_nop 4\n\t" \
  "buffer_load_dwordx4 v8, %[so], s68 offen lds\n\t" \
  "s_add_u32 m0, %[ob], 114688\n\t" \
  "v_readlane_b32 s68, v9, 28\n\t" \
  "s_nop 4\n\t" \
  "buffer_load_dwordx4 v8, %[so], s68 offen lds\n\t" \
  "s_add_u32 m0, %[ob], 118784\n\t" \
  "v_readlane_b32 s68, v9, 29\n\t" \
  "s_nop 4\n\t" \
  "buffer_load_dwordx4 v8, %[so], s68 offen lds\n\t" \
  "s_add_u32 m0, %[ob], 122880\n\t" \
  "v_readlane_b32 s68, v9, 30\n\t" \
  "s_nop 4\n\t" \
  "buffer_load_dwordx4 v8, %[so], s68 offen lds\n\t" \
  "s_add_u32 m0, %[ob], 126976\n\t" \
  "v_readlane_b32 s68, v9, 31\n\t" \
  "s_nop 4\n\t" \
  "buffer_load_dwordx4 v8, %[so], s68 offen lds\n\t" \
  "s_add_u32 m0, %[ob], 49152\n\t" \
  "v_readlane_b32 s68, v9, 12\n\t" \
  "s_nop 4\n\t" \
  "buffer_load_dwordx4 v8, %[so], s68 offen lds\n\t" \
  "s_add_u32 m0, %[ob], 53248\n\t" \
  "v_readlane_b32 s68, v9, 13\n\t" \
  "s_nop 4\n\t" \
  "buffer_load_dwordx4 v8, %[so], s68 offen lds\n\t" \
  "s_add_u32 m0, %[ob], 57344\n\t" \
  "v_readlane_b32 s68, v9, 14\n\t" \
  "s_nop 4\n\t" \
  "buffer_load_dwordx4 v8, %[so], s68 offen lds\n\t" \
  "s_add_u32 m0, %[ob], 61440\n\t" \
  "v_readlane_b32 s68, v9, 15\n\t" \
  "s_nop 4\n\t" \
  "buffer_load_dwordx4 v8, %[so], s68 offen lds\n\t" \
  "s_add_u32 m0, %[ob], 65536\n\t" \
  "v_readlane_b32 s68, v9, 16\n\t" \
  "s_nop 4\n\t" \
  "buffer_load_dwordx4 v8, %[so], s68 offen lds\n\t" \
  "s_add_u32 m0, %[ob], 69632\n\t" \
  "v_readlane_b32 s68, v9, 17\n\t" \
  "s_nop 4\n\t" \
  "buffer_load_dwordx4 v8, %[so], s68 offen lds\n\t" \
  "s_waitcnt vmcnt(0) lgkmcnt(0)\n\t"
#define LD_NBAR 17
#define LD_CO 0
#define LD_X5 1
#define LD_HT 8
#define LD_TP 3
#define LD_CLOB "v11", "s68", "s69", "s70", "s71", "s72", "s73", "s74", "s75", "s76", "s77", "s78", "s79", "s80", "s81", "s82", "s83", "s85"

#define ENDW
#define TS(i)
#define TSS(i)
#define TOUT(n)

#define SPIN(cond,slp) do{ int sp_=0; while(cond){ __builtin_amdgcn_s_sleep(slp); if(++sp_>(1<<22)) break; } }while(0)
__device__ __forceinline__ unsigned rd1(const char* p){ return __builtin_amdgcn_readfirstlane(*(volatile LDS_AS unsigned*)(LDS_AS char*)p); }
__device__ __forceinline__ unsigned lds_inc(const char* p, int l){ unsigned r=0; if(l==0) r=__hip_atomic_fetch_add((LDS_AS unsigned*)(LDS_AS char*)p, 1u, __ATOMIC_RELAXED, __HIP_MEMORY_SCOPE_WORKGROUP); return __builtin_amdgcn_readfirstlane(r); }
__device__ __forceinline__ void lds_add(unsigned adr){ asm volatile("s_mov_b64 exec, 1\n\tds_add_u32 %0, %1\n\ts_mov_b64 exec, -1" :: "v"(adr), "v"(1u) : "memory"); }
__device__ __forceinline__ void publish(unsigned cnt, const unsigned* base, unsigned voff, unsigned tag){
  unsigned t;
  asm volatile("s_waitcnt vmcnt(0)\n\ts_mov_b64 exec, 1\n\tds_add_rtn_u32 %0, %1, %2\n\ts_waitcnt lgkmcnt(0)\n\tv_cmp_eq_u32 vcc, 3, %0\n\ts_cbranch_vccz PB_%=\n\t"
    "s_mov_b64 exec, 0xffff\n\tglobal_store_dword %3, %4, %5 sc1\n\tPB_%=:\n\ts_mov_b64 exec, -1"
    : "=&v"(t) : "v"(cnt), "v"(1u), "v"(voff), "v"(tag), "s"(base) : "memory", "vcc");
}
__device__ __forceinline__ void bars(int n){
  asm volatile("BL_%=:\n\ts_barrier\n\ts_sub_u32 %0, %0, 1\n\ts_cmp_lg_u32 %0, 0\n\ts_cbranch_scc1 BL_%=" : "+s"(n) :: "memory", "scc");
}
#define barc(rem) barc_(rem)
#define CTRA
#define CTRP
__device__ __forceinline__ void barc_(unsigned& rem){
  asm volatile("s_cmp_eq_u32 %0, 0\n\ts_cbranch_scc1 BC_%=\n\ts_barrier\n\ts_sub_u32 %0, %0, 1\n\tBC_%=:" : "+s"(rem) :: "memory", "scc");
}
template<int SLP> __device__ __forceinline__ void wait_flag_b(const unsigned* p, unsigned voff, unsigned tag, unsigned& rem CTRA){
  int spins=0;
  while(true){
    unsigned v; asm volatile("global_load_dword %0, %1, %2 sc1" : "=v"(v) : "v"(voff), "s"(p) : "memory");
    barc(rem); if(PB2) barc(rem);
    asm volatile("s_waitcnt vmcnt(0)" : "+v"(v) :: "memory");
    if(__all(v==tag) || ++spins>(1<<21)) break;
    if(rem==0) __builtin_amdgcn_s_sleep(SLP);
  }
}
__device__ __forceinline__ void stout(void* sbase, unsigned voff, u32x4 v){
  st16cs(sbase,voff,v);
}

typedef unsigned u32x8 __attribute__((ext_vector_type(8)));
typedef unsigned u32x16 __attribute__((ext_vector_type(16)));
__device__ __forceinline__ char* ptr64(unsigned lo, unsigned hi){ return (char*)(((unsigned long long)hi<<32)|lo); }
__device__ __forceinline__ u32x4 srd(const char* p, unsigned bytes){ return u32x4{(unsigned)(size_t)p,(unsigned)((size_t)p>>32)&0xffffu,bytes,0x20000u}; }
constexpr size_t WS_O=0, WS_KV=(size_t)S*DM*2, WS_Y=(size_t)S*DM*4, WS_FL=(size_t)S*DM*12;

__global__ __launch_bounds__(768,3) void fused_kernel(const u16* x_, const u16* wqkv_, const u16* wo_, char* ws_, u16* out_,
    const u16* kin_, const u16* vin_, const u16* shk_, const u16* hk_, const u16* qin_, const u16* sD_)
{
  __shared__ __attribute__((aligned(128))) char smem[ALL_LDS];
  TS(0);
  int wv;
  asm volatile(
      "s_load_dwordx16 s[48:63], %[ka], 0x0\n\ts_load_dwordx4 s[64:67], %[ka], 0x40\n\ts_load_dwordx2 s[78:79], %[ka], 0x50\n\ts_getpc_b64 s[72:73]\n\tv_readfirstlane_b32 s75, v0\n\ts_lshr_b32 s74, %[b], 3\n\ts_mul_i32 s74, s74, 12\n\ts_lshr_b32 s75, s75, 6\n\t"
      "s_add_u32 s74, s74, s75\n\ts_min_u32 s74, s74, %[cl]\n\ts_lshl_b32 s74, s74, 7\n\ts_mov_b32 m0, %[sk]\n\tv_mov_b32 v1, s74\n\ts_mov_b64 exec, 1\n\tglobal_load_lds_dword v1, s[72:73]\n\ts_mov_b64 exec, -1\n\t"
      "s_mov_b32 s76, %[b]\n\ts_cmp_lt_u32 s75, 4\n\ts_cbranch_scc1 FK_G\n\ts_cmp_lt_u32 s75, 8\n\ts_cbranch_scc0 FK_C\n\t"
      "v_and_b32 v9, 7, v0\n\tv_and_b32 v10, 63, v0\n\ts_lshl_b32 s74, %[b], 19\n\ts_lshl_b32 s73, %[b], 11\n\ts_or_b32 s74, s74, s73\n\ts_and_b32 s74, s74, 0x3fc000\n\ts_lshl_b32 s73, s75, 9\n\t"
      "s_add_u32 s74, s74, s73\n\ts_add_u32 s74, s74, 0x7800\n\tv_lshl_add_u32 v9, v9, 11, s74\n\tv_and_b32 v1, 24, v0\n\ts_lshl2_add_u32 s74, %[b], s75\n\ts_mul_i32 s74, s74, %[nw]\n\tv_lshl_add_u32 v9, v1, 4, v9\n\t"
      "s_add_u32 s74, s74, %[w0]\n\tv_add_lshl_u32 v10, v10, s74, 7\n\t"
      "s_setprio %[lp]\n\ts_and_b32 s74, s75, 3\n\tv_lshrrev_b32 v1, 3, v0\n\tv_lshrrev_b32 v3, 4, v0\n\ts_and_b32 s72, %[b], 1\n\ts_and_b32 s73, %[b], 0xe0\n\ts_lshl_b32 s72, s72, 10\n\ts_lshl2_add_u32 s72, s73, s72\n\t"
      "v_and_b32 v4, 2, v1\n\tv_and_or_b32 v3, v3, 4, v4\n\tv_and_b32 v4, 31, v1\n\tv_xor_b32 v3, v3, v0\n\tv_mul_u32_u24 v4, 0x1800, v4\n\tv_and_b32 v3, 7, v3\n\tv_lshl_add_u32 v3, v3, 4, s72\n\t"
      "s_mul_i32 s39, s74, 0xc00\n\ts_lshl_b32 s38, s74, 12\n\ts_lshl_b32 s37, s74, 9\n\tv_add_u32 v5, v3, v4\n\tv_and_b32 v1, 7, v1\n\tv_add_u32 v6, 0x400, v5\n\tv_add_u32 v7, 0x800, v5\n\t"
      "s_and_b32 s72, %[b], 6\n\ts_and_b32 s73, %[b], 24\n\ts_lshl4_add_u32 s72, s72, s73\n\tv_xor_b32 v3, v1, v0\n\ts_lshl_b32 s72, s72, 15\n\tv_and_b32 v3, 7, v3\n\tv_lshl_add_u32 v1, v1, 11, s72\n\t"
      "s_mov_b32 s47, 0x20000\n\ts_mov_b32 s46, 0x600000\n\tv_lshl_add_u32 v1, v3, 4, v1\n\ts_mov_b32 s43, s47\n\ts_mov_b32 s42, 0x408000\n\ts_lshl_b32 s72, s74, 6\n\ts_and_b32 s72, s72, 64\n\t"
      "v_xor_b32 v4, s72, v1\n\ts_lshl_b32 s72, s74, 13\n\ts_add_u32 s72, s72, 0x40000\n\tv_add_u32 v4, s72, v4\n\ts_lshl_b32 s72, s74, 16\n\tv_add_u32 v0, s72, v1\n\ts_movk_i32 s73, 0x3c00\n\t"
      "v_add_u32 v1, s73, v0\n\ts_add_u32 s38, s38, %[xf]\n\tv_add_u32 v2, s73, v1\n\ts_add_u32 s37, s37, %[xf]+16384\n\tv_add_u32 v3, s73, v2\n\tv_max_u32 v0, 0x8000, v0\n\tv_max_u32 v1, 0x8000, v1\n\t"
      "s_waitcnt lgkmcnt(0)\n\ts_mov_b64 s[44:45], s[50:51]\n\ts_sub_u32 s40, s48, 0x8000\n\ts_subb_u32 s41, s49, 0\n\t"
      LD_ASM1
      "s_branch FK_L"
      : "=s"(wv)
      : "{v0}"(threadIdx.x), [ka]"s"(__builtin_amdgcn_kernarg_segment_ptr()), [b]"s"(blockIdx.x), [cl]"n"(CODEL-1+CODEX), [sk]"n"(SINK+256), [lp]"n"(LPR), [xf]"n"(X_OFF), [nw]"n"(3*TPW), [w0]"n"(TW0*3072-12*TPW), [wm]"n"((1<<(3*TPW))-1)
      : "memory", "scc", "vcc", "s48", "s49", "s50", "s51", "s52", "s53", "s54", "s55", "s72", "s73", "s74", "s75", "s76", "s78", "s79", "v1", "v2", "v3", "v4", "v5", "v6", "v7", "v9", "v10",
        "s40", "s41", "s42", "s43", "s44", "s45", "s46", "s47", "s37", "s38", "s39", "s56", "s57", "s58", "s59", "s60", "s61", "s62", "s63", "s64", "s65", "s66", "s67", "s68");
  #define ENTER(lbl) int w8, bid; u32x8 a0, a1; u32x4 a2; u32x2 a3; asm volatile(lbl ":" : "={s75}"(w8), "={s76}"(bid), "={s[48:55]}"(a0), "={s[56:63]}"(a1), "={s[64:67]}"(a2), "={s[78:79]}"(a3) :: "memory");
  #define ARGW asm volatile("s_waitcnt lgkmcnt(0)" : "+{s[48:55]}"(a0), "+{s[56:63]}"(a1), "+{s[64:67]}"(a2), "+{s[78:79]}"(a3));
  #define ROLE const int w=w8&3, xcd=bid&7, idx=bid>>3, rb=(xcd>>1)*4+(idx&3), hp=(xcd&1)*8+(idx>>2), s0=rb*128; (void)s0; (void)hp;
  #define LANEVARS int l; asm volatile("v_mbcnt_lo_u32_b32 %0, -1, 0\n\tv_mbcnt_hi_u32_b32 %0, -1, %0" : "=v"(l)); const int g=l>>4, fr=l&15, tq=(l&15)>>2, tp=l&3; (void)g; (void)fr; (void)tq; (void)tp;
  #define ARGS0 const char* x=ptr64(a0[0],a0[1]); const char* wqkv=ptr64(a0[2],a0[3]); const char* wo=ptr64(a0[4],a0[5]); char* ws=ptr64(a0[6],a0[7]); \
    unsigned* flags=(unsigned*)(ws+WS_FL); const int hot0=(3-(int)(a0[2]>>7))&7; (void)x; (void)wqkv; (void)wo; (void)flags; (void)hot0;
  #define OCOL ((hp&8)|((hp+(int)(a0[2]>>7)-(int)(a0[4]>>7)+OSH)&7))
  #define ORA ((unsigned)(hp-hot0-1)&7u)
  #define ORE(c) (ORA==7u? (c)-1u : (c))
  #define ORO(c) (ORE(c)>=NF && ORE(c)<NF+7u)
  #define ORD(c) (((((ORE(c)<14u? ((ORA==7u? 0u:ORA)+(ORO(c)? ORE(c)-NF : (ORE(c)<NF? ORE(c) : ORE(c)-7u)))%7u : 7u)+hot0+1)&7u))+8u*(((hp>>3)^(ORO(c) || (c)==15u))&1u))
  const unsigned sb0=__builtin_amdgcn_readfirstlane(lds_addr(smem));
  const char* syn=smem+SYN;

  if(__builtin_expect(wv==1,1)){
    ENTER("FK_L")
    u32x4 sx, sw; unsigned xt, xl, wl, xoA, xoB, xo, xo1, xo2, wof[3];
    asm volatile("; descriptors and LDS bases" : "={s[40:43]}"(sx), "={s[44:47]}"(sw), "={s38}"(xl), "={s39}"(wl), "={s37}"(xt));
    asm volatile("; lane offsets" : "={v0}"(xoA), "={v1}"(xoB), "={v2}"(xo), "={v3}"(xo1), "={v4}"(xo2), "={v5}"(wof[0]), "={v6}"(wof[1]), "={v7}"(wof[2]));
    unsigned ltr=0, ltr2=0, ltr3=0, sd; (void)ltr; (void)ltr2; (void)ltr3;
    const char* pw;
    {
      ROLE
      LANEVARS
      ARGS0
      const int hg=((idx>>2)-hot0)&7, hi=hot0*4+(idx&3);
      const unsigned long long hm= hg==0? 1ull : hg==HGR? 0xfffull : 0x3full;
      const unsigned hto=(unsigned)((hg? w*393216 : 0x40000000)+(l<6? l*2048+idx*12288 : (l-6)*2048+hi*12288));
      pw=wqkv+(idx*6144+(xcd&1)*1024+(xcd>>1)*256+w*64);
      asm volatile(LD_ASM2
        : "=&{s84}"(sd), "+s"(w8), "+s"(bid), "+s"(a0)
        : "{v0}"(xoA), "{v1}"(xoB), "{v2}"(xo), "{v3}"(xo1), "{v4}"(xo2), "{v5}"(wof[0]), "{v6}"(wof[1]), "{v7}"(wof[2]),
          "{v16}"(hto), [sh]"s"(srd(wqkv+((xcd&1)*8+hot0)*128,6291456u-1920u)), [hm]"s"(hm),
          "{s[40:43]}"(sx), "{s[44:47]}"(sw), "{s37}"(xt), "{s38}"(xl), "{s39}"(wl), [pw]"s"(pw), [tsk]"n"(SINK+256)
        : "memory", "scc", "vcc", LD_CLOB);
    }
    ROLE
    LANEVARS
    ARGS0
    const unsigned wd=OCOL*128;
    {
      const unsigned wof2=(unsigned)(w*16384+(l>>3)*2048+((((l&7)>>1)^((l>>4)&3))*32)+(l&1)*16);
      const unsigned to=ORD((unsigned)(l>>1)&15u);
      const unsigned tab=to*131072u+(l&1)*65536;
      asm volatile(LD_ASM3
        : "+{s84}"(sd)
        : "{v0}"(xoA), "{v1}"(xoB), "{v2}"(xo), "{v3}"(xo1), "{v4}"(xo2), "{v5}"(wof[0]), "{v6}"(wof[1]), "{v7}"(wof[2]), "{v8}"(wof2), "{v9}"(tab),
          "{s[40:43]}"(sx), "{s[44:47]}"(sw), [so]"s"(srd(wo+wd,2097152u-wd)), "{s37}"(xt), "{s38}"(xl), "{s39}"(wl), [ob]"s"(sb0+w*1024), [pw]"s"(pw),
          [pq]"s"(wo+((xcd&1)*1024+((3-(int)(a0[4]>>7))&7)*128+(idx*32+w*8)*2048))
        : "memory", "scc", "vcc", LD_CLOB);
    }
    const unsigned tag=rd1(syn+64)+1u;
    lds_add(sb0+SYN+12);
    TS(2);
    if(w>=4-NPOL){
      const unsigned* fp=flags+F_O+(rb*16+hp)*16; const unsigned fo=ORD((unsigned)l&15u)*4u;
      const unsigned madr=sb0+(l==0? SYN+20 : SINK+l*4);
      SPIN(rd1(syn+12)<4u || rd1(syn+16)<4u,1);
      if(w==3) PUBM(madr,1u);
      if(PD0) __builtin_amdgcn_s_sleep(PD0);
      if(w==2) __builtin_amdgcn_s_sleep(PD1); if(w==1) __builtin_amdgcn_s_sleep(2*PD1); if(w==0) __builtin_amdgcn_s_sleep(3*PD1);
      unsigned rdy=1; int spins=0;
      while(rdy!=0xFFFFu){
        unsigned v;
        asm volatile("s_mov_b64 exec, 0xffff\n\tglobal_atomic_or %0, %1, %3, %2 sc0 sc1\n\ts_mov_b64 exec, -1\n\ts_waitcnt vmcnt(0)" : "=&v"(v) : "v"(fo), "s"(fp), "v"(0u) : "memory");
        unsigned r=((unsigned)__ballot(v==tag)&0xFFFFu)|1u;
        if(++spins>(1<<21)) r=0xFFFFu;
        if(r!=rdy){ PUBM(madr,(unsigned)__builtin_ctz(~r)); rdy=r; if(w<3 && (r&0x3FFFu)==0x3FFFu) break; }
        else __builtin_amdgcn_s_sleep(OSLP);
      }
      TS(3);
      if(bid==0 && w==3){
        if(GWD){ SPIN(rd1(syn+28)==0u,8); __builtin_amdgcn_s_sleep(GWD); }
        int sp=0;
        while(true){
          u32x4 v; asm volatile("global_load_dwordx4 %0, %1, %2 sc1\n\ts_waitcnt vmcnt(0)" : "=&v"(v) : "v"(l*16), "s"(flags+F_Y) : "memory");
          if(__all(v[0]==tag && v[1]==tag && v[2]==tag && v[3]==tag) || ++sp>(1<<20)) break;
          __builtin_amdgcn_s_sleep(12);
        }
        if(l==0) stflag(flags, tag);
      }
    } else { TS(3); }
    TOUT(4);
    __builtin_amdgcn_endpgm();
  }

  if(wv==0){
    ENTER("FK_G")
    ROLE
    __builtin_amdgcn_s_setprio(GPR);
    const int h=w&1, r=w>>1;
    LANEVARS
    TSS(3);
    ARGW
    ARGS0
    if(PGT&1){ if(w==1 && l<7) touch(ws, l<6? l*2097152+PGO : (unsigned)WS_FL+2048, sb0+SINK+512); }
    if(PGT&2){ if(w==2 && l<4) touch(ptr64(a1[0],a1[1]), l*2097152+PGO, sb0+SINK+512); }
    if(PGT&4){ if(w==3 && l<1) touch(wo, PGO, sb0+SINK+512); if(w==0 && l<2) touch(wqkv, l*2097152+2097152, sb0+SINK+512); }
    TSS(4);
    {
      unsigned lx0=sb0+X_OFF+fr*128+((g^(fr&7))*16), lx1=sb0+X_OFF+fr*128+(((4+g)^(fr&7))*16);
      const int sw=wswz(8*g+tq);
      unsigned lw0=sb0+g*3072+tq*128+(unsigned)((((2*h)^sw)*32)+8*tp), lw1=sb0+g*3072+tq*128+(unsigned)((((2*h+1)^sw)*32)+8*tp);
      char* ot=ws+WS_O+(rb*16+hp)*16384+r*8192+h*2048;
      unsigned it; unsigned long long rm; asm("s_cmp_lg_u32 %1, 0\n\ts_cselect_b64 %0, -1, 0" : "=s"(rm) : "s"(r) : "scc");
      asm volatile(N2_ASM
        : [it]"=&s"(it), "+{v56}"(lx0), "+{v57}"(lx1), "+{v58}"(lw0), "+{v59}"(lw1)
        : "{v62}"(l*16), "{v63}"(sb0+EX_OFF+h*3072+l*16), [r]"s"(r), [ot]"s"(ot), [ou]"s"(ot+4096), [fa]"s"(sb0+SYN+32+w*4), [fp]"s"(sb0+SYN+32+(w^2)*4),
          [vo]"s"(2048-1024*r), [rm]"s"(rm), [z]"s"(rb|r)
        : "memory", "scc", "vcc", N2_CLOB);
    }
    TS(3);
    unsigned tb;
    {
      LANEVARS
      const unsigned tag=rd1(syn+64)+1u;
      const unsigned ra=sb0+(4*g+tq)*128+8*tp; const int sw=(2*g+(tq>>1))&3;
      unsigned b0=ra+((0^sw)*32), b1=ra+((1^sw)*32), b2=ra+((2^sw)*32), b3=ra+((3^sw)*32), vo=(unsigned)(w*4096+l*16), sy=sb0+SYN+20;
      unsigned sr=sb0+EX_OFF+w*2048+(l>>3)*128+(((l&7)^(l>>3))*16), sg=sb0+EX_OFF+w*2048+fr*128+(((g>>1)^(fr&7))*16)+(g&1)*8;
      tb=ORD((unsigned)l&15u)*16384u;
      asm volatile("" : "+{v160}"(b0), "+{v161}"(b1), "+{v162}"(b2), "+{v163}"(b3), "+{v164}"(vo), "+{v165}"(sy), "+{v166}"(sr), "+{v167}"(sg), "+{v96}"(tb));
      publish(sb0+SYN+16, flags+F_O+rb*256+hp, l*64, tag);
      TS(4);
      __builtin_amdgcn_s_setprio(G2P);
      unsigned iss=0, q0, q1, q2, cn=0;
      asm volatile(G3_ASM
        : [iss]"+&s"(iss), [t0]"=&s"(q0), [t1]"=&s"(q1), [t2]"=&s"(q2), [cn]"+&s"(cn),
          "+{v160}"(b0), "+{v161}"(b1), "+{v162}"(b2), "+{v163}"(b3), "+{v165}"(sy), "+{v96}"(tb)
        : "{v164}"(vo), "{v166}"(sr), "{v167}"(sg), [go]"s"(60+w*4), [sy]"n"(SYN+20), [ob]"s"(srd(ws+WS_O+rb*262144,262144u)), [op]"s"(ptr64(a1[0],a1[1])+((s0+w*32)*4096+2048+OCOL*128))
        : "memory", "scc", "vcc", G3_CLOB);
    }
    {
      ENDW TS(6); TOUT(7);
    }
    __builtin_amdgcn_endpgm();
  }

  {
    ENTER("FK_C")
    ROLE
    char* cr=smem+C_OFF+2048+w*4096;
    const unsigned crs=sb0+C_OFF+2048+w*4096;
    const int sblk=bid>>3, td0=(bid&7)*128+(w&1)*64;
    const int vb=(LOCA&1)? xcd*32+(idx&16)+(idx&1)*8+((idx>>1)&7) : bid, fb=(LOCA&2)? (idx&15)*16+xcd*2+(idx>>4) : bid;
    const int d=(vb>>4)*64+(((vb>>3)&1)*4+w)*8+(vb&7);
    int kv0=KV0+((KVT>>(4*(sblk&3)))&3);
    static_assert(KV0+3+PP_NB<=LD_NBAR && KV0>=EB, "barrier budget");
    unsigned tagv, ctr; f32x8 RB;
    const int fd0=(fb&15)*64, fs0=(fb>>4)*128+w*32;
    u32x4 qv[4];
    f32x16 A0,A1,A2,A3;
    const int rw=sblk*64+(w>>1)*32, j=rw>>5;
    unsigned k0,k1,k2,ko,ta,se,so,ha,l16,rb0[2];
    {
      LANEVARS
      if(w==0 && l<25 && l!=16) ((unsigned*)(smem+SYN))[l]=0u;
      if(l<32) *(u32x4*)(smem+C_OFF+w*512+l*16)=u32x4{0,0,0,0};
      asm volatile("" ::: "memory");
      __builtin_amdgcn_s_setprio(CPR);
      const int dg=l&7, rg=l>>3;
      ko=(unsigned)(rg*8192+dg*16);
      const bool top=(rw==0 && rg==0);
      k0=top? ko+6144:ko; k1=top? ko+6144:ko+2048; k2=top? ko+6144:ko+4096;
      ta=sb0+EX_OFF+(PP_SM? 0:w*1024)+(w&1)*512+dg*64; se=(l&8)? 0x03020706u : 0x05040100u; ha=crs+(32*g-4*tq+4*tp)*2; l16=l*16;
      so=(unsigned)(rg*32768+((j>>2)&3)*128+((dg&4)|((dg+j)&3))*16);
      #pragma unroll
      for(int k=0;k<2;k++){ const int q=fr-1-2*g+k; rb0[k]=sb0+C_OFF+(4+(q>>1))*256+((q>>3)&3)*64+((w+(q>>1))&3)*16+(q&1)*8; }
      asm volatile("" : "+{v152}"(k0), "+{v153}"(k1), "+{v154}"(k2), "+{v155}"(ko), "+{v156}"(ta), "+{v157}"(se), "+{v159}"(so), "+{v163}"(l16), "+{v165}"(ha), "+{v166}"(rb0[0]), "+{v167}"(rb0[1]));
    }
    TSS(3);
    ARGW
    if(EB==1) asm volatile("s_barrier" : "+s"(a1), "+s"(a2), "+s"(a3), "+s"(a0) :: "memory");
    if(EB==2) asm volatile("s_barrier\n\ts_barrier" : "+s"(a1), "+s"(a2), "+s"(a3), "+s"(a0) :: "memory");
    ARGS0
    char* out=ptr64(a1[0],a1[1]); const char* qp=ptr64(a2[2],a2[3])+(fs0*DM+fd0)*2;
    {
      const char* kb=ptr64(a1[2],a1[3])+(rw*DM+td0)*2; const char* vb=ptr64(a1[4],a1[5])+(rw*DM+td0)*2;
      unsigned long long zm; asm("s_cmp_eq_u32 %1, 0\n\ts_cselect_b64 %0, 0xff, 0" : "=s"(zm) : "s"(rw) : "scc");
      unsigned t0;
      {
        LANEVARS
        const int hi=4*hot0+(idx>>3), hv=xcd*32+(hi&16)+(hi&1)*8+((hi>>1)&7), hd=(hv>>4)*64+(((hv>>3)&1)*4+((idx>>1)&3))*8+(hv&7);
        const char* tb_= w==2? ptr64(a2[0],a2[1])+hd*4096+(idx&1)*2048 : (w? ptr64(a1[4],a1[5]) : ptr64(a1[2],a1[3]))+(hot0*256+idx*8)*2048+xcd*256;
        const unsigned to= w==2? l*128 : (l>>1)*2048+(l&1)*128;
        if(HH==2) SBAR();
        if(w<HW) asm volatile("s_mov_b32 m0, %1\n\ts_mov_b64 exec, 0xffff\n\tglobal_load_lds_dword %2, %0\n\ts_mov_b64 exec, -1" :: "s"(tb_), "s"(sb0+SINK+512), "v"(to) : "memory");
        if((idx>>2)==hot0) kv0=HKV;
      }
      if(PGT&8){ const char* tp= w==0? kb : w==1? vb : w==2? qp : ptr64(a2[0],a2[1])+d*4096;
        asm volatile("s_mov_b32 m0, %1\n\ts_mov_b64 exec, 1\n\tglobal_load_lds_dword v163, %0\n\ts_mov_b64 exec, -1" :: "s"(tp), "s"(sb0+SINK+512), "{v163}"(l16) : "memory"); }
      TSS(5);
      asm volatile(PP_ASM
        : [t0]"=&s"(t0), "=&{v164}"(ctr), "+{v159}"(so), "=&{v160}"(tagv), "=&{v[24:31]}"(RB), "=&{v[44:47]}"(qv[0]), "=&{v[48:51]}"(qv[1]), "=&{v[52:55]}"(qv[2]), "=&{v[56:59]}"(qv[3]),
          "=&{v[64:79]}"(A0), "=&{v[80:95]}"(A1), "=&{v[96:111]}"(A2), "=&{v[112:127]}"(A3)
        : "{v152}"(k0), "{v153}"(k1), "{v154}"(k2), "{v155}"(ko), "{v156}"(ta), "{v157}"(se), "{v166}"(rb0[0]), "{v167}"(rb0[1]), "{v165}"(ha), "{v163}"(l16),
          [qb0]"s"(qp), [qb2]"s"(qp+4096), [mk]"s"(0x180u), [em]"s"(1ull|(512ull<<w)), [hd]"s"(crs),
          [kb0]"s"(kb-6144), [kb3]"s"(kb), [kb5]"s"(kb+4096), [vb0]"s"(vb), [vb2]"s"(vb+4096), [sb]"s"(ws+WS_KV+td0*4096+j*512),
          [n0]"s"(kv0-EB-(HH>=2)), [ff]"s"(flags+F_PP+(td0>>6)*64+j), [zm]"s"(zm), [shd]"s"(sb0+EX_OFF+w*1024), [tgd]"s"(sb0+SYN+64), [shp]"s"(ptr64(a1[6],a1[7])+(bid&7)*1024), [fl]"s"(flags),
          [hp]"s"(ptr64(a2[0],a2[1])+d*4096), [dp]"s"(ptr64(a3[0],a3[1])+(PP_SM? d&~1:d)*2), [dsh]"s"((d&1)*16), [h0]"n"(C_OFF+2048),
          [ph]"s"(wqkv+(((xcd&1)*8+hot0)*128+idx*6144+(w>>1)*196608+(w&1)*64))
        : "memory", "scc", PP_CLOB);
    }
    LANEVARS
    const unsigned tag=__builtin_amdgcn_readfirstlane(tagv)+1u;
    unsigned rem=LD_NBAR-PP_NB-kv0;
    barc(rem);
    const unsigned* pf=flags+F_PP+(d>>6)*64+(KPOL? w*16:0);
    const char* kvp=ws+WS_KV+((vb>>4)*8+(vb&7))*32768+((vb>>3)&1)*64+w*8192;
    const unsigned kvo=(unsigned)((l>>2)*128+(l&3)*16);
    int bidx=((l>=16)? l-16 : (l==0? 63 : l+47))*4;
    asm volatile("" : "+{v32}"(bidx));
    __builtin_amdgcn_s_setprio(3);
    while(rem>PREM) barc(rem);
    wait_flag_b<8>(pf, (KPOL? fr:l)*4, tag, rem CTRP);
    #pragma unroll
    for(int i=0;i<4;i++){ if(i%KTH==0 && i) barc(rem); gldsc(kvp+i*2048, kvo, crs+i*1024); }
    barc(rem); if(PB2) barc(rem);
    WAITV(0);
    asm volatile("s_mov_b32 m0, %2\n\ts_mov_b64 exec, 1\n\tglobal_load_lds_dword %0, %1 sc1\n\ts_mov_b64 exec, -1" :: "v"(0u), "s"(pf), "s"(sb0+SYN+100+w*4) : "memory");
    f32x16 ac0, ac1; unsigned vtr, cn=0; (void)vtr;
    unsigned ca=sb0+CAR+w*1280+(fr*4+g)*4, cz= l? ca : sb0+C_OFF;
    asm volatile("s_nop 1\n\t" CV_ASM
      : "=&{v[128:143]}"(ac0), "=&{v[144:159]}"(ac1), [rem]"+&s"(rem), [cn]"+&s"(cn), "+{v40}"(ca), "+{v41}"(cz)
        , "+{v[44:47]}"(qv[0]), "+{v[48:51]}"(qv[1]), "+{v[52:55]}"(qv[2]), "+{v[56:59]}"(qv[3])
      : "{v[64:79]}"(A0), "{v[80:95]}"(A1), "{v[96:111]}"(A2), "{v[112:127]}"(A3),
        "{v[24:31]}"(RB), "{v32}"(bidx), "{v33}"(sb0+SYN+80+w*4), "{v61}"(sb0+SYN+100+(l&3)*4), [tg]"s"(tag),
        "{v42}"((256*(g&1)+16*fr+4*(g&2))*2), [yb]"s"(ws+WS_Y+d*4096), "{v43}"((l>>3)*8192+(l&7)*16), [q0]"s"(qp), [q2]"s"(qp+4096)
      : "memory", "scc", CV_CLOB);
    TS(4);
    __builtin_amdgcn_s_setprio(3);
    publish(sb0+SYN+24, flags+F_Y+vb, l*1024, tag);
    asm volatile("" : "+v"(qv[0]), "+v"(qv[1]), "+v"(qv[2]), "+v"(qv[3]));
    TS(5);
    float qf[32];
    #pragma unroll
    for(int i=0;i<16;i++){ qf[2*i]=bflo(qv[i>>2][i&3]); qf[2*i+1]=bfhi(qv[i>>2][i&3]); }
    unsigned ya=crs+(g*8+tq)*64+tp*8, wa0=crs+fr*128+((g^(fr&7))*16), wa1=crs+fr*128+(((4+g)^(fr&7))*16), ra[4], oo=(unsigned)((l>>3)*16384+(l&7)*16), yo2=(l>>2)*4096+(l&3)*16;
    #pragma unroll
    for(int i=0;i<4;i++){ const int row=4*(l>>3)+i; ra[i]=crs+row*128+(((l&7)^(row&7))*16); }
    char* op=out+fs0*4096+fd0*2; char* op2=op+8192; const char* yg=ws+WS_Y+fd0*4096+fs0*2;
    #pragma unroll
    for(int i=0;i<32;i++) asm volatile("" : "+v"(qf[i]));
    asm volatile("" : "+v"(ya), "+v"(wa0), "+v"(wa1), "+v"(ra[0]), "+v"(ra[1]), "+v"(ra[2]), "+v"(ra[3]), "+v"(oo), "+v"(yo2));
    asm volatile("" : "+s"(op), "+s"(op2), "+s"(yg));
    if((YPM>>w)&1){
      SPIN(rd1(syn+24)<4u,1);
      if(YPM>1){ if(w==1) __builtin_amdgcn_s_sleep(YPD); if(w==2) __builtin_amdgcn_s_sleep(2*YPD); if(w==3) __builtin_amdgcn_s_sleep(3*YPD); }
      { int sp=0;
        while(true){
          unsigned v=tag; asm volatile("s_mov_b64 exec, 0xffff\n\tglobal_atomic_or %0, %1, %3, %2 sc0 sc1\n\ts_mov_b64 exec, -1\n\ts_waitcnt vmcnt(0)" : "+&v"(v) : "v"(l*4), "s"(flags+F_Y+fb*16), "v"(0u) : "memory");
          if(__all(v==tag) || ++sp>(1<<21)) break;
          if(YPM>1){ if(rd1(syn+28)) break; } else __builtin_amdgcn_s_sleep(YSLP);
        } }
      PUB(sb0+(l==0? SYN+28 : SINK+l*4), 1u);
    } else SPIN(rd1(syn+28)==0u,YS2);
    #pragma unroll
    for(int i=0;i<4;i++) gldsc(yg+i*65536, yo2, crs+i*1024);
    {
      u32x4 y4[4];
      asm volatile("s_waitcnt vmcnt(0)\n\t"
        "ds_read_b64_tr_b16 v[0:1], %4\n\tds_read_b64_tr_b16 v[2:3], %4 offset:256\n\tds_read_b64_tr_b16 v[4:5], %4 offset:32\n\tds_read_b64_tr_b16 v[6:7], %4 offset:288\n\t"
        "ds_read_b64_tr_b16 v[8:9], %4 offset:2048\n\tds_read_b64_tr_b16 v[10:11], %4 offset:2304\n\tds_read_b64_tr_b16 v[12:13], %4 offset:2080\n\tds_read_b64_tr_b16 v[14:15], %4 offset:2336\n\t"
        "s_waitcnt lgkmcnt(6)\n\tds_write_b128 %5, v[0:3]\n\ts_waitcnt lgkmcnt(5)\n\tds_write_b128 %5, v[4:7] offset:2048\n\t"
        "s_waitcnt lgkmcnt(4)\n\tds_write_b128 %6, v[8:11]\n\ts_waitcnt lgkmcnt(3)\n\tds_write_b128 %6, v[12:15] offset:2048\n\t"
        "ds_read_b128 %0, %7\n\tds_read_b128 %1, %8\n\tds_read_b128 %2, %9\n\tds_read_b128 %3, %10\n\ts_waitcnt lgkmcnt(0)"
        : "=&v"(y4[0]), "=&v"(y4[1]), "=&v"(y4[2]), "=&v"(y4[3])
        : "v"(ya), "v"(wa0), "v"(wa1), "v"(ra[0]), "v"(ra[1]), "v"(ra[2]), "v"(ra[3])
        : "memory", "v0", "v1", "v2", "v3", "v4", "v5", "v6", "v7", "v8", "v9", "v10", "v11", "v12", "v13", "v14", "v15");
      #pragma unroll
      for(int i=0;i<4;i++){
        u32x4 ov;
        #pragma unroll
        for(int e=0;e<4;e++) ov[e]=pack2(mul1(qf[8*i+2*e],bflo(y4[i][e])), mul1(qf[8*i+2*e+1],bfhi(y4[i][e])));
        stout((i&2? op2:op)+(i&1)*4096, oo, ov);
      }
    }
    ENDW TS(6); TOUT(7);
    __builtin_amdgcn_endpgm();
  }
}

extern "C" void kernel_launch(void* const* d_in, const int* in_sizes, int n_in, void* d_out, int out_size, void* d_ws, size_t ws_size, hipStream_t stream){
  const u16* q=(const u16*)d_in[0]; const u16* k=(const u16*)d_in[1]; const u16* v=(const u16*)d_in[2];
  const u16* shk=(const u16*)d_in[3]; const u16* hk=(const u16*)d_in[4]; const u16* sD=(const u16*)d_in[5];
  const u16* x=(const u16*)d_in[6]; const u16* wqkv=(const u16*)d_in[7]; const u16* wo=(const u16*)d_in[8];
  fused_kernel<<<256,768,0,stream>>>(x,wqkv,wo,(char*)d_ws,(u16*)d_out,k,v,shk,hk,q,sD);
}
```
